```python
import torch

def _softmax(x, axis=-1):
    return torch.softmax(x, dim=axis)

def _gelu(x):
    return torch.nn.functional.gelu(x, approximate='tanh')

def _silu(x):
    return torch.nn.functional.silu(x)

def _sigmoid(x):
    return torch.sigmoid(x)

def _layernorm(x, axis=-1, eps=1e-05):
    m = x.mean(dim=axis, keepdim=True)
    v = x.var(dim=axis, keepdim=True, unbiased=False)
    return (x - m) / torch.sqrt(v + eps)

def _scatter_add(tgt, idx, src, axis=0):
    return tgt.clone().index_add_(axis, idx, src)

def _segment_sum(src, seg, n):
    out = torch.zeros((n,) + src.shape[1:], dtype=src.dtype)
    return out.index_add_(0, seg, src)

def _pad(x, pw):
    flat = tuple((v for pair in reversed(pw) for v in pair))
    return torch.nn.functional.pad(x, flat)
kernel_name = 'interleave_layers__memory_aug_recurrent_mem_tr__glu_gated_geglu__prompt'

def setup_inputs():

    _w = lambda a: (lambda t: t.to(torch.bfloat16) if t.is_floating_point() else t)(torch.as_tensor(a))

    def _A_setup():
        B, S, Dm = (1, 2048, 1024)
        H, Dh = (32, 128)
        M, seg = (8, 32)
        seg = min(seg, S)
        S = S // seg * seg
        return {'x': _w((lambda *s: torch.randn(*s, dtype=torch.bfloat16) * 0.05)(B, S, Dm)), 'mem0': _w((lambda *s: torch.randn(*s, dtype=torch.bfloat16) * 0.2)(B, M, Dm)), 'Wq': _w((lambda *s: torch.randn(*s, dtype=torch.bfloat16) * 1.6)(Dm, H, Dh) * Dm ** (-0.5)), 'Wk': _w((lambda *s: torch.randn(*s, dtype=torch.bfloat16) * 1.6)(Dm, H, Dh) * Dm ** (-0.5)), 'Wv': _w((lambda *s: torch.randn(*s, dtype=torch.bfloat16) * 0.2)(Dm, H, Dh) * Dm ** (-0.5)), 'Wo': _w((lambda *s: torch.randn(*s, dtype=torch.bfloat16) * 0.2)(H, Dh, Dm) * (H * Dh) ** (-0.5)), 'seg_len': seg}
    _A_in = _A_setup()

    def _B_setup():
        B, S, Dm, Df = (1, 2048, 1024, 3584)
        return {'x': _w((lambda *s: torch.randn(*s, dtype=torch.bfloat16) * 0.05)(B, S, Dm)), 'w_gate': _w((lambda *s: torch.randn(*s, dtype=torch.bfloat16) * 0.4)(Dm, Df) * Dm ** (-0.5)), 'w_up': _w((lambda *s: torch.randn(*s, dtype=torch.bfloat16) * 0.4)(Dm, Df) * Dm ** (-0.5)), 'w_down': _w((lambda *s: torch.randn(*s, dtype=torch.bfloat16) * 0.4)(Df, Dm) * Df ** (-0.5))}
    _B_in = _B_setup()
    return {'A_x': _A_in['x'], 'A_mem0': _A_in['mem0'], 'A_Wq': _A_in['Wq'], 'A_Wk': _A_in['Wk'], 'A_Wv': _A_in['Wv'], 'A_Wo': _A_in['Wo'], 'A_seg_len': _A_in['seg_len'], 'B_x': _B_in['x'], 'B_w_gate': _B_in['w_gate'], 'B_w_up': _B_in['w_up'], 'B_w_down': _B_in['w_down']}

def reference(A_x, A_mem0, A_Wq, A_Wk, A_Wv, A_Wo, A_seg_len, B_x, B_w_gate, B_w_up, B_w_down):

    def _A_ref(x, mem0, Wq, Wk, Wv, Wo, seg_len):
        B, S, Dm = x.shape
        H, Dh = (Wq.shape[1], Wq.shape[2])
        M = mem0.shape[1]
        scale = Dh ** (-0.5)
        n_seg = S // seg_len
        L = M + seg_len
        idx = torch.arange(L)
        mask = (idx[:, None] < M) | (idx[:, None] >= idx[None, :])
        mem = mem0
        outs = []
        for s in range(n_seg):
            xs = x[:, s * seg_len:(s + 1) * seg_len, :].clone()
            h = torch.concatenate([mem, xs], axis=1)
            q = torch.einsum('btd,dhe->bhte', h, Wq)
            k = torch.einsum('btd,dhe->bhte', h, Wk)
            v = torch.einsum('btd,dhe->bhte', h, Wv)
            scores = torch.einsum('bhqe,bhke->bhqk', q, k) * scale
            scores = torch.where(mask[None, None], scores, -1000000000.0)
            p = _softmax(scores, axis=-1)
            o = torch.einsum('bhqk,bhke->bhqe', p, v)
            o = torch.einsum('bhte,hed->btd', o, Wo)
            mem = o[:, :M, :].clone()
            outs.append(o[:, M:, :])
        return torch.concatenate(outs, axis=1)

    def _B_ref(x, w_gate, w_up, w_down):
        g = torch.einsum('bsd,df->bsf', x, w_gate)
        u = torch.einsum('bsd,df->bsf', x, w_up)
        h = _gelu(g) * u
        return torch.einsum('bsf,fd->bsd', h, w_down)
    x = A_x
    x = x + _A_ref(_layernorm(x, axis=-1), A_mem0, A_Wq, A_Wk, A_Wv, A_Wo, A_seg_len)
    x = x + _B_ref(_layernorm(x, axis=-1), B_w_gate, B_w_up, B_w_down)
    x = x + _A_ref(_layernorm(x, axis=-1), A_mem0, A_Wq, A_Wk, A_Wv, A_Wo, A_seg_len)
    x = x + _B_ref(_layernorm(x, axis=-1), B_w_gate, B_w_up, B_w_down)
    return x
```

```cpp
#include <hip/hip_runtime.h>
#include <stdint.h>

typedef unsigned short u16;
typedef unsigned long long u64;
typedef __attribute__((ext_vector_type(8))) short bf16x8;
typedef __attribute__((ext_vector_type(4))) short s16x4;
typedef __attribute__((ext_vector_type(4))) float f32x4;
typedef __attribute__((ext_vector_type(16))) float f32x16;
typedef __attribute__((ext_vector_type(2))) float f32x2;
typedef __attribute__((ext_vector_type(4))) unsigned v4u;
typedef __attribute__((ext_vector_type(2))) unsigned v2u;
typedef __amdgpu_buffer_rsrc_t rsrc_t;
typedef __attribute__((address_space(3))) char lds_char;
typedef __attribute__((address_space(3))) bf16x8 l8;
typedef __attribute__((address_space(3))) v4u l4;
typedef __attribute__((address_space(3))) v2u l2;
typedef __attribute__((address_space(3))) unsigned l1;
typedef __attribute__((address_space(3))) f32x4 lf4;
typedef __attribute__((address_space(3))) float lf1;
#define DEVI __device__ __forceinline__
#define AI __attribute__((always_inline))

constexpr int NSEG = 64;
constexpr float ATT_SCALE = 0.08838834764831845f;
constexpr float LOG2E = 1.4426950408889634f;
#define NPRB 3
constexpr unsigned SD = 8192;
DEVI unsigned sd(unsigned o) { return o ^ ((o >> 8) & SD); }
constexpr unsigned F_X0 = 0;
constexpr unsigned F_X = 4096;
constexpr unsigned F_A = 16384;
constexpr unsigned F_H = F_A + 2048;
constexpr unsigned F_D = F_H + 4096;
constexpr unsigned F_Y0 = 3584;
constexpr unsigned F_ABORT = 6400;
constexpr unsigned O_PM = 65536;
constexpr unsigned O_E1 = 131072;
constexpr unsigned O_PT = 262144;
constexpr unsigned O_PN = 524288;
constexpr unsigned O_UM = O_PN + 65536;
constexpr unsigned CTRL_END = O_UM + (1u << 20);
constexpr unsigned O_PRB = 0x1F0000, O_POL = 0x1FFF00;
constexpr unsigned O_XN = 4u << 20;
constexpr unsigned O_XR = O_XN + (8u << 20);
constexpr unsigned O_ST = O_XR + (8u << 20);
constexpr unsigned O_SL = O_ST + (8u << 20);
constexpr unsigned O_H = O_SL + (32u << 20);
constexpr unsigned O_Y = O_H + (36u << 20);
constexpr unsigned WS_NEED = O_Y + (8u << 20);
constexpr unsigned O_PROF = WS_NEED;
constexpr unsigned WS_RS = WS_NEED;
static_assert(CTRL_END <= O_PRB && CTRL_END % 16384 == 0 && O_Y % (8u << 20) == 0, "workspace");
constexpr unsigned OOB = 0x80000000u, OOB2 = 0x90000000u, SENT = 0xFFFFFFFFu;

struct Args { const u16 *x, *mem0, *Wq, *Wk, *Wv, *Wo, *Wg, *Wu, *Wd; char* ws; u16* out; };

typedef __attribute__((ext_vector_type(2))) __bf16 bf16x2_t;
DEVI unsigned pack2(float lo, float hi) { f32x2 v = {lo, hi}; bf16x2_t b = __builtin_convertvector(v, bf16x2_t); return __builtin_bit_cast(unsigned, b); }
DEVI float bflo(unsigned u) { return __uint_as_float(u << 16); }
DEVI float bfhi(unsigned u) { return __uint_as_float(u & 0xffff0000u); }
DEVI float rbf(float x) { return bflo(pack2(x, x)); }
DEVI bf16x8 as8(v4u v) { return __builtin_bit_cast(bf16x8, v); }
DEVI f32x4 mfma16(bf16x8 a, bf16x8 b, f32x4 c) { return __builtin_amdgcn_mfma_f32_16x16x32_bf16(a, b, c, 0, 0, 0); }
DEVI f32x16 mfma32(bf16x8 a, bf16x8 b, f32x16 c) { return __builtin_amdgcn_mfma_f32_32x32x16_bf16(a, b, c, 0, 0, 0); }
constexpr int AUX_SC1 = 16;
DEVI v4u ld16(rsrc_t r, unsigned vo, unsigned so) { return __builtin_amdgcn_raw_buffer_load_b128(r, vo, so, AUX_SC1); }
DEVI unsigned ld4(rsrc_t r, unsigned vo, unsigned so) { return __builtin_amdgcn_raw_buffer_load_b32(r, vo, so, AUX_SC1); }
DEVI void st16(v4u v, rsrc_t r, unsigned vo) { __builtin_amdgcn_raw_buffer_store_b128(v, r, vo, 0, AUX_SC1); }
DEVI void st8(v2u v, rsrc_t r, unsigned vo, unsigned so) { __builtin_amdgcn_raw_buffer_store_b64(v, r, vo, so, AUX_SC1); }
DEVI void st4(unsigned v, rsrc_t r, unsigned vo, unsigned so) { __builtin_amdgcn_raw_buffer_store_b32(v, r, vo, so, AUX_SC1); }
DEVI v2u trd(lds_char* p) { return __builtin_bit_cast(v2u, __builtin_amdgcn_ds_read_tr16_b64_v4i16((__attribute__((address_space(3))) s16x4*)p)); }
template <int O1, int O2> DEVI void tra(v2u& lo, v2u& hi, unsigned a) { asm volatile("ds_read_b64_tr_b16 %0, %2 offset:%3\n\tds_read_b64_tr_b16 %1, %2 offset:%4" : "=&v"(lo), "=&v"(hi) : "v"(a), "n"(O1), "n"(O2) : "memory"); }
DEVI void trw(v2u& a, v2u& b, v2u& c, v2u& d) { asm volatile("s_waitcnt lgkmcnt(0)" : "+v"(a), "+v"(b), "+v"(c), "+v"(d)::"memory"); }
DEVI rsrc_t mkrs(const void* p, unsigned n) { return __builtin_amdgcn_make_buffer_rsrc((void*)p, 0, (int)n, 0x00020000); }
#define CB asm volatile("" ::: "memory")
#define SB __builtin_amdgcn_sched_barrier(0)
#define WL0 asm volatile("s_waitcnt lgkmcnt(0)" ::: "memory")
#define WV0 asm volatile("s_waitcnt vmcnt(0)" ::: "memory")
#define WV(n) asm volatile("s_waitcnt vmcnt(%0)" ::"n"(n) : "memory")
#define BAR do { __builtin_amdgcn_s_barrier(); CB; } while (0)
#define RFL(x) __builtin_amdgcn_readfirstlane(x)
DEVI int lane_id() { int x; asm volatile("v_mbcnt_lo_u32_b32 %0, -1, 0\n\tv_mbcnt_hi_u32_b32 %0, -1, %0" : "=v"(x)); return x; }
template <int I> struct IC { static constexpr int v = I; };
template <int B, int E_, class F> DEVI void sfor(F&& f) { if constexpr (B < E_) { f(IC<B>{}); sfor<B + 1, E_>(f); } }
DEVI float red_x32(float x, bool mx) { unsigned u = __float_as_uint(x); auto r = __builtin_amdgcn_permlane32_swap(u, u, false, false); float a = __uint_as_float(r[0]), b = __uint_as_float(r[1]); return mx ? fmaxf(a, b) : a + b; }
DEVI float red_x16(float x, bool mx) { unsigned u = __float_as_uint(x); auto r = __builtin_amdgcn_permlane16_swap(u, u, false, false); float a = __uint_as_float(r[0]), b = __uint_as_float(r[1]); return mx ? fmaxf(a, b) : a + b; }
DEVI unsigned vmax4(v4u d) { return max(max(d[0], d[1]), max(d[2], d[3])); }
DEVI unsigned nosent(unsigned v) { return min(v, SENT - 1u); }
constexpr unsigned SPIN_CAP = 1u << 18;
DEVI bool spin_bail(unsigned& spins, rsrc_t rs) {
    if (++spins > SPIN_CAP) { st4(0u, rs, 0, F_ABORT); return true; }
    return (spins & 255u) == 0u && RFL(ld4(rs, 0, F_ABORT)) != SENT;
}
#define TSTAMP(idx)
constexpr int LDSB = 160 * 1024;
DEVI void idle(unsigned dl) { const unsigned t0 = (unsigned)__builtin_amdgcn_s_memrealtime(); while ((unsigned)__builtin_amdgcn_s_memrealtime() - t0 < dl) __builtin_amdgcn_s_sleep(8); }
#define DSLP 2

template <bool SW> DEVI void mf_a(f32x16& c, bf16x8 a, bf16x8 b) {
    if (SW) asm volatile("v_mfma_f32_32x32x16_bf16 %0, %2, %1, %0" : "+v"(c) : "a"(a), "v"(b));
    else asm volatile("v_mfma_f32_32x32x16_bf16 %0, %1, %2, %0" : "+v"(c) : "a"(a), "v"(b)); }
template <bool SW> DEVI void mf_v(f32x16& c, bf16x8 a, bf16x8 b) {
    if (SW) asm volatile("v_mfma_f32_32x32x16_bf16 %0, %2, %1, %0" : "+v"(c) : "v"(a), "v"(b));
    else asm volatile("v_mfma_f32_32x32x16_bf16 %0, %1, %2, %0" : "+v"(c) : "v"(a), "v"(b)); }
template <bool SW> DEVI void mf_a0(f32x16& c, bf16x8 a, bf16x8 b) {
    if (SW) asm volatile("v_mfma_f32_32x32x16_bf16 %0, %2, %1, 0" : "=&v"(c) : "a"(a), "v"(b));
    else asm volatile("v_mfma_f32_32x32x16_bf16 %0, %1, %2, 0" : "=&v"(c) : "a"(a), "v"(b)); }
template <bool SW> DEVI void mf_v0(f32x16& c, bf16x8 a, bf16x8 b) {
    if (SW) asm volatile("v_mfma_f32_32x32x16_bf16 %0, %2, %1, 0" : "=&v"(c) : "v"(a), "v"(b));
    else asm volatile("v_mfma_f32_32x32x16_bf16 %0, %1, %2, 0" : "=&v"(c) : "v"(a), "v"(b)); }

template <int K, bool Z> DEVI void mh(f32x4& c, bf16x8 a, bf16x8 b) {
    if (Z) { if (K) asm volatile("v_mfma_f32_16x16x32_bf16 %0, %1, %2, 0" : "=&v"(c) : "a"(a), "v"(b)); else asm volatile("v_mfma_f32_16x16x32_bf16 %0, %1, %2, 0" : "=&v"(c) : "v"(a), "v"(b)); }
    else { if (K) asm volatile("v_mfma_f32_16x16x32_bf16 %0, %1, %2, %0" : "+v"(c) : "a"(a), "v"(b)); else asm volatile("v_mfma_f32_16x16x32_bf16 %0, %1, %2, %0" : "+v"(c) : "v"(a), "v"(b)); } }
#define STD 4
#define ST2 2
#define AB 0
#define CVP 0
#define PSP64 2
#define OVR 0
#define NB1 0
#define E16 5
constexpr int XD = 3, NRG = 53;
#define PUBD 51
#define PUBG 36
#define GBUD 6
#define LROT 8
#define FD 1
template <int EPI, int NK_, bool M_> struct Sch {
    static constexpr int NK = NK_, NS = 2 * NK, NLW = NK - NRG, PER = EPI == 3 ? 8 : NK, PS = 2 * PER;
    static constexpr bool M = M_;
    static constexpr int PSP = NK_ == 64 ? PSP64 : 2, N1 = EPI != 3 && ((NB1 >> (EPI == 2 ? 2 : M_ ? 0 : 1)) & 1), NP0 = N1 ? 8 : 4, OV = EPI != 3 && ((OVR >> (EPI == 2 ? 2 : M_ ? 0 : 1)) & 1);
    static constexpr int S(int t) { return NK == 64 ? 16 * t : (t <= 2 ? 16 * t : 32 + 12 * (t - 2)); }
    static constexpr int np(int t) { return (S(t + 1) - S(t)) / 4; }
    static constexpr bool lds_step(int k) { return k % 5 == 4 && k / 5 < NLW; }
    static constexpr int lds_idx(int k) { return k / 5; }
    static constexpr int reg_idx(int k) { int n = 0; for (int i = 0; i < k; ++i) if (!lds_step(i)) ++n; return n; }
    static constexpr int kof(int i) { const int j = i % PS, b = i / PS * PER; return b + (j < 4 ? (j & 1) : j >= PS - 4 ? PER - 2 + (j & 1) : j / 2); }
    static constexpr int cof(int i) { const int j = i % PS; return j < 4 ? j / 2 : j >= PS - 4 ? (j - (PS - 4)) / 2 : j % 2; }
    static constexpr int bnd(int i) { if (i & 1) return -1; for (int t = 1 + N1; t <= 4; ++t) if (i / 2 + XD == S(t)) return t & 3; return -1; }
    static constexpr int tsl(int s) { const int kk = (s + XD - 1) % NK; int t = 0; while (t < 3 && kk >= S(t + 1)) ++t; return t; }
    static constexpr int tks(int s) { return (s + XD - 1) % NK - S(tsl(s)); }
    static constexpr int piece_of(int i) {
        if (!(i & 1)) return -1;
        const int s = i / 2, u = tks(s) - (EPI != 3 && tsl(s) == 0 ? 3 : 2);
        if (N1 && tsl(s) >= 2) return tsl(s) == 2 && u >= 0 && u % PSP < 2 && u / PSP * 2 + u % PSP < np(0) + np(1) ? u / PSP * 2 + u % PSP : -1;
        return s < NK - XD + 1 && u >= 0 && u % 3 == 0 && u / 3 < np((tsl(s) + 2) & 3) ? u / 3 : -1;
    }
    static constexpr bool fl_of(int i) { return (i & 1) && tks(i / 2) == 1 && (tsl(i / 2) + FD == 2 || tsl(i / 2) + FD == 3); }
    static constexpr int w_of(int i) { if (EPI != 3 && i < 3) return i == 2 ? 8 : -1; return !(i & 1) ? -1 : lds_step(i / 2 + 4) ? i + 7 : lds_step(i / 2 + 3) ? i + 6 : -1; }
    static constexpr int D2 = 2 * (S(2) - XD + 1), D3 = 2 * (S(3) - XD + 1), ROT = EPI == 3 ? 0 : LROT;
    static constexpr int fx(int i) {
        int n = !(i & 1) + (w_of(i) >= 0) + ((i & 1) || lds_step(i / 2)) + (piece_of(i) >= 0 ? 4 - (w_of(i) >= 0) : 0) + (fl_of(i) ? 3 : 0);
        if (i < 2 || i == ROT - 1 || (i >= D2 && i < D2 + 7) || bnd(i) >= 0) n = 6;
        return n;
    }
    struct GP { int a[128], st[2]; };
    static constexpr GP gp() {
        GP r{};
        int k = 0;
        for (int i = 0; i < 128; ++i) {
            r.a[i] = k;
            int n = fx(i);
            if (k >= 84 && !r.st[0] && n < 3) { r.st[0] = i; ++n; }
            if (k == 168 && !r.st[1] && n < 3) r.st[1] = i;
            while (k < 168) {
                const int j = k % 21, c = j == 8 || j == 9 || j == 14 || j == 15 ? 2 : 1;
                if (n + c > GBUD) break;
                n += c; ++k;
            }
        }
        return r;
    }
    static constexpr GP G = gp();
    static constexpr int st_of(int i) { if (EPI == 2) return i == G.st[0] ? 0 : i == G.st[1] ? 1 : -1; if (M) return i >= 2 && (i - 2) % STD == 0 && i < 2 + 4 * STD ? (i - 2) / STD : -1;
        if (EPI == 0) return i % ST2 == 0 && i < 4 * ST2 ? i / ST2 : -1;
        const int j = i % PS; return j == 0 ? 0 : j == 2 ? 1 : j == 4 ? 2 : j == 6 ? 3 : -1; }
    static constexpr int xr(int i, int h) { return !(i & 1) && h ? (i / 2 + XD) % NK : -1; }
    static constexpr int cv_of(int i, int h) {
        const int m = (2 * i + h + 8) % (2 * NS), t[2][8] = {{2, 3, 4, 5, 6, 7, 10, 11}, {3, 4, 6, 7, 8, 10, 11, 14}};
        for (int p = 0; p < 8; ++p) if (t[CVP][p] == m) return p;
        return -1;
    }
    static constexpr int lo(int i, int h) { return (w_of(i) >= 0 && (st_of(i) >= 0) == (h == 1)) + (h && bnd(i) >= 2); }
    static constexpr int xw(int i) { int n = 0; for (int m = 2 * i;; m = (m + 2 * NS - 1) % (2 * NS)) { n += lo(m / 2, m & 1); if (m == 2 * i - 5 && (i == D2 + 1 || i == D3 + 1)) return n - 1;
        if (xr(m / 2, m & 1) == (i / 2 + 1) % NK) return n; n += xr(m / 2, m & 1) >= 0; } }
    static constexpr int last_st = EPI == 2 ? G.st[1] : M ? 2 + 3 * STD : EPI == 0 ? 3 * ST2 : 6, PUB = EPI == 2 ? (last_st + PUBG) | 1 : 6 + PUBD, UPD = D3 + 18;
    static_assert(UPD + 2 < NS - 3 && PUB < UPD && PUB != D3 + 3 && st_of(D3 + 3) < 0 && piece_of(D3 + 3) < 0 && !fl_of(D3 + 3), "publication");
    static constexpr bool pub_of(int i) { return i == PUB; }
    static constexpr int nops(int i) { return (st_of(i) >= 0) + (piece_of(i) >= 0) + fl_of(i) + pub_of(i); }
    static constexpr int between(int ia, int ib) { int n = 0; for (int i = (ia + 1) % NS; i != ib; i = (i + 1) % NS) n += nops(i); return n; }
    static constexpr int cnt(int t) {
        int ib = 0, ip = 0, jf = 0;
        for (int i = 0; i < NS; ++i) {
            if (bnd(i) == t) ib = i;
            if (piece_of(i) >= 0 && ((tsl(i / 2) + 2) & 3) == t) ip = i;
            if (fl_of(i) && tsl(i / 2) + FD == t) jf = i;
            if (t == 0 && i == D3 + 2) ip = i;
        }
        if (t == 1) ip = NS - 1;
        int n = between(ip, ib);
        if (t >= 2 && between(jf, ib) < n) n = between(jf, ib);
        return n;
    }
};
namespace en { constexpr int L_FL = 0, L_W = 2048, L_RING = L_W + 4 * 11 * 2048, END = L_RING + 65536; }
static_assert(en::END <= LDSB, "LDS");
struct EP {
    const u16* W0; unsigned wbytes, ldw, w_off;
    unsigned in0, in1, in_st;
    unsigned out0, out1, out_st, out_mask;
    unsigned fset, fmask, fown0, fown1, fown_w;
    int prio, prof;
};

#define GI(k, s) ".if (%[ka] <= " #k "+gb_) && (" #k "+gb_ < %[kb])\n\t" s "\n\t.endif\n\t"
#define GL(g, u) GI(0, "v_lshlrev_b32 %0, 16, " g) GI(1, "v_and_b32 %1, 0xffff0000, " g) GI(2, "v_mul_f32 %0, %[a], %0") GI(3, "v_mul_f32 %1, %[a], %1") \
        GI(4, "v_fma_f32 %2, %0, %0, %[b]") GI(5, "v_fma_f32 %3, %1, %1, %[b]") GI(6, "v_mul_f32 %2, %2, %0") GI(7, "v_mul_f32 %3, %3, %1") GI(8, "v_exp_f32 %2, %2") GI(9, "v_exp_f32 %3, %3") \
        GI(10, "v_lshlrev_b32 " g ", 16, " u) GI(11, "v_fma_f32 %2, %2, %[a], %[a]") GI(12, "v_fma_f32 %3, %3, %[a], %[a]") GI(13, "v_and_b32 " u ", 0xffff0000, " u) GI(14, "v_rcp_f32 %2, %2") GI(15, "v_rcp_f32 %3, %3") \
        GI(16, "v_mul_f32 %0, %0, " g) GI(17, "v_mul_f32 %1, %1, " u) GI(18, "v_mul_f32 %0, %0, %2") GI(19, "v_mul_f32 %1, %1, %3") GI(20, "v_cvt_pk_bf16_f32 " g ", %0, %1")
template <int KA, int KB> DEVI void geglu(float& x0, float& x1, float& e0, float& e1, unsigned& g, unsigned& u, unsigned g2) {
    static_assert(KB <= 31, "next pair");
    asm volatile(".set gb_, 0\n\t" GL("%[g]", "%[u]") ".set gb_, 21\n\t" GL("%[h]", "%[h]")
        : "+v"(x0), "+v"(x1), "+v"(e0), "+v"(e1), [g] "+v"(g), [u] "+v"(u) : [a] "s"(-0.46866869305f), [b] "s"(4.91222953926f), [ka] "n"(KA), [kb] "n"(KB), [h] "v"(g2));
}
#undef GL
#undef GI
template <int EPI, int NK, bool M_ = false>
DEVI void engine(const Args& a, const int wv, const EP& e, lds_char* smem) {
    using namespace en;
    typedef Sch<EPI, NK, M_> Z;
    constexpr int NS = 2 * NK;
    constexpr bool SW = EPI == 3, M = Z::M;
    const int lane = lane_id(), tz = lane + wv * 64, w = wv;
    const rsrc_t rs = mkrs(a.ws, WS_RS);
    bf16x8 wt[NRG][2];
    unsigned xb = lane * 16 + L_RING, wb = lane * 16 + L_W + w * 11 * 2048, l4i = (unsigned)(size_t)smem + L_FL + lane * 4, l4g = ((lane & 31) >> 2) * 128 + (lane & 3) * 4;
    asm volatile("" : "+v"(xb), "+v"(wb), "+v"(l4i), "+v"(l4g));
    *(l1*)(smem + L_FL + tz * 4) = SENT;
    {
        constexpr bool G = EPI == 2;
        constexpr int DP = G ? 7 : 8;
        const rsrc_t r0 = mkrs(e.W0, e.wbytes);
        const unsigned vo = (lane >> 3) * e.ldw * 2 + (lane & 7) * 16, so = e.w_off + (G ? w >> 1 : w) * 128, sk = e.ldw * 16;
        const int q = (lane >> 2) & 3, cid = ((lane >> 4) & 1) * 4 + (lane & 3), qa = cid >> 1;
        lds_char* sb = smem + L_RING + w * 16384;
        const unsigned ta = (unsigned)(size_t)smem + L_RING + (G ? (w & 2) + (qa >> 1) : w) * 16384 + (8 * (lane >> 5) + q) * 128 + (8 * (cid & 1) + 4 * (qa & 1)) * 2 + (G ? (w & 1) * 64 : (qa >> 1) * 32);
        auto issue = [&](int k) AI {
            __builtin_amdgcn_raw_ptr_buffer_load_lds(r0, sb + (k & 7) * 2048, 16, vo, so + 2 * k * sk, 0, 0);
            __builtin_amdgcn_raw_ptr_buffer_load_lds(r0, sb + (k & 7) * 2048 + 1024, 16, vo, so + (2 * k + 1) * sk, 0, 0);
        };
#pragma unroll
        for (int k = 0; k < DP; ++k) issue(k);
        const unsigned th = (unsigned)(size_t)smem + L_RING + w * 16384 + (8 * (lane >> 4) + q) * 128 + (lane & 3) * 16;
        if constexpr (M) sfor<0, NK / 2>([&](auto K) AI {
            constexpr int k = 2 * decltype(K)::v + 1, o = ((k - 1) & 7) * 2048;
            WV(2 * (NK - 1 - k < DP - 2 ? NK - 1 - k : DP - 2));
            v2u lo[4], hi[4];
            sfor<0, 4>([&](auto TT) AI { constexpr int T = decltype(TT)::v, oo = o + (T >> 1) * 64 + (T & 1) * 8; tra<oo, oo + 512>(lo[T], hi[T], th); });
            trw(lo[0], hi[0], lo[1], hi[1]); trw(lo[2], hi[2], lo[3], hi[3]);
            if constexpr (k + DP < NK) { issue(k - 1 + DP); issue(k + DP); }
            sfor<0, 4>([&](auto TT) AI {
                constexpr int T = decltype(TT)::v, kk = k - 1 + (T >> 1), c = T & 1;
                const v4u f = {lo[T][0], lo[T][1], hi[T][0], hi[T][1]};
                if constexpr (!Z::lds_step(kk)) { constexpr int ri = Z::reg_idx(kk); wt[ri][c] = as8(f); if (ri < 32) asm volatile("" : "+a"(wt[ri][c])); else asm volatile("" : "+v"(wt[ri][c])); }
                else *(l4*)(smem + wb + (Z::lds_idx(kk) * 2 + c) * 1024) = f;
            });
        });
        else sfor<0, NK>([&](auto K) AI {
            constexpr int k = decltype(K)::v;
            WV(2 * (NK - 1 - k < DP - 1 ? NK - 1 - k : DP - 1));
            if constexpr (G) { BAR; if constexpr (k + DP < NK) issue(k + DP); }
            constexpr int o0 = (k & 7) * 2048, o1 = o0 + (G ? 32 : 64);
            v2u lo[2], hi[2];
            tra<o0, o0 + 512>(lo[0], hi[0], ta); tra<o1, o1 + 512>(lo[1], hi[1], ta);
            trw(lo[0], hi[0], lo[1], hi[1]);
            if constexpr (!G && k + DP < NK) issue(k + DP);
#pragma unroll
            for (int c = 0; c < 2; ++c) {
                const v4u f = {lo[c][0], lo[c][1], hi[c][0], hi[c][1]};
                if constexpr (!Z::lds_step(k)) { constexpr int ri = Z::reg_idx(k); wt[ri][c] = as8(f); if (ri < 32) asm volatile("" : "+a"(wt[ri][c])); else asm volatile("" : "+v"(wt[ri][c])); }
                else *(l4*)(smem + wb + (Z::lds_idx(k) * 2 + c) * 1024) = f;
            }
        });
    }
    auto piece = [&](int slot, int i, unsigned abase) AI {
        const int k = Z::S(slot) + i * 4;
        __builtin_amdgcn_raw_ptr_buffer_load_lds(rs, smem + L_RING + (k + w) * 1024, 16, xb, abase + ((k >> 3) * 16384 + (k & 7) * 1024), 0, AUX_SC1);
    };
    const unsigned wof = w * 1024 - L_RING;
    const unsigned fh = (lane >> 5) * 1024 + ((lane >> 4) & 1) * 512 + (lane & 15) * 16;
    unsigned xh = L_RING + fh;
    if (M) asm volatile("" : "+v"(xh));
    auto rdx = [&](int k) AI -> bf16x8 { return M ? *(l8*)(smem + xh + (k >> 1) * 2048 + (k & 1) * 256) : *(l8*)(smem + xb + k * 1024); };
    auto rdw = [&](int k, int c) AI -> bf16x8 { return *(l8*)(smem + wb + (Z::lds_idx(k) * 2 + c) * 1024); };
    const unsigned pso = w == 0 ? e.fset : OOB, fls = w == 0 ? 256 : 0, flb = w == 0 ? L_FL : L_FL + 1024;
    unsigned pk[16];
#pragma unroll
    for (int i = 0; i < 16; ++i) pk[i] = 0;
    unsigned fl_so = OOB, fl_val = 0;
    unsigned fo0 = e.fown0 + w * e.fown_w, fo1 = e.fown1 + w * e.fown_w;
    asm volatile("" : "+s"(fo0), "+s"(fo1));
    unsigned ovo = EPI == 2 ? w * 2048 + lane * 16 : (w >> 1) * 16384 + (w & 1) * 4096 + (EPI == 3 ? ((lane >> 4) & 1) * 1024 + (lane >> 5) * 256 + (lane & 15) * 16 : M ? fh : lane * 16);
    asm volatile("" : "+v"(ovo));
    unsigned ob = OOB2;
    float gx[2], ge[2];
    f32x16 acc[2];
    f32x4 ah[8];
    bf16x8 xf[M ? NK : XD + 1], wl[2];
    auto cvt = [&](int c, int q0, int q1) AI {
#pragma unroll
        for (int q = q0; q < q1; ++q) { pk[8 * c + q] = pack2(acc[c][2 * q], acc[c][2 * q + 1]); asm volatile("" : "+v"(pk[8 * c + q])); }
    };
    auto sto = [&](int s) AI { const int b = EPI == 2 ? 8 * s : 4 * s; st16(v4u{pk[b], pk[b + 1], pk[b + 2], pk[b + 3]}, rs, ob + (EPI == 3 ? (s >> 1) * 2048 + (s & 1) * 512 : M ? (s >> 1) * 2048 + (s & 1) * 256 : s * 1024)); };
    auto cvh = [&](int p) AI {
        const int q = (p & 3) * 2 + (p >> 2), b = (q >> 2) * 8 + (q & 1) * 4 + (q & 2);
        asm volatile("v_cvt_pk_bf16_f32 %0, %2, %3\n\tv_cvt_pk_bf16_f32 %1, %4, %5" : "=&v"(pk[b]), "=&v"(pk[b + 1]) : "v"(ah[q][0]), "v"(ah[q][1]), "v"(ah[q][2]), "v"(ah[q][3]));
    };
    auto epi = [&](auto P, auto KA, auto KB) AI {
        constexpr int p = decltype(P)::v, g = (p / 4) * 8 + p % 4;
        geglu<decltype(KA)::v, decltype(KB)::v>(gx[0], gx[1], ge[0], ge[1], pk[g], pk[g + 4], decltype(KB)::v > 21 ? pk[((p + 1) / 4 * 8 + (p + 1) % 4) & 15] : xb);
    };
    int c0 = 0, c1 = NSEG;
    int nxt = -1, cur = 0;
    unsigned an = OOB, a3 = OOB, a0 = 0, a1 = 0;
#pragma unroll
    for (int i = 0; i < 16; ++i) { acc[0][i] = 0; acc[1][i] = 0; if (M) ah[i & 7] = f32x4{0, 0, 0, 0}; }
    auto take = [&](int l) AI { nxt = l ? 256 | c1 : c0; an = sd(l ? e.in1 + c1 * e.in_st : e.in0 + c0 * e.in_st) + wof; c0 += 1 - l; c1 += l; };
    auto start = [&]() AI -> bool {
        if (w == 0) {
            unsigned spins = 0; int d;
            int f;
            for (;;) {
                CB;
                f = (int)ld4(rs, l4g, e.fset);
                const bool ok0 = ((unsigned)__ballot(f < c0) & e.fmask) == 0u, ok1 = ((unsigned)(__ballot(f < c1) >> 32) & e.fmask) == 0u;
                d = ok0 ? 0 : 1;
                if (ok0 | ok1) break;
                if (spin_bail(spins, rs)) { d = 2; break; }
                __builtin_amdgcn_s_sleep(DSLP);
            }
            *(l1*)(size_t)l4i = f;
            *(l1*)(smem + L_FL + 1280) = d;
            WL0;
        }
        BAR;
        const int d = RFL(*(l1*)(smem + L_FL + 1280));
        if (d == 2) return false;
        take(d);
        a0 = sd(e.in0 + c0 * e.in_st) + wof; a1 = sd(e.in1 + c1 * e.in_st) + wof;
#pragma unroll
        for (int s = 0; s < 2; ++s)
#pragma unroll
            for (int i = 0; i < Z::np(s); ++i) piece(s, i, an);
        WV(Z::N1 ? 0 : Z::np(1));
        BAR;
#pragma unroll
        for (int i = 0; i < XD; ++i) xf[i] = rdx(i);
        return true;
    };
    WL0;
    BAR;
    TSTAMP(e.prof);
    if (!start()) return;
    unsigned obs = 0, fs_n = 0, m0 = 0, m1 = 0, o0 = 0, o1 = 0;
    u64 b0 = 0, b1 = 0;
    int fw = 0;
    unsigned mw = (unsigned)(size_t)smem + L_RING + w * 1024;
    unsigned mf1 = (unsigned)(size_t)smem + flb + fls, mf2 = mf1 + fls;
    static_assert(!M || FD == 1, "flags");
    if (M) asm volatile("" : "+s"(mw), "+s"(mf1), "+s"(mf2));
#define AT(a, b, hb) (!M && i == (a) && h)
#define AM(b, hb) (M && i == (b) && h == (hb))
    auto ctl = [&](auto I, auto H) AI {
        constexpr int i = decltype(I)::v & 255, h = decltype(H)::v;
        if constexpr (EPI == 3 ? h && i % Z::PS == Z::PS - 3 : i == Z::UPD && h) { ob = ovo + (obs + (i / Z::PS) * 131072); asm volatile("" : "+v"(ob)); }
        if constexpr (i == Z::UPD + 2 && h == !M) { fl_so = fs_n; fl_val = cur & 255; asm volatile("" : "+v"(fl_val)); asm volatile("" : "+s"(fl_so)); }
        if constexpr (AT(Z::D3 + 4, Z::D3 + 6, 0)) { a0 = e.in0 + c0 * e.in_st; asm volatile("" : "+s"(a0)); }
        if constexpr (AT(Z::D3 + 6, Z::D3 + 6, 1)) { a1 = e.in1 + c1 * e.in_st; asm volatile("" : "+s"(a1)); }
        if constexpr (AT(Z::D3 + 7, Z::D3 + 10, 0)) { a0 = sd(a0) + wof; asm volatile("" : "+s"(a0)); }
        if constexpr (AT(Z::D3 + 8, Z::D3 + 10, 1)) { a1 = sd(a1) + wof; asm volatile("" : "+s"(a1)); }
        if constexpr (AT(Z::D3 + 10, Z::D3 + 14, 0) && EPI != 3) { obs = ((cur & 255) & e.out_mask) * e.out_st; asm volatile("" : "+s"(obs)); }
        if constexpr (AT(Z::D3 + 12, Z::D3 + 14, 1)) { if (EPI != 3) obs += (cur >> 8) ? e.out1 : e.out0; fs_n = (cur >> 8) ? fo1 : fo0; asm volatile("" : "+s"(obs), "+s"(fs_n)); }
        if constexpr (AT(Z::D3 + 14, Z::D3 + 18, 0) && EPI != 3) { obs = sd(obs); asm volatile("" : "+s"(obs)); }
        if constexpr (!M && h && (i == Z::D2 || i == Z::D3)) { b0 = __ballot(fw < c0); b1 = __ballot(fw < c1); asm volatile("" : "+s"(b0), "+s"(b1)); }
        if constexpr (AT(Z::D2 + 1, Z::D2 + 1, 0) || AT(Z::D3 + 1, Z::D3 + 1, 1)) { m0 = (unsigned)b0 & e.fmask; m1 = (unsigned)(b1 >> 32) & e.fmask; asm volatile("" : "+s"(m0), "+s"(m1)); }
        if constexpr (AT(Z::D2 + 1, Z::D2 + 1, 1)) { nxt = c1 | 256; an = OOB; asm volatile("" : "+s"(nxt), "+s"(an)); }
        if constexpr (AT(Z::D2 + 2, Z::D2 + 2, 0)) asm volatile("s_cmp_eq_u32 %2, 0\n\ts_cselect_b32 %0, %0, -1\n\ts_cselect_b32 %1, %3, %1\n\ts_cselect_b32 %2, 1, 0" : "+s"(nxt), "+s"(an), "+s"(m1) : "s"(a1) : "scc");
        if constexpr (AT(Z::D2 + 4, Z::D2 + 2, 1)) asm volatile("s_cmp_eq_u32 %3, 0\n\ts_cselect_b32 %0, %4, %0\n\ts_cselect_b32 %1, %5, %1\n\ts_cselect_b32 %2, 0, %2" : "+s"(nxt), "+s"(an), "+s"(m1) : "s"(m0), "s"(c0), "s"(a0) : "scc");
        if constexpr (AT(Z::D2 + 6, Z::D2 + 6, 0)) asm volatile("s_cmp_eq_u32 %2, 0\n\ts_addc_u32 %0, %0, 0\n\ts_add_u32 %1, %1, %3" : "+s"(c0), "+s"(c1) : "s"(m0), "s"(m1) : "scc");
        if constexpr (Z::OV && i == Z::D2 + 10 && h == !M) asm volatile("s_cmp_lt_u32 %1, 256\n\ts_cselect_b32 %0, 1, 0" : "=s"(o0) : "s"(nxt) : "scc");
        if constexpr (Z::OV && i == Z::D2 + 12 && h == !M) asm volatile("s_cmp_lt_i32 %1, 0\n\ts_cselect_b32 %0, 0, 1" : "=s"(o1) : "s"(nxt) : "scc");
        if constexpr (Z::OV && (AT(Z::D3 + 2, 0, 0) || AM(Z::D3 + 3, 0))) asm volatile("s_or_b32 %0, %0, %2\n\ts_or_b32 %1, %1, %3\n\ts_min_u32 %1, %0, %1" : "+s"(m0), "+s"(m1) : "s"(o0), "s"(o1) : "scc");
        if constexpr (!Z::OV && AT(Z::D3 + 2, Z::D3 + 2, 0)) asm volatile("s_min_u32 %0, %1, %0\n\ts_cmp_lt_i32 %2, 0\n\ts_cselect_b32 %0, %0, 1" : "+s"(m1) : "s"(m0), "s"(nxt) : "scc");
        if constexpr (AM(18, 0)) { obs = ((cur & 255) & e.out_mask) * e.out_st; asm volatile("" : "+s"(obs)); }
        if constexpr (AM(20, 0)) { m0 = (cur >> 8) ? e.out1 : e.out0; fs_n = (cur >> 8) ? fo1 : fo0; asm volatile("" : "+s"(m0), "+s"(fs_n)); }
        if constexpr (AM(22, 0)) { obs += m0; asm volatile("" : "+s"(obs)); }
        if constexpr (AM(24, 0)) { obs = sd(obs); asm volatile("" : "+s"(obs)); }
        if constexpr (AM(Z::D2, 0)) { nxt = c1 | 256; asm volatile("" : "+s"(nxt)); }
        if constexpr (AM(Z::D2 + 1, 1) || AM(Z::D3 + 1, 1)) { b1 = __ballot(fw < c1); asm volatile("" : "+s"(b1)); }
        if constexpr (AM(Z::D2 + 2, 0)) asm volatile("s_and_b32 %2, %3, %4\n\ts_cselect_b32 %0, -1, %0\n\ts_cselect_b32 %1, %6, %5" : "+s"(nxt), "=&s"(an), "=&s"(m1) : "s"((unsigned)(b1 >> 32)), "s"(e.fmask), "s"(a1), "n"(OOB) : "scc");
        if constexpr (AM(Z::D2 + 2, 1)) { b0 = __ballot(fw < c0); asm volatile("" : "+s"(b0)); }
        if constexpr (AM(Z::D2 + 4, 0)) asm volatile("s_and_b32 %2, %3, %4\n\ts_cselect_b32 %0, %0, %5\n\ts_cselect_b32 %1, %1, %6" : "+s"(nxt), "+s"(an), "=&s"(m0) : "s"((unsigned)b0), "s"(e.fmask), "s"(c0), "s"(a0) : "scc");
        if constexpr (AM(Z::D2 + 6, 0)) asm volatile("s_cmp_eq_u32 %1, 0\n\ts_addc_u32 %0, %0, 0" : "+s"(c0) : "s"(m0) : "scc");
        if constexpr (AM(Z::D2 + 8, 0)) asm volatile("s_cmp_gt_i32 %1, 255\n\ts_addc_u32 %0, %0, 0" : "+s"(c1) : "s"(nxt) : "scc");
        if constexpr (AM(Z::D3 + 2, 0)) { m1 = (unsigned)(b1 >> 32) & e.fmask; asm volatile("" : "+s"(m1)); SB; b0 = __ballot(fw < c0); asm volatile("" : "+s"(b0)); }
        if constexpr (AM(Z::D3 + 2, 1)) { m0 = (unsigned)b0 & e.fmask; asm volatile("" : "+s"(m0)); }
        if constexpr (!Z::OV && AM(Z::D3 + 3, 0)) asm volatile("s_min_u32 %0, %1, %0\n\ts_cmp_lt_i32 %2, 0\n\ts_cselect_b32 %0, %0, 1" : "+s"(m1) : "s"(m0), "s"(nxt) : "scc");
        if constexpr (AM(Z::D3 + 4, 0)) { a0 = e.in0 + c0 * e.in_st; asm volatile("" : "+s"(a0)); }
        if constexpr (AM(Z::D3 + 6, 0)) { a1 = e.in1 + c1 * e.in_st; asm volatile("" : "+s"(a1)); }
        if constexpr (AM(Z::D3 + 8, 0)) { a0 = sd(a0); asm volatile("" : "+s"(a0)); }
        if constexpr (AM(Z::D3 + 10, 0)) { a1 = sd(a1); asm volatile("" : "+s"(a1)); }
        if constexpr (AM(Z::D3 + 12, 0)) { a0 += wof; a1 += wof; asm volatile("" : "+s"(a0), "+s"(a1)); }
    };
#undef AT
#undef AM
    auto slot = [&](auto I) AI {
        constexpr bool nb = decltype(I)::v >= 256;
        constexpr int i = decltype(I)::v & 255, k = Z::kof(i), c = Z::cof(i), s = i / 2, bt = Z::bnd(i);
        constexpr bool first = k % Z::PER == 0;
        constexpr int j = i % Z::PS;
        if constexpr (M) {
            auto mf = [&](auto R) AI {
                constexpr int r = decltype(R)::v, q = (i & 1) * 4 + r * 2 + (s & 1), xi = s, ws = (s & ~1) + (i & 1), wc = r;
                if constexpr (Z::lds_step(ws)) mh<0, (i < 4)>(ah[q], wl[wc], xf[xi]);
                else mh<(Z::reg_idx(ws) < 32), (i < 4)>(ah[q], wt[Z::reg_idx(ws)][wc], xf[xi]);
                SB;
            };
            constexpr int pc = Z::piece_of(i), kp = Z::S((Z::tsl(s) + 2) & 3) + pc * 4;
            unsigned so = 0;
            mf(IC<0>{});
            if constexpr (i == 0) { cur = nxt; a3 = an; }
            if constexpr (bt >= 0) { if (!(AB & 64)) WV(Z::cnt(bt)); if (!(AB & 2)) BAR; }
            if constexpr (pc >= 0) asm volatile("s_add_u32 %0, %1, %2\n\ts_add_u32 m0, %3, %4" : "=s"(so) : "s"(Z::tsl(s) < 2 ? a3 : an), "n"((kp >> 3) * 16384 + (kp & 7) * 1024), "s"(mw), "n"(kp * 1024) : "scc", "m0");
            if constexpr (Z::fl_of(i)) asm volatile("s_mov_b32 m0, %0" : : "s"(Z::tsl(s) == 1 ? mf1 : mf2) : "m0");
            if constexpr (Z::pub_of(i) && !(AB & 128)) { WV(Z::between(Z::last_st, i)); st4(fl_val, rs, 0, fl_so); }
            if constexpr (Z::st_of(i) >= 0 && !(AB & 1)) sto(Z::st_of(i));
            if constexpr (Z::xr(i, 0) >= 0) { xf[Z::xr(i, 0)] = rdx(Z::xr(i, 0)); SB; }
            if constexpr (!(AB & 32) && Z::w_of(i) >= 0 && Z::st_of(i) < 0) wl[Z::w_of(i) & 1] = rdw(Z::w_of(i) >> 1, Z::w_of(i) & 1);
            SB;
            if constexpr (Z::cv_of(i, 0) >= 0 && !nb && !(AB & 8)) cvh(Z::cv_of(i, 0));
            ctl(I, IC<0>{});
            SB;
            mf(IC<1>{});
            if constexpr (i & 1) { __builtin_amdgcn_s_waitcnt(0xC07F | (Z::xw(i) << 8)); SB; }
            if constexpr (pc >= 0 && !(AB & 4)) asm volatile("buffer_load_dwordx4 %0, %1, %2 offen sc1 lds" : : "v"(xb), "s"(rs), "s"(so) : "memory");
            if constexpr (Z::xr(i, 1) >= 0) { xf[Z::xr(i, 1)] = rdx(Z::xr(i, 1)); SB; }
            if constexpr (bt >= 2) fw = (int)*(l1*)(size_t)(l4i + (bt - FD) * 256);
            if constexpr (!(AB & 32) && Z::w_of(i) >= 0 && Z::st_of(i) >= 0) wl[Z::w_of(i) & 1] = rdw(Z::w_of(i) >> 1, Z::w_of(i) & 1);
            if constexpr (Z::fl_of(i)) asm volatile("buffer_load_dword %0, %1, %2 offen sc1 lds" : : "v"(l4g), "s"(rs), "s"(pso) : "memory");
            SB;
            if constexpr (Z::cv_of(i, 1) >= 0 && !nb && !(AB & 8)) cvh(Z::cv_of(i, 1));
        } else {
        if constexpr (Z::lds_step(k)) { if constexpr (first) mf_v0<SW>(acc[c], wl[c], xf[k % (XD + 1)]); else mf_v<SW>(acc[c], wl[c], xf[k % (XD + 1)]); }
        else if constexpr (Z::reg_idx(k) < 32) { if constexpr (first) mf_a0<SW>(acc[c], wt[Z::reg_idx(k)][c], xf[k % (XD + 1)]); else mf_a<SW>(acc[c], wt[Z::reg_idx(k)][c], xf[k % (XD + 1)]); }
        else { if constexpr (first) mf_v0<SW>(acc[c], wt[Z::reg_idx(k)][c], xf[k % (XD + 1)]); else mf_v<SW>(acc[c], wt[Z::reg_idx(k)][c], xf[k % (XD + 1)]); }
        SB;
        if constexpr (i == 0) { cur = nxt; a3 = an; if (EPI == 3) obs = sd(e.out0 + (cur & e.out_mask) * e.out_st); }
        if constexpr (bt >= 0) { WV(Z::cnt(bt)); BAR; }
        constexpr bool cve = j == Z::PS - 2 || (j == 0 && !nb);
        if constexpr (cve && bt < 0) { if constexpr (j == 0) __builtin_amdgcn_s_waitcnt(0xC07F | (1 << 8)); else asm volatile("s_nop 0"); SB; }
        if constexpr (j == Z::PS - 1) { __builtin_amdgcn_s_waitcnt(0xC07F | ((2 + (Z::w_of(i - 2) >= 0) + (Z::w_of(i - 4) >= 0)) << 8)); SB; }
        if constexpr (Z::st_of(i) >= 0) { sto(Z::st_of(i)); SB; }
        if constexpr (!(i & 1)) xf[(s + XD) % (XD + 1)] = rdx((s + XD) % NK);
        if constexpr (bt >= 2) fw = (int)*(l1*)(size_t)(l4i + (bt - FD) * 256);
        if constexpr (Z::w_of(i) >= 0) wl[Z::w_of(i) & 1] = rdw(Z::w_of(i) >> 1, Z::w_of(i) & 1);
        if constexpr (Z::fl_of(i)) __builtin_amdgcn_raw_ptr_buffer_load_lds(rs, smem + flb + Z::tsl(s) * fls, 4, l4g, pso, 0, AUX_SC1);
        if constexpr (Z::piece_of(i) >= 0) piece((Z::tsl(s) + 2) & 3, Z::piece_of(i), Z::tsl(s) < 2 ? a3 : an);
        if constexpr (Z::pub_of(i)) { WV(Z::between(Z::last_st, i) + Z::nops(i) - 1); st4(fl_val, rs, 0, fl_so); }
        SB;
        if constexpr (EPI == 2 && i < NS - 1) {
            constexpr int ka = Z::G.a[i], kb = Z::G.a[i + 1];
            if constexpr (i == 1) asm volatile("" : "=v"(gx[0]), "=v"(gx[1]), "=v"(ge[0]), "=v"(ge[1]));
            if constexpr (kb > ka && !nb) { epi(IC<ka / 21>{}, IC<ka % 21>{}, IC<kb - ka / 21 * 21>{}); SB; }
        }
        if constexpr (cve) { asm volatile("s_nop %0" ::"n"(Z::st_of(i) >= 0 ? 0 : 1)); SB; cvt(j == 0, 0, 2); }
        if constexpr (j == 1 && !nb) cvt(1, 2, 8);
        if constexpr (j == Z::PS - 1) cvt(0, 2, 8);
        }
        ctl(I, IC<1>{});
        if constexpr (i == Z::D3 + 3) {
            if (__builtin_expect(m1 == 0u, 0)) {
                if (Z::OV) c1 -= o1;
                take(m0 != 0u);
#pragma unroll
                for (int j = 0; j < Z::NP0; ++j) piece(0, j, an);
            }
        }
        SB;
    };
    for (;;) {
        sfor<256, 256 + Z::ROT>(slot);
        do {
            sfor<Z::ROT, NS>(slot);
            sfor<0, Z::ROT>(slot);
        } while (__builtin_expect(nxt >= 0, 1));
        if constexpr (Z::ROT == 0) {
            asm volatile("s_nop 7\n\ts_nop 7" : "+v"(acc[1]));
            cvt(1, 0, 8);
        }
        if constexpr (EPI == 2) {
            constexpr int ka = Z::G.a[Z::ROT];
            static_assert(Z::G.st[0] >= Z::ROT, "store");
            sfor<ka / 21, 8>([&](auto P) AI { epi(P, IC<(decltype(P)::v == ka / 21 ? ka % 21 : 0)>{}, IC<21>{}); if constexpr (decltype(P)::v == 3) sto(0); });
            sto(1);
        } else sfor<Z::ROT, Z::last_st + 1>([&](auto I) AI { if constexpr (Z::st_of(decltype(I)::v) >= 0) sto(Z::st_of(decltype(I)::v)); });
        ob = OOB2;
        WV0;
        st4(fl_val, rs, 0, fl_so);
        fl_so = OOB;
        if (c0 + c1 >= 2 * NSEG) break;
        WL0;
        if (!start()) break;
    }
}

DEVI float red_rows(float s) {
    s += __uint_as_float(__builtin_amdgcn_update_dpp(0, __float_as_uint(s), 0x128, 0xf, 0xf, false));
    s = red_x16(s, false); return red_x32(s, false);
}
DEVI f32x2 up2(unsigned u) { return f32x2{bflo(u), bfhi(u)}; }
DEVI float ln_inv(float var) { return __builtin_amdgcn_rcpf(rbf(__builtin_amdgcn_sqrtf(rbf(rbf(fmaxf(var, 0.f)) + 1e-5f)))); }
DEVI void ln0(const Args& a, const int w, int b) {
    const int lane = lane_id(), row = w * 8 + (lane & 7);
    const rsrc_t rs = mkrs(a.ws, WS_RS), rx = mkrs(a.x, 2048 * 2048);
    v4u u[16];
#pragma unroll
    for (int i = 0; i < 16; ++i) u[i] = __builtin_amdgcn_raw_buffer_load_b128(rx, row * 2048 + (lane >> 3) * 16 + i * 128, b * 65536, 0);
    const float k0 = __shfl(bflo(u[0][0]), lane & 7);
    f32x2 s1 = {0.f, 0.f}, s2 = {0.f, 0.f};
#pragma unroll
    for (int i = 0; i < 16; ++i)
#pragma unroll
        for (int e = 0; e < 4; ++e) { const f32x2 d = up2(u[i][e]) - k0; s1 += d; s2 += d * d; }
    const float s = red_rows(s1[0] + s1[1]) * (1.0f / 1024.0f), q = red_rows(s2[0] + s2[1]) * (1.0f / 1024.0f);
    const float inv = ln_inv(q - s * s), c = -rbf(k0 + s) * inv;
    const unsigned o = sd(O_XN + b * 131072) + (lane >> 3) * 512 + row * 16;
#pragma unroll
    for (int i = 0; i < 16; ++i) {
        v4u r;
#pragma unroll
        for (int e = 0; e < 4; ++e) { const f32x2 z = up2(u[i][e]) * inv + c; r[e] = pack2(z[0], z[1]); }
        st16(r, rs, o + (i >> 1) * 16384 + (i & 1) * 4096);
    }
#pragma unroll
    for (int i = 0; i < 16; ++i) st16(u[i], rs, o + (O_XR - O_XN) + (i >> 1) * 16384 + (i & 1) * 4096);
    st16(u[0], rs, sd(O_ST + b * 131072) + w * 1024 + lane * 16); st16(u[0], rs, sd(O_ST + SD + b * 131072) + w * 1024 + lane * 16);
    WV0;
    st4(b, rs, 0, F_X0 + b * 16 + w * 4);
}
#define RSLP 1
template <int L> DEVI void r_role(const Args& a, const int w, lds_char* smem, int rq) {
    const int lane = lane_id(), row = rq * 8 + (lane & 7), g0 = w * 32 + (lane >> 3);
    const rsrc_t rs = mkrs(a.ws, WS_RS), ro = mkrs(a.out, 2048 * 2048);
    const unsigned fo = w * 32768 + (lane >> 3) * 512 + row * 16, xo = row * 2048 + g0 * 16;
    auto io = [](int i) -> unsigned { return (i >> 1) * 16384 + (i & 1) * 4096; };
    lf1* red = (lf1*)smem;
    const unsigned fd = F_D + L * SD + ((lane & 12) + w) * 128 + (lane & 3) * 4;
    for (int b = 0; b < NSEG; ++b) {
        unsigned spins = 0;
        for (;;) {
            CB;
            if (__ballot((int)ld4(rs, fd, 0) < b) == 0ull) break;
            if (spin_bail(spins, rs)) return;
            __builtin_amdgcn_s_sleep(RSLP);
        }
        const unsigned sxr = sd(O_XR + L * SD + b * 131072), ssl = sd(O_SL + L * SD + b * 524288);
        v4u un[4], t[4][4];
#pragma unroll
        for (int i = 0; i < 4; ++i) {
#pragma unroll
            for (int k = 0; k < 4; ++k) t[i][k] = ld16(rs, fo + io(i), ssl + k * 131072);
            un[i] = ld16(rs, fo + io(i), sxr);
        }
        f32x2 d[4][4], s1 = {0.f, 0.f}, s2 = {0.f, 0.f};
        v4u u[4];
        float k0 = 0.f;
        auto dev = [&](int i) AI {
#pragma unroll
            for (int e = 0; e < 4; ++e) { d[i][e] -= k0; s1 += d[i][e]; s2 += d[i][e] * d[i][e]; }
        };
#pragma unroll
        for (int i = 0; i < 4; ++i) {
#pragma unroll
            for (int e = 0; e < 4; ++e) {
                d[i][e] = up2(un[i][e]) + ((up2(t[i][0][e]) + up2(t[i][1][e])) + (up2(t[i][2][e]) + up2(t[i][3][e])));
                u[i][e] = pack2(d[i][e][0], d[i][e][1]);
            }
            if (L) __builtin_amdgcn_raw_buffer_store_b128(u[i], ro, xo + i * 128 + b * 65536, 0, 0);
            else { if (i == 0) k0 = __shfl(d[0][0][0], lane & 7); else { if (i == 1) dev(0); dev(i); } }
            SB;
        }
        if constexpr (!L) {
            lf1* rp = red + (b & 1) * 64 + (lane & 7) * 8;
            const float s = red_rows(s1[0] + s1[1]), q = red_rows(s2[0] + s2[1]);
            if (lane < 16) rp[(lane >> 3) * 4 + w] = lane < 8 ? k0 + s * (1.0f / 256.0f) : q - s * s * (1.0f / 256.0f);
            const unsigned oxn = sd(O_XN + SD + b * 131072), oxr = sd(O_XR + SD + b * 131072);
#pragma unroll
            for (int i = 0; i < 4; ++i) st16(u[i], rs, fo + io(i) + oxr);
            WL0; BAR;
            const f32x4 sa = *(lf4*)rp, qa = *(lf4*)(rp + 4);
            const float mean = ((sa[0] + sa[1]) + (sa[2] + sa[3])) * 0.25f;
            const f32x4 da = sa - mean;
            const float var = ((qa[0] + qa[1]) + (qa[2] + qa[3])) * (1.0f / 1024.0f) + ((da[0] * da[0] + da[1] * da[1]) + (da[2] * da[2] + da[3] * da[3])) * 0.25f;
            const float inv = ln_inv(var), c = (k0 - rbf(mean)) * inv;
#pragma unroll
            for (int i = 0; i < 4; ++i) {
                v4u o;
#pragma unroll
                for (int e = 0; e < 4; ++e) { const f32x2 z = d[i][e] * inv + c; o[e] = pack2(z[0], z[1]); }
                st16(o, rs, fo + io(i) + oxn);
                SB;
            }
            WV0;
            st4(b, rs, 0, F_X + SD + rq * 128 + w * 4);
        }
        if (rq == 0 && w == 0) TSTAMP(200000 + (L * 2 + 1) * 64 + b);
    }
}
#define QK_ASM \
"v_mbcnt_lo_u32_b32 v255, -1, 0\nv_mbcnt_hi_u32_b32 v255, -1, v255\nv_lshlrev_b32 v255, 2, v255\nv_add_u32 v255, %[tb], v255\nds_read_b32 v0, v255 offset:0\nds_read_b32 v1, v255 offset:256\nds_read_b32 v2, v255 offset:512\nds_read_b32 v3, v255 offset:768\n" \
"ds_read_b32 v4, v255 offset:1024\nds_read_b32 v5, v255 offset:1280\nds_read_b32 v6, v255 offset:1536\nds_read_b32 v7, v255 offset:1792\nds_read_b32 v8, v255 offset:2048\nds_read_b32 v9, v255 offset:2304\nds_read_b32 v10, v255 offset:2560\nds_read_b32 v11, v255 offset:2816\n" \
"ds_read_b32 v12, v255 offset:3072\nds_read_b32 v13, v255 offset:3328\nds_read_b32 v14, v255 offset:3584\nds_read_b32 v15, v255 offset:3840\nds_read_b32 v92, v255 offset:4096\nds_read_b32 v93, v255 offset:4352\nds_read_b32 v94, v255 offset:4608\ns_waitcnt lgkmcnt(0)\n" \
"s_add_u32 m0, %[lw], 16384\ns_add_u32 s44, %[wo], 0\nbuffer_load_dwordx4 v92, %[rw], s44 offen lds\ns_add_u32 m0, %[lw], 17408\ns_add_u32 s44, %[wo], 65536\nbuffer_load_dwordx4 v92, %[rw], s44 offen lds\ns_add_u32 m0, %[lw], 18432\ns_add_u32 s44, %[wo], 131072\n" \
"buffer_load_dwordx4 v92, %[rw], s44 offen lds\ns_add_u32 m0, %[lw], 19456\ns_add_u32 s44, %[wo], 196608\nbuffer_load_dwordx4 v92, %[rw], s44 offen lds\ns_add_u32 m0, %[lw], 20480\ns_add_u32 s44, %[wo], 262144\nbuffer_load_dwordx4 v92, %[rw], s44 offen lds\ns_add_u32 m0, %[lw], 21504\n" \
"s_add_u32 s44, %[wo], 327680\nbuffer_load_dwordx4 v92, %[rw], s44 offen lds\ns_add_u32 m0, %[lw], 22528\ns_add_u32 s44, %[wo], 393216\nbuffer_load_dwordx4 v92, %[rw], s44 offen lds\ns_add_u32 m0, %[lw], 23552\ns_add_u32 s44, %[wo], 458752\nbuffer_load_dwordx4 v92, %[rw], s44 offen lds\n" \
"s_add_u32 m0, %[tb], 0\ns_add_u32 s44, %[wo], 524288\nbuffer_load_dwordx4 v92, %[rw], s44 offen lds\ns_add_u32 m0, %[tb], 1024\ns_add_u32 s44, %[wo], 589824\nbuffer_load_dwordx4 v92, %[rw], s44 offen lds\ns_add_u32 m0, %[tb], 2048\ns_add_u32 s44, %[wo], 655360\n" \
"buffer_load_dwordx4 v92, %[rw], s44 offen lds\ns_add_u32 m0, %[tb], 3072\ns_add_u32 s44, %[wo], 720896\nbuffer_load_dwordx4 v92, %[rw], s44 offen lds\ns_add_u32 m0, %[tb], 4096\ns_add_u32 s44, %[wo], 786432\nbuffer_load_dwordx4 v92, %[rw], s44 offen lds\ns_add_u32 m0, %[tb], 5120\n" \
"s_add_u32 s44, %[wo], 851968\nbuffer_load_dwordx4 v92, %[rw], s44 offen lds\ns_add_u32 m0, %[tb], 6144\ns_add_u32 s44, %[wo], 917504\nbuffer_load_dwordx4 v92, %[rw], s44 offen lds\ns_add_u32 m0, %[tb], 7168\ns_add_u32 s44, %[wo], 983040\nbuffer_load_dwordx4 v92, %[rw], s44 offen lds\n" \
"s_add_u32 m0, %[tb], 8192\ns_add_u32 s44, %[wo], 1048576\nbuffer_load_dwordx4 v92, %[rw], s44 offen lds\ns_add_u32 m0, %[tb], 9216\ns_add_u32 s44, %[wo], 1114112\nbuffer_load_dwordx4 v92, %[rw], s44 offen lds\ns_add_u32 m0, %[tb], 10240\ns_add_u32 s44, %[wo], 1179648\n" \
"buffer_load_dwordx4 v92, %[rw], s44 offen lds\ns_add_u32 m0, %[tb], 11264\ns_add_u32 s44, %[wo], 1245184\nbuffer_load_dwordx4 v92, %[rw], s44 offen lds\ns_add_u32 m0, %[lw], 0\ns_add_u32 s44, %[wo], 1310720\nbuffer_load_dwordx4 v92, %[rw], s44 offen lds\ns_add_u32 m0, %[lw], 1024\n" \
"s_add_u32 s44, %[wo], 1376256\nbuffer_load_dwordx4 v92, %[rw], s44 offen lds\ns_add_u32 m0, %[lw], 2048\ns_add_u32 s44, %[wo], 1441792\nbuffer_load_dwordx4 v92, %[rw], s44 offen lds\ns_add_u32 m0, %[lw], 3072\ns_add_u32 s44, %[wo], 1507328\nbuffer_load_dwordx4 v92, %[rw], s44 offen lds\n" \
"s_add_u32 m0, %[lw], 4096\ns_add_u32 s44, %[wo], 1572864\nbuffer_load_dwordx4 v92, %[rw], s44 offen lds\ns_add_u32 m0, %[lw], 5120\ns_add_u32 s44, %[wo], 1638400\nbuffer_load_dwordx4 v92, %[rw], s44 offen lds\ns_add_u32 m0, %[lw], 6144\ns_add_u32 s44, %[wo], 1703936\n" \
"buffer_load_dwordx4 v92, %[rw], s44 offen lds\ns_add_u32 m0, %[lw], 7168\ns_add_u32 s44, %[wo], 1769472\nbuffer_load_dwordx4 v92, %[rw], s44 offen lds\ns_add_u32 m0, %[lw], 8192\ns_add_u32 s44, %[wo], 1835008\nbuffer_load_dwordx4 v92, %[rw], s44 offen lds\ns_add_u32 m0, %[lw], 9216\n" \
"s_add_u32 s44, %[wo], 1900544\nbuffer_load_dwordx4 v92, %[rw], s44 offen lds\ns_add_u32 m0, %[lw], 10240\ns_add_u32 s44, %[wo], 1966080\nbuffer_load_dwordx4 v92, %[rw], s44 offen lds\ns_add_u32 m0, %[lw], 11264\ns_add_u32 s44, %[wo], 2031616\nbuffer_load_dwordx4 v92, %[rw], s44 offen lds\n" \
"s_add_u32 m0, %[lw], 12288\ns_add_u32 s44, %[wo], 2097152\nbuffer_load_dwordx4 v92, %[rw], s44 offen lds\ns_add_u32 m0, %[lw], 13312\ns_add_u32 s44, %[wo], 2162688\nbuffer_load_dwordx4 v92, %[rw], s44 offen lds\ns_add_u32 m0, %[lw], 14336\ns_add_u32 s44, %[wo], 2228224\n" \
"buffer_load_dwordx4 v92, %[rw], s44 offen lds\ns_add_u32 m0, %[lw], 15360\ns_add_u32 s44, %[wo], 2293760\nbuffer_load_dwordx4 v92, %[rw], s44 offen lds\ns_waitcnt vmcnt(32)\nds_read_b64_tr_b16 a[0:1], v94 offset:16384\nds_read_b64_tr_b16 a[2:3], v94 offset:16896\nds_read_b64_tr_b16 a[8:9], v94 offset:16392\n" \
"ds_read_b64_tr_b16 a[10:11], v94 offset:16904\nds_read_b64_tr_b16 a[16:17], v94 offset:16448\nds_read_b64_tr_b16 a[18:19], v94 offset:16960\nds_read_b64_tr_b16 a[24:25], v94 offset:16456\nds_read_b64_tr_b16 a[26:27], v94 offset:16968\ns_waitcnt lgkmcnt(0)\ns_add_u32 m0, %[lw], 16384\ns_add_u32 s44, %[wo], 2359296\n" \
"buffer_load_dwordx4 v92, %[rw], s44 offen lds\ns_add_u32 m0, %[lw], 17408\ns_add_u32 s44, %[wo], 2424832\nbuffer_load_dwordx4 v92, %[rw], s44 offen lds\ns_add_u32 m0, %[lw], 18432\ns_add_u32 s44, %[wo], 2490368\nbuffer_load_dwordx4 v92, %[rw], s44 offen lds\ns_add_u32 m0, %[lw], 19456\n" \
"s_add_u32 s44, %[wo], 2555904\nbuffer_load_dwordx4 v92, %[rw], s44 offen lds\ns_waitcnt vmcnt(32)\nds_read_b64_tr_b16 a[4:5], v94 offset:20480\nds_read_b64_tr_b16 a[6:7], v94 offset:20992\nds_read_b64_tr_b16 a[12:13], v94 offset:20488\nds_read_b64_tr_b16 a[14:15], v94 offset:21000\nds_read_b64_tr_b16 a[20:21], v94 offset:20544\n" \
"ds_read_b64_tr_b16 a[22:23], v94 offset:21056\nds_read_b64_tr_b16 a[28:29], v94 offset:20552\nds_read_b64_tr_b16 a[30:31], v94 offset:21064\ns_waitcnt lgkmcnt(0)\ns_add_u32 m0, %[lw], 20480\ns_add_u32 s44, %[wo], 2621440\nbuffer_load_dwordx4 v92, %[rw], s44 offen lds\ns_add_u32 m0, %[lw], 21504\n" \
"s_add_u32 s44, %[wo], 2686976\nbuffer_load_dwordx4 v92, %[rw], s44 offen lds\ns_add_u32 m0, %[lw], 22528\ns_add_u32 s44, %[wo], 2752512\nbuffer_load_dwordx4 v92, %[rw], s44 offen lds\ns_add_u32 m0, %[lw], 23552\ns_add_u32 s44, %[wo], 2818048\nbuffer_load_dwordx4 v92, %[rw], s44 offen lds\n" \
"s_waitcnt vmcnt(32)\nds_read_b64_tr_b16 a[32:33], v93 offset:0\nds_read_b64_tr_b16 a[34:35], v93 offset:512\nds_read_b64_tr_b16 v[88:89], v93 offset:8\nds_read_b64_tr_b16 v[90:91], v93 offset:520\nds_read_b64_tr_b16 a[40:41], v93 offset:64\nds_read_b64_tr_b16 a[42:43], v93 offset:576\nds_read_b64_tr_b16 a[48:49], v93 offset:72\n" \
"ds_read_b64_tr_b16 a[50:51], v93 offset:584\ns_waitcnt lgkmcnt(0)\ns_add_u32 m0, %[tb], 0\ns_add_u32 s44, %[wo], 2883584\nbuffer_load_dwordx4 v92, %[rw], s44 offen lds\ns_add_u32 m0, %[tb], 1024\ns_add_u32 s44, %[wo], 2949120\nbuffer_load_dwordx4 v92, %[rw], s44 offen lds\n" \
"s_add_u32 m0, %[tb], 2048\ns_add_u32 s44, %[wo], 3014656\nbuffer_load_dwordx4 v92, %[rw], s44 offen lds\ns_add_u32 m0, %[tb], 3072\ns_add_u32 s44, %[wo], 3080192\nbuffer_load_dwordx4 v92, %[rw], s44 offen lds\ns_waitcnt vmcnt(32)\nds_read_b64_tr_b16 a[36:37], v93 offset:4096\n" \
"ds_read_b64_tr_b16 a[38:39], v93 offset:4608\nds_read_b64_tr_b16 v[84:85], v93 offset:4104\nds_read_b64_tr_b16 v[86:87], v93 offset:4616\nds_read_b64_tr_b16 a[44:45], v93 offset:4160\nds_read_b64_tr_b16 a[46:47], v93 offset:4672\nds_read_b64_tr_b16 a[52:53], v93 offset:4168\nds_read_b64_tr_b16 a[54:55], v93 offset:4680\ns_waitcnt lgkmcnt(0)\n" \
"s_add_u32 m0, %[tb], 4096\ns_add_u32 s44, %[wo], 3145728\nbuffer_load_dwordx4 v92, %[rw], s44 offen lds\ns_add_u32 m0, %[tb], 5120\ns_add_u32 s44, %[wo], 3211264\nbuffer_load_dwordx4 v92, %[rw], s44 offen lds\ns_add_u32 m0, %[tb], 6144\ns_add_u32 s44, %[wo], 3276800\n" \
"buffer_load_dwordx4 v92, %[rw], s44 offen lds\ns_add_u32 m0, %[tb], 7168\ns_add_u32 s44, %[wo], 3342336\nbuffer_load_dwordx4 v92, %[rw], s44 offen lds\ns_waitcnt vmcnt(32)\nds_read_b64_tr_b16 a[56:57], v93 offset:8192\nds_read_b64_tr_b16 a[58:59], v93 offset:8704\nds_read_b64_tr_b16 a[64:65], v93 offset:8200\n" \
"ds_read_b64_tr_b16 a[66:67], v93 offset:8712\nds_read_b64_tr_b16 v[80:81], v93 offset:8256\nds_read_b64_tr_b16 v[82:83], v93 offset:8768\nds_read_b64_tr_b16 a[72:73], v93 offset:8264\nds_read_b64_tr_b16 a[74:75], v93 offset:8776\ns_waitcnt lgkmcnt(0)\ns_add_u32 m0, %[tb], 8192\ns_add_u32 s44, %[wo], 3407872\n" \
"buffer_load_dwordx4 v92, %[rw], s44 offen lds\ns_add_u32 m0, %[tb], 9216\ns_add_u32 s44, %[wo], 3473408\nbuffer_load_dwordx4 v92, %[rw], s44 offen lds\ns_add_u32 m0, %[tb], 10240\ns_add_u32 s44, %[wo], 3538944\nbuffer_load_dwordx4 v92, %[rw], s44 offen lds\ns_add_u32 m0, %[tb], 11264\n" \
"s_add_u32 s44, %[wo], 3604480\nbuffer_load_dwordx4 v92, %[rw], s44 offen lds\ns_waitcnt vmcnt(32)\nds_read_b64_tr_b16 a[60:61], v94 offset:0\nds_read_b64_tr_b16 a[62:63], v94 offset:512\nds_read_b64_tr_b16 a[68:69], v94 offset:8\nds_read_b64_tr_b16 a[70:71], v94 offset:520\nds_read_b64_tr_b16 v[76:77], v94 offset:64\n" \
"ds_read_b64_tr_b16 v[78:79], v94 offset:576\nds_read_b64_tr_b16 a[76:77], v94 offset:72\nds_read_b64_tr_b16 a[78:79], v94 offset:584\ns_waitcnt lgkmcnt(0)\ns_add_u32 m0, %[lw], 0\ns_add_u32 s44, %[wo], 3670016\nbuffer_load_dwordx4 v92, %[rw], s44 offen lds\ns_add_u32 m0, %[lw], 1024\n" \
"s_add_u32 s44, %[wo], 3735552\nbuffer_load_dwordx4 v92, %[rw], s44 offen lds\ns_add_u32 m0, %[lw], 2048\ns_add_u32 s44, %[wo], 3801088\nbuffer_load_dwordx4 v92, %[rw], s44 offen lds\ns_add_u32 m0, %[lw], 3072\ns_add_u32 s44, %[wo], 3866624\nbuffer_load_dwordx4 v92, %[rw], s44 offen lds\n" \
"s_waitcnt vmcnt(32)\nds_read_b64_tr_b16 a[80:81], v94 offset:4096\nds_read_b64_tr_b16 a[82:83], v94 offset:4608\nds_read_b64_tr_b16 a[88:89], v94 offset:4104\nds_read_b64_tr_b16 a[90:91], v94 offset:4616\nds_read_b64_tr_b16 a[96:97], v94 offset:4160\nds_read_b64_tr_b16 a[98:99], v94 offset:4672\nds_read_b64_tr_b16 v[72:73], v94 offset:4168\n" \
"ds_read_b64_tr_b16 v[74:75], v94 offset:4680\ns_waitcnt lgkmcnt(0)\ns_add_u32 m0, %[lw], 4096\ns_add_u32 s44, %[wo], 3932160\nbuffer_load_dwordx4 v92, %[rw], s44 offen lds\ns_add_u32 m0, %[lw], 5120\ns_add_u32 s44, %[wo], 3997696\nbuffer_load_dwordx4 v92, %[rw], s44 offen lds\n" \
"s_add_u32 m0, %[lw], 6144\ns_add_u32 s44, %[wo], 4063232\nbuffer_load_dwordx4 v92, %[rw], s44 offen lds\ns_add_u32 m0, %[lw], 7168\ns_add_u32 s44, %[wo], 4128768\nbuffer_load_dwordx4 v92, %[rw], s44 offen lds\ns_waitcnt vmcnt(32)\nds_read_b64_tr_b16 a[84:85], v94 offset:8192\n" \
"ds_read_b64_tr_b16 a[86:87], v94 offset:8704\nds_read_b64_tr_b16 a[92:93], v94 offset:8200\nds_read_b64_tr_b16 a[94:95], v94 offset:8712\nds_read_b64_tr_b16 a[100:101], v94 offset:8256\nds_read_b64_tr_b16 a[102:103], v94 offset:8768\nds_read_b64_tr_b16 v[68:69], v94 offset:8264\nds_read_b64_tr_b16 v[70:71], v94 offset:8776\ns_waitcnt lgkmcnt(0)\n" \
"s_add_u32 m0, %[lw], 8192\ns_add_u32 s44, %[wo], 4194304\nbuffer_load_dwordx4 v92, %[rw], s44 offen lds\ns_add_u32 m0, %[lw], 9216\ns_add_u32 s44, %[wo], 4259840\nbuffer_load_dwordx4 v92, %[rw], s44 offen lds\ns_add_u32 m0, %[lw], 10240\ns_add_u32 s44, %[wo], 4325376\n" \
"buffer_load_dwordx4 v92, %[rw], s44 offen lds\ns_add_u32 m0, %[lw], 11264\ns_add_u32 s44, %[wo], 4390912\nbuffer_load_dwordx4 v92, %[rw], s44 offen lds\ns_waitcnt vmcnt(32)\nds_read_b64_tr_b16 a[104:105], v94 offset:12288\nds_read_b64_tr_b16 a[106:107], v94 offset:12800\nds_read_b64_tr_b16 a[112:113], v94 offset:12296\n" \
"ds_read_b64_tr_b16 a[114:115], v94 offset:12808\nds_read_b64_tr_b16 a[120:121], v94 offset:12352\nds_read_b64_tr_b16 a[122:123], v94 offset:12864\nds_read_b64_tr_b16 a[128:129], v94 offset:12360\nds_read_b64_tr_b16 a[130:131], v94 offset:12872\ns_waitcnt lgkmcnt(0)\ns_add_u32 m0, %[lw], 12288\ns_add_u32 s44, %[wo], 4456448\n" \
"buffer_load_dwordx4 v92, %[rw], s44 offen lds\ns_add_u32 m0, %[lw], 13312\ns_add_u32 s44, %[wo], 4521984\nbuffer_load_dwordx4 v92, %[rw], s44 offen lds\ns_add_u32 m0, %[lw], 14336\ns_add_u32 s44, %[wo], 4587520\nbuffer_load_dwordx4 v92, %[rw], s44 offen lds\ns_add_u32 m0, %[lw], 15360\n" \
"s_add_u32 s44, %[wo], 4653056\nbuffer_load_dwordx4 v92, %[rw], s44 offen lds\ns_waitcnt vmcnt(32)\nds_read_b64_tr_b16 a[108:109], v94 offset:16384\nds_read_b64_tr_b16 a[110:111], v94 offset:16896\nds_read_b64_tr_b16 a[116:117], v94 offset:16392\nds_read_b64_tr_b16 a[118:119], v94 offset:16904\nds_read_b64_tr_b16 a[124:125], v94 offset:16448\n" \
"ds_read_b64_tr_b16 a[126:127], v94 offset:16960\nds_read_b64_tr_b16 a[132:133], v94 offset:16456\nds_read_b64_tr_b16 a[134:135], v94 offset:16968\ns_waitcnt lgkmcnt(0)\ns_add_u32 m0, %[lw], 16384\ns_add_u32 s44, %[wo], 4718592\nbuffer_load_dwordx4 v92, %[rw], s44 offen lds\ns_add_u32 m0, %[lw], 17408\n" \
"s_add_u32 s44, %[wo], 4784128\nbuffer_load_dwordx4 v92, %[rw], s44 offen lds\ns_add_u32 m0, %[lw], 18432\ns_add_u32 s44, %[wo], 4849664\nbuffer_load_dwordx4 v92, %[rw], s44 offen lds\ns_add_u32 m0, %[lw], 19456\ns_add_u32 s44, %[wo], 4915200\nbuffer_load_dwordx4 v92, %[rw], s44 offen lds\n" \
"s_waitcnt vmcnt(32)\nds_read_b64_tr_b16 a[136:137], v94 offset:20480\nds_read_b64_tr_b16 a[138:139], v94 offset:20992\nds_read_b64_tr_b16 v[64:65], v94 offset:20488\nds_read_b64_tr_b16 v[66:67], v94 offset:21000\nds_read_b64_tr_b16 a[144:145], v94 offset:20544\nds_read_b64_tr_b16 a[146:147], v94 offset:21056\nds_read_b64_tr_b16 a[152:153], v94 offset:20552\n" \
"ds_read_b64_tr_b16 a[154:155], v94 offset:21064\ns_waitcnt lgkmcnt(0)\ns_add_u32 m0, %[lw], 20480\ns_add_u32 s44, %[wo], 4980736\nbuffer_load_dwordx4 v92, %[rw], s44 offen lds\ns_add_u32 m0, %[lw], 21504\ns_add_u32 s44, %[wo], 5046272\nbuffer_load_dwordx4 v92, %[rw], s44 offen lds\n" \
"s_add_u32 m0, %[lw], 22528\ns_add_u32 s44, %[wo], 5111808\nbuffer_load_dwordx4 v92, %[rw], s44 offen lds\ns_add_u32 m0, %[lw], 23552\ns_add_u32 s44, %[wo], 5177344\nbuffer_load_dwordx4 v92, %[rw], s44 offen lds\ns_waitcnt vmcnt(32)\nds_read_b64_tr_b16 a[140:141], v93 offset:0\n" \
"ds_read_b64_tr_b16 a[142:143], v93 offset:512\nds_read_b64_tr_b16 v[60:61], v93 offset:8\nds_read_b64_tr_b16 v[62:63], v93 offset:520\nds_read_b64_tr_b16 a[148:149], v93 offset:64\nds_read_b64_tr_b16 a[150:151], v93 offset:576\nds_read_b64_tr_b16 a[156:157], v93 offset:72\nds_read_b64_tr_b16 a[158:159], v93 offset:584\ns_waitcnt lgkmcnt(0)\n" \
"s_add_u32 m0, %[tb], 0\ns_add_u32 s44, %[wo], 5242880\nbuffer_load_dwordx4 v92, %[rw], s44 offen lds\ns_add_u32 m0, %[tb], 1024\ns_add_u32 s44, %[wo], 5308416\nbuffer_load_dwordx4 v92, %[rw], s44 offen lds\ns_add_u32 m0, %[tb], 2048\ns_add_u32 s44, %[wo], 5373952\n" \
"buffer_load_dwordx4 v92, %[rw], s44 offen lds\ns_add_u32 m0, %[tb], 3072\ns_add_u32 s44, %[wo], 5439488\nbuffer_load_dwordx4 v92, %[rw], s44 offen lds\ns_waitcnt vmcnt(32)\nds_read_b64_tr_b16 a[160:161], v93 offset:4096\nds_read_b64_tr_b16 a[162:163], v93 offset:4608\nds_read_b64_tr_b16 a[168:169], v93 offset:4104\n" \
"ds_read_b64_tr_b16 a[170:171], v93 offset:4616\nds_read_b64_tr_b16 v[56:57], v93 offset:4160\nds_read_b64_tr_b16 v[58:59], v93 offset:4672\nds_read_b64_tr_b16 a[176:177], v93 offset:4168\nds_read_b64_tr_b16 a[178:179], v93 offset:4680\ns_waitcnt lgkmcnt(0)\ns_add_u32 m0, %[tb], 4096\ns_add_u32 s44, %[wo], 5505024\n" \
"buffer_load_dwordx4 v92, %[rw], s44 offen lds\ns_add_u32 m0, %[tb], 5120\ns_add_u32 s44, %[wo], 5570560\nbuffer_load_dwordx4 v92, %[rw], s44 offen lds\ns_add_u32 m0, %[tb], 6144\ns_add_u32 s44, %[wo], 5636096\nbuffer_load_dwordx4 v92, %[rw], s44 offen lds\ns_add_u32 m0, %[tb], 7168\n" \
"s_add_u32 s44, %[wo], 5701632\nbuffer_load_dwordx4 v92, %[rw], s44 offen lds\ns_waitcnt vmcnt(32)\nds_read_b64_tr_b16 a[164:165], v93 offset:8192\nds_read_b64_tr_b16 a[166:167], v93 offset:8704\nds_read_b64_tr_b16 a[172:173], v93 offset:8200\nds_read_b64_tr_b16 a[174:175], v93 offset:8712\nds_read_b64_tr_b16 v[52:53], v93 offset:8256\n" \
"ds_read_b64_tr_b16 v[54:55], v93 offset:8768\nds_read_b64_tr_b16 a[180:181], v93 offset:8264\nds_read_b64_tr_b16 a[182:183], v93 offset:8776\ns_waitcnt lgkmcnt(0)\ns_add_u32 m0, %[tb], 8192\ns_add_u32 s44, %[wo], 5767168\nbuffer_load_dwordx4 v92, %[rw], s44 offen lds\ns_add_u32 m0, %[tb], 9216\n" \
"s_add_u32 s44, %[wo], 5832704\nbuffer_load_dwordx4 v92, %[rw], s44 offen lds\ns_add_u32 m0, %[tb], 10240\ns_add_u32 s44, %[wo], 5898240\nbuffer_load_dwordx4 v92, %[rw], s44 offen lds\ns_add_u32 m0, %[tb], 11264\ns_add_u32 s44, %[wo], 5963776\nbuffer_load_dwordx4 v92, %[rw], s44 offen lds\n" \
"s_waitcnt vmcnt(32)\nds_read_b64_tr_b16 a[184:185], v94 offset:0\nds_read_b64_tr_b16 a[186:187], v94 offset:512\nds_read_b64_tr_b16 a[192:193], v94 offset:8\nds_read_b64_tr_b16 a[194:195], v94 offset:520\nds_read_b64_tr_b16 a[200:201], v94 offset:64\nds_read_b64_tr_b16 a[202:203], v94 offset:576\nds_read_b64_tr_b16 v[48:49], v94 offset:72\n" \
"ds_read_b64_tr_b16 v[50:51], v94 offset:584\ns_waitcnt lgkmcnt(0)\ns_add_u32 m0, %[lw], 0\ns_add_u32 s44, %[wo], 6029312\nbuffer_load_dwordx4 v92, %[rw], s44 offen lds\ns_add_u32 m0, %[lw], 1024\ns_add_u32 s44, %[wo], 6094848\nbuffer_load_dwordx4 v92, %[rw], s44 offen lds\n" \
"s_add_u32 m0, %[lw], 2048\ns_add_u32 s44, %[wo], 6160384\nbuffer_load_dwordx4 v92, %[rw], s44 offen lds\ns_add_u32 m0, %[lw], 3072\ns_add_u32 s44, %[wo], 6225920\nbuffer_load_dwordx4 v92, %[rw], s44 offen lds\ns_waitcnt vmcnt(32)\nds_read_b64_tr_b16 a[188:189], v94 offset:4096\n" \
"ds_read_b64_tr_b16 a[190:191], v94 offset:4608\nds_read_b64_tr_b16 a[196:197], v94 offset:4104\nds_read_b64_tr_b16 a[198:199], v94 offset:4616\nds_read_b64_tr_b16 a[204:205], v94 offset:4160\nds_read_b64_tr_b16 a[206:207], v94 offset:4672\nds_read_b64_tr_b16 v[44:45], v94 offset:4168\nds_read_b64_tr_b16 v[46:47], v94 offset:4680\ns_waitcnt lgkmcnt(0)\n" \
"s_add_u32 m0, %[lw], 4096\ns_add_u32 s44, %[wo], 6291456\nbuffer_load_dwordx4 v92, %[rw], s44 offen lds\ns_add_u32 m0, %[lw], 5120\ns_add_u32 s44, %[wo], 6356992\nbuffer_load_dwordx4 v92, %[rw], s44 offen lds\ns_add_u32 m0, %[lw], 6144\ns_add_u32 s44, %[wo], 6422528\n" \
"buffer_load_dwordx4 v92, %[rw], s44 offen lds\ns_add_u32 m0, %[lw], 7168\ns_add_u32 s44, %[wo], 6488064\nbuffer_load_dwordx4 v92, %[rw], s44 offen lds\ns_waitcnt vmcnt(32)\nds_read_b64_tr_b16 a[208:209], v94 offset:8192\nds_read_b64_tr_b16 a[210:211], v94 offset:8704\nds_read_b64_tr_b16 a[216:217], v94 offset:8200\n" \
"ds_read_b64_tr_b16 a[218:219], v94 offset:8712\nds_read_b64_tr_b16 a[224:225], v94 offset:8256\nds_read_b64_tr_b16 a[226:227], v94 offset:8768\nds_read_b64_tr_b16 a[232:233], v94 offset:8264\nds_read_b64_tr_b16 a[234:235], v94 offset:8776\ns_waitcnt lgkmcnt(0)\ns_add_u32 m0, %[lw], 8192\ns_add_u32 s44, %[wo], 6553600\n" \
"buffer_load_dwordx4 v92, %[rw], s44 offen lds\ns_add_u32 m0, %[lw], 9216\ns_add_u32 s44, %[wo], 6619136\nbuffer_load_dwordx4 v92, %[rw], s44 offen lds\ns_add_u32 m0, %[lw], 10240\ns_add_u32 s44, %[wo], 6684672\nbuffer_load_dwordx4 v92, %[rw], s44 offen lds\ns_add_u32 m0, %[lw], 11264\n" \
"s_add_u32 s44, %[wo], 6750208\nbuffer_load_dwordx4 v92, %[rw], s44 offen lds\ns_waitcnt vmcnt(32)\nds_read_b64_tr_b16 a[212:213], v94 offset:12288\nds_read_b64_tr_b16 a[214:215], v94 offset:12800\nds_read_b64_tr_b16 a[220:221], v94 offset:12296\nds_read_b64_tr_b16 a[222:223], v94 offset:12808\nds_read_b64_tr_b16 a[228:229], v94 offset:12352\n" \
"ds_read_b64_tr_b16 a[230:231], v94 offset:12864\nds_read_b64_tr_b16 a[236:237], v94 offset:12360\nds_read_b64_tr_b16 a[238:239], v94 offset:12872\ns_waitcnt lgkmcnt(0)\ns_add_u32 m0, %[lw], 12288\ns_add_u32 s44, %[wo], 6815744\nbuffer_load_dwordx4 v92, %[rw], s44 offen lds\ns_add_u32 m0, %[lw], 13312\n" \
"s_add_u32 s44, %[wo], 6881280\nbuffer_load_dwordx4 v92, %[rw], s44 offen lds\ns_add_u32 m0, %[lw], 14336\ns_add_u32 s44, %[wo], 6946816\nbuffer_load_dwordx4 v92, %[rw], s44 offen lds\ns_add_u32 m0, %[lw], 15360\ns_add_u32 s44, %[wo], 7012352\nbuffer_load_dwordx4 v92, %[rw], s44 offen lds\n" \
"s_waitcnt vmcnt(32)\nds_read_b64_tr_b16 a[240:241], v94 offset:16384\nds_read_b64_tr_b16 a[242:243], v94 offset:16896\nds_read_b64_tr_b16 v[40:41], v94 offset:16392\nds_read_b64_tr_b16 v[42:43], v94 offset:16904\nds_read_b64_tr_b16 a[248:249], v94 offset:16448\nds_read_b64_tr_b16 a[250:251], v94 offset:16960\nds_read_b64_tr_b16 v[96:97], v94 offset:16456\n" \
"ds_read_b64_tr_b16 v[98:99], v94 offset:16968\ns_waitcnt lgkmcnt(0)\ns_add_u32 m0, %[lw], 16384\ns_add_u32 s44, %[wo], 7077888\nbuffer_load_dwordx4 v92, %[rw], s44 offen lds\ns_add_u32 m0, %[lw], 17408\ns_add_u32 s44, %[wo], 7143424\nbuffer_load_dwordx4 v92, %[rw], s44 offen lds\n" \
"s_add_u32 m0, %[lw], 18432\ns_add_u32 s44, %[wo], 7208960\nbuffer_load_dwordx4 v92, %[rw], s44 offen lds\ns_add_u32 m0, %[lw], 19456\ns_add_u32 s44, %[wo], 7274496\nbuffer_load_dwordx4 v92, %[rw], s44 offen lds\ns_waitcnt vmcnt(32)\nds_read_b64_tr_b16 a[244:245], v94 offset:20480\n" \
"ds_read_b64_tr_b16 a[246:247], v94 offset:20992\nds_read_b64_tr_b16 v[36:37], v94 offset:20488\nds_read_b64_tr_b16 v[38:39], v94 offset:21000\nds_read_b64_tr_b16 a[252:253], v94 offset:20544\nds_read_b64_tr_b16 a[254:255], v94 offset:21056\nds_read_b64_tr_b16 v[100:101], v94 offset:20552\nds_read_b64_tr_b16 v[102:103], v94 offset:21064\ns_waitcnt lgkmcnt(0)\n" \
"s_add_u32 m0, %[lw], 20480\ns_add_u32 s44, %[wo], 7340032\nbuffer_load_dwordx4 v92, %[rw], s44 offen lds\ns_add_u32 m0, %[lw], 21504\ns_add_u32 s44, %[wo], 7405568\nbuffer_load_dwordx4 v92, %[rw], s44 offen lds\ns_add_u32 m0, %[lw], 22528\ns_add_u32 s44, %[wo], 7471104\n" \
"buffer_load_dwordx4 v92, %[rw], s44 offen lds\ns_add_u32 m0, %[lw], 23552\ns_add_u32 s44, %[wo], 7536640\nbuffer_load_dwordx4 v92, %[rw], s44 offen lds\ns_waitcnt vmcnt(32)\nds_read_b64_tr_b16 v[104:105], v93 offset:0\nds_read_b64_tr_b16 v[106:107], v93 offset:512\nds_read_b64_tr_b16 v[112:113], v93 offset:8\n" \
"ds_read_b64_tr_b16 v[114:115], v93 offset:520\nds_read_b64_tr_b16 v[32:33], v93 offset:64\nds_read_b64_tr_b16 v[34:35], v93 offset:576\nds_read_b64_tr_b16 v[120:121], v93 offset:72\nds_read_b64_tr_b16 v[122:123], v93 offset:584\ns_waitcnt lgkmcnt(0)\ns_add_u32 m0, %[tb], 0\ns_add_u32 s44, %[wo], 7602176\n" \
"buffer_load_dwordx4 v92, %[rw], s44 offen lds\ns_add_u32 m0, %[tb], 1024\ns_add_u32 s44, %[wo], 7667712\nbuffer_load_dwordx4 v92, %[rw], s44 offen lds\ns_add_u32 m0, %[tb], 2048\ns_add_u32 s44, %[wo], 7733248\nbuffer_load_dwordx4 v92, %[rw], s44 offen lds\ns_add_u32 m0, %[tb], 3072\n" \
"s_add_u32 s44, %[wo], 7798784\nbuffer_load_dwordx4 v92, %[rw], s44 offen lds\ns_waitcnt vmcnt(32)\nds_read_b64_tr_b16 v[108:109], v93 offset:4096\nds_read_b64_tr_b16 v[110:111], v93 offset:4608\nds_read_b64_tr_b16 v[116:117], v93 offset:4104\nds_read_b64_tr_b16 v[118:119], v93 offset:4616\nds_read_b64_tr_b16 v[28:29], v93 offset:4160\n" \
"ds_read_b64_tr_b16 v[30:31], v93 offset:4672\nds_read_b64_tr_b16 v[124:125], v93 offset:4168\nds_read_b64_tr_b16 v[126:127], v93 offset:4680\ns_waitcnt lgkmcnt(0)\ns_add_u32 m0, %[tb], 4096\ns_add_u32 s44, %[wo], 7864320\nbuffer_load_dwordx4 v92, %[rw], s44 offen lds\ns_add_u32 m0, %[tb], 5120\n" \
"s_add_u32 s44, %[wo], 7929856\nbuffer_load_dwordx4 v92, %[rw], s44 offen lds\ns_add_u32 m0, %[tb], 6144\ns_add_u32 s44, %[wo], 7995392\nbuffer_load_dwordx4 v92, %[rw], s44 offen lds\ns_add_u32 m0, %[tb], 7168\ns_add_u32 s44, %[wo], 8060928\nbuffer_load_dwordx4 v92, %[rw], s44 offen lds\n" \
"s_waitcnt vmcnt(32)\nds_read_b64_tr_b16 v[128:129], v93 offset:8192\nds_read_b64_tr_b16 v[130:131], v93 offset:8704\nds_read_b64_tr_b16 v[136:137], v93 offset:8200\nds_read_b64_tr_b16 v[138:139], v93 offset:8712\nds_read_b64_tr_b16 v[144:145], v93 offset:8256\nds_read_b64_tr_b16 v[146:147], v93 offset:8768\nds_read_b64_tr_b16 v[24:25], v93 offset:8264\n" \
"ds_read_b64_tr_b16 v[26:27], v93 offset:8776\ns_waitcnt lgkmcnt(0)\ns_add_u32 m0, %[tb], 8192\ns_add_u32 s44, %[wo], 8126464\nbuffer_load_dwordx4 v92, %[rw], s44 offen lds\ns_add_u32 m0, %[tb], 9216\ns_add_u32 s44, %[wo], 8192000\nbuffer_load_dwordx4 v92, %[rw], s44 offen lds\n" \
"s_add_u32 m0, %[tb], 10240\ns_add_u32 s44, %[wo], 8257536\nbuffer_load_dwordx4 v92, %[rw], s44 offen lds\ns_add_u32 m0, %[tb], 11264\ns_add_u32 s44, %[wo], 8323072\nbuffer_load_dwordx4 v92, %[rw], s44 offen lds\ns_waitcnt vmcnt(32)\nds_read_b64_tr_b16 v[132:133], v94 offset:0\n" \
"ds_read_b64_tr_b16 v[134:135], v94 offset:512\nds_read_b64_tr_b16 v[140:141], v94 offset:8\nds_read_b64_tr_b16 v[142:143], v94 offset:520\nds_read_b64_tr_b16 v[148:149], v94 offset:64\nds_read_b64_tr_b16 v[150:151], v94 offset:576\nds_read_b64_tr_b16 v[20:21], v94 offset:72\nds_read_b64_tr_b16 v[22:23], v94 offset:584\ns_waitcnt lgkmcnt(0)\n" \
"ds_write_b128 v1, v[88:91] offset:0\nds_write_b128 v1, v[84:87] offset:1024\nds_write_b128 v1, v[80:83] offset:2048\nds_write_b128 v1, v[76:79] offset:3072\ns_waitcnt vmcnt(28)\nds_read_b64_tr_b16 v[152:153], v94 offset:4096\nds_read_b64_tr_b16 v[154:155], v94 offset:4608\nds_read_b64_tr_b16 v[160:161], v94 offset:4104\n" \
"ds_read_b64_tr_b16 v[162:163], v94 offset:4616\nds_read_b64_tr_b16 v[168:169], v94 offset:4160\nds_read_b64_tr_b16 v[170:171], v94 offset:4672\nds_read_b64_tr_b16 v[176:177], v94 offset:4168\nds_read_b64_tr_b16 v[178:179], v94 offset:4680\ns_waitcnt lgkmcnt(0)\nds_write_b128 v1, v[72:75] offset:4096\nds_write_b128 v1, v[68:71] offset:5120\n" \
"ds_write_b128 v1, v[64:67] offset:6144\nds_write_b128 v1, v[60:63] offset:7168\ns_waitcnt vmcnt(24)\nds_read_b64_tr_b16 v[156:157], v94 offset:8192\nds_read_b64_tr_b16 v[158:159], v94 offset:8704\nds_read_b64_tr_b16 v[164:165], v94 offset:8200\nds_read_b64_tr_b16 v[166:167], v94 offset:8712\nds_read_b64_tr_b16 v[172:173], v94 offset:8256\n" \
"ds_read_b64_tr_b16 v[174:175], v94 offset:8768\nds_read_b64_tr_b16 v[180:181], v94 offset:8264\nds_read_b64_tr_b16 v[182:183], v94 offset:8776\ns_waitcnt lgkmcnt(0)\nds_write_b128 v1, v[56:59] offset:8192\nds_write_b128 v1, v[52:55] offset:9216\nds_write_b128 v1, v[48:51] offset:10240\nds_write_b128 v1, v[44:47] offset:11264\n" \
"s_waitcnt vmcnt(20)\nds_read_b64_tr_b16 v[184:185], v94 offset:12288\nds_read_b64_tr_b16 v[186:187], v94 offset:12800\nds_read_b64_tr_b16 v[44:45], v94 offset:12296\nds_read_b64_tr_b16 v[46:47], v94 offset:12808\nds_read_b64_tr_b16 v[192:193], v94 offset:12352\nds_read_b64_tr_b16 v[194:195], v94 offset:12864\nds_read_b64_tr_b16 v[200:201], v94 offset:12360\n" \
"ds_read_b64_tr_b16 v[202:203], v94 offset:12872\ns_waitcnt lgkmcnt(0)\nds_write_b128 v1, v[40:43] offset:12288\nds_write_b128 v1, v[36:39] offset:13312\nds_write_b128 v1, v[32:35] offset:14336\nds_write_b128 v1, v[28:31] offset:15360\ns_waitcnt vmcnt(16)\nds_read_b64_tr_b16 v[188:189], v94 offset:16384\n" \
"ds_read_b64_tr_b16 v[190:191], v94 offset:16896\nds_read_b64_tr_b16 v[28:29], v94 offset:16392\nds_read_b64_tr_b16 v[30:31], v94 offset:16904\nds_read_b64_tr_b16 v[196:197], v94 offset:16448\nds_read_b64_tr_b16 v[198:199], v94 offset:16960\nds_read_b64_tr_b16 v[204:205], v94 offset:16456\nds_read_b64_tr_b16 v[206:207], v94 offset:16968\ns_waitcnt lgkmcnt(0)\n" \
"ds_write_b128 v1, v[24:27] offset:16384\nds_write_b128 v1, v[20:23] offset:17408\nds_write_b128 v1, v[44:47] offset:18432\nds_write_b128 v1, v[28:31] offset:19456\ns_waitcnt vmcnt(12)\nds_read_b64_tr_b16 v[208:209], v94 offset:20480\nds_read_b64_tr_b16 v[210:211], v94 offset:20992\nds_read_b64_tr_b16 v[216:217], v94 offset:20488\n" \
"ds_read_b64_tr_b16 v[218:219], v94 offset:21000\nds_read_b64_tr_b16 v[28:29], v94 offset:20544\nds_read_b64_tr_b16 v[30:31], v94 offset:21056\nds_read_b64_tr_b16 v[224:225], v94 offset:20552\nds_read_b64_tr_b16 v[226:227], v94 offset:21064\ns_waitcnt lgkmcnt(0)\nds_write_b128 v1, v[28:31] offset:20480\ns_waitcnt vmcnt(8)\n" \
"ds_read_b64_tr_b16 v[212:213], v93 offset:0\nds_read_b64_tr_b16 v[214:215], v93 offset:512\nds_read_b64_tr_b16 v[220:221], v93 offset:8\nds_read_b64_tr_b16 v[222:223], v93 offset:520\nds_read_b64_tr_b16 v[28:29], v93 offset:64\nds_read_b64_tr_b16 v[30:31], v93 offset:576\nds_read_b64_tr_b16 v[228:229], v93 offset:72\nds_read_b64_tr_b16 v[230:231], v93 offset:584\n" \
"s_waitcnt lgkmcnt(0)\nds_write_b128 v1, v[28:31] offset:21504\ns_waitcnt vmcnt(4)\nds_read_b64_tr_b16 v[232:233], v93 offset:4096\nds_read_b64_tr_b16 v[234:235], v93 offset:4608\nds_read_b64_tr_b16 v[240:241], v93 offset:4104\nds_read_b64_tr_b16 v[242:243], v93 offset:4616\nds_read_b64_tr_b16 v[248:249], v93 offset:4160\n" \
"ds_read_b64_tr_b16 v[250:251], v93 offset:4672\nds_read_b64_tr_b16 v[28:29], v93 offset:4168\nds_read_b64_tr_b16 v[30:31], v93 offset:4680\ns_waitcnt lgkmcnt(0)\nds_write_b128 v1, v[28:31] offset:22528\ns_waitcnt vmcnt(0)\nds_read_b64_tr_b16 v[236:237], v93 offset:8192\nds_read_b64_tr_b16 v[238:239], v93 offset:8704\n" \
"ds_read_b64_tr_b16 v[244:245], v93 offset:8200\nds_read_b64_tr_b16 v[246:247], v93 offset:8712\nds_read_b64_tr_b16 v[252:253], v93 offset:8256\nds_read_b64_tr_b16 v[254:255], v93 offset:8768\nds_read_b64_tr_b16 v[28:29], v93 offset:8264\nds_read_b64_tr_b16 v[30:31], v93 offset:8776\ns_waitcnt lgkmcnt(0)\nds_write_b128 v1, v[28:31] offset:23552\n" \
"s_waitcnt lgkmcnt(0)\ns_barrier\ns_mov_b32 s40, -1\ns_mov_b32 s43, 0\ns_mov_b32 s61, 0x7fff7fff\ns_mov_b32 s58, 0x3e0293ee\ns_mov_b32 s59, 0xbe0293ee\ns_lshl_b32 s57, %[w], 12\n" \
"s_mov_b32 s62, 0xff00ff00\ns_mov_b32 s63, 0xff00ff00\ns_mov_b32 s56, 0\ns_cmp_eq_u32 %[w], 3\ns_cbranch_scc1 .Lqk_fetch0%=\n.Lqk_proj%=:\ns_add_u32 s44, s40, 1\ns_lshl_b32 s44, s44, 17\n" \
"s_add_u32 s44, s44, %[xn]\ns_lshr_b32 s45, s44, 8\ns_and_b32 s45, s45, 0x2000\ns_xor_b32 s41, s44, s45\ns_add_u32 s40, s40, 1\ns_add_u32 s41, s41, %[xw]\ns_barrier\nds_read_b128 v[48:51], v2 offset:0\n" \
"ds_read_b128 v[52:55], v2 offset:256\nds_read_b128 v[56:59], v2 offset:2048\ns_and_b32 s42, s40, 3\ns_lshl_b32 s42, s42, 15\ns_waitcnt lgkmcnt(2)\nv_mfma_f32_16x16x32_bf16 v[16:19], a[0:3], v[48:51], 0\nds_read_b128 v[60:63], v2 offset:2304\nv_mfma_f32_16x16x32_bf16 v[20:23], a[8:11], v[48:51], 0\n" \
"ds_read_b128 v[72:75], v1 offset:0\nv_mfma_f32_16x16x32_bf16 v[24:27], a[16:19], v[48:51], 0\nv_mfma_f32_16x16x32_bf16 v[28:31], a[24:27], v[48:51], 0\ns_waitcnt lgkmcnt(3)\nv_mfma_f32_16x16x32_bf16 v[32:35], a[0:3], v[52:55], 0\nds_read_b128 v[48:51], v2 offset:4096\nv_mfma_f32_16x16x32_bf16 v[36:39], a[8:11], v[52:55], 0\nv_mfma_f32_16x16x32_bf16 v[40:43], a[16:19], v[52:55], 0\n" \
"v_mfma_f32_16x16x32_bf16 v[44:47], a[24:27], v[52:55], 0\ns_waitcnt lgkmcnt(3)\nv_mfma_f32_16x16x32_bf16 v[16:19], a[4:7], v[56:59], v[16:19]\nds_read_b128 v[52:55], v2 offset:4352\nv_mfma_f32_16x16x32_bf16 v[20:23], a[12:15], v[56:59], v[20:23]\nds_read_b128 v[76:79], v1 offset:1024\nv_mfma_f32_16x16x32_bf16 v[24:27], a[20:23], v[56:59], v[24:27]\nv_mfma_f32_16x16x32_bf16 v[28:31], a[28:31], v[56:59], v[28:31]\n" \
"s_waitcnt lgkmcnt(4)\nv_mfma_f32_16x16x32_bf16 v[32:35], a[4:7], v[60:63], v[32:35]\nds_read_b128 v[56:59], v2 offset:6144\nv_mfma_f32_16x16x32_bf16 v[36:39], a[12:15], v[60:63], v[36:39]\nv_mfma_f32_16x16x32_bf16 v[40:43], a[20:23], v[60:63], v[40:43]\nv_mfma_f32_16x16x32_bf16 v[44:47], a[28:31], v[60:63], v[44:47]\ns_waitcnt lgkmcnt(3)\nv_mfma_f32_16x16x32_bf16 v[16:19], a[32:35], v[48:51], v[16:19]\n" \
"ds_read_b128 v[60:63], v2 offset:6400\nv_mfma_f32_16x16x32_bf16 v[20:23], v[72:75], v[48:51], v[20:23]\nds_read_b128 v[64:67], v1 offset:2048\nv_mfma_f32_16x16x32_bf16 v[24:27], a[40:43], v[48:51], v[24:27]\nv_mfma_f32_16x16x32_bf16 v[28:31], a[48:51], v[48:51], v[28:31]\ns_waitcnt lgkmcnt(4)\nv_mfma_f32_16x16x32_bf16 v[32:35], a[32:35], v[52:55], v[32:35]\nds_read_b128 v[48:51], v2 offset:8192\n" \
"v_mfma_f32_16x16x32_bf16 v[36:39], v[72:75], v[52:55], v[36:39]\nv_mfma_f32_16x16x32_bf16 v[40:43], a[40:43], v[52:55], v[40:43]\ns_add_u32 m0, s57, 32768\ns_add_u32 s60, s41, 65536\nbuffer_load_dwordx4 v0, %[rs], s60 offen offset:0 sc1 lds\nv_mfma_f32_16x16x32_bf16 v[44:47], a[48:51], v[52:55], v[44:47]\ns_waitcnt lgkmcnt(3)\nv_mfma_f32_16x16x32_bf16 v[16:19], a[36:39], v[56:59], v[16:19]\n" \
"ds_read_b128 v[52:55], v2 offset:8448\nv_mfma_f32_16x16x32_bf16 v[20:23], v[76:79], v[56:59], v[20:23]\nds_read_b128 v[68:71], v1 offset:3072\nv_mfma_f32_16x16x32_bf16 v[24:27], a[44:47], v[56:59], v[24:27]\nv_mfma_f32_16x16x32_bf16 v[28:31], a[52:55], v[56:59], v[28:31]\ns_waitcnt lgkmcnt(4)\nv_mfma_f32_16x16x32_bf16 v[32:35], a[36:39], v[60:63], v[32:35]\nds_read_b128 v[56:59], v2 offset:10240\n" \
"v_mfma_f32_16x16x32_bf16 v[36:39], v[76:79], v[60:63], v[36:39]\nv_mfma_f32_16x16x32_bf16 v[40:43], a[44:47], v[60:63], v[40:43]\nbuffer_load_dwordx4 v0, %[rs], s60 offen offset:1024 sc1 lds\nv_mfma_f32_16x16x32_bf16 v[44:47], a[52:55], v[60:63], v[44:47]\ns_waitcnt lgkmcnt(3)\nv_mfma_f32_16x16x32_bf16 v[16:19], a[56:59], v[48:51], v[16:19]\nds_read_b128 v[60:63], v2 offset:10496\nv_mfma_f32_16x16x32_bf16 v[20:23], a[64:67], v[48:51], v[20:23]\n" \
"ds_read_b128 v[72:75], v1 offset:4096\nv_mfma_f32_16x16x32_bf16 v[24:27], v[64:67], v[48:51], v[24:27]\nv_mfma_f32_16x16x32_bf16 v[28:31], a[72:75], v[48:51], v[28:31]\ns_waitcnt lgkmcnt(4)\nv_mfma_f32_16x16x32_bf16 v[32:35], a[56:59], v[52:55], v[32:35]\nds_read_b128 v[48:51], v2 offset:12288\nv_mfma_f32_16x16x32_bf16 v[36:39], a[64:67], v[52:55], v[36:39]\nv_mfma_f32_16x16x32_bf16 v[40:43], v[64:67], v[52:55], v[40:43]\n" \
"buffer_load_dwordx4 v0, %[rs], s60 offen offset:2048 sc1 lds\nv_mfma_f32_16x16x32_bf16 v[44:47], a[72:75], v[52:55], v[44:47]\ns_waitcnt lgkmcnt(3)\nv_mfma_f32_16x16x32_bf16 v[16:19], a[60:63], v[56:59], v[16:19]\nds_read_b128 v[52:55], v2 offset:12544\nv_mfma_f32_16x16x32_bf16 v[20:23], a[68:71], v[56:59], v[20:23]\nds_read_b128 v[76:79], v1 offset:5120\nv_mfma_f32_16x16x32_bf16 v[24:27], v[68:71], v[56:59], v[24:27]\n" \
"v_mfma_f32_16x16x32_bf16 v[28:31], a[76:79], v[56:59], v[28:31]\ns_waitcnt lgkmcnt(4)\nv_mfma_f32_16x16x32_bf16 v[32:35], a[60:63], v[60:63], v[32:35]\nds_read_b128 v[56:59], v2 offset:14336\nv_mfma_f32_16x16x32_bf16 v[36:39], a[68:71], v[60:63], v[36:39]\nv_mfma_f32_16x16x32_bf16 v[40:43], v[68:71], v[60:63], v[40:43]\nbuffer_load_dwordx4 v0, %[rs], s60 offen offset:3072 sc1 lds\nv_mfma_f32_16x16x32_bf16 v[44:47], a[76:79], v[60:63], v[44:47]\n" \
"s_waitcnt lgkmcnt(3)\nv_mfma_f32_16x16x32_bf16 v[16:19], a[80:83], v[48:51], v[16:19]\nds_read_b128 v[60:63], v2 offset:14592\nv_mfma_f32_16x16x32_bf16 v[20:23], a[88:91], v[48:51], v[20:23]\nv_mfma_f32_16x16x32_bf16 v[24:27], a[96:99], v[48:51], v[24:27]\nv_mfma_f32_16x16x32_bf16 v[28:31], v[72:75], v[48:51], v[28:31]\ns_waitcnt lgkmcnt(3)\nv_mfma_f32_16x16x32_bf16 v[32:35], a[80:83], v[52:55], v[32:35]\n" \
"s_waitcnt vmcnt(4)\ns_barrier\nds_read_b128 v[48:51], v2 offset:16384\nv_mfma_f32_16x16x32_bf16 v[36:39], a[88:91], v[52:55], v[36:39]\nv_mfma_f32_16x16x32_bf16 v[40:43], a[96:99], v[52:55], v[40:43]\nv_mfma_f32_16x16x32_bf16 v[44:47], v[72:75], v[52:55], v[44:47]\ns_waitcnt lgkmcnt(2)\nv_mfma_f32_16x16x32_bf16 v[16:19], a[84:87], v[56:59], v[16:19]\n" \
"ds_read_b128 v[52:55], v2 offset:16640\nv_mfma_f32_16x16x32_bf16 v[20:23], a[92:95], v[56:59], v[20:23]\nv_mfma_f32_16x16x32_bf16 v[24:27], a[100:103], v[56:59], v[24:27]\nv_mfma_f32_16x16x32_bf16 v[28:31], v[76:79], v[56:59], v[28:31]\ns_waitcnt lgkmcnt(2)\nv_mfma_f32_16x16x32_bf16 v[32:35], a[84:87], v[60:63], v[32:35]\nds_read_b128 v[56:59], v2 offset:18432\nv_mfma_f32_16x16x32_bf16 v[36:39], a[92:95], v[60:63], v[36:39]\n" \
"v_mfma_f32_16x16x32_bf16 v[40:43], a[100:103], v[60:63], v[40:43]\nv_mfma_f32_16x16x32_bf16 v[44:47], v[76:79], v[60:63], v[44:47]\ns_waitcnt lgkmcnt(2)\nv_mfma_f32_16x16x32_bf16 v[16:19], a[104:107], v[48:51], v[16:19]\nds_read_b128 v[60:63], v2 offset:18688\nv_mfma_f32_16x16x32_bf16 v[20:23], a[112:115], v[48:51], v[20:23]\nds_read_b128 v[72:75], v1 offset:6144\nv_mfma_f32_16x16x32_bf16 v[24:27], a[120:123], v[48:51], v[24:27]\n" \
"v_mfma_f32_16x16x32_bf16 v[28:31], a[128:131], v[48:51], v[28:31]\ns_waitcnt lgkmcnt(3)\nv_mfma_f32_16x16x32_bf16 v[32:35], a[104:107], v[52:55], v[32:35]\nds_read_b128 v[48:51], v2 offset:20480\nv_mfma_f32_16x16x32_bf16 v[36:39], a[112:115], v[52:55], v[36:39]\nv_mfma_f32_16x16x32_bf16 v[40:43], a[120:123], v[52:55], v[40:43]\nv_mfma_f32_16x16x32_bf16 v[44:47], a[128:131], v[52:55], v[44:47]\ns_waitcnt lgkmcnt(3)\n" \
"v_mfma_f32_16x16x32_bf16 v[16:19], a[108:111], v[56:59], v[16:19]\nds_read_b128 v[52:55], v2 offset:20736\nv_mfma_f32_16x16x32_bf16 v[20:23], a[116:119], v[56:59], v[20:23]\nds_read_b128 v[76:79], v1 offset:7168\nv_mfma_f32_16x16x32_bf16 v[24:27], a[124:127], v[56:59], v[24:27]\nv_mfma_f32_16x16x32_bf16 v[28:31], a[132:135], v[56:59], v[28:31]\ns_waitcnt lgkmcnt(4)\nv_mfma_f32_16x16x32_bf16 v[32:35], a[108:111], v[60:63], v[32:35]\n" \
"ds_read_b128 v[56:59], v2 offset:22528\nv_mfma_f32_16x16x32_bf16 v[36:39], a[116:119], v[60:63], v[36:39]\nv_mfma_f32_16x16x32_bf16 v[40:43], a[124:127], v[60:63], v[40:43]\nv_mfma_f32_16x16x32_bf16 v[44:47], a[132:135], v[60:63], v[44:47]\ns_waitcnt lgkmcnt(3)\nv_mfma_f32_16x16x32_bf16 v[16:19], a[136:139], v[48:51], v[16:19]\nds_read_b128 v[60:63], v2 offset:22784\nv_mfma_f32_16x16x32_bf16 v[20:23], v[72:75], v[48:51], v[20:23]\n" \
"ds_read_b128 v[64:67], v1 offset:8192\nv_mfma_f32_16x16x32_bf16 v[24:27], a[144:147], v[48:51], v[24:27]\nv_mfma_f32_16x16x32_bf16 v[28:31], a[152:155], v[48:51], v[28:31]\ns_waitcnt lgkmcnt(4)\nv_mfma_f32_16x16x32_bf16 v[32:35], a[136:139], v[52:55], v[32:35]\nds_read_b128 v[48:51], v2 offset:24576\nv_mfma_f32_16x16x32_bf16 v[36:39], v[72:75], v[52:55], v[36:39]\nv_mfma_f32_16x16x32_bf16 v[40:43], a[144:147], v[52:55], v[40:43]\n" \
"v_mfma_f32_16x16x32_bf16 v[44:47], a[152:155], v[52:55], v[44:47]\ns_waitcnt lgkmcnt(3)\nv_mfma_f32_16x16x32_bf16 v[16:19], a[140:143], v[56:59], v[16:19]\nds_read_b128 v[52:55], v2 offset:24832\nv_mfma_f32_16x16x32_bf16 v[20:23], v[76:79], v[56:59], v[20:23]\nds_read_b128 v[68:71], v1 offset:9216\nv_mfma_f32_16x16x32_bf16 v[24:27], a[148:151], v[56:59], v[24:27]\nv_mfma_f32_16x16x32_bf16 v[28:31], a[156:159], v[56:59], v[28:31]\n" \
"s_waitcnt lgkmcnt(4)\nv_mfma_f32_16x16x32_bf16 v[32:35], a[140:143], v[60:63], v[32:35]\nds_read_b128 v[56:59], v2 offset:26624\nv_mfma_f32_16x16x32_bf16 v[36:39], v[76:79], v[60:63], v[36:39]\nv_mfma_f32_16x16x32_bf16 v[40:43], a[148:151], v[60:63], v[40:43]\nv_mfma_f32_16x16x32_bf16 v[44:47], a[156:159], v[60:63], v[44:47]\ns_waitcnt lgkmcnt(3)\nv_mfma_f32_16x16x32_bf16 v[16:19], a[160:163], v[48:51], v[16:19]\n" \
"ds_read_b128 v[60:63], v2 offset:26880\nv_mfma_f32_16x16x32_bf16 v[20:23], a[168:171], v[48:51], v[20:23]\nds_read_b128 v[72:75], v1 offset:10240\nv_mfma_f32_16x16x32_bf16 v[24:27], v[64:67], v[48:51], v[24:27]\nv_mfma_f32_16x16x32_bf16 v[28:31], a[176:179], v[48:51], v[28:31]\ns_waitcnt lgkmcnt(4)\nv_mfma_f32_16x16x32_bf16 v[32:35], a[160:163], v[52:55], v[32:35]\nds_read_b128 v[48:51], v2 offset:28672\n" \
"v_mfma_f32_16x16x32_bf16 v[36:39], a[168:171], v[52:55], v[36:39]\nv_mfma_f32_16x16x32_bf16 v[40:43], v[64:67], v[52:55], v[40:43]\nv_mfma_f32_16x16x32_bf16 v[44:47], a[176:179], v[52:55], v[44:47]\ns_waitcnt lgkmcnt(3)\nv_mfma_f32_16x16x32_bf16 v[16:19], a[164:167], v[56:59], v[16:19]\nds_read_b128 v[52:55], v2 offset:28928\nv_mfma_f32_16x16x32_bf16 v[20:23], a[172:175], v[56:59], v[20:23]\nds_read_b128 v[76:79], v1 offset:11264\n" \
"v_mfma_f32_16x16x32_bf16 v[24:27], v[68:71], v[56:59], v[24:27]\nv_mfma_f32_16x16x32_bf16 v[28:31], a[180:183], v[56:59], v[28:31]\ns_waitcnt lgkmcnt(4)\nv_mfma_f32_16x16x32_bf16 v[32:35], a[164:167], v[60:63], v[32:35]\nds_read_b128 v[56:59], v2 offset:30720\nv_mfma_f32_16x16x32_bf16 v[36:39], a[172:175], v[60:63], v[36:39]\nv_mfma_f32_16x16x32_bf16 v[40:43], v[68:71], v[60:63], v[40:43]\nv_mfma_f32_16x16x32_bf16 v[44:47], a[180:183], v[60:63], v[44:47]\n" \
"s_waitcnt lgkmcnt(3)\nv_mfma_f32_16x16x32_bf16 v[16:19], a[184:187], v[48:51], v[16:19]\nds_read_b128 v[60:63], v2 offset:30976\nv_mfma_f32_16x16x32_bf16 v[20:23], a[192:195], v[48:51], v[20:23]\nv_mfma_f32_16x16x32_bf16 v[24:27], a[200:203], v[48:51], v[24:27]\nv_mfma_f32_16x16x32_bf16 v[28:31], v[72:75], v[48:51], v[28:31]\ns_waitcnt lgkmcnt(3)\nv_mfma_f32_16x16x32_bf16 v[32:35], a[184:187], v[52:55], v[32:35]\n" \
"s_waitcnt vmcnt(0)\ns_barrier\nds_read_b128 v[48:51], v2 offset:32768\nv_mfma_f32_16x16x32_bf16 v[36:39], a[192:195], v[52:55], v[36:39]\nv_mfma_f32_16x16x32_bf16 v[40:43], a[200:203], v[52:55], v[40:43]\ns_add_u32 m0, s57, 0\ns_add_u32 s60, s41, 98304\nbuffer_load_dwordx4 v0, %[rs], s60 offen offset:0 sc1 lds\n" \
"v_mfma_f32_16x16x32_bf16 v[44:47], v[72:75], v[52:55], v[44:47]\ns_waitcnt lgkmcnt(2)\nv_mfma_f32_16x16x32_bf16 v[16:19], a[188:191], v[56:59], v[16:19]\nds_read_b128 v[52:55], v2 offset:33024\nv_mfma_f32_16x16x32_bf16 v[20:23], a[196:199], v[56:59], v[20:23]\nv_mfma_f32_16x16x32_bf16 v[24:27], a[204:207], v[56:59], v[24:27]\nbuffer_load_dwordx4 v0, %[rs], s60 offen offset:1024 sc1 lds\nv_mfma_f32_16x16x32_bf16 v[28:31], v[76:79], v[56:59], v[28:31]\n" \
"s_waitcnt lgkmcnt(2)\nv_mfma_f32_16x16x32_bf16 v[32:35], a[188:191], v[60:63], v[32:35]\nds_read_b128 v[56:59], v2 offset:34816\nv_mfma_f32_16x16x32_bf16 v[36:39], a[196:199], v[60:63], v[36:39]\nv_mfma_f32_16x16x32_bf16 v[40:43], a[204:207], v[60:63], v[40:43]\nbuffer_load_dwordx4 v0, %[rs], s60 offen offset:2048 sc1 lds\nv_mfma_f32_16x16x32_bf16 v[44:47], v[76:79], v[60:63], v[44:47]\ns_waitcnt lgkmcnt(2)\n" \
"v_mfma_f32_16x16x32_bf16 v[16:19], a[208:211], v[48:51], v[16:19]\nds_read_b128 v[60:63], v2 offset:35072\nv_mfma_f32_16x16x32_bf16 v[20:23], a[216:219], v[48:51], v[20:23]\nds_read_b128 v[72:75], v1 offset:12288\nv_mfma_f32_16x16x32_bf16 v[24:27], a[224:227], v[48:51], v[24:27]\nbuffer_load_dwordx4 v0, %[rs], s60 offen offset:3072 sc1 lds\nv_mfma_f32_16x16x32_bf16 v[28:31], a[232:235], v[48:51], v[28:31]\ns_waitcnt lgkmcnt(3)\n" \
"v_mfma_f32_16x16x32_bf16 v[32:35], a[208:211], v[52:55], v[32:35]\nds_read_b128 v[48:51], v2 offset:36864\nv_mfma_f32_16x16x32_bf16 v[36:39], a[216:219], v[52:55], v[36:39]\nv_mfma_f32_16x16x32_bf16 v[40:43], a[224:227], v[52:55], v[40:43]\nv_mfma_f32_16x16x32_bf16 v[44:47], a[232:235], v[52:55], v[44:47]\ns_waitcnt lgkmcnt(3)\nv_mfma_f32_16x16x32_bf16 v[16:19], a[212:215], v[56:59], v[16:19]\nds_read_b128 v[52:55], v2 offset:37120\n" \
"v_mfma_f32_16x16x32_bf16 v[20:23], a[220:223], v[56:59], v[20:23]\nds_read_b128 v[76:79], v1 offset:13312\nv_mfma_f32_16x16x32_bf16 v[24:27], a[228:231], v[56:59], v[24:27]\nv_mfma_f32_16x16x32_bf16 v[28:31], a[236:239], v[56:59], v[28:31]\ns_waitcnt lgkmcnt(4)\nv_mfma_f32_16x16x32_bf16 v[32:35], a[212:215], v[60:63], v[32:35]\nds_read_b128 v[56:59], v2 offset:38912\nv_mfma_f32_16x16x32_bf16 v[36:39], a[220:223], v[60:63], v[36:39]\n" \
"v_mfma_f32_16x16x32_bf16 v[40:43], a[228:231], v[60:63], v[40:43]\nv_mfma_f32_16x16x32_bf16 v[44:47], a[236:239], v[60:63], v[44:47]\ns_waitcnt lgkmcnt(3)\nv_mfma_f32_16x16x32_bf16 v[16:19], a[240:243], v[48:51], v[16:19]\nds_read_b128 v[60:63], v2 offset:39168\nv_mfma_f32_16x16x32_bf16 v[20:23], v[72:75], v[48:51], v[20:23]\nds_read_b128 v[64:67], v1 offset:14336\nv_mfma_f32_16x16x32_bf16 v[24:27], a[248:251], v[48:51], v[24:27]\n" \
"v_mfma_f32_16x16x32_bf16 v[28:31], v[96:99], v[48:51], v[28:31]\ns_waitcnt lgkmcnt(4)\nv_mfma_f32_16x16x32_bf16 v[32:35], a[240:243], v[52:55], v[32:35]\nds_read_b128 v[48:51], v2 offset:40960\nv_mfma_f32_16x16x32_bf16 v[36:39], v[72:75], v[52:55], v[36:39]\nv_mfma_f32_16x16x32_bf16 v[40:43], a[248:251], v[52:55], v[40:43]\nv_mfma_f32_16x16x32_bf16 v[44:47], v[96:99], v[52:55], v[44:47]\ns_waitcnt lgkmcnt(3)\n" \
"v_mfma_f32_16x16x32_bf16 v[16:19], a[244:247], v[56:59], v[16:19]\nds_read_b128 v[52:55], v2 offset:41216\nv_mfma_f32_16x16x32_bf16 v[20:23], v[76:79], v[56:59], v[20:23]\nds_read_b128 v[68:71], v1 offset:15360\nv_mfma_f32_16x16x32_bf16 v[24:27], a[252:255], v[56:59], v[24:27]\nv_mfma_f32_16x16x32_bf16 v[28:31], v[100:103], v[56:59], v[28:31]\ns_waitcnt lgkmcnt(4)\nv_mfma_f32_16x16x32_bf16 v[32:35], a[244:247], v[60:63], v[32:35]\n" \
"ds_read_b128 v[56:59], v2 offset:43008\nv_mfma_f32_16x16x32_bf16 v[36:39], v[76:79], v[60:63], v[36:39]\nv_mfma_f32_16x16x32_bf16 v[40:43], a[252:255], v[60:63], v[40:43]\nv_mfma_f32_16x16x32_bf16 v[44:47], v[100:103], v[60:63], v[44:47]\ns_waitcnt lgkmcnt(3)\nv_mfma_f32_16x16x32_bf16 v[16:19], v[104:107], v[48:51], v[16:19]\nds_read_b128 v[60:63], v2 offset:43264\nv_mfma_f32_16x16x32_bf16 v[20:23], v[112:115], v[48:51], v[20:23]\n" \
"ds_read_b128 v[72:75], v1 offset:16384\nv_mfma_f32_16x16x32_bf16 v[24:27], v[64:67], v[48:51], v[24:27]\nv_mfma_f32_16x16x32_bf16 v[28:31], v[120:123], v[48:51], v[28:31]\ns_waitcnt lgkmcnt(4)\nv_mfma_f32_16x16x32_bf16 v[32:35], v[104:107], v[52:55], v[32:35]\nds_read_b128 v[48:51], v2 offset:45056\nv_mfma_f32_16x16x32_bf16 v[36:39], v[112:115], v[52:55], v[36:39]\nv_mfma_f32_16x16x32_bf16 v[40:43], v[64:67], v[52:55], v[40:43]\n" \
"v_mfma_f32_16x16x32_bf16 v[44:47], v[120:123], v[52:55], v[44:47]\ns_waitcnt lgkmcnt(3)\nv_mfma_f32_16x16x32_bf16 v[16:19], v[108:111], v[56:59], v[16:19]\nds_read_b128 v[52:55], v2 offset:45312\nv_mfma_f32_16x16x32_bf16 v[20:23], v[116:119], v[56:59], v[20:23]\nds_read_b128 v[76:79], v1 offset:17408\nv_mfma_f32_16x16x32_bf16 v[24:27], v[68:71], v[56:59], v[24:27]\nv_mfma_f32_16x16x32_bf16 v[28:31], v[124:127], v[56:59], v[28:31]\n" \
"s_waitcnt lgkmcnt(4)\nv_mfma_f32_16x16x32_bf16 v[32:35], v[108:111], v[60:63], v[32:35]\nds_read_b128 v[56:59], v2 offset:47104\nv_mfma_f32_16x16x32_bf16 v[36:39], v[116:119], v[60:63], v[36:39]\nv_mfma_f32_16x16x32_bf16 v[40:43], v[68:71], v[60:63], v[40:43]\nv_mfma_f32_16x16x32_bf16 v[44:47], v[124:127], v[60:63], v[44:47]\ns_waitcnt lgkmcnt(3)\nv_mfma_f32_16x16x32_bf16 v[16:19], v[128:131], v[48:51], v[16:19]\n" \
"ds_read_b128 v[60:63], v2 offset:47360\nv_mfma_f32_16x16x32_bf16 v[20:23], v[136:139], v[48:51], v[20:23]\nv_mfma_f32_16x16x32_bf16 v[24:27], v[144:147], v[48:51], v[24:27]\nv_mfma_f32_16x16x32_bf16 v[28:31], v[72:75], v[48:51], v[28:31]\ns_waitcnt lgkmcnt(3)\nv_mfma_f32_16x16x32_bf16 v[32:35], v[128:131], v[52:55], v[32:35]\ns_waitcnt lgkmcnt(0)\ns_waitcnt vmcnt(0)\n" \
"s_barrier\nds_read_b128 v[48:51], v2 offset:0\nv_mfma_f32_16x16x32_bf16 v[36:39], v[136:139], v[52:55], v[36:39]\nv_mfma_f32_16x16x32_bf16 v[40:43], v[144:147], v[52:55], v[40:43]\nv_mfma_f32_16x16x32_bf16 v[44:47], v[72:75], v[52:55], v[44:47]\nv_mfma_f32_16x16x32_bf16 v[16:19], v[132:135], v[56:59], v[16:19]\nds_read_b128 v[52:55], v2 offset:256\nv_mfma_f32_16x16x32_bf16 v[20:23], v[140:143], v[56:59], v[20:23]\n" \
"v_mfma_f32_16x16x32_bf16 v[24:27], v[148:151], v[56:59], v[24:27]\nv_mfma_f32_16x16x32_bf16 v[28:31], v[76:79], v[56:59], v[28:31]\nv_mfma_f32_16x16x32_bf16 v[32:35], v[132:135], v[60:63], v[32:35]\nds_read_b128 v[56:59], v2 offset:2048\nv_mfma_f32_16x16x32_bf16 v[36:39], v[140:143], v[60:63], v[36:39]\nv_mfma_f32_16x16x32_bf16 v[40:43], v[148:151], v[60:63], v[40:43]\nv_mfma_f32_16x16x32_bf16 v[44:47], v[76:79], v[60:63], v[44:47]\ns_waitcnt lgkmcnt(2)\n" \
"v_mfma_f32_16x16x32_bf16 v[16:19], v[152:155], v[48:51], v[16:19]\nds_read_b128 v[60:63], v2 offset:2304\nv_mfma_f32_16x16x32_bf16 v[20:23], v[160:163], v[48:51], v[20:23]\nds_read_b128 v[72:75], v1 offset:18432\nv_mfma_f32_16x16x32_bf16 v[24:27], v[168:171], v[48:51], v[24:27]\nv_mfma_f32_16x16x32_bf16 v[28:31], v[176:179], v[48:51], v[28:31]\ns_waitcnt lgkmcnt(3)\nv_mfma_f32_16x16x32_bf16 v[32:35], v[152:155], v[52:55], v[32:35]\n" \
"ds_read_b128 v[48:51], v2 offset:4096\nv_mfma_f32_16x16x32_bf16 v[36:39], v[160:163], v[52:55], v[36:39]\nv_mfma_f32_16x16x32_bf16 v[40:43], v[168:171], v[52:55], v[40:43]\nv_mfma_f32_16x16x32_bf16 v[44:47], v[176:179], v[52:55], v[44:47]\ns_waitcnt lgkmcnt(3)\nv_mfma_f32_16x16x32_bf16 v[16:19], v[156:159], v[56:59], v[16:19]\nds_read_b128 v[52:55], v2 offset:4352\nv_mfma_f32_16x16x32_bf16 v[20:23], v[164:167], v[56:59], v[20:23]\n" \
"ds_read_b128 v[76:79], v1 offset:19456\nv_mfma_f32_16x16x32_bf16 v[24:27], v[172:175], v[56:59], v[24:27]\nv_mfma_f32_16x16x32_bf16 v[28:31], v[180:183], v[56:59], v[28:31]\ns_waitcnt lgkmcnt(4)\nv_mfma_f32_16x16x32_bf16 v[32:35], v[156:159], v[60:63], v[32:35]\nds_read_b128 v[56:59], v2 offset:6144\nv_mfma_f32_16x16x32_bf16 v[36:39], v[164:167], v[60:63], v[36:39]\nv_mfma_f32_16x16x32_bf16 v[40:43], v[172:175], v[60:63], v[40:43]\n" \
"v_mfma_f32_16x16x32_bf16 v[44:47], v[180:183], v[60:63], v[44:47]\ns_waitcnt lgkmcnt(3)\nv_mfma_f32_16x16x32_bf16 v[16:19], v[184:187], v[48:51], v[16:19]\nds_read_b128 v[60:63], v2 offset:6400\nv_mfma_f32_16x16x32_bf16 v[20:23], v[72:75], v[48:51], v[20:23]\nds_read_b128 v[64:67], v1 offset:20480\nv_mfma_f32_16x16x32_bf16 v[24:27], v[192:195], v[48:51], v[24:27]\nv_mfma_f32_16x16x32_bf16 v[28:31], v[200:203], v[48:51], v[28:31]\n" \
"s_waitcnt lgkmcnt(4)\nv_mfma_f32_16x16x32_bf16 v[32:35], v[184:187], v[52:55], v[32:35]\nds_read_b128 v[48:51], v2 offset:8192\nv_mfma_f32_16x16x32_bf16 v[36:39], v[72:75], v[52:55], v[36:39]\nv_mfma_f32_16x16x32_bf16 v[40:43], v[192:195], v[52:55], v[40:43]\nv_mfma_f32_16x16x32_bf16 v[44:47], v[200:203], v[52:55], v[44:47]\ns_waitcnt lgkmcnt(3)\nv_mfma_f32_16x16x32_bf16 v[16:19], v[188:191], v[56:59], v[16:19]\n" \
"ds_read_b128 v[52:55], v2 offset:10240\nv_mfma_f32_16x16x32_bf16 v[20:23], v[76:79], v[56:59], v[20:23]\nds_read_b128 v[68:71], v1 offset:21504\nv_mfma_f32_16x16x32_bf16 v[24:27], v[196:199], v[56:59], v[24:27]\nbuffer_load_dwordx4 v[80:83], v3, %[rs], s42 offen offset:0 sc1\nv_mfma_f32_16x16x32_bf16 v[28:31], v[204:207], v[56:59], v[28:31]\ns_waitcnt lgkmcnt(4)\nv_mfma_f32_16x16x32_bf16 v[32:35], v[188:191], v[60:63], v[32:35]\n" \
"ds_read_b128 v[56:59], v2 offset:12288\nv_mfma_f32_16x16x32_bf16 v[36:39], v[76:79], v[60:63], v[36:39]\nds_read_b128 v[72:75], v1 offset:22528\nv_mfma_f32_16x16x32_bf16 v[40:43], v[196:199], v[60:63], v[40:43]\nbuffer_load_dwordx4 v[84:87], v3, %[rs], s42 offen offset:1024 sc1\nv_mfma_f32_16x16x32_bf16 v[44:47], v[204:207], v[60:63], v[44:47]\ns_waitcnt lgkmcnt(4)\nv_mfma_f32_16x16x32_bf16 v[16:19], v[208:211], v[48:51], v[16:19]\n" \
"ds_read_b128 v[60:63], v2 offset:14336\nv_mfma_f32_16x16x32_bf16 v[20:23], v[216:219], v[48:51], v[20:23]\nds_read_b128 v[76:79], v1 offset:23552\nv_mfma_f32_16x16x32_bf16 v[24:27], v[64:67], v[48:51], v[24:27]\nbuffer_load_dwordx4 v[88:91], v3, %[rs], s42 offen offset:2048 sc1\nv_mfma_f32_16x16x32_bf16 v[28:31], v[224:227], v[48:51], v[28:31]\ns_waitcnt lgkmcnt(5)\nv_mfma_f32_16x16x32_bf16 v[16:19], v[212:215], v[52:55], v[16:19]\n" \
"ds_read_b128 v[48:51], v2 offset:8448\nv_mfma_f32_16x16x32_bf16 v[20:23], v[220:223], v[52:55], v[20:23]\ns_waitcnt lgkmcnt(5)\nv_mfma_f32_16x16x32_bf16 v[24:27], v[68:71], v[52:55], v[24:27]\nbuffer_load_dwordx4 v[92:95], v3, %[rs], s42 offen offset:3072 sc1\nv_mfma_f32_16x16x32_bf16 v[28:31], v[228:231], v[52:55], v[28:31]\ns_waitcnt lgkmcnt(4)\nv_mfma_f32_16x16x32_bf16 v[16:19], v[232:235], v[56:59], v[16:19]\n" \
"ds_read_b128 v[52:55], v2 offset:10496\nv_mfma_f32_16x16x32_bf16 v[20:23], v[240:243], v[56:59], v[20:23]\nv_mfma_f32_16x16x32_bf16 v[24:27], v[248:251], v[56:59], v[24:27]\ns_waitcnt lgkmcnt(4)\nv_mfma_f32_16x16x32_bf16 v[28:31], v[72:75], v[56:59], v[28:31]\ns_waitcnt lgkmcnt(3)\nv_mfma_f32_16x16x32_bf16 v[16:19], v[236:239], v[60:63], v[16:19]\nds_read_b128 v[56:59], v2 offset:12544\n" \
"v_mfma_f32_16x16x32_bf16 v[20:23], v[244:247], v[60:63], v[20:23]\nv_mfma_f32_16x16x32_bf16 v[24:27], v[252:255], v[60:63], v[24:27]\ns_waitcnt lgkmcnt(3)\nv_mfma_f32_16x16x32_bf16 v[28:31], v[76:79], v[60:63], v[28:31]\ns_waitcnt lgkmcnt(2)\nv_mfma_f32_16x16x32_bf16 v[32:35], v[208:211], v[48:51], v[32:35]\nds_read_b128 v[60:63], v2 offset:14592\nv_mfma_f32_16x16x32_bf16 v[36:39], v[216:219], v[48:51], v[36:39]\n" \
"v_cvt_pk_bf16_f32 v20, v16, v20\nv_cvt_pk_bf16_f32 v21, v17, v21\nv_mfma_f32_16x16x32_bf16 v[40:43], v[64:67], v[48:51], v[40:43]\nv_cvt_pk_bf16_f32 v22, v18, v22\nv_cvt_pk_bf16_f32 v23, v19, v23\nv_mfma_f32_16x16x32_bf16 v[44:47], v[224:227], v[48:51], v[44:47]\nds_write_b128 v5, v[20:23] offset:0\ns_waitcnt lgkmcnt(3)\n" \
"v_mfma_f32_16x16x32_bf16 v[32:35], v[212:215], v[52:55], v[32:35]\nv_cvt_pk_bf16_f32 v28, v24, v28\nv_cvt_pk_bf16_f32 v29, v25, v29\nv_mfma_f32_16x16x32_bf16 v[36:39], v[220:223], v[52:55], v[36:39]\nv_cvt_pk_bf16_f32 v30, v26, v30\nv_cvt_pk_bf16_f32 v31, v27, v31\nv_mfma_f32_16x16x32_bf16 v[40:43], v[68:71], v[52:55], v[40:43]\nv_mov_b64 v[48:49], 0\n" \
"v_mov_b64 v[50:51], 0\nv_mfma_f32_16x16x32_bf16 v[44:47], v[228:231], v[52:55], v[44:47]\nds_write_b128 v5, v[28:31] offset:512\ns_waitcnt lgkmcnt(3)\nv_mfma_f32_16x16x32_bf16 v[32:35], v[232:235], v[56:59], v[32:35]\nv_mov_b64 v[52:53], 0\nv_mov_b64 v[54:55], 0\nv_mfma_f32_16x16x32_bf16 v[36:39], v[240:243], v[56:59], v[36:39]\n" \
"s_waitcnt lgkmcnt(2)\nv_mfma_f32_16x16x32_bf16 v[32:35], v[236:239], v[60:63], v[32:35]\nv_mfma_f32_16x16x32_bf16 v[36:39], v[244:247], v[60:63], v[36:39]\nv_mfma_f32_16x16x32_bf16 v[40:43], v[248:251], v[56:59], v[40:43]\nv_mfma_f32_16x16x32_bf16 v[44:47], v[72:75], v[56:59], v[44:47]\nv_cvt_pk_bf16_f32 v36, v32, v36\nv_cvt_pk_bf16_f32 v37, v33, v37\nv_mfma_f32_16x16x32_bf16 v[40:43], v[252:255], v[60:63], v[40:43]\n" \
"v_cvt_pk_bf16_f32 v38, v34, v38\nv_cvt_pk_bf16_f32 v39, v35, v39\nv_mfma_f32_16x16x32_bf16 v[44:47], v[76:79], v[60:63], v[44:47]\nds_write_b128 v5, v[36:39] offset:256\nv_mov_b64 v[56:57], 0\nv_mov_b64 v[58:59], 0\nv_mov_b64 v[60:61], 0\nv_mov_b64 v[62:63], 0\n" \
"s_nop 2\nv_cvt_pk_bf16_f32 v44, v40, v44\nv_cvt_pk_bf16_f32 v45, v41, v45\nv_cvt_pk_bf16_f32 v46, v42, v46\nv_cvt_pk_bf16_f32 v47, v43, v47\nds_write_b128 v5, v[44:47] offset:768\ns_cmp_eq_u32 %[w], 1\ns_cbranch_scc1 .Lqk_c1%=\n" \
"s_cmp_eq_u32 %[w], 2\ns_cbranch_scc1 .Lqk_c2%=\ns_cmp_eq_u32 %[w], 3\ns_cbranch_scc1 .Lqk_c3%=\n.Lqk_c0%=:\nds_read_b128 v[64:67], v1 offset:0\nds_read_b128 v[68:71], v1 offset:1024\nds_read_b128 v[72:75], v1 offset:2048\n" \
"ds_read_b128 v[76:79], v1 offset:3072\ns_bfe_i32 s54, s40, 0x10000\ns_and_b32 s44, s40, 1\ns_lshl_b32 s55, s44, 17\ns_lshl_b32 s45, s44, 12\n.Lqk_poll0%=:\ns_waitcnt vmcnt(0)\nv_smfmac_f32_16x16x64_bf16 v[48:51], v[80:83], a[0:7], v4\n" \
"ds_write_b128 v0, v[80:83] offset:16384\nv_max3_u32 v40, v80, v82, v84\nv_smfmac_f32_16x16x64_bf16 v[52:55], v[80:83], a[8:15], v4\nds_write_b128 v0, v[84:87] offset:17408\nv_max3_u32 v40, v40, v86, v88\nv_smfmac_f32_16x16x64_bf16 v[56:59], v[80:83], a[16:23], v4\nds_write_b128 v0, v[88:91] offset:18432\nv_max3_u32 v40, v40, v90, v92\n" \
"v_smfmac_f32_16x16x64_bf16 v[60:63], v[80:83], a[24:31], v4\nds_write_b128 v0, v[92:95] offset:19456\nv_max_u32 v40, v40, v94\nv_smfmac_f32_16x16x64_bf16 v[48:51], v[84:87], a[32:39], v4\nv_cmp_eq_u32 vcc, -1, v40\ns_waitcnt lgkmcnt(6)\nv_smfmac_f32_16x16x64_bf16 v[52:55], v[84:87], v[64:71], v4\ns_cbranch_vccnz .Lqk_re0%=\n" \
"v_smfmac_f32_16x16x64_bf16 v[56:59], v[84:87], a[40:47], v4\ns_waitcnt lgkmcnt(0)\ns_barrier\nds_read_b128 v[80:83], v0 offset:20480\nv_smfmac_f32_16x16x64_bf16 v[60:63], v[84:87], a[48:55], v4\nds_read_b128 v[84:87], v0 offset:21504\nv_smfmac_f32_16x16x64_bf16 v[48:51], v[88:91], a[56:63], v4\nds_read_b128 v[64:67], v1 offset:4096\n" \
"ds_read_b128 v[68:71], v1 offset:5120\nv_smfmac_f32_16x16x64_bf16 v[52:55], v[88:91], a[64:71], v4\nv_smfmac_f32_16x16x64_bf16 v[56:59], v[88:91], v[72:79], v4\nds_read_b128 v[72:75], v1 offset:6144\nds_read_b128 v[76:79], v1 offset:7168\nv_smfmac_f32_16x16x64_bf16 v[60:63], v[88:91], a[72:79], v4\nds_read_b128 v[88:91], v0 offset:22528\nv_smfmac_f32_16x16x64_bf16 v[48:51], v[92:95], a[80:87], v4\n" \
"ds_read_b128 v[16:19], v7 offset:0\nv_smfmac_f32_16x16x64_bf16 v[52:55], v[92:95], a[88:95], v4\nv_smfmac_f32_16x16x64_bf16 v[56:59], v[92:95], a[96:103], v4\ns_waitcnt lgkmcnt(4)\nv_smfmac_f32_16x16x64_bf16 v[60:63], v[92:95], v[64:71], v4\nds_read_b128 v[92:95], v0 offset:23552\nds_read_b128 v[64:67], v1 offset:8192\nds_read_b128 v[68:71], v1 offset:9216\n" \
"v_smfmac_f32_16x16x64_bf16 v[48:51], v[80:83], a[104:111], v4\nds_read_b128 v[20:23], v7 offset:64\nv_smfmac_f32_16x16x64_bf16 v[52:55], v[80:83], a[112:119], v4\nv_smfmac_f32_16x16x64_bf16 v[56:59], v[80:83], a[120:127], v4\nv_smfmac_f32_16x16x64_bf16 v[60:63], v[80:83], a[128:135], v4\nds_read_b128 v[80:83], v0 offset:24576\nv_smfmac_f32_16x16x64_bf16 v[48:51], v[84:87], a[136:143], v4\nds_read_b128 v[24:27], v7 offset:2048\n" \
"s_waitcnt lgkmcnt(8)\nv_smfmac_f32_16x16x64_bf16 v[52:55], v[84:87], v[72:79], v4\nds_read_b128 v[72:75], v1 offset:10240\nds_read_b128 v[76:79], v1 offset:11264\nv_smfmac_f32_16x16x64_bf16 v[56:59], v[84:87], a[144:151], v4\nv_smfmac_f32_16x16x64_bf16 v[60:63], v[84:87], a[152:159], v4\nds_read_b128 v[84:87], v0 offset:25600\ns_waitcnt lgkmcnt(10)\n" \
"v_smfmac_f32_16x16x64_bf16 v[48:51], v[88:91], a[160:167], v4\nds_read_b128 v[28:31], v7 offset:2112\nv_smfmac_f32_16x16x64_bf16 v[52:55], v[88:91], a[168:175], v4\ns_waitcnt lgkmcnt(7)\nv_smfmac_f32_16x16x64_bf16 v[56:59], v[88:91], v[64:71], v4\nds_read_b128 v[64:67], v1 offset:12288\nds_read_b128 v[68:71], v1 offset:13312\nv_smfmac_f32_16x16x64_bf16 v[60:63], v[88:91], a[176:183], v4\n" \
"ds_read_b128 v[88:91], v0 offset:26624\nv_smfmac_f32_16x16x64_bf16 v[48:51], v[92:95], a[184:191], v4\nds_read_b128 v[32:35], v7 offset:4096\nv_smfmac_f32_16x16x64_bf16 v[52:55], v[92:95], a[192:199], v4\nv_smfmac_f32_16x16x64_bf16 v[56:59], v[92:95], a[200:207], v4\ns_waitcnt lgkmcnt(6)\nv_smfmac_f32_16x16x64_bf16 v[60:63], v[92:95], v[72:79], v4\nds_read_b128 v[92:95], v0 offset:27648\n" \
"ds_read_b128 v[72:75], v1 offset:14336\nds_read_b128 v[76:79], v1 offset:15360\nv_smfmac_f32_16x16x64_bf16 v[48:51], v[80:83], a[208:215], v4\nds_read_b128 v[36:39], v7 offset:4160\nv_smfmac_f32_16x16x64_bf16 v[52:55], v[80:83], a[216:223], v4\nv_smfmac_f32_16x16x64_bf16 v[56:59], v[80:83], a[224:231], v4\nv_smfmac_f32_16x16x64_bf16 v[60:63], v[80:83], a[232:239], v4\nds_read_b128 v[80:83], v0 offset:28672\n" \
"s_waitcnt lgkmcnt(10)\nv_smfmac_f32_16x16x64_bf16 v[48:51], v[84:87], a[240:247], v4\nds_read_b128 v[40:43], v7 offset:6144\ns_waitcnt lgkmcnt(8)\nv_smfmac_f32_16x16x64_bf16 v[52:55], v[84:87], v[64:71], v4\nds_read_b128 v[64:67], v1 offset:16384\nds_read_b128 v[68:71], v1 offset:17408\nv_smfmac_f32_16x16x64_bf16 v[56:59], v[84:87], a[248:255], v4\n" \
"v_smfmac_f32_16x16x64_bf16 v[60:63], v[84:87], v[96:103], v4\nds_read_b128 v[84:87], v0 offset:29696\ns_waitcnt lgkmcnt(10)\nv_smfmac_f32_16x16x64_bf16 v[48:51], v[88:91], v[104:111], v4\nds_read_b128 v[44:47], v7 offset:6208\nv_smfmac_f32_16x16x64_bf16 v[52:55], v[88:91], v[112:119], v4\ns_waitcnt lgkmcnt(7)\nv_smfmac_f32_16x16x64_bf16 v[56:59], v[88:91], v[72:79], v4\n" \
"ds_read_b128 v[72:75], v1 offset:18432\nds_read_b128 v[76:79], v1 offset:19456\nv_smfmac_f32_16x16x64_bf16 v[60:63], v[88:91], v[120:127], v4\nds_read_b128 v[88:91], v0 offset:30720\nv_smfmac_f32_16x16x64_bf16 v[48:51], v[92:95], v[128:135], v4\nv_smfmac_f32_16x16x64_bf16 v[52:55], v[92:95], v[136:143], v4\nv_smfmac_f32_16x16x64_bf16 v[56:59], v[92:95], v[144:151], v4\ns_waitcnt lgkmcnt(5)\n" \
"v_smfmac_f32_16x16x64_bf16 v[60:63], v[92:95], v[64:71], v4\nds_read_b128 v[92:95], v0 offset:31744\nds_read_b128 v[64:67], v1 offset:20480\nds_read_b128 v[68:71], v1 offset:21504\nv_smfmac_f32_16x16x64_bf16 v[48:51], v[80:83], v[152:159], v4\nv_smfmac_f32_16x16x64_bf16 v[52:55], v[80:83], v[160:167], v4\nv_smfmac_f32_16x16x64_bf16 v[56:59], v[80:83], v[168:175], v4\nv_smfmac_f32_16x16x64_bf16 v[60:63], v[80:83], v[176:183], v4\n" \
"s_waitcnt lgkmcnt(7)\nv_smfmac_f32_16x16x64_bf16 v[48:51], v[84:87], v[184:191], v4\ns_waitcnt lgkmcnt(4)\nv_smfmac_f32_16x16x64_bf16 v[52:55], v[84:87], v[72:79], v4\nds_read_b128 v[72:75], v1 offset:22528\nds_read_b128 v[76:79], v1 offset:23552\nv_smfmac_f32_16x16x64_bf16 v[56:59], v[84:87], v[192:199], v4\nv_smfmac_f32_16x16x64_bf16 v[60:63], v[84:87], v[200:207], v4\n" \
"s_waitcnt lgkmcnt(5)\nv_smfmac_f32_16x16x64_bf16 v[48:51], v[88:91], v[208:215], v4\nv_smfmac_f32_16x16x64_bf16 v[52:55], v[88:91], v[216:223], v4\ns_waitcnt lgkmcnt(4)\nv_smfmac_f32_16x16x64_bf16 v[48:51], v[92:95], v[232:239], v4\nv_smfmac_f32_16x16x64_bf16 v[52:55], v[92:95], v[240:247], v4\ns_waitcnt lgkmcnt(2)\nv_smfmac_f32_16x16x64_bf16 v[56:59], v[88:91], v[64:71], v4\n" \
"v_add_f32 v48, v48, v50\nv_smfmac_f32_16x16x64_bf16 v[60:63], v[88:91], v[224:231], v4\nv_add_f32 v52, v52, v54\nv_add_f32 v49, v49, v51\nv_smfmac_f32_16x16x64_bf16 v[56:59], v[92:95], v[248:255], v4\nv_add_f32 v53, v53, v55\nv_cvt_pk_bf16_f32 v50, v48, v52\ns_waitcnt lgkmcnt(0)\n" \
"v_smfmac_f32_16x16x64_bf16 v[60:63], v[92:95], v[72:79], v4\nv_cvt_pk_bf16_f32 v51, v49, v53\nds_write2_b32 v6, v50, v51 offset0:0 offset1:4\ns_nop 1\nv_add_f32 v56, v56, v58\nv_add_f32 v57, v57, v59\ns_nop 1\nv_add_f32 v60, v60, v62\n" \
"v_add_f32 v61, v61, v63\nv_cvt_pk_bf16_f32 v54, v56, v60\nv_cvt_pk_bf16_f32 v55, v57, v61\nds_write2_b32 v6, v54, v55 offset0:36 offset1:40\nv_mov_b32 v63, s54\ns_waitcnt lgkmcnt(0)\ns_barrier\nds_read_b128 v[80:83], v8 offset:0\n" \
"ds_read_b128 v[84:87], v8 offset:576\nds_read_b128 v[88:91], v8 offset:1152\nds_read_b128 v[92:95], v8 offset:1728\nds_read_b128 v[64:67], v9 offset:0\nds_read_b128 v[68:71], v9 offset:576\nds_read_b128 v[72:75], v9 offset:1152\nds_read_b128 v[76:79], v9 offset:1728\ns_waitcnt lgkmcnt(7)\n" \
"v_mfma_f32_16x16x32_bf16 v[52:55], v[16:19], v[80:83], 0\nv_mfma_f32_16x16x32_bf16 v[56:59], v[20:23], v[80:83], 0\ns_waitcnt lgkmcnt(6)\nv_mfma_f32_16x16x32_bf16 v[52:55], v[24:27], v[84:87], v[52:55]\nv_mfma_f32_16x16x32_bf16 v[56:59], v[28:31], v[84:87], v[56:59]\ns_waitcnt lgkmcnt(5)\nv_mfma_f32_16x16x32_bf16 v[52:55], v[32:35], v[88:91], v[52:55]\nv_mfma_f32_16x16x32_bf16 v[56:59], v[36:39], v[88:91], v[56:59]\n" \
"s_waitcnt lgkmcnt(4)\nv_mfma_f32_16x16x32_bf16 v[52:55], v[40:43], v[92:95], v[52:55]\nv_mfma_f32_16x16x32_bf16 v[56:59], v[44:47], v[92:95], v[56:59]\ns_waitcnt lgkmcnt(3)\nv_mfma_f32_16x16x32_bf16 v[48:51], v[64:67], v[80:83], 0\ns_waitcnt lgkmcnt(2)\nv_mfma_f32_16x16x32_bf16 v[48:51], v[68:71], v[84:87], v[48:51]\ns_waitcnt lgkmcnt(1)\n" \
"v_mfma_f32_16x16x32_bf16 v[48:51], v[72:75], v[88:91], v[48:51]\ns_waitcnt lgkmcnt(0)\nv_mfma_f32_16x16x32_bf16 v[48:51], v[76:79], v[92:95], v[48:51]\nv_max3_f32 v16, v52, v53, v54\nv_max3_f32 v17, v55, v56, v57\nv_max3_f32 v16, v16, v58, v59\nv_cndmask_b32 v52, v52, v56, s[62:63]\nv_cndmask_b32 v53, v53, v57, s[62:63]\n" \
"v_cndmask_b32 v54, v54, v58, s[62:63]\nv_cndmask_b32 v55, v55, v59, s[62:63]\nv_max_f32 v16, v16, v17\nv_max3_f32 v17, v16, v48, v49\nv_max3_f32 v16, v16, v48, v49\ns_nop 1\nv_permlane16_swap_b32 v16, v17\nv_max_f32 v18, v16, v17\n" \
"v_max_f32 v16, v16, v17\ns_nop 1\nv_permlane32_swap_b32 v16, v18\nv_max_f32 v16, v16, v18\nv_mul_f32 v16, s59, v16\nv_fma_f32 v52, v52, s58, v16\nv_fma_f32 v53, v53, s58, v16\nv_exp_f32 v52, v52\n" \
"v_fma_f32 v54, v54, s58, v16\nv_exp_f32 v53, v53\nv_fma_f32 v55, v55, s58, v16\nv_exp_f32 v54, v54\nv_add_f32 v17, v52, v53\nv_fma_f32 v48, v48, s58, v16\nv_exp_f32 v55, v55\nv_add_f32 v17, v17, v54\n" \
"v_fma_f32 v49, v49, s58, v16\nv_exp_f32 v48, v48\nv_add_f32 v17, v17, v55\nv_exp_f32 v49, v49\nv_add_f32 v18, v48, v17\nv_add_f32 v18, v18, v49\ns_nop 1\nv_add_f32_dpp v16, v17, v18 row_ror:8 row_mask:0xf bank_mask:0xf\n" \
"v_add_f32_dpp v18, v17, v18 row_ror:8 row_mask:0xf bank_mask:0xf\ns_nop 1\nv_permlane16_swap_b32 v16, v18\nv_add_f32 v17, v16, v18\nv_add_f32 v18, v16, v18\ns_nop 1\nv_permlane32_swap_b32 v17, v18\nv_add_f32 v17, v17, v18\n" \
"v_rcp_f32 v17, v17\ns_nop 0\nv_mul_f32 v52, v52, v17\nv_mul_f32 v53, v53, v17\nv_mul_f32 v54, v54, v17\nv_mul_f32 v55, v55, v17\nv_mul_f32 v48, v48, v17\nv_mul_f32 v49, v49, v17\n" \
"v_cvt_pk_bf16_f32 v52, v52, v53\nv_cvt_pk_bf16_f32 v53, v54, v55\nv_cvt_pk_bf16_f32 v48, v48, v49\nv_bfi_b32 v52, s61, v52, v63\nv_bfi_b32 v53, s61, v53, v63\nbuffer_store_dwordx2 v[52:53], v10, %[rs], 0 offen sc1\nv_bfi_b32 v48, s61, v48, v63\nbuffer_store_dword v48, v11, %[rs], 0 offen sc1\n" \
"s_cmp_lt_i32 s40, 63\ns_cbranch_scc1 .Lqk_proj%=\ns_branch .Lqk_end%=\n.Lqk_re0%=:\nv_max_u32 v40, v80, v82\nv_cmp_eq_u32 s[46:47], -1, v40\nv_max_u32 v40, v84, v86\nv_cmp_eq_u32 s[48:49], -1, v40\n" \
"v_max_u32 v40, v88, v90\nv_cmp_eq_u32 s[50:51], -1, v40\nv_max_u32 v40, v92, v94\nv_cmp_eq_u32 s[52:53], -1, v40\ns_mov_b64 exec, s[46:47]\nbuffer_load_dwordx4 v[80:83], v3, %[rs], s42 offen offset:0 sc1\ns_mov_b64 exec, s[48:49]\nbuffer_load_dwordx4 v[84:87], v3, %[rs], s42 offen offset:1024 sc1\n" \
"s_mov_b64 exec, s[50:51]\nbuffer_load_dwordx4 v[88:91], v3, %[rs], s42 offen offset:2048 sc1\ns_mov_b64 exec, s[52:53]\nbuffer_load_dwordx4 v[92:95], v3, %[rs], s42 offen offset:3072 sc1\ns_mov_b64 exec, -1\nv_mov_b64 v[48:49], 0\nv_mov_b64 v[50:51], 0\nv_mov_b64 v[52:53], 0\n" \
"v_mov_b64 v[54:55], 0\nv_mov_b64 v[56:57], 0\nv_mov_b64 v[58:59], 0\nv_mov_b64 v[60:61], 0\nv_mov_b64 v[62:63], 0\ns_add_u32 s43, s43, 1\ns_cmp_lt_u32 s43, 262144\ns_cbranch_scc1 .Lqk_poll0%=\n" \
"s_branch .Lqk_abort%=\n.Lqk_c1%=:\nds_read_b128 v[64:67], v1 offset:6144\nds_read_b128 v[68:71], v1 offset:7168\nds_read_b128 v[72:75], v1 offset:8192\nds_read_b128 v[76:79], v1 offset:9216\ns_bfe_i32 s54, s40, 0x10001\ns_and_b32 s44, s40, 1\n" \
"s_lshl_b32 s55, s44, 17\ns_lshl_b32 s45, s44, 12\n.Lqk_poll1%=:\ns_waitcnt vmcnt(0)\nv_smfmac_f32_16x16x64_bf16 v[48:51], v[80:83], a[104:111], v4\nds_write_b128 v0, v[80:83] offset:20480\nv_max3_u32 v40, v80, v82, v84\nv_smfmac_f32_16x16x64_bf16 v[52:55], v[80:83], a[112:119], v4\n" \
"ds_write_b128 v0, v[84:87] offset:21504\nv_max3_u32 v40, v40, v86, v88\nv_smfmac_f32_16x16x64_bf16 v[56:59], v[80:83], a[120:127], v4\nds_write_b128 v0, v[88:91] offset:22528\nv_max3_u32 v40, v40, v90, v92\nv_smfmac_f32_16x16x64_bf16 v[60:63], v[80:83], a[128:135], v4\nds_write_b128 v0, v[92:95] offset:23552\nv_max_u32 v40, v40, v94\n" \
"v_smfmac_f32_16x16x64_bf16 v[48:51], v[84:87], a[136:143], v4\nv_cmp_eq_u32 vcc, -1, v40\ns_waitcnt lgkmcnt(6)\nv_smfmac_f32_16x16x64_bf16 v[52:55], v[84:87], v[64:71], v4\ns_cbranch_vccnz .Lqk_re1%=\nv_smfmac_f32_16x16x64_bf16 v[56:59], v[84:87], a[144:151], v4\ns_waitcnt lgkmcnt(0)\ns_barrier\n" \
"ds_read_b128 v[80:83], v0 offset:24576\nv_smfmac_f32_16x16x64_bf16 v[60:63], v[84:87], a[152:159], v4\nds_read_b128 v[84:87], v0 offset:25600\nv_smfmac_f32_16x16x64_bf16 v[48:51], v[88:91], a[160:167], v4\nds_read_b128 v[64:67], v1 offset:10240\nds_read_b128 v[68:71], v1 offset:11264\nv_smfmac_f32_16x16x64_bf16 v[52:55], v[88:91], a[168:175], v4\nv_smfmac_f32_16x16x64_bf16 v[56:59], v[88:91], v[72:79], v4\n" \
"ds_read_b128 v[72:75], v1 offset:12288\nds_read_b128 v[76:79], v1 offset:13312\nv_smfmac_f32_16x16x64_bf16 v[60:63], v[88:91], a[176:183], v4\nds_read_b128 v[88:91], v0 offset:26624\nv_smfmac_f32_16x16x64_bf16 v[48:51], v[92:95], a[184:191], v4\nds_read_b128 v[16:19], v7 offset:0\nv_smfmac_f32_16x16x64_bf16 v[52:55], v[92:95], a[192:199], v4\nv_smfmac_f32_16x16x64_bf16 v[56:59], v[92:95], a[200:207], v4\n" \
"s_waitcnt lgkmcnt(4)\nv_smfmac_f32_16x16x64_bf16 v[60:63], v[92:95], v[64:71], v4\nds_read_b128 v[92:95], v0 offset:27648\nds_read_b128 v[64:67], v1 offset:14336\nds_read_b128 v[68:71], v1 offset:15360\nv_smfmac_f32_16x16x64_bf16 v[48:51], v[80:83], a[208:215], v4\nds_read_b128 v[20:23], v7 offset:2048\nv_smfmac_f32_16x16x64_bf16 v[52:55], v[80:83], a[216:223], v4\n" \
"v_smfmac_f32_16x16x64_bf16 v[56:59], v[80:83], a[224:231], v4\nv_smfmac_f32_16x16x64_bf16 v[60:63], v[80:83], a[232:239], v4\nds_read_b128 v[80:83], v0 offset:28672\nv_smfmac_f32_16x16x64_bf16 v[48:51], v[84:87], a[240:247], v4\nds_read_b128 v[24:27], v7 offset:4096\ns_waitcnt lgkmcnt(8)\nv_smfmac_f32_16x16x64_bf16 v[52:55], v[84:87], v[72:79], v4\nds_read_b128 v[72:75], v1 offset:16384\n" \
"ds_read_b128 v[76:79], v1 offset:17408\nv_smfmac_f32_16x16x64_bf16 v[56:59], v[84:87], a[248:255], v4\nv_smfmac_f32_16x16x64_bf16 v[60:63], v[84:87], v[96:103], v4\nds_read_b128 v[84:87], v0 offset:29696\ns_waitcnt lgkmcnt(10)\nv_smfmac_f32_16x16x64_bf16 v[48:51], v[88:91], v[104:111], v4\nds_read_b128 v[28:31], v7 offset:6144\nv_smfmac_f32_16x16x64_bf16 v[52:55], v[88:91], v[112:119], v4\n" \
"s_waitcnt lgkmcnt(7)\nv_smfmac_f32_16x16x64_bf16 v[56:59], v[88:91], v[64:71], v4\nds_read_b128 v[64:67], v1 offset:18432\nds_read_b128 v[68:71], v1 offset:19456\nv_smfmac_f32_16x16x64_bf16 v[60:63], v[88:91], v[120:127], v4\nds_read_b128 v[88:91], v0 offset:30720\nv_smfmac_f32_16x16x64_bf16 v[48:51], v[92:95], v[128:135], v4\nds_read_b128 v[32:35], v7 offset:8192\n" \
"v_smfmac_f32_16x16x64_bf16 v[52:55], v[92:95], v[136:143], v4\nv_smfmac_f32_16x16x64_bf16 v[56:59], v[92:95], v[144:151], v4\ns_waitcnt lgkmcnt(6)\nv_smfmac_f32_16x16x64_bf16 v[60:63], v[92:95], v[72:79], v4\nds_read_b128 v[92:95], v0 offset:31744\nds_read_b128 v[72:75], v1 offset:20480\nds_read_b128 v[76:79], v1 offset:21504\nv_smfmac_f32_16x16x64_bf16 v[48:51], v[80:83], v[152:159], v4\n" \
"ds_read_b128 v[36:39], v7 offset:10240\nv_smfmac_f32_16x16x64_bf16 v[52:55], v[80:83], v[160:167], v4\nv_smfmac_f32_16x16x64_bf16 v[56:59], v[80:83], v[168:175], v4\nv_smfmac_f32_16x16x64_bf16 v[60:63], v[80:83], v[176:183], v4\nds_read_b128 v[80:83], v0 offset:16384\ns_waitcnt lgkmcnt(10)\nv_smfmac_f32_16x16x64_bf16 v[48:51], v[84:87], v[184:191], v4\nds_read_b128 v[40:43], v7 offset:12288\n" \
"s_waitcnt lgkmcnt(8)\nv_smfmac_f32_16x16x64_bf16 v[52:55], v[84:87], v[64:71], v4\nds_read_b128 v[64:67], v1 offset:22528\nds_read_b128 v[68:71], v1 offset:23552\nv_smfmac_f32_16x16x64_bf16 v[56:59], v[84:87], v[192:199], v4\nv_smfmac_f32_16x16x64_bf16 v[60:63], v[84:87], v[200:207], v4\nds_read_b128 v[84:87], v0 offset:17408\ns_waitcnt lgkmcnt(10)\n" \
"v_smfmac_f32_16x16x64_bf16 v[48:51], v[88:91], v[208:215], v4\nds_read_b128 v[44:47], v7 offset:14336\nv_smfmac_f32_16x16x64_bf16 v[52:55], v[88:91], v[216:223], v4\ns_waitcnt lgkmcnt(7)\nv_smfmac_f32_16x16x64_bf16 v[56:59], v[88:91], v[72:79], v4\nds_read_b128 v[72:75], v1 offset:0\nds_read_b128 v[76:79], v1 offset:1024\nv_smfmac_f32_16x16x64_bf16 v[60:63], v[88:91], v[224:231], v4\n" \
"ds_read_b128 v[88:91], v0 offset:18432\nv_smfmac_f32_16x16x64_bf16 v[48:51], v[92:95], v[232:239], v4\nv_smfmac_f32_16x16x64_bf16 v[52:55], v[92:95], v[240:247], v4\nv_smfmac_f32_16x16x64_bf16 v[56:59], v[92:95], v[248:255], v4\ns_waitcnt lgkmcnt(5)\nv_smfmac_f32_16x16x64_bf16 v[60:63], v[92:95], v[64:71], v4\nds_read_b128 v[92:95], v0 offset:19456\nds_read_b128 v[64:67], v1 offset:2048\n" \
"ds_read_b128 v[68:71], v1 offset:3072\nv_smfmac_f32_16x16x64_bf16 v[48:51], v[80:83], a[0:7], v4\nv_smfmac_f32_16x16x64_bf16 v[52:55], v[80:83], a[8:15], v4\nv_smfmac_f32_16x16x64_bf16 v[56:59], v[80:83], a[16:23], v4\nv_smfmac_f32_16x16x64_bf16 v[60:63], v[80:83], a[24:31], v4\ns_waitcnt lgkmcnt(7)\nv_smfmac_f32_16x16x64_bf16 v[48:51], v[84:87], a[32:39], v4\ns_waitcnt lgkmcnt(4)\n" \
"v_smfmac_f32_16x16x64_bf16 v[52:55], v[84:87], v[72:79], v4\nds_read_b128 v[72:75], v1 offset:4096\nds_read_b128 v[76:79], v1 offset:5120\nv_smfmac_f32_16x16x64_bf16 v[56:59], v[84:87], a[40:47], v4\nv_smfmac_f32_16x16x64_bf16 v[60:63], v[84:87], a[48:55], v4\ns_waitcnt lgkmcnt(5)\nv_smfmac_f32_16x16x64_bf16 v[48:51], v[88:91], a[56:63], v4\nv_smfmac_f32_16x16x64_bf16 v[52:55], v[88:91], a[64:71], v4\n" \
"s_waitcnt lgkmcnt(4)\nv_smfmac_f32_16x16x64_bf16 v[48:51], v[92:95], a[80:87], v4\nv_smfmac_f32_16x16x64_bf16 v[52:55], v[92:95], a[88:95], v4\ns_waitcnt lgkmcnt(2)\nv_smfmac_f32_16x16x64_bf16 v[56:59], v[88:91], v[64:71], v4\nv_add_f32 v48, v48, v50\nv_smfmac_f32_16x16x64_bf16 v[60:63], v[88:91], a[72:79], v4\nv_add_f32 v52, v52, v54\n" \
"v_add_f32 v49, v49, v51\nv_smfmac_f32_16x16x64_bf16 v[56:59], v[92:95], a[96:103], v4\nv_add_f32 v53, v53, v55\nv_cvt_pk_bf16_f32 v50, v48, v52\ns_waitcnt lgkmcnt(0)\nv_smfmac_f32_16x16x64_bf16 v[60:63], v[92:95], v[72:79], v4\nv_cvt_pk_bf16_f32 v51, v49, v53\nds_write2_b32 v6, v50, v51 offset0:0 offset1:4\n" \
"s_nop 1\nv_add_f32 v56, v56, v58\nv_add_f32 v57, v57, v59\ns_nop 1\nv_add_f32 v60, v60, v62\nv_add_f32 v61, v61, v63\nv_cvt_pk_bf16_f32 v54, v56, v60\nv_cvt_pk_bf16_f32 v55, v57, v61\n" \
"ds_write2_b32 v6, v54, v55 offset0:36 offset1:40\nv_mov_b32 v63, s54\ns_waitcnt lgkmcnt(0)\ns_barrier\nds_read_b128 v[64:67], v9 offset:0\nds_read_b128 v[68:71], v9 offset:576\nds_read_b128 v[72:75], v9 offset:1152\nds_read_b128 v[76:79], v9 offset:1728\n" \
"v_mfma_f32_16x16x32_bf16 v[52:55], v[32:35], v[16:19], v[12:15]\nv_mfma_f32_16x16x32_bf16 v[52:55], v[36:39], v[20:23], v[52:55]\nv_mfma_f32_16x16x32_bf16 v[52:55], v[40:43], v[24:27], v[52:55]\nv_mfma_f32_16x16x32_bf16 v[52:55], v[44:47], v[28:31], v[52:55]\ns_waitcnt lgkmcnt(3)\nv_mfma_f32_16x16x32_bf16 v[48:51], v[64:67], v[16:19], 0\ns_waitcnt lgkmcnt(2)\nv_mfma_f32_16x16x32_bf16 v[48:51], v[68:71], v[20:23], v[48:51]\n" \
"s_waitcnt lgkmcnt(1)\nv_mfma_f32_16x16x32_bf16 v[48:51], v[72:75], v[24:27], v[48:51]\ns_waitcnt lgkmcnt(0)\nv_mfma_f32_16x16x32_bf16 v[48:51], v[76:79], v[28:31], v[48:51]\nv_max3_f32 v32, v52, v53, v54\ns_nop 6\nv_max3_f32 v32, v32, v55, v48\nv_max_f32 v33, v32, v49\n" \
"v_max_f32 v32, v32, v49\ns_nop 1\nv_permlane16_swap_b32 v32, v33\nv_max_f32 v34, v32, v33\nv_max_f32 v32, v32, v33\ns_nop 1\nv_permlane32_swap_b32 v32, v34\nv_max_f32 v32, v32, v34\n" \
"v_mul_f32 v32, s59, v32\nv_fma_f32 v52, v52, s58, v32\nv_fma_f32 v53, v53, s58, v32\nv_exp_f32 v52, v52\nv_fma_f32 v54, v54, s58, v32\nv_exp_f32 v53, v53\nv_fma_f32 v55, v55, s58, v32\nv_exp_f32 v54, v54\n" \
"v_add_f32 v33, v52, v53\nv_fma_f32 v48, v48, s58, v32\nv_exp_f32 v55, v55\nv_add_f32 v33, v33, v54\nv_fma_f32 v49, v49, s58, v32\nv_exp_f32 v48, v48\nv_add_f32 v33, v33, v55\nv_exp_f32 v49, v49\n" \
"v_add_f32 v33, v33, v48\nv_add_f32 v32, v33, v49\nv_add_f32 v34, v33, v49\ns_nop 1\nv_permlane16_swap_b32 v32, v34\nv_add_f32 v33, v32, v34\nv_add_f32 v34, v32, v34\ns_nop 1\n" \
"v_permlane32_swap_b32 v33, v34\nv_add_f32 v33, v33, v34\nv_rcp_f32 v33, v33\ns_nop 0\nv_mul_f32 v52, v52, v33\nv_mul_f32 v53, v53, v33\nv_mul_f32 v54, v54, v33\nv_mul_f32 v55, v55, v33\n" \
"v_mul_f32 v48, v48, v33\nv_mul_f32 v49, v49, v33\nv_cvt_pk_bf16_f32 v52, v52, v53\nv_cvt_pk_bf16_f32 v53, v54, v55\nv_cvt_pk_bf16_f32 v48, v48, v49\nv_mov_b32 v54, 0\nv_mov_b32 v55, 0\nv_bfi_b32 v52, s61, v52, v63\n" \
"v_bfi_b32 v53, s61, v53, v63\nv_bfi_b32 v54, s61, v54, v63\nv_bfi_b32 v55, s61, v55, v63\nv_bfi_b32 v48, s61, v48, v63\nbuffer_store_dwordx4 v[52:55], v10, %[rs], s55 offen sc1\nbuffer_store_dword v48, v11, %[rs], s45 offen sc1\ns_cmp_lt_i32 s40, 63\ns_cbranch_scc1 .Lqk_proj%=\n" \
"s_branch .Lqk_end%=\n.Lqk_re1%=:\nv_max_u32 v40, v80, v82\nv_cmp_eq_u32 s[46:47], -1, v40\nv_max_u32 v40, v84, v86\nv_cmp_eq_u32 s[48:49], -1, v40\nv_max_u32 v40, v88, v90\nv_cmp_eq_u32 s[50:51], -1, v40\n" \
"v_max_u32 v40, v92, v94\nv_cmp_eq_u32 s[52:53], -1, v40\ns_mov_b64 exec, s[46:47]\nbuffer_load_dwordx4 v[80:83], v3, %[rs], s42 offen offset:0 sc1\ns_mov_b64 exec, s[48:49]\nbuffer_load_dwordx4 v[84:87], v3, %[rs], s42 offen offset:1024 sc1\ns_mov_b64 exec, s[50:51]\nbuffer_load_dwordx4 v[88:91], v3, %[rs], s42 offen offset:2048 sc1\n" \
"s_mov_b64 exec, s[52:53]\nbuffer_load_dwordx4 v[92:95], v3, %[rs], s42 offen offset:3072 sc1\ns_mov_b64 exec, -1\nv_mov_b64 v[48:49], 0\nv_mov_b64 v[50:51], 0\nv_mov_b64 v[52:53], 0\nv_mov_b64 v[54:55], 0\nv_mov_b64 v[56:57], 0\n" \
"v_mov_b64 v[58:59], 0\nv_mov_b64 v[60:61], 0\nv_mov_b64 v[62:63], 0\ns_add_u32 s43, s43, 1\ns_cmp_lt_u32 s43, 262144\ns_cbranch_scc1 .Lqk_poll1%=\ns_branch .Lqk_abort%=\n.Lqk_c2%=:\n" \
"ds_read_b128 v[64:67], v1 offset:12288\nds_read_b128 v[68:71], v1 offset:13312\nds_read_b128 v[72:75], v1 offset:14336\nds_read_b128 v[76:79], v1 offset:15360\ns_bfe_i32 s54, s40, 0x10001\ns_and_b32 s44, s40, 1\ns_lshl_b32 s55, s44, 17\ns_lshl_b32 s45, s44, 12\n" \
".Lqk_poll2%=:\ns_waitcnt vmcnt(0)\nv_smfmac_f32_16x16x64_bf16 v[48:51], v[80:83], a[208:215], v4\nds_write_b128 v0, v[80:83] offset:24576\nv_max3_u32 v40, v80, v82, v84\nv_smfmac_f32_16x16x64_bf16 v[52:55], v[80:83], a[216:223], v4\nds_write_b128 v0, v[84:87] offset:25600\nv_max3_u32 v40, v40, v86, v88\n" \
"v_smfmac_f32_16x16x64_bf16 v[56:59], v[80:83], a[224:231], v4\nds_write_b128 v0, v[88:91] offset:26624\nv_max3_u32 v40, v40, v90, v92\nv_smfmac_f32_16x16x64_bf16 v[60:63], v[80:83], a[232:239], v4\nds_write_b128 v0, v[92:95] offset:27648\nv_max_u32 v40, v40, v94\nv_smfmac_f32_16x16x64_bf16 v[48:51], v[84:87], a[240:247], v4\nv_cmp_eq_u32 vcc, -1, v40\n" \
"s_waitcnt lgkmcnt(6)\nv_smfmac_f32_16x16x64_bf16 v[52:55], v[84:87], v[64:71], v4\ns_cbranch_vccnz .Lqk_re2%=\nv_smfmac_f32_16x16x64_bf16 v[56:59], v[84:87], a[248:255], v4\ns_waitcnt lgkmcnt(0)\ns_barrier\nds_read_b128 v[80:83], v0 offset:28672\nv_smfmac_f32_16x16x64_bf16 v[60:63], v[84:87], v[96:103], v4\n" \
"ds_read_b128 v[84:87], v0 offset:29696\nv_smfmac_f32_16x16x64_bf16 v[48:51], v[88:91], v[104:111], v4\nds_read_b128 v[64:67], v1 offset:16384\nds_read_b128 v[68:71], v1 offset:17408\nv_smfmac_f32_16x16x64_bf16 v[52:55], v[88:91], v[112:119], v4\nv_smfmac_f32_16x16x64_bf16 v[56:59], v[88:91], v[72:79], v4\nds_read_b128 v[72:75], v1 offset:18432\nds_read_b128 v[76:79], v1 offset:19456\n" \
"v_smfmac_f32_16x16x64_bf16 v[60:63], v[88:91], v[120:127], v4\nds_read_b128 v[88:91], v0 offset:30720\nv_smfmac_f32_16x16x64_bf16 v[48:51], v[92:95], v[128:135], v4\nds_read_b128 v[16:19], v7 offset:256\nv_smfmac_f32_16x16x64_bf16 v[52:55], v[92:95], v[136:143], v4\nv_smfmac_f32_16x16x64_bf16 v[56:59], v[92:95], v[144:151], v4\ns_waitcnt lgkmcnt(4)\nv_smfmac_f32_16x16x64_bf16 v[60:63], v[92:95], v[64:71], v4\n" \
"ds_read_b128 v[92:95], v0 offset:31744\nds_read_b128 v[64:67], v1 offset:20480\nds_read_b128 v[68:71], v1 offset:21504\nv_smfmac_f32_16x16x64_bf16 v[48:51], v[80:83], v[152:159], v4\nds_read_b128 v[20:23], v7 offset:2304\nv_smfmac_f32_16x16x64_bf16 v[52:55], v[80:83], v[160:167], v4\nv_smfmac_f32_16x16x64_bf16 v[56:59], v[80:83], v[168:175], v4\nv_smfmac_f32_16x16x64_bf16 v[60:63], v[80:83], v[176:183], v4\n" \
"ds_read_b128 v[80:83], v0 offset:16384\nv_smfmac_f32_16x16x64_bf16 v[48:51], v[84:87], v[184:191], v4\nds_read_b128 v[24:27], v7 offset:4352\ns_waitcnt lgkmcnt(8)\nv_smfmac_f32_16x16x64_bf16 v[52:55], v[84:87], v[72:79], v4\nds_read_b128 v[72:75], v1 offset:22528\nds_read_b128 v[76:79], v1 offset:23552\nv_smfmac_f32_16x16x64_bf16 v[56:59], v[84:87], v[192:199], v4\n" \
"v_smfmac_f32_16x16x64_bf16 v[60:63], v[84:87], v[200:207], v4\nds_read_b128 v[84:87], v0 offset:17408\ns_waitcnt lgkmcnt(10)\nv_smfmac_f32_16x16x64_bf16 v[48:51], v[88:91], v[208:215], v4\nds_read_b128 v[28:31], v7 offset:6400\nv_smfmac_f32_16x16x64_bf16 v[52:55], v[88:91], v[216:223], v4\ns_waitcnt lgkmcnt(7)\nv_smfmac_f32_16x16x64_bf16 v[56:59], v[88:91], v[64:71], v4\n" \
"ds_read_b128 v[64:67], v1 offset:0\nds_read_b128 v[68:71], v1 offset:1024\nv_smfmac_f32_16x16x64_bf16 v[60:63], v[88:91], v[224:231], v4\nds_read_b128 v[88:91], v0 offset:18432\nv_smfmac_f32_16x16x64_bf16 v[48:51], v[92:95], v[232:239], v4\nds_read_b128 v[32:35], v7 offset:8192\nv_smfmac_f32_16x16x64_bf16 v[52:55], v[92:95], v[240:247], v4\nv_smfmac_f32_16x16x64_bf16 v[56:59], v[92:95], v[248:255], v4\n" \
"s_waitcnt lgkmcnt(6)\nv_smfmac_f32_16x16x64_bf16 v[60:63], v[92:95], v[72:79], v4\nds_read_b128 v[92:95], v0 offset:19456\nds_read_b128 v[72:75], v1 offset:2048\nds_read_b128 v[76:79], v1 offset:3072\nv_smfmac_f32_16x16x64_bf16 v[48:51], v[80:83], a[0:7], v4\nds_read_b128 v[36:39], v7 offset:10240\nv_smfmac_f32_16x16x64_bf16 v[52:55], v[80:83], a[8:15], v4\n" \
"v_smfmac_f32_16x16x64_bf16 v[56:59], v[80:83], a[16:23], v4\nv_smfmac_f32_16x16x64_bf16 v[60:63], v[80:83], a[24:31], v4\nds_read_b128 v[80:83], v0 offset:20480\ns_waitcnt lgkmcnt(10)\nv_smfmac_f32_16x16x64_bf16 v[48:51], v[84:87], a[32:39], v4\nds_read_b128 v[40:43], v7 offset:12288\ns_waitcnt lgkmcnt(8)\nv_smfmac_f32_16x16x64_bf16 v[52:55], v[84:87], v[64:71], v4\n" \
"ds_read_b128 v[64:67], v1 offset:4096\nds_read_b128 v[68:71], v1 offset:5120\nv_smfmac_f32_16x16x64_bf16 v[56:59], v[84:87], a[40:47], v4\nv_smfmac_f32_16x16x64_bf16 v[60:63], v[84:87], a[48:55], v4\nds_read_b128 v[84:87], v0 offset:21504\ns_waitcnt lgkmcnt(10)\nv_smfmac_f32_16x16x64_bf16 v[48:51], v[88:91], a[56:63], v4\nds_read_b128 v[44:47], v7 offset:14336\n" \
"v_smfmac_f32_16x16x64_bf16 v[52:55], v[88:91], a[64:71], v4\ns_waitcnt lgkmcnt(7)\nv_smfmac_f32_16x16x64_bf16 v[56:59], v[88:91], v[72:79], v4\nds_read_b128 v[72:75], v1 offset:6144\nds_read_b128 v[76:79], v1 offset:7168\nv_smfmac_f32_16x16x64_bf16 v[60:63], v[88:91], a[72:79], v4\nds_read_b128 v[88:91], v0 offset:22528\nv_smfmac_f32_16x16x64_bf16 v[48:51], v[92:95], a[80:87], v4\n" \
"v_smfmac_f32_16x16x64_bf16 v[52:55], v[92:95], a[88:95], v4\nv_smfmac_f32_16x16x64_bf16 v[56:59], v[92:95], a[96:103], v4\ns_waitcnt lgkmcnt(5)\nv_smfmac_f32_16x16x64_bf16 v[60:63], v[92:95], v[64:71], v4\nds_read_b128 v[92:95], v0 offset:23552\nds_read_b128 v[64:67], v1 offset:8192\nds_read_b128 v[68:71], v1 offset:9216\nv_smfmac_f32_16x16x64_bf16 v[48:51], v[80:83], a[104:111], v4\n" \
"v_smfmac_f32_16x16x64_bf16 v[52:55], v[80:83], a[112:119], v4\nv_smfmac_f32_16x16x64_bf16 v[56:59], v[80:83], a[120:127], v4\nv_smfmac_f32_16x16x64_bf16 v[60:63], v[80:83], a[128:135], v4\ns_waitcnt lgkmcnt(7)\nv_smfmac_f32_16x16x64_bf16 v[48:51], v[84:87], a[136:143], v4\ns_waitcnt lgkmcnt(4)\nv_smfmac_f32_16x16x64_bf16 v[52:55], v[84:87], v[72:79], v4\nds_read_b128 v[72:75], v1 offset:10240\n" \
"ds_read_b128 v[76:79], v1 offset:11264\nv_smfmac_f32_16x16x64_bf16 v[56:59], v[84:87], a[144:151], v4\nv_smfmac_f32_16x16x64_bf16 v[60:63], v[84:87], a[152:159], v4\ns_waitcnt lgkmcnt(5)\nv_smfmac_f32_16x16x64_bf16 v[48:51], v[88:91], a[160:167], v4\nv_smfmac_f32_16x16x64_bf16 v[52:55], v[88:91], a[168:175], v4\ns_waitcnt lgkmcnt(4)\nv_smfmac_f32_16x16x64_bf16 v[48:51], v[92:95], a[184:191], v4\n" \
"v_smfmac_f32_16x16x64_bf16 v[52:55], v[92:95], a[192:199], v4\ns_waitcnt lgkmcnt(2)\nv_smfmac_f32_16x16x64_bf16 v[56:59], v[88:91], v[64:71], v4\nv_add_f32 v48, v48, v50\nv_smfmac_f32_16x16x64_bf16 v[60:63], v[88:91], a[176:183], v4\nv_add_f32 v52, v52, v54\nv_add_f32 v49, v49, v51\nv_smfmac_f32_16x16x64_bf16 v[56:59], v[92:95], a[200:207], v4\n" \
"v_add_f32 v53, v53, v55\nv_cvt_pk_bf16_f32 v50, v48, v52\ns_waitcnt lgkmcnt(0)\nv_smfmac_f32_16x16x64_bf16 v[60:63], v[92:95], v[72:79], v4\nv_cvt_pk_bf16_f32 v51, v49, v53\nds_write2_b32 v6, v50, v51 offset0:0 offset1:4\ns_nop 1\nv_add_f32 v56, v56, v58\n" \
"v_add_f32 v57, v57, v59\ns_nop 1\nv_add_f32 v60, v60, v62\nv_add_f32 v61, v61, v63\nv_cvt_pk_bf16_f32 v54, v56, v60\nv_cvt_pk_bf16_f32 v55, v57, v61\nds_write2_b32 v6, v54, v55 offset0:36 offset1:40\nv_mov_b32 v63, s54\n" \
"s_waitcnt lgkmcnt(0)\ns_barrier\nds_read_b128 v[64:67], v9 offset:0\nds_read_b128 v[68:71], v9 offset:576\nds_read_b128 v[72:75], v9 offset:1152\nds_read_b128 v[76:79], v9 offset:1728\nds_read_b128 v[80:83], v7 offset:8448\nds_read_b128 v[84:87], v7 offset:10496\n" \
"ds_read_b128 v[88:91], v7 offset:12544\nds_read_b128 v[92:95], v7 offset:14592\nv_mfma_f32_16x16x32_bf16 v[52:55], v[32:35], v[16:19], 0\nv_mfma_f32_16x16x32_bf16 v[52:55], v[36:39], v[20:23], v[52:55]\nv_mfma_f32_16x16x32_bf16 v[52:55], v[40:43], v[24:27], v[52:55]\nv_mfma_f32_16x16x32_bf16 v[52:55], v[44:47], v[28:31], v[52:55]\ns_waitcnt lgkmcnt(7)\nv_mfma_f32_16x16x32_bf16 v[48:51], v[64:67], v[16:19], 0\n" \
"s_waitcnt lgkmcnt(6)\nv_mfma_f32_16x16x32_bf16 v[48:51], v[68:71], v[20:23], v[48:51]\ns_waitcnt lgkmcnt(5)\nv_mfma_f32_16x16x32_bf16 v[48:51], v[72:75], v[24:27], v[48:51]\ns_waitcnt lgkmcnt(4)\nv_mfma_f32_16x16x32_bf16 v[48:51], v[76:79], v[28:31], v[48:51]\ns_waitcnt lgkmcnt(3)\nv_mfma_f32_16x16x32_bf16 v[56:59], v[80:83], v[16:19], v[12:15]\n" \
"s_waitcnt lgkmcnt(2)\nv_mfma_f32_16x16x32_bf16 v[56:59], v[84:87], v[20:23], v[56:59]\ns_waitcnt lgkmcnt(1)\nv_mfma_f32_16x16x32_bf16 v[56:59], v[88:91], v[24:27], v[56:59]\ns_waitcnt lgkmcnt(0)\nv_mfma_f32_16x16x32_bf16 v[56:59], v[92:95], v[28:31], v[56:59]\nv_max3_f32 v32, v52, v53, v54\nv_max3_f32 v32, v32, v55, v48\n" \
"s_nop 5\nv_max3_f32 v32, v32, v49, v56\nv_max_f32 v32, v32, v57\nv_max3_f32 v33, v32, v58, v59\nv_max3_f32 v32, v32, v58, v59\ns_nop 1\nv_permlane16_swap_b32 v32, v33\nv_max_f32 v34, v32, v33\n" \
"v_max_f32 v32, v32, v33\ns_nop 1\nv_permlane32_swap_b32 v32, v34\nv_max_f32 v32, v32, v34\nv_mul_f32 v32, s59, v32\nv_fma_f32 v52, v52, s58, v32\nv_fma_f32 v53, v53, s58, v32\nv_exp_f32 v52, v52\n" \
"v_fma_f32 v54, v54, s58, v32\nv_exp_f32 v53, v53\nv_fma_f32 v55, v55, s58, v32\nv_exp_f32 v54, v54\nv_add_f32 v33, v52, v53\nv_fma_f32 v48, v48, s58, v32\nv_exp_f32 v55, v55\nv_add_f32 v33, v33, v54\n" \
"v_fma_f32 v49, v49, s58, v32\nv_exp_f32 v48, v48\nv_add_f32 v33, v33, v55\nv_fma_f32 v56, v56, s58, v32\nv_exp_f32 v49, v49\nv_add_f32 v33, v33, v48\nv_fma_f32 v57, v57, s58, v32\nv_exp_f32 v56, v56\n" \
"v_add_f32 v33, v33, v49\nv_fma_f32 v58, v58, s58, v32\nv_exp_f32 v57, v57\nv_add_f32 v33, v33, v56\nv_fma_f32 v59, v59, s58, v32\nv_exp_f32 v58, v58\nv_add_f32 v33, v33, v57\nv_exp_f32 v59, v59\n" \
"v_add_f32 v33, v33, v58\nv_add_f32 v32, v33, v59\nv_add_f32 v34, v33, v59\ns_nop 1\nv_permlane16_swap_b32 v32, v34\nv_add_f32 v33, v32, v34\nv_add_f32 v34, v32, v34\ns_nop 1\n" \
"v_permlane32_swap_b32 v33, v34\nv_add_f32 v33, v33, v34\nv_rcp_f32 v33, v33\ns_nop 0\nv_mul_f32 v52, v52, v33\nv_mul_f32 v53, v53, v33\nv_mul_f32 v54, v54, v33\nv_mul_f32 v55, v55, v33\n" \
"v_mul_f32 v48, v48, v33\nv_mul_f32 v49, v49, v33\nv_mul_f32 v56, v56, v33\nv_mul_f32 v57, v57, v33\nv_mul_f32 v58, v58, v33\nv_mul_f32 v59, v59, v33\nv_cvt_pk_bf16_f32 v52, v52, v53\nv_cvt_pk_bf16_f32 v53, v54, v55\n" \
"v_cvt_pk_bf16_f32 v48, v48, v49\nv_cvt_pk_bf16_f32 v54, v56, v57\nv_cvt_pk_bf16_f32 v55, v58, v59\nv_bfi_b32 v52, s61, v52, v63\nv_bfi_b32 v53, s61, v53, v63\nv_bfi_b32 v54, s61, v54, v63\nv_bfi_b32 v55, s61, v55, v63\nv_bfi_b32 v48, s61, v48, v63\n" \
"buffer_store_dwordx4 v[52:55], v10, %[rs], s55 offen sc1\nbuffer_store_dword v48, v11, %[rs], s45 offen sc1\ns_cmp_lt_i32 s40, 63\ns_cbranch_scc1 .Lqk_proj%=\ns_branch .Lqk_end%=\n.Lqk_re2%=:\nv_max_u32 v40, v80, v82\nv_cmp_eq_u32 s[46:47], -1, v40\n" \
"v_max_u32 v40, v84, v86\nv_cmp_eq_u32 s[48:49], -1, v40\nv_max_u32 v40, v88, v90\nv_cmp_eq_u32 s[50:51], -1, v40\nv_max_u32 v40, v92, v94\nv_cmp_eq_u32 s[52:53], -1, v40\ns_mov_b64 exec, s[46:47]\nbuffer_load_dwordx4 v[80:83], v3, %[rs], s42 offen offset:0 sc1\n" \
"s_mov_b64 exec, s[48:49]\nbuffer_load_dwordx4 v[84:87], v3, %[rs], s42 offen offset:1024 sc1\ns_mov_b64 exec, s[50:51]\nbuffer_load_dwordx4 v[88:91], v3, %[rs], s42 offen offset:2048 sc1\ns_mov_b64 exec, s[52:53]\nbuffer_load_dwordx4 v[92:95], v3, %[rs], s42 offen offset:3072 sc1\ns_mov_b64 exec, -1\nv_mov_b64 v[48:49], 0\n" \
"v_mov_b64 v[50:51], 0\nv_mov_b64 v[52:53], 0\nv_mov_b64 v[54:55], 0\nv_mov_b64 v[56:57], 0\nv_mov_b64 v[58:59], 0\nv_mov_b64 v[60:61], 0\nv_mov_b64 v[62:63], 0\ns_add_u32 s43, s43, 1\n" \
"s_cmp_lt_u32 s43, 262144\ns_cbranch_scc1 .Lqk_poll2%=\ns_branch .Lqk_abort%=\n.Lqk_c3%=:\nds_read_b128 v[64:67], v1 offset:18432\nds_read_b128 v[68:71], v1 offset:19456\nds_read_b128 v[72:75], v1 offset:20480\nds_read_b128 v[76:79], v1 offset:21504\n" \
"s_add_u32 s56, s56, %[fs]\ns_add_u32 s44, s40, 1\ns_lshl_b32 s44, s44, 17\ns_add_u32 s44, s44, %[xn]\ns_lshr_b32 s45, s44, 8\ns_and_b32 s45, s45, 0x2000\ns_xor_b32 s41, s44, s45\nbuffer_load_dword v16, v12, %[rs], s56 offen sc1\n" \
".Lqk_poll3%=:\ns_waitcnt vmcnt(1)\nv_smfmac_f32_16x16x64_bf16 v[48:51], v[80:83], v[152:159], v4\nds_write_b128 v0, v[80:83] offset:28672\nv_max3_u32 v40, v80, v82, v84\nv_smfmac_f32_16x16x64_bf16 v[52:55], v[80:83], v[160:167], v4\nds_write_b128 v0, v[84:87] offset:29696\nv_max3_u32 v40, v40, v86, v88\n" \
"v_smfmac_f32_16x16x64_bf16 v[56:59], v[80:83], v[168:175], v4\nds_write_b128 v0, v[88:91] offset:30720\nv_max3_u32 v40, v40, v90, v92\nv_smfmac_f32_16x16x64_bf16 v[60:63], v[80:83], v[176:183], v4\nds_write_b128 v0, v[92:95] offset:31744\nv_max_u32 v40, v40, v94\nv_smfmac_f32_16x16x64_bf16 v[48:51], v[84:87], v[184:191], v4\nv_cmp_eq_u32 vcc, -1, v40\n" \
"s_waitcnt lgkmcnt(6)\nv_smfmac_f32_16x16x64_bf16 v[52:55], v[84:87], v[64:71], v4\ns_cbranch_vccnz .Lqk_re3%=\nv_smfmac_f32_16x16x64_bf16 v[56:59], v[84:87], v[192:199], v4\ns_waitcnt lgkmcnt(0)\ns_barrier\nds_read_b128 v[80:83], v0 offset:16384\nv_smfmac_f32_16x16x64_bf16 v[60:63], v[84:87], v[200:207], v4\n" \
"ds_read_b128 v[84:87], v0 offset:17408\nv_smfmac_f32_16x16x64_bf16 v[48:51], v[88:91], v[208:215], v4\nds_read_b128 v[64:67], v1 offset:22528\nds_read_b128 v[68:71], v1 offset:23552\nv_smfmac_f32_16x16x64_bf16 v[52:55], v[88:91], v[216:223], v4\nv_smfmac_f32_16x16x64_bf16 v[56:59], v[88:91], v[72:79], v4\nds_read_b128 v[72:75], v1 offset:0\nds_read_b128 v[76:79], v1 offset:1024\n" \
"v_smfmac_f32_16x16x64_bf16 v[60:63], v[88:91], v[224:231], v4\nds_read_b128 v[88:91], v0 offset:18432\nv_smfmac_f32_16x16x64_bf16 v[48:51], v[92:95], v[232:239], v4\nv_smfmac_f32_16x16x64_bf16 v[52:55], v[92:95], v[240:247], v4\nv_smfmac_f32_16x16x64_bf16 v[56:59], v[92:95], v[248:255], v4\ns_waitcnt lgkmcnt(3)\nv_smfmac_f32_16x16x64_bf16 v[60:63], v[92:95], v[64:71], v4\nds_read_b128 v[92:95], v0 offset:19456\n" \
"ds_read_b128 v[64:67], v1 offset:2048\nds_read_b128 v[68:71], v1 offset:3072\nv_smfmac_f32_16x16x64_bf16 v[48:51], v[80:83], a[0:7], v4\ns_waitcnt vmcnt(0)\nv_cmp_ge_i32 vcc, s40, v16\nv_smfmac_f32_16x16x64_bf16 v[52:55], v[80:83], a[8:15], v4\ns_cmp_eq_u64 vcc, 0\ns_cselect_b32 s61, s41, -2147483648\n" \
"v_smfmac_f32_16x16x64_bf16 v[56:59], v[80:83], a[16:23], v4\ns_add_u32 m0, %[l0], 0\ns_add_u32 s60, s61, 0\nbuffer_load_dwordx4 v0, %[rs], s60 offen offset:0 sc1 lds\nv_smfmac_f32_16x16x64_bf16 v[60:63], v[80:83], a[24:31], v4\nds_read_b128 v[80:83], v0 offset:20480\nv_smfmac_f32_16x16x64_bf16 v[48:51], v[84:87], a[32:39], v4\nbuffer_load_dwordx4 v0, %[rs], s60 offen offset:1024 sc1 lds\n" \
"s_waitcnt lgkmcnt(5)\nv_smfmac_f32_16x16x64_bf16 v[52:55], v[84:87], v[72:79], v4\nds_read_b128 v[72:75], v1 offset:4096\nds_read_b128 v[76:79], v1 offset:5120\nv_smfmac_f32_16x16x64_bf16 v[56:59], v[84:87], a[40:47], v4\nbuffer_load_dwordx4 v0, %[rs], s60 offen offset:2048 sc1 lds\nv_smfmac_f32_16x16x64_bf16 v[60:63], v[84:87], a[48:55], v4\nds_read_b128 v[84:87], v0 offset:21504\n" \
"s_waitcnt lgkmcnt(7)\nv_smfmac_f32_16x16x64_bf16 v[48:51], v[88:91], a[56:63], v4\nbuffer_load_dwordx4 v0, %[rs], s60 offen offset:3072 sc1 lds\nv_smfmac_f32_16x16x64_bf16 v[52:55], v[88:91], a[64:71], v4\ns_waitcnt lgkmcnt(4)\nv_smfmac_f32_16x16x64_bf16 v[56:59], v[88:91], v[64:71], v4\nds_read_b128 v[64:67], v1 offset:6144\nds_read_b128 v[68:71], v1 offset:7168\n" \
"s_add_u32 m0, %[l0], 4096\ns_add_u32 s60, s61, 4096\nbuffer_load_dwordx4 v0, %[rs], s60 offen offset:0 sc1 lds\nv_smfmac_f32_16x16x64_bf16 v[60:63], v[88:91], a[72:79], v4\nds_read_b128 v[88:91], v0 offset:22528\nv_smfmac_f32_16x16x64_bf16 v[48:51], v[92:95], a[80:87], v4\nbuffer_load_dwordx4 v0, %[rs], s60 offen offset:1024 sc1 lds\nv_smfmac_f32_16x16x64_bf16 v[52:55], v[92:95], a[88:95], v4\n" \
"v_smfmac_f32_16x16x64_bf16 v[56:59], v[92:95], a[96:103], v4\nbuffer_load_dwordx4 v0, %[rs], s60 offen offset:2048 sc1 lds\ns_waitcnt lgkmcnt(4)\nv_smfmac_f32_16x16x64_bf16 v[60:63], v[92:95], v[72:79], v4\nds_read_b128 v[92:95], v0 offset:23552\nds_read_b128 v[72:75], v1 offset:8192\nds_read_b128 v[76:79], v1 offset:9216\nv_smfmac_f32_16x16x64_bf16 v[48:51], v[80:83], a[104:111], v4\n" \
"buffer_load_dwordx4 v0, %[rs], s60 offen offset:3072 sc1 lds\nv_smfmac_f32_16x16x64_bf16 v[52:55], v[80:83], a[112:119], v4\nv_smfmac_f32_16x16x64_bf16 v[56:59], v[80:83], a[120:127], v4\ns_add_u32 m0, %[l0], 8192\ns_add_u32 s60, s61, 16384\nbuffer_load_dwordx4 v0, %[rs], s60 offen offset:0 sc1 lds\nv_smfmac_f32_16x16x64_bf16 v[60:63], v[80:83], a[128:135], v4\nds_read_b128 v[80:83], v0 offset:24576\n" \
"s_waitcnt lgkmcnt(7)\nv_smfmac_f32_16x16x64_bf16 v[48:51], v[84:87], a[136:143], v4\nbuffer_load_dwordx4 v0, %[rs], s60 offen offset:1024 sc1 lds\ns_waitcnt lgkmcnt(5)\nv_smfmac_f32_16x16x64_bf16 v[52:55], v[84:87], v[64:71], v4\nds_read_b128 v[64:67], v1 offset:10240\nds_read_b128 v[68:71], v1 offset:11264\nv_smfmac_f32_16x16x64_bf16 v[56:59], v[84:87], a[144:151], v4\n" \
"buffer_load_dwordx4 v0, %[rs], s60 offen offset:2048 sc1 lds\nv_smfmac_f32_16x16x64_bf16 v[60:63], v[84:87], a[152:159], v4\nds_read_b128 v[84:87], v0 offset:25600\ns_waitcnt lgkmcnt(7)\nv_smfmac_f32_16x16x64_bf16 v[48:51], v[88:91], a[160:167], v4\nbuffer_load_dwordx4 v0, %[rs], s60 offen offset:3072 sc1 lds\nv_smfmac_f32_16x16x64_bf16 v[52:55], v[88:91], a[168:175], v4\ns_waitcnt lgkmcnt(4)\n" \
"v_smfmac_f32_16x16x64_bf16 v[56:59], v[88:91], v[72:79], v4\nds_read_b128 v[72:75], v1 offset:12288\nds_read_b128 v[76:79], v1 offset:13312\ns_add_u32 m0, %[l0], 12288\ns_add_u32 s60, s61, 20480\nbuffer_load_dwordx4 v0, %[rs], s60 offen offset:0 sc1 lds\nv_smfmac_f32_16x16x64_bf16 v[60:63], v[88:91], a[176:183], v4\nds_read_b128 v[88:91], v0 offset:26624\n" \
"v_smfmac_f32_16x16x64_bf16 v[48:51], v[92:95], a[184:191], v4\nbuffer_load_dwordx4 v0, %[rs], s60 offen offset:1024 sc1 lds\nv_smfmac_f32_16x16x64_bf16 v[52:55], v[92:95], a[192:199], v4\nv_smfmac_f32_16x16x64_bf16 v[56:59], v[92:95], a[200:207], v4\nbuffer_load_dwordx4 v0, %[rs], s60 offen offset:2048 sc1 lds\ns_waitcnt lgkmcnt(4)\nv_smfmac_f32_16x16x64_bf16 v[60:63], v[92:95], v[64:71], v4\nds_read_b128 v[92:95], v0 offset:27648\n" \
"ds_read_b128 v[64:67], v1 offset:14336\nds_read_b128 v[68:71], v1 offset:15360\nv_smfmac_f32_16x16x64_bf16 v[48:51], v[80:83], a[208:215], v4\nbuffer_load_dwordx4 v0, %[rs], s60 offen offset:3072 sc1 lds\nv_smfmac_f32_16x16x64_bf16 v[52:55], v[80:83], a[216:223], v4\nv_smfmac_f32_16x16x64_bf16 v[56:59], v[80:83], a[224:231], v4\nv_smfmac_f32_16x16x64_bf16 v[60:63], v[80:83], a[232:239], v4\ns_waitcnt lgkmcnt(6)\n" \
"v_smfmac_f32_16x16x64_bf16 v[48:51], v[84:87], a[240:247], v4\ns_waitcnt lgkmcnt(4)\nv_smfmac_f32_16x16x64_bf16 v[52:55], v[84:87], v[72:79], v4\nds_read_b128 v[72:75], v1 offset:16384\nds_read_b128 v[76:79], v1 offset:17408\nv_smfmac_f32_16x16x64_bf16 v[56:59], v[84:87], a[248:255], v4\nv_smfmac_f32_16x16x64_bf16 v[60:63], v[84:87], v[96:103], v4\ns_waitcnt lgkmcnt(5)\n" \
"v_smfmac_f32_16x16x64_bf16 v[48:51], v[88:91], v[104:111], v4\nv_smfmac_f32_16x16x64_bf16 v[52:55], v[88:91], v[112:119], v4\ns_waitcnt lgkmcnt(4)\nv_smfmac_f32_16x16x64_bf16 v[48:51], v[92:95], v[128:135], v4\nv_smfmac_f32_16x16x64_bf16 v[52:55], v[92:95], v[136:143], v4\ns_waitcnt lgkmcnt(2)\nv_smfmac_f32_16x16x64_bf16 v[56:59], v[88:91], v[64:71], v4\nv_add_f32 v48, v48, v50\n" \
"v_smfmac_f32_16x16x64_bf16 v[60:63], v[88:91], v[120:127], v4\nv_add_f32 v52, v52, v54\nv_add_f32 v49, v49, v51\nv_smfmac_f32_16x16x64_bf16 v[56:59], v[92:95], v[144:151], v4\nv_add_f32 v53, v53, v55\nv_cvt_pk_bf16_f32 v50, v48, v52\ns_waitcnt lgkmcnt(0)\nv_smfmac_f32_16x16x64_bf16 v[60:63], v[92:95], v[72:79], v4\n" \
"v_cvt_pk_bf16_f32 v51, v49, v53\nds_write2_b32 v6, v50, v51 offset0:0 offset1:4\ns_nop 1\nv_add_f32 v56, v56, v58\nv_add_f32 v57, v57, v59\ns_nop 1\nv_add_f32 v60, v60, v62\nv_add_f32 v61, v61, v63\n" \
"v_cvt_pk_bf16_f32 v54, v56, v60\nv_cvt_pk_bf16_f32 v55, v57, v61\nds_write2_b32 v6, v54, v55 offset0:36 offset1:40\ns_waitcnt lgkmcnt(0)\ns_barrier\ns_cbranch_vccz .Lqk_fq1%=\ns_cmp_ge_i32 s40, 63\ns_cbranch_scc1 .Lqk_end%=\n" \
".Lqk_fetch0%=:\nbuffer_load_dword v16, v12, %[rs], s56 offen sc1\ns_waitcnt vmcnt(0)\nv_cmp_ge_i32 vcc, s40, v16\ns_cbranch_vccz .Lqk_fok%=\ns_add_u32 s43, s43, 1\ns_cmp_lt_u32 s43, 262144\ns_cbranch_scc1 .Lqk_fetch0%=\n" \
"s_branch .Lqk_abort%=\n.Lqk_fok%=:\ns_add_u32 s44, s40, 1\ns_lshl_b32 s44, s44, 17\ns_add_u32 s44, s44, %[xn]\ns_lshr_b32 s45, s44, 8\ns_and_b32 s45, s45, 0x2000\ns_xor_b32 s41, s44, s45\n" \
"s_add_u32 m0, %[l0], 0\ns_add_u32 s60, s41, 0\nbuffer_load_dwordx4 v0, %[rs], s60 offen offset:0 sc1 lds\nbuffer_load_dwordx4 v0, %[rs], s60 offen offset:1024 sc1 lds\nbuffer_load_dwordx4 v0, %[rs], s60 offen offset:2048 sc1 lds\nbuffer_load_dwordx4 v0, %[rs], s60 offen offset:3072 sc1 lds\ns_add_u32 m0, %[l0], 4096\ns_add_u32 s60, s41, 4096\n" \
"buffer_load_dwordx4 v0, %[rs], s60 offen offset:0 sc1 lds\nbuffer_load_dwordx4 v0, %[rs], s60 offen offset:1024 sc1 lds\nbuffer_load_dwordx4 v0, %[rs], s60 offen offset:2048 sc1 lds\nbuffer_load_dwordx4 v0, %[rs], s60 offen offset:3072 sc1 lds\ns_add_u32 m0, %[l0], 8192\ns_add_u32 s60, s41, 16384\nbuffer_load_dwordx4 v0, %[rs], s60 offen offset:0 sc1 lds\nbuffer_load_dwordx4 v0, %[rs], s60 offen offset:1024 sc1 lds\n" \
"buffer_load_dwordx4 v0, %[rs], s60 offen offset:2048 sc1 lds\nbuffer_load_dwordx4 v0, %[rs], s60 offen offset:3072 sc1 lds\ns_add_u32 m0, %[l0], 12288\ns_add_u32 s60, s41, 20480\nbuffer_load_dwordx4 v0, %[rs], s60 offen offset:0 sc1 lds\nbuffer_load_dwordx4 v0, %[rs], s60 offen offset:1024 sc1 lds\nbuffer_load_dwordx4 v0, %[rs], s60 offen offset:2048 sc1 lds\nbuffer_load_dwordx4 v0, %[rs], s60 offen offset:3072 sc1 lds\n" \
".Lqk_fq1%=:\ns_add_u32 m0, %[l0], 16384\ns_add_u32 s60, s41, 32768\nbuffer_load_dwordx4 v0, %[rs], s60 offen offset:0 sc1 lds\nbuffer_load_dwordx4 v0, %[rs], s60 offen offset:1024 sc1 lds\nbuffer_load_dwordx4 v0, %[rs], s60 offen offset:2048 sc1 lds\nbuffer_load_dwordx4 v0, %[rs], s60 offen offset:3072 sc1 lds\ns_add_u32 m0, %[l0], 20480\n" \
"s_add_u32 s60, s41, 36864\nbuffer_load_dwordx4 v0, %[rs], s60 offen offset:0 sc1 lds\nbuffer_load_dwordx4 v0, %[rs], s60 offen offset:1024 sc1 lds\nbuffer_load_dwordx4 v0, %[rs], s60 offen offset:2048 sc1 lds\nbuffer_load_dwordx4 v0, %[rs], s60 offen offset:3072 sc1 lds\ns_add_u32 m0, %[l0], 24576\ns_add_u32 s60, s41, 49152\nbuffer_load_dwordx4 v0, %[rs], s60 offen offset:0 sc1 lds\n" \
"buffer_load_dwordx4 v0, %[rs], s60 offen offset:1024 sc1 lds\nbuffer_load_dwordx4 v0, %[rs], s60 offen offset:2048 sc1 lds\nbuffer_load_dwordx4 v0, %[rs], s60 offen offset:3072 sc1 lds\ns_add_u32 m0, %[l0], 28672\ns_add_u32 s60, s41, 53248\nbuffer_load_dwordx4 v0, %[rs], s60 offen offset:0 sc1 lds\nbuffer_load_dwordx4 v0, %[rs], s60 offen offset:1024 sc1 lds\nbuffer_load_dwordx4 v0, %[rs], s60 offen offset:2048 sc1 lds\n" \
"buffer_load_dwordx4 v0, %[rs], s60 offen offset:3072 sc1 lds\ns_waitcnt vmcnt(16)\ns_cmp_lt_i32 s40, 63\ns_cbranch_scc1 .Lqk_proj%=\ns_branch .Lqk_end%=\n.Lqk_re3%=:\nv_max_u32 v40, v80, v82\nv_cmp_eq_u32 s[46:47], -1, v40\n" \
"v_max_u32 v40, v84, v86\nv_cmp_eq_u32 s[48:49], -1, v40\nv_max_u32 v40, v88, v90\nv_cmp_eq_u32 s[50:51], -1, v40\nv_max_u32 v40, v92, v94\nv_cmp_eq_u32 s[52:53], -1, v40\ns_mov_b64 exec, s[46:47]\nbuffer_load_dwordx4 v[80:83], v3, %[rs], s42 offen offset:0 sc1\n" \
"s_mov_b64 exec, s[48:49]\nbuffer_load_dwordx4 v[84:87], v3, %[rs], s42 offen offset:1024 sc1\ns_mov_b64 exec, s[50:51]\nbuffer_load_dwordx4 v[88:91], v3, %[rs], s42 offen offset:2048 sc1\ns_mov_b64 exec, s[52:53]\nbuffer_load_dwordx4 v[92:95], v3, %[rs], s42 offen offset:3072 sc1\ns_mov_b64 exec, -1\nbuffer_load_dword v16, v12, %[rs], s56 offen sc1\n" \
"v_mov_b64 v[48:49], 0\nv_mov_b64 v[50:51], 0\nv_mov_b64 v[52:53], 0\nv_mov_b64 v[54:55], 0\nv_mov_b64 v[56:57], 0\nv_mov_b64 v[58:59], 0\nv_mov_b64 v[60:61], 0\nv_mov_b64 v[62:63], 0\n" \
"s_add_u32 s43, s43, 1\ns_cmp_lt_u32 s43, 262144\ns_cbranch_scc1 .Lqk_poll3%=\ns_branch .Lqk_abort%=\n.Lqk_abort%=:\ns_mov_b64 exec, -1\nv_mov_b32 v16, 0\ns_movk_i32 s44, 6400\n" \
"buffer_store_dword v16, off, %[rs], s44 sc1\n.Lqk_end%=:\ns_waitcnt vmcnt(0) lgkmcnt(0)\n" \
""
#define VU_ASM \
"v_mbcnt_lo_u32_b32 v255, -1, 0\nv_mbcnt_hi_u32_b32 v255, -1, v255\nv_lshlrev_b32 v255, 2, v255\nv_add_u32 v255, %[tb], v255\nds_read_b32 v0, v255 offset:0\nds_read_b32 v1, v255 offset:256\nds_read_b32 v2, v255 offset:512\nds_read_b32 v3, v255 offset:768\n" \
"ds_read_b32 v4, v255 offset:1024\nds_read_b32 v5, v255 offset:1280\nds_read_b32 v6, v255 offset:1536\nds_read_b32 v7, v255 offset:1792\nds_read_b32 v8, v255 offset:2048\nds_read_b32 v11, v255 offset:2304\nds_read_b32 v90, v255 offset:2560\nds_read_b32 v91, v255 offset:2816\n" \
"ds_read_b32 v92, v255 offset:3072\nds_read_b32 v93, v255 offset:3328\nds_read_b32 v94, v255 offset:3584\nds_read_b32 v95, v255 offset:3840\ns_waitcnt lgkmcnt(0)\ns_add_u32 m0, %[lw], 16384\ns_add_u32 s44, %[wo], 0\nbuffer_load_dwordx4 v90, %[rw], s44 offen lds\n" \
"s_add_u32 m0, %[lw], 17408\ns_add_u32 s44, %[wo], 131072\nbuffer_load_dwordx4 v90, %[rw], s44 offen lds\ns_add_u32 m0, %[lw], 18432\ns_add_u32 s44, %[wo], 262144\nbuffer_load_dwordx4 v90, %[rw], s44 offen lds\ns_add_u32 m0, %[lw], 19456\ns_add_u32 s44, %[wo], 393216\n" \
"buffer_load_dwordx4 v90, %[rw], s44 offen lds\ns_add_u32 m0, %[lw], 20480\ns_add_u32 s44, %[wo], 524288\nbuffer_load_dwordx4 v90, %[rw], s44 offen lds\ns_add_u32 m0, %[lw], 21504\ns_add_u32 s44, %[wo], 655360\nbuffer_load_dwordx4 v90, %[rw], s44 offen lds\ns_add_u32 m0, %[lw], 22528\n" \
"s_add_u32 s44, %[wo], 786432\nbuffer_load_dwordx4 v90, %[rw], s44 offen lds\ns_add_u32 m0, %[lw], 23552\ns_add_u32 s44, %[wo], 917504\nbuffer_load_dwordx4 v90, %[rw], s44 offen lds\ns_add_u32 m0, %[tb], 0\ns_add_u32 s44, %[wo], 1048576\nbuffer_load_dwordx4 v90, %[rw], s44 offen lds\n" \
"s_add_u32 m0, %[tb], 1024\ns_add_u32 s44, %[wo], 1179648\nbuffer_load_dwordx4 v90, %[rw], s44 offen lds\ns_add_u32 m0, %[tb], 2048\ns_add_u32 s44, %[wo], 1310720\nbuffer_load_dwordx4 v90, %[rw], s44 offen lds\ns_add_u32 m0, %[tb], 3072\ns_add_u32 s44, %[wo], 1441792\n" \
"buffer_load_dwordx4 v90, %[rw], s44 offen lds\ns_add_u32 m0, %[tb], 4096\ns_add_u32 s44, %[wo], 1572864\nbuffer_load_dwordx4 v90, %[rw], s44 offen lds\ns_add_u32 m0, %[tb], 5120\ns_add_u32 s44, %[wo], 1703936\nbuffer_load_dwordx4 v90, %[rw], s44 offen lds\ns_add_u32 m0, %[tb], 6144\n" \
"s_add_u32 s44, %[wo], 1835008\nbuffer_load_dwordx4 v90, %[rw], s44 offen lds\ns_add_u32 m0, %[tb], 7168\ns_add_u32 s44, %[wo], 1966080\nbuffer_load_dwordx4 v90, %[rw], s44 offen lds\ns_add_u32 m0, %[tb], 8192\ns_add_u32 s44, %[wo], 2097152\nbuffer_load_dwordx4 v90, %[rw], s44 offen lds\n" \
"s_add_u32 m0, %[tb], 9216\ns_add_u32 s44, %[wo], 2228224\nbuffer_load_dwordx4 v90, %[rw], s44 offen lds\ns_add_u32 m0, %[tb], 10240\ns_add_u32 s44, %[wo], 2359296\nbuffer_load_dwordx4 v90, %[rw], s44 offen lds\ns_add_u32 m0, %[tb], 11264\ns_add_u32 s44, %[wo], 2490368\n" \
"buffer_load_dwordx4 v90, %[rw], s44 offen lds\ns_add_u32 m0, %[lw], 0\ns_add_u32 s44, %[wo], 2621440\nbuffer_load_dwordx4 v90, %[rw], s44 offen lds\ns_add_u32 m0, %[lw], 1024\ns_add_u32 s44, %[wo], 2752512\nbuffer_load_dwordx4 v90, %[rw], s44 offen lds\ns_add_u32 m0, %[lw], 2048\n" \
"s_add_u32 s44, %[wo], 2883584\nbuffer_load_dwordx4 v90, %[rw], s44 offen lds\ns_add_u32 m0, %[lw], 3072\ns_add_u32 s44, %[wo], 3014656\nbuffer_load_dwordx4 v90, %[rw], s44 offen lds\ns_add_u32 m0, %[lw], 4096\ns_add_u32 s44, %[wo], 3145728\nbuffer_load_dwordx4 v90, %[rw], s44 offen lds\n" \
"s_add_u32 m0, %[lw], 5120\ns_add_u32 s44, %[wo], 3276800\nbuffer_load_dwordx4 v90, %[rw], s44 offen lds\ns_add_u32 m0, %[lw], 6144\ns_add_u32 s44, %[wo], 3407872\nbuffer_load_dwordx4 v90, %[rw], s44 offen lds\ns_add_u32 m0, %[lw], 7168\ns_add_u32 s44, %[wo], 3538944\n" \
"buffer_load_dwordx4 v90, %[rw], s44 offen lds\ns_add_u32 m0, %[lw], 8192\ns_add_u32 s44, %[wo], 3670016\nbuffer_load_dwordx4 v90, %[rw], s44 offen lds\ns_add_u32 m0, %[lw], 9216\ns_add_u32 s44, %[wo], 3801088\nbuffer_load_dwordx4 v90, %[rw], s44 offen lds\ns_add_u32 m0, %[lw], 10240\n" \
"s_add_u32 s44, %[wo], 3932160\nbuffer_load_dwordx4 v90, %[rw], s44 offen lds\ns_add_u32 m0, %[lw], 11264\ns_add_u32 s44, %[wo], 4063232\nbuffer_load_dwordx4 v90, %[rw], s44 offen lds\ns_add_u32 m0, %[lw], 12288\ns_add_u32 s44, %[wo], 4194304\nbuffer_load_dwordx4 v90, %[rw], s44 offen lds\n" \
"s_add_u32 m0, %[lw], 13312\ns_add_u32 s44, %[wo], 4325376\nbuffer_load_dwordx4 v90, %[rw], s44 offen lds\ns_add_u32 m0, %[lw], 14336\ns_add_u32 s44, %[wo], 4456448\nbuffer_load_dwordx4 v90, %[rw], s44 offen lds\ns_add_u32 m0, %[lw], 15360\ns_add_u32 s44, %[wo], 4587520\n" \
"buffer_load_dwordx4 v90, %[rw], s44 offen lds\ns_waitcnt vmcnt(32)\nds_read_b64_tr_b16 a[0:1], v93 offset:16384\nds_read_b64_tr_b16 a[2:3], v93 offset:16640\nds_read_b64_tr_b16 a[16:17], v93 offset:16392\nds_read_b64_tr_b16 a[18:19], v93 offset:16648\nds_read_b64_tr_b16 a[4:5], v93 offset:18432\nds_read_b64_tr_b16 a[6:7], v93 offset:18688\n" \
"ds_read_b64_tr_b16 a[20:21], v93 offset:18440\nds_read_b64_tr_b16 a[22:23], v93 offset:18696\ns_waitcnt lgkmcnt(0)\ns_add_u32 m0, %[lw], 16384\ns_add_u32 s44, %[wo], 4718592\nbuffer_load_dwordx4 v90, %[rw], s44 offen lds\ns_add_u32 m0, %[lw], 17408\ns_add_u32 s44, %[wo], 4849664\n" \
"buffer_load_dwordx4 v90, %[rw], s44 offen lds\ns_add_u32 m0, %[lw], 18432\ns_add_u32 s44, %[wo], 4980736\nbuffer_load_dwordx4 v90, %[rw], s44 offen lds\ns_add_u32 m0, %[lw], 19456\ns_add_u32 s44, %[wo], 5111808\nbuffer_load_dwordx4 v90, %[rw], s44 offen lds\ns_waitcnt vmcnt(32)\n" \
"ds_read_b64_tr_b16 a[32:33], v93 offset:20480\nds_read_b64_tr_b16 a[34:35], v93 offset:20736\nds_read_b64_tr_b16 a[48:49], v93 offset:20488\nds_read_b64_tr_b16 a[50:51], v93 offset:20744\nds_read_b64_tr_b16 a[36:37], v93 offset:22528\nds_read_b64_tr_b16 a[38:39], v93 offset:22784\nds_read_b64_tr_b16 a[52:53], v93 offset:22536\nds_read_b64_tr_b16 a[54:55], v93 offset:22792\n" \
"s_waitcnt lgkmcnt(0)\ns_add_u32 m0, %[lw], 20480\ns_add_u32 s44, %[wo], 5242880\nbuffer_load_dwordx4 v90, %[rw], s44 offen lds\ns_add_u32 m0, %[lw], 21504\ns_add_u32 s44, %[wo], 5373952\nbuffer_load_dwordx4 v90, %[rw], s44 offen lds\ns_add_u32 m0, %[lw], 22528\n" \
"s_add_u32 s44, %[wo], 5505024\nbuffer_load_dwordx4 v90, %[rw], s44 offen lds\ns_add_u32 m0, %[lw], 23552\ns_add_u32 s44, %[wo], 5636096\nbuffer_load_dwordx4 v90, %[rw], s44 offen lds\ns_waitcnt vmcnt(32)\nds_read_b64_tr_b16 a[56:57], v92 offset:0\nds_read_b64_tr_b16 a[58:59], v92 offset:256\n" \
"ds_read_b64_tr_b16 v[84:85], v92 offset:8\nds_read_b64_tr_b16 v[86:87], v92 offset:264\nds_read_b64_tr_b16 a[60:61], v92 offset:2048\nds_read_b64_tr_b16 a[62:63], v92 offset:2304\nds_read_b64_tr_b16 v[80:81], v92 offset:2056\nds_read_b64_tr_b16 v[82:83], v92 offset:2312\ns_waitcnt lgkmcnt(0)\ns_add_u32 m0, %[tb], 0\n" \
"s_add_u32 s44, %[wo], 5767168\nbuffer_load_dwordx4 v90, %[rw], s44 offen lds\ns_add_u32 m0, %[tb], 1024\ns_add_u32 s44, %[wo], 5898240\nbuffer_load_dwordx4 v90, %[rw], s44 offen lds\ns_add_u32 m0, %[tb], 2048\ns_add_u32 s44, %[wo], 6029312\nbuffer_load_dwordx4 v90, %[rw], s44 offen lds\n" \
"s_add_u32 m0, %[tb], 3072\ns_add_u32 s44, %[wo], 6160384\nbuffer_load_dwordx4 v90, %[rw], s44 offen lds\ns_waitcnt vmcnt(32)\nds_read_b64_tr_b16 a[80:81], v92 offset:4096\nds_read_b64_tr_b16 a[82:83], v92 offset:4352\nds_read_b64_tr_b16 a[96:97], v92 offset:4104\nds_read_b64_tr_b16 a[98:99], v92 offset:4360\n" \
"ds_read_b64_tr_b16 a[84:85], v92 offset:6144\nds_read_b64_tr_b16 a[86:87], v92 offset:6400\nds_read_b64_tr_b16 a[100:101], v92 offset:6152\nds_read_b64_tr_b16 a[102:103], v92 offset:6408\ns_waitcnt lgkmcnt(0)\ns_add_u32 m0, %[tb], 4096\ns_add_u32 s44, %[wo], 6291456\nbuffer_load_dwordx4 v90, %[rw], s44 offen lds\n" \
"s_add_u32 m0, %[tb], 5120\ns_add_u32 s44, %[wo], 6422528\nbuffer_load_dwordx4 v90, %[rw], s44 offen lds\ns_add_u32 m0, %[tb], 6144\ns_add_u32 s44, %[wo], 6553600\nbuffer_load_dwordx4 v90, %[rw], s44 offen lds\ns_add_u32 m0, %[tb], 7168\ns_add_u32 s44, %[wo], 6684672\n" \
"buffer_load_dwordx4 v90, %[rw], s44 offen lds\ns_waitcnt vmcnt(32)\nds_read_b64_tr_b16 a[104:105], v92 offset:8192\nds_read_b64_tr_b16 a[106:107], v92 offset:8448\nds_read_b64_tr_b16 a[120:121], v92 offset:8200\nds_read_b64_tr_b16 a[122:123], v92 offset:8456\nds_read_b64_tr_b16 a[108:109], v92 offset:10240\nds_read_b64_tr_b16 a[110:111], v92 offset:10496\n" \
"ds_read_b64_tr_b16 a[124:125], v92 offset:10248\nds_read_b64_tr_b16 a[126:127], v92 offset:10504\ns_waitcnt lgkmcnt(0)\ns_add_u32 m0, %[tb], 8192\ns_add_u32 s44, %[wo], 6815744\nbuffer_load_dwordx4 v90, %[rw], s44 offen lds\ns_add_u32 m0, %[tb], 9216\ns_add_u32 s44, %[wo], 6946816\n" \
"buffer_load_dwordx4 v90, %[rw], s44 offen lds\ns_add_u32 m0, %[tb], 10240\ns_add_u32 s44, %[wo], 7077888\nbuffer_load_dwordx4 v90, %[rw], s44 offen lds\ns_add_u32 m0, %[tb], 11264\ns_add_u32 s44, %[wo], 7208960\nbuffer_load_dwordx4 v90, %[rw], s44 offen lds\ns_waitcnt vmcnt(32)\n" \
"ds_read_b64_tr_b16 a[136:137], v93 offset:0\nds_read_b64_tr_b16 a[138:139], v93 offset:256\nds_read_b64_tr_b16 a[152:153], v93 offset:8\nds_read_b64_tr_b16 a[154:155], v93 offset:264\nds_read_b64_tr_b16 a[140:141], v93 offset:2048\nds_read_b64_tr_b16 a[142:143], v93 offset:2304\nds_read_b64_tr_b16 a[156:157], v93 offset:2056\nds_read_b64_tr_b16 a[158:159], v93 offset:2312\n" \
"s_waitcnt lgkmcnt(0)\ns_add_u32 m0, %[lw], 0\ns_add_u32 s44, %[wo], 7340032\nbuffer_load_dwordx4 v90, %[rw], s44 offen lds\ns_add_u32 m0, %[lw], 1024\ns_add_u32 s44, %[wo], 7471104\nbuffer_load_dwordx4 v90, %[rw], s44 offen lds\ns_add_u32 m0, %[lw], 2048\n" \
"s_add_u32 s44, %[wo], 7602176\nbuffer_load_dwordx4 v90, %[rw], s44 offen lds\ns_add_u32 m0, %[lw], 3072\ns_add_u32 s44, %[wo], 7733248\nbuffer_load_dwordx4 v90, %[rw], s44 offen lds\ns_waitcnt vmcnt(32)\nds_read_b64_tr_b16 a[160:161], v93 offset:4096\nds_read_b64_tr_b16 a[162:163], v93 offset:4352\n" \
"ds_read_b64_tr_b16 v[76:77], v93 offset:4104\nds_read_b64_tr_b16 v[78:79], v93 offset:4360\nds_read_b64_tr_b16 a[164:165], v93 offset:6144\nds_read_b64_tr_b16 a[166:167], v93 offset:6400\nds_read_b64_tr_b16 v[72:73], v93 offset:6152\nds_read_b64_tr_b16 v[74:75], v93 offset:6408\ns_waitcnt lgkmcnt(0)\ns_add_u32 m0, %[lw], 4096\n" \
"s_add_u32 s44, %[wo], 7864320\nbuffer_load_dwordx4 v90, %[rw], s44 offen lds\ns_add_u32 m0, %[lw], 5120\ns_add_u32 s44, %[wo], 7995392\nbuffer_load_dwordx4 v90, %[rw], s44 offen lds\ns_add_u32 m0, %[lw], 6144\ns_add_u32 s44, %[wo], 8126464\nbuffer_load_dwordx4 v90, %[rw], s44 offen lds\n" \
"s_add_u32 m0, %[lw], 7168\ns_add_u32 s44, %[wo], 8257536\nbuffer_load_dwordx4 v90, %[rw], s44 offen lds\ns_waitcnt vmcnt(32)\nds_read_b64_tr_b16 a[184:185], v93 offset:8192\nds_read_b64_tr_b16 a[186:187], v93 offset:8448\nds_read_b64_tr_b16 a[200:201], v93 offset:8200\nds_read_b64_tr_b16 a[202:203], v93 offset:8456\n" \
"ds_read_b64_tr_b16 a[188:189], v93 offset:10240\nds_read_b64_tr_b16 a[190:191], v93 offset:10496\nds_read_b64_tr_b16 a[204:205], v93 offset:10248\nds_read_b64_tr_b16 a[206:207], v93 offset:10504\ns_waitcnt lgkmcnt(0)\ns_add_u32 m0, %[lw], 8192\ns_add_u32 s44, %[w2], 0\nbuffer_load_dwordx4 v91, %[ro], s44 offen lds\n" \
"s_add_u32 m0, %[lw], 9216\ns_add_u32 s44, %[w2], 16384\nbuffer_load_dwordx4 v91, %[ro], s44 offen lds\ns_add_u32 m0, %[lw], 10240\ns_add_u32 s44, %[w2], 32768\nbuffer_load_dwordx4 v91, %[ro], s44 offen lds\ns_add_u32 m0, %[lw], 11264\ns_add_u32 s44, %[w2], 49152\n" \
"buffer_load_dwordx4 v91, %[ro], s44 offen lds\ns_waitcnt vmcnt(32)\nds_read_b64_tr_b16 a[208:209], v93 offset:12288\nds_read_b64_tr_b16 a[210:211], v93 offset:12544\nds_read_b64_tr_b16 a[224:225], v93 offset:12296\nds_read_b64_tr_b16 a[226:227], v93 offset:12552\nds_read_b64_tr_b16 a[212:213], v93 offset:14336\nds_read_b64_tr_b16 a[214:215], v93 offset:14592\n" \
"ds_read_b64_tr_b16 a[228:229], v93 offset:14344\nds_read_b64_tr_b16 a[230:231], v93 offset:14600\ns_waitcnt lgkmcnt(0)\ns_add_u32 m0, %[lw], 12288\ns_add_u32 s44, %[w2], 128\nbuffer_load_dwordx4 v91, %[ro], s44 offen lds\ns_add_u32 m0, %[lw], 13312\ns_add_u32 s44, %[w2], 16512\n" \
"buffer_load_dwordx4 v91, %[ro], s44 offen lds\ns_add_u32 m0, %[lw], 14336\ns_add_u32 s44, %[w2], 32896\nbuffer_load_dwordx4 v91, %[ro], s44 offen lds\ns_add_u32 m0, %[lw], 15360\ns_add_u32 s44, %[w2], 49280\nbuffer_load_dwordx4 v91, %[ro], s44 offen lds\ns_waitcnt vmcnt(32)\n" \
"ds_read_b64_tr_b16 a[240:241], v93 offset:16384\nds_read_b64_tr_b16 a[242:243], v93 offset:16640\nds_read_b64_tr_b16 v[96:97], v93 offset:16392\nds_read_b64_tr_b16 v[98:99], v93 offset:16648\nds_read_b64_tr_b16 a[244:245], v93 offset:18432\nds_read_b64_tr_b16 a[246:247], v93 offset:18688\nds_read_b64_tr_b16 v[100:101], v93 offset:18440\nds_read_b64_tr_b16 v[102:103], v93 offset:18696\n" \
"s_waitcnt lgkmcnt(0)\ns_add_u32 m0, %[lw], 16384\ns_add_u32 s44, %[w2], 256\nbuffer_load_dwordx4 v91, %[ro], s44 offen lds\ns_add_u32 m0, %[lw], 17408\ns_add_u32 s44, %[w2], 16640\nbuffer_load_dwordx4 v91, %[ro], s44 offen lds\ns_add_u32 m0, %[lw], 18432\n" \
"s_add_u32 s44, %[w2], 33024\nbuffer_load_dwordx4 v91, %[ro], s44 offen lds\ns_add_u32 m0, %[lw], 19456\ns_add_u32 s44, %[w2], 49408\nbuffer_load_dwordx4 v91, %[ro], s44 offen lds\ns_waitcnt vmcnt(32)\nds_read_b64_tr_b16 v[104:105], v93 offset:20480\nds_read_b64_tr_b16 v[106:107], v93 offset:20736\n" \
"ds_read_b64_tr_b16 v[68:69], v93 offset:20488\nds_read_b64_tr_b16 v[70:71], v93 offset:20744\nds_read_b64_tr_b16 v[108:109], v93 offset:22528\nds_read_b64_tr_b16 v[110:111], v93 offset:22784\nds_read_b64_tr_b16 v[64:65], v93 offset:22536\nds_read_b64_tr_b16 v[66:67], v93 offset:22792\ns_waitcnt lgkmcnt(0)\ns_add_u32 m0, %[lw], 20480\n" \
"s_add_u32 s44, %[w2], 384\nbuffer_load_dwordx4 v91, %[ro], s44 offen lds\ns_add_u32 m0, %[lw], 21504\ns_add_u32 s44, %[w2], 16768\nbuffer_load_dwordx4 v91, %[ro], s44 offen lds\ns_add_u32 m0, %[lw], 22528\ns_add_u32 s44, %[w2], 33152\nbuffer_load_dwordx4 v91, %[ro], s44 offen lds\n" \
"s_add_u32 m0, %[lw], 23552\ns_add_u32 s44, %[w2], 49536\nbuffer_load_dwordx4 v91, %[ro], s44 offen lds\ns_waitcnt vmcnt(32)\nds_read_b64_tr_b16 v[128:129], v92 offset:0\nds_read_b64_tr_b16 v[130:131], v92 offset:256\nds_read_b64_tr_b16 v[144:145], v92 offset:8\nds_read_b64_tr_b16 v[146:147], v92 offset:264\n" \
"ds_read_b64_tr_b16 v[132:133], v92 offset:2048\nds_read_b64_tr_b16 v[134:135], v92 offset:2304\nds_read_b64_tr_b16 v[148:149], v92 offset:2056\nds_read_b64_tr_b16 v[150:151], v92 offset:2312\ns_waitcnt lgkmcnt(0)\ns_add_u32 m0, %[tb], 0\ns_add_u32 s44, %[w2], 65536\nbuffer_load_dwordx4 v91, %[ro], s44 offen lds\n" \
"s_add_u32 m0, %[tb], 1024\ns_add_u32 s44, %[w2], 81920\nbuffer_load_dwordx4 v91, %[ro], s44 offen lds\ns_add_u32 m0, %[tb], 2048\ns_add_u32 s44, %[w2], 98304\nbuffer_load_dwordx4 v91, %[ro], s44 offen lds\ns_add_u32 m0, %[tb], 3072\ns_add_u32 s44, %[w2], 114688\n" \
"buffer_load_dwordx4 v91, %[ro], s44 offen lds\ns_waitcnt vmcnt(32)\nds_read_b64_tr_b16 v[152:153], v92 offset:4096\nds_read_b64_tr_b16 v[154:155], v92 offset:4352\nds_read_b64_tr_b16 v[168:169], v92 offset:4104\nds_read_b64_tr_b16 v[170:171], v92 offset:4360\nds_read_b64_tr_b16 v[156:157], v92 offset:6144\nds_read_b64_tr_b16 v[158:159], v92 offset:6400\n" \
"ds_read_b64_tr_b16 v[172:173], v92 offset:6152\nds_read_b64_tr_b16 v[174:175], v92 offset:6408\ns_waitcnt lgkmcnt(0)\ns_add_u32 m0, %[tb], 4096\ns_add_u32 s44, %[w2], 65664\nbuffer_load_dwordx4 v91, %[ro], s44 offen lds\ns_add_u32 m0, %[tb], 5120\ns_add_u32 s44, %[w2], 82048\n" \
"buffer_load_dwordx4 v91, %[ro], s44 offen lds\ns_add_u32 m0, %[tb], 6144\ns_add_u32 s44, %[w2], 98432\nbuffer_load_dwordx4 v91, %[ro], s44 offen lds\ns_add_u32 m0, %[tb], 7168\ns_add_u32 s44, %[w2], 114816\nbuffer_load_dwordx4 v91, %[ro], s44 offen lds\ns_waitcnt vmcnt(32)\n" \
"ds_read_b64_tr_b16 v[184:185], v92 offset:8192\nds_read_b64_tr_b16 v[186:187], v92 offset:8448\nds_read_b64_tr_b16 v[200:201], v92 offset:8200\nds_read_b64_tr_b16 v[202:203], v92 offset:8456\nds_read_b64_tr_b16 v[188:189], v92 offset:10240\nds_read_b64_tr_b16 v[190:191], v92 offset:10496\nds_read_b64_tr_b16 v[204:205], v92 offset:10248\nds_read_b64_tr_b16 v[206:207], v92 offset:10504\n" \
"s_waitcnt lgkmcnt(0)\ns_add_u32 m0, %[tb], 8192\ns_add_u32 s44, %[w2], 65792\nbuffer_load_dwordx4 v91, %[ro], s44 offen lds\ns_add_u32 m0, %[tb], 9216\ns_add_u32 s44, %[w2], 82176\nbuffer_load_dwordx4 v91, %[ro], s44 offen lds\ns_add_u32 m0, %[tb], 10240\n" \
"s_add_u32 s44, %[w2], 98560\nbuffer_load_dwordx4 v91, %[ro], s44 offen lds\ns_add_u32 m0, %[tb], 11264\ns_add_u32 s44, %[w2], 114944\nbuffer_load_dwordx4 v91, %[ro], s44 offen lds\ns_waitcnt vmcnt(32)\nds_read_b64_tr_b16 v[208:209], v93 offset:0\nds_read_b64_tr_b16 v[210:211], v93 offset:256\n" \
"ds_read_b64_tr_b16 v[60:61], v93 offset:8\nds_read_b64_tr_b16 v[62:63], v93 offset:264\nds_read_b64_tr_b16 v[212:213], v93 offset:2048\nds_read_b64_tr_b16 v[214:215], v93 offset:2304\nds_read_b64_tr_b16 v[56:57], v93 offset:2056\nds_read_b64_tr_b16 v[58:59], v93 offset:2312\ns_waitcnt lgkmcnt(0)\ns_add_u32 m0, %[lw], 0\n" \
"s_add_u32 s44, %[w2], 65920\nbuffer_load_dwordx4 v91, %[ro], s44 offen lds\ns_add_u32 m0, %[lw], 1024\ns_add_u32 s44, %[w2], 82304\nbuffer_load_dwordx4 v91, %[ro], s44 offen lds\ns_add_u32 m0, %[lw], 2048\ns_add_u32 s44, %[w2], 98688\nbuffer_load_dwordx4 v91, %[ro], s44 offen lds\n" \
"s_add_u32 m0, %[lw], 3072\ns_add_u32 s44, %[w2], 115072\nbuffer_load_dwordx4 v91, %[ro], s44 offen lds\ns_waitcnt vmcnt(32)\nds_read_b64_tr_b16 v[232:233], v93 offset:4096\nds_read_b64_tr_b16 v[234:235], v93 offset:4352\nds_read_b64_tr_b16 v[248:249], v93 offset:4104\nds_read_b64_tr_b16 v[250:251], v93 offset:4360\n" \
"ds_read_b64_tr_b16 v[236:237], v93 offset:6144\nds_read_b64_tr_b16 v[238:239], v93 offset:6400\nds_read_b64_tr_b16 v[252:253], v93 offset:6152\nds_read_b64_tr_b16 v[254:255], v93 offset:6408\ns_waitcnt lgkmcnt(0)\ns_add_u32 m0, %[lw], 4096\ns_add_u32 s44, %[w2], 131072\nbuffer_load_dwordx4 v91, %[ro], s44 offen lds\n" \
"s_add_u32 m0, %[lw], 5120\ns_add_u32 s44, %[w2], 147456\nbuffer_load_dwordx4 v91, %[ro], s44 offen lds\ns_add_u32 m0, %[lw], 6144\ns_add_u32 s44, %[w2], 163840\nbuffer_load_dwordx4 v91, %[ro], s44 offen lds\ns_add_u32 m0, %[lw], 7168\ns_add_u32 s44, %[w2], 180224\n" \
"buffer_load_dwordx4 v91, %[ro], s44 offen lds\ns_waitcnt vmcnt(32)\nds_read_b64_tr_b16 a[8:9], v95 offset:8192\nds_read_b64_tr_b16 a[10:11], v95 offset:8448\nds_read_b64_tr_b16 a[112:113], v95 offset:8224\nds_read_b64_tr_b16 a[114:115], v95 offset:8480\nds_read_b64_tr_b16 a[216:217], v95 offset:8256\nds_read_b64_tr_b16 a[218:219], v95 offset:8512\n" \
"ds_read_b64_tr_b16 v[160:161], v95 offset:8288\nds_read_b64_tr_b16 v[162:163], v95 offset:8544\ns_waitcnt lgkmcnt(0)\ns_add_u32 m0, %[lw], 8192\ns_add_u32 s44, %[w2], 131200\nbuffer_load_dwordx4 v91, %[ro], s44 offen lds\ns_add_u32 m0, %[lw], 9216\ns_add_u32 s44, %[w2], 147584\n" \
"buffer_load_dwordx4 v91, %[ro], s44 offen lds\ns_add_u32 m0, %[lw], 10240\ns_add_u32 s44, %[w2], 163968\nbuffer_load_dwordx4 v91, %[ro], s44 offen lds\ns_add_u32 m0, %[lw], 11264\ns_add_u32 s44, %[w2], 180352\nbuffer_load_dwordx4 v91, %[ro], s44 offen lds\ns_waitcnt vmcnt(32)\n" \
"ds_read_b64_tr_b16 a[40:41], v95 offset:12288\nds_read_b64_tr_b16 a[42:43], v95 offset:12544\nds_read_b64_tr_b16 a[144:145], v95 offset:12320\nds_read_b64_tr_b16 a[146:147], v95 offset:12576\nds_read_b64_tr_b16 a[248:249], v95 offset:12352\nds_read_b64_tr_b16 a[250:251], v95 offset:12608\nds_read_b64_tr_b16 v[192:193], v95 offset:12384\nds_read_b64_tr_b16 v[194:195], v95 offset:12640\n" \
"s_waitcnt lgkmcnt(0)\ns_add_u32 m0, %[lw], 12288\ns_add_u32 s44, %[w2], 131328\nbuffer_load_dwordx4 v91, %[ro], s44 offen lds\ns_add_u32 m0, %[lw], 13312\ns_add_u32 s44, %[w2], 147712\nbuffer_load_dwordx4 v91, %[ro], s44 offen lds\ns_add_u32 m0, %[lw], 14336\n" \
"s_add_u32 s44, %[w2], 164096\nbuffer_load_dwordx4 v91, %[ro], s44 offen lds\ns_add_u32 m0, %[lw], 15360\ns_add_u32 s44, %[w2], 180480\nbuffer_load_dwordx4 v91, %[ro], s44 offen lds\ns_waitcnt vmcnt(32)\nds_read_b64_tr_b16 a[64:65], v95 offset:16384\nds_read_b64_tr_b16 a[66:67], v95 offset:16640\n" \
"ds_read_b64_tr_b16 a[168:169], v95 offset:16416\nds_read_b64_tr_b16 a[170:171], v95 offset:16672\nds_read_b64_tr_b16 v[112:113], v95 offset:16448\nds_read_b64_tr_b16 v[114:115], v95 offset:16704\nds_read_b64_tr_b16 v[216:217], v95 offset:16480\nds_read_b64_tr_b16 v[218:219], v95 offset:16736\ns_waitcnt lgkmcnt(0)\ns_add_u32 m0, %[lw], 16384\n" \
"s_add_u32 s44, %[w2], 131456\nbuffer_load_dwordx4 v91, %[ro], s44 offen lds\ns_add_u32 m0, %[lw], 17408\ns_add_u32 s44, %[w2], 147840\nbuffer_load_dwordx4 v91, %[ro], s44 offen lds\ns_add_u32 m0, %[lw], 18432\ns_add_u32 s44, %[w2], 164224\nbuffer_load_dwordx4 v91, %[ro], s44 offen lds\n" \
"s_add_u32 m0, %[lw], 19456\ns_add_u32 s44, %[w2], 180608\nbuffer_load_dwordx4 v91, %[ro], s44 offen lds\ns_waitcnt vmcnt(32)\nds_read_b64_tr_b16 a[88:89], v95 offset:20480\nds_read_b64_tr_b16 a[90:91], v95 offset:20736\nds_read_b64_tr_b16 a[192:193], v95 offset:20512\nds_read_b64_tr_b16 a[194:195], v95 offset:20768\n" \
"ds_read_b64_tr_b16 v[136:137], v95 offset:20544\nds_read_b64_tr_b16 v[138:139], v95 offset:20800\nds_read_b64_tr_b16 v[240:241], v95 offset:20576\nds_read_b64_tr_b16 v[242:243], v95 offset:20832\ns_waitcnt lgkmcnt(0)\ns_add_u32 m0, %[lw], 20480\ns_add_u32 s44, %[w2], 196608\nbuffer_load_dwordx4 v91, %[ro], s44 offen lds\n" \
"s_add_u32 m0, %[lw], 21504\ns_add_u32 s44, %[w2], 212992\nbuffer_load_dwordx4 v91, %[ro], s44 offen lds\ns_add_u32 m0, %[lw], 22528\ns_add_u32 s44, %[w2], 229376\nbuffer_load_dwordx4 v91, %[ro], s44 offen lds\ns_add_u32 m0, %[lw], 23552\ns_add_u32 s44, %[w2], 245760\n" \
"buffer_load_dwordx4 v91, %[ro], s44 offen lds\ns_waitcnt vmcnt(32)\nds_read_b64_tr_b16 a[12:13], v94 offset:0\nds_read_b64_tr_b16 a[14:15], v94 offset:256\nds_read_b64_tr_b16 a[116:117], v94 offset:32\nds_read_b64_tr_b16 a[118:119], v94 offset:288\nds_read_b64_tr_b16 a[220:221], v94 offset:64\nds_read_b64_tr_b16 a[222:223], v94 offset:320\n" \
"ds_read_b64_tr_b16 v[164:165], v94 offset:96\nds_read_b64_tr_b16 v[166:167], v94 offset:352\ns_waitcnt lgkmcnt(0)\ns_add_u32 m0, %[tb], 0\ns_add_u32 s44, %[w2], 196736\nbuffer_load_dwordx4 v91, %[ro], s44 offen lds\ns_add_u32 m0, %[tb], 1024\ns_add_u32 s44, %[w2], 213120\n" \
"buffer_load_dwordx4 v91, %[ro], s44 offen lds\ns_add_u32 m0, %[tb], 2048\ns_add_u32 s44, %[w2], 229504\nbuffer_load_dwordx4 v91, %[ro], s44 offen lds\ns_add_u32 m0, %[tb], 3072\ns_add_u32 s44, %[w2], 245888\nbuffer_load_dwordx4 v91, %[ro], s44 offen lds\ns_waitcnt vmcnt(32)\n" \
"ds_read_b64_tr_b16 a[44:45], v94 offset:4096\nds_read_b64_tr_b16 a[46:47], v94 offset:4352\nds_read_b64_tr_b16 a[148:149], v94 offset:4128\nds_read_b64_tr_b16 a[150:151], v94 offset:4384\nds_read_b64_tr_b16 a[252:253], v94 offset:4160\nds_read_b64_tr_b16 a[254:255], v94 offset:4416\nds_read_b64_tr_b16 v[196:197], v94 offset:4192\nds_read_b64_tr_b16 v[198:199], v94 offset:4448\n" \
"s_waitcnt lgkmcnt(0)\ns_add_u32 m0, %[tb], 4096\ns_add_u32 s44, %[w2], 196864\nbuffer_load_dwordx4 v91, %[ro], s44 offen lds\ns_add_u32 m0, %[tb], 5120\ns_add_u32 s44, %[w2], 213248\nbuffer_load_dwordx4 v91, %[ro], s44 offen lds\ns_add_u32 m0, %[tb], 6144\n" \
"s_add_u32 s44, %[w2], 229632\nbuffer_load_dwordx4 v91, %[ro], s44 offen lds\ns_add_u32 m0, %[tb], 7168\ns_add_u32 s44, %[w2], 246016\nbuffer_load_dwordx4 v91, %[ro], s44 offen lds\ns_waitcnt vmcnt(32)\nds_read_b64_tr_b16 a[68:69], v94 offset:8192\nds_read_b64_tr_b16 a[70:71], v94 offset:8448\n" \
"ds_read_b64_tr_b16 a[172:173], v94 offset:8224\nds_read_b64_tr_b16 a[174:175], v94 offset:8480\nds_read_b64_tr_b16 v[116:117], v94 offset:8256\nds_read_b64_tr_b16 v[118:119], v94 offset:8512\nds_read_b64_tr_b16 v[220:221], v94 offset:8288\nds_read_b64_tr_b16 v[222:223], v94 offset:8544\ns_waitcnt lgkmcnt(0)\ns_add_u32 m0, %[tb], 8192\n" \
"s_add_u32 s44, %[w2], 196992\nbuffer_load_dwordx4 v91, %[ro], s44 offen lds\ns_add_u32 m0, %[tb], 9216\ns_add_u32 s44, %[w2], 213376\nbuffer_load_dwordx4 v91, %[ro], s44 offen lds\ns_add_u32 m0, %[tb], 10240\ns_add_u32 s44, %[w2], 229760\nbuffer_load_dwordx4 v91, %[ro], s44 offen lds\n" \
"s_add_u32 m0, %[tb], 11264\ns_add_u32 s44, %[w2], 246144\nbuffer_load_dwordx4 v91, %[ro], s44 offen lds\ns_waitcnt vmcnt(32)\nds_read_b64_tr_b16 a[92:93], v95 offset:0\nds_read_b64_tr_b16 a[94:95], v95 offset:256\nds_read_b64_tr_b16 a[196:197], v95 offset:32\nds_read_b64_tr_b16 a[198:199], v95 offset:288\n" \
"ds_read_b64_tr_b16 v[140:141], v95 offset:64\nds_read_b64_tr_b16 v[142:143], v95 offset:320\nds_read_b64_tr_b16 v[244:245], v95 offset:96\nds_read_b64_tr_b16 v[246:247], v95 offset:352\ns_waitcnt lgkmcnt(0)\nds_write_b128 v1, v[84:87] offset:0\nds_write_b128 v1, v[80:83] offset:1024\nds_write_b128 v1, v[76:79] offset:2048\n" \
"ds_write_b128 v1, v[72:75] offset:3072\ns_waitcnt vmcnt(28)\nds_read_b64_tr_b16 a[24:25], v95 offset:4096\nds_read_b64_tr_b16 a[26:27], v95 offset:4352\nds_read_b64_tr_b16 a[128:129], v95 offset:4128\nds_read_b64_tr_b16 a[130:131], v95 offset:4384\nds_read_b64_tr_b16 a[232:233], v95 offset:4160\nds_read_b64_tr_b16 a[234:235], v95 offset:4416\n" \
"ds_read_b64_tr_b16 v[176:177], v95 offset:4192\nds_read_b64_tr_b16 v[178:179], v95 offset:4448\ns_waitcnt lgkmcnt(0)\nds_write_b128 v1, v[68:71] offset:4096\nds_write_b128 v1, v[64:67] offset:5120\nds_write_b128 v1, v[60:63] offset:6144\nds_write_b128 v1, v[56:59] offset:7168\ns_waitcnt vmcnt(24)\n" \
"ds_read_b64_tr_b16 v[56:57], v95 offset:8192\nds_read_b64_tr_b16 v[58:59], v95 offset:8448\nds_read_b64_tr_b16 v[60:61], v95 offset:8224\nds_read_b64_tr_b16 v[62:63], v95 offset:8480\nds_read_b64_tr_b16 v[64:65], v95 offset:8256\nds_read_b64_tr_b16 v[66:67], v95 offset:8512\nds_read_b64_tr_b16 v[68:69], v95 offset:8288\nds_read_b64_tr_b16 v[70:71], v95 offset:8544\n" \
"s_waitcnt lgkmcnt(0)\nds_write_b128 v1, v[56:59] offset:8192\ns_waitcnt vmcnt(20)\nds_read_b64_tr_b16 a[72:73], v95 offset:12288\nds_read_b64_tr_b16 a[74:75], v95 offset:12544\nds_read_b64_tr_b16 a[176:177], v95 offset:12320\nds_read_b64_tr_b16 a[178:179], v95 offset:12576\nds_read_b64_tr_b16 v[120:121], v95 offset:12352\n" \
"ds_read_b64_tr_b16 v[122:123], v95 offset:12608\nds_read_b64_tr_b16 v[224:225], v95 offset:12384\nds_read_b64_tr_b16 v[226:227], v95 offset:12640\ns_waitcnt lgkmcnt(0)\nds_write_b128 v1, v[60:63] offset:12288\ns_waitcnt vmcnt(16)\nds_read_b64_tr_b16 v[60:61], v95 offset:16384\nds_read_b64_tr_b16 v[62:63], v95 offset:16640\n" \
"ds_read_b64_tr_b16 v[56:57], v95 offset:16416\nds_read_b64_tr_b16 v[58:59], v95 offset:16672\nds_read_b64_tr_b16 v[72:73], v95 offset:16448\nds_read_b64_tr_b16 v[74:75], v95 offset:16704\nds_read_b64_tr_b16 v[76:77], v95 offset:16480\nds_read_b64_tr_b16 v[78:79], v95 offset:16736\ns_waitcnt lgkmcnt(0)\nds_write_b128 v1, v[64:67] offset:16384\n" \
"ds_write_b128 v1, v[60:63] offset:10240\nds_write_b128 v1, v[56:59] offset:14336\nds_write_b128 v1, v[72:75] offset:18432\ns_waitcnt vmcnt(12)\nds_read_b64_tr_b16 a[28:29], v95 offset:20480\nds_read_b64_tr_b16 a[30:31], v95 offset:20736\nds_read_b64_tr_b16 a[132:133], v95 offset:20512\nds_read_b64_tr_b16 a[134:135], v95 offset:20768\n" \
"ds_read_b64_tr_b16 a[236:237], v95 offset:20544\nds_read_b64_tr_b16 a[238:239], v95 offset:20800\nds_read_b64_tr_b16 v[180:181], v95 offset:20576\nds_read_b64_tr_b16 v[182:183], v95 offset:20832\ns_waitcnt lgkmcnt(0)\nds_write_b128 v1, v[68:71] offset:20480\nds_write_b128 v1, v[76:79] offset:22528\ns_waitcnt vmcnt(8)\n" \
"ds_read_b64_tr_b16 v[76:77], v94 offset:0\nds_read_b64_tr_b16 v[78:79], v94 offset:256\nds_read_b64_tr_b16 v[68:69], v94 offset:32\nds_read_b64_tr_b16 v[70:71], v94 offset:288\nds_read_b64_tr_b16 v[72:73], v94 offset:64\nds_read_b64_tr_b16 v[74:75], v94 offset:320\nds_read_b64_tr_b16 v[56:57], v94 offset:96\nds_read_b64_tr_b16 v[58:59], v94 offset:352\n" \
"s_waitcnt lgkmcnt(0)\nds_write_b128 v1, v[76:79] offset:9216\nds_write_b128 v1, v[68:71] offset:13312\nds_write_b128 v1, v[72:75] offset:17408\nds_write_b128 v1, v[56:59] offset:21504\ns_waitcnt vmcnt(4)\nds_read_b64_tr_b16 a[76:77], v94 offset:4096\nds_read_b64_tr_b16 a[78:79], v94 offset:4352\n" \
"ds_read_b64_tr_b16 a[180:181], v94 offset:4128\nds_read_b64_tr_b16 a[182:183], v94 offset:4384\nds_read_b64_tr_b16 v[124:125], v94 offset:4160\nds_read_b64_tr_b16 v[126:127], v94 offset:4416\nds_read_b64_tr_b16 v[228:229], v94 offset:4192\nds_read_b64_tr_b16 v[230:231], v94 offset:4448\ns_waitcnt lgkmcnt(0)\ns_waitcnt vmcnt(0)\n" \
"ds_read_b64_tr_b16 v[56:57], v94 offset:8192\nds_read_b64_tr_b16 v[58:59], v94 offset:8448\nds_read_b64_tr_b16 v[72:73], v94 offset:8224\nds_read_b64_tr_b16 v[74:75], v94 offset:8480\nds_read_b64_tr_b16 v[68:69], v94 offset:8256\nds_read_b64_tr_b16 v[70:71], v94 offset:8512\nds_read_b64_tr_b16 v[76:77], v94 offset:8288\nds_read_b64_tr_b16 v[78:79], v94 offset:8544\n" \
"s_waitcnt lgkmcnt(0)\nds_write_b128 v1, v[56:59] offset:11264\nds_write_b128 v1, v[72:75] offset:15360\nds_write_b128 v1, v[68:71] offset:19456\nds_write_b128 v1, v[76:79] offset:23552\ns_waitcnt lgkmcnt(0)\ns_barrier\ns_mov_b32 s40, -1\n" \
"s_mov_b32 s43, 0\ns_lshl_b32 s57, %[w], 12\ns_mov_b32 s56, 0\ns_mov_b32 s42, 0\n.Lvu_slow%=:\ns_cmp_ge_i32 s40, 63\ns_cbranch_scc1 .Lvu_nov%=\n.Lvu_slow2%=:\n" \
"buffer_load_dword v12, v8, %[rs], s56 offen sc1\ns_waitcnt vmcnt(0)\nv_cmp_ge_i32 vcc, s40, v12\ns_cbranch_vccz .Lvu_fok%=\ns_add_u32 s43, s43, 1\ns_cmp_lt_u32 s43, 262144\ns_cbranch_scc1 .Lvu_slow2%=\ns_branch .Lvu_abort%=\n" \
".Lvu_fok%=:\ns_add_u32 s44, s40, 1\ns_lshl_b32 s44, s44, 17\ns_add_u32 s44, s44, %[xn]\ns_lshr_b32 s45, s44, 8\ns_and_b32 s45, s45, 0x2000\ns_xor_b32 s41, s44, s45\ns_add_u32 s41, s41, %[xw]\n" \
"s_add_u32 m0, s57, 0\ns_add_u32 s60, s41, 0\nbuffer_load_dwordx4 v0, %[rs], s60 offen offset:0 sc1 lds\nbuffer_load_dwordx4 v0, %[rs], s60 offen offset:1024 sc1 lds\nbuffer_load_dwordx4 v0, %[rs], s60 offen offset:2048 sc1 lds\nbuffer_load_dwordx4 v0, %[rs], s60 offen offset:3072 sc1 lds\ns_add_u32 m0, s57, 16384\ns_add_u32 s60, s41, 32768\n" \
"buffer_load_dwordx4 v0, %[rs], s60 offen offset:0 sc1 lds\nbuffer_load_dwordx4 v0, %[rs], s60 offen offset:1024 sc1 lds\nbuffer_load_dwordx4 v0, %[rs], s60 offen offset:2048 sc1 lds\nbuffer_load_dwordx4 v0, %[rs], s60 offen offset:3072 sc1 lds\ns_waitcnt vmcnt(0)\n.Lvu_tok%=:\ns_waitcnt vmcnt(12)\ns_barrier\n" \
"ds_read_b128 v[28:31], v2 offset:0\nds_read_b128 v[32:35], v2 offset:256\nds_read_b128 v[36:39], v2 offset:2048\nds_read_b128 v[40:43], v2 offset:2304\nds_read_b128 v[44:47], v2 offset:4096\nds_read_b128 v[60:63], v1 offset:0\nds_read_b128 v[64:67], v1 offset:1024\ns_waitcnt lgkmcnt(6)\n" \
"v_mfma_f32_16x16x32_bf16 v[12:15], a[0:3], v[28:31], 0\nds_read_b128 v[48:51], v2 offset:4352\nv_mfma_f32_16x16x32_bf16 v[16:19], a[16:19], v[28:31], 0\ns_waitcnt lgkmcnt(6)\nv_mfma_f32_16x16x32_bf16 v[20:23], a[0:3], v[32:35], 0\nds_read_b128 v[28:31], v2 offset:6144\nv_mfma_f32_16x16x32_bf16 v[24:27], a[16:19], v[32:35], 0\ns_add_u32 m0, s57, 32768\n" \
"s_add_u32 s60, s41, 65536\nbuffer_load_dwordx4 v0, %[rs], s60 offen offset:0 sc1 lds\ns_waitcnt lgkmcnt(6)\nv_mfma_f32_16x16x32_bf16 v[12:15], a[4:7], v[36:39], v[12:15]\nds_read_b128 v[32:35], v2 offset:6400\nv_mfma_f32_16x16x32_bf16 v[16:19], a[20:23], v[36:39], v[16:19]\ns_waitcnt lgkmcnt(6)\nv_mfma_f32_16x16x32_bf16 v[20:23], a[4:7], v[40:43], v[20:23]\n" \
"ds_read_b128 v[36:39], v2 offset:8192\nv_mfma_f32_16x16x32_bf16 v[24:27], a[20:23], v[40:43], v[24:27]\nbuffer_load_dwordx4 v0, %[rs], s60 offen offset:1024 sc1 lds\ns_waitcnt lgkmcnt(6)\nv_mfma_f32_16x16x32_bf16 v[12:15], a[32:35], v[44:47], v[12:15]\nds_read_b128 v[40:43], v2 offset:8448\nv_mfma_f32_16x16x32_bf16 v[16:19], a[48:51], v[44:47], v[16:19]\ns_waitcnt lgkmcnt(4)\n" \
"v_mfma_f32_16x16x32_bf16 v[20:23], a[32:35], v[48:51], v[20:23]\nds_read_b128 v[44:47], v2 offset:10240\nv_mfma_f32_16x16x32_bf16 v[24:27], a[48:51], v[48:51], v[24:27]\nbuffer_load_dwordx4 v0, %[rs], s60 offen offset:2048 sc1 lds\ns_waitcnt lgkmcnt(4)\nv_mfma_f32_16x16x32_bf16 v[12:15], a[36:39], v[28:31], v[12:15]\nds_read_b128 v[48:51], v2 offset:10496\nv_mfma_f32_16x16x32_bf16 v[16:19], a[52:55], v[28:31], v[16:19]\n" \
"s_waitcnt lgkmcnt(4)\nv_mfma_f32_16x16x32_bf16 v[20:23], a[36:39], v[32:35], v[20:23]\nds_read_b128 v[28:31], v2 offset:12288\nv_mfma_f32_16x16x32_bf16 v[24:27], a[52:55], v[32:35], v[24:27]\nbuffer_load_dwordx4 v0, %[rs], s60 offen offset:3072 sc1 lds\ns_waitcnt lgkmcnt(4)\nv_mfma_f32_16x16x32_bf16 v[12:15], a[56:59], v[36:39], v[12:15]\nds_read_b128 v[32:35], v2 offset:12544\n" \
"v_mfma_f32_16x16x32_bf16 v[16:19], v[60:63], v[36:39], v[16:19]\ns_waitcnt lgkmcnt(4)\nv_mfma_f32_16x16x32_bf16 v[20:23], a[56:59], v[40:43], v[20:23]\nds_read_b128 v[36:39], v2 offset:14336\nv_mfma_f32_16x16x32_bf16 v[24:27], v[60:63], v[40:43], v[24:27]\ns_waitcnt lgkmcnt(4)\nv_mfma_f32_16x16x32_bf16 v[12:15], a[60:63], v[44:47], v[12:15]\nds_read_b128 v[40:43], v2 offset:14592\n" \
"v_mfma_f32_16x16x32_bf16 v[16:19], v[64:67], v[44:47], v[16:19]\ns_waitcnt lgkmcnt(4)\nv_mfma_f32_16x16x32_bf16 v[20:23], a[60:63], v[48:51], v[20:23]\ns_waitcnt vmcnt(8)\ns_barrier\nds_read_b128 v[44:47], v2 offset:16384\nv_mfma_f32_16x16x32_bf16 v[24:27], v[64:67], v[48:51], v[24:27]\ns_waitcnt lgkmcnt(4)\n" \
"v_mfma_f32_16x16x32_bf16 v[12:15], a[80:83], v[28:31], v[12:15]\nds_read_b128 v[48:51], v2 offset:16640\nv_mfma_f32_16x16x32_bf16 v[16:19], a[96:99], v[28:31], v[16:19]\ns_waitcnt lgkmcnt(4)\nv_mfma_f32_16x16x32_bf16 v[20:23], a[80:83], v[32:35], v[20:23]\nds_read_b128 v[28:31], v2 offset:18432\nv_mfma_f32_16x16x32_bf16 v[24:27], a[96:99], v[32:35], v[24:27]\nds_read_b128 v[60:63], v1 offset:2048\n" \
"ds_read_b128 v[64:67], v1 offset:3072\ns_waitcnt lgkmcnt(6)\nv_mfma_f32_16x16x32_bf16 v[12:15], a[84:87], v[36:39], v[12:15]\nds_read_b128 v[32:35], v2 offset:18688\nv_mfma_f32_16x16x32_bf16 v[16:19], a[100:103], v[36:39], v[16:19]\ns_waitcnt lgkmcnt(6)\nv_mfma_f32_16x16x32_bf16 v[20:23], a[84:87], v[40:43], v[20:23]\nds_read_b128 v[36:39], v2 offset:20480\n" \
"v_mfma_f32_16x16x32_bf16 v[24:27], a[100:103], v[40:43], v[24:27]\ns_waitcnt lgkmcnt(6)\nv_mfma_f32_16x16x32_bf16 v[12:15], a[104:107], v[44:47], v[12:15]\nds_read_b128 v[40:43], v2 offset:20736\nv_mfma_f32_16x16x32_bf16 v[16:19], a[120:123], v[44:47], v[16:19]\ns_waitcnt lgkmcnt(6)\nv_mfma_f32_16x16x32_bf16 v[20:23], a[104:107], v[48:51], v[20:23]\ns_barrier\n" \
"ds_read_b128 v[44:47], v2 offset:22528\nv_mfma_f32_16x16x32_bf16 v[24:27], a[120:123], v[48:51], v[24:27]\ns_add_u32 m0, s57, 0\ns_add_u32 s60, s41, 98304\nbuffer_load_dwordx4 v0, %[rs], s60 offen offset:0 sc1 lds\ns_waitcnt lgkmcnt(6)\nv_mfma_f32_16x16x32_bf16 v[12:15], a[108:111], v[28:31], v[12:15]\nds_read_b128 v[48:51], v2 offset:22784\n" \
"v_mfma_f32_16x16x32_bf16 v[16:19], a[124:127], v[28:31], v[16:19]\ns_waitcnt lgkmcnt(4)\nv_mfma_f32_16x16x32_bf16 v[20:23], a[108:111], v[32:35], v[20:23]\nds_read_b128 v[28:31], v2 offset:24576\nv_mfma_f32_16x16x32_bf16 v[24:27], a[124:127], v[32:35], v[24:27]\nbuffer_load_dwordx4 v0, %[rs], s60 offen offset:1024 sc1 lds\ns_waitcnt lgkmcnt(4)\nv_mfma_f32_16x16x32_bf16 v[12:15], a[136:139], v[36:39], v[12:15]\n" \
"ds_read_b128 v[32:35], v2 offset:24832\nv_mfma_f32_16x16x32_bf16 v[16:19], a[152:155], v[36:39], v[16:19]\ns_waitcnt lgkmcnt(4)\nv_mfma_f32_16x16x32_bf16 v[20:23], a[136:139], v[40:43], v[20:23]\nds_read_b128 v[36:39], v2 offset:26624\nv_mfma_f32_16x16x32_bf16 v[24:27], a[152:155], v[40:43], v[24:27]\nbuffer_load_dwordx4 v0, %[rs], s60 offen offset:2048 sc1 lds\ns_waitcnt lgkmcnt(4)\n" \
"v_mfma_f32_16x16x32_bf16 v[12:15], a[140:143], v[44:47], v[12:15]\nds_read_b128 v[40:43], v2 offset:26880\nv_mfma_f32_16x16x32_bf16 v[16:19], a[156:159], v[44:47], v[16:19]\ns_waitcnt lgkmcnt(4)\nv_mfma_f32_16x16x32_bf16 v[20:23], a[140:143], v[48:51], v[20:23]\nds_read_b128 v[44:47], v2 offset:28672\nv_mfma_f32_16x16x32_bf16 v[24:27], a[156:159], v[48:51], v[24:27]\nbuffer_load_dwordx4 v0, %[rs], s60 offen offset:3072 sc1 lds\n" \
"s_waitcnt lgkmcnt(4)\nv_mfma_f32_16x16x32_bf16 v[12:15], a[160:163], v[28:31], v[12:15]\nds_read_b128 v[48:51], v2 offset:28928\nv_mfma_f32_16x16x32_bf16 v[16:19], v[60:63], v[28:31], v[16:19]\ns_waitcnt lgkmcnt(4)\nv_mfma_f32_16x16x32_bf16 v[20:23], a[160:163], v[32:35], v[20:23]\nds_read_b128 v[28:31], v2 offset:30720\nv_mfma_f32_16x16x32_bf16 v[24:27], v[60:63], v[32:35], v[24:27]\n" \
"s_waitcnt lgkmcnt(4)\nv_mfma_f32_16x16x32_bf16 v[12:15], a[164:167], v[36:39], v[12:15]\nds_read_b128 v[32:35], v2 offset:30976\nv_mfma_f32_16x16x32_bf16 v[16:19], v[64:67], v[36:39], v[16:19]\ns_waitcnt lgkmcnt(4)\nv_mfma_f32_16x16x32_bf16 v[20:23], a[164:167], v[40:43], v[20:23]\ns_waitcnt vmcnt(4)\ns_barrier\n" \
"ds_read_b128 v[36:39], v2 offset:32768\nv_mfma_f32_16x16x32_bf16 v[24:27], v[64:67], v[40:43], v[24:27]\ns_waitcnt lgkmcnt(4)\nv_mfma_f32_16x16x32_bf16 v[12:15], a[184:187], v[44:47], v[12:15]\nds_read_b128 v[40:43], v2 offset:33024\nv_mfma_f32_16x16x32_bf16 v[16:19], a[200:203], v[44:47], v[16:19]\ns_waitcnt lgkmcnt(4)\nv_mfma_f32_16x16x32_bf16 v[20:23], a[184:187], v[48:51], v[20:23]\n" \
"ds_read_b128 v[44:47], v2 offset:34816\nv_mfma_f32_16x16x32_bf16 v[24:27], a[200:203], v[48:51], v[24:27]\nds_read_b128 v[60:63], v1 offset:4096\nds_read_b128 v[64:67], v1 offset:5120\ns_waitcnt lgkmcnt(6)\nv_mfma_f32_16x16x32_bf16 v[12:15], a[188:191], v[28:31], v[12:15]\nds_read_b128 v[48:51], v2 offset:35072\nv_mfma_f32_16x16x32_bf16 v[16:19], a[204:207], v[28:31], v[16:19]\n" \
"s_waitcnt lgkmcnt(6)\nv_mfma_f32_16x16x32_bf16 v[20:23], a[188:191], v[32:35], v[20:23]\nds_read_b128 v[28:31], v2 offset:36864\nv_mfma_f32_16x16x32_bf16 v[24:27], a[204:207], v[32:35], v[24:27]\ns_waitcnt lgkmcnt(6)\nv_mfma_f32_16x16x32_bf16 v[12:15], a[208:211], v[36:39], v[12:15]\nds_read_b128 v[32:35], v2 offset:37120\nv_mfma_f32_16x16x32_bf16 v[16:19], a[224:227], v[36:39], v[16:19]\n" \
"s_waitcnt lgkmcnt(6)\nv_mfma_f32_16x16x32_bf16 v[20:23], a[208:211], v[40:43], v[20:23]\nds_read_b128 v[36:39], v2 offset:38912\nv_mfma_f32_16x16x32_bf16 v[24:27], a[224:227], v[40:43], v[24:27]\ns_waitcnt lgkmcnt(6)\nv_mfma_f32_16x16x32_bf16 v[12:15], a[212:215], v[44:47], v[12:15]\nds_read_b128 v[40:43], v2 offset:39168\nv_mfma_f32_16x16x32_bf16 v[16:19], a[228:231], v[44:47], v[16:19]\n" \
"s_waitcnt lgkmcnt(4)\nv_mfma_f32_16x16x32_bf16 v[20:23], a[212:215], v[48:51], v[20:23]\nds_read_b128 v[44:47], v2 offset:40960\nv_mfma_f32_16x16x32_bf16 v[24:27], a[228:231], v[48:51], v[24:27]\ns_waitcnt lgkmcnt(4)\nv_mfma_f32_16x16x32_bf16 v[12:15], a[240:243], v[28:31], v[12:15]\nds_read_b128 v[48:51], v2 offset:41216\nv_mfma_f32_16x16x32_bf16 v[16:19], v[96:99], v[28:31], v[16:19]\n" \
"s_waitcnt lgkmcnt(4)\nv_mfma_f32_16x16x32_bf16 v[20:23], a[240:243], v[32:35], v[20:23]\nds_read_b128 v[28:31], v2 offset:43008\nv_mfma_f32_16x16x32_bf16 v[24:27], v[96:99], v[32:35], v[24:27]\ns_waitcnt lgkmcnt(4)\nv_mfma_f32_16x16x32_bf16 v[12:15], a[244:247], v[36:39], v[12:15]\nds_read_b128 v[32:35], v2 offset:43264\nv_mfma_f32_16x16x32_bf16 v[16:19], v[100:103], v[36:39], v[16:19]\n" \
"s_waitcnt lgkmcnt(4)\nv_mfma_f32_16x16x32_bf16 v[20:23], a[244:247], v[40:43], v[20:23]\nds_read_b128 v[36:39], v2 offset:45056\nv_mfma_f32_16x16x32_bf16 v[24:27], v[100:103], v[40:43], v[24:27]\ns_waitcnt lgkmcnt(4)\nv_mfma_f32_16x16x32_bf16 v[12:15], v[104:107], v[44:47], v[12:15]\nds_read_b128 v[40:43], v2 offset:45312\nv_mfma_f32_16x16x32_bf16 v[16:19], v[60:63], v[44:47], v[16:19]\n" \
"s_waitcnt lgkmcnt(4)\nv_mfma_f32_16x16x32_bf16 v[20:23], v[104:107], v[48:51], v[20:23]\nds_read_b128 v[44:47], v2 offset:47104\nv_mfma_f32_16x16x32_bf16 v[24:27], v[60:63], v[48:51], v[24:27]\ns_waitcnt lgkmcnt(4)\nv_mfma_f32_16x16x32_bf16 v[12:15], v[108:111], v[28:31], v[12:15]\nds_read_b128 v[48:51], v2 offset:47360\nv_mfma_f32_16x16x32_bf16 v[16:19], v[64:67], v[28:31], v[16:19]\n" \
"s_waitcnt lgkmcnt(4)\nv_mfma_f32_16x16x32_bf16 v[20:23], v[108:111], v[32:35], v[20:23]\ns_waitcnt vmcnt(0)\ns_barrier\nds_read_b128 v[28:31], v2 offset:0\nv_mfma_f32_16x16x32_bf16 v[24:27], v[64:67], v[32:35], v[24:27]\ns_waitcnt lgkmcnt(4)\nv_mfma_f32_16x16x32_bf16 v[12:15], v[128:131], v[36:39], v[12:15]\n" \
"ds_read_b128 v[32:35], v2 offset:256\nv_mfma_f32_16x16x32_bf16 v[16:19], v[144:147], v[36:39], v[16:19]\ns_waitcnt lgkmcnt(4)\nv_mfma_f32_16x16x32_bf16 v[20:23], v[128:131], v[40:43], v[20:23]\nds_read_b128 v[36:39], v2 offset:2048\nv_mfma_f32_16x16x32_bf16 v[24:27], v[144:147], v[40:43], v[24:27]\nds_read_b128 v[60:63], v1 offset:6144\nds_read_b128 v[64:67], v1 offset:7168\n" \
"s_waitcnt lgkmcnt(6)\nv_mfma_f32_16x16x32_bf16 v[12:15], v[132:135], v[44:47], v[12:15]\nds_read_b128 v[40:43], v2 offset:2304\nv_mfma_f32_16x16x32_bf16 v[16:19], v[148:151], v[44:47], v[16:19]\nbuffer_load_dwordx4 v[80:83], v3, %[rs], s42 offen offset:0 sc1\ns_waitcnt lgkmcnt(6)\nv_mfma_f32_16x16x32_bf16 v[20:23], v[132:135], v[48:51], v[20:23]\nds_read_b128 v[44:47], v2 offset:4096\n" \
"v_mfma_f32_16x16x32_bf16 v[24:27], v[148:151], v[48:51], v[24:27]\ns_waitcnt lgkmcnt(6)\nv_mfma_f32_16x16x32_bf16 v[12:15], v[152:155], v[28:31], v[12:15]\nds_read_b128 v[48:51], v2 offset:4352\nv_mfma_f32_16x16x32_bf16 v[16:19], v[168:171], v[28:31], v[16:19]\nbuffer_load_dwordx4 v[84:87], v3, %[rs], s42 offen offset:1024 sc1\ns_waitcnt lgkmcnt(6)\nv_mfma_f32_16x16x32_bf16 v[20:23], v[152:155], v[32:35], v[20:23]\n" \
"ds_read_b128 v[28:31], v2 offset:6144\nv_mfma_f32_16x16x32_bf16 v[24:27], v[168:171], v[32:35], v[24:27]\ns_waitcnt lgkmcnt(6)\nv_mfma_f32_16x16x32_bf16 v[12:15], v[156:159], v[36:39], v[12:15]\nds_read_b128 v[32:35], v2 offset:6400\nv_mfma_f32_16x16x32_bf16 v[16:19], v[172:175], v[36:39], v[16:19]\nbuffer_load_dwordx4 v[88:91], v3, %[rs], s42 offen offset:2048 sc1\ns_waitcnt lgkmcnt(4)\n" \
"v_mfma_f32_16x16x32_bf16 v[20:23], v[156:159], v[40:43], v[20:23]\nds_read_b128 v[36:39], v2 offset:8192\nv_mfma_f32_16x16x32_bf16 v[24:27], v[172:175], v[40:43], v[24:27]\ns_waitcnt lgkmcnt(4)\nv_mfma_f32_16x16x32_bf16 v[12:15], v[184:187], v[44:47], v[12:15]\nds_read_b128 v[40:43], v2 offset:8448\nv_mfma_f32_16x16x32_bf16 v[16:19], v[200:203], v[44:47], v[16:19]\nbuffer_load_dwordx4 v[92:95], v3, %[rs], s42 offen offset:3072 sc1\n" \
"s_waitcnt lgkmcnt(4)\nv_mfma_f32_16x16x32_bf16 v[20:23], v[184:187], v[48:51], v[20:23]\nds_read_b128 v[44:47], v2 offset:10240\nv_mfma_f32_16x16x32_bf16 v[24:27], v[200:203], v[48:51], v[24:27]\ns_waitcnt lgkmcnt(4)\nv_mfma_f32_16x16x32_bf16 v[12:15], v[188:191], v[28:31], v[12:15]\nds_read_b128 v[48:51], v2 offset:10496\nv_mfma_f32_16x16x32_bf16 v[16:19], v[204:207], v[28:31], v[16:19]\n" \
"s_waitcnt lgkmcnt(4)\nv_mfma_f32_16x16x32_bf16 v[20:23], v[188:191], v[32:35], v[20:23]\nds_read_b128 v[28:31], v2 offset:12288\nv_mfma_f32_16x16x32_bf16 v[24:27], v[204:207], v[32:35], v[24:27]\ns_waitcnt lgkmcnt(4)\nv_mfma_f32_16x16x32_bf16 v[12:15], v[208:211], v[36:39], v[12:15]\nds_read_b128 v[32:35], v2 offset:14336\nv_mfma_f32_16x16x32_bf16 v[16:19], v[60:63], v[36:39], v[16:19]\n" \
"s_waitcnt lgkmcnt(4)\nv_mfma_f32_16x16x32_bf16 v[20:23], v[208:211], v[40:43], v[20:23]\nds_read_b128 v[36:39], v2 offset:12544\nv_mfma_f32_16x16x32_bf16 v[24:27], v[60:63], v[40:43], v[24:27]\ns_waitcnt lgkmcnt(4)\nv_mfma_f32_16x16x32_bf16 v[12:15], v[212:215], v[44:47], v[12:15]\nds_read_b128 v[40:43], v2 offset:14592\nv_mfma_f32_16x16x32_bf16 v[16:19], v[64:67], v[44:47], v[16:19]\n" \
"s_waitcnt lgkmcnt(4)\nv_mfma_f32_16x16x32_bf16 v[20:23], v[212:215], v[48:51], v[20:23]\nv_mfma_f32_16x16x32_bf16 v[24:27], v[64:67], v[48:51], v[24:27]\ns_waitcnt lgkmcnt(3)\nv_mfma_f32_16x16x32_bf16 v[12:15], v[232:235], v[28:31], v[12:15]\nv_mov_b64 v[56:57], 0\nv_mfma_f32_16x16x32_bf16 v[16:19], v[248:251], v[28:31], v[16:19]\nv_mov_b64 v[58:59], 0\n" \
"s_waitcnt lgkmcnt(2)\nv_mfma_f32_16x16x32_bf16 v[12:15], v[236:239], v[32:35], v[12:15]\nv_mov_b64 v[76:77], 0\nv_mfma_f32_16x16x32_bf16 v[16:19], v[252:255], v[32:35], v[16:19]\nv_mov_b64 v[78:79], 0\ns_waitcnt lgkmcnt(1)\nv_mfma_f32_16x16x32_bf16 v[20:23], v[232:235], v[36:39], v[20:23]\nv_mfma_f32_16x16x32_bf16 v[24:27], v[248:251], v[36:39], v[24:27]\n" \
"s_waitcnt lgkmcnt(0)\nv_mfma_f32_16x16x32_bf16 v[20:23], v[236:239], v[40:43], v[20:23]\nv_cvt_pk_bf16_f32 v68, v12, v16\nv_cvt_pk_bf16_f32 v69, v13, v17\nv_mfma_f32_16x16x32_bf16 v[24:27], v[252:255], v[40:43], v[24:27]\nv_cvt_pk_bf16_f32 v70, v14, v18\nv_cvt_pk_bf16_f32 v71, v15, v19\nds_write_b128 v11, v[68:71]\n" \
"s_nop 4\nv_cvt_pk_bf16_f32 v72, v20, v24\nv_cvt_pk_bf16_f32 v73, v21, v25\nv_cvt_pk_bf16_f32 v74, v22, v26\nv_cvt_pk_bf16_f32 v75, v23, v27\nds_write_b128 v11, v[72:75] offset:1024\ns_cmp_lt_i32 s40, 0\ns_cbranch_scc1 .Lvu_first%=\n" \
".Lvu_crit%=:\ns_cmp_eq_u32 %[w], 1\ns_cbranch_scc1 .Lvu_c1%=\ns_cmp_eq_u32 %[w], 2\ns_cbranch_scc1 .Lvu_c2%=\ns_cmp_eq_u32 %[w], 3\ns_cbranch_scc1 .Lvu_c3%=\n.Lvu_c0%=:\n" \
"ds_read_b128 v[60:63], v1 offset:0\nds_read_b128 v[64:67], v1 offset:1024\nds_read_b128 v[68:71], v1 offset:2048\nds_read_b128 v[72:75], v1 offset:3072\n.Lvu_poll0%=:\ns_waitcnt vmcnt(0)\nv_smfmac_f32_16x16x64_bf16 v[56:59], v[80:83], a[0:7], v4\nds_write_b128 v0, v[80:83] offset:16384\n" \
"v_max3_u32 v16, v80, v82, v84\nv_smfmac_f32_16x16x64_bf16 v[76:79], v[80:83], a[16:23], v4\nds_write_b128 v0, v[84:87] offset:17408\nv_max3_u32 v16, v16, v86, v88\nv_smfmac_f32_16x16x64_bf16 v[56:59], v[84:87], a[32:39], v4\nds_write_b128 v0, v[88:91] offset:18432\nv_max3_u32 v16, v16, v90, v92\nv_smfmac_f32_16x16x64_bf16 v[76:79], v[84:87], a[48:55], v4\n" \
"ds_write_b128 v0, v[92:95] offset:19456\nv_max_u32 v16, v16, v94\nv_smfmac_f32_16x16x64_bf16 v[56:59], v[88:91], a[56:63], v4\nv_cmp_eq_u32 vcc, -1, v16\ns_waitcnt lgkmcnt(6)\nv_smfmac_f32_16x16x64_bf16 v[76:79], v[88:91], v[60:67], v4\ns_cbranch_vccnz .Lvu_re0%=\ns_waitcnt lgkmcnt(0)\n" \
"s_barrier\nds_read_b128 v[12:15], v0 offset:20480\nds_read_b128 v[16:19], v0 offset:21504\nv_smfmac_f32_16x16x64_bf16 v[56:59], v[92:95], a[80:87], v4\nds_read_b128 v[20:23], v0 offset:22528\nds_read_b128 v[24:27], v0 offset:23552\nv_mov_b64 v[84:85], 0\nv_smfmac_f32_16x16x64_bf16 v[76:79], v[92:95], a[96:103], v4\n" \
"ds_read_b128 v[28:31], v0 offset:24576\nds_read_b128 v[32:35], v0 offset:25600\nv_mov_b64 v[86:87], 0\ns_waitcnt lgkmcnt(5)\nv_smfmac_f32_16x16x64_bf16 v[56:59], v[12:15], a[104:111], v4\nds_read_b128 v[36:39], v0 offset:26624\nds_read_b128 v[40:43], v0 offset:27648\nv_mov_b64 v[88:89], 0\n" \
"v_smfmac_f32_16x16x64_bf16 v[76:79], v[12:15], a[120:127], v4\nds_read_b128 v[44:47], v0 offset:28672\nds_read_b128 v[48:51], v0 offset:29696\nv_mov_b64 v[90:91], 0\ns_waitcnt lgkmcnt(8)\nv_smfmac_f32_16x16x64_bf16 v[56:59], v[16:19], a[136:143], v4\nds_read_b128 v[52:55], v0 offset:30720\nds_read_b128 v[80:83], v0 offset:31744\n" \
"v_mov_b64 v[12:13], 0\nv_smfmac_f32_16x16x64_bf16 v[76:79], v[16:19], a[152:159], v4\nds_read_b128 v[60:63], v1 offset:4096\nds_read_b128 v[64:67], v1 offset:5120\nv_mov_b64 v[14:15], 0\ns_waitcnt lgkmcnt(11)\nv_smfmac_f32_16x16x64_bf16 v[56:59], v[20:23], a[160:167], v4\nv_mov_b64 v[16:17], 0\n" \
"v_smfmac_f32_16x16x64_bf16 v[76:79], v[20:23], v[68:75], v4\nv_mov_b64 v[18:19], 0\ns_waitcnt lgkmcnt(10)\nv_smfmac_f32_16x16x64_bf16 v[56:59], v[24:27], a[184:191], v4\nds_read_b128 v[68:71], v1 offset:6144\nds_read_b128 v[72:75], v1 offset:7168\nv_mov_b64 v[20:21], 0\nv_smfmac_f32_16x16x64_bf16 v[76:79], v[24:27], a[200:207], v4\n" \
"v_mov_b64 v[22:23], 0\ns_waitcnt lgkmcnt(11)\nv_smfmac_f32_16x16x64_bf16 v[56:59], v[28:31], a[208:215], v4\nv_mov_b64 v[24:25], 0\nv_smfmac_f32_16x16x64_bf16 v[76:79], v[28:31], a[224:231], v4\nv_mov_b64 v[26:27], 0\ns_waitcnt lgkmcnt(10)\nv_smfmac_f32_16x16x64_bf16 v[56:59], v[32:35], a[240:247], v4\n" \
"v_mov_b64 v[28:29], 0\nv_smfmac_f32_16x16x64_bf16 v[76:79], v[32:35], v[96:103], v4\nv_mov_b64 v[30:31], 0\ns_waitcnt lgkmcnt(9)\nv_smfmac_f32_16x16x64_bf16 v[56:59], v[36:39], v[104:111], v4\nv_mov_b64 v[32:33], 0\ns_waitcnt lgkmcnt(2)\nv_smfmac_f32_16x16x64_bf16 v[76:79], v[36:39], v[60:67], v4\n" \
"v_mov_b64 v[34:35], 0\nv_smfmac_f32_16x16x64_bf16 v[56:59], v[40:43], v[128:135], v4\nv_mov_b64 v[36:37], 0\nv_smfmac_f32_16x16x64_bf16 v[76:79], v[40:43], v[144:151], v4\nds_read_b128 v[60:63], v1 offset:8192\nds_read_b128 v[64:67], v1 offset:9216\nv_mov_b64 v[38:39], 0\nv_smfmac_f32_16x16x64_bf16 v[56:59], v[44:47], v[152:159], v4\n" \
"v_mov_b64 v[40:41], 0\nv_smfmac_f32_16x16x64_bf16 v[76:79], v[44:47], v[168:175], v4\nv_mov_b64 v[42:43], 0\nv_smfmac_f32_16x16x64_bf16 v[56:59], v[48:51], v[184:191], v4\nv_mov_b64 v[44:45], 0\nv_smfmac_f32_16x16x64_bf16 v[76:79], v[48:51], v[200:207], v4\nv_mov_b64 v[46:47], 0\nv_smfmac_f32_16x16x64_bf16 v[56:59], v[52:55], v[208:215], v4\n" \
"v_mov_b64 v[48:49], 0\ns_waitcnt lgkmcnt(2)\nv_smfmac_f32_16x16x64_bf16 v[76:79], v[52:55], v[68:75], v4\nv_mov_b64 v[50:51], 0\nv_smfmac_f32_16x16x64_bf16 v[56:59], v[80:83], v[232:239], v4\nv_mov_b64 v[52:53], 0\nv_smfmac_f32_16x16x64_bf16 v[76:79], v[80:83], v[248:255], v4\nds_read_b128 v[68:71], v1 offset:10240\n" \
"ds_read_b128 v[72:75], v1 offset:11264\nv_mov_b64 v[54:55], 0\ns_branch .Lvu_u%=\n.Lvu_re0%=:\nv_max_u32 v16, v80, v82\nv_cmp_eq_u32 s[46:47], -1, v16\nv_max_u32 v16, v84, v86\nv_cmp_eq_u32 s[48:49], -1, v16\n" \
"v_max_u32 v16, v88, v90\nv_cmp_eq_u32 s[50:51], -1, v16\nv_max_u32 v16, v92, v94\nv_cmp_eq_u32 s[52:53], -1, v16\ns_mov_b64 exec, s[46:47]\nbuffer_load_dwordx4 v[80:83], v3, %[rs], s42 offen offset:0 sc1\ns_mov_b64 exec, s[48:49]\nbuffer_load_dwordx4 v[84:87], v3, %[rs], s42 offen offset:1024 sc1\n" \
"s_mov_b64 exec, s[50:51]\nbuffer_load_dwordx4 v[88:91], v3, %[rs], s42 offen offset:2048 sc1\ns_mov_b64 exec, s[52:53]\nbuffer_load_dwordx4 v[92:95], v3, %[rs], s42 offen offset:3072 sc1\ns_mov_b64 exec, -1\nv_mov_b64 v[56:57], 0\nv_mov_b64 v[58:59], 0\nv_mov_b64 v[76:77], 0\n" \
"v_mov_b64 v[78:79], 0\ns_add_u32 s43, s43, 1\ns_cmp_lt_u32 s43, 262144\ns_cbranch_scc1 .Lvu_poll0%=\ns_branch .Lvu_abort%=\n.Lvu_c1%=:\nds_read_b128 v[60:63], v1 offset:2048\nds_read_b128 v[64:67], v1 offset:3072\n" \
"ds_read_b128 v[68:71], v1 offset:4096\nds_read_b128 v[72:75], v1 offset:5120\n.Lvu_poll1%=:\ns_waitcnt vmcnt(0)\nv_smfmac_f32_16x16x64_bf16 v[56:59], v[80:83], a[104:111], v4\nds_write_b128 v0, v[80:83] offset:20480\nv_max3_u32 v16, v80, v82, v84\nv_smfmac_f32_16x16x64_bf16 v[76:79], v[80:83], a[120:127], v4\n" \
"ds_write_b128 v0, v[84:87] offset:21504\nv_max3_u32 v16, v16, v86, v88\nv_smfmac_f32_16x16x64_bf16 v[56:59], v[84:87], a[136:143], v4\nds_write_b128 v0, v[88:91] offset:22528\nv_max3_u32 v16, v16, v90, v92\nv_smfmac_f32_16x16x64_bf16 v[76:79], v[84:87], a[152:159], v4\nds_write_b128 v0, v[92:95] offset:23552\nv_max_u32 v16, v16, v94\n" \
"v_smfmac_f32_16x16x64_bf16 v[56:59], v[88:91], a[160:167], v4\nv_cmp_eq_u32 vcc, -1, v16\ns_waitcnt lgkmcnt(6)\nv_smfmac_f32_16x16x64_bf16 v[76:79], v[88:91], v[60:67], v4\ns_cbranch_vccnz .Lvu_re1%=\ns_waitcnt lgkmcnt(0)\ns_barrier\nds_read_b128 v[12:15], v0 offset:24576\n" \
"ds_read_b128 v[16:19], v0 offset:25600\nv_smfmac_f32_16x16x64_bf16 v[56:59], v[92:95], a[184:191], v4\nds_read_b128 v[20:23], v0 offset:26624\nds_read_b128 v[24:27], v0 offset:27648\nv_mov_b64 v[84:85], 0\nv_smfmac_f32_16x16x64_bf16 v[76:79], v[92:95], a[200:207], v4\nds_read_b128 v[28:31], v0 offset:28672\nds_read_b128 v[32:35], v0 offset:29696\n" \
"v_mov_b64 v[86:87], 0\ns_waitcnt lgkmcnt(5)\nv_smfmac_f32_16x16x64_bf16 v[56:59], v[12:15], a[208:215], v4\nds_read_b128 v[36:39], v0 offset:30720\nds_read_b128 v[40:43], v0 offset:31744\nv_mov_b64 v[88:89], 0\nv_smfmac_f32_16x16x64_bf16 v[76:79], v[12:15], a[224:231], v4\nds_read_b128 v[44:47], v0 offset:16384\n" \
"ds_read_b128 v[48:51], v0 offset:17408\nv_mov_b64 v[90:91], 0\ns_waitcnt lgkmcnt(8)\nv_smfmac_f32_16x16x64_bf16 v[56:59], v[16:19], a[240:247], v4\nds_read_b128 v[52:55], v0 offset:18432\nds_read_b128 v[80:83], v0 offset:19456\nv_mov_b64 v[12:13], 0\nv_smfmac_f32_16x16x64_bf16 v[76:79], v[16:19], v[96:103], v4\n" \
"ds_read_b128 v[60:63], v1 offset:6144\nds_read_b128 v[64:67], v1 offset:7168\nv_mov_b64 v[14:15], 0\ns_waitcnt lgkmcnt(11)\nv_smfmac_f32_16x16x64_bf16 v[56:59], v[20:23], v[104:111], v4\nv_mov_b64 v[16:17], 0\nv_smfmac_f32_16x16x64_bf16 v[76:79], v[20:23], v[68:75], v4\nv_mov_b64 v[18:19], 0\n" \
"s_waitcnt lgkmcnt(10)\nv_smfmac_f32_16x16x64_bf16 v[56:59], v[24:27], v[128:135], v4\nds_read_b128 v[68:71], v1 offset:0\nds_read_b128 v[72:75], v1 offset:1024\nv_mov_b64 v[20:21], 0\nv_smfmac_f32_16x16x64_bf16 v[76:79], v[24:27], v[144:151], v4\nv_mov_b64 v[22:23], 0\ns_waitcnt lgkmcnt(11)\n" \
"v_smfmac_f32_16x16x64_bf16 v[56:59], v[28:31], v[152:159], v4\nv_mov_b64 v[24:25], 0\nv_smfmac_f32_16x16x64_bf16 v[76:79], v[28:31], v[168:175], v4\nv_mov_b64 v[26:27], 0\ns_waitcnt lgkmcnt(10)\nv_smfmac_f32_16x16x64_bf16 v[56:59], v[32:35], v[184:191], v4\nv_mov_b64 v[28:29], 0\nv_smfmac_f32_16x16x64_bf16 v[76:79], v[32:35], v[200:207], v4\n" \
"v_mov_b64 v[30:31], 0\ns_waitcnt lgkmcnt(9)\nv_smfmac_f32_16x16x64_bf16 v[56:59], v[36:39], v[208:215], v4\nv_mov_b64 v[32:33], 0\ns_waitcnt lgkmcnt(2)\nv_smfmac_f32_16x16x64_bf16 v[76:79], v[36:39], v[60:67], v4\nv_mov_b64 v[34:35], 0\nv_smfmac_f32_16x16x64_bf16 v[56:59], v[40:43], v[232:239], v4\n" \
"v_mov_b64 v[36:37], 0\nv_smfmac_f32_16x16x64_bf16 v[76:79], v[40:43], v[248:255], v4\nds_read_b128 v[60:63], v1 offset:8192\nds_read_b128 v[64:67], v1 offset:9216\nv_mov_b64 v[38:39], 0\nv_smfmac_f32_16x16x64_bf16 v[56:59], v[44:47], a[0:7], v4\nv_mov_b64 v[40:41], 0\nv_smfmac_f32_16x16x64_bf16 v[76:79], v[44:47], a[16:23], v4\n" \
"v_mov_b64 v[42:43], 0\nv_smfmac_f32_16x16x64_bf16 v[56:59], v[48:51], a[32:39], v4\nv_mov_b64 v[44:45], 0\nv_smfmac_f32_16x16x64_bf16 v[76:79], v[48:51], a[48:55], v4\nv_mov_b64 v[46:47], 0\nv_smfmac_f32_16x16x64_bf16 v[56:59], v[52:55], a[56:63], v4\nv_mov_b64 v[48:49], 0\ns_waitcnt lgkmcnt(2)\n" \
"v_smfmac_f32_16x16x64_bf16 v[76:79], v[52:55], v[68:75], v4\nv_mov_b64 v[50:51], 0\nv_smfmac_f32_16x16x64_bf16 v[56:59], v[80:83], a[80:87], v4\nv_mov_b64 v[52:53], 0\nv_smfmac_f32_16x16x64_bf16 v[76:79], v[80:83], a[96:103], v4\nds_read_b128 v[68:71], v1 offset:10240\nds_read_b128 v[72:75], v1 offset:11264\nv_mov_b64 v[54:55], 0\n" \
"s_branch .Lvu_u%=\n.Lvu_re1%=:\nv_max_u32 v16, v80, v82\nv_cmp_eq_u32 s[46:47], -1, v16\nv_max_u32 v16, v84, v86\nv_cmp_eq_u32 s[48:49], -1, v16\nv_max_u32 v16, v88, v90\nv_cmp_eq_u32 s[50:51], -1, v16\n" \
"v_max_u32 v16, v92, v94\nv_cmp_eq_u32 s[52:53], -1, v16\ns_mov_b64 exec, s[46:47]\nbuffer_load_dwordx4 v[80:83], v3, %[rs], s42 offen offset:0 sc1\ns_mov_b64 exec, s[48:49]\nbuffer_load_dwordx4 v[84:87], v3, %[rs], s42 offen offset:1024 sc1\ns_mov_b64 exec, s[50:51]\nbuffer_load_dwordx4 v[88:91], v3, %[rs], s42 offen offset:2048 sc1\n" \
"s_mov_b64 exec, s[52:53]\nbuffer_load_dwordx4 v[92:95], v3, %[rs], s42 offen offset:3072 sc1\ns_mov_b64 exec, -1\nv_mov_b64 v[56:57], 0\nv_mov_b64 v[58:59], 0\nv_mov_b64 v[76:77], 0\nv_mov_b64 v[78:79], 0\ns_add_u32 s43, s43, 1\n" \
"s_cmp_lt_u32 s43, 262144\ns_cbranch_scc1 .Lvu_poll1%=\ns_branch .Lvu_abort%=\n.Lvu_c2%=:\nds_read_b128 v[60:63], v1 offset:4096\nds_read_b128 v[64:67], v1 offset:5120\nds_read_b128 v[68:71], v1 offset:6144\nds_read_b128 v[72:75], v1 offset:7168\n" \
".Lvu_poll2%=:\ns_waitcnt vmcnt(0)\nv_smfmac_f32_16x16x64_bf16 v[56:59], v[80:83], a[208:215], v4\nds_write_b128 v0, v[80:83] offset:24576\nv_max3_u32 v16, v80, v82, v84\nv_smfmac_f32_16x16x64_bf16 v[76:79], v[80:83], a[224:231], v4\nds_write_b128 v0, v[84:87] offset:25600\nv_max3_u32 v16, v16, v86, v88\n" \
"v_smfmac_f32_16x16x64_bf16 v[56:59], v[84:87], a[240:247], v4\nds_write_b128 v0, v[88:91] offset:26624\nv_max3_u32 v16, v16, v90, v92\nv_smfmac_f32_16x16x64_bf16 v[76:79], v[84:87], v[96:103], v4\nds_write_b128 v0, v[92:95] offset:27648\nv_max_u32 v16, v16, v94\nv_smfmac_f32_16x16x64_bf16 v[56:59], v[88:91], v[104:111], v4\nv_cmp_eq_u32 vcc, -1, v16\n" \
"s_waitcnt lgkmcnt(6)\nv_smfmac_f32_16x16x64_bf16 v[76:79], v[88:91], v[60:67], v4\ns_cbranch_vccnz .Lvu_re2%=\ns_waitcnt lgkmcnt(0)\ns_barrier\nds_read_b128 v[12:15], v0 offset:28672\nds_read_b128 v[16:19], v0 offset:29696\nv_smfmac_f32_16x16x64_bf16 v[56:59], v[92:95], v[128:135], v4\n" \
"ds_read_b128 v[20:23], v0 offset:30720\nds_read_b128 v[24:27], v0 offset:31744\nv_mov_b64 v[84:85], 0\nv_smfmac_f32_16x16x64_bf16 v[76:79], v[92:95], v[144:151], v4\nds_read_b128 v[28:31], v0 offset:16384\nds_read_b128 v[32:35], v0 offset:17408\nv_mov_b64 v[86:87], 0\ns_waitcnt lgkmcnt(5)\n" \
"v_smfmac_f32_16x16x64_bf16 v[56:59], v[12:15], v[152:159], v4\nds_read_b128 v[36:39], v0 offset:18432\nds_read_b128 v[40:43], v0 offset:19456\nv_mov_b64 v[88:89], 0\nv_smfmac_f32_16x16x64_bf16 v[76:79], v[12:15], v[168:175], v4\nds_read_b128 v[44:47], v0 offset:20480\nds_read_b128 v[48:51], v0 offset:21504\nv_mov_b64 v[90:91], 0\n" \
"s_waitcnt lgkmcnt(8)\nv_smfmac_f32_16x16x64_bf16 v[56:59], v[16:19], v[184:191], v4\nds_read_b128 v[52:55], v0 offset:22528\nds_read_b128 v[80:83], v0 offset:23552\nv_mov_b64 v[12:13], 0\nv_smfmac_f32_16x16x64_bf16 v[76:79], v[16:19], v[200:207], v4\nds_read_b128 v[60:63], v1 offset:0\nds_read_b128 v[64:67], v1 offset:1024\n" \
"v_mov_b64 v[14:15], 0\ns_waitcnt lgkmcnt(11)\nv_smfmac_f32_16x16x64_bf16 v[56:59], v[20:23], v[208:215], v4\nv_mov_b64 v[16:17], 0\nv_smfmac_f32_16x16x64_bf16 v[76:79], v[20:23], v[68:75], v4\nv_mov_b64 v[18:19], 0\ns_waitcnt lgkmcnt(10)\nv_smfmac_f32_16x16x64_bf16 v[56:59], v[24:27], v[232:239], v4\n" \
"ds_read_b128 v[68:71], v1 offset:2048\nds_read_b128 v[72:75], v1 offset:3072\nv_mov_b64 v[20:21], 0\nv_smfmac_f32_16x16x64_bf16 v[76:79], v[24:27], v[248:255], v4\nv_mov_b64 v[22:23], 0\ns_waitcnt lgkmcnt(11)\nv_smfmac_f32_16x16x64_bf16 v[56:59], v[28:31], a[0:7], v4\nv_mov_b64 v[24:25], 0\n" \
"v_smfmac_f32_16x16x64_bf16 v[76:79], v[28:31], a[16:23], v4\nv_mov_b64 v[26:27], 0\ns_waitcnt lgkmcnt(10)\nv_smfmac_f32_16x16x64_bf16 v[56:59], v[32:35], a[32:39], v4\nv_mov_b64 v[28:29], 0\nv_smfmac_f32_16x16x64_bf16 v[76:79], v[32:35], a[48:55], v4\nv_mov_b64 v[30:31], 0\ns_waitcnt lgkmcnt(9)\n" \
"v_smfmac_f32_16x16x64_bf16 v[56:59], v[36:39], a[56:63], v4\nv_mov_b64 v[32:33], 0\ns_waitcnt lgkmcnt(2)\nv_smfmac_f32_16x16x64_bf16 v[76:79], v[36:39], v[60:67], v4\nv_mov_b64 v[34:35], 0\nv_smfmac_f32_16x16x64_bf16 v[56:59], v[40:43], a[80:87], v4\nv_mov_b64 v[36:37], 0\nv_smfmac_f32_16x16x64_bf16 v[76:79], v[40:43], a[96:103], v4\n" \
"ds_read_b128 v[60:63], v1 offset:8192\nds_read_b128 v[64:67], v1 offset:9216\nv_mov_b64 v[38:39], 0\nv_smfmac_f32_16x16x64_bf16 v[56:59], v[44:47], a[104:111], v4\nv_mov_b64 v[40:41], 0\nv_smfmac_f32_16x16x64_bf16 v[76:79], v[44:47], a[120:127], v4\nv_mov_b64 v[42:43], 0\nv_smfmac_f32_16x16x64_bf16 v[56:59], v[48:51], a[136:143], v4\n" \
"v_mov_b64 v[44:45], 0\nv_smfmac_f32_16x16x64_bf16 v[76:79], v[48:51], a[152:159], v4\nv_mov_b64 v[46:47], 0\nv_smfmac_f32_16x16x64_bf16 v[56:59], v[52:55], a[160:167], v4\nv_mov_b64 v[48:49], 0\ns_waitcnt lgkmcnt(2)\nv_smfmac_f32_16x16x64_bf16 v[76:79], v[52:55], v[68:75], v4\nv_mov_b64 v[50:51], 0\n" \
"v_smfmac_f32_16x16x64_bf16 v[56:59], v[80:83], a[184:191], v4\nv_mov_b64 v[52:53], 0\nv_smfmac_f32_16x16x64_bf16 v[76:79], v[80:83], a[200:207], v4\nds_read_b128 v[68:71], v1 offset:10240\nds_read_b128 v[72:75], v1 offset:11264\nv_mov_b64 v[54:55], 0\ns_branch .Lvu_u%=\n.Lvu_re2%=:\n" \
"v_max_u32 v16, v80, v82\nv_cmp_eq_u32 s[46:47], -1, v16\nv_max_u32 v16, v84, v86\nv_cmp_eq_u32 s[48:49], -1, v16\nv_max_u32 v16, v88, v90\nv_cmp_eq_u32 s[50:51], -1, v16\nv_max_u32 v16, v92, v94\nv_cmp_eq_u32 s[52:53], -1, v16\n" \
"s_mov_b64 exec, s[46:47]\nbuffer_load_dwordx4 v[80:83], v3, %[rs], s42 offen offset:0 sc1\ns_mov_b64 exec, s[48:49]\nbuffer_load_dwordx4 v[84:87], v3, %[rs], s42 offen offset:1024 sc1\ns_mov_b64 exec, s[50:51]\nbuffer_load_dwordx4 v[88:91], v3, %[rs], s42 offen offset:2048 sc1\ns_mov_b64 exec, s[52:53]\nbuffer_load_dwordx4 v[92:95], v3, %[rs], s42 offen offset:3072 sc1\n" \
"s_mov_b64 exec, -1\nv_mov_b64 v[56:57], 0\nv_mov_b64 v[58:59], 0\nv_mov_b64 v[76:77], 0\nv_mov_b64 v[78:79], 0\ns_add_u32 s43, s43, 1\ns_cmp_lt_u32 s43, 262144\ns_cbranch_scc1 .Lvu_poll2%=\n" \
"s_branch .Lvu_abort%=\n.Lvu_c3%=:\nds_read_b128 v[60:63], v1 offset:6144\nds_read_b128 v[64:67], v1 offset:7168\nds_read_b128 v[68:71], v1 offset:0\nds_read_b128 v[72:75], v1 offset:1024\n.Lvu_poll3%=:\ns_waitcnt vmcnt(0)\n" \
"v_smfmac_f32_16x16x64_bf16 v[56:59], v[80:83], v[152:159], v4\nds_write_b128 v0, v[80:83] offset:28672\nv_max3_u32 v16, v80, v82, v84\nv_smfmac_f32_16x16x64_bf16 v[76:79], v[80:83], v[168:175], v4\nds_write_b128 v0, v[84:87] offset:29696\nv_max3_u32 v16, v16, v86, v88\nv_smfmac_f32_16x16x64_bf16 v[56:59], v[84:87], v[184:191], v4\nds_write_b128 v0, v[88:91] offset:30720\n" \
"v_max3_u32 v16, v16, v90, v92\nv_smfmac_f32_16x16x64_bf16 v[76:79], v[84:87], v[200:207], v4\nds_write_b128 v0, v[92:95] offset:31744\nv_max_u32 v16, v16, v94\nv_smfmac_f32_16x16x64_bf16 v[56:59], v[88:91], v[208:215], v4\nv_cmp_eq_u32 vcc, -1, v16\ns_waitcnt lgkmcnt(6)\nv_smfmac_f32_16x16x64_bf16 v[76:79], v[88:91], v[60:67], v4\n" \
"s_cbranch_vccnz .Lvu_re3%=\ns_waitcnt lgkmcnt(0)\ns_barrier\nds_read_b128 v[12:15], v0 offset:16384\nds_read_b128 v[16:19], v0 offset:17408\nv_smfmac_f32_16x16x64_bf16 v[56:59], v[92:95], v[232:239], v4\nds_read_b128 v[20:23], v0 offset:18432\nds_read_b128 v[24:27], v0 offset:19456\n" \
"v_mov_b64 v[84:85], 0\nv_smfmac_f32_16x16x64_bf16 v[76:79], v[92:95], v[248:255], v4\nds_read_b128 v[28:31], v0 offset:20480\nds_read_b128 v[32:35], v0 offset:21504\nv_mov_b64 v[86:87], 0\ns_waitcnt lgkmcnt(5)\nv_smfmac_f32_16x16x64_bf16 v[56:59], v[12:15], a[0:7], v4\nds_read_b128 v[36:39], v0 offset:22528\n" \
"ds_read_b128 v[40:43], v0 offset:23552\nv_mov_b64 v[88:89], 0\nv_smfmac_f32_16x16x64_bf16 v[76:79], v[12:15], a[16:23], v4\nds_read_b128 v[44:47], v0 offset:24576\nds_read_b128 v[48:51], v0 offset:25600\nv_mov_b64 v[90:91], 0\ns_waitcnt lgkmcnt(8)\nv_smfmac_f32_16x16x64_bf16 v[56:59], v[16:19], a[32:39], v4\n" \
"ds_read_b128 v[52:55], v0 offset:26624\nds_read_b128 v[80:83], v0 offset:27648\nv_mov_b64 v[12:13], 0\nv_smfmac_f32_16x16x64_bf16 v[76:79], v[16:19], a[48:55], v4\nds_read_b128 v[60:63], v1 offset:2048\nds_read_b128 v[64:67], v1 offset:3072\nv_mov_b64 v[14:15], 0\ns_waitcnt lgkmcnt(11)\n" \
"v_smfmac_f32_16x16x64_bf16 v[56:59], v[20:23], a[56:63], v4\nv_mov_b64 v[16:17], 0\nv_smfmac_f32_16x16x64_bf16 v[76:79], v[20:23], v[68:75], v4\nv_mov_b64 v[18:19], 0\ns_waitcnt lgkmcnt(10)\nv_smfmac_f32_16x16x64_bf16 v[56:59], v[24:27], a[80:87], v4\nds_read_b128 v[68:71], v1 offset:4096\nds_read_b128 v[72:75], v1 offset:5120\n" \
"v_mov_b64 v[20:21], 0\nv_smfmac_f32_16x16x64_bf16 v[76:79], v[24:27], a[96:103], v4\nv_mov_b64 v[22:23], 0\ns_waitcnt lgkmcnt(11)\nv_smfmac_f32_16x16x64_bf16 v[56:59], v[28:31], a[104:111], v4\nv_mov_b64 v[24:25], 0\nv_smfmac_f32_16x16x64_bf16 v[76:79], v[28:31], a[120:127], v4\nv_mov_b64 v[26:27], 0\n" \
"s_waitcnt lgkmcnt(10)\nv_smfmac_f32_16x16x64_bf16 v[56:59], v[32:35], a[136:143], v4\nv_mov_b64 v[28:29], 0\nv_smfmac_f32_16x16x64_bf16 v[76:79], v[32:35], a[152:159], v4\nv_mov_b64 v[30:31], 0\ns_waitcnt lgkmcnt(9)\nv_smfmac_f32_16x16x64_bf16 v[56:59], v[36:39], a[160:167], v4\nv_mov_b64 v[32:33], 0\n" \
"s_waitcnt lgkmcnt(2)\nv_smfmac_f32_16x16x64_bf16 v[76:79], v[36:39], v[60:67], v4\nv_mov_b64 v[34:35], 0\nv_smfmac_f32_16x16x64_bf16 v[56:59], v[40:43], a[184:191], v4\nv_mov_b64 v[36:37], 0\nv_smfmac_f32_16x16x64_bf16 v[76:79], v[40:43], a[200:207], v4\nds_read_b128 v[60:63], v1 offset:8192\nds_read_b128 v[64:67], v1 offset:9216\n" \
"v_mov_b64 v[38:39], 0\nv_smfmac_f32_16x16x64_bf16 v[56:59], v[44:47], a[208:215], v4\nv_mov_b64 v[40:41], 0\nv_smfmac_f32_16x16x64_bf16 v[76:79], v[44:47], a[224:231], v4\nv_mov_b64 v[42:43], 0\nv_smfmac_f32_16x16x64_bf16 v[56:59], v[48:51], a[240:247], v4\nv_mov_b64 v[44:45], 0\nv_smfmac_f32_16x16x64_bf16 v[76:79], v[48:51], v[96:103], v4\n" \
"v_mov_b64 v[46:47], 0\nv_smfmac_f32_16x16x64_bf16 v[56:59], v[52:55], v[104:111], v4\nv_mov_b64 v[48:49], 0\ns_waitcnt lgkmcnt(2)\nv_smfmac_f32_16x16x64_bf16 v[76:79], v[52:55], v[68:75], v4\nv_mov_b64 v[50:51], 0\nv_smfmac_f32_16x16x64_bf16 v[56:59], v[80:83], v[128:135], v4\nv_mov_b64 v[52:53], 0\n" \
"v_smfmac_f32_16x16x64_bf16 v[76:79], v[80:83], v[144:151], v4\nds_read_b128 v[68:71], v1 offset:10240\nds_read_b128 v[72:75], v1 offset:11264\nv_mov_b64 v[54:55], 0\n.Lvu_u%=:\nv_mov_b64 v[80:81], 0\nv_mov_b64 v[82:83], 0\nv_mov_b64 v[92:93], 0\n" \
"v_mov_b64 v[94:95], 0\ns_nop 2\nv_add_f32 v56, v56, v58\nv_add_f32 v57, v57, v59\nv_add_f32 v76, v76, v78\nv_add_f32 v77, v77, v79\nv_cvt_pk_bf16_f32 v58, v56, v76\nv_cvt_pk_bf16_f32 v59, v57, v77\n" \
"ds_write2_b32 v5, v58, v59 offset0:0 offset1:4\ns_waitcnt lgkmcnt(0)\ns_barrier\nds_read_b128 v[56:59], v0 offset:57344\nds_read_b128 v[76:79], v0 offset:58368\ns_waitcnt lgkmcnt(1)\nv_smfmac_f32_16x16x64_bf16 v[28:31], v[56:59], a[8:15], v4\ns_waitcnt lgkmcnt(0)\n" \
"v_smfmac_f32_16x16x64_bf16 v[28:31], v[76:79], a[24:31], v4\nv_smfmac_f32_16x16x64_bf16 v[32:35], v[56:59], a[40:47], v4\nv_smfmac_f32_16x16x64_bf16 v[32:35], v[76:79], v[60:67], v4\nds_read_b128 v[60:63], v1 offset:12288\nds_read_b128 v[64:67], v1 offset:13312\nv_add_f32 v28, v28, v30\nv_add_f32 v29, v29, v31\nv_cvt_pk_bf16_f32 v28, v28, v29\n" \
"v_smfmac_f32_16x16x64_bf16 v[36:39], v[56:59], a[64:71], v4\nv_smfmac_f32_16x16x64_bf16 v[36:39], v[76:79], a[72:79], v4\nv_add_f32 v32, v32, v34\nv_add_f32 v33, v33, v35\nv_cvt_pk_bf16_f32 v29, v32, v33\nv_smfmac_f32_16x16x64_bf16 v[40:43], v[56:59], a[88:95], v4\nv_smfmac_f32_16x16x64_bf16 v[40:43], v[76:79], v[68:75], v4\nds_read_b128 v[68:71], v1 offset:14336\n" \
"ds_read_b128 v[72:75], v1 offset:15360\nv_add_f32 v36, v36, v38\nv_add_f32 v37, v37, v39\nv_cvt_pk_bf16_f32 v30, v36, v37\nv_smfmac_f32_16x16x64_bf16 v[44:47], v[56:59], a[112:119], v4\nv_permlane16_swap_b32 v28, v29\nv_smfmac_f32_16x16x64_bf16 v[44:47], v[76:79], a[128:135], v4\nv_add_f32 v40, v40, v42\n" \
"v_add_f32 v41, v41, v43\nv_cvt_pk_bf16_f32 v31, v40, v41\nv_smfmac_f32_16x16x64_bf16 v[48:51], v[56:59], a[144:151], v4\ns_nop 0\nv_permlane16_swap_b32 v30, v31\ns_nop 1\nv_permlane32_swap_b32 v28, v30\nv_permlane32_swap_b32 v29, v31\n" \
"s_waitcnt lgkmcnt(2)\nv_smfmac_f32_16x16x64_bf16 v[48:51], v[76:79], v[60:67], v4\nds_read_b128 v[60:63], v1 offset:16384\nds_read_b128 v[64:67], v1 offset:17408\nv_and_or_b32 v28, v28, -2, v9\nv_and_or_b32 v30, v30, -2, v9\nv_smfmac_f32_16x16x64_bf16 v[52:55], v[56:59], a[168:175], v4\ns_mov_b32 s44, 0\n" \
"buffer_store_dwordx4 v[28:31], v6, %[rs], s44 offen sc1\nv_add_f32 v44, v44, v46\nv_add_f32 v45, v45, v47\nv_smfmac_f32_16x16x64_bf16 v[52:55], v[76:79], a[176:183], v4\nv_cvt_pk_bf16_f32 v44, v44, v45\nv_add_f32 v48, v48, v50\nv_add_f32 v49, v49, v51\nv_smfmac_f32_16x16x64_bf16 v[12:15], v[56:59], a[192:199], v4\n" \
"v_cvt_pk_bf16_f32 v45, v48, v49\ns_waitcnt lgkmcnt(2)\nv_smfmac_f32_16x16x64_bf16 v[12:15], v[76:79], v[68:75], v4\nds_read_b128 v[68:71], v1 offset:18432\nds_read_b128 v[72:75], v1 offset:19456\nv_add_f32 v52, v52, v54\nv_add_f32 v53, v53, v55\nv_cvt_pk_bf16_f32 v46, v52, v53\n" \
"v_smfmac_f32_16x16x64_bf16 v[16:19], v[56:59], a[216:223], v4\nv_permlane16_swap_b32 v44, v45\nv_smfmac_f32_16x16x64_bf16 v[16:19], v[76:79], a[232:239], v4\nv_add_f32 v12, v12, v14\nv_add_f32 v13, v13, v15\nv_cvt_pk_bf16_f32 v47, v12, v13\nv_smfmac_f32_16x16x64_bf16 v[20:23], v[56:59], a[248:255], v4\ns_nop 0\n" \
"v_permlane16_swap_b32 v46, v47\ns_nop 1\nv_permlane32_swap_b32 v44, v46\nv_permlane32_swap_b32 v45, v47\ns_waitcnt lgkmcnt(2)\nv_smfmac_f32_16x16x64_bf16 v[20:23], v[76:79], v[60:67], v4\nds_read_b128 v[60:63], v1 offset:20480\nds_read_b128 v[64:67], v1 offset:21504\n" \
"v_and_or_b32 v44, v44, -2, v9\nv_and_or_b32 v46, v46, -2, v9\nv_smfmac_f32_16x16x64_bf16 v[24:27], v[56:59], v[112:119], v4\ns_mov_b32 s44, 2048\nbuffer_store_dwordx4 v[44:47], v6, %[rs], s44 offen sc1\nv_add_f32 v16, v16, v18\nv_add_f32 v17, v17, v19\nv_smfmac_f32_16x16x64_bf16 v[24:27], v[76:79], v[120:127], v4\n" \
"v_cvt_pk_bf16_f32 v16, v16, v17\nv_add_f32 v20, v20, v22\nv_add_f32 v21, v21, v23\nv_smfmac_f32_16x16x64_bf16 v[80:83], v[56:59], v[136:143], v4\nv_cvt_pk_bf16_f32 v17, v20, v21\nv_mov_b64 v[20:21], 0\nv_mov_b64 v[22:23], 0\ns_waitcnt lgkmcnt(2)\n" \
"v_smfmac_f32_16x16x64_bf16 v[80:83], v[76:79], v[68:75], v4\nds_read_b128 v[68:71], v1 offset:22528\nds_read_b128 v[72:75], v1 offset:23552\nv_add_f32 v24, v24, v26\nv_add_f32 v25, v25, v27\nv_cvt_pk_bf16_f32 v18, v24, v25\nv_smfmac_f32_16x16x64_bf16 v[84:87], v[56:59], v[160:167], v4\nv_permlane16_swap_b32 v16, v17\n" \
"v_smfmac_f32_16x16x64_bf16 v[84:87], v[76:79], v[176:183], v4\nv_add_f32 v80, v80, v82\nv_add_f32 v81, v81, v83\nv_cvt_pk_bf16_f32 v19, v80, v81\nv_smfmac_f32_16x16x64_bf16 v[88:91], v[56:59], v[192:199], v4\ns_nop 0\nv_permlane16_swap_b32 v18, v19\ns_nop 1\n" \
"v_permlane32_swap_b32 v16, v18\nv_permlane32_swap_b32 v17, v19\ns_waitcnt lgkmcnt(2)\nv_smfmac_f32_16x16x64_bf16 v[88:91], v[76:79], v[60:67], v4\nv_and_or_b32 v16, v16, -2, v9\nv_and_or_b32 v18, v18, -2, v9\nv_smfmac_f32_16x16x64_bf16 v[92:95], v[56:59], v[216:223], v4\nds_read_b128 v[60:63], v1 offset:8192\n" \
"ds_read_b128 v[64:67], v1 offset:9216\ns_mov_b32 s44, 4096\nbuffer_store_dwordx4 v[16:19], v6, %[rs], s44 offen sc1\nv_add_f32 v84, v84, v86\nv_add_f32 v85, v85, v87\nv_smfmac_f32_16x16x64_bf16 v[92:95], v[76:79], v[224:231], v4\nv_cvt_pk_bf16_f32 v84, v84, v85\nv_add_f32 v88, v88, v90\n" \
"v_add_f32 v89, v89, v91\nv_smfmac_f32_16x16x64_bf16 v[20:23], v[56:59], v[240:247], v4\nv_cvt_pk_bf16_f32 v85, v88, v89\ns_waitcnt lgkmcnt(2)\nv_smfmac_f32_16x16x64_bf16 v[20:23], v[76:79], v[68:75], v4\nv_add_f32 v92, v92, v94\nv_add_f32 v93, v93, v95\nv_cvt_pk_bf16_f32 v86, v92, v93\n" \
"ds_read_b128 v[28:31], v0 offset:49152\nds_read_b128 v[32:35], v0 offset:50176\nds_read_b128 v[36:39], v0 offset:51200\nds_read_b128 v[40:43], v0 offset:52224\nds_read_b128 v[44:47], v0 offset:53248\nds_read_b128 v[48:51], v0 offset:54272\nds_read_b128 v[52:55], v0 offset:55296\nv_permlane16_swap_b32 v84, v85\n" \
"v_add_f32 v20, v20, v22\nv_add_f32 v21, v21, v23\nv_cvt_pk_bf16_f32 v87, v20, v21\ns_nop 1\nv_permlane16_swap_b32 v86, v87\ns_nop 1\nv_permlane32_swap_b32 v84, v86\nv_permlane32_swap_b32 v85, v87\n" \
"v_and_or_b32 v84, v84, -2, v9\nv_and_or_b32 v86, v86, -2, v9\ns_mov_b32 s44, 6144\nbuffer_store_dwordx4 v[84:87], v6, %[rs], s44 offen sc1\nds_read_b128 v[56:59], v0 offset:56320\ns_cmp_ge_i32 s40, 63\ns_cbranch_scc1 .Lvu_end%=\n.Lvu_y%=:\n" \
"s_add_u32 s40, s40, 1\ns_add_u32 s56, s56, %[fs]\nbuffer_load_dword v76, v8, %[rs], s56 offen sc1\ns_and_b32 s44, s40, 1\ns_lshl_b32 s55, s44, 22\ns_bfe_u32 s45, s40, 0x10001\nv_mov_b32 v9, s44\nv_mov_b32 v10, s45\n" \
"s_waitcnt lgkmcnt(7)\nv_mfma_f32_16x16x32_bf16 v[12:15], v[28:31], a[8:11], 0\ns_waitcnt lgkmcnt(6)\nv_mfma_f32_16x16x32_bf16 v[16:19], v[32:35], a[8:11], 0\ns_and_b32 s42, s40, 3\ns_lshl_b32 s42, s42, 15\ns_waitcnt lgkmcnt(5)\nv_mfma_f32_16x16x32_bf16 v[12:15], v[36:39], a[12:15], v[12:15]\n" \
"s_add_u32 s44, s40, 1\ns_lshl_b32 s44, s44, 17\ns_waitcnt lgkmcnt(4)\nv_mfma_f32_16x16x32_bf16 v[16:19], v[40:43], a[12:15], v[16:19]\ns_add_u32 s44, s44, %[xn]\ns_lshr_b32 s45, s44, 8\ns_waitcnt lgkmcnt(3)\nv_mfma_f32_16x16x32_bf16 v[12:15], v[44:47], a[24:27], v[12:15]\n" \
"s_and_b32 s45, s45, 0x2000\ns_xor_b32 s41, s44, s45\ns_waitcnt lgkmcnt(2)\nv_mfma_f32_16x16x32_bf16 v[16:19], v[48:51], a[24:27], v[16:19]\ns_add_u32 s41, s41, %[xw]\ns_waitcnt lgkmcnt(1)\nv_mfma_f32_16x16x32_bf16 v[12:15], v[52:55], a[28:31], v[12:15]\ns_waitcnt lgkmcnt(0)\n" \
"v_mfma_f32_16x16x32_bf16 v[16:19], v[56:59], a[28:31], v[16:19]\nv_mfma_f32_16x16x32_bf16 v[20:23], v[28:31], a[40:43], 0\ns_add_u32 s60, s55, 0\nv_mfma_f32_16x16x32_bf16 v[24:27], v[32:35], a[40:43], 0\nv_cvt_pk_bf16_f32 v68, v12, v13\nv_cvt_pk_bf16_f32 v69, v14, v15\nv_mfma_f32_16x16x32_bf16 v[20:23], v[36:39], a[44:47], v[20:23]\nv_cvt_pk_bf16_f32 v70, v16, v17\n" \
"v_cvt_pk_bf16_f32 v71, v18, v19\nv_mfma_f32_16x16x32_bf16 v[24:27], v[40:43], a[44:47], v[24:27]\nv_and_or_b32 v68, v68, -2, v10\nv_and_or_b32 v70, v70, -2, v10\nv_mfma_f32_16x16x32_bf16 v[20:23], v[44:47], v[60:63], v[20:23]\nbuffer_store_dwordx4 v[68:71], v7, %[rs], s60 offen offset:0 sc1\nv_mfma_f32_16x16x32_bf16 v[24:27], v[48:51], v[60:63], v[24:27]\nv_mfma_f32_16x16x32_bf16 v[20:23], v[52:55], v[64:67], v[20:23]\n" \
"v_mfma_f32_16x16x32_bf16 v[24:27], v[56:59], v[64:67], v[24:27]\nv_mfma_f32_16x16x32_bf16 v[12:15], v[28:31], a[64:67], 0\nds_read_b128 v[60:63], v1 offset:10240\nds_read_b128 v[64:67], v1 offset:11264\ns_add_u32 s60, s55, 4096\nv_mfma_f32_16x16x32_bf16 v[16:19], v[32:35], a[64:67], 0\nv_cvt_pk_bf16_f32 v72, v20, v21\nv_cvt_pk_bf16_f32 v73, v22, v23\n" \
"v_mfma_f32_16x16x32_bf16 v[12:15], v[36:39], a[68:71], v[12:15]\nv_cvt_pk_bf16_f32 v74, v24, v25\nv_cvt_pk_bf16_f32 v75, v26, v27\nv_mfma_f32_16x16x32_bf16 v[16:19], v[40:43], a[68:71], v[16:19]\nv_and_or_b32 v72, v72, -2, v10\nv_and_or_b32 v74, v74, -2, v10\nv_mfma_f32_16x16x32_bf16 v[12:15], v[44:47], a[72:75], v[12:15]\nbuffer_store_dwordx4 v[72:75], v7, %[rs], s60 offen offset:0 sc1\n" \
"v_mfma_f32_16x16x32_bf16 v[16:19], v[48:51], a[72:75], v[16:19]\nv_mfma_f32_16x16x32_bf16 v[12:15], v[52:55], a[76:79], v[12:15]\nv_mfma_f32_16x16x32_bf16 v[16:19], v[56:59], a[76:79], v[16:19]\nv_mfma_f32_16x16x32_bf16 v[20:23], v[28:31], a[88:91], 0\ns_add_u32 s60, s55, 16384\nv_mfma_f32_16x16x32_bf16 v[24:27], v[32:35], a[88:91], 0\nv_cvt_pk_bf16_f32 v68, v12, v13\nv_cvt_pk_bf16_f32 v69, v14, v15\n" \
"v_mfma_f32_16x16x32_bf16 v[20:23], v[36:39], a[92:95], v[20:23]\nv_cvt_pk_bf16_f32 v70, v16, v17\nv_cvt_pk_bf16_f32 v71, v18, v19\nv_mfma_f32_16x16x32_bf16 v[24:27], v[40:43], a[92:95], v[24:27]\nv_and_or_b32 v68, v68, -2, v10\nv_and_or_b32 v70, v70, -2, v10\ns_waitcnt lgkmcnt(0)\nv_mfma_f32_16x16x32_bf16 v[20:23], v[44:47], v[60:63], v[20:23]\n" \
"buffer_store_dwordx4 v[68:71], v7, %[rs], s60 offen offset:0 sc1\nv_mfma_f32_16x16x32_bf16 v[24:27], v[48:51], v[60:63], v[24:27]\nv_mfma_f32_16x16x32_bf16 v[20:23], v[52:55], v[64:67], v[20:23]\nv_mfma_f32_16x16x32_bf16 v[24:27], v[56:59], v[64:67], v[24:27]\nv_mfma_f32_16x16x32_bf16 v[12:15], v[28:31], a[112:115], 0\nds_read_b128 v[60:63], v1 offset:12288\nds_read_b128 v[64:67], v1 offset:13312\ns_add_u32 s60, s55, 20480\n" \
"v_mfma_f32_16x16x32_bf16 v[16:19], v[32:35], a[112:115], 0\nv_cvt_pk_bf16_f32 v72, v20, v21\nv_cvt_pk_bf16_f32 v73, v22, v23\nv_mfma_f32_16x16x32_bf16 v[12:15], v[36:39], a[116:119], v[12:15]\nv_cvt_pk_bf16_f32 v74, v24, v25\nv_cvt_pk_bf16_f32 v75, v26, v27\nv_mfma_f32_16x16x32_bf16 v[16:19], v[40:43], a[116:119], v[16:19]\nv_and_or_b32 v72, v72, -2, v10\n" \
"v_and_or_b32 v74, v74, -2, v10\nv_mfma_f32_16x16x32_bf16 v[12:15], v[44:47], a[128:131], v[12:15]\nbuffer_store_dwordx4 v[72:75], v7, %[rs], s60 offen offset:0 sc1\nv_mfma_f32_16x16x32_bf16 v[16:19], v[48:51], a[128:131], v[16:19]\nv_mfma_f32_16x16x32_bf16 v[12:15], v[52:55], a[132:135], v[12:15]\nv_mfma_f32_16x16x32_bf16 v[16:19], v[56:59], a[132:135], v[16:19]\nv_mfma_f32_16x16x32_bf16 v[20:23], v[28:31], a[144:147], 0\ns_add_u32 s60, s55, 0\n" \
"v_mfma_f32_16x16x32_bf16 v[24:27], v[32:35], a[144:147], 0\nv_cvt_pk_bf16_f32 v68, v12, v13\nv_cvt_pk_bf16_f32 v69, v14, v15\nv_mfma_f32_16x16x32_bf16 v[20:23], v[36:39], a[148:151], v[20:23]\nv_cvt_pk_bf16_f32 v70, v16, v17\nv_cvt_pk_bf16_f32 v71, v18, v19\nv_mfma_f32_16x16x32_bf16 v[24:27], v[40:43], a[148:151], v[24:27]\nv_and_or_b32 v68, v68, -2, v10\n" \
"v_and_or_b32 v70, v70, -2, v10\ns_waitcnt lgkmcnt(0)\nv_mfma_f32_16x16x32_bf16 v[20:23], v[44:47], v[60:63], v[20:23]\nbuffer_store_dwordx4 v[68:71], v7, %[rs], s60 offen offset:1024 sc1\ns_waitcnt vmcnt(5)\nv_cmp_ge_i32 vcc, s40, v76\nv_mfma_f32_16x16x32_bf16 v[24:27], v[48:51], v[60:63], v[24:27]\ns_cmp_eq_u64 vcc, 0\n" \
"s_cselect_b32 s61, s41, -2147483648\nv_mfma_f32_16x16x32_bf16 v[20:23], v[52:55], v[64:67], v[20:23]\nv_mfma_f32_16x16x32_bf16 v[24:27], v[56:59], v[64:67], v[24:27]\ns_add_u32 m0, s57, 0\ns_add_u32 s58, s61, 0\nbuffer_load_dwordx4 v0, %[rs], s58 offen offset:0 sc1 lds\nv_mfma_f32_16x16x32_bf16 v[12:15], v[28:31], a[168:171], 0\nds_read_b128 v[60:63], v1 offset:14336\n" \
"ds_read_b128 v[64:67], v1 offset:15360\ns_add_u32 s60, s55, 4096\nv_mfma_f32_16x16x32_bf16 v[16:19], v[32:35], a[168:171], 0\nv_cvt_pk_bf16_f32 v72, v20, v21\nv_cvt_pk_bf16_f32 v73, v22, v23\nv_mfma_f32_16x16x32_bf16 v[12:15], v[36:39], a[172:175], v[12:15]\nv_cvt_pk_bf16_f32 v74, v24, v25\nv_cvt_pk_bf16_f32 v75, v26, v27\n" \
"v_mfma_f32_16x16x32_bf16 v[16:19], v[40:43], a[172:175], v[16:19]\nv_and_or_b32 v72, v72, -2, v10\nv_and_or_b32 v74, v74, -2, v10\nv_mfma_f32_16x16x32_bf16 v[12:15], v[44:47], a[176:179], v[12:15]\nbuffer_store_dwordx4 v[72:75], v7, %[rs], s60 offen offset:1024 sc1\nv_mfma_f32_16x16x32_bf16 v[16:19], v[48:51], a[176:179], v[16:19]\nv_mfma_f32_16x16x32_bf16 v[12:15], v[52:55], a[180:183], v[12:15]\nv_mfma_f32_16x16x32_bf16 v[16:19], v[56:59], a[180:183], v[16:19]\n" \
"buffer_load_dwordx4 v0, %[rs], s58 offen offset:1024 sc1 lds\nv_mfma_f32_16x16x32_bf16 v[20:23], v[28:31], a[192:195], 0\ns_add_u32 s60, s55, 16384\nv_mfma_f32_16x16x32_bf16 v[24:27], v[32:35], a[192:195], 0\nv_cvt_pk_bf16_f32 v68, v12, v13\nv_cvt_pk_bf16_f32 v69, v14, v15\nv_mfma_f32_16x16x32_bf16 v[20:23], v[36:39], a[196:199], v[20:23]\nv_cvt_pk_bf16_f32 v70, v16, v17\n" \
"v_cvt_pk_bf16_f32 v71, v18, v19\nv_mfma_f32_16x16x32_bf16 v[24:27], v[40:43], a[196:199], v[24:27]\nv_and_or_b32 v68, v68, -2, v10\nv_and_or_b32 v70, v70, -2, v10\ns_waitcnt lgkmcnt(0)\nv_mfma_f32_16x16x32_bf16 v[20:23], v[44:47], v[60:63], v[20:23]\nbuffer_store_dwordx4 v[68:71], v7, %[rs], s60 offen offset:1024 sc1\nv_mfma_f32_16x16x32_bf16 v[24:27], v[48:51], v[60:63], v[24:27]\n" \
"v_mfma_f32_16x16x32_bf16 v[20:23], v[52:55], v[64:67], v[20:23]\nv_mfma_f32_16x16x32_bf16 v[24:27], v[56:59], v[64:67], v[24:27]\nbuffer_load_dwordx4 v0, %[rs], s58 offen offset:2048 sc1 lds\nv_mfma_f32_16x16x32_bf16 v[12:15], v[28:31], a[216:219], 0\nds_read_b128 v[60:63], v1 offset:16384\nds_read_b128 v[64:67], v1 offset:17408\ns_add_u32 s60, s55, 20480\nv_mfma_f32_16x16x32_bf16 v[16:19], v[32:35], a[216:219], 0\n" \
"v_cvt_pk_bf16_f32 v72, v20, v21\nv_cvt_pk_bf16_f32 v73, v22, v23\nv_mfma_f32_16x16x32_bf16 v[12:15], v[36:39], a[220:223], v[12:15]\nv_cvt_pk_bf16_f32 v74, v24, v25\nv_cvt_pk_bf16_f32 v75, v26, v27\nv_mfma_f32_16x16x32_bf16 v[16:19], v[40:43], a[220:223], v[16:19]\nv_and_or_b32 v72, v72, -2, v10\nv_and_or_b32 v74, v74, -2, v10\n" \
"v_mfma_f32_16x16x32_bf16 v[12:15], v[44:47], a[232:235], v[12:15]\nbuffer_store_dwordx4 v[72:75], v7, %[rs], s60 offen offset:1024 sc1\nv_mfma_f32_16x16x32_bf16 v[16:19], v[48:51], a[232:235], v[16:19]\nv_mfma_f32_16x16x32_bf16 v[12:15], v[52:55], a[236:239], v[12:15]\nv_mfma_f32_16x16x32_bf16 v[16:19], v[56:59], a[236:239], v[16:19]\nbuffer_load_dwordx4 v0, %[rs], s58 offen offset:3072 sc1 lds\nv_mfma_f32_16x16x32_bf16 v[20:23], v[28:31], a[248:251], 0\ns_add_u32 s60, s55, 0\n" \
"v_mfma_f32_16x16x32_bf16 v[24:27], v[32:35], a[248:251], 0\nv_cvt_pk_bf16_f32 v68, v12, v13\nv_cvt_pk_bf16_f32 v69, v14, v15\nv_mfma_f32_16x16x32_bf16 v[20:23], v[36:39], a[252:255], v[20:23]\nv_cvt_pk_bf16_f32 v70, v16, v17\nv_cvt_pk_bf16_f32 v71, v18, v19\nv_mfma_f32_16x16x32_bf16 v[24:27], v[40:43], a[252:255], v[24:27]\nv_and_or_b32 v68, v68, -2, v10\n" \
"v_and_or_b32 v70, v70, -2, v10\ns_waitcnt lgkmcnt(0)\nv_mfma_f32_16x16x32_bf16 v[20:23], v[44:47], v[60:63], v[20:23]\nbuffer_store_dwordx4 v[68:71], v7, %[rs], s60 offen offset:2048 sc1\nv_mfma_f32_16x16x32_bf16 v[24:27], v[48:51], v[60:63], v[24:27]\nv_mfma_f32_16x16x32_bf16 v[20:23], v[52:55], v[64:67], v[20:23]\nv_mfma_f32_16x16x32_bf16 v[24:27], v[56:59], v[64:67], v[24:27]\ns_add_u32 m0, s57, 16384\n" \
"s_add_u32 s58, s61, 32768\nbuffer_load_dwordx4 v0, %[rs], s58 offen offset:0 sc1 lds\nv_mfma_f32_16x16x32_bf16 v[12:15], v[28:31], v[112:115], 0\nds_read_b128 v[60:63], v1 offset:18432\nds_read_b128 v[64:67], v1 offset:19456\ns_add_u32 s60, s55, 4096\nv_mfma_f32_16x16x32_bf16 v[16:19], v[32:35], v[112:115], 0\nv_cvt_pk_bf16_f32 v72, v20, v21\n" \
"v_cvt_pk_bf16_f32 v73, v22, v23\nv_mfma_f32_16x16x32_bf16 v[12:15], v[36:39], v[116:119], v[12:15]\nv_cvt_pk_bf16_f32 v74, v24, v25\nv_cvt_pk_bf16_f32 v75, v26, v27\nv_mfma_f32_16x16x32_bf16 v[16:19], v[40:43], v[116:119], v[16:19]\nv_and_or_b32 v72, v72, -2, v10\nv_and_or_b32 v74, v74, -2, v10\nv_mfma_f32_16x16x32_bf16 v[12:15], v[44:47], v[120:123], v[12:15]\n" \
"buffer_store_dwordx4 v[72:75], v7, %[rs], s60 offen offset:2048 sc1\nv_mfma_f32_16x16x32_bf16 v[16:19], v[48:51], v[120:123], v[16:19]\nv_mfma_f32_16x16x32_bf16 v[12:15], v[52:55], v[124:127], v[12:15]\nv_mfma_f32_16x16x32_bf16 v[16:19], v[56:59], v[124:127], v[16:19]\nbuffer_load_dwordx4 v0, %[rs], s58 offen offset:1024 sc1 lds\nv_mfma_f32_16x16x32_bf16 v[20:23], v[28:31], v[136:139], 0\ns_add_u32 s60, s55, 16384\nv_mfma_f32_16x16x32_bf16 v[24:27], v[32:35], v[136:139], 0\n" \
"v_cvt_pk_bf16_f32 v68, v12, v13\nv_cvt_pk_bf16_f32 v69, v14, v15\nv_mfma_f32_16x16x32_bf16 v[20:23], v[36:39], v[140:143], v[20:23]\nv_cvt_pk_bf16_f32 v70, v16, v17\nv_cvt_pk_bf16_f32 v71, v18, v19\nv_mfma_f32_16x16x32_bf16 v[24:27], v[40:43], v[140:143], v[24:27]\nv_and_or_b32 v68, v68, -2, v10\nv_and_or_b32 v70, v70, -2, v10\n" \
"s_waitcnt lgkmcnt(0)\nv_mfma_f32_16x16x32_bf16 v[20:23], v[44:47], v[60:63], v[20:23]\nbuffer_store_dwordx4 v[68:71], v7, %[rs], s60 offen offset:2048 sc1\nv_mfma_f32_16x16x32_bf16 v[24:27], v[48:51], v[60:63], v[24:27]\nv_mfma_f32_16x16x32_bf16 v[20:23], v[52:55], v[64:67], v[20:23]\nv_mfma_f32_16x16x32_bf16 v[24:27], v[56:59], v[64:67], v[24:27]\nbuffer_load_dwordx4 v0, %[rs], s58 offen offset:2048 sc1 lds\nv_mfma_f32_16x16x32_bf16 v[12:15], v[28:31], v[160:163], 0\n" \
"ds_read_b128 v[60:63], v1 offset:20480\nds_read_b128 v[64:67], v1 offset:21504\ns_add_u32 s60, s55, 20480\nv_mfma_f32_16x16x32_bf16 v[16:19], v[32:35], v[160:163], 0\nv_cvt_pk_bf16_f32 v72, v20, v21\nv_cvt_pk_bf16_f32 v73, v22, v23\nv_mfma_f32_16x16x32_bf16 v[12:15], v[36:39], v[164:167], v[12:15]\nv_cvt_pk_bf16_f32 v74, v24, v25\n" \
"v_cvt_pk_bf16_f32 v75, v26, v27\nv_mfma_f32_16x16x32_bf16 v[16:19], v[40:43], v[164:167], v[16:19]\nv_and_or_b32 v72, v72, -2, v10\nv_and_or_b32 v74, v74, -2, v10\nv_mfma_f32_16x16x32_bf16 v[12:15], v[44:47], v[176:179], v[12:15]\nbuffer_store_dwordx4 v[72:75], v7, %[rs], s60 offen offset:2048 sc1\nv_mfma_f32_16x16x32_bf16 v[16:19], v[48:51], v[176:179], v[16:19]\nv_mfma_f32_16x16x32_bf16 v[12:15], v[52:55], v[180:183], v[12:15]\n" \
"v_mfma_f32_16x16x32_bf16 v[16:19], v[56:59], v[180:183], v[16:19]\nbuffer_load_dwordx4 v0, %[rs], s58 offen offset:3072 sc1 lds\nv_mfma_f32_16x16x32_bf16 v[20:23], v[28:31], v[192:195], 0\ns_add_u32 s60, s55, 0\nv_mfma_f32_16x16x32_bf16 v[24:27], v[32:35], v[192:195], 0\nv_cvt_pk_bf16_f32 v68, v12, v13\nv_cvt_pk_bf16_f32 v69, v14, v15\nv_mfma_f32_16x16x32_bf16 v[20:23], v[36:39], v[196:199], v[20:23]\n" \
"v_cvt_pk_bf16_f32 v70, v16, v17\nv_cvt_pk_bf16_f32 v71, v18, v19\nv_mfma_f32_16x16x32_bf16 v[24:27], v[40:43], v[196:199], v[24:27]\nv_and_or_b32 v68, v68, -2, v10\nv_and_or_b32 v70, v70, -2, v10\ns_waitcnt lgkmcnt(0)\nv_mfma_f32_16x16x32_bf16 v[20:23], v[44:47], v[60:63], v[20:23]\nbuffer_store_dwordx4 v[68:71], v7, %[rs], s60 offen offset:3072 sc1\n" \
"v_mfma_f32_16x16x32_bf16 v[24:27], v[48:51], v[60:63], v[24:27]\nv_mfma_f32_16x16x32_bf16 v[20:23], v[52:55], v[64:67], v[20:23]\nv_mfma_f32_16x16x32_bf16 v[24:27], v[56:59], v[64:67], v[24:27]\nv_mfma_f32_16x16x32_bf16 v[12:15], v[28:31], v[216:219], 0\nds_read_b128 v[60:63], v1 offset:22528\nds_read_b128 v[64:67], v1 offset:23552\ns_add_u32 s60, s55, 4096\nv_mfma_f32_16x16x32_bf16 v[16:19], v[32:35], v[216:219], 0\n" \
"v_cvt_pk_bf16_f32 v72, v20, v21\nv_cvt_pk_bf16_f32 v73, v22, v23\nv_mfma_f32_16x16x32_bf16 v[12:15], v[36:39], v[220:223], v[12:15]\nv_cvt_pk_bf16_f32 v74, v24, v25\nv_cvt_pk_bf16_f32 v75, v26, v27\nv_mfma_f32_16x16x32_bf16 v[16:19], v[40:43], v[220:223], v[16:19]\nv_and_or_b32 v72, v72, -2, v10\nv_and_or_b32 v74, v74, -2, v10\n" \
"v_mfma_f32_16x16x32_bf16 v[12:15], v[44:47], v[224:227], v[12:15]\nbuffer_store_dwordx4 v[72:75], v7, %[rs], s60 offen offset:3072 sc1\nv_mfma_f32_16x16x32_bf16 v[16:19], v[48:51], v[224:227], v[16:19]\nv_mfma_f32_16x16x32_bf16 v[12:15], v[52:55], v[228:231], v[12:15]\nv_mfma_f32_16x16x32_bf16 v[16:19], v[56:59], v[228:231], v[16:19]\nv_mfma_f32_16x16x32_bf16 v[20:23], v[28:31], v[240:243], 0\ns_add_u32 s60, s55, 16384\nv_mfma_f32_16x16x32_bf16 v[24:27], v[32:35], v[240:243], 0\n" \
"v_cvt_pk_bf16_f32 v68, v12, v13\nv_cvt_pk_bf16_f32 v69, v14, v15\nv_mfma_f32_16x16x32_bf16 v[20:23], v[36:39], v[244:247], v[20:23]\nv_cvt_pk_bf16_f32 v70, v16, v17\nv_cvt_pk_bf16_f32 v71, v18, v19\nv_mfma_f32_16x16x32_bf16 v[24:27], v[40:43], v[244:247], v[24:27]\nv_and_or_b32 v68, v68, -2, v10\nv_and_or_b32 v70, v70, -2, v10\n" \
"s_waitcnt lgkmcnt(0)\nv_mfma_f32_16x16x32_bf16 v[20:23], v[44:47], v[60:63], v[20:23]\nbuffer_store_dwordx4 v[68:71], v7, %[rs], s60 offen offset:3072 sc1\nv_mfma_f32_16x16x32_bf16 v[24:27], v[48:51], v[60:63], v[24:27]\nv_mfma_f32_16x16x32_bf16 v[20:23], v[52:55], v[64:67], v[20:23]\nv_mfma_f32_16x16x32_bf16 v[24:27], v[56:59], v[64:67], v[24:27]\ns_add_u32 s60, s55, 20480\ns_nop 2\n" \
"v_cvt_pk_bf16_f32 v72, v20, v21\nv_cvt_pk_bf16_f32 v73, v22, v23\ns_nop 1\nv_cvt_pk_bf16_f32 v74, v24, v25\nv_cvt_pk_bf16_f32 v75, v26, v27\nv_and_or_b32 v72, v72, -2, v10\nv_and_or_b32 v74, v74, -2, v10\nbuffer_store_dwordx4 v[72:75], v7, %[rs], s60 offen offset:3072 sc1\n" \
"s_cmp_lt_u32 s40, 2\ns_cbranch_scc1 .Lvu_yf%=\n.Lvu_yfr%=:\ns_cbranch_vccz .Lvu_tok%=\ns_branch .Lvu_slow%=\n.Lvu_re3%=:\nv_max_u32 v16, v80, v82\nv_cmp_eq_u32 s[46:47], -1, v16\n" \
"v_max_u32 v16, v84, v86\nv_cmp_eq_u32 s[48:49], -1, v16\nv_max_u32 v16, v88, v90\nv_cmp_eq_u32 s[50:51], -1, v16\nv_max_u32 v16, v92, v94\nv_cmp_eq_u32 s[52:53], -1, v16\ns_mov_b64 exec, s[46:47]\nbuffer_load_dwordx4 v[80:83], v3, %[rs], s42 offen offset:0 sc1\n" \
"s_mov_b64 exec, s[48:49]\nbuffer_load_dwordx4 v[84:87], v3, %[rs], s42 offen offset:1024 sc1\ns_mov_b64 exec, s[50:51]\nbuffer_load_dwordx4 v[88:91], v3, %[rs], s42 offen offset:2048 sc1\ns_mov_b64 exec, s[52:53]\nbuffer_load_dwordx4 v[92:95], v3, %[rs], s42 offen offset:3072 sc1\ns_mov_b64 exec, -1\nv_mov_b64 v[56:57], 0\n" \
"v_mov_b64 v[58:59], 0\nv_mov_b64 v[76:77], 0\nv_mov_b64 v[78:79], 0\ns_add_u32 s43, s43, 1\ns_cmp_lt_u32 s43, 262144\ns_cbranch_scc1 .Lvu_poll3%=\ns_branch .Lvu_abort%=\n.Lvu_yf%=:\n" \
"v_mov_b32 v12, s40\ns_waitcnt vmcnt(0)\nbuffer_store_dword v12, off, %[rs], %[fy] sc1\ns_branch .Lvu_yfr%=\n.Lvu_first%=:\ns_waitcnt vmcnt(0) lgkmcnt(0)\ns_barrier\nds_read_b128 v[60:63], v1 offset:8192\n" \
"ds_read_b128 v[64:67], v1 offset:9216\nds_read_b128 v[28:31], v0 offset:49152\nds_read_b128 v[32:35], v0 offset:50176\nds_read_b128 v[36:39], v0 offset:51200\nds_read_b128 v[40:43], v0 offset:52224\nds_read_b128 v[44:47], v0 offset:53248\nds_read_b128 v[48:51], v0 offset:54272\nds_read_b128 v[52:55], v0 offset:55296\n" \
"ds_read_b128 v[56:59], v0 offset:56320\ns_waitcnt lgkmcnt(0)\ns_branch .Lvu_y%=\n.Lvu_nov%=:\nbuffer_load_dwordx4 v[80:83], v3, %[rs], s42 offen offset:0 sc1\nbuffer_load_dwordx4 v[84:87], v3, %[rs], s42 offen offset:1024 sc1\nbuffer_load_dwordx4 v[88:91], v3, %[rs], s42 offen offset:2048 sc1\nbuffer_load_dwordx4 v[92:95], v3, %[rs], s42 offen offset:3072 sc1\n" \
"v_mov_b64 v[56:57], 0\nv_mov_b64 v[58:59], 0\nv_mov_b64 v[76:77], 0\nv_mov_b64 v[78:79], 0\ns_branch .Lvu_crit%=\n.Lvu_abort%=:\ns_mov_b64 exec, -1\nv_mov_b32 v16, 0\n" \
"s_movk_i32 s44, 6400\nbuffer_store_dword v16, off, %[rs], s44 sc1\n.Lvu_end%=:\ns_waitcnt vmcnt(0) lgkmcnt(0)\n" \
""
#define RD_ASM \
"v_mbcnt_lo_u32_b32 v255, -1, 0\nv_mbcnt_hi_u32_b32 v255, -1, v255\nv_lshlrev_b32 v255, 2, v255\nv_add_u32 v255, %[tb], v255\nds_read_b32 v196, v255 offset:0\nds_read_b32 v197, v255 offset:256\nds_read_b32 v198, v255 offset:512\nds_read_b32 v199, v255 offset:768\n" \
"ds_read_b32 v200, v255 offset:1024\nds_read_b32 v201, v255 offset:1280\nds_read_b32 v202, v255 offset:1536\nds_read_b32 v203, v255 offset:1792\nds_read_b32 v204, v255 offset:2048\nds_read_b32 v205, v255 offset:2304\nds_read_b32 v206, v255 offset:2560\ns_waitcnt lgkmcnt(0)\n" \
"v_accvgpr_write_b32 a32, v206\ns_mov_b32 s40, 0\ns_mov_b32 s43, 0\ns_add_u32 s57, %[su], 4096\ns_add_u32 s56, %[sp], 4096\ns_mov_b32 s41, %[sy]\n.Lrd_yflag%=:\nv_accvgpr_read_b32 v206, a32\n" \
"buffer_load_dword v208, v206, %[rs], 0 offen sc1\ns_waitcnt vmcnt(0)\nv_cmp_gt_i32 vcc, s40, v208\ns_cbranch_vccz .Lrd_yld%=\ns_add_u32 s43, s43, 1\ns_cmp_lt_u32 s43, 262144\ns_cbranch_scc1 .Lrd_yflag%=\ns_branch .Lrd_abort%=\n" \
".Lrd_yld%=:\ns_add_u32 s60, s41, 0\nbuffer_load_dwordx4 v[0:3], v196, %[rs], s60 offen offset:0 sc1\ns_add_u32 s60, s41, 0\nbuffer_load_dwordx4 v[32:35], v196, %[rs], s60 offen offset:1024 sc1\ns_add_u32 s60, s41, 0\nbuffer_load_dwordx4 v[64:67], v196, %[rs], s60 offen offset:2048 sc1\ns_add_u32 s60, s41, 131072\n" \
"buffer_load_dwordx4 v[4:7], v196, %[rs], s60 offen offset:0 sc1\ns_add_u32 s60, s41, 131072\nbuffer_load_dwordx4 v[36:39], v196, %[rs], s60 offen offset:1024 sc1\ns_add_u32 s60, s41, 131072\nbuffer_load_dwordx4 v[68:71], v196, %[rs], s60 offen offset:2048 sc1\ns_add_u32 s60, s41, 262144\nbuffer_load_dwordx4 v[8:11], v196, %[rs], s60 offen offset:0 sc1\ns_add_u32 s60, s41, 262144\n" \
"buffer_load_dwordx4 v[40:43], v196, %[rs], s60 offen offset:1024 sc1\ns_add_u32 s60, s41, 262144\nbuffer_load_dwordx4 v[72:75], v196, %[rs], s60 offen offset:2048 sc1\ns_add_u32 s60, s41, 393216\nbuffer_load_dwordx4 v[12:15], v196, %[rs], s60 offen offset:0 sc1\ns_add_u32 s60, s41, 393216\nbuffer_load_dwordx4 v[44:47], v196, %[rs], s60 offen offset:1024 sc1\ns_add_u32 s60, s41, 393216\n" \
"buffer_load_dwordx4 v[76:79], v196, %[rs], s60 offen offset:2048 sc1\ns_add_u32 s60, s41, 524288\nbuffer_load_dwordx4 v[16:19], v196, %[rs], s60 offen offset:0 sc1\ns_add_u32 s60, s41, 524288\nbuffer_load_dwordx4 v[48:51], v196, %[rs], s60 offen offset:1024 sc1\ns_add_u32 s60, s41, 524288\nbuffer_load_dwordx4 v[80:83], v196, %[rs], s60 offen offset:2048 sc1\ns_add_u32 s60, s41, 655360\n" \
"buffer_load_dwordx4 v[20:23], v196, %[rs], s60 offen offset:0 sc1\ns_add_u32 s60, s41, 655360\nbuffer_load_dwordx4 v[52:55], v196, %[rs], s60 offen offset:1024 sc1\ns_add_u32 s60, s41, 655360\nbuffer_load_dwordx4 v[84:87], v196, %[rs], s60 offen offset:2048 sc1\ns_add_u32 s60, s41, 786432\nbuffer_load_dwordx4 v[24:27], v196, %[rs], s60 offen offset:0 sc1\ns_add_u32 s60, s41, 786432\n" \
"buffer_load_dwordx4 v[56:59], v196, %[rs], s60 offen offset:1024 sc1\ns_add_u32 s60, s41, 786432\nbuffer_load_dwordx4 v[88:91], v196, %[rs], s60 offen offset:2048 sc1\ns_add_u32 s60, s41, 917504\nbuffer_load_dwordx4 v[28:31], v196, %[rs], s60 offen offset:0 sc1\ns_add_u32 s60, s41, 917504\nbuffer_load_dwordx4 v[60:63], v196, %[rs], s60 offen offset:1024 sc1\ns_add_u32 s60, s41, 917504\n" \
"buffer_load_dwordx4 v[92:95], v196, %[rs], s60 offen offset:2048 sc1\ns_add_u32 s60, s41, 0\nbuffer_load_dwordx4 v[96:99], v196, %[rs], s60 offen offset:3072 sc1\ns_add_u32 s60, s41, 131072\nbuffer_load_dwordx4 v[100:103], v196, %[rs], s60 offen offset:3072 sc1\ns_add_u32 s60, s41, 262144\nbuffer_load_dwordx4 v[104:107], v196, %[rs], s60 offen offset:3072 sc1\ns_add_u32 s60, s41, 393216\n" \
"buffer_load_dwordx4 v[108:111], v196, %[rs], s60 offen offset:3072 sc1\ns_add_u32 s60, s41, 524288\nbuffer_load_dwordx4 v[112:115], v196, %[rs], s60 offen offset:3072 sc1\ns_add_u32 s60, s41, 655360\nbuffer_load_dwordx4 v[116:119], v196, %[rs], s60 offen offset:3072 sc1\ns_add_u32 s60, s41, 786432\nbuffer_load_dwordx4 v[120:123], v196, %[rs], s60 offen offset:3072 sc1\ns_add_u32 s60, s41, 917504\n" \
"buffer_load_dwordx4 v[124:127], v196, %[rs], s60 offen offset:3072 sc1\ns_mov_b64 exec, -1\nbuffer_load_dwordx4 v[128:131], v196, %[rs], %[su] offen offset:0 sc1\nbuffer_load_dwordx4 v[132:135], v196, %[rs], %[su] offen offset:1024 sc1\nbuffer_load_dwordx4 v[136:139], v196, %[rs], %[su] offen offset:2048 sc1\nbuffer_load_dwordx4 v[140:143], v196, %[rs], %[su] offen offset:3072 sc1\nbuffer_load_dwordx4 v[144:147], v196, %[rs], s57 offen offset:0 sc1\nbuffer_load_dwordx4 v[148:151], v196, %[rs], s57 offen offset:1024 sc1\n" \
"buffer_load_dwordx4 v[152:155], v196, %[rs], s57 offen offset:2048 sc1\nbuffer_load_dwordx4 v[156:159], v196, %[rs], s57 offen offset:3072 sc1\nbuffer_load_dwordx4 v[160:163], v196, %[rs], %[sp] offen offset:0 sc1\nbuffer_load_dwordx4 v[164:167], v196, %[rs], %[sp] offen offset:1024 sc1\nbuffer_load_dwordx4 v[168:171], v196, %[rs], %[sp] offen offset:2048 sc1\nbuffer_load_dwordx4 v[172:175], v196, %[rs], %[sp] offen offset:3072 sc1\nbuffer_load_dwordx4 v[176:179], v196, %[rs], s56 offen offset:0 sc1\ns_waitcnt vmcnt(0)\n" \
"s_branch .Lrd_ychk%=\n.Lrd_u2%=:\nbuffer_load_dwordx4 v[128:131], v196, %[rs], %[su] offen offset:0 sc1\nbuffer_load_dwordx4 v[132:135], v196, %[rs], %[su] offen offset:1024 sc1\ns_add_u32 s43, s43, 1\ns_cmp_ge_u32 s43, 262144\ns_cbranch_scc1 .Lrd_abort%=\ns_waitcnt vmcnt(0)\n" \
"v_bitop3_b32 v208, v128, v130, s54 bitop3:0x7e\nv_bitop3_b32 v209, v132, v134, s54 bitop3:0x7e\nv_bitop3_b32 v208, v208, 1, v209 bitop3:0xc8\nv_cmp_ne_u32 vcc, 0, v208\ns_cbranch_vccnz .Lrd_u2%=\n.Lrd_u3%=:\nbuffer_load_dwordx4 v[136:139], v196, %[rs], %[su] offen offset:2048 sc1\nbuffer_load_dwordx4 v[140:143], v196, %[rs], %[su] offen offset:3072 sc1\n" \
"buffer_load_dwordx4 v[144:147], v196, %[rs], s57 offen offset:0 sc1\nbuffer_load_dwordx4 v[148:151], v196, %[rs], s57 offen offset:1024 sc1\ns_mov_b64 exec, -1\ns_add_u32 s43, s43, 1\ns_cmp_ge_u32 s43, 262144\ns_cbranch_scc1 .Lrd_abort%=\ns_waitcnt vmcnt(0)\nv_bitop3_b32 v208, v136, v138, s54 bitop3:0x7e\n" \
"v_bitop3_b32 v209, v140, v142, s54 bitop3:0x7e\nv_bitop3_b32 v210, v144, v146, s54 bitop3:0x7e\nv_bitop3_b32 v211, v148, v150, s54 bitop3:0x7e\nv_or3_b32 v208, v208, v209, v210\nv_bitop3_b32 v208, v208, 1, v211 bitop3:0xc8\nv_cmp_ne_u32 vcc, 0, v208\ns_mov_b64 exec, vcc\ns_cbranch_vccnz .Lrd_u3%=\n" \
"s_mov_b64 exec, -1\nbuffer_load_dwordx4 v[160:163], v196, %[rs], %[sp] offen offset:0 sc1\nbuffer_load_dwordx4 v[164:167], v196, %[rs], %[sp] offen offset:1024 sc1\nbuffer_load_dwordx4 v[168:171], v196, %[rs], %[sp] offen offset:2048 sc1\nbuffer_load_dwordx4 v[172:175], v196, %[rs], %[sp] offen offset:3072 sc1\nbuffer_load_dwordx4 v[176:179], v196, %[rs], s56 offen offset:0 sc1\nbuffer_load_dwordx4 v[152:155], v196, %[rs], s57 offen offset:2048 sc1\nbuffer_load_dwordx4 v[156:159], v196, %[rs], s57 offen offset:3072 sc1\n" \
".Lrd_ra%=:\ns_waitcnt vmcnt(6)\nv_smfmac_f32_16x16x64_bf16 v[180:183], v[160:163], v[0:7], v197\nv_bitop3_b32 v208, v160, v162, s54 bitop3:0x7e\nv_smfmac_f32_16x16x64_bf16 v[184:187], v[160:163], v[32:39], v197\nv_cmp_gt_i32 s[46:47], 0, v208\nv_smfmac_f32_16x16x64_bf16 v[188:191], v[160:163], v[64:71], v197\nv_smfmac_f32_16x16x64_bf16 v[192:195], v[160:163], v[96:103], v197\n" \
"s_waitcnt vmcnt(5)\nv_smfmac_f32_16x16x64_bf16 v[180:183], v[164:167], v[8:15], v197\nv_bitop3_b32 v208, v164, v166, s54 bitop3:0x7e\nv_smfmac_f32_16x16x64_bf16 v[184:187], v[164:167], v[40:47], v197\nv_cmp_gt_i32 s[48:49], 0, v208\nv_smfmac_f32_16x16x64_bf16 v[188:191], v[164:167], v[72:79], v197\nv_smfmac_f32_16x16x64_bf16 v[192:195], v[164:167], v[104:111], v197\ns_or_b64 vcc, s[46:47], s[48:49]\n" \
"s_waitcnt vmcnt(4)\nv_smfmac_f32_16x16x64_bf16 v[180:183], v[168:171], v[16:23], v197\nv_bitop3_b32 v208, v168, v170, s54 bitop3:0x7e\nv_smfmac_f32_16x16x64_bf16 v[184:187], v[168:171], v[48:55], v197\nv_cmp_gt_i32 s[50:51], 0, v208\nv_smfmac_f32_16x16x64_bf16 v[188:191], v[168:171], v[80:87], v197\nv_smfmac_f32_16x16x64_bf16 v[192:195], v[168:171], v[112:119], v197\ns_or_b64 vcc, vcc, s[50:51]\n" \
"s_waitcnt vmcnt(3)\nv_smfmac_f32_16x16x64_bf16 v[180:183], v[172:175], v[24:31], v197\nv_bitop3_b32 v208, v172, v174, s54 bitop3:0x7e\ns_waitcnt vmcnt(2)\nv_smfmac_f32_16x16x64_bf16 v[180:183], v[176:179], v[128:135], v197\nv_bitop3_b32 v209, v176, v178, s54 bitop3:0x7e\nv_smfmac_f32_16x16x64_bf16 v[184:187], v[172:175], v[56:63], v197\nv_bitop3_b32 v210, v177, v179, s54 bitop3:0x7e\n" \
"v_cmp_gt_i32 s[52:53], 0, v208\ns_waitcnt vmcnt(0)\nv_smfmac_f32_16x16x64_bf16 v[184:187], v[176:179], v[136:143], v197\nv_or_b32 v209, v209, v210\nv_bitop3_b32 v208, v152, v154, s54 bitop3:0x7e\nv_smfmac_f32_16x16x64_bf16 v[188:191], v[172:175], v[88:95], v197\ns_or_b64 vcc, vcc, s[52:53]\nv_bitop3_b32 v210, v156, v158, s54 bitop3:0x7e\n" \
"v_cmp_gt_i32 s[64:65], 0, v209\nv_smfmac_f32_16x16x64_bf16 v[188:191], v[176:179], v[144:151], v197\nv_bitop3_b32 v208, v208, 1, v210 bitop3:0xc8\nv_smfmac_f32_16x16x64_bf16 v[192:195], v[172:175], v[120:127], v197\ns_or_b64 vcc, vcc, s[64:65]\nv_cmp_ne_u32 s[62:63], 0, v208\nv_smfmac_f32_16x16x64_bf16 v[192:195], v[176:179], v[152:159], v197\nv_add_f32 v180, v180, v182\n" \
"v_add_f32 v181, v181, v183\nds_write2_b32 v198, v180, v181 offset0:0 offset1:8\ns_or_b64 vcc, vcc, s[62:63]\ns_cbranch_scc1 .Lrd_rb%=\nv_add_f32 v184, v184, v186\nv_add_f32 v185, v185, v187\nds_write2_b32 v198, v184, v185 offset0:32 offset1:40\nv_add_f32 v188, v188, v190\n" \
"v_add_f32 v189, v189, v191\nds_write2_b32 v198, v188, v189 offset0:64 offset1:72\nv_add_f32 v192, v192, v194\nv_add_f32 v193, v193, v195\nds_write2_b32 v198, v192, v193 offset0:96 offset1:104\ns_add_u32 s60, s58, 0\nbuffer_load_dwordx4 v[212:215], v196, %[rs], s60 offen offset:0 sc1\ns_waitcnt lgkmcnt(0)\n" \
"s_barrier\nds_read_b128 v[180:183], v199 offset:0\nds_read_b128 v[184:187], v199 offset:4096\nds_read_b128 v[188:191], v199 offset:8192\nds_read_b128 v[192:195], v199 offset:12288\ns_and_b32 s44, s54, 0x80008000\ns_add_u32 s60, s58, 0\nbuffer_load_dwordx4 v[216:219], v196, %[rs], s60 offen offset:1024 sc1\n" \
"s_waitcnt lgkmcnt(2)\nv_pk_add_f32 v[182:183], v[182:183], v[186:187]\nv_pk_add_f32 v[180:181], v[180:181], v[184:185]\ns_waitcnt lgkmcnt(0)\nv_pk_add_f32 v[186:187], v[188:189], v[192:193]\nv_pk_add_f32 v[184:185], v[190:191], v[194:195]\nv_pk_add_f32 v[180:181], v[180:181], v[186:187]\nv_pk_add_f32 v[182:183], v[182:183], v[184:185]\n" \
"v_cvt_pk_bf16_f32 v180, v180, v181\nv_xor_b32 v180, s44, v180\nv_cvt_pk_bf16_f32 v181, v182, v183\nv_min_u32 v180, -2, v180\nv_xor_b32 v209, s44, v181\nv_or_b32 v208, v180, v201\nbuffer_store_dwordx2 v[208:209], v206, %[rs], 0 offen sc1\ns_add_u32 s60, s58, 0\n" \
"buffer_load_dwordx4 v[220:223], v196, %[rs], s60 offen offset:2048 sc1\ns_add_u32 s60, s58, 0\nbuffer_load_dwordx4 v[224:227], v196, %[rs], s60 offen offset:3072 sc1\ns_add_u32 s60, s58, 4096\nbuffer_load_dwordx4 v[228:231], v196, %[rs], s60 offen offset:0 sc1\ns_add_u32 s60, s58, 4096\nbuffer_load_dwordx4 v[232:235], v196, %[rs], s60 offen offset:1024 sc1\ns_add_u32 s60, s58, 4096\n" \
"buffer_load_dwordx4 v[236:239], v196, %[rs], s60 offen offset:2048 sc1\ns_add_u32 s60, s58, 4096\nbuffer_load_dwordx4 v[240:243], v196, %[rs], s60 offen offset:3072 sc1\ns_add_u32 s60, s58, 16384\nbuffer_load_dwordx4 v[244:247], v196, %[rs], s60 offen offset:0 sc1\ns_add_u32 s60, s58, 16384\nbuffer_load_dwordx4 v[248:251], v196, %[rs], s60 offen offset:1024 sc1\ns_add_u32 s60, s58, 16384\n" \
"buffer_load_dwordx4 v[252:255], v196, %[rs], s60 offen offset:2048 sc1\ns_add_u32 s60, s58, 16384\nbuffer_load_dwordx4 v[160:163], v196, %[rs], s60 offen offset:3072 sc1\ns_add_u32 s60, s58, 20480\nbuffer_load_dwordx4 v[164:167], v196, %[rs], s60 offen offset:0 sc1\ns_add_u32 s60, s58, 20480\nbuffer_load_dwordx4 v[168:171], v196, %[rs], s60 offen offset:2048 sc1\ns_xor_b32 s41, s41, 4194304\n" \
"v_mov_b32 v207, s40\ns_waitcnt vmcnt(14)\nv_bitop3_b32 v209, v212, v214, s61 bitop3:0x7e\nv_cmp_gt_i32 vcc, 0, v209\ns_cbranch_vccnz .Lrd_tre0%=\n.Lrd_tl0%=:\ns_waitcnt vmcnt(13)\nv_mfma_f32_16x16x32_bf16 a[0:3], v[0:3], v[212:215], 0\n" \
"s_add_u32 s60, s59, 0\nbuffer_load_dwordx4 v[172:175], v196, %[rs], s60 offen offset:0 sc1\nv_bitop3_b32 v209, v216, v218, s61 bitop3:0x7e\nv_mfma_f32_16x16x32_bf16 a[8:11], v[32:35], v[212:215], 0\nv_mfma_f32_16x16x32_bf16 a[16:19], v[64:67], v[212:215], 0\nv_cmp_gt_i32 vcc, 0, v209\nv_mfma_f32_16x16x32_bf16 a[24:27], v[96:99], v[212:215], 0\ns_cbranch_vccnz .Lrd_tre1%=\n" \
".Lrd_tl1%=:\ns_waitcnt vmcnt(12)\nv_mfma_f32_16x16x32_bf16 a[4:7], v[0:3], v[216:219], 0\ns_add_u32 s60, s59, 0\nbuffer_load_dwordx4 v[176:179], v196, %[rs], s60 offen offset:2048 sc1\nv_bitop3_b32 v209, v220, v222, s61 bitop3:0x7e\nv_mfma_f32_16x16x32_bf16 a[12:15], v[32:35], v[216:219], 0\ns_lshl_b32 s42, s40, 17\n" \
"s_add_u32 s42, s42, %[at]\nv_mfma_f32_16x16x32_bf16 a[20:23], v[64:67], v[216:219], 0\nv_cmp_gt_i32 vcc, 0, v209\nv_mfma_f32_16x16x32_bf16 a[28:31], v[96:99], v[216:219], 0\ns_cbranch_vccnz .Lrd_tre2%=\n.Lrd_tl2%=:\ns_waitcnt vmcnt(12)\nv_mfma_f32_16x16x32_bf16 a[0:3], v[4:7], v[220:223], a[0:3]\n" \
"s_add_u32 s60, s58, 20480\nbuffer_load_dwordx4 v[180:183], v196, %[rs], s60 offen offset:1024 sc1\nv_bitop3_b32 v209, v224, v226, s61 bitop3:0x7e\nv_mfma_f32_16x16x32_bf16 a[8:11], v[36:39], v[220:223], a[8:11]\ns_lshr_b32 s45, s42, 8\ns_and_b32 s45, s45, 0x2000\nv_mfma_f32_16x16x32_bf16 a[16:19], v[68:71], v[220:223], a[16:19]\nv_cmp_gt_i32 vcc, 0, v209\n" \
"v_mfma_f32_16x16x32_bf16 a[24:27], v[100:103], v[220:223], a[24:27]\ns_cbranch_vccnz .Lrd_tre3%=\n.Lrd_tl3%=:\ns_waitcnt vmcnt(12)\nv_mfma_f32_16x16x32_bf16 a[4:7], v[4:7], v[224:227], a[4:7]\ns_add_u32 s60, s58, 20480\nbuffer_load_dwordx4 v[184:187], v196, %[rs], s60 offen offset:3072 sc1\nv_bitop3_b32 v209, v228, v230, s61 bitop3:0x7e\n" \
"v_mfma_f32_16x16x32_bf16 a[12:15], v[36:39], v[224:227], a[12:15]\ns_xor_b32 s42, s42, s45\ns_add_u32 s40, s40, 1\nv_mfma_f32_16x16x32_bf16 a[20:23], v[68:71], v[224:227], a[20:23]\nv_cmp_gt_i32 vcc, 0, v209\nv_mfma_f32_16x16x32_bf16 a[28:31], v[100:103], v[224:227], a[28:31]\ns_cbranch_vccnz .Lrd_tre4%=\n.Lrd_tl4%=:\n" \
"s_waitcnt vmcnt(12)\nv_mfma_f32_16x16x32_bf16 a[0:3], v[8:11], v[228:231], a[0:3]\ns_add_u32 s60, s59, 0\nbuffer_load_dwordx4 v[188:191], v196, %[rs], s60 offen offset:1024 sc1\nv_bitop3_b32 v209, v232, v234, s61 bitop3:0x7e\nv_mfma_f32_16x16x32_bf16 a[8:11], v[40:43], v[228:231], a[8:11]\nv_mfma_f32_16x16x32_bf16 a[16:19], v[72:75], v[228:231], a[16:19]\nbuffer_load_dwordx4 v[212:215], v204, %[rs], s42 offen sc1\n" \
"v_cmp_gt_i32 vcc, 0, v209\nv_mfma_f32_16x16x32_bf16 a[24:27], v[104:107], v[228:231], a[24:27]\ns_cbranch_vccnz .Lrd_tre5%=\n.Lrd_tl5%=:\ns_waitcnt vmcnt(13)\nv_mfma_f32_16x16x32_bf16 a[4:7], v[8:11], v[232:235], a[4:7]\ns_add_u32 s60, s59, 0\nbuffer_load_dwordx4 v[192:195], v196, %[rs], s60 offen offset:3072 sc1\n" \
"v_bitop3_b32 v209, v236, v238, s61 bitop3:0x7e\nv_mfma_f32_16x16x32_bf16 a[12:15], v[40:43], v[232:235], a[12:15]\ns_lshl_b32 s62, s55, 31\ns_or_b32 s62, s62, 1.0\ns_mov_b32 s63, s62\nv_mfma_f32_16x16x32_bf16 a[20:23], v[72:75], v[232:235], a[20:23]\nv_cmp_gt_i32 vcc, 0, v209\nv_mfma_f32_16x16x32_bf16 a[28:31], v[104:107], v[232:235], a[28:31]\n" \
"s_cbranch_vccnz .Lrd_tre6%=\n.Lrd_tl6%=:\ns_waitcnt vmcnt(13)\nv_mfma_f32_16x16x32_bf16 a[0:3], v[12:15], v[236:239], a[0:3]\ns_add_u32 s60, s41, 0\nbuffer_load_dwordx4 v[0:3], v196, %[rs], s60 offen offset:0 sc1\nv_bitop3_b32 v209, v240, v242, s61 bitop3:0x7e\nv_mfma_f32_16x16x32_bf16 a[8:11], v[44:47], v[236:239], a[8:11]\n" \
"v_mfma_f32_16x16x32_bf16 a[16:19], v[76:79], v[236:239], a[16:19]\nv_cmp_gt_i32 vcc, 0, v209\nv_mfma_f32_16x16x32_bf16 a[24:27], v[108:111], v[236:239], a[24:27]\ns_cbranch_vccnz .Lrd_tre7%=\n.Lrd_tl7%=:\ns_waitcnt vmcnt(13)\nv_mfma_f32_16x16x32_bf16 a[4:7], v[12:15], v[240:243], a[4:7]\ns_add_u32 s60, s41, 0\n" \
"buffer_load_dwordx4 v[32:35], v196, %[rs], s60 offen offset:1024 sc1\nv_bitop3_b32 v209, v244, v246, s61 bitop3:0x7e\nv_mfma_f32_16x16x32_bf16 a[12:15], v[44:47], v[240:243], a[12:15]\nv_mfma_f32_16x16x32_bf16 a[20:23], v[76:79], v[240:243], a[20:23]\nv_cmp_gt_i32 vcc, 0, v209\nv_mfma_f32_16x16x32_bf16 a[28:31], v[108:111], v[240:243], a[28:31]\ns_cbranch_vccnz .Lrd_tre8%=\n.Lrd_tl8%=:\n" \
"s_waitcnt vmcnt(13)\nv_mfma_f32_16x16x32_bf16 a[0:3], v[16:19], v[244:247], a[0:3]\ns_add_u32 s60, s41, 0\nbuffer_load_dwordx4 v[64:67], v196, %[rs], s60 offen offset:2048 sc1\nv_bitop3_b32 v209, v248, v250, s61 bitop3:0x7e\nv_mfma_f32_16x16x32_bf16 a[8:11], v[48:51], v[244:247], a[8:11]\nv_mfma_f32_16x16x32_bf16 a[16:19], v[80:83], v[244:247], a[16:19]\nv_cmp_gt_i32 vcc, 0, v209\n" \
"v_mfma_f32_16x16x32_bf16 a[24:27], v[112:115], v[244:247], a[24:27]\ns_cbranch_vccnz .Lrd_tre9%=\n.Lrd_tl9%=:\ns_waitcnt vmcnt(13)\nv_mfma_f32_16x16x32_bf16 a[4:7], v[16:19], v[248:251], a[4:7]\ns_add_u32 s60, s41, 131072\nbuffer_load_dwordx4 v[4:7], v196, %[rs], s60 offen offset:0 sc1\nv_bitop3_b32 v209, v252, v254, s61 bitop3:0x7e\n" \
"v_mfma_f32_16x16x32_bf16 a[12:15], v[48:51], v[248:251], a[12:15]\nv_mfma_f32_16x16x32_bf16 a[20:23], v[80:83], v[248:251], a[20:23]\nv_cmp_gt_i32 vcc, 0, v209\nv_mfma_f32_16x16x32_bf16 a[28:31], v[112:115], v[248:251], a[28:31]\ns_cbranch_vccnz .Lrd_tre10%=\n.Lrd_tl10%=:\ns_waitcnt vmcnt(13)\nv_mfma_f32_16x16x32_bf16 a[0:3], v[20:23], v[252:255], a[0:3]\n" \
"s_add_u32 s60, s41, 131072\nbuffer_load_dwordx4 v[36:39], v196, %[rs], s60 offen offset:1024 sc1\nv_bitop3_b32 v209, v160, v162, s61 bitop3:0x7e\nv_mfma_f32_16x16x32_bf16 a[8:11], v[52:55], v[252:255], a[8:11]\nv_mfma_f32_16x16x32_bf16 a[16:19], v[84:87], v[252:255], a[16:19]\nv_cmp_gt_i32 vcc, 0, v209\nv_mfma_f32_16x16x32_bf16 a[24:27], v[116:119], v[252:255], a[24:27]\ns_cbranch_vccnz .Lrd_tre11%=\n" \
".Lrd_tl11%=:\ns_waitcnt vmcnt(13)\nv_mfma_f32_16x16x32_bf16 a[4:7], v[20:23], v[160:163], a[4:7]\ns_add_u32 s60, s41, 131072\nbuffer_load_dwordx4 v[68:71], v196, %[rs], s60 offen offset:2048 sc1\nv_bitop3_b32 v209, v164, v166, s61 bitop3:0x7e\nv_mfma_f32_16x16x32_bf16 a[12:15], v[52:55], v[160:163], a[12:15]\nv_mfma_f32_16x16x32_bf16 a[20:23], v[84:87], v[160:163], a[20:23]\n" \
"v_cmp_gt_i32 vcc, 0, v209\nv_mfma_f32_16x16x32_bf16 a[28:31], v[116:119], v[160:163], a[28:31]\ns_cbranch_vccnz .Lrd_tre12%=\n.Lrd_tl12%=:\ns_waitcnt vmcnt(13)\nv_mfma_f32_16x16x32_bf16 a[0:3], v[24:27], v[164:167], a[0:3]\ns_add_u32 s60, s41, 262144\nbuffer_load_dwordx4 v[8:11], v196, %[rs], s60 offen offset:0 sc1\n" \
"v_bitop3_b32 v209, v168, v170, s61 bitop3:0x7e\nv_mfma_f32_16x16x32_bf16 a[8:11], v[56:59], v[164:167], a[8:11]\nv_mfma_f32_16x16x32_bf16 a[16:19], v[88:91], v[164:167], a[16:19]\nv_cmp_gt_i32 vcc, 0, v209\nv_mfma_f32_16x16x32_bf16 a[24:27], v[120:123], v[164:167], a[24:27]\ns_cbranch_vccnz .Lrd_tre13%=\n.Lrd_tl13%=:\ns_waitcnt vmcnt(13)\n" \
"v_mfma_f32_16x16x32_bf16 a[0:3], v[28:31], v[168:171], a[0:3]\ns_add_u32 s60, s41, 262144\nbuffer_load_dwordx4 v[40:43], v196, %[rs], s60 offen offset:1024 sc1\nv_bitop3_b32 v209, v172, v174, s61 bitop3:0x7e\nv_mfma_f32_16x16x32_bf16 a[8:11], v[60:63], v[168:171], a[8:11]\nv_bitop3_b32 v210, v173, v175, s61 bitop3:0x7e\nv_or_b32 v209, v209, v210\nv_mfma_f32_16x16x32_bf16 a[16:19], v[92:95], v[168:171], a[16:19]\n" \
"v_cmp_gt_i32 vcc, 0, v209\nv_mfma_f32_16x16x32_bf16 a[24:27], v[124:127], v[168:171], a[24:27]\ns_cbranch_vccnz .Lrd_tre14%=\n.Lrd_tl14%=:\ns_waitcnt vmcnt(13)\nv_mfma_f32_16x16x32_bf16 a[0:3], v[128:131], v[172:175], a[0:3]\ns_add_u32 s60, s41, 262144\nbuffer_load_dwordx4 v[72:75], v196, %[rs], s60 offen offset:2048 sc1\n" \
"v_bitop3_b32 v209, v176, v178, s61 bitop3:0x7e\nv_mfma_f32_16x16x32_bf16 a[8:11], v[136:139], v[172:175], a[8:11]\nv_bitop3_b32 v210, v177, v179, s61 bitop3:0x7e\nv_or_b32 v209, v209, v210\nv_mfma_f32_16x16x32_bf16 a[16:19], v[144:147], v[172:175], a[16:19]\nv_cmp_gt_i32 vcc, 0, v209\nv_mfma_f32_16x16x32_bf16 a[24:27], v[152:155], v[172:175], a[24:27]\ns_cbranch_vccnz .Lrd_tre15%=\n" \
".Lrd_tl15%=:\ns_waitcnt vmcnt(13)\nv_mfma_f32_16x16x32_bf16 a[0:3], v[132:135], v[176:179], a[0:3]\ns_add_u32 s60, s41, 393216\nbuffer_load_dwordx4 v[12:15], v196, %[rs], s60 offen offset:0 sc1\nv_bitop3_b32 v209, v180, v182, s61 bitop3:0x7e\nv_mfma_f32_16x16x32_bf16 a[8:11], v[140:143], v[176:179], a[8:11]\nv_mfma_f32_16x16x32_bf16 a[16:19], v[148:151], v[176:179], a[16:19]\n" \
"v_cmp_gt_i32 vcc, 0, v209\nv_mfma_f32_16x16x32_bf16 a[24:27], v[156:159], v[176:179], a[24:27]\ns_cbranch_vccnz .Lrd_tre16%=\n.Lrd_tl16%=:\ns_waitcnt vmcnt(13)\nv_mfma_f32_16x16x32_bf16 a[4:7], v[24:27], v[180:183], a[4:7]\ns_add_u32 s60, s41, 393216\nbuffer_load_dwordx4 v[44:47], v196, %[rs], s60 offen offset:1024 sc1\n" \
"v_bitop3_b32 v209, v184, v186, s61 bitop3:0x7e\nv_mfma_f32_16x16x32_bf16 a[12:15], v[56:59], v[180:183], a[12:15]\nv_mfma_f32_16x16x32_bf16 a[20:23], v[88:91], v[180:183], a[20:23]\nds_write_b128 v202, a[0:3] offset:0\nv_cmp_gt_i32 vcc, 0, v209\nv_mfma_f32_16x16x32_bf16 a[28:31], v[120:123], v[180:183], a[28:31]\ns_cbranch_vccnz .Lrd_tre17%=\n.Lrd_tl17%=:\n" \
"s_waitcnt vmcnt(13)\nv_mfma_f32_16x16x32_bf16 a[4:7], v[28:31], v[184:187], a[4:7]\ns_add_u32 s60, s41, 393216\nbuffer_load_dwordx4 v[76:79], v196, %[rs], s60 offen offset:2048 sc1\nv_bitop3_b32 v209, v188, v190, s61 bitop3:0x7e\nv_mfma_f32_16x16x32_bf16 a[12:15], v[60:63], v[184:187], a[12:15]\nv_bitop3_b32 v210, v189, v191, s61 bitop3:0x7e\nv_or_b32 v209, v209, v210\n" \
"v_mfma_f32_16x16x32_bf16 a[20:23], v[92:95], v[184:187], a[20:23]\nds_write_b128 v202, a[8:11] offset:2048\nv_cmp_gt_i32 vcc, 0, v209\nv_mfma_f32_16x16x32_bf16 a[28:31], v[124:127], v[184:187], a[28:31]\ns_cbranch_vccnz .Lrd_tre18%=\n.Lrd_tl18%=:\ns_waitcnt vmcnt(12)\nv_mfma_f32_16x16x32_bf16 a[4:7], v[128:131], v[188:191], a[4:7]\n" \
"s_add_u32 s60, s41, 524288\nbuffer_load_dwordx4 v[16:19], v196, %[rs], s60 offen offset:0 sc1\nv_bitop3_b32 v209, v192, v194, s61 bitop3:0x7e\nv_mfma_f32_16x16x32_bf16 a[12:15], v[136:139], v[188:191], a[12:15]\nv_bitop3_b32 v210, v193, v195, s61 bitop3:0x7e\nv_or_b32 v209, v209, v210\nv_mfma_f32_16x16x32_bf16 a[20:23], v[144:147], v[188:191], a[20:23]\nds_write_b128 v202, a[16:19] offset:4096\n" \
"v_cmp_gt_i32 vcc, 0, v209\nv_mfma_f32_16x16x32_bf16 a[28:31], v[152:155], v[188:191], a[28:31]\ns_cbranch_vccnz .Lrd_tre19%=\n.Lrd_tl19%=:\nv_mfma_f32_16x16x32_bf16 a[4:7], v[132:135], v[192:195], a[4:7]\ns_add_u32 s60, s41, 524288\nbuffer_load_dwordx4 v[48:51], v196, %[rs], s60 offen offset:1024 sc1\nv_lshlrev_b32 v216, 16, v212\n" \
"v_and_b32 v217, 0xffff0000, v212\nv_mfma_f32_16x16x32_bf16 a[12:15], v[140:143], v[192:195], a[12:15]\nv_lshlrev_b32 v218, 16, v213\nv_and_b32 v219, 0xffff0000, v213\nv_mfma_f32_16x16x32_bf16 a[20:23], v[148:151], v[192:195], a[20:23]\nds_write_b128 v202, a[24:27] offset:6144\nv_lshlrev_b32 v220, 16, v214\nv_and_b32 v221, 0xffff0000, v214\n" \
"v_mfma_f32_16x16x32_bf16 a[28:31], v[156:159], v[192:195], a[28:31]\nv_lshlrev_b32 v222, 16, v215\nv_and_b32 v223, 0xffff0000, v215\ns_add_u32 s60, s41, 524288\nbuffer_load_dwordx4 v[80:83], v196, %[rs], s60 offen offset:2048 sc1\nds_write_b128 v202, a[4:7] offset:1024\ns_add_u32 s60, s41, 655360\nbuffer_load_dwordx4 v[20:23], v196, %[rs], s60 offen offset:0 sc1\n" \
"ds_write_b128 v202, a[12:15] offset:3072\ns_add_u32 s60, s41, 655360\nbuffer_load_dwordx4 v[52:55], v196, %[rs], s60 offen offset:1024 sc1\nds_write_b128 v202, a[20:23] offset:5120\ns_add_u32 s60, s41, 655360\nbuffer_load_dwordx4 v[84:87], v196, %[rs], s60 offen offset:2048 sc1\nds_write_b128 v202, a[28:31] offset:7168\ns_waitcnt lgkmcnt(0)\n" \
"s_barrier\nds_read_b128 v[160:163], v203 offset:0\nds_read_b128 v[164:167], v203 offset:256\nds_read_b128 v[168:171], v203 offset:8192\nds_read_b128 v[172:175], v203 offset:8448\nds_read_b128 v[176:179], v203 offset:16384\nds_read_b128 v[180:183], v203 offset:16640\nds_read_b128 v[184:187], v203 offset:24576\n" \
"ds_read_b128 v[188:191], v203 offset:24832\ns_add_u32 s60, s41, 786432\nbuffer_load_dwordx4 v[24:27], v196, %[rs], s60 offen offset:0 sc1\ns_waitcnt lgkmcnt(4)\nv_pk_add_f32 v[160:161], v[160:161], v[168:169]\nv_pk_add_f32 v[162:163], v[162:163], v[170:171]\nv_pk_add_f32 v[164:165], v[164:165], v[172:173]\nv_pk_add_f32 v[166:167], v[166:167], v[174:175]\n" \
"s_add_u32 s60, s41, 786432\nbuffer_load_dwordx4 v[56:59], v196, %[rs], s60 offen offset:1024 sc1\ns_waitcnt lgkmcnt(2)\nv_pk_add_f32 v[160:161], v[160:161], v[176:177]\nv_pk_add_f32 v[162:163], v[162:163], v[178:179]\nv_pk_add_f32 v[164:165], v[164:165], v[180:181]\nv_pk_add_f32 v[166:167], v[166:167], v[182:183]\ns_add_u32 s60, s41, 786432\n" \
"buffer_load_dwordx4 v[88:91], v196, %[rs], s60 offen offset:2048 sc1\ns_waitcnt lgkmcnt(0)\nv_pk_add_f32 v[160:161], v[160:161], v[184:185]\nv_pk_add_f32 v[162:163], v[162:163], v[186:187]\nv_pk_add_f32 v[164:165], v[164:165], v[188:189]\nv_pk_add_f32 v[166:167], v[166:167], v[190:191]\nv_pk_fma_f32 v[160:161], v[160:161], s[62:63], v[216:217]\nv_pk_fma_f32 v[162:163], v[162:163], s[62:63], v[218:219]\n" \
"v_pk_fma_f32 v[164:165], v[164:165], s[62:63], v[220:221]\nv_pk_fma_f32 v[166:167], v[166:167], s[62:63], v[222:223]\nv_cvt_pk_bf16_f32 v208, v160, v161\nv_cvt_pk_bf16_f32 v209, v162, v163\nv_cvt_pk_bf16_f32 v210, v164, v165\nv_cvt_pk_bf16_f32 v211, v166, v167\nbuffer_store_dwordx4 v[208:211], v204, %[rs], s42 offen sc1\nv_pk_add_f32 v[168:169], v[160:161], v[162:163]\n" \
"v_pk_add_f32 v[170:171], v[164:165], v[166:167]\nv_pk_add_f32 v[168:169], v[168:169], v[170:171]\ns_add_u32 s60, s41, 917504\nbuffer_load_dwordx4 v[28:31], v196, %[rs], s60 offen offset:0 sc1\nv_add_f32 v168, v168, v169\ns_nop 1\nv_add_f32_dpp v169, v168, v168 row_ror:8 row_mask:0xf bank_mask:0xf\nv_add_f32_dpp v170, v168, v168 row_ror:8 row_mask:0xf bank_mask:0xf\n" \
"s_nop 1\nv_permlane16_swap_b32 v169, v170\nv_add_f32 v168, v169, v170\nv_add_f32 v169, v169, v170\ns_nop 1\nv_permlane32_swap_b32 v168, v169\nv_add_f32 v168, v168, v169\nv_mul_f32 v168, 0x3c800000, v168\n" \
"v_pk_add_f32 v[160:161], v[160:161], v[168:169] op_sel_hi:[1,0] neg_lo:[0,1] neg_hi:[0,1]\nv_pk_add_f32 v[162:163], v[162:163], v[168:169] op_sel_hi:[1,0] neg_lo:[0,1] neg_hi:[0,1]\nv_pk_add_f32 v[164:165], v[164:165], v[168:169] op_sel_hi:[1,0] neg_lo:[0,1] neg_hi:[0,1]\nv_pk_add_f32 v[166:167], v[166:167], v[168:169] op_sel_hi:[1,0] neg_lo:[0,1] neg_hi:[0,1]\ns_add_u32 s60, s41, 917504\nbuffer_load_dwordx4 v[60:63], v196, %[rs], s60 offen offset:1024 sc1\nv_pk_mul_f32 v[172:173], v[160:161], v[160:161]\nv_pk_mul_f32 v[174:175], v[162:163], v[162:163]\n" \
"v_pk_fma_f32 v[172:173], v[164:165], v[164:165], v[172:173]\nv_pk_fma_f32 v[174:175], v[166:167], v[166:167], v[174:175]\nv_pk_add_f32 v[172:173], v[172:173], v[174:175]\nv_add_f32 v172, v172, v173\ns_nop 1\nv_add_f32_dpp v173, v172, v172 row_ror:8 row_mask:0xf bank_mask:0xf\nv_add_f32_dpp v174, v172, v172 row_ror:8 row_mask:0xf bank_mask:0xf\ns_nop 1\n" \
"v_permlane16_swap_b32 v173, v174\nv_add_f32 v172, v173, v174\nv_add_f32 v173, v173, v174\ns_nop 1\nv_permlane32_swap_b32 v172, v173\nv_add_f32 v172, v172, v173\nv_mov_b32 v169, v172\nbuffer_store_dwordx2 v[168:169], v205, %[rs], s42 offen sc1\n" \
"s_add_u32 s60, s41, 917504\nbuffer_load_dwordx4 v[92:95], v196, %[rs], s60 offen offset:2048 sc1\ns_add_u32 s60, s41, 0\nbuffer_load_dwordx4 v[96:99], v196, %[rs], s60 offen offset:3072 sc1\ns_add_u32 s60, s41, 131072\nbuffer_load_dwordx4 v[100:103], v196, %[rs], s60 offen offset:3072 sc1\ns_add_u32 s60, s41, 262144\nbuffer_load_dwordx4 v[104:107], v196, %[rs], s60 offen offset:3072 sc1\n" \
"s_add_u32 s60, s41, 393216\nbuffer_load_dwordx4 v[108:111], v196, %[rs], s60 offen offset:3072 sc1\ns_add_u32 s60, s41, 524288\nbuffer_load_dwordx4 v[112:115], v196, %[rs], s60 offen offset:3072 sc1\ns_add_u32 s60, s41, 655360\nbuffer_load_dwordx4 v[116:119], v196, %[rs], s60 offen offset:3072 sc1\ns_add_u32 s60, s41, 786432\nbuffer_load_dwordx4 v[120:123], v196, %[rs], s60 offen offset:3072 sc1\n" \
"s_add_u32 s60, s41, 917504\nbuffer_load_dwordx4 v[124:127], v196, %[rs], s60 offen offset:3072 sc1\ns_waitcnt vmcnt(9)\nbuffer_store_dword v207, off, %[rs], %[fa] sc1\nbuffer_load_dwordx4 v[128:131], v196, %[rs], %[su] offen offset:0 sc1\nbuffer_load_dwordx4 v[132:135], v196, %[rs], %[su] offen offset:1024 sc1\nbuffer_load_dwordx4 v[136:139], v196, %[rs], %[su] offen offset:2048 sc1\nbuffer_load_dwordx4 v[140:143], v196, %[rs], %[su] offen offset:3072 sc1\n" \
"buffer_load_dwordx4 v[144:147], v196, %[rs], s57 offen offset:0 sc1\nbuffer_load_dwordx4 v[148:151], v196, %[rs], s57 offen offset:1024 sc1\nbuffer_load_dwordx4 v[160:163], v196, %[rs], %[sp] offen offset:0 sc1\nbuffer_load_dwordx4 v[164:167], v196, %[rs], %[sp] offen offset:1024 sc1\nbuffer_load_dwordx4 v[168:171], v196, %[rs], %[sp] offen offset:2048 sc1\nbuffer_load_dwordx4 v[172:175], v196, %[rs], %[sp] offen offset:3072 sc1\nbuffer_load_dwordx4 v[176:179], v196, %[rs], s56 offen offset:0 sc1\nbuffer_load_dwordx4 v[152:155], v196, %[rs], s57 offen offset:2048 sc1\n" \
"buffer_load_dwordx4 v[156:159], v196, %[rs], s57 offen offset:3072 sc1\ns_cmp_ge_u32 s40, 64\ns_cbranch_scc1 .Lrd_end%=\ns_cmp_lt_u32 s40, 2\ns_cbranch_scc1 .Lrd_yflag%=\n.Lrd_ychk%=:\ns_and_b32 s44, s40, 1\ns_bfe_u32 s55, s40, 0x10001\n" \
"s_mul_i32 s54, s44, 0x80008001\ns_mul_i32 s61, s55, 0x80008000\ns_lshl_b32 s58, s44, 17\ns_add_u32 s58, s58, %[st]\ns_lshl_b32 s59, s44, 12\ns_add_u32 s59, s59, %[sn]\ns_add_u32 s44, s40, 1\ns_and_b32 s44, s44, 3\n" \
"s_lshl_b32 s44, s44, 15\nv_xor_b32 v206, s44, v200\nv_mov_b64 v[180:181], 0\nv_mov_b64 v[182:183], 0\nv_mov_b64 v[184:185], 0\nv_mov_b64 v[186:187], 0\nv_mov_b64 v[188:189], 0\nv_mov_b64 v[190:191], 0\n" \
"v_mov_b64 v[192:193], 0\nv_mov_b64 v[194:195], 0\nv_bitop3_b32 v209, v0, v2, s55 bitop3:0x7e\nv_bitop3_b32 v210, v32, v34, s55 bitop3:0x7e\nv_or3_b32 v208, v209, v210, 0\nv_bitop3_b32 v209, v64, v66, s55 bitop3:0x7e\nv_bitop3_b32 v210, v4, v6, s55 bitop3:0x7e\nv_or3_b32 v208, v209, v210, v208\n" \
"v_bitop3_b32 v209, v36, v38, s55 bitop3:0x7e\nv_bitop3_b32 v210, v68, v70, s55 bitop3:0x7e\nv_or3_b32 v208, v209, v210, v208\nv_bitop3_b32 v209, v8, v10, s55 bitop3:0x7e\nv_bitop3_b32 v210, v40, v42, s55 bitop3:0x7e\nv_or3_b32 v208, v209, v210, v208\nv_bitop3_b32 v209, v72, v74, s55 bitop3:0x7e\nv_bitop3_b32 v210, v12, v14, s55 bitop3:0x7e\n" \
"v_or3_b32 v208, v209, v210, v208\nv_bitop3_b32 v209, v44, v46, s55 bitop3:0x7e\nv_bitop3_b32 v210, v76, v78, s55 bitop3:0x7e\nv_or3_b32 v208, v209, v210, v208\nv_bitop3_b32 v209, v16, v18, s55 bitop3:0x7e\nv_bitop3_b32 v210, v48, v50, s55 bitop3:0x7e\nv_or3_b32 v208, v209, v210, v208\nv_bitop3_b32 v209, v80, v82, s55 bitop3:0x7e\n" \
"v_bitop3_b32 v210, v20, v22, s55 bitop3:0x7e\nv_or3_b32 v208, v209, v210, v208\nv_bitop3_b32 v209, v52, v54, s55 bitop3:0x7e\nv_bitop3_b32 v210, v84, v86, s55 bitop3:0x7e\nv_or3_b32 v208, v209, v210, v208\nv_bitop3_b32 v209, v24, v26, s55 bitop3:0x7e\nv_bitop3_b32 v210, v56, v58, s55 bitop3:0x7e\nv_or3_b32 v208, v209, v210, v208\n" \
"s_waitcnt vmcnt(22)\nv_bitop3_b32 v209, v88, v90, s55 bitop3:0x7e\nv_bitop3_b32 v210, v28, v30, s55 bitop3:0x7e\nv_or3_b32 v208, v209, v210, v208\nv_bitop3_b32 v209, v60, v62, s55 bitop3:0x7e\nv_bitop3_b32 v210, v92, v94, s55 bitop3:0x7e\nv_or3_b32 v208, v209, v210, v208\ns_waitcnt vmcnt(18)\n" \
"v_bitop3_b32 v209, v96, v98, s55 bitop3:0x7e\nv_bitop3_b32 v210, v100, v102, s55 bitop3:0x7e\nv_or3_b32 v208, v209, v210, v208\nv_bitop3_b32 v209, v104, v106, s55 bitop3:0x7e\nv_bitop3_b32 v210, v108, v110, s55 bitop3:0x7e\nv_or3_b32 v208, v209, v210, v208\ns_waitcnt vmcnt(14)\nv_bitop3_b32 v209, v112, v114, s55 bitop3:0x7e\n" \
"v_bitop3_b32 v210, v116, v118, s55 bitop3:0x7e\nv_or3_b32 v208, v209, v210, v208\nv_bitop3_b32 v209, v120, v122, s55 bitop3:0x7e\nv_bitop3_b32 v210, v124, v126, s55 bitop3:0x7e\nv_or3_b32 v208, v209, v210, v208\nv_and_b32 v208, 1, v208\nv_cmp_ne_u32 vcc, 0, v208\ns_cbranch_vccnz .Lrd_yre%=\n" \
"s_waitcnt vmcnt(7)\nv_bitop3_b32 v208, v128, v130, s54 bitop3:0x7e\nv_bitop3_b32 v209, v132, v134, s54 bitop3:0x7e\nv_bitop3_b32 v208, v208, 1, v209 bitop3:0xc8\nv_cmp_ne_u32 vcc, 0, v208\ns_cbranch_vccnz .Lrd_u2%=\nv_bitop3_b32 v208, v136, v138, s54 bitop3:0x7e\nv_bitop3_b32 v209, v140, v142, s54 bitop3:0x7e\n" \
"v_bitop3_b32 v210, v144, v146, s54 bitop3:0x7e\nv_bitop3_b32 v211, v148, v150, s54 bitop3:0x7e\nv_or3_b32 v208, v208, v209, v210\nv_bitop3_b32 v208, v208, 1, v211 bitop3:0xc8\nv_cmp_ne_u32 vcc, 0, v208\ns_cbranch_vccz .Lrd_ra%=\ns_mov_b64 exec, vcc\ns_branch .Lrd_u3%=\n" \
".Lrd_yre%=:\ns_mov_b64 exec, vcc\ns_add_u32 s43, s43, 1\ns_cmp_lt_u32 s43, 262144\ns_cbranch_scc1 .Lrd_yld%=\ns_branch .Lrd_abort%=\n.Lrd_rb%=:\ns_mov_b64 exec, s[46:47]\n" \
"buffer_load_dwordx4 v[160:163], v196, %[rs], %[sp] offen offset:0 sc1\ns_mov_b64 exec, s[48:49]\nbuffer_load_dwordx4 v[164:167], v196, %[rs], %[sp] offen offset:1024 sc1\ns_mov_b64 exec, s[50:51]\nbuffer_load_dwordx4 v[168:171], v196, %[rs], %[sp] offen offset:2048 sc1\ns_mov_b64 exec, s[52:53]\nbuffer_load_dwordx4 v[172:175], v196, %[rs], %[sp] offen offset:3072 sc1\ns_mov_b64 exec, s[64:65]\n" \
"buffer_load_dwordx4 v[176:179], v196, %[rs], s56 offen offset:0 sc1\ns_mov_b64 exec, s[62:63]\nbuffer_load_dwordx4 v[152:155], v196, %[rs], s57 offen offset:2048 sc1\nbuffer_load_dwordx4 v[156:159], v196, %[rs], s57 offen offset:3072 sc1\ns_mov_b64 exec, -1\nv_mov_b64 v[180:181], 0\nv_mov_b64 v[182:183], 0\nv_mov_b64 v[184:185], 0\n" \
"v_mov_b64 v[186:187], 0\nv_mov_b64 v[188:189], 0\nv_mov_b64 v[190:191], 0\nv_mov_b64 v[192:193], 0\nv_mov_b64 v[194:195], 0\ns_add_u32 s43, s43, 1\ns_cmp_lt_u32 s43, 262144\ns_cbranch_scc1 .Lrd_ra%=\n" \
"s_branch .Lrd_abort%=\n.Lrd_tre0%=:\ns_add_u32 s60, s58, 0\nbuffer_load_dwordx4 v[212:215], v196, %[rs], s60 offen offset:0 sc1\ns_waitcnt vmcnt(0)\nv_bitop3_b32 v209, v212, v214, s61 bitop3:0x7e\nv_cmp_gt_i32 vcc, 0, v209\ns_cbranch_vccz .Lrd_tl0%=\n" \
"s_add_u32 s43, s43, 1\ns_cmp_lt_u32 s43, 262144\ns_cbranch_scc1 .Lrd_tre0%=\ns_branch .Lrd_abort%=\n.Lrd_tre1%=:\ns_add_u32 s60, s58, 0\nbuffer_load_dwordx4 v[216:219], v196, %[rs], s60 offen offset:1024 sc1\ns_waitcnt vmcnt(0)\n" \
"v_bitop3_b32 v209, v216, v218, s61 bitop3:0x7e\nv_cmp_gt_i32 vcc, 0, v209\ns_cbranch_vccz .Lrd_tl1%=\ns_add_u32 s43, s43, 1\ns_cmp_lt_u32 s43, 262144\ns_cbranch_scc1 .Lrd_tre1%=\ns_branch .Lrd_abort%=\n.Lrd_tre2%=:\n" \
"s_add_u32 s60, s58, 0\nbuffer_load_dwordx4 v[220:223], v196, %[rs], s60 offen offset:2048 sc1\ns_waitcnt vmcnt(0)\nv_bitop3_b32 v209, v220, v222, s61 bitop3:0x7e\nv_cmp_gt_i32 vcc, 0, v209\ns_cbranch_vccz .Lrd_tl2%=\ns_add_u32 s43, s43, 1\ns_cmp_lt_u32 s43, 262144\n" \
"s_cbranch_scc1 .Lrd_tre2%=\ns_branch .Lrd_abort%=\n.Lrd_tre3%=:\ns_add_u32 s60, s58, 0\nbuffer_load_dwordx4 v[224:227], v196, %[rs], s60 offen offset:3072 sc1\ns_waitcnt vmcnt(0)\nv_bitop3_b32 v209, v224, v226, s61 bitop3:0x7e\nv_cmp_gt_i32 vcc, 0, v209\n" \
"s_cbranch_vccz .Lrd_tl3%=\ns_add_u32 s43, s43, 1\ns_cmp_lt_u32 s43, 262144\ns_cbranch_scc1 .Lrd_tre3%=\ns_branch .Lrd_abort%=\n.Lrd_tre4%=:\ns_add_u32 s60, s58, 4096\nbuffer_load_dwordx4 v[228:231], v196, %[rs], s60 offen offset:0 sc1\n" \
"s_waitcnt vmcnt(0)\nv_bitop3_b32 v209, v228, v230, s61 bitop3:0x7e\nv_cmp_gt_i32 vcc, 0, v209\ns_cbranch_vccz .Lrd_tl4%=\ns_add_u32 s43, s43, 1\ns_cmp_lt_u32 s43, 262144\ns_cbranch_scc1 .Lrd_tre4%=\ns_branch .Lrd_abort%=\n" \
".Lrd_tre5%=:\ns_add_u32 s60, s58, 4096\nbuffer_load_dwordx4 v[232:235], v196, %[rs], s60 offen offset:1024 sc1\ns_waitcnt vmcnt(0)\nv_bitop3_b32 v209, v232, v234, s61 bitop3:0x7e\nv_cmp_gt_i32 vcc, 0, v209\ns_cbranch_vccz .Lrd_tl5%=\ns_add_u32 s43, s43, 1\n" \
"s_cmp_lt_u32 s43, 262144\ns_cbranch_scc1 .Lrd_tre5%=\ns_branch .Lrd_abort%=\n.Lrd_tre6%=:\ns_add_u32 s60, s58, 4096\nbuffer_load_dwordx4 v[236:239], v196, %[rs], s60 offen offset:2048 sc1\ns_waitcnt vmcnt(0)\nv_bitop3_b32 v209, v236, v238, s61 bitop3:0x7e\n" \
"v_cmp_gt_i32 vcc, 0, v209\ns_cbranch_vccz .Lrd_tl6%=\ns_add_u32 s43, s43, 1\ns_cmp_lt_u32 s43, 262144\ns_cbranch_scc1 .Lrd_tre6%=\ns_branch .Lrd_abort%=\n.Lrd_tre7%=:\ns_add_u32 s60, s58, 4096\n" \
"buffer_load_dwordx4 v[240:243], v196, %[rs], s60 offen offset:3072 sc1\ns_waitcnt vmcnt(0)\nv_bitop3_b32 v209, v240, v242, s61 bitop3:0x7e\nv_cmp_gt_i32 vcc, 0, v209\ns_cbranch_vccz .Lrd_tl7%=\ns_add_u32 s43, s43, 1\ns_cmp_lt_u32 s43, 262144\ns_cbranch_scc1 .Lrd_tre7%=\n" \
"s_branch .Lrd_abort%=\n.Lrd_tre8%=:\ns_add_u32 s60, s58, 16384\nbuffer_load_dwordx4 v[244:247], v196, %[rs], s60 offen offset:0 sc1\ns_waitcnt vmcnt(0)\nv_bitop3_b32 v209, v244, v246, s61 bitop3:0x7e\nv_cmp_gt_i32 vcc, 0, v209\ns_cbranch_vccz .Lrd_tl8%=\n" \
"s_add_u32 s43, s43, 1\ns_cmp_lt_u32 s43, 262144\ns_cbranch_scc1 .Lrd_tre8%=\ns_branch .Lrd_abort%=\n.Lrd_tre9%=:\ns_add_u32 s60, s58, 16384\nbuffer_load_dwordx4 v[248:251], v196, %[rs], s60 offen offset:1024 sc1\ns_waitcnt vmcnt(0)\n" \
"v_bitop3_b32 v209, v248, v250, s61 bitop3:0x7e\nv_cmp_gt_i32 vcc, 0, v209\ns_cbranch_vccz .Lrd_tl9%=\ns_add_u32 s43, s43, 1\ns_cmp_lt_u32 s43, 262144\ns_cbranch_scc1 .Lrd_tre9%=\ns_branch .Lrd_abort%=\n.Lrd_tre10%=:\n" \
"s_add_u32 s60, s58, 16384\nbuffer_load_dwordx4 v[252:255], v196, %[rs], s60 offen offset:2048 sc1\ns_waitcnt vmcnt(0)\nv_bitop3_b32 v209, v252, v254, s61 bitop3:0x7e\nv_cmp_gt_i32 vcc, 0, v209\ns_cbranch_vccz .Lrd_tl10%=\ns_add_u32 s43, s43, 1\ns_cmp_lt_u32 s43, 262144\n" \
"s_cbranch_scc1 .Lrd_tre10%=\ns_branch .Lrd_abort%=\n.Lrd_tre11%=:\ns_add_u32 s60, s58, 16384\nbuffer_load_dwordx4 v[160:163], v196, %[rs], s60 offen offset:3072 sc1\ns_waitcnt vmcnt(0)\nv_bitop3_b32 v209, v160, v162, s61 bitop3:0x7e\nv_cmp_gt_i32 vcc, 0, v209\n" \
"s_cbranch_vccz .Lrd_tl11%=\ns_add_u32 s43, s43, 1\ns_cmp_lt_u32 s43, 262144\ns_cbranch_scc1 .Lrd_tre11%=\ns_branch .Lrd_abort%=\n.Lrd_tre12%=:\ns_add_u32 s60, s58, 20480\nbuffer_load_dwordx4 v[164:167], v196, %[rs], s60 offen offset:0 sc1\n" \
"s_waitcnt vmcnt(0)\nv_bitop3_b32 v209, v164, v166, s61 bitop3:0x7e\nv_cmp_gt_i32 vcc, 0, v209\ns_cbranch_vccz .Lrd_tl12%=\ns_add_u32 s43, s43, 1\ns_cmp_lt_u32 s43, 262144\ns_cbranch_scc1 .Lrd_tre12%=\ns_branch .Lrd_abort%=\n" \
".Lrd_tre13%=:\ns_add_u32 s60, s58, 20480\nbuffer_load_dwordx4 v[168:171], v196, %[rs], s60 offen offset:2048 sc1\ns_waitcnt vmcnt(0)\nv_bitop3_b32 v209, v168, v170, s61 bitop3:0x7e\nv_cmp_gt_i32 vcc, 0, v209\ns_cbranch_vccz .Lrd_tl13%=\ns_add_u32 s43, s43, 1\n" \
"s_cmp_lt_u32 s43, 262144\ns_cbranch_scc1 .Lrd_tre13%=\ns_branch .Lrd_abort%=\n.Lrd_tre14%=:\ns_add_u32 s60, s59, 0\nbuffer_load_dwordx4 v[172:175], v196, %[rs], s60 offen offset:0 sc1\ns_waitcnt vmcnt(0)\nv_bitop3_b32 v209, v172, v174, s61 bitop3:0x7e\n" \
"v_bitop3_b32 v210, v173, v175, s61 bitop3:0x7e\nv_or_b32 v209, v209, v210\nv_cmp_gt_i32 vcc, 0, v209\ns_cbranch_vccz .Lrd_tl14%=\ns_add_u32 s43, s43, 1\ns_cmp_lt_u32 s43, 262144\ns_cbranch_scc1 .Lrd_tre14%=\ns_branch .Lrd_abort%=\n" \
".Lrd_tre15%=:\ns_add_u32 s60, s59, 0\nbuffer_load_dwordx4 v[176:179], v196, %[rs], s60 offen offset:2048 sc1\ns_waitcnt vmcnt(0)\nv_bitop3_b32 v209, v176, v178, s61 bitop3:0x7e\nv_bitop3_b32 v210, v177, v179, s61 bitop3:0x7e\nv_or_b32 v209, v209, v210\nv_cmp_gt_i32 vcc, 0, v209\n" \
"s_cbranch_vccz .Lrd_tl15%=\ns_add_u32 s43, s43, 1\ns_cmp_lt_u32 s43, 262144\ns_cbranch_scc1 .Lrd_tre15%=\ns_branch .Lrd_abort%=\n.Lrd_tre16%=:\ns_add_u32 s60, s58, 20480\nbuffer_load_dwordx4 v[180:183], v196, %[rs], s60 offen offset:1024 sc1\n" \
"s_waitcnt vmcnt(0)\nv_bitop3_b32 v209, v180, v182, s61 bitop3:0x7e\nv_cmp_gt_i32 vcc, 0, v209\ns_cbranch_vccz .Lrd_tl16%=\ns_add_u32 s43, s43, 1\ns_cmp_lt_u32 s43, 262144\ns_cbranch_scc1 .Lrd_tre16%=\ns_branch .Lrd_abort%=\n" \
".Lrd_tre17%=:\ns_add_u32 s60, s58, 20480\nbuffer_load_dwordx4 v[184:187], v196, %[rs], s60 offen offset:3072 sc1\ns_waitcnt vmcnt(0)\nv_bitop3_b32 v209, v184, v186, s61 bitop3:0x7e\nv_cmp_gt_i32 vcc, 0, v209\ns_cbranch_vccz .Lrd_tl17%=\ns_add_u32 s43, s43, 1\n" \
"s_cmp_lt_u32 s43, 262144\ns_cbranch_scc1 .Lrd_tre17%=\ns_branch .Lrd_abort%=\n.Lrd_tre18%=:\ns_add_u32 s60, s59, 0\nbuffer_load_dwordx4 v[188:191], v196, %[rs], s60 offen offset:1024 sc1\ns_waitcnt vmcnt(0)\nv_bitop3_b32 v209, v188, v190, s61 bitop3:0x7e\n" \
"v_bitop3_b32 v210, v189, v191, s61 bitop3:0x7e\nv_or_b32 v209, v209, v210\nv_cmp_gt_i32 vcc, 0, v209\ns_cbranch_vccz .Lrd_tl18%=\ns_add_u32 s43, s43, 1\ns_cmp_lt_u32 s43, 262144\ns_cbranch_scc1 .Lrd_tre18%=\ns_branch .Lrd_abort%=\n" \
".Lrd_tre19%=:\ns_add_u32 s60, s59, 0\nbuffer_load_dwordx4 v[192:195], v196, %[rs], s60 offen offset:3072 sc1\ns_waitcnt vmcnt(0)\nv_bitop3_b32 v209, v192, v194, s61 bitop3:0x7e\nv_bitop3_b32 v210, v193, v195, s61 bitop3:0x7e\nv_or_b32 v209, v209, v210\nv_cmp_gt_i32 vcc, 0, v209\n" \
"s_cbranch_vccz .Lrd_tl19%=\ns_add_u32 s43, s43, 1\ns_cmp_lt_u32 s43, 262144\ns_cbranch_scc1 .Lrd_tre19%=\ns_branch .Lrd_abort%=\n.Lrd_abort%=:\ns_mov_b64 exec, -1\nv_mov_b32 v16, 0\n" \
"s_movk_i32 s44, 6400\nbuffer_store_dword v16, off, %[rs], s44 sc1\n.Lrd_end%=:\ns_waitcnt vmcnt(0) lgkmcnt(0)\n" \
""
#define GG_ASM \
"v_mbcnt_lo_u32_b32 v255, -1, 0\nv_mbcnt_hi_u32_b32 v255, -1, v255\nv_lshlrev_b32 v255, 2, v255\nv_add_u32 v255, %[tb], v255\nds_read_b32 v0, v255 offset:0\nds_read_b32 v1, v255 offset:256\nds_read_b32 v2, v255 offset:512\nds_read_b32 v3, v255 offset:768\n" \
"ds_read_b32 v4, v255 offset:1024\nds_read_b32 v5, v255 offset:1280\nds_read_b32 v92, v255 offset:1536\nds_read_b32 v93, v255 offset:1792\nds_read_b32 v94, v255 offset:2048\ns_waitcnt lgkmcnt(0)\ns_add_u32 m0, %[lw], 16384\ns_add_u32 s44, %[wo], 0\n" \
"buffer_load_dwordx4 v92, %[rw], s44 offen lds\ns_add_u32 m0, %[lw], 17408\ns_add_u32 s44, %[wo], 114688\nbuffer_load_dwordx4 v92, %[rw], s44 offen lds\ns_add_u32 m0, %[lw], 18432\ns_add_u32 s44, %[wo], 229376\nbuffer_load_dwordx4 v92, %[rw], s44 offen lds\ns_add_u32 m0, %[lw], 19456\n" \
"s_add_u32 s44, %[wo], 344064\nbuffer_load_dwordx4 v92, %[rw], s44 offen lds\ns_add_u32 m0, %[lw], 20480\ns_add_u32 s44, %[wo], 0\nbuffer_load_dwordx4 v92, %[ro], s44 offen lds\ns_add_u32 m0, %[lw], 21504\ns_add_u32 s44, %[wo], 114688\nbuffer_load_dwordx4 v92, %[ro], s44 offen lds\n" \
"s_add_u32 m0, %[lw], 22528\ns_add_u32 s44, %[wo], 229376\nbuffer_load_dwordx4 v92, %[ro], s44 offen lds\ns_add_u32 m0, %[lw], 23552\ns_add_u32 s44, %[wo], 344064\nbuffer_load_dwordx4 v92, %[ro], s44 offen lds\ns_add_u32 m0, %[tb], 0\ns_add_u32 s44, %[wo], 458752\n" \
"buffer_load_dwordx4 v92, %[rw], s44 offen lds\ns_add_u32 m0, %[tb], 1024\ns_add_u32 s44, %[wo], 573440\nbuffer_load_dwordx4 v92, %[rw], s44 offen lds\ns_add_u32 m0, %[tb], 2048\ns_add_u32 s44, %[wo], 688128\nbuffer_load_dwordx4 v92, %[rw], s44 offen lds\ns_add_u32 m0, %[tb], 3072\n" \
"s_add_u32 s44, %[wo], 802816\nbuffer_load_dwordx4 v92, %[rw], s44 offen lds\ns_add_u32 m0, %[tb], 4096\ns_add_u32 s44, %[wo], 458752\nbuffer_load_dwordx4 v92, %[ro], s44 offen lds\ns_add_u32 m0, %[tb], 5120\ns_add_u32 s44, %[wo], 573440\nbuffer_load_dwordx4 v92, %[ro], s44 offen lds\n" \
"s_add_u32 m0, %[tb], 6144\ns_add_u32 s44, %[wo], 688128\nbuffer_load_dwordx4 v92, %[ro], s44 offen lds\ns_add_u32 m0, %[tb], 7168\ns_add_u32 s44, %[wo], 802816\nbuffer_load_dwordx4 v92, %[ro], s44 offen lds\ns_add_u32 m0, %[tb], 8192\ns_add_u32 s44, %[wo], 917504\n" \
"buffer_load_dwordx4 v92, %[rw], s44 offen lds\ns_add_u32 m0, %[tb], 9216\ns_add_u32 s44, %[wo], 1032192\nbuffer_load_dwordx4 v92, %[rw], s44 offen lds\ns_add_u32 m0, %[tb], 10240\ns_add_u32 s44, %[wo], 1146880\nbuffer_load_dwordx4 v92, %[rw], s44 offen lds\ns_add_u32 m0, %[tb], 11264\n" \
"s_add_u32 s44, %[wo], 1261568\nbuffer_load_dwordx4 v92, %[rw], s44 offen lds\ns_add_u32 m0, %[lw], 0\ns_add_u32 s44, %[wo], 917504\nbuffer_load_dwordx4 v92, %[ro], s44 offen lds\ns_add_u32 m0, %[lw], 1024\ns_add_u32 s44, %[wo], 1032192\nbuffer_load_dwordx4 v92, %[ro], s44 offen lds\n" \
"s_add_u32 m0, %[lw], 2048\ns_add_u32 s44, %[wo], 1146880\nbuffer_load_dwordx4 v92, %[ro], s44 offen lds\ns_add_u32 m0, %[lw], 3072\ns_add_u32 s44, %[wo], 1261568\nbuffer_load_dwordx4 v92, %[ro], s44 offen lds\ns_add_u32 m0, %[lw], 4096\ns_add_u32 s44, %[wo], 1376256\n" \
"buffer_load_dwordx4 v92, %[rw], s44 offen lds\ns_add_u32 m0, %[lw], 5120\ns_add_u32 s44, %[wo], 1490944\nbuffer_load_dwordx4 v92, %[rw], s44 offen lds\ns_add_u32 m0, %[lw], 6144\ns_add_u32 s44, %[wo], 1605632\nbuffer_load_dwordx4 v92, %[rw], s44 offen lds\ns_add_u32 m0, %[lw], 7168\n" \
"s_add_u32 s44, %[wo], 1720320\nbuffer_load_dwordx4 v92, %[rw], s44 offen lds\ns_add_u32 m0, %[lw], 8192\ns_add_u32 s44, %[wo], 1376256\nbuffer_load_dwordx4 v92, %[ro], s44 offen lds\ns_add_u32 m0, %[lw], 9216\ns_add_u32 s44, %[wo], 1490944\nbuffer_load_dwordx4 v92, %[ro], s44 offen lds\n" \
"s_add_u32 m0, %[lw], 10240\ns_add_u32 s44, %[wo], 1605632\nbuffer_load_dwordx4 v92, %[ro], s44 offen lds\ns_add_u32 m0, %[lw], 11264\ns_add_u32 s44, %[wo], 1720320\nbuffer_load_dwordx4 v92, %[ro], s44 offen lds\ns_add_u32 m0, %[lw], 12288\ns_add_u32 s44, %[wo], 1835008\n" \
"buffer_load_dwordx4 v92, %[rw], s44 offen lds\ns_add_u32 m0, %[lw], 13312\ns_add_u32 s44, %[wo], 1949696\nbuffer_load_dwordx4 v92, %[rw], s44 offen lds\ns_add_u32 m0, %[lw], 14336\ns_add_u32 s44, %[wo], 2064384\nbuffer_load_dwordx4 v92, %[rw], s44 offen lds\ns_add_u32 m0, %[lw], 15360\n" \
"s_add_u32 s44, %[wo], 2179072\nbuffer_load_dwordx4 v92, %[rw], s44 offen lds\ns_waitcnt vmcnt(32)\nds_read_b64_tr_b16 a[0:1], v94 offset:16384\nds_read_b64_tr_b16 a[2:3], v94 offset:16640\nds_read_b64_tr_b16 a[8:9], v94 offset:16392\nds_read_b64_tr_b16 a[10:11], v94 offset:16648\nds_read_b64_tr_b16 a[4:5], v94 offset:18432\n" \
"ds_read_b64_tr_b16 a[6:7], v94 offset:18688\nds_read_b64_tr_b16 a[12:13], v94 offset:18440\nds_read_b64_tr_b16 a[14:15], v94 offset:18696\ns_waitcnt lgkmcnt(0)\ns_add_u32 m0, %[lw], 16384\ns_add_u32 s44, %[wo], 1835008\nbuffer_load_dwordx4 v92, %[ro], s44 offen lds\ns_add_u32 m0, %[lw], 17408\n" \
"s_add_u32 s44, %[wo], 1949696\nbuffer_load_dwordx4 v92, %[ro], s44 offen lds\ns_add_u32 m0, %[lw], 18432\ns_add_u32 s44, %[wo], 2064384\nbuffer_load_dwordx4 v92, %[ro], s44 offen lds\ns_add_u32 m0, %[lw], 19456\ns_add_u32 s44, %[wo], 2179072\nbuffer_load_dwordx4 v92, %[ro], s44 offen lds\n" \
"s_waitcnt vmcnt(32)\nds_read_b64_tr_b16 a[16:17], v94 offset:20480\nds_read_b64_tr_b16 a[18:19], v94 offset:20736\nds_read_b64_tr_b16 a[24:25], v94 offset:20488\nds_read_b64_tr_b16 a[26:27], v94 offset:20744\nds_read_b64_tr_b16 a[20:21], v94 offset:22528\nds_read_b64_tr_b16 a[22:23], v94 offset:22784\nds_read_b64_tr_b16 a[28:29], v94 offset:22536\n" \
"ds_read_b64_tr_b16 a[30:31], v94 offset:22792\ns_waitcnt lgkmcnt(0)\ns_add_u32 m0, %[lw], 20480\ns_add_u32 s44, %[wo], 2293760\nbuffer_load_dwordx4 v92, %[rw], s44 offen lds\ns_add_u32 m0, %[lw], 21504\ns_add_u32 s44, %[wo], 2408448\nbuffer_load_dwordx4 v92, %[rw], s44 offen lds\n" \
"s_add_u32 m0, %[lw], 22528\ns_add_u32 s44, %[wo], 2523136\nbuffer_load_dwordx4 v92, %[rw], s44 offen lds\ns_add_u32 m0, %[lw], 23552\ns_add_u32 s44, %[wo], 2637824\nbuffer_load_dwordx4 v92, %[rw], s44 offen lds\ns_waitcnt vmcnt(32)\nds_read_b64_tr_b16 a[32:33], v93 offset:0\n" \
"ds_read_b64_tr_b16 a[34:35], v93 offset:256\nds_read_b64_tr_b16 v[88:89], v93 offset:8\nds_read_b64_tr_b16 v[90:91], v93 offset:264\nds_read_b64_tr_b16 a[36:37], v93 offset:2048\nds_read_b64_tr_b16 a[38:39], v93 offset:2304\nds_read_b64_tr_b16 v[84:85], v93 offset:2056\nds_read_b64_tr_b16 v[86:87], v93 offset:2312\ns_waitcnt lgkmcnt(0)\n" \
"s_add_u32 m0, %[tb], 0\ns_add_u32 s44, %[wo], 2293760\nbuffer_load_dwordx4 v92, %[ro], s44 offen lds\ns_add_u32 m0, %[tb], 1024\ns_add_u32 s44, %[wo], 2408448\nbuffer_load_dwordx4 v92, %[ro], s44 offen lds\ns_add_u32 m0, %[tb], 2048\ns_add_u32 s44, %[wo], 2523136\n" \
"buffer_load_dwordx4 v92, %[ro], s44 offen lds\ns_add_u32 m0, %[tb], 3072\ns_add_u32 s44, %[wo], 2637824\nbuffer_load_dwordx4 v92, %[ro], s44 offen lds\ns_waitcnt vmcnt(32)\nds_read_b64_tr_b16 a[40:41], v93 offset:4096\nds_read_b64_tr_b16 a[42:43], v93 offset:4352\nds_read_b64_tr_b16 a[48:49], v93 offset:4104\n" \
"ds_read_b64_tr_b16 a[50:51], v93 offset:4360\nds_read_b64_tr_b16 a[44:45], v93 offset:6144\nds_read_b64_tr_b16 a[46:47], v93 offset:6400\nds_read_b64_tr_b16 a[52:53], v93 offset:6152\nds_read_b64_tr_b16 a[54:55], v93 offset:6408\ns_waitcnt lgkmcnt(0)\ns_add_u32 m0, %[tb], 4096\ns_add_u32 s44, %[wo], 2752512\n" \
"buffer_load_dwordx4 v92, %[rw], s44 offen lds\ns_add_u32 m0, %[tb], 5120\ns_add_u32 s44, %[wo], 2867200\nbuffer_load_dwordx4 v92, %[rw], s44 offen lds\ns_add_u32 m0, %[tb], 6144\ns_add_u32 s44, %[wo], 2981888\nbuffer_load_dwordx4 v92, %[rw], s44 offen lds\ns_add_u32 m0, %[tb], 7168\n" \
"s_add_u32 s44, %[wo], 3096576\nbuffer_load_dwordx4 v92, %[rw], s44 offen lds\ns_waitcnt vmcnt(32)\nds_read_b64_tr_b16 a[56:57], v93 offset:8192\nds_read_b64_tr_b16 a[58:59], v93 offset:8448\nds_read_b64_tr_b16 a[64:65], v93 offset:8200\nds_read_b64_tr_b16 a[66:67], v93 offset:8456\nds_read_b64_tr_b16 a[60:61], v93 offset:10240\n" \
"ds_read_b64_tr_b16 a[62:63], v93 offset:10496\nds_read_b64_tr_b16 a[68:69], v93 offset:10248\nds_read_b64_tr_b16 a[70:71], v93 offset:10504\ns_waitcnt lgkmcnt(0)\ns_add_u32 m0, %[tb], 8192\ns_add_u32 s44, %[wo], 2752512\nbuffer_load_dwordx4 v92, %[ro], s44 offen lds\ns_add_u32 m0, %[tb], 9216\n" \
"s_add_u32 s44, %[wo], 2867200\nbuffer_load_dwordx4 v92, %[ro], s44 offen lds\ns_add_u32 m0, %[tb], 10240\ns_add_u32 s44, %[wo], 2981888\nbuffer_load_dwordx4 v92, %[ro], s44 offen lds\ns_add_u32 m0, %[tb], 11264\ns_add_u32 s44, %[wo], 3096576\nbuffer_load_dwordx4 v92, %[ro], s44 offen lds\n" \
"s_waitcnt vmcnt(32)\nds_read_b64_tr_b16 v[80:81], v94 offset:0\nds_read_b64_tr_b16 v[82:83], v94 offset:256\nds_read_b64_tr_b16 a[72:73], v94 offset:8\nds_read_b64_tr_b16 a[74:75], v94 offset:264\nds_read_b64_tr_b16 v[76:77], v94 offset:2048\nds_read_b64_tr_b16 v[78:79], v94 offset:2304\nds_read_b64_tr_b16 a[76:77], v94 offset:2056\n" \
"ds_read_b64_tr_b16 a[78:79], v94 offset:2312\ns_waitcnt lgkmcnt(0)\ns_add_u32 m0, %[lw], 0\ns_add_u32 s44, %[wo], 3211264\nbuffer_load_dwordx4 v92, %[rw], s44 offen lds\ns_add_u32 m0, %[lw], 1024\ns_add_u32 s44, %[wo], 3325952\nbuffer_load_dwordx4 v92, %[rw], s44 offen lds\n" \
"s_add_u32 m0, %[lw], 2048\ns_add_u32 s44, %[wo], 3440640\nbuffer_load_dwordx4 v92, %[rw], s44 offen lds\ns_add_u32 m0, %[lw], 3072\ns_add_u32 s44, %[wo], 3555328\nbuffer_load_dwordx4 v92, %[rw], s44 offen lds\ns_waitcnt vmcnt(32)\nds_read_b64_tr_b16 a[80:81], v94 offset:4096\n" \
"ds_read_b64_tr_b16 a[82:83], v94 offset:4352\nds_read_b64_tr_b16 a[88:89], v94 offset:4104\nds_read_b64_tr_b16 a[90:91], v94 offset:4360\nds_read_b64_tr_b16 a[84:85], v94 offset:6144\nds_read_b64_tr_b16 a[86:87], v94 offset:6400\nds_read_b64_tr_b16 a[92:93], v94 offset:6152\nds_read_b64_tr_b16 a[94:95], v94 offset:6408\ns_waitcnt lgkmcnt(0)\n" \
"s_add_u32 m0, %[lw], 4096\ns_add_u32 s44, %[wo], 3211264\nbuffer_load_dwordx4 v92, %[ro], s44 offen lds\ns_add_u32 m0, %[lw], 5120\ns_add_u32 s44, %[wo], 3325952\nbuffer_load_dwordx4 v92, %[ro], s44 offen lds\ns_add_u32 m0, %[lw], 6144\ns_add_u32 s44, %[wo], 3440640\n" \
"buffer_load_dwordx4 v92, %[ro], s44 offen lds\ns_add_u32 m0, %[lw], 7168\ns_add_u32 s44, %[wo], 3555328\nbuffer_load_dwordx4 v92, %[ro], s44 offen lds\ns_waitcnt vmcnt(32)\nds_read_b64_tr_b16 a[96:97], v94 offset:8192\nds_read_b64_tr_b16 a[98:99], v94 offset:8448\nds_read_b64_tr_b16 v[72:73], v94 offset:8200\n" \
"ds_read_b64_tr_b16 v[74:75], v94 offset:8456\nds_read_b64_tr_b16 a[100:101], v94 offset:10240\nds_read_b64_tr_b16 a[102:103], v94 offset:10496\nds_read_b64_tr_b16 v[68:69], v94 offset:10248\nds_read_b64_tr_b16 v[70:71], v94 offset:10504\ns_waitcnt lgkmcnt(0)\ns_add_u32 m0, %[lw], 8192\ns_add_u32 s44, %[wo], 3670016\n" \
"buffer_load_dwordx4 v92, %[rw], s44 offen lds\ns_add_u32 m0, %[lw], 9216\ns_add_u32 s44, %[wo], 3784704\nbuffer_load_dwordx4 v92, %[rw], s44 offen lds\ns_add_u32 m0, %[lw], 10240\ns_add_u32 s44, %[wo], 3899392\nbuffer_load_dwordx4 v92, %[rw], s44 offen lds\ns_add_u32 m0, %[lw], 11264\n" \
"s_add_u32 s44, %[wo], 4014080\nbuffer_load_dwordx4 v92, %[rw], s44 offen lds\ns_waitcnt vmcnt(32)\nds_read_b64_tr_b16 a[104:105], v94 offset:12288\nds_read_b64_tr_b16 a[106:107], v94 offset:12544\nds_read_b64_tr_b16 a[112:113], v94 offset:12296\nds_read_b64_tr_b16 a[114:115], v94 offset:12552\nds_read_b64_tr_b16 a[108:109], v94 offset:14336\n" \
"ds_read_b64_tr_b16 a[110:111], v94 offset:14592\nds_read_b64_tr_b16 a[116:117], v94 offset:14344\nds_read_b64_tr_b16 a[118:119], v94 offset:14600\ns_waitcnt lgkmcnt(0)\ns_add_u32 m0, %[lw], 12288\ns_add_u32 s44, %[wo], 3670016\nbuffer_load_dwordx4 v92, %[ro], s44 offen lds\ns_add_u32 m0, %[lw], 13312\n" \
"s_add_u32 s44, %[wo], 3784704\nbuffer_load_dwordx4 v92, %[ro], s44 offen lds\ns_add_u32 m0, %[lw], 14336\ns_add_u32 s44, %[wo], 3899392\nbuffer_load_dwordx4 v92, %[ro], s44 offen lds\ns_add_u32 m0, %[lw], 15360\ns_add_u32 s44, %[wo], 4014080\nbuffer_load_dwordx4 v92, %[ro], s44 offen lds\n" \
"s_waitcnt vmcnt(32)\nds_read_b64_tr_b16 a[120:121], v94 offset:16384\nds_read_b64_tr_b16 a[122:123], v94 offset:16640\nds_read_b64_tr_b16 a[128:129], v94 offset:16392\nds_read_b64_tr_b16 a[130:131], v94 offset:16648\nds_read_b64_tr_b16 a[124:125], v94 offset:18432\nds_read_b64_tr_b16 a[126:127], v94 offset:18688\nds_read_b64_tr_b16 a[132:133], v94 offset:18440\n" \
"ds_read_b64_tr_b16 a[134:135], v94 offset:18696\ns_waitcnt lgkmcnt(0)\ns_add_u32 m0, %[lw], 16384\ns_add_u32 s44, %[wo], 4128768\nbuffer_load_dwordx4 v92, %[rw], s44 offen lds\ns_add_u32 m0, %[lw], 17408\ns_add_u32 s44, %[wo], 4243456\nbuffer_load_dwordx4 v92, %[rw], s44 offen lds\n" \
"s_add_u32 m0, %[lw], 18432\ns_add_u32 s44, %[wo], 4358144\nbuffer_load_dwordx4 v92, %[rw], s44 offen lds\ns_add_u32 m0, %[lw], 19456\ns_add_u32 s44, %[wo], 4472832\nbuffer_load_dwordx4 v92, %[rw], s44 offen lds\ns_waitcnt vmcnt(32)\nds_read_b64_tr_b16 a[136:137], v94 offset:20480\n" \
"ds_read_b64_tr_b16 a[138:139], v94 offset:20736\nds_read_b64_tr_b16 v[64:65], v94 offset:20488\nds_read_b64_tr_b16 v[66:67], v94 offset:20744\nds_read_b64_tr_b16 a[140:141], v94 offset:22528\nds_read_b64_tr_b16 a[142:143], v94 offset:22784\nds_read_b64_tr_b16 v[60:61], v94 offset:22536\nds_read_b64_tr_b16 v[62:63], v94 offset:22792\ns_waitcnt lgkmcnt(0)\n" \
"s_add_u32 m0, %[lw], 20480\ns_add_u32 s44, %[wo], 4128768\nbuffer_load_dwordx4 v92, %[ro], s44 offen lds\ns_add_u32 m0, %[lw], 21504\ns_add_u32 s44, %[wo], 4243456\nbuffer_load_dwordx4 v92, %[ro], s44 offen lds\ns_add_u32 m0, %[lw], 22528\ns_add_u32 s44, %[wo], 4358144\n" \
"buffer_load_dwordx4 v92, %[ro], s44 offen lds\ns_add_u32 m0, %[lw], 23552\ns_add_u32 s44, %[wo], 4472832\nbuffer_load_dwordx4 v92, %[ro], s44 offen lds\ns_waitcnt vmcnt(32)\nds_read_b64_tr_b16 a[144:145], v93 offset:0\nds_read_b64_tr_b16 a[146:147], v93 offset:256\nds_read_b64_tr_b16 a[152:153], v93 offset:8\n" \
"ds_read_b64_tr_b16 a[154:155], v93 offset:264\nds_read_b64_tr_b16 a[148:149], v93 offset:2048\nds_read_b64_tr_b16 a[150:151], v93 offset:2304\nds_read_b64_tr_b16 a[156:157], v93 offset:2056\nds_read_b64_tr_b16 a[158:159], v93 offset:2312\ns_waitcnt lgkmcnt(0)\ns_add_u32 m0, %[tb], 0\ns_add_u32 s44, %[wo], 4587520\n" \
"buffer_load_dwordx4 v92, %[rw], s44 offen lds\ns_add_u32 m0, %[tb], 1024\ns_add_u32 s44, %[wo], 4702208\nbuffer_load_dwordx4 v92, %[rw], s44 offen lds\ns_add_u32 m0, %[tb], 2048\ns_add_u32 s44, %[wo], 4816896\nbuffer_load_dwordx4 v92, %[rw], s44 offen lds\ns_add_u32 m0, %[tb], 3072\n" \
"s_add_u32 s44, %[wo], 4931584\nbuffer_load_dwordx4 v92, %[rw], s44 offen lds\ns_waitcnt vmcnt(32)\nds_read_b64_tr_b16 a[160:161], v93 offset:4096\nds_read_b64_tr_b16 a[162:163], v93 offset:4352\nds_read_b64_tr_b16 a[168:169], v93 offset:4104\nds_read_b64_tr_b16 a[170:171], v93 offset:4360\nds_read_b64_tr_b16 a[164:165], v93 offset:6144\n" \
"ds_read_b64_tr_b16 a[166:167], v93 offset:6400\nds_read_b64_tr_b16 a[172:173], v93 offset:6152\nds_read_b64_tr_b16 a[174:175], v93 offset:6408\ns_waitcnt lgkmcnt(0)\ns_add_u32 m0, %[tb], 4096\ns_add_u32 s44, %[wo], 4587520\nbuffer_load_dwordx4 v92, %[ro], s44 offen lds\ns_add_u32 m0, %[tb], 5120\n" \
"s_add_u32 s44, %[wo], 4702208\nbuffer_load_dwordx4 v92, %[ro], s44 offen lds\ns_add_u32 m0, %[tb], 6144\ns_add_u32 s44, %[wo], 4816896\nbuffer_load_dwordx4 v92, %[ro], s44 offen lds\ns_add_u32 m0, %[tb], 7168\ns_add_u32 s44, %[wo], 4931584\nbuffer_load_dwordx4 v92, %[ro], s44 offen lds\n" \
"s_waitcnt vmcnt(32)\nds_read_b64_tr_b16 v[56:57], v93 offset:8192\nds_read_b64_tr_b16 v[58:59], v93 offset:8448\nds_read_b64_tr_b16 a[176:177], v93 offset:8200\nds_read_b64_tr_b16 a[178:179], v93 offset:8456\nds_read_b64_tr_b16 v[52:53], v93 offset:10240\nds_read_b64_tr_b16 v[54:55], v93 offset:10496\nds_read_b64_tr_b16 a[180:181], v93 offset:10248\n" \
"ds_read_b64_tr_b16 a[182:183], v93 offset:10504\ns_waitcnt lgkmcnt(0)\ns_add_u32 m0, %[tb], 8192\ns_add_u32 s44, %[wo], 5046272\nbuffer_load_dwordx4 v92, %[rw], s44 offen lds\ns_add_u32 m0, %[tb], 9216\ns_add_u32 s44, %[wo], 5160960\nbuffer_load_dwordx4 v92, %[rw], s44 offen lds\n" \
"s_add_u32 m0, %[tb], 10240\ns_add_u32 s44, %[wo], 5275648\nbuffer_load_dwordx4 v92, %[rw], s44 offen lds\ns_add_u32 m0, %[tb], 11264\ns_add_u32 s44, %[wo], 5390336\nbuffer_load_dwordx4 v92, %[rw], s44 offen lds\ns_waitcnt vmcnt(32)\nds_read_b64_tr_b16 a[184:185], v94 offset:0\n" \
"ds_read_b64_tr_b16 a[186:187], v94 offset:256\nds_read_b64_tr_b16 a[192:193], v94 offset:8\nds_read_b64_tr_b16 a[194:195], v94 offset:264\nds_read_b64_tr_b16 a[188:189], v94 offset:2048\nds_read_b64_tr_b16 a[190:191], v94 offset:2304\nds_read_b64_tr_b16 a[196:197], v94 offset:2056\nds_read_b64_tr_b16 a[198:199], v94 offset:2312\ns_waitcnt lgkmcnt(0)\n" \
"s_add_u32 m0, %[lw], 0\ns_add_u32 s44, %[wo], 5046272\nbuffer_load_dwordx4 v92, %[ro], s44 offen lds\ns_add_u32 m0, %[lw], 1024\ns_add_u32 s44, %[wo], 5160960\nbuffer_load_dwordx4 v92, %[ro], s44 offen lds\ns_add_u32 m0, %[lw], 2048\ns_add_u32 s44, %[wo], 5275648\n" \
"buffer_load_dwordx4 v92, %[ro], s44 offen lds\ns_add_u32 m0, %[lw], 3072\ns_add_u32 s44, %[wo], 5390336\nbuffer_load_dwordx4 v92, %[ro], s44 offen lds\ns_waitcnt vmcnt(32)\nds_read_b64_tr_b16 a[200:201], v94 offset:4096\nds_read_b64_tr_b16 a[202:203], v94 offset:4352\nds_read_b64_tr_b16 v[48:49], v94 offset:4104\n" \
"ds_read_b64_tr_b16 v[50:51], v94 offset:4360\nds_read_b64_tr_b16 a[204:205], v94 offset:6144\nds_read_b64_tr_b16 a[206:207], v94 offset:6400\nds_read_b64_tr_b16 v[44:45], v94 offset:6152\nds_read_b64_tr_b16 v[46:47], v94 offset:6408\ns_waitcnt lgkmcnt(0)\ns_add_u32 m0, %[lw], 4096\ns_add_u32 s44, %[wo], 5505024\n" \
"buffer_load_dwordx4 v92, %[rw], s44 offen lds\ns_add_u32 m0, %[lw], 5120\ns_add_u32 s44, %[wo], 5619712\nbuffer_load_dwordx4 v92, %[rw], s44 offen lds\ns_add_u32 m0, %[lw], 6144\ns_add_u32 s44, %[wo], 5734400\nbuffer_load_dwordx4 v92, %[rw], s44 offen lds\ns_add_u32 m0, %[lw], 7168\n" \
"s_add_u32 s44, %[wo], 5849088\nbuffer_load_dwordx4 v92, %[rw], s44 offen lds\ns_waitcnt vmcnt(32)\nds_read_b64_tr_b16 a[208:209], v94 offset:8192\nds_read_b64_tr_b16 a[210:211], v94 offset:8448\nds_read_b64_tr_b16 a[216:217], v94 offset:8200\nds_read_b64_tr_b16 a[218:219], v94 offset:8456\nds_read_b64_tr_b16 a[212:213], v94 offset:10240\n" \
"ds_read_b64_tr_b16 a[214:215], v94 offset:10496\nds_read_b64_tr_b16 a[220:221], v94 offset:10248\nds_read_b64_tr_b16 a[222:223], v94 offset:10504\ns_waitcnt lgkmcnt(0)\ns_add_u32 m0, %[lw], 8192\ns_add_u32 s44, %[wo], 5505024\nbuffer_load_dwordx4 v92, %[ro], s44 offen lds\ns_add_u32 m0, %[lw], 9216\n" \
"s_add_u32 s44, %[wo], 5619712\nbuffer_load_dwordx4 v92, %[ro], s44 offen lds\ns_add_u32 m0, %[lw], 10240\ns_add_u32 s44, %[wo], 5734400\nbuffer_load_dwordx4 v92, %[ro], s44 offen lds\ns_add_u32 m0, %[lw], 11264\ns_add_u32 s44, %[wo], 5849088\nbuffer_load_dwordx4 v92, %[ro], s44 offen lds\n" \
"s_waitcnt vmcnt(32)\nds_read_b64_tr_b16 a[224:225], v94 offset:12288\nds_read_b64_tr_b16 a[226:227], v94 offset:12544\nds_read_b64_tr_b16 a[232:233], v94 offset:12296\nds_read_b64_tr_b16 a[234:235], v94 offset:12552\nds_read_b64_tr_b16 a[228:229], v94 offset:14336\nds_read_b64_tr_b16 a[230:231], v94 offset:14592\nds_read_b64_tr_b16 a[236:237], v94 offset:14344\n" \
"ds_read_b64_tr_b16 a[238:239], v94 offset:14600\ns_waitcnt lgkmcnt(0)\ns_add_u32 m0, %[lw], 12288\ns_add_u32 s44, %[wo], 5963776\nbuffer_load_dwordx4 v92, %[rw], s44 offen lds\ns_add_u32 m0, %[lw], 13312\ns_add_u32 s44, %[wo], 6078464\nbuffer_load_dwordx4 v92, %[rw], s44 offen lds\n" \
"s_add_u32 m0, %[lw], 14336\ns_add_u32 s44, %[wo], 6193152\nbuffer_load_dwordx4 v92, %[rw], s44 offen lds\ns_add_u32 m0, %[lw], 15360\ns_add_u32 s44, %[wo], 6307840\nbuffer_load_dwordx4 v92, %[rw], s44 offen lds\ns_waitcnt vmcnt(32)\nds_read_b64_tr_b16 a[240:241], v94 offset:16384\n" \
"ds_read_b64_tr_b16 a[242:243], v94 offset:16640\nds_read_b64_tr_b16 v[40:41], v94 offset:16392\nds_read_b64_tr_b16 v[42:43], v94 offset:16648\nds_read_b64_tr_b16 a[244:245], v94 offset:18432\nds_read_b64_tr_b16 a[246:247], v94 offset:18688\nds_read_b64_tr_b16 v[36:37], v94 offset:18440\nds_read_b64_tr_b16 v[38:39], v94 offset:18696\ns_waitcnt lgkmcnt(0)\n" \
"s_add_u32 m0, %[lw], 16384\ns_add_u32 s44, %[wo], 5963776\nbuffer_load_dwordx4 v92, %[ro], s44 offen lds\ns_add_u32 m0, %[lw], 17408\ns_add_u32 s44, %[wo], 6078464\nbuffer_load_dwordx4 v92, %[ro], s44 offen lds\ns_add_u32 m0, %[lw], 18432\ns_add_u32 s44, %[wo], 6193152\n" \
"buffer_load_dwordx4 v92, %[ro], s44 offen lds\ns_add_u32 m0, %[lw], 19456\ns_add_u32 s44, %[wo], 6307840\nbuffer_load_dwordx4 v92, %[ro], s44 offen lds\ns_waitcnt vmcnt(32)\nds_read_b64_tr_b16 a[248:249], v94 offset:20480\nds_read_b64_tr_b16 a[250:251], v94 offset:20736\nds_read_b64_tr_b16 v[96:97], v94 offset:20488\n" \
"ds_read_b64_tr_b16 v[98:99], v94 offset:20744\nds_read_b64_tr_b16 a[252:253], v94 offset:22528\nds_read_b64_tr_b16 a[254:255], v94 offset:22784\nds_read_b64_tr_b16 v[100:101], v94 offset:22536\nds_read_b64_tr_b16 v[102:103], v94 offset:22792\ns_waitcnt lgkmcnt(0)\ns_add_u32 m0, %[lw], 20480\ns_add_u32 s44, %[wo], 6422528\n" \
"buffer_load_dwordx4 v92, %[rw], s44 offen lds\ns_add_u32 m0, %[lw], 21504\ns_add_u32 s44, %[wo], 6537216\nbuffer_load_dwordx4 v92, %[rw], s44 offen lds\ns_add_u32 m0, %[lw], 22528\ns_add_u32 s44, %[wo], 6651904\nbuffer_load_dwordx4 v92, %[rw], s44 offen lds\ns_add_u32 m0, %[lw], 23552\n" \
"s_add_u32 s44, %[wo], 6766592\nbuffer_load_dwordx4 v92, %[rw], s44 offen lds\ns_waitcnt vmcnt(32)\nds_read_b64_tr_b16 v[104:105], v93 offset:0\nds_read_b64_tr_b16 v[106:107], v93 offset:256\nds_read_b64_tr_b16 v[112:113], v93 offset:8\nds_read_b64_tr_b16 v[114:115], v93 offset:264\nds_read_b64_tr_b16 v[108:109], v93 offset:2048\n" \
"ds_read_b64_tr_b16 v[110:111], v93 offset:2304\nds_read_b64_tr_b16 v[116:117], v93 offset:2056\nds_read_b64_tr_b16 v[118:119], v93 offset:2312\ns_waitcnt lgkmcnt(0)\ns_add_u32 m0, %[tb], 0\ns_add_u32 s44, %[wo], 6422528\nbuffer_load_dwordx4 v92, %[ro], s44 offen lds\ns_add_u32 m0, %[tb], 1024\n" \
"s_add_u32 s44, %[wo], 6537216\nbuffer_load_dwordx4 v92, %[ro], s44 offen lds\ns_add_u32 m0, %[tb], 2048\ns_add_u32 s44, %[wo], 6651904\nbuffer_load_dwordx4 v92, %[ro], s44 offen lds\ns_add_u32 m0, %[tb], 3072\ns_add_u32 s44, %[wo], 6766592\nbuffer_load_dwordx4 v92, %[ro], s44 offen lds\n" \
"s_waitcnt vmcnt(32)\nds_read_b64_tr_b16 v[32:33], v93 offset:4096\nds_read_b64_tr_b16 v[34:35], v93 offset:4352\nds_read_b64_tr_b16 v[120:121], v93 offset:4104\nds_read_b64_tr_b16 v[122:123], v93 offset:4360\nds_read_b64_tr_b16 v[28:29], v93 offset:6144\nds_read_b64_tr_b16 v[30:31], v93 offset:6400\nds_read_b64_tr_b16 v[124:125], v93 offset:6152\n" \
"ds_read_b64_tr_b16 v[126:127], v93 offset:6408\ns_waitcnt lgkmcnt(0)\ns_add_u32 m0, %[tb], 4096\ns_add_u32 s44, %[wo], 6881280\nbuffer_load_dwordx4 v92, %[rw], s44 offen lds\ns_add_u32 m0, %[tb], 5120\ns_add_u32 s44, %[wo], 6995968\nbuffer_load_dwordx4 v92, %[rw], s44 offen lds\n" \
"s_add_u32 m0, %[tb], 6144\ns_add_u32 s44, %[wo], 7110656\nbuffer_load_dwordx4 v92, %[rw], s44 offen lds\ns_add_u32 m0, %[tb], 7168\ns_add_u32 s44, %[wo], 7225344\nbuffer_load_dwordx4 v92, %[rw], s44 offen lds\ns_waitcnt vmcnt(32)\nds_read_b64_tr_b16 v[128:129], v93 offset:8192\n" \
"ds_read_b64_tr_b16 v[130:131], v93 offset:8448\nds_read_b64_tr_b16 v[136:137], v93 offset:8200\nds_read_b64_tr_b16 v[138:139], v93 offset:8456\nds_read_b64_tr_b16 v[132:133], v93 offset:10240\nds_read_b64_tr_b16 v[134:135], v93 offset:10496\nds_read_b64_tr_b16 v[140:141], v93 offset:10248\nds_read_b64_tr_b16 v[142:143], v93 offset:10504\ns_waitcnt lgkmcnt(0)\n" \
"s_add_u32 m0, %[tb], 8192\ns_add_u32 s44, %[wo], 6881280\nbuffer_load_dwordx4 v92, %[ro], s44 offen lds\ns_add_u32 m0, %[tb], 9216\ns_add_u32 s44, %[wo], 6995968\nbuffer_load_dwordx4 v92, %[ro], s44 offen lds\ns_add_u32 m0, %[tb], 10240\ns_add_u32 s44, %[wo], 7110656\n" \
"buffer_load_dwordx4 v92, %[ro], s44 offen lds\ns_add_u32 m0, %[tb], 11264\ns_add_u32 s44, %[wo], 7225344\nbuffer_load_dwordx4 v92, %[ro], s44 offen lds\ns_waitcnt vmcnt(32)\nds_read_b64_tr_b16 v[144:145], v94 offset:0\nds_read_b64_tr_b16 v[146:147], v94 offset:256\nds_read_b64_tr_b16 v[24:25], v94 offset:8\n" \
"ds_read_b64_tr_b16 v[26:27], v94 offset:264\nds_read_b64_tr_b16 v[148:149], v94 offset:2048\nds_read_b64_tr_b16 v[150:151], v94 offset:2304\nds_read_b64_tr_b16 v[20:21], v94 offset:2056\nds_read_b64_tr_b16 v[22:23], v94 offset:2312\ns_waitcnt lgkmcnt(0)\nds_write_b128 v1, v[88:91] offset:0\nds_write_b128 v1, v[84:87] offset:1024\n" \
"ds_write_b128 v1, v[80:83] offset:2048\nds_write_b128 v1, v[76:79] offset:3072\ns_waitcnt vmcnt(28)\nds_read_b64_tr_b16 v[152:153], v94 offset:4096\nds_read_b64_tr_b16 v[154:155], v94 offset:4352\nds_read_b64_tr_b16 v[160:161], v94 offset:4104\nds_read_b64_tr_b16 v[162:163], v94 offset:4360\nds_read_b64_tr_b16 v[156:157], v94 offset:6144\n" \
"ds_read_b64_tr_b16 v[158:159], v94 offset:6400\nds_read_b64_tr_b16 v[164:165], v94 offset:6152\nds_read_b64_tr_b16 v[166:167], v94 offset:6408\ns_waitcnt lgkmcnt(0)\nds_write_b128 v1, v[72:75] offset:4096\nds_write_b128 v1, v[68:71] offset:5120\nds_write_b128 v1, v[64:67] offset:6144\nds_write_b128 v1, v[60:63] offset:7168\n" \
"s_waitcnt vmcnt(24)\nds_read_b64_tr_b16 v[168:169], v94 offset:8192\nds_read_b64_tr_b16 v[170:171], v94 offset:8448\nds_read_b64_tr_b16 v[176:177], v94 offset:8200\nds_read_b64_tr_b16 v[178:179], v94 offset:8456\nds_read_b64_tr_b16 v[172:173], v94 offset:10240\nds_read_b64_tr_b16 v[174:175], v94 offset:10496\nds_read_b64_tr_b16 v[180:181], v94 offset:10248\n" \
"ds_read_b64_tr_b16 v[182:183], v94 offset:10504\ns_waitcnt lgkmcnt(0)\nds_write_b128 v1, v[56:59] offset:8192\nds_write_b128 v1, v[52:55] offset:9216\nds_write_b128 v1, v[48:51] offset:10240\nds_write_b128 v1, v[44:47] offset:11264\ns_waitcnt vmcnt(20)\nds_read_b64_tr_b16 v[184:185], v94 offset:12288\n" \
"ds_read_b64_tr_b16 v[186:187], v94 offset:12544\nds_read_b64_tr_b16 v[44:45], v94 offset:12296\nds_read_b64_tr_b16 v[46:47], v94 offset:12552\nds_read_b64_tr_b16 v[188:189], v94 offset:14336\nds_read_b64_tr_b16 v[190:191], v94 offset:14592\nds_read_b64_tr_b16 v[48:49], v94 offset:14344\nds_read_b64_tr_b16 v[50:51], v94 offset:14600\ns_waitcnt lgkmcnt(0)\n" \
"ds_write_b128 v1, v[40:43] offset:12288\nds_write_b128 v1, v[36:39] offset:13312\nds_write_b128 v1, v[32:35] offset:14336\nds_write_b128 v1, v[28:31] offset:15360\ns_waitcnt vmcnt(16)\nds_read_b64_tr_b16 v[192:193], v94 offset:16384\nds_read_b64_tr_b16 v[194:195], v94 offset:16640\nds_read_b64_tr_b16 v[200:201], v94 offset:16392\n" \
"ds_read_b64_tr_b16 v[202:203], v94 offset:16648\nds_read_b64_tr_b16 v[196:197], v94 offset:18432\nds_read_b64_tr_b16 v[198:199], v94 offset:18688\nds_read_b64_tr_b16 v[204:205], v94 offset:18440\nds_read_b64_tr_b16 v[206:207], v94 offset:18696\ns_waitcnt lgkmcnt(0)\nds_write_b128 v1, v[24:27] offset:16384\nds_write_b128 v1, v[20:23] offset:17408\n" \
"ds_write_b128 v1, v[44:47] offset:18432\nds_write_b128 v1, v[48:51] offset:19456\ns_waitcnt vmcnt(12)\nds_read_b64_tr_b16 v[208:209], v94 offset:20480\nds_read_b64_tr_b16 v[210:211], v94 offset:20736\nds_read_b64_tr_b16 v[216:217], v94 offset:20488\nds_read_b64_tr_b16 v[218:219], v94 offset:20744\nds_read_b64_tr_b16 v[212:213], v94 offset:22528\n" \
"ds_read_b64_tr_b16 v[214:215], v94 offset:22784\nds_read_b64_tr_b16 v[220:221], v94 offset:22536\nds_read_b64_tr_b16 v[222:223], v94 offset:22792\ns_waitcnt lgkmcnt(0)\ns_waitcnt vmcnt(8)\nds_read_b64_tr_b16 v[48:49], v93 offset:0\nds_read_b64_tr_b16 v[50:51], v93 offset:256\nds_read_b64_tr_b16 v[224:225], v93 offset:8\n" \
"ds_read_b64_tr_b16 v[226:227], v93 offset:264\nds_read_b64_tr_b16 v[44:45], v93 offset:2048\nds_read_b64_tr_b16 v[46:47], v93 offset:2304\nds_read_b64_tr_b16 v[228:229], v93 offset:2056\nds_read_b64_tr_b16 v[230:231], v93 offset:2312\ns_waitcnt lgkmcnt(0)\nds_write_b128 v1, v[48:51] offset:20480\nds_write_b128 v1, v[44:47] offset:21504\n" \
"s_waitcnt vmcnt(4)\nds_read_b64_tr_b16 v[232:233], v93 offset:4096\nds_read_b64_tr_b16 v[234:235], v93 offset:4352\nds_read_b64_tr_b16 v[240:241], v93 offset:4104\nds_read_b64_tr_b16 v[242:243], v93 offset:4360\nds_read_b64_tr_b16 v[236:237], v93 offset:6144\nds_read_b64_tr_b16 v[238:239], v93 offset:6400\nds_read_b64_tr_b16 v[244:245], v93 offset:6152\n" \
"ds_read_b64_tr_b16 v[246:247], v93 offset:6408\ns_waitcnt lgkmcnt(0)\ns_waitcnt vmcnt(0)\nds_read_b64_tr_b16 v[248:249], v93 offset:8192\nds_read_b64_tr_b16 v[250:251], v93 offset:8448\nds_read_b64_tr_b16 v[44:45], v93 offset:8200\nds_read_b64_tr_b16 v[46:47], v93 offset:8456\nds_read_b64_tr_b16 v[252:253], v93 offset:10240\n" \
"ds_read_b64_tr_b16 v[254:255], v93 offset:10496\nds_read_b64_tr_b16 v[48:49], v93 offset:10248\nds_read_b64_tr_b16 v[50:51], v93 offset:10504\ns_waitcnt lgkmcnt(0)\nds_write_b128 v1, v[44:47] offset:22528\nds_write_b128 v1, v[48:51] offset:23552\ns_waitcnt lgkmcnt(0)\ns_barrier\n" \
"s_mov_b32 s40, 0\ns_mov_b32 s43, 0\ns_lshl_b32 s57, %[w], 12\ns_mov_b32 s58, 0xbeeff558\ns_mov_b32 s59, 0x409d30fc\ns_mov_b32 s56, -2147483648\nv_mov_b32 v56, 0x3f803f80\nv_mov_b32 v57, 0x3f803f80\n" \
"v_mov_b32 v58, 0x3f803f80\nv_mov_b32 v59, 0x3f803f80\ns_nop 1\nv_mfma_f32_16x16x32_bf16 v[8:11], a[0:3], v[56:59], 0\nv_mfma_f32_16x16x32_bf16 v[12:15], a[8:11], v[56:59], 0\nv_mfma_f32_16x16x32_bf16 v[16:19], a[16:19], v[56:59], 0\nv_mfma_f32_16x16x32_bf16 v[20:23], a[24:27], v[56:59], 0\nv_mfma_f32_16x16x32_bf16 v[8:11], a[4:7], v[56:59], v[8:11]\n" \
"v_mfma_f32_16x16x32_bf16 v[12:15], a[12:15], v[56:59], v[12:15]\nv_mfma_f32_16x16x32_bf16 v[16:19], a[20:23], v[56:59], v[16:19]\nv_mfma_f32_16x16x32_bf16 v[20:23], a[28:31], v[56:59], v[20:23]\nds_read_b128 v[80:83], v1 offset:0\nv_mfma_f32_16x16x32_bf16 v[8:11], a[32:35], v[56:59], v[8:11]\ns_waitcnt lgkmcnt(0)\nv_mfma_f32_16x16x32_bf16 v[12:15], v[80:83], v[56:59], v[12:15]\nv_mfma_f32_16x16x32_bf16 v[16:19], a[40:43], v[56:59], v[16:19]\n" \
"v_mfma_f32_16x16x32_bf16 v[20:23], a[48:51], v[56:59], v[20:23]\nds_read_b128 v[72:75], v1 offset:1024\nv_mfma_f32_16x16x32_bf16 v[8:11], a[36:39], v[56:59], v[8:11]\ns_waitcnt lgkmcnt(0)\nv_mfma_f32_16x16x32_bf16 v[12:15], v[72:75], v[56:59], v[12:15]\nv_mfma_f32_16x16x32_bf16 v[16:19], a[44:47], v[56:59], v[16:19]\nv_mfma_f32_16x16x32_bf16 v[20:23], a[52:55], v[56:59], v[20:23]\nds_read_b128 v[76:79], v1 offset:2048\n" \
"v_mfma_f32_16x16x32_bf16 v[8:11], a[56:59], v[56:59], v[8:11]\nv_mfma_f32_16x16x32_bf16 v[12:15], a[64:67], v[56:59], v[12:15]\ns_waitcnt lgkmcnt(0)\nv_mfma_f32_16x16x32_bf16 v[16:19], v[76:79], v[56:59], v[16:19]\nv_mfma_f32_16x16x32_bf16 v[20:23], a[72:75], v[56:59], v[20:23]\nds_read_b128 v[80:83], v1 offset:3072\nv_mfma_f32_16x16x32_bf16 v[8:11], a[60:63], v[56:59], v[8:11]\nv_mfma_f32_16x16x32_bf16 v[12:15], a[68:71], v[56:59], v[12:15]\n" \
"s_waitcnt lgkmcnt(0)\nv_mfma_f32_16x16x32_bf16 v[16:19], v[80:83], v[56:59], v[16:19]\nv_mfma_f32_16x16x32_bf16 v[20:23], a[76:79], v[56:59], v[20:23]\nds_read_b128 v[72:75], v1 offset:4096\nv_mfma_f32_16x16x32_bf16 v[8:11], a[80:83], v[56:59], v[8:11]\nv_mfma_f32_16x16x32_bf16 v[12:15], a[88:91], v[56:59], v[12:15]\nv_mfma_f32_16x16x32_bf16 v[16:19], a[96:99], v[56:59], v[16:19]\ns_waitcnt lgkmcnt(0)\n" \
"v_mfma_f32_16x16x32_bf16 v[20:23], v[72:75], v[56:59], v[20:23]\nds_read_b128 v[76:79], v1 offset:5120\nv_mfma_f32_16x16x32_bf16 v[8:11], a[84:87], v[56:59], v[8:11]\nv_mfma_f32_16x16x32_bf16 v[12:15], a[92:95], v[56:59], v[12:15]\nv_mfma_f32_16x16x32_bf16 v[16:19], a[100:103], v[56:59], v[16:19]\ns_waitcnt lgkmcnt(0)\nv_mfma_f32_16x16x32_bf16 v[20:23], v[76:79], v[56:59], v[20:23]\nv_mfma_f32_16x16x32_bf16 v[8:11], a[104:107], v[56:59], v[8:11]\n" \
"v_mfma_f32_16x16x32_bf16 v[12:15], a[112:115], v[56:59], v[12:15]\nv_mfma_f32_16x16x32_bf16 v[16:19], a[120:123], v[56:59], v[16:19]\nv_mfma_f32_16x16x32_bf16 v[20:23], a[128:131], v[56:59], v[20:23]\nv_mfma_f32_16x16x32_bf16 v[8:11], a[108:111], v[56:59], v[8:11]\nv_mfma_f32_16x16x32_bf16 v[12:15], a[116:119], v[56:59], v[12:15]\nv_mfma_f32_16x16x32_bf16 v[16:19], a[124:127], v[56:59], v[16:19]\nv_mfma_f32_16x16x32_bf16 v[20:23], a[132:135], v[56:59], v[20:23]\nds_read_b128 v[76:79], v1 offset:6144\n" \
"v_mfma_f32_16x16x32_bf16 v[8:11], a[136:139], v[56:59], v[8:11]\ns_waitcnt lgkmcnt(0)\nv_mfma_f32_16x16x32_bf16 v[12:15], v[76:79], v[56:59], v[12:15]\nv_mfma_f32_16x16x32_bf16 v[16:19], a[144:147], v[56:59], v[16:19]\nv_mfma_f32_16x16x32_bf16 v[20:23], a[152:155], v[56:59], v[20:23]\nds_read_b128 v[80:83], v1 offset:7168\nv_mfma_f32_16x16x32_bf16 v[8:11], a[140:143], v[56:59], v[8:11]\ns_waitcnt lgkmcnt(0)\n" \
"v_mfma_f32_16x16x32_bf16 v[12:15], v[80:83], v[56:59], v[12:15]\nv_mfma_f32_16x16x32_bf16 v[16:19], a[148:151], v[56:59], v[16:19]\nv_mfma_f32_16x16x32_bf16 v[20:23], a[156:159], v[56:59], v[20:23]\nds_read_b128 v[72:75], v1 offset:8192\nv_mfma_f32_16x16x32_bf16 v[8:11], a[160:163], v[56:59], v[8:11]\nv_mfma_f32_16x16x32_bf16 v[12:15], a[168:171], v[56:59], v[12:15]\ns_waitcnt lgkmcnt(0)\nv_mfma_f32_16x16x32_bf16 v[16:19], v[72:75], v[56:59], v[16:19]\n" \
"v_mfma_f32_16x16x32_bf16 v[20:23], a[176:179], v[56:59], v[20:23]\nds_read_b128 v[76:79], v1 offset:9216\nv_mfma_f32_16x16x32_bf16 v[8:11], a[164:167], v[56:59], v[8:11]\nv_mfma_f32_16x16x32_bf16 v[12:15], a[172:175], v[56:59], v[12:15]\ns_waitcnt lgkmcnt(0)\nv_mfma_f32_16x16x32_bf16 v[16:19], v[76:79], v[56:59], v[16:19]\nv_mfma_f32_16x16x32_bf16 v[20:23], a[180:183], v[56:59], v[20:23]\nds_read_b128 v[80:83], v1 offset:10240\n" \
"v_mfma_f32_16x16x32_bf16 v[8:11], a[184:187], v[56:59], v[8:11]\nv_mfma_f32_16x16x32_bf16 v[12:15], a[192:195], v[56:59], v[12:15]\nv_mfma_f32_16x16x32_bf16 v[16:19], a[200:203], v[56:59], v[16:19]\ns_waitcnt lgkmcnt(0)\nv_mfma_f32_16x16x32_bf16 v[20:23], v[80:83], v[56:59], v[20:23]\nds_read_b128 v[72:75], v1 offset:11264\nv_mfma_f32_16x16x32_bf16 v[8:11], a[188:191], v[56:59], v[8:11]\nv_mfma_f32_16x16x32_bf16 v[12:15], a[196:199], v[56:59], v[12:15]\n" \
"v_mfma_f32_16x16x32_bf16 v[16:19], a[204:207], v[56:59], v[16:19]\ns_waitcnt lgkmcnt(0)\nv_mfma_f32_16x16x32_bf16 v[20:23], v[72:75], v[56:59], v[20:23]\nv_mfma_f32_16x16x32_bf16 v[8:11], a[208:211], v[56:59], v[8:11]\nv_mfma_f32_16x16x32_bf16 v[12:15], a[216:219], v[56:59], v[12:15]\nv_mfma_f32_16x16x32_bf16 v[16:19], a[224:227], v[56:59], v[16:19]\nv_mfma_f32_16x16x32_bf16 v[20:23], a[232:235], v[56:59], v[20:23]\nv_mfma_f32_16x16x32_bf16 v[8:11], a[212:215], v[56:59], v[8:11]\n" \
"v_mfma_f32_16x16x32_bf16 v[12:15], a[220:223], v[56:59], v[12:15]\nv_mfma_f32_16x16x32_bf16 v[16:19], a[228:231], v[56:59], v[16:19]\nv_mfma_f32_16x16x32_bf16 v[20:23], a[236:239], v[56:59], v[20:23]\nds_read_b128 v[72:75], v1 offset:12288\nv_mfma_f32_16x16x32_bf16 v[8:11], a[240:243], v[56:59], v[8:11]\ns_waitcnt lgkmcnt(0)\nv_mfma_f32_16x16x32_bf16 v[12:15], v[72:75], v[56:59], v[12:15]\nv_mfma_f32_16x16x32_bf16 v[16:19], a[248:251], v[56:59], v[16:19]\n" \
"v_mfma_f32_16x16x32_bf16 v[20:23], v[96:99], v[56:59], v[20:23]\nds_read_b128 v[76:79], v1 offset:13312\nv_mfma_f32_16x16x32_bf16 v[8:11], a[244:247], v[56:59], v[8:11]\ns_waitcnt lgkmcnt(0)\nv_mfma_f32_16x16x32_bf16 v[12:15], v[76:79], v[56:59], v[12:15]\nv_mfma_f32_16x16x32_bf16 v[16:19], a[252:255], v[56:59], v[16:19]\nv_mfma_f32_16x16x32_bf16 v[20:23], v[100:103], v[56:59], v[20:23]\nds_read_b128 v[80:83], v1 offset:14336\n" \
"v_mfma_f32_16x16x32_bf16 v[8:11], v[104:107], v[56:59], v[8:11]\nv_mfma_f32_16x16x32_bf16 v[12:15], v[112:115], v[56:59], v[12:15]\ns_waitcnt lgkmcnt(0)\nv_mfma_f32_16x16x32_bf16 v[16:19], v[80:83], v[56:59], v[16:19]\nv_mfma_f32_16x16x32_bf16 v[20:23], v[120:123], v[56:59], v[20:23]\nds_read_b128 v[72:75], v1 offset:15360\nv_mfma_f32_16x16x32_bf16 v[8:11], v[108:111], v[56:59], v[8:11]\nv_mfma_f32_16x16x32_bf16 v[12:15], v[116:119], v[56:59], v[12:15]\n" \
"s_waitcnt lgkmcnt(0)\nv_mfma_f32_16x16x32_bf16 v[16:19], v[72:75], v[56:59], v[16:19]\nv_mfma_f32_16x16x32_bf16 v[20:23], v[124:127], v[56:59], v[20:23]\nds_read_b128 v[76:79], v1 offset:16384\nv_mfma_f32_16x16x32_bf16 v[8:11], v[128:131], v[56:59], v[8:11]\nv_mfma_f32_16x16x32_bf16 v[12:15], v[136:139], v[56:59], v[12:15]\nv_mfma_f32_16x16x32_bf16 v[16:19], v[144:147], v[56:59], v[16:19]\ns_waitcnt lgkmcnt(0)\n" \
"v_mfma_f32_16x16x32_bf16 v[20:23], v[76:79], v[56:59], v[20:23]\nds_read_b128 v[80:83], v1 offset:17408\nv_mfma_f32_16x16x32_bf16 v[8:11], v[132:135], v[56:59], v[8:11]\nv_mfma_f32_16x16x32_bf16 v[12:15], v[140:143], v[56:59], v[12:15]\nv_mfma_f32_16x16x32_bf16 v[16:19], v[148:151], v[56:59], v[16:19]\ns_waitcnt lgkmcnt(0)\nv_mfma_f32_16x16x32_bf16 v[20:23], v[80:83], v[56:59], v[20:23]\nv_mfma_f32_16x16x32_bf16 v[8:11], v[152:155], v[56:59], v[8:11]\n" \
"v_mfma_f32_16x16x32_bf16 v[12:15], v[160:163], v[56:59], v[12:15]\nv_mfma_f32_16x16x32_bf16 v[16:19], v[168:171], v[56:59], v[16:19]\nv_mfma_f32_16x16x32_bf16 v[20:23], v[176:179], v[56:59], v[20:23]\nv_mfma_f32_16x16x32_bf16 v[8:11], v[156:159], v[56:59], v[8:11]\nv_mfma_f32_16x16x32_bf16 v[12:15], v[164:167], v[56:59], v[12:15]\nv_mfma_f32_16x16x32_bf16 v[16:19], v[172:175], v[56:59], v[16:19]\nv_mfma_f32_16x16x32_bf16 v[20:23], v[180:183], v[56:59], v[20:23]\nds_read_b128 v[80:83], v1 offset:18432\n" \
"v_mfma_f32_16x16x32_bf16 v[8:11], v[184:187], v[56:59], v[8:11]\ns_waitcnt lgkmcnt(0)\nv_mfma_f32_16x16x32_bf16 v[12:15], v[80:83], v[56:59], v[12:15]\nv_mfma_f32_16x16x32_bf16 v[16:19], v[192:195], v[56:59], v[16:19]\nv_mfma_f32_16x16x32_bf16 v[20:23], v[200:203], v[56:59], v[20:23]\nds_read_b128 v[72:75], v1 offset:19456\nv_mfma_f32_16x16x32_bf16 v[8:11], v[188:191], v[56:59], v[8:11]\ns_waitcnt lgkmcnt(0)\n" \
"v_mfma_f32_16x16x32_bf16 v[12:15], v[72:75], v[56:59], v[12:15]\nv_mfma_f32_16x16x32_bf16 v[16:19], v[196:199], v[56:59], v[16:19]\nv_mfma_f32_16x16x32_bf16 v[20:23], v[204:207], v[56:59], v[20:23]\nds_read_b128 v[76:79], v1 offset:20480\nv_mfma_f32_16x16x32_bf16 v[8:11], v[208:211], v[56:59], v[8:11]\nv_mfma_f32_16x16x32_bf16 v[12:15], v[216:219], v[56:59], v[12:15]\ns_waitcnt lgkmcnt(0)\nv_mfma_f32_16x16x32_bf16 v[16:19], v[76:79], v[56:59], v[16:19]\n" \
"v_mfma_f32_16x16x32_bf16 v[20:23], v[224:227], v[56:59], v[20:23]\nds_read_b128 v[80:83], v1 offset:21504\nv_mfma_f32_16x16x32_bf16 v[8:11], v[212:215], v[56:59], v[8:11]\nv_mfma_f32_16x16x32_bf16 v[12:15], v[220:223], v[56:59], v[12:15]\ns_waitcnt lgkmcnt(0)\nv_mfma_f32_16x16x32_bf16 v[16:19], v[80:83], v[56:59], v[16:19]\nv_mfma_f32_16x16x32_bf16 v[20:23], v[228:231], v[56:59], v[20:23]\nds_read_b128 v[72:75], v1 offset:22528\n" \
"v_mfma_f32_16x16x32_bf16 v[8:11], v[232:235], v[56:59], v[8:11]\nv_mfma_f32_16x16x32_bf16 v[12:15], v[240:243], v[56:59], v[12:15]\nv_mfma_f32_16x16x32_bf16 v[16:19], v[248:251], v[56:59], v[16:19]\ns_waitcnt lgkmcnt(0)\nv_mfma_f32_16x16x32_bf16 v[20:23], v[72:75], v[56:59], v[20:23]\nds_read_b128 v[76:79], v1 offset:23552\nv_mfma_f32_16x16x32_bf16 v[8:11], v[236:239], v[56:59], v[8:11]\nv_mfma_f32_16x16x32_bf16 v[12:15], v[244:247], v[56:59], v[12:15]\n" \
"v_mfma_f32_16x16x32_bf16 v[16:19], v[252:255], v[56:59], v[16:19]\ns_waitcnt lgkmcnt(0)\nv_mfma_f32_16x16x32_bf16 v[20:23], v[76:79], v[56:59], v[20:23]\n.Lgg_next%=:\ns_cmp_lg_u32 %[w], 0\ns_cbranch_scc1 .Lgg_w1%=\nbuffer_load_dword v56, v3, %[rs], 0 offen sc1\ns_waitcnt vmcnt(0)\n" \
"buffer_store_dword v90, off, %[rs], s56 sc1\n.Lgg_poll%=:\nv_cmp_gt_i32 vcc, s40, v56\ns_cbranch_vccz .Lgg_go%=\ns_sleep 4\nbuffer_load_dword v56, v3, %[rs], 0 offen sc1\ns_waitcnt vmcnt(0)\ns_add_u32 s43, s43, 1\n" \
"s_cmp_lt_u32 s43, 262144\ns_cbranch_scc1 .Lgg_poll%=\ns_branch .Lgg_abort%=\n.Lgg_w1%=:\ns_waitcnt vmcnt(0)\nbuffer_store_dword v90, off, %[rs], s56 sc1\n.Lgg_go%=:\ns_add_u32 s44, s40, 0\n" \
"s_lshl_b32 s44, s44, 17\ns_add_u32 s44, s44, %[xn]\ns_lshr_b32 s45, s44, 8\ns_and_b32 s45, s45, 0x2000\ns_xor_b32 s41, s44, s45\ns_add_u32 s61, s41, %[xw]\ns_barrier\ns_add_u32 m0, s57, 0\n" \
"s_add_u32 s60, s61, 0\nbuffer_load_dwordx4 v0, %[rs], s60 offen offset:0 sc1 lds\nbuffer_load_dwordx4 v0, %[rs], s60 offen offset:1024 sc1 lds\nbuffer_load_dwordx4 v0, %[rs], s60 offen offset:2048 sc1 lds\nbuffer_load_dwordx4 v0, %[rs], s60 offen offset:3072 sc1 lds\ns_add_u32 m0, s57, 16384\ns_add_u32 s60, s61, 32768\nbuffer_load_dwordx4 v0, %[rs], s60 offen offset:0 sc1 lds\n" \
"buffer_load_dwordx4 v0, %[rs], s60 offen offset:1024 sc1 lds\nbuffer_load_dwordx4 v0, %[rs], s60 offen offset:2048 sc1 lds\nbuffer_load_dwordx4 v0, %[rs], s60 offen offset:3072 sc1 lds\ns_add_u32 m0, s57, 32768\ns_add_u32 s60, s61, 65536\nbuffer_load_dwordx4 v0, %[rs], s60 offen offset:0 sc1 lds\nbuffer_load_dwordx4 v0, %[rs], s60 offen offset:1024 sc1 lds\ns_lshl_b32 s44, s40, 19\n" \
"s_add_u32 s44, s44, %[ho]\ns_lshr_b32 s45, s44, 8\ns_and_b32 s45, s45, 0x2000\ns_xor_b32 s55, s44, s45\ns_waitcnt vmcnt(6)\ns_barrier\nds_read_b128 v[56:59], v2 offset:0\nds_read_b128 v[60:63], v2 offset:2048\n" \
"ds_read_b128 v[64:67], v2 offset:4096\ns_waitcnt lgkmcnt(2)\nv_mfma_f32_16x16x32_bf16 v[24:27], a[0:3], v[56:59], 0\nds_read_b128 v[68:71], v2 offset:6144\nds_read_b128 v[80:83], v1 offset:0\nv_mfma_f32_16x16x32_bf16 v[28:31], a[8:11], v[56:59], 0\nbuffer_load_dwordx4 v0, %[rs], s60 offen offset:2048 sc1 lds\nv_mfma_f32_16x16x32_bf16 v[32:35], a[16:19], v[56:59], 0\n" \
"v_mfma_f32_16x16x32_bf16 v[36:39], a[24:27], v[56:59], 0\nds_read_b128 v[72:75], v1 offset:1024\ns_waitcnt lgkmcnt(4)\nv_mfma_f32_16x16x32_bf16 v[24:27], a[4:7], v[60:63], v[24:27]\nds_read_b128 v[56:59], v2 offset:8192\nv_mfma_f32_16x16x32_bf16 v[28:31], a[12:15], v[60:63], v[28:31]\nbuffer_load_dwordx4 v0, %[rs], s60 offen offset:3072 sc1 lds\nv_mfma_f32_16x16x32_bf16 v[32:35], a[20:23], v[60:63], v[32:35]\n" \
"v_mfma_f32_16x16x32_bf16 v[36:39], a[28:31], v[60:63], v[36:39]\nds_read_b128 v[76:79], v1 offset:2048\ns_waitcnt lgkmcnt(5)\nv_mfma_f32_16x16x32_bf16 v[24:27], a[32:35], v[64:67], v[24:27]\nds_read_b128 v[60:63], v2 offset:10240\ns_waitcnt lgkmcnt(4)\nv_mfma_f32_16x16x32_bf16 v[28:31], v[80:83], v[64:67], v[28:31]\nbuffer_load_dwordx4 v[40:43], v5, %[rs], s41 offen offset:0 sc1\n" \
"v_mfma_f32_16x16x32_bf16 v[32:35], a[40:43], v[64:67], v[32:35]\nv_mfma_f32_16x16x32_bf16 v[36:39], a[48:51], v[64:67], v[36:39]\nds_read_b128 v[80:83], v1 offset:3072\nv_mfma_f32_16x16x32_bf16 v[24:27], a[36:39], v[68:71], v[24:27]\nds_read_b128 v[64:67], v2 offset:12288\ns_waitcnt lgkmcnt(5)\nv_mfma_f32_16x16x32_bf16 v[28:31], v[72:75], v[68:71], v[28:31]\nbuffer_load_dwordx4 v[44:47], v5, %[rs], s41 offen offset:256 sc1\n" \
"v_mfma_f32_16x16x32_bf16 v[32:35], a[44:47], v[68:71], v[32:35]\nv_mfma_f32_16x16x32_bf16 v[36:39], a[52:55], v[68:71], v[36:39]\nds_read_b128 v[72:75], v1 offset:4096\ns_waitcnt lgkmcnt(5)\nv_mfma_f32_16x16x32_bf16 v[24:27], a[56:59], v[56:59], v[24:27]\nds_read_b128 v[68:71], v2 offset:14336\nv_mfma_f32_16x16x32_bf16 v[28:31], a[64:67], v[56:59], v[28:31]\nbuffer_load_dwordx4 v[48:51], v5, %[rs], s41 offen offset:512 sc1\n" \
"s_waitcnt lgkmcnt(5)\nv_mfma_f32_16x16x32_bf16 v[32:35], v[76:79], v[56:59], v[32:35]\nv_mfma_f32_16x16x32_bf16 v[36:39], a[72:75], v[56:59], v[36:39]\nds_read_b128 v[76:79], v1 offset:5120\ns_waitcnt lgkmcnt(5)\nv_mfma_f32_16x16x32_bf16 v[24:27], a[60:63], v[60:63], v[24:27]\ns_waitcnt vmcnt(7)\ns_barrier\n" \
"ds_read_b128 v[56:59], v2 offset:16384\nv_mfma_f32_16x16x32_bf16 v[28:31], a[68:71], v[60:63], v[28:31]\nbuffer_load_dwordx4 v[52:55], v5, %[rs], s41 offen offset:768 sc1\ns_waitcnt lgkmcnt(5)\nv_mfma_f32_16x16x32_bf16 v[32:35], v[80:83], v[60:63], v[32:35]\nv_mfma_f32_16x16x32_bf16 v[36:39], a[76:79], v[60:63], v[36:39]\ns_waitcnt lgkmcnt(4)\nv_mfma_f32_16x16x32_bf16 v[24:27], a[80:83], v[64:67], v[24:27]\n" \
"ds_read_b128 v[60:63], v2 offset:18432\nv_mfma_f32_16x16x32_bf16 v[28:31], a[88:91], v[64:67], v[28:31]\ns_add_u32 m0, s57, 49152\ns_add_u32 s60, s61, 98304\nbuffer_load_dwordx4 v0, %[rs], s60 offen offset:0 sc1 lds\nv_mfma_f32_16x16x32_bf16 v[32:35], a[96:99], v[64:67], v[32:35]\ns_waitcnt lgkmcnt(4)\nv_mfma_f32_16x16x32_bf16 v[36:39], v[72:75], v[64:67], v[36:39]\n" \
"s_waitcnt lgkmcnt(3)\nv_mfma_f32_16x16x32_bf16 v[24:27], a[84:87], v[68:71], v[24:27]\nds_read_b128 v[64:67], v2 offset:20480\nv_mfma_f32_16x16x32_bf16 v[28:31], a[92:95], v[68:71], v[28:31]\nbuffer_load_dwordx4 v0, %[rs], s60 offen offset:1024 sc1 lds\nv_mfma_f32_16x16x32_bf16 v[32:35], a[100:103], v[68:71], v[32:35]\ns_waitcnt lgkmcnt(3)\nv_mfma_f32_16x16x32_bf16 v[36:39], v[76:79], v[68:71], v[36:39]\n" \
"ds_read_b128 v[76:79], v1 offset:6144\ns_waitcnt lgkmcnt(3)\nv_mfma_f32_16x16x32_bf16 v[24:27], a[104:107], v[56:59], v[24:27]\nds_read_b128 v[68:71], v2 offset:22528\nv_mfma_f32_16x16x32_bf16 v[28:31], a[112:115], v[56:59], v[28:31]\nbuffer_load_dwordx4 v0, %[rs], s60 offen offset:2048 sc1 lds\nv_mfma_f32_16x16x32_bf16 v[32:35], a[120:123], v[56:59], v[32:35]\nv_mfma_f32_16x16x32_bf16 v[36:39], a[128:131], v[56:59], v[36:39]\n" \
"ds_read_b128 v[80:83], v1 offset:7168\ns_waitcnt lgkmcnt(4)\nv_mfma_f32_16x16x32_bf16 v[24:27], a[108:111], v[60:63], v[24:27]\nds_read_b128 v[56:59], v2 offset:24576\nv_mfma_f32_16x16x32_bf16 v[28:31], a[116:119], v[60:63], v[28:31]\nbuffer_load_dwordx4 v0, %[rs], s60 offen offset:3072 sc1 lds\nv_mfma_f32_16x16x32_bf16 v[32:35], a[124:127], v[60:63], v[32:35]\nv_mfma_f32_16x16x32_bf16 v[36:39], a[132:135], v[60:63], v[36:39]\n" \
"ds_read_b128 v[72:75], v1 offset:8192\ns_waitcnt lgkmcnt(5)\nv_mfma_f32_16x16x32_bf16 v[24:27], a[136:139], v[64:67], v[24:27]\nds_read_b128 v[60:63], v2 offset:26624\ns_waitcnt lgkmcnt(5)\nv_mfma_f32_16x16x32_bf16 v[28:31], v[76:79], v[64:67], v[28:31]\nv_mfma_f32_16x16x32_bf16 v[32:35], a[144:147], v[64:67], v[32:35]\nv_mfma_f32_16x16x32_bf16 v[36:39], a[152:155], v[64:67], v[36:39]\n" \
"ds_read_b128 v[76:79], v1 offset:9216\ns_waitcnt lgkmcnt(5)\nv_mfma_f32_16x16x32_bf16 v[24:27], a[140:143], v[68:71], v[24:27]\nds_read_b128 v[64:67], v2 offset:28672\ns_waitcnt lgkmcnt(5)\nv_mfma_f32_16x16x32_bf16 v[28:31], v[80:83], v[68:71], v[28:31]\nv_mfma_f32_16x16x32_bf16 v[32:35], a[148:151], v[68:71], v[32:35]\nv_mfma_f32_16x16x32_bf16 v[36:39], a[156:159], v[68:71], v[36:39]\n" \
"ds_read_b128 v[80:83], v1 offset:10240\ns_waitcnt lgkmcnt(5)\nv_mfma_f32_16x16x32_bf16 v[24:27], a[160:163], v[56:59], v[24:27]\nds_read_b128 v[68:71], v2 offset:30720\nv_mfma_f32_16x16x32_bf16 v[28:31], a[168:171], v[56:59], v[28:31]\ns_waitcnt lgkmcnt(5)\nv_mfma_f32_16x16x32_bf16 v[32:35], v[72:75], v[56:59], v[32:35]\nv_mfma_f32_16x16x32_bf16 v[36:39], a[176:179], v[56:59], v[36:39]\n" \
"ds_read_b128 v[72:75], v1 offset:11264\ns_waitcnt lgkmcnt(5)\nv_mfma_f32_16x16x32_bf16 v[24:27], a[164:167], v[60:63], v[24:27]\ns_waitcnt vmcnt(8)\ns_barrier\nds_read_b128 v[56:59], v2 offset:32768\nv_mfma_f32_16x16x32_bf16 v[28:31], a[172:175], v[60:63], v[28:31]\ns_waitcnt lgkmcnt(5)\n" \
"v_mfma_f32_16x16x32_bf16 v[32:35], v[76:79], v[60:63], v[32:35]\nv_mfma_f32_16x16x32_bf16 v[36:39], a[180:183], v[60:63], v[36:39]\ns_waitcnt lgkmcnt(4)\nv_mfma_f32_16x16x32_bf16 v[24:27], a[184:187], v[64:67], v[24:27]\nds_read_b128 v[60:63], v2 offset:34816\ns_waitcnt vmcnt(4)\nv_add_f32 v84, v40, v44\nv_mfma_f32_16x16x32_bf16 v[28:31], a[192:195], v[64:67], v[28:31]\n" \
"v_add_f32 v85, v48, v52\nv_add_f32 v84, v84, v85\nv_mfma_f32_16x16x32_bf16 v[32:35], a[200:203], v[64:67], v[32:35]\nv_mov_b32 v85, v84\ns_nop 0\ns_nop 0\nv_permlane16_swap_b32 v84, v85\ns_waitcnt lgkmcnt(4)\n" \
"v_mfma_f32_16x16x32_bf16 v[36:39], v[80:83], v[64:67], v[36:39]\nv_add_f32 v84, v84, v85\nv_mov_b32 v85, v84\ns_waitcnt lgkmcnt(3)\nv_mfma_f32_16x16x32_bf16 v[24:27], a[188:191], v[68:71], v[24:27]\nds_read_b128 v[64:67], v2 offset:36864\ns_nop 0\nv_permlane32_swap_b32 v84, v85\n" \
"v_mfma_f32_16x16x32_bf16 v[28:31], a[196:199], v[68:71], v[28:31]\nv_add_f32 v84, v84, v85\nv_mul_f32 v84, 0xbd800000, v84\nv_mfma_f32_16x16x32_bf16 v[32:35], a[204:207], v[68:71], v[32:35]\nv_add_f32 v40, v40, v84\nv_mul_f32 v40, 0x41000000, v40\ns_waitcnt lgkmcnt(3)\nv_mfma_f32_16x16x32_bf16 v[36:39], v[72:75], v[68:71], v[36:39]\n" \
"ds_read_b128 v[72:75], v1 offset:12288\nv_fma_f32 v41, v40, v40, v41\nv_add_f32 v44, v44, v84\ns_waitcnt lgkmcnt(3)\nv_mfma_f32_16x16x32_bf16 v[24:27], a[208:211], v[56:59], v[24:27]\nds_read_b128 v[68:71], v2 offset:38912\nv_mul_f32 v44, 0x41000000, v44\nv_mfma_f32_16x16x32_bf16 v[28:31], a[216:219], v[56:59], v[28:31]\n" \
"v_fma_f32 v45, v44, v44, v45\nv_add_f32 v48, v48, v84\nv_mfma_f32_16x16x32_bf16 v[32:35], a[224:227], v[56:59], v[32:35]\nv_mul_f32 v48, 0x41000000, v48\nv_fma_f32 v49, v48, v48, v49\nv_mfma_f32_16x16x32_bf16 v[36:39], a[232:235], v[56:59], v[36:39]\nds_read_b128 v[76:79], v1 offset:13312\nv_add_f32 v52, v52, v84\n" \
"v_mul_f32 v52, 0x41000000, v52\ns_waitcnt lgkmcnt(4)\nv_mfma_f32_16x16x32_bf16 v[24:27], a[212:215], v[60:63], v[24:27]\nds_read_b128 v[56:59], v2 offset:40960\nv_fma_f32 v53, v52, v52, v53\nv_mfma_f32_16x16x32_bf16 v[28:31], a[220:223], v[60:63], v[28:31]\nv_add_f32 v85, v41, v45\nv_add_f32 v86, v49, v53\n" \
"v_mfma_f32_16x16x32_bf16 v[32:35], a[228:231], v[60:63], v[32:35]\nv_add_f32 v85, v85, v86\nv_mov_b32 v86, v85\nv_mfma_f32_16x16x32_bf16 v[36:39], a[236:239], v[60:63], v[36:39]\nds_read_b128 v[80:83], v1 offset:14336\ns_nop 0\nv_permlane16_swap_b32 v85, v86\ns_waitcnt lgkmcnt(5)\n" \
"v_mfma_f32_16x16x32_bf16 v[24:27], a[240:243], v[64:67], v[24:27]\nds_read_b128 v[60:63], v2 offset:43008\nv_add_f32 v85, v85, v86\ns_waitcnt lgkmcnt(5)\nv_mfma_f32_16x16x32_bf16 v[28:31], v[72:75], v[64:67], v[28:31]\nv_mov_b32 v86, v85\ns_nop 0\ns_nop 0\n" \
"v_permlane32_swap_b32 v85, v86\nv_mfma_f32_16x16x32_bf16 v[32:35], a[248:251], v[64:67], v[32:35]\nv_add_f32 v85, v85, v86\nv_mul_f32 v85, 0x3a800000, v85\nv_mfma_f32_16x16x32_bf16 v[36:39], v[96:99], v[64:67], v[36:39]\nds_read_b128 v[72:75], v1 offset:15360\nv_cvt_pk_bf16_f32 v85, v85, v85\nv_and_b32 v85, 0xffff0000, v85\n" \
"s_waitcnt lgkmcnt(5)\nv_mfma_f32_16x16x32_bf16 v[24:27], a[244:247], v[68:71], v[24:27]\nds_read_b128 v[64:67], v2 offset:45056\nv_add_f32 v85, 0x3727c5ac, v85\ns_waitcnt lgkmcnt(5)\nv_mfma_f32_16x16x32_bf16 v[28:31], v[76:79], v[68:71], v[28:31]\nv_cvt_pk_bf16_f32 v85, v85, v85\nv_and_b32 v85, 0xffff0000, v85\n" \
"v_mfma_f32_16x16x32_bf16 v[32:35], a[252:255], v[68:71], v[32:35]\nv_sqrt_f32 v85, v85\nv_mfma_f32_16x16x32_bf16 v[36:39], v[100:103], v[68:71], v[36:39]\nds_read_b128 v[76:79], v1 offset:16384\nv_cvt_pk_bf16_f32 v84, v84, v84\nv_and_b32 v84, 0xffff0000, v84\ns_waitcnt lgkmcnt(5)\nv_mfma_f32_16x16x32_bf16 v[24:27], v[104:107], v[56:59], v[24:27]\n" \
"ds_read_b128 v[68:71], v2 offset:47104\nv_cvt_pk_bf16_f32 v85, v85, v85\nv_mfma_f32_16x16x32_bf16 v[28:31], v[112:115], v[56:59], v[28:31]\nv_and_b32 v85, 0xffff0000, v85\nv_mul_f32 v86, s58, v85\ns_waitcnt lgkmcnt(5)\nv_mfma_f32_16x16x32_bf16 v[32:35], v[80:83], v[56:59], v[32:35]\nv_rcp_f32 v85, v85\n" \
"v_mfma_f32_16x16x32_bf16 v[36:39], v[120:123], v[56:59], v[36:39]\nds_read_b128 v[80:83], v1 offset:17408\ns_nop 0\nv_mul_f32 v85, s58, v85\nv_add_f32 v87, v42, v46\ns_waitcnt lgkmcnt(5)\nv_mfma_f32_16x16x32_bf16 v[24:27], v[108:111], v[60:63], v[24:27]\ns_waitcnt vmcnt(0)\n" \
"s_barrier\nds_read_b128 v[56:59], v2 offset:49152\nv_mfma_f32_16x16x32_bf16 v[28:31], v[116:119], v[60:63], v[28:31]\nv_add_f32 v88, v50, v54\nv_add_f32 v87, v87, v88\ns_waitcnt lgkmcnt(5)\nv_mfma_f32_16x16x32_bf16 v[32:35], v[72:75], v[60:63], v[32:35]\nv_mov_b32 v88, v87\n" \
"s_nop 0\ns_nop 0\nv_permlane16_swap_b32 v87, v88\nv_mfma_f32_16x16x32_bf16 v[36:39], v[124:127], v[60:63], v[36:39]\nv_add_f32 v87, v87, v88\nv_mov_b32 v88, v87\ns_waitcnt lgkmcnt(4)\nv_mfma_f32_16x16x32_bf16 v[24:27], v[128:131], v[64:67], v[24:27]\n" \
"ds_read_b128 v[60:63], v2 offset:51200\ns_nop 0\nv_permlane32_swap_b32 v87, v88\nv_mfma_f32_16x16x32_bf16 v[28:31], v[136:139], v[64:67], v[28:31]\nv_add_f32 v87, v87, v88\nv_mul_f32 v87, 0xbd800000, v87\nv_mfma_f32_16x16x32_bf16 v[32:35], v[144:147], v[64:67], v[32:35]\nv_add_f32 v42, v42, v87\n" \
"v_mul_f32 v42, 0x41000000, v42\ns_waitcnt lgkmcnt(4)\nv_mfma_f32_16x16x32_bf16 v[36:39], v[76:79], v[64:67], v[36:39]\nv_fma_f32 v43, v42, v42, v43\nv_add_f32 v46, v46, v87\ns_waitcnt lgkmcnt(3)\nv_mfma_f32_16x16x32_bf16 v[24:27], v[132:135], v[68:71], v[24:27]\nds_read_b128 v[64:67], v2 offset:53248\n" \
"v_mul_f32 v46, 0x41000000, v46\nv_mfma_f32_16x16x32_bf16 v[28:31], v[140:143], v[68:71], v[28:31]\nv_fma_f32 v47, v46, v46, v47\nv_add_f32 v50, v50, v87\nv_mfma_f32_16x16x32_bf16 v[32:35], v[148:151], v[68:71], v[32:35]\nv_mul_f32 v50, 0x41000000, v50\nv_fma_f32 v51, v50, v50, v51\ns_waitcnt lgkmcnt(3)\n" \
"v_mfma_f32_16x16x32_bf16 v[36:39], v[80:83], v[68:71], v[36:39]\nds_read_b128 v[80:83], v1 offset:18432\nv_add_f32 v54, v54, v87\nv_mul_f32 v54, 0x41000000, v54\ns_waitcnt lgkmcnt(3)\nv_mfma_f32_16x16x32_bf16 v[24:27], v[152:155], v[56:59], v[24:27]\nds_read_b128 v[68:71], v2 offset:55296\nv_fma_f32 v55, v54, v54, v55\n" \
"v_mfma_f32_16x16x32_bf16 v[28:31], v[160:163], v[56:59], v[28:31]\nv_add_f32 v88, v43, v47\nv_add_f32 v89, v51, v55\nv_mfma_f32_16x16x32_bf16 v[32:35], v[168:171], v[56:59], v[32:35]\nv_add_f32 v88, v88, v89\nv_mov_b32 v89, v88\nv_mfma_f32_16x16x32_bf16 v[36:39], v[176:179], v[56:59], v[36:39]\nds_read_b128 v[72:75], v1 offset:19456\n" \
"s_nop 0\nv_permlane16_swap_b32 v88, v89\ns_waitcnt lgkmcnt(4)\nv_mfma_f32_16x16x32_bf16 v[24:27], v[156:159], v[60:63], v[24:27]\nds_read_b128 v[56:59], v2 offset:57344\nv_add_f32 v88, v88, v89\nv_mfma_f32_16x16x32_bf16 v[28:31], v[164:167], v[60:63], v[28:31]\nv_mov_b32 v89, v88\n" \
"s_nop 0\ns_nop 0\nv_permlane32_swap_b32 v88, v89\nv_mfma_f32_16x16x32_bf16 v[32:35], v[172:175], v[60:63], v[32:35]\nv_add_f32 v88, v88, v89\nv_mul_f32 v88, 0x3a800000, v88\nv_mfma_f32_16x16x32_bf16 v[36:39], v[180:183], v[60:63], v[36:39]\nds_read_b128 v[76:79], v1 offset:20480\n" \
"v_cvt_pk_bf16_f32 v88, v88, v88\nv_and_b32 v88, 0xffff0000, v88\ns_waitcnt lgkmcnt(5)\nv_mfma_f32_16x16x32_bf16 v[24:27], v[184:187], v[64:67], v[24:27]\nds_read_b128 v[60:63], v2 offset:59392\nv_add_f32 v88, 0x3727c5ac, v88\ns_waitcnt lgkmcnt(5)\nv_mfma_f32_16x16x32_bf16 v[28:31], v[80:83], v[64:67], v[28:31]\n" \
"v_cvt_pk_bf16_f32 v88, v88, v88\nv_and_b32 v88, 0xffff0000, v88\nv_mfma_f32_16x16x32_bf16 v[32:35], v[192:195], v[64:67], v[32:35]\nv_sqrt_f32 v88, v88\nv_mfma_f32_16x16x32_bf16 v[36:39], v[200:203], v[64:67], v[36:39]\nds_read_b128 v[80:83], v1 offset:21504\nv_cvt_pk_bf16_f32 v87, v87, v87\nv_and_b32 v87, 0xffff0000, v87\n" \
"s_waitcnt lgkmcnt(5)\nv_mfma_f32_16x16x32_bf16 v[24:27], v[188:191], v[68:71], v[24:27]\nds_read_b128 v[64:67], v2 offset:61440\nv_cvt_pk_bf16_f32 v88, v88, v88\ns_waitcnt lgkmcnt(5)\nv_mfma_f32_16x16x32_bf16 v[28:31], v[72:75], v[68:71], v[28:31]\nv_and_b32 v88, 0xffff0000, v88\nv_mul_f32 v89, s58, v88\n" \
"v_mfma_f32_16x16x32_bf16 v[32:35], v[196:199], v[68:71], v[32:35]\nv_rcp_f32 v88, v88\nv_mfma_f32_16x16x32_bf16 v[36:39], v[204:207], v[68:71], v[36:39]\nds_read_b128 v[72:75], v1 offset:22528\ns_nop 0\nv_mul_f32 v88, s58, v88\ns_waitcnt lgkmcnt(5)\nv_mfma_f32_16x16x32_bf16 v[24:27], v[208:211], v[56:59], v[24:27]\n" \
"ds_read_b128 v[68:71], v2 offset:63488\nv_mfma_f32_16x16x32_bf16 v[28:31], v[216:219], v[56:59], v[28:31]\ns_waitcnt lgkmcnt(5)\nv_mfma_f32_16x16x32_bf16 v[32:35], v[76:79], v[56:59], v[32:35]\nv_mfma_f32_16x16x32_bf16 v[36:39], v[224:227], v[56:59], v[36:39]\nds_read_b128 v[76:79], v1 offset:23552\ns_waitcnt lgkmcnt(5)\nv_mfma_f32_16x16x32_bf16 v[24:27], v[212:215], v[60:63], v[24:27]\n" \
"ds_read_b128 v[56:59], v2 offset:256\nv_mfma_f32_16x16x32_bf16 v[28:31], v[220:223], v[60:63], v[28:31]\ns_waitcnt lgkmcnt(5)\nv_mfma_f32_16x16x32_bf16 v[32:35], v[80:83], v[60:63], v[32:35]\nv_mfma_f32_16x16x32_bf16 v[36:39], v[228:231], v[60:63], v[36:39]\ns_waitcnt lgkmcnt(4)\nv_mfma_f32_16x16x32_bf16 v[24:27], v[232:235], v[64:67], v[24:27]\nds_read_b128 v[60:63], v2 offset:2304\n" \
"v_mfma_f32_16x16x32_bf16 v[28:31], v[240:243], v[64:67], v[28:31]\nv_mfma_f32_16x16x32_bf16 v[32:35], v[248:251], v[64:67], v[32:35]\ns_waitcnt lgkmcnt(4)\nv_mfma_f32_16x16x32_bf16 v[36:39], v[72:75], v[64:67], v[36:39]\ns_waitcnt lgkmcnt(3)\nv_mfma_f32_16x16x32_bf16 v[24:27], v[236:239], v[68:71], v[24:27]\nds_read_b128 v[64:67], v2 offset:4352\nv_mfma_f32_16x16x32_bf16 v[28:31], v[244:247], v[68:71], v[28:31]\n" \
"v_mfma_f32_16x16x32_bf16 v[32:35], v[252:255], v[68:71], v[32:35]\ns_waitcnt lgkmcnt(3)\nv_mfma_f32_16x16x32_bf16 v[36:39], v[76:79], v[68:71], v[36:39]\nds_read_b128 v[76:79], v1 offset:0\ns_waitcnt lgkmcnt(3)\nv_mfma_f32_16x16x32_bf16 v[40:43], a[0:3], v[56:59], 0\nds_read_b128 v[68:71], v2 offset:6400\nv_mfma_f32_16x16x32_bf16 v[44:47], a[8:11], v[56:59], 0\n" \
"v_mfma_f32_16x16x32_bf16 v[48:51], a[16:19], v[56:59], 0\nv_mfma_f32_16x16x32_bf16 v[52:55], a[24:27], v[56:59], 0\nds_read_b128 v[80:83], v1 offset:1024\nv_fma_f32 v24, v8, v84, v24\nv_fma_f32 v25, v9, v84, v25\ns_waitcnt lgkmcnt(4)\nv_mfma_f32_16x16x32_bf16 v[40:43], a[4:7], v[60:63], v[40:43]\nds_read_b128 v[56:59], v2 offset:8448\n" \
"v_fma_f32 v32, v16, v84, v32\nv_mfma_f32_16x16x32_bf16 v[44:47], a[12:15], v[60:63], v[44:47]\nv_fma_f32 v33, v17, v84, v33\nv_mul_f32 v24, v24, v85\nv_mfma_f32_16x16x32_bf16 v[48:51], a[20:23], v[60:63], v[48:51]\nv_mul_f32 v25, v25, v85\nv_fma_f32 v91, v24, v24, s59\nv_mfma_f32_16x16x32_bf16 v[52:55], a[28:31], v[60:63], v[52:55]\n" \
"ds_read_b128 v[72:75], v1 offset:2048\nv_fma_f32 v92, v25, v25, s59\nv_mul_f32 v91, v91, v24\ns_waitcnt lgkmcnt(5)\nv_mfma_f32_16x16x32_bf16 v[40:43], a[32:35], v[64:67], v[40:43]\nds_read_b128 v[60:63], v2 offset:10496\nv_mul_f32 v92, v92, v25\ns_waitcnt lgkmcnt(5)\n" \
"v_mfma_f32_16x16x32_bf16 v[44:47], v[76:79], v[64:67], v[44:47]\nv_exp_f32 v91, v91\nv_mfma_f32_16x16x32_bf16 v[48:51], a[40:43], v[64:67], v[48:51]\nv_exp_f32 v92, v92\nv_mfma_f32_16x16x32_bf16 v[52:55], a[48:51], v[64:67], v[52:55]\nds_read_b128 v[76:79], v1 offset:3072\nv_mul_f32 v24, v24, v32\nv_mul_f32 v25, v25, v33\n" \
"s_waitcnt lgkmcnt(5)\nv_mfma_f32_16x16x32_bf16 v[40:43], a[36:39], v[68:71], v[40:43]\nds_read_b128 v[64:67], v2 offset:12544\nv_fma_f32 v91, v91, v86, v86\ns_waitcnt lgkmcnt(5)\nv_mfma_f32_16x16x32_bf16 v[44:47], v[80:83], v[68:71], v[44:47]\nv_fma_f32 v92, v92, v86, v86\nv_rcp_f32 v91, v91\n" \
"v_mfma_f32_16x16x32_bf16 v[48:51], a[44:47], v[68:71], v[48:51]\nv_rcp_f32 v92, v92\nv_mfma_f32_16x16x32_bf16 v[52:55], a[52:55], v[68:71], v[52:55]\nds_read_b128 v[80:83], v1 offset:4096\nv_mul_f32 v24, v24, v91\nv_mul_f32 v25, v25, v92\ns_waitcnt lgkmcnt(5)\nv_mfma_f32_16x16x32_bf16 v[40:43], a[56:59], v[56:59], v[40:43]\n" \
"ds_read_b128 v[68:71], v2 offset:14592\nv_cvt_pk_bf16_f32 v32, v24, v25\nv_mfma_f32_16x16x32_bf16 v[44:47], a[64:67], v[56:59], v[44:47]\nv_fma_f32 v26, v10, v84, v26\nv_fma_f32 v27, v11, v84, v27\ns_waitcnt lgkmcnt(5)\nv_mfma_f32_16x16x32_bf16 v[48:51], v[72:75], v[56:59], v[48:51]\nv_fma_f32 v34, v18, v84, v34\n" \
"v_fma_f32 v35, v19, v84, v35\nv_mfma_f32_16x16x32_bf16 v[52:55], a[72:75], v[56:59], v[52:55]\nds_read_b128 v[72:75], v1 offset:5120\nv_mul_f32 v26, v26, v85\nv_mul_f32 v27, v27, v85\ns_waitcnt lgkmcnt(5)\nv_mfma_f32_16x16x32_bf16 v[40:43], a[60:63], v[60:63], v[40:43]\nds_read_b128 v[56:59], v2 offset:16640\n" \
"v_fma_f32 v91, v26, v26, s59\nv_mfma_f32_16x16x32_bf16 v[44:47], a[68:71], v[60:63], v[44:47]\nv_fma_f32 v92, v27, v27, s59\nv_mul_f32 v91, v91, v26\ns_waitcnt lgkmcnt(5)\nv_mfma_f32_16x16x32_bf16 v[48:51], v[76:79], v[60:63], v[48:51]\nv_mul_f32 v92, v92, v27\nv_exp_f32 v91, v91\n" \
"v_mfma_f32_16x16x32_bf16 v[52:55], a[76:79], v[60:63], v[52:55]\nv_exp_f32 v92, v92\ns_waitcnt lgkmcnt(4)\nv_mfma_f32_16x16x32_bf16 v[40:43], a[80:83], v[64:67], v[40:43]\nds_read_b128 v[60:63], v2 offset:18688\nv_mul_f32 v26, v26, v34\nv_mfma_f32_16x16x32_bf16 v[44:47], a[88:91], v[64:67], v[44:47]\nv_mul_f32 v27, v27, v35\n" \
"v_fma_f32 v91, v91, v86, v86\nv_mfma_f32_16x16x32_bf16 v[48:51], a[96:99], v[64:67], v[48:51]\nv_fma_f32 v92, v92, v86, v86\nv_rcp_f32 v91, v91\ns_waitcnt lgkmcnt(4)\nv_mfma_f32_16x16x32_bf16 v[52:55], v[80:83], v[64:67], v[52:55]\nv_rcp_f32 v92, v92\ns_waitcnt lgkmcnt(3)\n" \
"v_mfma_f32_16x16x32_bf16 v[40:43], a[84:87], v[68:71], v[40:43]\nds_read_b128 v[64:67], v2 offset:20736\nv_mul_f32 v26, v26, v91\nv_mfma_f32_16x16x32_bf16 v[44:47], a[92:95], v[68:71], v[44:47]\nv_mul_f32 v27, v27, v92\nv_cvt_pk_bf16_f32 v33, v26, v27\nv_mfma_f32_16x16x32_bf16 v[48:51], a[100:103], v[68:71], v[48:51]\nv_fma_f32 v28, v12, v84, v28\n" \
"v_fma_f32 v29, v13, v84, v29\ns_waitcnt lgkmcnt(3)\nv_mfma_f32_16x16x32_bf16 v[52:55], v[72:75], v[68:71], v[52:55]\nds_read_b128 v[72:75], v1 offset:6144\nv_fma_f32 v36, v20, v84, v36\nv_fma_f32 v37, v21, v84, v37\ns_waitcnt lgkmcnt(3)\nv_mfma_f32_16x16x32_bf16 v[40:43], a[104:107], v[56:59], v[40:43]\n" \
"ds_read_b128 v[68:71], v2 offset:22784\nv_mul_f32 v28, v28, v85\nv_mfma_f32_16x16x32_bf16 v[44:47], a[112:115], v[56:59], v[44:47]\nv_mul_f32 v29, v29, v85\nv_fma_f32 v91, v28, v28, s59\nv_mfma_f32_16x16x32_bf16 v[48:51], a[120:123], v[56:59], v[48:51]\nv_fma_f32 v92, v29, v29, s59\nv_mul_f32 v91, v91, v28\n" \
"v_mfma_f32_16x16x32_bf16 v[52:55], a[128:131], v[56:59], v[52:55]\nds_read_b128 v[76:79], v1 offset:7168\nv_mul_f32 v92, v92, v29\nv_exp_f32 v91, v91\ns_waitcnt lgkmcnt(4)\nv_mfma_f32_16x16x32_bf16 v[40:43], a[108:111], v[60:63], v[40:43]\nds_read_b128 v[56:59], v2 offset:24832\nv_exp_f32 v92, v92\n" \
"v_mfma_f32_16x16x32_bf16 v[44:47], a[116:119], v[60:63], v[44:47]\nv_mul_f32 v28, v28, v36\nv_mul_f32 v29, v29, v37\nv_mfma_f32_16x16x32_bf16 v[48:51], a[124:127], v[60:63], v[48:51]\nv_fma_f32 v91, v91, v86, v86\nv_fma_f32 v92, v92, v86, v86\nv_mfma_f32_16x16x32_bf16 v[52:55], a[132:135], v[60:63], v[52:55]\nds_read_b128 v[80:83], v1 offset:8192\n" \
"v_rcp_f32 v91, v91\ns_waitcnt lgkmcnt(5)\nv_mfma_f32_16x16x32_bf16 v[40:43], a[136:139], v[64:67], v[40:43]\nds_read_b128 v[60:63], v2 offset:26880\nv_rcp_f32 v92, v92\ns_waitcnt lgkmcnt(5)\nv_mfma_f32_16x16x32_bf16 v[44:47], v[72:75], v[64:67], v[44:47]\nv_mul_f32 v28, v28, v91\n" \
"v_mul_f32 v29, v29, v92\nv_mfma_f32_16x16x32_bf16 v[48:51], a[144:147], v[64:67], v[48:51]\nv_cvt_pk_bf16_f32 v34, v28, v29\nv_fma_f32 v30, v14, v84, v30\nv_mfma_f32_16x16x32_bf16 v[52:55], a[152:155], v[64:67], v[52:55]\nds_read_b128 v[72:75], v1 offset:9216\nv_fma_f32 v31, v15, v84, v31\nv_fma_f32 v38, v22, v84, v38\n" \
"s_waitcnt lgkmcnt(5)\nv_mfma_f32_16x16x32_bf16 v[40:43], a[140:143], v[68:71], v[40:43]\nds_read_b128 v[64:67], v2 offset:28928\nv_fma_f32 v39, v23, v84, v39\ns_waitcnt lgkmcnt(5)\nv_mfma_f32_16x16x32_bf16 v[44:47], v[76:79], v[68:71], v[44:47]\nv_mul_f32 v30, v30, v85\nv_mul_f32 v31, v31, v85\n" \
"v_mfma_f32_16x16x32_bf16 v[48:51], a[148:151], v[68:71], v[48:51]\nv_fma_f32 v91, v30, v30, s59\nv_fma_f32 v92, v31, v31, s59\nv_mfma_f32_16x16x32_bf16 v[52:55], a[156:159], v[68:71], v[52:55]\nds_read_b128 v[76:79], v1 offset:10240\nv_mul_f32 v91, v91, v30\nv_mul_f32 v92, v92, v31\ns_waitcnt lgkmcnt(5)\n" \
"v_mfma_f32_16x16x32_bf16 v[40:43], a[160:163], v[56:59], v[40:43]\nds_read_b128 v[68:71], v2 offset:30976\nv_exp_f32 v91, v91\nv_mfma_f32_16x16x32_bf16 v[44:47], a[168:171], v[56:59], v[44:47]\nv_exp_f32 v92, v92\ns_waitcnt lgkmcnt(5)\nv_mfma_f32_16x16x32_bf16 v[48:51], v[80:83], v[56:59], v[48:51]\nv_mul_f32 v30, v30, v38\n" \
"v_mul_f32 v31, v31, v39\nv_mfma_f32_16x16x32_bf16 v[52:55], a[176:179], v[56:59], v[52:55]\nds_read_b128 v[80:83], v1 offset:11264\nv_fma_f32 v91, v91, v86, v86\nv_fma_f32 v92, v92, v86, v86\ns_waitcnt lgkmcnt(5)\nv_mfma_f32_16x16x32_bf16 v[40:43], a[164:167], v[60:63], v[40:43]\nds_read_b128 v[56:59], v2 offset:33024\n" \
"v_rcp_f32 v91, v91\nv_mfma_f32_16x16x32_bf16 v[44:47], a[172:175], v[60:63], v[44:47]\nv_rcp_f32 v92, v92\ns_waitcnt lgkmcnt(5)\nv_mfma_f32_16x16x32_bf16 v[48:51], v[72:75], v[60:63], v[48:51]\nv_mul_f32 v30, v30, v91\nv_mul_f32 v31, v31, v92\nv_mfma_f32_16x16x32_bf16 v[52:55], a[180:183], v[60:63], v[52:55]\n" \
"v_cvt_pk_bf16_f32 v35, v30, v31\nbuffer_store_dwordx4 v[32:35], v4, %[rs], s55 offen offset:0 sc1\ns_waitcnt lgkmcnt(4)\nv_mfma_f32_16x16x32_bf16 v[40:43], a[184:187], v[64:67], v[40:43]\nds_read_b128 v[60:63], v2 offset:35072\nv_mfma_f32_16x16x32_bf16 v[44:47], a[192:195], v[64:67], v[44:47]\nv_mfma_f32_16x16x32_bf16 v[48:51], a[200:203], v[64:67], v[48:51]\ns_waitcnt lgkmcnt(4)\n" \
"v_mfma_f32_16x16x32_bf16 v[52:55], v[76:79], v[64:67], v[52:55]\ns_waitcnt lgkmcnt(3)\nv_mfma_f32_16x16x32_bf16 v[40:43], a[188:191], v[68:71], v[40:43]\nds_read_b128 v[64:67], v2 offset:37120\nv_mfma_f32_16x16x32_bf16 v[44:47], a[196:199], v[68:71], v[44:47]\nv_mfma_f32_16x16x32_bf16 v[48:51], a[204:207], v[68:71], v[48:51]\ns_waitcnt lgkmcnt(3)\nv_mfma_f32_16x16x32_bf16 v[52:55], v[80:83], v[68:71], v[52:55]\n" \
"ds_read_b128 v[80:83], v1 offset:12288\ns_waitcnt lgkmcnt(3)\nv_mfma_f32_16x16x32_bf16 v[40:43], a[208:211], v[56:59], v[40:43]\nds_read_b128 v[68:71], v2 offset:39168\nv_mfma_f32_16x16x32_bf16 v[44:47], a[216:219], v[56:59], v[44:47]\nv_mfma_f32_16x16x32_bf16 v[48:51], a[224:227], v[56:59], v[48:51]\nv_mfma_f32_16x16x32_bf16 v[52:55], a[232:235], v[56:59], v[52:55]\nds_read_b128 v[72:75], v1 offset:13312\n" \
"s_waitcnt lgkmcnt(4)\nv_mfma_f32_16x16x32_bf16 v[40:43], a[212:215], v[60:63], v[40:43]\nds_read_b128 v[56:59], v2 offset:41216\nv_mfma_f32_16x16x32_bf16 v[44:47], a[220:223], v[60:63], v[44:47]\nv_mfma_f32_16x16x32_bf16 v[48:51], a[228:231], v[60:63], v[48:51]\nv_mfma_f32_16x16x32_bf16 v[52:55], a[236:239], v[60:63], v[52:55]\nds_read_b128 v[76:79], v1 offset:14336\ns_waitcnt lgkmcnt(5)\n" \
"v_mfma_f32_16x16x32_bf16 v[40:43], a[240:243], v[64:67], v[40:43]\nds_read_b128 v[60:63], v2 offset:43264\ns_waitcnt lgkmcnt(5)\nv_mfma_f32_16x16x32_bf16 v[44:47], v[80:83], v[64:67], v[44:47]\nv_mfma_f32_16x16x32_bf16 v[48:51], a[248:251], v[64:67], v[48:51]\nv_mfma_f32_16x16x32_bf16 v[52:55], v[96:99], v[64:67], v[52:55]\nds_read_b128 v[80:83], v1 offset:15360\ns_waitcnt lgkmcnt(5)\n" \
"v_mfma_f32_16x16x32_bf16 v[40:43], a[244:247], v[68:71], v[40:43]\nds_read_b128 v[64:67], v2 offset:45312\ns_waitcnt lgkmcnt(5)\nv_mfma_f32_16x16x32_bf16 v[44:47], v[72:75], v[68:71], v[44:47]\nv_mfma_f32_16x16x32_bf16 v[48:51], a[252:255], v[68:71], v[48:51]\nv_mfma_f32_16x16x32_bf16 v[52:55], v[100:103], v[68:71], v[52:55]\nds_read_b128 v[72:75], v1 offset:16384\ns_waitcnt lgkmcnt(5)\n" \
"v_mfma_f32_16x16x32_bf16 v[40:43], v[104:107], v[56:59], v[40:43]\nds_read_b128 v[68:71], v2 offset:47360\nv_mfma_f32_16x16x32_bf16 v[44:47], v[112:115], v[56:59], v[44:47]\ns_waitcnt lgkmcnt(5)\nv_mfma_f32_16x16x32_bf16 v[48:51], v[76:79], v[56:59], v[48:51]\nv_mfma_f32_16x16x32_bf16 v[52:55], v[120:123], v[56:59], v[52:55]\nds_read_b128 v[76:79], v1 offset:17408\ns_waitcnt lgkmcnt(5)\n" \
"v_mfma_f32_16x16x32_bf16 v[40:43], v[108:111], v[60:63], v[40:43]\nds_read_b128 v[56:59], v2 offset:49408\nv_mfma_f32_16x16x32_bf16 v[44:47], v[116:119], v[60:63], v[44:47]\ns_waitcnt lgkmcnt(5)\nv_mfma_f32_16x16x32_bf16 v[48:51], v[80:83], v[60:63], v[48:51]\nv_mfma_f32_16x16x32_bf16 v[52:55], v[124:127], v[60:63], v[52:55]\ns_waitcnt lgkmcnt(4)\nv_mfma_f32_16x16x32_bf16 v[40:43], v[128:131], v[64:67], v[40:43]\n" \
"ds_read_b128 v[60:63], v2 offset:51456\nv_mfma_f32_16x16x32_bf16 v[44:47], v[136:139], v[64:67], v[44:47]\nv_mfma_f32_16x16x32_bf16 v[48:51], v[144:147], v[64:67], v[48:51]\ns_waitcnt lgkmcnt(4)\nv_mfma_f32_16x16x32_bf16 v[52:55], v[72:75], v[64:67], v[52:55]\ns_waitcnt lgkmcnt(3)\nv_mfma_f32_16x16x32_bf16 v[40:43], v[132:135], v[68:71], v[40:43]\nds_read_b128 v[64:67], v2 offset:53504\n" \
"v_mfma_f32_16x16x32_bf16 v[44:47], v[140:143], v[68:71], v[44:47]\nv_mfma_f32_16x16x32_bf16 v[48:51], v[148:151], v[68:71], v[48:51]\ns_waitcnt lgkmcnt(3)\nv_mfma_f32_16x16x32_bf16 v[52:55], v[76:79], v[68:71], v[52:55]\nds_read_b128 v[76:79], v1 offset:18432\ns_waitcnt lgkmcnt(3)\nv_mfma_f32_16x16x32_bf16 v[40:43], v[152:155], v[56:59], v[40:43]\nds_read_b128 v[68:71], v2 offset:55552\n" \
"v_mfma_f32_16x16x32_bf16 v[44:47], v[160:163], v[56:59], v[44:47]\nv_mfma_f32_16x16x32_bf16 v[48:51], v[168:171], v[56:59], v[48:51]\nv_mfma_f32_16x16x32_bf16 v[52:55], v[176:179], v[56:59], v[52:55]\nds_read_b128 v[80:83], v1 offset:19456\ns_waitcnt lgkmcnt(4)\nv_mfma_f32_16x16x32_bf16 v[40:43], v[156:159], v[60:63], v[40:43]\nds_read_b128 v[56:59], v2 offset:57600\nv_mfma_f32_16x16x32_bf16 v[44:47], v[164:167], v[60:63], v[44:47]\n" \
"v_mfma_f32_16x16x32_bf16 v[48:51], v[172:175], v[60:63], v[48:51]\nv_mfma_f32_16x16x32_bf16 v[52:55], v[180:183], v[60:63], v[52:55]\nds_read_b128 v[72:75], v1 offset:20480\ns_waitcnt lgkmcnt(5)\nv_mfma_f32_16x16x32_bf16 v[40:43], v[184:187], v[64:67], v[40:43]\nds_read_b128 v[60:63], v2 offset:59648\ns_waitcnt lgkmcnt(5)\nv_mfma_f32_16x16x32_bf16 v[44:47], v[76:79], v[64:67], v[44:47]\n" \
"v_mfma_f32_16x16x32_bf16 v[48:51], v[192:195], v[64:67], v[48:51]\nv_mfma_f32_16x16x32_bf16 v[52:55], v[200:203], v[64:67], v[52:55]\nds_read_b128 v[76:79], v1 offset:21504\ns_waitcnt lgkmcnt(5)\nv_mfma_f32_16x16x32_bf16 v[40:43], v[188:191], v[68:71], v[40:43]\nds_read_b128 v[64:67], v2 offset:61696\ns_waitcnt lgkmcnt(5)\nv_mfma_f32_16x16x32_bf16 v[44:47], v[80:83], v[68:71], v[44:47]\n" \
"v_mfma_f32_16x16x32_bf16 v[48:51], v[196:199], v[68:71], v[48:51]\nv_mfma_f32_16x16x32_bf16 v[52:55], v[204:207], v[68:71], v[52:55]\nds_read_b128 v[80:83], v1 offset:22528\ns_waitcnt lgkmcnt(5)\nv_mfma_f32_16x16x32_bf16 v[40:43], v[208:211], v[56:59], v[40:43]\nds_read_b128 v[68:71], v2 offset:63744\nv_mfma_f32_16x16x32_bf16 v[44:47], v[216:219], v[56:59], v[44:47]\ns_waitcnt lgkmcnt(5)\n" \
"v_mfma_f32_16x16x32_bf16 v[48:51], v[72:75], v[56:59], v[48:51]\nv_mfma_f32_16x16x32_bf16 v[52:55], v[224:227], v[56:59], v[52:55]\nds_read_b128 v[72:75], v1 offset:23552\ns_waitcnt lgkmcnt(5)\nv_mfma_f32_16x16x32_bf16 v[40:43], v[212:215], v[60:63], v[40:43]\nv_mfma_f32_16x16x32_bf16 v[44:47], v[220:223], v[60:63], v[44:47]\ns_waitcnt lgkmcnt(4)\nv_mfma_f32_16x16x32_bf16 v[48:51], v[76:79], v[60:63], v[48:51]\n" \
"v_mfma_f32_16x16x32_bf16 v[52:55], v[228:231], v[60:63], v[52:55]\ns_waitcnt lgkmcnt(3)\nv_mfma_f32_16x16x32_bf16 v[40:43], v[232:235], v[64:67], v[40:43]\nv_mfma_f32_16x16x32_bf16 v[44:47], v[240:243], v[64:67], v[44:47]\nv_mfma_f32_16x16x32_bf16 v[48:51], v[248:251], v[64:67], v[48:51]\ns_waitcnt lgkmcnt(2)\nv_mfma_f32_16x16x32_bf16 v[52:55], v[80:83], v[64:67], v[52:55]\ns_waitcnt lgkmcnt(1)\n" \
"v_mfma_f32_16x16x32_bf16 v[40:43], v[236:239], v[68:71], v[40:43]\nv_mfma_f32_16x16x32_bf16 v[44:47], v[244:247], v[68:71], v[44:47]\nv_mfma_f32_16x16x32_bf16 v[48:51], v[252:255], v[68:71], v[48:51]\ns_waitcnt lgkmcnt(0)\nv_mfma_f32_16x16x32_bf16 v[52:55], v[72:75], v[68:71], v[52:55]\nv_fma_f32 v40, v8, v87, v40\nv_fma_f32 v41, v9, v87, v41\ns_nop 1\n" \
"v_fma_f32 v48, v16, v87, v48\nv_fma_f32 v49, v17, v87, v49\nv_mul_f32 v40, v40, v88\nv_mul_f32 v41, v41, v88\nv_fma_f32 v91, v40, v40, s59\nv_fma_f32 v92, v41, v41, s59\nv_mul_f32 v91, v91, v40\nv_mul_f32 v92, v92, v41\n" \
"v_exp_f32 v91, v91\nv_exp_f32 v92, v92\nv_mul_f32 v40, v40, v48\nv_mul_f32 v41, v41, v49\nv_fma_f32 v91, v91, v89, v89\nv_fma_f32 v92, v92, v89, v89\nv_rcp_f32 v91, v91\nv_rcp_f32 v92, v92\n" \
"v_mul_f32 v40, v40, v91\nv_mul_f32 v41, v41, v92\nv_cvt_pk_bf16_f32 v48, v40, v41\nv_fma_f32 v42, v10, v87, v42\nv_fma_f32 v43, v11, v87, v43\nv_fma_f32 v50, v18, v87, v50\nv_fma_f32 v51, v19, v87, v51\nv_mul_f32 v42, v42, v88\n" \
"v_mul_f32 v43, v43, v88\nv_fma_f32 v91, v42, v42, s59\nv_fma_f32 v92, v43, v43, s59\nv_mul_f32 v91, v91, v42\nv_mul_f32 v92, v92, v43\nv_exp_f32 v91, v91\nv_exp_f32 v92, v92\nv_mul_f32 v42, v42, v50\n" \
"v_mul_f32 v43, v43, v51\nv_fma_f32 v91, v91, v89, v89\nv_fma_f32 v92, v92, v89, v89\nv_rcp_f32 v91, v91\nv_rcp_f32 v92, v92\nv_mul_f32 v42, v42, v91\nv_mul_f32 v43, v43, v92\nv_cvt_pk_bf16_f32 v49, v42, v43\n" \
"v_fma_f32 v44, v12, v87, v44\nv_fma_f32 v45, v13, v87, v45\nv_fma_f32 v52, v20, v87, v52\nv_fma_f32 v53, v21, v87, v53\nv_mul_f32 v44, v44, v88\nv_mul_f32 v45, v45, v88\nv_fma_f32 v91, v44, v44, s59\nv_fma_f32 v92, v45, v45, s59\n" \
"v_mul_f32 v91, v91, v44\nv_mul_f32 v92, v92, v45\nv_exp_f32 v91, v91\nv_exp_f32 v92, v92\nv_mul_f32 v44, v44, v52\nv_mul_f32 v45, v45, v53\nv_fma_f32 v91, v91, v89, v89\nv_fma_f32 v92, v92, v89, v89\n" \
"v_rcp_f32 v91, v91\nv_rcp_f32 v92, v92\nv_mul_f32 v44, v44, v91\nv_mul_f32 v45, v45, v92\nv_cvt_pk_bf16_f32 v50, v44, v45\nv_fma_f32 v46, v14, v87, v46\nv_fma_f32 v47, v15, v87, v47\nv_fma_f32 v54, v22, v87, v54\n" \
"v_fma_f32 v55, v23, v87, v55\nv_mul_f32 v46, v46, v88\nv_mul_f32 v47, v47, v88\nv_fma_f32 v91, v46, v46, s59\nv_fma_f32 v92, v47, v47, s59\nv_mul_f32 v91, v91, v46\nv_mul_f32 v92, v92, v47\nv_exp_f32 v91, v91\n" \
"v_exp_f32 v92, v92\nv_mul_f32 v46, v46, v54\nv_mul_f32 v47, v47, v55\nv_fma_f32 v91, v91, v89, v89\nv_fma_f32 v92, v92, v89, v89\nv_rcp_f32 v91, v91\nv_rcp_f32 v92, v92\nv_mul_f32 v46, v46, v91\n" \
"v_mul_f32 v47, v47, v92\nv_cvt_pk_bf16_f32 v51, v46, v47\nbuffer_store_dwordx4 v[48:51], v4, %[rs], s55 offen offset:256 sc1\nv_mov_b32 v90, s40\ns_mov_b32 s56, %[fh]\ns_add_u32 s40, s40, 1\ns_cmp_lt_u32 s40, 64\ns_cbranch_scc1 .Lgg_next%=\n" \
"s_waitcnt vmcnt(0)\nbuffer_store_dword v90, off, %[rs], s56 sc1\ns_branch .Lgg_end%=\n.Lgg_abort%=:\ns_mov_b64 exec, -1\nv_mov_b32 v16, 0\ns_movk_i32 s44, 6400\nbuffer_store_dword v16, off, %[rs], s44 sc1\n" \
".Lgg_end%=:\ns_waitcnt vmcnt(0) lgkmcnt(0)\n" \
""

#define V10(p, x) p #x "0", p #x "1", p #x "2", p #x "3", p #x "4", p #x "5", p #x "6", p #x "7", p #x "8", p #x "9"
#define V256(p) V10(p, ), V10(p, 1), V10(p, 2), V10(p, 3), V10(p, 4), V10(p, 5), V10(p, 6), V10(p, 7), V10(p, 8), V10(p, 9), V10(p, 10), V10(p, 11), V10(p, 12), V10(p, 13), V10(p, 14), V10(p, 15), V10(p, 16), \
    V10(p, 17), V10(p, 18), V10(p, 19), V10(p, 20), V10(p, 21), V10(p, 22), V10(p, 23), V10(p, 24), p "250", p "251", p "252", p "253", p "254", p "255"
#define CLOB "memory", "vcc", "scc", V256("v"), V256("a"), V10("s", 4), V10("s", 5), V10("s", 6), V10("s", 7), V10("s", 8)
template <int N> DEVI void table(lds_char* p, const unsigned (&t)[N]) {
#pragma unroll
    for (int i = 0; i < N; ++i) *(l1*)(p + i * 256) = t[i];
    WL0;
}
DEVI unsigned prof_at(int cu, int w) { return O_PROF + (262144 + cu * 2560 + w * 8) * 4; }
DEVI unsigned sp_idx(int lane) { return (lane & 2) ? 0xEEEEu : 0x4444u; }
DEVI int sp_row(int r, int hf) { return 4 * (r >> 1) + 2 * hf + (r & 1); }

DEVI void qk_role(const Args& a, const int w, lds_char* smem, int l, int h) {
    const int lane = lane_id(), g = lane >> 4, n = lane & 15, b = n & 3, p = n >> 2;
    const unsigned l0 = (unsigned)(size_t)smem, tb = l0 + w * 12288, lw = l0 + 54272 + w * 24576, ls = l * SD, IMG = l0 + 32768, MIM = l0 + 49152;
    const unsigned xw = (w >> 1) * 16384 + (w & 1) * 4096, oc = (h >> 3) * 16384 + ls, tr = (8 * g + p) * 128 + b * 16;
    const unsigned cm[4] = {4 * g > n ? 0xF149F2CAu : 0u, 4 * g + 1 > n ? 0xF149F2CAu : 0u, 4 * g + 2 > n ? 0xF149F2CAu : 0u, 4 * g + 3 > n ? 0xF149F2CAu : 0u};
    const unsigned t[19] = {
        l0 + lane * 16, lw + lane * 16, l0 + (g >> 1) * 1024 + (g & 1) * 512 + n * 16, O_E1 + ls + xw + lane * 16, sp_idx(lane),
        IMG + (w >> 1) * 8192 + ((w & 1) * 8 + 2 * g) * 512 + n * 16,
        MIM + (w * 8 + 2 * p) * 144 + g * 32 + b * 4,
        IMG + g * 512 + (w ? n * 16 : 8192 + (16 * (b >> 1) + 8 * (p >> 1) + 2 * (p & 1) + (b & 1)) * 16),
        MIM + g * 144 + (n & 7) * 16, MIM + 2304 + g * 144 + (2 * p + (b & 1)) * 16,
        w ? O_PT + 2 * (h >> 3) * 16384 + ((h >> 2) & 1) * 16384 + ls + (h & 3) * 2048 + (w - 1) * 1024 + lane * 16
          : O_PM + oc + ((h >> 1) & 3) * 1024 + ((2 * (h & 1) + (g >> 1)) * 16 + sp_row(n & 7, g & 1)) * 16 + (n >> 3) * 8,
        w ? O_PN + oc + ((h >> 2) & 1) * 2048 + (w - 1) * 1024 + (h & 3) * 256 + n * 16 + g * 4
          : n >> 3 ? OOB : O_PM + oc + 4096 + ((h & 7) >> 1) * 256 + sp_row(n & 7, g & 1) * 16 + (h & 1) * 8 + (g >> 1) * 4,
        w == 3 ? (l ? F_X + SD + p * 128 + b * 4 : F_X0 + b * 4) : cm[0], cm[1], cm[2], cm[3],
        (lane >> 3) * 8192u + (lane & 7) * 16, tb + tr, lw + tr};
    table(smem + w * 12288 + lane * 4, t);
    asm volatile(QK_ASM : : [rs] "s"(mkrs(a.ws, WS_RS)), [rw] "s"(mkrs(w < 2 ? a.Wq : a.Wk, 8u << 20)), [wo] "s"(h * 256 + (w & 1) * 128), [tb] "s"(tb), [lw] "s"(lw), [l0] "s"(l0), [w] "s"(w),
        [xn] "s"(O_XN + ls), [xw] "s"(xw), [fs] "s"(l ? 0 : 16), [pr] "s"(prof_at(l * 80 + h, w) + (w == 3 ? 0 : 128)) : CLOB);
}
DEVI void vu_role(const Args& a, const int w, lds_char* smem, int l, int h) {
    const int lane = lane_id(), g = lane >> 4, n = lane & 15, b = n & 3, p = n >> 2;
    const unsigned l0 = (unsigned)(size_t)smem, tb = l0 + w * 12288, lw = l0 + 59392 + w * 24576, ls = l * SD;
    const unsigned xw = (w >> 1) * 16384 + (w & 1) * 4096, tv = (8 * g + p) * 64 + b * 16, to = (8 * g + 4 * (p & 1) + (p >> 1)) * 128 + b * 8;
    const unsigned t[16] = {
        l0 + lane * 16, lw + lane * 16, l0 + (g >> 1) * 1024 + (g & 1) * 512 + n * 16, O_E1 + ls + xw + lane * 16, sp_idx(lane),
        l0 + 57344 + (w >> 1) * 1024 + ((2 * (w & 1) + (n >> 3)) * 16 + 4 * g + 2 * (n & 1)) * 16 + ((n >> 1) & 3) * 4,
        O_UM + ls + w * 262144 + (h >> 3) * 16384 + g * 65536 + (h & 7) * 256 + n * 16,
        sd(O_Y + h * 131072 + ls) + w * 32768 + lane * 16,
        l ? F_X + SD + p * 128 + b * 4 : F_X0 + b * 4, l0 + 49152 + w * 2048 + lane * 16,
        (lane >> 2) * 8192u + (lane & 3) * 16, (lane >> 3) * 2048u + (lane & 7) * 16, tb + tv, lw + tv, tb + to, lw + to};
    table(smem + w * 12288 + lane * 4, t);
    asm volatile(VU_ASM : : [rs] "s"(mkrs(a.ws, WS_RS)), [rw] "s"(mkrs(a.Wv, 8u << 20)), [ro] "s"(mkrs(a.Wo, 8u << 20)), [wo] "s"(h * 256 + w * 64), [w2] "s"(h * 262144 + w * 512), [tb] "s"(tb), [lw] "s"(lw), [w] "s"(w),
        [xn] "s"(O_XN + ls), [xw] "s"(xw), [fs] "s"(l ? 0 : 16), [fy] "s"(F_Y0 + ls + (w * 32 + h) * 4), [pr] "s"(prof_at(l * 80 + 32 + h, w)) : CLOB);
}
DEVI void rd_role(const Args& a, const int w, lds_char* smem, int l, int c) {
    const int lane = lane_id(), g = lane >> 4, n = lane & 15, r = 2 * (n >> 2) + (n & 1), hf = (n >> 1) & 1;
    const rsrc_t rs = mkrs(a.ws, WS_RS);
    const unsigned l0 = (unsigned)(size_t)smem, tb = l0 + 65536 + w * 4096, ls = l * SD, vp = O_E1 + ls + (c >> 3) * 16384 + (c & 7) * 1024 + w * 256 + n * 16;
    st4(g & 1 ? *(const unsigned*)(a.mem0 + r * 1024 + c * 64 + w * 16 + 4 * g + 2 * hf) : nosent(*(const unsigned*)(a.mem0 + r * 1024 + c * 64 + w * 16 + 4 * g + 2 * hf)), rs, g & 1 ? vp + g * 4 : OOB, 0);
    WV0;
    st4(nosent(*(const unsigned*)(a.mem0 + r * 1024 + c * 64 + w * 16 + 4 * g + 2 * hf)), rs, g & 1 ? OOB : vp + g * 4, 0);
    const int gi = lane >> 3, r8 = lane & 7;
    const unsigned t[11] = {
        (unsigned)lane * 16, sp_idx(lane), l0 + w * 4096 + g * 576 + ((n >> 3) * 16 + hf * 4 + 2 * ((n >> 2) & 1) + (n & 1)) * 4,
        l0 + (r >> 1) * 576 + w * 128 + (g >> 1) * 64 + (r & 1) * 32 + hf * 16,
        (vp ^ ((g & 1) * 65536)) + (g >> 1) * 8, g & 1 ? SENT : 0u,
        l0 + 16384 + w * 8192 + lane * 16, l0 + 16384 + ((gi >> 1) * 2 + (w >> 1)) * 1024 + ((gi & 1) * 32 + (w & 1) * 8 + r8) * 16,
        (c >> 1) * 16384u + (c & 1) * 4096 + gi * 512 + (w * 8 + r8) * 16,
        gi ? OOB : O_ST - O_XR + c * 256 + ((w & 1) * 8 + r8) * 16 + (w >> 1) * 8,
        F_Y0 + ls + ((c >> 2) * 32 + w * 8 + (lane & 7)) * 4};
    table(smem + 65536 + w * 4096 + lane * 4, t);
    asm volatile(RD_ASM : : [rs] "s"(rs), [tb] "s"(tb), [su] "s"(O_UM + ls + c * 65536 + w * 16384), [sp] "s"(O_PM + ls + w * 16384), [st] "s"(O_PT + ls + w * 32768), [sn] "s"(O_PN + ls + w * 16384),
        [sy] "s"(sd(O_Y + w * (1u << 20) + ls) + (c >> 1) * 16384 + (c & 1) * 4096), [at] "s"(O_XR + ls), [fa] "s"(F_A + ls + c * 128 + w * 4), [pr] "s"(prof_at(l * 80 + 64 + c, w) + 128) : CLOB);
}
DEVI void gg_role(const Args& a, const int w, lds_char* smem, int l, int gi) {
    const int lane = lane_id(), g = lane >> 4, n = lane & 15, b = n & 3, p = n >> 2;
    const unsigned l0 = (unsigned)(size_t)smem, tb = l0 + w * 12288, lw = l0 + 65536 + w * 24576, ls = l * SD, tv = (8 * g + p) * 64 + b * 16, fh = (g >> 1) * 1024 + (g & 1) * 512 + n * 16;
    const unsigned t[9] = {l0 + lane * 16, lw + lane * 16, l0 + fh, F_A + ls + (lane >> 2) * 128 + (lane & 3) * 4, w * 2048 + fh, O_ST - O_XR + g * 1024 + n * 16, (lane >> 2) * 7168u + (lane & 3) * 16, tb + tv, lw + tv};
    table(smem + w * 12288 + lane * 4, t);
    asm volatile(GG_ASM : : [rs] "s"(mkrs(a.ws, WS_RS)), [rw] "s"(mkrs(a.Wg, 3584 * 2048)), [ro] "s"(mkrs(a.Wu, 3584 * 2048)), [wo] "s"(gi * 256 + w * 64), [tb] "s"(tb), [lw] "s"(lw), [w] "s"(w),
        [xn] "s"(O_XR + ls), [xw] "s"((w >> 1) * 16384 + (w & 1) * 4096), [ho] "s"(O_H + ls + gi * 16384), [fh] "s"(F_H + ls + (gi / 7) * 1024 + (gi % 7) * 128 + w * 4), [pr] "s"(prof_at(160 + l * 28 + gi, w) + 128) : CLOB);
}

#define DLG 600
#define DLD 1000
#define DL1 1000
#define DLG1 2000
#define DLD1 2400
__global__ __launch_bounds__(256, 1) void sys_kernel(Args a) {
    extern __shared__ __attribute__((aligned(16))) char smem_[];
    lds_char* smem = (lds_char*)smem_;
    {
        const unsigned* t = (const unsigned*)(a.ws + O_POL);
        int v = 0;
#pragma unroll
        for (int i = 0; i < 8; ++i) v += (t[8 + i] < t[i]) != (i >= 4);
        a.ws += v > 4 ? 2u << 20 : 0u;
    }
    const int x = blockIdx.x & 7, j = blockIdx.x >> 3, wv = RFL(threadIdx.x >> 6), l = x >> 2, xx = x & 3;
    if (wv == 0) TSTAMP(210000 + blockIdx.x);
    if (j < 16) {
        if (l) { ln0(a, wv, j * 4 + xx); idle(DL1); }
        if (j < 8) qk_role(a, wv, smem, l, xx * 8 + j); else vu_role(a, wv, smem, l, xx * 8 + j - 8);
        return;
    }
    if (j < 20) { rd_role(a, wv, smem, l, xx * 4 + j - 16); return; }
    if (j < 27) { idle(l ? DLG1 : DLG); gg_role(a, wv, smem, l, xx * 7 + j - 20); return; }
    if (j == 31) { if (l) r_role<1>(a, wv, smem, xx); else r_role<0>(a, wv, smem, xx); return; }
    EP e;
    const unsigned fs = l * SD;
    e.wbytes = 3584 * 2048; e.out_st = 524288; e.out_mask = 255; e.fown_w = 4; e.prio = 0;
    e.prof = blockIdx.x * 256;
    if (j >= 27) {
        const int mb = j - 27;
        e.out0 = e.out1 = O_SL + xx * 131072 + mb * 32768 + fs;
        e.W0 = a.Wd; e.ldw = 1024; e.w_off = xx * (896 * 2048) + mb * 512;
        e.in0 = e.in1 = O_H + xx * 7 * 16384 + fs; e.in_st = 524288;
        e.fset = F_H + xx * 1024 + fs; e.fmask = 0x0FFFFFFF;
        e.fown0 = e.fown1 = F_D + fs + (xx * 4 + mb) * 128;
        idle(l ? DLD1 : DLD);
        engine<0, 56, true>(a, wv, e, smem);
    }
}

__global__ __launch_bounds__(256) void init_kernel(char* ws) {
    const rsrc_t rs = mkrs(ws, 6u << 20);
    if (blockIdx.x < 16) {
        if (threadIdx.x >= 64) return;
        unsigned t = ~0u;
        for (int i = 0; i < NPRB; ++i) {
            const unsigned t0 = (unsigned)__builtin_amdgcn_s_memtime();
            WL0;
            st4(0u, rs, 0, O_PRB + blockIdx.x * 1024 + i * 128);
            WV0;
            t = min(t, (unsigned)__builtin_amdgcn_s_memtime() - t0);
        }
        st4(t, rs, 0, O_POL + blockIdx.x * 4);
        return;
    }
    const unsigned o = (blockIdx.x - 16) * 16384 + threadIdx.x * 16;
#pragma unroll
    for (int i = 0; i < 8; ++i) __builtin_amdgcn_raw_buffer_store_b128(v4u{SENT, SENT, SENT, SENT}, rs, o + (i & 3) * 4096 + (i >> 2) * (2u << 20), 0, 0);
}

extern "C" void kernel_launch(void* const* d_in, const int* in_sizes, int n_in, void* d_out, int out_size, void* d_ws, size_t ws_size, hipStream_t stream) {
    (void)in_sizes; (void)n_in; (void)out_size; (void)ws_size;
    Args a;
    a.x = (const u16*)d_in[0]; a.mem0 = (const u16*)d_in[1]; a.Wq = (const u16*)d_in[2]; a.Wk = (const u16*)d_in[3]; a.Wv = (const u16*)d_in[4]; a.Wo = (const u16*)d_in[5];
    a.Wg = (const u16*)d_in[8]; a.Wu = (const u16*)d_in[9]; a.Wd = (const u16*)d_in[10];
    a.ws = (char*)(((uintptr_t)d_ws + (2u << 20) - 1) & ~(uintptr_t)((2u << 20) - 1)); a.out = (u16*)d_out;
    (void)hipFuncSetAttribute((const void*)sys_kernel, hipFuncAttributeMaxDynamicSharedMemorySize, LDSB);
    hipLaunchKernelGGL(init_kernel, dim3(16 + CTRL_END / 16384), dim3(256), 0, stream, a.ws);
    hipLaunchKernelGGL(sys_kernel, dim3(256), dim3(256), LDSB, stream, a);
}
```

```cpp
#include <hip/hip_runtime.h>
#include <stdint.h>

typedef unsigned short u16;
typedef unsigned long long u64;
typedef __attribute__((ext_vector_type(8))) short bf16x8;
typedef __attribute__((ext_vector_type(4))) short s16x4;
typedef __attribute__((ext_vector_type(4))) float f32x4;
typedef __attribute__((ext_vector_type(16))) float f32x16;
typedef __attribute__((ext_vector_type(2))) float f32x2;
typedef __attribute__((ext_vector_type(4))) unsigned v4u;
typedef __attribute__((ext_vector_type(2))) unsigned v2u;
typedef __amdgpu_buffer_rsrc_t rsrc_t;
typedef __attribute__((address_space(3))) char lds_char;
typedef __attribute__((address_space(3))) bf16x8 l8;
typedef __attribute__((address_space(3))) v4u l4;
typedef __attribute__((address_space(3))) v2u l2;
typedef __attribute__((address_space(3))) unsigned l1;
typedef __attribute__((address_space(3))) f32x4 lf4;
typedef __attribute__((address_space(3))) float lf1;
#define DEVI __device__ __forceinline__
#define AI __attribute__((always_inline))

constexpr int NSEG = 64;
constexpr float ATT_SCALE = 0.08838834764831845f;
constexpr float LOG2E = 1.4426950408889634f;
#define NPRB 3
constexpr unsigned SD = 8192;
DEVI unsigned sd(unsigned o) { return o ^ ((o >> 8) & SD); }
constexpr unsigned F_X0 = 32768;
constexpr unsigned F_X = 4096;
constexpr unsigned F_A = 16384;
constexpr unsigned F_H = F_A + 2048;
constexpr unsigned F_D = F_H + 4096;
constexpr unsigned F_Y0 = 49152;
constexpr unsigned F_ABORT = 6400;
constexpr unsigned O_PM = 65536;
constexpr unsigned O_E1 = 131072;
constexpr unsigned O_PT = 262144;
constexpr unsigned O_UM = 524288;
constexpr unsigned CTRL_END = O_UM + (1u << 20);
constexpr unsigned O_PRB = 0x1F0000, O_POL = 0x1FFF00;
constexpr unsigned O_XN = 4u << 20;
constexpr unsigned O_XR = O_XN + (8u << 20);
constexpr unsigned O_ST = O_XR + (8u << 20);
constexpr unsigned O_SL = O_ST + (8u << 20);
constexpr unsigned O_H = O_SL + (32u << 20);
constexpr unsigned O_Y = O_H + (36u << 20);
constexpr unsigned WS_NEED = O_Y + (8u << 20);
constexpr unsigned O_PROF = WS_NEED;
constexpr unsigned WS_RS = WS_NEED;
static_assert(CTRL_END <= O_PRB && CTRL_END % 16384 == 0 && O_Y % (8u << 20) == 0, "workspace");
constexpr unsigned OOB = 0x80000000u, OOB2 = 0x90000000u, SENT = 0xFFFFFFFFu;

struct Args { const u16 *x, *mem0, *Wq, *Wk, *Wv, *Wo, *Wg, *Wu, *Wd; char* ws; u16* out; };

typedef __attribute__((ext_vector_type(2))) __bf16 bf16x2_t;
DEVI unsigned pack2(float lo, float hi) { f32x2 v = {lo, hi}; bf16x2_t b = __builtin_convertvector(v, bf16x2_t); return __builtin_bit_cast(unsigned, b); }
DEVI float bflo(unsigned u) { return __uint_as_float(u << 16); }
DEVI float bfhi(unsigned u) { return __uint_as_float(u & 0xffff0000u); }
DEVI float rbf(float x) { return bflo(pack2(x, x)); }
DEVI bf16x8 as8(v4u v) { return __builtin_bit_cast(bf16x8, v); }
DEVI f32x4 mfma16(bf16x8 a, bf16x8 b, f32x4 c) { return __builtin_amdgcn_mfma_f32_16x16x32_bf16(a, b, c, 0, 0, 0); }
DEVI f32x16 mfma32(bf16x8 a, bf16x8 b, f32x16 c) { return __builtin_amdgcn_mfma_f32_32x32x16_bf16(a, b, c, 0, 0, 0); }
constexpr int AUX_SC1 = 16;
DEVI v4u ld16(rsrc_t r, unsigned vo, unsigned so) { return __builtin_amdgcn_raw_buffer_load_b128(r, vo, so, AUX_SC1); }
DEVI unsigned ld4(rsrc_t r, unsigned vo, unsigned so) { return __builtin_amdgcn_raw_buffer_load_b32(r, vo, so, AUX_SC1); }
DEVI void st16(v4u v, rsrc_t r, unsigned vo) { __builtin_amdgcn_raw_buffer_store_b128(v, r, vo, 0, AUX_SC1); }
DEVI void st8(v2u v, rsrc_t r, unsigned vo, unsigned so) { __builtin_amdgcn_raw_buffer_store_b64(v, r, vo, so, AUX_SC1); }
DEVI void st4(unsigned v, rsrc_t r, unsigned vo, unsigned so) { __builtin_amdgcn_raw_buffer_store_b32(v, r, vo, so, AUX_SC1); }
DEVI v2u trd(lds_char* p) { return __builtin_bit_cast(v2u, __builtin_amdgcn_ds_read_tr16_b64_v4i16((__attribute__((address_space(3))) s16x4*)p)); }
template <int O1, int O2> DEVI void tra(v2u& lo, v2u& hi, unsigned a) { asm volatile("ds_read_b64_tr_b16 %0, %2 offset:%3\n\tds_read_b64_tr_b16 %1, %2 offset:%4" : "=&v"(lo), "=&v"(hi) : "v"(a), "n"(O1), "n"(O2) : "memory"); }
DEVI void trw(v2u& a, v2u& b, v2u& c, v2u& d) { asm volatile("s_waitcnt lgkmcnt(0)" : "+v"(a), "+v"(b), "+v"(c), "+v"(d)::"memory"); }
DEVI rsrc_t mkrs(const void* p, unsigned n) { return __builtin_amdgcn_make_buffer_rsrc((void*)p, 0, (int)n, 0x00020000); }
#define CB asm volatile("" ::: "memory")
#define SB __builtin_amdgcn_sched_barrier(0)
#define WL0 asm volatile("s_waitcnt lgkmcnt(0)" ::: "memory")
#define WV0 asm volatile("s_waitcnt vmcnt(0)" ::: "memory")
#define WV(n) asm volatile("s_waitcnt vmcnt(%0)" ::"n"(n) : "memory")
#define BAR do { __builtin_amdgcn_s_barrier(); CB; } while (0)
#define RFL(x) __builtin_amdgcn_readfirstlane(x)
DEVI int lane_id() { int x; asm volatile("v_mbcnt_lo_u32_b32 %0, -1, 0\n\tv_mbcnt_hi_u32_b32 %0, -1, %0" : "=v"(x)); return x; }
template <int I> struct IC { static constexpr int v = I; };
template <int B, int E_, class F> DEVI void sfor(F&& f) { if constexpr (B < E_) { f(IC<B>{}); sfor<B + 1, E_>(f); } }
DEVI float red_x32(float x, bool mx) { unsigned u = __float_as_uint(x); auto r = __builtin_amdgcn_permlane32_swap(u, u, false, false); float a = __uint_as_float(r[0]), b = __uint_as_float(r[1]); return mx ? fmaxf(a, b) : a + b; }
DEVI float red_x16(float x, bool mx) { unsigned u = __float_as_uint(x); auto r = __builtin_amdgcn_permlane16_swap(u, u, false, false); float a = __uint_as_float(r[0]), b = __uint_as_float(r[1]); return mx ? fmaxf(a, b) : a + b; }
DEVI unsigned vmax4(v4u d) { return max(max(d[0], d[1]), max(d[2], d[3])); }
DEVI unsigned nosent(unsigned v) { return min(v, SENT - 1u); }
constexpr unsigned SPIN_CAP = 1u << 18;
DEVI bool spin_bail(unsigned& spins, rsrc_t rs) {
    if (++spins > SPIN_CAP) { st4(0u, rs, 0, F_ABORT); return true; }
    return (spins & 255u) == 0u && RFL(ld4(rs, 0, F_ABORT)) != SENT;
}
#define TSTAMP(idx)
constexpr int LDSB = 160 * 1024;
DEVI void idle(unsigned dl) { const unsigned t0 = (unsigned)__builtin_amdgcn_s_memrealtime(); while ((unsigned)__builtin_amdgcn_s_memrealtime() - t0 < dl) __builtin_amdgcn_s_sleep(8); }
#define DSLP 2

template <bool SW> DEVI void mf_a(f32x16& c, bf16x8 a, bf16x8 b) {
    if (SW) asm volatile("v_mfma_f32_32x32x16_bf16 %0, %2, %1, %0" : "+v"(c) : "a"(a), "v"(b));
    else asm volatile("v_mfma_f32_32x32x16_bf16 %0, %1, %2, %0" : "+v"(c) : "a"(a), "v"(b)); }
template <bool SW> DEVI void mf_v(f32x16& c, bf16x8 a, bf16x8 b) {
    if (SW) asm volatile("v_mfma_f32_32x32x16_bf16 %0, %2, %1, %0" : "+v"(c) : "v"(a), "v"(b));
    else asm volatile("v_mfma_f32_32x32x16_bf16 %0, %1, %2, %0" : "+v"(c) : "v"(a), "v"(b)); }
template <bool SW> DEVI void mf_a0(f32x16& c, bf16x8 a, bf16x8 b) {
    if (SW) asm volatile("v_mfma_f32_32x32x16_bf16 %0, %2, %1, 0" : "=&v"(c) : "a"(a), "v"(b));
    else asm volatile("v_mfma_f32_32x32x16_bf16 %0, %1, %2, 0" : "=&v"(c) : "a"(a), "v"(b)); }
template <bool SW> DEVI void mf_v0(f32x16& c, bf16x8 a, bf16x8 b) {
    if (SW) asm volatile("v_mfma_f32_32x32x16_bf16 %0, %2, %1, 0" : "=&v"(c) : "v"(a), "v"(b));
    else asm volatile("v_mfma_f32_32x32x16_bf16 %0, %1, %2, 0" : "=&v"(c) : "v"(a), "v"(b)); }

template <int K, bool Z> DEVI void mh(f32x4& c, bf16x8 a, bf16x8 b) {
    if (Z) { if (K) asm volatile("v_mfma_f32_16x16x32_bf16 %0, %1, %2, 0" : "=&v"(c) : "a"(a), "v"(b)); else asm volatile("v_mfma_f32_16x16x32_bf16 %0, %1, %2, 0" : "=&v"(c) : "v"(a), "v"(b)); }
    else { if (K) asm volatile("v_mfma_f32_16x16x32_bf16 %0, %1, %2, %0" : "+v"(c) : "a"(a), "v"(b)); else asm volatile("v_mfma_f32_16x16x32_bf16 %0, %1, %2, %0" : "+v"(c) : "v"(a), "v"(b)); } }
#define STD 4
#define ST2 2
#define AB 0
#define CVP 0
#define PSP64 2
#define OVR 0
#define NB1 0
#define E16 5
constexpr int XD = 3, NRG = 53;
#define PUBD 51
#define PUBG 36
#define GBUD 6
#define LROT 8
#define FD 1
template <int EPI, int NK_, bool M_> struct Sch {
    static constexpr int NK = NK_, NS = 2 * NK, NLW = NK - NRG, PER = EPI == 3 ? 8 : NK, PS = 2 * PER;
    static constexpr bool M = M_;
    static constexpr int PSP = NK_ == 64 ? PSP64 : 2, N1 = EPI != 3 && ((NB1 >> (EPI == 2 ? 2 : M_ ? 0 : 1)) & 1), NP0 = N1 ? 8 : 4, OV = EPI != 3 && ((OVR >> (EPI == 2 ? 2 : M_ ? 0 : 1)) & 1);
    static constexpr int S(int t) { return NK == 64 ? 16 * t : (t <= 2 ? 16 * t : 32 + 12 * (t - 2)); }
    static constexpr int np(int t) { return (S(t + 1) - S(t)) / 4; }
    static constexpr bool lds_step(int k) { return k % 5 == 4 && k / 5 < NLW; }
    static constexpr int lds_idx(int k) { return k / 5; }
    static constexpr int reg_idx(int k) { int n = 0; for (int i = 0; i < k; ++i) if (!lds_step(i)) ++n; return n; }
    static constexpr int kof(int i) { const int j = i % PS, b = i / PS * PER; return b + (j < 4 ? (j & 1) : j >= PS - 4 ? PER - 2 + (j & 1) : j / 2); }
    static constexpr int cof(int i) { const int j = i % PS; return j < 4 ? j / 2 : j >= PS - 4 ? (j - (PS - 4)) / 2 : j % 2; }
    static constexpr int bnd(int i) { if (i & 1) return -1; for (int t = 1 + N1; t <= 4; ++t) if (i / 2 + XD == S(t)) return t & 3; return -1; }
    static constexpr int tsl(int s) { const int kk = (s + XD - 1) % NK; int t = 0; while (t < 3 && kk >= S(t + 1)) ++t; return t; }
    static constexpr int tks(int s) { return (s + XD - 1) % NK - S(tsl(s)); }
    static constexpr int piece_of(int i) {
        if (!(i & 1)) return -1;
        const int s = i / 2, u = tks(s) - (EPI != 3 && tsl(s) == 0 ? 3 : 2);
        if (N1 && tsl(s) >= 2) return tsl(s) == 2 && u >= 0 && u % PSP < 2 && u / PSP * 2 + u % PSP < np(0) + np(1) ? u / PSP * 2 + u % PSP : -1;
        return s < NK - XD + 1 && u >= 0 && u % 3 == 0 && u / 3 < np((tsl(s) + 2) & 3) ? u / 3 : -1;
    }
    static constexpr bool fl_of(int i) { return (i & 1) && tks(i / 2) == 1 && (tsl(i / 2) + FD == 2 || tsl(i / 2) + FD == 3); }
    static constexpr int w_of(int i) { if (EPI != 3 && i < 3) return i == 2 ? 8 : -1; return !(i & 1) ? -1 : lds_step(i / 2 + 4) ? i + 7 : lds_step(i / 2 + 3) ? i + 6 : -1; }
    static constexpr int D2 = 2 * (S(2) - XD + 1), D3 = 2 * (S(3) - XD + 1), ROT = EPI == 3 ? 0 : LROT;
    static constexpr int fx(int i) {
        int n = !(i & 1) + (w_of(i) >= 0) + ((i & 1) || lds_step(i / 2)) + (piece_of(i) >= 0 ? 4 - (w_of(i) >= 0) : 0) + (fl_of(i) ? 3 : 0);
        if (i < 2 || i == ROT - 1 || (i >= D2 && i < D2 + 7) || bnd(i) >= 0) n = 6;
        return n;
    }
    struct GP { int a[128], st[2]; };
    static constexpr GP gp() {
        GP r{};
        int k = 0;
        for (int i = 0; i < 128; ++i) {
            r.a[i] = k;
            int n = fx(i);
            if (k >= 84 && !r.st[0] && n < 3) { r.st[0] = i; ++n; }
            if (k == 168 && !r.st[1] && n < 3) r.st[1] = i;
            while (k < 168) {
                const int j = k % 21, c = j == 8 || j == 9 || j == 14 || j == 15 ? 2 : 1;
                if (n + c > GBUD) break;
                n += c; ++k;
            }
        }
        return r;
    }
    static constexpr GP G = gp();
    static constexpr int st_of(int i) { if (EPI == 2) return i == G.st[0] ? 0 : i == G.st[1] ? 1 : -1; if (M) return i >= 2 && (i - 2) % STD == 0 && i < 2 + 4 * STD ? (i - 2) / STD : -1;
        if (EPI == 0) return i % ST2 == 0 && i < 4 * ST2 ? i / ST2 : -1;
        const int j = i % PS; return j == 0 ? 0 : j == 2 ? 1 : j == 4 ? 2 : j == 6 ? 3 : -1; }
    static constexpr int xr(int i, int h) { return !(i & 1) && h ? (i / 2 + XD) % NK : -1; }
    static constexpr int cv_of(int i, int h) {
        const int m = (2 * i + h + 8) % (2 * NS), t[2][8] = {{2, 3, 4, 5, 6, 7, 10, 11}, {3, 4, 6, 7, 8, 10, 11, 14}};
        for (int p = 0; p < 8; ++p) if (t[CVP][p] == m) return p;
        return -1;
    }
    static constexpr int lo(int i, int h) { return (w_of(i) >= 0 && (st_of(i) >= 0) == (h == 1)) + (h && bnd(i) >= 2); }
    static constexpr int xw(int i) { int n = 0; for (int m = 2 * i;; m = (m + 2 * NS - 1) % (2 * NS)) { n += lo(m / 2, m & 1); if (m == 2 * i - 5 && (i == D2 + 1 || i == D3 + 1)) return n - 1;
        if (xr(m / 2, m & 1) == (i / 2 + 1) % NK) return n; n += xr(m / 2, m & 1) >= 0; } }
    static constexpr int last_st = EPI == 2 ? G.st[1] : M ? 2 + 3 * STD : EPI == 0 ? 3 * ST2 : 6, PUB = EPI == 2 ? (last_st + PUBG) | 1 : 6 + PUBD, UPD = D3 + 18;
    static_assert(UPD + 2 < NS - 3 && PUB < UPD && PUB != D3 + 3 && st_of(D3 + 3) < 0 && piece_of(D3 + 3) < 0 && !fl_of(D3 + 3), "publication");
    static constexpr bool pub_of(int i) { return i == PUB; }
    static constexpr int nops(int i) { return (st_of(i) >= 0) + (piece_of(i) >= 0) + fl_of(i) + pub_of(i); }
    static constexpr int between(int ia, int ib) { int n = 0; for (int i = (ia + 1) % NS; i != ib; i = (i + 1) % NS) n += nops(i); return n; }
    static constexpr int cnt(int t) {
        int ib = 0, ip = 0, jf = 0;
        for (int i = 0; i < NS; ++i) {
            if (bnd(i) == t) ib = i;
            if (piece_of(i) >= 0 && ((tsl(i / 2) + 2) & 3) == t) ip = i;
            if (fl_of(i) && tsl(i / 2) + FD == t) jf = i;
            if (t == 0 && i == D3 + 2) ip = i;
        }
        if (t == 1) ip = NS - 1;
        int n = between(ip, ib);
        if (t >= 2 && between(jf, ib) < n) n = between(jf, ib);
        return n;
    }
};
namespace en { constexpr int L_FL = 0, L_W = 2048, L_RING = L_W + 4 * 11 * 2048, END = L_RING + 65536; }
static_assert(en::END <= LDSB, "LDS");
struct EP {
    const u16* W0; unsigned wbytes, ldw, w_off;
    unsigned in0, in1, in_st;
    unsigned out0, out1, out_st, out_mask;
    unsigned fset, fmask, fown0, fown1, fown_w;
    int prio, prof;
};

#define GI(k, s) ".if (%[ka] <= " #k "+gb_) && (" #k "+gb_ < %[kb])\n\t" s "\n\t.endif\n\t"
#define GL(g, u) GI(0, "v_lshlrev_b32 %0, 16, " g) GI(1, "v_and_b32 %1, 0xffff0000, " g) GI(2, "v_mul_f32 %0, %[a], %0") GI(3, "v_mul_f32 %1, %[a], %1") \
        GI(4, "v_fma_f32 %2, %0, %0, %[b]") GI(5, "v_fma_f32 %3, %1, %1, %[b]") GI(6, "v_mul_f32 %2, %2, %0") GI(7, "v_mul_f32 %3, %3, %1") GI(8, "v_exp_f32 %2, %2") GI(9, "v_exp_f32 %3, %3") \
        GI(10, "v_lshlrev_b32 " g ", 16, " u) GI(11, "v_fma_f32 %2, %2, %[a], %[a]") GI(12, "v_fma_f32 %3, %3, %[a], %[a]") GI(13, "v_and_b32 " u ", 0xffff0000, " u) GI(14, "v_rcp_f32 %2, %2") GI(15, "v_rcp_f32 %3, %3") \
        GI(16, "v_mul_f32 %0, %0, " g) GI(17, "v_mul_f32 %1, %1, " u) GI(18, "v_mul_f32 %0, %0, %2") GI(19, "v_mul_f32 %1, %1, %3") GI(20, "v_cvt_pk_bf16_f32 " g ", %0, %1")
template <int KA, int KB> DEVI void geglu(float& x0, float& x1, float& e0, float& e1, unsigned& g, unsigned& u, unsigned g2) {
    static_assert(KB <= 31, "next pair");
    asm volatile(".set gb_, 0\n\t" GL("%[g]", "%[u]") ".set gb_, 21\n\t" GL("%[h]", "%[h]")
        : "+v"(x0), "+v"(x1), "+v"(e0), "+v"(e1), [g] "+v"(g), [u] "+v"(u) : [a] "s"(-0.46866869305f), [b] "s"(4.91222953926f), [ka] "n"(KA), [kb] "n"(KB), [h] "v"(g2));
}
#undef GL
#undef GI
template <int EPI, int NK, bool M_ = false>
DEVI void engine(const Args& a, const int wv, const EP& e, lds_char* smem) {
    using namespace en;
    typedef Sch<EPI, NK, M_> Z;
    constexpr int NS = 2 * NK;
    constexpr bool SW = EPI == 3, M = Z::M;
    const int lane = lane_id(), tz = lane + wv * 64, w = wv;
    const rsrc_t rs = mkrs(a.ws, WS_RS);
    bf16x8 wt[NRG][2];
    unsigned xb = lane * 16 + L_RING, wb = lane * 16 + L_W + w * 11 * 2048, l4i = (unsigned)(size_t)smem + L_FL + lane * 4, l4g = ((lane & 31) >> 2) * 128 + (lane & 3) * 4;
    asm volatile("" : "+v"(xb), "+v"(wb), "+v"(l4i), "+v"(l4g));
    *(l1*)(smem + L_FL + tz * 4) = SENT;
    {
        constexpr bool G = EPI == 2;
        constexpr int DP = G ? 7 : 8;
        const rsrc_t r0 = mkrs(e.W0, e.wbytes);
        const unsigned vo = (lane >> 3) * e.ldw * 2 + (lane & 7) * 16, so = e.w_off + (G ? w >> 1 : w) * 128, sk = e.ldw * 16;
        const int q = (lane >> 2) & 3, cid = ((lane >> 4) & 1) * 4 + (lane & 3), qa = cid >> 1;
        lds_char* sb = smem + L_RING + w * 16384;
        const unsigned ta = (unsigned)(size_t)smem + L_RING + (G ? (w & 2) + (qa >> 1) : w) * 16384 + (8 * (lane >> 5) + q) * 128 + (8 * (cid & 1) + 4 * (qa & 1)) * 2 + (G ? (w & 1) * 64 : (qa >> 1) * 32);
        auto issue = [&](int k) AI {
            __builtin_amdgcn_raw_ptr_buffer_load_lds(r0, sb + (k & 7) * 2048, 16, vo, so + 2 * k * sk, 0, 2);
            __builtin_amdgcn_raw_ptr_buffer_load_lds(r0, sb + (k & 7) * 2048 + 1024, 16, vo, so + (2 * k + 1) * sk, 0, 2);
        };
#pragma unroll
        for (int k = 0; k < DP; ++k) issue(k);
        const unsigned th = (unsigned)(size_t)smem + L_RING + w * 16384 + (8 * (lane >> 4) + q) * 128 + (lane & 3) * 16;
        if constexpr (M) sfor<0, NK / 2>([&](auto K) AI {
            constexpr int k = 2 * decltype(K)::v + 1, o = ((k - 1) & 7) * 2048;
            WV(2 * (NK - 1 - k < DP - 2 ? NK - 1 - k : DP - 2));
            v2u lo[4], hi[4];
            sfor<0, 4>([&](auto TT) AI { constexpr int T = decltype(TT)::v, oo = o + (T >> 1) * 64 + (T & 1) * 8; tra<oo, oo + 512>(lo[T], hi[T], th); });
            trw(lo[0], hi[0], lo[1], hi[1]); trw(lo[2], hi[2], lo[3], hi[3]);
            if constexpr (k + DP < NK) { issue(k - 1 + DP); issue(k + DP); }
            sfor<0, 4>([&](auto TT) AI {
                constexpr int T = decltype(TT)::v, kk = k - 1 + (T >> 1), c = T & 1;
                const v4u f = {lo[T][0], lo[T][1], hi[T][0], hi[T][1]};
                if constexpr (!Z::lds_step(kk)) { constexpr int ri = Z::reg_idx(kk); wt[ri][c] = as8(f); if (ri < 32) asm volatile("" : "+a"(wt[ri][c])); else asm volatile("" : "+v"(wt[ri][c])); }
                else *(l4*)(smem + wb + (Z::lds_idx(kk) * 2 + c) * 1024) = f;
            });
        });
        else sfor<0, NK>([&](auto K) AI {
            constexpr int k = decltype(K)::v;
            WV(2 * (NK - 1 - k < DP - 1 ? NK - 1 - k : DP - 1));
            if constexpr (G) { BAR; if constexpr (k + DP < NK) issue(k + DP); }
            constexpr int o0 = (k & 7) * 2048, o1 = o0 + (G ? 32 : 64);
            v2u lo[2], hi[2];
            tra<o0, o0 + 512>(lo[0], hi[0], ta); tra<o1, o1 + 512>(lo[1], hi[1], ta);
            trw(lo[0], hi[0], lo[1], hi[1]);
            if constexpr (!G && k + DP < NK) issue(k + DP);
#pragma unroll
            for (int c = 0; c < 2; ++c) {
                const v4u f = {lo[c][0], lo[c][1], hi[c][0], hi[c][1]};
                if constexpr (!Z::lds_step(k)) { constexpr int ri = Z::reg_idx(k); wt[ri][c] = as8(f); if (ri < 32) asm volatile("" : "+a"(wt[ri][c])); else asm volatile("" : "+v"(wt[ri][c])); }
                else *(l4*)(smem + wb + (Z::lds_idx(k) * 2 + c) * 1024) = f;
            }
        });
    }
    auto piece = [&](int slot, int i, unsigned abase) AI {
        const int k = Z::S(slot) + i * 4;
        __builtin_amdgcn_raw_ptr_buffer_load_lds(rs, smem + L_RING + (k + w) * 1024, 16, xb, abase + ((k >> 3) * 16384 + (k & 7) * 1024), 0, AUX_SC1);
    };
    const unsigned wof = w * 1024 - L_RING;
    const unsigned fh = (lane >> 5) * 1024 + ((lane >> 4) & 1) * 512 + (lane & 15) * 16;
    unsigned xh = L_RING + fh;
    if (M) asm volatile("" : "+v"(xh));
    auto rdx = [&](int k) AI -> bf16x8 { return M ? *(l8*)(smem + xh + (k >> 1) * 2048 + (k & 1) * 256) : *(l8*)(smem + xb + k * 1024); };
    auto rdw = [&](int k, int c) AI -> bf16x8 { return *(l8*)(smem + wb + (Z::lds_idx(k) * 2 + c) * 1024); };
    const unsigned pso = w == 0 ? e.fset : OOB, fls = w == 0 ? 256 : 0, flb = w == 0 ? L_FL : L_FL + 1024;
    unsigned pk[16];
#pragma unroll
    for (int i = 0; i < 16; ++i) pk[i] = 0;
    unsigned fl_so = OOB, fl_val = 0;
    unsigned fo0 = e.fown0 + w * e.fown_w, fo1 = e.fown1 + w * e.fown_w;
    asm volatile("" : "+s"(fo0), "+s"(fo1));
    unsigned ovo = EPI == 2 ? w * 2048 + lane * 16 : (w >> 1) * 16384 + (w & 1) * 4096 + (EPI == 3 ? ((lane >> 4) & 1) * 1024 + (lane >> 5) * 256 + (lane & 15) * 16 : M ? fh : lane * 16);
    asm volatile("" : "+v"(ovo));
    unsigned ob = OOB2;
    float gx[2], ge[2];
    f32x16 acc[2];
    f32x4 ah[8];
    bf16x8 xf[M ? NK : XD + 1], wl[2];
    auto cvt = [&](int c, int q0, int q1) AI {
#pragma unroll
        for (int q = q0; q < q1; ++q) { pk[8 * c + q] = pack2(acc[c][2 * q], acc[c][2 * q + 1]); asm volatile("" : "+v"(pk[8 * c + q])); }
    };
    auto sto = [&](int s) AI { const int b = EPI == 2 ? 8 * s : 4 * s; st16(v4u{pk[b], pk[b + 1], pk[b + 2], pk[b + 3]}, rs, ob + (EPI == 3 ? (s >> 1) * 2048 + (s & 1) * 512 : M ? (s >> 1) * 2048 + (s & 1) * 256 : s * 1024)); };
    auto cvh = [&](int p) AI {
        const int q = (p & 3) * 2 + (p >> 2), b = (q >> 2) * 8 + (q & 1) * 4 + (q & 2);
        asm volatile("v_cvt_pk_bf16_f32 %0, %2, %3\n\tv_cvt_pk_bf16_f32 %1, %4, %5" : "=&v"(pk[b]), "=&v"(pk[b + 1]) : "v"(ah[q][0]), "v"(ah[q][1]), "v"(ah[q][2]), "v"(ah[q][3]));
    };
    auto epi = [&](auto P, auto KA, auto KB) AI {
        constexpr int p = decltype(P)::v, g = (p / 4) * 8 + p % 4;
        geglu<decltype(KA)::v, decltype(KB)::v>(gx[0], gx[1], ge[0], ge[1], pk[g], pk[g + 4], decltype(KB)::v > 21 ? pk[((p + 1) / 4 * 8 + (p + 1) % 4) & 15] : xb);
    };
    int c0 = 0, c1 = NSEG;
    int nxt = -1, cur = 0;
    unsigned an = OOB, a3 = OOB, a0 = 0, a1 = 0;
#pragma unroll
    for (int i = 0; i < 16; ++i) { acc[0][i] = 0; acc[1][i] = 0; if (M) ah[i & 7] = f32x4{0, 0, 0, 0}; }
    auto take = [&](int l) AI { nxt = l ? 256 | c1 : c0; an = sd(l ? e.in1 + c1 * e.in_st : e.in0 + c0 * e.in_st) + wof; c0 += 1 - l; c1 += l; };
    auto start = [&]() AI -> bool {
        if (w == 0) {
            unsigned spins = 0; int d;
            int f;
            for (;;) {
                CB;
                f = (int)ld4(rs, l4g, e.fset);
                const bool ok0 = ((unsigned)__ballot(f < c0) & e.fmask) == 0u, ok1 = ((unsigned)(__ballot(f < c1) >> 32) & e.fmask) == 0u;
                d = ok0 ? 0 : 1;
                if (ok0 | ok1) break;
                if (spin_bail(spins, rs)) { d = 2; break; }
                __builtin_amdgcn_s_sleep(DSLP);
            }
            *(l1*)(size_t)l4i = f;
            *(l1*)(smem + L_FL + 1280) = d;
            WL0;
        }
        BAR;
        const int d = RFL(*(l1*)(smem + L_FL + 1280));
        if (d == 2) return false;
        take(d);
        a0 = sd(e.in0 + c0 * e.in_st) + wof; a1 = sd(e.in1 + c1 * e.in_st) + wof;
#pragma unroll
        for (int s = 0; s < 2; ++s)
#pragma unroll
            for (int i = 0; i < Z::np(s); ++i) piece(s, i, an);
        WV(Z::N1 ? 0 : Z::np(1));
        BAR;
#pragma unroll
        for (int i = 0; i < XD; ++i) xf[i] = rdx(i);
        return true;
    };
    WL0;
    BAR;
    TSTAMP(e.prof);
    if (!start()) return;
    unsigned obs = 0, fs_n = 0, m0 = 0, m1 = 0, o0 = 0, o1 = 0;
    u64 b0 = 0, b1 = 0;
    int fw = 0;
    unsigned mw = (unsigned)(size_t)smem + L_RING + w * 1024;
    unsigned mf1 = (unsigned)(size_t)smem + flb + fls, mf2 = mf1 + fls;
    static_assert(!M || FD == 1, "flags");
    if (M) asm volatile("" : "+s"(mw), "+s"(mf1), "+s"(mf2));
#define AT(a, b, hb) (!M && i == (a) && h)
#define AM(b, hb) (M && i == (b) && h == (hb))
    auto ctl = [&](auto I, auto H) AI {
        constexpr int i = decltype(I)::v & 255, h = decltype(H)::v;
        if constexpr (EPI == 3 ? h && i % Z::PS == Z::PS - 3 : i == Z::UPD && h) { ob = ovo + (obs + (i / Z::PS) * 131072); asm volatile("" : "+v"(ob)); }
        if constexpr (i == Z::UPD + 2 && h == !M) { fl_so = fs_n; fl_val = cur & 255; asm volatile("" : "+v"(fl_val)); asm volatile("" : "+s"(fl_so)); }
        if constexpr (AT(Z::D3 + 4, Z::D3 + 6, 0)) { a0 = e.in0 + c0 * e.in_st; asm volatile("" : "+s"(a0)); }
        if constexpr (AT(Z::D3 + 6, Z::D3 + 6, 1)) { a1 = e.in1 + c1 * e.in_st; asm volatile("" : "+s"(a1)); }
        if constexpr (AT(Z::D3 + 7, Z::D3 + 10, 0)) { a0 = sd(a0) + wof; asm volatile("" : "+s"(a0)); }
        if constexpr (AT(Z::D3 + 8, Z::D3 + 10, 1)) { a1 = sd(a1) + wof; asm volatile("" : "+s"(a1)); }
        if constexpr (AT(Z::D3 + 10, Z::D3 + 14, 0) && EPI != 3) { obs = ((cur & 255) & e.out_mask) * e.out_st; asm volatile("" : "+s"(obs)); }
        if constexpr (AT(Z::D3 + 12, Z::D3 + 14, 1)) { if (EPI != 3) obs += (cur >> 8) ? e.out1 : e.out0; fs_n = (cur >> 8) ? fo1 : fo0; asm volatile("" : "+s"(obs), "+s"(fs_n)); }
        if constexpr (AT(Z::D3 + 14, Z::D3 + 18, 0) && EPI != 3) { obs = sd(obs); asm volatile("" : "+s"(obs)); }
        if constexpr (!M && h && (i == Z::D2 || i == Z::D3)) { b0 = __ballot(fw < c0); b1 = __ballot(fw < c1); asm volatile("" : "+s"(b0), "+s"(b1)); }
        if constexpr (AT(Z::D2 + 1, Z::D2 + 1, 0) || AT(Z::D3 + 1, Z::D3 + 1, 1)) { m0 = (unsigned)b0 & e.fmask; m1 = (unsigned)(b1 >> 32) & e.fmask; asm volatile("" : "+s"(m0), "+s"(m1)); }
        if constexpr (AT(Z::D2 + 1, Z::D2 + 1, 1)) { nxt = c1 | 256; an = OOB; asm volatile("" : "+s"(nxt), "+s"(an)); }
        if constexpr (AT(Z::D2 + 2, Z::D2 + 2, 0)) asm volatile("s_cmp_eq_u32 %2, 0\n\ts_cselect_b32 %0, %0, -1\n\ts_cselect_b32 %1, %3, %1\n\ts_cselect_b32 %2, 1, 0" : "+s"(nxt), "+s"(an), "+s"(m1) : "s"(a1) : "scc");
        if constexpr (AT(Z::D2 + 4, Z::D2 + 2, 1)) asm volatile("s_cmp_eq_u32 %3, 0\n\ts_cselect_b32 %0, %4, %0\n\ts_cselect_b32 %1, %5, %1\n\ts_cselect_b32 %2, 0, %2" : "+s"(nxt), "+s"(an), "+s"(m1) : "s"(m0), "s"(c0), "s"(a0) : "scc");
        if constexpr (AT(Z::D2 + 6, Z::D2 + 6, 0)) asm volatile("s_cmp_eq_u32 %2, 0\n\ts_addc_u32 %0, %0, 0\n\ts_add_u32 %1, %1, %3" : "+s"(c0), "+s"(c1) : "s"(m0), "s"(m1) : "scc");
        if constexpr (Z::OV && i == Z::D2 + 10 && h == !M) asm volatile("s_cmp_lt_u32 %1, 256\n\ts_cselect_b32 %0, 1, 0" : "=s"(o0) : "s"(nxt) : "scc");
        if constexpr (Z::OV && i == Z::D2 + 12 && h == !M) asm volatile("s_cmp_lt_i32 %1, 0\n\ts_cselect_b32 %0, 0, 1" : "=s"(o1) : "s"(nxt) : "scc");
        if constexpr (Z::OV && (AT(Z::D3 + 2, 0, 0) || AM(Z::D3 + 3, 0))) asm volatile("s_or_b32 %0, %0, %2\n\ts_or_b32 %1, %1, %3\n\ts_min_u32 %1, %0, %1" : "+s"(m0), "+s"(m1) : "s"(o0), "s"(o1) : "scc");
        if constexpr (!Z::OV && AT(Z::D3 + 2, Z::D3 + 2, 0)) asm volatile("s_min_u32 %0, %1, %0\n\ts_cmp_lt_i32 %2, 0\n\ts_cselect_b32 %0, %0, 1" : "+s"(m1) : "s"(m0), "s"(nxt) : "scc");
        if constexpr (AM(18, 0)) { obs = ((cur & 255) & e.out_mask) * e.out_st; asm volatile("" : "+s"(obs)); }
        if constexpr (AM(20, 0)) { m0 = (cur >> 8) ? e.out1 : e.out0; fs_n = (cur >> 8) ? fo1 : fo0; asm volatile("" : "+s"(m0), "+s"(fs_n)); }
        if constexpr (AM(22, 0)) { obs += m0; asm volatile("" : "+s"(obs)); }
        if constexpr (AM(24, 0)) { obs = sd(obs); asm volatile("" : "+s"(obs)); }
        if constexpr (AM(Z::D2, 0)) { nxt = c1 | 256; asm volatile("" : "+s"(nxt)); }
        if constexpr (AM(Z::D2 + 1, 1) || AM(Z::D3 + 1, 1)) { b1 = __ballot(fw < c1); asm volatile("" : "+s"(b1)); }
        if constexpr (AM(Z::D2 + 2, 0)) asm volatile("s_and_b32 %2, %3, %4\n\ts_cselect_b32 %0, -1, %0\n\ts_cselect_b32 %1, %6, %5" : "+s"(nxt), "=&s"(an), "=&s"(m1) : "s"((unsigned)(b1 >> 32)), "s"(e.fmask), "s"(a1), "n"(OOB) : "scc");
        if constexpr (AM(Z::D2 + 2, 1)) { b0 = __ballot(fw < c0); asm volatile("" : "+s"(b0)); }
        if constexpr (AM(Z::D2 + 4, 0)) asm volatile("s_and_b32 %2, %3, %4\n\ts_cselect_b32 %0, %0, %5\n\ts_cselect_b32 %1, %1, %6" : "+s"(nxt), "+s"(an), "=&s"(m0) : "s"((unsigned)b0), "s"(e.fmask), "s"(c0), "s"(a0) : "scc");
        if constexpr (AM(Z::D2 + 6, 0)) asm volatile("s_cmp_eq_u32 %1, 0\n\ts_addc_u32 %0, %0, 0" : "+s"(c0) : "s"(m0) : "scc");
        if constexpr (AM(Z::D2 + 8, 0)) asm volatile("s_cmp_gt_i32 %1, 255\n\ts_addc_u32 %0, %0, 0" : "+s"(c1) : "s"(nxt) : "scc");
        if constexpr (AM(Z::D3 + 2, 0)) { m1 = (unsigned)(b1 >> 32) & e.fmask; asm volatile("" : "+s"(m1)); SB; b0 = __ballot(fw < c0); asm volatile("" : "+s"(b0)); }
        if constexpr (AM(Z::D3 + 2, 1)) { m0 = (unsigned)b0 & e.fmask; asm volatile("" : "+s"(m0)); }
        if constexpr (!Z::OV && AM(Z::D3 + 3, 0)) asm volatile("s_min_u32 %0, %1, %0\n\ts_cmp_lt_i32 %2, 0\n\ts_cselect_b32 %0, %0, 1" : "+s"(m1) : "s"(m0), "s"(nxt) : "scc");
        if constexpr (AM(Z::D3 + 4, 0)) { a0 = e.in0 + c0 * e.in_st; asm volatile("" : "+s"(a0)); }
        if constexpr (AM(Z::D3 + 6, 0)) { a1 = e.in1 + c1 * e.in_st; asm volatile("" : "+s"(a1)); }
        if constexpr (AM(Z::D3 + 8, 0)) { a0 = sd(a0); asm volatile("" : "+s"(a0)); }
        if constexpr (AM(Z::D3 + 10, 0)) { a1 = sd(a1); asm volatile("" : "+s"(a1)); }
        if constexpr (AM(Z::D3 + 12, 0)) { a0 += wof; a1 += wof; asm volatile("" : "+s"(a0), "+s"(a1)); }
    };
#undef AT
#undef AM
    auto slot = [&](auto I) AI {
        constexpr bool nb = decltype(I)::v >= 256;
        constexpr int i = decltype(I)::v & 255, k = Z::kof(i), c = Z::cof(i), s = i / 2, bt = Z::bnd(i);
        constexpr bool first = k % Z::PER == 0;
        constexpr int j = i % Z::PS;
        if constexpr (M) {
            auto mf = [&](auto R) AI {
                constexpr int r = decltype(R)::v, q = (i & 1) * 4 + r * 2 + (s & 1), xi = s, ws = (s & ~1) + (i & 1), wc = r;
                if constexpr (Z::lds_step(ws)) mh<0, (i < 4)>(ah[q], wl[wc], xf[xi]);
                else mh<(Z::reg_idx(ws) < 32), (i < 4)>(ah[q], wt[Z::reg_idx(ws)][wc], xf[xi]);
                SB;
            };
            constexpr int pc = Z::piece_of(i), kp = Z::S((Z::tsl(s) + 2) & 3) + pc * 4;
            unsigned so = 0;
            mf(IC<0>{});
            if constexpr (i == 0) { cur = nxt; a3 = an; }
            if constexpr (bt >= 0) { if (!(AB & 64)) WV(Z::cnt(bt)); if (!(AB & 2)) BAR; }
            if constexpr (pc >= 0) asm volatile("s_add_u32 %0, %1, %2\n\ts_add_u32 m0, %3, %4" : "=s"(so) : "s"(Z::tsl(s) < 2 ? a3 : an), "n"((kp >> 3) * 16384 + (kp & 7) * 1024), "s"(mw), "n"(kp * 1024) : "scc", "m0");
            if constexpr (Z::fl_of(i)) asm volatile("s_mov_b32 m0, %0" : : "s"(Z::tsl(s) == 1 ? mf1 : mf2) : "m0");
            if constexpr (Z::pub_of(i) && !(AB & 128)) { WV(Z::between(Z::last_st, i)); st4(fl_val, rs, 0, fl_so); }
            if constexpr (Z::st_of(i) >= 0 && !(AB & 1)) sto(Z::st_of(i));
            if constexpr (Z::xr(i, 0) >= 0) { xf[Z::xr(i, 0)] = rdx(Z::xr(i, 0)); SB; }
            if constexpr (!(AB & 32) && Z::w_of(i) >= 0 && Z::st_of(i) < 0) wl[Z::w_of(i) & 1] = rdw(Z::w_of(i) >> 1, Z::w_of(i) & 1);
            SB;
            if constexpr (Z::cv_of(i, 0) >= 0 && !nb && !(AB & 8)) cvh(Z::cv_of(i, 0));
            ctl(I, IC<0>{});
            SB;
            mf(IC<1>{});
            if constexpr (i & 1) { __builtin_amdgcn_s_waitcnt(0xC07F | (Z::xw(i) << 8)); SB; }
            if constexpr (pc >= 0 && !(AB & 4)) asm volatile("buffer_load_dwordx4 %0, %1, %2 offen sc1 lds" : : "v"(xb), "s"(rs), "s"(so) : "memory");
            if constexpr (Z::xr(i, 1) >= 0) { xf[Z::xr(i, 1)] = rdx(Z::xr(i, 1)); SB; }
            if constexpr (bt >= 2) fw = (int)*(l1*)(size_t)(l4i + (bt - FD) * 256);
            if constexpr (!(AB & 32) && Z::w_of(i) >= 0 && Z::st_of(i) >= 0) wl[Z::w_of(i) & 1] = rdw(Z::w_of(i) >> 1, Z::w_of(i) & 1);
            if constexpr (Z::fl_of(i)) asm volatile("buffer_load_dword %0, %1, %2 offen sc1 lds" : : "v"(l4g), "s"(rs), "s"(pso) : "memory");
            SB;
            if constexpr (Z::cv_of(i, 1) >= 0 && !nb && !(AB & 8)) cvh(Z::cv_of(i, 1));
        } else {
        if constexpr (Z::lds_step(k)) { if constexpr (first) mf_v0<SW>(acc[c], wl[c], xf[k % (XD + 1)]); else mf_v<SW>(acc[c], wl[c], xf[k % (XD + 1)]); }
        else if constexpr (Z::reg_idx(k) < 32) { if constexpr (first) mf_a0<SW>(acc[c], wt[Z::reg_idx(k)][c], xf[k % (XD + 1)]); else mf_a<SW>(acc[c], wt[Z::reg_idx(k)][c], xf[k % (XD + 1)]); }
        else { if constexpr (first) mf_v0<SW>(acc[c], wt[Z::reg_idx(k)][c], xf[k % (XD + 1)]); else mf_v<SW>(acc[c], wt[Z::reg_idx(k)][c], xf[k % (XD + 1)]); }
        SB;
        if constexpr (i == 0) { cur = nxt; a3 = an; if (EPI == 3) obs = sd(e.out0 + (cur & e.out_mask) * e.out_st); }
        if constexpr (bt >= 0) { WV(Z::cnt(bt)); BAR; }
        constexpr bool cve = j == Z::PS - 2 || (j == 0 && !nb);
        if constexpr (cve && bt < 0) { if constexpr (j == 0) __builtin_amdgcn_s_waitcnt(0xC07F | (1 << 8)); else asm volatile("s_nop 0"); SB; }
        if constexpr (j == Z::PS - 1) { __builtin_amdgcn_s_waitcnt(0xC07F | ((2 + (Z::w_of(i - 2) >= 0) + (Z::w_of(i - 4) >= 0)) << 8)); SB; }
        if constexpr (Z::st_of(i) >= 0) { sto(Z::st_of(i)); SB; }
        if constexpr (!(i & 1)) xf[(s + XD) % (XD + 1)] = rdx((s + XD) % NK);
        if constexpr (bt >= 2) fw = (int)*(l1*)(size_t)(l4i + (bt - FD) * 256);
        if constexpr (Z::w_of(i) >= 0) wl[Z::w_of(i) & 1] = rdw(Z::w_of(i) >> 1, Z::w_of(i) & 1);
        if constexpr (Z::fl_of(i)) __builtin_amdgcn_raw_ptr_buffer_load_lds(rs, smem + flb + Z::tsl(s) * fls, 4, l4g, pso, 0, AUX_SC1);
        if constexpr (Z::piece_of(i) >= 0) piece((Z::tsl(s) + 2) & 3, Z::piece_of(i), Z::tsl(s) < 2 ? a3 : an);
        if constexpr (Z::pub_of(i)) { WV(Z::between(Z::last_st, i) + Z::nops(i) - 1); st4(fl_val, rs, 0, fl_so); }
        SB;
        if constexpr (EPI == 2 && i < NS - 1) {
            constexpr int ka = Z::G.a[i], kb = Z::G.a[i + 1];
            if constexpr (i == 1) asm volatile("" : "=v"(gx[0]), "=v"(gx[1]), "=v"(ge[0]), "=v"(ge[1]));
            if constexpr (kb > ka && !nb) { epi(IC<ka / 21>{}, IC<ka % 21>{}, IC<kb - ka / 21 * 21>{}); SB; }
        }
        if constexpr (cve) { asm volatile("s_nop %0" ::"n"(Z::st_of(i) >= 0 ? 0 : 1)); SB; cvt(j == 0, 0, 2); }
        if constexpr (j == 1 && !nb) cvt(1, 2, 8);
        if constexpr (j == Z::PS - 1) cvt(0, 2, 8);
        }
        ctl(I, IC<1>{});
        if constexpr (i == Z::D3 + 3) {
            if (__builtin_expect(m1 == 0u, 0)) {
                if (Z::OV) c1 -= o1;
                take(m0 != 0u);
#pragma unroll
                for (int j = 0; j < Z::NP0; ++j) piece(0, j, an);
            }
        }
        SB;
    };
    for (;;) {
        sfor<256, 256 + Z::ROT>(slot);
        do {
            sfor<Z::ROT, NS>(slot);
            sfor<0, Z::ROT>(slot);
        } while (__builtin_expect(nxt >= 0, 1));
        if constexpr (Z::ROT == 0) {
            asm volatile("s_nop 7\n\ts_nop 7" : "+v"(acc[1]));
            cvt(1, 0, 8);
        }
        if constexpr (EPI == 2) {
            constexpr int ka = Z::G.a[Z::ROT];
            static_assert(Z::G.st[0] >= Z::ROT, "store");
            sfor<ka / 21, 8>([&](auto P) AI { epi(P, IC<(decltype(P)::v == ka / 21 ? ka % 21 : 0)>{}, IC<21>{}); if constexpr (decltype(P)::v == 3) sto(0); });
            sto(1);
        } else sfor<Z::ROT, Z::last_st + 1>([&](auto I) AI { if constexpr (Z::st_of(decltype(I)::v) >= 0) sto(Z::st_of(decltype(I)::v)); });
        ob = OOB2;
        WV0;
        st4(fl_val, rs, 0, fl_so);
        fl_so = OOB;
        if (c0 + c1 >= 2 * NSEG) break;
        WL0;
        if (!start()) break;
    }
}

DEVI float red_rows(float s) {
    s += __uint_as_float(__builtin_amdgcn_update_dpp(0, __float_as_uint(s), 0x128, 0xf, 0xf, false));
    s = red_x16(s, false); return red_x32(s, false);
}
DEVI f32x2 up2(unsigned u) { return f32x2{bflo(u), bfhi(u)}; }
DEVI float ln_inv(float var) { return __builtin_amdgcn_rcpf(rbf(__builtin_amdgcn_sqrtf(rbf(rbf(fmaxf(var, 0.f)) + 1e-5f)))); }
DEVI void ln0(const Args& a, const int w, int b) {
    const int lane = lane_id(), row = w * 8 + (lane & 7);
    const rsrc_t rs = mkrs(a.ws, WS_RS), rx = mkrs(a.x, 2048 * 2048);
    v4u u[16];
#pragma unroll
    for (int i = 0; i < 16; ++i) u[i] = __builtin_amdgcn_raw_buffer_load_b128(rx, row * 2048 + (lane >> 3) * 16 + i * 128, b * 65536, 0);
    const float k0 = __shfl(bflo(u[0][0]), lane & 7);
    f32x2 s1 = {0.f, 0.f}, s2 = {0.f, 0.f};
#pragma unroll
    for (int i = 0; i < 16; ++i)
#pragma unroll
        for (int e = 0; e < 4; ++e) { const f32x2 d = up2(u[i][e]) - k0; s1 += d; s2 += d * d; }
    const float s = red_rows(s1[0] + s1[1]) * (1.0f / 1024.0f), q = red_rows(s2[0] + s2[1]) * (1.0f / 1024.0f);
    const float inv = ln_inv(q - s * s), c = -rbf(k0 + s) * inv;
    const unsigned o = sd(O_XN + b * 131072) + (lane >> 3) * 512 + row * 16;
#pragma unroll
    for (int i = 0; i < 16; ++i) {
        v4u r;
#pragma unroll
        for (int e = 0; e < 4; ++e) { const f32x2 z = up2(u[i][e]) * inv + c; r[e] = pack2(z[0], z[1]); }
        st16(r, rs, o + (i >> 1) * 16384 + (i & 1) * 4096);
    }
#pragma unroll
    for (int i = 0; i < 16; ++i) st16(u[i], rs, o + (O_XR - O_XN) + (i >> 1) * 16384 + (i & 1) * 4096);
    st16(u[0], rs, sd(O_ST + b * 131072) + w * 1024 + lane * 16); st16(u[0], rs, sd(O_ST + SD + b * 131072) + w * 1024 + lane * 16);
    WV0;
    st4(b, rs, 0, F_X0 + b * 128 + w * 4);
}
#define RSLP 1
template <int L> DEVI void r_role(const Args& a, const int w, lds_char* smem, int rq) {
    const int lane = lane_id(), row = rq * 8 + (lane & 7), g0 = w * 32 + (lane >> 3);
    const rsrc_t rs = mkrs(a.ws, WS_RS), ro = mkrs(a.out, 2048 * 2048);
    const unsigned fo = w * 32768 + (lane >> 3) * 512 + row * 16, xo = row * 2048 + g0 * 16;
    auto io = [](int i) -> unsigned { return (i >> 1) * 16384 + (i & 1) * 4096; };
    lf1* red = (lf1*)smem;
    const unsigned fd = F_D + L * SD + ((lane & 12) + w) * 128 + (lane & 3) * 4;
    for (int b = 0; b < NSEG; ++b) {
        unsigned spins = 0;
        for (;;) {
            CB;
            if (__ballot((int)ld4(rs, fd, 0) < b) == 0ull) break;
            if (spin_bail(spins, rs)) return;
            __builtin_amdgcn_s_sleep(RSLP);
        }
        const unsigned sxr = sd(O_XR + L * SD + b * 131072), ssl = sd(O_SL + L * SD + b * 524288);
        v4u un[4], t[4][4];
#pragma unroll
        for (int i = 0; i < 4; ++i) {
#pragma unroll
            for (int k = 0; k < 4; ++k) t[i][k] = ld16(rs, fo + io(i), ssl + k * 131072);
            un[i] = ld16(rs, fo + io(i), sxr);
        }
        f32x2 d[4][4], s1 = {0.f, 0.f}, s2 = {0.f, 0.f};
        v4u u[4];
        float k0 = 0.f;
        auto dev = [&](int i) AI {
#pragma unroll
            for (int e = 0; e < 4; ++e) { d[i][e] -= k0; s1 += d[i][e]; s2 += d[i][e] * d[i][e]; }
        };
#pragma unroll
        for (int i = 0; i < 4; ++i) {
#pragma unroll
            for (int e = 0; e < 4; ++e) {
                d[i][e] = up2(un[i][e]) + ((up2(t[i][0][e]) + up2(t[i][1][e])) + (up2(t[i][2][e]) + up2(t[i][3][e])));
                u[i][e] = pack2(d[i][e][0], d[i][e][1]);
            }
            if (L) __builtin_amdgcn_raw_buffer_store_b128(u[i], ro, xo + i * 128 + b * 65536, 0, 0);
            else { if (i == 0) k0 = __shfl(d[0][0][0], lane & 7); else { if (i == 1) dev(0); dev(i); } }
            SB;
        }
        if constexpr (!L) {
            lf1* rp = red + (b & 1) * 64 + (lane & 7) * 8;
            const float s = red_rows(s1[0] + s1[1]), q = red_rows(s2[0] + s2[1]);
            if (lane < 16) rp[(lane >> 3) * 4 + w] = lane < 8 ? k0 + s * (1.0f / 256.0f) : q - s * s * (1.0f / 256.0f);
            const unsigned oxn = sd(O_XN + SD + b * 131072), oxr = sd(O_XR + SD + b * 131072);
#pragma unroll
            for (int i = 0; i < 4; ++i) st16(u[i], rs, fo + io(i) + oxr);
            WL0; BAR;
            const f32x4 sa = *(lf4*)rp, qa = *(lf4*)(rp + 4);
            const float mean = ((sa[0] + sa[1]) + (sa[2] + sa[3])) * 0.25f;
            const f32x4 da = sa - mean;
            const float var = ((qa[0] + qa[1]) + (qa[2] + qa[3])) * (1.0f / 1024.0f) + ((da[0] * da[0] + da[1] * da[1]) + (da[2] * da[2] + da[3] * da[3])) * 0.25f;
            const float inv = ln_inv(var), c = (k0 - rbf(mean)) * inv;
#pragma unroll
            for (int i = 0; i < 4; ++i) {
                v4u o;
#pragma unroll
                for (int e = 0; e < 4; ++e) { const f32x2 z = d[i][e] * inv + c; o[e] = pack2(z[0], z[1]); }
                st16(o, rs, fo + io(i) + oxn);
                SB;
            }
            WV0;
            st4(b, rs, 0, F_X + SD + rq * 128 + w * 4);
        }
        if (rq == 0 && w == 0) TSTAMP(200000 + (L * 2 + 1) * 64 + b);
    }
}
#define QK_ASM \
"v_mbcnt_lo_u32_b32 v255, -1, 0\nv_mbcnt_hi_u32_b32 v255, -1, v255\nv_lshlrev_b32 v255, 2, v255\nv_add_u32 v255, %[tb], v255\nds_read_b32 v0, v255 offset:0\nds_read_b32 v1, v255 offset:256\nds_read_b32 v2, v255 offset:512\nds_read_b32 v3, v255 offset:768\n" \
"ds_read_b32 v4, v255 offset:1024\nds_read_b32 v5, v255 offset:1280\nds_read_b32 v6, v255 offset:1536\nds_read_b32 v7, v255 offset:1792\nds_read_b32 v8, v255 offset:2048\nds_read_b32 v9, v255 offset:2304\nds_read_b32 v10, v255 offset:2560\nds_read_b32 v11, v255 offset:2816\n" \
"ds_read_b32 v12, v255 offset:3072\nds_read_b32 v13, v255 offset:3328\nds_read_b32 v14, v255 offset:3584\nds_read_b32 v15, v255 offset:3840\nds_read_b32 v92, v255 offset:4096\nds_read_b32 v93, v255 offset:4352\nds_read_b32 v94, v255 offset:4608\ns_waitcnt lgkmcnt(0)\n" \
"s_add_u32 m0, %[lw], 16384\ns_add_u32 s44, %[wo], 0\nbuffer_load_dwordx4 v92, %[rw], s44 offen nt lds\ns_add_u32 m0, %[lw], 17408\ns_add_u32 s44, %[wo], 65536\nbuffer_load_dwordx4 v92, %[rw], s44 offen nt lds\ns_add_u32 m0, %[lw], 18432\ns_add_u32 s44, %[wo], 131072\n" \
"buffer_load_dwordx4 v92, %[rw], s44 offen nt lds\ns_add_u32 m0, %[lw], 19456\ns_add_u32 s44, %[wo], 196608\nbuffer_load_dwordx4 v92, %[rw], s44 offen nt lds\ns_add_u32 m0, %[lw], 20480\ns_add_u32 s44, %[wo], 262144\nbuffer_load_dwordx4 v92, %[rw], s44 offen nt lds\ns_add_u32 m0, %[lw], 21504\n" \
"s_add_u32 s44, %[wo], 327680\nbuffer_load_dwordx4 v92, %[rw], s44 offen nt lds\ns_add_u32 m0, %[lw], 22528\ns_add_u32 s44, %[wo], 393216\nbuffer_load_dwordx4 v92, %[rw], s44 offen nt lds\ns_add_u32 m0, %[lw], 23552\ns_add_u32 s44, %[wo], 458752\nbuffer_load_dwordx4 v92, %[rw], s44 offen nt lds\n" \
"s_add_u32 m0, %[tb], 0\ns_add_u32 s44, %[wo], 524288\nbuffer_load_dwordx4 v92, %[rw], s44 offen nt lds\ns_add_u32 m0, %[tb], 1024\ns_add_u32 s44, %[wo], 589824\nbuffer_load_dwordx4 v92, %[rw], s44 offen nt lds\ns_add_u32 m0, %[tb], 2048\ns_add_u32 s44, %[wo], 655360\n" \
"buffer_load_dwordx4 v92, %[rw], s44 offen nt lds\ns_add_u32 m0, %[tb], 3072\ns_add_u32 s44, %[wo], 720896\nbuffer_load_dwordx4 v92, %[rw], s44 offen nt lds\ns_add_u32 m0, %[tb], 4096\ns_add_u32 s44, %[wo], 786432\nbuffer_load_dwordx4 v92, %[rw], s44 offen nt lds\ns_add_u32 m0, %[tb], 5120\n" \
"s_add_u32 s44, %[wo], 851968\nbuffer_load_dwordx4 v92, %[rw], s44 offen nt lds\ns_add_u32 m0, %[tb], 6144\ns_add_u32 s44, %[wo], 917504\nbuffer_load_dwordx4 v92, %[rw], s44 offen nt lds\ns_add_u32 m0, %[tb], 7168\ns_add_u32 s44, %[wo], 983040\nbuffer_load_dwordx4 v92, %[rw], s44 offen nt lds\n" \
"s_add_u32 m0, %[tb], 8192\ns_add_u32 s44, %[wo], 1048576\nbuffer_load_dwordx4 v92, %[rw], s44 offen nt lds\ns_add_u32 m0, %[tb], 9216\ns_add_u32 s44, %[wo], 1114112\nbuffer_load_dwordx4 v92, %[rw], s44 offen nt lds\ns_add_u32 m0, %[tb], 10240\ns_add_u32 s44, %[wo], 1179648\n" \
"buffer_load_dwordx4 v92, %[rw], s44 offen nt lds\ns_add_u32 m0, %[tb], 11264\ns_add_u32 s44, %[wo], 1245184\nbuffer_load_dwordx4 v92, %[rw], s44 offen nt lds\ns_add_u32 m0, %[lw], 0\ns_add_u32 s44, %[wo], 1310720\nbuffer_load_dwordx4 v92, %[rw], s44 offen nt lds\ns_add_u32 m0, %[lw], 1024\n" \
"s_add_u32 s44, %[wo], 1376256\nbuffer_load_dwordx4 v92, %[rw], s44 offen nt lds\ns_add_u32 m0, %[lw], 2048\ns_add_u32 s44, %[wo], 1441792\nbuffer_load_dwordx4 v92, %[rw], s44 offen nt lds\ns_add_u32 m0, %[lw], 3072\ns_add_u32 s44, %[wo], 1507328\nbuffer_load_dwordx4 v92, %[rw], s44 offen nt lds\n" \
"s_add_u32 m0, %[lw], 4096\ns_add_u32 s44, %[wo], 1572864\nbuffer_load_dwordx4 v92, %[rw], s44 offen nt lds\ns_add_u32 m0, %[lw], 5120\ns_add_u32 s44, %[wo], 1638400\nbuffer_load_dwordx4 v92, %[rw], s44 offen nt lds\ns_add_u32 m0, %[lw], 6144\ns_add_u32 s44, %[wo], 1703936\n" \
"buffer_load_dwordx4 v92, %[rw], s44 offen nt lds\ns_add_u32 m0, %[lw], 7168\ns_add_u32 s44, %[wo], 1769472\nbuffer_load_dwordx4 v92, %[rw], s44 offen nt lds\ns_add_u32 m0, %[lw], 8192\ns_add_u32 s44, %[wo], 1835008\nbuffer_load_dwordx4 v92, %[rw], s44 offen nt lds\ns_add_u32 m0, %[lw], 9216\n" \
"s_add_u32 s44, %[wo], 1900544\nbuffer_load_dwordx4 v92, %[rw], s44 offen nt lds\ns_add_u32 m0, %[lw], 10240\ns_add_u32 s44, %[wo], 1966080\nbuffer_load_dwordx4 v92, %[rw], s44 offen nt lds\ns_add_u32 m0, %[lw], 11264\ns_add_u32 s44, %[wo], 2031616\nbuffer_load_dwordx4 v92, %[rw], s44 offen nt lds\n" \
"s_add_u32 m0, %[lw], 12288\ns_add_u32 s44, %[wo], 2097152\nbuffer_load_dwordx4 v92, %[rw], s44 offen nt lds\ns_add_u32 m0, %[lw], 13312\ns_add_u32 s44, %[wo], 2162688\nbuffer_load_dwordx4 v92, %[rw], s44 offen nt lds\ns_add_u32 m0, %[lw], 14336\ns_add_u32 s44, %[wo], 2228224\n" \
"buffer_load_dwordx4 v92, %[rw], s44 offen nt lds\ns_add_u32 m0, %[lw], 15360\ns_add_u32 s44, %[wo], 2293760\nbuffer_load_dwordx4 v92, %[rw], s44 offen nt lds\ns_waitcnt vmcnt(32)\nds_read_b64_tr_b16 a[0:1], v94 offset:16384\nds_read_b64_tr_b16 a[2:3], v94 offset:16896\nds_read_b64_tr_b16 a[8:9], v94 offset:16392\n" \
"ds_read_b64_tr_b16 a[10:11], v94 offset:16904\nds_read_b64_tr_b16 a[16:17], v94 offset:16448\nds_read_b64_tr_b16 a[18:19], v94 offset:16960\nds_read_b64_tr_b16 a[24:25], v94 offset:16456\nds_read_b64_tr_b16 a[26:27], v94 offset:16968\ns_waitcnt lgkmcnt(0)\ns_add_u32 m0, %[lw], 16384\ns_add_u32 s44, %[wo], 2359296\n" \
"buffer_load_dwordx4 v92, %[rw], s44 offen nt lds\ns_add_u32 m0, %[lw], 17408\ns_add_u32 s44, %[wo], 2424832\nbuffer_load_dwordx4 v92, %[rw], s44 offen nt lds\ns_add_u32 m0, %[lw], 18432\ns_add_u32 s44, %[wo], 2490368\nbuffer_load_dwordx4 v92, %[rw], s44 offen nt lds\ns_add_u32 m0, %[lw], 19456\n" \
"s_add_u32 s44, %[wo], 2555904\nbuffer_load_dwordx4 v92, %[rw], s44 offen nt lds\ns_waitcnt vmcnt(32)\nds_read_b64_tr_b16 a[4:5], v94 offset:20480\nds_read_b64_tr_b16 a[6:7], v94 offset:20992\nds_read_b64_tr_b16 a[12:13], v94 offset:20488\nds_read_b64_tr_b16 a[14:15], v94 offset:21000\nds_read_b64_tr_b16 a[20:21], v94 offset:20544\n" \
"ds_read_b64_tr_b16 a[22:23], v94 offset:21056\nds_read_b64_tr_b16 a[28:29], v94 offset:20552\nds_read_b64_tr_b16 a[30:31], v94 offset:21064\ns_waitcnt lgkmcnt(0)\ns_add_u32 m0, %[lw], 20480\ns_add_u32 s44, %[wo], 2621440\nbuffer_load_dwordx4 v92, %[rw], s44 offen nt lds\ns_add_u32 m0, %[lw], 21504\n" \
"s_add_u32 s44, %[wo], 2686976\nbuffer_load_dwordx4 v92, %[rw], s44 offen nt lds\ns_add_u32 m0, %[lw], 22528\ns_add_u32 s44, %[wo], 2752512\nbuffer_load_dwordx4 v92, %[rw], s44 offen nt lds\ns_add_u32 m0, %[lw], 23552\ns_add_u32 s44, %[wo], 2818048\nbuffer_load_dwordx4 v92, %[rw], s44 offen nt lds\n" \
"s_waitcnt vmcnt(32)\nds_read_b64_tr_b16 a[32:33], v93 offset:0\nds_read_b64_tr_b16 a[34:35], v93 offset:512\nds_read_b64_tr_b16 v[88:89], v93 offset:8\nds_read_b64_tr_b16 v[90:91], v93 offset:520\nds_read_b64_tr_b16 a[40:41], v93 offset:64\nds_read_b64_tr_b16 a[42:43], v93 offset:576\nds_read_b64_tr_b16 a[48:49], v93 offset:72\n" \
"ds_read_b64_tr_b16 a[50:51], v93 offset:584\ns_waitcnt lgkmcnt(0)\ns_add_u32 m0, %[tb], 0\ns_add_u32 s44, %[wo], 2883584\nbuffer_load_dwordx4 v92, %[rw], s44 offen nt lds\ns_add_u32 m0, %[tb], 1024\ns_add_u32 s44, %[wo], 2949120\nbuffer_load_dwordx4 v92, %[rw], s44 offen nt lds\n" \
"s_add_u32 m0, %[tb], 2048\ns_add_u32 s44, %[wo], 3014656\nbuffer_load_dwordx4 v92, %[rw], s44 offen nt lds\ns_add_u32 m0, %[tb], 3072\ns_add_u32 s44, %[wo], 3080192\nbuffer_load_dwordx4 v92, %[rw], s44 offen nt lds\ns_waitcnt vmcnt(32)\nds_read_b64_tr_b16 a[36:37], v93 offset:4096\n" \
"ds_read_b64_tr_b16 a[38:39], v93 offset:4608\nds_read_b64_tr_b16 v[84:85], v93 offset:4104\nds_read_b64_tr_b16 v[86:87], v93 offset:4616\nds_read_b64_tr_b16 a[44:45], v93 offset:4160\nds_read_b64_tr_b16 a[46:47], v93 offset:4672\nds_read_b64_tr_b16 a[52:53], v93 offset:4168\nds_read_b64_tr_b16 a[54:55], v93 offset:4680\ns_waitcnt lgkmcnt(0)\n" \
"s_add_u32 m0, %[tb], 4096\ns_add_u32 s44, %[wo], 3145728\nbuffer_load_dwordx4 v92, %[rw], s44 offen nt lds\ns_add_u32 m0, %[tb], 5120\ns_add_u32 s44, %[wo], 3211264\nbuffer_load_dwordx4 v92, %[rw], s44 offen nt lds\ns_add_u32 m0, %[tb], 6144\ns_add_u32 s44, %[wo], 3276800\n" \
"buffer_load_dwordx4 v92, %[rw], s44 offen nt lds\ns_add_u32 m0, %[tb], 7168\ns_add_u32 s44, %[wo], 3342336\nbuffer_load_dwordx4 v92, %[rw], s44 offen nt lds\ns_waitcnt vmcnt(32)\nds_read_b64_tr_b16 a[56:57], v93 offset:8192\nds_read_b64_tr_b16 a[58:59], v93 offset:8704\nds_read_b64_tr_b16 a[64:65], v93 offset:8200\n" \
"ds_read_b64_tr_b16 a[66:67], v93 offset:8712\nds_read_b64_tr_b16 v[80:81], v93 offset:8256\nds_read_b64_tr_b16 v[82:83], v93 offset:8768\nds_read_b64_tr_b16 a[72:73], v93 offset:8264\nds_read_b64_tr_b16 a[74:75], v93 offset:8776\ns_waitcnt lgkmcnt(0)\ns_add_u32 m0, %[tb], 8192\ns_add_u32 s44, %[wo], 3407872\n" \
"buffer_load_dwordx4 v92, %[rw], s44 offen nt lds\ns_add_u32 m0, %[tb], 9216\ns_add_u32 s44, %[wo], 3473408\nbuffer_load_dwordx4 v92, %[rw], s44 offen nt lds\ns_add_u32 m0, %[tb], 10240\ns_add_u32 s44, %[wo], 3538944\nbuffer_load_dwordx4 v92, %[rw], s44 offen nt lds\ns_add_u32 m0, %[tb], 11264\n" \
"s_add_u32 s44, %[wo], 3604480\nbuffer_load_dwordx4 v92, %[rw], s44 offen nt lds\ns_waitcnt vmcnt(32)\nds_read_b64_tr_b16 a[60:61], v94 offset:0\nds_read_b64_tr_b16 a[62:63], v94 offset:512\nds_read_b64_tr_b16 a[68:69], v94 offset:8\nds_read_b64_tr_b16 a[70:71], v94 offset:520\nds_read_b64_tr_b16 v[76:77], v94 offset:64\n" \
"ds_read_b64_tr_b16 v[78:79], v94 offset:576\nds_read_b64_tr_b16 a[76:77], v94 offset:72\nds_read_b64_tr_b16 a[78:79], v94 offset:584\ns_waitcnt lgkmcnt(0)\ns_add_u32 m0, %[lw], 0\ns_add_u32 s44, %[wo], 3670016\nbuffer_load_dwordx4 v92, %[rw], s44 offen nt lds\ns_add_u32 m0, %[lw], 1024\n" \
"s_add_u32 s44, %[wo], 3735552\nbuffer_load_dwordx4 v92, %[rw], s44 offen nt lds\ns_add_u32 m0, %[lw], 2048\ns_add_u32 s44, %[wo], 3801088\nbuffer_load_dwordx4 v92, %[rw], s44 offen nt lds\ns_add_u32 m0, %[lw], 3072\ns_add_u32 s44, %[wo], 3866624\nbuffer_load_dwordx4 v92, %[rw], s44 offen nt lds\n" \
"s_waitcnt vmcnt(32)\nds_read_b64_tr_b16 a[80:81], v94 offset:4096\nds_read_b64_tr_b16 a[82:83], v94 offset:4608\nds_read_b64_tr_b16 a[88:89], v94 offset:4104\nds_read_b64_tr_b16 a[90:91], v94 offset:4616\nds_read_b64_tr_b16 a[96:97], v94 offset:4160\nds_read_b64_tr_b16 a[98:99], v94 offset:4672\nds_read_b64_tr_b16 v[72:73], v94 offset:4168\n" \
"ds_read_b64_tr_b16 v[74:75], v94 offset:4680\ns_waitcnt lgkmcnt(0)\ns_add_u32 m0, %[lw], 4096\ns_add_u32 s44, %[wo], 3932160\nbuffer_load_dwordx4 v92, %[rw], s44 offen nt lds\ns_add_u32 m0, %[lw], 5120\ns_add_u32 s44, %[wo], 3997696\nbuffer_load_dwordx4 v92, %[rw], s44 offen nt lds\n" \
"s_add_u32 m0, %[lw], 6144\ns_add_u32 s44, %[wo], 4063232\nbuffer_load_dwordx4 v92, %[rw], s44 offen nt lds\ns_add_u32 m0, %[lw], 7168\ns_add_u32 s44, %[wo], 4128768\nbuffer_load_dwordx4 v92, %[rw], s44 offen nt lds\ns_waitcnt vmcnt(32)\nds_read_b64_tr_b16 a[84:85], v94 offset:8192\n" \
"ds_read_b64_tr_b16 a[86:87], v94 offset:8704\nds_read_b64_tr_b16 a[92:93], v94 offset:8200\nds_read_b64_tr_b16 a[94:95], v94 offset:8712\nds_read_b64_tr_b16 a[100:101], v94 offset:8256\nds_read_b64_tr_b16 a[102:103], v94 offset:8768\nds_read_b64_tr_b16 v[68:69], v94 offset:8264\nds_read_b64_tr_b16 v[70:71], v94 offset:8776\ns_waitcnt lgkmcnt(0)\n" \
"s_add_u32 m0, %[lw], 8192\ns_add_u32 s44, %[wo], 4194304\nbuffer_load_dwordx4 v92, %[rw], s44 offen nt lds\ns_add_u32 m0, %[lw], 9216\ns_add_u32 s44, %[wo], 4259840\nbuffer_load_dwordx4 v92, %[rw], s44 offen nt lds\ns_add_u32 m0, %[lw], 10240\ns_add_u32 s44, %[wo], 4325376\n" \
"buffer_load_dwordx4 v92, %[rw], s44 offen nt lds\ns_add_u32 m0, %[lw], 11264\ns_add_u32 s44, %[wo], 4390912\nbuffer_load_dwordx4 v92, %[rw], s44 offen nt lds\ns_waitcnt vmcnt(32)\nds_read_b64_tr_b16 a[104:105], v94 offset:12288\nds_read_b64_tr_b16 a[106:107], v94 offset:12800\nds_read_b64_tr_b16 a[112:113], v94 offset:12296\n" \
"ds_read_b64_tr_b16 a[114:115], v94 offset:12808\nds_read_b64_tr_b16 a[120:121], v94 offset:12352\nds_read_b64_tr_b16 a[122:123], v94 offset:12864\nds_read_b64_tr_b16 a[128:129], v94 offset:12360\nds_read_b64_tr_b16 a[130:131], v94 offset:12872\ns_waitcnt lgkmcnt(0)\ns_add_u32 m0, %[lw], 12288\ns_add_u32 s44, %[wo], 4456448\n" \
"buffer_load_dwordx4 v92, %[rw], s44 offen nt lds\ns_add_u32 m0, %[lw], 13312\ns_add_u32 s44, %[wo], 4521984\nbuffer_load_dwordx4 v92, %[rw], s44 offen nt lds\ns_add_u32 m0, %[lw], 14336\ns_add_u32 s44, %[wo], 4587520\nbuffer_load_dwordx4 v92, %[rw], s44 offen nt lds\ns_add_u32 m0, %[lw], 15360\n" \
"s_add_u32 s44, %[wo], 4653056\nbuffer_load_dwordx4 v92, %[rw], s44 offen nt lds\ns_waitcnt vmcnt(32)\nds_read_b64_tr_b16 a[108:109], v94 offset:16384\nds_read_b64_tr_b16 a[110:111], v94 offset:16896\nds_read_b64_tr_b16 a[116:117], v94 offset:16392\nds_read_b64_tr_b16 a[118:119], v94 offset:16904\nds_read_b64_tr_b16 a[124:125], v94 offset:16448\n" \
"ds_read_b64_tr_b16 a[126:127], v94 offset:16960\nds_read_b64_tr_b16 a[132:133], v94 offset:16456\nds_read_b64_tr_b16 a[134:135], v94 offset:16968\ns_waitcnt lgkmcnt(0)\ns_add_u32 m0, %[lw], 16384\ns_add_u32 s44, %[wo], 4718592\nbuffer_load_dwordx4 v92, %[rw], s44 offen nt lds\ns_add_u32 m0, %[lw], 17408\n" \
"s_add_u32 s44, %[wo], 4784128\nbuffer_load_dwordx4 v92, %[rw], s44 offen nt lds\ns_add_u32 m0, %[lw], 18432\ns_add_u32 s44, %[wo], 4849664\nbuffer_load_dwordx4 v92, %[rw], s44 offen nt lds\ns_add_u32 m0, %[lw], 19456\ns_add_u32 s44, %[wo], 4915200\nbuffer_load_dwordx4 v92, %[rw], s44 offen nt lds\n" \
"s_waitcnt vmcnt(32)\nds_read_b64_tr_b16 a[136:137], v94 offset:20480\nds_read_b64_tr_b16 a[138:139], v94 offset:20992\nds_read_b64_tr_b16 v[64:65], v94 offset:20488\nds_read_b64_tr_b16 v[66:67], v94 offset:21000\nds_read_b64_tr_b16 a[144:145], v94 offset:20544\nds_read_b64_tr_b16 a[146:147], v94 offset:21056\nds_read_b64_tr_b16 a[152:153], v94 offset:20552\n" \
"ds_read_b64_tr_b16 a[154:155], v94 offset:21064\ns_waitcnt lgkmcnt(0)\ns_add_u32 m0, %[lw], 20480\ns_add_u32 s44, %[wo], 4980736\nbuffer_load_dwordx4 v92, %[rw], s44 offen nt lds\ns_add_u32 m0, %[lw], 21504\ns_add_u32 s44, %[wo], 5046272\nbuffer_load_dwordx4 v92, %[rw], s44 offen nt lds\n" \
"s_add_u32 m0, %[lw], 22528\ns_add_u32 s44, %[wo], 5111808\nbuffer_load_dwordx4 v92, %[rw], s44 offen nt lds\ns_add_u32 m0, %[lw], 23552\ns_add_u32 s44, %[wo], 5177344\nbuffer_load_dwordx4 v92, %[rw], s44 offen nt lds\ns_waitcnt vmcnt(32)\nds_read_b64_tr_b16 a[140:141], v93 offset:0\n" \
"ds_read_b64_tr_b16 a[142:143], v93 offset:512\nds_read_b64_tr_b16 v[60:61], v93 offset:8\nds_read_b64_tr_b16 v[62:63], v93 offset:520\nds_read_b64_tr_b16 a[148:149], v93 offset:64\nds_read_b64_tr_b16 a[150:151], v93 offset:576\nds_read_b64_tr_b16 a[156:157], v93 offset:72\nds_read_b64_tr_b16 a[158:159], v93 offset:584\ns_waitcnt lgkmcnt(0)\n" \
"s_add_u32 m0, %[tb], 0\ns_add_u32 s44, %[wo], 5242880\nbuffer_load_dwordx4 v92, %[rw], s44 offen nt lds\ns_add_u32 m0, %[tb], 1024\ns_add_u32 s44, %[wo], 5308416\nbuffer_load_dwordx4 v92, %[rw], s44 offen nt lds\ns_add_u32 m0, %[tb], 2048\ns_add_u32 s44, %[wo], 5373952\n" \
"buffer_load_dwordx4 v92, %[rw], s44 offen nt lds\ns_add_u32 m0, %[tb], 3072\ns_add_u32 s44, %[wo], 5439488\nbuffer_load_dwordx4 v92, %[rw], s44 offen nt lds\ns_waitcnt vmcnt(32)\nds_read_b64_tr_b16 a[160:161], v93 offset:4096\nds_read_b64_tr_b16 a[162:163], v93 offset:4608\nds_read_b64_tr_b16 a[168:169], v93 offset:4104\n" \
"ds_read_b64_tr_b16 a[170:171], v93 offset:4616\nds_read_b64_tr_b16 v[56:57], v93 offset:4160\nds_read_b64_tr_b16 v[58:59], v93 offset:4672\nds_read_b64_tr_b16 a[176:177], v93 offset:4168\nds_read_b64_tr_b16 a[178:179], v93 offset:4680\ns_waitcnt lgkmcnt(0)\ns_add_u32 m0, %[tb], 4096\ns_add_u32 s44, %[wo], 5505024\n" \
"buffer_load_dwordx4 v92, %[rw], s44 offen nt lds\ns_add_u32 m0, %[tb], 5120\ns_add_u32 s44, %[wo], 5570560\nbuffer_load_dwordx4 v92, %[rw], s44 offen nt lds\ns_add_u32 m0, %[tb], 6144\ns_add_u32 s44, %[wo], 5636096\nbuffer_load_dwordx4 v92, %[rw], s44 offen nt lds\ns_add_u32 m0, %[tb], 7168\n" \
"s_add_u32 s44, %[wo], 5701632\nbuffer_load_dwordx4 v92, %[rw], s44 offen nt lds\ns_waitcnt vmcnt(32)\nds_read_b64_tr_b16 a[164:165], v93 offset:8192\nds_read_b64_tr_b16 a[166:167], v93 offset:8704\nds_read_b64_tr_b16 a[172:173], v93 offset:8200\nds_read_b64_tr_b16 a[174:175], v93 offset:8712\nds_read_b64_tr_b16 v[52:53], v93 offset:8256\n" \
"ds_read_b64_tr_b16 v[54:55], v93 offset:8768\nds_read_b64_tr_b16 a[180:181], v93 offset:8264\nds_read_b64_tr_b16 a[182:183], v93 offset:8776\ns_waitcnt lgkmcnt(0)\ns_add_u32 m0, %[tb], 8192\ns_add_u32 s44, %[wo], 5767168\nbuffer_load_dwordx4 v92, %[rw], s44 offen nt lds\ns_add_u32 m0, %[tb], 9216\n" \
"s_add_u32 s44, %[wo], 5832704\nbuffer_load_dwordx4 v92, %[rw], s44 offen nt lds\ns_add_u32 m0, %[tb], 10240\ns_add_u32 s44, %[wo], 5898240\nbuffer_load_dwordx4 v92, %[rw], s44 offen nt lds\ns_add_u32 m0, %[tb], 11264\ns_add_u32 s44, %[wo], 5963776\nbuffer_load_dwordx4 v92, %[rw], s44 offen nt lds\n" \
"s_waitcnt vmcnt(32)\nds_read_b64_tr_b16 a[184:185], v94 offset:0\nds_read_b64_tr_b16 a[186:187], v94 offset:512\nds_read_b64_tr_b16 a[192:193], v94 offset:8\nds_read_b64_tr_b16 a[194:195], v94 offset:520\nds_read_b64_tr_b16 a[200:201], v94 offset:64\nds_read_b64_tr_b16 a[202:203], v94 offset:576\nds_read_b64_tr_b16 v[48:49], v94 offset:72\n" \
"ds_read_b64_tr_b16 v[50:51], v94 offset:584\ns_waitcnt lgkmcnt(0)\ns_add_u32 m0, %[lw], 0\ns_add_u32 s44, %[wo], 6029312\nbuffer_load_dwordx4 v92, %[rw], s44 offen nt lds\ns_add_u32 m0, %[lw], 1024\ns_add_u32 s44, %[wo], 6094848\nbuffer_load_dwordx4 v92, %[rw], s44 offen nt lds\n" \
"s_add_u32 m0, %[lw], 2048\ns_add_u32 s44, %[wo], 6160384\nbuffer_load_dwordx4 v92, %[rw], s44 offen nt lds\ns_add_u32 m0, %[lw], 3072\ns_add_u32 s44, %[wo], 6225920\nbuffer_load_dwordx4 v92, %[rw], s44 offen nt lds\ns_waitcnt vmcnt(32)\nds_read_b64_tr_b16 a[188:189], v94 offset:4096\n" \
"ds_read_b64_tr_b16 a[190:191], v94 offset:4608\nds_read_b64_tr_b16 a[196:197], v94 offset:4104\nds_read_b64_tr_b16 a[198:199], v94 offset:4616\nds_read_b64_tr_b16 a[204:205], v94 offset:4160\nds_read_b64_tr_b16 a[206:207], v94 offset:4672\nds_read_b64_tr_b16 v[44:45], v94 offset:4168\nds_read_b64_tr_b16 v[46:47], v94 offset:4680\ns_waitcnt lgkmcnt(0)\n" \
"s_add_u32 m0, %[lw], 4096\ns_add_u32 s44, %[wo], 6291456\nbuffer_load_dwordx4 v92, %[rw], s44 offen nt lds\ns_add_u32 m0, %[lw], 5120\ns_add_u32 s44, %[wo], 6356992\nbuffer_load_dwordx4 v92, %[rw], s44 offen nt lds\ns_add_u32 m0, %[lw], 6144\ns_add_u32 s44, %[wo], 6422528\n" \
"buffer_load_dwordx4 v92, %[rw], s44 offen nt lds\ns_add_u32 m0, %[lw], 7168\ns_add_u32 s44, %[wo], 6488064\nbuffer_load_dwordx4 v92, %[rw], s44 offen nt lds\ns_waitcnt vmcnt(32)\nds_read_b64_tr_b16 a[208:209], v94 offset:8192\nds_read_b64_tr_b16 a[210:211], v94 offset:8704\nds_read_b64_tr_b16 a[216:217], v94 offset:8200\n" \
"ds_read_b64_tr_b16 a[218:219], v94 offset:8712\nds_read_b64_tr_b16 a[224:225], v94 offset:8256\nds_read_b64_tr_b16 a[226:227], v94 offset:8768\nds_read_b64_tr_b16 a[232:233], v94 offset:8264\nds_read_b64_tr_b16 a[234:235], v94 offset:8776\ns_waitcnt lgkmcnt(0)\ns_add_u32 m0, %[lw], 8192\ns_add_u32 s44, %[wo], 6553600\n" \
"buffer_load_dwordx4 v92, %[rw], s44 offen nt lds\ns_add_u32 m0, %[lw], 9216\ns_add_u32 s44, %[wo], 6619136\nbuffer_load_dwordx4 v92, %[rw], s44 offen nt lds\ns_add_u32 m0, %[lw], 10240\ns_add_u32 s44, %[wo], 6684672\nbuffer_load_dwordx4 v92, %[rw], s44 offen nt lds\ns_add_u32 m0, %[lw], 11264\n" \
"s_add_u32 s44, %[wo], 6750208\nbuffer_load_dwordx4 v92, %[rw], s44 offen nt lds\ns_waitcnt vmcnt(32)\nds_read_b64_tr_b16 a[212:213], v94 offset:12288\nds_read_b64_tr_b16 a[214:215], v94 offset:12800\nds_read_b64_tr_b16 a[220:221], v94 offset:12296\nds_read_b64_tr_b16 a[222:223], v94 offset:12808\nds_read_b64_tr_b16 a[228:229], v94 offset:12352\n" \
"ds_read_b64_tr_b16 a[230:231], v94 offset:12864\nds_read_b64_tr_b16 a[236:237], v94 offset:12360\nds_read_b64_tr_b16 a[238:239], v94 offset:12872\ns_waitcnt lgkmcnt(0)\ns_add_u32 m0, %[lw], 12288\ns_add_u32 s44, %[wo], 6815744\nbuffer_load_dwordx4 v92, %[rw], s44 offen nt lds\ns_add_u32 m0, %[lw], 13312\n" \
"s_add_u32 s44, %[wo], 6881280\nbuffer_load_dwordx4 v92, %[rw], s44 offen nt lds\ns_add_u32 m0, %[lw], 14336\ns_add_u32 s44, %[wo], 6946816\nbuffer_load_dwordx4 v92, %[rw], s44 offen nt lds\ns_add_u32 m0, %[lw], 15360\ns_add_u32 s44, %[wo], 7012352\nbuffer_load_dwordx4 v92, %[rw], s44 offen nt lds\n" \
"s_waitcnt vmcnt(32)\nds_read_b64_tr_b16 a[240:241], v94 offset:16384\nds_read_b64_tr_b16 a[242:243], v94 offset:16896\nds_read_b64_tr_b16 v[40:41], v94 offset:16392\nds_read_b64_tr_b16 v[42:43], v94 offset:16904\nds_read_b64_tr_b16 a[248:249], v94 offset:16448\nds_read_b64_tr_b16 a[250:251], v94 offset:16960\nds_read_b64_tr_b16 v[96:97], v94 offset:16456\n" \
"ds_read_b64_tr_b16 v[98:99], v94 offset:16968\ns_waitcnt lgkmcnt(0)\ns_add_u32 m0, %[lw], 16384\ns_add_u32 s44, %[wo], 7077888\nbuffer_load_dwordx4 v92, %[rw], s44 offen nt lds\ns_add_u32 m0, %[lw], 17408\ns_add_u32 s44, %[wo], 7143424\nbuffer_load_dwordx4 v92, %[rw], s44 offen nt lds\n" \
"s_add_u32 m0, %[lw], 18432\ns_add_u32 s44, %[wo], 7208960\nbuffer_load_dwordx4 v92, %[rw], s44 offen nt lds\ns_add_u32 m0, %[lw], 19456\ns_add_u32 s44, %[wo], 7274496\nbuffer_load_dwordx4 v92, %[rw], s44 offen nt lds\ns_waitcnt vmcnt(32)\nds_read_b64_tr_b16 a[244:245], v94 offset:20480\n" \
"ds_read_b64_tr_b16 a[246:247], v94 offset:20992\nds_read_b64_tr_b16 v[36:37], v94 offset:20488\nds_read_b64_tr_b16 v[38:39], v94 offset:21000\nds_read_b64_tr_b16 a[252:253], v94 offset:20544\nds_read_b64_tr_b16 a[254:255], v94 offset:21056\nds_read_b64_tr_b16 v[100:101], v94 offset:20552\nds_read_b64_tr_b16 v[102:103], v94 offset:21064\ns_waitcnt lgkmcnt(0)\n" \
"s_add_u32 m0, %[lw], 20480\ns_add_u32 s44, %[wo], 7340032\nbuffer_load_dwordx4 v92, %[rw], s44 offen nt lds\ns_add_u32 m0, %[lw], 21504\ns_add_u32 s44, %[wo], 7405568\nbuffer_load_dwordx4 v92, %[rw], s44 offen nt lds\ns_add_u32 m0, %[lw], 22528\ns_add_u32 s44, %[wo], 7471104\n" \
"buffer_load_dwordx4 v92, %[rw], s44 offen nt lds\ns_add_u32 m0, %[lw], 23552\ns_add_u32 s44, %[wo], 7536640\nbuffer_load_dwordx4 v92, %[rw], s44 offen nt lds\ns_waitcnt vmcnt(32)\nds_read_b64_tr_b16 v[104:105], v93 offset:0\nds_read_b64_tr_b16 v[106:107], v93 offset:512\nds_read_b64_tr_b16 v[112:113], v93 offset:8\n" \
"ds_read_b64_tr_b16 v[114:115], v93 offset:520\nds_read_b64_tr_b16 v[32:33], v93 offset:64\nds_read_b64_tr_b16 v[34:35], v93 offset:576\nds_read_b64_tr_b16 v[120:121], v93 offset:72\nds_read_b64_tr_b16 v[122:123], v93 offset:584\ns_waitcnt lgkmcnt(0)\ns_add_u32 m0, %[tb], 0\ns_add_u32 s44, %[wo], 7602176\n" \
"buffer_load_dwordx4 v92, %[rw], s44 offen nt lds\ns_add_u32 m0, %[tb], 1024\ns_add_u32 s44, %[wo], 7667712\nbuffer_load_dwordx4 v92, %[rw], s44 offen nt lds\ns_add_u32 m0, %[tb], 2048\ns_add_u32 s44, %[wo], 7733248\nbuffer_load_dwordx4 v92, %[rw], s44 offen nt lds\ns_add_u32 m0, %[tb], 3072\n" \
"s_add_u32 s44, %[wo], 7798784\nbuffer_load_dwordx4 v92, %[rw], s44 offen nt lds\ns_waitcnt vmcnt(32)\nds_read_b64_tr_b16 v[108:109], v93 offset:4096\nds_read_b64_tr_b16 v[110:111], v93 offset:4608\nds_read_b64_tr_b16 v[116:117], v93 offset:4104\nds_read_b64_tr_b16 v[118:119], v93 offset:4616\nds_read_b64_tr_b16 v[28:29], v93 offset:4160\n" \
"ds_read_b64_tr_b16 v[30:31], v93 offset:4672\nds_read_b64_tr_b16 v[124:125], v93 offset:4168\nds_read_b64_tr_b16 v[126:127], v93 offset:4680\ns_waitcnt lgkmcnt(0)\ns_add_u32 m0, %[tb], 4096\ns_add_u32 s44, %[wo], 7864320\nbuffer_load_dwordx4 v92, %[rw], s44 offen nt lds\ns_add_u32 m0, %[tb], 5120\n" \
"s_add_u32 s44, %[wo], 7929856\nbuffer_load_dwordx4 v92, %[rw], s44 offen nt lds\ns_add_u32 m0, %[tb], 6144\ns_add_u32 s44, %[wo], 7995392\nbuffer_load_dwordx4 v92, %[rw], s44 offen nt lds\ns_add_u32 m0, %[tb], 7168\ns_add_u32 s44, %[wo], 8060928\nbuffer_load_dwordx4 v92, %[rw], s44 offen nt lds\n" \
"s_waitcnt vmcnt(32)\nds_read_b64_tr_b16 v[128:129], v93 offset:8192\nds_read_b64_tr_b16 v[130:131], v93 offset:8704\nds_read_b64_tr_b16 v[136:137], v93 offset:8200\nds_read_b64_tr_b16 v[138:139], v93 offset:8712\nds_read_b64_tr_b16 v[144:145], v93 offset:8256\nds_read_b64_tr_b16 v[146:147], v93 offset:8768\nds_read_b64_tr_b16 v[24:25], v93 offset:8264\n" \
"ds_read_b64_tr_b16 v[26:27], v93 offset:8776\ns_waitcnt lgkmcnt(0)\ns_add_u32 m0, %[tb], 8192\ns_add_u32 s44, %[wo], 8126464\nbuffer_load_dwordx4 v92, %[rw], s44 offen nt lds\ns_add_u32 m0, %[tb], 9216\ns_add_u32 s44, %[wo], 8192000\nbuffer_load_dwordx4 v92, %[rw], s44 offen nt lds\n" \
"s_add_u32 m0, %[tb], 10240\ns_add_u32 s44, %[wo], 8257536\nbuffer_load_dwordx4 v92, %[rw], s44 offen nt lds\ns_add_u32 m0, %[tb], 11264\ns_add_u32 s44, %[wo], 8323072\nbuffer_load_dwordx4 v92, %[rw], s44 offen nt lds\ns_waitcnt vmcnt(32)\nds_read_b64_tr_b16 v[132:133], v94 offset:0\n" \
"ds_read_b64_tr_b16 v[134:135], v94 offset:512\nds_read_b64_tr_b16 v[140:141], v94 offset:8\nds_read_b64_tr_b16 v[142:143], v94 offset:520\nds_read_b64_tr_b16 v[148:149], v94 offset:64\nds_read_b64_tr_b16 v[150:151], v94 offset:576\nds_read_b64_tr_b16 v[20:21], v94 offset:72\nds_read_b64_tr_b16 v[22:23], v94 offset:584\ns_waitcnt lgkmcnt(0)\n" \
"ds_write_b128 v1, v[88:91] offset:0\nds_write_b128 v1, v[84:87] offset:1024\nds_write_b128 v1, v[80:83] offset:2048\nds_write_b128 v1, v[76:79] offset:3072\ns_waitcnt vmcnt(28)\nds_read_b64_tr_b16 v[152:153], v94 offset:4096\nds_read_b64_tr_b16 v[154:155], v94 offset:4608\nds_read_b64_tr_b16 v[160:161], v94 offset:4104\n" \
"ds_read_b64_tr_b16 v[162:163], v94 offset:4616\nds_read_b64_tr_b16 v[168:169], v94 offset:4160\nds_read_b64_tr_b16 v[170:171], v94 offset:4672\nds_read_b64_tr_b16 v[176:177], v94 offset:4168\nds_read_b64_tr_b16 v[178:179], v94 offset:4680\ns_waitcnt lgkmcnt(0)\nds_write_b128 v1, v[72:75] offset:4096\nds_write_b128 v1, v[68:71] offset:5120\n" \
"ds_write_b128 v1, v[64:67] offset:6144\nds_write_b128 v1, v[60:63] offset:7168\ns_waitcnt vmcnt(24)\nds_read_b64_tr_b16 v[156:157], v94 offset:8192\nds_read_b64_tr_b16 v[158:159], v94 offset:8704\nds_read_b64_tr_b16 v[164:165], v94 offset:8200\nds_read_b64_tr_b16 v[166:167], v94 offset:8712\nds_read_b64_tr_b16 v[172:173], v94 offset:8256\n" \
"ds_read_b64_tr_b16 v[174:175], v94 offset:8768\nds_read_b64_tr_b16 v[180:181], v94 offset:8264\nds_read_b64_tr_b16 v[182:183], v94 offset:8776\ns_waitcnt lgkmcnt(0)\nds_write_b128 v1, v[56:59] offset:8192\nds_write_b128 v1, v[52:55] offset:9216\nds_write_b128 v1, v[48:51] offset:10240\nds_write_b128 v1, v[44:47] offset:11264\n" \
"s_waitcnt vmcnt(20)\nds_read_b64_tr_b16 v[184:185], v94 offset:12288\nds_read_b64_tr_b16 v[186:187], v94 offset:12800\nds_read_b64_tr_b16 v[44:45], v94 offset:12296\nds_read_b64_tr_b16 v[46:47], v94 offset:12808\nds_read_b64_tr_b16 v[192:193], v94 offset:12352\nds_read_b64_tr_b16 v[194:195], v94 offset:12864\nds_read_b64_tr_b16 v[200:201], v94 offset:12360\n" \
"ds_read_b64_tr_b16 v[202:203], v94 offset:12872\ns_waitcnt lgkmcnt(0)\nds_write_b128 v1, v[40:43] offset:12288\nds_write_b128 v1, v[36:39] offset:13312\nds_write_b128 v1, v[32:35] offset:14336\nds_write_b128 v1, v[28:31] offset:15360\ns_waitcnt vmcnt(16)\nds_read_b64_tr_b16 v[188:189], v94 offset:16384\n" \
"ds_read_b64_tr_b16 v[190:191], v94 offset:16896\nds_read_b64_tr_b16 v[28:29], v94 offset:16392\nds_read_b64_tr_b16 v[30:31], v94 offset:16904\nds_read_b64_tr_b16 v[196:197], v94 offset:16448\nds_read_b64_tr_b16 v[198:199], v94 offset:16960\nds_read_b64_tr_b16 v[204:205], v94 offset:16456\nds_read_b64_tr_b16 v[206:207], v94 offset:16968\ns_waitcnt lgkmcnt(0)\n" \
"ds_write_b128 v1, v[24:27] offset:16384\nds_write_b128 v1, v[20:23] offset:17408\nds_write_b128 v1, v[44:47] offset:18432\nds_write_b128 v1, v[28:31] offset:19456\ns_waitcnt vmcnt(12)\nds_read_b64_tr_b16 v[208:209], v94 offset:20480\nds_read_b64_tr_b16 v[210:211], v94 offset:20992\nds_read_b64_tr_b16 v[216:217], v94 offset:20488\n" \
"ds_read_b64_tr_b16 v[218:219], v94 offset:21000\nds_read_b64_tr_b16 v[28:29], v94 offset:20544\nds_read_b64_tr_b16 v[30:31], v94 offset:21056\nds_read_b64_tr_b16 v[224:225], v94 offset:20552\nds_read_b64_tr_b16 v[226:227], v94 offset:21064\ns_waitcnt lgkmcnt(0)\nds_write_b128 v1, v[28:31] offset:20480\ns_waitcnt vmcnt(8)\n" \
"ds_read_b64_tr_b16 v[212:213], v93 offset:0\nds_read_b64_tr_b16 v[214:215], v93 offset:512\nds_read_b64_tr_b16 v[220:221], v93 offset:8\nds_read_b64_tr_b16 v[222:223], v93 offset:520\nds_read_b64_tr_b16 v[28:29], v93 offset:64\nds_read_b64_tr_b16 v[30:31], v93 offset:576\nds_read_b64_tr_b16 v[228:229], v93 offset:72\nds_read_b64_tr_b16 v[230:231], v93 offset:584\n" \
"s_waitcnt lgkmcnt(0)\nds_write_b128 v1, v[28:31] offset:21504\ns_waitcnt vmcnt(4)\nds_read_b64_tr_b16 v[232:233], v93 offset:4096\nds_read_b64_tr_b16 v[234:235], v93 offset:4608\nds_read_b64_tr_b16 v[240:241], v93 offset:4104\nds_read_b64_tr_b16 v[242:243], v93 offset:4616\nds_read_b64_tr_b16 v[248:249], v93 offset:4160\n" \
"ds_read_b64_tr_b16 v[250:251], v93 offset:4672\nds_read_b64_tr_b16 v[28:29], v93 offset:4168\nds_read_b64_tr_b16 v[30:31], v93 offset:4680\ns_waitcnt lgkmcnt(0)\nds_write_b128 v1, v[28:31] offset:22528\ns_waitcnt vmcnt(0)\nds_read_b64_tr_b16 v[236:237], v93 offset:8192\nds_read_b64_tr_b16 v[238:239], v93 offset:8704\n" \
"ds_read_b64_tr_b16 v[244:245], v93 offset:8200\nds_read_b64_tr_b16 v[246:247], v93 offset:8712\nds_read_b64_tr_b16 v[252:253], v93 offset:8256\nds_read_b64_tr_b16 v[254:255], v93 offset:8768\nds_read_b64_tr_b16 v[28:29], v93 offset:8264\nds_read_b64_tr_b16 v[30:31], v93 offset:8776\ns_waitcnt lgkmcnt(0)\nds_write_b128 v1, v[28:31] offset:23552\n" \
"s_waitcnt lgkmcnt(0)\ns_barrier\ns_mov_b32 s40, -1\ns_mov_b32 s43, 0\ns_mov_b32 s61, 0x7fff7fff\ns_mov_b32 s58, 0x3e0293ee\ns_mov_b32 s59, 0xbe0293ee\ns_lshl_b32 s57, %[w], 12\n" \
"s_mov_b32 s62, 0xff00ff00\ns_mov_b32 s63, 0xff00ff00\ns_mov_b32 s56, 0\ns_cmp_eq_u32 %[w], 3\ns_cbranch_scc1 .Lqk_fetch0%=\n.Lqk_proj%=:\ns_add_u32 s44, s40, 1\ns_lshl_b32 s44, s44, 17\n" \
"s_add_u32 s44, s44, %[xn]\ns_lshr_b32 s45, s44, 8\ns_and_b32 s45, s45, 0x2000\ns_xor_b32 s41, s44, s45\ns_add_u32 s40, s40, 1\ns_add_u32 s41, s41, %[xw]\ns_barrier\nds_read_b128 v[48:51], v2 offset:0\n" \
"ds_read_b128 v[52:55], v2 offset:256\nds_read_b128 v[56:59], v2 offset:2048\ns_and_b32 s42, s40, 3\ns_lshl_b32 s42, s42, 15\ns_waitcnt lgkmcnt(2)\nv_mfma_f32_16x16x32_bf16 v[16:19], a[0:3], v[48:51], 0\nds_read_b128 v[60:63], v2 offset:2304\nv_mfma_f32_16x16x32_bf16 v[20:23], a[8:11], v[48:51], 0\n" \
"ds_read_b128 v[72:75], v1 offset:0\nv_mfma_f32_16x16x32_bf16 v[24:27], a[16:19], v[48:51], 0\nv_mfma_f32_16x16x32_bf16 v[28:31], a[24:27], v[48:51], 0\ns_waitcnt lgkmcnt(3)\nv_mfma_f32_16x16x32_bf16 v[32:35], a[0:3], v[52:55], 0\nds_read_b128 v[48:51], v2 offset:4096\nv_mfma_f32_16x16x32_bf16 v[36:39], a[8:11], v[52:55], 0\nv_mfma_f32_16x16x32_bf16 v[40:43], a[16:19], v[52:55], 0\n" \
"v_mfma_f32_16x16x32_bf16 v[44:47], a[24:27], v[52:55], 0\ns_waitcnt lgkmcnt(3)\nv_mfma_f32_16x16x32_bf16 v[16:19], a[4:7], v[56:59], v[16:19]\nds_read_b128 v[52:55], v2 offset:4352\nv_mfma_f32_16x16x32_bf16 v[20:23], a[12:15], v[56:59], v[20:23]\nds_read_b128 v[76:79], v1 offset:1024\nv_mfma_f32_16x16x32_bf16 v[24:27], a[20:23], v[56:59], v[24:27]\nv_mfma_f32_16x16x32_bf16 v[28:31], a[28:31], v[56:59], v[28:31]\n" \
"s_waitcnt lgkmcnt(4)\nv_mfma_f32_16x16x32_bf16 v[32:35], a[4:7], v[60:63], v[32:35]\nds_read_b128 v[56:59], v2 offset:6144\nv_mfma_f32_16x16x32_bf16 v[36:39], a[12:15], v[60:63], v[36:39]\nv_mfma_f32_16x16x32_bf16 v[40:43], a[20:23], v[60:63], v[40:43]\nv_mfma_f32_16x16x32_bf16 v[44:47], a[28:31], v[60:63], v[44:47]\ns_waitcnt lgkmcnt(3)\nv_mfma_f32_16x16x32_bf16 v[16:19], a[32:35], v[48:51], v[16:19]\n" \
"ds_read_b128 v[60:63], v2 offset:6400\nv_mfma_f32_16x16x32_bf16 v[20:23], v[72:75], v[48:51], v[20:23]\nds_read_b128 v[64:67], v1 offset:2048\nv_mfma_f32_16x16x32_bf16 v[24:27], a[40:43], v[48:51], v[24:27]\nv_mfma_f32_16x16x32_bf16 v[28:31], a[48:51], v[48:51], v[28:31]\ns_waitcnt lgkmcnt(4)\nv_mfma_f32_16x16x32_bf16 v[32:35], a[32:35], v[52:55], v[32:35]\nds_read_b128 v[48:51], v2 offset:8192\n" \
"v_mfma_f32_16x16x32_bf16 v[36:39], v[72:75], v[52:55], v[36:39]\nv_mfma_f32_16x16x32_bf16 v[40:43], a[40:43], v[52:55], v[40:43]\ns_add_u32 m0, s57, 32768\ns_add_u32 s60, s41, 65536\nbuffer_load_dwordx4 v0, %[rs], s60 offen offset:0 sc1 lds\nv_mfma_f32_16x16x32_bf16 v[44:47], a[48:51], v[52:55], v[44:47]\ns_waitcnt lgkmcnt(3)\nv_mfma_f32_16x16x32_bf16 v[16:19], a[36:39], v[56:59], v[16:19]\n" \
"ds_read_b128 v[52:55], v2 offset:8448\nv_mfma_f32_16x16x32_bf16 v[20:23], v[76:79], v[56:59], v[20:23]\nds_read_b128 v[68:71], v1 offset:3072\nv_mfma_f32_16x16x32_bf16 v[24:27], a[44:47], v[56:59], v[24:27]\nv_mfma_f32_16x16x32_bf16 v[28:31], a[52:55], v[56:59], v[28:31]\ns_waitcnt lgkmcnt(4)\nv_mfma_f32_16x16x32_bf16 v[32:35], a[36:39], v[60:63], v[32:35]\nds_read_b128 v[56:59], v2 offset:10240\n" \
"v_mfma_f32_16x16x32_bf16 v[36:39], v[76:79], v[60:63], v[36:39]\nv_mfma_f32_16x16x32_bf16 v[40:43], a[44:47], v[60:63], v[40:43]\nbuffer_load_dwordx4 v0, %[rs], s60 offen offset:1024 sc1 lds\nv_mfma_f32_16x16x32_bf16 v[44:47], a[52:55], v[60:63], v[44:47]\ns_waitcnt lgkmcnt(3)\nv_mfma_f32_16x16x32_bf16 v[16:19], a[56:59], v[48:51], v[16:19]\nds_read_b128 v[60:63], v2 offset:10496\nv_mfma_f32_16x16x32_bf16 v[20:23], a[64:67], v[48:51], v[20:23]\n" \
"ds_read_b128 v[72:75], v1 offset:4096\nv_mfma_f32_16x16x32_bf16 v[24:27], v[64:67], v[48:51], v[24:27]\nv_mfma_f32_16x16x32_bf16 v[28:31], a[72:75], v[48:51], v[28:31]\ns_waitcnt lgkmcnt(4)\nv_mfma_f32_16x16x32_bf16 v[32:35], a[56:59], v[52:55], v[32:35]\nds_read_b128 v[48:51], v2 offset:12288\nv_mfma_f32_16x16x32_bf16 v[36:39], a[64:67], v[52:55], v[36:39]\nv_mfma_f32_16x16x32_bf16 v[40:43], v[64:67], v[52:55], v[40:43]\n" \
"buffer_load_dwordx4 v0, %[rs], s60 offen offset:2048 sc1 lds\nv_mfma_f32_16x16x32_bf16 v[44:47], a[72:75], v[52:55], v[44:47]\ns_waitcnt lgkmcnt(3)\nv_mfma_f32_16x16x32_bf16 v[16:19], a[60:63], v[56:59], v[16:19]\nds_read_b128 v[52:55], v2 offset:12544\nv_mfma_f32_16x16x32_bf16 v[20:23], a[68:71], v[56:59], v[20:23]\nds_read_b128 v[76:79], v1 offset:5120\nv_mfma_f32_16x16x32_bf16 v[24:27], v[68:71], v[56:59], v[24:27]\n" \
"v_mfma_f32_16x16x32_bf16 v[28:31], a[76:79], v[56:59], v[28:31]\ns_waitcnt lgkmcnt(4)\nv_mfma_f32_16x16x32_bf16 v[32:35], a[60:63], v[60:63], v[32:35]\nds_read_b128 v[56:59], v2 offset:14336\nv_mfma_f32_16x16x32_bf16 v[36:39], a[68:71], v[60:63], v[36:39]\nv_mfma_f32_16x16x32_bf16 v[40:43], v[68:71], v[60:63], v[40:43]\nbuffer_load_dwordx4 v0, %[rs], s60 offen offset:3072 sc1 lds\nv_mfma_f32_16x16x32_bf16 v[44:47], a[76:79], v[60:63], v[44:47]\n" \
"s_waitcnt lgkmcnt(3)\nv_mfma_f32_16x16x32_bf16 v[16:19], a[80:83], v[48:51], v[16:19]\nds_read_b128 v[60:63], v2 offset:14592\nv_mfma_f32_16x16x32_bf16 v[20:23], a[88:91], v[48:51], v[20:23]\nv_mfma_f32_16x16x32_bf16 v[24:27], a[96:99], v[48:51], v[24:27]\nv_mfma_f32_16x16x32_bf16 v[28:31], v[72:75], v[48:51], v[28:31]\ns_waitcnt lgkmcnt(3)\nv_mfma_f32_16x16x32_bf16 v[32:35], a[80:83], v[52:55], v[32:35]\n" \
"s_waitcnt vmcnt(4)\ns_barrier\nds_read_b128 v[48:51], v2 offset:16384\nv_mfma_f32_16x16x32_bf16 v[36:39], a[88:91], v[52:55], v[36:39]\nv_mfma_f32_16x16x32_bf16 v[40:43], a[96:99], v[52:55], v[40:43]\nv_mfma_f32_16x16x32_bf16 v[44:47], v[72:75], v[52:55], v[44:47]\ns_waitcnt lgkmcnt(2)\nv_mfma_f32_16x16x32_bf16 v[16:19], a[84:87], v[56:59], v[16:19]\n" \
"ds_read_b128 v[52:55], v2 offset:16640\nv_mfma_f32_16x16x32_bf16 v[20:23], a[92:95], v[56:59], v[20:23]\nv_mfma_f32_16x16x32_bf16 v[24:27], a[100:103], v[56:59], v[24:27]\nv_mfma_f32_16x16x32_bf16 v[28:31], v[76:79], v[56:59], v[28:31]\ns_waitcnt lgkmcnt(2)\nv_mfma_f32_16x16x32_bf16 v[32:35], a[84:87], v[60:63], v[32:35]\nds_read_b128 v[56:59], v2 offset:18432\nv_mfma_f32_16x16x32_bf16 v[36:39], a[92:95], v[60:63], v[36:39]\n" \
"v_mfma_f32_16x16x32_bf16 v[40:43], a[100:103], v[60:63], v[40:43]\nv_mfma_f32_16x16x32_bf16 v[44:47], v[76:79], v[60:63], v[44:47]\ns_waitcnt lgkmcnt(2)\nv_mfma_f32_16x16x32_bf16 v[16:19], a[104:107], v[48:51], v[16:19]\nds_read_b128 v[60:63], v2 offset:18688\nv_mfma_f32_16x16x32_bf16 v[20:23], a[112:115], v[48:51], v[20:23]\nds_read_b128 v[72:75], v1 offset:6144\nv_mfma_f32_16x16x32_bf16 v[24:27], a[120:123], v[48:51], v[24:27]\n" \
"v_mfma_f32_16x16x32_bf16 v[28:31], a[128:131], v[48:51], v[28:31]\ns_waitcnt lgkmcnt(3)\nv_mfma_f32_16x16x32_bf16 v[32:35], a[104:107], v[52:55], v[32:35]\nds_read_b128 v[48:51], v2 offset:20480\nv_mfma_f32_16x16x32_bf16 v[36:39], a[112:115], v[52:55], v[36:39]\nv_mfma_f32_16x16x32_bf16 v[40:43], a[120:123], v[52:55], v[40:43]\nv_mfma_f32_16x16x32_bf16 v[44:47], a[128:131], v[52:55], v[44:47]\ns_waitcnt lgkmcnt(3)\n" \
"v_mfma_f32_16x16x32_bf16 v[16:19], a[108:111], v[56:59], v[16:19]\nds_read_b128 v[52:55], v2 offset:20736\nv_mfma_f32_16x16x32_bf16 v[20:23], a[116:119], v[56:59], v[20:23]\nds_read_b128 v[76:79], v1 offset:7168\nv_mfma_f32_16x16x32_bf16 v[24:27], a[124:127], v[56:59], v[24:27]\nv_mfma_f32_16x16x32_bf16 v[28:31], a[132:135], v[56:59], v[28:31]\ns_waitcnt lgkmcnt(4)\nv_mfma_f32_16x16x32_bf16 v[32:35], a[108:111], v[60:63], v[32:35]\n" \
"ds_read_b128 v[56:59], v2 offset:22528\nv_mfma_f32_16x16x32_bf16 v[36:39], a[116:119], v[60:63], v[36:39]\nv_mfma_f32_16x16x32_bf16 v[40:43], a[124:127], v[60:63], v[40:43]\nv_mfma_f32_16x16x32_bf16 v[44:47], a[132:135], v[60:63], v[44:47]\ns_waitcnt lgkmcnt(3)\nv_mfma_f32_16x16x32_bf16 v[16:19], a[136:139], v[48:51], v[16:19]\nds_read_b128 v[60:63], v2 offset:22784\nv_mfma_f32_16x16x32_bf16 v[20:23], v[72:75], v[48:51], v[20:23]\n" \
"ds_read_b128 v[64:67], v1 offset:8192\nv_mfma_f32_16x16x32_bf16 v[24:27], a[144:147], v[48:51], v[24:27]\nv_mfma_f32_16x16x32_bf16 v[28:31], a[152:155], v[48:51], v[28:31]\ns_waitcnt lgkmcnt(4)\nv_mfma_f32_16x16x32_bf16 v[32:35], a[136:139], v[52:55], v[32:35]\nds_read_b128 v[48:51], v2 offset:24576\nv_mfma_f32_16x16x32_bf16 v[36:39], v[72:75], v[52:55], v[36:39]\nv_mfma_f32_16x16x32_bf16 v[40:43], a[144:147], v[52:55], v[40:43]\n" \
"v_mfma_f32_16x16x32_bf16 v[44:47], a[152:155], v[52:55], v[44:47]\ns_waitcnt lgkmcnt(3)\nv_mfma_f32_16x16x32_bf16 v[16:19], a[140:143], v[56:59], v[16:19]\nds_read_b128 v[52:55], v2 offset:24832\nv_mfma_f32_16x16x32_bf16 v[20:23], v[76:79], v[56:59], v[20:23]\nds_read_b128 v[68:71], v1 offset:9216\nv_mfma_f32_16x16x32_bf16 v[24:27], a[148:151], v[56:59], v[24:27]\nv_mfma_f32_16x16x32_bf16 v[28:31], a[156:159], v[56:59], v[28:31]\n" \
"s_waitcnt lgkmcnt(4)\nv_mfma_f32_16x16x32_bf16 v[32:35], a[140:143], v[60:63], v[32:35]\nds_read_b128 v[56:59], v2 offset:26624\nv_mfma_f32_16x16x32_bf16 v[36:39], v[76:79], v[60:63], v[36:39]\nv_mfma_f32_16x16x32_bf16 v[40:43], a[148:151], v[60:63], v[40:43]\nv_mfma_f32_16x16x32_bf16 v[44:47], a[156:159], v[60:63], v[44:47]\ns_waitcnt lgkmcnt(3)\nv_mfma_f32_16x16x32_bf16 v[16:19], a[160:163], v[48:51], v[16:19]\n" \
"ds_read_b128 v[60:63], v2 offset:26880\nv_mfma_f32_16x16x32_bf16 v[20:23], a[168:171], v[48:51], v[20:23]\nds_read_b128 v[72:75], v1 offset:10240\nv_mfma_f32_16x16x32_bf16 v[24:27], v[64:67], v[48:51], v[24:27]\nv_mfma_f32_16x16x32_bf16 v[28:31], a[176:179], v[48:51], v[28:31]\ns_waitcnt lgkmcnt(4)\nv_mfma_f32_16x16x32_bf16 v[32:35], a[160:163], v[52:55], v[32:35]\nds_read_b128 v[48:51], v2 offset:28672\n" \
"v_mfma_f32_16x16x32_bf16 v[36:39], a[168:171], v[52:55], v[36:39]\nv_mfma_f32_16x16x32_bf16 v[40:43], v[64:67], v[52:55], v[40:43]\nv_mfma_f32_16x16x32_bf16 v[44:47], a[176:179], v[52:55], v[44:47]\ns_waitcnt lgkmcnt(3)\nv_mfma_f32_16x16x32_bf16 v[16:19], a[164:167], v[56:59], v[16:19]\nds_read_b128 v[52:55], v2 offset:28928\nv_mfma_f32_16x16x32_bf16 v[20:23], a[172:175], v[56:59], v[20:23]\nds_read_b128 v[76:79], v1 offset:11264\n" \
"v_mfma_f32_16x16x32_bf16 v[24:27], v[68:71], v[56:59], v[24:27]\nv_mfma_f32_16x16x32_bf16 v[28:31], a[180:183], v[56:59], v[28:31]\ns_waitcnt lgkmcnt(4)\nv_mfma_f32_16x16x32_bf16 v[32:35], a[164:167], v[60:63], v[32:35]\nds_read_b128 v[56:59], v2 offset:30720\nv_mfma_f32_16x16x32_bf16 v[36:39], a[172:175], v[60:63], v[36:39]\nv_mfma_f32_16x16x32_bf16 v[40:43], v[68:71], v[60:63], v[40:43]\nv_mfma_f32_16x16x32_bf16 v[44:47], a[180:183], v[60:63], v[44:47]\n" \
"s_waitcnt lgkmcnt(3)\nv_mfma_f32_16x16x32_bf16 v[16:19], a[184:187], v[48:51], v[16:19]\nds_read_b128 v[60:63], v2 offset:30976\nv_mfma_f32_16x16x32_bf16 v[20:23], a[192:195], v[48:51], v[20:23]\nv_mfma_f32_16x16x32_bf16 v[24:27], a[200:203], v[48:51], v[24:27]\nv_mfma_f32_16x16x32_bf16 v[28:31], v[72:75], v[48:51], v[28:31]\ns_waitcnt lgkmcnt(3)\nv_mfma_f32_16x16x32_bf16 v[32:35], a[184:187], v[52:55], v[32:35]\n" \
"s_waitcnt vmcnt(0)\ns_barrier\nds_read_b128 v[48:51], v2 offset:32768\nv_mfma_f32_16x16x32_bf16 v[36:39], a[192:195], v[52:55], v[36:39]\nv_mfma_f32_16x16x32_bf16 v[40:43], a[200:203], v[52:55], v[40:43]\ns_add_u32 m0, s57, 0\ns_add_u32 s60, s41, 98304\nbuffer_load_dwordx4 v0, %[rs], s60 offen offset:0 sc1 lds\n" \
"v_mfma_f32_16x16x32_bf16 v[44:47], v[72:75], v[52:55], v[44:47]\ns_waitcnt lgkmcnt(2)\nv_mfma_f32_16x16x32_bf16 v[16:19], a[188:191], v[56:59], v[16:19]\nds_read_b128 v[52:55], v2 offset:33024\nv_mfma_f32_16x16x32_bf16 v[20:23], a[196:199], v[56:59], v[20:23]\nv_mfma_f32_16x16x32_bf16 v[24:27], a[204:207], v[56:59], v[24:27]\nbuffer_load_dwordx4 v0, %[rs], s60 offen offset:1024 sc1 lds\nv_mfma_f32_16x16x32_bf16 v[28:31], v[76:79], v[56:59], v[28:31]\n" \
"s_waitcnt lgkmcnt(2)\nv_mfma_f32_16x16x32_bf16 v[32:35], a[188:191], v[60:63], v[32:35]\nds_read_b128 v[56:59], v2 offset:34816\nv_mfma_f32_16x16x32_bf16 v[36:39], a[196:199], v[60:63], v[36:39]\nv_mfma_f32_16x16x32_bf16 v[40:43], a[204:207], v[60:63], v[40:43]\nbuffer_load_dwordx4 v0, %[rs], s60 offen offset:2048 sc1 lds\nv_mfma_f32_16x16x32_bf16 v[44:47], v[76:79], v[60:63], v[44:47]\ns_waitcnt lgkmcnt(2)\n" \
"v_mfma_f32_16x16x32_bf16 v[16:19], a[208:211], v[48:51], v[16:19]\nds_read_b128 v[60:63], v2 offset:35072\nv_mfma_f32_16x16x32_bf16 v[20:23], a[216:219], v[48:51], v[20:23]\nds_read_b128 v[72:75], v1 offset:12288\nv_mfma_f32_16x16x32_bf16 v[24:27], a[224:227], v[48:51], v[24:27]\nbuffer_load_dwordx4 v0, %[rs], s60 offen offset:3072 sc1 lds\nv_mfma_f32_16x16x32_bf16 v[28:31], a[232:235], v[48:51], v[28:31]\ns_waitcnt lgkmcnt(3)\n" \
"v_mfma_f32_16x16x32_bf16 v[32:35], a[208:211], v[52:55], v[32:35]\nds_read_b128 v[48:51], v2 offset:36864\nv_mfma_f32_16x16x32_bf16 v[36:39], a[216:219], v[52:55], v[36:39]\nv_mfma_f32_16x16x32_bf16 v[40:43], a[224:227], v[52:55], v[40:43]\nv_mfma_f32_16x16x32_bf16 v[44:47], a[232:235], v[52:55], v[44:47]\ns_waitcnt lgkmcnt(3)\nv_mfma_f32_16x16x32_bf16 v[16:19], a[212:215], v[56:59], v[16:19]\nds_read_b128 v[52:55], v2 offset:37120\n" \
"v_mfma_f32_16x16x32_bf16 v[20:23], a[220:223], v[56:59], v[20:23]\nds_read_b128 v[76:79], v1 offset:13312\nv_mfma_f32_16x16x32_bf16 v[24:27], a[228:231], v[56:59], v[24:27]\nv_mfma_f32_16x16x32_bf16 v[28:31], a[236:239], v[56:59], v[28:31]\ns_waitcnt lgkmcnt(4)\nv_mfma_f32_16x16x32_bf16 v[32:35], a[212:215], v[60:63], v[32:35]\nds_read_b128 v[56:59], v2 offset:38912\nv_mfma_f32_16x16x32_bf16 v[36:39], a[220:223], v[60:63], v[36:39]\n" \
"v_mfma_f32_16x16x32_bf16 v[40:43], a[228:231], v[60:63], v[40:43]\nv_mfma_f32_16x16x32_bf16 v[44:47], a[236:239], v[60:63], v[44:47]\ns_waitcnt lgkmcnt(3)\nv_mfma_f32_16x16x32_bf16 v[16:19], a[240:243], v[48:51], v[16:19]\nds_read_b128 v[60:63], v2 offset:39168\nv_mfma_f32_16x16x32_bf16 v[20:23], v[72:75], v[48:51], v[20:23]\nds_read_b128 v[64:67], v1 offset:14336\nv_mfma_f32_16x16x32_bf16 v[24:27], a[248:251], v[48:51], v[24:27]\n" \
"v_mfma_f32_16x16x32_bf16 v[28:31], v[96:99], v[48:51], v[28:31]\ns_waitcnt lgkmcnt(4)\nv_mfma_f32_16x16x32_bf16 v[32:35], a[240:243], v[52:55], v[32:35]\nds_read_b128 v[48:51], v2 offset:40960\nv_mfma_f32_16x16x32_bf16 v[36:39], v[72:75], v[52:55], v[36:39]\nv_mfma_f32_16x16x32_bf16 v[40:43], a[248:251], v[52:55], v[40:43]\nv_mfma_f32_16x16x32_bf16 v[44:47], v[96:99], v[52:55], v[44:47]\ns_waitcnt lgkmcnt(3)\n" \
"v_mfma_f32_16x16x32_bf16 v[16:19], a[244:247], v[56:59], v[16:19]\nds_read_b128 v[52:55], v2 offset:41216\nv_mfma_f32_16x16x32_bf16 v[20:23], v[76:79], v[56:59], v[20:23]\nds_read_b128 v[68:71], v1 offset:15360\nv_mfma_f32_16x16x32_bf16 v[24:27], a[252:255], v[56:59], v[24:27]\nv_mfma_f32_16x16x32_bf16 v[28:31], v[100:103], v[56:59], v[28:31]\ns_waitcnt lgkmcnt(4)\nv_mfma_f32_16x16x32_bf16 v[32:35], a[244:247], v[60:63], v[32:35]\n" \
"ds_read_b128 v[56:59], v2 offset:43008\nv_mfma_f32_16x16x32_bf16 v[36:39], v[76:79], v[60:63], v[36:39]\nv_mfma_f32_16x16x32_bf16 v[40:43], a[252:255], v[60:63], v[40:43]\nv_mfma_f32_16x16x32_bf16 v[44:47], v[100:103], v[60:63], v[44:47]\ns_waitcnt lgkmcnt(3)\nv_mfma_f32_16x16x32_bf16 v[16:19], v[104:107], v[48:51], v[16:19]\nds_read_b128 v[60:63], v2 offset:43264\nv_mfma_f32_16x16x32_bf16 v[20:23], v[112:115], v[48:51], v[20:23]\n" \
"ds_read_b128 v[72:75], v1 offset:16384\nv_mfma_f32_16x16x32_bf16 v[24:27], v[64:67], v[48:51], v[24:27]\nv_mfma_f32_16x16x32_bf16 v[28:31], v[120:123], v[48:51], v[28:31]\ns_waitcnt lgkmcnt(4)\nv_mfma_f32_16x16x32_bf16 v[32:35], v[104:107], v[52:55], v[32:35]\nds_read_b128 v[48:51], v2 offset:45056\nv_mfma_f32_16x16x32_bf16 v[36:39], v[112:115], v[52:55], v[36:39]\nv_mfma_f32_16x16x32_bf16 v[40:43], v[64:67], v[52:55], v[40:43]\n" \
"v_mfma_f32_16x16x32_bf16 v[44:47], v[120:123], v[52:55], v[44:47]\ns_waitcnt lgkmcnt(3)\nv_mfma_f32_16x16x32_bf16 v[16:19], v[108:111], v[56:59], v[16:19]\nds_read_b128 v[52:55], v2 offset:45312\nv_mfma_f32_16x16x32_bf16 v[20:23], v[116:119], v[56:59], v[20:23]\nds_read_b128 v[76:79], v1 offset:17408\nv_mfma_f32_16x16x32_bf16 v[24:27], v[68:71], v[56:59], v[24:27]\nv_mfma_f32_16x16x32_bf16 v[28:31], v[124:127], v[56:59], v[28:31]\n" \
"s_waitcnt lgkmcnt(4)\nv_mfma_f32_16x16x32_bf16 v[32:35], v[108:111], v[60:63], v[32:35]\nds_read_b128 v[56:59], v2 offset:47104\nv_mfma_f32_16x16x32_bf16 v[36:39], v[116:119], v[60:63], v[36:39]\nv_mfma_f32_16x16x32_bf16 v[40:43], v[68:71], v[60:63], v[40:43]\nv_mfma_f32_16x16x32_bf16 v[44:47], v[124:127], v[60:63], v[44:47]\ns_waitcnt lgkmcnt(3)\nv_mfma_f32_16x16x32_bf16 v[16:19], v[128:131], v[48:51], v[16:19]\n" \
"ds_read_b128 v[60:63], v2 offset:47360\nv_mfma_f32_16x16x32_bf16 v[20:23], v[136:139], v[48:51], v[20:23]\nv_mfma_f32_16x16x32_bf16 v[24:27], v[144:147], v[48:51], v[24:27]\nv_mfma_f32_16x16x32_bf16 v[28:31], v[72:75], v[48:51], v[28:31]\ns_waitcnt lgkmcnt(3)\nv_mfma_f32_16x16x32_bf16 v[32:35], v[128:131], v[52:55], v[32:35]\ns_waitcnt lgkmcnt(0)\ns_waitcnt vmcnt(0)\n" \
"s_barrier\nds_read_b128 v[48:51], v2 offset:0\nv_mfma_f32_16x16x32_bf16 v[36:39], v[136:139], v[52:55], v[36:39]\nv_mfma_f32_16x16x32_bf16 v[40:43], v[144:147], v[52:55], v[40:43]\nv_mfma_f32_16x16x32_bf16 v[44:47], v[72:75], v[52:55], v[44:47]\nv_mfma_f32_16x16x32_bf16 v[16:19], v[132:135], v[56:59], v[16:19]\nds_read_b128 v[52:55], v2 offset:256\nv_mfma_f32_16x16x32_bf16 v[20:23], v[140:143], v[56:59], v[20:23]\n" \
"v_mfma_f32_16x16x32_bf16 v[24:27], v[148:151], v[56:59], v[24:27]\nv_mfma_f32_16x16x32_bf16 v[28:31], v[76:79], v[56:59], v[28:31]\nv_mfma_f32_16x16x32_bf16 v[32:35], v[132:135], v[60:63], v[32:35]\nds_read_b128 v[56:59], v2 offset:2048\nv_mfma_f32_16x16x32_bf16 v[36:39], v[140:143], v[60:63], v[36:39]\nv_mfma_f32_16x16x32_bf16 v[40:43], v[148:151], v[60:63], v[40:43]\nv_mfma_f32_16x16x32_bf16 v[44:47], v[76:79], v[60:63], v[44:47]\ns_waitcnt lgkmcnt(2)\n" \
"v_mfma_f32_16x16x32_bf16 v[16:19], v[152:155], v[48:51], v[16:19]\nds_read_b128 v[60:63], v2 offset:2304\nv_mfma_f32_16x16x32_bf16 v[20:23], v[160:163], v[48:51], v[20:23]\nds_read_b128 v[72:75], v1 offset:18432\nv_mfma_f32_16x16x32_bf16 v[24:27], v[168:171], v[48:51], v[24:27]\nv_mfma_f32_16x16x32_bf16 v[28:31], v[176:179], v[48:51], v[28:31]\ns_waitcnt lgkmcnt(3)\nv_mfma_f32_16x16x32_bf16 v[32:35], v[152:155], v[52:55], v[32:35]\n" \
"ds_read_b128 v[48:51], v2 offset:4096\nv_mfma_f32_16x16x32_bf16 v[36:39], v[160:163], v[52:55], v[36:39]\nv_mfma_f32_16x16x32_bf16 v[40:43], v[168:171], v[52:55], v[40:43]\nv_mfma_f32_16x16x32_bf16 v[44:47], v[176:179], v[52:55], v[44:47]\ns_waitcnt lgkmcnt(3)\nv_mfma_f32_16x16x32_bf16 v[16:19], v[156:159], v[56:59], v[16:19]\nds_read_b128 v[52:55], v2 offset:4352\nv_mfma_f32_16x16x32_bf16 v[20:23], v[164:167], v[56:59], v[20:23]\n" \
"ds_read_b128 v[76:79], v1 offset:19456\nv_mfma_f32_16x16x32_bf16 v[24:27], v[172:175], v[56:59], v[24:27]\nv_mfma_f32_16x16x32_bf16 v[28:31], v[180:183], v[56:59], v[28:31]\ns_waitcnt lgkmcnt(4)\nv_mfma_f32_16x16x32_bf16 v[32:35], v[156:159], v[60:63], v[32:35]\nds_read_b128 v[56:59], v2 offset:6144\nv_mfma_f32_16x16x32_bf16 v[36:39], v[164:167], v[60:63], v[36:39]\nv_mfma_f32_16x16x32_bf16 v[40:43], v[172:175], v[60:63], v[40:43]\n" \
"v_mfma_f32_16x16x32_bf16 v[44:47], v[180:183], v[60:63], v[44:47]\ns_waitcnt lgkmcnt(3)\nv_mfma_f32_16x16x32_bf16 v[16:19], v[184:187], v[48:51], v[16:19]\nds_read_b128 v[60:63], v2 offset:6400\nv_mfma_f32_16x16x32_bf16 v[20:23], v[72:75], v[48:51], v[20:23]\nds_read_b128 v[64:67], v1 offset:20480\nv_mfma_f32_16x16x32_bf16 v[24:27], v[192:195], v[48:51], v[24:27]\nv_mfma_f32_16x16x32_bf16 v[28:31], v[200:203], v[48:51], v[28:31]\n" \
"s_waitcnt lgkmcnt(4)\nv_mfma_f32_16x16x32_bf16 v[32:35], v[184:187], v[52:55], v[32:35]\nds_read_b128 v[48:51], v2 offset:8192\nv_mfma_f32_16x16x32_bf16 v[36:39], v[72:75], v[52:55], v[36:39]\nv_mfma_f32_16x16x32_bf16 v[40:43], v[192:195], v[52:55], v[40:43]\nv_mfma_f32_16x16x32_bf16 v[44:47], v[200:203], v[52:55], v[44:47]\ns_waitcnt lgkmcnt(3)\nv_mfma_f32_16x16x32_bf16 v[16:19], v[188:191], v[56:59], v[16:19]\n" \
"ds_read_b128 v[52:55], v2 offset:10240\nv_mfma_f32_16x16x32_bf16 v[20:23], v[76:79], v[56:59], v[20:23]\nds_read_b128 v[68:71], v1 offset:21504\nv_mfma_f32_16x16x32_bf16 v[24:27], v[196:199], v[56:59], v[24:27]\nbuffer_load_dwordx4 v[80:83], v3, %[rs], s42 offen offset:0 sc1\nv_mfma_f32_16x16x32_bf16 v[28:31], v[204:207], v[56:59], v[28:31]\ns_waitcnt lgkmcnt(4)\nv_mfma_f32_16x16x32_bf16 v[32:35], v[188:191], v[60:63], v[32:35]\n" \
"ds_read_b128 v[56:59], v2 offset:12288\nv_mfma_f32_16x16x32_bf16 v[36:39], v[76:79], v[60:63], v[36:39]\nds_read_b128 v[72:75], v1 offset:22528\nv_mfma_f32_16x16x32_bf16 v[40:43], v[196:199], v[60:63], v[40:43]\nbuffer_load_dwordx4 v[84:87], v3, %[rs], s42 offen offset:1024 sc1\nv_mfma_f32_16x16x32_bf16 v[44:47], v[204:207], v[60:63], v[44:47]\ns_waitcnt lgkmcnt(4)\nv_mfma_f32_16x16x32_bf16 v[16:19], v[208:211], v[48:51], v[16:19]\n" \
"ds_read_b128 v[60:63], v2 offset:14336\nv_mfma_f32_16x16x32_bf16 v[20:23], v[216:219], v[48:51], v[20:23]\nds_read_b128 v[76:79], v1 offset:23552\nv_mfma_f32_16x16x32_bf16 v[24:27], v[64:67], v[48:51], v[24:27]\nbuffer_load_dwordx4 v[88:91], v3, %[rs], s42 offen offset:2048 sc1\nv_mfma_f32_16x16x32_bf16 v[28:31], v[224:227], v[48:51], v[28:31]\ns_waitcnt lgkmcnt(5)\nv_mfma_f32_16x16x32_bf16 v[16:19], v[212:215], v[52:55], v[16:19]\n" \
"ds_read_b128 v[48:51], v2 offset:8448\nv_mfma_f32_16x16x32_bf16 v[20:23], v[220:223], v[52:55], v[20:23]\ns_waitcnt lgkmcnt(5)\nv_mfma_f32_16x16x32_bf16 v[24:27], v[68:71], v[52:55], v[24:27]\nbuffer_load_dwordx4 v[92:95], v3, %[rs], s42 offen offset:3072 sc1\nv_mfma_f32_16x16x32_bf16 v[28:31], v[228:231], v[52:55], v[28:31]\ns_waitcnt lgkmcnt(4)\nv_mfma_f32_16x16x32_bf16 v[16:19], v[232:235], v[56:59], v[16:19]\n" \
"ds_read_b128 v[52:55], v2 offset:10496\nv_mfma_f32_16x16x32_bf16 v[20:23], v[240:243], v[56:59], v[20:23]\nv_mfma_f32_16x16x32_bf16 v[24:27], v[248:251], v[56:59], v[24:27]\ns_waitcnt lgkmcnt(4)\nv_mfma_f32_16x16x32_bf16 v[28:31], v[72:75], v[56:59], v[28:31]\ns_waitcnt lgkmcnt(3)\nv_mfma_f32_16x16x32_bf16 v[16:19], v[236:239], v[60:63], v[16:19]\nds_read_b128 v[56:59], v2 offset:12544\n" \
"v_mfma_f32_16x16x32_bf16 v[20:23], v[244:247], v[60:63], v[20:23]\nv_mfma_f32_16x16x32_bf16 v[24:27], v[252:255], v[60:63], v[24:27]\ns_waitcnt lgkmcnt(3)\nv_mfma_f32_16x16x32_bf16 v[28:31], v[76:79], v[60:63], v[28:31]\ns_waitcnt lgkmcnt(2)\nv_mfma_f32_16x16x32_bf16 v[32:35], v[208:211], v[48:51], v[32:35]\nds_read_b128 v[60:63], v2 offset:14592\nv_mfma_f32_16x16x32_bf16 v[36:39], v[216:219], v[48:51], v[36:39]\n" \
"v_cvt_pk_bf16_f32 v20, v16, v20\nv_cvt_pk_bf16_f32 v21, v17, v21\nv_mfma_f32_16x16x32_bf16 v[40:43], v[64:67], v[48:51], v[40:43]\nv_cvt_pk_bf16_f32 v22, v18, v22\nv_cvt_pk_bf16_f32 v23, v19, v23\nv_mfma_f32_16x16x32_bf16 v[44:47], v[224:227], v[48:51], v[44:47]\ns_waitcnt lgkmcnt(2)\nds_write_b128 v5, v[20:23] offset:0\n" \
"v_mfma_f32_16x16x32_bf16 v[32:35], v[212:215], v[52:55], v[32:35]\nv_cvt_pk_bf16_f32 v28, v24, v28\nv_cvt_pk_bf16_f32 v29, v25, v29\nv_mfma_f32_16x16x32_bf16 v[36:39], v[220:223], v[52:55], v[36:39]\nv_cvt_pk_bf16_f32 v30, v26, v30\nv_cvt_pk_bf16_f32 v31, v27, v31\nv_mfma_f32_16x16x32_bf16 v[40:43], v[68:71], v[52:55], v[40:43]\nv_mov_b64 v[48:49], 0\n" \
"v_mov_b64 v[50:51], 0\nv_mfma_f32_16x16x32_bf16 v[44:47], v[228:231], v[52:55], v[44:47]\ns_waitcnt lgkmcnt(2)\nds_write_b128 v5, v[28:31] offset:512\nv_mfma_f32_16x16x32_bf16 v[32:35], v[232:235], v[56:59], v[32:35]\nv_mov_b64 v[52:53], 0\nv_mov_b64 v[54:55], 0\nv_mfma_f32_16x16x32_bf16 v[36:39], v[240:243], v[56:59], v[36:39]\n" \
"s_waitcnt lgkmcnt(2)\nv_mfma_f32_16x16x32_bf16 v[32:35], v[236:239], v[60:63], v[32:35]\nv_mfma_f32_16x16x32_bf16 v[36:39], v[244:247], v[60:63], v[36:39]\nv_mfma_f32_16x16x32_bf16 v[40:43], v[248:251], v[56:59], v[40:43]\nv_mfma_f32_16x16x32_bf16 v[44:47], v[72:75], v[56:59], v[44:47]\nv_cvt_pk_bf16_f32 v36, v32, v36\nv_cvt_pk_bf16_f32 v37, v33, v37\nv_mfma_f32_16x16x32_bf16 v[40:43], v[252:255], v[60:63], v[40:43]\n" \
"v_cvt_pk_bf16_f32 v38, v34, v38\nv_cvt_pk_bf16_f32 v39, v35, v39\nv_mfma_f32_16x16x32_bf16 v[44:47], v[76:79], v[60:63], v[44:47]\nds_write_b128 v5, v[36:39] offset:256\nv_mov_b64 v[56:57], 0\nv_mov_b64 v[58:59], 0\nv_mov_b64 v[60:61], 0\nv_mov_b64 v[62:63], 0\n" \
"s_nop 2\nv_cvt_pk_bf16_f32 v44, v40, v44\nv_cvt_pk_bf16_f32 v45, v41, v45\nv_cvt_pk_bf16_f32 v46, v42, v46\nv_cvt_pk_bf16_f32 v47, v43, v47\nds_write_b128 v5, v[44:47] offset:768\ns_cmp_eq_u32 %[w], 1\ns_cbranch_scc1 .Lqk_c1%=\n" \
"s_cmp_eq_u32 %[w], 2\ns_cbranch_scc1 .Lqk_c2%=\ns_cmp_eq_u32 %[w], 3\ns_cbranch_scc1 .Lqk_c3%=\n.Lqk_c0%=:\nds_read_b128 v[64:67], v1 offset:0\nds_read_b128 v[68:71], v1 offset:1024\nds_read_b128 v[72:75], v1 offset:2048\n" \
"ds_read_b128 v[76:79], v1 offset:3072\ns_bfe_i32 s54, s40, 0x10000\ns_and_b32 s44, s40, 1\ns_lshl_b32 s55, s44, 17\n.Lqk_poll0%=:\ns_waitcnt vmcnt(0)\nv_smfmac_f32_16x16x64_bf16 v[48:51], v[80:83], a[0:7], v4\nds_write_b128 v0, v[80:83] offset:16384\n" \
"v_max3_u32 v40, v80, v82, v84\nv_smfmac_f32_16x16x64_bf16 v[52:55], v[80:83], a[8:15], v4\nds_write_b128 v0, v[84:87] offset:17408\nv_max3_u32 v40, v40, v86, v88\nv_smfmac_f32_16x16x64_bf16 v[56:59], v[80:83], a[16:23], v4\nds_write_b128 v0, v[88:91] offset:18432\nv_max3_u32 v40, v40, v90, v92\nv_smfmac_f32_16x16x64_bf16 v[60:63], v[80:83], a[24:31], v4\n" \
"ds_write_b128 v0, v[92:95] offset:19456\nv_max_u32 v40, v40, v94\nv_smfmac_f32_16x16x64_bf16 v[48:51], v[84:87], a[32:39], v4\ns_waitcnt lgkmcnt(6)\nv_cmp_eq_u32 vcc, -1, v40\nv_smfmac_f32_16x16x64_bf16 v[52:55], v[84:87], v[64:71], v4\ns_cbranch_vccnz .Lqk_re0%=\nv_smfmac_f32_16x16x64_bf16 v[56:59], v[84:87], a[40:47], v4\n" \
"s_waitcnt lgkmcnt(0)\ns_barrier\nds_read_b128 v[80:83], v0 offset:20480\nv_smfmac_f32_16x16x64_bf16 v[60:63], v[84:87], a[48:55], v4\nds_read_b128 v[84:87], v0 offset:21504\nv_smfmac_f32_16x16x64_bf16 v[48:51], v[88:91], a[56:63], v4\nds_read_b128 v[64:67], v1 offset:4096\nds_read_b128 v[68:71], v1 offset:5120\n" \
"v_smfmac_f32_16x16x64_bf16 v[52:55], v[88:91], a[64:71], v4\nv_smfmac_f32_16x16x64_bf16 v[56:59], v[88:91], v[72:79], v4\nds_read_b128 v[72:75], v1 offset:6144\nds_read_b128 v[76:79], v1 offset:7168\nv_smfmac_f32_16x16x64_bf16 v[60:63], v[88:91], a[72:79], v4\nds_read_b128 v[88:91], v0 offset:22528\nv_smfmac_f32_16x16x64_bf16 v[48:51], v[92:95], a[80:87], v4\nds_read_b128 v[16:19], v7 offset:0\n" \
"v_smfmac_f32_16x16x64_bf16 v[52:55], v[92:95], a[88:95], v4\nv_smfmac_f32_16x16x64_bf16 v[56:59], v[92:95], a[96:103], v4\ns_waitcnt lgkmcnt(4)\nv_smfmac_f32_16x16x64_bf16 v[60:63], v[92:95], v[64:71], v4\nds_read_b128 v[92:95], v0 offset:23552\nds_read_b128 v[64:67], v1 offset:8192\nds_read_b128 v[68:71], v1 offset:9216\nv_smfmac_f32_16x16x64_bf16 v[48:51], v[80:83], a[104:111], v4\n" \
"ds_read_b128 v[20:23], v7 offset:64\nv_smfmac_f32_16x16x64_bf16 v[52:55], v[80:83], a[112:119], v4\nv_smfmac_f32_16x16x64_bf16 v[56:59], v[80:83], a[120:127], v4\nv_smfmac_f32_16x16x64_bf16 v[60:63], v[80:83], a[128:135], v4\nds_read_b128 v[80:83], v0 offset:24576\nv_smfmac_f32_16x16x64_bf16 v[48:51], v[84:87], a[136:143], v4\ns_waitcnt lgkmcnt(7)\nds_read_b128 v[24:27], v7 offset:2048\n" \
"v_smfmac_f32_16x16x64_bf16 v[52:55], v[84:87], v[72:79], v4\nds_read_b128 v[72:75], v1 offset:10240\nds_read_b128 v[76:79], v1 offset:11264\nv_smfmac_f32_16x16x64_bf16 v[56:59], v[84:87], a[144:151], v4\nv_smfmac_f32_16x16x64_bf16 v[60:63], v[84:87], a[152:159], v4\ns_waitcnt lgkmcnt(9)\nds_read_b128 v[84:87], v0 offset:25600\nv_smfmac_f32_16x16x64_bf16 v[48:51], v[88:91], a[160:167], v4\n" \
"ds_read_b128 v[28:31], v7 offset:2112\nv_smfmac_f32_16x16x64_bf16 v[52:55], v[88:91], a[168:175], v4\ns_waitcnt lgkmcnt(7)\nv_smfmac_f32_16x16x64_bf16 v[56:59], v[88:91], v[64:71], v4\nds_read_b128 v[64:67], v1 offset:12288\nds_read_b128 v[68:71], v1 offset:13312\nv_smfmac_f32_16x16x64_bf16 v[60:63], v[88:91], a[176:183], v4\nds_read_b128 v[88:91], v0 offset:26624\n" \
"v_smfmac_f32_16x16x64_bf16 v[48:51], v[92:95], a[184:191], v4\nds_read_b128 v[32:35], v7 offset:4096\nv_smfmac_f32_16x16x64_bf16 v[52:55], v[92:95], a[192:199], v4\nv_smfmac_f32_16x16x64_bf16 v[56:59], v[92:95], a[200:207], v4\ns_waitcnt lgkmcnt(6)\nv_smfmac_f32_16x16x64_bf16 v[60:63], v[92:95], v[72:79], v4\nds_read_b128 v[92:95], v0 offset:27648\nds_read_b128 v[72:75], v1 offset:14336\n" \
"ds_read_b128 v[76:79], v1 offset:15360\nv_smfmac_f32_16x16x64_bf16 v[48:51], v[80:83], a[208:215], v4\nds_read_b128 v[36:39], v7 offset:4160\nv_smfmac_f32_16x16x64_bf16 v[52:55], v[80:83], a[216:223], v4\nv_smfmac_f32_16x16x64_bf16 v[56:59], v[80:83], a[224:231], v4\nv_smfmac_f32_16x16x64_bf16 v[60:63], v[80:83], a[232:239], v4\ns_waitcnt lgkmcnt(9)\nds_read_b128 v[80:83], v0 offset:28672\n" \
"v_smfmac_f32_16x16x64_bf16 v[48:51], v[84:87], a[240:247], v4\ns_waitcnt lgkmcnt(7)\nds_read_b128 v[40:43], v7 offset:6144\nv_smfmac_f32_16x16x64_bf16 v[52:55], v[84:87], v[64:71], v4\nds_read_b128 v[64:67], v1 offset:16384\nds_read_b128 v[68:71], v1 offset:17408\nv_smfmac_f32_16x16x64_bf16 v[56:59], v[84:87], a[248:255], v4\nv_smfmac_f32_16x16x64_bf16 v[60:63], v[84:87], v[96:103], v4\n" \
"s_waitcnt lgkmcnt(9)\nds_read_b128 v[84:87], v0 offset:29696\nv_smfmac_f32_16x16x64_bf16 v[48:51], v[88:91], v[104:111], v4\nds_read_b128 v[44:47], v7 offset:6208\nv_smfmac_f32_16x16x64_bf16 v[52:55], v[88:91], v[112:119], v4\ns_waitcnt lgkmcnt(7)\nv_smfmac_f32_16x16x64_bf16 v[56:59], v[88:91], v[72:79], v4\nds_read_b128 v[72:75], v1 offset:18432\n" \
"ds_read_b128 v[76:79], v1 offset:19456\nv_smfmac_f32_16x16x64_bf16 v[60:63], v[88:91], v[120:127], v4\nds_read_b128 v[88:91], v0 offset:30720\nv_smfmac_f32_16x16x64_bf16 v[48:51], v[92:95], v[128:135], v4\nv_smfmac_f32_16x16x64_bf16 v[52:55], v[92:95], v[136:143], v4\nv_smfmac_f32_16x16x64_bf16 v[56:59], v[92:95], v[144:151], v4\ns_waitcnt lgkmcnt(5)\nv_smfmac_f32_16x16x64_bf16 v[60:63], v[92:95], v[64:71], v4\n" \
"ds_read_b128 v[92:95], v0 offset:31744\nds_read_b128 v[64:67], v1 offset:20480\nds_read_b128 v[68:71], v1 offset:21504\nv_smfmac_f32_16x16x64_bf16 v[48:51], v[80:83], v[152:159], v4\nv_smfmac_f32_16x16x64_bf16 v[52:55], v[80:83], v[160:167], v4\nv_smfmac_f32_16x16x64_bf16 v[56:59], v[80:83], v[168:175], v4\nv_smfmac_f32_16x16x64_bf16 v[60:63], v[80:83], v[176:183], v4\ns_waitcnt lgkmcnt(7)\n" \
"v_smfmac_f32_16x16x64_bf16 v[48:51], v[84:87], v[184:191], v4\ns_waitcnt lgkmcnt(4)\nv_smfmac_f32_16x16x64_bf16 v[52:55], v[84:87], v[72:79], v4\nds_read_b128 v[72:75], v1 offset:22528\nds_read_b128 v[76:79], v1 offset:23552\nv_smfmac_f32_16x16x64_bf16 v[56:59], v[84:87], v[192:199], v4\nv_smfmac_f32_16x16x64_bf16 v[60:63], v[84:87], v[200:207], v4\ns_waitcnt lgkmcnt(5)\n" \
"v_smfmac_f32_16x16x64_bf16 v[48:51], v[88:91], v[208:215], v4\nv_smfmac_f32_16x16x64_bf16 v[52:55], v[88:91], v[216:223], v4\ns_waitcnt lgkmcnt(4)\nv_smfmac_f32_16x16x64_bf16 v[48:51], v[92:95], v[232:239], v4\nv_smfmac_f32_16x16x64_bf16 v[52:55], v[92:95], v[240:247], v4\ns_waitcnt lgkmcnt(2)\nv_smfmac_f32_16x16x64_bf16 v[56:59], v[88:91], v[64:71], v4\nv_add_f32 v48, v48, v50\n" \
"v_smfmac_f32_16x16x64_bf16 v[60:63], v[88:91], v[224:231], v4\nv_add_f32 v52, v52, v54\nv_add_f32 v49, v49, v51\nv_smfmac_f32_16x16x64_bf16 v[56:59], v[92:95], v[248:255], v4\ns_waitcnt lgkmcnt(0)\nv_add_f32 v53, v53, v55\nv_cvt_pk_bf16_f32 v50, v48, v52\nv_smfmac_f32_16x16x64_bf16 v[60:63], v[92:95], v[72:79], v4\n" \
"v_cvt_pk_bf16_f32 v51, v49, v53\nds_write2_b32 v6, v50, v51 offset0:0 offset1:4\ns_nop 1\nv_add_f32 v56, v56, v58\nv_add_f32 v57, v57, v59\ns_nop 1\nv_add_f32 v60, v60, v62\nv_add_f32 v61, v61, v63\n" \
"v_cvt_pk_bf16_f32 v54, v56, v60\nv_cvt_pk_bf16_f32 v55, v57, v61\nds_write2_b32 v6, v54, v55 offset0:36 offset1:40\nv_mov_b32 v63, s54\ns_waitcnt lgkmcnt(0)\ns_barrier\nds_read_b128 v[80:83], v8 offset:0\nds_read_b128 v[84:87], v8 offset:576\n" \
"ds_read_b128 v[88:91], v8 offset:1152\nds_read_b128 v[92:95], v8 offset:1728\nds_read_b128 v[64:67], v9 offset:0\nds_read_b128 v[68:71], v9 offset:576\nds_read_b128 v[72:75], v9 offset:1152\nds_read_b128 v[76:79], v9 offset:1728\ns_waitcnt lgkmcnt(7)\nv_mfma_f32_16x16x32_bf16 v[52:55], v[16:19], v[80:83], 0\n" \
"v_mfma_f32_16x16x32_bf16 v[56:59], v[20:23], v[80:83], 0\ns_waitcnt lgkmcnt(6)\nv_mfma_f32_16x16x32_bf16 v[52:55], v[24:27], v[84:87], v[52:55]\nv_mfma_f32_16x16x32_bf16 v[56:59], v[28:31], v[84:87], v[56:59]\ns_waitcnt lgkmcnt(5)\nv_mfma_f32_16x16x32_bf16 v[52:55], v[32:35], v[88:91], v[52:55]\nv_mfma_f32_16x16x32_bf16 v[56:59], v[36:39], v[88:91], v[56:59]\ns_waitcnt lgkmcnt(4)\n" \
"v_mfma_f32_16x16x32_bf16 v[52:55], v[40:43], v[92:95], v[52:55]\nv_mfma_f32_16x16x32_bf16 v[56:59], v[44:47], v[92:95], v[56:59]\ns_waitcnt lgkmcnt(3)\nv_mfma_f32_16x16x32_bf16 v[48:51], v[64:67], v[80:83], 0\ns_waitcnt lgkmcnt(2)\nv_mfma_f32_16x16x32_bf16 v[48:51], v[68:71], v[84:87], v[48:51]\ns_waitcnt lgkmcnt(1)\nv_mfma_f32_16x16x32_bf16 v[48:51], v[72:75], v[88:91], v[48:51]\n" \
"s_waitcnt lgkmcnt(0)\nv_mfma_f32_16x16x32_bf16 v[48:51], v[76:79], v[92:95], v[48:51]\nv_max3_f32 v16, v52, v53, v54\nv_max3_f32 v17, v55, v56, v57\nv_max3_f32 v16, v16, v58, v59\nv_cndmask_b32 v52, v52, v56, s[62:63]\nv_cndmask_b32 v53, v53, v57, s[62:63]\nv_cndmask_b32 v54, v54, v58, s[62:63]\n" \
"v_cndmask_b32 v55, v55, v59, s[62:63]\nv_max_f32 v16, v16, v17\nv_max3_f32 v17, v16, v48, v49\nv_max3_f32 v16, v16, v48, v49\ns_nop 1\nv_permlane16_swap_b32 v16, v17\nv_max_f32 v18, v16, v17\nv_max_f32 v16, v16, v17\n" \
"s_nop 1\nv_permlane32_swap_b32 v16, v18\nv_max_f32 v16, v16, v18\nv_mul_f32 v16, s59, v16\nv_fma_f32 v52, v52, s58, v16\nv_fma_f32 v53, v53, s58, v16\nv_exp_f32 v52, v52\nv_fma_f32 v54, v54, s58, v16\n" \
"v_exp_f32 v53, v53\nv_fma_f32 v55, v55, s58, v16\nv_exp_f32 v54, v54\nv_add_f32 v17, v52, v53\nv_fma_f32 v48, v48, s58, v16\nv_exp_f32 v55, v55\nv_add_f32 v17, v17, v54\nv_fma_f32 v49, v49, s58, v16\n" \
"v_exp_f32 v48, v48\nv_add_f32 v17, v17, v55\nv_exp_f32 v49, v49\nv_add_f32 v18, v48, v17\nv_add_f32 v18, v18, v49\ns_nop 1\nv_add_f32_dpp v16, v17, v18 row_ror:8 row_mask:0xf bank_mask:0xf\nv_add_f32_dpp v18, v17, v18 row_ror:8 row_mask:0xf bank_mask:0xf\n" \
"s_nop 1\nv_permlane16_swap_b32 v16, v18\nv_add_f32 v17, v16, v18\nv_add_f32 v18, v16, v18\ns_nop 1\nv_permlane32_swap_b32 v17, v18\nv_add_f32 v17, v17, v18\nv_rcp_f32 v17, v17\n" \
"s_nop 0\nv_mul_f32 v52, v52, v17\nv_mul_f32 v53, v53, v17\nv_mul_f32 v54, v54, v17\nv_mul_f32 v55, v55, v17\nv_mul_f32 v48, v48, v17\nv_mul_f32 v49, v49, v17\nv_cvt_pk_bf16_f32 v52, v52, v53\n" \
"v_cvt_pk_bf16_f32 v53, v54, v55\nv_cvt_pk_bf16_f32 v48, v48, v49\nv_bfi_b32 v52, s61, v52, v63\nv_bfi_b32 v53, s61, v53, v63\nbuffer_store_dwordx2 v[52:53], v10, %[rs], 0 offen sc1\nv_bfi_b32 v48, s61, v48, v63\nbuffer_store_dword v48, v11, %[rs], 0 offen sc1\ns_cmp_lt_i32 s40, 63\n" \
"s_cbranch_scc1 .Lqk_proj%=\ns_branch .Lqk_end%=\n.Lqk_re0%=:\nv_max_u32 v40, v80, v82\nv_cmp_eq_u32 s[46:47], -1, v40\nv_max_u32 v40, v84, v86\nv_cmp_eq_u32 s[48:49], -1, v40\nv_max_u32 v40, v88, v90\n" \
"v_cmp_eq_u32 s[50:51], -1, v40\nv_max_u32 v40, v92, v94\nv_cmp_eq_u32 s[52:53], -1, v40\ns_mov_b64 exec, s[46:47]\nbuffer_load_dwordx4 v[80:83], v3, %[rs], s42 offen offset:0 sc1\ns_mov_b64 exec, s[48:49]\nbuffer_load_dwordx4 v[84:87], v3, %[rs], s42 offen offset:1024 sc1\ns_mov_b64 exec, s[50:51]\n" \
"buffer_load_dwordx4 v[88:91], v3, %[rs], s42 offen offset:2048 sc1\ns_mov_b64 exec, s[52:53]\nbuffer_load_dwordx4 v[92:95], v3, %[rs], s42 offen offset:3072 sc1\ns_mov_b64 exec, -1\nv_mov_b64 v[48:49], 0\nv_mov_b64 v[50:51], 0\nv_mov_b64 v[52:53], 0\nv_mov_b64 v[54:55], 0\n" \
"v_mov_b64 v[56:57], 0\nv_mov_b64 v[58:59], 0\nv_mov_b64 v[60:61], 0\nv_mov_b64 v[62:63], 0\ns_add_u32 s43, s43, 1\ns_cmp_lt_u32 s43, 262144\ns_cbranch_scc1 .Lqk_poll0%=\ns_branch .Lqk_abort%=\n" \
".Lqk_c1%=:\nds_read_b128 v[64:67], v1 offset:6144\nds_read_b128 v[68:71], v1 offset:7168\nds_read_b128 v[72:75], v1 offset:8192\nds_read_b128 v[76:79], v1 offset:9216\ns_bfe_i32 s54, s40, 0x10001\ns_and_b32 s44, s40, 1\ns_lshl_b32 s55, s44, 17\n" \
".Lqk_poll1%=:\ns_waitcnt vmcnt(0)\nv_smfmac_f32_16x16x64_bf16 v[48:51], v[80:83], a[104:111], v4\nds_write_b128 v0, v[80:83] offset:20480\nv_max3_u32 v40, v80, v82, v84\nv_smfmac_f32_16x16x64_bf16 v[52:55], v[80:83], a[112:119], v4\nds_write_b128 v0, v[84:87] offset:21504\nv_max3_u32 v40, v40, v86, v88\n" \
"v_smfmac_f32_16x16x64_bf16 v[56:59], v[80:83], a[120:127], v4\nds_write_b128 v0, v[88:91] offset:22528\nv_max3_u32 v40, v40, v90, v92\nv_smfmac_f32_16x16x64_bf16 v[60:63], v[80:83], a[128:135], v4\nds_write_b128 v0, v[92:95] offset:23552\nv_max_u32 v40, v40, v94\nv_smfmac_f32_16x16x64_bf16 v[48:51], v[84:87], a[136:143], v4\ns_waitcnt lgkmcnt(6)\n" \
"v_cmp_eq_u32 vcc, -1, v40\nv_smfmac_f32_16x16x64_bf16 v[52:55], v[84:87], v[64:71], v4\ns_cbranch_vccnz .Lqk_re1%=\nv_smfmac_f32_16x16x64_bf16 v[56:59], v[84:87], a[144:151], v4\ns_waitcnt lgkmcnt(0)\ns_barrier\nds_read_b128 v[80:83], v0 offset:24576\nv_smfmac_f32_16x16x64_bf16 v[60:63], v[84:87], a[152:159], v4\n" \
"ds_read_b128 v[84:87], v0 offset:25600\nv_smfmac_f32_16x16x64_bf16 v[48:51], v[88:91], a[160:167], v4\nds_read_b128 v[64:67], v1 offset:10240\nds_read_b128 v[68:71], v1 offset:11264\nv_smfmac_f32_16x16x64_bf16 v[52:55], v[88:91], a[168:175], v4\nv_smfmac_f32_16x16x64_bf16 v[56:59], v[88:91], v[72:79], v4\nds_read_b128 v[72:75], v1 offset:12288\nds_read_b128 v[76:79], v1 offset:13312\n" \
"v_smfmac_f32_16x16x64_bf16 v[60:63], v[88:91], a[176:183], v4\nds_read_b128 v[88:91], v0 offset:26624\nv_smfmac_f32_16x16x64_bf16 v[48:51], v[92:95], a[184:191], v4\nds_read_b128 v[16:19], v7 offset:0\nv_smfmac_f32_16x16x64_bf16 v[52:55], v[92:95], a[192:199], v4\nv_smfmac_f32_16x16x64_bf16 v[56:59], v[92:95], a[200:207], v4\ns_waitcnt lgkmcnt(4)\nv_smfmac_f32_16x16x64_bf16 v[60:63], v[92:95], v[64:71], v4\n" \
"ds_read_b128 v[92:95], v0 offset:27648\nds_read_b128 v[64:67], v1 offset:14336\nds_read_b128 v[68:71], v1 offset:15360\nv_smfmac_f32_16x16x64_bf16 v[48:51], v[80:83], a[208:215], v4\nds_read_b128 v[20:23], v7 offset:2048\nv_smfmac_f32_16x16x64_bf16 v[52:55], v[80:83], a[216:223], v4\nv_smfmac_f32_16x16x64_bf16 v[56:59], v[80:83], a[224:231], v4\nv_smfmac_f32_16x16x64_bf16 v[60:63], v[80:83], a[232:239], v4\n" \
"ds_read_b128 v[80:83], v0 offset:28672\nv_smfmac_f32_16x16x64_bf16 v[48:51], v[84:87], a[240:247], v4\ns_waitcnt lgkmcnt(7)\nds_read_b128 v[24:27], v7 offset:4096\nv_smfmac_f32_16x16x64_bf16 v[52:55], v[84:87], v[72:79], v4\nds_read_b128 v[72:75], v1 offset:16384\nds_read_b128 v[76:79], v1 offset:17408\nv_smfmac_f32_16x16x64_bf16 v[56:59], v[84:87], a[248:255], v4\n" \
"v_smfmac_f32_16x16x64_bf16 v[60:63], v[84:87], v[96:103], v4\ns_waitcnt lgkmcnt(9)\nds_read_b128 v[84:87], v0 offset:29696\nv_smfmac_f32_16x16x64_bf16 v[48:51], v[88:91], v[104:111], v4\nds_read_b128 v[28:31], v7 offset:6144\nv_smfmac_f32_16x16x64_bf16 v[52:55], v[88:91], v[112:119], v4\ns_waitcnt lgkmcnt(7)\nv_smfmac_f32_16x16x64_bf16 v[56:59], v[88:91], v[64:71], v4\n" \
"ds_read_b128 v[64:67], v1 offset:18432\nds_read_b128 v[68:71], v1 offset:19456\nv_smfmac_f32_16x16x64_bf16 v[60:63], v[88:91], v[120:127], v4\nds_read_b128 v[88:91], v0 offset:30720\nv_smfmac_f32_16x16x64_bf16 v[48:51], v[92:95], v[128:135], v4\nds_read_b128 v[32:35], v7 offset:8192\nv_smfmac_f32_16x16x64_bf16 v[52:55], v[92:95], v[136:143], v4\nv_smfmac_f32_16x16x64_bf16 v[56:59], v[92:95], v[144:151], v4\n" \
"s_waitcnt lgkmcnt(6)\nv_smfmac_f32_16x16x64_bf16 v[60:63], v[92:95], v[72:79], v4\nds_read_b128 v[92:95], v0 offset:31744\nds_read_b128 v[72:75], v1 offset:20480\nds_read_b128 v[76:79], v1 offset:21504\nv_smfmac_f32_16x16x64_bf16 v[48:51], v[80:83], v[152:159], v4\nds_read_b128 v[36:39], v7 offset:10240\nv_smfmac_f32_16x16x64_bf16 v[52:55], v[80:83], v[160:167], v4\n" \
"v_smfmac_f32_16x16x64_bf16 v[56:59], v[80:83], v[168:175], v4\nv_smfmac_f32_16x16x64_bf16 v[60:63], v[80:83], v[176:183], v4\ns_waitcnt lgkmcnt(9)\nds_read_b128 v[80:83], v0 offset:16384\nv_smfmac_f32_16x16x64_bf16 v[48:51], v[84:87], v[184:191], v4\ns_waitcnt lgkmcnt(7)\nds_read_b128 v[40:43], v7 offset:12288\nv_smfmac_f32_16x16x64_bf16 v[52:55], v[84:87], v[64:71], v4\n" \
"ds_read_b128 v[64:67], v1 offset:22528\nds_read_b128 v[68:71], v1 offset:23552\nv_smfmac_f32_16x16x64_bf16 v[56:59], v[84:87], v[192:199], v4\nv_smfmac_f32_16x16x64_bf16 v[60:63], v[84:87], v[200:207], v4\ns_waitcnt lgkmcnt(9)\nds_read_b128 v[84:87], v0 offset:17408\nv_smfmac_f32_16x16x64_bf16 v[48:51], v[88:91], v[208:215], v4\nds_read_b128 v[44:47], v7 offset:14336\n" \
"v_smfmac_f32_16x16x64_bf16 v[52:55], v[88:91], v[216:223], v4\ns_waitcnt lgkmcnt(7)\nv_smfmac_f32_16x16x64_bf16 v[56:59], v[88:91], v[72:79], v4\nds_read_b128 v[72:75], v1 offset:0\nds_read_b128 v[76:79], v1 offset:1024\nv_smfmac_f32_16x16x64_bf16 v[60:63], v[88:91], v[224:231], v4\nds_read_b128 v[88:91], v0 offset:18432\nv_smfmac_f32_16x16x64_bf16 v[48:51], v[92:95], v[232:239], v4\n" \
"v_smfmac_f32_16x16x64_bf16 v[52:55], v[92:95], v[240:247], v4\nv_smfmac_f32_16x16x64_bf16 v[56:59], v[92:95], v[248:255], v4\ns_waitcnt lgkmcnt(5)\nv_smfmac_f32_16x16x64_bf16 v[60:63], v[92:95], v[64:71], v4\nds_read_b128 v[92:95], v0 offset:19456\nds_read_b128 v[64:67], v1 offset:2048\nds_read_b128 v[68:71], v1 offset:3072\nv_smfmac_f32_16x16x64_bf16 v[48:51], v[80:83], a[0:7], v4\n" \
"v_smfmac_f32_16x16x64_bf16 v[52:55], v[80:83], a[8:15], v4\nv_smfmac_f32_16x16x64_bf16 v[56:59], v[80:83], a[16:23], v4\nv_smfmac_f32_16x16x64_bf16 v[60:63], v[80:83], a[24:31], v4\ns_waitcnt lgkmcnt(7)\nv_smfmac_f32_16x16x64_bf16 v[48:51], v[84:87], a[32:39], v4\ns_waitcnt lgkmcnt(4)\nv_smfmac_f32_16x16x64_bf16 v[52:55], v[84:87], v[72:79], v4\nds_read_b128 v[72:75], v1 offset:4096\n" \
"ds_read_b128 v[76:79], v1 offset:5120\nv_smfmac_f32_16x16x64_bf16 v[56:59], v[84:87], a[40:47], v4\nv_smfmac_f32_16x16x64_bf16 v[60:63], v[84:87], a[48:55], v4\ns_waitcnt lgkmcnt(5)\nv_smfmac_f32_16x16x64_bf16 v[48:51], v[88:91], a[56:63], v4\nv_smfmac_f32_16x16x64_bf16 v[52:55], v[88:91], a[64:71], v4\ns_waitcnt lgkmcnt(4)\nv_smfmac_f32_16x16x64_bf16 v[48:51], v[92:95], a[80:87], v4\n" \
"v_smfmac_f32_16x16x64_bf16 v[52:55], v[92:95], a[88:95], v4\ns_waitcnt lgkmcnt(2)\nv_smfmac_f32_16x16x64_bf16 v[56:59], v[88:91], v[64:71], v4\nv_add_f32 v48, v48, v50\nv_smfmac_f32_16x16x64_bf16 v[60:63], v[88:91], a[72:79], v4\nv_add_f32 v52, v52, v54\nv_add_f32 v49, v49, v51\nv_smfmac_f32_16x16x64_bf16 v[56:59], v[92:95], a[96:103], v4\n" \
"s_waitcnt lgkmcnt(0)\nv_add_f32 v53, v53, v55\nv_cvt_pk_bf16_f32 v50, v48, v52\nv_smfmac_f32_16x16x64_bf16 v[60:63], v[92:95], v[72:79], v4\nv_cvt_pk_bf16_f32 v51, v49, v53\nds_write2_b32 v6, v50, v51 offset0:0 offset1:4\ns_nop 1\nv_add_f32 v56, v56, v58\n" \
"v_add_f32 v57, v57, v59\ns_nop 1\nv_add_f32 v60, v60, v62\nv_add_f32 v61, v61, v63\nv_cvt_pk_bf16_f32 v54, v56, v60\nv_cvt_pk_bf16_f32 v55, v57, v61\nds_write2_b32 v6, v54, v55 offset0:36 offset1:40\nv_mov_b32 v63, s54\n" \
"s_waitcnt lgkmcnt(0)\ns_barrier\nds_read_b128 v[64:67], v9 offset:0\nds_read_b128 v[68:71], v9 offset:576\nds_read_b128 v[72:75], v9 offset:1152\nds_read_b128 v[76:79], v9 offset:1728\nv_mfma_f32_16x16x32_bf16 v[52:55], v[32:35], v[16:19], v[12:15]\nv_mfma_f32_16x16x32_bf16 v[52:55], v[36:39], v[20:23], v[52:55]\n" \
"v_mfma_f32_16x16x32_bf16 v[52:55], v[40:43], v[24:27], v[52:55]\nv_mfma_f32_16x16x32_bf16 v[52:55], v[44:47], v[28:31], v[52:55]\ns_waitcnt lgkmcnt(3)\nv_mfma_f32_16x16x32_bf16 v[48:51], v[64:67], v[16:19], 0\ns_waitcnt lgkmcnt(2)\nv_mfma_f32_16x16x32_bf16 v[48:51], v[68:71], v[20:23], v[48:51]\ns_waitcnt lgkmcnt(1)\nv_mfma_f32_16x16x32_bf16 v[48:51], v[72:75], v[24:27], v[48:51]\n" \
"s_waitcnt lgkmcnt(0)\nv_mfma_f32_16x16x32_bf16 v[48:51], v[76:79], v[28:31], v[48:51]\nv_max3_f32 v32, v52, v53, v54\ns_nop 6\nv_max3_f32 v32, v32, v55, v48\nv_max_f32 v33, v32, v49\nv_max_f32 v32, v32, v49\ns_nop 1\n" \
"v_permlane16_swap_b32 v32, v33\nv_max_f32 v34, v32, v33\nv_max_f32 v32, v32, v33\ns_nop 1\nv_permlane32_swap_b32 v32, v34\nv_max_f32 v32, v32, v34\nv_mul_f32 v32, s59, v32\nv_fma_f32 v52, v52, s58, v32\n" \
"v_fma_f32 v53, v53, s58, v32\nv_exp_f32 v52, v52\nv_fma_f32 v54, v54, s58, v32\nv_exp_f32 v53, v53\nv_fma_f32 v55, v55, s58, v32\nv_exp_f32 v54, v54\nv_add_f32 v33, v52, v53\nv_fma_f32 v48, v48, s58, v32\n" \
"v_exp_f32 v55, v55\nv_add_f32 v33, v33, v54\nv_fma_f32 v49, v49, s58, v32\nv_exp_f32 v48, v48\nv_add_f32 v33, v33, v55\nv_exp_f32 v49, v49\nv_add_f32 v33, v33, v48\nv_add_f32 v32, v33, v49\n" \
"v_add_f32 v34, v33, v49\ns_nop 1\nv_permlane16_swap_b32 v32, v34\nv_add_f32 v33, v32, v34\nv_add_f32 v34, v32, v34\ns_nop 1\nv_permlane32_swap_b32 v33, v34\nv_add_f32 v33, v33, v34\n" \
"v_rcp_f32 v33, v33\ns_nop 0\nv_mul_f32 v52, v52, v33\nv_mul_f32 v53, v53, v33\nv_mul_f32 v54, v54, v33\nv_mul_f32 v55, v55, v33\nv_mul_f32 v48, v48, v33\nv_mul_f32 v49, v49, v33\n" \
"v_cvt_pk_bf16_f32 v52, v52, v53\nv_cvt_pk_bf16_f32 v53, v54, v55\nv_cvt_pk_bf16_f32 v48, v48, v49\nv_bfi_b32 v52, s61, v52, v63\nv_bfi_b32 v53, s61, v53, v63\nv_bfi_b32 v48, s61, v48, v63\nbuffer_store_dwordx2 v[52:53], v10, %[rs], s55 offen sc1\nbuffer_store_dword v48, v11, %[rs], s55 offen sc1\n" \
"s_cmp_lt_i32 s40, 63\ns_cbranch_scc1 .Lqk_proj%=\ns_branch .Lqk_end%=\n.Lqk_re1%=:\nv_max_u32 v40, v80, v82\nv_cmp_eq_u32 s[46:47], -1, v40\nv_max_u32 v40, v84, v86\nv_cmp_eq_u32 s[48:49], -1, v40\n" \
"v_max_u32 v40, v88, v90\nv_cmp_eq_u32 s[50:51], -1, v40\nv_max_u32 v40, v92, v94\nv_cmp_eq_u32 s[52:53], -1, v40\ns_mov_b64 exec, s[46:47]\nbuffer_load_dwordx4 v[80:83], v3, %[rs], s42 offen offset:0 sc1\ns_mov_b64 exec, s[48:49]\nbuffer_load_dwordx4 v[84:87], v3, %[rs], s42 offen offset:1024 sc1\n" \
"s_mov_b64 exec, s[50:51]\nbuffer_load_dwordx4 v[88:91], v3, %[rs], s42 offen offset:2048 sc1\ns_mov_b64 exec, s[52:53]\nbuffer_load_dwordx4 v[92:95], v3, %[rs], s42 offen offset:3072 sc1\ns_mov_b64 exec, -1\nv_mov_b64 v[48:49], 0\nv_mov_b64 v[50:51], 0\nv_mov_b64 v[52:53], 0\n" \
"v_mov_b64 v[54:55], 0\nv_mov_b64 v[56:57], 0\nv_mov_b64 v[58:59], 0\nv_mov_b64 v[60:61], 0\nv_mov_b64 v[62:63], 0\ns_add_u32 s43, s43, 1\ns_cmp_lt_u32 s43, 262144\ns_cbranch_scc1 .Lqk_poll1%=\n" \
"s_branch .Lqk_abort%=\n.Lqk_c2%=:\nds_read_b128 v[64:67], v1 offset:12288\nds_read_b128 v[68:71], v1 offset:13312\nds_read_b128 v[72:75], v1 offset:14336\nds_read_b128 v[76:79], v1 offset:15360\ns_bfe_i32 s54, s40, 0x10001\ns_and_b32 s44, s40, 1\n" \
"s_lshl_b32 s55, s44, 17\n.Lqk_poll2%=:\ns_waitcnt vmcnt(0)\nv_smfmac_f32_16x16x64_bf16 v[48:51], v[80:83], a[208:215], v4\nds_write_b128 v0, v[80:83] offset:24576\nv_max3_u32 v40, v80, v82, v84\nv_smfmac_f32_16x16x64_bf16 v[52:55], v[80:83], a[216:223], v4\nds_write_b128 v0, v[84:87] offset:25600\n" \
"v_max3_u32 v40, v40, v86, v88\nv_smfmac_f32_16x16x64_bf16 v[56:59], v[80:83], a[224:231], v4\nds_write_b128 v0, v[88:91] offset:26624\nv_max3_u32 v40, v40, v90, v92\nv_smfmac_f32_16x16x64_bf16 v[60:63], v[80:83], a[232:239], v4\nds_write_b128 v0, v[92:95] offset:27648\nv_max_u32 v40, v40, v94\nv_smfmac_f32_16x16x64_bf16 v[48:51], v[84:87], a[240:247], v4\n" \
"s_waitcnt lgkmcnt(6)\nv_cmp_eq_u32 vcc, -1, v40\nv_smfmac_f32_16x16x64_bf16 v[52:55], v[84:87], v[64:71], v4\ns_cbranch_vccnz .Lqk_re2%=\nv_smfmac_f32_16x16x64_bf16 v[56:59], v[84:87], a[248:255], v4\ns_waitcnt lgkmcnt(0)\ns_barrier\nds_read_b128 v[80:83], v0 offset:28672\n" \
"v_smfmac_f32_16x16x64_bf16 v[60:63], v[84:87], v[96:103], v4\nds_read_b128 v[84:87], v0 offset:29696\nv_smfmac_f32_16x16x64_bf16 v[48:51], v[88:91], v[104:111], v4\nds_read_b128 v[64:67], v1 offset:16384\nds_read_b128 v[68:71], v1 offset:17408\nv_smfmac_f32_16x16x64_bf16 v[52:55], v[88:91], v[112:119], v4\nv_smfmac_f32_16x16x64_bf16 v[56:59], v[88:91], v[72:79], v4\nds_read_b128 v[72:75], v1 offset:18432\n" \
"ds_read_b128 v[76:79], v1 offset:19456\nv_smfmac_f32_16x16x64_bf16 v[60:63], v[88:91], v[120:127], v4\nds_read_b128 v[88:91], v0 offset:30720\nv_smfmac_f32_16x16x64_bf16 v[48:51], v[92:95], v[128:135], v4\nds_read_b128 v[16:19], v7 offset:256\nv_smfmac_f32_16x16x64_bf16 v[52:55], v[92:95], v[136:143], v4\nv_smfmac_f32_16x16x64_bf16 v[56:59], v[92:95], v[144:151], v4\ns_waitcnt lgkmcnt(4)\n" \
"v_smfmac_f32_16x16x64_bf16 v[60:63], v[92:95], v[64:71], v4\nds_read_b128 v[92:95], v0 offset:31744\nds_read_b128 v[64:67], v1 offset:20480\nds_read_b128 v[68:71], v1 offset:21504\nv_smfmac_f32_16x16x64_bf16 v[48:51], v[80:83], v[152:159], v4\nds_read_b128 v[20:23], v7 offset:2304\nv_smfmac_f32_16x16x64_bf16 v[52:55], v[80:83], v[160:167], v4\nv_smfmac_f32_16x16x64_bf16 v[56:59], v[80:83], v[168:175], v4\n" \
"v_smfmac_f32_16x16x64_bf16 v[60:63], v[80:83], v[176:183], v4\nds_read_b128 v[80:83], v0 offset:16384\nv_smfmac_f32_16x16x64_bf16 v[48:51], v[84:87], v[184:191], v4\ns_waitcnt lgkmcnt(7)\nds_read_b128 v[24:27], v7 offset:4352\nv_smfmac_f32_16x16x64_bf16 v[52:55], v[84:87], v[72:79], v4\nds_read_b128 v[72:75], v1 offset:22528\nds_read_b128 v[76:79], v1 offset:23552\n" \
"v_smfmac_f32_16x16x64_bf16 v[56:59], v[84:87], v[192:199], v4\nv_smfmac_f32_16x16x64_bf16 v[60:63], v[84:87], v[200:207], v4\ns_waitcnt lgkmcnt(9)\nds_read_b128 v[84:87], v0 offset:17408\nv_smfmac_f32_16x16x64_bf16 v[48:51], v[88:91], v[208:215], v4\nds_read_b128 v[28:31], v7 offset:6400\nv_smfmac_f32_16x16x64_bf16 v[52:55], v[88:91], v[216:223], v4\ns_waitcnt lgkmcnt(7)\n" \
"v_smfmac_f32_16x16x64_bf16 v[56:59], v[88:91], v[64:71], v4\nds_read_b128 v[64:67], v1 offset:0\nds_read_b128 v[68:71], v1 offset:1024\nv_smfmac_f32_16x16x64_bf16 v[60:63], v[88:91], v[224:231], v4\nds_read_b128 v[88:91], v0 offset:18432\nv_smfmac_f32_16x16x64_bf16 v[48:51], v[92:95], v[232:239], v4\nds_read_b128 v[32:35], v7 offset:8192\nv_smfmac_f32_16x16x64_bf16 v[52:55], v[92:95], v[240:247], v4\n" \
"v_smfmac_f32_16x16x64_bf16 v[56:59], v[92:95], v[248:255], v4\ns_waitcnt lgkmcnt(6)\nv_smfmac_f32_16x16x64_bf16 v[60:63], v[92:95], v[72:79], v4\nds_read_b128 v[92:95], v0 offset:19456\nds_read_b128 v[72:75], v1 offset:2048\nds_read_b128 v[76:79], v1 offset:3072\nv_smfmac_f32_16x16x64_bf16 v[48:51], v[80:83], a[0:7], v4\nds_read_b128 v[36:39], v7 offset:10240\n" \
"v_smfmac_f32_16x16x64_bf16 v[52:55], v[80:83], a[8:15], v4\nv_smfmac_f32_16x16x64_bf16 v[56:59], v[80:83], a[16:23], v4\nv_smfmac_f32_16x16x64_bf16 v[60:63], v[80:83], a[24:31], v4\ns_waitcnt lgkmcnt(9)\nds_read_b128 v[80:83], v0 offset:20480\nv_smfmac_f32_16x16x64_bf16 v[48:51], v[84:87], a[32:39], v4\ns_waitcnt lgkmcnt(7)\nds_read_b128 v[40:43], v7 offset:12288\n" \
"v_smfmac_f32_16x16x64_bf16 v[52:55], v[84:87], v[64:71], v4\nds_read_b128 v[64:67], v1 offset:4096\nds_read_b128 v[68:71], v1 offset:5120\nv_smfmac_f32_16x16x64_bf16 v[56:59], v[84:87], a[40:47], v4\nv_smfmac_f32_16x16x64_bf16 v[60:63], v[84:87], a[48:55], v4\ns_waitcnt lgkmcnt(9)\nds_read_b128 v[84:87], v0 offset:21504\nv_smfmac_f32_16x16x64_bf16 v[48:51], v[88:91], a[56:63], v4\n" \
"ds_read_b128 v[44:47], v7 offset:14336\nv_smfmac_f32_16x16x64_bf16 v[52:55], v[88:91], a[64:71], v4\ns_waitcnt lgkmcnt(7)\nv_smfmac_f32_16x16x64_bf16 v[56:59], v[88:91], v[72:79], v4\nds_read_b128 v[72:75], v1 offset:6144\nds_read_b128 v[76:79], v1 offset:7168\nv_smfmac_f32_16x16x64_bf16 v[60:63], v[88:91], a[72:79], v4\nds_read_b128 v[88:91], v0 offset:22528\n" \
"v_smfmac_f32_16x16x64_bf16 v[48:51], v[92:95], a[80:87], v4\nv_smfmac_f32_16x16x64_bf16 v[52:55], v[92:95], a[88:95], v4\nv_smfmac_f32_16x16x64_bf16 v[56:59], v[92:95], a[96:103], v4\ns_waitcnt lgkmcnt(5)\nv_smfmac_f32_16x16x64_bf16 v[60:63], v[92:95], v[64:71], v4\nds_read_b128 v[92:95], v0 offset:23552\nds_read_b128 v[64:67], v1 offset:8192\nds_read_b128 v[68:71], v1 offset:9216\n" \
"v_smfmac_f32_16x16x64_bf16 v[48:51], v[80:83], a[104:111], v4\nv_smfmac_f32_16x16x64_bf16 v[52:55], v[80:83], a[112:119], v4\nv_smfmac_f32_16x16x64_bf16 v[56:59], v[80:83], a[120:127], v4\nv_smfmac_f32_16x16x64_bf16 v[60:63], v[80:83], a[128:135], v4\ns_waitcnt lgkmcnt(7)\nv_smfmac_f32_16x16x64_bf16 v[48:51], v[84:87], a[136:143], v4\ns_waitcnt lgkmcnt(4)\nv_smfmac_f32_16x16x64_bf16 v[52:55], v[84:87], v[72:79], v4\n" \
"ds_read_b128 v[72:75], v1 offset:10240\nds_read_b128 v[76:79], v1 offset:11264\nv_smfmac_f32_16x16x64_bf16 v[56:59], v[84:87], a[144:151], v4\nv_smfmac_f32_16x16x64_bf16 v[60:63], v[84:87], a[152:159], v4\ns_waitcnt lgkmcnt(5)\nv_smfmac_f32_16x16x64_bf16 v[48:51], v[88:91], a[160:167], v4\nv_smfmac_f32_16x16x64_bf16 v[52:55], v[88:91], a[168:175], v4\ns_waitcnt lgkmcnt(4)\n" \
"v_smfmac_f32_16x16x64_bf16 v[48:51], v[92:95], a[184:191], v4\nv_smfmac_f32_16x16x64_bf16 v[52:55], v[92:95], a[192:199], v4\ns_waitcnt lgkmcnt(2)\nv_smfmac_f32_16x16x64_bf16 v[56:59], v[88:91], v[64:71], v4\nv_add_f32 v48, v48, v50\nv_smfmac_f32_16x16x64_bf16 v[60:63], v[88:91], a[176:183], v4\nv_add_f32 v52, v52, v54\nv_add_f32 v49, v49, v51\n" \
"v_smfmac_f32_16x16x64_bf16 v[56:59], v[92:95], a[200:207], v4\ns_waitcnt lgkmcnt(0)\nv_add_f32 v53, v53, v55\nv_cvt_pk_bf16_f32 v50, v48, v52\nv_smfmac_f32_16x16x64_bf16 v[60:63], v[92:95], v[72:79], v4\nv_cvt_pk_bf16_f32 v51, v49, v53\nds_write2_b32 v6, v50, v51 offset0:0 offset1:4\ns_nop 1\n" \
"v_add_f32 v56, v56, v58\nv_add_f32 v57, v57, v59\ns_nop 1\nv_add_f32 v60, v60, v62\nv_add_f32 v61, v61, v63\nv_cvt_pk_bf16_f32 v54, v56, v60\nv_cvt_pk_bf16_f32 v55, v57, v61\nds_write2_b32 v6, v54, v55 offset0:36 offset1:40\n" \
"v_mov_b32 v63, s54\ns_waitcnt lgkmcnt(0)\ns_barrier\nds_read_b128 v[64:67], v9 offset:0\nds_read_b128 v[68:71], v9 offset:576\nds_read_b128 v[72:75], v9 offset:1152\nds_read_b128 v[76:79], v9 offset:1728\nds_read_b128 v[80:83], v7 offset:8448\n" \
"ds_read_b128 v[84:87], v7 offset:10496\nds_read_b128 v[88:91], v7 offset:12544\nds_read_b128 v[92:95], v7 offset:14592\nv_mfma_f32_16x16x32_bf16 v[52:55], v[32:35], v[16:19], 0\nv_mfma_f32_16x16x32_bf16 v[52:55], v[36:39], v[20:23], v[52:55]\nv_mfma_f32_16x16x32_bf16 v[52:55], v[40:43], v[24:27], v[52:55]\nv_mfma_f32_16x16x32_bf16 v[52:55], v[44:47], v[28:31], v[52:55]\ns_waitcnt lgkmcnt(7)\n" \
"v_mfma_f32_16x16x32_bf16 v[48:51], v[64:67], v[16:19], 0\ns_waitcnt lgkmcnt(6)\nv_mfma_f32_16x16x32_bf16 v[48:51], v[68:71], v[20:23], v[48:51]\ns_waitcnt lgkmcnt(5)\nv_mfma_f32_16x16x32_bf16 v[48:51], v[72:75], v[24:27], v[48:51]\ns_waitcnt lgkmcnt(4)\nv_mfma_f32_16x16x32_bf16 v[48:51], v[76:79], v[28:31], v[48:51]\ns_waitcnt lgkmcnt(3)\n" \
"v_mfma_f32_16x16x32_bf16 v[56:59], v[80:83], v[16:19], v[12:15]\ns_waitcnt lgkmcnt(2)\nv_mfma_f32_16x16x32_bf16 v[56:59], v[84:87], v[20:23], v[56:59]\ns_waitcnt lgkmcnt(1)\nv_mfma_f32_16x16x32_bf16 v[56:59], v[88:91], v[24:27], v[56:59]\ns_waitcnt lgkmcnt(0)\nv_mfma_f32_16x16x32_bf16 v[56:59], v[92:95], v[28:31], v[56:59]\nv_max3_f32 v32, v52, v53, v54\n" \
"v_max3_f32 v32, v32, v55, v48\ns_nop 5\nv_max3_f32 v32, v32, v49, v56\nv_max_f32 v32, v32, v57\nv_max3_f32 v33, v32, v58, v59\nv_max3_f32 v32, v32, v58, v59\ns_nop 1\nv_permlane16_swap_b32 v32, v33\n" \
"v_max_f32 v34, v32, v33\nv_max_f32 v32, v32, v33\ns_nop 1\nv_permlane32_swap_b32 v32, v34\nv_max_f32 v32, v32, v34\nv_mul_f32 v32, s59, v32\nv_fma_f32 v52, v52, s58, v32\nv_fma_f32 v53, v53, s58, v32\n" \
"v_exp_f32 v52, v52\nv_fma_f32 v54, v54, s58, v32\nv_exp_f32 v53, v53\nv_fma_f32 v55, v55, s58, v32\nv_exp_f32 v54, v54\nv_add_f32 v33, v52, v53\nv_fma_f32 v48, v48, s58, v32\nv_exp_f32 v55, v55\n" \
"v_add_f32 v33, v33, v54\nv_fma_f32 v49, v49, s58, v32\nv_exp_f32 v48, v48\nv_add_f32 v33, v33, v55\nv_fma_f32 v56, v56, s58, v32\nv_exp_f32 v49, v49\nv_add_f32 v33, v33, v48\nv_fma_f32 v57, v57, s58, v32\n" \
"v_exp_f32 v56, v56\nv_add_f32 v33, v33, v49\nv_fma_f32 v58, v58, s58, v32\nv_exp_f32 v57, v57\nv_add_f32 v33, v33, v56\nv_fma_f32 v59, v59, s58, v32\nv_exp_f32 v58, v58\nv_add_f32 v33, v33, v57\n" \
"v_exp_f32 v59, v59\nv_add_f32 v33, v33, v58\nv_add_f32 v32, v33, v59\nv_add_f32 v34, v33, v59\ns_nop 1\nv_permlane16_swap_b32 v32, v34\nv_add_f32 v33, v32, v34\nv_add_f32 v34, v32, v34\n" \
"s_nop 1\nv_permlane32_swap_b32 v33, v34\nv_add_f32 v33, v33, v34\nv_rcp_f32 v33, v33\ns_nop 0\nv_mul_f32 v52, v52, v33\nv_mul_f32 v53, v53, v33\nv_mul_f32 v54, v54, v33\n" \
"v_mul_f32 v55, v55, v33\nv_mul_f32 v48, v48, v33\nv_mul_f32 v49, v49, v33\nv_mul_f32 v56, v56, v33\nv_mul_f32 v57, v57, v33\nv_mul_f32 v58, v58, v33\nv_mul_f32 v59, v59, v33\nv_cvt_pk_bf16_f32 v52, v52, v53\n" \
"v_cvt_pk_bf16_f32 v53, v54, v55\nv_cvt_pk_bf16_f32 v48, v48, v49\nv_cvt_pk_bf16_f32 v54, v56, v57\nv_cvt_pk_bf16_f32 v55, v58, v59\nv_bfi_b32 v52, s61, v52, v63\nv_bfi_b32 v53, s61, v53, v63\nv_bfi_b32 v54, s61, v54, v63\nv_bfi_b32 v55, s61, v55, v63\n" \
"v_bfi_b32 v48, s61, v48, v63\nbuffer_store_dwordx4 v[52:55], v10, %[rs], s55 offen sc1\nbuffer_store_dword v48, v11, %[rs], s55 offen sc1\ns_cmp_lt_i32 s40, 63\ns_cbranch_scc1 .Lqk_proj%=\ns_branch .Lqk_end%=\n.Lqk_re2%=:\nv_max_u32 v40, v80, v82\n" \
"v_cmp_eq_u32 s[46:47], -1, v40\nv_max_u32 v40, v84, v86\nv_cmp_eq_u32 s[48:49], -1, v40\nv_max_u32 v40, v88, v90\nv_cmp_eq_u32 s[50:51], -1, v40\nv_max_u32 v40, v92, v94\nv_cmp_eq_u32 s[52:53], -1, v40\ns_mov_b64 exec, s[46:47]\n" \
"buffer_load_dwordx4 v[80:83], v3, %[rs], s42 offen offset:0 sc1\ns_mov_b64 exec, s[48:49]\nbuffer_load_dwordx4 v[84:87], v3, %[rs], s42 offen offset:1024 sc1\ns_mov_b64 exec, s[50:51]\nbuffer_load_dwordx4 v[88:91], v3, %[rs], s42 offen offset:2048 sc1\ns_mov_b64 exec, s[52:53]\nbuffer_load_dwordx4 v[92:95], v3, %[rs], s42 offen offset:3072 sc1\ns_mov_b64 exec, -1\n" \
"v_mov_b64 v[48:49], 0\nv_mov_b64 v[50:51], 0\nv_mov_b64 v[52:53], 0\nv_mov_b64 v[54:55], 0\nv_mov_b64 v[56:57], 0\nv_mov_b64 v[58:59], 0\nv_mov_b64 v[60:61], 0\nv_mov_b64 v[62:63], 0\n" \
"s_add_u32 s43, s43, 1\ns_cmp_lt_u32 s43, 262144\ns_cbranch_scc1 .Lqk_poll2%=\ns_branch .Lqk_abort%=\n.Lqk_c3%=:\nds_read_b128 v[64:67], v1 offset:18432\nds_read_b128 v[68:71], v1 offset:19456\nds_read_b128 v[72:75], v1 offset:20480\n" \
"ds_read_b128 v[76:79], v1 offset:21504\ns_add_u32 s56, s56, %[fs]\ns_add_u32 s44, s40, 1\ns_lshl_b32 s44, s44, 17\ns_add_u32 s44, s44, %[xn]\ns_lshr_b32 s45, s44, 8\ns_and_b32 s45, s45, 0x2000\ns_xor_b32 s41, s44, s45\n" \
"buffer_load_dword v16, v12, %[rs], s56 offen sc1\n.Lqk_poll3%=:\ns_waitcnt vmcnt(1)\nv_smfmac_f32_16x16x64_bf16 v[48:51], v[80:83], v[152:159], v4\nds_write_b128 v0, v[80:83] offset:28672\nv_max3_u32 v40, v80, v82, v84\nv_smfmac_f32_16x16x64_bf16 v[52:55], v[80:83], v[160:167], v4\nds_write_b128 v0, v[84:87] offset:29696\n" \
"v_max3_u32 v40, v40, v86, v88\nv_smfmac_f32_16x16x64_bf16 v[56:59], v[80:83], v[168:175], v4\nds_write_b128 v0, v[88:91] offset:30720\nv_max3_u32 v40, v40, v90, v92\nv_smfmac_f32_16x16x64_bf16 v[60:63], v[80:83], v[176:183], v4\nds_write_b128 v0, v[92:95] offset:31744\nv_max_u32 v40, v40, v94\nv_smfmac_f32_16x16x64_bf16 v[48:51], v[84:87], v[184:191], v4\n" \
"s_waitcnt lgkmcnt(6)\nv_cmp_eq_u32 vcc, -1, v40\nv_smfmac_f32_16x16x64_bf16 v[52:55], v[84:87], v[64:71], v4\ns_cbranch_vccnz .Lqk_re3%=\nv_smfmac_f32_16x16x64_bf16 v[56:59], v[84:87], v[192:199], v4\ns_waitcnt lgkmcnt(0)\ns_barrier\nds_read_b128 v[80:83], v0 offset:16384\n" \
"v_smfmac_f32_16x16x64_bf16 v[60:63], v[84:87], v[200:207], v4\nds_read_b128 v[84:87], v0 offset:17408\nv_smfmac_f32_16x16x64_bf16 v[48:51], v[88:91], v[208:215], v4\nds_read_b128 v[64:67], v1 offset:22528\nds_read_b128 v[68:71], v1 offset:23552\nv_smfmac_f32_16x16x64_bf16 v[52:55], v[88:91], v[216:223], v4\nv_smfmac_f32_16x16x64_bf16 v[56:59], v[88:91], v[72:79], v4\nds_read_b128 v[72:75], v1 offset:0\n" \
"ds_read_b128 v[76:79], v1 offset:1024\nv_smfmac_f32_16x16x64_bf16 v[60:63], v[88:91], v[224:231], v4\nds_read_b128 v[88:91], v0 offset:18432\nv_smfmac_f32_16x16x64_bf16 v[48:51], v[92:95], v[232:239], v4\nv_smfmac_f32_16x16x64_bf16 v[52:55], v[92:95], v[240:247], v4\nv_smfmac_f32_16x16x64_bf16 v[56:59], v[92:95], v[248:255], v4\ns_waitcnt lgkmcnt(3)\nv_smfmac_f32_16x16x64_bf16 v[60:63], v[92:95], v[64:71], v4\n" \
"ds_read_b128 v[92:95], v0 offset:19456\nds_read_b128 v[64:67], v1 offset:2048\nds_read_b128 v[68:71], v1 offset:3072\nv_smfmac_f32_16x16x64_bf16 v[48:51], v[80:83], a[0:7], v4\ns_waitcnt vmcnt(0)\nv_cmp_ge_i32 vcc, s40, v16\nv_smfmac_f32_16x16x64_bf16 v[52:55], v[80:83], a[8:15], v4\ns_cmp_eq_u64 vcc, 0\n" \
"s_cselect_b32 s61, s41, -2147483648\nv_smfmac_f32_16x16x64_bf16 v[56:59], v[80:83], a[16:23], v4\ns_add_u32 m0, %[l0], 0\ns_add_u32 s60, s61, 0\nbuffer_load_dwordx4 v0, %[rs], s60 offen offset:0 sc1 lds\nv_smfmac_f32_16x16x64_bf16 v[60:63], v[80:83], a[24:31], v4\nds_read_b128 v[80:83], v0 offset:20480\nv_smfmac_f32_16x16x64_bf16 v[48:51], v[84:87], a[32:39], v4\n" \
"s_waitcnt lgkmcnt(5)\nbuffer_load_dwordx4 v0, %[rs], s60 offen offset:1024 sc1 lds\nv_smfmac_f32_16x16x64_bf16 v[52:55], v[84:87], v[72:79], v4\nds_read_b128 v[72:75], v1 offset:4096\nds_read_b128 v[76:79], v1 offset:5120\nv_smfmac_f32_16x16x64_bf16 v[56:59], v[84:87], a[40:47], v4\nbuffer_load_dwordx4 v0, %[rs], s60 offen offset:2048 sc1 lds\nv_smfmac_f32_16x16x64_bf16 v[60:63], v[84:87], a[48:55], v4\n" \
"s_waitcnt lgkmcnt(6)\nds_read_b128 v[84:87], v0 offset:21504\nv_smfmac_f32_16x16x64_bf16 v[48:51], v[88:91], a[56:63], v4\nbuffer_load_dwordx4 v0, %[rs], s60 offen offset:3072 sc1 lds\nv_smfmac_f32_16x16x64_bf16 v[52:55], v[88:91], a[64:71], v4\ns_waitcnt lgkmcnt(4)\nv_smfmac_f32_16x16x64_bf16 v[56:59], v[88:91], v[64:71], v4\ns_add_u32 m0, %[l0], 4096\n" \
"s_add_u32 s60, s61, 4096\nds_read_b128 v[64:67], v1 offset:6144\nds_read_b128 v[68:71], v1 offset:7168\nbuffer_load_dwordx4 v0, %[rs], s60 offen offset:0 sc1 lds\nv_smfmac_f32_16x16x64_bf16 v[60:63], v[88:91], a[72:79], v4\nds_read_b128 v[88:91], v0 offset:22528\nv_smfmac_f32_16x16x64_bf16 v[48:51], v[92:95], a[80:87], v4\nbuffer_load_dwordx4 v0, %[rs], s60 offen offset:1024 sc1 lds\n" \
"v_smfmac_f32_16x16x64_bf16 v[52:55], v[92:95], a[88:95], v4\nv_smfmac_f32_16x16x64_bf16 v[56:59], v[92:95], a[96:103], v4\ns_waitcnt lgkmcnt(4)\nbuffer_load_dwordx4 v0, %[rs], s60 offen offset:2048 sc1 lds\nv_smfmac_f32_16x16x64_bf16 v[60:63], v[92:95], v[72:79], v4\nds_read_b128 v[92:95], v0 offset:23552\nds_read_b128 v[72:75], v1 offset:8192\nds_read_b128 v[76:79], v1 offset:9216\n" \
"v_smfmac_f32_16x16x64_bf16 v[48:51], v[80:83], a[104:111], v4\nbuffer_load_dwordx4 v0, %[rs], s60 offen offset:3072 sc1 lds\nv_smfmac_f32_16x16x64_bf16 v[52:55], v[80:83], a[112:119], v4\nv_smfmac_f32_16x16x64_bf16 v[56:59], v[80:83], a[120:127], v4\ns_add_u32 m0, %[l0], 8192\ns_add_u32 s60, s61, 16384\nbuffer_load_dwordx4 v0, %[rs], s60 offen offset:0 sc1 lds\nv_smfmac_f32_16x16x64_bf16 v[60:63], v[80:83], a[128:135], v4\n" \
"s_waitcnt lgkmcnt(6)\nds_read_b128 v[80:83], v0 offset:24576\nv_smfmac_f32_16x16x64_bf16 v[48:51], v[84:87], a[136:143], v4\ns_waitcnt lgkmcnt(5)\nbuffer_load_dwordx4 v0, %[rs], s60 offen offset:1024 sc1 lds\nv_smfmac_f32_16x16x64_bf16 v[52:55], v[84:87], v[64:71], v4\nds_read_b128 v[64:67], v1 offset:10240\nds_read_b128 v[68:71], v1 offset:11264\n" \
"v_smfmac_f32_16x16x64_bf16 v[56:59], v[84:87], a[144:151], v4\nbuffer_load_dwordx4 v0, %[rs], s60 offen offset:2048 sc1 lds\nv_smfmac_f32_16x16x64_bf16 v[60:63], v[84:87], a[152:159], v4\ns_waitcnt lgkmcnt(6)\nds_read_b128 v[84:87], v0 offset:25600\nv_smfmac_f32_16x16x64_bf16 v[48:51], v[88:91], a[160:167], v4\nbuffer_load_dwordx4 v0, %[rs], s60 offen offset:3072 sc1 lds\nv_smfmac_f32_16x16x64_bf16 v[52:55], v[88:91], a[168:175], v4\n" \
"s_waitcnt lgkmcnt(4)\nv_smfmac_f32_16x16x64_bf16 v[56:59], v[88:91], v[72:79], v4\ns_add_u32 m0, %[l0], 12288\ns_add_u32 s60, s61, 20480\nds_read_b128 v[72:75], v1 offset:12288\nds_read_b128 v[76:79], v1 offset:13312\nbuffer_load_dwordx4 v0, %[rs], s60 offen offset:0 sc1 lds\nv_smfmac_f32_16x16x64_bf16 v[60:63], v[88:91], a[176:183], v4\n" \
"ds_read_b128 v[88:91], v0 offset:26624\nv_smfmac_f32_16x16x64_bf16 v[48:51], v[92:95], a[184:191], v4\nbuffer_load_dwordx4 v0, %[rs], s60 offen offset:1024 sc1 lds\nv_smfmac_f32_16x16x64_bf16 v[52:55], v[92:95], a[192:199], v4\nv_smfmac_f32_16x16x64_bf16 v[56:59], v[92:95], a[200:207], v4\ns_waitcnt lgkmcnt(4)\nbuffer_load_dwordx4 v0, %[rs], s60 offen offset:2048 sc1 lds\nv_smfmac_f32_16x16x64_bf16 v[60:63], v[92:95], v[64:71], v4\n" \
"ds_read_b128 v[92:95], v0 offset:27648\nds_read_b128 v[64:67], v1 offset:14336\nds_read_b128 v[68:71], v1 offset:15360\nv_smfmac_f32_16x16x64_bf16 v[48:51], v[80:83], a[208:215], v4\nbuffer_load_dwordx4 v0, %[rs], s60 offen offset:3072 sc1 lds\nv_smfmac_f32_16x16x64_bf16 v[52:55], v[80:83], a[216:223], v4\nv_smfmac_f32_16x16x64_bf16 v[56:59], v[80:83], a[224:231], v4\nv_smfmac_f32_16x16x64_bf16 v[60:63], v[80:83], a[232:239], v4\n" \
"s_waitcnt lgkmcnt(6)\nv_smfmac_f32_16x16x64_bf16 v[48:51], v[84:87], a[240:247], v4\ns_waitcnt lgkmcnt(4)\nv_smfmac_f32_16x16x64_bf16 v[52:55], v[84:87], v[72:79], v4\nds_read_b128 v[72:75], v1 offset:16384\nds_read_b128 v[76:79], v1 offset:17408\nv_smfmac_f32_16x16x64_bf16 v[56:59], v[84:87], a[248:255], v4\nv_smfmac_f32_16x16x64_bf16 v[60:63], v[84:87], v[96:103], v4\n" \
"s_waitcnt lgkmcnt(5)\nv_smfmac_f32_16x16x64_bf16 v[48:51], v[88:91], v[104:111], v4\nv_smfmac_f32_16x16x64_bf16 v[52:55], v[88:91], v[112:119], v4\ns_waitcnt lgkmcnt(4)\nv_smfmac_f32_16x16x64_bf16 v[48:51], v[92:95], v[128:135], v4\nv_smfmac_f32_16x16x64_bf16 v[52:55], v[92:95], v[136:143], v4\ns_waitcnt lgkmcnt(2)\nv_smfmac_f32_16x16x64_bf16 v[56:59], v[88:91], v[64:71], v4\n" \
"v_add_f32 v48, v48, v50\nv_smfmac_f32_16x16x64_bf16 v[60:63], v[88:91], v[120:127], v4\nv_add_f32 v52, v52, v54\nv_add_f32 v49, v49, v51\nv_smfmac_f32_16x16x64_bf16 v[56:59], v[92:95], v[144:151], v4\ns_waitcnt lgkmcnt(0)\nv_add_f32 v53, v53, v55\nv_cvt_pk_bf16_f32 v50, v48, v52\n" \
"v_smfmac_f32_16x16x64_bf16 v[60:63], v[92:95], v[72:79], v4\nv_cvt_pk_bf16_f32 v51, v49, v53\nds_write2_b32 v6, v50, v51 offset0:0 offset1:4\ns_nop 1\nv_add_f32 v56, v56, v58\nv_add_f32 v57, v57, v59\ns_nop 1\nv_add_f32 v60, v60, v62\n" \
"v_add_f32 v61, v61, v63\nv_cvt_pk_bf16_f32 v54, v56, v60\nv_cvt_pk_bf16_f32 v55, v57, v61\nds_write2_b32 v6, v54, v55 offset0:36 offset1:40\ns_waitcnt lgkmcnt(0)\ns_barrier\ns_cbranch_vccz .Lqk_fq1%=\ns_cmp_ge_i32 s40, 63\n" \
"s_cbranch_scc1 .Lqk_end%=\n.Lqk_fetch0%=:\nbuffer_load_dword v16, v12, %[rs], s56 offen sc1\ns_waitcnt vmcnt(0)\nv_cmp_ge_i32 vcc, s40, v16\ns_cbranch_vccz .Lqk_fok%=\ns_add_u32 s43, s43, 1\ns_cmp_lt_u32 s43, 262144\n" \
"s_cbranch_scc1 .Lqk_fetch0%=\ns_branch .Lqk_abort%=\n.Lqk_fok%=:\ns_add_u32 s44, s40, 1\ns_lshl_b32 s44, s44, 17\ns_add_u32 s44, s44, %[xn]\ns_lshr_b32 s45, s44, 8\ns_and_b32 s45, s45, 0x2000\n" \
"s_xor_b32 s41, s44, s45\ns_add_u32 m0, %[l0], 0\ns_add_u32 s60, s41, 0\nbuffer_load_dwordx4 v0, %[rs], s60 offen offset:0 sc1 lds\nbuffer_load_dwordx4 v0, %[rs], s60 offen offset:1024 sc1 lds\nbuffer_load_dwordx4 v0, %[rs], s60 offen offset:2048 sc1 lds\nbuffer_load_dwordx4 v0, %[rs], s60 offen offset:3072 sc1 lds\ns_add_u32 m0, %[l0], 4096\n" \
"s_add_u32 s60, s41, 4096\nbuffer_load_dwordx4 v0, %[rs], s60 offen offset:0 sc1 lds\nbuffer_load_dwordx4 v0, %[rs], s60 offen offset:1024 sc1 lds\nbuffer_load_dwordx4 v0, %[rs], s60 offen offset:2048 sc1 lds\nbuffer_load_dwordx4 v0, %[rs], s60 offen offset:3072 sc1 lds\ns_add_u32 m0, %[l0], 8192\ns_add_u32 s60, s41, 16384\nbuffer_load_dwordx4 v0, %[rs], s60 offen offset:0 sc1 lds\n" \
"buffer_load_dwordx4 v0, %[rs], s60 offen offset:1024 sc1 lds\nbuffer_load_dwordx4 v0, %[rs], s60 offen offset:2048 sc1 lds\nbuffer_load_dwordx4 v0, %[rs], s60 offen offset:3072 sc1 lds\ns_add_u32 m0, %[l0], 12288\ns_add_u32 s60, s41, 20480\nbuffer_load_dwordx4 v0, %[rs], s60 offen offset:0 sc1 lds\nbuffer_load_dwordx4 v0, %[rs], s60 offen offset:1024 sc1 lds\nbuffer_load_dwordx4 v0, %[rs], s60 offen offset:2048 sc1 lds\n" \
"buffer_load_dwordx4 v0, %[rs], s60 offen offset:3072 sc1 lds\n.Lqk_fq1%=:\ns_add_u32 m0, %[l0], 16384\ns_add_u32 s60, s41, 32768\nbuffer_load_dwordx4 v0, %[rs], s60 offen offset:0 sc1 lds\nbuffer_load_dwordx4 v0, %[rs], s60 offen offset:1024 sc1 lds\nbuffer_load_dwordx4 v0, %[rs], s60 offen offset:2048 sc1 lds\nbuffer_load_dwordx4 v0, %[rs], s60 offen offset:3072 sc1 lds\n" \
"s_add_u32 m0, %[l0], 20480\ns_add_u32 s60, s41, 36864\nbuffer_load_dwordx4 v0, %[rs], s60 offen offset:0 sc1 lds\nbuffer_load_dwordx4 v0, %[rs], s60 offen offset:1024 sc1 lds\nbuffer_load_dwordx4 v0, %[rs], s60 offen offset:2048 sc1 lds\nbuffer_load_dwordx4 v0, %[rs], s60 offen offset:3072 sc1 lds\ns_add_u32 m0, %[l0], 24576\ns_add_u32 s60, s41, 49152\n" \
"buffer_load_dwordx4 v0, %[rs], s60 offen offset:0 sc1 lds\nbuffer_load_dwordx4 v0, %[rs], s60 offen offset:1024 sc1 lds\nbuffer_load_dwordx4 v0, %[rs], s60 offen offset:2048 sc1 lds\nbuffer_load_dwordx4 v0, %[rs], s60 offen offset:3072 sc1 lds\ns_add_u32 m0, %[l0], 28672\ns_add_u32 s60, s41, 53248\nbuffer_load_dwordx4 v0, %[rs], s60 offen offset:0 sc1 lds\nbuffer_load_dwordx4 v0, %[rs], s60 offen offset:1024 sc1 lds\n" \
"buffer_load_dwordx4 v0, %[rs], s60 offen offset:2048 sc1 lds\nbuffer_load_dwordx4 v0, %[rs], s60 offen offset:3072 sc1 lds\ns_waitcnt vmcnt(16)\ns_cmp_lt_i32 s40, 63\ns_cbranch_scc1 .Lqk_proj%=\ns_branch .Lqk_end%=\n.Lqk_re3%=:\nv_max_u32 v40, v80, v82\n" \
"v_cmp_eq_u32 s[46:47], -1, v40\nv_max_u32 v40, v84, v86\nv_cmp_eq_u32 s[48:49], -1, v40\nv_max_u32 v40, v88, v90\nv_cmp_eq_u32 s[50:51], -1, v40\nv_max_u32 v40, v92, v94\nv_cmp_eq_u32 s[52:53], -1, v40\ns_mov_b64 exec, s[46:47]\n" \
"buffer_load_dwordx4 v[80:83], v3, %[rs], s42 offen offset:0 sc1\ns_mov_b64 exec, s[48:49]\nbuffer_load_dwordx4 v[84:87], v3, %[rs], s42 offen offset:1024 sc1\ns_mov_b64 exec, s[50:51]\nbuffer_load_dwordx4 v[88:91], v3, %[rs], s42 offen offset:2048 sc1\ns_mov_b64 exec, s[52:53]\nbuffer_load_dwordx4 v[92:95], v3, %[rs], s42 offen offset:3072 sc1\ns_mov_b64 exec, -1\n" \
"buffer_load_dword v16, v12, %[rs], s56 offen sc1\nv_mov_b64 v[48:49], 0\nv_mov_b64 v[50:51], 0\nv_mov_b64 v[52:53], 0\nv_mov_b64 v[54:55], 0\nv_mov_b64 v[56:57], 0\nv_mov_b64 v[58:59], 0\nv_mov_b64 v[60:61], 0\n" \
"v_mov_b64 v[62:63], 0\ns_add_u32 s43, s43, 1\ns_cmp_lt_u32 s43, 262144\ns_cbranch_scc1 .Lqk_poll3%=\ns_branch .Lqk_abort%=\n.Lqk_abort%=:\ns_mov_b64 exec, -1\nv_mov_b32 v16, 0\n" \
"s_movk_i32 s44, 6400\nbuffer_store_dword v16, off, %[rs], s44 sc1\n.Lqk_end%=:\ns_waitcnt vmcnt(0) lgkmcnt(0)\n" \
""
#define VU_ASM \
"v_mbcnt_lo_u32_b32 v255, -1, 0\nv_mbcnt_hi_u32_b32 v255, -1, v255\nv_lshlrev_b32 v255, 2, v255\nv_add_u32 v255, %[tb], v255\nds_read_b32 v0, v255 offset:0\nds_read_b32 v1, v255 offset:256\nds_read_b32 v2, v255 offset:512\nds_read_b32 v3, v255 offset:768\n" \
"ds_read_b32 v4, v255 offset:1024\nds_read_b32 v5, v255 offset:1280\nds_read_b32 v6, v255 offset:1536\nds_read_b32 v7, v255 offset:1792\nds_read_b32 v8, v255 offset:2048\nds_read_b32 v9, v255 offset:2304\nds_read_b32 v10, v255 offset:2560\nds_read_b32 v11, v255 offset:2816\n" \
"ds_read_b32 v90, v255 offset:3072\nds_read_b32 v91, v255 offset:3328\nds_read_b32 v92, v255 offset:3584\nds_read_b32 v93, v255 offset:3840\nds_read_b32 v94, v255 offset:4096\nds_read_b32 v95, v255 offset:4352\ns_waitcnt lgkmcnt(0)\ns_add_u32 m0, %[lw], 16384\n" \
"s_add_u32 s44, %[wo], 0\nbuffer_load_dwordx4 v90, %[rw], s44 offen nt lds\ns_add_u32 m0, %[lw], 17408\ns_add_u32 s44, %[wo], 65536\nbuffer_load_dwordx4 v90, %[rw], s44 offen nt lds\ns_add_u32 m0, %[lw], 18432\ns_add_u32 s44, %[wo], 131072\nbuffer_load_dwordx4 v90, %[rw], s44 offen nt lds\n" \
"s_add_u32 m0, %[lw], 19456\ns_add_u32 s44, %[wo], 196608\nbuffer_load_dwordx4 v90, %[rw], s44 offen nt lds\ns_add_u32 m0, %[lw], 20480\ns_add_u32 s44, %[wo], 524288\nbuffer_load_dwordx4 v90, %[rw], s44 offen nt lds\ns_add_u32 m0, %[lw], 21504\ns_add_u32 s44, %[wo], 589824\n" \
"buffer_load_dwordx4 v90, %[rw], s44 offen nt lds\ns_add_u32 m0, %[lw], 22528\ns_add_u32 s44, %[wo], 655360\nbuffer_load_dwordx4 v90, %[rw], s44 offen nt lds\ns_add_u32 m0, %[lw], 23552\ns_add_u32 s44, %[wo], 720896\nbuffer_load_dwordx4 v90, %[rw], s44 offen nt lds\ns_add_u32 m0, %[tb], 0\n" \
"s_add_u32 s44, %[wo], 1048576\nbuffer_load_dwordx4 v90, %[rw], s44 offen nt lds\ns_add_u32 m0, %[tb], 1024\ns_add_u32 s44, %[wo], 1114112\nbuffer_load_dwordx4 v90, %[rw], s44 offen nt lds\ns_add_u32 m0, %[tb], 2048\ns_add_u32 s44, %[wo], 1179648\nbuffer_load_dwordx4 v90, %[rw], s44 offen nt lds\n" \
"s_add_u32 m0, %[tb], 3072\ns_add_u32 s44, %[wo], 1245184\nbuffer_load_dwordx4 v90, %[rw], s44 offen nt lds\ns_add_u32 m0, %[tb], 4096\ns_add_u32 s44, %[wo], 1572864\nbuffer_load_dwordx4 v90, %[rw], s44 offen nt lds\ns_add_u32 m0, %[tb], 5120\ns_add_u32 s44, %[wo], 1638400\n" \
"buffer_load_dwordx4 v90, %[rw], s44 offen nt lds\ns_add_u32 m0, %[tb], 6144\ns_add_u32 s44, %[wo], 1703936\nbuffer_load_dwordx4 v90, %[rw], s44 offen nt lds\ns_add_u32 m0, %[tb], 7168\ns_add_u32 s44, %[wo], 1769472\nbuffer_load_dwordx4 v90, %[rw], s44 offen nt lds\ns_add_u32 m0, %[tb], 8192\n" \
"s_add_u32 s44, %[wo], 2097152\nbuffer_load_dwordx4 v90, %[rw], s44 offen nt lds\ns_add_u32 m0, %[tb], 9216\ns_add_u32 s44, %[wo], 2162688\nbuffer_load_dwordx4 v90, %[rw], s44 offen nt lds\ns_add_u32 m0, %[tb], 10240\ns_add_u32 s44, %[wo], 2228224\nbuffer_load_dwordx4 v90, %[rw], s44 offen nt lds\n" \
"s_add_u32 m0, %[tb], 11264\ns_add_u32 s44, %[wo], 2293760\nbuffer_load_dwordx4 v90, %[rw], s44 offen nt lds\ns_add_u32 m0, %[lw], 0\ns_add_u32 s44, %[wo], 2621440\nbuffer_load_dwordx4 v90, %[rw], s44 offen nt lds\ns_add_u32 m0, %[lw], 1024\ns_add_u32 s44, %[wo], 2686976\n" \
"buffer_load_dwordx4 v90, %[rw], s44 offen nt lds\ns_add_u32 m0, %[lw], 2048\ns_add_u32 s44, %[wo], 2752512\nbuffer_load_dwordx4 v90, %[rw], s44 offen nt lds\ns_add_u32 m0, %[lw], 3072\ns_add_u32 s44, %[wo], 2818048\nbuffer_load_dwordx4 v90, %[rw], s44 offen nt lds\ns_add_u32 m0, %[lw], 4096\n" \
"s_add_u32 s44, %[wo], 3145728\nbuffer_load_dwordx4 v90, %[rw], s44 offen nt lds\ns_add_u32 m0, %[lw], 5120\ns_add_u32 s44, %[wo], 3211264\nbuffer_load_dwordx4 v90, %[rw], s44 offen nt lds\ns_add_u32 m0, %[lw], 6144\ns_add_u32 s44, %[wo], 3276800\nbuffer_load_dwordx4 v90, %[rw], s44 offen nt lds\n" \
"s_add_u32 m0, %[lw], 7168\ns_add_u32 s44, %[wo], 3342336\nbuffer_load_dwordx4 v90, %[rw], s44 offen nt lds\ns_add_u32 m0, %[lw], 8192\ns_add_u32 s44, %[wo], 3670016\nbuffer_load_dwordx4 v90, %[rw], s44 offen nt lds\ns_add_u32 m0, %[lw], 9216\ns_add_u32 s44, %[wo], 3735552\n" \
"buffer_load_dwordx4 v90, %[rw], s44 offen nt lds\ns_add_u32 m0, %[lw], 10240\ns_add_u32 s44, %[wo], 3801088\nbuffer_load_dwordx4 v90, %[rw], s44 offen nt lds\ns_add_u32 m0, %[lw], 11264\ns_add_u32 s44, %[wo], 3866624\nbuffer_load_dwordx4 v90, %[rw], s44 offen nt lds\ns_waitcnt vmcnt(28)\n" \
"s_barrier\ns_add_u32 m0, %[lw], 12288\ns_add_u32 s44, %[wo], 4194304\nbuffer_load_dwordx4 v90, %[rw], s44 offen nt lds\ns_add_u32 m0, %[lw], 13312\ns_add_u32 s44, %[wo], 4259840\nbuffer_load_dwordx4 v90, %[rw], s44 offen nt lds\ns_add_u32 m0, %[lw], 14336\n" \
"s_add_u32 s44, %[wo], 4325376\nbuffer_load_dwordx4 v90, %[rw], s44 offen nt lds\ns_add_u32 m0, %[lw], 15360\ns_add_u32 s44, %[wo], 4390912\nbuffer_load_dwordx4 v90, %[rw], s44 offen nt lds\nds_read_b64_tr_b16 a[0:1], v93 offset:16384\nds_read_b64_tr_b16 a[2:3], v93 offset:16896\nds_read_b64_tr_b16 a[16:17], v93 offset:16392\n" \
"ds_read_b64_tr_b16 a[18:19], v93 offset:16904\nds_read_b64_tr_b16 a[4:5], v93 offset:40960\nds_read_b64_tr_b16 a[6:7], v93 offset:41472\nds_read_b64_tr_b16 a[20:21], v93 offset:40968\nds_read_b64_tr_b16 a[22:23], v93 offset:41480\ns_waitcnt lgkmcnt(0)\ns_waitcnt vmcnt(28)\ns_barrier\n" \
"s_add_u32 m0, %[lw], 16384\ns_add_u32 s44, %[wo], 4718592\nbuffer_load_dwordx4 v90, %[rw], s44 offen nt lds\ns_add_u32 m0, %[lw], 17408\ns_add_u32 s44, %[wo], 4784128\nbuffer_load_dwordx4 v90, %[rw], s44 offen nt lds\ns_add_u32 m0, %[lw], 18432\ns_add_u32 s44, %[wo], 4849664\n" \
"buffer_load_dwordx4 v90, %[rw], s44 offen nt lds\ns_add_u32 m0, %[lw], 19456\ns_add_u32 s44, %[wo], 4915200\nbuffer_load_dwordx4 v90, %[rw], s44 offen nt lds\nds_read_b64_tr_b16 a[32:33], v93 offset:20480\nds_read_b64_tr_b16 a[34:35], v93 offset:20992\nds_read_b64_tr_b16 a[40:41], v93 offset:20488\nds_read_b64_tr_b16 a[42:43], v93 offset:21000\n" \
"ds_read_b64_tr_b16 a[36:37], v93 offset:45056\nds_read_b64_tr_b16 a[38:39], v93 offset:45568\nds_read_b64_tr_b16 a[44:45], v93 offset:45064\nds_read_b64_tr_b16 a[46:47], v93 offset:45576\ns_waitcnt lgkmcnt(0)\ns_waitcnt vmcnt(28)\ns_barrier\ns_add_u32 m0, %[lw], 20480\n" \
"s_add_u32 s44, %[wo], 5242880\nbuffer_load_dwordx4 v90, %[rw], s44 offen nt lds\ns_add_u32 m0, %[lw], 21504\ns_add_u32 s44, %[wo], 5308416\nbuffer_load_dwordx4 v90, %[rw], s44 offen nt lds\ns_add_u32 m0, %[lw], 22528\ns_add_u32 s44, %[wo], 5373952\nbuffer_load_dwordx4 v90, %[rw], s44 offen nt lds\n" \
"s_add_u32 m0, %[lw], 23552\ns_add_u32 s44, %[wo], 5439488\nbuffer_load_dwordx4 v90, %[rw], s44 offen nt lds\nds_read_b64_tr_b16 a[56:57], v92 offset:0\nds_read_b64_tr_b16 a[58:59], v92 offset:512\nds_read_b64_tr_b16 v[84:85], v92 offset:8\nds_read_b64_tr_b16 v[86:87], v92 offset:520\nds_read_b64_tr_b16 a[60:61], v92 offset:12288\n" \
"ds_read_b64_tr_b16 a[62:63], v92 offset:12800\nds_read_b64_tr_b16 v[80:81], v92 offset:12296\nds_read_b64_tr_b16 v[82:83], v92 offset:12808\ns_waitcnt lgkmcnt(0)\ns_waitcnt vmcnt(28)\ns_barrier\ns_add_u32 m0, %[tb], 0\ns_add_u32 s44, %[wo], 5767168\n" \
"buffer_load_dwordx4 v90, %[rw], s44 offen nt lds\ns_add_u32 m0, %[tb], 1024\ns_add_u32 s44, %[wo], 5832704\nbuffer_load_dwordx4 v90, %[rw], s44 offen nt lds\ns_add_u32 m0, %[tb], 2048\ns_add_u32 s44, %[wo], 5898240\nbuffer_load_dwordx4 v90, %[rw], s44 offen nt lds\ns_add_u32 m0, %[tb], 3072\n" \
"s_add_u32 s44, %[wo], 5963776\nbuffer_load_dwordx4 v90, %[rw], s44 offen nt lds\nds_read_b64_tr_b16 a[80:81], v92 offset:4096\nds_read_b64_tr_b16 a[82:83], v92 offset:4608\nds_read_b64_tr_b16 a[88:89], v92 offset:4104\nds_read_b64_tr_b16 a[90:91], v92 offset:4616\nds_read_b64_tr_b16 a[84:85], v92 offset:16384\nds_read_b64_tr_b16 a[86:87], v92 offset:16896\n" \
"ds_read_b64_tr_b16 a[92:93], v92 offset:16392\nds_read_b64_tr_b16 a[94:95], v92 offset:16904\ns_waitcnt lgkmcnt(0)\ns_waitcnt vmcnt(28)\ns_barrier\ns_add_u32 m0, %[tb], 4096\ns_add_u32 s44, %[wo], 6291456\nbuffer_load_dwordx4 v90, %[rw], s44 offen nt lds\n" \
"s_add_u32 m0, %[tb], 5120\ns_add_u32 s44, %[wo], 6356992\nbuffer_load_dwordx4 v90, %[rw], s44 offen nt lds\ns_add_u32 m0, %[tb], 6144\ns_add_u32 s44, %[wo], 6422528\nbuffer_load_dwordx4 v90, %[rw], s44 offen nt lds\ns_add_u32 m0, %[tb], 7168\ns_add_u32 s44, %[wo], 6488064\n" \
"buffer_load_dwordx4 v90, %[rw], s44 offen nt lds\nds_read_b64_tr_b16 a[104:105], v92 offset:8192\nds_read_b64_tr_b16 a[106:107], v92 offset:8704\nds_read_b64_tr_b16 a[120:121], v92 offset:8200\nds_read_b64_tr_b16 a[122:123], v92 offset:8712\nds_read_b64_tr_b16 a[108:109], v92 offset:20480\nds_read_b64_tr_b16 a[110:111], v92 offset:20992\nds_read_b64_tr_b16 a[124:125], v92 offset:20488\n" \
"ds_read_b64_tr_b16 a[126:127], v92 offset:21000\ns_waitcnt lgkmcnt(0)\ns_waitcnt vmcnt(28)\ns_barrier\ns_add_u32 m0, %[tb], 8192\ns_add_u32 s44, %[wo], 6815744\nbuffer_load_dwordx4 v90, %[rw], s44 offen nt lds\ns_add_u32 m0, %[tb], 9216\n" \
"s_add_u32 s44, %[wo], 6881280\nbuffer_load_dwordx4 v90, %[rw], s44 offen nt lds\ns_add_u32 m0, %[tb], 10240\ns_add_u32 s44, %[wo], 6946816\nbuffer_load_dwordx4 v90, %[rw], s44 offen nt lds\ns_add_u32 m0, %[tb], 11264\ns_add_u32 s44, %[wo], 7012352\nbuffer_load_dwordx4 v90, %[rw], s44 offen nt lds\n" \
"ds_read_b64_tr_b16 a[136:137], v93 offset:0\nds_read_b64_tr_b16 a[138:139], v93 offset:512\nds_read_b64_tr_b16 a[144:145], v93 offset:8\nds_read_b64_tr_b16 a[146:147], v93 offset:520\nds_read_b64_tr_b16 a[140:141], v93 offset:24576\nds_read_b64_tr_b16 a[142:143], v93 offset:25088\nds_read_b64_tr_b16 a[148:149], v93 offset:24584\nds_read_b64_tr_b16 a[150:151], v93 offset:25096\n" \
"s_waitcnt lgkmcnt(0)\ns_waitcnt vmcnt(28)\ns_barrier\ns_add_u32 m0, %[lw], 0\ns_add_u32 s44, %[wo], 7340032\nbuffer_load_dwordx4 v90, %[rw], s44 offen nt lds\ns_add_u32 m0, %[lw], 1024\ns_add_u32 s44, %[wo], 7405568\n" \
"buffer_load_dwordx4 v90, %[rw], s44 offen nt lds\ns_add_u32 m0, %[lw], 2048\ns_add_u32 s44, %[wo], 7471104\nbuffer_load_dwordx4 v90, %[rw], s44 offen nt lds\ns_add_u32 m0, %[lw], 3072\ns_add_u32 s44, %[wo], 7536640\nbuffer_load_dwordx4 v90, %[rw], s44 offen nt lds\nds_read_b64_tr_b16 a[160:161], v93 offset:4096\n" \
"ds_read_b64_tr_b16 a[162:163], v93 offset:4608\nds_read_b64_tr_b16 v[76:77], v93 offset:4104\nds_read_b64_tr_b16 v[78:79], v93 offset:4616\nds_read_b64_tr_b16 a[164:165], v93 offset:28672\nds_read_b64_tr_b16 a[166:167], v93 offset:29184\nds_read_b64_tr_b16 v[72:73], v93 offset:28680\nds_read_b64_tr_b16 v[74:75], v93 offset:29192\ns_waitcnt lgkmcnt(0)\n" \
"s_waitcnt vmcnt(28)\ns_barrier\ns_add_u32 m0, %[lw], 4096\ns_add_u32 s44, %[wo], 7864320\nbuffer_load_dwordx4 v90, %[rw], s44 offen nt lds\ns_add_u32 m0, %[lw], 5120\ns_add_u32 s44, %[wo], 7929856\nbuffer_load_dwordx4 v90, %[rw], s44 offen nt lds\n" \
"s_add_u32 m0, %[lw], 6144\ns_add_u32 s44, %[wo], 7995392\nbuffer_load_dwordx4 v90, %[rw], s44 offen nt lds\ns_add_u32 m0, %[lw], 7168\ns_add_u32 s44, %[wo], 8060928\nbuffer_load_dwordx4 v90, %[rw], s44 offen nt lds\nds_read_b64_tr_b16 a[184:185], v93 offset:8192\nds_read_b64_tr_b16 a[186:187], v93 offset:8704\n" \
"ds_read_b64_tr_b16 a[192:193], v93 offset:8200\nds_read_b64_tr_b16 a[194:195], v93 offset:8712\nds_read_b64_tr_b16 a[188:189], v93 offset:32768\nds_read_b64_tr_b16 a[190:191], v93 offset:33280\nds_read_b64_tr_b16 a[196:197], v93 offset:32776\nds_read_b64_tr_b16 a[198:199], v93 offset:33288\ns_waitcnt lgkmcnt(0)\ns_waitcnt vmcnt(28)\n" \
"s_barrier\ns_add_u32 m0, %[lw], 8192\ns_add_u32 s44, %[w2], 0\nbuffer_load_dwordx4 v91, %[ro], s44 offen nt lds\ns_add_u32 m0, %[lw], 9216\ns_add_u32 s44, %[w2], 16384\nbuffer_load_dwordx4 v91, %[ro], s44 offen nt lds\ns_add_u32 m0, %[lw], 10240\n" \
"s_add_u32 s44, %[w2], 32768\nbuffer_load_dwordx4 v91, %[ro], s44 offen nt lds\ns_add_u32 m0, %[lw], 11264\ns_add_u32 s44, %[w2], 49152\nbuffer_load_dwordx4 v91, %[ro], s44 offen nt lds\nds_read_b64_tr_b16 a[208:209], v93 offset:12288\nds_read_b64_tr_b16 a[210:211], v93 offset:12800\nds_read_b64_tr_b16 a[224:225], v93 offset:12296\n" \
"ds_read_b64_tr_b16 a[226:227], v93 offset:12808\nds_read_b64_tr_b16 a[212:213], v93 offset:36864\nds_read_b64_tr_b16 a[214:215], v93 offset:37376\nds_read_b64_tr_b16 a[228:229], v93 offset:36872\nds_read_b64_tr_b16 a[230:231], v93 offset:37384\ns_waitcnt lgkmcnt(0)\ns_waitcnt vmcnt(28)\ns_barrier\n" \
"s_add_u32 m0, %[lw], 12288\ns_add_u32 s44, %[w2], 128\nbuffer_load_dwordx4 v91, %[ro], s44 offen nt lds\ns_add_u32 m0, %[lw], 13312\ns_add_u32 s44, %[w2], 16512\nbuffer_load_dwordx4 v91, %[ro], s44 offen nt lds\ns_add_u32 m0, %[lw], 14336\ns_add_u32 s44, %[w2], 32896\n" \
"buffer_load_dwordx4 v91, %[ro], s44 offen nt lds\ns_add_u32 m0, %[lw], 15360\ns_add_u32 s44, %[w2], 49280\nbuffer_load_dwordx4 v91, %[ro], s44 offen nt lds\nds_read_b64_tr_b16 a[240:241], v93 offset:16384\nds_read_b64_tr_b16 a[242:243], v93 offset:16896\nds_read_b64_tr_b16 a[248:249], v93 offset:16392\nds_read_b64_tr_b16 a[250:251], v93 offset:16904\n" \
"ds_read_b64_tr_b16 a[244:245], v93 offset:40960\nds_read_b64_tr_b16 a[246:247], v93 offset:41472\nds_read_b64_tr_b16 a[252:253], v93 offset:40968\nds_read_b64_tr_b16 a[254:255], v93 offset:41480\ns_waitcnt lgkmcnt(0)\ns_waitcnt vmcnt(28)\ns_barrier\ns_add_u32 m0, %[lw], 16384\n" \
"s_add_u32 s44, %[w2], 256\nbuffer_load_dwordx4 v91, %[ro], s44 offen nt lds\ns_add_u32 m0, %[lw], 17408\ns_add_u32 s44, %[w2], 16640\nbuffer_load_dwordx4 v91, %[ro], s44 offen nt lds\ns_add_u32 m0, %[lw], 18432\ns_add_u32 s44, %[w2], 33024\nbuffer_load_dwordx4 v91, %[ro], s44 offen nt lds\n" \
"s_add_u32 m0, %[lw], 19456\ns_add_u32 s44, %[w2], 49408\nbuffer_load_dwordx4 v91, %[ro], s44 offen nt lds\nds_read_b64_tr_b16 v[104:105], v93 offset:20480\nds_read_b64_tr_b16 v[106:107], v93 offset:20992\nds_read_b64_tr_b16 v[68:69], v93 offset:20488\nds_read_b64_tr_b16 v[70:71], v93 offset:21000\nds_read_b64_tr_b16 v[108:109], v93 offset:45056\n" \
"ds_read_b64_tr_b16 v[110:111], v93 offset:45568\nds_read_b64_tr_b16 v[64:65], v93 offset:45064\nds_read_b64_tr_b16 v[66:67], v93 offset:45576\ns_waitcnt lgkmcnt(0)\ns_waitcnt vmcnt(28)\ns_barrier\ns_add_u32 m0, %[lw], 20480\ns_add_u32 s44, %[w2], 384\n" \
"buffer_load_dwordx4 v91, %[ro], s44 offen nt lds\ns_add_u32 m0, %[lw], 21504\ns_add_u32 s44, %[w2], 16768\nbuffer_load_dwordx4 v91, %[ro], s44 offen nt lds\ns_add_u32 m0, %[lw], 22528\ns_add_u32 s44, %[w2], 33152\nbuffer_load_dwordx4 v91, %[ro], s44 offen nt lds\ns_add_u32 m0, %[lw], 23552\n" \
"s_add_u32 s44, %[w2], 49536\nbuffer_load_dwordx4 v91, %[ro], s44 offen nt lds\nds_read_b64_tr_b16 v[128:129], v92 offset:0\nds_read_b64_tr_b16 v[130:131], v92 offset:512\nds_read_b64_tr_b16 v[136:137], v92 offset:8\nds_read_b64_tr_b16 v[138:139], v92 offset:520\nds_read_b64_tr_b16 v[132:133], v92 offset:12288\nds_read_b64_tr_b16 v[134:135], v92 offset:12800\n" \
"ds_read_b64_tr_b16 v[140:141], v92 offset:12296\nds_read_b64_tr_b16 v[142:143], v92 offset:12808\ns_waitcnt lgkmcnt(0)\ns_waitcnt vmcnt(28)\ns_barrier\ns_add_u32 m0, %[tb], 0\ns_add_u32 s44, %[w2], 65536\nbuffer_load_dwordx4 v91, %[ro], s44 offen nt lds\n" \
"s_add_u32 m0, %[tb], 1024\ns_add_u32 s44, %[w2], 81920\nbuffer_load_dwordx4 v91, %[ro], s44 offen nt lds\ns_add_u32 m0, %[tb], 2048\ns_add_u32 s44, %[w2], 98304\nbuffer_load_dwordx4 v91, %[ro], s44 offen nt lds\ns_add_u32 m0, %[tb], 3072\ns_add_u32 s44, %[w2], 114688\n" \
"buffer_load_dwordx4 v91, %[ro], s44 offen nt lds\nds_read_b64_tr_b16 v[152:153], v92 offset:4096\nds_read_b64_tr_b16 v[154:155], v92 offset:4608\nds_read_b64_tr_b16 v[168:169], v92 offset:4104\nds_read_b64_tr_b16 v[170:171], v92 offset:4616\nds_read_b64_tr_b16 v[156:157], v92 offset:16384\nds_read_b64_tr_b16 v[158:159], v92 offset:16896\nds_read_b64_tr_b16 v[172:173], v92 offset:16392\n" \
"ds_read_b64_tr_b16 v[174:175], v92 offset:16904\ns_waitcnt lgkmcnt(0)\ns_waitcnt vmcnt(28)\ns_barrier\ns_add_u32 m0, %[tb], 4096\ns_add_u32 s44, %[w2], 65664\nbuffer_load_dwordx4 v91, %[ro], s44 offen nt lds\ns_add_u32 m0, %[tb], 5120\n" \
"s_add_u32 s44, %[w2], 82048\nbuffer_load_dwordx4 v91, %[ro], s44 offen nt lds\ns_add_u32 m0, %[tb], 6144\ns_add_u32 s44, %[w2], 98432\nbuffer_load_dwordx4 v91, %[ro], s44 offen nt lds\ns_add_u32 m0, %[tb], 7168\ns_add_u32 s44, %[w2], 114816\nbuffer_load_dwordx4 v91, %[ro], s44 offen nt lds\n" \
"ds_read_b64_tr_b16 v[184:185], v92 offset:8192\nds_read_b64_tr_b16 v[186:187], v92 offset:8704\nds_read_b64_tr_b16 v[192:193], v92 offset:8200\nds_read_b64_tr_b16 v[194:195], v92 offset:8712\nds_read_b64_tr_b16 v[188:189], v92 offset:20480\nds_read_b64_tr_b16 v[190:191], v92 offset:20992\nds_read_b64_tr_b16 v[196:197], v92 offset:20488\nds_read_b64_tr_b16 v[198:199], v92 offset:21000\n" \
"s_waitcnt lgkmcnt(0)\ns_waitcnt vmcnt(28)\ns_barrier\ns_add_u32 m0, %[tb], 8192\ns_add_u32 s44, %[w2], 65792\nbuffer_load_dwordx4 v91, %[ro], s44 offen nt lds\ns_add_u32 m0, %[tb], 9216\ns_add_u32 s44, %[w2], 82176\n" \
"buffer_load_dwordx4 v91, %[ro], s44 offen nt lds\ns_add_u32 m0, %[tb], 10240\ns_add_u32 s44, %[w2], 98560\nbuffer_load_dwordx4 v91, %[ro], s44 offen nt lds\ns_add_u32 m0, %[tb], 11264\ns_add_u32 s44, %[w2], 114944\nbuffer_load_dwordx4 v91, %[ro], s44 offen nt lds\nds_read_b64_tr_b16 v[208:209], v93 offset:0\n" \
"ds_read_b64_tr_b16 v[210:211], v93 offset:512\nds_read_b64_tr_b16 v[60:61], v93 offset:8\nds_read_b64_tr_b16 v[62:63], v93 offset:520\nds_read_b64_tr_b16 v[212:213], v93 offset:24576\nds_read_b64_tr_b16 v[214:215], v93 offset:25088\nds_read_b64_tr_b16 v[56:57], v93 offset:24584\nds_read_b64_tr_b16 v[58:59], v93 offset:25096\ns_waitcnt lgkmcnt(0)\n" \
"s_waitcnt vmcnt(28)\ns_barrier\ns_add_u32 m0, %[lw], 0\ns_add_u32 s44, %[w2], 65920\nbuffer_load_dwordx4 v91, %[ro], s44 offen nt lds\ns_add_u32 m0, %[lw], 1024\ns_add_u32 s44, %[w2], 82304\nbuffer_load_dwordx4 v91, %[ro], s44 offen nt lds\n" \
"s_add_u32 m0, %[lw], 2048\ns_add_u32 s44, %[w2], 98688\nbuffer_load_dwordx4 v91, %[ro], s44 offen nt lds\ns_add_u32 m0, %[lw], 3072\ns_add_u32 s44, %[w2], 115072\nbuffer_load_dwordx4 v91, %[ro], s44 offen nt lds\nds_read_b64_tr_b16 v[232:233], v93 offset:4096\nds_read_b64_tr_b16 v[234:235], v93 offset:4608\n" \
"ds_read_b64_tr_b16 v[240:241], v93 offset:4104\nds_read_b64_tr_b16 v[242:243], v93 offset:4616\nds_read_b64_tr_b16 v[236:237], v93 offset:28672\nds_read_b64_tr_b16 v[238:239], v93 offset:29184\nds_read_b64_tr_b16 v[244:245], v93 offset:28680\nds_read_b64_tr_b16 v[246:247], v93 offset:29192\ns_waitcnt lgkmcnt(0)\ns_waitcnt vmcnt(28)\n" \
"s_barrier\ns_add_u32 m0, %[lw], 4096\ns_add_u32 s44, %[w2], 196608\nbuffer_load_dwordx4 v91, %[ro], s44 offen nt lds\ns_add_u32 m0, %[lw], 5120\ns_add_u32 s44, %[w2], 212992\nbuffer_load_dwordx4 v91, %[ro], s44 offen nt lds\ns_add_u32 m0, %[lw], 6144\n" \
"s_add_u32 s44, %[w2], 229376\nbuffer_load_dwordx4 v91, %[ro], s44 offen nt lds\ns_add_u32 m0, %[lw], 7168\ns_add_u32 s44, %[w2], 245760\nbuffer_load_dwordx4 v91, %[ro], s44 offen nt lds\nds_read_b64_tr_b16 a[8:9], v95 offset:8192\nds_read_b64_tr_b16 a[10:11], v95 offset:8320\nds_read_b64_tr_b16 a[12:13], v95 offset:10240\n" \
"ds_read_b64_tr_b16 a[14:15], v95 offset:10368\nds_read_b64_tr_b16 a[216:217], v95 offset:8256\nds_read_b64_tr_b16 a[218:219], v95 offset:8384\nds_read_b64_tr_b16 a[220:221], v95 offset:10304\nds_read_b64_tr_b16 a[222:223], v95 offset:10432\ns_waitcnt lgkmcnt(0)\ns_waitcnt vmcnt(28)\ns_barrier\n" \
"s_add_u32 m0, %[lw], 8192\ns_add_u32 s44, %[w2], 196736\nbuffer_load_dwordx4 v91, %[ro], s44 offen nt lds\ns_add_u32 m0, %[lw], 9216\ns_add_u32 s44, %[w2], 213120\nbuffer_load_dwordx4 v91, %[ro], s44 offen nt lds\ns_add_u32 m0, %[lw], 10240\ns_add_u32 s44, %[w2], 229504\n" \
"buffer_load_dwordx4 v91, %[ro], s44 offen nt lds\ns_add_u32 m0, %[lw], 11264\ns_add_u32 s44, %[w2], 245888\nbuffer_load_dwordx4 v91, %[ro], s44 offen nt lds\nds_read_b64_tr_b16 a[64:65], v95 offset:12288\nds_read_b64_tr_b16 a[66:67], v95 offset:12416\nds_read_b64_tr_b16 a[68:69], v95 offset:14336\nds_read_b64_tr_b16 a[70:71], v95 offset:14464\n" \
"ds_read_b64_tr_b16 v[112:113], v95 offset:12352\nds_read_b64_tr_b16 v[114:115], v95 offset:12480\nds_read_b64_tr_b16 v[116:117], v95 offset:14400\nds_read_b64_tr_b16 v[118:119], v95 offset:14528\ns_waitcnt lgkmcnt(0)\ns_waitcnt vmcnt(28)\ns_barrier\ns_add_u32 m0, %[lw], 12288\n" \
"s_add_u32 s44, %[w2], 196864\nbuffer_load_dwordx4 v91, %[ro], s44 offen nt lds\ns_add_u32 m0, %[lw], 13312\ns_add_u32 s44, %[w2], 213248\nbuffer_load_dwordx4 v91, %[ro], s44 offen nt lds\ns_add_u32 m0, %[lw], 14336\ns_add_u32 s44, %[w2], 229632\nbuffer_load_dwordx4 v91, %[ro], s44 offen nt lds\n" \
"s_add_u32 m0, %[lw], 15360\ns_add_u32 s44, %[w2], 246016\nbuffer_load_dwordx4 v91, %[ro], s44 offen nt lds\nds_read_b64_tr_b16 a[112:113], v95 offset:16384\nds_read_b64_tr_b16 a[114:115], v95 offset:16512\nds_read_b64_tr_b16 a[116:117], v95 offset:18432\nds_read_b64_tr_b16 a[118:119], v95 offset:18560\nds_read_b64_tr_b16 v[160:161], v95 offset:16448\n" \
"ds_read_b64_tr_b16 v[162:163], v95 offset:16576\nds_read_b64_tr_b16 v[164:165], v95 offset:18496\nds_read_b64_tr_b16 v[166:167], v95 offset:18624\ns_waitcnt lgkmcnt(0)\ns_waitcnt vmcnt(28)\ns_barrier\ns_add_u32 m0, %[lw], 16384\ns_add_u32 s44, %[w2], 196992\n" \
"buffer_load_dwordx4 v91, %[ro], s44 offen nt lds\ns_add_u32 m0, %[lw], 17408\ns_add_u32 s44, %[w2], 213376\nbuffer_load_dwordx4 v91, %[ro], s44 offen nt lds\ns_add_u32 m0, %[lw], 18432\ns_add_u32 s44, %[w2], 229760\nbuffer_load_dwordx4 v91, %[ro], s44 offen nt lds\ns_add_u32 m0, %[lw], 19456\n" \
"s_add_u32 s44, %[w2], 246144\nbuffer_load_dwordx4 v91, %[ro], s44 offen nt lds\nds_read_b64_tr_b16 a[168:169], v95 offset:20480\nds_read_b64_tr_b16 a[170:171], v95 offset:20608\nds_read_b64_tr_b16 a[172:173], v95 offset:22528\nds_read_b64_tr_b16 a[174:175], v95 offset:22656\nds_read_b64_tr_b16 v[216:217], v95 offset:20544\nds_read_b64_tr_b16 v[218:219], v95 offset:20672\n" \
"ds_read_b64_tr_b16 v[220:221], v95 offset:22592\nds_read_b64_tr_b16 v[222:223], v95 offset:22720\ns_waitcnt lgkmcnt(0)\ns_waitcnt vmcnt(28)\ns_barrier\ns_add_u32 m0, %[lw], 20480\ns_add_u32 s44, %[w2], 131072\nbuffer_load_dwordx4 v91, %[ro], s44 offen nt lds\n" \
"s_add_u32 m0, %[lw], 21504\ns_add_u32 s44, %[w2], 147456\nbuffer_load_dwordx4 v91, %[ro], s44 offen nt lds\ns_add_u32 m0, %[lw], 22528\ns_add_u32 s44, %[w2], 163840\nbuffer_load_dwordx4 v91, %[ro], s44 offen nt lds\ns_add_u32 m0, %[lw], 23552\ns_add_u32 s44, %[w2], 180224\n" \
"buffer_load_dwordx4 v91, %[ro], s44 offen nt lds\nds_read_b64_tr_b16 a[24:25], v94 offset:0\nds_read_b64_tr_b16 a[26:27], v94 offset:128\nds_read_b64_tr_b16 a[28:29], v94 offset:2048\nds_read_b64_tr_b16 a[30:31], v94 offset:2176\nds_read_b64_tr_b16 a[232:233], v94 offset:64\nds_read_b64_tr_b16 a[234:235], v94 offset:192\nds_read_b64_tr_b16 a[236:237], v94 offset:2112\n" \
"ds_read_b64_tr_b16 a[238:239], v94 offset:2240\ns_waitcnt lgkmcnt(0)\ns_waitcnt vmcnt(28)\ns_barrier\ns_add_u32 m0, %[tb], 0\ns_add_u32 s44, %[w2], 131200\nbuffer_load_dwordx4 v91, %[ro], s44 offen nt lds\ns_add_u32 m0, %[tb], 1024\n" \
"s_add_u32 s44, %[w2], 147584\nbuffer_load_dwordx4 v91, %[ro], s44 offen nt lds\ns_add_u32 m0, %[tb], 2048\ns_add_u32 s44, %[w2], 163968\nbuffer_load_dwordx4 v91, %[ro], s44 offen nt lds\ns_add_u32 m0, %[tb], 3072\ns_add_u32 s44, %[w2], 180352\nbuffer_load_dwordx4 v91, %[ro], s44 offen nt lds\n" \
"ds_read_b64_tr_b16 a[72:73], v94 offset:4096\nds_read_b64_tr_b16 a[74:75], v94 offset:4224\nds_read_b64_tr_b16 a[76:77], v94 offset:6144\nds_read_b64_tr_b16 a[78:79], v94 offset:6272\nds_read_b64_tr_b16 v[120:121], v94 offset:4160\nds_read_b64_tr_b16 v[122:123], v94 offset:4288\nds_read_b64_tr_b16 v[124:125], v94 offset:6208\nds_read_b64_tr_b16 v[126:127], v94 offset:6336\n" \
"s_waitcnt lgkmcnt(0)\ns_waitcnt vmcnt(28)\ns_barrier\ns_add_u32 m0, %[tb], 4096\ns_add_u32 s44, %[w2], 131328\nbuffer_load_dwordx4 v91, %[ro], s44 offen nt lds\ns_add_u32 m0, %[tb], 5120\ns_add_u32 s44, %[w2], 147712\n" \
"buffer_load_dwordx4 v91, %[ro], s44 offen nt lds\ns_add_u32 m0, %[tb], 6144\ns_add_u32 s44, %[w2], 164096\nbuffer_load_dwordx4 v91, %[ro], s44 offen nt lds\ns_add_u32 m0, %[tb], 7168\ns_add_u32 s44, %[w2], 180480\nbuffer_load_dwordx4 v91, %[ro], s44 offen nt lds\nds_read_b64_tr_b16 a[128:129], v94 offset:8192\n" \
"ds_read_b64_tr_b16 a[130:131], v94 offset:8320\nds_read_b64_tr_b16 a[132:133], v94 offset:10240\nds_read_b64_tr_b16 a[134:135], v94 offset:10368\nds_read_b64_tr_b16 v[176:177], v94 offset:8256\nds_read_b64_tr_b16 v[178:179], v94 offset:8384\nds_read_b64_tr_b16 v[180:181], v94 offset:10304\nds_read_b64_tr_b16 v[182:183], v94 offset:10432\ns_waitcnt lgkmcnt(0)\n" \
"s_waitcnt vmcnt(28)\ns_barrier\ns_add_u32 m0, %[tb], 8192\ns_add_u32 s44, %[w2], 131456\nbuffer_load_dwordx4 v91, %[ro], s44 offen nt lds\ns_add_u32 m0, %[tb], 9216\ns_add_u32 s44, %[w2], 147840\nbuffer_load_dwordx4 v91, %[ro], s44 offen nt lds\n" \
"s_add_u32 m0, %[tb], 10240\ns_add_u32 s44, %[w2], 164224\nbuffer_load_dwordx4 v91, %[ro], s44 offen nt lds\ns_add_u32 m0, %[tb], 11264\ns_add_u32 s44, %[w2], 180608\nbuffer_load_dwordx4 v91, %[ro], s44 offen nt lds\nds_read_b64_tr_b16 a[176:177], v95 offset:0\nds_read_b64_tr_b16 a[178:179], v95 offset:128\n" \
"ds_read_b64_tr_b16 a[180:181], v95 offset:2048\nds_read_b64_tr_b16 a[182:183], v95 offset:2176\nds_read_b64_tr_b16 v[224:225], v95 offset:64\nds_read_b64_tr_b16 v[226:227], v95 offset:192\nds_read_b64_tr_b16 v[228:229], v95 offset:2112\nds_read_b64_tr_b16 v[230:231], v95 offset:2240\ns_waitcnt lgkmcnt(0)\ns_waitcnt vmcnt(28)\n" \
"s_barrier\nds_read_b64_tr_b16 a[48:49], v95 offset:4096\nds_read_b64_tr_b16 a[50:51], v95 offset:4224\nds_read_b64_tr_b16 a[52:53], v95 offset:6144\nds_read_b64_tr_b16 a[54:55], v95 offset:6272\nds_read_b64_tr_b16 v[96:97], v95 offset:4160\nds_read_b64_tr_b16 v[98:99], v95 offset:4288\nds_read_b64_tr_b16 v[100:101], v95 offset:6208\n" \
"ds_read_b64_tr_b16 v[102:103], v95 offset:6336\ns_waitcnt lgkmcnt(0)\nds_write_b128 v1, v[84:87] offset:0\nds_write_b128 v1, v[80:83] offset:1024\nds_write_b128 v1, v[76:79] offset:2048\nds_write_b128 v1, v[72:75] offset:3072\ns_waitcnt vmcnt(24)\ns_barrier\n" \
"ds_read_b64_tr_b16 a[96:97], v95 offset:8192\nds_read_b64_tr_b16 a[98:99], v95 offset:8320\nds_read_b64_tr_b16 a[100:101], v95 offset:10240\nds_read_b64_tr_b16 a[102:103], v95 offset:10368\nds_read_b64_tr_b16 v[144:145], v95 offset:8256\nds_read_b64_tr_b16 v[146:147], v95 offset:8384\nds_read_b64_tr_b16 v[148:149], v95 offset:10304\nds_read_b64_tr_b16 v[150:151], v95 offset:10432\n" \
"s_waitcnt lgkmcnt(0)\nds_write_b128 v1, v[68:71] offset:4096\nds_write_b128 v1, v[64:67] offset:5120\nds_write_b128 v1, v[60:63] offset:6144\nds_write_b128 v1, v[56:59] offset:7168\ns_waitcnt vmcnt(20)\ns_barrier\nds_read_b64_tr_b16 a[152:153], v95 offset:12288\n" \
"ds_read_b64_tr_b16 a[154:155], v95 offset:12416\nds_read_b64_tr_b16 a[156:157], v95 offset:14336\nds_read_b64_tr_b16 a[158:159], v95 offset:14464\nds_read_b64_tr_b16 v[200:201], v95 offset:12352\nds_read_b64_tr_b16 v[202:203], v95 offset:12480\nds_read_b64_tr_b16 v[204:205], v95 offset:14400\nds_read_b64_tr_b16 v[206:207], v95 offset:14528\ns_waitcnt lgkmcnt(0)\n" \
"s_waitcnt vmcnt(16)\ns_barrier\nds_read_b64_tr_b16 a[200:201], v95 offset:16384\nds_read_b64_tr_b16 a[202:203], v95 offset:16512\nds_read_b64_tr_b16 a[204:205], v95 offset:18432\nds_read_b64_tr_b16 a[206:207], v95 offset:18560\nds_read_b64_tr_b16 v[248:249], v95 offset:16448\nds_read_b64_tr_b16 v[250:251], v95 offset:16576\n" \
"ds_read_b64_tr_b16 v[252:253], v95 offset:18496\nds_read_b64_tr_b16 v[254:255], v95 offset:18624\ns_waitcnt lgkmcnt(0)\ns_waitcnt vmcnt(12)\ns_barrier\nds_read_b64_tr_b16 v[56:57], v95 offset:20480\nds_read_b64_tr_b16 v[58:59], v95 offset:20608\nds_read_b64_tr_b16 v[60:61], v95 offset:22528\n" \
"ds_read_b64_tr_b16 v[62:63], v95 offset:22656\nds_read_b64_tr_b16 v[64:65], v95 offset:20544\nds_read_b64_tr_b16 v[66:67], v95 offset:20672\nds_read_b64_tr_b16 v[68:69], v95 offset:22592\nds_read_b64_tr_b16 v[70:71], v95 offset:22720\ns_waitcnt lgkmcnt(0)\nds_write_b128 v1, v[56:59] offset:8192\nds_write_b128 v1, v[60:63] offset:9216\n" \
"ds_write_b128 v1, v[64:67] offset:16384\nds_write_b128 v1, v[68:71] offset:17408\ns_waitcnt vmcnt(8)\ns_barrier\nds_read_b64_tr_b16 v[68:69], v94 offset:0\nds_read_b64_tr_b16 v[70:71], v94 offset:128\nds_read_b64_tr_b16 v[64:65], v94 offset:2048\nds_read_b64_tr_b16 v[66:67], v94 offset:2176\n" \
"ds_read_b64_tr_b16 v[60:61], v94 offset:64\nds_read_b64_tr_b16 v[62:63], v94 offset:192\nds_read_b64_tr_b16 v[56:57], v94 offset:2112\nds_read_b64_tr_b16 v[58:59], v94 offset:2240\ns_waitcnt lgkmcnt(0)\nds_write_b128 v1, v[68:71] offset:10240\nds_write_b128 v1, v[64:67] offset:11264\nds_write_b128 v1, v[60:63] offset:18432\n" \
"ds_write_b128 v1, v[56:59] offset:19456\ns_waitcnt vmcnt(4)\ns_barrier\nds_read_b64_tr_b16 v[56:57], v94 offset:4096\nds_read_b64_tr_b16 v[58:59], v94 offset:4224\nds_read_b64_tr_b16 v[60:61], v94 offset:6144\nds_read_b64_tr_b16 v[62:63], v94 offset:6272\nds_read_b64_tr_b16 v[64:65], v94 offset:4160\n" \
"ds_read_b64_tr_b16 v[66:67], v94 offset:4288\nds_read_b64_tr_b16 v[68:69], v94 offset:6208\nds_read_b64_tr_b16 v[70:71], v94 offset:6336\ns_waitcnt lgkmcnt(0)\nds_write_b128 v1, v[56:59] offset:12288\nds_write_b128 v1, v[60:63] offset:13312\nds_write_b128 v1, v[64:67] offset:20480\nds_write_b128 v1, v[68:71] offset:21504\n" \
"s_waitcnt vmcnt(0)\ns_barrier\nds_read_b64_tr_b16 v[68:69], v94 offset:8192\nds_read_b64_tr_b16 v[70:71], v94 offset:8320\nds_read_b64_tr_b16 v[64:65], v94 offset:10240\nds_read_b64_tr_b16 v[66:67], v94 offset:10368\nds_read_b64_tr_b16 v[60:61], v94 offset:8256\nds_read_b64_tr_b16 v[62:63], v94 offset:8384\n" \
"ds_read_b64_tr_b16 v[56:57], v94 offset:10304\nds_read_b64_tr_b16 v[58:59], v94 offset:10432\ns_waitcnt lgkmcnt(0)\nds_write_b128 v1, v[68:71] offset:14336\nds_write_b128 v1, v[64:67] offset:15360\nds_write_b128 v1, v[60:63] offset:22528\nds_write_b128 v1, v[56:59] offset:23552\ns_waitcnt lgkmcnt(0)\n" \
"s_barrier\ns_mov_b32 s40, -1\ns_mov_b32 s43, 0\ns_lshl_b32 s57, %[w], 12\ns_mov_b32 s56, 0\ns_mov_b32 s42, 0\ns_mov_b32 s60, -2147483648\ns_mov_b32 s54, -2147483648\n" \
"s_add_u32 s44, s40, 1\ns_lshl_b32 s44, s44, 17\ns_add_u32 s44, s44, %[xn]\ns_lshr_b32 s45, s44, 8\ns_and_b32 s45, s45, 0x2000\ns_xor_b32 s41, s44, s45\ns_add_u32 s41, s41, %[xw]\n.Lvu_f0%=:\n" \
"buffer_load_dword v14, v8, %[rs], s56 offen sc1\ns_waitcnt vmcnt(0)\nv_cmp_ge_i32 vcc, s40, v14\ns_cbranch_vccz .Lvu_f0fok%=\ns_add_u32 s43, s43, 1\ns_cmp_lt_u32 s43, 262144\ns_cbranch_scc1 .Lvu_f0%=\ns_branch .Lvu_abort%=\n" \
".Lvu_f0fok%=:\ns_add_u32 m0, s57, 0\ns_add_u32 s58, s41, 0\nbuffer_load_dwordx4 v0, %[rs], s58 offen offset:0 sc1 lds\nbuffer_load_dwordx4 v0, %[rs], s58 offen offset:1024 sc1 lds\nbuffer_load_dwordx4 v0, %[rs], s58 offen offset:2048 sc1 lds\nbuffer_load_dwordx4 v0, %[rs], s58 offen offset:3072 sc1 lds\ns_add_u32 m0, s57, 32768\n" \
"s_add_u32 s58, s41, 32768\nbuffer_load_dwordx4 v0, %[rs], s58 offen offset:0 sc1 lds\nbuffer_load_dwordx4 v0, %[rs], s58 offen offset:1024 sc1 lds\nbuffer_load_dwordx4 v0, %[rs], s58 offen offset:2048 sc1 lds\nbuffer_load_dwordx4 v0, %[rs], s58 offen offset:3072 sc1 lds\ns_waitcnt vmcnt(0)\ns_barrier\nds_read_b128 v[40:43], v2 offset:0\n" \
"ds_read_b128 v[44:47], v2 offset:256\n.Lvu_tok%=:\nds_read_b128 v[32:35], v2 offset:2048\nds_read_b128 v[36:39], v2 offset:2304\nds_read_b128 v[64:67], v2 offset:4096\nds_read_b128 v[68:71], v2 offset:4352\nds_read_b128 v[56:59], v1 offset:0\nds_read_b128 v[60:63], v1 offset:1024\n" \
"s_waitcnt lgkmcnt(7)\nv_mfma_f32_16x16x32_bf16 v[16:19], a[0:3], v[40:43], 0\nv_cvt_pk_bf16_f32 v76, v80, v81\nv_cvt_pk_bf16_f32 v77, v82, v83\nv_mfma_f32_16x16x32_bf16 v[20:23], a[16:19], v[40:43], 0\ns_waitcnt lgkmcnt(6)\nds_read_b128 v[40:43], v2 offset:6144\nv_cvt_pk_bf16_f32 v78, v84, v85\n" \
"v_cvt_pk_bf16_f32 v79, v86, v87\nv_mfma_f32_16x16x32_bf16 v[24:27], a[0:3], v[44:47], 0\nv_and_or_b32 v76, v76, -2, v13\nv_and_or_b32 v78, v78, -2, v13\nv_mfma_f32_16x16x32_bf16 v[28:31], a[16:19], v[44:47], 0\ns_waitcnt lgkmcnt(6)\nbuffer_store_dwordx4 v[76:79], v7, %[rs], s60 offen offset:2560 sc1\nds_read_b128 v[44:47], v2 offset:6400\n" \
"v_mfma_f32_16x16x32_bf16 v[16:19], a[4:7], v[32:35], v[16:19]\nv_mfma_f32_16x16x32_bf16 v[20:23], a[20:23], v[32:35], v[20:23]\ns_waitcnt lgkmcnt(6)\nds_read_b128 v[32:35], v2 offset:8192\nv_mfma_f32_16x16x32_bf16 v[24:27], a[4:7], v[36:39], v[24:27]\ns_add_u32 m0, s57, 16384\ns_add_u32 s60, s41, 65536\nbuffer_load_dwordx4 v0, %[rs], s60 offen offset:0 sc1 lds\n" \
"v_mfma_f32_16x16x32_bf16 v[28:31], a[20:23], v[36:39], v[28:31]\ns_waitcnt lgkmcnt(6)\nds_read_b128 v[36:39], v2 offset:8448\nv_mfma_f32_16x16x32_bf16 v[16:19], a[32:35], v[64:67], v[16:19]\nv_mfma_f32_16x16x32_bf16 v[20:23], a[40:43], v[64:67], v[20:23]\ns_waitcnt lgkmcnt(6)\nds_read_b128 v[64:67], v2 offset:10240\nv_mfma_f32_16x16x32_bf16 v[24:27], a[32:35], v[68:71], v[24:27]\n" \
"buffer_load_dwordx4 v0, %[rs], s60 offen offset:1024 sc1 lds\nv_mfma_f32_16x16x32_bf16 v[28:31], a[40:43], v[68:71], v[28:31]\ns_waitcnt lgkmcnt(4)\nds_read_b128 v[68:71], v2 offset:10496\nv_mfma_f32_16x16x32_bf16 v[16:19], a[36:39], v[40:43], v[16:19]\nv_mfma_f32_16x16x32_bf16 v[20:23], a[44:47], v[40:43], v[20:23]\ns_waitcnt lgkmcnt(4)\nds_read_b128 v[40:43], v2 offset:12288\n" \
"v_mfma_f32_16x16x32_bf16 v[24:27], a[36:39], v[44:47], v[24:27]\nbuffer_load_dwordx4 v0, %[rs], s60 offen offset:2048 sc1 lds\nv_mfma_f32_16x16x32_bf16 v[28:31], a[44:47], v[44:47], v[28:31]\ns_waitcnt lgkmcnt(4)\nds_read_b128 v[44:47], v2 offset:12544\nv_mfma_f32_16x16x32_bf16 v[16:19], a[56:59], v[32:35], v[16:19]\nv_mfma_f32_16x16x32_bf16 v[20:23], v[56:59], v[32:35], v[20:23]\ns_waitcnt lgkmcnt(4)\n" \
"ds_read_b128 v[32:35], v2 offset:14336\nv_mfma_f32_16x16x32_bf16 v[24:27], a[56:59], v[36:39], v[24:27]\nbuffer_load_dwordx4 v0, %[rs], s60 offen offset:3072 sc1 lds\nv_mfma_f32_16x16x32_bf16 v[28:31], v[56:59], v[36:39], v[28:31]\ns_waitcnt lgkmcnt(4)\nds_read_b128 v[36:39], v2 offset:14592\nv_mfma_f32_16x16x32_bf16 v[16:19], a[60:63], v[64:67], v[16:19]\nv_mfma_f32_16x16x32_bf16 v[20:23], v[60:63], v[64:67], v[20:23]\n" \
"s_waitcnt lgkmcnt(4)\nds_read_b128 v[64:67], v2 offset:32768\nv_mfma_f32_16x16x32_bf16 v[24:27], a[60:63], v[68:71], v[24:27]\nv_mfma_f32_16x16x32_bf16 v[28:31], v[60:63], v[68:71], v[28:31]\ns_waitcnt lgkmcnt(4)\nds_read_b128 v[68:71], v2 offset:33024\nv_mfma_f32_16x16x32_bf16 v[16:19], a[80:83], v[40:43], v[16:19]\nv_mfma_f32_16x16x32_bf16 v[20:23], a[88:91], v[40:43], v[20:23]\n" \
"s_waitcnt lgkmcnt(4)\nds_read_b128 v[40:43], v2 offset:34816\nv_mfma_f32_16x16x32_bf16 v[24:27], a[80:83], v[44:47], v[24:27]\nds_read_b128 v[56:59], v1 offset:2048\nds_read_b128 v[60:63], v1 offset:3072\nv_mfma_f32_16x16x32_bf16 v[28:31], a[88:91], v[44:47], v[28:31]\ns_waitcnt lgkmcnt(6)\nds_read_b128 v[44:47], v2 offset:35072\n" \
"v_mfma_f32_16x16x32_bf16 v[16:19], a[84:87], v[32:35], v[16:19]\nv_mfma_f32_16x16x32_bf16 v[20:23], a[92:95], v[32:35], v[20:23]\ns_waitcnt lgkmcnt(6)\nds_read_b128 v[32:35], v2 offset:36864\nv_mfma_f32_16x16x32_bf16 v[24:27], a[84:87], v[36:39], v[24:27]\nv_mfma_f32_16x16x32_bf16 v[28:31], a[92:95], v[36:39], v[28:31]\ns_waitcnt lgkmcnt(6)\nds_read_b128 v[36:39], v2 offset:37120\n" \
"v_mfma_f32_16x16x32_bf16 v[16:19], a[104:107], v[64:67], v[16:19]\nv_mfma_f32_16x16x32_bf16 v[20:23], a[120:123], v[64:67], v[20:23]\ns_waitcnt lgkmcnt(6)\nds_read_b128 v[64:67], v2 offset:38912\nv_mfma_f32_16x16x32_bf16 v[24:27], a[104:107], v[68:71], v[24:27]\ns_barrier\nv_mfma_f32_16x16x32_bf16 v[28:31], a[120:123], v[68:71], v[28:31]\ns_waitcnt lgkmcnt(6)\n" \
"ds_read_b128 v[68:71], v2 offset:39168\nv_mfma_f32_16x16x32_bf16 v[16:19], a[108:111], v[40:43], v[16:19]\ns_add_u32 m0, s57, 0\ns_add_u32 s60, s41, 98304\nbuffer_load_dwordx4 v0, %[rs], s60 offen offset:0 sc1 lds\nv_mfma_f32_16x16x32_bf16 v[20:23], a[124:127], v[40:43], v[20:23]\ns_waitcnt lgkmcnt(4)\nds_read_b128 v[40:43], v2 offset:40960\n" \
"v_mfma_f32_16x16x32_bf16 v[24:27], a[108:111], v[44:47], v[24:27]\nv_mfma_f32_16x16x32_bf16 v[28:31], a[124:127], v[44:47], v[28:31]\ns_waitcnt lgkmcnt(4)\nds_read_b128 v[44:47], v2 offset:41216\nv_mfma_f32_16x16x32_bf16 v[16:19], a[136:139], v[32:35], v[16:19]\nbuffer_load_dwordx4 v0, %[rs], s60 offen offset:1024 sc1 lds\nv_mfma_f32_16x16x32_bf16 v[20:23], a[144:147], v[32:35], v[20:23]\ns_waitcnt lgkmcnt(4)\n" \
"ds_read_b128 v[32:35], v2 offset:43008\nv_mfma_f32_16x16x32_bf16 v[24:27], a[136:139], v[36:39], v[24:27]\nv_mfma_f32_16x16x32_bf16 v[28:31], a[144:147], v[36:39], v[28:31]\ns_waitcnt lgkmcnt(4)\nds_read_b128 v[36:39], v2 offset:43264\nv_mfma_f32_16x16x32_bf16 v[16:19], a[140:143], v[64:67], v[16:19]\nbuffer_load_dwordx4 v0, %[rs], s60 offen offset:2048 sc1 lds\nv_mfma_f32_16x16x32_bf16 v[20:23], a[148:151], v[64:67], v[20:23]\n" \
"s_waitcnt lgkmcnt(4)\nds_read_b128 v[64:67], v2 offset:45056\nv_mfma_f32_16x16x32_bf16 v[24:27], a[140:143], v[68:71], v[24:27]\nv_mfma_f32_16x16x32_bf16 v[28:31], a[148:151], v[68:71], v[28:31]\ns_waitcnt lgkmcnt(4)\nds_read_b128 v[68:71], v2 offset:45312\nv_mfma_f32_16x16x32_bf16 v[16:19], a[160:163], v[40:43], v[16:19]\nbuffer_load_dwordx4 v0, %[rs], s60 offen offset:3072 sc1 lds\n" \
"v_mfma_f32_16x16x32_bf16 v[20:23], v[56:59], v[40:43], v[20:23]\ns_waitcnt lgkmcnt(4)\nds_read_b128 v[40:43], v2 offset:47104\nv_mfma_f32_16x16x32_bf16 v[24:27], a[160:163], v[44:47], v[24:27]\nv_mfma_f32_16x16x32_bf16 v[28:31], v[56:59], v[44:47], v[28:31]\ns_waitcnt lgkmcnt(4)\nds_read_b128 v[44:47], v2 offset:47360\nv_mfma_f32_16x16x32_bf16 v[16:19], a[164:167], v[32:35], v[16:19]\n" \
"v_mfma_f32_16x16x32_bf16 v[20:23], v[60:63], v[32:35], v[20:23]\ns_waitcnt vmcnt(4)\ns_barrier\ns_waitcnt lgkmcnt(4)\nds_read_b128 v[32:35], v2 offset:16384\nv_mfma_f32_16x16x32_bf16 v[24:27], a[164:167], v[36:39], v[24:27]\ns_waitcnt vmcnt(4)\nbuffer_store_dword v15, off, %[rs], s54 sc1\n" \
"v_mfma_f32_16x16x32_bf16 v[28:31], v[60:63], v[36:39], v[28:31]\ns_waitcnt lgkmcnt(4)\nds_read_b128 v[36:39], v2 offset:16640\nv_mfma_f32_16x16x32_bf16 v[16:19], a[184:187], v[64:67], v[16:19]\ns_add_u32 s56, s56, %[fs]\ns_add_u32 s62, s40, 1\nv_mfma_f32_16x16x32_bf16 v[20:23], a[192:195], v[64:67], v[20:23]\ns_waitcnt lgkmcnt(4)\n" \
"ds_read_b128 v[64:67], v2 offset:18432\nv_mfma_f32_16x16x32_bf16 v[24:27], a[184:187], v[68:71], v[24:27]\ns_add_u32 s44, s40, 2\ns_lshl_b32 s44, s44, 17\nds_read_b128 v[56:59], v1 offset:4096\nds_read_b128 v[60:63], v1 offset:5120\nbuffer_load_dword v14, v8, %[rs], s56 offen sc1\nv_mfma_f32_16x16x32_bf16 v[28:31], a[192:195], v[68:71], v[28:31]\n" \
"s_waitcnt lgkmcnt(6)\nds_read_b128 v[68:71], v2 offset:18688\nv_mfma_f32_16x16x32_bf16 v[16:19], a[188:191], v[40:43], v[16:19]\ns_add_u32 s44, s44, %[xn]\ns_lshr_b32 s45, s44, 8\nv_mfma_f32_16x16x32_bf16 v[20:23], a[196:199], v[40:43], v[20:23]\ns_waitcnt lgkmcnt(6)\nds_read_b128 v[40:43], v2 offset:20480\n" \
"v_mfma_f32_16x16x32_bf16 v[24:27], a[188:191], v[44:47], v[24:27]\ns_and_b32 s45, s45, 0x2000\ns_xor_b32 s41, s44, s45\nv_mfma_f32_16x16x32_bf16 v[28:31], a[196:199], v[44:47], v[28:31]\ns_waitcnt lgkmcnt(6)\nds_read_b128 v[44:47], v2 offset:20736\nv_mfma_f32_16x16x32_bf16 v[16:19], a[208:211], v[32:35], v[16:19]\ns_add_u32 s41, s41, %[xw]\n" \
"v_mfma_f32_16x16x32_bf16 v[20:23], a[224:227], v[32:35], v[20:23]\ns_waitcnt lgkmcnt(6)\nds_read_b128 v[32:35], v2 offset:22528\nv_mfma_f32_16x16x32_bf16 v[24:27], a[208:211], v[36:39], v[24:27]\nv_mfma_f32_16x16x32_bf16 v[28:31], a[224:227], v[36:39], v[28:31]\ns_waitcnt lgkmcnt(6)\nds_read_b128 v[36:39], v2 offset:22784\nv_mfma_f32_16x16x32_bf16 v[16:19], a[212:215], v[64:67], v[16:19]\n" \
"v_mfma_f32_16x16x32_bf16 v[20:23], a[228:231], v[64:67], v[20:23]\ns_waitcnt lgkmcnt(4)\nds_read_b128 v[64:67], v2 offset:24576\nv_mfma_f32_16x16x32_bf16 v[24:27], a[212:215], v[68:71], v[24:27]\nv_mfma_f32_16x16x32_bf16 v[28:31], a[228:231], v[68:71], v[28:31]\ns_waitcnt lgkmcnt(4)\nds_read_b128 v[68:71], v2 offset:24832\nv_mfma_f32_16x16x32_bf16 v[16:19], a[240:243], v[40:43], v[16:19]\n" \
"v_mfma_f32_16x16x32_bf16 v[20:23], a[248:251], v[40:43], v[20:23]\ns_waitcnt lgkmcnt(4)\nds_read_b128 v[40:43], v2 offset:26624\nv_mfma_f32_16x16x32_bf16 v[24:27], a[240:243], v[44:47], v[24:27]\nv_mfma_f32_16x16x32_bf16 v[28:31], a[248:251], v[44:47], v[28:31]\ns_waitcnt lgkmcnt(4)\nds_read_b128 v[44:47], v2 offset:26880\nv_mfma_f32_16x16x32_bf16 v[16:19], a[244:247], v[32:35], v[16:19]\n" \
"v_mfma_f32_16x16x32_bf16 v[20:23], a[252:255], v[32:35], v[20:23]\ns_waitcnt lgkmcnt(4)\nds_read_b128 v[32:35], v2 offset:28672\nv_mfma_f32_16x16x32_bf16 v[24:27], a[244:247], v[36:39], v[24:27]\nv_mfma_f32_16x16x32_bf16 v[28:31], a[252:255], v[36:39], v[28:31]\ns_waitcnt lgkmcnt(4)\nds_read_b128 v[36:39], v2 offset:28928\nv_mfma_f32_16x16x32_bf16 v[16:19], v[104:107], v[64:67], v[16:19]\n" \
"v_mfma_f32_16x16x32_bf16 v[20:23], v[56:59], v[64:67], v[20:23]\ns_waitcnt lgkmcnt(4)\nds_read_b128 v[64:67], v2 offset:30720\nv_mfma_f32_16x16x32_bf16 v[24:27], v[104:107], v[68:71], v[24:27]\nv_mfma_f32_16x16x32_bf16 v[28:31], v[56:59], v[68:71], v[28:31]\ns_waitcnt lgkmcnt(4)\nds_read_b128 v[68:71], v2 offset:30976\nv_mfma_f32_16x16x32_bf16 v[16:19], v[108:111], v[40:43], v[16:19]\n" \
"v_mfma_f32_16x16x32_bf16 v[20:23], v[60:63], v[40:43], v[20:23]\ns_waitcnt vmcnt(2)\ns_barrier\ns_waitcnt lgkmcnt(4)\nds_read_b128 v[40:43], v2 offset:0\nv_mfma_f32_16x16x32_bf16 v[24:27], v[108:111], v[44:47], v[24:27]\ns_waitcnt vmcnt(0)\nv_cmp_ge_i32 s[64:65], s62, v14\n" \
"v_mfma_f32_16x16x32_bf16 v[28:31], v[60:63], v[44:47], v[28:31]\ns_cmp_eq_u64 s[64:65], 0\ns_cselect_b32 s61, s41, -2147483648\ns_waitcnt lgkmcnt(4)\nds_read_b128 v[44:47], v2 offset:256\nv_mfma_f32_16x16x32_bf16 v[16:19], v[128:131], v[32:35], v[16:19]\ns_add_u32 m0, s57, 32768\ns_add_u32 s60, s61, 32768\n" \
"buffer_load_dwordx4 v0, %[rs], s60 offen offset:0 sc1 lds\nv_mfma_f32_16x16x32_bf16 v[20:23], v[136:139], v[32:35], v[20:23]\ns_waitcnt lgkmcnt(4)\nds_read_b128 v[32:35], v2 offset:2048\nv_mfma_f32_16x16x32_bf16 v[24:27], v[128:131], v[36:39], v[24:27]\nds_read_b128 v[56:59], v1 offset:6144\nds_read_b128 v[60:63], v1 offset:7168\nv_mfma_f32_16x16x32_bf16 v[28:31], v[136:139], v[36:39], v[28:31]\n" \
"s_waitcnt lgkmcnt(6)\nds_read_b128 v[36:39], v2 offset:2304\nv_mfma_f32_16x16x32_bf16 v[16:19], v[132:135], v[64:67], v[16:19]\nbuffer_load_dwordx4 v0, %[rs], s60 offen offset:1024 sc1 lds\nv_mfma_f32_16x16x32_bf16 v[20:23], v[140:143], v[64:67], v[20:23]\ns_waitcnt lgkmcnt(6)\nds_read_b128 v[64:67], v2 offset:4096\nv_mfma_f32_16x16x32_bf16 v[24:27], v[132:135], v[68:71], v[24:27]\n" \
"v_mfma_f32_16x16x32_bf16 v[28:31], v[140:143], v[68:71], v[28:31]\ns_waitcnt lgkmcnt(6)\nds_read_b128 v[68:71], v2 offset:4352\nv_mfma_f32_16x16x32_bf16 v[16:19], v[152:155], v[40:43], v[16:19]\nbuffer_load_dwordx4 v0, %[rs], s60 offen offset:2048 sc1 lds\nv_mfma_f32_16x16x32_bf16 v[20:23], v[168:171], v[40:43], v[20:23]\ns_waitcnt lgkmcnt(6)\nds_read_b128 v[40:43], v2 offset:6144\n" \
"v_mfma_f32_16x16x32_bf16 v[24:27], v[152:155], v[44:47], v[24:27]\nv_mfma_f32_16x16x32_bf16 v[28:31], v[168:171], v[44:47], v[28:31]\ns_waitcnt lgkmcnt(6)\nds_read_b128 v[44:47], v2 offset:6400\nv_mfma_f32_16x16x32_bf16 v[16:19], v[156:159], v[32:35], v[16:19]\nbuffer_load_dwordx4 v0, %[rs], s60 offen offset:3072 sc1 lds\nv_mfma_f32_16x16x32_bf16 v[20:23], v[172:175], v[32:35], v[20:23]\ns_waitcnt lgkmcnt(4)\n" \
"ds_read_b128 v[32:35], v2 offset:8192\nv_mfma_f32_16x16x32_bf16 v[24:27], v[156:159], v[36:39], v[24:27]\nv_mfma_f32_16x16x32_bf16 v[28:31], v[172:175], v[36:39], v[28:31]\ns_waitcnt lgkmcnt(4)\nds_read_b128 v[36:39], v2 offset:8448\nv_mfma_f32_16x16x32_bf16 v[16:19], v[184:187], v[64:67], v[16:19]\nbuffer_load_dwordx4 v[80:83], v3, %[rs], s42 offen offset:0 sc1\nv_mfma_f32_16x16x32_bf16 v[20:23], v[192:195], v[64:67], v[20:23]\n" \
"s_waitcnt lgkmcnt(4)\nds_read_b128 v[64:67], v2 offset:10240\nv_mfma_f32_16x16x32_bf16 v[24:27], v[184:187], v[68:71], v[24:27]\nv_mfma_f32_16x16x32_bf16 v[28:31], v[192:195], v[68:71], v[28:31]\ns_waitcnt lgkmcnt(4)\nds_read_b128 v[68:71], v2 offset:10496\nv_mfma_f32_16x16x32_bf16 v[16:19], v[188:191], v[40:43], v[16:19]\nbuffer_load_dwordx4 v[84:87], v3, %[rs], s42 offen offset:1024 sc1\n" \
"v_mfma_f32_16x16x32_bf16 v[20:23], v[196:199], v[40:43], v[20:23]\ns_waitcnt lgkmcnt(4)\nds_read_b128 v[40:43], v2 offset:12288\nv_mfma_f32_16x16x32_bf16 v[24:27], v[188:191], v[44:47], v[24:27]\nv_mfma_f32_16x16x32_bf16 v[28:31], v[196:199], v[44:47], v[28:31]\ns_waitcnt lgkmcnt(4)\nds_read_b128 v[44:47], v2 offset:14336\nv_mfma_f32_16x16x32_bf16 v[16:19], v[208:211], v[32:35], v[16:19]\n" \
"buffer_load_dwordx4 v[88:91], v3, %[rs], s42 offen offset:2048 sc1\nv_mfma_f32_16x16x32_bf16 v[20:23], v[56:59], v[32:35], v[20:23]\ns_waitcnt lgkmcnt(4)\nds_read_b128 v[32:35], v2 offset:12544\nv_mfma_f32_16x16x32_bf16 v[24:27], v[208:211], v[36:39], v[24:27]\nv_mfma_f32_16x16x32_bf16 v[28:31], v[56:59], v[36:39], v[28:31]\ns_waitcnt lgkmcnt(4)\nds_read_b128 v[36:39], v2 offset:14592\n" \
"v_mfma_f32_16x16x32_bf16 v[16:19], v[212:215], v[64:67], v[16:19]\nbuffer_load_dwordx4 v[92:95], v3, %[rs], s42 offen offset:3072 sc1\nv_mfma_f32_16x16x32_bf16 v[20:23], v[60:63], v[64:67], v[20:23]\ns_waitcnt lgkmcnt(4)\nv_mfma_f32_16x16x32_bf16 v[24:27], v[212:215], v[68:71], v[24:27]\nv_mfma_f32_16x16x32_bf16 v[28:31], v[60:63], v[68:71], v[28:31]\ns_waitcnt lgkmcnt(3)\nv_mfma_f32_16x16x32_bf16 v[16:19], v[232:235], v[40:43], v[16:19]\n" \
"v_mov_b64 v[48:49], 0\nv_mfma_f32_16x16x32_bf16 v[20:23], v[240:243], v[40:43], v[20:23]\ns_waitcnt lgkmcnt(2)\nv_mov_b64 v[50:51], 0\nv_mfma_f32_16x16x32_bf16 v[16:19], v[236:239], v[44:47], v[16:19]\nv_mov_b64 v[52:53], 0\nv_mfma_f32_16x16x32_bf16 v[20:23], v[244:247], v[44:47], v[20:23]\ns_waitcnt lgkmcnt(1)\n" \
"v_mov_b64 v[54:55], 0\nv_mfma_f32_16x16x32_bf16 v[24:27], v[232:235], v[32:35], v[24:27]\nv_mfma_f32_16x16x32_bf16 v[28:31], v[240:243], v[32:35], v[28:31]\ns_waitcnt lgkmcnt(0)\nv_mfma_f32_16x16x32_bf16 v[24:27], v[236:239], v[36:39], v[24:27]\nv_cvt_pk_bf16_f32 v72, v16, v20\nv_cvt_pk_bf16_f32 v73, v17, v21\nv_mfma_f32_16x16x32_bf16 v[28:31], v[244:247], v[36:39], v[28:31]\n" \
"v_cvt_pk_bf16_f32 v74, v18, v22\nv_cvt_pk_bf16_f32 v75, v19, v23\nds_write_b128 v9, v[72:75]\ns_nop 4\nv_cvt_pk_bf16_f32 v76, v24, v28\nv_cvt_pk_bf16_f32 v77, v25, v29\nv_cvt_pk_bf16_f32 v78, v26, v30\nv_cvt_pk_bf16_f32 v79, v27, v31\n" \
"ds_write_b128 v9, v[76:79] offset:1024\ns_cmp_lt_i32 s40, 0\ns_cbranch_scc1 .Lvu_first%=\n.Lvu_crit%=:\ns_cmp_eq_u32 %[w], 1\ns_cbranch_scc1 .Lvu_c1%=\ns_cmp_eq_u32 %[w], 2\ns_cbranch_scc1 .Lvu_c2%=\n" \
"s_cmp_eq_u32 %[w], 3\ns_cbranch_scc1 .Lvu_c3%=\n.Lvu_c0%=:\nds_read_b128 v[56:59], v1 offset:0\nds_read_b128 v[60:63], v1 offset:1024\nds_read_b128 v[64:67], v1 offset:2048\nds_read_b128 v[68:71], v1 offset:3072\n.Lvu_poll0%=:\n" \
"s_waitcnt vmcnt(0)\nv_smfmac_f32_16x16x64_bf16 v[48:51], v[80:83], a[0:7], v4\nds_write_b128 v0, v[80:83] offset:16384\nv_max3_u32 v14, v80, v82, v84\nv_smfmac_f32_16x16x64_bf16 v[52:55], v[80:83], a[16:23], v4\nds_write_b128 v0, v[84:87] offset:17408\nv_max3_u32 v14, v14, v86, v88\nv_smfmac_f32_16x16x64_bf16 v[48:51], v[84:87], a[32:39], v4\n" \
"ds_write_b128 v0, v[88:91] offset:18432\nv_max3_u32 v14, v14, v90, v92\nv_smfmac_f32_16x16x64_bf16 v[52:55], v[84:87], a[40:47], v4\nds_write_b128 v0, v[92:95] offset:19456\nv_max_u32 v14, v14, v94\nv_smfmac_f32_16x16x64_bf16 v[48:51], v[88:91], a[56:63], v4\ns_waitcnt lgkmcnt(6)\nv_cmp_eq_u32 vcc, -1, v14\n" \
"v_smfmac_f32_16x16x64_bf16 v[52:55], v[88:91], v[56:63], v4\ns_cbranch_vccnz .Lvu_re0%=\ns_waitcnt lgkmcnt(0)\ns_barrier\nds_read_b128 v[16:19], v0 offset:20480\nds_read_b128 v[20:23], v0 offset:21504\nv_smfmac_f32_16x16x64_bf16 v[48:51], v[92:95], a[80:87], v4\nds_read_b128 v[24:27], v0 offset:22528\n" \
"ds_read_b128 v[28:31], v0 offset:23552\nv_smfmac_f32_16x16x64_bf16 v[52:55], v[92:95], a[88:95], v4\ns_waitcnt lgkmcnt(3)\nds_read_b128 v[32:35], v0 offset:24576\nds_read_b128 v[36:39], v0 offset:25600\nv_smfmac_f32_16x16x64_bf16 v[48:51], v[16:19], a[104:111], v4\nds_read_b128 v[40:43], v0 offset:26624\nds_read_b128 v[44:47], v0 offset:27648\n" \
"v_smfmac_f32_16x16x64_bf16 v[52:55], v[16:19], a[120:127], v4\ns_waitcnt lgkmcnt(6)\nds_read_b128 v[80:83], v0 offset:28672\nds_read_b128 v[84:87], v0 offset:29696\nv_mov_b64 v[16:17], 0\nv_smfmac_f32_16x16x64_bf16 v[48:51], v[20:23], a[136:143], v4\nds_read_b128 v[88:91], v0 offset:30720\nds_read_b128 v[92:95], v0 offset:31744\n" \
"v_mov_b64 v[18:19], 0\nv_smfmac_f32_16x16x64_bf16 v[52:55], v[20:23], a[144:151], v4\ns_waitcnt lgkmcnt(9)\nds_read_b128 v[56:59], v1 offset:4096\nds_read_b128 v[60:63], v1 offset:5120\nv_mov_b64 v[20:21], 0\nv_smfmac_f32_16x16x64_bf16 v[48:51], v[24:27], a[160:167], v4\ns_add_u32 m0, s57, 0\n" \
"s_add_u32 s58, s61, 0\nbuffer_load_dwordx4 v0, %[rs], s58 offen offset:0 sc1 lds\nv_smfmac_f32_16x16x64_bf16 v[52:55], v[24:27], v[64:71], v4\ns_waitcnt lgkmcnt(10)\nv_mov_b64 v[22:23], 0\nv_smfmac_f32_16x16x64_bf16 v[48:51], v[28:31], a[184:191], v4\nds_read_b128 v[64:67], v1 offset:6144\nds_read_b128 v[68:71], v1 offset:7168\n" \
"v_mov_b64 v[24:25], 0\nv_smfmac_f32_16x16x64_bf16 v[52:55], v[28:31], a[192:199], v4\ns_waitcnt lgkmcnt(11)\nv_mov_b64 v[26:27], 0\nv_smfmac_f32_16x16x64_bf16 v[48:51], v[32:35], a[208:215], v4\nv_mov_b64 v[28:29], 0\nv_smfmac_f32_16x16x64_bf16 v[52:55], v[32:35], a[224:231], v4\ns_waitcnt lgkmcnt(10)\n" \
"v_mov_b64 v[30:31], 0\nv_smfmac_f32_16x16x64_bf16 v[48:51], v[36:39], a[240:247], v4\nbuffer_load_dwordx4 v0, %[rs], s58 offen offset:1024 sc1 lds\nv_smfmac_f32_16x16x64_bf16 v[52:55], v[36:39], a[248:255], v4\ns_waitcnt lgkmcnt(9)\nv_mov_b64 v[32:33], 0\nv_smfmac_f32_16x16x64_bf16 v[48:51], v[40:43], v[104:111], v4\ns_waitcnt lgkmcnt(2)\n" \
"v_mov_b64 v[34:35], 0\nv_smfmac_f32_16x16x64_bf16 v[52:55], v[40:43], v[56:63], v4\nv_mov_b64 v[36:37], 0\nv_smfmac_f32_16x16x64_bf16 v[48:51], v[44:47], v[128:135], v4\nv_mov_b64 v[38:39], 0\nv_smfmac_f32_16x16x64_bf16 v[52:55], v[44:47], v[136:143], v4\nds_read_b128 v[56:59], v1 offset:8192\nds_read_b128 v[60:63], v1 offset:9216\n" \
"v_mov_b64 v[40:41], 0\nv_smfmac_f32_16x16x64_bf16 v[48:51], v[80:83], v[152:159], v4\nbuffer_load_dwordx4 v0, %[rs], s58 offen offset:2048 sc1 lds\nv_smfmac_f32_16x16x64_bf16 v[52:55], v[80:83], v[168:175], v4\nv_mov_b64 v[42:43], 0\nv_smfmac_f32_16x16x64_bf16 v[48:51], v[84:87], v[184:191], v4\nv_mov_b64 v[44:45], 0\nv_smfmac_f32_16x16x64_bf16 v[52:55], v[84:87], v[192:199], v4\n" \
"v_mov_b64 v[46:47], 0\nv_smfmac_f32_16x16x64_bf16 v[48:51], v[88:91], v[208:215], v4\ns_waitcnt lgkmcnt(2)\nv_smfmac_f32_16x16x64_bf16 v[52:55], v[88:91], v[64:71], v4\nv_smfmac_f32_16x16x64_bf16 v[48:51], v[92:95], v[232:239], v4\nbuffer_load_dwordx4 v0, %[rs], s58 offen offset:3072 sc1 lds\nv_smfmac_f32_16x16x64_bf16 v[52:55], v[92:95], v[240:247], v4\nds_read_b128 v[64:67], v1 offset:10240\n" \
"ds_read_b128 v[68:71], v1 offset:11264\ns_branch .Lvu_u%=\n.Lvu_re0%=:\nv_max_u32 v14, v80, v82\nv_cmp_eq_u32 s[46:47], -1, v14\nv_max_u32 v14, v84, v86\nv_cmp_eq_u32 s[48:49], -1, v14\nv_max_u32 v14, v88, v90\n" \
"v_cmp_eq_u32 s[50:51], -1, v14\nv_max_u32 v14, v92, v94\nv_cmp_eq_u32 s[52:53], -1, v14\ns_mov_b64 exec, s[46:47]\nbuffer_load_dwordx4 v[80:83], v3, %[rs], s42 offen offset:0 sc1\ns_mov_b64 exec, s[48:49]\nbuffer_load_dwordx4 v[84:87], v3, %[rs], s42 offen offset:1024 sc1\ns_mov_b64 exec, s[50:51]\n" \
"buffer_load_dwordx4 v[88:91], v3, %[rs], s42 offen offset:2048 sc1\ns_mov_b64 exec, s[52:53]\nbuffer_load_dwordx4 v[92:95], v3, %[rs], s42 offen offset:3072 sc1\ns_mov_b64 exec, -1\nv_mov_b64 v[48:49], 0\nv_mov_b64 v[50:51], 0\nv_mov_b64 v[52:53], 0\nv_mov_b64 v[54:55], 0\n" \
"s_add_u32 s43, s43, 1\ns_cmp_lt_u32 s43, 262144\ns_cbranch_scc1 .Lvu_poll0%=\ns_branch .Lvu_abort%=\n.Lvu_c1%=:\nds_read_b128 v[56:59], v1 offset:2048\nds_read_b128 v[60:63], v1 offset:3072\nds_read_b128 v[64:67], v1 offset:4096\n" \
"ds_read_b128 v[68:71], v1 offset:5120\n.Lvu_poll1%=:\ns_waitcnt vmcnt(0)\nv_smfmac_f32_16x16x64_bf16 v[48:51], v[80:83], a[104:111], v4\nds_write_b128 v0, v[80:83] offset:20480\nv_max3_u32 v14, v80, v82, v84\nv_smfmac_f32_16x16x64_bf16 v[52:55], v[80:83], a[120:127], v4\nds_write_b128 v0, v[84:87] offset:21504\n" \
"v_max3_u32 v14, v14, v86, v88\nv_smfmac_f32_16x16x64_bf16 v[48:51], v[84:87], a[136:143], v4\nds_write_b128 v0, v[88:91] offset:22528\nv_max3_u32 v14, v14, v90, v92\nv_smfmac_f32_16x16x64_bf16 v[52:55], v[84:87], a[144:151], v4\nds_write_b128 v0, v[92:95] offset:23552\nv_max_u32 v14, v14, v94\nv_smfmac_f32_16x16x64_bf16 v[48:51], v[88:91], a[160:167], v4\n" \
"s_waitcnt lgkmcnt(6)\nv_cmp_eq_u32 vcc, -1, v14\nv_smfmac_f32_16x16x64_bf16 v[52:55], v[88:91], v[56:63], v4\ns_cbranch_vccnz .Lvu_re1%=\ns_waitcnt lgkmcnt(0)\ns_barrier\nds_read_b128 v[16:19], v0 offset:24576\nds_read_b128 v[20:23], v0 offset:25600\n" \
"v_smfmac_f32_16x16x64_bf16 v[48:51], v[92:95], a[184:191], v4\nds_read_b128 v[24:27], v0 offset:26624\nds_read_b128 v[28:31], v0 offset:27648\nv_smfmac_f32_16x16x64_bf16 v[52:55], v[92:95], a[192:199], v4\ns_waitcnt lgkmcnt(3)\nds_read_b128 v[32:35], v0 offset:28672\nds_read_b128 v[36:39], v0 offset:29696\nv_smfmac_f32_16x16x64_bf16 v[48:51], v[16:19], a[208:215], v4\n" \
"ds_read_b128 v[40:43], v0 offset:30720\nds_read_b128 v[44:47], v0 offset:31744\nv_smfmac_f32_16x16x64_bf16 v[52:55], v[16:19], a[224:231], v4\ns_waitcnt lgkmcnt(6)\nds_read_b128 v[80:83], v0 offset:16384\nds_read_b128 v[84:87], v0 offset:17408\nv_mov_b64 v[16:17], 0\nv_smfmac_f32_16x16x64_bf16 v[48:51], v[20:23], a[240:247], v4\n" \
"ds_read_b128 v[88:91], v0 offset:18432\nds_read_b128 v[92:95], v0 offset:19456\nv_mov_b64 v[18:19], 0\nv_smfmac_f32_16x16x64_bf16 v[52:55], v[20:23], a[248:255], v4\ns_waitcnt lgkmcnt(9)\nds_read_b128 v[56:59], v1 offset:6144\nds_read_b128 v[60:63], v1 offset:7168\nv_mov_b64 v[20:21], 0\n" \
"v_smfmac_f32_16x16x64_bf16 v[48:51], v[24:27], v[104:111], v4\ns_add_u32 m0, s57, 0\ns_add_u32 s58, s61, 0\nbuffer_load_dwordx4 v0, %[rs], s58 offen offset:0 sc1 lds\nv_smfmac_f32_16x16x64_bf16 v[52:55], v[24:27], v[64:71], v4\ns_waitcnt lgkmcnt(10)\nv_mov_b64 v[22:23], 0\nv_smfmac_f32_16x16x64_bf16 v[48:51], v[28:31], v[128:135], v4\n" \
"ds_read_b128 v[64:67], v1 offset:0\nds_read_b128 v[68:71], v1 offset:1024\nv_mov_b64 v[24:25], 0\nv_smfmac_f32_16x16x64_bf16 v[52:55], v[28:31], v[136:143], v4\ns_waitcnt lgkmcnt(11)\nv_mov_b64 v[26:27], 0\nv_smfmac_f32_16x16x64_bf16 v[48:51], v[32:35], v[152:159], v4\nv_mov_b64 v[28:29], 0\n" \
"v_smfmac_f32_16x16x64_bf16 v[52:55], v[32:35], v[168:175], v4\ns_waitcnt lgkmcnt(10)\nv_mov_b64 v[30:31], 0\nv_smfmac_f32_16x16x64_bf16 v[48:51], v[36:39], v[184:191], v4\nbuffer_load_dwordx4 v0, %[rs], s58 offen offset:1024 sc1 lds\nv_smfmac_f32_16x16x64_bf16 v[52:55], v[36:39], v[192:199], v4\ns_waitcnt lgkmcnt(9)\nv_mov_b64 v[32:33], 0\n" \
"v_smfmac_f32_16x16x64_bf16 v[48:51], v[40:43], v[208:215], v4\ns_waitcnt lgkmcnt(2)\nv_mov_b64 v[34:35], 0\nv_smfmac_f32_16x16x64_bf16 v[52:55], v[40:43], v[56:63], v4\nv_mov_b64 v[36:37], 0\nv_smfmac_f32_16x16x64_bf16 v[48:51], v[44:47], v[232:239], v4\nv_mov_b64 v[38:39], 0\nv_smfmac_f32_16x16x64_bf16 v[52:55], v[44:47], v[240:247], v4\n" \
"ds_read_b128 v[56:59], v1 offset:8192\nds_read_b128 v[60:63], v1 offset:9216\nv_mov_b64 v[40:41], 0\nv_smfmac_f32_16x16x64_bf16 v[48:51], v[80:83], a[0:7], v4\nbuffer_load_dwordx4 v0, %[rs], s58 offen offset:2048 sc1 lds\nv_smfmac_f32_16x16x64_bf16 v[52:55], v[80:83], a[16:23], v4\nv_mov_b64 v[42:43], 0\nv_smfmac_f32_16x16x64_bf16 v[48:51], v[84:87], a[32:39], v4\n" \
"v_mov_b64 v[44:45], 0\nv_smfmac_f32_16x16x64_bf16 v[52:55], v[84:87], a[40:47], v4\nv_mov_b64 v[46:47], 0\nv_smfmac_f32_16x16x64_bf16 v[48:51], v[88:91], a[56:63], v4\ns_waitcnt lgkmcnt(2)\nv_smfmac_f32_16x16x64_bf16 v[52:55], v[88:91], v[64:71], v4\nv_smfmac_f32_16x16x64_bf16 v[48:51], v[92:95], a[80:87], v4\nbuffer_load_dwordx4 v0, %[rs], s58 offen offset:3072 sc1 lds\n" \
"v_smfmac_f32_16x16x64_bf16 v[52:55], v[92:95], a[88:95], v4\nds_read_b128 v[64:67], v1 offset:10240\nds_read_b128 v[68:71], v1 offset:11264\ns_branch .Lvu_u%=\n.Lvu_re1%=:\nv_max_u32 v14, v80, v82\nv_cmp_eq_u32 s[46:47], -1, v14\nv_max_u32 v14, v84, v86\n" \
"v_cmp_eq_u32 s[48:49], -1, v14\nv_max_u32 v14, v88, v90\nv_cmp_eq_u32 s[50:51], -1, v14\nv_max_u32 v14, v92, v94\nv_cmp_eq_u32 s[52:53], -1, v14\ns_mov_b64 exec, s[46:47]\nbuffer_load_dwordx4 v[80:83], v3, %[rs], s42 offen offset:0 sc1\ns_mov_b64 exec, s[48:49]\n" \
"buffer_load_dwordx4 v[84:87], v3, %[rs], s42 offen offset:1024 sc1\ns_mov_b64 exec, s[50:51]\nbuffer_load_dwordx4 v[88:91], v3, %[rs], s42 offen offset:2048 sc1\ns_mov_b64 exec, s[52:53]\nbuffer_load_dwordx4 v[92:95], v3, %[rs], s42 offen offset:3072 sc1\ns_mov_b64 exec, -1\nv_mov_b64 v[48:49], 0\nv_mov_b64 v[50:51], 0\n" \
"v_mov_b64 v[52:53], 0\nv_mov_b64 v[54:55], 0\ns_add_u32 s43, s43, 1\ns_cmp_lt_u32 s43, 262144\ns_cbranch_scc1 .Lvu_poll1%=\ns_branch .Lvu_abort%=\n.Lvu_c2%=:\nds_read_b128 v[56:59], v1 offset:4096\n" \
"ds_read_b128 v[60:63], v1 offset:5120\nds_read_b128 v[64:67], v1 offset:6144\nds_read_b128 v[68:71], v1 offset:7168\n.Lvu_poll2%=:\ns_waitcnt vmcnt(0)\nv_smfmac_f32_16x16x64_bf16 v[48:51], v[80:83], a[208:215], v4\nds_write_b128 v0, v[80:83] offset:24576\nv_max3_u32 v14, v80, v82, v84\n" \
"v_smfmac_f32_16x16x64_bf16 v[52:55], v[80:83], a[224:231], v4\nds_write_b128 v0, v[84:87] offset:25600\nv_max3_u32 v14, v14, v86, v88\nv_smfmac_f32_16x16x64_bf16 v[48:51], v[84:87], a[240:247], v4\nds_write_b128 v0, v[88:91] offset:26624\nv_max3_u32 v14, v14, v90, v92\nv_smfmac_f32_16x16x64_bf16 v[52:55], v[84:87], a[248:255], v4\nds_write_b128 v0, v[92:95] offset:27648\n" \
"v_max_u32 v14, v14, v94\nv_smfmac_f32_16x16x64_bf16 v[48:51], v[88:91], v[104:111], v4\ns_waitcnt lgkmcnt(6)\nv_cmp_eq_u32 vcc, -1, v14\nv_smfmac_f32_16x16x64_bf16 v[52:55], v[88:91], v[56:63], v4\ns_cbranch_vccnz .Lvu_re2%=\ns_waitcnt lgkmcnt(0)\ns_barrier\n" \
"ds_read_b128 v[16:19], v0 offset:28672\nds_read_b128 v[20:23], v0 offset:29696\nv_smfmac_f32_16x16x64_bf16 v[48:51], v[92:95], v[128:135], v4\nds_read_b128 v[24:27], v0 offset:30720\nds_read_b128 v[28:31], v0 offset:31744\nv_smfmac_f32_16x16x64_bf16 v[52:55], v[92:95], v[136:143], v4\ns_waitcnt lgkmcnt(3)\nds_read_b128 v[32:35], v0 offset:16384\n" \
"ds_read_b128 v[36:39], v0 offset:17408\nv_smfmac_f32_16x16x64_bf16 v[48:51], v[16:19], v[152:159], v4\nds_read_b128 v[40:43], v0 offset:18432\nds_read_b128 v[44:47], v0 offset:19456\nv_smfmac_f32_16x16x64_bf16 v[52:55], v[16:19], v[168:175], v4\ns_waitcnt lgkmcnt(6)\nds_read_b128 v[80:83], v0 offset:20480\nds_read_b128 v[84:87], v0 offset:21504\n" \
"v_mov_b64 v[16:17], 0\nv_smfmac_f32_16x16x64_bf16 v[48:51], v[20:23], v[184:191], v4\nds_read_b128 v[88:91], v0 offset:22528\nds_read_b128 v[92:95], v0 offset:23552\nv_mov_b64 v[18:19], 0\nv_smfmac_f32_16x16x64_bf16 v[52:55], v[20:23], v[192:199], v4\ns_waitcnt lgkmcnt(9)\nds_read_b128 v[56:59], v1 offset:0\n" \
"ds_read_b128 v[60:63], v1 offset:1024\nv_mov_b64 v[20:21], 0\nv_smfmac_f32_16x16x64_bf16 v[48:51], v[24:27], v[208:215], v4\ns_add_u32 m0, s57, 0\ns_add_u32 s58, s61, 0\nbuffer_load_dwordx4 v0, %[rs], s58 offen offset:0 sc1 lds\nv_smfmac_f32_16x16x64_bf16 v[52:55], v[24:27], v[64:71], v4\ns_waitcnt lgkmcnt(10)\n" \
"v_mov_b64 v[22:23], 0\nv_smfmac_f32_16x16x64_bf16 v[48:51], v[28:31], v[232:239], v4\nds_read_b128 v[64:67], v1 offset:2048\nds_read_b128 v[68:71], v1 offset:3072\nv_mov_b64 v[24:25], 0\nv_smfmac_f32_16x16x64_bf16 v[52:55], v[28:31], v[240:247], v4\ns_waitcnt lgkmcnt(11)\nv_mov_b64 v[26:27], 0\n" \
"v_smfmac_f32_16x16x64_bf16 v[48:51], v[32:35], a[0:7], v4\nv_mov_b64 v[28:29], 0\nv_smfmac_f32_16x16x64_bf16 v[52:55], v[32:35], a[16:23], v4\ns_waitcnt lgkmcnt(10)\nv_mov_b64 v[30:31], 0\nv_smfmac_f32_16x16x64_bf16 v[48:51], v[36:39], a[32:39], v4\nbuffer_load_dwordx4 v0, %[rs], s58 offen offset:1024 sc1 lds\nv_smfmac_f32_16x16x64_bf16 v[52:55], v[36:39], a[40:47], v4\n" \
"s_waitcnt lgkmcnt(9)\nv_mov_b64 v[32:33], 0\nv_smfmac_f32_16x16x64_bf16 v[48:51], v[40:43], a[56:63], v4\ns_waitcnt lgkmcnt(2)\nv_mov_b64 v[34:35], 0\nv_smfmac_f32_16x16x64_bf16 v[52:55], v[40:43], v[56:63], v4\nv_mov_b64 v[36:37], 0\nv_smfmac_f32_16x16x64_bf16 v[48:51], v[44:47], a[80:87], v4\n" \
"v_mov_b64 v[38:39], 0\nv_smfmac_f32_16x16x64_bf16 v[52:55], v[44:47], a[88:95], v4\nds_read_b128 v[56:59], v1 offset:8192\nds_read_b128 v[60:63], v1 offset:9216\nv_mov_b64 v[40:41], 0\nv_smfmac_f32_16x16x64_bf16 v[48:51], v[80:83], a[104:111], v4\nbuffer_load_dwordx4 v0, %[rs], s58 offen offset:2048 sc1 lds\nv_smfmac_f32_16x16x64_bf16 v[52:55], v[80:83], a[120:127], v4\n" \
"v_mov_b64 v[42:43], 0\nv_smfmac_f32_16x16x64_bf16 v[48:51], v[84:87], a[136:143], v4\nv_mov_b64 v[44:45], 0\nv_smfmac_f32_16x16x64_bf16 v[52:55], v[84:87], a[144:151], v4\nv_mov_b64 v[46:47], 0\nv_smfmac_f32_16x16x64_bf16 v[48:51], v[88:91], a[160:167], v4\ns_waitcnt lgkmcnt(2)\nv_smfmac_f32_16x16x64_bf16 v[52:55], v[88:91], v[64:71], v4\n" \
"v_smfmac_f32_16x16x64_bf16 v[48:51], v[92:95], a[184:191], v4\nbuffer_load_dwordx4 v0, %[rs], s58 offen offset:3072 sc1 lds\nv_smfmac_f32_16x16x64_bf16 v[52:55], v[92:95], a[192:199], v4\nds_read_b128 v[64:67], v1 offset:10240\nds_read_b128 v[68:71], v1 offset:11264\ns_branch .Lvu_u%=\n.Lvu_re2%=:\nv_max_u32 v14, v80, v82\n" \
"v_cmp_eq_u32 s[46:47], -1, v14\nv_max_u32 v14, v84, v86\nv_cmp_eq_u32 s[48:49], -1, v14\nv_max_u32 v14, v88, v90\nv_cmp_eq_u32 s[50:51], -1, v14\nv_max_u32 v14, v92, v94\nv_cmp_eq_u32 s[52:53], -1, v14\ns_mov_b64 exec, s[46:47]\n" \
"buffer_load_dwordx4 v[80:83], v3, %[rs], s42 offen offset:0 sc1\ns_mov_b64 exec, s[48:49]\nbuffer_load_dwordx4 v[84:87], v3, %[rs], s42 offen offset:1024 sc1\ns_mov_b64 exec, s[50:51]\nbuffer_load_dwordx4 v[88:91], v3, %[rs], s42 offen offset:2048 sc1\ns_mov_b64 exec, s[52:53]\nbuffer_load_dwordx4 v[92:95], v3, %[rs], s42 offen offset:3072 sc1\ns_mov_b64 exec, -1\n" \
"v_mov_b64 v[48:49], 0\nv_mov_b64 v[50:51], 0\nv_mov_b64 v[52:53], 0\nv_mov_b64 v[54:55], 0\ns_add_u32 s43, s43, 1\ns_cmp_lt_u32 s43, 262144\ns_cbranch_scc1 .Lvu_poll2%=\ns_branch .Lvu_abort%=\n" \
".Lvu_c3%=:\nds_read_b128 v[56:59], v1 offset:6144\nds_read_b128 v[60:63], v1 offset:7168\nds_read_b128 v[64:67], v1 offset:0\nds_read_b128 v[68:71], v1 offset:1024\n.Lvu_poll3%=:\ns_waitcnt vmcnt(0)\nv_smfmac_f32_16x16x64_bf16 v[48:51], v[80:83], v[152:159], v4\n" \
"ds_write_b128 v0, v[80:83] offset:28672\nv_max3_u32 v14, v80, v82, v84\nv_smfmac_f32_16x16x64_bf16 v[52:55], v[80:83], v[168:175], v4\nds_write_b128 v0, v[84:87] offset:29696\nv_max3_u32 v14, v14, v86, v88\nv_smfmac_f32_16x16x64_bf16 v[48:51], v[84:87], v[184:191], v4\nds_write_b128 v0, v[88:91] offset:30720\nv_max3_u32 v14, v14, v90, v92\n" \
"v_smfmac_f32_16x16x64_bf16 v[52:55], v[84:87], v[192:199], v4\nds_write_b128 v0, v[92:95] offset:31744\nv_max_u32 v14, v14, v94\nv_smfmac_f32_16x16x64_bf16 v[48:51], v[88:91], v[208:215], v4\ns_waitcnt lgkmcnt(6)\nv_cmp_eq_u32 vcc, -1, v14\nv_smfmac_f32_16x16x64_bf16 v[52:55], v[88:91], v[56:63], v4\ns_cbranch_vccnz .Lvu_re3%=\n" \
"s_waitcnt lgkmcnt(0)\ns_barrier\nds_read_b128 v[16:19], v0 offset:16384\nds_read_b128 v[20:23], v0 offset:17408\nv_smfmac_f32_16x16x64_bf16 v[48:51], v[92:95], v[232:239], v4\nds_read_b128 v[24:27], v0 offset:18432\nds_read_b128 v[28:31], v0 offset:19456\nv_smfmac_f32_16x16x64_bf16 v[52:55], v[92:95], v[240:247], v4\n" \
"s_waitcnt lgkmcnt(3)\nds_read_b128 v[32:35], v0 offset:20480\nds_read_b128 v[36:39], v0 offset:21504\nv_smfmac_f32_16x16x64_bf16 v[48:51], v[16:19], a[0:7], v4\nds_read_b128 v[40:43], v0 offset:22528\nds_read_b128 v[44:47], v0 offset:23552\nv_smfmac_f32_16x16x64_bf16 v[52:55], v[16:19], a[16:23], v4\ns_waitcnt lgkmcnt(6)\n" \
"ds_read_b128 v[80:83], v0 offset:24576\nds_read_b128 v[84:87], v0 offset:25600\nv_mov_b64 v[16:17], 0\nv_smfmac_f32_16x16x64_bf16 v[48:51], v[20:23], a[32:39], v4\nds_read_b128 v[88:91], v0 offset:26624\nds_read_b128 v[92:95], v0 offset:27648\nv_mov_b64 v[18:19], 0\nv_smfmac_f32_16x16x64_bf16 v[52:55], v[20:23], a[40:47], v4\n" \
"s_waitcnt lgkmcnt(9)\nds_read_b128 v[56:59], v1 offset:2048\nds_read_b128 v[60:63], v1 offset:3072\nv_mov_b64 v[20:21], 0\nv_smfmac_f32_16x16x64_bf16 v[48:51], v[24:27], a[56:63], v4\ns_add_u32 m0, s57, 0\ns_add_u32 s58, s61, 0\nbuffer_load_dwordx4 v0, %[rs], s58 offen offset:0 sc1 lds\n" \
"v_smfmac_f32_16x16x64_bf16 v[52:55], v[24:27], v[64:71], v4\ns_waitcnt lgkmcnt(10)\nv_mov_b64 v[22:23], 0\nv_smfmac_f32_16x16x64_bf16 v[48:51], v[28:31], a[80:87], v4\nds_read_b128 v[64:67], v1 offset:4096\nds_read_b128 v[68:71], v1 offset:5120\nv_mov_b64 v[24:25], 0\nv_smfmac_f32_16x16x64_bf16 v[52:55], v[28:31], a[88:95], v4\n" \
"s_waitcnt lgkmcnt(11)\nv_mov_b64 v[26:27], 0\nv_smfmac_f32_16x16x64_bf16 v[48:51], v[32:35], a[104:111], v4\nv_mov_b64 v[28:29], 0\nv_smfmac_f32_16x16x64_bf16 v[52:55], v[32:35], a[120:127], v4\ns_waitcnt lgkmcnt(10)\nv_mov_b64 v[30:31], 0\nv_smfmac_f32_16x16x64_bf16 v[48:51], v[36:39], a[136:143], v4\n" \
"buffer_load_dwordx4 v0, %[rs], s58 offen offset:1024 sc1 lds\nv_smfmac_f32_16x16x64_bf16 v[52:55], v[36:39], a[144:151], v4\ns_waitcnt lgkmcnt(9)\nv_mov_b64 v[32:33], 0\nv_smfmac_f32_16x16x64_bf16 v[48:51], v[40:43], a[160:167], v4\ns_waitcnt lgkmcnt(2)\nv_mov_b64 v[34:35], 0\nv_smfmac_f32_16x16x64_bf16 v[52:55], v[40:43], v[56:63], v4\n" \
"v_mov_b64 v[36:37], 0\nv_smfmac_f32_16x16x64_bf16 v[48:51], v[44:47], a[184:191], v4\nv_mov_b64 v[38:39], 0\nv_smfmac_f32_16x16x64_bf16 v[52:55], v[44:47], a[192:199], v4\nds_read_b128 v[56:59], v1 offset:8192\nds_read_b128 v[60:63], v1 offset:9216\nv_mov_b64 v[40:41], 0\nv_smfmac_f32_16x16x64_bf16 v[48:51], v[80:83], a[208:215], v4\n" \
"buffer_load_dwordx4 v0, %[rs], s58 offen offset:2048 sc1 lds\nv_smfmac_f32_16x16x64_bf16 v[52:55], v[80:83], a[224:231], v4\nv_mov_b64 v[42:43], 0\nv_smfmac_f32_16x16x64_bf16 v[48:51], v[84:87], a[240:247], v4\nv_mov_b64 v[44:45], 0\nv_smfmac_f32_16x16x64_bf16 v[52:55], v[84:87], a[248:255], v4\nv_mov_b64 v[46:47], 0\nv_smfmac_f32_16x16x64_bf16 v[48:51], v[88:91], v[104:111], v4\n" \
"s_waitcnt lgkmcnt(2)\nv_smfmac_f32_16x16x64_bf16 v[52:55], v[88:91], v[64:71], v4\nv_smfmac_f32_16x16x64_bf16 v[48:51], v[92:95], v[128:135], v4\nbuffer_load_dwordx4 v0, %[rs], s58 offen offset:3072 sc1 lds\nv_smfmac_f32_16x16x64_bf16 v[52:55], v[92:95], v[136:143], v4\nds_read_b128 v[64:67], v1 offset:10240\nds_read_b128 v[68:71], v1 offset:11264\n.Lvu_u%=:\n" \
"s_nop 2\nv_add_f32 v48, v48, v50\nv_add_f32 v49, v49, v51\ns_nop 1\nv_add_f32 v52, v52, v54\nv_add_f32 v53, v53, v55\nv_cvt_pk_bf16_f32 v14, v48, v52\nv_cvt_pk_bf16_f32 v15, v49, v53\n" \
"ds_write2_b32 v5, v14, v15 offset0:0 offset1:4\nv_mov_b64 v[48:49], 0\nv_mov_b64 v[50:51], 0\nv_mov_b64 v[52:53], 0\nv_mov_b64 v[54:55], 0\ns_mov_b32 s59, 0x21000\ns_waitcnt lgkmcnt(0)\ns_barrier\n" \
"ds_read_b128 v[80:83], v11 offset:57344\nds_read_b128 v[84:87], v11 offset:58368\nds_read_b128 v[88:91], v11 offset:59392\nds_read_b128 v[92:95], v11 offset:60416\ns_waitcnt lgkmcnt(3)\nv_smfmac_f32_16x16x64_bf16 v[16:19], v[80:83], a[8:15], v10\ns_waitcnt lgkmcnt(2)\nv_smfmac_f32_16x16x64_bf16 v[16:19], v[84:87], a[24:31], v10\n" \
"s_waitcnt lgkmcnt(1)\nv_smfmac_f32_16x16x64_bf16 v[16:19], v[88:91], v[56:63], v10\ns_waitcnt lgkmcnt(0)\nds_read_b128 v[56:59], v1 offset:12288\nv_smfmac_f32_16x16x64_bf16 v[16:19], v[92:95], a[48:55], v10\nds_read_b128 v[60:63], v1 offset:13312\nv_smfmac_f32_16x16x64_bf16 v[20:23], v[80:83], a[64:71], v10\nv_smfmac_f32_16x16x64_bf16 v[20:23], v[84:87], a[72:79], v10\n" \
"v_smfmac_f32_16x16x64_bf16 v[20:23], v[88:91], v[64:71], v10\nds_read_b128 v[64:67], v1 offset:14336\nv_smfmac_f32_16x16x64_bf16 v[20:23], v[92:95], a[96:103], v10\nds_read_b128 v[68:71], v1 offset:15360\nv_cvt_pk_bf16_f32 v72, v16, v17\nv_cvt_pk_bf16_f32 v73, v18, v19\nv_smfmac_f32_16x16x64_bf16 v[24:27], v[80:83], a[112:119], v10\nv_and_or_b32 v72, v72, -2, v12\n" \
"v_smfmac_f32_16x16x64_bf16 v[24:27], v[84:87], a[128:135], v10\ns_waitcnt lgkmcnt(2)\nv_cvt_pk_bf16_f32 v74, v20, v21\nv_smfmac_f32_16x16x64_bf16 v[24:27], v[88:91], v[56:63], v10\nds_read_b128 v[56:59], v1 offset:16384\nv_cvt_pk_bf16_f32 v75, v22, v23\nv_smfmac_f32_16x16x64_bf16 v[24:27], v[92:95], a[152:159], v10\nds_read_b128 v[60:63], v1 offset:17408\n" \
"v_and_or_b32 v74, v74, -2, v12\nv_smfmac_f32_16x16x64_bf16 v[28:31], v[80:83], a[168:175], v10\nv_permlane16_swap_b32 v73, v75\nv_smfmac_f32_16x16x64_bf16 v[28:31], v[84:87], a[176:183], v10\ns_waitcnt lgkmcnt(2)\nv_permlane16_swap_b32 v72, v74\nv_smfmac_f32_16x16x64_bf16 v[28:31], v[88:91], v[64:71], v10\nds_read_b128 v[64:67], v1 offset:18432\n" \
"v_smfmac_f32_16x16x64_bf16 v[28:31], v[92:95], a[200:207], v10\ns_mov_b32 s44, 0\nds_read_b128 v[68:71], v1 offset:19456\nbuffer_store_dwordx4 v[72:75], v6, %[rs], s44 offen sc1\nv_cvt_pk_bf16_f32 v76, v24, v25\nv_cvt_pk_bf16_f32 v77, v26, v27\nv_smfmac_f32_16x16x64_bf16 v[32:35], v[80:83], a[216:223], v10\nds_read_b128 v[16:19], v11 offset:49152\n" \
"v_and_or_b32 v76, v76, -2, v12\nv_smfmac_f32_16x16x64_bf16 v[32:35], v[84:87], a[232:239], v10\ns_waitcnt lgkmcnt(3)\nds_read_b128 v[20:23], v11 offset:50176\nv_cvt_pk_bf16_f32 v78, v28, v29\nv_smfmac_f32_16x16x64_bf16 v[32:35], v[88:91], v[56:63], v10\nds_read_b128 v[56:59], v1 offset:20480\nv_cvt_pk_bf16_f32 v79, v30, v31\n" \
"v_smfmac_f32_16x16x64_bf16 v[32:35], v[92:95], v[96:103], v10\nds_read_b128 v[60:63], v1 offset:21504\nv_and_or_b32 v78, v78, -2, v12\nv_smfmac_f32_16x16x64_bf16 v[36:39], v[80:83], v[112:119], v10\nds_read_b128 v[24:27], v11 offset:51200\nv_permlane16_swap_b32 v77, v79\nv_smfmac_f32_16x16x64_bf16 v[36:39], v[84:87], v[120:127], v10\ns_waitcnt lgkmcnt(5)\n" \
"ds_read_b128 v[28:31], v11 offset:52224\nv_permlane16_swap_b32 v76, v78\nv_smfmac_f32_16x16x64_bf16 v[36:39], v[88:91], v[64:71], v10\nds_read_b128 v[64:67], v1 offset:22528\nv_smfmac_f32_16x16x64_bf16 v[36:39], v[92:95], v[144:151], v10\ns_mov_b32 s44, 131072\nds_read_b128 v[68:71], v1 offset:23552\nbuffer_store_dwordx4 v[76:79], v6, %[rs], s44 offen sc1\n" \
"v_cvt_pk_bf16_f32 v72, v32, v33\nv_cvt_pk_bf16_f32 v73, v34, v35\nv_smfmac_f32_16x16x64_bf16 v[40:43], v[80:83], v[160:167], v10\nds_read_b128 v[32:35], v11 offset:53248\nv_and_or_b32 v72, v72, -2, v12\nv_smfmac_f32_16x16x64_bf16 v[40:43], v[84:87], v[176:183], v10\ns_waitcnt lgkmcnt(5)\nv_cvt_pk_bf16_f32 v74, v36, v37\n" \
"v_smfmac_f32_16x16x64_bf16 v[40:43], v[88:91], v[56:63], v10\nds_read_b128 v[56:59], v1 offset:8192\nv_cvt_pk_bf16_f32 v75, v38, v39\nv_smfmac_f32_16x16x64_bf16 v[40:43], v[92:95], v[200:207], v10\nds_read_b128 v[60:63], v1 offset:9216\nv_and_or_b32 v74, v74, -2, v12\nv_smfmac_f32_16x16x64_bf16 v[44:47], v[80:83], v[216:223], v10\nds_read_b128 v[36:39], v11 offset:54272\n" \
"v_permlane16_swap_b32 v73, v75\nv_smfmac_f32_16x16x64_bf16 v[44:47], v[84:87], v[224:231], v10\ns_waitcnt lgkmcnt(4)\nv_permlane16_swap_b32 v72, v74\nv_smfmac_f32_16x16x64_bf16 v[44:47], v[88:91], v[64:71], v10\nds_read_b128 v[64:67], v1 offset:10240\nds_read_b128 v[88:91], v11 offset:55296\nv_smfmac_f32_16x16x64_bf16 v[44:47], v[92:95], v[248:255], v10\n" \
"s_mov_b32 s44, 4096\ns_cmp_ge_i32 s40, 63\nds_read_b128 v[68:71], v1 offset:11264\nds_read_b128 v[92:95], v11 offset:56320\nbuffer_store_dwordx4 v[72:75], v6, %[rs], s44 offen sc1\nv_cvt_pk_bf16_f32 v76, v40, v41\nv_cvt_pk_bf16_f32 v77, v42, v43\ns_cbranch_scc1 .Lvu_last%=\n" \
".Lvu_y%=:\nv_smfmac_f32_16x16x64_bf16 v[48:51], v[16:19], a[8:15], v10\ns_add_u32 s40, s40, 1\nv_and_or_b32 v76, v76, -2, v12\nv_smfmac_f32_16x16x64_bf16 v[52:55], v[20:23], a[8:15], v10\ns_and_b32 s44, s40, 1\ns_lshl_b32 s55, s44, 22\nv_cvt_pk_bf16_f32 v78, v44, v45\n" \
"v_smfmac_f32_16x16x64_bf16 v[48:51], v[24:27], a[24:31], v10\ns_bfe_u32 s45, s40, 0x10001\nv_cvt_pk_bf16_f32 v79, v46, v47\nv_smfmac_f32_16x16x64_bf16 v[52:55], v[28:31], a[24:31], v10\ns_waitcnt lgkmcnt(5)\nv_and_or_b32 v78, v78, -2, v12\nv_smfmac_f32_16x16x64_bf16 v[48:51], v[32:35], v[56:63], v10\ns_waitcnt lgkmcnt(4)\n" \
"v_permlane16_swap_b32 v77, v79\nv_smfmac_f32_16x16x64_bf16 v[52:55], v[36:39], v[56:63], v10\ns_waitcnt lgkmcnt(2)\nv_permlane16_swap_b32 v76, v78\nv_smfmac_f32_16x16x64_bf16 v[48:51], v[88:91], a[48:55], v10\ns_waitcnt lgkmcnt(0)\nv_mov_b64 v[80:81], 0\nv_smfmac_f32_16x16x64_bf16 v[52:55], v[92:95], a[48:55], v10\n" \
"buffer_store_dwordx4 v[76:79], v6, %[rs], s59 offen sc1\nv_mov_b64 v[82:83], 0\nv_mov_b64 v[84:85], 0\nv_mov_b64 v[86:87], 0\nv_mov_b32 v13, s45\nv_smfmac_f32_16x16x64_bf16 v[80:83], v[16:19], a[64:71], v10\nds_read_b128 v[56:59], v1 offset:12288\nv_cvt_pk_bf16_f32 v72, v48, v49\n" \
"v_smfmac_f32_16x16x64_bf16 v[84:87], v[20:23], a[64:71], v10\nds_read_b128 v[60:63], v1 offset:13312\nv_cvt_pk_bf16_f32 v73, v50, v51\nv_smfmac_f32_16x16x64_bf16 v[80:83], v[24:27], a[72:79], v10\nv_cvt_pk_bf16_f32 v74, v52, v53\nv_smfmac_f32_16x16x64_bf16 v[84:87], v[28:31], a[72:79], v10\ns_add_u32 s60, s55, 0\nv_cvt_pk_bf16_f32 v75, v54, v55\n" \
"v_smfmac_f32_16x16x64_bf16 v[80:83], v[32:35], v[64:71], v10\nv_and_or_b32 v72, v72, -2, v13\nv_smfmac_f32_16x16x64_bf16 v[84:87], v[36:39], v[64:71], v10\ns_and_b32 s42, s40, 3\ns_lshl_b32 s42, s42, 15\nv_and_or_b32 v74, v74, -2, v13\nv_smfmac_f32_16x16x64_bf16 v[80:83], v[88:91], a[96:103], v10\nbuffer_store_dwordx4 v[72:75], v7, %[rs], s60 offen offset:0 sc1\n" \
"v_mov_b64 v[48:49], 0\nv_mov_b64 v[50:51], 0\nv_mov_b64 v[52:53], 0\nv_mov_b64 v[54:55], 0\nv_smfmac_f32_16x16x64_bf16 v[84:87], v[92:95], a[96:103], v10\nv_mov_b32 v12, s44\nv_smfmac_f32_16x16x64_bf16 v[48:51], v[16:19], a[112:119], v10\nds_read_b128 v[64:67], v1 offset:14336\n" \
"v_cvt_pk_bf16_f32 v76, v80, v81\nv_smfmac_f32_16x16x64_bf16 v[52:55], v[20:23], a[112:119], v10\nds_read_b128 v[68:71], v1 offset:15360\nv_cvt_pk_bf16_f32 v77, v82, v83\nv_smfmac_f32_16x16x64_bf16 v[48:51], v[24:27], a[128:135], v10\nv_cvt_pk_bf16_f32 v78, v84, v85\nv_smfmac_f32_16x16x64_bf16 v[52:55], v[28:31], a[128:135], v10\ns_add_u32 s60, s55, 4096\n" \
"s_waitcnt lgkmcnt(2)\nv_cvt_pk_bf16_f32 v79, v86, v87\nv_smfmac_f32_16x16x64_bf16 v[48:51], v[32:35], v[56:63], v10\nv_and_or_b32 v76, v76, -2, v13\nv_smfmac_f32_16x16x64_bf16 v[52:55], v[36:39], v[56:63], v10\nv_and_or_b32 v78, v78, -2, v13\nv_smfmac_f32_16x16x64_bf16 v[48:51], v[88:91], a[152:159], v10\nbuffer_store_dwordx4 v[76:79], v7, %[rs], s60 offen offset:0 sc1\n" \
"v_mov_b64 v[80:81], 0\nv_mov_b64 v[82:83], 0\nv_mov_b64 v[84:85], 0\nv_mov_b64 v[86:87], 0\nv_smfmac_f32_16x16x64_bf16 v[52:55], v[92:95], a[152:159], v10\nv_smfmac_f32_16x16x64_bf16 v[80:83], v[16:19], a[168:175], v10\nds_read_b128 v[56:59], v1 offset:16384\nv_cvt_pk_bf16_f32 v72, v48, v49\n" \
"v_smfmac_f32_16x16x64_bf16 v[84:87], v[20:23], a[168:175], v10\nds_read_b128 v[60:63], v1 offset:17408\nv_cvt_pk_bf16_f32 v73, v50, v51\nv_smfmac_f32_16x16x64_bf16 v[80:83], v[24:27], a[176:183], v10\ns_cmp_lt_u32 s40, 2\ns_cselect_b32 s54, %[fy], -2147483648\nv_cvt_pk_bf16_f32 v74, v52, v53\nv_smfmac_f32_16x16x64_bf16 v[84:87], v[28:31], a[176:183], v10\n" \
"s_add_u32 s60, s55, 16384\ns_waitcnt lgkmcnt(2)\nv_cvt_pk_bf16_f32 v75, v54, v55\nv_smfmac_f32_16x16x64_bf16 v[80:83], v[32:35], v[64:71], v10\nv_and_or_b32 v72, v72, -2, v13\nv_smfmac_f32_16x16x64_bf16 v[84:87], v[36:39], v[64:71], v10\nv_and_or_b32 v74, v74, -2, v13\nv_smfmac_f32_16x16x64_bf16 v[80:83], v[88:91], a[200:207], v10\n" \
"buffer_store_dwordx4 v[72:75], v7, %[rs], s60 offen offset:0 sc1\nv_mov_b64 v[48:49], 0\nv_mov_b64 v[50:51], 0\nv_mov_b64 v[52:53], 0\nv_mov_b64 v[54:55], 0\nv_smfmac_f32_16x16x64_bf16 v[84:87], v[92:95], a[200:207], v10\nv_smfmac_f32_16x16x64_bf16 v[48:51], v[16:19], a[216:223], v10\nds_read_b128 v[64:67], v1 offset:18432\n" \
"v_cvt_pk_bf16_f32 v76, v80, v81\nv_smfmac_f32_16x16x64_bf16 v[52:55], v[20:23], a[216:223], v10\nds_read_b128 v[68:71], v1 offset:19456\nv_cvt_pk_bf16_f32 v77, v82, v83\nv_smfmac_f32_16x16x64_bf16 v[48:51], v[24:27], a[232:239], v10\nv_cvt_pk_bf16_f32 v78, v84, v85\nv_smfmac_f32_16x16x64_bf16 v[52:55], v[28:31], a[232:239], v10\ns_add_u32 s60, s55, 20480\n" \
"s_waitcnt lgkmcnt(2)\nv_cvt_pk_bf16_f32 v79, v86, v87\nv_smfmac_f32_16x16x64_bf16 v[48:51], v[32:35], v[56:63], v10\nv_and_or_b32 v76, v76, -2, v13\nv_smfmac_f32_16x16x64_bf16 v[52:55], v[36:39], v[56:63], v10\nv_and_or_b32 v78, v78, -2, v13\nv_smfmac_f32_16x16x64_bf16 v[48:51], v[88:91], v[96:103], v10\nbuffer_store_dwordx4 v[76:79], v7, %[rs], s60 offen offset:0 sc1\n" \
"v_mov_b64 v[80:81], 0\nv_mov_b64 v[82:83], 0\nv_mov_b64 v[84:85], 0\nv_mov_b64 v[86:87], 0\nv_smfmac_f32_16x16x64_bf16 v[52:55], v[92:95], v[96:103], v10\nv_smfmac_f32_16x16x64_bf16 v[80:83], v[16:19], v[112:119], v10\nds_read_b128 v[56:59], v1 offset:20480\nv_cvt_pk_bf16_f32 v72, v48, v49\n" \
"v_smfmac_f32_16x16x64_bf16 v[84:87], v[20:23], v[112:119], v10\nds_read_b128 v[60:63], v1 offset:21504\nv_cvt_pk_bf16_f32 v73, v50, v51\nv_smfmac_f32_16x16x64_bf16 v[80:83], v[24:27], v[120:127], v10\nv_cvt_pk_bf16_f32 v74, v52, v53\nv_smfmac_f32_16x16x64_bf16 v[84:87], v[28:31], v[120:127], v10\ns_add_u32 s60, s55, 0\ns_waitcnt lgkmcnt(2)\n" \
"v_cvt_pk_bf16_f32 v75, v54, v55\nv_smfmac_f32_16x16x64_bf16 v[80:83], v[32:35], v[64:71], v10\nv_and_or_b32 v72, v72, -2, v13\nv_smfmac_f32_16x16x64_bf16 v[84:87], v[36:39], v[64:71], v10\nv_and_or_b32 v74, v74, -2, v13\nv_smfmac_f32_16x16x64_bf16 v[80:83], v[88:91], v[144:151], v10\nbuffer_store_dwordx4 v[72:75], v7, %[rs], s60 offen offset:2048 sc1\nv_mov_b64 v[48:49], 0\n" \
"v_mov_b64 v[50:51], 0\nv_mov_b64 v[52:53], 0\nv_mov_b64 v[54:55], 0\nv_smfmac_f32_16x16x64_bf16 v[84:87], v[92:95], v[144:151], v10\nv_smfmac_f32_16x16x64_bf16 v[48:51], v[16:19], v[160:167], v10\nds_read_b128 v[64:67], v1 offset:22528\nv_cvt_pk_bf16_f32 v76, v80, v81\nv_smfmac_f32_16x16x64_bf16 v[52:55], v[20:23], v[160:167], v10\n" \
"ds_read_b128 v[68:71], v1 offset:23552\nv_cvt_pk_bf16_f32 v77, v82, v83\nv_smfmac_f32_16x16x64_bf16 v[48:51], v[24:27], v[176:183], v10\nv_cvt_pk_bf16_f32 v78, v84, v85\nv_smfmac_f32_16x16x64_bf16 v[52:55], v[28:31], v[176:183], v10\ns_add_u32 s60, s55, 4096\ns_waitcnt lgkmcnt(2)\nv_cvt_pk_bf16_f32 v79, v86, v87\n" \
"v_smfmac_f32_16x16x64_bf16 v[48:51], v[32:35], v[56:63], v10\nv_and_or_b32 v76, v76, -2, v13\nv_smfmac_f32_16x16x64_bf16 v[52:55], v[36:39], v[56:63], v10\nv_and_or_b32 v78, v78, -2, v13\nv_smfmac_f32_16x16x64_bf16 v[48:51], v[88:91], v[200:207], v10\nbuffer_store_dwordx4 v[76:79], v7, %[rs], s60 offen offset:2048 sc1\nv_mov_b64 v[80:81], 0\nv_mov_b64 v[82:83], 0\n" \
"v_mov_b64 v[84:85], 0\nv_mov_b64 v[86:87], 0\nv_smfmac_f32_16x16x64_bf16 v[52:55], v[92:95], v[200:207], v10\nv_smfmac_f32_16x16x64_bf16 v[80:83], v[16:19], v[216:223], v10\nds_read_b128 v[56:59], v1 offset:8192\nds_read_b128 v[16:19], v11 offset:49280\nv_cvt_pk_bf16_f32 v72, v48, v49\nv_smfmac_f32_16x16x64_bf16 v[84:87], v[20:23], v[216:223], v10\n" \
"ds_read_b128 v[60:63], v1 offset:9216\nds_read_b128 v[20:23], v11 offset:50304\nv_cvt_pk_bf16_f32 v73, v50, v51\nv_smfmac_f32_16x16x64_bf16 v[80:83], v[24:27], v[224:231], v10\nds_read_b128 v[24:27], v11 offset:51328\nv_cvt_pk_bf16_f32 v74, v52, v53\nv_smfmac_f32_16x16x64_bf16 v[84:87], v[28:31], v[224:231], v10\ns_add_u32 s60, s55, 16384\n" \
"s_waitcnt lgkmcnt(5)\nds_read_b128 v[28:31], v11 offset:52352\nv_cvt_pk_bf16_f32 v75, v54, v55\nv_smfmac_f32_16x16x64_bf16 v[80:83], v[32:35], v[64:71], v10\nds_read_b128 v[32:35], v11 offset:53376\nv_and_or_b32 v72, v72, -2, v13\nv_smfmac_f32_16x16x64_bf16 v[84:87], v[36:39], v[64:71], v10\nds_read_b128 v[36:39], v11 offset:54400\n" \
"v_and_or_b32 v74, v74, -2, v13\nv_smfmac_f32_16x16x64_bf16 v[80:83], v[88:91], v[248:255], v10\nds_read_b128 v[88:91], v11 offset:55424\nbuffer_store_dwordx4 v[72:75], v7, %[rs], s60 offen offset:2048 sc1\nv_mov_b64 v[48:49], 0\nv_mov_b64 v[50:51], 0\nv_mov_b64 v[52:53], 0\nv_mov_b64 v[54:55], 0\n" \
"v_smfmac_f32_16x16x64_bf16 v[84:87], v[92:95], v[248:255], v10\ns_waitcnt lgkmcnt(7)\nds_read_b128 v[92:95], v11 offset:56448\nv_smfmac_f32_16x16x64_bf16 v[48:51], v[16:19], a[8:15], v10\ns_waitcnt lgkmcnt(6)\nds_read_b128 v[64:67], v1 offset:10240\nv_cvt_pk_bf16_f32 v76, v80, v81\nv_smfmac_f32_16x16x64_bf16 v[52:55], v[20:23], a[8:15], v10\n" \
"s_waitcnt lgkmcnt(6)\nds_read_b128 v[68:71], v1 offset:11264\nv_cvt_pk_bf16_f32 v77, v82, v83\nv_smfmac_f32_16x16x64_bf16 v[48:51], v[24:27], a[24:31], v10\ns_waitcnt lgkmcnt(6)\nv_cvt_pk_bf16_f32 v78, v84, v85\nv_smfmac_f32_16x16x64_bf16 v[52:55], v[28:31], a[24:31], v10\ns_add_u32 s60, s55, 20480\n" \
"s_waitcnt lgkmcnt(5)\nv_cvt_pk_bf16_f32 v79, v86, v87\nv_smfmac_f32_16x16x64_bf16 v[48:51], v[32:35], v[56:63], v10\ns_waitcnt lgkmcnt(4)\nv_and_or_b32 v76, v76, -2, v13\nv_smfmac_f32_16x16x64_bf16 v[52:55], v[36:39], v[56:63], v10\ns_waitcnt lgkmcnt(3)\nv_and_or_b32 v78, v78, -2, v13\n" \
"v_smfmac_f32_16x16x64_bf16 v[48:51], v[88:91], a[48:55], v10\ns_waitcnt lgkmcnt(2)\nbuffer_store_dwordx4 v[76:79], v7, %[rs], s60 offen offset:2048 sc1\nv_mov_b64 v[80:81], 0\nv_mov_b64 v[82:83], 0\nv_mov_b64 v[84:85], 0\nv_mov_b64 v[86:87], 0\nv_smfmac_f32_16x16x64_bf16 v[52:55], v[92:95], a[48:55], v10\n" \
"v_smfmac_f32_16x16x64_bf16 v[80:83], v[16:19], a[64:71], v10\nds_read_b128 v[56:59], v1 offset:12288\nv_cvt_pk_bf16_f32 v72, v48, v49\nv_smfmac_f32_16x16x64_bf16 v[84:87], v[20:23], a[64:71], v10\nds_read_b128 v[60:63], v1 offset:13312\nv_cvt_pk_bf16_f32 v73, v50, v51\nv_smfmac_f32_16x16x64_bf16 v[80:83], v[24:27], a[72:79], v10\nv_cvt_pk_bf16_f32 v74, v52, v53\n" \
"v_smfmac_f32_16x16x64_bf16 v[84:87], v[28:31], a[72:79], v10\ns_add_u32 s60, s55, 0\ns_waitcnt lgkmcnt(2)\nv_cvt_pk_bf16_f32 v75, v54, v55\nv_smfmac_f32_16x16x64_bf16 v[80:83], v[32:35], v[64:71], v10\nv_and_or_b32 v72, v72, -2, v13\nv_smfmac_f32_16x16x64_bf16 v[84:87], v[36:39], v[64:71], v10\nv_and_or_b32 v74, v74, -2, v13\n" \
"v_smfmac_f32_16x16x64_bf16 v[80:83], v[88:91], a[96:103], v10\nbuffer_store_dwordx4 v[72:75], v7, %[rs], s60 offen offset:512 sc1\nv_mov_b64 v[48:49], 0\nv_mov_b64 v[50:51], 0\nv_mov_b64 v[52:53], 0\nv_mov_b64 v[54:55], 0\nv_smfmac_f32_16x16x64_bf16 v[84:87], v[92:95], a[96:103], v10\nv_smfmac_f32_16x16x64_bf16 v[48:51], v[16:19], a[112:119], v10\n" \
"ds_read_b128 v[64:67], v1 offset:14336\nv_cvt_pk_bf16_f32 v76, v80, v81\nv_smfmac_f32_16x16x64_bf16 v[52:55], v[20:23], a[112:119], v10\nds_read_b128 v[68:71], v1 offset:15360\nv_cvt_pk_bf16_f32 v77, v82, v83\nv_smfmac_f32_16x16x64_bf16 v[48:51], v[24:27], a[128:135], v10\nv_cvt_pk_bf16_f32 v78, v84, v85\nv_smfmac_f32_16x16x64_bf16 v[52:55], v[28:31], a[128:135], v10\n" \
"s_add_u32 s60, s55, 4096\ns_waitcnt lgkmcnt(2)\nv_cvt_pk_bf16_f32 v79, v86, v87\nv_smfmac_f32_16x16x64_bf16 v[48:51], v[32:35], v[56:63], v10\nv_and_or_b32 v76, v76, -2, v13\nv_smfmac_f32_16x16x64_bf16 v[52:55], v[36:39], v[56:63], v10\nv_and_or_b32 v78, v78, -2, v13\nv_smfmac_f32_16x16x64_bf16 v[48:51], v[88:91], a[152:159], v10\n" \
"buffer_store_dwordx4 v[76:79], v7, %[rs], s60 offen offset:512 sc1\nv_mov_b64 v[80:81], 0\nv_mov_b64 v[82:83], 0\nv_mov_b64 v[84:85], 0\nv_mov_b64 v[86:87], 0\nv_smfmac_f32_16x16x64_bf16 v[52:55], v[92:95], a[152:159], v10\nv_smfmac_f32_16x16x64_bf16 v[80:83], v[16:19], a[168:175], v10\nds_read_b128 v[56:59], v1 offset:16384\n" \
"v_cvt_pk_bf16_f32 v72, v48, v49\nv_smfmac_f32_16x16x64_bf16 v[84:87], v[20:23], a[168:175], v10\nds_read_b128 v[60:63], v1 offset:17408\nv_cvt_pk_bf16_f32 v73, v50, v51\nv_smfmac_f32_16x16x64_bf16 v[80:83], v[24:27], a[176:183], v10\nv_cvt_pk_bf16_f32 v74, v52, v53\nv_smfmac_f32_16x16x64_bf16 v[84:87], v[28:31], a[176:183], v10\ns_add_u32 s60, s55, 16384\n" \
"s_waitcnt lgkmcnt(2)\nv_cvt_pk_bf16_f32 v75, v54, v55\nv_smfmac_f32_16x16x64_bf16 v[80:83], v[32:35], v[64:71], v10\nv_and_or_b32 v72, v72, -2, v13\nv_smfmac_f32_16x16x64_bf16 v[84:87], v[36:39], v[64:71], v10\nv_and_or_b32 v74, v74, -2, v13\nv_smfmac_f32_16x16x64_bf16 v[80:83], v[88:91], a[200:207], v10\nbuffer_store_dwordx4 v[72:75], v7, %[rs], s60 offen offset:512 sc1\n" \
"v_mov_b64 v[48:49], 0\nv_mov_b64 v[50:51], 0\nv_mov_b64 v[52:53], 0\nv_mov_b64 v[54:55], 0\nv_smfmac_f32_16x16x64_bf16 v[84:87], v[92:95], a[200:207], v10\nv_smfmac_f32_16x16x64_bf16 v[48:51], v[16:19], a[216:223], v10\nds_read_b128 v[64:67], v1 offset:18432\nv_cvt_pk_bf16_f32 v76, v80, v81\n" \
"v_smfmac_f32_16x16x64_bf16 v[52:55], v[20:23], a[216:223], v10\nds_read_b128 v[68:71], v1 offset:19456\nv_cvt_pk_bf16_f32 v77, v82, v83\nv_smfmac_f32_16x16x64_bf16 v[48:51], v[24:27], a[232:239], v10\nv_cvt_pk_bf16_f32 v78, v84, v85\nv_smfmac_f32_16x16x64_bf16 v[52:55], v[28:31], a[232:239], v10\ns_add_u32 s60, s55, 20480\ns_waitcnt lgkmcnt(2)\n" \
"v_cvt_pk_bf16_f32 v79, v86, v87\nv_smfmac_f32_16x16x64_bf16 v[48:51], v[32:35], v[56:63], v10\nv_and_or_b32 v76, v76, -2, v13\nv_smfmac_f32_16x16x64_bf16 v[52:55], v[36:39], v[56:63], v10\nv_and_or_b32 v78, v78, -2, v13\nv_smfmac_f32_16x16x64_bf16 v[48:51], v[88:91], v[96:103], v10\nbuffer_store_dwordx4 v[76:79], v7, %[rs], s60 offen offset:512 sc1\nv_mov_b64 v[80:81], 0\n" \
"v_mov_b64 v[82:83], 0\nv_mov_b64 v[84:85], 0\nv_mov_b64 v[86:87], 0\nv_smfmac_f32_16x16x64_bf16 v[52:55], v[92:95], v[96:103], v10\nv_smfmac_f32_16x16x64_bf16 v[80:83], v[16:19], v[112:119], v10\nds_read_b128 v[56:59], v1 offset:20480\nv_cvt_pk_bf16_f32 v72, v48, v49\nv_smfmac_f32_16x16x64_bf16 v[84:87], v[20:23], v[112:119], v10\n" \
"ds_read_b128 v[60:63], v1 offset:21504\nv_cvt_pk_bf16_f32 v73, v50, v51\nv_smfmac_f32_16x16x64_bf16 v[80:83], v[24:27], v[120:127], v10\nv_cvt_pk_bf16_f32 v74, v52, v53\nv_smfmac_f32_16x16x64_bf16 v[84:87], v[28:31], v[120:127], v10\ns_add_u32 s60, s55, 0\ns_waitcnt lgkmcnt(2)\nv_cvt_pk_bf16_f32 v75, v54, v55\n" \
"v_smfmac_f32_16x16x64_bf16 v[80:83], v[32:35], v[64:71], v10\nv_and_or_b32 v72, v72, -2, v13\nv_smfmac_f32_16x16x64_bf16 v[84:87], v[36:39], v[64:71], v10\nv_and_or_b32 v74, v74, -2, v13\nv_smfmac_f32_16x16x64_bf16 v[80:83], v[88:91], v[144:151], v10\nbuffer_store_dwordx4 v[72:75], v7, %[rs], s60 offen offset:2560 sc1\nv_mov_b64 v[48:49], 0\nv_mov_b64 v[50:51], 0\n" \
"v_mov_b64 v[52:53], 0\nv_mov_b64 v[54:55], 0\nv_smfmac_f32_16x16x64_bf16 v[84:87], v[92:95], v[144:151], v10\nv_smfmac_f32_16x16x64_bf16 v[48:51], v[16:19], v[160:167], v10\nds_read_b128 v[64:67], v1 offset:22528\nv_cvt_pk_bf16_f32 v76, v80, v81\nv_smfmac_f32_16x16x64_bf16 v[52:55], v[20:23], v[160:167], v10\nds_read_b128 v[68:71], v1 offset:23552\n" \
"v_cvt_pk_bf16_f32 v77, v82, v83\nv_smfmac_f32_16x16x64_bf16 v[48:51], v[24:27], v[176:183], v10\nv_cvt_pk_bf16_f32 v78, v84, v85\nv_smfmac_f32_16x16x64_bf16 v[52:55], v[28:31], v[176:183], v10\ns_add_u32 s60, s55, 4096\ns_waitcnt lgkmcnt(2)\nv_cvt_pk_bf16_f32 v79, v86, v87\nv_smfmac_f32_16x16x64_bf16 v[48:51], v[32:35], v[56:63], v10\n" \
"v_and_or_b32 v76, v76, -2, v13\nv_smfmac_f32_16x16x64_bf16 v[52:55], v[36:39], v[56:63], v10\nv_and_or_b32 v78, v78, -2, v13\nv_smfmac_f32_16x16x64_bf16 v[48:51], v[88:91], v[200:207], v10\nbuffer_store_dwordx4 v[76:79], v7, %[rs], s60 offen offset:2560 sc1\nv_mov_b64 v[80:81], 0\nv_mov_b64 v[82:83], 0\nv_mov_b64 v[84:85], 0\n" \
"v_mov_b64 v[86:87], 0\nv_smfmac_f32_16x16x64_bf16 v[52:55], v[92:95], v[200:207], v10\ns_cmp_lg_u64 s[64:65], 0\ns_cbranch_scc1 .Lvu_slow%=\n.Lvu_slowr%=:\ns_waitcnt vmcnt(18)\ns_barrier\nds_read_b128 v[40:43], v2 offset:0\n" \
"ds_read_b128 v[44:47], v2 offset:256\nv_smfmac_f32_16x16x64_bf16 v[80:83], v[16:19], v[216:223], v10\nv_cvt_pk_bf16_f32 v72, v48, v49\nv_smfmac_f32_16x16x64_bf16 v[84:87], v[20:23], v[216:223], v10\nv_cvt_pk_bf16_f32 v73, v50, v51\nv_smfmac_f32_16x16x64_bf16 v[80:83], v[24:27], v[224:231], v10\nv_cvt_pk_bf16_f32 v74, v52, v53\nv_smfmac_f32_16x16x64_bf16 v[84:87], v[28:31], v[224:231], v10\n" \
"s_add_u32 s60, s55, 16384\ns_waitcnt lgkmcnt(2)\nv_cvt_pk_bf16_f32 v75, v54, v55\nv_smfmac_f32_16x16x64_bf16 v[80:83], v[32:35], v[64:71], v10\nv_and_or_b32 v72, v72, -2, v13\nv_smfmac_f32_16x16x64_bf16 v[84:87], v[36:39], v[64:71], v10\nv_and_or_b32 v74, v74, -2, v13\nv_smfmac_f32_16x16x64_bf16 v[80:83], v[88:91], v[248:255], v10\n" \
"buffer_store_dwordx4 v[72:75], v7, %[rs], s60 offen offset:2560 sc1\nv_mov_b64 v[48:49], 0\nv_mov_b64 v[50:51], 0\nv_mov_b64 v[52:53], 0\nv_mov_b64 v[54:55], 0\nv_smfmac_f32_16x16x64_bf16 v[84:87], v[92:95], v[248:255], v10\ns_add_u32 s60, s55, 20480\ns_cmp_lt_i32 s40, 63\n" \
"v_mov_b32 v15, s40\ns_cbranch_scc1 .Lvu_tok%=\nv_cvt_pk_bf16_f32 v76, v80, v81\nv_cvt_pk_bf16_f32 v77, v82, v83\ns_nop 1\nv_cvt_pk_bf16_f32 v78, v84, v85\nv_cvt_pk_bf16_f32 v79, v86, v87\nv_and_or_b32 v76, v76, -2, v13\n" \
"v_and_or_b32 v78, v78, -2, v13\nbuffer_store_dwordx4 v[76:79], v7, %[rs], s60 offen offset:2560 sc1\nbuffer_load_dwordx4 v[80:83], v3, %[rs], s42 offen offset:0 sc1\nbuffer_load_dwordx4 v[84:87], v3, %[rs], s42 offen offset:1024 sc1\nbuffer_load_dwordx4 v[88:91], v3, %[rs], s42 offen offset:2048 sc1\nbuffer_load_dwordx4 v[92:95], v3, %[rs], s42 offen offset:3072 sc1\ns_mov_b32 s61, -2147483648\ns_waitcnt lgkmcnt(0)\n" \
"s_branch .Lvu_crit%=\n.Lvu_re3%=:\nv_max_u32 v14, v80, v82\nv_cmp_eq_u32 s[46:47], -1, v14\nv_max_u32 v14, v84, v86\nv_cmp_eq_u32 s[48:49], -1, v14\nv_max_u32 v14, v88, v90\nv_cmp_eq_u32 s[50:51], -1, v14\n" \
"v_max_u32 v14, v92, v94\nv_cmp_eq_u32 s[52:53], -1, v14\ns_mov_b64 exec, s[46:47]\nbuffer_load_dwordx4 v[80:83], v3, %[rs], s42 offen offset:0 sc1\ns_mov_b64 exec, s[48:49]\nbuffer_load_dwordx4 v[84:87], v3, %[rs], s42 offen offset:1024 sc1\ns_mov_b64 exec, s[50:51]\nbuffer_load_dwordx4 v[88:91], v3, %[rs], s42 offen offset:2048 sc1\n" \
"s_mov_b64 exec, s[52:53]\nbuffer_load_dwordx4 v[92:95], v3, %[rs], s42 offen offset:3072 sc1\ns_mov_b64 exec, -1\nv_mov_b64 v[48:49], 0\nv_mov_b64 v[50:51], 0\nv_mov_b64 v[52:53], 0\nv_mov_b64 v[54:55], 0\ns_add_u32 s43, s43, 1\n" \
"s_cmp_lt_u32 s43, 262144\ns_cbranch_scc1 .Lvu_poll3%=\ns_branch .Lvu_abort%=\n.Lvu_slow%=:\ns_cmp_ge_i32 s40, 63\ns_cbranch_scc1 .Lvu_slowr%=\n.Lvu_f1%=:\nbuffer_load_dword v14, v8, %[rs], s56 offen sc1\n" \
"s_waitcnt vmcnt(0)\nv_cmp_ge_i32 vcc, s40, v14\ns_cbranch_vccz .Lvu_f1fok%=\ns_add_u32 s43, s43, 1\ns_cmp_lt_u32 s43, 262144\ns_cbranch_scc1 .Lvu_f1%=\ns_branch .Lvu_abort%=\n.Lvu_f1fok%=:\n" \
"s_add_u32 m0, s57, 0\ns_add_u32 s58, s41, 0\nbuffer_load_dwordx4 v0, %[rs], s58 offen offset:0 sc1 lds\nbuffer_load_dwordx4 v0, %[rs], s58 offen offset:1024 sc1 lds\nbuffer_load_dwordx4 v0, %[rs], s58 offen offset:2048 sc1 lds\nbuffer_load_dwordx4 v0, %[rs], s58 offen offset:3072 sc1 lds\ns_add_u32 m0, s57, 32768\ns_add_u32 s58, s41, 32768\n" \
"buffer_load_dwordx4 v0, %[rs], s58 offen offset:0 sc1 lds\nbuffer_load_dwordx4 v0, %[rs], s58 offen offset:1024 sc1 lds\nbuffer_load_dwordx4 v0, %[rs], s58 offen offset:2048 sc1 lds\nbuffer_load_dwordx4 v0, %[rs], s58 offen offset:3072 sc1 lds\ns_waitcnt vmcnt(0)\ns_branch .Lvu_slowr%=\n.Lvu_first%=:\ns_waitcnt vmcnt(0) lgkmcnt(0)\n" \
"s_barrier\nds_read_b128 v[56:59], v1 offset:8192\nds_read_b128 v[60:63], v1 offset:9216\nds_read_b128 v[64:67], v1 offset:10240\nds_read_b128 v[68:71], v1 offset:11264\nds_read_b128 v[16:19], v11 offset:49152\nds_read_b128 v[20:23], v11 offset:50176\nds_read_b128 v[24:27], v11 offset:51200\n" \
"ds_read_b128 v[28:31], v11 offset:52224\nds_read_b128 v[32:35], v11 offset:53248\nds_read_b128 v[36:39], v11 offset:54272\nds_read_b128 v[88:91], v11 offset:55296\nds_read_b128 v[92:95], v11 offset:56320\ns_add_u32 m0, s57, 0\ns_add_u32 s58, s61, 0\nbuffer_load_dwordx4 v0, %[rs], s58 offen offset:0 sc1 lds\n" \
"buffer_load_dwordx4 v0, %[rs], s58 offen offset:1024 sc1 lds\nbuffer_load_dwordx4 v0, %[rs], s58 offen offset:2048 sc1 lds\nbuffer_load_dwordx4 v0, %[rs], s58 offen offset:3072 sc1 lds\ns_mov_b32 s59, -2147483648\nbuffer_store_dword v14, off, %[rs], s59\nbuffer_store_dword v14, off, %[rs], s59\nbuffer_store_dword v14, off, %[rs], s59\ns_waitcnt lgkmcnt(0)\n" \
"s_branch .Lvu_y%=\n.Lvu_last%=:\ns_nop 7\nv_and_or_b32 v76, v76, -2, v12\nv_cvt_pk_bf16_f32 v78, v44, v45\nv_cvt_pk_bf16_f32 v79, v46, v47\nv_and_or_b32 v78, v78, -2, v12\ns_nop 0\n" \
"v_permlane16_swap_b32 v77, v79\nv_permlane16_swap_b32 v76, v78\nbuffer_store_dwordx4 v[76:79], v6, %[rs], s59 offen sc1\ns_branch .Lvu_end%=\n.Lvu_abort%=:\ns_mov_b64 exec, -1\nv_mov_b32 v16, 0\ns_movk_i32 s44, 6400\n" \
"buffer_store_dword v16, off, %[rs], s44 sc1\n.Lvu_end%=:\ns_waitcnt vmcnt(0) lgkmcnt(0)\n" \
""
#define RD_ASM \
"v_mbcnt_lo_u32_b32 v255, -1, 0\nv_mbcnt_hi_u32_b32 v255, -1, v255\nv_lshlrev_b32 v255, 2, v255\nv_add_u32 v255, %[tb], v255\nds_read_b32 v196, v255 offset:0\nds_read_b32 v197, v255 offset:256\nds_read_b32 v198, v255 offset:512\nds_read_b32 v199, v255 offset:768\n" \
"ds_read_b32 v200, v255 offset:1024\nds_read_b32 v201, v255 offset:1280\nds_read_b32 v202, v255 offset:1536\nds_read_b32 v203, v255 offset:1792\nds_read_b32 v204, v255 offset:2048\nds_read_b32 v205, v255 offset:2304\nds_read_b32 v206, v255 offset:2560\ns_waitcnt lgkmcnt(0)\n" \
"v_accvgpr_write_b32 a32, v206\ns_mov_b32 s40, 0\ns_mov_b32 s43, 0\ns_add_u32 s57, %[su], 4096\ns_add_u32 s56, %[sp], 4096\ns_mov_b32 s41, %[sy]\n.Lrd_yflag%=:\nv_accvgpr_read_b32 v206, a32\n" \
"buffer_load_dword v208, v206, %[rs], 0 offen sc1\ns_waitcnt vmcnt(0)\nv_cmp_gt_i32 vcc, s40, v208\ns_cbranch_vccz .Lrd_yld%=\ns_add_u32 s43, s43, 1\ns_cmp_lt_u32 s43, 262144\ns_cbranch_scc1 .Lrd_yflag%=\ns_branch .Lrd_abort%=\n" \
".Lrd_yld%=:\ns_add_u32 s60, s41, 0\nbuffer_load_dwordx4 v[0:3], v196, %[rs], s60 offen offset:0 sc1\ns_add_u32 s60, s41, 0\nbuffer_load_dwordx4 v[32:35], v196, %[rs], s60 offen offset:1024 sc1\ns_add_u32 s60, s41, 0\nbuffer_load_dwordx4 v[64:67], v196, %[rs], s60 offen offset:2048 sc1\ns_add_u32 s60, s41, 131072\n" \
"buffer_load_dwordx4 v[4:7], v196, %[rs], s60 offen offset:0 sc1\ns_add_u32 s60, s41, 131072\nbuffer_load_dwordx4 v[36:39], v196, %[rs], s60 offen offset:1024 sc1\ns_add_u32 s60, s41, 131072\nbuffer_load_dwordx4 v[68:71], v196, %[rs], s60 offen offset:2048 sc1\ns_add_u32 s60, s41, 262144\nbuffer_load_dwordx4 v[8:11], v196, %[rs], s60 offen offset:0 sc1\ns_add_u32 s60, s41, 262144\n" \
"buffer_load_dwordx4 v[40:43], v196, %[rs], s60 offen offset:1024 sc1\ns_add_u32 s60, s41, 262144\nbuffer_load_dwordx4 v[72:75], v196, %[rs], s60 offen offset:2048 sc1\ns_add_u32 s60, s41, 393216\nbuffer_load_dwordx4 v[12:15], v196, %[rs], s60 offen offset:0 sc1\ns_add_u32 s60, s41, 393216\nbuffer_load_dwordx4 v[44:47], v196, %[rs], s60 offen offset:1024 sc1\ns_add_u32 s60, s41, 393216\n" \
"buffer_load_dwordx4 v[76:79], v196, %[rs], s60 offen offset:2048 sc1\ns_add_u32 s60, s41, 524288\nbuffer_load_dwordx4 v[16:19], v196, %[rs], s60 offen offset:0 sc1\ns_add_u32 s60, s41, 524288\nbuffer_load_dwordx4 v[48:51], v196, %[rs], s60 offen offset:1024 sc1\ns_add_u32 s60, s41, 524288\nbuffer_load_dwordx4 v[80:83], v196, %[rs], s60 offen offset:2048 sc1\ns_add_u32 s60, s41, 655360\n" \
"buffer_load_dwordx4 v[20:23], v196, %[rs], s60 offen offset:0 sc1\ns_add_u32 s60, s41, 655360\nbuffer_load_dwordx4 v[52:55], v196, %[rs], s60 offen offset:1024 sc1\ns_add_u32 s60, s41, 655360\nbuffer_load_dwordx4 v[84:87], v196, %[rs], s60 offen offset:2048 sc1\ns_add_u32 s60, s41, 786432\nbuffer_load_dwordx4 v[24:27], v196, %[rs], s60 offen offset:0 sc1\ns_add_u32 s60, s41, 786432\n" \
"buffer_load_dwordx4 v[56:59], v196, %[rs], s60 offen offset:1024 sc1\ns_add_u32 s60, s41, 786432\nbuffer_load_dwordx4 v[88:91], v196, %[rs], s60 offen offset:2048 sc1\ns_add_u32 s60, s41, 917504\nbuffer_load_dwordx4 v[28:31], v196, %[rs], s60 offen offset:0 sc1\ns_add_u32 s60, s41, 917504\nbuffer_load_dwordx4 v[60:63], v196, %[rs], s60 offen offset:1024 sc1\ns_add_u32 s60, s41, 917504\n" \
"buffer_load_dwordx4 v[92:95], v196, %[rs], s60 offen offset:2048 sc1\ns_add_u32 s60, s41, 0\nbuffer_load_dwordx4 v[96:99], v196, %[rs], s60 offen offset:3072 sc1\ns_add_u32 s60, s41, 131072\nbuffer_load_dwordx4 v[100:103], v196, %[rs], s60 offen offset:3072 sc1\ns_add_u32 s60, s41, 262144\nbuffer_load_dwordx4 v[104:107], v196, %[rs], s60 offen offset:3072 sc1\ns_add_u32 s60, s41, 393216\n" \
"buffer_load_dwordx4 v[108:111], v196, %[rs], s60 offen offset:3072 sc1\ns_add_u32 s60, s41, 524288\nbuffer_load_dwordx4 v[112:115], v196, %[rs], s60 offen offset:3072 sc1\ns_add_u32 s60, s41, 655360\nbuffer_load_dwordx4 v[116:119], v196, %[rs], s60 offen offset:3072 sc1\ns_add_u32 s60, s41, 786432\nbuffer_load_dwordx4 v[120:123], v196, %[rs], s60 offen offset:3072 sc1\ns_add_u32 s60, s41, 917504\n" \
"buffer_load_dwordx4 v[124:127], v196, %[rs], s60 offen offset:3072 sc1\ns_mov_b64 exec, -1\nbuffer_load_dwordx4 v[136:139], v196, %[rs], %[su] offen offset:2048 sc1\nbuffer_load_dwordx4 v[140:143], v196, %[rs], %[su] offen offset:3072 sc1\ns_waitcnt vmcnt(0)\ns_branch .Lrd_ychk%=\n.Lrd_u2%=:\nbuffer_load_dwordx4 v[136:139], v196, %[rs], %[su] offen offset:2048 sc1\n" \
"buffer_load_dwordx4 v[140:143], v196, %[rs], %[su] offen offset:3072 sc1\ns_add_u32 s43, s43, 1\ns_cmp_ge_u32 s43, 262144\ns_cbranch_scc1 .Lrd_abort%=\ns_waitcnt vmcnt(0)\nv_bitop3_b32 v208, v136, v138, s54 bitop3:0x7e\nv_bitop3_b32 v209, v140, v142, s54 bitop3:0x7e\nv_bitop3_b32 v208, v208, 1, v209 bitop3:0xc8\n" \
"v_cmp_ne_u32 vcc, 0, v208\ns_cbranch_vccnz .Lrd_u2%=\n.Lrd_u3%=:\nbuffer_load_dwordx4 v[128:131], v196, %[rs], %[su] offen offset:0 sc1\nbuffer_load_dwordx4 v[132:135], v196, %[rs], %[su] offen offset:1024 sc1\nbuffer_load_dwordx4 v[144:147], v196, %[rs], s57 offen offset:0 sc1\nbuffer_load_dwordx4 v[148:151], v196, %[rs], s57 offen offset:1024 sc1\nbuffer_load_dwordx4 v[152:155], v196, %[rs], s57 offen offset:2048 sc1\n" \
"buffer_load_dwordx4 v[156:159], v196, %[rs], s57 offen offset:3072 sc1\nbuffer_load_dwordx4 v[160:163], v196, %[rs], %[sp] offen offset:0 sc1\nbuffer_load_dwordx4 v[164:167], v196, %[rs], %[sp] offen offset:1024 sc1\nbuffer_load_dwordx4 v[168:171], v196, %[rs], %[sp] offen offset:2048 sc1\nbuffer_load_dwordx4 v[172:175], v196, %[rs], %[sp] offen offset:3072 sc1\nbuffer_load_dwordx4 v[176:179], v196, %[rs], s56 offen offset:0 sc1\n.Lrd_ra%=:\ns_waitcnt vmcnt(5)\n" \
"v_bitop3_b32 v208, v128, v130, s54 bitop3:0x7e\nv_bitop3_b32 v209, v132, v134, s54 bitop3:0x7e\nv_bitop3_b32 v210, v144, v146, s54 bitop3:0x7e\nv_bitop3_b32 v211, v148, v150, s54 bitop3:0x7e\nv_or3_b32 v208, v208, v209, v210\nv_bitop3_b32 v209, v152, v154, s54 bitop3:0x7e\nv_bitop3_b32 v210, v156, v158, s54 bitop3:0x7e\nv_or3_b32 v208, v208, v209, v211\n" \
"v_bitop3_b32 v208, v208, 1, v210 bitop3:0xc8\nv_cmp_ne_u32 s[62:63], 0, v208\ns_waitcnt vmcnt(4)\nv_smfmac_f32_16x16x64_bf16 v[180:183], v[160:163], v[0:7], v197\nv_bitop3_b32 v208, v160, v162, s54 bitop3:0x7e\nv_smfmac_f32_16x16x64_bf16 v[184:187], v[160:163], v[32:39], v197\nv_cmp_gt_i32 s[46:47], 0, v208\nv_smfmac_f32_16x16x64_bf16 v[188:191], v[160:163], v[64:71], v197\n" \
"v_smfmac_f32_16x16x64_bf16 v[192:195], v[160:163], v[96:103], v197\ns_or_b64 vcc, s[62:63], s[46:47]\ns_waitcnt vmcnt(3)\nv_smfmac_f32_16x16x64_bf16 v[180:183], v[164:167], v[8:15], v197\nv_bitop3_b32 v208, v164, v166, s54 bitop3:0x7e\nv_smfmac_f32_16x16x64_bf16 v[184:187], v[164:167], v[40:47], v197\nv_cmp_gt_i32 s[48:49], 0, v208\nv_smfmac_f32_16x16x64_bf16 v[188:191], v[164:167], v[72:79], v197\n" \
"v_smfmac_f32_16x16x64_bf16 v[192:195], v[164:167], v[104:111], v197\ns_or_b64 vcc, vcc, s[48:49]\ns_waitcnt vmcnt(2)\nv_smfmac_f32_16x16x64_bf16 v[180:183], v[168:171], v[16:23], v197\nv_bitop3_b32 v208, v168, v170, s54 bitop3:0x7e\nv_smfmac_f32_16x16x64_bf16 v[184:187], v[168:171], v[48:55], v197\nv_cmp_gt_i32 s[50:51], 0, v208\nv_smfmac_f32_16x16x64_bf16 v[188:191], v[168:171], v[80:87], v197\n" \
"v_smfmac_f32_16x16x64_bf16 v[192:195], v[168:171], v[112:119], v197\ns_or_b64 vcc, vcc, s[50:51]\ns_waitcnt vmcnt(1)\nv_smfmac_f32_16x16x64_bf16 v[180:183], v[172:175], v[24:31], v197\ns_waitcnt vmcnt(0)\nv_bitop3_b32 v208, v172, v174, s54 bitop3:0x7e\nv_smfmac_f32_16x16x64_bf16 v[180:183], v[176:179], v[128:135], v197\nv_bitop3_b32 v209, v176, v178, s54 bitop3:0x7e\n" \
"v_smfmac_f32_16x16x64_bf16 v[184:187], v[172:175], v[56:63], v197\nv_bitop3_b32 v210, v177, v179, s54 bitop3:0x7e\nv_smfmac_f32_16x16x64_bf16 v[184:187], v[176:179], v[136:143], v197\nv_cmp_gt_i32 s[52:53], 0, v208\nv_smfmac_f32_16x16x64_bf16 v[188:191], v[172:175], v[88:95], v197\nv_or_b32 v209, v209, v210\nv_smfmac_f32_16x16x64_bf16 v[188:191], v[176:179], v[144:151], v197\ns_or_b64 vcc, vcc, s[52:53]\n" \
"v_cmp_gt_i32 s[64:65], 0, v209\nv_smfmac_f32_16x16x64_bf16 v[192:195], v[172:175], v[120:127], v197\nv_smfmac_f32_16x16x64_bf16 v[192:195], v[176:179], v[152:159], v197\ns_or_b64 vcc, vcc, s[64:65]\ns_cbranch_scc1 .Lrd_rb%=\ns_add_u32 s60, s58, 16384\nv_add_f32 v180, v180, v182\nv_add_f32 v181, v181, v183\n" \
"ds_write2_b32 v198, v180, v181 offset0:0 offset1:8\nbuffer_load_dwordx4 v[212:215], v196, %[rs], s60 offen offset:0 sc1\nv_add_f32 v184, v184, v186\nv_add_f32 v185, v185, v187\nds_write2_b32 v198, v184, v185 offset0:32 offset1:40\nv_add_f32 v188, v188, v190\nv_add_f32 v189, v189, v191\nds_write2_b32 v198, v188, v189 offset0:64 offset1:72\n" \
"s_add_u32 s60, s58, 0\nbuffer_load_dwordx4 v[216:219], v196, %[rs], s60 offen offset:0 sc1\nv_add_f32 v192, v192, v194\nv_add_f32 v193, v193, v195\nds_write2_b32 v198, v192, v193 offset0:96 offset1:104\ns_waitcnt lgkmcnt(0)\ns_barrier\nds_read_b128 v[180:183], v199 offset:0\n" \
"ds_read_b128 v[184:187], v199 offset:4096\nds_read_b128 v[188:191], v199 offset:8192\nds_read_b128 v[192:195], v199 offset:12288\ns_and_b32 s44, s54, 0x80008000\ns_add_u32 s60, s58, 0\nbuffer_load_dwordx4 v[220:223], v196, %[rs], s60 offen offset:1024 sc1\ns_waitcnt lgkmcnt(2)\nv_pk_add_f32 v[182:183], v[182:183], v[186:187]\n" \
"v_pk_add_f32 v[180:181], v[180:181], v[184:185]\ns_waitcnt lgkmcnt(0)\nv_pk_add_f32 v[186:187], v[188:189], v[192:193]\nv_pk_add_f32 v[184:185], v[190:191], v[194:195]\nv_pk_add_f32 v[180:181], v[180:181], v[186:187]\nv_pk_add_f32 v[182:183], v[182:183], v[184:185]\nv_cvt_pk_bf16_f32 v180, v180, v181\nv_xor_b32 v180, s44, v180\n" \
"v_cvt_pk_bf16_f32 v181, v182, v183\nv_min_u32 v180, -2, v180\nv_xor_b32 v209, s44, v181\nv_or_b32 v208, v180, v201\nbuffer_store_dwordx2 v[208:209], v206, %[rs], 0 offen sc1\ns_add_u32 s60, s58, 16384\nbuffer_load_dwordx4 v[224:227], v196, %[rs], s60 offen offset:1024 sc1\ns_add_u32 s60, s58, 0\n" \
"buffer_load_dwordx4 v[228:231], v196, %[rs], s60 offen offset:2048 sc1\ns_add_u32 s60, s58, 0\nbuffer_load_dwordx4 v[232:235], v196, %[rs], s60 offen offset:3072 sc1\ns_add_u32 s60, s58, 16384\nbuffer_load_dwordx4 v[236:239], v196, %[rs], s60 offen offset:2048 sc1\ns_add_u32 s60, s58, 4096\nbuffer_load_dwordx4 v[240:243], v196, %[rs], s60 offen offset:0 sc1\ns_add_u32 s60, s58, 4096\n" \
"buffer_load_dwordx4 v[244:247], v196, %[rs], s60 offen offset:1024 sc1\ns_add_u32 s60, s58, 16384\nbuffer_load_dwordx4 v[248:251], v196, %[rs], s60 offen offset:3072 sc1\ns_add_u32 s60, s58, 20480\nbuffer_load_dwordx4 v[252:255], v196, %[rs], s60 offen offset:0 sc1\ns_add_u32 s60, s58, 20480\nbuffer_load_dwordx4 v[160:163], v196, %[rs], s60 offen offset:2048 sc1\ns_xor_b32 s41, s41, 4194304\n" \
"v_mov_b32 v207, s40\ns_waitcnt vmcnt(12)\nv_bitop3_b32 v209, v212, v214, s61 bitop3:0x7e\nv_cmp_gt_i32 vcc, 0, v209\ns_cbranch_vccnz .Lrd_tre0%=\n.Lrd_tl0%=:\nv_mfma_f32_16x16x16_bf16 a[0:3], v[0:1], v[212:213], 0\ns_add_u32 s60, s58, 4096\n" \
"buffer_load_dwordx4 v[164:167], v196, %[rs], s60 offen offset:2048 sc1\nv_mfma_f32_16x16x16_bf16 a[8:11], v[32:33], v[212:213], 0\nv_mfma_f32_16x16x16_bf16 a[16:19], v[64:65], v[212:213], 0\nv_mfma_f32_16x16x16_bf16 a[24:27], v[96:97], v[212:213], 0\ns_waitcnt vmcnt(12)\nv_mfma_f32_16x16x16_bf16 a[0:3], v[4:5], v[214:215], a[0:3]\ns_add_u32 s60, s58, 4096\nbuffer_load_dwordx4 v[168:171], v196, %[rs], s60 offen offset:3072 sc1\n" \
"v_bitop3_b32 v209, v216, v218, s61 bitop3:0x7e\nv_mfma_f32_16x16x16_bf16 a[8:11], v[36:37], v[214:215], a[8:11]\ns_lshl_b32 s42, s40, 17\ns_add_u32 s42, s42, %[at]\nv_mfma_f32_16x16x16_bf16 a[16:19], v[68:69], v[214:215], a[16:19]\nv_cmp_gt_i32 vcc, 0, v209\nv_mfma_f32_16x16x16_bf16 a[24:27], v[100:101], v[214:215], a[24:27]\ns_cbranch_vccnz .Lrd_tre1%=\n" \
".Lrd_tl1%=:\ns_waitcnt vmcnt(12)\nv_mfma_f32_16x16x32_bf16 a[4:7], v[0:3], v[216:219], 0\ns_add_u32 s60, s58, 20480\nbuffer_load_dwordx4 v[172:175], v196, %[rs], s60 offen offset:1024 sc1\nv_bitop3_b32 v209, v220, v222, s61 bitop3:0x7e\nv_mfma_f32_16x16x32_bf16 a[12:15], v[32:35], v[216:219], 0\ns_lshr_b32 s45, s42, 8\n" \
"s_and_b32 s45, s45, 0x2000\nv_mfma_f32_16x16x32_bf16 a[20:23], v[64:67], v[216:219], 0\nv_cmp_gt_i32 vcc, 0, v209\nv_mfma_f32_16x16x32_bf16 a[28:31], v[96:99], v[216:219], 0\ns_cbranch_vccnz .Lrd_tre2%=\n.Lrd_tl2%=:\ns_waitcnt vmcnt(11)\nv_mfma_f32_16x16x32_bf16 a[4:7], v[4:7], v[220:223], a[4:7]\n" \
"s_add_u32 s60, s58, 20480\nbuffer_load_dwordx4 v[176:179], v196, %[rs], s60 offen offset:3072 sc1\nv_bitop3_b32 v209, v224, v226, s61 bitop3:0x7e\nv_mfma_f32_16x16x32_bf16 a[12:15], v[36:39], v[220:223], a[12:15]\ns_xor_b32 s42, s42, s45\ns_add_u32 s40, s40, 1\nv_mfma_f32_16x16x32_bf16 a[20:23], v[68:71], v[220:223], a[20:23]\nv_cmp_gt_i32 vcc, 0, v209\n" \
"v_mfma_f32_16x16x32_bf16 a[28:31], v[100:103], v[220:223], a[28:31]\ns_cbranch_vccnz .Lrd_tre3%=\n.Lrd_tl3%=:\nv_mfma_f32_16x16x16_bf16 a[0:3], v[8:9], v[224:225], a[0:3]\ns_add_u32 s60, s41, 0\nbuffer_load_dwordx4 v[0:3], v196, %[rs], s60 offen offset:0 sc1\nv_mfma_f32_16x16x16_bf16 a[8:11], v[40:41], v[224:225], a[8:11]\nv_mfma_f32_16x16x16_bf16 a[16:19], v[72:73], v[224:225], a[16:19]\n" \
"buffer_load_dwordx4 v[212:215], v204, %[rs], s42 offen sc1\nv_mfma_f32_16x16x16_bf16 a[24:27], v[104:105], v[224:225], a[24:27]\ns_waitcnt vmcnt(13)\nv_mfma_f32_16x16x16_bf16 a[0:3], v[12:13], v[226:227], a[0:3]\ns_add_u32 s60, s41, 0\nbuffer_load_dwordx4 v[32:35], v196, %[rs], s60 offen offset:1024 sc1\nv_bitop3_b32 v209, v228, v230, s61 bitop3:0x7e\nv_mfma_f32_16x16x16_bf16 a[8:11], v[44:45], v[226:227], a[8:11]\n" \
"s_lshl_b32 s62, s55, 31\ns_or_b32 s62, s62, 1.0\ns_mov_b32 s63, s62\nv_mfma_f32_16x16x16_bf16 a[16:19], v[76:77], v[226:227], a[16:19]\nv_cmp_gt_i32 vcc, 0, v209\nv_mfma_f32_16x16x16_bf16 a[24:27], v[108:109], v[226:227], a[24:27]\ns_cbranch_vccnz .Lrd_tre4%=\n.Lrd_tl4%=:\n" \
"s_waitcnt vmcnt(13)\nv_mfma_f32_16x16x32_bf16 a[4:7], v[8:11], v[228:231], a[4:7]\ns_add_u32 s60, s41, 0\nbuffer_load_dwordx4 v[64:67], v196, %[rs], s60 offen offset:2048 sc1\nv_bitop3_b32 v209, v232, v234, s61 bitop3:0x7e\nv_mfma_f32_16x16x32_bf16 a[12:15], v[40:43], v[228:231], a[12:15]\nv_mfma_f32_16x16x32_bf16 a[20:23], v[72:75], v[228:231], a[20:23]\nv_cmp_gt_i32 vcc, 0, v209\n" \
"v_mfma_f32_16x16x32_bf16 a[28:31], v[104:107], v[228:231], a[28:31]\ns_cbranch_vccnz .Lrd_tre5%=\n.Lrd_tl5%=:\ns_waitcnt vmcnt(13)\nv_mfma_f32_16x16x32_bf16 a[4:7], v[12:15], v[232:235], a[4:7]\ns_add_u32 s60, s41, 131072\nbuffer_load_dwordx4 v[4:7], v196, %[rs], s60 offen offset:0 sc1\nv_bitop3_b32 v209, v236, v238, s61 bitop3:0x7e\n" \
"v_mfma_f32_16x16x32_bf16 a[12:15], v[44:47], v[232:235], a[12:15]\nv_mfma_f32_16x16x32_bf16 a[20:23], v[76:79], v[232:235], a[20:23]\nv_cmp_gt_i32 vcc, 0, v209\nv_mfma_f32_16x16x32_bf16 a[28:31], v[108:111], v[232:235], a[28:31]\ns_cbranch_vccnz .Lrd_tre6%=\n.Lrd_tl6%=:\nv_mfma_f32_16x16x16_bf16 a[0:3], v[16:17], v[236:237], a[0:3]\ns_add_u32 s60, s41, 131072\n" \
"buffer_load_dwordx4 v[36:39], v196, %[rs], s60 offen offset:1024 sc1\nv_mfma_f32_16x16x16_bf16 a[8:11], v[48:49], v[236:237], a[8:11]\nv_mfma_f32_16x16x16_bf16 a[16:19], v[80:81], v[236:237], a[16:19]\nv_mfma_f32_16x16x16_bf16 a[24:27], v[112:113], v[236:237], a[24:27]\ns_waitcnt vmcnt(14)\nv_mfma_f32_16x16x16_bf16 a[0:3], v[20:21], v[238:239], a[0:3]\ns_add_u32 s60, s41, 131072\nbuffer_load_dwordx4 v[68:71], v196, %[rs], s60 offen offset:2048 sc1\n" \
"v_bitop3_b32 v209, v240, v242, s61 bitop3:0x7e\nv_mfma_f32_16x16x16_bf16 a[8:11], v[52:53], v[238:239], a[8:11]\nv_mfma_f32_16x16x16_bf16 a[16:19], v[84:85], v[238:239], a[16:19]\nv_cmp_gt_i32 vcc, 0, v209\nv_mfma_f32_16x16x16_bf16 a[24:27], v[116:117], v[238:239], a[24:27]\ns_cbranch_vccnz .Lrd_tre7%=\n.Lrd_tl7%=:\ns_waitcnt vmcnt(14)\n" \
"v_mfma_f32_16x16x32_bf16 a[4:7], v[16:19], v[240:243], a[4:7]\ns_add_u32 s60, s41, 262144\nbuffer_load_dwordx4 v[8:11], v196, %[rs], s60 offen offset:0 sc1\nv_bitop3_b32 v209, v244, v246, s61 bitop3:0x7e\nv_mfma_f32_16x16x32_bf16 a[12:15], v[48:51], v[240:243], a[12:15]\nv_mfma_f32_16x16x32_bf16 a[20:23], v[80:83], v[240:243], a[20:23]\nv_cmp_gt_i32 vcc, 0, v209\nv_mfma_f32_16x16x32_bf16 a[28:31], v[112:115], v[240:243], a[28:31]\n" \
"s_cbranch_vccnz .Lrd_tre8%=\n.Lrd_tl8%=:\ns_waitcnt vmcnt(14)\nv_mfma_f32_16x16x32_bf16 a[4:7], v[20:23], v[244:247], a[4:7]\ns_add_u32 s60, s41, 262144\nbuffer_load_dwordx4 v[40:43], v196, %[rs], s60 offen offset:1024 sc1\nv_bitop3_b32 v209, v248, v250, s61 bitop3:0x7e\nv_mfma_f32_16x16x32_bf16 a[12:15], v[52:55], v[244:247], a[12:15]\n" \
"v_mfma_f32_16x16x32_bf16 a[20:23], v[84:87], v[244:247], a[20:23]\nv_cmp_gt_i32 vcc, 0, v209\nv_mfma_f32_16x16x32_bf16 a[28:31], v[116:119], v[244:247], a[28:31]\ns_cbranch_vccnz .Lrd_tre9%=\n.Lrd_tl9%=:\nv_mfma_f32_16x16x16_bf16 a[0:3], v[24:25], v[248:249], a[0:3]\ns_add_u32 s60, s41, 262144\nbuffer_load_dwordx4 v[72:75], v196, %[rs], s60 offen offset:2048 sc1\n" \
"v_mfma_f32_16x16x16_bf16 a[8:11], v[56:57], v[248:249], a[8:11]\nv_mfma_f32_16x16x16_bf16 a[16:19], v[88:89], v[248:249], a[16:19]\nv_mfma_f32_16x16x16_bf16 a[24:27], v[120:121], v[248:249], a[24:27]\ns_waitcnt vmcnt(15)\nv_mfma_f32_16x16x16_bf16 a[0:3], v[28:29], v[250:251], a[0:3]\ns_add_u32 s60, s41, 393216\nbuffer_load_dwordx4 v[12:15], v196, %[rs], s60 offen offset:0 sc1\nv_bitop3_b32 v209, v252, v254, s61 bitop3:0x7e\n" \
"v_mfma_f32_16x16x16_bf16 a[8:11], v[60:61], v[250:251], a[8:11]\nv_bitop3_b32 v210, v253, v255, s61 bitop3:0x7e\nv_or_b32 v209, v209, v210\nv_mfma_f32_16x16x16_bf16 a[16:19], v[92:93], v[250:251], a[16:19]\nv_cmp_gt_i32 vcc, 0, v209\nv_mfma_f32_16x16x16_bf16 a[24:27], v[124:125], v[250:251], a[24:27]\ns_cbranch_vccnz .Lrd_tre10%=\n.Lrd_tl10%=:\n" \
"s_waitcnt vmcnt(15)\nv_mfma_f32_16x16x32_bf16 a[0:3], v[128:131], v[252:255], a[0:3]\ns_add_u32 s60, s41, 393216\nbuffer_load_dwordx4 v[44:47], v196, %[rs], s60 offen offset:1024 sc1\nv_bitop3_b32 v209, v160, v162, s61 bitop3:0x7e\nv_mfma_f32_16x16x32_bf16 a[8:11], v[136:139], v[252:255], a[8:11]\nv_bitop3_b32 v210, v161, v163, s61 bitop3:0x7e\nv_or_b32 v209, v209, v210\n" \
"v_mfma_f32_16x16x32_bf16 a[16:19], v[144:147], v[252:255], a[16:19]\nv_cmp_gt_i32 vcc, 0, v209\nv_mfma_f32_16x16x32_bf16 a[24:27], v[152:155], v[252:255], a[24:27]\ns_cbranch_vccnz .Lrd_tre11%=\n.Lrd_tl11%=:\ns_waitcnt vmcnt(15)\nv_mfma_f32_16x16x32_bf16 a[0:3], v[132:135], v[160:163], a[0:3]\ns_add_u32 s60, s41, 393216\n" \
"buffer_load_dwordx4 v[76:79], v196, %[rs], s60 offen offset:2048 sc1\nv_bitop3_b32 v209, v164, v166, s61 bitop3:0x7e\nv_mfma_f32_16x16x32_bf16 a[8:11], v[140:143], v[160:163], a[8:11]\nv_mfma_f32_16x16x32_bf16 a[16:19], v[148:151], v[160:163], a[16:19]\nv_cmp_gt_i32 vcc, 0, v209\nv_mfma_f32_16x16x32_bf16 a[24:27], v[156:159], v[160:163], a[24:27]\ns_cbranch_vccnz .Lrd_tre12%=\n.Lrd_tl12%=:\n" \
"s_waitcnt vmcnt(15)\nv_mfma_f32_16x16x32_bf16 a[4:7], v[24:27], v[164:167], a[4:7]\ns_add_u32 s60, s41, 524288\nbuffer_load_dwordx4 v[16:19], v196, %[rs], s60 offen offset:0 sc1\nv_bitop3_b32 v209, v168, v170, s61 bitop3:0x7e\nv_mfma_f32_16x16x32_bf16 a[12:15], v[56:59], v[164:167], a[12:15]\nv_mfma_f32_16x16x32_bf16 a[20:23], v[88:91], v[164:167], a[20:23]\nds_write_b128 v202, a[0:3] offset:0\n" \
"v_cmp_gt_i32 vcc, 0, v209\nv_mfma_f32_16x16x32_bf16 a[28:31], v[120:123], v[164:167], a[28:31]\ns_cbranch_vccnz .Lrd_tre13%=\n.Lrd_tl13%=:\ns_waitcnt vmcnt(15)\nv_mfma_f32_16x16x32_bf16 a[4:7], v[28:31], v[168:171], a[4:7]\ns_add_u32 s60, s41, 524288\nbuffer_load_dwordx4 v[48:51], v196, %[rs], s60 offen offset:1024 sc1\n" \
"v_bitop3_b32 v209, v172, v174, s61 bitop3:0x7e\nv_mfma_f32_16x16x32_bf16 a[12:15], v[60:63], v[168:171], a[12:15]\nv_bitop3_b32 v210, v173, v175, s61 bitop3:0x7e\nv_or_b32 v209, v209, v210\nv_mfma_f32_16x16x32_bf16 a[20:23], v[92:95], v[168:171], a[20:23]\nds_write_b128 v202, a[8:11] offset:2048\nv_cmp_gt_i32 vcc, 0, v209\nv_mfma_f32_16x16x32_bf16 a[28:31], v[124:127], v[168:171], a[28:31]\n" \
"s_cbranch_vccnz .Lrd_tre14%=\n.Lrd_tl14%=:\ns_waitcnt vmcnt(15)\nv_mfma_f32_16x16x32_bf16 a[4:7], v[128:131], v[172:175], a[4:7]\ns_add_u32 s60, s41, 524288\nbuffer_load_dwordx4 v[80:83], v196, %[rs], s60 offen offset:2048 sc1\nv_bitop3_b32 v209, v176, v178, s61 bitop3:0x7e\nv_mfma_f32_16x16x32_bf16 a[12:15], v[136:139], v[172:175], a[12:15]\n" \
"v_bitop3_b32 v210, v177, v179, s61 bitop3:0x7e\nv_or_b32 v209, v209, v210\nv_mfma_f32_16x16x32_bf16 a[20:23], v[144:147], v[172:175], a[20:23]\nds_write_b128 v202, a[16:19] offset:4096\nv_cmp_gt_i32 vcc, 0, v209\nv_mfma_f32_16x16x32_bf16 a[28:31], v[152:155], v[172:175], a[28:31]\ns_cbranch_vccnz .Lrd_tre15%=\n.Lrd_tl15%=:\n" \
"v_mfma_f32_16x16x32_bf16 a[4:7], v[132:135], v[176:179], a[4:7]\ns_add_u32 s60, s41, 655360\ns_waitcnt vmcnt(14)\nbuffer_load_dwordx4 v[20:23], v196, %[rs], s60 offen offset:0 sc1\nv_lshlrev_b32 v216, 16, v212\nv_and_b32 v217, 0xffff0000, v212\nv_mfma_f32_16x16x32_bf16 a[12:15], v[140:143], v[176:179], a[12:15]\nv_lshlrev_b32 v218, 16, v213\n" \
"v_and_b32 v219, 0xffff0000, v213\nv_mfma_f32_16x16x32_bf16 a[20:23], v[148:151], v[176:179], a[20:23]\nds_write_b128 v202, a[24:27] offset:6144\nv_lshlrev_b32 v220, 16, v214\nv_and_b32 v221, 0xffff0000, v214\nv_mfma_f32_16x16x32_bf16 a[28:31], v[156:159], v[176:179], a[28:31]\ns_add_u32 s60, s41, 655360\nbuffer_load_dwordx4 v[52:55], v196, %[rs], s60 offen offset:1024 sc1\n" \
"s_add_u32 s60, s41, 655360\nds_write_b128 v202, a[4:7] offset:1024\nbuffer_load_dwordx4 v[84:87], v196, %[rs], s60 offen offset:2048 sc1\nds_write_b128 v202, a[12:15] offset:3072\nv_lshlrev_b32 v222, 16, v215\nv_and_b32 v223, 0xffff0000, v215\ns_add_u32 s60, s41, 786432\nbuffer_load_dwordx4 v[24:27], v196, %[rs], s60 offen offset:0 sc1\n" \
"ds_write_b128 v202, a[20:23] offset:5120\ns_add_u32 s60, s41, 786432\nbuffer_load_dwordx4 v[56:59], v196, %[rs], s60 offen offset:1024 sc1\nds_write_b128 v202, a[28:31] offset:7168\ns_waitcnt lgkmcnt(0)\ns_barrier\nds_read_b128 v[160:163], v203 offset:0\nds_read_b128 v[164:167], v203 offset:256\n" \
"ds_read_b128 v[168:171], v203 offset:8192\nds_read_b128 v[172:175], v203 offset:8448\nds_read_b128 v[176:179], v203 offset:16384\nds_read_b128 v[180:183], v203 offset:16640\nds_read_b128 v[184:187], v203 offset:24576\nds_read_b128 v[188:191], v203 offset:24832\ns_add_u32 s60, s41, 786432\nbuffer_load_dwordx4 v[88:91], v196, %[rs], s60 offen offset:2048 sc1\n" \
"s_waitcnt lgkmcnt(4)\nv_pk_add_f32 v[160:161], v[160:161], v[168:169]\nv_pk_add_f32 v[162:163], v[162:163], v[170:171]\nv_pk_add_f32 v[164:165], v[164:165], v[172:173]\nv_pk_add_f32 v[166:167], v[166:167], v[174:175]\ns_add_u32 s60, s41, 917504\nbuffer_load_dwordx4 v[28:31], v196, %[rs], s60 offen offset:0 sc1\ns_waitcnt lgkmcnt(2)\n" \
"v_pk_add_f32 v[160:161], v[160:161], v[176:177]\nv_pk_add_f32 v[162:163], v[162:163], v[178:179]\nv_pk_add_f32 v[164:165], v[164:165], v[180:181]\nv_pk_add_f32 v[166:167], v[166:167], v[182:183]\ns_add_u32 s60, s41, 917504\nbuffer_load_dwordx4 v[60:63], v196, %[rs], s60 offen offset:1024 sc1\ns_waitcnt lgkmcnt(0)\nv_pk_add_f32 v[160:161], v[160:161], v[184:185]\n" \
"v_pk_add_f32 v[162:163], v[162:163], v[186:187]\nv_pk_add_f32 v[164:165], v[164:165], v[188:189]\nv_pk_add_f32 v[166:167], v[166:167], v[190:191]\nv_pk_fma_f32 v[160:161], v[160:161], s[62:63], v[216:217]\nv_pk_fma_f32 v[162:163], v[162:163], s[62:63], v[218:219]\nv_pk_fma_f32 v[164:165], v[164:165], s[62:63], v[220:221]\nv_pk_fma_f32 v[166:167], v[166:167], s[62:63], v[222:223]\nv_cvt_pk_bf16_f32 v208, v160, v161\n" \
"v_cvt_pk_bf16_f32 v209, v162, v163\nv_cvt_pk_bf16_f32 v210, v164, v165\nv_cvt_pk_bf16_f32 v211, v166, v167\nbuffer_store_dwordx4 v[208:211], v204, %[rs], s42 offen sc1\nv_pk_add_f32 v[168:169], v[160:161], v[162:163]\nv_pk_add_f32 v[170:171], v[164:165], v[166:167]\nv_pk_add_f32 v[168:169], v[168:169], v[170:171]\ns_add_u32 s60, s41, 917504\n" \
"buffer_load_dwordx4 v[92:95], v196, %[rs], s60 offen offset:2048 sc1\nv_add_f32 v168, v168, v169\ns_nop 1\nv_add_f32_dpp v169, v168, v168 row_ror:8 row_mask:0xf bank_mask:0xf\nv_add_f32_dpp v170, v168, v168 row_ror:8 row_mask:0xf bank_mask:0xf\ns_nop 1\nv_permlane16_swap_b32 v169, v170\nv_add_f32 v168, v169, v170\n" \
"v_add_f32 v169, v169, v170\ns_nop 1\nv_permlane32_swap_b32 v168, v169\nv_add_f32 v168, v168, v169\nv_mul_f32 v168, 0x3c800000, v168\nv_pk_add_f32 v[160:161], v[160:161], v[168:169] op_sel_hi:[1,0] neg_lo:[0,1] neg_hi:[0,1]\nv_pk_add_f32 v[162:163], v[162:163], v[168:169] op_sel_hi:[1,0] neg_lo:[0,1] neg_hi:[0,1]\nv_pk_add_f32 v[164:165], v[164:165], v[168:169] op_sel_hi:[1,0] neg_lo:[0,1] neg_hi:[0,1]\n" \
"v_pk_add_f32 v[166:167], v[166:167], v[168:169] op_sel_hi:[1,0] neg_lo:[0,1] neg_hi:[0,1]\ns_add_u32 s60, s41, 0\nbuffer_load_dwordx4 v[96:99], v196, %[rs], s60 offen offset:3072 sc1\nv_pk_mul_f32 v[172:173], v[160:161], v[160:161]\nv_pk_mul_f32 v[174:175], v[162:163], v[162:163]\nv_pk_fma_f32 v[172:173], v[164:165], v[164:165], v[172:173]\nv_pk_fma_f32 v[174:175], v[166:167], v[166:167], v[174:175]\nv_pk_add_f32 v[172:173], v[172:173], v[174:175]\n" \
"v_add_f32 v172, v172, v173\ns_nop 1\nv_add_f32_dpp v173, v172, v172 row_ror:8 row_mask:0xf bank_mask:0xf\nv_add_f32_dpp v174, v172, v172 row_ror:8 row_mask:0xf bank_mask:0xf\ns_nop 1\nv_permlane16_swap_b32 v173, v174\nv_add_f32 v172, v173, v174\nv_add_f32 v173, v173, v174\n" \
"s_nop 1\nv_permlane32_swap_b32 v172, v173\nv_add_f32 v172, v172, v173\nv_mov_b32 v169, v172\nbuffer_store_dwordx2 v[168:169], v205, %[rs], s42 offen sc1\ns_add_u32 s60, s41, 131072\nbuffer_load_dwordx4 v[100:103], v196, %[rs], s60 offen offset:3072 sc1\ns_add_u32 s60, s41, 262144\n" \
"buffer_load_dwordx4 v[104:107], v196, %[rs], s60 offen offset:3072 sc1\ns_add_u32 s60, s41, 393216\nbuffer_load_dwordx4 v[108:111], v196, %[rs], s60 offen offset:3072 sc1\ns_add_u32 s60, s41, 524288\nbuffer_load_dwordx4 v[112:115], v196, %[rs], s60 offen offset:3072 sc1\ns_add_u32 s60, s41, 655360\nbuffer_load_dwordx4 v[116:119], v196, %[rs], s60 offen offset:3072 sc1\ns_add_u32 s60, s41, 786432\n" \
"buffer_load_dwordx4 v[120:123], v196, %[rs], s60 offen offset:3072 sc1\ns_add_u32 s60, s41, 917504\nbuffer_load_dwordx4 v[124:127], v196, %[rs], s60 offen offset:3072 sc1\ns_waitcnt vmcnt(7)\nbuffer_store_dword v207, off, %[rs], %[fa] sc1\nbuffer_load_dwordx4 v[136:139], v196, %[rs], %[su] offen offset:2048 sc1\nbuffer_load_dwordx4 v[140:143], v196, %[rs], %[su] offen offset:3072 sc1\ns_cmp_ge_u32 s40, 64\n" \
"s_cbranch_scc1 .Lrd_end%=\ns_cmp_lt_u32 s40, 2\ns_cbranch_scc1 .Lrd_yflag%=\n.Lrd_ychk%=:\ns_and_b32 s44, s40, 1\ns_bfe_u32 s55, s40, 0x10001\ns_mul_i32 s54, s44, 0x80008001\ns_mul_i32 s61, s55, 0x80008000\n" \
"s_lshl_b32 s58, s44, 17\ns_add_u32 s58, s58, %[st]\ns_add_u32 s44, s40, 1\ns_and_b32 s44, s44, 3\ns_lshl_b32 s44, s44, 15\nv_xor_b32 v206, s44, v200\nv_mov_b64 v[180:181], 0\nv_mov_b64 v[182:183], 0\n" \
"v_mov_b64 v[184:185], 0\nv_mov_b64 v[186:187], 0\nv_mov_b64 v[188:189], 0\nv_mov_b64 v[190:191], 0\nv_mov_b64 v[192:193], 0\nv_mov_b64 v[194:195], 0\nv_bitop3_b32 v209, v0, v2, s55 bitop3:0x7e\nv_bitop3_b32 v210, v32, v34, s55 bitop3:0x7e\n" \
"v_or3_b32 v208, v209, v210, 0\nv_bitop3_b32 v209, v64, v66, s55 bitop3:0x7e\nv_bitop3_b32 v210, v4, v6, s55 bitop3:0x7e\nv_or3_b32 v208, v209, v210, v208\nv_bitop3_b32 v209, v36, v38, s55 bitop3:0x7e\nv_bitop3_b32 v210, v68, v70, s55 bitop3:0x7e\nv_or3_b32 v208, v209, v210, v208\nv_bitop3_b32 v209, v8, v10, s55 bitop3:0x7e\n" \
"v_bitop3_b32 v210, v40, v42, s55 bitop3:0x7e\nv_or3_b32 v208, v209, v210, v208\nv_bitop3_b32 v209, v72, v74, s55 bitop3:0x7e\nv_bitop3_b32 v210, v12, v14, s55 bitop3:0x7e\nv_or3_b32 v208, v209, v210, v208\nv_bitop3_b32 v209, v44, v46, s55 bitop3:0x7e\nv_bitop3_b32 v210, v76, v78, s55 bitop3:0x7e\nv_or3_b32 v208, v209, v210, v208\n" \
"v_bitop3_b32 v209, v16, v18, s55 bitop3:0x7e\nv_bitop3_b32 v210, v48, v50, s55 bitop3:0x7e\nv_or3_b32 v208, v209, v210, v208\nv_bitop3_b32 v209, v80, v82, s55 bitop3:0x7e\nv_bitop3_b32 v210, v20, v22, s55 bitop3:0x7e\nv_or3_b32 v208, v209, v210, v208\nv_bitop3_b32 v209, v52, v54, s55 bitop3:0x7e\nv_bitop3_b32 v210, v84, v86, s55 bitop3:0x7e\n" \
"v_or3_b32 v208, v209, v210, v208\nv_bitop3_b32 v209, v24, v26, s55 bitop3:0x7e\nv_bitop3_b32 v210, v56, v58, s55 bitop3:0x7e\nv_or3_b32 v208, v209, v210, v208\nv_bitop3_b32 v209, v88, v90, s55 bitop3:0x7e\nv_bitop3_b32 v210, v28, v30, s55 bitop3:0x7e\nv_or3_b32 v208, v209, v210, v208\nv_bitop3_b32 v209, v60, v62, s55 bitop3:0x7e\n" \
"v_bitop3_b32 v210, v92, v94, s55 bitop3:0x7e\nv_or3_b32 v208, v209, v210, v208\ns_waitcnt vmcnt(7)\nv_bitop3_b32 v209, v96, v98, s55 bitop3:0x7e\nv_bitop3_b32 v210, v100, v102, s55 bitop3:0x7e\nv_or3_b32 v208, v209, v210, v208\nv_bitop3_b32 v209, v104, v106, s55 bitop3:0x7e\nv_bitop3_b32 v210, v108, v110, s55 bitop3:0x7e\n" \
"v_or3_b32 v208, v209, v210, v208\ns_waitcnt vmcnt(3)\nv_bitop3_b32 v209, v112, v114, s55 bitop3:0x7e\nv_bitop3_b32 v210, v116, v118, s55 bitop3:0x7e\nv_or3_b32 v208, v209, v210, v208\nv_bitop3_b32 v209, v120, v122, s55 bitop3:0x7e\nv_bitop3_b32 v210, v124, v126, s55 bitop3:0x7e\nv_or3_b32 v208, v209, v210, v208\n" \
"v_and_b32 v208, 1, v208\nv_cmp_ne_u32 vcc, 0, v208\ns_cbranch_vccnz .Lrd_yre%=\ns_waitcnt vmcnt(0)\nv_bitop3_b32 v208, v136, v138, s54 bitop3:0x7e\nv_bitop3_b32 v209, v140, v142, s54 bitop3:0x7e\nv_bitop3_b32 v208, v208, 1, v209 bitop3:0xc8\nv_cmp_ne_u32 vcc, 0, v208\n" \
"s_cbranch_vccnz .Lrd_u2%=\ns_branch .Lrd_u3%=\n.Lrd_yre%=:\ns_mov_b64 exec, vcc\ns_add_u32 s43, s43, 1\ns_cmp_lt_u32 s43, 262144\ns_cbranch_scc1 .Lrd_yld%=\ns_branch .Lrd_abort%=\n" \
".Lrd_rb%=:\ns_mov_b64 exec, s[62:63]\nbuffer_load_dwordx4 v[128:131], v196, %[rs], %[su] offen offset:0 sc1\nbuffer_load_dwordx4 v[132:135], v196, %[rs], %[su] offen offset:1024 sc1\nbuffer_load_dwordx4 v[144:147], v196, %[rs], s57 offen offset:0 sc1\nbuffer_load_dwordx4 v[148:151], v196, %[rs], s57 offen offset:1024 sc1\nbuffer_load_dwordx4 v[152:155], v196, %[rs], s57 offen offset:2048 sc1\nbuffer_load_dwordx4 v[156:159], v196, %[rs], s57 offen offset:3072 sc1\n" \
"s_mov_b64 exec, s[46:47]\nbuffer_load_dwordx4 v[160:163], v196, %[rs], %[sp] offen offset:0 sc1\ns_mov_b64 exec, s[48:49]\nbuffer_load_dwordx4 v[164:167], v196, %[rs], %[sp] offen offset:1024 sc1\ns_mov_b64 exec, s[50:51]\nbuffer_load_dwordx4 v[168:171], v196, %[rs], %[sp] offen offset:2048 sc1\ns_mov_b64 exec, s[52:53]\nbuffer_load_dwordx4 v[172:175], v196, %[rs], %[sp] offen offset:3072 sc1\n" \
"s_mov_b64 exec, s[64:65]\nbuffer_load_dwordx4 v[176:179], v196, %[rs], s56 offen offset:0 sc1\ns_mov_b64 exec, -1\nv_mov_b64 v[180:181], 0\nv_mov_b64 v[182:183], 0\nv_mov_b64 v[184:185], 0\nv_mov_b64 v[186:187], 0\nv_mov_b64 v[188:189], 0\n" \
"v_mov_b64 v[190:191], 0\nv_mov_b64 v[192:193], 0\nv_mov_b64 v[194:195], 0\ns_add_u32 s43, s43, 1\ns_cmp_lt_u32 s43, 262144\ns_cbranch_scc1 .Lrd_ra%=\ns_branch .Lrd_abort%=\n.Lrd_tre0%=:\n" \
"s_add_u32 s60, s58, 16384\nbuffer_load_dwordx4 v[212:215], v196, %[rs], s60 offen offset:0 sc1\ns_waitcnt vmcnt(0)\nv_bitop3_b32 v209, v212, v214, s61 bitop3:0x7e\nv_cmp_gt_i32 vcc, 0, v209\ns_cbranch_vccz .Lrd_tl0%=\ns_add_u32 s43, s43, 1\ns_cmp_lt_u32 s43, 262144\n" \
"s_cbranch_scc1 .Lrd_tre0%=\ns_branch .Lrd_abort%=\n.Lrd_tre1%=:\ns_add_u32 s60, s58, 0\nbuffer_load_dwordx4 v[216:219], v196, %[rs], s60 offen offset:0 sc1\ns_waitcnt vmcnt(0)\nv_bitop3_b32 v209, v216, v218, s61 bitop3:0x7e\nv_cmp_gt_i32 vcc, 0, v209\n" \
"s_cbranch_vccz .Lrd_tl1%=\ns_add_u32 s43, s43, 1\ns_cmp_lt_u32 s43, 262144\ns_cbranch_scc1 .Lrd_tre1%=\ns_branch .Lrd_abort%=\n.Lrd_tre2%=:\ns_add_u32 s60, s58, 0\nbuffer_load_dwordx4 v[220:223], v196, %[rs], s60 offen offset:1024 sc1\n" \
"s_waitcnt vmcnt(0)\nv_bitop3_b32 v209, v220, v222, s61 bitop3:0x7e\nv_cmp_gt_i32 vcc, 0, v209\ns_cbranch_vccz .Lrd_tl2%=\ns_add_u32 s43, s43, 1\ns_cmp_lt_u32 s43, 262144\ns_cbranch_scc1 .Lrd_tre2%=\ns_branch .Lrd_abort%=\n" \
".Lrd_tre3%=:\ns_add_u32 s60, s58, 16384\nbuffer_load_dwordx4 v[224:227], v196, %[rs], s60 offen offset:1024 sc1\ns_waitcnt vmcnt(0)\nv_bitop3_b32 v209, v224, v226, s61 bitop3:0x7e\nv_cmp_gt_i32 vcc, 0, v209\ns_cbranch_vccz .Lrd_tl3%=\ns_add_u32 s43, s43, 1\n" \
"s_cmp_lt_u32 s43, 262144\ns_cbranch_scc1 .Lrd_tre3%=\ns_branch .Lrd_abort%=\n.Lrd_tre4%=:\ns_add_u32 s60, s58, 0\nbuffer_load_dwordx4 v[228:231], v196, %[rs], s60 offen offset:2048 sc1\ns_waitcnt vmcnt(0)\nv_bitop3_b32 v209, v228, v230, s61 bitop3:0x7e\n" \
"v_cmp_gt_i32 vcc, 0, v209\ns_cbranch_vccz .Lrd_tl4%=\ns_add_u32 s43, s43, 1\ns_cmp_lt_u32 s43, 262144\ns_cbranch_scc1 .Lrd_tre4%=\ns_branch .Lrd_abort%=\n.Lrd_tre5%=:\ns_add_u32 s60, s58, 0\n" \
"buffer_load_dwordx4 v[232:235], v196, %[rs], s60 offen offset:3072 sc1\ns_waitcnt vmcnt(0)\nv_bitop3_b32 v209, v232, v234, s61 bitop3:0x7e\nv_cmp_gt_i32 vcc, 0, v209\ns_cbranch_vccz .Lrd_tl5%=\ns_add_u32 s43, s43, 1\ns_cmp_lt_u32 s43, 262144\ns_cbranch_scc1 .Lrd_tre5%=\n" \
"s_branch .Lrd_abort%=\n.Lrd_tre6%=:\ns_add_u32 s60, s58, 16384\nbuffer_load_dwordx4 v[236:239], v196, %[rs], s60 offen offset:2048 sc1\ns_waitcnt vmcnt(0)\nv_bitop3_b32 v209, v236, v238, s61 bitop3:0x7e\nv_cmp_gt_i32 vcc, 0, v209\ns_cbranch_vccz .Lrd_tl6%=\n" \
"s_add_u32 s43, s43, 1\ns_cmp_lt_u32 s43, 262144\ns_cbranch_scc1 .Lrd_tre6%=\ns_branch .Lrd_abort%=\n.Lrd_tre7%=:\ns_add_u32 s60, s58, 4096\nbuffer_load_dwordx4 v[240:243], v196, %[rs], s60 offen offset:0 sc1\ns_waitcnt vmcnt(0)\n" \
"v_bitop3_b32 v209, v240, v242, s61 bitop3:0x7e\nv_cmp_gt_i32 vcc, 0, v209\ns_cbranch_vccz .Lrd_tl7%=\ns_add_u32 s43, s43, 1\ns_cmp_lt_u32 s43, 262144\ns_cbranch_scc1 .Lrd_tre7%=\ns_branch .Lrd_abort%=\n.Lrd_tre8%=:\n" \
"s_add_u32 s60, s58, 4096\nbuffer_load_dwordx4 v[244:247], v196, %[rs], s60 offen offset:1024 sc1\ns_waitcnt vmcnt(0)\nv_bitop3_b32 v209, v244, v246, s61 bitop3:0x7e\nv_cmp_gt_i32 vcc, 0, v209\ns_cbranch_vccz .Lrd_tl8%=\ns_add_u32 s43, s43, 1\ns_cmp_lt_u32 s43, 262144\n" \
"s_cbranch_scc1 .Lrd_tre8%=\ns_branch .Lrd_abort%=\n.Lrd_tre9%=:\ns_add_u32 s60, s58, 16384\nbuffer_load_dwordx4 v[248:251], v196, %[rs], s60 offen offset:3072 sc1\ns_waitcnt vmcnt(0)\nv_bitop3_b32 v209, v248, v250, s61 bitop3:0x7e\nv_cmp_gt_i32 vcc, 0, v209\n" \
"s_cbranch_vccz .Lrd_tl9%=\ns_add_u32 s43, s43, 1\ns_cmp_lt_u32 s43, 262144\ns_cbranch_scc1 .Lrd_tre9%=\ns_branch .Lrd_abort%=\n.Lrd_tre10%=:\ns_add_u32 s60, s58, 20480\nbuffer_load_dwordx4 v[252:255], v196, %[rs], s60 offen offset:0 sc1\n" \
"s_waitcnt vmcnt(0)\nv_bitop3_b32 v209, v252, v254, s61 bitop3:0x7e\nv_bitop3_b32 v210, v253, v255, s61 bitop3:0x7e\nv_or_b32 v209, v209, v210\nv_cmp_gt_i32 vcc, 0, v209\ns_cbranch_vccz .Lrd_tl10%=\ns_add_u32 s43, s43, 1\ns_cmp_lt_u32 s43, 262144\n" \
"s_cbranch_scc1 .Lrd_tre10%=\ns_branch .Lrd_abort%=\n.Lrd_tre11%=:\ns_add_u32 s60, s58, 20480\nbuffer_load_dwordx4 v[160:163], v196, %[rs], s60 offen offset:2048 sc1\ns_waitcnt vmcnt(0)\nv_bitop3_b32 v209, v160, v162, s61 bitop3:0x7e\nv_bitop3_b32 v210, v161, v163, s61 bitop3:0x7e\n" \
"v_or_b32 v209, v209, v210\nv_cmp_gt_i32 vcc, 0, v209\ns_cbranch_vccz .Lrd_tl11%=\ns_add_u32 s43, s43, 1\ns_cmp_lt_u32 s43, 262144\ns_cbranch_scc1 .Lrd_tre11%=\ns_branch .Lrd_abort%=\n.Lrd_tre12%=:\n" \
"s_add_u32 s60, s58, 4096\nbuffer_load_dwordx4 v[164:167], v196, %[rs], s60 offen offset:2048 sc1\ns_waitcnt vmcnt(0)\nv_bitop3_b32 v209, v164, v166, s61 bitop3:0x7e\nv_cmp_gt_i32 vcc, 0, v209\ns_cbranch_vccz .Lrd_tl12%=\ns_add_u32 s43, s43, 1\ns_cmp_lt_u32 s43, 262144\n" \
"s_cbranch_scc1 .Lrd_tre12%=\ns_branch .Lrd_abort%=\n.Lrd_tre13%=:\ns_add_u32 s60, s58, 4096\nbuffer_load_dwordx4 v[168:171], v196, %[rs], s60 offen offset:3072 sc1\ns_waitcnt vmcnt(0)\nv_bitop3_b32 v209, v168, v170, s61 bitop3:0x7e\nv_cmp_gt_i32 vcc, 0, v209\n" \
"s_cbranch_vccz .Lrd_tl13%=\ns_add_u32 s43, s43, 1\ns_cmp_lt_u32 s43, 262144\ns_cbranch_scc1 .Lrd_tre13%=\ns_branch .Lrd_abort%=\n.Lrd_tre14%=:\ns_add_u32 s60, s58, 20480\nbuffer_load_dwordx4 v[172:175], v196, %[rs], s60 offen offset:1024 sc1\n" \
"s_waitcnt vmcnt(0)\nv_bitop3_b32 v209, v172, v174, s61 bitop3:0x7e\nv_bitop3_b32 v210, v173, v175, s61 bitop3:0x7e\nv_or_b32 v209, v209, v210\nv_cmp_gt_i32 vcc, 0, v209\ns_cbranch_vccz .Lrd_tl14%=\ns_add_u32 s43, s43, 1\ns_cmp_lt_u32 s43, 262144\n" \
"s_cbranch_scc1 .Lrd_tre14%=\ns_branch .Lrd_abort%=\n.Lrd_tre15%=:\ns_add_u32 s60, s58, 20480\nbuffer_load_dwordx4 v[176:179], v196, %[rs], s60 offen offset:3072 sc1\ns_waitcnt vmcnt(0)\nv_bitop3_b32 v209, v176, v178, s61 bitop3:0x7e\nv_bitop3_b32 v210, v177, v179, s61 bitop3:0x7e\n" \
"v_or_b32 v209, v209, v210\nv_cmp_gt_i32 vcc, 0, v209\ns_cbranch_vccz .Lrd_tl15%=\ns_add_u32 s43, s43, 1\ns_cmp_lt_u32 s43, 262144\ns_cbranch_scc1 .Lrd_tre15%=\ns_branch .Lrd_abort%=\n.Lrd_abort%=:\n" \
"s_mov_b64 exec, -1\nv_mov_b32 v16, 0\ns_movk_i32 s44, 6400\nbuffer_store_dword v16, off, %[rs], s44 sc1\n.Lrd_end%=:\ns_waitcnt vmcnt(0) lgkmcnt(0)\n" \
""
#define GG_ASM \
"v_mbcnt_lo_u32_b32 v255, -1, 0\nv_mbcnt_hi_u32_b32 v255, -1, v255\nv_lshlrev_b32 v255, 2, v255\nv_add_u32 v255, %[tb], v255\nds_read_b32 v0, v255 offset:0\nds_read_b32 v1, v255 offset:256\nds_read_b32 v2, v255 offset:512\nds_read_b32 v3, v255 offset:768\n" \
"ds_read_b32 v4, v255 offset:1024\nds_read_b32 v5, v255 offset:1280\nds_read_b32 v92, v255 offset:1536\nds_read_b32 v93, v255 offset:1792\nds_read_b32 v94, v255 offset:2048\ns_waitcnt lgkmcnt(0)\ns_add_u32 m0, %[lw], 16384\ns_add_u32 s44, %[wo], 0\n" \
"buffer_load_dwordx4 v92, %[rw], s44 offen nt lds\ns_add_u32 m0, %[lw], 17408\ns_add_u32 s44, %[wo], 57344\nbuffer_load_dwordx4 v92, %[rw], s44 offen nt lds\ns_add_u32 m0, %[lw], 18432\ns_add_u32 s44, %[wo], 114688\nbuffer_load_dwordx4 v92, %[rw], s44 offen nt lds\ns_add_u32 m0, %[lw], 19456\n" \
"s_add_u32 s44, %[wo], 172032\nbuffer_load_dwordx4 v92, %[rw], s44 offen nt lds\ns_add_u32 m0, %[lw], 20480\ns_add_u32 s44, %[wo], 229376\nbuffer_load_dwordx4 v92, %[rw], s44 offen nt lds\ns_add_u32 m0, %[lw], 21504\ns_add_u32 s44, %[wo], 286720\nbuffer_load_dwordx4 v92, %[rw], s44 offen nt lds\n" \
"s_add_u32 m0, %[lw], 22528\ns_add_u32 s44, %[wo], 344064\nbuffer_load_dwordx4 v92, %[rw], s44 offen nt lds\ns_add_u32 m0, %[lw], 23552\ns_add_u32 s44, %[wo], 401408\nbuffer_load_dwordx4 v92, %[rw], s44 offen nt lds\ns_add_u32 m0, %[tb], 0\ns_add_u32 s44, %[wo], 458752\n" \
"buffer_load_dwordx4 v92, %[rw], s44 offen nt lds\ns_add_u32 m0, %[tb], 1024\ns_add_u32 s44, %[wo], 516096\nbuffer_load_dwordx4 v92, %[rw], s44 offen nt lds\ns_add_u32 m0, %[tb], 2048\ns_add_u32 s44, %[wo], 573440\nbuffer_load_dwordx4 v92, %[rw], s44 offen nt lds\ns_add_u32 m0, %[tb], 3072\n" \
"s_add_u32 s44, %[wo], 630784\nbuffer_load_dwordx4 v92, %[rw], s44 offen nt lds\ns_add_u32 m0, %[tb], 4096\ns_add_u32 s44, %[wo], 688128\nbuffer_load_dwordx4 v92, %[rw], s44 offen nt lds\ns_add_u32 m0, %[tb], 5120\ns_add_u32 s44, %[wo], 745472\nbuffer_load_dwordx4 v92, %[rw], s44 offen nt lds\n" \
"s_add_u32 m0, %[tb], 6144\ns_add_u32 s44, %[wo], 802816\nbuffer_load_dwordx4 v92, %[rw], s44 offen nt lds\ns_add_u32 m0, %[tb], 7168\ns_add_u32 s44, %[wo], 860160\nbuffer_load_dwordx4 v92, %[rw], s44 offen nt lds\ns_add_u32 m0, %[tb], 8192\ns_add_u32 s44, %[wo], 917504\n" \
"buffer_load_dwordx4 v92, %[rw], s44 offen nt lds\ns_add_u32 m0, %[tb], 9216\ns_add_u32 s44, %[wo], 974848\nbuffer_load_dwordx4 v92, %[rw], s44 offen nt lds\ns_add_u32 m0, %[tb], 10240\ns_add_u32 s44, %[wo], 1032192\nbuffer_load_dwordx4 v92, %[rw], s44 offen nt lds\ns_add_u32 m0, %[tb], 11264\n" \
"s_add_u32 s44, %[wo], 1089536\nbuffer_load_dwordx4 v92, %[rw], s44 offen nt lds\ns_add_u32 m0, %[lw], 0\ns_add_u32 s44, %[wo], 1146880\nbuffer_load_dwordx4 v92, %[rw], s44 offen nt lds\ns_add_u32 m0, %[lw], 1024\ns_add_u32 s44, %[wo], 1204224\nbuffer_load_dwordx4 v92, %[rw], s44 offen nt lds\n" \
"s_add_u32 m0, %[lw], 2048\ns_add_u32 s44, %[wo], 1261568\nbuffer_load_dwordx4 v92, %[rw], s44 offen nt lds\ns_add_u32 m0, %[lw], 3072\ns_add_u32 s44, %[wo], 1318912\nbuffer_load_dwordx4 v92, %[rw], s44 offen nt lds\ns_add_u32 m0, %[lw], 4096\ns_add_u32 s44, %[wo], 1376256\n" \
"buffer_load_dwordx4 v92, %[rw], s44 offen nt lds\ns_add_u32 m0, %[lw], 5120\ns_add_u32 s44, %[wo], 1433600\nbuffer_load_dwordx4 v92, %[rw], s44 offen nt lds\ns_add_u32 m0, %[lw], 6144\ns_add_u32 s44, %[wo], 1490944\nbuffer_load_dwordx4 v92, %[rw], s44 offen nt lds\ns_add_u32 m0, %[lw], 7168\n" \
"s_add_u32 s44, %[wo], 1548288\nbuffer_load_dwordx4 v92, %[rw], s44 offen nt lds\ns_add_u32 m0, %[lw], 8192\ns_add_u32 s44, %[wo], 1605632\nbuffer_load_dwordx4 v92, %[rw], s44 offen nt lds\ns_add_u32 m0, %[lw], 9216\ns_add_u32 s44, %[wo], 1662976\nbuffer_load_dwordx4 v92, %[rw], s44 offen nt lds\n" \
"s_add_u32 m0, %[lw], 10240\ns_add_u32 s44, %[wo], 1720320\nbuffer_load_dwordx4 v92, %[rw], s44 offen nt lds\ns_add_u32 m0, %[lw], 11264\ns_add_u32 s44, %[wo], 1777664\nbuffer_load_dwordx4 v92, %[rw], s44 offen nt lds\ns_waitcnt vmcnt(28)\ns_barrier\n" \
"s_add_u32 m0, %[lw], 12288\ns_add_u32 s44, %[wo], 1835008\nbuffer_load_dwordx4 v92, %[rw], s44 offen nt lds\ns_add_u32 m0, %[lw], 13312\ns_add_u32 s44, %[wo], 1892352\nbuffer_load_dwordx4 v92, %[rw], s44 offen nt lds\ns_add_u32 m0, %[lw], 14336\ns_add_u32 s44, %[wo], 1949696\n" \
"buffer_load_dwordx4 v92, %[rw], s44 offen nt lds\ns_add_u32 m0, %[lw], 15360\ns_add_u32 s44, %[wo], 2007040\nbuffer_load_dwordx4 v92, %[rw], s44 offen nt lds\nds_read_b64_tr_b16 a[0:1], v94 offset:16384\nds_read_b64_tr_b16 a[2:3], v94 offset:16896\nds_read_b64_tr_b16 a[8:9], v94 offset:16392\nds_read_b64_tr_b16 a[10:11], v94 offset:16904\n" \
"ds_read_b64_tr_b16 a[16:17], v94 offset:40960\nds_read_b64_tr_b16 a[18:19], v94 offset:41472\nds_read_b64_tr_b16 a[24:25], v94 offset:40968\nds_read_b64_tr_b16 a[26:27], v94 offset:41480\ns_waitcnt lgkmcnt(0)\ns_waitcnt vmcnt(28)\ns_barrier\ns_add_u32 m0, %[lw], 16384\n" \
"s_add_u32 s44, %[wo], 2064384\nbuffer_load_dwordx4 v92, %[rw], s44 offen nt lds\ns_add_u32 m0, %[lw], 17408\ns_add_u32 s44, %[wo], 2121728\nbuffer_load_dwordx4 v92, %[rw], s44 offen nt lds\ns_add_u32 m0, %[lw], 18432\ns_add_u32 s44, %[wo], 2179072\nbuffer_load_dwordx4 v92, %[rw], s44 offen nt lds\n" \
"s_add_u32 m0, %[lw], 19456\ns_add_u32 s44, %[wo], 2236416\nbuffer_load_dwordx4 v92, %[rw], s44 offen nt lds\nds_read_b64_tr_b16 a[4:5], v94 offset:20480\nds_read_b64_tr_b16 a[6:7], v94 offset:20992\nds_read_b64_tr_b16 a[12:13], v94 offset:20488\nds_read_b64_tr_b16 a[14:15], v94 offset:21000\nds_read_b64_tr_b16 a[20:21], v94 offset:45056\n" \
"ds_read_b64_tr_b16 a[22:23], v94 offset:45568\nds_read_b64_tr_b16 a[28:29], v94 offset:45064\nds_read_b64_tr_b16 a[30:31], v94 offset:45576\ns_waitcnt lgkmcnt(0)\ns_waitcnt vmcnt(28)\ns_barrier\ns_add_u32 m0, %[lw], 20480\ns_add_u32 s44, %[wo], 2293760\n" \
"buffer_load_dwordx4 v92, %[rw], s44 offen nt lds\ns_add_u32 m0, %[lw], 21504\ns_add_u32 s44, %[wo], 2351104\nbuffer_load_dwordx4 v92, %[rw], s44 offen nt lds\ns_add_u32 m0, %[lw], 22528\ns_add_u32 s44, %[wo], 2408448\nbuffer_load_dwordx4 v92, %[rw], s44 offen nt lds\ns_add_u32 m0, %[lw], 23552\n" \
"s_add_u32 s44, %[wo], 2465792\nbuffer_load_dwordx4 v92, %[rw], s44 offen nt lds\nds_read_b64_tr_b16 a[32:33], v93 offset:0\nds_read_b64_tr_b16 a[34:35], v93 offset:512\nds_read_b64_tr_b16 v[88:89], v93 offset:8\nds_read_b64_tr_b16 v[90:91], v93 offset:520\nds_read_b64_tr_b16 a[40:41], v93 offset:12288\nds_read_b64_tr_b16 a[42:43], v93 offset:12800\n" \
"ds_read_b64_tr_b16 a[48:49], v93 offset:12296\nds_read_b64_tr_b16 a[50:51], v93 offset:12808\ns_waitcnt lgkmcnt(0)\ns_waitcnt vmcnt(28)\ns_barrier\ns_add_u32 m0, %[tb], 0\ns_add_u32 s44, %[wo], 2523136\nbuffer_load_dwordx4 v92, %[rw], s44 offen nt lds\n" \
"s_add_u32 m0, %[tb], 1024\ns_add_u32 s44, %[wo], 2580480\nbuffer_load_dwordx4 v92, %[rw], s44 offen nt lds\ns_add_u32 m0, %[tb], 2048\ns_add_u32 s44, %[wo], 2637824\nbuffer_load_dwordx4 v92, %[rw], s44 offen nt lds\ns_add_u32 m0, %[tb], 3072\ns_add_u32 s44, %[wo], 2695168\n" \
"buffer_load_dwordx4 v92, %[rw], s44 offen nt lds\nds_read_b64_tr_b16 a[36:37], v93 offset:4096\nds_read_b64_tr_b16 a[38:39], v93 offset:4608\nds_read_b64_tr_b16 v[84:85], v93 offset:4104\nds_read_b64_tr_b16 v[86:87], v93 offset:4616\nds_read_b64_tr_b16 a[44:45], v93 offset:16384\nds_read_b64_tr_b16 a[46:47], v93 offset:16896\nds_read_b64_tr_b16 a[52:53], v93 offset:16392\n" \
"ds_read_b64_tr_b16 a[54:55], v93 offset:16904\ns_waitcnt lgkmcnt(0)\ns_waitcnt vmcnt(28)\ns_barrier\ns_add_u32 m0, %[tb], 4096\ns_add_u32 s44, %[wo], 2752512\nbuffer_load_dwordx4 v92, %[rw], s44 offen nt lds\ns_add_u32 m0, %[tb], 5120\n" \
"s_add_u32 s44, %[wo], 2809856\nbuffer_load_dwordx4 v92, %[rw], s44 offen nt lds\ns_add_u32 m0, %[tb], 6144\ns_add_u32 s44, %[wo], 2867200\nbuffer_load_dwordx4 v92, %[rw], s44 offen nt lds\ns_add_u32 m0, %[tb], 7168\ns_add_u32 s44, %[wo], 2924544\nbuffer_load_dwordx4 v92, %[rw], s44 offen nt lds\n" \
"ds_read_b64_tr_b16 a[56:57], v93 offset:8192\nds_read_b64_tr_b16 a[58:59], v93 offset:8704\nds_read_b64_tr_b16 a[64:65], v93 offset:8200\nds_read_b64_tr_b16 a[66:67], v93 offset:8712\nds_read_b64_tr_b16 v[80:81], v93 offset:20480\nds_read_b64_tr_b16 v[82:83], v93 offset:20992\nds_read_b64_tr_b16 a[72:73], v93 offset:20488\nds_read_b64_tr_b16 a[74:75], v93 offset:21000\n" \
"s_waitcnt lgkmcnt(0)\ns_waitcnt vmcnt(28)\ns_barrier\ns_add_u32 m0, %[tb], 8192\ns_add_u32 s44, %[wo], 2981888\nbuffer_load_dwordx4 v92, %[rw], s44 offen nt lds\ns_add_u32 m0, %[tb], 9216\ns_add_u32 s44, %[wo], 3039232\n" \
"buffer_load_dwordx4 v92, %[rw], s44 offen nt lds\ns_add_u32 m0, %[tb], 10240\ns_add_u32 s44, %[wo], 3096576\nbuffer_load_dwordx4 v92, %[rw], s44 offen nt lds\ns_add_u32 m0, %[tb], 11264\ns_add_u32 s44, %[wo], 3153920\nbuffer_load_dwordx4 v92, %[rw], s44 offen nt lds\nds_read_b64_tr_b16 a[60:61], v94 offset:0\n" \
"ds_read_b64_tr_b16 a[62:63], v94 offset:512\nds_read_b64_tr_b16 a[68:69], v94 offset:8\nds_read_b64_tr_b16 a[70:71], v94 offset:520\nds_read_b64_tr_b16 v[76:77], v94 offset:24576\nds_read_b64_tr_b16 v[78:79], v94 offset:25088\nds_read_b64_tr_b16 a[76:77], v94 offset:24584\nds_read_b64_tr_b16 a[78:79], v94 offset:25096\ns_waitcnt lgkmcnt(0)\n" \
"s_waitcnt vmcnt(28)\ns_barrier\ns_add_u32 m0, %[lw], 0\ns_add_u32 s44, %[wo], 3211264\nbuffer_load_dwordx4 v92, %[rw], s44 offen nt lds\ns_add_u32 m0, %[lw], 1024\ns_add_u32 s44, %[wo], 3268608\nbuffer_load_dwordx4 v92, %[rw], s44 offen nt lds\n" \
"s_add_u32 m0, %[lw], 2048\ns_add_u32 s44, %[wo], 3325952\nbuffer_load_dwordx4 v92, %[rw], s44 offen nt lds\ns_add_u32 m0, %[lw], 3072\ns_add_u32 s44, %[wo], 3383296\nbuffer_load_dwordx4 v92, %[rw], s44 offen nt lds\nds_read_b64_tr_b16 a[80:81], v94 offset:4096\nds_read_b64_tr_b16 a[82:83], v94 offset:4608\n" \
"ds_read_b64_tr_b16 a[88:89], v94 offset:4104\nds_read_b64_tr_b16 a[90:91], v94 offset:4616\nds_read_b64_tr_b16 a[96:97], v94 offset:28672\nds_read_b64_tr_b16 a[98:99], v94 offset:29184\nds_read_b64_tr_b16 v[72:73], v94 offset:28680\nds_read_b64_tr_b16 v[74:75], v94 offset:29192\ns_waitcnt lgkmcnt(0)\ns_waitcnt vmcnt(28)\n" \
"s_barrier\ns_add_u32 m0, %[lw], 4096\ns_add_u32 s44, %[wo], 3440640\nbuffer_load_dwordx4 v92, %[rw], s44 offen nt lds\ns_add_u32 m0, %[lw], 5120\ns_add_u32 s44, %[wo], 3497984\nbuffer_load_dwordx4 v92, %[rw], s44 offen nt lds\ns_add_u32 m0, %[lw], 6144\n" \
"s_add_u32 s44, %[wo], 3555328\nbuffer_load_dwordx4 v92, %[rw], s44 offen nt lds\ns_add_u32 m0, %[lw], 7168\ns_add_u32 s44, %[wo], 3612672\nbuffer_load_dwordx4 v92, %[rw], s44 offen nt lds\nds_read_b64_tr_b16 a[84:85], v94 offset:8192\nds_read_b64_tr_b16 a[86:87], v94 offset:8704\nds_read_b64_tr_b16 a[92:93], v94 offset:8200\n" \
"ds_read_b64_tr_b16 a[94:95], v94 offset:8712\nds_read_b64_tr_b16 a[100:101], v94 offset:32768\nds_read_b64_tr_b16 a[102:103], v94 offset:33280\nds_read_b64_tr_b16 v[68:69], v94 offset:32776\nds_read_b64_tr_b16 v[70:71], v94 offset:33288\ns_waitcnt lgkmcnt(0)\ns_waitcnt vmcnt(28)\ns_barrier\n" \
"s_add_u32 m0, %[lw], 8192\ns_add_u32 s44, %[wo], 3670016\nbuffer_load_dwordx4 v92, %[rw], s44 offen nt lds\ns_add_u32 m0, %[lw], 9216\ns_add_u32 s44, %[wo], 3727360\nbuffer_load_dwordx4 v92, %[rw], s44 offen nt lds\ns_add_u32 m0, %[lw], 10240\ns_add_u32 s44, %[wo], 3784704\n" \
"buffer_load_dwordx4 v92, %[rw], s44 offen nt lds\ns_add_u32 m0, %[lw], 11264\ns_add_u32 s44, %[wo], 3842048\nbuffer_load_dwordx4 v92, %[rw], s44 offen nt lds\nds_read_b64_tr_b16 a[104:105], v94 offset:12288\nds_read_b64_tr_b16 a[106:107], v94 offset:12800\nds_read_b64_tr_b16 a[112:113], v94 offset:12296\nds_read_b64_tr_b16 a[114:115], v94 offset:12808\n" \
"ds_read_b64_tr_b16 a[120:121], v94 offset:36864\nds_read_b64_tr_b16 a[122:123], v94 offset:37376\nds_read_b64_tr_b16 a[128:129], v94 offset:36872\nds_read_b64_tr_b16 a[130:131], v94 offset:37384\ns_waitcnt lgkmcnt(0)\ns_waitcnt vmcnt(28)\ns_barrier\ns_add_u32 m0, %[lw], 12288\n" \
"s_add_u32 s44, %[wo], 3899392\nbuffer_load_dwordx4 v92, %[rw], s44 offen nt lds\ns_add_u32 m0, %[lw], 13312\ns_add_u32 s44, %[wo], 3956736\nbuffer_load_dwordx4 v92, %[rw], s44 offen nt lds\ns_add_u32 m0, %[lw], 14336\ns_add_u32 s44, %[wo], 4014080\nbuffer_load_dwordx4 v92, %[rw], s44 offen nt lds\n" \
"s_add_u32 m0, %[lw], 15360\ns_add_u32 s44, %[wo], 4071424\nbuffer_load_dwordx4 v92, %[rw], s44 offen nt lds\nds_read_b64_tr_b16 a[108:109], v94 offset:16384\nds_read_b64_tr_b16 a[110:111], v94 offset:16896\nds_read_b64_tr_b16 a[116:117], v94 offset:16392\nds_read_b64_tr_b16 a[118:119], v94 offset:16904\nds_read_b64_tr_b16 a[124:125], v94 offset:40960\n" \
"ds_read_b64_tr_b16 a[126:127], v94 offset:41472\nds_read_b64_tr_b16 a[132:133], v94 offset:40968\nds_read_b64_tr_b16 a[134:135], v94 offset:41480\ns_waitcnt lgkmcnt(0)\ns_waitcnt vmcnt(28)\ns_barrier\ns_add_u32 m0, %[lw], 16384\ns_add_u32 s44, %[wo], 4128768\n" \
"buffer_load_dwordx4 v92, %[rw], s44 offen nt lds\ns_add_u32 m0, %[lw], 17408\ns_add_u32 s44, %[wo], 4186112\nbuffer_load_dwordx4 v92, %[rw], s44 offen nt lds\ns_add_u32 m0, %[lw], 18432\ns_add_u32 s44, %[wo], 4243456\nbuffer_load_dwordx4 v92, %[rw], s44 offen nt lds\ns_add_u32 m0, %[lw], 19456\n" \
"s_add_u32 s44, %[wo], 4300800\nbuffer_load_dwordx4 v92, %[rw], s44 offen nt lds\nds_read_b64_tr_b16 a[136:137], v94 offset:20480\nds_read_b64_tr_b16 a[138:139], v94 offset:20992\nds_read_b64_tr_b16 v[64:65], v94 offset:20488\nds_read_b64_tr_b16 v[66:67], v94 offset:21000\nds_read_b64_tr_b16 a[144:145], v94 offset:45056\nds_read_b64_tr_b16 a[146:147], v94 offset:45568\n" \
"ds_read_b64_tr_b16 a[152:153], v94 offset:45064\nds_read_b64_tr_b16 a[154:155], v94 offset:45576\ns_waitcnt lgkmcnt(0)\ns_waitcnt vmcnt(28)\ns_barrier\ns_add_u32 m0, %[lw], 20480\ns_add_u32 s44, %[wo], 4358144\nbuffer_load_dwordx4 v92, %[rw], s44 offen nt lds\n" \
"s_add_u32 m0, %[lw], 21504\ns_add_u32 s44, %[wo], 4415488\nbuffer_load_dwordx4 v92, %[rw], s44 offen nt lds\ns_add_u32 m0, %[lw], 22528\ns_add_u32 s44, %[wo], 4472832\nbuffer_load_dwordx4 v92, %[rw], s44 offen nt lds\ns_add_u32 m0, %[lw], 23552\ns_add_u32 s44, %[wo], 4530176\n" \
"buffer_load_dwordx4 v92, %[rw], s44 offen nt lds\nds_read_b64_tr_b16 a[140:141], v93 offset:0\nds_read_b64_tr_b16 a[142:143], v93 offset:512\nds_read_b64_tr_b16 v[60:61], v93 offset:8\nds_read_b64_tr_b16 v[62:63], v93 offset:520\nds_read_b64_tr_b16 a[148:149], v93 offset:12288\nds_read_b64_tr_b16 a[150:151], v93 offset:12800\nds_read_b64_tr_b16 a[156:157], v93 offset:12296\n" \
"ds_read_b64_tr_b16 a[158:159], v93 offset:12808\ns_waitcnt lgkmcnt(0)\ns_waitcnt vmcnt(28)\ns_barrier\ns_add_u32 m0, %[tb], 0\ns_add_u32 s44, %[wo], 4587520\nbuffer_load_dwordx4 v92, %[rw], s44 offen nt lds\ns_add_u32 m0, %[tb], 1024\n" \
"s_add_u32 s44, %[wo], 4644864\nbuffer_load_dwordx4 v92, %[rw], s44 offen nt lds\ns_add_u32 m0, %[tb], 2048\ns_add_u32 s44, %[wo], 4702208\nbuffer_load_dwordx4 v92, %[rw], s44 offen nt lds\ns_add_u32 m0, %[tb], 3072\ns_add_u32 s44, %[wo], 4759552\nbuffer_load_dwordx4 v92, %[rw], s44 offen nt lds\n" \
"ds_read_b64_tr_b16 a[160:161], v93 offset:4096\nds_read_b64_tr_b16 a[162:163], v93 offset:4608\nds_read_b64_tr_b16 a[168:169], v93 offset:4104\nds_read_b64_tr_b16 a[170:171], v93 offset:4616\nds_read_b64_tr_b16 v[56:57], v93 offset:16384\nds_read_b64_tr_b16 v[58:59], v93 offset:16896\nds_read_b64_tr_b16 a[176:177], v93 offset:16392\nds_read_b64_tr_b16 a[178:179], v93 offset:16904\n" \
"s_waitcnt lgkmcnt(0)\ns_waitcnt vmcnt(28)\ns_barrier\ns_add_u32 m0, %[tb], 4096\ns_add_u32 s44, %[wo], 4816896\nbuffer_load_dwordx4 v92, %[rw], s44 offen nt lds\ns_add_u32 m0, %[tb], 5120\ns_add_u32 s44, %[wo], 4874240\n" \
"buffer_load_dwordx4 v92, %[rw], s44 offen nt lds\ns_add_u32 m0, %[tb], 6144\ns_add_u32 s44, %[wo], 4931584\nbuffer_load_dwordx4 v92, %[rw], s44 offen nt lds\ns_add_u32 m0, %[tb], 7168\ns_add_u32 s44, %[wo], 4988928\nbuffer_load_dwordx4 v92, %[rw], s44 offen nt lds\nds_read_b64_tr_b16 a[164:165], v93 offset:8192\n" \
"ds_read_b64_tr_b16 a[166:167], v93 offset:8704\nds_read_b64_tr_b16 a[172:173], v93 offset:8200\nds_read_b64_tr_b16 a[174:175], v93 offset:8712\nds_read_b64_tr_b16 v[52:53], v93 offset:20480\nds_read_b64_tr_b16 v[54:55], v93 offset:20992\nds_read_b64_tr_b16 a[180:181], v93 offset:20488\nds_read_b64_tr_b16 a[182:183], v93 offset:21000\ns_waitcnt lgkmcnt(0)\n" \
"s_waitcnt vmcnt(28)\ns_barrier\ns_add_u32 m0, %[tb], 8192\ns_add_u32 s44, %[wo], 5046272\nbuffer_load_dwordx4 v92, %[rw], s44 offen nt lds\ns_add_u32 m0, %[tb], 9216\ns_add_u32 s44, %[wo], 5103616\nbuffer_load_dwordx4 v92, %[rw], s44 offen nt lds\n" \
"s_add_u32 m0, %[tb], 10240\ns_add_u32 s44, %[wo], 5160960\nbuffer_load_dwordx4 v92, %[rw], s44 offen nt lds\ns_add_u32 m0, %[tb], 11264\ns_add_u32 s44, %[wo], 5218304\nbuffer_load_dwordx4 v92, %[rw], s44 offen nt lds\nds_read_b64_tr_b16 a[184:185], v94 offset:0\nds_read_b64_tr_b16 a[186:187], v94 offset:512\n" \
"ds_read_b64_tr_b16 a[192:193], v94 offset:8\nds_read_b64_tr_b16 a[194:195], v94 offset:520\nds_read_b64_tr_b16 a[200:201], v94 offset:24576\nds_read_b64_tr_b16 a[202:203], v94 offset:25088\nds_read_b64_tr_b16 v[48:49], v94 offset:24584\nds_read_b64_tr_b16 v[50:51], v94 offset:25096\ns_waitcnt lgkmcnt(0)\ns_waitcnt vmcnt(28)\n" \
"s_barrier\ns_add_u32 m0, %[lw], 0\ns_add_u32 s44, %[wo], 5275648\nbuffer_load_dwordx4 v92, %[rw], s44 offen nt lds\ns_add_u32 m0, %[lw], 1024\ns_add_u32 s44, %[wo], 5332992\nbuffer_load_dwordx4 v92, %[rw], s44 offen nt lds\ns_add_u32 m0, %[lw], 2048\n" \
"s_add_u32 s44, %[wo], 5390336\nbuffer_load_dwordx4 v92, %[rw], s44 offen nt lds\ns_add_u32 m0, %[lw], 3072\ns_add_u32 s44, %[wo], 5447680\nbuffer_load_dwordx4 v92, %[rw], s44 offen nt lds\nds_read_b64_tr_b16 a[188:189], v94 offset:4096\nds_read_b64_tr_b16 a[190:191], v94 offset:4608\nds_read_b64_tr_b16 a[196:197], v94 offset:4104\n" \
"ds_read_b64_tr_b16 a[198:199], v94 offset:4616\nds_read_b64_tr_b16 a[204:205], v94 offset:28672\nds_read_b64_tr_b16 a[206:207], v94 offset:29184\nds_read_b64_tr_b16 v[44:45], v94 offset:28680\nds_read_b64_tr_b16 v[46:47], v94 offset:29192\ns_waitcnt lgkmcnt(0)\ns_waitcnt vmcnt(28)\ns_barrier\n" \
"s_add_u32 m0, %[lw], 4096\ns_add_u32 s44, %[wo], 5505024\nbuffer_load_dwordx4 v92, %[rw], s44 offen nt lds\ns_add_u32 m0, %[lw], 5120\ns_add_u32 s44, %[wo], 5562368\nbuffer_load_dwordx4 v92, %[rw], s44 offen nt lds\ns_add_u32 m0, %[lw], 6144\ns_add_u32 s44, %[wo], 5619712\n" \
"buffer_load_dwordx4 v92, %[rw], s44 offen nt lds\ns_add_u32 m0, %[lw], 7168\ns_add_u32 s44, %[wo], 5677056\nbuffer_load_dwordx4 v92, %[rw], s44 offen nt lds\nds_read_b64_tr_b16 a[208:209], v94 offset:8192\nds_read_b64_tr_b16 a[210:211], v94 offset:8704\nds_read_b64_tr_b16 a[216:217], v94 offset:8200\nds_read_b64_tr_b16 a[218:219], v94 offset:8712\n" \
"ds_read_b64_tr_b16 a[224:225], v94 offset:32768\nds_read_b64_tr_b16 a[226:227], v94 offset:33280\nds_read_b64_tr_b16 a[232:233], v94 offset:32776\nds_read_b64_tr_b16 a[234:235], v94 offset:33288\ns_waitcnt lgkmcnt(0)\ns_waitcnt vmcnt(28)\ns_barrier\ns_add_u32 m0, %[lw], 8192\n" \
"s_add_u32 s44, %[wo], 5734400\nbuffer_load_dwordx4 v92, %[rw], s44 offen nt lds\ns_add_u32 m0, %[lw], 9216\ns_add_u32 s44, %[wo], 5791744\nbuffer_load_dwordx4 v92, %[rw], s44 offen nt lds\ns_add_u32 m0, %[lw], 10240\ns_add_u32 s44, %[wo], 5849088\nbuffer_load_dwordx4 v92, %[rw], s44 offen nt lds\n" \
"s_add_u32 m0, %[lw], 11264\ns_add_u32 s44, %[wo], 5906432\nbuffer_load_dwordx4 v92, %[rw], s44 offen nt lds\nds_read_b64_tr_b16 a[212:213], v94 offset:12288\nds_read_b64_tr_b16 a[214:215], v94 offset:12800\nds_read_b64_tr_b16 a[220:221], v94 offset:12296\nds_read_b64_tr_b16 a[222:223], v94 offset:12808\nds_read_b64_tr_b16 a[228:229], v94 offset:36864\n" \
"ds_read_b64_tr_b16 a[230:231], v94 offset:37376\nds_read_b64_tr_b16 a[236:237], v94 offset:36872\nds_read_b64_tr_b16 a[238:239], v94 offset:37384\ns_waitcnt lgkmcnt(0)\ns_waitcnt vmcnt(28)\ns_barrier\ns_add_u32 m0, %[lw], 12288\ns_add_u32 s44, %[wo], 5963776\n" \
"buffer_load_dwordx4 v92, %[rw], s44 offen nt lds\ns_add_u32 m0, %[lw], 13312\ns_add_u32 s44, %[wo], 6021120\nbuffer_load_dwordx4 v92, %[rw], s44 offen nt lds\ns_add_u32 m0, %[lw], 14336\ns_add_u32 s44, %[wo], 6078464\nbuffer_load_dwordx4 v92, %[rw], s44 offen nt lds\ns_add_u32 m0, %[lw], 15360\n" \
"s_add_u32 s44, %[wo], 6135808\nbuffer_load_dwordx4 v92, %[rw], s44 offen nt lds\nds_read_b64_tr_b16 a[240:241], v94 offset:16384\nds_read_b64_tr_b16 a[242:243], v94 offset:16896\nds_read_b64_tr_b16 v[40:41], v94 offset:16392\nds_read_b64_tr_b16 v[42:43], v94 offset:16904\nds_read_b64_tr_b16 a[248:249], v94 offset:40960\nds_read_b64_tr_b16 a[250:251], v94 offset:41472\n" \
"ds_read_b64_tr_b16 v[96:97], v94 offset:40968\nds_read_b64_tr_b16 v[98:99], v94 offset:41480\ns_waitcnt lgkmcnt(0)\ns_waitcnt vmcnt(28)\ns_barrier\ns_add_u32 m0, %[lw], 16384\ns_add_u32 s44, %[wo], 6193152\nbuffer_load_dwordx4 v92, %[rw], s44 offen nt lds\n" \
"s_add_u32 m0, %[lw], 17408\ns_add_u32 s44, %[wo], 6250496\nbuffer_load_dwordx4 v92, %[rw], s44 offen nt lds\ns_add_u32 m0, %[lw], 18432\ns_add_u32 s44, %[wo], 6307840\nbuffer_load_dwordx4 v92, %[rw], s44 offen nt lds\ns_add_u32 m0, %[lw], 19456\ns_add_u32 s44, %[wo], 6365184\n" \
"buffer_load_dwordx4 v92, %[rw], s44 offen nt lds\nds_read_b64_tr_b16 a[244:245], v94 offset:20480\nds_read_b64_tr_b16 a[246:247], v94 offset:20992\nds_read_b64_tr_b16 v[36:37], v94 offset:20488\nds_read_b64_tr_b16 v[38:39], v94 offset:21000\nds_read_b64_tr_b16 a[252:253], v94 offset:45056\nds_read_b64_tr_b16 a[254:255], v94 offset:45568\nds_read_b64_tr_b16 v[100:101], v94 offset:45064\n" \
"ds_read_b64_tr_b16 v[102:103], v94 offset:45576\ns_waitcnt lgkmcnt(0)\ns_waitcnt vmcnt(28)\ns_barrier\ns_add_u32 m0, %[lw], 20480\ns_add_u32 s44, %[wo], 6422528\nbuffer_load_dwordx4 v92, %[rw], s44 offen nt lds\ns_add_u32 m0, %[lw], 21504\n" \
"s_add_u32 s44, %[wo], 6479872\nbuffer_load_dwordx4 v92, %[rw], s44 offen nt lds\ns_add_u32 m0, %[lw], 22528\ns_add_u32 s44, %[wo], 6537216\nbuffer_load_dwordx4 v92, %[rw], s44 offen nt lds\ns_add_u32 m0, %[lw], 23552\ns_add_u32 s44, %[wo], 6594560\nbuffer_load_dwordx4 v92, %[rw], s44 offen nt lds\n" \
"ds_read_b64_tr_b16 v[104:105], v93 offset:0\nds_read_b64_tr_b16 v[106:107], v93 offset:512\nds_read_b64_tr_b16 v[112:113], v93 offset:8\nds_read_b64_tr_b16 v[114:115], v93 offset:520\nds_read_b64_tr_b16 v[32:33], v93 offset:12288\nds_read_b64_tr_b16 v[34:35], v93 offset:12800\nds_read_b64_tr_b16 v[120:121], v93 offset:12296\nds_read_b64_tr_b16 v[122:123], v93 offset:12808\n" \
"s_waitcnt lgkmcnt(0)\ns_waitcnt vmcnt(28)\ns_barrier\ns_add_u32 m0, %[tb], 0\ns_add_u32 s44, %[wo], 6651904\nbuffer_load_dwordx4 v92, %[rw], s44 offen nt lds\ns_add_u32 m0, %[tb], 1024\ns_add_u32 s44, %[wo], 6709248\n" \
"buffer_load_dwordx4 v92, %[rw], s44 offen nt lds\ns_add_u32 m0, %[tb], 2048\ns_add_u32 s44, %[wo], 6766592\nbuffer_load_dwordx4 v92, %[rw], s44 offen nt lds\ns_add_u32 m0, %[tb], 3072\ns_add_u32 s44, %[wo], 6823936\nbuffer_load_dwordx4 v92, %[rw], s44 offen nt lds\nds_read_b64_tr_b16 v[108:109], v93 offset:4096\n" \
"ds_read_b64_tr_b16 v[110:111], v93 offset:4608\nds_read_b64_tr_b16 v[116:117], v93 offset:4104\nds_read_b64_tr_b16 v[118:119], v93 offset:4616\nds_read_b64_tr_b16 v[28:29], v93 offset:16384\nds_read_b64_tr_b16 v[30:31], v93 offset:16896\nds_read_b64_tr_b16 v[124:125], v93 offset:16392\nds_read_b64_tr_b16 v[126:127], v93 offset:16904\ns_waitcnt lgkmcnt(0)\n" \
"s_waitcnt vmcnt(28)\ns_barrier\ns_add_u32 m0, %[tb], 4096\ns_add_u32 s44, %[wo], 6881280\nbuffer_load_dwordx4 v92, %[rw], s44 offen nt lds\ns_add_u32 m0, %[tb], 5120\ns_add_u32 s44, %[wo], 6938624\nbuffer_load_dwordx4 v92, %[rw], s44 offen nt lds\n" \
"s_add_u32 m0, %[tb], 6144\ns_add_u32 s44, %[wo], 6995968\nbuffer_load_dwordx4 v92, %[rw], s44 offen nt lds\ns_add_u32 m0, %[tb], 7168\ns_add_u32 s44, %[wo], 7053312\nbuffer_load_dwordx4 v92, %[rw], s44 offen nt lds\nds_read_b64_tr_b16 v[128:129], v93 offset:8192\nds_read_b64_tr_b16 v[130:131], v93 offset:8704\n" \
"ds_read_b64_tr_b16 v[136:137], v93 offset:8200\nds_read_b64_tr_b16 v[138:139], v93 offset:8712\nds_read_b64_tr_b16 v[144:145], v93 offset:20480\nds_read_b64_tr_b16 v[146:147], v93 offset:20992\nds_read_b64_tr_b16 v[24:25], v93 offset:20488\nds_read_b64_tr_b16 v[26:27], v93 offset:21000\ns_waitcnt lgkmcnt(0)\ns_waitcnt vmcnt(28)\n" \
"s_barrier\ns_add_u32 m0, %[tb], 8192\ns_add_u32 s44, %[wo], 7110656\nbuffer_load_dwordx4 v92, %[rw], s44 offen nt lds\ns_add_u32 m0, %[tb], 9216\ns_add_u32 s44, %[wo], 7168000\nbuffer_load_dwordx4 v92, %[rw], s44 offen nt lds\ns_add_u32 m0, %[tb], 10240\n" \
"s_add_u32 s44, %[wo], 7225344\nbuffer_load_dwordx4 v92, %[rw], s44 offen nt lds\ns_add_u32 m0, %[tb], 11264\ns_add_u32 s44, %[wo], 7282688\nbuffer_load_dwordx4 v92, %[rw], s44 offen nt lds\nds_read_b64_tr_b16 v[132:133], v94 offset:0\nds_read_b64_tr_b16 v[134:135], v94 offset:512\nds_read_b64_tr_b16 v[140:141], v94 offset:8\n" \
"ds_read_b64_tr_b16 v[142:143], v94 offset:520\nds_read_b64_tr_b16 v[148:149], v94 offset:24576\nds_read_b64_tr_b16 v[150:151], v94 offset:25088\nds_read_b64_tr_b16 v[20:21], v94 offset:24584\nds_read_b64_tr_b16 v[22:23], v94 offset:25096\ns_waitcnt lgkmcnt(0)\ns_waitcnt vmcnt(28)\ns_barrier\n" \
"ds_read_b64_tr_b16 v[152:153], v94 offset:4096\nds_read_b64_tr_b16 v[154:155], v94 offset:4608\nds_read_b64_tr_b16 v[160:161], v94 offset:4104\nds_read_b64_tr_b16 v[162:163], v94 offset:4616\nds_read_b64_tr_b16 v[168:169], v94 offset:28672\nds_read_b64_tr_b16 v[170:171], v94 offset:29184\nds_read_b64_tr_b16 v[176:177], v94 offset:28680\nds_read_b64_tr_b16 v[178:179], v94 offset:29192\n" \
"s_waitcnt lgkmcnt(0)\nds_write_b128 v1, v[88:91] offset:0\nds_write_b128 v1, v[84:87] offset:1024\nds_write_b128 v1, v[80:83] offset:2048\nds_write_b128 v1, v[76:79] offset:3072\ns_waitcnt vmcnt(24)\ns_barrier\nds_read_b64_tr_b16 v[156:157], v94 offset:8192\n" \
"ds_read_b64_tr_b16 v[158:159], v94 offset:8704\nds_read_b64_tr_b16 v[164:165], v94 offset:8200\nds_read_b64_tr_b16 v[166:167], v94 offset:8712\nds_read_b64_tr_b16 v[172:173], v94 offset:32768\nds_read_b64_tr_b16 v[174:175], v94 offset:33280\nds_read_b64_tr_b16 v[180:181], v94 offset:32776\nds_read_b64_tr_b16 v[182:183], v94 offset:33288\ns_waitcnt lgkmcnt(0)\n" \
"ds_write_b128 v1, v[72:75] offset:4096\nds_write_b128 v1, v[68:71] offset:5120\nds_write_b128 v1, v[64:67] offset:6144\nds_write_b128 v1, v[60:63] offset:7168\ns_waitcnt vmcnt(20)\ns_barrier\nds_read_b64_tr_b16 v[184:185], v94 offset:12288\nds_read_b64_tr_b16 v[186:187], v94 offset:12800\n" \
"ds_read_b64_tr_b16 v[60:61], v94 offset:12296\nds_read_b64_tr_b16 v[62:63], v94 offset:12808\nds_read_b64_tr_b16 v[192:193], v94 offset:36864\nds_read_b64_tr_b16 v[194:195], v94 offset:37376\nds_read_b64_tr_b16 v[200:201], v94 offset:36872\nds_read_b64_tr_b16 v[202:203], v94 offset:37384\ns_waitcnt lgkmcnt(0)\nds_write_b128 v1, v[56:59] offset:8192\n" \
"ds_write_b128 v1, v[52:55] offset:9216\nds_write_b128 v1, v[48:51] offset:10240\nds_write_b128 v1, v[44:47] offset:11264\ns_waitcnt vmcnt(16)\ns_barrier\nds_read_b64_tr_b16 v[188:189], v94 offset:16384\nds_read_b64_tr_b16 v[190:191], v94 offset:16896\nds_read_b64_tr_b16 v[44:45], v94 offset:16392\n" \
"ds_read_b64_tr_b16 v[46:47], v94 offset:16904\nds_read_b64_tr_b16 v[196:197], v94 offset:40960\nds_read_b64_tr_b16 v[198:199], v94 offset:41472\nds_read_b64_tr_b16 v[204:205], v94 offset:40968\nds_read_b64_tr_b16 v[206:207], v94 offset:41480\ns_waitcnt lgkmcnt(0)\nds_write_b128 v1, v[40:43] offset:12288\nds_write_b128 v1, v[36:39] offset:13312\n" \
"ds_write_b128 v1, v[32:35] offset:14336\nds_write_b128 v1, v[28:31] offset:15360\ns_waitcnt vmcnt(12)\ns_barrier\nds_read_b64_tr_b16 v[208:209], v94 offset:20480\nds_read_b64_tr_b16 v[210:211], v94 offset:20992\nds_read_b64_tr_b16 v[216:217], v94 offset:20488\nds_read_b64_tr_b16 v[218:219], v94 offset:21000\n" \
"ds_read_b64_tr_b16 v[28:29], v94 offset:45056\nds_read_b64_tr_b16 v[30:31], v94 offset:45568\nds_read_b64_tr_b16 v[224:225], v94 offset:45064\nds_read_b64_tr_b16 v[226:227], v94 offset:45576\ns_waitcnt lgkmcnt(0)\nds_write_b128 v1, v[24:27] offset:16384\nds_write_b128 v1, v[20:23] offset:17408\nds_write_b128 v1, v[60:63] offset:18432\n" \
"ds_write_b128 v1, v[44:47] offset:19456\ns_waitcnt vmcnt(8)\ns_barrier\nds_read_b64_tr_b16 v[212:213], v93 offset:0\nds_read_b64_tr_b16 v[214:215], v93 offset:512\nds_read_b64_tr_b16 v[220:221], v93 offset:8\nds_read_b64_tr_b16 v[222:223], v93 offset:520\nds_read_b64_tr_b16 v[44:45], v93 offset:12288\n" \
"ds_read_b64_tr_b16 v[46:47], v93 offset:12800\nds_read_b64_tr_b16 v[228:229], v93 offset:12296\nds_read_b64_tr_b16 v[230:231], v93 offset:12808\ns_waitcnt lgkmcnt(0)\nds_write_b128 v1, v[28:31] offset:20480\nds_write_b128 v1, v[44:47] offset:21504\ns_waitcnt vmcnt(4)\ns_barrier\n" \
"ds_read_b64_tr_b16 v[232:233], v93 offset:4096\nds_read_b64_tr_b16 v[234:235], v93 offset:4608\nds_read_b64_tr_b16 v[240:241], v93 offset:4104\nds_read_b64_tr_b16 v[242:243], v93 offset:4616\nds_read_b64_tr_b16 v[248:249], v93 offset:16384\nds_read_b64_tr_b16 v[250:251], v93 offset:16896\nds_read_b64_tr_b16 v[44:45], v93 offset:16392\nds_read_b64_tr_b16 v[46:47], v93 offset:16904\n" \
"s_waitcnt lgkmcnt(0)\nds_write_b128 v1, v[44:47] offset:22528\ns_waitcnt vmcnt(0)\ns_barrier\nds_read_b64_tr_b16 v[236:237], v93 offset:8192\nds_read_b64_tr_b16 v[238:239], v93 offset:8704\nds_read_b64_tr_b16 v[244:245], v93 offset:8200\nds_read_b64_tr_b16 v[246:247], v93 offset:8712\n" \
"ds_read_b64_tr_b16 v[252:253], v93 offset:20480\nds_read_b64_tr_b16 v[254:255], v93 offset:20992\nds_read_b64_tr_b16 v[44:45], v93 offset:20488\nds_read_b64_tr_b16 v[46:47], v93 offset:21000\ns_waitcnt lgkmcnt(0)\nds_write_b128 v1, v[44:47] offset:23552\ns_waitcnt lgkmcnt(0)\ns_barrier\n" \
"s_mov_b32 s40, 0\ns_mov_b32 s43, 0\ns_lshl_b32 s57, %[w], 12\ns_mov_b32 s58, 0xbeeff558\ns_mov_b32 s59, 0x409d30fc\ns_mov_b32 s56, -2147483648\nv_mov_b32 v56, 0x3f803f80\nv_mov_b32 v57, 0x3f803f80\n" \
"v_mov_b32 v58, 0x3f803f80\nv_mov_b32 v59, 0x3f803f80\ns_nop 1\nv_mfma_f32_16x16x32_bf16 v[8:11], a[0:3], v[56:59], 0\nv_mfma_f32_16x16x32_bf16 v[12:15], a[8:11], v[56:59], 0\nv_mfma_f32_16x16x32_bf16 v[16:19], a[16:19], v[56:59], 0\nv_mfma_f32_16x16x32_bf16 v[20:23], a[24:27], v[56:59], 0\nv_mfma_f32_16x16x32_bf16 v[8:11], a[4:7], v[56:59], v[8:11]\n" \
"v_mfma_f32_16x16x32_bf16 v[12:15], a[12:15], v[56:59], v[12:15]\nv_mfma_f32_16x16x32_bf16 v[16:19], a[20:23], v[56:59], v[16:19]\nv_mfma_f32_16x16x32_bf16 v[20:23], a[28:31], v[56:59], v[20:23]\nds_read_b128 v[80:83], v1 offset:0\nv_mfma_f32_16x16x32_bf16 v[8:11], a[32:35], v[56:59], v[8:11]\ns_waitcnt lgkmcnt(0)\nv_mfma_f32_16x16x32_bf16 v[12:15], v[80:83], v[56:59], v[12:15]\nv_mfma_f32_16x16x32_bf16 v[16:19], a[40:43], v[56:59], v[16:19]\n" \
"v_mfma_f32_16x16x32_bf16 v[20:23], a[48:51], v[56:59], v[20:23]\nds_read_b128 v[72:75], v1 offset:1024\nv_mfma_f32_16x16x32_bf16 v[8:11], a[36:39], v[56:59], v[8:11]\ns_waitcnt lgkmcnt(0)\nv_mfma_f32_16x16x32_bf16 v[12:15], v[72:75], v[56:59], v[12:15]\nv_mfma_f32_16x16x32_bf16 v[16:19], a[44:47], v[56:59], v[16:19]\nv_mfma_f32_16x16x32_bf16 v[20:23], a[52:55], v[56:59], v[20:23]\nds_read_b128 v[76:79], v1 offset:2048\n" \
"v_mfma_f32_16x16x32_bf16 v[8:11], a[56:59], v[56:59], v[8:11]\nv_mfma_f32_16x16x32_bf16 v[12:15], a[64:67], v[56:59], v[12:15]\ns_waitcnt lgkmcnt(0)\nv_mfma_f32_16x16x32_bf16 v[16:19], v[76:79], v[56:59], v[16:19]\nv_mfma_f32_16x16x32_bf16 v[20:23], a[72:75], v[56:59], v[20:23]\nds_read_b128 v[80:83], v1 offset:3072\nv_mfma_f32_16x16x32_bf16 v[8:11], a[60:63], v[56:59], v[8:11]\nv_mfma_f32_16x16x32_bf16 v[12:15], a[68:71], v[56:59], v[12:15]\n" \
"s_waitcnt lgkmcnt(0)\nv_mfma_f32_16x16x32_bf16 v[16:19], v[80:83], v[56:59], v[16:19]\nv_mfma_f32_16x16x32_bf16 v[20:23], a[76:79], v[56:59], v[20:23]\nds_read_b128 v[72:75], v1 offset:4096\nv_mfma_f32_16x16x32_bf16 v[8:11], a[80:83], v[56:59], v[8:11]\nv_mfma_f32_16x16x32_bf16 v[12:15], a[88:91], v[56:59], v[12:15]\nv_mfma_f32_16x16x32_bf16 v[16:19], a[96:99], v[56:59], v[16:19]\ns_waitcnt lgkmcnt(0)\n" \
"v_mfma_f32_16x16x32_bf16 v[20:23], v[72:75], v[56:59], v[20:23]\nds_read_b128 v[76:79], v1 offset:5120\nv_mfma_f32_16x16x32_bf16 v[8:11], a[84:87], v[56:59], v[8:11]\nv_mfma_f32_16x16x32_bf16 v[12:15], a[92:95], v[56:59], v[12:15]\nv_mfma_f32_16x16x32_bf16 v[16:19], a[100:103], v[56:59], v[16:19]\ns_waitcnt lgkmcnt(0)\nv_mfma_f32_16x16x32_bf16 v[20:23], v[76:79], v[56:59], v[20:23]\nv_mfma_f32_16x16x32_bf16 v[8:11], a[104:107], v[56:59], v[8:11]\n" \
"v_mfma_f32_16x16x32_bf16 v[12:15], a[112:115], v[56:59], v[12:15]\nv_mfma_f32_16x16x32_bf16 v[16:19], a[120:123], v[56:59], v[16:19]\nv_mfma_f32_16x16x32_bf16 v[20:23], a[128:131], v[56:59], v[20:23]\nv_mfma_f32_16x16x32_bf16 v[8:11], a[108:111], v[56:59], v[8:11]\nv_mfma_f32_16x16x32_bf16 v[12:15], a[116:119], v[56:59], v[12:15]\nv_mfma_f32_16x16x32_bf16 v[16:19], a[124:127], v[56:59], v[16:19]\nv_mfma_f32_16x16x32_bf16 v[20:23], a[132:135], v[56:59], v[20:23]\nds_read_b128 v[76:79], v1 offset:6144\n" \
"v_mfma_f32_16x16x32_bf16 v[8:11], a[136:139], v[56:59], v[8:11]\ns_waitcnt lgkmcnt(0)\nv_mfma_f32_16x16x32_bf16 v[12:15], v[76:79], v[56:59], v[12:15]\nv_mfma_f32_16x16x32_bf16 v[16:19], a[144:147], v[56:59], v[16:19]\nv_mfma_f32_16x16x32_bf16 v[20:23], a[152:155], v[56:59], v[20:23]\nds_read_b128 v[80:83], v1 offset:7168\nv_mfma_f32_16x16x32_bf16 v[8:11], a[140:143], v[56:59], v[8:11]\ns_waitcnt lgkmcnt(0)\n" \
"v_mfma_f32_16x16x32_bf16 v[12:15], v[80:83], v[56:59], v[12:15]\nv_mfma_f32_16x16x32_bf16 v[16:19], a[148:151], v[56:59], v[16:19]\nv_mfma_f32_16x16x32_bf16 v[20:23], a[156:159], v[56:59], v[20:23]\nds_read_b128 v[72:75], v1 offset:8192\nv_mfma_f32_16x16x32_bf16 v[8:11], a[160:163], v[56:59], v[8:11]\nv_mfma_f32_16x16x32_bf16 v[12:15], a[168:171], v[56:59], v[12:15]\ns_waitcnt lgkmcnt(0)\nv_mfma_f32_16x16x32_bf16 v[16:19], v[72:75], v[56:59], v[16:19]\n" \
"v_mfma_f32_16x16x32_bf16 v[20:23], a[176:179], v[56:59], v[20:23]\nds_read_b128 v[76:79], v1 offset:9216\nv_mfma_f32_16x16x32_bf16 v[8:11], a[164:167], v[56:59], v[8:11]\nv_mfma_f32_16x16x32_bf16 v[12:15], a[172:175], v[56:59], v[12:15]\ns_waitcnt lgkmcnt(0)\nv_mfma_f32_16x16x32_bf16 v[16:19], v[76:79], v[56:59], v[16:19]\nv_mfma_f32_16x16x32_bf16 v[20:23], a[180:183], v[56:59], v[20:23]\nds_read_b128 v[80:83], v1 offset:10240\n" \
"v_mfma_f32_16x16x32_bf16 v[8:11], a[184:187], v[56:59], v[8:11]\nv_mfma_f32_16x16x32_bf16 v[12:15], a[192:195], v[56:59], v[12:15]\nv_mfma_f32_16x16x32_bf16 v[16:19], a[200:203], v[56:59], v[16:19]\ns_waitcnt lgkmcnt(0)\nv_mfma_f32_16x16x32_bf16 v[20:23], v[80:83], v[56:59], v[20:23]\nds_read_b128 v[72:75], v1 offset:11264\nv_mfma_f32_16x16x32_bf16 v[8:11], a[188:191], v[56:59], v[8:11]\nv_mfma_f32_16x16x32_bf16 v[12:15], a[196:199], v[56:59], v[12:15]\n" \
"v_mfma_f32_16x16x32_bf16 v[16:19], a[204:207], v[56:59], v[16:19]\ns_waitcnt lgkmcnt(0)\nv_mfma_f32_16x16x32_bf16 v[20:23], v[72:75], v[56:59], v[20:23]\nv_mfma_f32_16x16x32_bf16 v[8:11], a[208:211], v[56:59], v[8:11]\nv_mfma_f32_16x16x32_bf16 v[12:15], a[216:219], v[56:59], v[12:15]\nv_mfma_f32_16x16x32_bf16 v[16:19], a[224:227], v[56:59], v[16:19]\nv_mfma_f32_16x16x32_bf16 v[20:23], a[232:235], v[56:59], v[20:23]\nv_mfma_f32_16x16x32_bf16 v[8:11], a[212:215], v[56:59], v[8:11]\n" \
"v_mfma_f32_16x16x32_bf16 v[12:15], a[220:223], v[56:59], v[12:15]\nv_mfma_f32_16x16x32_bf16 v[16:19], a[228:231], v[56:59], v[16:19]\nv_mfma_f32_16x16x32_bf16 v[20:23], a[236:239], v[56:59], v[20:23]\nds_read_b128 v[72:75], v1 offset:12288\nv_mfma_f32_16x16x32_bf16 v[8:11], a[240:243], v[56:59], v[8:11]\ns_waitcnt lgkmcnt(0)\nv_mfma_f32_16x16x32_bf16 v[12:15], v[72:75], v[56:59], v[12:15]\nv_mfma_f32_16x16x32_bf16 v[16:19], a[248:251], v[56:59], v[16:19]\n" \
"v_mfma_f32_16x16x32_bf16 v[20:23], v[96:99], v[56:59], v[20:23]\nds_read_b128 v[76:79], v1 offset:13312\nv_mfma_f32_16x16x32_bf16 v[8:11], a[244:247], v[56:59], v[8:11]\ns_waitcnt lgkmcnt(0)\nv_mfma_f32_16x16x32_bf16 v[12:15], v[76:79], v[56:59], v[12:15]\nv_mfma_f32_16x16x32_bf16 v[16:19], a[252:255], v[56:59], v[16:19]\nv_mfma_f32_16x16x32_bf16 v[20:23], v[100:103], v[56:59], v[20:23]\nds_read_b128 v[80:83], v1 offset:14336\n" \
"v_mfma_f32_16x16x32_bf16 v[8:11], v[104:107], v[56:59], v[8:11]\nv_mfma_f32_16x16x32_bf16 v[12:15], v[112:115], v[56:59], v[12:15]\ns_waitcnt lgkmcnt(0)\nv_mfma_f32_16x16x32_bf16 v[16:19], v[80:83], v[56:59], v[16:19]\nv_mfma_f32_16x16x32_bf16 v[20:23], v[120:123], v[56:59], v[20:23]\nds_read_b128 v[72:75], v1 offset:15360\nv_mfma_f32_16x16x32_bf16 v[8:11], v[108:111], v[56:59], v[8:11]\nv_mfma_f32_16x16x32_bf16 v[12:15], v[116:119], v[56:59], v[12:15]\n" \
"s_waitcnt lgkmcnt(0)\nv_mfma_f32_16x16x32_bf16 v[16:19], v[72:75], v[56:59], v[16:19]\nv_mfma_f32_16x16x32_bf16 v[20:23], v[124:127], v[56:59], v[20:23]\nds_read_b128 v[76:79], v1 offset:16384\nv_mfma_f32_16x16x32_bf16 v[8:11], v[128:131], v[56:59], v[8:11]\nv_mfma_f32_16x16x32_bf16 v[12:15], v[136:139], v[56:59], v[12:15]\nv_mfma_f32_16x16x32_bf16 v[16:19], v[144:147], v[56:59], v[16:19]\ns_waitcnt lgkmcnt(0)\n" \
"v_mfma_f32_16x16x32_bf16 v[20:23], v[76:79], v[56:59], v[20:23]\nds_read_b128 v[80:83], v1 offset:17408\nv_mfma_f32_16x16x32_bf16 v[8:11], v[132:135], v[56:59], v[8:11]\nv_mfma_f32_16x16x32_bf16 v[12:15], v[140:143], v[56:59], v[12:15]\nv_mfma_f32_16x16x32_bf16 v[16:19], v[148:151], v[56:59], v[16:19]\ns_waitcnt lgkmcnt(0)\nv_mfma_f32_16x16x32_bf16 v[20:23], v[80:83], v[56:59], v[20:23]\nv_mfma_f32_16x16x32_bf16 v[8:11], v[152:155], v[56:59], v[8:11]\n" \
"v_mfma_f32_16x16x32_bf16 v[12:15], v[160:163], v[56:59], v[12:15]\nv_mfma_f32_16x16x32_bf16 v[16:19], v[168:171], v[56:59], v[16:19]\nv_mfma_f32_16x16x32_bf16 v[20:23], v[176:179], v[56:59], v[20:23]\nv_mfma_f32_16x16x32_bf16 v[8:11], v[156:159], v[56:59], v[8:11]\nv_mfma_f32_16x16x32_bf16 v[12:15], v[164:167], v[56:59], v[12:15]\nv_mfma_f32_16x16x32_bf16 v[16:19], v[172:175], v[56:59], v[16:19]\nv_mfma_f32_16x16x32_bf16 v[20:23], v[180:183], v[56:59], v[20:23]\nds_read_b128 v[80:83], v1 offset:18432\n" \
"v_mfma_f32_16x16x32_bf16 v[8:11], v[184:187], v[56:59], v[8:11]\ns_waitcnt lgkmcnt(0)\nv_mfma_f32_16x16x32_bf16 v[12:15], v[80:83], v[56:59], v[12:15]\nv_mfma_f32_16x16x32_bf16 v[16:19], v[192:195], v[56:59], v[16:19]\nv_mfma_f32_16x16x32_bf16 v[20:23], v[200:203], v[56:59], v[20:23]\nds_read_b128 v[72:75], v1 offset:19456\nv_mfma_f32_16x16x32_bf16 v[8:11], v[188:191], v[56:59], v[8:11]\ns_waitcnt lgkmcnt(0)\n" \
"v_mfma_f32_16x16x32_bf16 v[12:15], v[72:75], v[56:59], v[12:15]\nv_mfma_f32_16x16x32_bf16 v[16:19], v[196:199], v[56:59], v[16:19]\nv_mfma_f32_16x16x32_bf16 v[20:23], v[204:207], v[56:59], v[20:23]\nds_read_b128 v[76:79], v1 offset:20480\nv_mfma_f32_16x16x32_bf16 v[8:11], v[208:211], v[56:59], v[8:11]\nv_mfma_f32_16x16x32_bf16 v[12:15], v[216:219], v[56:59], v[12:15]\ns_waitcnt lgkmcnt(0)\nv_mfma_f32_16x16x32_bf16 v[16:19], v[76:79], v[56:59], v[16:19]\n" \
"v_mfma_f32_16x16x32_bf16 v[20:23], v[224:227], v[56:59], v[20:23]\nds_read_b128 v[80:83], v1 offset:21504\nv_mfma_f32_16x16x32_bf16 v[8:11], v[212:215], v[56:59], v[8:11]\nv_mfma_f32_16x16x32_bf16 v[12:15], v[220:223], v[56:59], v[12:15]\ns_waitcnt lgkmcnt(0)\nv_mfma_f32_16x16x32_bf16 v[16:19], v[80:83], v[56:59], v[16:19]\nv_mfma_f32_16x16x32_bf16 v[20:23], v[228:231], v[56:59], v[20:23]\nds_read_b128 v[72:75], v1 offset:22528\n" \
"v_mfma_f32_16x16x32_bf16 v[8:11], v[232:235], v[56:59], v[8:11]\nv_mfma_f32_16x16x32_bf16 v[12:15], v[240:243], v[56:59], v[12:15]\nv_mfma_f32_16x16x32_bf16 v[16:19], v[248:251], v[56:59], v[16:19]\ns_waitcnt lgkmcnt(0)\nv_mfma_f32_16x16x32_bf16 v[20:23], v[72:75], v[56:59], v[20:23]\nds_read_b128 v[76:79], v1 offset:23552\nv_mfma_f32_16x16x32_bf16 v[8:11], v[236:239], v[56:59], v[8:11]\nv_mfma_f32_16x16x32_bf16 v[12:15], v[244:247], v[56:59], v[12:15]\n" \
"v_mfma_f32_16x16x32_bf16 v[16:19], v[252:255], v[56:59], v[16:19]\ns_waitcnt lgkmcnt(0)\nv_mfma_f32_16x16x32_bf16 v[20:23], v[76:79], v[56:59], v[20:23]\n.Lgg_next%=:\ns_cmp_lg_u32 %[w], 0\ns_cbranch_scc1 .Lgg_w1%=\nbuffer_load_dword v56, v3, %[rs], 0 offen sc1\ns_waitcnt vmcnt(0)\n" \
"buffer_store_dword v90, off, %[rs], s56 sc1\n.Lgg_poll%=:\nv_cmp_gt_i32 vcc, s40, v56\ns_cbranch_vccz .Lgg_go%=\ns_sleep 4\nbuffer_load_dword v56, v3, %[rs], 0 offen sc1\ns_waitcnt vmcnt(0)\ns_add_u32 s43, s43, 1\n" \
"s_cmp_lt_u32 s43, 262144\ns_cbranch_scc1 .Lgg_poll%=\ns_branch .Lgg_abort%=\n.Lgg_w1%=:\ns_waitcnt vmcnt(0)\nbuffer_store_dword v90, off, %[rs], s56 sc1\n.Lgg_go%=:\ns_add_u32 s44, s40, 0\n" \
"s_lshl_b32 s44, s44, 17\ns_add_u32 s44, s44, %[xn]\ns_lshr_b32 s45, s44, 8\ns_and_b32 s45, s45, 0x2000\ns_xor_b32 s41, s44, s45\ns_add_u32 s61, s41, %[xw]\ns_barrier\ns_add_u32 m0, s57, 0\n" \
"s_add_u32 s60, s61, 0\nbuffer_load_dwordx4 v0, %[rs], s60 offen offset:0 sc1 lds\nbuffer_load_dwordx4 v0, %[rs], s60 offen offset:1024 sc1 lds\nbuffer_load_dwordx4 v0, %[rs], s60 offen offset:2048 sc1 lds\nbuffer_load_dwordx4 v0, %[rs], s60 offen offset:3072 sc1 lds\ns_add_u32 m0, s57, 16384\ns_add_u32 s60, s61, 32768\nbuffer_load_dwordx4 v0, %[rs], s60 offen offset:0 sc1 lds\n" \
"buffer_load_dwordx4 v0, %[rs], s60 offen offset:1024 sc1 lds\nbuffer_load_dwordx4 v0, %[rs], s60 offen offset:2048 sc1 lds\nbuffer_load_dwordx4 v0, %[rs], s60 offen offset:3072 sc1 lds\ns_add_u32 m0, s57, 32768\ns_add_u32 s60, s61, 65536\nbuffer_load_dwordx4 v0, %[rs], s60 offen offset:0 sc1 lds\nbuffer_load_dwordx4 v0, %[rs], s60 offen offset:1024 sc1 lds\ns_lshl_b32 s44, s40, 19\n" \
"s_add_u32 s44, s44, %[ho]\ns_lshr_b32 s45, s44, 8\ns_and_b32 s45, s45, 0x2000\ns_xor_b32 s55, s44, s45\ns_waitcnt vmcnt(6)\ns_barrier\nds_read_b128 v[56:59], v2 offset:0\nds_read_b128 v[60:63], v2 offset:2048\n" \
"ds_read_b128 v[64:67], v2 offset:4096\ns_waitcnt lgkmcnt(2)\nv_mfma_f32_16x16x32_bf16 v[24:27], a[0:3], v[56:59], 0\nds_read_b128 v[68:71], v2 offset:6144\nds_read_b128 v[80:83], v1 offset:0\nv_mfma_f32_16x16x32_bf16 v[28:31], a[8:11], v[56:59], 0\nbuffer_load_dwordx4 v0, %[rs], s60 offen offset:2048 sc1 lds\nv_mfma_f32_16x16x32_bf16 v[32:35], a[16:19], v[56:59], 0\n" \
"v_mfma_f32_16x16x32_bf16 v[36:39], a[24:27], v[56:59], 0\ns_waitcnt lgkmcnt(3)\nds_read_b128 v[72:75], v1 offset:1024\nv_mfma_f32_16x16x32_bf16 v[24:27], a[4:7], v[60:63], v[24:27]\nds_read_b128 v[56:59], v2 offset:8192\nv_mfma_f32_16x16x32_bf16 v[28:31], a[12:15], v[60:63], v[28:31]\nbuffer_load_dwordx4 v0, %[rs], s60 offen offset:3072 sc1 lds\nv_mfma_f32_16x16x32_bf16 v[32:35], a[20:23], v[60:63], v[32:35]\n" \
"v_mfma_f32_16x16x32_bf16 v[36:39], a[28:31], v[60:63], v[36:39]\ns_waitcnt lgkmcnt(4)\nds_read_b128 v[76:79], v1 offset:2048\nv_mfma_f32_16x16x32_bf16 v[24:27], a[32:35], v[64:67], v[24:27]\ns_waitcnt lgkmcnt(3)\nds_read_b128 v[60:63], v2 offset:10240\nv_mfma_f32_16x16x32_bf16 v[28:31], v[80:83], v[64:67], v[28:31]\nbuffer_load_dwordx4 v[40:43], v5, %[rs], s41 offen offset:0 sc1\n" \
"v_mfma_f32_16x16x32_bf16 v[32:35], a[40:43], v[64:67], v[32:35]\nv_mfma_f32_16x16x32_bf16 v[36:39], a[48:51], v[64:67], v[36:39]\nds_read_b128 v[80:83], v1 offset:3072\nv_mfma_f32_16x16x32_bf16 v[24:27], a[36:39], v[68:71], v[24:27]\ns_waitcnt lgkmcnt(4)\nds_read_b128 v[64:67], v2 offset:12288\nv_mfma_f32_16x16x32_bf16 v[28:31], v[72:75], v[68:71], v[28:31]\nbuffer_load_dwordx4 v[44:47], v5, %[rs], s41 offen offset:256 sc1\n" \
"v_mfma_f32_16x16x32_bf16 v[32:35], a[44:47], v[68:71], v[32:35]\nv_mfma_f32_16x16x32_bf16 v[36:39], a[52:55], v[68:71], v[36:39]\ns_waitcnt lgkmcnt(4)\nds_read_b128 v[72:75], v1 offset:4096\nv_mfma_f32_16x16x32_bf16 v[24:27], a[56:59], v[56:59], v[24:27]\nds_read_b128 v[68:71], v2 offset:14336\nv_mfma_f32_16x16x32_bf16 v[28:31], a[64:67], v[56:59], v[28:31]\ns_waitcnt lgkmcnt(5)\n" \
"buffer_load_dwordx4 v[48:51], v5, %[rs], s41 offen offset:512 sc1\nv_mfma_f32_16x16x32_bf16 v[32:35], v[76:79], v[56:59], v[32:35]\nv_mfma_f32_16x16x32_bf16 v[36:39], a[72:75], v[56:59], v[36:39]\ns_waitcnt lgkmcnt(4)\nds_read_b128 v[76:79], v1 offset:5120\nv_mfma_f32_16x16x32_bf16 v[24:27], a[60:63], v[60:63], v[24:27]\ns_waitcnt vmcnt(7)\ns_barrier\n" \
"ds_read_b128 v[56:59], v2 offset:16384\nv_mfma_f32_16x16x32_bf16 v[28:31], a[68:71], v[60:63], v[28:31]\ns_waitcnt lgkmcnt(5)\nbuffer_load_dwordx4 v[52:55], v5, %[rs], s41 offen offset:768 sc1\nv_mfma_f32_16x16x32_bf16 v[32:35], v[80:83], v[60:63], v[32:35]\nv_mfma_f32_16x16x32_bf16 v[36:39], a[76:79], v[60:63], v[36:39]\ns_waitcnt lgkmcnt(4)\nv_mfma_f32_16x16x32_bf16 v[24:27], a[80:83], v[64:67], v[24:27]\n" \
"ds_read_b128 v[60:63], v2 offset:18432\nv_mfma_f32_16x16x32_bf16 v[28:31], a[88:91], v[64:67], v[28:31]\ns_add_u32 m0, s57, 49152\ns_add_u32 s60, s61, 98304\nbuffer_load_dwordx4 v0, %[rs], s60 offen offset:0 sc1 lds\nv_mfma_f32_16x16x32_bf16 v[32:35], a[96:99], v[64:67], v[32:35]\ns_waitcnt lgkmcnt(4)\nv_mfma_f32_16x16x32_bf16 v[36:39], v[72:75], v[64:67], v[36:39]\n" \
"s_waitcnt lgkmcnt(3)\nv_mfma_f32_16x16x32_bf16 v[24:27], a[84:87], v[68:71], v[24:27]\nds_read_b128 v[64:67], v2 offset:20480\nv_mfma_f32_16x16x32_bf16 v[28:31], a[92:95], v[68:71], v[28:31]\nbuffer_load_dwordx4 v0, %[rs], s60 offen offset:1024 sc1 lds\nv_mfma_f32_16x16x32_bf16 v[32:35], a[100:103], v[68:71], v[32:35]\ns_waitcnt lgkmcnt(3)\nv_mfma_f32_16x16x32_bf16 v[36:39], v[76:79], v[68:71], v[36:39]\n" \
"s_waitcnt lgkmcnt(2)\nds_read_b128 v[76:79], v1 offset:6144\nv_mfma_f32_16x16x32_bf16 v[24:27], a[104:107], v[56:59], v[24:27]\nds_read_b128 v[68:71], v2 offset:22528\nv_mfma_f32_16x16x32_bf16 v[28:31], a[112:115], v[56:59], v[28:31]\nbuffer_load_dwordx4 v0, %[rs], s60 offen offset:2048 sc1 lds\nv_mfma_f32_16x16x32_bf16 v[32:35], a[120:123], v[56:59], v[32:35]\nv_mfma_f32_16x16x32_bf16 v[36:39], a[128:131], v[56:59], v[36:39]\n" \
"s_waitcnt lgkmcnt(3)\nds_read_b128 v[80:83], v1 offset:7168\nv_mfma_f32_16x16x32_bf16 v[24:27], a[108:111], v[60:63], v[24:27]\nds_read_b128 v[56:59], v2 offset:24576\nv_mfma_f32_16x16x32_bf16 v[28:31], a[116:119], v[60:63], v[28:31]\nbuffer_load_dwordx4 v0, %[rs], s60 offen offset:3072 sc1 lds\nv_mfma_f32_16x16x32_bf16 v[32:35], a[124:127], v[60:63], v[32:35]\nv_mfma_f32_16x16x32_bf16 v[36:39], a[132:135], v[60:63], v[36:39]\n" \
"s_waitcnt lgkmcnt(4)\nds_read_b128 v[72:75], v1 offset:8192\nv_mfma_f32_16x16x32_bf16 v[24:27], a[136:139], v[64:67], v[24:27]\ns_waitcnt lgkmcnt(4)\nds_read_b128 v[60:63], v2 offset:26624\nv_mfma_f32_16x16x32_bf16 v[28:31], v[76:79], v[64:67], v[28:31]\nv_mfma_f32_16x16x32_bf16 v[32:35], a[144:147], v[64:67], v[32:35]\nv_mfma_f32_16x16x32_bf16 v[36:39], a[152:155], v[64:67], v[36:39]\n" \
"s_waitcnt lgkmcnt(4)\nds_read_b128 v[76:79], v1 offset:9216\nv_mfma_f32_16x16x32_bf16 v[24:27], a[140:143], v[68:71], v[24:27]\ns_waitcnt lgkmcnt(4)\nds_read_b128 v[64:67], v2 offset:28672\nv_mfma_f32_16x16x32_bf16 v[28:31], v[80:83], v[68:71], v[28:31]\nv_mfma_f32_16x16x32_bf16 v[32:35], a[148:151], v[68:71], v[32:35]\nv_mfma_f32_16x16x32_bf16 v[36:39], a[156:159], v[68:71], v[36:39]\n" \
"s_waitcnt lgkmcnt(4)\nds_read_b128 v[80:83], v1 offset:10240\nv_mfma_f32_16x16x32_bf16 v[24:27], a[160:163], v[56:59], v[24:27]\nds_read_b128 v[68:71], v2 offset:30720\nv_mfma_f32_16x16x32_bf16 v[28:31], a[168:171], v[56:59], v[28:31]\ns_waitcnt lgkmcnt(5)\nv_mfma_f32_16x16x32_bf16 v[32:35], v[72:75], v[56:59], v[32:35]\nv_mfma_f32_16x16x32_bf16 v[36:39], a[176:179], v[56:59], v[36:39]\n" \
"s_waitcnt lgkmcnt(4)\nds_read_b128 v[72:75], v1 offset:11264\nv_mfma_f32_16x16x32_bf16 v[24:27], a[164:167], v[60:63], v[24:27]\ns_waitcnt vmcnt(8)\ns_barrier\nds_read_b128 v[56:59], v2 offset:32768\nv_mfma_f32_16x16x32_bf16 v[28:31], a[172:175], v[60:63], v[28:31]\ns_waitcnt lgkmcnt(5)\n" \
"v_mfma_f32_16x16x32_bf16 v[32:35], v[76:79], v[60:63], v[32:35]\nv_mfma_f32_16x16x32_bf16 v[36:39], a[180:183], v[60:63], v[36:39]\ns_waitcnt lgkmcnt(4)\nv_mfma_f32_16x16x32_bf16 v[24:27], a[184:187], v[64:67], v[24:27]\ns_waitcnt vmcnt(4)\nds_read_b128 v[60:63], v2 offset:34816\nv_add_f32 v84, v40, v44\nv_mfma_f32_16x16x32_bf16 v[28:31], a[192:195], v[64:67], v[28:31]\n" \
"v_add_f32 v85, v48, v52\nv_add_f32 v84, v84, v85\nv_mfma_f32_16x16x32_bf16 v[32:35], a[200:203], v[64:67], v[32:35]\nv_mov_b32 v85, v84\ns_nop 0\ns_waitcnt lgkmcnt(4)\nv_permlane16_swap_b32 v84, v85\nv_mfma_f32_16x16x32_bf16 v[36:39], v[80:83], v[64:67], v[36:39]\n" \
"s_waitcnt lgkmcnt(3)\nv_add_f32 v84, v84, v85\nv_mov_b32 v85, v84\nv_mfma_f32_16x16x32_bf16 v[24:27], a[188:191], v[68:71], v[24:27]\nds_read_b128 v[64:67], v2 offset:36864\ns_nop 0\nv_permlane32_swap_b32 v84, v85\nv_mfma_f32_16x16x32_bf16 v[28:31], a[196:199], v[68:71], v[28:31]\n" \
"v_add_f32 v84, v84, v85\nv_mul_f32 v84, 0xbd800000, v84\nv_mfma_f32_16x16x32_bf16 v[32:35], a[204:207], v[68:71], v[32:35]\ns_waitcnt lgkmcnt(3)\nv_add_f32 v40, v40, v84\nv_mul_f32 v40, 0x41000000, v40\nv_mfma_f32_16x16x32_bf16 v[36:39], v[72:75], v[68:71], v[36:39]\ns_waitcnt lgkmcnt(2)\n" \
"ds_read_b128 v[72:75], v1 offset:12288\nv_fma_f32 v41, v40, v40, v41\nv_add_f32 v44, v44, v84\nv_mfma_f32_16x16x32_bf16 v[24:27], a[208:211], v[56:59], v[24:27]\nds_read_b128 v[68:71], v2 offset:38912\nv_mul_f32 v44, 0x41000000, v44\nv_mfma_f32_16x16x32_bf16 v[28:31], a[216:219], v[56:59], v[28:31]\nv_fma_f32 v45, v44, v44, v45\n" \
"v_add_f32 v48, v48, v84\nv_mfma_f32_16x16x32_bf16 v[32:35], a[224:227], v[56:59], v[32:35]\nv_mul_f32 v48, 0x41000000, v48\nv_fma_f32 v49, v48, v48, v49\nv_mfma_f32_16x16x32_bf16 v[36:39], a[232:235], v[56:59], v[36:39]\ns_waitcnt lgkmcnt(3)\nds_read_b128 v[76:79], v1 offset:13312\nv_add_f32 v52, v52, v84\n" \
"v_mul_f32 v52, 0x41000000, v52\nv_mfma_f32_16x16x32_bf16 v[24:27], a[212:215], v[60:63], v[24:27]\nds_read_b128 v[56:59], v2 offset:40960\nv_fma_f32 v53, v52, v52, v53\nv_mfma_f32_16x16x32_bf16 v[28:31], a[220:223], v[60:63], v[28:31]\nv_add_f32 v85, v41, v45\nv_add_f32 v86, v49, v53\nv_mfma_f32_16x16x32_bf16 v[32:35], a[228:231], v[60:63], v[32:35]\n" \
"v_add_f32 v85, v85, v86\nv_mov_b32 v86, v85\nv_mfma_f32_16x16x32_bf16 v[36:39], a[236:239], v[60:63], v[36:39]\nds_read_b128 v[80:83], v1 offset:14336\ns_nop 0\ns_waitcnt lgkmcnt(5)\nv_permlane16_swap_b32 v85, v86\nv_mfma_f32_16x16x32_bf16 v[24:27], a[240:243], v[64:67], v[24:27]\n" \
"s_waitcnt lgkmcnt(4)\nds_read_b128 v[60:63], v2 offset:43008\nv_add_f32 v85, v85, v86\nv_mfma_f32_16x16x32_bf16 v[28:31], v[72:75], v[64:67], v[28:31]\nv_mov_b32 v86, v85\ns_nop 0\ns_nop 0\nv_permlane32_swap_b32 v85, v86\n" \
"v_mfma_f32_16x16x32_bf16 v[32:35], a[248:251], v[64:67], v[32:35]\nv_add_f32 v85, v85, v86\nv_mul_f32 v85, 0x3a800000, v85\nv_mfma_f32_16x16x32_bf16 v[36:39], v[96:99], v[64:67], v[36:39]\ns_waitcnt lgkmcnt(4)\nds_read_b128 v[72:75], v1 offset:15360\nv_cvt_pk_bf16_f32 v85, v85, v85\nv_and_b32 v85, 0xffff0000, v85\n" \
"v_mfma_f32_16x16x32_bf16 v[24:27], a[244:247], v[68:71], v[24:27]\ns_waitcnt lgkmcnt(4)\nds_read_b128 v[64:67], v2 offset:45056\nv_add_f32 v85, 0x3727c5ac, v85\nv_mfma_f32_16x16x32_bf16 v[28:31], v[76:79], v[68:71], v[28:31]\nv_cvt_pk_bf16_f32 v85, v85, v85\nv_and_b32 v85, 0xffff0000, v85\nv_mfma_f32_16x16x32_bf16 v[32:35], a[252:255], v[68:71], v[32:35]\n" \
"v_sqrt_f32 v85, v85\nv_mfma_f32_16x16x32_bf16 v[36:39], v[100:103], v[68:71], v[36:39]\ns_waitcnt lgkmcnt(4)\nds_read_b128 v[76:79], v1 offset:16384\nv_cvt_pk_bf16_f32 v84, v84, v84\nv_and_b32 v84, 0xffff0000, v84\nv_mfma_f32_16x16x32_bf16 v[24:27], v[104:107], v[56:59], v[24:27]\nds_read_b128 v[68:71], v2 offset:47104\n" \
"v_cvt_pk_bf16_f32 v85, v85, v85\nv_mfma_f32_16x16x32_bf16 v[28:31], v[112:115], v[56:59], v[28:31]\ns_waitcnt lgkmcnt(5)\nv_and_b32 v85, 0xffff0000, v85\nv_mul_f32 v86, s58, v85\nv_mfma_f32_16x16x32_bf16 v[32:35], v[80:83], v[56:59], v[32:35]\nv_rcp_f32 v85, v85\nv_mfma_f32_16x16x32_bf16 v[36:39], v[120:123], v[56:59], v[36:39]\n" \
"ds_read_b128 v[80:83], v1 offset:17408\ns_nop 0\ns_waitcnt lgkmcnt(5)\nv_mul_f32 v85, s58, v85\nv_add_f32 v87, v42, v46\nv_mfma_f32_16x16x32_bf16 v[24:27], v[108:111], v[60:63], v[24:27]\ns_waitcnt vmcnt(0)\ns_barrier\n" \
"ds_read_b128 v[56:59], v2 offset:49152\nv_mfma_f32_16x16x32_bf16 v[28:31], v[116:119], v[60:63], v[28:31]\ns_waitcnt lgkmcnt(5)\nv_add_f32 v88, v50, v54\nv_add_f32 v87, v87, v88\nv_mfma_f32_16x16x32_bf16 v[32:35], v[72:75], v[60:63], v[32:35]\nv_mov_b32 v88, v87\ns_nop 0\n" \
"s_nop 0\nv_permlane16_swap_b32 v87, v88\nv_mfma_f32_16x16x32_bf16 v[36:39], v[124:127], v[60:63], v[36:39]\ns_waitcnt lgkmcnt(4)\nv_add_f32 v87, v87, v88\nv_mov_b32 v88, v87\nv_mfma_f32_16x16x32_bf16 v[24:27], v[128:131], v[64:67], v[24:27]\nds_read_b128 v[60:63], v2 offset:51200\n" \
"s_nop 0\nv_permlane32_swap_b32 v87, v88\nv_mfma_f32_16x16x32_bf16 v[28:31], v[136:139], v[64:67], v[28:31]\nv_add_f32 v87, v87, v88\nv_mul_f32 v87, 0xbd800000, v87\nv_mfma_f32_16x16x32_bf16 v[32:35], v[144:147], v[64:67], v[32:35]\ns_waitcnt lgkmcnt(4)\nv_add_f32 v42, v42, v87\n" \
"v_mul_f32 v42, 0x41000000, v42\nv_mfma_f32_16x16x32_bf16 v[36:39], v[76:79], v[64:67], v[36:39]\ns_waitcnt lgkmcnt(3)\nv_fma_f32 v43, v42, v42, v43\nv_add_f32 v46, v46, v87\nv_mfma_f32_16x16x32_bf16 v[24:27], v[132:135], v[68:71], v[24:27]\nds_read_b128 v[64:67], v2 offset:53248\nv_mul_f32 v46, 0x41000000, v46\n" \
"v_mfma_f32_16x16x32_bf16 v[28:31], v[140:143], v[68:71], v[28:31]\nv_fma_f32 v47, v46, v46, v47\nv_add_f32 v50, v50, v87\nv_mfma_f32_16x16x32_bf16 v[32:35], v[148:151], v[68:71], v[32:35]\ns_waitcnt lgkmcnt(3)\nv_mul_f32 v50, 0x41000000, v50\nv_fma_f32 v51, v50, v50, v51\nv_mfma_f32_16x16x32_bf16 v[36:39], v[80:83], v[68:71], v[36:39]\n" \
"s_waitcnt lgkmcnt(2)\nds_read_b128 v[80:83], v1 offset:18432\nv_add_f32 v54, v54, v87\nv_mul_f32 v54, 0x41000000, v54\nv_mfma_f32_16x16x32_bf16 v[24:27], v[152:155], v[56:59], v[24:27]\nds_read_b128 v[68:71], v2 offset:55296\nv_fma_f32 v55, v54, v54, v55\nv_mfma_f32_16x16x32_bf16 v[28:31], v[160:163], v[56:59], v[28:31]\n" \
"v_add_f32 v88, v43, v47\nv_add_f32 v89, v51, v55\nv_mfma_f32_16x16x32_bf16 v[32:35], v[168:171], v[56:59], v[32:35]\nv_add_f32 v88, v88, v89\nv_mov_b32 v89, v88\nv_mfma_f32_16x16x32_bf16 v[36:39], v[176:179], v[56:59], v[36:39]\nds_read_b128 v[72:75], v1 offset:19456\ns_nop 0\n" \
"s_waitcnt lgkmcnt(4)\nv_permlane16_swap_b32 v88, v89\nv_mfma_f32_16x16x32_bf16 v[24:27], v[156:159], v[60:63], v[24:27]\nds_read_b128 v[56:59], v2 offset:57344\nv_add_f32 v88, v88, v89\nv_mfma_f32_16x16x32_bf16 v[28:31], v[164:167], v[60:63], v[28:31]\nv_mov_b32 v89, v88\ns_nop 0\n" \
"s_nop 0\nv_permlane32_swap_b32 v88, v89\nv_mfma_f32_16x16x32_bf16 v[32:35], v[172:175], v[60:63], v[32:35]\nv_add_f32 v88, v88, v89\nv_mul_f32 v88, 0x3a800000, v88\nv_mfma_f32_16x16x32_bf16 v[36:39], v[180:183], v[60:63], v[36:39]\ns_waitcnt lgkmcnt(4)\nds_read_b128 v[76:79], v1 offset:20480\n" \
"v_cvt_pk_bf16_f32 v88, v88, v88\nv_and_b32 v88, 0xffff0000, v88\nv_mfma_f32_16x16x32_bf16 v[24:27], v[184:187], v[64:67], v[24:27]\ns_waitcnt lgkmcnt(4)\nds_read_b128 v[60:63], v2 offset:59392\nv_add_f32 v88, 0x3727c5ac, v88\nv_mfma_f32_16x16x32_bf16 v[28:31], v[80:83], v[64:67], v[28:31]\nv_cvt_pk_bf16_f32 v88, v88, v88\n" \
"v_and_b32 v88, 0xffff0000, v88\nv_mfma_f32_16x16x32_bf16 v[32:35], v[192:195], v[64:67], v[32:35]\nv_sqrt_f32 v88, v88\nv_mfma_f32_16x16x32_bf16 v[36:39], v[200:203], v[64:67], v[36:39]\ns_waitcnt lgkmcnt(4)\nds_read_b128 v[80:83], v1 offset:21504\nv_cvt_pk_bf16_f32 v87, v87, v87\nv_and_b32 v87, 0xffff0000, v87\n" \
"v_mfma_f32_16x16x32_bf16 v[24:27], v[188:191], v[68:71], v[24:27]\ns_waitcnt lgkmcnt(4)\nds_read_b128 v[64:67], v2 offset:61440\nv_cvt_pk_bf16_f32 v88, v88, v88\nv_mfma_f32_16x16x32_bf16 v[28:31], v[72:75], v[68:71], v[28:31]\nv_and_b32 v88, 0xffff0000, v88\nv_mul_f32 v89, s58, v88\nv_mfma_f32_16x16x32_bf16 v[32:35], v[196:199], v[68:71], v[32:35]\n" \
"v_rcp_f32 v88, v88\nv_mfma_f32_16x16x32_bf16 v[36:39], v[204:207], v[68:71], v[36:39]\nds_read_b128 v[72:75], v1 offset:22528\ns_nop 0\ns_waitcnt lgkmcnt(5)\nv_mul_f32 v88, s58, v88\nv_mfma_f32_16x16x32_bf16 v[24:27], v[208:211], v[56:59], v[24:27]\nds_read_b128 v[68:71], v2 offset:63488\n" \
"v_mfma_f32_16x16x32_bf16 v[28:31], v[216:219], v[56:59], v[28:31]\ns_waitcnt lgkmcnt(5)\nv_mfma_f32_16x16x32_bf16 v[32:35], v[76:79], v[56:59], v[32:35]\nv_mfma_f32_16x16x32_bf16 v[36:39], v[224:227], v[56:59], v[36:39]\ns_waitcnt lgkmcnt(4)\nds_read_b128 v[76:79], v1 offset:23552\nv_mfma_f32_16x16x32_bf16 v[24:27], v[212:215], v[60:63], v[24:27]\nds_read_b128 v[56:59], v2 offset:256\n" \
"v_mfma_f32_16x16x32_bf16 v[28:31], v[220:223], v[60:63], v[28:31]\ns_waitcnt lgkmcnt(5)\nv_mfma_f32_16x16x32_bf16 v[32:35], v[80:83], v[60:63], v[32:35]\nv_mfma_f32_16x16x32_bf16 v[36:39], v[228:231], v[60:63], v[36:39]\ns_waitcnt lgkmcnt(4)\nv_mfma_f32_16x16x32_bf16 v[24:27], v[232:235], v[64:67], v[24:27]\nds_read_b128 v[60:63], v2 offset:2304\nv_mfma_f32_16x16x32_bf16 v[28:31], v[240:243], v[64:67], v[28:31]\n" \
"v_mfma_f32_16x16x32_bf16 v[32:35], v[248:251], v[64:67], v[32:35]\ns_waitcnt lgkmcnt(4)\nv_mfma_f32_16x16x32_bf16 v[36:39], v[72:75], v[64:67], v[36:39]\ns_waitcnt lgkmcnt(3)\nv_mfma_f32_16x16x32_bf16 v[24:27], v[236:239], v[68:71], v[24:27]\nds_read_b128 v[64:67], v2 offset:4352\nv_mfma_f32_16x16x32_bf16 v[28:31], v[244:247], v[68:71], v[28:31]\nv_mfma_f32_16x16x32_bf16 v[32:35], v[252:255], v[68:71], v[32:35]\n" \
"s_waitcnt lgkmcnt(3)\nv_mfma_f32_16x16x32_bf16 v[36:39], v[76:79], v[68:71], v[36:39]\ns_waitcnt lgkmcnt(2)\nds_read_b128 v[76:79], v1 offset:0\nv_mfma_f32_16x16x32_bf16 v[40:43], a[0:3], v[56:59], 0\nds_read_b128 v[68:71], v2 offset:6400\nv_mfma_f32_16x16x32_bf16 v[44:47], a[8:11], v[56:59], 0\nv_mfma_f32_16x16x32_bf16 v[48:51], a[16:19], v[56:59], 0\n" \
"v_mfma_f32_16x16x32_bf16 v[52:55], a[24:27], v[56:59], 0\ns_waitcnt lgkmcnt(3)\nds_read_b128 v[80:83], v1 offset:1024\nv_fma_f32 v24, v8, v84, v24\nv_fma_f32 v25, v9, v84, v25\nv_mfma_f32_16x16x32_bf16 v[40:43], a[4:7], v[60:63], v[40:43]\nds_read_b128 v[56:59], v2 offset:8448\nv_fma_f32 v32, v16, v84, v32\n" \
"v_mfma_f32_16x16x32_bf16 v[44:47], a[12:15], v[60:63], v[44:47]\nv_fma_f32 v33, v17, v84, v33\nv_mul_f32 v24, v24, v85\nv_mfma_f32_16x16x32_bf16 v[48:51], a[20:23], v[60:63], v[48:51]\nv_mul_f32 v25, v25, v85\nv_fma_f32 v91, v24, v24, s59\nv_mfma_f32_16x16x32_bf16 v[52:55], a[28:31], v[60:63], v[52:55]\ns_waitcnt lgkmcnt(4)\n" \
"ds_read_b128 v[72:75], v1 offset:2048\nv_fma_f32 v92, v25, v25, s59\nv_mul_f32 v91, v91, v24\nv_mfma_f32_16x16x32_bf16 v[40:43], a[32:35], v[64:67], v[40:43]\ns_waitcnt lgkmcnt(4)\nds_read_b128 v[60:63], v2 offset:10496\nv_mul_f32 v92, v92, v25\nv_mfma_f32_16x16x32_bf16 v[44:47], v[76:79], v[64:67], v[44:47]\n" \
"v_exp_f32 v91, v91\nv_mfma_f32_16x16x32_bf16 v[48:51], a[40:43], v[64:67], v[48:51]\nv_exp_f32 v92, v92\nv_mfma_f32_16x16x32_bf16 v[52:55], a[48:51], v[64:67], v[52:55]\ns_waitcnt lgkmcnt(4)\nds_read_b128 v[76:79], v1 offset:3072\nv_mul_f32 v24, v24, v32\nv_mul_f32 v25, v25, v33\n" \
"v_mfma_f32_16x16x32_bf16 v[40:43], a[36:39], v[68:71], v[40:43]\ns_waitcnt lgkmcnt(4)\nds_read_b128 v[64:67], v2 offset:12544\nv_fma_f32 v91, v91, v86, v86\nv_mfma_f32_16x16x32_bf16 v[44:47], v[80:83], v[68:71], v[44:47]\nv_fma_f32 v92, v92, v86, v86\nv_rcp_f32 v91, v91\nv_mfma_f32_16x16x32_bf16 v[48:51], a[44:47], v[68:71], v[48:51]\n" \
"v_rcp_f32 v92, v92\nv_mfma_f32_16x16x32_bf16 v[52:55], a[52:55], v[68:71], v[52:55]\ns_waitcnt lgkmcnt(4)\nds_read_b128 v[80:83], v1 offset:4096\nv_mul_f32 v24, v24, v91\nv_mul_f32 v25, v25, v92\nv_mfma_f32_16x16x32_bf16 v[40:43], a[56:59], v[56:59], v[40:43]\nds_read_b128 v[68:71], v2 offset:14592\n" \
"v_cvt_pk_bf16_f32 v32, v24, v25\nv_mfma_f32_16x16x32_bf16 v[44:47], a[64:67], v[56:59], v[44:47]\ns_waitcnt lgkmcnt(5)\nv_fma_f32 v26, v10, v84, v26\nv_fma_f32 v27, v11, v84, v27\nv_mfma_f32_16x16x32_bf16 v[48:51], v[72:75], v[56:59], v[48:51]\nv_fma_f32 v34, v18, v84, v34\nv_fma_f32 v35, v19, v84, v35\n" \
"v_mfma_f32_16x16x32_bf16 v[52:55], a[72:75], v[56:59], v[52:55]\ns_waitcnt lgkmcnt(4)\nds_read_b128 v[72:75], v1 offset:5120\nv_mul_f32 v26, v26, v85\nv_mul_f32 v27, v27, v85\nv_mfma_f32_16x16x32_bf16 v[40:43], a[60:63], v[60:63], v[40:43]\nds_read_b128 v[56:59], v2 offset:16640\nv_fma_f32 v91, v26, v26, s59\n" \
"v_mfma_f32_16x16x32_bf16 v[44:47], a[68:71], v[60:63], v[44:47]\ns_waitcnt lgkmcnt(5)\nv_fma_f32 v92, v27, v27, s59\nv_mul_f32 v91, v91, v26\nv_mfma_f32_16x16x32_bf16 v[48:51], v[76:79], v[60:63], v[48:51]\nv_mul_f32 v92, v92, v27\nv_exp_f32 v91, v91\nv_mfma_f32_16x16x32_bf16 v[52:55], a[76:79], v[60:63], v[52:55]\n" \
"s_waitcnt lgkmcnt(4)\nv_exp_f32 v92, v92\nv_mfma_f32_16x16x32_bf16 v[40:43], a[80:83], v[64:67], v[40:43]\nds_read_b128 v[60:63], v2 offset:18688\nv_mul_f32 v26, v26, v34\nv_mfma_f32_16x16x32_bf16 v[44:47], a[88:91], v[64:67], v[44:47]\nv_mul_f32 v27, v27, v35\nv_fma_f32 v91, v91, v86, v86\n" \
"v_mfma_f32_16x16x32_bf16 v[48:51], a[96:99], v[64:67], v[48:51]\ns_waitcnt lgkmcnt(4)\nv_fma_f32 v92, v92, v86, v86\nv_rcp_f32 v91, v91\nv_mfma_f32_16x16x32_bf16 v[52:55], v[80:83], v[64:67], v[52:55]\ns_waitcnt lgkmcnt(3)\nv_rcp_f32 v92, v92\nv_mfma_f32_16x16x32_bf16 v[40:43], a[84:87], v[68:71], v[40:43]\n" \
"ds_read_b128 v[64:67], v2 offset:20736\nv_mul_f32 v26, v26, v91\nv_mfma_f32_16x16x32_bf16 v[44:47], a[92:95], v[68:71], v[44:47]\nv_mul_f32 v27, v27, v92\nv_cvt_pk_bf16_f32 v33, v26, v27\nv_mfma_f32_16x16x32_bf16 v[48:51], a[100:103], v[68:71], v[48:51]\ns_waitcnt lgkmcnt(3)\nv_fma_f32 v28, v12, v84, v28\n" \
"v_fma_f32 v29, v13, v84, v29\nv_mfma_f32_16x16x32_bf16 v[52:55], v[72:75], v[68:71], v[52:55]\ns_waitcnt lgkmcnt(2)\nds_read_b128 v[72:75], v1 offset:6144\nv_fma_f32 v36, v20, v84, v36\nv_fma_f32 v37, v21, v84, v37\nv_mfma_f32_16x16x32_bf16 v[40:43], a[104:107], v[56:59], v[40:43]\nds_read_b128 v[68:71], v2 offset:22784\n" \
"v_mul_f32 v28, v28, v85\nv_mfma_f32_16x16x32_bf16 v[44:47], a[112:115], v[56:59], v[44:47]\nv_mul_f32 v29, v29, v85\nv_fma_f32 v91, v28, v28, s59\nv_mfma_f32_16x16x32_bf16 v[48:51], a[120:123], v[56:59], v[48:51]\nv_fma_f32 v92, v29, v29, s59\nv_mul_f32 v91, v91, v28\nv_mfma_f32_16x16x32_bf16 v[52:55], a[128:131], v[56:59], v[52:55]\n" \
"s_waitcnt lgkmcnt(3)\nds_read_b128 v[76:79], v1 offset:7168\nv_mul_f32 v92, v92, v29\nv_exp_f32 v91, v91\nv_mfma_f32_16x16x32_bf16 v[40:43], a[108:111], v[60:63], v[40:43]\nds_read_b128 v[56:59], v2 offset:24832\nv_exp_f32 v92, v92\nv_mfma_f32_16x16x32_bf16 v[44:47], a[116:119], v[60:63], v[44:47]\n" \
"v_mul_f32 v28, v28, v36\nv_mul_f32 v29, v29, v37\nv_mfma_f32_16x16x32_bf16 v[48:51], a[124:127], v[60:63], v[48:51]\nv_fma_f32 v91, v91, v86, v86\nv_fma_f32 v92, v92, v86, v86\nv_mfma_f32_16x16x32_bf16 v[52:55], a[132:135], v[60:63], v[52:55]\ns_waitcnt lgkmcnt(4)\nds_read_b128 v[80:83], v1 offset:8192\n" \
"v_rcp_f32 v91, v91\nv_mfma_f32_16x16x32_bf16 v[40:43], a[136:139], v[64:67], v[40:43]\ns_waitcnt lgkmcnt(4)\nds_read_b128 v[60:63], v2 offset:26880\nv_rcp_f32 v92, v92\nv_mfma_f32_16x16x32_bf16 v[44:47], v[72:75], v[64:67], v[44:47]\nv_mul_f32 v28, v28, v91\nv_mul_f32 v29, v29, v92\n" \
"v_mfma_f32_16x16x32_bf16 v[48:51], a[144:147], v[64:67], v[48:51]\nv_cvt_pk_bf16_f32 v34, v28, v29\nv_fma_f32 v30, v14, v84, v30\nv_mfma_f32_16x16x32_bf16 v[52:55], a[152:155], v[64:67], v[52:55]\ns_waitcnt lgkmcnt(4)\nds_read_b128 v[72:75], v1 offset:9216\nv_fma_f32 v31, v15, v84, v31\nv_fma_f32 v38, v22, v84, v38\n" \
"v_mfma_f32_16x16x32_bf16 v[40:43], a[140:143], v[68:71], v[40:43]\ns_waitcnt lgkmcnt(4)\nds_read_b128 v[64:67], v2 offset:28928\nv_fma_f32 v39, v23, v84, v39\nv_mfma_f32_16x16x32_bf16 v[44:47], v[76:79], v[68:71], v[44:47]\nv_mul_f32 v30, v30, v85\nv_mul_f32 v31, v31, v85\nv_mfma_f32_16x16x32_bf16 v[48:51], a[148:151], v[68:71], v[48:51]\n" \
"v_fma_f32 v91, v30, v30, s59\nv_fma_f32 v92, v31, v31, s59\nv_mfma_f32_16x16x32_bf16 v[52:55], a[156:159], v[68:71], v[52:55]\ns_waitcnt lgkmcnt(4)\nds_read_b128 v[76:79], v1 offset:10240\nv_mul_f32 v91, v91, v30\nv_mul_f32 v92, v92, v31\nv_mfma_f32_16x16x32_bf16 v[40:43], a[160:163], v[56:59], v[40:43]\n" \
"ds_read_b128 v[68:71], v2 offset:30976\nv_exp_f32 v91, v91\nv_mfma_f32_16x16x32_bf16 v[44:47], a[168:171], v[56:59], v[44:47]\ns_waitcnt lgkmcnt(5)\nv_exp_f32 v92, v92\nv_mfma_f32_16x16x32_bf16 v[48:51], v[80:83], v[56:59], v[48:51]\nv_mul_f32 v30, v30, v38\nv_mul_f32 v31, v31, v39\n" \
"v_mfma_f32_16x16x32_bf16 v[52:55], a[176:179], v[56:59], v[52:55]\ns_waitcnt lgkmcnt(4)\nds_read_b128 v[80:83], v1 offset:11264\nv_fma_f32 v91, v91, v86, v86\nv_fma_f32 v92, v92, v86, v86\nv_mfma_f32_16x16x32_bf16 v[40:43], a[164:167], v[60:63], v[40:43]\nds_read_b128 v[56:59], v2 offset:33024\nv_rcp_f32 v91, v91\n" \
"v_mfma_f32_16x16x32_bf16 v[44:47], a[172:175], v[60:63], v[44:47]\ns_waitcnt lgkmcnt(5)\nv_rcp_f32 v92, v92\nv_mfma_f32_16x16x32_bf16 v[48:51], v[72:75], v[60:63], v[48:51]\nv_mul_f32 v30, v30, v91\nv_mul_f32 v31, v31, v92\nv_mfma_f32_16x16x32_bf16 v[52:55], a[180:183], v[60:63], v[52:55]\ns_waitcnt lgkmcnt(4)\n" \
"v_cvt_pk_bf16_f32 v35, v30, v31\nbuffer_store_dwordx4 v[32:35], v4, %[rs], s55 offen offset:0 sc1\nv_mfma_f32_16x16x32_bf16 v[40:43], a[184:187], v[64:67], v[40:43]\nds_read_b128 v[60:63], v2 offset:35072\nv_mfma_f32_16x16x32_bf16 v[44:47], a[192:195], v[64:67], v[44:47]\nv_mfma_f32_16x16x32_bf16 v[48:51], a[200:203], v[64:67], v[48:51]\ns_waitcnt lgkmcnt(4)\nv_mfma_f32_16x16x32_bf16 v[52:55], v[76:79], v[64:67], v[52:55]\n" \
"s_waitcnt lgkmcnt(3)\nv_mfma_f32_16x16x32_bf16 v[40:43], a[188:191], v[68:71], v[40:43]\nds_read_b128 v[64:67], v2 offset:37120\nv_mfma_f32_16x16x32_bf16 v[44:47], a[196:199], v[68:71], v[44:47]\nv_mfma_f32_16x16x32_bf16 v[48:51], a[204:207], v[68:71], v[48:51]\ns_waitcnt lgkmcnt(3)\nv_mfma_f32_16x16x32_bf16 v[52:55], v[80:83], v[68:71], v[52:55]\ns_waitcnt lgkmcnt(2)\n" \
"ds_read_b128 v[80:83], v1 offset:12288\nv_mfma_f32_16x16x32_bf16 v[40:43], a[208:211], v[56:59], v[40:43]\nds_read_b128 v[68:71], v2 offset:39168\nv_mfma_f32_16x16x32_bf16 v[44:47], a[216:219], v[56:59], v[44:47]\nv_mfma_f32_16x16x32_bf16 v[48:51], a[224:227], v[56:59], v[48:51]\nv_mfma_f32_16x16x32_bf16 v[52:55], a[232:235], v[56:59], v[52:55]\ns_waitcnt lgkmcnt(3)\nds_read_b128 v[72:75], v1 offset:13312\n" \
"v_mfma_f32_16x16x32_bf16 v[40:43], a[212:215], v[60:63], v[40:43]\nds_read_b128 v[56:59], v2 offset:41216\nv_mfma_f32_16x16x32_bf16 v[44:47], a[220:223], v[60:63], v[44:47]\nv_mfma_f32_16x16x32_bf16 v[48:51], a[228:231], v[60:63], v[48:51]\nv_mfma_f32_16x16x32_bf16 v[52:55], a[236:239], v[60:63], v[52:55]\ns_waitcnt lgkmcnt(4)\nds_read_b128 v[76:79], v1 offset:14336\nv_mfma_f32_16x16x32_bf16 v[40:43], a[240:243], v[64:67], v[40:43]\n" \
"s_waitcnt lgkmcnt(4)\nds_read_b128 v[60:63], v2 offset:43264\nv_mfma_f32_16x16x32_bf16 v[44:47], v[80:83], v[64:67], v[44:47]\nv_mfma_f32_16x16x32_bf16 v[48:51], a[248:251], v[64:67], v[48:51]\nv_mfma_f32_16x16x32_bf16 v[52:55], v[96:99], v[64:67], v[52:55]\ns_waitcnt lgkmcnt(4)\nds_read_b128 v[80:83], v1 offset:15360\nv_mfma_f32_16x16x32_bf16 v[40:43], a[244:247], v[68:71], v[40:43]\n" \
"s_waitcnt lgkmcnt(4)\nds_read_b128 v[64:67], v2 offset:45312\nv_mfma_f32_16x16x32_bf16 v[44:47], v[72:75], v[68:71], v[44:47]\nv_mfma_f32_16x16x32_bf16 v[48:51], a[252:255], v[68:71], v[48:51]\nv_mfma_f32_16x16x32_bf16 v[52:55], v[100:103], v[68:71], v[52:55]\ns_waitcnt lgkmcnt(4)\nds_read_b128 v[72:75], v1 offset:16384\nv_mfma_f32_16x16x32_bf16 v[40:43], v[104:107], v[56:59], v[40:43]\n" \
"ds_read_b128 v[68:71], v2 offset:47360\nv_mfma_f32_16x16x32_bf16 v[44:47], v[112:115], v[56:59], v[44:47]\ns_waitcnt lgkmcnt(5)\nv_mfma_f32_16x16x32_bf16 v[48:51], v[76:79], v[56:59], v[48:51]\nv_mfma_f32_16x16x32_bf16 v[52:55], v[120:123], v[56:59], v[52:55]\ns_waitcnt lgkmcnt(4)\nds_read_b128 v[76:79], v1 offset:17408\nv_mfma_f32_16x16x32_bf16 v[40:43], v[108:111], v[60:63], v[40:43]\n" \
"ds_read_b128 v[56:59], v2 offset:49408\nv_mfma_f32_16x16x32_bf16 v[44:47], v[116:119], v[60:63], v[44:47]\ns_waitcnt lgkmcnt(5)\nv_mfma_f32_16x16x32_bf16 v[48:51], v[80:83], v[60:63], v[48:51]\nv_mfma_f32_16x16x32_bf16 v[52:55], v[124:127], v[60:63], v[52:55]\ns_waitcnt lgkmcnt(4)\nv_mfma_f32_16x16x32_bf16 v[40:43], v[128:131], v[64:67], v[40:43]\nds_read_b128 v[60:63], v2 offset:51456\n" \
"v_mfma_f32_16x16x32_bf16 v[44:47], v[136:139], v[64:67], v[44:47]\nv_mfma_f32_16x16x32_bf16 v[48:51], v[144:147], v[64:67], v[48:51]\ns_waitcnt lgkmcnt(4)\nv_mfma_f32_16x16x32_bf16 v[52:55], v[72:75], v[64:67], v[52:55]\ns_waitcnt lgkmcnt(3)\nv_mfma_f32_16x16x32_bf16 v[40:43], v[132:135], v[68:71], v[40:43]\nds_read_b128 v[64:67], v2 offset:53504\nv_mfma_f32_16x16x32_bf16 v[44:47], v[140:143], v[68:71], v[44:47]\n" \
"v_mfma_f32_16x16x32_bf16 v[48:51], v[148:151], v[68:71], v[48:51]\ns_waitcnt lgkmcnt(3)\nv_mfma_f32_16x16x32_bf16 v[52:55], v[76:79], v[68:71], v[52:55]\ns_waitcnt lgkmcnt(2)\nds_read_b128 v[76:79], v1 offset:18432\nv_mfma_f32_16x16x32_bf16 v[40:43], v[152:155], v[56:59], v[40:43]\nds_read_b128 v[68:71], v2 offset:55552\nv_mfma_f32_16x16x32_bf16 v[44:47], v[160:163], v[56:59], v[44:47]\n" \
"v_mfma_f32_16x16x32_bf16 v[48:51], v[168:171], v[56:59], v[48:51]\nv_mfma_f32_16x16x32_bf16 v[52:55], v[176:179], v[56:59], v[52:55]\ns_waitcnt lgkmcnt(3)\nds_read_b128 v[80:83], v1 offset:19456\nv_mfma_f32_16x16x32_bf16 v[40:43], v[156:159], v[60:63], v[40:43]\nds_read_b128 v[56:59], v2 offset:57600\nv_mfma_f32_16x16x32_bf16 v[44:47], v[164:167], v[60:63], v[44:47]\nv_mfma_f32_16x16x32_bf16 v[48:51], v[172:175], v[60:63], v[48:51]\n" \
"v_mfma_f32_16x16x32_bf16 v[52:55], v[180:183], v[60:63], v[52:55]\ns_waitcnt lgkmcnt(4)\nds_read_b128 v[72:75], v1 offset:20480\nv_mfma_f32_16x16x32_bf16 v[40:43], v[184:187], v[64:67], v[40:43]\ns_waitcnt lgkmcnt(4)\nds_read_b128 v[60:63], v2 offset:59648\nv_mfma_f32_16x16x32_bf16 v[44:47], v[76:79], v[64:67], v[44:47]\nv_mfma_f32_16x16x32_bf16 v[48:51], v[192:195], v[64:67], v[48:51]\n" \
"v_mfma_f32_16x16x32_bf16 v[52:55], v[200:203], v[64:67], v[52:55]\ns_waitcnt lgkmcnt(4)\nds_read_b128 v[76:79], v1 offset:21504\nv_mfma_f32_16x16x32_bf16 v[40:43], v[188:191], v[68:71], v[40:43]\ns_waitcnt lgkmcnt(4)\nds_read_b128 v[64:67], v2 offset:61696\nv_mfma_f32_16x16x32_bf16 v[44:47], v[80:83], v[68:71], v[44:47]\nv_mfma_f32_16x16x32_bf16 v[48:51], v[196:199], v[68:71], v[48:51]\n" \
"v_mfma_f32_16x16x32_bf16 v[52:55], v[204:207], v[68:71], v[52:55]\ns_waitcnt lgkmcnt(4)\nds_read_b128 v[80:83], v1 offset:22528\nv_mfma_f32_16x16x32_bf16 v[40:43], v[208:211], v[56:59], v[40:43]\nds_read_b128 v[68:71], v2 offset:63744\nv_mfma_f32_16x16x32_bf16 v[44:47], v[216:219], v[56:59], v[44:47]\ns_waitcnt lgkmcnt(5)\nv_mfma_f32_16x16x32_bf16 v[48:51], v[72:75], v[56:59], v[48:51]\n" \
"v_mfma_f32_16x16x32_bf16 v[52:55], v[224:227], v[56:59], v[52:55]\ns_waitcnt lgkmcnt(4)\nds_read_b128 v[72:75], v1 offset:23552\nv_mfma_f32_16x16x32_bf16 v[40:43], v[212:215], v[60:63], v[40:43]\nv_mfma_f32_16x16x32_bf16 v[44:47], v[220:223], v[60:63], v[44:47]\ns_waitcnt lgkmcnt(4)\nv_mfma_f32_16x16x32_bf16 v[48:51], v[76:79], v[60:63], v[48:51]\nv_mfma_f32_16x16x32_bf16 v[52:55], v[228:231], v[60:63], v[52:55]\n" \
"s_waitcnt lgkmcnt(3)\nv_mfma_f32_16x16x32_bf16 v[40:43], v[232:235], v[64:67], v[40:43]\nv_mfma_f32_16x16x32_bf16 v[44:47], v[240:243], v[64:67], v[44:47]\nv_mfma_f32_16x16x32_bf16 v[48:51], v[248:251], v[64:67], v[48:51]\ns_waitcnt lgkmcnt(2)\nv_mfma_f32_16x16x32_bf16 v[52:55], v[80:83], v[64:67], v[52:55]\ns_waitcnt lgkmcnt(1)\nv_mfma_f32_16x16x32_bf16 v[40:43], v[236:239], v[68:71], v[40:43]\n" \
"v_mfma_f32_16x16x32_bf16 v[44:47], v[244:247], v[68:71], v[44:47]\nv_mfma_f32_16x16x32_bf16 v[48:51], v[252:255], v[68:71], v[48:51]\ns_waitcnt lgkmcnt(0)\nv_mfma_f32_16x16x32_bf16 v[52:55], v[72:75], v[68:71], v[52:55]\nv_fma_f32 v40, v8, v87, v40\nv_fma_f32 v41, v9, v87, v41\ns_nop 1\nv_fma_f32 v48, v16, v87, v48\n" \
"v_fma_f32 v49, v17, v87, v49\nv_mul_f32 v40, v40, v88\nv_mul_f32 v41, v41, v88\nv_fma_f32 v91, v40, v40, s59\nv_fma_f32 v92, v41, v41, s59\nv_mul_f32 v91, v91, v40\nv_mul_f32 v92, v92, v41\nv_exp_f32 v91, v91\n" \
"v_exp_f32 v92, v92\nv_mul_f32 v40, v40, v48\nv_mul_f32 v41, v41, v49\nv_fma_f32 v91, v91, v89, v89\nv_fma_f32 v92, v92, v89, v89\nv_rcp_f32 v91, v91\nv_rcp_f32 v92, v92\nv_mul_f32 v40, v40, v91\n" \
"v_mul_f32 v41, v41, v92\nv_cvt_pk_bf16_f32 v48, v40, v41\nv_fma_f32 v42, v10, v87, v42\nv_fma_f32 v43, v11, v87, v43\nv_fma_f32 v50, v18, v87, v50\nv_fma_f32 v51, v19, v87, v51\nv_mul_f32 v42, v42, v88\nv_mul_f32 v43, v43, v88\n" \
"v_fma_f32 v91, v42, v42, s59\nv_fma_f32 v92, v43, v43, s59\nv_mul_f32 v91, v91, v42\nv_mul_f32 v92, v92, v43\nv_exp_f32 v91, v91\nv_exp_f32 v92, v92\nv_mul_f32 v42, v42, v50\nv_mul_f32 v43, v43, v51\n" \
"v_fma_f32 v91, v91, v89, v89\nv_fma_f32 v92, v92, v89, v89\nv_rcp_f32 v91, v91\nv_rcp_f32 v92, v92\nv_mul_f32 v42, v42, v91\nv_mul_f32 v43, v43, v92\nv_cvt_pk_bf16_f32 v49, v42, v43\nv_fma_f32 v44, v12, v87, v44\n" \
"v_fma_f32 v45, v13, v87, v45\nv_fma_f32 v52, v20, v87, v52\nv_fma_f32 v53, v21, v87, v53\nv_mul_f32 v44, v44, v88\nv_mul_f32 v45, v45, v88\nv_fma_f32 v91, v44, v44, s59\nv_fma_f32 v92, v45, v45, s59\nv_mul_f32 v91, v91, v44\n" \
"v_mul_f32 v92, v92, v45\nv_exp_f32 v91, v91\nv_exp_f32 v92, v92\nv_mul_f32 v44, v44, v52\nv_mul_f32 v45, v45, v53\nv_fma_f32 v91, v91, v89, v89\nv_fma_f32 v92, v92, v89, v89\nv_rcp_f32 v91, v91\n" \
"v_rcp_f32 v92, v92\nv_mul_f32 v44, v44, v91\nv_mul_f32 v45, v45, v92\nv_cvt_pk_bf16_f32 v50, v44, v45\nv_fma_f32 v46, v14, v87, v46\nv_fma_f32 v47, v15, v87, v47\nv_fma_f32 v54, v22, v87, v54\nv_fma_f32 v55, v23, v87, v55\n" \
"v_mul_f32 v46, v46, v88\nv_mul_f32 v47, v47, v88\nv_fma_f32 v91, v46, v46, s59\nv_fma_f32 v92, v47, v47, s59\nv_mul_f32 v91, v91, v46\nv_mul_f32 v92, v92, v47\nv_exp_f32 v91, v91\nv_exp_f32 v92, v92\n" \
"v_mul_f32 v46, v46, v54\nv_mul_f32 v47, v47, v55\nv_fma_f32 v91, v91, v89, v89\nv_fma_f32 v92, v92, v89, v89\nv_rcp_f32 v91, v91\nv_rcp_f32 v92, v92\nv_mul_f32 v46, v46, v91\nv_mul_f32 v47, v47, v92\n" \
"v_cvt_pk_bf16_f32 v51, v46, v47\nbuffer_store_dwordx4 v[48:51], v4, %[rs], s55 offen offset:256 sc1\nv_mov_b32 v90, s40\ns_mov_b32 s56, %[fh]\ns_add_u32 s40, s40, 1\ns_cmp_lt_u32 s40, 64\ns_cbranch_scc1 .Lgg_next%=\ns_waitcnt vmcnt(0)\n" \
"buffer_store_dword v90, off, %[rs], s56 sc1\ns_branch .Lgg_end%=\n.Lgg_abort%=:\ns_mov_b64 exec, -1\nv_mov_b32 v16, 0\ns_movk_i32 s44, 6400\nbuffer_store_dword v16, off, %[rs], s44 sc1\n.Lgg_end%=:\n" \
"s_waitcnt vmcnt(0) lgkmcnt(0)\n" \
""

#define V10(p, x) p #x "0", p #x "1", p #x "2", p #x "3", p #x "4", p #x "5", p #x "6", p #x "7", p #x "8", p #x "9"
#define V256(p) V10(p, ), V10(p, 1), V10(p, 2), V10(p, 3), V10(p, 4), V10(p, 5), V10(p, 6), V10(p, 7), V10(p, 8), V10(p, 9), V10(p, 10), V10(p, 11), V10(p, 12), V10(p, 13), V10(p, 14), V10(p, 15), V10(p, 16), \
    V10(p, 17), V10(p, 18), V10(p, 19), V10(p, 20), V10(p, 21), V10(p, 22), V10(p, 23), V10(p, 24), p "250", p "251", p "252", p "253", p "254", p "255"
#define CLOB "memory", "vcc", "scc", V256("v"), V256("a"), V10("s", 4), V10("s", 5), V10("s", 6), V10("s", 7), V10("s", 8)
template <int N> DEVI void table(lds_char* p, const unsigned (&t)[N]) {
#pragma unroll
    for (int i = 0; i < N; ++i) *(l1*)(p + i * 256) = t[i];
    WL0;
}
DEVI unsigned prof_at(int cu, int w) { return O_PROF + (262144 + cu * 2560 + w * 8) * 4; }
DEVI unsigned sp_idx(int lane) { return (lane & 2) ? 0xEEEEu : 0x4444u; }
DEVI int sp_row(int r, int hf) { return 4 * (r >> 1) + 2 * hf + (r & 1); }

DEVI void qk_role(const Args& a, const int w, lds_char* smem, int l, int h) {
    const int lane = lane_id(), g = lane >> 4, n = lane & 15, b = n & 3, p = n >> 2;
    const unsigned l0 = (unsigned)(size_t)smem, tb = l0 + w * 12288, lw = l0 + 54272 + w * 24576, ls = l * SD, IMG = l0 + 32768, MIM = l0 + 49152;
    const unsigned xw = (w >> 1) * 16384 + (w & 1) * 4096, oc = (h >> 3) * 16384 + ls, tr = (8 * g + p) * 128 + b * 16;
    const unsigned cm[4] = {4 * g > n ? 0xF149F2CAu : 0u, 4 * g + 1 > n ? 0xF149F2CAu : 0u, 4 * g + 2 > n ? 0xF149F2CAu : 0u, 4 * g + 3 > n ? 0xF149F2CAu : 0u};
    const unsigned t[19] = {
        l0 + lane * 16, lw + lane * 16, l0 + (g >> 1) * 1024 + (g & 1) * 512 + n * 16, O_E1 + ls + xw + lane * 16, sp_idx(lane),
        IMG + (w >> 1) * 8192 + ((w & 1) * 8 + 2 * g) * 512 + n * 16,
        MIM + (w * 8 + 2 * p) * 144 + g * 32 + b * 4,
        IMG + g * 512 + (w ? n * 16 : 8192 + (16 * (b >> 1) + 8 * (p >> 1) + 2 * (p & 1) + (b & 1)) * 16),
        MIM + g * 144 + (n & 7) * 16, MIM + 2304 + g * 144 + (2 * p + (b & 1)) * 16,
        w ? O_PT + (h >> 3) * 32768 + ls + lane * 16 + (w == 1 ? 16384 + ((h & 7) >> 1) * 1024 + (h & 1) * 8 : (h & 7) * 1024)
          : O_PM + oc + ((h >> 1) & 3) * 1024 + ((2 * (h & 1) + (g >> 1)) * 16 + sp_row(n & 7, g & 1)) * 16 + (n >> 3) * 8,
        w ? O_PT + (h >> 3) * 32768 + ls + 20480 + ((h >> 2) & 1) * 2048 + (w - 1) * 1024 + (h & 3) * 256 + n * 16 + g * 4
          : n >> 3 ? OOB : O_PM + oc + 4096 + ((h & 7) >> 1) * 256 + sp_row(n & 7, g & 1) * 16 + (h & 1) * 8 + (g >> 1) * 4,
        w == 3 ? (l ? F_X + SD + p * 128 + b * 4 : F_X0 + b * 4) : cm[0], cm[1], cm[2], cm[3],
        (lane >> 3) * 8192u + (lane & 7) * 16, tb + tr, lw + tr};
    table(smem + w * 12288 + lane * 4, t);
    asm volatile(QK_ASM : : [rs] "s"(mkrs(a.ws, WS_RS)), [rw] "s"(mkrs(w < 2 ? a.Wq : a.Wk, 8u << 20)), [wo] "s"(h * 256 + (w & 1) * 128), [tb] "s"(tb), [lw] "s"(lw), [l0] "s"(l0), [w] "s"(w),
        [xn] "s"(O_XN + ls), [xw] "s"(xw), [fs] "s"(l ? 0 : 128), [pr] "s"(prof_at(l * 80 + h, w) + (w == 3 ? 0 : 128)) : CLOB);
}
DEVI void vu_role(const Args& a, const int w, lds_char* smem, int l, int h) {
    const int lane = lane_id(), g = lane >> 4, n = lane & 15, b = n & 3, p = n >> 2;
    const unsigned l0 = (unsigned)(size_t)smem, tb = l0 + w * 12288, lw = l0 + 61440 + w * 24576, ls = l * SD;
    const unsigned xw = (w >> 1) * 16384 + (w & 1) * 4096, tv = (8 * g + p) * 128 + b * 16 + (w & 1) * 64, to = (8 * (g >> 1) + 4 * (p & 1) + 2 * (g & 1)) * 128 + (p >> 1) * 32 + b * 8;
    const unsigned t[18] = {
        l0 + lane * 16, lw + lane * 16, l0 + (g >> 1) * 1024 + (g & 1) * 512 + n * 16, O_E1 + ls + xw + lane * 16, sp_idx(lane),
        l0 + 57344 + w * 1024 + p * 256 + g * 32 + b * 4,
        O_UM + ls + (4 * w + (g & 1)) * 65536 + (h >> 3) * 16384 + (g >> 1) * 2048 + (h & 7) * 256 + n * 16,
        sd(O_Y + h * 131072 + ls) + w * 32768 + (g >> 1) * 1024 + (g & 1) * 256 + n * 16,
        l ? F_X + SD + p * 128 + b * 4 : F_X0 + b * 4, l0 + 49152 + w * 2048 + lane * 16, sp_idx(lane >> 2), l0 + g * 256 + (n & 7) * 16,
        (lane >> 3) * 8192u + (lane & 7) * 16, (lane >> 3) * 2048u + (lane & 7) * 16, tb + tv - (w & 1) * 12288, lw + tv - (w & 1) * 24576, tb + to, lw + to};
    table(smem + w * 12288 + lane * 4, t);
    asm volatile(VU_ASM : : [rs] "s"(mkrs(a.ws, WS_RS)), [rw] "s"(mkrs(a.Wv, 8u << 20)), [ro] "s"(mkrs(a.Wo, 8u << 20)), [wo] "s"(h * 256 + (w >> 1) * 128 + (w & 1) * 262144), [w2] "s"(h * 262144 + w * 512), [tb] "s"(tb), [lw] "s"(lw), [w] "s"(w),
        [xn] "s"(O_XN + ls), [xw] "s"(xw), [fs] "s"(l ? 0 : 128), [fy] "s"(F_Y0 + ls + h * 128 + w * 4), [pr] "s"(prof_at(l * 80 + 32 + h, w)) : CLOB);
}
DEVI void rd_role(const Args& a, const int w, lds_char* smem, int l, int c) {
    const int lane = lane_id(), g = lane >> 4, n = lane & 15, r = 2 * (n >> 2) + (n & 1), hf = (n >> 1) & 1;
    const rsrc_t rs = mkrs(a.ws, WS_RS);
    const unsigned l0 = (unsigned)(size_t)smem, tb = l0 + 65536 + w * 4096, ls = l * SD, vp = O_E1 + ls + (c >> 3) * 16384 + (c & 7) * 1024 + w * 256 + n * 16;
    st4(g & 1 ? *(const unsigned*)(a.mem0 + r * 1024 + c * 64 + w * 16 + 4 * g + 2 * hf) : nosent(*(const unsigned*)(a.mem0 + r * 1024 + c * 64 + w * 16 + 4 * g + 2 * hf)), rs, g & 1 ? vp + g * 4 : OOB, 0);
    WV0;
    st4(nosent(*(const unsigned*)(a.mem0 + r * 1024 + c * 64 + w * 16 + 4 * g + 2 * hf)), rs, g & 1 ? OOB : vp + g * 4, 0);
    const int gi = lane >> 3, r8 = lane & 7;
    const unsigned t[11] = {
        (unsigned)lane * 16, sp_idx(lane), l0 + w * 4096 + g * 576 + ((n >> 3) * 16 + hf * 4 + 2 * ((n >> 2) & 1) + (n & 1)) * 4,
        l0 + (r >> 1) * 576 + w * 128 + (g >> 1) * 64 + (r & 1) * 32 + hf * 16,
        (vp ^ ((g & 1) * 65536)) + (g >> 1) * 8, g & 1 ? SENT : 0u,
        l0 + 16384 + w * 8192 + lane * 16, l0 + 16384 + ((gi >> 1) * 2 + (w >> 1)) * 1024 + ((gi & 1) * 32 + (w & 1) * 8 + r8) * 16,
        (c >> 1) * 16384u + (c & 1) * 4096 + gi * 512 + (w * 8 + r8) * 16,
        gi ? OOB : O_ST - O_XR + c * 256 + ((w & 1) * 8 + r8) * 16 + (w >> 1) * 8,
        F_Y0 + ls + (w * 8 + (lane & 7)) * 128 + (c >> 2) * 4};
    table(smem + 65536 + w * 4096 + lane * 4, t);
    asm volatile(RD_ASM : : [rs] "s"(rs), [tb] "s"(tb), [su] "s"(O_UM + ls + c * 65536 + w * 16384), [sp] "s"(O_PM + ls + w * 16384), [st] "s"(O_PT + ls + w * 32768),
        [sy] "s"(sd(O_Y + w * (1u << 20) + ls) + (c >> 1) * 16384 + (c & 1) * 4096), [at] "s"(O_XR + ls), [fa] "s"(F_A + ls + c * 128 + w * 4), [pr] "s"(prof_at(l * 80 + 64 + c, w) + 128) : CLOB);
}
DEVI void gg_role(const Args& a, const int w, lds_char* smem, int l, int gi) {
    const int lane = lane_id(), g = lane >> 4, n = lane & 15, b = n & 3, p = n >> 2;
    const unsigned l0 = (unsigned)(size_t)smem, tb = l0 + w * 12288, lw = l0 + 65536 + w * 24576, ls = l * SD, tv = (8 * g + p) * 128 + b * 16 + (w & 1) * 64, fh = (g >> 1) * 1024 + (g & 1) * 512 + n * 16;
    const unsigned t[9] = {l0 + lane * 16, lw + lane * 16, l0 + fh, F_A + ls + (lane >> 2) * 128 + (lane & 3) * 4, w * 2048 + fh, O_ST - O_XR + g * 1024 + n * 16, (lane >> 3) * 7168u + (lane & 7) * 16, tb + tv - (w & 1) * 12288, lw + tv - (w & 1) * 24576};
    table(smem + w * 12288 + lane * 4, t);
    asm volatile(GG_ASM : : [rs] "s"(mkrs(a.ws, WS_RS)), [rw] "s"(mkrs(w & 1 ? a.Wu : a.Wg, 3584 * 2048)), [wo] "s"(gi * 256 + (w >> 1) * 128), [tb] "s"(tb), [lw] "s"(lw), [w] "s"(w),
        [xn] "s"(O_XR + ls), [xw] "s"((w >> 1) * 16384 + (w & 1) * 4096), [ho] "s"(O_H + ls + gi * 16384), [fh] "s"(F_H + ls + (gi / 7) * 1024 + (gi % 7) * 128 + w * 4), [pr] "s"(prof_at(160 + l * 28 + gi, w) + 128) : CLOB);
}

#define PLAN 0
#define PFA 0
#define PFB 2
#define PFL 1
#define VUX 1
#define LNS 50
#define DLG 800
#define DLD 1300
#define DL1 1700
#define DLG1 2600
#define DLD1 3000
__global__ __launch_bounds__(256, 1) void sys_kernel(Args a) {
    extern __shared__ __attribute__((aligned(16))) char smem_[];
    lds_char* smem = (lds_char*)smem_;
    {
        const unsigned* t = (const unsigned*)(a.ws + O_POL);
        int v = 0;
#pragma unroll
        for (int i = 0; i < 8; ++i) v += (t[8 + i] < t[i]) != (i >= 4);
        a.ws += v > 4 ? 2u << 20 : 0u;
    }
    const int x = blockIdx.x & 7, j = blockIdx.x >> 3, wv = RFL(threadIdx.x >> 6), l = x >> 2, xx = x & 3;
    if (wv == 0) TSTAMP(210000 + blockIdx.x);
    if (j < 16) {
        if (l) { const unsigned t0 = (unsigned)__builtin_amdgcn_s_memrealtime(); idle(j * LNS); ln0(a, wv, j * 4 + xx); while ((unsigned)__builtin_amdgcn_s_memrealtime() - t0 < DL1) __builtin_amdgcn_s_sleep(8); }
        if (j < 8) qk_role(a, wv, smem, l, xx * 8 + j); else vu_role(a, wv, smem, l, (xx ^ VUX) * 8 + j - 8);
        return;
    }
    {
        const int lane = lane_id(), q = (j - 16) * 4 + wv, m = q >> 4;
        const u16 *W0 = a.Wq, *W1 = a.Wk, *W2 = a.Wv, *W3 = a.Wo;
        asm volatile("" : "+s"(W0), "+s"(W1), "+s"(W2), "+s"(W3));
        const u16* W = m == 0 ? W0 : m == 1 ? W1 : m == 2 ? W2 : W3;
        const rsrc_t rw = mkrs(W, 8u << 20);
        if (l) {
#pragma unroll
            for (int i = 0; i < PFA; ++i) __builtin_amdgcn_raw_ptr_buffer_load_lds(rw, smem + LDSB - 1024 + wv * 256, 4, lane * 256, i * (1u << 20) + (xx * 16 + (q & 15)) * 16384, 0, 0);
        } else {
            const unsigned po = (0x100u - (unsigned)(size_t)W) & 0x300u, hx = m < 2 ? xx : xx ^ VUX;
            const unsigned vo = m == 3 ? (lane >> 1) * 1024 + (lane & 1) * 128 : (lane >> 2) * 8192 + ((lane >> 1) & 1) * 1024 + (lane & 1) * 128;
            const unsigned so = po + (m == 3 ? hx * (2u << 20) + (q & 15) * 32768 : hx * 2048 + (q & 15) * 131072);
#pragma unroll
            for (int i = 0; i < PFB; ++i) __builtin_amdgcn_raw_ptr_buffer_load_lds(rw, smem + LDSB - 1024 + wv * 256, 4, vo, so + i * (m == 3 ? 524288 : 2u << 20), 0, 0);
            const unsigned pl = (0x1F00u - (unsigned)(size_t)W) & 0x1F00u;
#pragma unroll
            for (int i = 0; i < PFL; ++i) __builtin_amdgcn_raw_ptr_buffer_load_lds(rw, smem + LDSB - 1024 + wv * 256, 4, (lane >> 1) * 8192 + (lane & 1) * 128, (pl >> 11) == hx && m < 3 ? pl + ((q & 15) + 16 * i) * 262144 : OOB, 0, 0);
        }
    }
    int ro, ix;
    if (j < 20) { ro = 0; ix = xx * 4 + j - 16; } else if (j < 27) { ro = 1; ix = xx * 7 + j - 20; } else if (j < 31) { ro = 2; ix = xx * 4 + j - 27; } else { ro = 3; ix = xx; }
    if (ro == 0) { rd_role(a, wv, smem, l, ix); return; }
    if (ro == 1) { idle(l ? DLG1 : DLG); WV0; BAR; gg_role(a, wv, smem, l, ix); return; }
    if (ro == 3) { if (l) r_role<1>(a, wv, smem, ix); else r_role<0>(a, wv, smem, ix); return; }
    EP e;
    const unsigned fs = l * SD;
    const int kg = ix >> 2, mb = ix & 3;
    e.wbytes = 3584 * 2048; e.out_st = 524288; e.out_mask = 255; e.fown_w = 4; e.prio = 0;
    e.prof = blockIdx.x * 256;
    e.out0 = e.out1 = O_SL + kg * 131072 + mb * 32768 + fs;
    e.W0 = a.Wd; e.ldw = 1024; e.w_off = kg * (896 * 2048) + mb * 512;
    e.in0 = e.in1 = O_H + kg * 7 * 16384 + fs; e.in_st = 524288;
    e.fset = F_H + kg * 1024 + fs; e.fmask = 0x0FFFFFFF;
    e.fown0 = e.fown1 = F_D + fs + ix * 128;
    idle(l ? DLD1 : DLD); WV0; BAR;
    engine<0, 56, true>(a, wv, e, smem);
}

__global__ __launch_bounds__(256) void init_kernel(char* ws) {
    const rsrc_t rs = mkrs(ws, 6u << 20);
    if (blockIdx.x < 16) {
        if (threadIdx.x >= 64) return;
        unsigned t = ~0u;
        for (int i = 0; i < NPRB; ++i) {
            const unsigned t0 = (unsigned)__builtin_amdgcn_s_memtime();
            WL0;
            st4(0u, rs, 0, O_PRB + blockIdx.x * 1024 + i * 128);
            WV0;
            t = min(t, (unsigned)__builtin_amdgcn_s_memtime() - t0);
        }
        st4(t, rs, 0, O_POL + blockIdx.x * 4);
        return;
    }
    const unsigned o = (blockIdx.x - 16) * 16384 + threadIdx.x * 16;
#pragma unroll
    for (int i = 0; i < 8; ++i) __builtin_amdgcn_raw_buffer_store_b128(v4u{SENT, SENT, SENT, SENT}, rs, o + (i & 3) * 4096 + (i >> 2) * (2u << 20), 0, 0);
}

extern "C" void kernel_launch(void* const* d_in, const int* in_sizes, int n_in, void* d_out, int out_size, void* d_ws, size_t ws_size, hipStream_t stream) {
    (void)in_sizes; (void)n_in; (void)out_size; (void)ws_size;
    Args a;
    a.x = (const u16*)d_in[0]; a.mem0 = (const u16*)d_in[1]; a.Wq = (const u16*)d_in[2]; a.Wk = (const u16*)d_in[3]; a.Wv = (const u16*)d_in[4]; a.Wo = (const u16*)d_in[5];
    a.Wg = (const u16*)d_in[8]; a.Wu = (const u16*)d_in[9]; a.Wd = (const u16*)d_in[10];
    a.ws = (char*)(((uintptr_t)d_ws + (2u << 20) - 1) & ~(uintptr_t)((2u << 20) - 1)); a.out = (u16*)d_out;
    (void)hipFuncSetAttribute((const void*)sys_kernel, hipFuncAttributeMaxDynamicSharedMemorySize, LDSB);
    hipLaunchKernelGGL(init_kernel, dim3(16 + CTRL_END / 16384), dim3(256), 0, stream, a.ws);
    hipLaunchKernelGGL(sys_kernel, dim3(256), dim3(256), LDSB, stream, a);
}
```

```cpp
#include <hip/hip_runtime.h>
#include <stdint.h>

typedef unsigned short u16;
typedef unsigned long long u64;
typedef __attribute__((ext_vector_type(8))) short bf16x8;
typedef __attribute__((ext_vector_type(4))) short s16x4;
typedef __attribute__((ext_vector_type(4))) float f32x4;
typedef __attribute__((ext_vector_type(16))) float f32x16;
typedef __attribute__((ext_vector_type(2))) float f32x2;
typedef __attribute__((ext_vector_type(4))) unsigned v4u;
typedef __attribute__((ext_vector_type(2))) unsigned v2u;
typedef __amdgpu_buffer_rsrc_t rsrc_t;
typedef __attribute__((address_space(3))) char lds_char;
typedef __attribute__((address_space(3))) bf16x8 l8;
typedef __attribute__((address_space(3))) v4u l4;
typedef __attribute__((address_space(3))) v2u l2;
typedef __attribute__((address_space(3))) unsigned l1;
typedef __attribute__((address_space(3))) f32x4 lf4;
typedef __attribute__((address_space(3))) float lf1;
#define DEVI __device__ __forceinline__
#define AI __attribute__((always_inline))

constexpr int NSEG = 64;
constexpr float ATT_SCALE = 0.08838834764831845f;
constexpr float LOG2E = 1.4426950408889634f;
#define NPRB 4
constexpr unsigned SD = 8192;
DEVI unsigned sd(unsigned o) { return o ^ ((o >> 8) & SD); }
constexpr unsigned F_X0 = 32768;
constexpr unsigned F_X = 4096;
constexpr unsigned F_A = 16384;
constexpr unsigned F_H = F_A + 2048;
constexpr unsigned F_D = F_H + 4096;
constexpr unsigned F_Y0 = 49152;
constexpr unsigned F_ABORT = 6400;
constexpr unsigned O_PM = 65536;
constexpr unsigned O_E1 = 131072;
constexpr unsigned O_PT = 262144;
constexpr unsigned O_UM = 524288;
constexpr unsigned CTRL_END = O_UM + (1u << 20);
constexpr unsigned O_PRB = 0x1F0000, O_HB = 0x1FFF00;
constexpr unsigned O_XN = 4u << 20;
constexpr unsigned O_XR = O_XN + (8u << 20);
constexpr unsigned O_ST = O_XR + (8u << 20);
constexpr unsigned O_H = O_ST + (8u << 20);
constexpr unsigned O_Y = O_H + (36u << 20);
constexpr unsigned WS_NEED = O_Y + (8u << 20);
constexpr unsigned O_PROF = WS_NEED;
constexpr unsigned WS_RS = WS_NEED;
static_assert(CTRL_END <= O_PRB && CTRL_END % 16384 == 0 && O_Y % (8u << 20) == 0, "workspace");
constexpr unsigned OOB = 0x80000000u, OOB2 = 0x90000000u, SENT = 0xFFFFFFFFu;

struct Args { const u16 *x, *mem0, *Wq, *Wk, *Wv, *Wo, *Wg, *Wu, *Wd; char* ws; u16* out; };

typedef __attribute__((ext_vector_type(2))) __bf16 bf16x2_t;
DEVI unsigned pack2(float lo, float hi) { f32x2 v = {lo, hi}; bf16x2_t b = __builtin_convertvector(v, bf16x2_t); return __builtin_bit_cast(unsigned, b); }
DEVI float bflo(unsigned u) { return __uint_as_float(u << 16); }
DEVI float bfhi(unsigned u) { return __uint_as_float(u & 0xffff0000u); }
DEVI float rbf(float x) { return bflo(pack2(x, x)); }
DEVI bf16x8 as8(v4u v) { return __builtin_bit_cast(bf16x8, v); }
DEVI f32x4 mfma16(bf16x8 a, bf16x8 b, f32x4 c) { return __builtin_amdgcn_mfma_f32_16x16x32_bf16(a, b, c, 0, 0, 0); }
DEVI f32x16 mfma32(bf16x8 a, bf16x8 b, f32x16 c) { return __builtin_amdgcn_mfma_f32_32x32x16_bf16(a, b, c, 0, 0, 0); }
constexpr int AUX_SC1 = 16;
DEVI v4u ld16(rsrc_t r, unsigned vo, unsigned so) { return __builtin_amdgcn_raw_buffer_load_b128(r, vo, so, AUX_SC1); }
DEVI unsigned ld4(rsrc_t r, unsigned vo, unsigned so) { return __builtin_amdgcn_raw_buffer_load_b32(r, vo, so, AUX_SC1); }
DEVI void st16(v4u v, rsrc_t r, unsigned vo) { __builtin_amdgcn_raw_buffer_store_b128(v, r, vo, 0, AUX_SC1); }
DEVI void st8(v2u v, rsrc_t r, unsigned vo, unsigned so) { __builtin_amdgcn_raw_buffer_store_b64(v, r, vo, so, AUX_SC1); }
DEVI void st4(unsigned v, rsrc_t r, unsigned vo, unsigned so) { __builtin_amdgcn_raw_buffer_store_b32(v, r, vo, so, AUX_SC1); }
DEVI v2u trd(lds_char* p) { return __builtin_bit_cast(v2u, __builtin_amdgcn_ds_read_tr16_b64_v4i16((__attribute__((address_space(3))) s16x4*)p)); }
template <int O1, int O2> DEVI void tra(v2u& lo, v2u& hi, unsigned a) { asm volatile("ds_read_b64_tr_b16 %0, %2 offset:%3\n\tds_read_b64_tr_b16 %1, %2 offset:%4" : "=&v"(lo), "=&v"(hi) : "v"(a), "n"(O1), "n"(O2) : "memory"); }
DEVI void trw(v2u& a, v2u& b, v2u& c, v2u& d) { asm volatile("s_waitcnt lgkmcnt(0)" : "+v"(a), "+v"(b), "+v"(c), "+v"(d)::"memory"); }
DEVI rsrc_t mkrs(const void* p, unsigned n) { return __builtin_amdgcn_make_buffer_rsrc((void*)p, 0, (int)n, 0x00020000); }
#define CB asm volatile("" ::: "memory")
#define SB __builtin_amdgcn_sched_barrier(0)
#define WL0 asm volatile("s_waitcnt lgkmcnt(0)" ::: "memory")
#define WV0 asm volatile("s_waitcnt vmcnt(0)" ::: "memory")
#define WV(n) asm volatile("s_waitcnt vmcnt(%0)" ::"n"(n) : "memory")
#define BAR do { __builtin_amdgcn_s_barrier(); CB; } while (0)
#define RFL(x) __builtin_amdgcn_readfirstlane(x)
DEVI int lane_id() { int x; asm volatile("v_mbcnt_lo_u32_b32 %0, -1, 0\n\tv_mbcnt_hi_u32_b32 %0, -1, %0" : "=v"(x)); return x; }
template <int I> struct IC { static constexpr int v = I; };
template <int B, int E_, class F> DEVI void sfor(F&& f) { if constexpr (B < E_) { f(IC<B>{}); sfor<B + 1, E_>(f); } }
DEVI float red_x32(float x, bool mx) { unsigned u = __float_as_uint(x); auto r = __builtin_amdgcn_permlane32_swap(u, u, false, false); float a = __uint_as_float(r[0]), b = __uint_as_float(r[1]); return mx ? fmaxf(a, b) : a + b; }
DEVI float red_x16(float x, bool mx) { unsigned u = __float_as_uint(x); auto r = __builtin_amdgcn_permlane16_swap(u, u, false, false); float a = __uint_as_float(r[0]), b = __uint_as_float(r[1]); return mx ? fmaxf(a, b) : a + b; }
DEVI unsigned vmax4(v4u d) { return max(max(d[0], d[1]), max(d[2], d[3])); }
DEVI unsigned nosent(unsigned v) { return min(v, SENT - 1u); }
constexpr unsigned SPIN_CAP = 1u << 18;
DEVI bool spin_bail(unsigned& spins, rsrc_t rs) {
    if (++spins > SPIN_CAP) { st4(0u, rs, 0, F_ABORT); return true; }
    return (spins & 255u) == 0u && RFL(ld4(rs, 0, F_ABORT)) != SENT;
}
#define TSTAMP(idx)
constexpr int LDSB = 160 * 1024;
DEVI void idle(unsigned dl) { const unsigned t0 = (unsigned)__builtin_amdgcn_s_memrealtime(); while ((unsigned)__builtin_amdgcn_s_memrealtime() - t0 < dl) __builtin_amdgcn_s_sleep(8); }

DEVI float red_rows(float s) {
    s += __uint_as_float(__builtin_amdgcn_update_dpp(0, __float_as_uint(s), 0x128, 0xf, 0xf, false));
    s = red_x16(s, false); return red_x32(s, false);
}
DEVI f32x2 up2(unsigned u) { return f32x2{bflo(u), bfhi(u)}; }
DEVI float ln_inv(float var) { return __builtin_amdgcn_rcpf(rbf(__builtin_amdgcn_sqrtf(rbf(rbf(fmaxf(var, 0.f)) + 1e-5f)))); }
template <class F> DEVI void ln0(const Args& a, const int w, int b, F&& base) {
    const int lane = lane_id(), row = w * 8 + (lane & 7);
    const rsrc_t rx = mkrs(a.x, 2048 * 2048);
    v4u u[16];
#pragma unroll
    for (int i = 0; i < 16; ++i) u[i] = __builtin_amdgcn_raw_buffer_load_b128(rx, row * 2048 + (lane >> 3) * 16 + i * 128, b * 65536, 0);
    const float k0 = __shfl(bflo(u[0][0]), lane & 7);
    f32x2 s1 = {0.f, 0.f}, s2 = {0.f, 0.f};
#pragma unroll
    for (int i = 0; i < 16; ++i)
#pragma unroll
        for (int e = 0; e < 4; ++e) { const f32x2 d = up2(u[i][e]) - k0; s1 += d; s2 += d * d; }
    const float s = red_rows(s1[0] + s1[1]) * (1.0f / 1024.0f), q = red_rows(s2[0] + s2[1]) * (1.0f / 1024.0f);
    float inv = ln_inv(q - s * s), c = -rbf(k0 + s) * inv;
    asm volatile("" : "+v"(inv), "+v"(c));
    const rsrc_t rs = mkrs(a.ws + base(), WS_RS);
    const unsigned o = sd(O_XN + b * 131072) + (lane >> 3) * 512 + row * 16;
#pragma unroll
    for (int i = 0; i < 16; ++i) {
        v4u r;
#pragma unroll
        for (int e = 0; e < 4; ++e) { const f32x2 z = up2(u[i][e]) * inv + c; r[e] = pack2(z[0], z[1]); }
        st16(r, rs, o + (i >> 1) * 16384 + (i & 1) * 4096);
    }
#pragma unroll
    for (int i = 0; i < 16; ++i) st16(u[i], rs, o + (O_XR - O_XN) + (i >> 1) * 16384 + (i & 1) * 4096);
    st16(u[0], rs, sd(O_ST + b * 131072) + w * 1024 + lane * 16); st16(u[0], rs, sd(O_ST + b * 131072) + 4096 + w * 1024 + lane * 16); st16(u[0], rs, sd(O_ST + SD + b * 131072) + w * 1024 + lane * 16);
    WV0;
    st4(b, rs, 0, F_X0 + b * 128 + w * 4);
}
#define RSLP 8
DEVI void r_role(const Args& a, const int w, int rq) {
    const int lane = lane_id(), row = (w >> 1) * 16 + (rq & 1) * 8 + (lane & 7), cq = (rq >> 1) * 2 + (w & 1);
    const rsrc_t rs = mkrs(a.ws, WS_RS);
    const unsigned fo = cq * 32768 + (lane >> 3) * 512 + row * 16, so = O_ST - O_XR + 4096 + (lane >> 3) * 512 + row * 8;
    auto io = [](int i) -> unsigned { return (i >> 1) * 16384 + (i & 1) * 4096; };
    const unsigned fd = F_D + (lane & 15) * 128 + (row >> 3) * 4;
    for (int b = 0; b < NSEG; ++b) {
        unsigned spins = 0;
        for (;;) {
            CB;
            if (__ballot((int)ld4(rs, fd, 0) < b) == 0ull) break;
            if (spin_bail(spins, rs)) return;
            __builtin_amdgcn_s_sleep(RSLP);
        }
        const unsigned sx = sd(O_XR + b * 131072);
        const v2u s0 = __builtin_amdgcn_raw_buffer_load_b64(rs, so, sx, AUX_SC1), s1 = __builtin_amdgcn_raw_buffer_load_b64(rs, so + 256, sx, AUX_SC1);
        v4u u[4];
#pragma unroll
        for (int i = 0; i < 4; ++i) u[i] = ld16(rs, fo + io(i), sx);
        const float m0 = __uint_as_float(s0[0]), m1 = __uint_as_float(s1[0]), mean = red_rows(m0 + m1) * 0.0625f;
        const float var = red_rows(__uint_as_float(s0[1]) + __uint_as_float(s1[1]) + 64.f * ((m0 - mean) * (m0 - mean) + (m1 - mean) * (m1 - mean))) * (1.0f / 1024.0f);
        const float inv = ln_inv(var), c = -rbf(mean) * inv;
#pragma unroll
        for (int i = 0; i < 4; ++i) {
            v4u o;
#pragma unroll
            for (int e = 0; e < 4; ++e) { const f32x2 z = up2(u[i][e]) * inv + c; o[e] = pack2(z[0], z[1]); }
            st16(o, rs, fo + io(i) + ((sx ^ SD) - (O_XR - O_XN)));
        }
#pragma unroll
        for (int i = 0; i < 4; ++i) st16(u[i], rs, fo + io(i) + (sx ^ SD));
        WV0;
        st4(b, rs, 0, F_X + SD + rq * 128 + w * 4);
        if (rq == 0 && w == 0) TSTAMP(200000 + 64 + b);
    }
}
#define QK_ASM \
"v_mbcnt_lo_u32_b32 v255, -1, 0\nv_mbcnt_hi_u32_b32 v255, -1, v255\nv_lshlrev_b32 v255, 2, v255\nv_add_u32 v255, %[tb], v255\nds_read_b32 v0, v255 offset:0\nds_read_b32 v1, v255 offset:256\nds_read_b32 v2, v255 offset:512\nds_read_b32 v3, v255 offset:768\n" \
"ds_read_b32 v4, v255 offset:1024\nds_read_b32 v5, v255 offset:1280\nds_read_b32 v6, v255 offset:1536\nds_read_b32 v7, v255 offset:1792\nds_read_b32 v8, v255 offset:2048\nds_read_b32 v9, v255 offset:2304\nds_read_b32 v10, v255 offset:2560\nds_read_b32 v11, v255 offset:2816\n" \
"ds_read_b32 v12, v255 offset:3072\nds_read_b32 v13, v255 offset:3328\nds_read_b32 v14, v255 offset:3584\nds_read_b32 v15, v255 offset:3840\nds_read_b32 v92, v255 offset:4096\nds_read_b32 v93, v255 offset:4352\nds_read_b32 v94, v255 offset:4608\ns_waitcnt lgkmcnt(0)\n" \
"s_mov_b64 s[36:37], %[ws]\ns_mov_b32 s38, %[sz]\ns_mov_b32 s39, 0x20000\ns_mov_b32 s45, 2096896\nbuffer_load_dword v95, off, s[36:39], s45 sc1\ns_add_u32 m0, %[lw], 16384\ns_add_u32 s44, %[wo], 0\nbuffer_load_dwordx4 v92, %[rw], s44 offen nt lds\n" \
"s_add_u32 m0, %[lw], 17408\ns_add_u32 s44, %[wo], 65536\nbuffer_load_dwordx4 v92, %[rw], s44 offen nt lds\ns_add_u32 m0, %[lw], 18432\ns_add_u32 s44, %[wo], 131072\nbuffer_load_dwordx4 v92, %[rw], s44 offen nt lds\ns_add_u32 m0, %[lw], 19456\ns_add_u32 s44, %[wo], 196608\n" \
"buffer_load_dwordx4 v92, %[rw], s44 offen nt lds\ns_add_u32 m0, %[lw], 20480\ns_add_u32 s44, %[wo], 262144\nbuffer_load_dwordx4 v92, %[rw], s44 offen nt lds\ns_add_u32 m0, %[lw], 21504\ns_add_u32 s44, %[wo], 327680\nbuffer_load_dwordx4 v92, %[rw], s44 offen nt lds\ns_add_u32 m0, %[lw], 22528\n" \
"s_add_u32 s44, %[wo], 393216\nbuffer_load_dwordx4 v92, %[rw], s44 offen nt lds\ns_add_u32 m0, %[lw], 23552\ns_add_u32 s44, %[wo], 458752\nbuffer_load_dwordx4 v92, %[rw], s44 offen nt lds\ns_add_u32 m0, %[tb], 0\ns_add_u32 s44, %[wo], 524288\nbuffer_load_dwordx4 v92, %[rw], s44 offen nt lds\n" \
"s_add_u32 m0, %[tb], 1024\ns_add_u32 s44, %[wo], 589824\nbuffer_load_dwordx4 v92, %[rw], s44 offen nt lds\ns_add_u32 m0, %[tb], 2048\ns_add_u32 s44, %[wo], 655360\nbuffer_load_dwordx4 v92, %[rw], s44 offen nt lds\ns_add_u32 m0, %[tb], 3072\ns_add_u32 s44, %[wo], 720896\n" \
"buffer_load_dwordx4 v92, %[rw], s44 offen nt lds\ns_add_u32 m0, %[tb], 4096\ns_add_u32 s44, %[wo], 786432\nbuffer_load_dwordx4 v92, %[rw], s44 offen nt lds\ns_add_u32 m0, %[tb], 5120\ns_add_u32 s44, %[wo], 851968\nbuffer_load_dwordx4 v92, %[rw], s44 offen nt lds\ns_add_u32 m0, %[tb], 6144\n" \
"s_add_u32 s44, %[wo], 917504\nbuffer_load_dwordx4 v92, %[rw], s44 offen nt lds\ns_add_u32 m0, %[tb], 7168\ns_add_u32 s44, %[wo], 983040\nbuffer_load_dwordx4 v92, %[rw], s44 offen nt lds\ns_add_u32 m0, %[tb], 8192\ns_add_u32 s44, %[wo], 1048576\nbuffer_load_dwordx4 v92, %[rw], s44 offen nt lds\n" \
"s_add_u32 m0, %[tb], 9216\ns_add_u32 s44, %[wo], 1114112\nbuffer_load_dwordx4 v92, %[rw], s44 offen nt lds\ns_add_u32 m0, %[tb], 10240\ns_add_u32 s44, %[wo], 1179648\nbuffer_load_dwordx4 v92, %[rw], s44 offen nt lds\ns_add_u32 m0, %[tb], 11264\ns_add_u32 s44, %[wo], 1245184\n" \
"buffer_load_dwordx4 v92, %[rw], s44 offen nt lds\ns_add_u32 m0, %[lw], 0\ns_add_u32 s44, %[wo], 1310720\nbuffer_load_dwordx4 v92, %[rw], s44 offen nt lds\ns_add_u32 m0, %[lw], 1024\ns_add_u32 s44, %[wo], 1376256\nbuffer_load_dwordx4 v92, %[rw], s44 offen nt lds\ns_add_u32 m0, %[lw], 2048\n" \
"s_add_u32 s44, %[wo], 1441792\nbuffer_load_dwordx4 v92, %[rw], s44 offen nt lds\ns_add_u32 m0, %[lw], 3072\ns_add_u32 s44, %[wo], 1507328\nbuffer_load_dwordx4 v92, %[rw], s44 offen nt lds\ns_add_u32 m0, %[lw], 4096\ns_add_u32 s44, %[wo], 1572864\nbuffer_load_dwordx4 v92, %[rw], s44 offen nt lds\n" \
"s_add_u32 m0, %[lw], 5120\ns_add_u32 s44, %[wo], 1638400\nbuffer_load_dwordx4 v92, %[rw], s44 offen nt lds\ns_add_u32 m0, %[lw], 6144\ns_add_u32 s44, %[wo], 1703936\nbuffer_load_dwordx4 v92, %[rw], s44 offen nt lds\ns_add_u32 m0, %[lw], 7168\ns_add_u32 s44, %[wo], 1769472\n" \
"buffer_load_dwordx4 v92, %[rw], s44 offen nt lds\ns_add_u32 m0, %[lw], 8192\ns_add_u32 s44, %[wo], 1835008\nbuffer_load_dwordx4 v92, %[rw], s44 offen nt lds\ns_add_u32 m0, %[lw], 9216\ns_add_u32 s44, %[wo], 1900544\nbuffer_load_dwordx4 v92, %[rw], s44 offen nt lds\ns_add_u32 m0, %[lw], 10240\n" \
"s_add_u32 s44, %[wo], 1966080\nbuffer_load_dwordx4 v92, %[rw], s44 offen nt lds\ns_add_u32 m0, %[lw], 11264\ns_add_u32 s44, %[wo], 2031616\nbuffer_load_dwordx4 v92, %[rw], s44 offen nt lds\ns_add_u32 m0, %[lw], 12288\ns_add_u32 s44, %[wo], 2097152\nbuffer_load_dwordx4 v92, %[rw], s44 offen nt lds\n" \
"s_add_u32 m0, %[lw], 13312\ns_add_u32 s44, %[wo], 2162688\nbuffer_load_dwordx4 v92, %[rw], s44 offen nt lds\ns_add_u32 m0, %[lw], 14336\ns_add_u32 s44, %[wo], 2228224\nbuffer_load_dwordx4 v92, %[rw], s44 offen nt lds\ns_add_u32 m0, %[lw], 15360\ns_add_u32 s44, %[wo], 2293760\n" \
"buffer_load_dwordx4 v92, %[rw], s44 offen nt lds\ns_waitcnt vmcnt(32)\nds_read_b64_tr_b16 a[0:1], v94 offset:16384\nds_read_b64_tr_b16 a[2:3], v94 offset:16896\nds_read_b64_tr_b16 a[8:9], v94 offset:16392\nds_read_b64_tr_b16 a[10:11], v94 offset:16904\nds_read_b64_tr_b16 a[16:17], v94 offset:16448\nds_read_b64_tr_b16 a[18:19], v94 offset:16960\n" \
"ds_read_b64_tr_b16 a[24:25], v94 offset:16456\nds_read_b64_tr_b16 a[26:27], v94 offset:16968\ns_waitcnt lgkmcnt(0)\ns_add_u32 m0, %[lw], 16384\ns_add_u32 s44, %[wo], 2359296\nbuffer_load_dwordx4 v92, %[rw], s44 offen nt lds\ns_add_u32 m0, %[lw], 17408\ns_add_u32 s44, %[wo], 2424832\n" \
"buffer_load_dwordx4 v92, %[rw], s44 offen nt lds\ns_add_u32 m0, %[lw], 18432\ns_add_u32 s44, %[wo], 2490368\nbuffer_load_dwordx4 v92, %[rw], s44 offen nt lds\ns_add_u32 m0, %[lw], 19456\ns_add_u32 s44, %[wo], 2555904\nbuffer_load_dwordx4 v92, %[rw], s44 offen nt lds\ns_waitcnt vmcnt(32)\n" \
"ds_read_b64_tr_b16 a[4:5], v94 offset:20480\nds_read_b64_tr_b16 a[6:7], v94 offset:20992\nds_read_b64_tr_b16 a[12:13], v94 offset:20488\nds_read_b64_tr_b16 a[14:15], v94 offset:21000\nds_read_b64_tr_b16 a[20:21], v94 offset:20544\nds_read_b64_tr_b16 a[22:23], v94 offset:21056\nds_read_b64_tr_b16 a[28:29], v94 offset:20552\nds_read_b64_tr_b16 a[30:31], v94 offset:21064\n" \
"s_waitcnt lgkmcnt(0)\ns_add_u32 m0, %[lw], 20480\ns_add_u32 s44, %[wo], 2621440\nbuffer_load_dwordx4 v92, %[rw], s44 offen nt lds\ns_add_u32 m0, %[lw], 21504\ns_add_u32 s44, %[wo], 2686976\nbuffer_load_dwordx4 v92, %[rw], s44 offen nt lds\ns_add_u32 m0, %[lw], 22528\n" \
"s_add_u32 s44, %[wo], 2752512\nbuffer_load_dwordx4 v92, %[rw], s44 offen nt lds\ns_add_u32 m0, %[lw], 23552\ns_add_u32 s44, %[wo], 2818048\nbuffer_load_dwordx4 v92, %[rw], s44 offen nt lds\ns_waitcnt vmcnt(32)\nds_read_b64_tr_b16 a[32:33], v93 offset:0\nds_read_b64_tr_b16 a[34:35], v93 offset:512\n" \
"ds_read_b64_tr_b16 v[88:89], v93 offset:8\nds_read_b64_tr_b16 v[90:91], v93 offset:520\nds_read_b64_tr_b16 a[40:41], v93 offset:64\nds_read_b64_tr_b16 a[42:43], v93 offset:576\nds_read_b64_tr_b16 a[48:49], v93 offset:72\nds_read_b64_tr_b16 a[50:51], v93 offset:584\ns_waitcnt lgkmcnt(0)\ns_add_u32 m0, %[tb], 0\n" \
"s_add_u32 s44, %[wo], 2883584\nbuffer_load_dwordx4 v92, %[rw], s44 offen nt lds\ns_add_u32 m0, %[tb], 1024\ns_add_u32 s44, %[wo], 2949120\nbuffer_load_dwordx4 v92, %[rw], s44 offen nt lds\ns_add_u32 m0, %[tb], 2048\ns_add_u32 s44, %[wo], 3014656\nbuffer_load_dwordx4 v92, %[rw], s44 offen nt lds\n" \
"s_add_u32 m0, %[tb], 3072\ns_add_u32 s44, %[wo], 3080192\nbuffer_load_dwordx4 v92, %[rw], s44 offen nt lds\ns_waitcnt vmcnt(32)\nds_read_b64_tr_b16 a[36:37], v93 offset:4096\nds_read_b64_tr_b16 a[38:39], v93 offset:4608\nds_read_b64_tr_b16 v[84:85], v93 offset:4104\nds_read_b64_tr_b16 v[86:87], v93 offset:4616\n" \
"ds_read_b64_tr_b16 a[44:45], v93 offset:4160\nds_read_b64_tr_b16 a[46:47], v93 offset:4672\nds_read_b64_tr_b16 a[52:53], v93 offset:4168\nds_read_b64_tr_b16 a[54:55], v93 offset:4680\ns_waitcnt lgkmcnt(0)\ns_add_u32 m0, %[tb], 4096\ns_add_u32 s44, %[wo], 3145728\nbuffer_load_dwordx4 v92, %[rw], s44 offen nt lds\n" \
"s_add_u32 m0, %[tb], 5120\ns_add_u32 s44, %[wo], 3211264\nbuffer_load_dwordx4 v92, %[rw], s44 offen nt lds\ns_add_u32 m0, %[tb], 6144\ns_add_u32 s44, %[wo], 3276800\nbuffer_load_dwordx4 v92, %[rw], s44 offen nt lds\ns_add_u32 m0, %[tb], 7168\ns_add_u32 s44, %[wo], 3342336\n" \
"buffer_load_dwordx4 v92, %[rw], s44 offen nt lds\ns_waitcnt vmcnt(32)\nds_read_b64_tr_b16 a[56:57], v93 offset:8192\nds_read_b64_tr_b16 a[58:59], v93 offset:8704\nds_read_b64_tr_b16 a[64:65], v93 offset:8200\nds_read_b64_tr_b16 a[66:67], v93 offset:8712\nds_read_b64_tr_b16 v[80:81], v93 offset:8256\nds_read_b64_tr_b16 v[82:83], v93 offset:8768\n" \
"ds_read_b64_tr_b16 a[72:73], v93 offset:8264\nds_read_b64_tr_b16 a[74:75], v93 offset:8776\ns_waitcnt lgkmcnt(0)\ns_add_u32 m0, %[tb], 8192\ns_add_u32 s44, %[wo], 3407872\nbuffer_load_dwordx4 v92, %[rw], s44 offen nt lds\ns_add_u32 m0, %[tb], 9216\ns_add_u32 s44, %[wo], 3473408\n" \
"buffer_load_dwordx4 v92, %[rw], s44 offen nt lds\ns_add_u32 m0, %[tb], 10240\ns_add_u32 s44, %[wo], 3538944\nbuffer_load_dwordx4 v92, %[rw], s44 offen nt lds\ns_add_u32 m0, %[tb], 11264\ns_add_u32 s44, %[wo], 3604480\nbuffer_load_dwordx4 v92, %[rw], s44 offen nt lds\ns_waitcnt vmcnt(32)\n" \
"ds_read_b64_tr_b16 a[60:61], v94 offset:0\nds_read_b64_tr_b16 a[62:63], v94 offset:512\nds_read_b64_tr_b16 a[68:69], v94 offset:8\nds_read_b64_tr_b16 a[70:71], v94 offset:520\nds_read_b64_tr_b16 v[76:77], v94 offset:64\nds_read_b64_tr_b16 v[78:79], v94 offset:576\nds_read_b64_tr_b16 a[76:77], v94 offset:72\nds_read_b64_tr_b16 a[78:79], v94 offset:584\n" \
"s_waitcnt lgkmcnt(0)\ns_add_u32 m0, %[lw], 0\ns_add_u32 s44, %[wo], 3670016\nbuffer_load_dwordx4 v92, %[rw], s44 offen nt lds\ns_add_u32 m0, %[lw], 1024\ns_add_u32 s44, %[wo], 3735552\nbuffer_load_dwordx4 v92, %[rw], s44 offen nt lds\ns_add_u32 m0, %[lw], 2048\n" \
"s_add_u32 s44, %[wo], 3801088\nbuffer_load_dwordx4 v92, %[rw], s44 offen nt lds\ns_add_u32 m0, %[lw], 3072\ns_add_u32 s44, %[wo], 3866624\nbuffer_load_dwordx4 v92, %[rw], s44 offen nt lds\ns_waitcnt vmcnt(32)\nds_read_b64_tr_b16 a[80:81], v94 offset:4096\nds_read_b64_tr_b16 a[82:83], v94 offset:4608\n" \
"ds_read_b64_tr_b16 a[88:89], v94 offset:4104\nds_read_b64_tr_b16 a[90:91], v94 offset:4616\nds_read_b64_tr_b16 a[96:97], v94 offset:4160\nds_read_b64_tr_b16 a[98:99], v94 offset:4672\nds_read_b64_tr_b16 v[72:73], v94 offset:4168\nds_read_b64_tr_b16 v[74:75], v94 offset:4680\ns_waitcnt lgkmcnt(0)\ns_add_u32 m0, %[lw], 4096\n" \
"s_add_u32 s44, %[wo], 3932160\nbuffer_load_dwordx4 v92, %[rw], s44 offen nt lds\ns_add_u32 m0, %[lw], 5120\ns_add_u32 s44, %[wo], 3997696\nbuffer_load_dwordx4 v92, %[rw], s44 offen nt lds\ns_add_u32 m0, %[lw], 6144\ns_add_u32 s44, %[wo], 4063232\nbuffer_load_dwordx4 v92, %[rw], s44 offen nt lds\n" \
"s_add_u32 m0, %[lw], 7168\ns_add_u32 s44, %[wo], 4128768\nbuffer_load_dwordx4 v92, %[rw], s44 offen nt lds\ns_waitcnt vmcnt(32)\nds_read_b64_tr_b16 a[84:85], v94 offset:8192\nds_read_b64_tr_b16 a[86:87], v94 offset:8704\nds_read_b64_tr_b16 a[92:93], v94 offset:8200\nds_read_b64_tr_b16 a[94:95], v94 offset:8712\n" \
"ds_read_b64_tr_b16 a[100:101], v94 offset:8256\nds_read_b64_tr_b16 a[102:103], v94 offset:8768\nds_read_b64_tr_b16 v[68:69], v94 offset:8264\nds_read_b64_tr_b16 v[70:71], v94 offset:8776\ns_waitcnt lgkmcnt(0)\ns_add_u32 m0, %[lw], 8192\ns_add_u32 s44, %[wo], 4194304\nbuffer_load_dwordx4 v92, %[rw], s44 offen nt lds\n" \
"s_add_u32 m0, %[lw], 9216\ns_add_u32 s44, %[wo], 4259840\nbuffer_load_dwordx4 v92, %[rw], s44 offen nt lds\ns_add_u32 m0, %[lw], 10240\ns_add_u32 s44, %[wo], 4325376\nbuffer_load_dwordx4 v92, %[rw], s44 offen nt lds\ns_add_u32 m0, %[lw], 11264\ns_add_u32 s44, %[wo], 4390912\n" \
"buffer_load_dwordx4 v92, %[rw], s44 offen nt lds\ns_waitcnt vmcnt(32)\nds_read_b64_tr_b16 a[104:105], v94 offset:12288\nds_read_b64_tr_b16 a[106:107], v94 offset:12800\nds_read_b64_tr_b16 a[112:113], v94 offset:12296\nds_read_b64_tr_b16 a[114:115], v94 offset:12808\nds_read_b64_tr_b16 a[120:121], v94 offset:12352\nds_read_b64_tr_b16 a[122:123], v94 offset:12864\n" \
"ds_read_b64_tr_b16 a[128:129], v94 offset:12360\nds_read_b64_tr_b16 a[130:131], v94 offset:12872\ns_waitcnt lgkmcnt(0)\ns_add_u32 m0, %[lw], 12288\ns_add_u32 s44, %[wo], 4456448\nbuffer_load_dwordx4 v92, %[rw], s44 offen nt lds\ns_add_u32 m0, %[lw], 13312\ns_add_u32 s44, %[wo], 4521984\n" \
"buffer_load_dwordx4 v92, %[rw], s44 offen nt lds\ns_add_u32 m0, %[lw], 14336\ns_add_u32 s44, %[wo], 4587520\nbuffer_load_dwordx4 v92, %[rw], s44 offen nt lds\ns_add_u32 m0, %[lw], 15360\ns_add_u32 s44, %[wo], 4653056\nbuffer_load_dwordx4 v92, %[rw], s44 offen nt lds\ns_waitcnt vmcnt(32)\n" \
"ds_read_b64_tr_b16 a[108:109], v94 offset:16384\nds_read_b64_tr_b16 a[110:111], v94 offset:16896\nds_read_b64_tr_b16 a[116:117], v94 offset:16392\nds_read_b64_tr_b16 a[118:119], v94 offset:16904\nds_read_b64_tr_b16 a[124:125], v94 offset:16448\nds_read_b64_tr_b16 a[126:127], v94 offset:16960\nds_read_b64_tr_b16 a[132:133], v94 offset:16456\nds_read_b64_tr_b16 a[134:135], v94 offset:16968\n" \
"s_waitcnt lgkmcnt(0)\ns_add_u32 m0, %[lw], 16384\ns_add_u32 s44, %[wo], 4718592\nbuffer_load_dwordx4 v92, %[rw], s44 offen nt lds\ns_add_u32 m0, %[lw], 17408\ns_add_u32 s44, %[wo], 4784128\nbuffer_load_dwordx4 v92, %[rw], s44 offen nt lds\ns_add_u32 m0, %[lw], 18432\n" \
"s_add_u32 s44, %[wo], 4849664\nbuffer_load_dwordx4 v92, %[rw], s44 offen nt lds\ns_add_u32 m0, %[lw], 19456\ns_add_u32 s44, %[wo], 4915200\nbuffer_load_dwordx4 v92, %[rw], s44 offen nt lds\ns_waitcnt vmcnt(32)\nds_read_b64_tr_b16 a[136:137], v94 offset:20480\nds_read_b64_tr_b16 a[138:139], v94 offset:20992\n" \
"ds_read_b64_tr_b16 v[64:65], v94 offset:20488\nds_read_b64_tr_b16 v[66:67], v94 offset:21000\nds_read_b64_tr_b16 a[144:145], v94 offset:20544\nds_read_b64_tr_b16 a[146:147], v94 offset:21056\nds_read_b64_tr_b16 a[152:153], v94 offset:20552\nds_read_b64_tr_b16 a[154:155], v94 offset:21064\ns_waitcnt lgkmcnt(0)\ns_add_u32 m0, %[lw], 20480\n" \
"s_add_u32 s44, %[wo], 4980736\nbuffer_load_dwordx4 v92, %[rw], s44 offen nt lds\ns_add_u32 m0, %[lw], 21504\ns_add_u32 s44, %[wo], 5046272\nbuffer_load_dwordx4 v92, %[rw], s44 offen nt lds\ns_add_u32 m0, %[lw], 22528\ns_add_u32 s44, %[wo], 5111808\nbuffer_load_dwordx4 v92, %[rw], s44 offen nt lds\n" \
"s_add_u32 m0, %[lw], 23552\ns_add_u32 s44, %[wo], 5177344\nbuffer_load_dwordx4 v92, %[rw], s44 offen nt lds\ns_waitcnt vmcnt(32)\nds_read_b64_tr_b16 a[140:141], v93 offset:0\nds_read_b64_tr_b16 a[142:143], v93 offset:512\nds_read_b64_tr_b16 v[60:61], v93 offset:8\nds_read_b64_tr_b16 v[62:63], v93 offset:520\n" \
"ds_read_b64_tr_b16 a[148:149], v93 offset:64\nds_read_b64_tr_b16 a[150:151], v93 offset:576\nds_read_b64_tr_b16 a[156:157], v93 offset:72\nds_read_b64_tr_b16 a[158:159], v93 offset:584\ns_waitcnt lgkmcnt(0)\ns_add_u32 m0, %[tb], 0\ns_add_u32 s44, %[wo], 5242880\nbuffer_load_dwordx4 v92, %[rw], s44 offen nt lds\n" \
"s_add_u32 m0, %[tb], 1024\ns_add_u32 s44, %[wo], 5308416\nbuffer_load_dwordx4 v92, %[rw], s44 offen nt lds\ns_add_u32 m0, %[tb], 2048\ns_add_u32 s44, %[wo], 5373952\nbuffer_load_dwordx4 v92, %[rw], s44 offen nt lds\ns_add_u32 m0, %[tb], 3072\ns_add_u32 s44, %[wo], 5439488\n" \
"buffer_load_dwordx4 v92, %[rw], s44 offen nt lds\ns_waitcnt vmcnt(32)\nds_read_b64_tr_b16 a[160:161], v93 offset:4096\nds_read_b64_tr_b16 a[162:163], v93 offset:4608\nds_read_b64_tr_b16 a[168:169], v93 offset:4104\nds_read_b64_tr_b16 a[170:171], v93 offset:4616\nds_read_b64_tr_b16 v[56:57], v93 offset:4160\nds_read_b64_tr_b16 v[58:59], v93 offset:4672\n" \
"ds_read_b64_tr_b16 a[176:177], v93 offset:4168\nds_read_b64_tr_b16 a[178:179], v93 offset:4680\ns_waitcnt lgkmcnt(0)\ns_add_u32 m0, %[tb], 4096\ns_add_u32 s44, %[wo], 5505024\nbuffer_load_dwordx4 v92, %[rw], s44 offen nt lds\ns_add_u32 m0, %[tb], 5120\ns_add_u32 s44, %[wo], 5570560\n" \
"buffer_load_dwordx4 v92, %[rw], s44 offen nt lds\ns_add_u32 m0, %[tb], 6144\ns_add_u32 s44, %[wo], 5636096\nbuffer_load_dwordx4 v92, %[rw], s44 offen nt lds\ns_add_u32 m0, %[tb], 7168\ns_add_u32 s44, %[wo], 5701632\nbuffer_load_dwordx4 v92, %[rw], s44 offen nt lds\ns_waitcnt vmcnt(32)\n" \
"ds_read_b64_tr_b16 a[164:165], v93 offset:8192\nds_read_b64_tr_b16 a[166:167], v93 offset:8704\nds_read_b64_tr_b16 a[172:173], v93 offset:8200\nds_read_b64_tr_b16 a[174:175], v93 offset:8712\nds_read_b64_tr_b16 v[52:53], v93 offset:8256\nds_read_b64_tr_b16 v[54:55], v93 offset:8768\nds_read_b64_tr_b16 a[180:181], v93 offset:8264\nds_read_b64_tr_b16 a[182:183], v93 offset:8776\n" \
"s_waitcnt lgkmcnt(0)\ns_add_u32 m0, %[tb], 8192\ns_add_u32 s44, %[wo], 5767168\nbuffer_load_dwordx4 v92, %[rw], s44 offen nt lds\ns_add_u32 m0, %[tb], 9216\ns_add_u32 s44, %[wo], 5832704\nbuffer_load_dwordx4 v92, %[rw], s44 offen nt lds\ns_add_u32 m0, %[tb], 10240\n" \
"s_add_u32 s44, %[wo], 5898240\nbuffer_load_dwordx4 v92, %[rw], s44 offen nt lds\ns_add_u32 m0, %[tb], 11264\ns_add_u32 s44, %[wo], 5963776\nbuffer_load_dwordx4 v92, %[rw], s44 offen nt lds\ns_waitcnt vmcnt(32)\nds_read_b64_tr_b16 a[184:185], v94 offset:0\nds_read_b64_tr_b16 a[186:187], v94 offset:512\n" \
"ds_read_b64_tr_b16 a[192:193], v94 offset:8\nds_read_b64_tr_b16 a[194:195], v94 offset:520\nds_read_b64_tr_b16 a[200:201], v94 offset:64\nds_read_b64_tr_b16 a[202:203], v94 offset:576\nds_read_b64_tr_b16 v[48:49], v94 offset:72\nds_read_b64_tr_b16 v[50:51], v94 offset:584\ns_waitcnt lgkmcnt(0)\ns_add_u32 m0, %[lw], 0\n" \
"s_add_u32 s44, %[wo], 6029312\nbuffer_load_dwordx4 v92, %[rw], s44 offen nt lds\ns_add_u32 m0, %[lw], 1024\ns_add_u32 s44, %[wo], 6094848\nbuffer_load_dwordx4 v92, %[rw], s44 offen nt lds\ns_add_u32 m0, %[lw], 2048\ns_add_u32 s44, %[wo], 6160384\nbuffer_load_dwordx4 v92, %[rw], s44 offen nt lds\n" \
"s_add_u32 m0, %[lw], 3072\ns_add_u32 s44, %[wo], 6225920\nbuffer_load_dwordx4 v92, %[rw], s44 offen nt lds\ns_waitcnt vmcnt(32)\nds_read_b64_tr_b16 a[188:189], v94 offset:4096\nds_read_b64_tr_b16 a[190:191], v94 offset:4608\nds_read_b64_tr_b16 a[196:197], v94 offset:4104\nds_read_b64_tr_b16 a[198:199], v94 offset:4616\n" \
"ds_read_b64_tr_b16 a[204:205], v94 offset:4160\nds_read_b64_tr_b16 a[206:207], v94 offset:4672\nds_read_b64_tr_b16 v[44:45], v94 offset:4168\nds_read_b64_tr_b16 v[46:47], v94 offset:4680\ns_waitcnt lgkmcnt(0)\ns_add_u32 m0, %[lw], 4096\ns_add_u32 s44, %[wo], 6291456\nbuffer_load_dwordx4 v92, %[rw], s44 offen nt lds\n" \
"s_add_u32 m0, %[lw], 5120\ns_add_u32 s44, %[wo], 6356992\nbuffer_load_dwordx4 v92, %[rw], s44 offen nt lds\ns_add_u32 m0, %[lw], 6144\ns_add_u32 s44, %[wo], 6422528\nbuffer_load_dwordx4 v92, %[rw], s44 offen nt lds\ns_add_u32 m0, %[lw], 7168\ns_add_u32 s44, %[wo], 6488064\n" \
"buffer_load_dwordx4 v92, %[rw], s44 offen nt lds\ns_waitcnt vmcnt(32)\nds_read_b64_tr_b16 a[208:209], v94 offset:8192\nds_read_b64_tr_b16 a[210:211], v94 offset:8704\nds_read_b64_tr_b16 a[216:217], v94 offset:8200\nds_read_b64_tr_b16 a[218:219], v94 offset:8712\nds_read_b64_tr_b16 a[224:225], v94 offset:8256\nds_read_b64_tr_b16 a[226:227], v94 offset:8768\n" \
"ds_read_b64_tr_b16 a[232:233], v94 offset:8264\nds_read_b64_tr_b16 a[234:235], v94 offset:8776\ns_waitcnt lgkmcnt(0)\ns_add_u32 m0, %[lw], 8192\ns_add_u32 s44, %[wo], 6553600\nbuffer_load_dwordx4 v92, %[rw], s44 offen nt lds\ns_add_u32 m0, %[lw], 9216\ns_add_u32 s44, %[wo], 6619136\n" \
"buffer_load_dwordx4 v92, %[rw], s44 offen nt lds\ns_add_u32 m0, %[lw], 10240\ns_add_u32 s44, %[wo], 6684672\nbuffer_load_dwordx4 v92, %[rw], s44 offen nt lds\ns_add_u32 m0, %[lw], 11264\ns_add_u32 s44, %[wo], 6750208\nbuffer_load_dwordx4 v92, %[rw], s44 offen nt lds\ns_waitcnt vmcnt(32)\n" \
"ds_read_b64_tr_b16 a[212:213], v94 offset:12288\nds_read_b64_tr_b16 a[214:215], v94 offset:12800\nds_read_b64_tr_b16 a[220:221], v94 offset:12296\nds_read_b64_tr_b16 a[222:223], v94 offset:12808\nds_read_b64_tr_b16 a[228:229], v94 offset:12352\nds_read_b64_tr_b16 a[230:231], v94 offset:12864\nds_read_b64_tr_b16 a[236:237], v94 offset:12360\nds_read_b64_tr_b16 a[238:239], v94 offset:12872\n" \
"s_waitcnt lgkmcnt(0)\ns_add_u32 m0, %[lw], 12288\ns_add_u32 s44, %[wo], 6815744\nbuffer_load_dwordx4 v92, %[rw], s44 offen nt lds\ns_add_u32 m0, %[lw], 13312\ns_add_u32 s44, %[wo], 6881280\nbuffer_load_dwordx4 v92, %[rw], s44 offen nt lds\ns_add_u32 m0, %[lw], 14336\n" \
"s_add_u32 s44, %[wo], 6946816\nbuffer_load_dwordx4 v92, %[rw], s44 offen nt lds\ns_add_u32 m0, %[lw], 15360\ns_add_u32 s44, %[wo], 7012352\nbuffer_load_dwordx4 v92, %[rw], s44 offen nt lds\ns_waitcnt vmcnt(32)\nds_read_b64_tr_b16 a[240:241], v94 offset:16384\nds_read_b64_tr_b16 a[242:243], v94 offset:16896\n" \
"ds_read_b64_tr_b16 v[40:41], v94 offset:16392\nds_read_b64_tr_b16 v[42:43], v94 offset:16904\nds_read_b64_tr_b16 a[248:249], v94 offset:16448\nds_read_b64_tr_b16 a[250:251], v94 offset:16960\nds_read_b64_tr_b16 v[96:97], v94 offset:16456\nds_read_b64_tr_b16 v[98:99], v94 offset:16968\ns_waitcnt lgkmcnt(0)\ns_add_u32 m0, %[lw], 16384\n" \
"s_add_u32 s44, %[wo], 7077888\nbuffer_load_dwordx4 v92, %[rw], s44 offen nt lds\ns_add_u32 m0, %[lw], 17408\ns_add_u32 s44, %[wo], 7143424\nbuffer_load_dwordx4 v92, %[rw], s44 offen nt lds\ns_add_u32 m0, %[lw], 18432\ns_add_u32 s44, %[wo], 7208960\nbuffer_load_dwordx4 v92, %[rw], s44 offen nt lds\n" \
"s_add_u32 m0, %[lw], 19456\ns_add_u32 s44, %[wo], 7274496\nbuffer_load_dwordx4 v92, %[rw], s44 offen nt lds\ns_waitcnt vmcnt(32)\nds_read_b64_tr_b16 a[244:245], v94 offset:20480\nds_read_b64_tr_b16 a[246:247], v94 offset:20992\nds_read_b64_tr_b16 v[36:37], v94 offset:20488\nds_read_b64_tr_b16 v[38:39], v94 offset:21000\n" \
"ds_read_b64_tr_b16 a[252:253], v94 offset:20544\nds_read_b64_tr_b16 a[254:255], v94 offset:21056\nds_read_b64_tr_b16 v[100:101], v94 offset:20552\nds_read_b64_tr_b16 v[102:103], v94 offset:21064\ns_waitcnt lgkmcnt(0)\ns_add_u32 m0, %[lw], 20480\ns_add_u32 s44, %[wo], 7340032\nbuffer_load_dwordx4 v92, %[rw], s44 offen nt lds\n" \
"s_add_u32 m0, %[lw], 21504\ns_add_u32 s44, %[wo], 7405568\nbuffer_load_dwordx4 v92, %[rw], s44 offen nt lds\ns_add_u32 m0, %[lw], 22528\ns_add_u32 s44, %[wo], 7471104\nbuffer_load_dwordx4 v92, %[rw], s44 offen nt lds\ns_add_u32 m0, %[lw], 23552\ns_add_u32 s44, %[wo], 7536640\n" \
"buffer_load_dwordx4 v92, %[rw], s44 offen nt lds\ns_waitcnt vmcnt(32)\nds_read_b64_tr_b16 v[104:105], v93 offset:0\nds_read_b64_tr_b16 v[106:107], v93 offset:512\nds_read_b64_tr_b16 v[112:113], v93 offset:8\nds_read_b64_tr_b16 v[114:115], v93 offset:520\nds_read_b64_tr_b16 v[32:33], v93 offset:64\nds_read_b64_tr_b16 v[34:35], v93 offset:576\n" \
"ds_read_b64_tr_b16 v[120:121], v93 offset:72\nds_read_b64_tr_b16 v[122:123], v93 offset:584\ns_waitcnt lgkmcnt(0)\ns_add_u32 m0, %[tb], 0\ns_add_u32 s44, %[wo], 7602176\nbuffer_load_dwordx4 v92, %[rw], s44 offen nt lds\ns_add_u32 m0, %[tb], 1024\ns_add_u32 s44, %[wo], 7667712\n" \
"buffer_load_dwordx4 v92, %[rw], s44 offen nt lds\ns_add_u32 m0, %[tb], 2048\ns_add_u32 s44, %[wo], 7733248\nbuffer_load_dwordx4 v92, %[rw], s44 offen nt lds\ns_add_u32 m0, %[tb], 3072\ns_add_u32 s44, %[wo], 7798784\nbuffer_load_dwordx4 v92, %[rw], s44 offen nt lds\ns_waitcnt vmcnt(32)\n" \
"ds_read_b64_tr_b16 v[108:109], v93 offset:4096\nds_read_b64_tr_b16 v[110:111], v93 offset:4608\nds_read_b64_tr_b16 v[116:117], v93 offset:4104\nds_read_b64_tr_b16 v[118:119], v93 offset:4616\nds_read_b64_tr_b16 v[28:29], v93 offset:4160\nds_read_b64_tr_b16 v[30:31], v93 offset:4672\nds_read_b64_tr_b16 v[124:125], v93 offset:4168\nds_read_b64_tr_b16 v[126:127], v93 offset:4680\n" \
"s_waitcnt lgkmcnt(0)\ns_add_u32 m0, %[tb], 4096\ns_add_u32 s44, %[wo], 7864320\nbuffer_load_dwordx4 v92, %[rw], s44 offen nt lds\ns_add_u32 m0, %[tb], 5120\ns_add_u32 s44, %[wo], 7929856\nbuffer_load_dwordx4 v92, %[rw], s44 offen nt lds\ns_add_u32 m0, %[tb], 6144\n" \
"s_add_u32 s44, %[wo], 7995392\nbuffer_load_dwordx4 v92, %[rw], s44 offen nt lds\ns_add_u32 m0, %[tb], 7168\ns_add_u32 s44, %[wo], 8060928\nbuffer_load_dwordx4 v92, %[rw], s44 offen nt lds\ns_waitcnt vmcnt(32)\nds_read_b64_tr_b16 v[128:129], v93 offset:8192\nds_read_b64_tr_b16 v[130:131], v93 offset:8704\n" \
"ds_read_b64_tr_b16 v[136:137], v93 offset:8200\nds_read_b64_tr_b16 v[138:139], v93 offset:8712\nds_read_b64_tr_b16 v[144:145], v93 offset:8256\nds_read_b64_tr_b16 v[146:147], v93 offset:8768\nds_read_b64_tr_b16 v[24:25], v93 offset:8264\nds_read_b64_tr_b16 v[26:27], v93 offset:8776\ns_waitcnt lgkmcnt(0)\ns_add_u32 m0, %[tb], 8192\n" \
"s_add_u32 s44, %[wo], 8126464\nbuffer_load_dwordx4 v92, %[rw], s44 offen nt lds\ns_add_u32 m0, %[tb], 9216\ns_add_u32 s44, %[wo], 8192000\nbuffer_load_dwordx4 v92, %[rw], s44 offen nt lds\ns_add_u32 m0, %[tb], 10240\ns_add_u32 s44, %[wo], 8257536\nbuffer_load_dwordx4 v92, %[rw], s44 offen nt lds\n" \
"s_add_u32 m0, %[tb], 11264\ns_add_u32 s44, %[wo], 8323072\nbuffer_load_dwordx4 v92, %[rw], s44 offen nt lds\ns_waitcnt vmcnt(32)\nds_read_b64_tr_b16 v[132:133], v94 offset:0\nds_read_b64_tr_b16 v[134:135], v94 offset:512\nds_read_b64_tr_b16 v[140:141], v94 offset:8\nds_read_b64_tr_b16 v[142:143], v94 offset:520\n" \
"ds_read_b64_tr_b16 v[148:149], v94 offset:64\nds_read_b64_tr_b16 v[150:151], v94 offset:576\nds_read_b64_tr_b16 v[20:21], v94 offset:72\nds_read_b64_tr_b16 v[22:23], v94 offset:584\ns_waitcnt lgkmcnt(0)\nds_write_b128 v1, v[88:91] offset:0\nds_write_b128 v1, v[84:87] offset:1024\nds_write_b128 v1, v[80:83] offset:2048\n" \
"ds_write_b128 v1, v[76:79] offset:3072\ns_waitcnt vmcnt(28)\nds_read_b64_tr_b16 v[152:153], v94 offset:4096\nds_read_b64_tr_b16 v[154:155], v94 offset:4608\nds_read_b64_tr_b16 v[160:161], v94 offset:4104\nds_read_b64_tr_b16 v[162:163], v94 offset:4616\nds_read_b64_tr_b16 v[168:169], v94 offset:4160\nds_read_b64_tr_b16 v[170:171], v94 offset:4672\n" \
"ds_read_b64_tr_b16 v[176:177], v94 offset:4168\nds_read_b64_tr_b16 v[178:179], v94 offset:4680\ns_waitcnt lgkmcnt(0)\nds_write_b128 v1, v[72:75] offset:4096\nds_write_b128 v1, v[68:71] offset:5120\nds_write_b128 v1, v[64:67] offset:6144\nds_write_b128 v1, v[60:63] offset:7168\ns_waitcnt vmcnt(24)\n" \
"ds_read_b64_tr_b16 v[156:157], v94 offset:8192\nds_read_b64_tr_b16 v[158:159], v94 offset:8704\nds_read_b64_tr_b16 v[164:165], v94 offset:8200\nds_read_b64_tr_b16 v[166:167], v94 offset:8712\nds_read_b64_tr_b16 v[172:173], v94 offset:8256\nds_read_b64_tr_b16 v[174:175], v94 offset:8768\nds_read_b64_tr_b16 v[180:181], v94 offset:8264\nds_read_b64_tr_b16 v[182:183], v94 offset:8776\n" \
"s_waitcnt lgkmcnt(0)\nds_write_b128 v1, v[56:59] offset:8192\nds_write_b128 v1, v[52:55] offset:9216\nds_write_b128 v1, v[48:51] offset:10240\nds_write_b128 v1, v[44:47] offset:11264\ns_waitcnt vmcnt(20)\nds_read_b64_tr_b16 v[184:185], v94 offset:12288\nds_read_b64_tr_b16 v[186:187], v94 offset:12800\n" \
"ds_read_b64_tr_b16 v[44:45], v94 offset:12296\nds_read_b64_tr_b16 v[46:47], v94 offset:12808\nds_read_b64_tr_b16 v[192:193], v94 offset:12352\nds_read_b64_tr_b16 v[194:195], v94 offset:12864\nds_read_b64_tr_b16 v[200:201], v94 offset:12360\nds_read_b64_tr_b16 v[202:203], v94 offset:12872\ns_waitcnt lgkmcnt(0)\nds_write_b128 v1, v[40:43] offset:12288\n" \
"ds_write_b128 v1, v[36:39] offset:13312\nds_write_b128 v1, v[32:35] offset:14336\nds_write_b128 v1, v[28:31] offset:15360\ns_waitcnt vmcnt(16)\nds_read_b64_tr_b16 v[188:189], v94 offset:16384\nds_read_b64_tr_b16 v[190:191], v94 offset:16896\nds_read_b64_tr_b16 v[28:29], v94 offset:16392\nds_read_b64_tr_b16 v[30:31], v94 offset:16904\n" \
"ds_read_b64_tr_b16 v[196:197], v94 offset:16448\nds_read_b64_tr_b16 v[198:199], v94 offset:16960\nds_read_b64_tr_b16 v[204:205], v94 offset:16456\nds_read_b64_tr_b16 v[206:207], v94 offset:16968\ns_waitcnt lgkmcnt(0)\nds_write_b128 v1, v[24:27] offset:16384\nds_write_b128 v1, v[20:23] offset:17408\nds_write_b128 v1, v[44:47] offset:18432\n" \
"ds_write_b128 v1, v[28:31] offset:19456\ns_waitcnt vmcnt(12)\nds_read_b64_tr_b16 v[208:209], v94 offset:20480\nds_read_b64_tr_b16 v[210:211], v94 offset:20992\nds_read_b64_tr_b16 v[216:217], v94 offset:20488\nds_read_b64_tr_b16 v[218:219], v94 offset:21000\nds_read_b64_tr_b16 v[28:29], v94 offset:20544\nds_read_b64_tr_b16 v[30:31], v94 offset:21056\n" \
"ds_read_b64_tr_b16 v[224:225], v94 offset:20552\nds_read_b64_tr_b16 v[226:227], v94 offset:21064\ns_waitcnt lgkmcnt(0)\nds_write_b128 v1, v[28:31] offset:20480\ns_waitcnt vmcnt(8)\nds_read_b64_tr_b16 v[212:213], v93 offset:0\nds_read_b64_tr_b16 v[214:215], v93 offset:512\nds_read_b64_tr_b16 v[220:221], v93 offset:8\n" \
"ds_read_b64_tr_b16 v[222:223], v93 offset:520\nds_read_b64_tr_b16 v[28:29], v93 offset:64\nds_read_b64_tr_b16 v[30:31], v93 offset:576\nds_read_b64_tr_b16 v[228:229], v93 offset:72\nds_read_b64_tr_b16 v[230:231], v93 offset:584\ns_waitcnt lgkmcnt(0)\nds_write_b128 v1, v[28:31] offset:21504\ns_waitcnt vmcnt(4)\n" \
"ds_read_b64_tr_b16 v[232:233], v93 offset:4096\nds_read_b64_tr_b16 v[234:235], v93 offset:4608\nds_read_b64_tr_b16 v[240:241], v93 offset:4104\nds_read_b64_tr_b16 v[242:243], v93 offset:4616\nds_read_b64_tr_b16 v[248:249], v93 offset:4160\nds_read_b64_tr_b16 v[250:251], v93 offset:4672\nds_read_b64_tr_b16 v[28:29], v93 offset:4168\nds_read_b64_tr_b16 v[30:31], v93 offset:4680\n" \
"s_waitcnt lgkmcnt(0)\nds_write_b128 v1, v[28:31] offset:22528\ns_waitcnt vmcnt(0)\nds_read_b64_tr_b16 v[236:237], v93 offset:8192\nds_read_b64_tr_b16 v[238:239], v93 offset:8704\nds_read_b64_tr_b16 v[244:245], v93 offset:8200\nds_read_b64_tr_b16 v[246:247], v93 offset:8712\nds_read_b64_tr_b16 v[252:253], v93 offset:8256\n" \
"ds_read_b64_tr_b16 v[254:255], v93 offset:8768\nds_read_b64_tr_b16 v[28:29], v93 offset:8264\nds_read_b64_tr_b16 v[30:31], v93 offset:8776\ns_waitcnt lgkmcnt(0)\nds_write_b128 v1, v[28:31] offset:23552\ns_waitcnt lgkmcnt(0)\ns_barrier\ns_mov_b32 s43, 0\n" \
".Lqk_hb%=:\nbuffer_load_dwordx2 v[16:17], off, s[36:39], s45 sc1\ns_waitcnt vmcnt(0)\nv_cmp_ne_u32 vcc, v95, v16\ns_cbranch_vccnz .Lqk_hb1%=\ns_sleep 4\ns_add_u32 s43, s43, 1\ns_cmp_lt_u32 s43, 262144\n" \
"s_cbranch_scc1 .Lqk_hb%=\ns_branch .Lqk_abort%=\n.Lqk_hb1%=:\nv_readfirstlane_b32 s44, v17\ns_and_b32 s44, s44, 0x200000\ns_add_u32 s36, s36, s44\ns_addc_u32 s37, s37, 0\ns_mov_b32 s40, -1\n" \
"s_mov_b32 s43, 0\ns_mov_b32 s61, 0x7fff7fff\ns_mov_b32 s58, 0x3e0293ee\ns_mov_b32 s59, 0xbe0293ee\ns_lshl_b32 s57, %[w], 12\ns_mov_b32 s62, 0xff00ff00\ns_mov_b32 s63, 0xff00ff00\ns_mov_b32 s56, 0\n" \
"s_cmp_eq_u32 %[w], 3\ns_cbranch_scc1 .Lqk_fetch0%=\n.Lqk_pwait%=:\ns_barrier\nds_read_b128 v[80:83], v2 offset:0\nds_read_b128 v[84:87], v2 offset:256\nds_read_b128 v[88:91], v2 offset:2048\n.Lqk_proj%=:\n" \
"s_waitcnt lgkmcnt(2)\nv_mfma_f32_16x16x32_bf16 v[16:19], a[0:3], v[80:83], 0\ns_add_u32 s40, s40, 1\nds_read_b128 v[92:95], v2 offset:2304\nv_mfma_f32_16x16x32_bf16 v[20:23], a[8:11], v[80:83], 0\ns_lshl_b32 s44, s40, 17\nds_read_b128 v[72:75], v1 offset:0\nv_mfma_f32_16x16x32_bf16 v[24:27], a[16:19], v[80:83], 0\n" \
"s_add_u32 s44, s44, %[xn]\nv_mfma_f32_16x16x32_bf16 v[28:31], a[24:27], v[80:83], 0\ns_lshr_b32 s45, s44, 8\ns_waitcnt lgkmcnt(3)\nv_mfma_f32_16x16x32_bf16 v[32:35], a[0:3], v[84:87], 0\ns_and_b32 s45, s45, 0x2000\nds_read_b128 v[80:83], v2 offset:4096\nv_mfma_f32_16x16x32_bf16 v[36:39], a[8:11], v[84:87], 0\n" \
"s_xor_b32 s41, s44, s45\nv_mfma_f32_16x16x32_bf16 v[40:43], a[16:19], v[84:87], 0\ns_add_u32 s41, s41, %[xw]\nv_mfma_f32_16x16x32_bf16 v[44:47], a[24:27], v[84:87], 0\ns_and_b32 s42, s40, 3\ns_waitcnt lgkmcnt(3)\nv_mfma_f32_16x16x32_bf16 v[16:19], a[4:7], v[88:91], v[16:19]\ns_lshl_b32 s42, s42, 15\n" \
"ds_read_b128 v[84:87], v2 offset:4352\nv_mfma_f32_16x16x32_bf16 v[20:23], a[12:15], v[88:91], v[20:23]\ns_and_b32 s44, s40, 1\nds_read_b128 v[76:79], v1 offset:1024\nv_mfma_f32_16x16x32_bf16 v[24:27], a[20:23], v[88:91], v[24:27]\ns_lshl_b32 s55, s44, 17\nv_mfma_f32_16x16x32_bf16 v[28:31], a[28:31], v[88:91], v[28:31]\ns_add_u32 s56, s56, %[fs]\n" \
"s_waitcnt lgkmcnt(4)\nv_mfma_f32_16x16x32_bf16 v[32:35], a[4:7], v[92:95], v[32:35]\nds_read_b128 v[88:91], v2 offset:6144\nv_mfma_f32_16x16x32_bf16 v[36:39], a[12:15], v[92:95], v[36:39]\nv_mfma_f32_16x16x32_bf16 v[40:43], a[20:23], v[92:95], v[40:43]\nv_mfma_f32_16x16x32_bf16 v[44:47], a[28:31], v[92:95], v[44:47]\ns_waitcnt lgkmcnt(3)\nv_mfma_f32_16x16x32_bf16 v[16:19], a[32:35], v[80:83], v[16:19]\n" \
"ds_read_b128 v[92:95], v2 offset:6400\nv_mfma_f32_16x16x32_bf16 v[20:23], v[72:75], v[80:83], v[20:23]\nds_read_b128 v[64:67], v1 offset:2048\nv_mfma_f32_16x16x32_bf16 v[24:27], a[40:43], v[80:83], v[24:27]\nv_mfma_f32_16x16x32_bf16 v[28:31], a[48:51], v[80:83], v[28:31]\ns_waitcnt lgkmcnt(4)\nv_mfma_f32_16x16x32_bf16 v[32:35], a[32:35], v[84:87], v[32:35]\nds_read_b128 v[80:83], v2 offset:8192\n" \
"v_mfma_f32_16x16x32_bf16 v[36:39], v[72:75], v[84:87], v[36:39]\nv_mfma_f32_16x16x32_bf16 v[40:43], a[40:43], v[84:87], v[40:43]\ns_add_u32 m0, s57, 32768\ns_add_u32 s60, s41, 65536\nbuffer_load_dwordx4 v0, s[36:39], s60 offen offset:0 sc1 lds\nv_mfma_f32_16x16x32_bf16 v[44:47], a[48:51], v[84:87], v[44:47]\ns_waitcnt lgkmcnt(3)\nv_mfma_f32_16x16x32_bf16 v[16:19], a[36:39], v[88:91], v[16:19]\n" \
"ds_read_b128 v[84:87], v2 offset:8448\nv_mfma_f32_16x16x32_bf16 v[20:23], v[76:79], v[88:91], v[20:23]\nds_read_b128 v[68:71], v1 offset:3072\nv_mfma_f32_16x16x32_bf16 v[24:27], a[44:47], v[88:91], v[24:27]\nv_mfma_f32_16x16x32_bf16 v[28:31], a[52:55], v[88:91], v[28:31]\ns_waitcnt lgkmcnt(4)\nv_mfma_f32_16x16x32_bf16 v[32:35], a[36:39], v[92:95], v[32:35]\nds_read_b128 v[88:91], v2 offset:10240\n" \
"v_mfma_f32_16x16x32_bf16 v[36:39], v[76:79], v[92:95], v[36:39]\nv_mfma_f32_16x16x32_bf16 v[40:43], a[44:47], v[92:95], v[40:43]\nbuffer_load_dwordx4 v0, s[36:39], s60 offen offset:1024 sc1 lds\nv_mfma_f32_16x16x32_bf16 v[44:47], a[52:55], v[92:95], v[44:47]\ns_waitcnt lgkmcnt(3)\nv_mfma_f32_16x16x32_bf16 v[16:19], a[56:59], v[80:83], v[16:19]\nds_read_b128 v[92:95], v2 offset:10496\nv_mfma_f32_16x16x32_bf16 v[20:23], a[64:67], v[80:83], v[20:23]\n" \
"ds_read_b128 v[72:75], v1 offset:4096\nv_mfma_f32_16x16x32_bf16 v[24:27], v[64:67], v[80:83], v[24:27]\nv_mfma_f32_16x16x32_bf16 v[28:31], a[72:75], v[80:83], v[28:31]\ns_waitcnt lgkmcnt(4)\nv_mfma_f32_16x16x32_bf16 v[32:35], a[56:59], v[84:87], v[32:35]\nds_read_b128 v[80:83], v2 offset:12288\nv_mfma_f32_16x16x32_bf16 v[36:39], a[64:67], v[84:87], v[36:39]\nv_mfma_f32_16x16x32_bf16 v[40:43], v[64:67], v[84:87], v[40:43]\n" \
"buffer_load_dwordx4 v0, s[36:39], s60 offen offset:2048 sc1 lds\nv_mfma_f32_16x16x32_bf16 v[44:47], a[72:75], v[84:87], v[44:47]\ns_waitcnt lgkmcnt(3)\nv_mfma_f32_16x16x32_bf16 v[16:19], a[60:63], v[88:91], v[16:19]\nds_read_b128 v[84:87], v2 offset:12544\nv_mfma_f32_16x16x32_bf16 v[20:23], a[68:71], v[88:91], v[20:23]\nds_read_b128 v[76:79], v1 offset:5120\nv_mfma_f32_16x16x32_bf16 v[24:27], v[68:71], v[88:91], v[24:27]\n" \
"v_mfma_f32_16x16x32_bf16 v[28:31], a[76:79], v[88:91], v[28:31]\ns_waitcnt lgkmcnt(4)\nv_mfma_f32_16x16x32_bf16 v[32:35], a[60:63], v[92:95], v[32:35]\nds_read_b128 v[88:91], v2 offset:14336\nv_mfma_f32_16x16x32_bf16 v[36:39], a[68:71], v[92:95], v[36:39]\nv_mfma_f32_16x16x32_bf16 v[40:43], v[68:71], v[92:95], v[40:43]\nbuffer_load_dwordx4 v0, s[36:39], s60 offen offset:3072 sc1 lds\nv_mfma_f32_16x16x32_bf16 v[44:47], a[76:79], v[92:95], v[44:47]\n" \
"s_waitcnt lgkmcnt(3)\nv_mfma_f32_16x16x32_bf16 v[16:19], a[80:83], v[80:83], v[16:19]\nds_read_b128 v[92:95], v2 offset:14592\nv_mfma_f32_16x16x32_bf16 v[20:23], a[88:91], v[80:83], v[20:23]\nv_mfma_f32_16x16x32_bf16 v[24:27], a[96:99], v[80:83], v[24:27]\nv_mfma_f32_16x16x32_bf16 v[28:31], v[72:75], v[80:83], v[28:31]\ns_waitcnt lgkmcnt(3)\nv_mfma_f32_16x16x32_bf16 v[32:35], a[80:83], v[84:87], v[32:35]\n" \
"s_waitcnt vmcnt(4)\ns_barrier\nds_read_b128 v[80:83], v2 offset:16384\nv_mfma_f32_16x16x32_bf16 v[36:39], a[88:91], v[84:87], v[36:39]\nv_mfma_f32_16x16x32_bf16 v[40:43], a[96:99], v[84:87], v[40:43]\nv_mfma_f32_16x16x32_bf16 v[44:47], v[72:75], v[84:87], v[44:47]\ns_waitcnt lgkmcnt(2)\nv_mfma_f32_16x16x32_bf16 v[16:19], a[84:87], v[88:91], v[16:19]\n" \
"ds_read_b128 v[84:87], v2 offset:16640\nv_mfma_f32_16x16x32_bf16 v[20:23], a[92:95], v[88:91], v[20:23]\nv_mfma_f32_16x16x32_bf16 v[24:27], a[100:103], v[88:91], v[24:27]\nv_mfma_f32_16x16x32_bf16 v[28:31], v[76:79], v[88:91], v[28:31]\ns_waitcnt lgkmcnt(2)\nv_mfma_f32_16x16x32_bf16 v[32:35], a[84:87], v[92:95], v[32:35]\nds_read_b128 v[88:91], v2 offset:18432\nv_mfma_f32_16x16x32_bf16 v[36:39], a[92:95], v[92:95], v[36:39]\n" \
"v_mfma_f32_16x16x32_bf16 v[40:43], a[100:103], v[92:95], v[40:43]\nv_mfma_f32_16x16x32_bf16 v[44:47], v[76:79], v[92:95], v[44:47]\ns_waitcnt lgkmcnt(2)\nv_mfma_f32_16x16x32_bf16 v[16:19], a[104:107], v[80:83], v[16:19]\nds_read_b128 v[92:95], v2 offset:18688\nv_mfma_f32_16x16x32_bf16 v[20:23], a[112:115], v[80:83], v[20:23]\nds_read_b128 v[72:75], v1 offset:6144\nv_mfma_f32_16x16x32_bf16 v[24:27], a[120:123], v[80:83], v[24:27]\n" \
"v_mfma_f32_16x16x32_bf16 v[28:31], a[128:131], v[80:83], v[28:31]\ns_waitcnt lgkmcnt(3)\nv_mfma_f32_16x16x32_bf16 v[32:35], a[104:107], v[84:87], v[32:35]\nds_read_b128 v[80:83], v2 offset:20480\nv_mfma_f32_16x16x32_bf16 v[36:39], a[112:115], v[84:87], v[36:39]\nv_mfma_f32_16x16x32_bf16 v[40:43], a[120:123], v[84:87], v[40:43]\nv_mfma_f32_16x16x32_bf16 v[44:47], a[128:131], v[84:87], v[44:47]\ns_waitcnt lgkmcnt(3)\n" \
"v_mfma_f32_16x16x32_bf16 v[16:19], a[108:111], v[88:91], v[16:19]\nds_read_b128 v[84:87], v2 offset:20736\nv_mfma_f32_16x16x32_bf16 v[20:23], a[116:119], v[88:91], v[20:23]\nds_read_b128 v[76:79], v1 offset:7168\nv_mfma_f32_16x16x32_bf16 v[24:27], a[124:127], v[88:91], v[24:27]\nv_mfma_f32_16x16x32_bf16 v[28:31], a[132:135], v[88:91], v[28:31]\ns_waitcnt lgkmcnt(4)\nv_mfma_f32_16x16x32_bf16 v[32:35], a[108:111], v[92:95], v[32:35]\n" \
"ds_read_b128 v[88:91], v2 offset:22528\nv_mfma_f32_16x16x32_bf16 v[36:39], a[116:119], v[92:95], v[36:39]\nv_mfma_f32_16x16x32_bf16 v[40:43], a[124:127], v[92:95], v[40:43]\nv_mfma_f32_16x16x32_bf16 v[44:47], a[132:135], v[92:95], v[44:47]\ns_waitcnt lgkmcnt(3)\nv_mfma_f32_16x16x32_bf16 v[16:19], a[136:139], v[80:83], v[16:19]\nds_read_b128 v[92:95], v2 offset:22784\nv_mfma_f32_16x16x32_bf16 v[20:23], v[72:75], v[80:83], v[20:23]\n" \
"ds_read_b128 v[64:67], v1 offset:8192\nv_mfma_f32_16x16x32_bf16 v[24:27], a[144:147], v[80:83], v[24:27]\nv_mfma_f32_16x16x32_bf16 v[28:31], a[152:155], v[80:83], v[28:31]\ns_waitcnt lgkmcnt(4)\nv_mfma_f32_16x16x32_bf16 v[32:35], a[136:139], v[84:87], v[32:35]\nds_read_b128 v[80:83], v2 offset:24576\nv_mfma_f32_16x16x32_bf16 v[36:39], v[72:75], v[84:87], v[36:39]\nv_mfma_f32_16x16x32_bf16 v[40:43], a[144:147], v[84:87], v[40:43]\n" \
"v_mfma_f32_16x16x32_bf16 v[44:47], a[152:155], v[84:87], v[44:47]\ns_waitcnt lgkmcnt(3)\nv_mfma_f32_16x16x32_bf16 v[16:19], a[140:143], v[88:91], v[16:19]\nds_read_b128 v[84:87], v2 offset:24832\nv_mfma_f32_16x16x32_bf16 v[20:23], v[76:79], v[88:91], v[20:23]\nds_read_b128 v[68:71], v1 offset:9216\nv_mfma_f32_16x16x32_bf16 v[24:27], a[148:151], v[88:91], v[24:27]\nv_mfma_f32_16x16x32_bf16 v[28:31], a[156:159], v[88:91], v[28:31]\n" \
"s_waitcnt lgkmcnt(4)\nv_mfma_f32_16x16x32_bf16 v[32:35], a[140:143], v[92:95], v[32:35]\nds_read_b128 v[88:91], v2 offset:26624\nv_mfma_f32_16x16x32_bf16 v[36:39], v[76:79], v[92:95], v[36:39]\nv_mfma_f32_16x16x32_bf16 v[40:43], a[148:151], v[92:95], v[40:43]\nv_mfma_f32_16x16x32_bf16 v[44:47], a[156:159], v[92:95], v[44:47]\ns_waitcnt lgkmcnt(3)\nv_mfma_f32_16x16x32_bf16 v[16:19], a[160:163], v[80:83], v[16:19]\n" \
"ds_read_b128 v[92:95], v2 offset:26880\nv_mfma_f32_16x16x32_bf16 v[20:23], a[168:171], v[80:83], v[20:23]\nds_read_b128 v[72:75], v1 offset:10240\nv_mfma_f32_16x16x32_bf16 v[24:27], v[64:67], v[80:83], v[24:27]\nv_mfma_f32_16x16x32_bf16 v[28:31], a[176:179], v[80:83], v[28:31]\ns_waitcnt lgkmcnt(4)\nv_mfma_f32_16x16x32_bf16 v[32:35], a[160:163], v[84:87], v[32:35]\nds_read_b128 v[80:83], v2 offset:28672\n" \
"v_mfma_f32_16x16x32_bf16 v[36:39], a[168:171], v[84:87], v[36:39]\nv_mfma_f32_16x16x32_bf16 v[40:43], v[64:67], v[84:87], v[40:43]\nv_mfma_f32_16x16x32_bf16 v[44:47], a[176:179], v[84:87], v[44:47]\ns_waitcnt lgkmcnt(3)\nv_mfma_f32_16x16x32_bf16 v[16:19], a[164:167], v[88:91], v[16:19]\nds_read_b128 v[84:87], v2 offset:28928\nv_mfma_f32_16x16x32_bf16 v[20:23], a[172:175], v[88:91], v[20:23]\nds_read_b128 v[76:79], v1 offset:11264\n" \
"v_mfma_f32_16x16x32_bf16 v[24:27], v[68:71], v[88:91], v[24:27]\nv_mfma_f32_16x16x32_bf16 v[28:31], a[180:183], v[88:91], v[28:31]\ns_waitcnt lgkmcnt(4)\nv_mfma_f32_16x16x32_bf16 v[32:35], a[164:167], v[92:95], v[32:35]\nds_read_b128 v[88:91], v2 offset:30720\nv_mfma_f32_16x16x32_bf16 v[36:39], a[172:175], v[92:95], v[36:39]\nv_mfma_f32_16x16x32_bf16 v[40:43], v[68:71], v[92:95], v[40:43]\nv_mfma_f32_16x16x32_bf16 v[44:47], a[180:183], v[92:95], v[44:47]\n" \
"s_waitcnt lgkmcnt(3)\nv_mfma_f32_16x16x32_bf16 v[16:19], a[184:187], v[80:83], v[16:19]\nds_read_b128 v[92:95], v2 offset:30976\nv_mfma_f32_16x16x32_bf16 v[20:23], a[192:195], v[80:83], v[20:23]\nv_mfma_f32_16x16x32_bf16 v[24:27], a[200:203], v[80:83], v[24:27]\nv_mfma_f32_16x16x32_bf16 v[28:31], v[72:75], v[80:83], v[28:31]\ns_waitcnt lgkmcnt(3)\nv_mfma_f32_16x16x32_bf16 v[32:35], a[184:187], v[84:87], v[32:35]\n" \
"s_waitcnt vmcnt(0)\ns_barrier\nds_read_b128 v[48:51], v2 offset:32768\nv_mfma_f32_16x16x32_bf16 v[36:39], a[192:195], v[84:87], v[36:39]\nv_mfma_f32_16x16x32_bf16 v[40:43], a[200:203], v[84:87], v[40:43]\ns_add_u32 m0, s57, 0\ns_add_u32 s60, s41, 98304\nbuffer_load_dwordx4 v0, s[36:39], s60 offen offset:0 sc1 lds\n" \
"v_mfma_f32_16x16x32_bf16 v[44:47], v[72:75], v[84:87], v[44:47]\ns_waitcnt lgkmcnt(2)\nv_mfma_f32_16x16x32_bf16 v[16:19], a[188:191], v[88:91], v[16:19]\nds_read_b128 v[52:55], v2 offset:33024\nv_mfma_f32_16x16x32_bf16 v[20:23], a[196:199], v[88:91], v[20:23]\nv_mfma_f32_16x16x32_bf16 v[24:27], a[204:207], v[88:91], v[24:27]\nbuffer_load_dwordx4 v0, s[36:39], s60 offen offset:1024 sc1 lds\nv_mfma_f32_16x16x32_bf16 v[28:31], v[76:79], v[88:91], v[28:31]\n" \
"s_waitcnt lgkmcnt(2)\nv_mfma_f32_16x16x32_bf16 v[32:35], a[188:191], v[92:95], v[32:35]\nds_read_b128 v[56:59], v2 offset:34816\nv_mfma_f32_16x16x32_bf16 v[36:39], a[196:199], v[92:95], v[36:39]\nv_mfma_f32_16x16x32_bf16 v[40:43], a[204:207], v[92:95], v[40:43]\nbuffer_load_dwordx4 v0, s[36:39], s60 offen offset:2048 sc1 lds\nv_mfma_f32_16x16x32_bf16 v[44:47], v[76:79], v[92:95], v[44:47]\ns_waitcnt lgkmcnt(2)\n" \
"v_mfma_f32_16x16x32_bf16 v[16:19], a[208:211], v[48:51], v[16:19]\nds_read_b128 v[60:63], v2 offset:35072\nv_mfma_f32_16x16x32_bf16 v[20:23], a[216:219], v[48:51], v[20:23]\nds_read_b128 v[72:75], v1 offset:12288\nv_mfma_f32_16x16x32_bf16 v[24:27], a[224:227], v[48:51], v[24:27]\nbuffer_load_dwordx4 v0, s[36:39], s60 offen offset:3072 sc1 lds\nv_mfma_f32_16x16x32_bf16 v[28:31], a[232:235], v[48:51], v[28:31]\ns_waitcnt lgkmcnt(3)\n" \
"v_mfma_f32_16x16x32_bf16 v[32:35], a[208:211], v[52:55], v[32:35]\nds_read_b128 v[48:51], v2 offset:36864\nv_mfma_f32_16x16x32_bf16 v[36:39], a[216:219], v[52:55], v[36:39]\nv_mfma_f32_16x16x32_bf16 v[40:43], a[224:227], v[52:55], v[40:43]\nv_mfma_f32_16x16x32_bf16 v[44:47], a[232:235], v[52:55], v[44:47]\ns_waitcnt lgkmcnt(3)\nv_mfma_f32_16x16x32_bf16 v[16:19], a[212:215], v[56:59], v[16:19]\nds_read_b128 v[52:55], v2 offset:37120\n" \
"v_mfma_f32_16x16x32_bf16 v[20:23], a[220:223], v[56:59], v[20:23]\nds_read_b128 v[76:79], v1 offset:13312\nv_mfma_f32_16x16x32_bf16 v[24:27], a[228:231], v[56:59], v[24:27]\nv_mfma_f32_16x16x32_bf16 v[28:31], a[236:239], v[56:59], v[28:31]\ns_waitcnt lgkmcnt(4)\nv_mfma_f32_16x16x32_bf16 v[32:35], a[212:215], v[60:63], v[32:35]\nds_read_b128 v[56:59], v2 offset:38912\nv_mfma_f32_16x16x32_bf16 v[36:39], a[220:223], v[60:63], v[36:39]\n" \
"v_mfma_f32_16x16x32_bf16 v[40:43], a[228:231], v[60:63], v[40:43]\nv_mfma_f32_16x16x32_bf16 v[44:47], a[236:239], v[60:63], v[44:47]\ns_waitcnt lgkmcnt(3)\nv_mfma_f32_16x16x32_bf16 v[16:19], a[240:243], v[48:51], v[16:19]\nds_read_b128 v[60:63], v2 offset:39168\nv_mfma_f32_16x16x32_bf16 v[20:23], v[72:75], v[48:51], v[20:23]\nds_read_b128 v[64:67], v1 offset:14336\nv_mfma_f32_16x16x32_bf16 v[24:27], a[248:251], v[48:51], v[24:27]\n" \
"v_mfma_f32_16x16x32_bf16 v[28:31], v[96:99], v[48:51], v[28:31]\ns_waitcnt lgkmcnt(4)\nv_mfma_f32_16x16x32_bf16 v[32:35], a[240:243], v[52:55], v[32:35]\nds_read_b128 v[48:51], v2 offset:40960\nv_mfma_f32_16x16x32_bf16 v[36:39], v[72:75], v[52:55], v[36:39]\nv_mfma_f32_16x16x32_bf16 v[40:43], a[248:251], v[52:55], v[40:43]\nv_mfma_f32_16x16x32_bf16 v[44:47], v[96:99], v[52:55], v[44:47]\ns_waitcnt lgkmcnt(3)\n" \
"v_mfma_f32_16x16x32_bf16 v[16:19], a[244:247], v[56:59], v[16:19]\nds_read_b128 v[52:55], v2 offset:41216\nv_mfma_f32_16x16x32_bf16 v[20:23], v[76:79], v[56:59], v[20:23]\nds_read_b128 v[68:71], v1 offset:15360\nv_mfma_f32_16x16x32_bf16 v[24:27], a[252:255], v[56:59], v[24:27]\nv_mfma_f32_16x16x32_bf16 v[28:31], v[100:103], v[56:59], v[28:31]\ns_waitcnt lgkmcnt(4)\nv_mfma_f32_16x16x32_bf16 v[32:35], a[244:247], v[60:63], v[32:35]\n" \
"ds_read_b128 v[56:59], v2 offset:43008\nv_mfma_f32_16x16x32_bf16 v[36:39], v[76:79], v[60:63], v[36:39]\nv_mfma_f32_16x16x32_bf16 v[40:43], a[252:255], v[60:63], v[40:43]\nv_mfma_f32_16x16x32_bf16 v[44:47], v[100:103], v[60:63], v[44:47]\ns_waitcnt lgkmcnt(3)\nv_mfma_f32_16x16x32_bf16 v[16:19], v[104:107], v[48:51], v[16:19]\nds_read_b128 v[60:63], v2 offset:43264\nv_mfma_f32_16x16x32_bf16 v[20:23], v[112:115], v[48:51], v[20:23]\n" \
"ds_read_b128 v[72:75], v1 offset:16384\nv_mfma_f32_16x16x32_bf16 v[24:27], v[64:67], v[48:51], v[24:27]\nv_mfma_f32_16x16x32_bf16 v[28:31], v[120:123], v[48:51], v[28:31]\ns_waitcnt lgkmcnt(4)\nv_mfma_f32_16x16x32_bf16 v[32:35], v[104:107], v[52:55], v[32:35]\nds_read_b128 v[48:51], v2 offset:45056\nv_mfma_f32_16x16x32_bf16 v[36:39], v[112:115], v[52:55], v[36:39]\nv_mfma_f32_16x16x32_bf16 v[40:43], v[64:67], v[52:55], v[40:43]\n" \
"v_mfma_f32_16x16x32_bf16 v[44:47], v[120:123], v[52:55], v[44:47]\ns_waitcnt lgkmcnt(3)\nv_mfma_f32_16x16x32_bf16 v[16:19], v[108:111], v[56:59], v[16:19]\nds_read_b128 v[52:55], v2 offset:45312\nv_mfma_f32_16x16x32_bf16 v[20:23], v[116:119], v[56:59], v[20:23]\nds_read_b128 v[76:79], v1 offset:17408\nv_mfma_f32_16x16x32_bf16 v[24:27], v[68:71], v[56:59], v[24:27]\nv_mfma_f32_16x16x32_bf16 v[28:31], v[124:127], v[56:59], v[28:31]\n" \
"s_waitcnt lgkmcnt(4)\nv_mfma_f32_16x16x32_bf16 v[32:35], v[108:111], v[60:63], v[32:35]\nds_read_b128 v[56:59], v2 offset:47104\nv_mfma_f32_16x16x32_bf16 v[36:39], v[116:119], v[60:63], v[36:39]\nv_mfma_f32_16x16x32_bf16 v[40:43], v[68:71], v[60:63], v[40:43]\nv_mfma_f32_16x16x32_bf16 v[44:47], v[124:127], v[60:63], v[44:47]\ns_waitcnt lgkmcnt(3)\nv_mfma_f32_16x16x32_bf16 v[16:19], v[128:131], v[48:51], v[16:19]\n" \
"ds_read_b128 v[60:63], v2 offset:47360\nv_mfma_f32_16x16x32_bf16 v[20:23], v[136:139], v[48:51], v[20:23]\nv_mfma_f32_16x16x32_bf16 v[24:27], v[144:147], v[48:51], v[24:27]\nv_mfma_f32_16x16x32_bf16 v[28:31], v[72:75], v[48:51], v[28:31]\ns_waitcnt lgkmcnt(3)\nv_mfma_f32_16x16x32_bf16 v[32:35], v[128:131], v[52:55], v[32:35]\ns_waitcnt lgkmcnt(0)\ns_waitcnt vmcnt(0)\n" \
"s_barrier\nds_read_b128 v[48:51], v2 offset:0\nv_mfma_f32_16x16x32_bf16 v[36:39], v[136:139], v[52:55], v[36:39]\nv_mfma_f32_16x16x32_bf16 v[40:43], v[144:147], v[52:55], v[40:43]\nv_mfma_f32_16x16x32_bf16 v[44:47], v[72:75], v[52:55], v[44:47]\nv_mfma_f32_16x16x32_bf16 v[16:19], v[132:135], v[56:59], v[16:19]\nds_read_b128 v[52:55], v2 offset:256\nv_mfma_f32_16x16x32_bf16 v[20:23], v[140:143], v[56:59], v[20:23]\n" \
"v_mfma_f32_16x16x32_bf16 v[24:27], v[148:151], v[56:59], v[24:27]\nv_mfma_f32_16x16x32_bf16 v[28:31], v[76:79], v[56:59], v[28:31]\nv_mfma_f32_16x16x32_bf16 v[32:35], v[132:135], v[60:63], v[32:35]\nds_read_b128 v[56:59], v2 offset:2048\nv_mfma_f32_16x16x32_bf16 v[36:39], v[140:143], v[60:63], v[36:39]\nv_mfma_f32_16x16x32_bf16 v[40:43], v[148:151], v[60:63], v[40:43]\nv_mfma_f32_16x16x32_bf16 v[44:47], v[76:79], v[60:63], v[44:47]\ns_waitcnt lgkmcnt(2)\n" \
"v_mfma_f32_16x16x32_bf16 v[16:19], v[152:155], v[48:51], v[16:19]\nds_read_b128 v[60:63], v2 offset:2304\nv_mfma_f32_16x16x32_bf16 v[20:23], v[160:163], v[48:51], v[20:23]\nds_read_b128 v[72:75], v1 offset:18432\nv_mfma_f32_16x16x32_bf16 v[24:27], v[168:171], v[48:51], v[24:27]\nv_mfma_f32_16x16x32_bf16 v[28:31], v[176:179], v[48:51], v[28:31]\ns_waitcnt lgkmcnt(3)\nv_mfma_f32_16x16x32_bf16 v[32:35], v[152:155], v[52:55], v[32:35]\n" \
"ds_read_b128 v[48:51], v2 offset:4096\nv_mfma_f32_16x16x32_bf16 v[36:39], v[160:163], v[52:55], v[36:39]\nv_mfma_f32_16x16x32_bf16 v[40:43], v[168:171], v[52:55], v[40:43]\nv_mfma_f32_16x16x32_bf16 v[44:47], v[176:179], v[52:55], v[44:47]\ns_waitcnt lgkmcnt(3)\nv_mfma_f32_16x16x32_bf16 v[16:19], v[156:159], v[56:59], v[16:19]\nds_read_b128 v[52:55], v2 offset:4352\nv_mfma_f32_16x16x32_bf16 v[20:23], v[164:167], v[56:59], v[20:23]\n" \
"ds_read_b128 v[76:79], v1 offset:19456\nv_mfma_f32_16x16x32_bf16 v[24:27], v[172:175], v[56:59], v[24:27]\nv_mfma_f32_16x16x32_bf16 v[28:31], v[180:183], v[56:59], v[28:31]\ns_waitcnt lgkmcnt(4)\nv_mfma_f32_16x16x32_bf16 v[32:35], v[156:159], v[60:63], v[32:35]\nds_read_b128 v[56:59], v2 offset:6144\nv_mfma_f32_16x16x32_bf16 v[36:39], v[164:167], v[60:63], v[36:39]\nv_mfma_f32_16x16x32_bf16 v[40:43], v[172:175], v[60:63], v[40:43]\n" \
"v_mfma_f32_16x16x32_bf16 v[44:47], v[180:183], v[60:63], v[44:47]\ns_waitcnt lgkmcnt(3)\nv_mfma_f32_16x16x32_bf16 v[16:19], v[184:187], v[48:51], v[16:19]\nds_read_b128 v[60:63], v2 offset:6400\nv_mfma_f32_16x16x32_bf16 v[20:23], v[72:75], v[48:51], v[20:23]\nds_read_b128 v[64:67], v1 offset:20480\nv_mfma_f32_16x16x32_bf16 v[24:27], v[192:195], v[48:51], v[24:27]\nv_mfma_f32_16x16x32_bf16 v[28:31], v[200:203], v[48:51], v[28:31]\n" \
"s_waitcnt lgkmcnt(4)\nv_mfma_f32_16x16x32_bf16 v[32:35], v[184:187], v[52:55], v[32:35]\nds_read_b128 v[48:51], v2 offset:8192\nv_mfma_f32_16x16x32_bf16 v[36:39], v[72:75], v[52:55], v[36:39]\nv_mfma_f32_16x16x32_bf16 v[40:43], v[192:195], v[52:55], v[40:43]\nv_mfma_f32_16x16x32_bf16 v[44:47], v[200:203], v[52:55], v[44:47]\ns_waitcnt lgkmcnt(3)\nv_mfma_f32_16x16x32_bf16 v[16:19], v[188:191], v[56:59], v[16:19]\n" \
"ds_read_b128 v[52:55], v2 offset:10240\nv_mfma_f32_16x16x32_bf16 v[20:23], v[76:79], v[56:59], v[20:23]\nds_read_b128 v[68:71], v1 offset:21504\nv_mfma_f32_16x16x32_bf16 v[24:27], v[196:199], v[56:59], v[24:27]\nbuffer_load_dwordx4 v[80:83], v3, s[36:39], s42 offen offset:0 sc1\nv_mfma_f32_16x16x32_bf16 v[28:31], v[204:207], v[56:59], v[28:31]\ns_waitcnt lgkmcnt(4)\nv_mfma_f32_16x16x32_bf16 v[32:35], v[188:191], v[60:63], v[32:35]\n" \
"ds_read_b128 v[56:59], v2 offset:12288\nv_mfma_f32_16x16x32_bf16 v[36:39], v[76:79], v[60:63], v[36:39]\nds_read_b128 v[72:75], v1 offset:22528\nv_mfma_f32_16x16x32_bf16 v[40:43], v[196:199], v[60:63], v[40:43]\nbuffer_load_dwordx4 v[84:87], v3, s[36:39], s42 offen offset:1024 sc1\nv_mfma_f32_16x16x32_bf16 v[44:47], v[204:207], v[60:63], v[44:47]\ns_waitcnt lgkmcnt(4)\nv_mfma_f32_16x16x32_bf16 v[16:19], v[208:211], v[48:51], v[16:19]\n" \
"ds_read_b128 v[60:63], v2 offset:14336\nv_mfma_f32_16x16x32_bf16 v[20:23], v[216:219], v[48:51], v[20:23]\nds_read_b128 v[76:79], v1 offset:23552\nv_mfma_f32_16x16x32_bf16 v[24:27], v[64:67], v[48:51], v[24:27]\nbuffer_load_dwordx4 v[88:91], v3, s[36:39], s42 offen offset:2048 sc1\nv_mfma_f32_16x16x32_bf16 v[28:31], v[224:227], v[48:51], v[28:31]\ns_waitcnt lgkmcnt(5)\nv_mfma_f32_16x16x32_bf16 v[16:19], v[212:215], v[52:55], v[16:19]\n" \
"ds_read_b128 v[48:51], v2 offset:8448\nv_mfma_f32_16x16x32_bf16 v[20:23], v[220:223], v[52:55], v[20:23]\ns_waitcnt lgkmcnt(5)\nv_mfma_f32_16x16x32_bf16 v[24:27], v[68:71], v[52:55], v[24:27]\nbuffer_load_dwordx4 v[92:95], v3, s[36:39], s42 offen offset:3072 sc1\nv_mfma_f32_16x16x32_bf16 v[28:31], v[228:231], v[52:55], v[28:31]\ns_waitcnt lgkmcnt(4)\nv_mfma_f32_16x16x32_bf16 v[16:19], v[232:235], v[56:59], v[16:19]\n" \
"ds_read_b128 v[52:55], v2 offset:10496\nv_mfma_f32_16x16x32_bf16 v[20:23], v[240:243], v[56:59], v[20:23]\nv_mfma_f32_16x16x32_bf16 v[24:27], v[248:251], v[56:59], v[24:27]\ns_waitcnt lgkmcnt(4)\nv_mfma_f32_16x16x32_bf16 v[28:31], v[72:75], v[56:59], v[28:31]\ns_waitcnt lgkmcnt(3)\nv_mfma_f32_16x16x32_bf16 v[16:19], v[236:239], v[60:63], v[16:19]\nds_read_b128 v[56:59], v2 offset:12544\n" \
"v_mfma_f32_16x16x32_bf16 v[20:23], v[244:247], v[60:63], v[20:23]\nv_mfma_f32_16x16x32_bf16 v[24:27], v[252:255], v[60:63], v[24:27]\ns_waitcnt lgkmcnt(3)\nv_mfma_f32_16x16x32_bf16 v[28:31], v[76:79], v[60:63], v[28:31]\ns_waitcnt lgkmcnt(2)\nv_mfma_f32_16x16x32_bf16 v[32:35], v[208:211], v[48:51], v[32:35]\nds_read_b128 v[60:63], v2 offset:14592\nv_mfma_f32_16x16x32_bf16 v[36:39], v[216:219], v[48:51], v[36:39]\n" \
"v_cvt_pk_bf16_f32 v20, v16, v20\nv_cvt_pk_bf16_f32 v21, v17, v21\nv_mfma_f32_16x16x32_bf16 v[40:43], v[64:67], v[48:51], v[40:43]\nv_cvt_pk_bf16_f32 v22, v18, v22\nv_cvt_pk_bf16_f32 v23, v19, v23\nv_mfma_f32_16x16x32_bf16 v[44:47], v[224:227], v[48:51], v[44:47]\ns_waitcnt lgkmcnt(2)\nds_write_b128 v5, v[20:23] offset:0\n" \
"v_mfma_f32_16x16x32_bf16 v[32:35], v[212:215], v[52:55], v[32:35]\nv_cvt_pk_bf16_f32 v28, v24, v28\nv_cvt_pk_bf16_f32 v29, v25, v29\nv_mfma_f32_16x16x32_bf16 v[36:39], v[220:223], v[52:55], v[36:39]\nv_cvt_pk_bf16_f32 v30, v26, v30\nv_cvt_pk_bf16_f32 v31, v27, v31\nv_mfma_f32_16x16x32_bf16 v[40:43], v[68:71], v[52:55], v[40:43]\nv_mov_b64 v[48:49], 0\n" \
"v_mov_b64 v[50:51], 0\nv_mfma_f32_16x16x32_bf16 v[44:47], v[228:231], v[52:55], v[44:47]\ns_waitcnt lgkmcnt(2)\nds_write_b128 v5, v[28:31] offset:512\nv_mfma_f32_16x16x32_bf16 v[32:35], v[232:235], v[56:59], v[32:35]\nv_mov_b64 v[52:53], 0\nv_mov_b64 v[54:55], 0\nv_mfma_f32_16x16x32_bf16 v[36:39], v[240:243], v[56:59], v[36:39]\n" \
"s_waitcnt lgkmcnt(2)\nv_mfma_f32_16x16x32_bf16 v[32:35], v[236:239], v[60:63], v[32:35]\nv_mfma_f32_16x16x32_bf16 v[36:39], v[244:247], v[60:63], v[36:39]\nv_mfma_f32_16x16x32_bf16 v[40:43], v[248:251], v[56:59], v[40:43]\nv_mfma_f32_16x16x32_bf16 v[44:47], v[72:75], v[56:59], v[44:47]\nv_cvt_pk_bf16_f32 v36, v32, v36\nv_cvt_pk_bf16_f32 v37, v33, v37\nv_mfma_f32_16x16x32_bf16 v[40:43], v[252:255], v[60:63], v[40:43]\n" \
"v_cvt_pk_bf16_f32 v38, v34, v38\nv_cvt_pk_bf16_f32 v39, v35, v39\nv_mfma_f32_16x16x32_bf16 v[44:47], v[76:79], v[60:63], v[44:47]\ns_cmp_eq_u32 %[w], 1\nds_write_b128 v5, v[36:39] offset:256\nv_mov_b64 v[56:57], 0\nv_mov_b64 v[58:59], 0\nv_mov_b64 v[60:61], 0\n" \
"v_mov_b64 v[62:63], 0\ns_cbranch_scc1 .Lqk_c1%=\ns_cmp_eq_u32 %[w], 2\ns_cbranch_scc1 .Lqk_c2%=\ns_cmp_eq_u32 %[w], 3\ns_cbranch_scc1 .Lqk_c3%=\n.Lqk_c0%=:\nds_read_b128 v[64:67], v1 offset:0\n" \
"ds_read_b128 v[68:71], v1 offset:1024\nds_read_b128 v[72:75], v1 offset:2048\nds_read_b128 v[76:79], v1 offset:3072\nv_cvt_pk_bf16_f32 v44, v40, v44\nv_cvt_pk_bf16_f32 v45, v41, v45\nv_cvt_pk_bf16_f32 v46, v42, v46\nv_cvt_pk_bf16_f32 v47, v43, v47\nds_write_b128 v5, v[44:47] offset:768\n" \
"s_bfe_i32 s54, s40, 0x10000\n.Lqk_poll0%=:\ns_waitcnt vmcnt(0)\nv_smfmac_f32_16x16x64_bf16 v[48:51], v[80:83], a[0:7], v4\nds_write_b128 v0, v[80:83] offset:16384\nv_max3_u32 v40, v80, v82, v84\nv_smfmac_f32_16x16x64_bf16 v[52:55], v[80:83], a[8:15], v4\nds_write_b128 v0, v[84:87] offset:17408\n" \
"v_max3_u32 v40, v40, v86, v88\nv_smfmac_f32_16x16x64_bf16 v[56:59], v[80:83], a[16:23], v4\nds_write_b128 v0, v[88:91] offset:18432\nv_max3_u32 v40, v40, v90, v92\nv_max_u32 v40, v40, v94\nv_smfmac_f32_16x16x64_bf16 v[60:63], v[80:83], a[24:31], v4\nds_write_b128 v0, v[92:95] offset:19456\nv_cmp_eq_u32 vcc, -1, v40\n" \
"v_smfmac_f32_16x16x64_bf16 v[48:51], v[84:87], a[32:39], v4\ns_cbranch_vccnz .Lqk_re0%=\ns_waitcnt lgkmcnt(7)\nv_smfmac_f32_16x16x64_bf16 v[52:55], v[84:87], v[64:71], v4\ns_waitcnt lgkmcnt(0)\ns_barrier\nds_read_b128 v[80:83], v0 offset:20480\nds_read_b128 v[64:67], v1 offset:4096\n" \
"ds_read_b128 v[68:71], v1 offset:5120\nv_smfmac_f32_16x16x64_bf16 v[56:59], v[84:87], a[40:47], v4\nv_smfmac_f32_16x16x64_bf16 v[60:63], v[84:87], a[48:55], v4\nds_read_b128 v[84:87], v0 offset:21504\nv_smfmac_f32_16x16x64_bf16 v[48:51], v[88:91], a[56:63], v4\nv_smfmac_f32_16x16x64_bf16 v[52:55], v[88:91], a[64:71], v4\nv_smfmac_f32_16x16x64_bf16 v[56:59], v[88:91], v[72:79], v4\nds_read_b128 v[72:75], v1 offset:6144\n" \
"ds_read_b128 v[76:79], v1 offset:7168\nv_smfmac_f32_16x16x64_bf16 v[60:63], v[88:91], a[72:79], v4\nds_read_b128 v[88:91], v0 offset:22528\nv_smfmac_f32_16x16x64_bf16 v[48:51], v[92:95], a[80:87], v4\nds_read_b128 v[16:19], v7 offset:0\nv_smfmac_f32_16x16x64_bf16 v[52:55], v[92:95], a[88:95], v4\nv_smfmac_f32_16x16x64_bf16 v[56:59], v[92:95], a[96:103], v4\ns_waitcnt lgkmcnt(5)\n" \
"v_smfmac_f32_16x16x64_bf16 v[60:63], v[92:95], v[64:71], v4\nds_read_b128 v[92:95], v0 offset:23552\nds_read_b128 v[64:67], v1 offset:8192\nds_read_b128 v[68:71], v1 offset:9216\nv_smfmac_f32_16x16x64_bf16 v[48:51], v[80:83], a[104:111], v4\nds_read_b128 v[20:23], v7 offset:64\nv_smfmac_f32_16x16x64_bf16 v[52:55], v[80:83], a[112:119], v4\nv_smfmac_f32_16x16x64_bf16 v[56:59], v[80:83], a[120:127], v4\n" \
"v_smfmac_f32_16x16x64_bf16 v[60:63], v[80:83], a[128:135], v4\ns_waitcnt lgkmcnt(8)\nds_read_b128 v[80:83], v0 offset:24576\nv_smfmac_f32_16x16x64_bf16 v[48:51], v[84:87], a[136:143], v4\ns_waitcnt lgkmcnt(7)\nds_read_b128 v[24:27], v7 offset:2048\nv_smfmac_f32_16x16x64_bf16 v[52:55], v[84:87], v[72:79], v4\nds_read_b128 v[72:75], v1 offset:10240\n" \
"ds_read_b128 v[76:79], v1 offset:11264\nv_smfmac_f32_16x16x64_bf16 v[56:59], v[84:87], a[144:151], v4\nv_smfmac_f32_16x16x64_bf16 v[60:63], v[84:87], a[152:159], v4\ns_waitcnt lgkmcnt(9)\nds_read_b128 v[84:87], v0 offset:25600\nv_smfmac_f32_16x16x64_bf16 v[48:51], v[88:91], a[160:167], v4\nds_read_b128 v[28:31], v7 offset:2112\nv_smfmac_f32_16x16x64_bf16 v[52:55], v[88:91], a[168:175], v4\n" \
"s_waitcnt lgkmcnt(7)\nv_smfmac_f32_16x16x64_bf16 v[56:59], v[88:91], v[64:71], v4\nds_read_b128 v[64:67], v1 offset:12288\nds_read_b128 v[68:71], v1 offset:13312\nv_smfmac_f32_16x16x64_bf16 v[60:63], v[88:91], a[176:183], v4\nds_read_b128 v[88:91], v0 offset:26624\nv_smfmac_f32_16x16x64_bf16 v[48:51], v[92:95], a[184:191], v4\nds_read_b128 v[32:35], v7 offset:4096\n" \
"v_smfmac_f32_16x16x64_bf16 v[52:55], v[92:95], a[192:199], v4\nv_smfmac_f32_16x16x64_bf16 v[56:59], v[92:95], a[200:207], v4\ns_waitcnt lgkmcnt(6)\nv_smfmac_f32_16x16x64_bf16 v[60:63], v[92:95], v[72:79], v4\nds_read_b128 v[92:95], v0 offset:27648\nds_read_b128 v[72:75], v1 offset:14336\nds_read_b128 v[76:79], v1 offset:15360\nv_smfmac_f32_16x16x64_bf16 v[48:51], v[80:83], a[208:215], v4\n" \
"ds_read_b128 v[36:39], v7 offset:4160\nv_smfmac_f32_16x16x64_bf16 v[52:55], v[80:83], a[216:223], v4\nv_smfmac_f32_16x16x64_bf16 v[56:59], v[80:83], a[224:231], v4\nv_smfmac_f32_16x16x64_bf16 v[60:63], v[80:83], a[232:239], v4\ns_waitcnt lgkmcnt(9)\nds_read_b128 v[80:83], v0 offset:28672\nv_smfmac_f32_16x16x64_bf16 v[48:51], v[84:87], a[240:247], v4\ns_waitcnt lgkmcnt(7)\n" \
"ds_read_b128 v[40:43], v7 offset:6144\nv_smfmac_f32_16x16x64_bf16 v[52:55], v[84:87], v[64:71], v4\nds_read_b128 v[64:67], v1 offset:16384\nds_read_b128 v[68:71], v1 offset:17408\nv_smfmac_f32_16x16x64_bf16 v[56:59], v[84:87], a[248:255], v4\nv_smfmac_f32_16x16x64_bf16 v[60:63], v[84:87], v[96:103], v4\ns_waitcnt lgkmcnt(9)\nds_read_b128 v[84:87], v0 offset:29696\n" \
"v_smfmac_f32_16x16x64_bf16 v[48:51], v[88:91], v[104:111], v4\nds_read_b128 v[44:47], v7 offset:6208\nv_smfmac_f32_16x16x64_bf16 v[52:55], v[88:91], v[112:119], v4\ns_waitcnt lgkmcnt(7)\nv_smfmac_f32_16x16x64_bf16 v[56:59], v[88:91], v[72:79], v4\nds_read_b128 v[72:75], v1 offset:18432\nds_read_b128 v[76:79], v1 offset:19456\nv_smfmac_f32_16x16x64_bf16 v[60:63], v[88:91], v[120:127], v4\n" \
"ds_read_b128 v[88:91], v0 offset:30720\nv_smfmac_f32_16x16x64_bf16 v[48:51], v[92:95], v[128:135], v4\nv_smfmac_f32_16x16x64_bf16 v[52:55], v[92:95], v[136:143], v4\nv_smfmac_f32_16x16x64_bf16 v[56:59], v[92:95], v[144:151], v4\ns_waitcnt lgkmcnt(5)\nv_smfmac_f32_16x16x64_bf16 v[60:63], v[92:95], v[64:71], v4\nds_read_b128 v[92:95], v0 offset:31744\nds_read_b128 v[64:67], v1 offset:20480\n" \
"ds_read_b128 v[68:71], v1 offset:21504\nv_smfmac_f32_16x16x64_bf16 v[48:51], v[80:83], v[152:159], v4\nv_smfmac_f32_16x16x64_bf16 v[52:55], v[80:83], v[160:167], v4\nv_smfmac_f32_16x16x64_bf16 v[56:59], v[80:83], v[168:175], v4\nv_smfmac_f32_16x16x64_bf16 v[60:63], v[80:83], v[176:183], v4\ns_waitcnt lgkmcnt(7)\nv_smfmac_f32_16x16x64_bf16 v[48:51], v[84:87], v[184:191], v4\ns_waitcnt lgkmcnt(4)\n" \
"v_smfmac_f32_16x16x64_bf16 v[52:55], v[84:87], v[72:79], v4\nds_read_b128 v[72:75], v1 offset:22528\nds_read_b128 v[76:79], v1 offset:23552\nv_smfmac_f32_16x16x64_bf16 v[56:59], v[84:87], v[192:199], v4\nv_smfmac_f32_16x16x64_bf16 v[60:63], v[84:87], v[200:207], v4\ns_waitcnt lgkmcnt(5)\nv_smfmac_f32_16x16x64_bf16 v[48:51], v[88:91], v[208:215], v4\nv_smfmac_f32_16x16x64_bf16 v[52:55], v[88:91], v[216:223], v4\n" \
"s_waitcnt lgkmcnt(4)\nv_smfmac_f32_16x16x64_bf16 v[48:51], v[92:95], v[232:239], v4\nv_smfmac_f32_16x16x64_bf16 v[52:55], v[92:95], v[240:247], v4\ns_waitcnt lgkmcnt(2)\nv_smfmac_f32_16x16x64_bf16 v[56:59], v[88:91], v[64:71], v4\nv_add_f32 v48, v48, v50\nv_smfmac_f32_16x16x64_bf16 v[60:63], v[88:91], v[224:231], v4\nv_add_f32 v52, v52, v54\n" \
"v_add_f32 v49, v49, v51\nv_smfmac_f32_16x16x64_bf16 v[56:59], v[92:95], v[248:255], v4\ns_waitcnt lgkmcnt(0)\nv_add_f32 v53, v53, v55\nv_cvt_pk_bf16_f32 v50, v48, v52\nv_smfmac_f32_16x16x64_bf16 v[60:63], v[92:95], v[72:79], v4\nv_cvt_pk_bf16_f32 v51, v49, v53\nds_write2_b32 v6, v50, v51 offset0:0 offset1:4\n" \
"s_nop 1\nv_add_f32 v56, v56, v58\nv_add_f32 v57, v57, v59\ns_nop 1\nv_add_f32 v60, v60, v62\nv_add_f32 v61, v61, v63\nv_cvt_pk_bf16_f32 v62, v56, v60\nv_cvt_pk_bf16_f32 v63, v57, v61\n" \
"ds_write2_b32 v6, v62, v63 offset0:36 offset1:40\nv_mov_b32 v61, s54\ns_waitcnt lgkmcnt(0)\ns_barrier\nds_read_b128 v[80:83], v8 offset:0\nds_read_b128 v[84:87], v8 offset:576\nds_read_b128 v[88:91], v8 offset:1152\nds_read_b128 v[92:95], v8 offset:1728\n" \
"ds_read_b128 v[64:67], v9 offset:0\nds_read_b128 v[68:71], v9 offset:576\nds_read_b128 v[72:75], v9 offset:1152\nds_read_b128 v[76:79], v9 offset:1728\nds_read_b32 v60, v0 offset:53760\ns_waitcnt lgkmcnt(8)\nv_mfma_f32_16x16x32_bf16 v[52:55], v[16:19], v[80:83], 0\nv_mfma_f32_16x16x32_bf16 v[56:59], v[20:23], v[80:83], 0\n" \
"s_waitcnt lgkmcnt(7)\nv_mfma_f32_16x16x32_bf16 v[52:55], v[24:27], v[84:87], v[52:55]\nv_mfma_f32_16x16x32_bf16 v[56:59], v[28:31], v[84:87], v[56:59]\ns_waitcnt lgkmcnt(6)\nv_mfma_f32_16x16x32_bf16 v[52:55], v[32:35], v[88:91], v[52:55]\nv_mfma_f32_16x16x32_bf16 v[56:59], v[36:39], v[88:91], v[56:59]\ns_waitcnt lgkmcnt(5)\nv_mfma_f32_16x16x32_bf16 v[52:55], v[40:43], v[92:95], v[52:55]\n" \
"v_mfma_f32_16x16x32_bf16 v[56:59], v[44:47], v[92:95], v[56:59]\ns_waitcnt lgkmcnt(4)\nv_mfma_f32_16x16x32_bf16 v[48:51], v[64:67], v[80:83], 0\ns_waitcnt lgkmcnt(3)\nv_mfma_f32_16x16x32_bf16 v[48:51], v[68:71], v[84:87], v[48:51]\ns_waitcnt lgkmcnt(2)\nds_read_b128 v[80:83], v2 offset:0\nv_mfma_f32_16x16x32_bf16 v[48:51], v[72:75], v[88:91], v[48:51]\n" \
"s_waitcnt lgkmcnt(2)\nds_read_b128 v[84:87], v2 offset:256\nv_mfma_f32_16x16x32_bf16 v[48:51], v[76:79], v[92:95], v[48:51]\nds_read_b128 v[88:91], v2 offset:2048\nv_max3_f32 v16, v52, v53, v54\nv_max3_f32 v17, v55, v56, v57\nv_max3_f32 v16, v16, v58, v59\nv_cndmask_b32 v52, v52, v56, s[62:63]\n" \
"v_cndmask_b32 v53, v53, v57, s[62:63]\nv_cndmask_b32 v54, v54, v58, s[62:63]\nv_cndmask_b32 v55, v55, v59, s[62:63]\nv_max_f32 v16, v16, v17\nv_max3_f32 v17, v16, v48, v49\nv_max3_f32 v16, v16, v48, v49\ns_nop 1\nv_permlane16_swap_b32 v16, v17\n" \
"v_max_f32 v18, v16, v17\nv_max_f32 v16, v16, v17\ns_nop 1\nv_permlane32_swap_b32 v16, v18\nv_max_f32 v16, v16, v18\nv_mul_f32 v16, s59, v16\nv_fma_f32 v52, v52, s58, v16\nv_fma_f32 v53, v53, s58, v16\n" \
"v_exp_f32 v52, v52\nv_fma_f32 v54, v54, s58, v16\nv_exp_f32 v53, v53\nv_fma_f32 v55, v55, s58, v16\nv_exp_f32 v54, v54\nv_add_f32 v17, v52, v53\nv_fma_f32 v48, v48, s58, v16\nv_exp_f32 v55, v55\n" \
"v_add_f32 v17, v17, v54\nv_fma_f32 v49, v49, s58, v16\nv_exp_f32 v48, v48\nv_add_f32 v17, v17, v55\nv_exp_f32 v49, v49\nv_add_f32 v18, v48, v17\nv_add_f32 v18, v18, v49\ns_nop 1\n" \
"v_add_f32_dpp v16, v17, v18 row_ror:8 row_mask:0xf bank_mask:0xf\nv_add_f32_dpp v18, v17, v18 row_ror:8 row_mask:0xf bank_mask:0xf\ns_nop 1\nv_permlane16_swap_b32 v16, v18\nv_add_f32 v17, v16, v18\nv_add_f32 v18, v16, v18\ns_nop 1\nv_permlane32_swap_b32 v17, v18\n" \
"v_add_f32 v17, v17, v18\nv_rcp_f32 v17, v17\ns_nop 0\nv_mul_f32 v52, v52, v17\nv_mul_f32 v53, v53, v17\nv_mul_f32 v54, v54, v17\nv_mul_f32 v55, v55, v17\nv_mul_f32 v48, v48, v17\n" \
"v_mul_f32 v49, v49, v17\nv_cvt_pk_bf16_f32 v52, v52, v53\nv_cvt_pk_bf16_f32 v53, v54, v55\nv_cvt_pk_bf16_f32 v48, v48, v49\nv_bfi_b32 v52, s61, v52, v61\nv_bfi_b32 v53, s61, v53, v61\nbuffer_store_dwordx2 v[52:53], v10, s[36:39], 0 offen sc1\nv_bfi_b32 v48, s61, v48, v61\n" \
"buffer_store_dword v48, v11, s[36:39], 0 offen sc1\ns_waitcnt lgkmcnt(3)\nv_cmp_ne_u32 vcc, 0, v60\ns_cbranch_vccnz .Lqk_proj%=\ns_cmp_lt_i32 s40, 63\ns_cbranch_scc1 .Lqk_pwait%=\ns_branch .Lqk_end%=\n.Lqk_re0%=:\n" \
"v_max_u32 v40, v80, v82\nv_cmp_eq_u32 vcc, -1, v40\ns_cbranch_vccz .Lqk_sk1522%=\ns_mov_b64 exec, vcc\nbuffer_load_dwordx4 v[80:83], v3, s[36:39], s42 offen offset:0 sc1\ns_mov_b64 exec, -1\n.Lqk_sk1522%=:\nv_max_u32 v40, v84, v86\n" \
"v_cmp_eq_u32 vcc, -1, v40\ns_cbranch_vccz .Lqk_sk1529%=\ns_mov_b64 exec, vcc\nbuffer_load_dwordx4 v[84:87], v3, s[36:39], s42 offen offset:1024 sc1\ns_mov_b64 exec, -1\n.Lqk_sk1529%=:\nv_max_u32 v40, v88, v90\nv_cmp_eq_u32 vcc, -1, v40\n" \
"s_cbranch_vccz .Lqk_sk1536%=\ns_mov_b64 exec, vcc\nbuffer_load_dwordx4 v[88:91], v3, s[36:39], s42 offen offset:2048 sc1\ns_mov_b64 exec, -1\n.Lqk_sk1536%=:\nv_max_u32 v40, v92, v94\nv_cmp_eq_u32 vcc, -1, v40\ns_cbranch_vccz .Lqk_sk1543%=\n" \
"s_mov_b64 exec, vcc\nbuffer_load_dwordx4 v[92:95], v3, s[36:39], s42 offen offset:3072 sc1\ns_mov_b64 exec, -1\n.Lqk_sk1543%=:\nv_mov_b64 v[48:49], 0\nv_mov_b64 v[50:51], 0\nv_mov_b64 v[52:53], 0\nv_mov_b64 v[54:55], 0\n" \
"v_mov_b64 v[56:57], 0\nv_mov_b64 v[58:59], 0\nv_mov_b64 v[60:61], 0\nv_mov_b64 v[62:63], 0\ns_add_u32 s43, s43, 1\ns_cmp_lt_u32 s43, 262144\ns_cbranch_scc1 .Lqk_poll0%=\ns_branch .Lqk_abort%=\n" \
".Lqk_c1%=:\nds_read_b128 v[64:67], v1 offset:6144\nds_read_b128 v[68:71], v1 offset:7168\nds_read_b128 v[72:75], v1 offset:8192\nds_read_b128 v[76:79], v1 offset:9216\ns_nop 1\nv_cvt_pk_bf16_f32 v44, v40, v44\nv_cvt_pk_bf16_f32 v45, v41, v45\n" \
"v_cvt_pk_bf16_f32 v46, v42, v46\nv_cvt_pk_bf16_f32 v47, v43, v47\nds_write_b128 v5, v[44:47] offset:768\ns_bfe_i32 s54, s40, 0x10001\n.Lqk_poll1%=:\ns_waitcnt vmcnt(0)\nv_smfmac_f32_16x16x64_bf16 v[48:51], v[80:83], a[104:111], v4\nds_write_b128 v0, v[80:83] offset:20480\n" \
"v_max3_u32 v40, v80, v82, v84\nv_smfmac_f32_16x16x64_bf16 v[52:55], v[80:83], a[112:119], v4\nds_write_b128 v0, v[84:87] offset:21504\nv_max3_u32 v40, v40, v86, v88\nv_smfmac_f32_16x16x64_bf16 v[56:59], v[80:83], a[120:127], v4\nds_write_b128 v0, v[88:91] offset:22528\nv_max3_u32 v40, v40, v90, v92\nv_max_u32 v40, v40, v94\n" \
"v_smfmac_f32_16x16x64_bf16 v[60:63], v[80:83], a[128:135], v4\nds_write_b128 v0, v[92:95] offset:23552\nv_cmp_eq_u32 vcc, -1, v40\nv_smfmac_f32_16x16x64_bf16 v[48:51], v[84:87], a[136:143], v4\ns_cbranch_vccnz .Lqk_re1%=\ns_waitcnt lgkmcnt(7)\nv_smfmac_f32_16x16x64_bf16 v[52:55], v[84:87], v[64:71], v4\ns_waitcnt lgkmcnt(0)\n" \
"s_barrier\nds_read_b128 v[80:83], v0 offset:24576\nds_read_b128 v[64:67], v1 offset:10240\nds_read_b128 v[68:71], v1 offset:11264\nv_smfmac_f32_16x16x64_bf16 v[56:59], v[84:87], a[144:151], v4\nv_smfmac_f32_16x16x64_bf16 v[60:63], v[84:87], a[152:159], v4\nds_read_b128 v[84:87], v0 offset:25600\nv_smfmac_f32_16x16x64_bf16 v[48:51], v[88:91], a[160:167], v4\n" \
"v_smfmac_f32_16x16x64_bf16 v[52:55], v[88:91], a[168:175], v4\nv_smfmac_f32_16x16x64_bf16 v[56:59], v[88:91], v[72:79], v4\nds_read_b128 v[72:75], v1 offset:12288\nds_read_b128 v[76:79], v1 offset:13312\nv_smfmac_f32_16x16x64_bf16 v[60:63], v[88:91], a[176:183], v4\nds_read_b128 v[88:91], v0 offset:26624\nv_smfmac_f32_16x16x64_bf16 v[48:51], v[92:95], a[184:191], v4\nds_read_b128 v[16:19], v7 offset:0\n" \
"v_smfmac_f32_16x16x64_bf16 v[52:55], v[92:95], a[192:199], v4\nv_smfmac_f32_16x16x64_bf16 v[56:59], v[92:95], a[200:207], v4\ns_waitcnt lgkmcnt(5)\nv_smfmac_f32_16x16x64_bf16 v[60:63], v[92:95], v[64:71], v4\nds_read_b128 v[92:95], v0 offset:27648\nds_read_b128 v[64:67], v1 offset:14336\nds_read_b128 v[68:71], v1 offset:15360\nv_smfmac_f32_16x16x64_bf16 v[48:51], v[80:83], a[208:215], v4\n" \
"ds_read_b128 v[20:23], v7 offset:2048\nv_smfmac_f32_16x16x64_bf16 v[52:55], v[80:83], a[216:223], v4\nv_smfmac_f32_16x16x64_bf16 v[56:59], v[80:83], a[224:231], v4\nv_smfmac_f32_16x16x64_bf16 v[60:63], v[80:83], a[232:239], v4\ns_waitcnt lgkmcnt(8)\nds_read_b128 v[80:83], v0 offset:28672\nv_smfmac_f32_16x16x64_bf16 v[48:51], v[84:87], a[240:247], v4\ns_waitcnt lgkmcnt(7)\n" \
"ds_read_b128 v[24:27], v7 offset:4096\nv_smfmac_f32_16x16x64_bf16 v[52:55], v[84:87], v[72:79], v4\nds_read_b128 v[72:75], v1 offset:16384\nds_read_b128 v[76:79], v1 offset:17408\nv_smfmac_f32_16x16x64_bf16 v[56:59], v[84:87], a[248:255], v4\nv_smfmac_f32_16x16x64_bf16 v[60:63], v[84:87], v[96:103], v4\ns_waitcnt lgkmcnt(9)\nds_read_b128 v[84:87], v0 offset:29696\n" \
"v_smfmac_f32_16x16x64_bf16 v[48:51], v[88:91], v[104:111], v4\nds_read_b128 v[28:31], v7 offset:6144\nv_smfmac_f32_16x16x64_bf16 v[52:55], v[88:91], v[112:119], v4\ns_waitcnt lgkmcnt(7)\nv_smfmac_f32_16x16x64_bf16 v[56:59], v[88:91], v[64:71], v4\nds_read_b128 v[64:67], v1 offset:18432\nds_read_b128 v[68:71], v1 offset:19456\nv_smfmac_f32_16x16x64_bf16 v[60:63], v[88:91], v[120:127], v4\n" \
"ds_read_b128 v[88:91], v0 offset:30720\nv_smfmac_f32_16x16x64_bf16 v[48:51], v[92:95], v[128:135], v4\nds_read_b128 v[32:35], v7 offset:8192\nv_smfmac_f32_16x16x64_bf16 v[52:55], v[92:95], v[136:143], v4\nv_smfmac_f32_16x16x64_bf16 v[56:59], v[92:95], v[144:151], v4\ns_waitcnt lgkmcnt(6)\nv_smfmac_f32_16x16x64_bf16 v[60:63], v[92:95], v[72:79], v4\nds_read_b128 v[92:95], v0 offset:31744\n" \
"ds_read_b128 v[72:75], v1 offset:20480\nds_read_b128 v[76:79], v1 offset:21504\nv_smfmac_f32_16x16x64_bf16 v[48:51], v[80:83], v[152:159], v4\nds_read_b128 v[36:39], v7 offset:10240\nv_smfmac_f32_16x16x64_bf16 v[52:55], v[80:83], v[160:167], v4\nv_smfmac_f32_16x16x64_bf16 v[56:59], v[80:83], v[168:175], v4\nv_smfmac_f32_16x16x64_bf16 v[60:63], v[80:83], v[176:183], v4\ns_waitcnt lgkmcnt(9)\n" \
"ds_read_b128 v[80:83], v0 offset:16384\nv_smfmac_f32_16x16x64_bf16 v[48:51], v[84:87], v[184:191], v4\ns_waitcnt lgkmcnt(7)\nds_read_b128 v[40:43], v7 offset:12288\nv_smfmac_f32_16x16x64_bf16 v[52:55], v[84:87], v[64:71], v4\nds_read_b128 v[64:67], v1 offset:22528\nds_read_b128 v[68:71], v1 offset:23552\nv_smfmac_f32_16x16x64_bf16 v[56:59], v[84:87], v[192:199], v4\n" \
"v_smfmac_f32_16x16x64_bf16 v[60:63], v[84:87], v[200:207], v4\ns_waitcnt lgkmcnt(9)\nds_read_b128 v[84:87], v0 offset:17408\nv_smfmac_f32_16x16x64_bf16 v[48:51], v[88:91], v[208:215], v4\nds_read_b128 v[44:47], v7 offset:14336\nv_smfmac_f32_16x16x64_bf16 v[52:55], v[88:91], v[216:223], v4\ns_waitcnt lgkmcnt(7)\nv_smfmac_f32_16x16x64_bf16 v[56:59], v[88:91], v[72:79], v4\n" \
"ds_read_b128 v[72:75], v1 offset:0\nds_read_b128 v[76:79], v1 offset:1024\nv_smfmac_f32_16x16x64_bf16 v[60:63], v[88:91], v[224:231], v4\nds_read_b128 v[88:91], v0 offset:18432\nv_smfmac_f32_16x16x64_bf16 v[48:51], v[92:95], v[232:239], v4\nv_smfmac_f32_16x16x64_bf16 v[52:55], v[92:95], v[240:247], v4\nv_smfmac_f32_16x16x64_bf16 v[56:59], v[92:95], v[248:255], v4\ns_waitcnt lgkmcnt(5)\n" \
"v_smfmac_f32_16x16x64_bf16 v[60:63], v[92:95], v[64:71], v4\nds_read_b128 v[92:95], v0 offset:19456\nds_read_b128 v[64:67], v1 offset:2048\nds_read_b128 v[68:71], v1 offset:3072\nv_smfmac_f32_16x16x64_bf16 v[48:51], v[80:83], a[0:7], v4\nv_smfmac_f32_16x16x64_bf16 v[52:55], v[80:83], a[8:15], v4\nv_smfmac_f32_16x16x64_bf16 v[56:59], v[80:83], a[16:23], v4\nv_smfmac_f32_16x16x64_bf16 v[60:63], v[80:83], a[24:31], v4\n" \
"s_waitcnt lgkmcnt(7)\nv_smfmac_f32_16x16x64_bf16 v[48:51], v[84:87], a[32:39], v4\ns_waitcnt lgkmcnt(4)\nv_smfmac_f32_16x16x64_bf16 v[52:55], v[84:87], v[72:79], v4\nds_read_b128 v[72:75], v1 offset:4096\nds_read_b128 v[76:79], v1 offset:5120\nv_smfmac_f32_16x16x64_bf16 v[56:59], v[84:87], a[40:47], v4\nv_smfmac_f32_16x16x64_bf16 v[60:63], v[84:87], a[48:55], v4\n" \
"s_waitcnt lgkmcnt(5)\nv_smfmac_f32_16x16x64_bf16 v[48:51], v[88:91], a[56:63], v4\nv_smfmac_f32_16x16x64_bf16 v[52:55], v[88:91], a[64:71], v4\ns_waitcnt lgkmcnt(4)\nv_smfmac_f32_16x16x64_bf16 v[48:51], v[92:95], a[80:87], v4\nv_smfmac_f32_16x16x64_bf16 v[52:55], v[92:95], a[88:95], v4\ns_waitcnt lgkmcnt(2)\nv_smfmac_f32_16x16x64_bf16 v[56:59], v[88:91], v[64:71], v4\n" \
"v_add_f32 v48, v48, v50\nv_smfmac_f32_16x16x64_bf16 v[60:63], v[88:91], a[72:79], v4\nv_add_f32 v52, v52, v54\nv_add_f32 v49, v49, v51\nv_smfmac_f32_16x16x64_bf16 v[56:59], v[92:95], a[96:103], v4\ns_waitcnt lgkmcnt(0)\nv_add_f32 v53, v53, v55\nv_cvt_pk_bf16_f32 v50, v48, v52\n" \
"v_smfmac_f32_16x16x64_bf16 v[60:63], v[92:95], v[72:79], v4\nv_cvt_pk_bf16_f32 v51, v49, v53\nds_write2_b32 v6, v50, v51 offset0:0 offset1:4\ns_nop 1\nv_add_f32 v56, v56, v58\nv_add_f32 v57, v57, v59\ns_nop 1\nv_add_f32 v60, v60, v62\n" \
"v_add_f32 v61, v61, v63\nv_cvt_pk_bf16_f32 v62, v56, v60\nv_cvt_pk_bf16_f32 v63, v57, v61\nds_write2_b32 v6, v62, v63 offset0:36 offset1:40\nv_mov_b32 v61, s54\nv_mfma_f32_16x16x32_bf16 v[52:55], v[32:35], v[16:19], v[12:15]\nv_mfma_f32_16x16x32_bf16 v[52:55], v[36:39], v[20:23], v[52:55]\nv_mfma_f32_16x16x32_bf16 v[52:55], v[40:43], v[24:27], v[52:55]\n" \
"v_mfma_f32_16x16x32_bf16 v[52:55], v[44:47], v[28:31], v[52:55]\ns_waitcnt lgkmcnt(0)\ns_barrier\nds_read_b128 v[32:35], v9 offset:0\nds_read_b128 v[36:39], v9 offset:576\nds_read_b128 v[40:43], v9 offset:1152\nds_read_b128 v[44:47], v9 offset:1728\nds_read_b32 v60, v0 offset:53760\n" \
"ds_read_b128 v[80:83], v2 offset:0\nds_read_b128 v[84:87], v2 offset:256\nds_read_b128 v[88:91], v2 offset:2048\ns_waitcnt lgkmcnt(7)\nv_mfma_f32_16x16x32_bf16 v[48:51], v[32:35], v[16:19], 0\ns_waitcnt lgkmcnt(6)\nv_mfma_f32_16x16x32_bf16 v[48:51], v[36:39], v[20:23], v[48:51]\ns_waitcnt lgkmcnt(5)\n" \
"v_mfma_f32_16x16x32_bf16 v[48:51], v[40:43], v[24:27], v[48:51]\ns_waitcnt lgkmcnt(4)\nv_mfma_f32_16x16x32_bf16 v[48:51], v[44:47], v[28:31], v[48:51]\nv_max3_f32 v64, v52, v53, v54\nv_max_f32 v64, v64, v55\ns_nop 5\nv_max3_f32 v65, v64, v48, v49\nv_max3_f32 v64, v64, v48, v49\n" \
"s_nop 1\nv_permlane16_swap_b32 v64, v65\nv_max_f32 v66, v64, v65\nv_max_f32 v64, v64, v65\ns_nop 1\nv_permlane32_swap_b32 v64, v66\nv_max_f32 v64, v64, v66\nv_mul_f32 v64, s59, v64\n" \
"v_fma_f32 v52, v52, s58, v64\nv_fma_f32 v53, v53, s58, v64\nv_exp_f32 v52, v52\nv_fma_f32 v54, v54, s58, v64\nv_exp_f32 v53, v53\nv_fma_f32 v55, v55, s58, v64\nv_exp_f32 v54, v54\nv_add_f32 v65, v52, v53\n" \
"v_fma_f32 v48, v48, s58, v64\nv_exp_f32 v55, v55\nv_add_f32 v65, v65, v54\nv_fma_f32 v49, v49, s58, v64\nv_exp_f32 v48, v48\nv_add_f32 v65, v65, v55\nv_exp_f32 v49, v49\nv_add_f32 v65, v65, v48\n" \
"v_add_f32 v64, v65, v49\nv_add_f32 v66, v65, v49\ns_nop 1\nv_permlane16_swap_b32 v64, v66\nv_add_f32 v65, v64, v66\nv_add_f32 v66, v64, v66\ns_nop 1\nv_permlane32_swap_b32 v65, v66\n" \
"v_add_f32 v65, v65, v66\nv_rcp_f32 v65, v65\ns_nop 0\nv_mul_f32 v52, v52, v65\nv_mul_f32 v53, v53, v65\nv_mul_f32 v54, v54, v65\nv_mul_f32 v55, v55, v65\nv_mul_f32 v48, v48, v65\n" \
"v_mul_f32 v49, v49, v65\nv_cvt_pk_bf16_f32 v52, v52, v53\nv_cvt_pk_bf16_f32 v53, v54, v55\nv_cvt_pk_bf16_f32 v48, v48, v49\nv_bfi_b32 v52, s61, v52, v61\nv_bfi_b32 v53, s61, v53, v61\nv_bfi_b32 v48, s61, v48, v61\nbuffer_store_dwordx2 v[52:53], v10, s[36:39], s55 offen sc1\n" \
"buffer_store_dword v48, v11, s[36:39], s55 offen sc1\ns_waitcnt lgkmcnt(3)\nv_cmp_ne_u32 vcc, 0, v60\ns_cbranch_vccnz .Lqk_proj%=\ns_cmp_lt_i32 s40, 63\ns_cbranch_scc1 .Lqk_pwait%=\ns_branch .Lqk_end%=\n.Lqk_re1%=:\n" \
"v_max_u32 v40, v80, v82\nv_cmp_eq_u32 vcc, -1, v40\ns_cbranch_vccz .Lqk_sk1802%=\ns_mov_b64 exec, vcc\nbuffer_load_dwordx4 v[80:83], v3, s[36:39], s42 offen offset:0 sc1\ns_mov_b64 exec, -1\n.Lqk_sk1802%=:\nv_max_u32 v40, v84, v86\n" \
"v_cmp_eq_u32 vcc, -1, v40\ns_cbranch_vccz .Lqk_sk1809%=\ns_mov_b64 exec, vcc\nbuffer_load_dwordx4 v[84:87], v3, s[36:39], s42 offen offset:1024 sc1\ns_mov_b64 exec, -1\n.Lqk_sk1809%=:\nv_max_u32 v40, v88, v90\nv_cmp_eq_u32 vcc, -1, v40\n" \
"s_cbranch_vccz .Lqk_sk1816%=\ns_mov_b64 exec, vcc\nbuffer_load_dwordx4 v[88:91], v3, s[36:39], s42 offen offset:2048 sc1\ns_mov_b64 exec, -1\n.Lqk_sk1816%=:\nv_max_u32 v40, v92, v94\nv_cmp_eq_u32 vcc, -1, v40\ns_cbranch_vccz .Lqk_sk1823%=\n" \
"s_mov_b64 exec, vcc\nbuffer_load_dwordx4 v[92:95], v3, s[36:39], s42 offen offset:3072 sc1\ns_mov_b64 exec, -1\n.Lqk_sk1823%=:\nv_mov_b64 v[48:49], 0\nv_mov_b64 v[50:51], 0\nv_mov_b64 v[52:53], 0\nv_mov_b64 v[54:55], 0\n" \
"v_mov_b64 v[56:57], 0\nv_mov_b64 v[58:59], 0\nv_mov_b64 v[60:61], 0\nv_mov_b64 v[62:63], 0\ns_add_u32 s43, s43, 1\ns_cmp_lt_u32 s43, 262144\ns_cbranch_scc1 .Lqk_poll1%=\ns_branch .Lqk_abort%=\n" \
".Lqk_c2%=:\nds_read_b128 v[64:67], v1 offset:12288\nds_read_b128 v[68:71], v1 offset:13312\nds_read_b128 v[72:75], v1 offset:14336\nds_read_b128 v[76:79], v1 offset:15360\ns_nop 1\nv_cvt_pk_bf16_f32 v44, v40, v44\nv_cvt_pk_bf16_f32 v45, v41, v45\n" \
"v_cvt_pk_bf16_f32 v46, v42, v46\nv_cvt_pk_bf16_f32 v47, v43, v47\nds_write_b128 v5, v[44:47] offset:768\ns_bfe_i32 s54, s40, 0x10001\n.Lqk_poll2%=:\ns_waitcnt vmcnt(0)\nv_smfmac_f32_16x16x64_bf16 v[48:51], v[80:83], a[208:215], v4\nds_write_b128 v0, v[80:83] offset:24576\n" \
"v_max3_u32 v40, v80, v82, v84\nv_smfmac_f32_16x16x64_bf16 v[52:55], v[80:83], a[216:223], v4\nds_write_b128 v0, v[84:87] offset:25600\nv_max3_u32 v40, v40, v86, v88\nv_smfmac_f32_16x16x64_bf16 v[56:59], v[80:83], a[224:231], v4\nds_write_b128 v0, v[88:91] offset:26624\nv_max3_u32 v40, v40, v90, v92\nv_max_u32 v40, v40, v94\n" \
"v_smfmac_f32_16x16x64_bf16 v[60:63], v[80:83], a[232:239], v4\nds_write_b128 v0, v[92:95] offset:27648\nv_cmp_eq_u32 vcc, -1, v40\nv_smfmac_f32_16x16x64_bf16 v[48:51], v[84:87], a[240:247], v4\ns_cbranch_vccnz .Lqk_re2%=\ns_waitcnt lgkmcnt(7)\nv_smfmac_f32_16x16x64_bf16 v[52:55], v[84:87], v[64:71], v4\ns_waitcnt lgkmcnt(0)\n" \
"s_barrier\nds_read_b128 v[80:83], v0 offset:28672\nds_read_b128 v[64:67], v1 offset:16384\nds_read_b128 v[68:71], v1 offset:17408\nv_smfmac_f32_16x16x64_bf16 v[56:59], v[84:87], a[248:255], v4\nv_smfmac_f32_16x16x64_bf16 v[60:63], v[84:87], v[96:103], v4\nds_read_b128 v[84:87], v0 offset:29696\nv_smfmac_f32_16x16x64_bf16 v[48:51], v[88:91], v[104:111], v4\n" \
"v_smfmac_f32_16x16x64_bf16 v[52:55], v[88:91], v[112:119], v4\nv_smfmac_f32_16x16x64_bf16 v[56:59], v[88:91], v[72:79], v4\nds_read_b128 v[72:75], v1 offset:18432\nds_read_b128 v[76:79], v1 offset:19456\nv_smfmac_f32_16x16x64_bf16 v[60:63], v[88:91], v[120:127], v4\nds_read_b128 v[88:91], v0 offset:30720\nv_smfmac_f32_16x16x64_bf16 v[48:51], v[92:95], v[128:135], v4\nds_read_b128 v[16:19], v7 offset:256\n" \
"v_smfmac_f32_16x16x64_bf16 v[52:55], v[92:95], v[136:143], v4\nv_smfmac_f32_16x16x64_bf16 v[56:59], v[92:95], v[144:151], v4\ns_waitcnt lgkmcnt(5)\nv_smfmac_f32_16x16x64_bf16 v[60:63], v[92:95], v[64:71], v4\nds_read_b128 v[92:95], v0 offset:31744\nds_read_b128 v[64:67], v1 offset:20480\nds_read_b128 v[68:71], v1 offset:21504\nv_smfmac_f32_16x16x64_bf16 v[48:51], v[80:83], v[152:159], v4\n" \
"ds_read_b128 v[20:23], v7 offset:2304\nv_smfmac_f32_16x16x64_bf16 v[52:55], v[80:83], v[160:167], v4\nv_smfmac_f32_16x16x64_bf16 v[56:59], v[80:83], v[168:175], v4\nv_smfmac_f32_16x16x64_bf16 v[60:63], v[80:83], v[176:183], v4\ns_waitcnt lgkmcnt(8)\nds_read_b128 v[80:83], v0 offset:16384\nv_smfmac_f32_16x16x64_bf16 v[48:51], v[84:87], v[184:191], v4\ns_waitcnt lgkmcnt(7)\n" \
"ds_read_b128 v[24:27], v7 offset:4352\nv_smfmac_f32_16x16x64_bf16 v[52:55], v[84:87], v[72:79], v4\nds_read_b128 v[72:75], v1 offset:22528\nds_read_b128 v[76:79], v1 offset:23552\nv_smfmac_f32_16x16x64_bf16 v[56:59], v[84:87], v[192:199], v4\nv_smfmac_f32_16x16x64_bf16 v[60:63], v[84:87], v[200:207], v4\ns_waitcnt lgkmcnt(9)\nds_read_b128 v[84:87], v0 offset:17408\n" \
"v_smfmac_f32_16x16x64_bf16 v[48:51], v[88:91], v[208:215], v4\nds_read_b128 v[28:31], v7 offset:6400\nv_smfmac_f32_16x16x64_bf16 v[52:55], v[88:91], v[216:223], v4\ns_waitcnt lgkmcnt(7)\nv_smfmac_f32_16x16x64_bf16 v[56:59], v[88:91], v[64:71], v4\nds_read_b128 v[64:67], v1 offset:0\nds_read_b128 v[68:71], v1 offset:1024\nv_smfmac_f32_16x16x64_bf16 v[60:63], v[88:91], v[224:231], v4\n" \
"ds_read_b128 v[88:91], v0 offset:18432\nv_smfmac_f32_16x16x64_bf16 v[48:51], v[92:95], v[232:239], v4\nds_read_b128 v[32:35], v7 offset:8192\nv_smfmac_f32_16x16x64_bf16 v[52:55], v[92:95], v[240:247], v4\nv_smfmac_f32_16x16x64_bf16 v[56:59], v[92:95], v[248:255], v4\ns_waitcnt lgkmcnt(6)\nv_smfmac_f32_16x16x64_bf16 v[60:63], v[92:95], v[72:79], v4\nds_read_b128 v[92:95], v0 offset:19456\n" \
"ds_read_b128 v[72:75], v1 offset:2048\nds_read_b128 v[76:79], v1 offset:3072\nv_smfmac_f32_16x16x64_bf16 v[48:51], v[80:83], a[0:7], v4\nds_read_b128 v[36:39], v7 offset:10240\nv_smfmac_f32_16x16x64_bf16 v[52:55], v[80:83], a[8:15], v4\nv_smfmac_f32_16x16x64_bf16 v[56:59], v[80:83], a[16:23], v4\nv_smfmac_f32_16x16x64_bf16 v[60:63], v[80:83], a[24:31], v4\ns_waitcnt lgkmcnt(9)\n" \
"ds_read_b128 v[80:83], v0 offset:20480\nv_smfmac_f32_16x16x64_bf16 v[48:51], v[84:87], a[32:39], v4\ns_waitcnt lgkmcnt(7)\nds_read_b128 v[40:43], v7 offset:12288\nv_smfmac_f32_16x16x64_bf16 v[52:55], v[84:87], v[64:71], v4\nds_read_b128 v[64:67], v1 offset:4096\nds_read_b128 v[68:71], v1 offset:5120\nv_smfmac_f32_16x16x64_bf16 v[56:59], v[84:87], a[40:47], v4\n" \
"v_smfmac_f32_16x16x64_bf16 v[60:63], v[84:87], a[48:55], v4\ns_waitcnt lgkmcnt(9)\nds_read_b128 v[84:87], v0 offset:21504\nv_smfmac_f32_16x16x64_bf16 v[48:51], v[88:91], a[56:63], v4\nds_read_b128 v[44:47], v7 offset:14336\nv_smfmac_f32_16x16x64_bf16 v[52:55], v[88:91], a[64:71], v4\ns_waitcnt lgkmcnt(7)\nv_smfmac_f32_16x16x64_bf16 v[56:59], v[88:91], v[72:79], v4\n" \
"ds_read_b128 v[72:75], v1 offset:6144\nds_read_b128 v[76:79], v1 offset:7168\nv_smfmac_f32_16x16x64_bf16 v[60:63], v[88:91], a[72:79], v4\nds_read_b128 v[88:91], v0 offset:22528\nv_smfmac_f32_16x16x64_bf16 v[48:51], v[92:95], a[80:87], v4\nv_smfmac_f32_16x16x64_bf16 v[52:55], v[92:95], a[88:95], v4\nv_smfmac_f32_16x16x64_bf16 v[56:59], v[92:95], a[96:103], v4\ns_waitcnt lgkmcnt(5)\n" \
"v_smfmac_f32_16x16x64_bf16 v[60:63], v[92:95], v[64:71], v4\nds_read_b128 v[92:95], v0 offset:23552\nds_read_b128 v[64:67], v1 offset:8192\nds_read_b128 v[68:71], v1 offset:9216\nv_smfmac_f32_16x16x64_bf16 v[48:51], v[80:83], a[104:111], v4\nv_smfmac_f32_16x16x64_bf16 v[52:55], v[80:83], a[112:119], v4\nv_smfmac_f32_16x16x64_bf16 v[56:59], v[80:83], a[120:127], v4\nv_smfmac_f32_16x16x64_bf16 v[60:63], v[80:83], a[128:135], v4\n" \
"s_waitcnt lgkmcnt(7)\nv_smfmac_f32_16x16x64_bf16 v[48:51], v[84:87], a[136:143], v4\ns_waitcnt lgkmcnt(4)\nds_read_b128 v[80:83], v7 offset:8448\nv_smfmac_f32_16x16x64_bf16 v[52:55], v[84:87], v[72:79], v4\nds_read_b128 v[72:75], v1 offset:10240\nds_read_b128 v[76:79], v1 offset:11264\nv_smfmac_f32_16x16x64_bf16 v[56:59], v[84:87], a[144:151], v4\n" \
"v_smfmac_f32_16x16x64_bf16 v[60:63], v[84:87], a[152:159], v4\ns_waitcnt lgkmcnt(6)\nv_smfmac_f32_16x16x64_bf16 v[48:51], v[88:91], a[160:167], v4\nds_read_b128 v[84:87], v7 offset:10496\nv_smfmac_f32_16x16x64_bf16 v[52:55], v[88:91], a[168:175], v4\ns_waitcnt lgkmcnt(6)\nv_smfmac_f32_16x16x64_bf16 v[48:51], v[92:95], a[184:191], v4\nv_smfmac_f32_16x16x64_bf16 v[52:55], v[92:95], a[192:199], v4\n" \
"s_waitcnt lgkmcnt(4)\nv_smfmac_f32_16x16x64_bf16 v[56:59], v[88:91], v[64:71], v4\nds_read_b128 v[64:67], v7 offset:12544\nv_add_f32 v48, v48, v50\nv_smfmac_f32_16x16x64_bf16 v[60:63], v[88:91], a[176:183], v4\nds_read_b128 v[68:71], v7 offset:14592\nv_add_f32 v52, v52, v54\nv_add_f32 v49, v49, v51\n" \
"v_smfmac_f32_16x16x64_bf16 v[56:59], v[92:95], a[200:207], v4\ns_waitcnt lgkmcnt(3)\nv_add_f32 v53, v53, v55\nv_cvt_pk_bf16_f32 v50, v48, v52\nv_smfmac_f32_16x16x64_bf16 v[60:63], v[92:95], v[72:79], v4\nv_cvt_pk_bf16_f32 v51, v49, v53\nds_write2_b32 v6, v50, v51 offset0:0 offset1:4\ns_nop 1\n" \
"v_add_f32 v56, v56, v58\nv_add_f32 v57, v57, v59\ns_nop 1\nv_add_f32 v60, v60, v62\nv_add_f32 v61, v61, v63\nv_cvt_pk_bf16_f32 v62, v56, v60\nv_cvt_pk_bf16_f32 v63, v57, v61\nds_write2_b32 v6, v62, v63 offset0:36 offset1:40\n" \
"v_mov_b32 v61, s54\nv_mfma_f32_16x16x32_bf16 v[52:55], v[32:35], v[16:19], 0\nv_mfma_f32_16x16x32_bf16 v[52:55], v[36:39], v[20:23], v[52:55]\nv_mfma_f32_16x16x32_bf16 v[52:55], v[40:43], v[24:27], v[52:55]\nv_mfma_f32_16x16x32_bf16 v[52:55], v[44:47], v[28:31], v[52:55]\ns_waitcnt lgkmcnt(0)\ns_barrier\nds_read_b128 v[32:35], v9 offset:0\n" \
"ds_read_b128 v[36:39], v9 offset:576\nds_read_b128 v[40:43], v9 offset:1152\nds_read_b128 v[44:47], v9 offset:1728\nds_read_b32 v60, v0 offset:53760\nv_mfma_f32_16x16x32_bf16 v[56:59], v[80:83], v[16:19], v[12:15]\nv_mfma_f32_16x16x32_bf16 v[56:59], v[84:87], v[20:23], v[56:59]\nv_mfma_f32_16x16x32_bf16 v[56:59], v[64:67], v[24:27], v[56:59]\nv_mfma_f32_16x16x32_bf16 v[56:59], v[68:71], v[28:31], v[56:59]\n" \
"s_waitcnt lgkmcnt(4)\nds_read_b128 v[80:83], v2 offset:0\nds_read_b128 v[84:87], v2 offset:256\nds_read_b128 v[88:91], v2 offset:2048\nv_mfma_f32_16x16x32_bf16 v[48:51], v[32:35], v[16:19], 0\ns_waitcnt lgkmcnt(6)\nv_mfma_f32_16x16x32_bf16 v[48:51], v[36:39], v[20:23], v[48:51]\ns_waitcnt lgkmcnt(5)\n" \
"v_mfma_f32_16x16x32_bf16 v[48:51], v[40:43], v[24:27], v[48:51]\ns_waitcnt lgkmcnt(4)\nv_mfma_f32_16x16x32_bf16 v[48:51], v[44:47], v[28:31], v[48:51]\nv_max3_f32 v64, v52, v53, v54\nv_max3_f32 v64, v64, v56, v57\nv_max3_f32 v64, v64, v58, v59\nv_max_f32 v64, v64, v55\ns_nop 3\n" \
"v_max3_f32 v65, v64, v48, v49\nv_max3_f32 v64, v64, v48, v49\ns_nop 1\nv_permlane16_swap_b32 v64, v65\nv_max_f32 v66, v64, v65\nv_max_f32 v64, v64, v65\ns_nop 1\nv_permlane32_swap_b32 v64, v66\n" \
"v_max_f32 v64, v64, v66\nv_mul_f32 v64, s59, v64\nv_fma_f32 v52, v52, s58, v64\nv_fma_f32 v53, v53, s58, v64\nv_exp_f32 v52, v52\nv_fma_f32 v54, v54, s58, v64\nv_exp_f32 v53, v53\nv_fma_f32 v55, v55, s58, v64\n" \
"v_exp_f32 v54, v54\nv_add_f32 v65, v52, v53\nv_fma_f32 v56, v56, s58, v64\nv_exp_f32 v55, v55\nv_add_f32 v65, v65, v54\nv_fma_f32 v57, v57, s58, v64\nv_exp_f32 v56, v56\nv_add_f32 v65, v65, v55\n" \
"v_fma_f32 v58, v58, s58, v64\nv_exp_f32 v57, v57\nv_add_f32 v65, v65, v56\nv_fma_f32 v59, v59, s58, v64\nv_exp_f32 v58, v58\nv_add_f32 v65, v65, v57\nv_fma_f32 v48, v48, s58, v64\nv_exp_f32 v59, v59\n" \
"v_add_f32 v65, v65, v58\nv_fma_f32 v49, v49, s58, v64\nv_exp_f32 v48, v48\nv_add_f32 v65, v65, v59\nv_exp_f32 v49, v49\nv_add_f32 v65, v65, v48\nv_add_f32 v64, v65, v49\nv_add_f32 v66, v65, v49\n" \
"s_nop 1\nv_permlane16_swap_b32 v64, v66\nv_add_f32 v65, v64, v66\nv_add_f32 v66, v64, v66\ns_nop 1\nv_permlane32_swap_b32 v65, v66\nv_add_f32 v65, v65, v66\nv_rcp_f32 v65, v65\n" \
"s_nop 0\nv_mul_f32 v52, v52, v65\nv_mul_f32 v53, v53, v65\nv_mul_f32 v54, v54, v65\nv_mul_f32 v55, v55, v65\nv_mul_f32 v56, v56, v65\nv_mul_f32 v57, v57, v65\nv_mul_f32 v58, v58, v65\n" \
"v_mul_f32 v59, v59, v65\nv_mul_f32 v48, v48, v65\nv_mul_f32 v49, v49, v65\nv_cvt_pk_bf16_f32 v52, v52, v53\nv_cvt_pk_bf16_f32 v53, v54, v55\nv_cvt_pk_bf16_f32 v48, v48, v49\nv_cvt_pk_bf16_f32 v54, v56, v57\nv_cvt_pk_bf16_f32 v55, v58, v59\n" \
"v_bfi_b32 v52, s61, v52, v61\nv_bfi_b32 v53, s61, v53, v61\nv_bfi_b32 v54, s61, v54, v61\nv_bfi_b32 v55, s61, v55, v61\nv_bfi_b32 v48, s61, v48, v61\nbuffer_store_dwordx4 v[52:55], v10, s[36:39], s55 offen sc1\nbuffer_store_dword v48, v11, s[36:39], s55 offen sc1\ns_waitcnt lgkmcnt(3)\n" \
"v_cmp_ne_u32 vcc, 0, v60\ns_cbranch_vccnz .Lqk_proj%=\ns_cmp_lt_i32 s40, 63\ns_cbranch_scc1 .Lqk_pwait%=\ns_branch .Lqk_end%=\n.Lqk_re2%=:\nv_max_u32 v40, v80, v82\nv_cmp_eq_u32 vcc, -1, v40\n" \
"s_cbranch_vccz .Lqk_sk2112%=\ns_mov_b64 exec, vcc\nbuffer_load_dwordx4 v[80:83], v3, s[36:39], s42 offen offset:0 sc1\ns_mov_b64 exec, -1\n.Lqk_sk2112%=:\nv_max_u32 v40, v84, v86\nv_cmp_eq_u32 vcc, -1, v40\ns_cbranch_vccz .Lqk_sk2119%=\n" \
"s_mov_b64 exec, vcc\nbuffer_load_dwordx4 v[84:87], v3, s[36:39], s42 offen offset:1024 sc1\ns_mov_b64 exec, -1\n.Lqk_sk2119%=:\nv_max_u32 v40, v88, v90\nv_cmp_eq_u32 vcc, -1, v40\ns_cbranch_vccz .Lqk_sk2126%=\ns_mov_b64 exec, vcc\n" \
"buffer_load_dwordx4 v[88:91], v3, s[36:39], s42 offen offset:2048 sc1\ns_mov_b64 exec, -1\n.Lqk_sk2126%=:\nv_max_u32 v40, v92, v94\nv_cmp_eq_u32 vcc, -1, v40\ns_cbranch_vccz .Lqk_sk2133%=\ns_mov_b64 exec, vcc\nbuffer_load_dwordx4 v[92:95], v3, s[36:39], s42 offen offset:3072 sc1\n" \
"s_mov_b64 exec, -1\n.Lqk_sk2133%=:\nv_mov_b64 v[48:49], 0\nv_mov_b64 v[50:51], 0\nv_mov_b64 v[52:53], 0\nv_mov_b64 v[54:55], 0\nv_mov_b64 v[56:57], 0\nv_mov_b64 v[58:59], 0\n" \
"v_mov_b64 v[60:61], 0\nv_mov_b64 v[62:63], 0\ns_add_u32 s43, s43, 1\ns_cmp_lt_u32 s43, 262144\ns_cbranch_scc1 .Lqk_poll2%=\ns_branch .Lqk_abort%=\n.Lqk_c3%=:\nds_read_b128 v[64:67], v1 offset:18432\n" \
"ds_read_b128 v[68:71], v1 offset:19456\nds_read_b128 v[72:75], v1 offset:20480\nds_read_b128 v[76:79], v1 offset:21504\ns_nop 1\nv_cvt_pk_bf16_f32 v44, v40, v44\nv_cvt_pk_bf16_f32 v45, v41, v45\nv_cvt_pk_bf16_f32 v46, v42, v46\nv_cvt_pk_bf16_f32 v47, v43, v47\n" \
"ds_write_b128 v5, v[44:47] offset:768\ns_add_u32 s44, s40, 1\ns_lshl_b32 s44, s44, 17\ns_add_u32 s44, s44, %[xn]\ns_lshr_b32 s45, s44, 8\ns_and_b32 s45, s45, 0x2000\ns_xor_b32 s41, s44, s45\nbuffer_load_dword v16, v12, s[36:39], s56 offen sc1\n" \
".Lqk_poll3%=:\ns_waitcnt vmcnt(1)\nv_smfmac_f32_16x16x64_bf16 v[48:51], v[80:83], v[152:159], v4\nds_write_b128 v0, v[80:83] offset:28672\nv_max3_u32 v40, v80, v82, v84\nv_smfmac_f32_16x16x64_bf16 v[52:55], v[80:83], v[160:167], v4\nds_write_b128 v0, v[84:87] offset:29696\nv_max3_u32 v40, v40, v86, v88\n" \
"v_smfmac_f32_16x16x64_bf16 v[56:59], v[80:83], v[168:175], v4\nds_write_b128 v0, v[88:91] offset:30720\nv_max3_u32 v40, v40, v90, v92\nv_max_u32 v40, v40, v94\nv_smfmac_f32_16x16x64_bf16 v[60:63], v[80:83], v[176:183], v4\nds_write_b128 v0, v[92:95] offset:31744\nv_cmp_eq_u32 vcc, -1, v40\nv_smfmac_f32_16x16x64_bf16 v[48:51], v[84:87], v[184:191], v4\n" \
"s_cbranch_vccnz .Lqk_re3%=\ns_waitcnt lgkmcnt(7)\nv_smfmac_f32_16x16x64_bf16 v[52:55], v[84:87], v[64:71], v4\ns_waitcnt lgkmcnt(0)\ns_barrier\nds_read_b128 v[80:83], v0 offset:16384\nds_read_b128 v[64:67], v1 offset:22528\nds_read_b128 v[68:71], v1 offset:23552\n" \
"v_smfmac_f32_16x16x64_bf16 v[56:59], v[84:87], v[192:199], v4\nv_smfmac_f32_16x16x64_bf16 v[60:63], v[84:87], v[200:207], v4\nds_read_b128 v[84:87], v0 offset:17408\nv_smfmac_f32_16x16x64_bf16 v[48:51], v[88:91], v[208:215], v4\nv_smfmac_f32_16x16x64_bf16 v[52:55], v[88:91], v[216:223], v4\nv_smfmac_f32_16x16x64_bf16 v[56:59], v[88:91], v[72:79], v4\ns_waitcnt vmcnt(0)\nds_read_b128 v[72:75], v1 offset:0\n" \
"ds_read_b128 v[76:79], v1 offset:1024\nv_cmp_ge_i32 vcc, s40, v16\nv_smfmac_f32_16x16x64_bf16 v[60:63], v[88:91], v[224:231], v4\ns_cmp_eq_u64 vcc, 0\ns_cselect_b32 s61, s41, -2147483648\ns_cselect_b32 s44, 1, 0\nds_read_b128 v[88:91], v0 offset:18432\nv_smfmac_f32_16x16x64_bf16 v[48:51], v[92:95], v[232:239], v4\n" \
"s_add_u32 m0, %[l0], 0\ns_add_u32 s60, s61, 0\nbuffer_load_dwordx4 v0, s[36:39], s60 offen offset:0 sc1 lds\nv_smfmac_f32_16x16x64_bf16 v[52:55], v[92:95], v[240:247], v4\nv_mov_b32 v16, s44\nv_smfmac_f32_16x16x64_bf16 v[56:59], v[92:95], v[248:255], v4\ns_waitcnt lgkmcnt(4)\nbuffer_load_dwordx4 v0, s[36:39], s60 offen offset:1024 sc1 lds\n" \
"v_smfmac_f32_16x16x64_bf16 v[60:63], v[92:95], v[64:71], v4\nds_read_b128 v[92:95], v0 offset:19456\nds_read_b128 v[64:67], v1 offset:2048\nds_read_b128 v[68:71], v1 offset:3072\nds_write_b32 v0, v16 offset:53760\nv_smfmac_f32_16x16x64_bf16 v[48:51], v[80:83], a[0:7], v4\nbuffer_load_dwordx4 v0, s[36:39], s60 offen offset:2048 sc1 lds\nv_smfmac_f32_16x16x64_bf16 v[52:55], v[80:83], a[8:15], v4\n" \
"v_smfmac_f32_16x16x64_bf16 v[56:59], v[80:83], a[16:23], v4\nbuffer_load_dwordx4 v0, s[36:39], s60 offen offset:3072 sc1 lds\nv_smfmac_f32_16x16x64_bf16 v[60:63], v[80:83], a[24:31], v4\ns_waitcnt lgkmcnt(7)\nds_read_b128 v[80:83], v0 offset:20480\nv_smfmac_f32_16x16x64_bf16 v[48:51], v[84:87], a[32:39], v4\ns_add_u32 m0, %[l0], 4096\ns_add_u32 s60, s61, 4096\n" \
"s_waitcnt lgkmcnt(6)\nbuffer_load_dwordx4 v0, s[36:39], s60 offen offset:0 sc1 lds\nv_smfmac_f32_16x16x64_bf16 v[52:55], v[84:87], v[72:79], v4\nds_read_b128 v[72:75], v1 offset:4096\nds_read_b128 v[76:79], v1 offset:5120\nv_smfmac_f32_16x16x64_bf16 v[56:59], v[84:87], a[40:47], v4\nbuffer_load_dwordx4 v0, s[36:39], s60 offen offset:1024 sc1 lds\nv_smfmac_f32_16x16x64_bf16 v[60:63], v[84:87], a[48:55], v4\n" \
"s_waitcnt lgkmcnt(7)\nds_read_b128 v[84:87], v0 offset:21504\nv_smfmac_f32_16x16x64_bf16 v[48:51], v[88:91], a[56:63], v4\nbuffer_load_dwordx4 v0, s[36:39], s60 offen offset:2048 sc1 lds\nv_smfmac_f32_16x16x64_bf16 v[52:55], v[88:91], a[64:71], v4\ns_waitcnt lgkmcnt(5)\nv_smfmac_f32_16x16x64_bf16 v[56:59], v[88:91], v[64:71], v4\nds_read_b128 v[64:67], v1 offset:6144\n" \
"ds_read_b128 v[68:71], v1 offset:7168\nbuffer_load_dwordx4 v0, s[36:39], s60 offen offset:3072 sc1 lds\nv_smfmac_f32_16x16x64_bf16 v[60:63], v[88:91], a[72:79], v4\nds_read_b128 v[88:91], v0 offset:22528\nv_smfmac_f32_16x16x64_bf16 v[48:51], v[92:95], a[80:87], v4\ns_add_u32 m0, %[l0], 8192\ns_add_u32 s60, s61, 16384\nbuffer_load_dwordx4 v0, s[36:39], s60 offen offset:0 sc1 lds\n" \
"v_smfmac_f32_16x16x64_bf16 v[52:55], v[92:95], a[88:95], v4\nv_smfmac_f32_16x16x64_bf16 v[56:59], v[92:95], a[96:103], v4\ns_waitcnt lgkmcnt(4)\nbuffer_load_dwordx4 v0, s[36:39], s60 offen offset:1024 sc1 lds\nv_smfmac_f32_16x16x64_bf16 v[60:63], v[92:95], v[72:79], v4\nds_read_b128 v[92:95], v0 offset:23552\nds_read_b128 v[72:75], v1 offset:8192\nds_read_b128 v[76:79], v1 offset:9216\n" \
"v_smfmac_f32_16x16x64_bf16 v[48:51], v[80:83], a[104:111], v4\nbuffer_load_dwordx4 v0, s[36:39], s60 offen offset:2048 sc1 lds\nv_smfmac_f32_16x16x64_bf16 v[52:55], v[80:83], a[112:119], v4\nv_smfmac_f32_16x16x64_bf16 v[56:59], v[80:83], a[120:127], v4\nbuffer_load_dwordx4 v0, s[36:39], s60 offen offset:3072 sc1 lds\nv_smfmac_f32_16x16x64_bf16 v[60:63], v[80:83], a[128:135], v4\ns_waitcnt lgkmcnt(6)\nds_read_b128 v[80:83], v0 offset:24576\n" \
"v_smfmac_f32_16x16x64_bf16 v[48:51], v[84:87], a[136:143], v4\ns_add_u32 m0, %[l0], 12288\ns_add_u32 s60, s61, 20480\ns_waitcnt lgkmcnt(5)\nbuffer_load_dwordx4 v0, s[36:39], s60 offen offset:0 sc1 lds\nv_smfmac_f32_16x16x64_bf16 v[52:55], v[84:87], v[64:71], v4\nds_read_b128 v[64:67], v1 offset:10240\nds_read_b128 v[68:71], v1 offset:11264\n" \
"v_smfmac_f32_16x16x64_bf16 v[56:59], v[84:87], a[144:151], v4\nbuffer_load_dwordx4 v0, s[36:39], s60 offen offset:1024 sc1 lds\nv_smfmac_f32_16x16x64_bf16 v[60:63], v[84:87], a[152:159], v4\ns_waitcnt lgkmcnt(6)\nds_read_b128 v[84:87], v0 offset:25600\nv_smfmac_f32_16x16x64_bf16 v[48:51], v[88:91], a[160:167], v4\nbuffer_load_dwordx4 v0, s[36:39], s60 offen offset:2048 sc1 lds\nv_smfmac_f32_16x16x64_bf16 v[52:55], v[88:91], a[168:175], v4\n" \
"s_waitcnt lgkmcnt(4)\nv_smfmac_f32_16x16x64_bf16 v[56:59], v[88:91], v[72:79], v4\nds_read_b128 v[72:75], v1 offset:12288\nds_read_b128 v[76:79], v1 offset:13312\nbuffer_load_dwordx4 v0, s[36:39], s60 offen offset:3072 sc1 lds\nv_smfmac_f32_16x16x64_bf16 v[60:63], v[88:91], a[176:183], v4\nds_read_b128 v[88:91], v0 offset:26624\nv_smfmac_f32_16x16x64_bf16 v[48:51], v[92:95], a[184:191], v4\n" \
"v_smfmac_f32_16x16x64_bf16 v[52:55], v[92:95], a[192:199], v4\nv_smfmac_f32_16x16x64_bf16 v[56:59], v[92:95], a[200:207], v4\ns_waitcnt lgkmcnt(4)\nv_smfmac_f32_16x16x64_bf16 v[60:63], v[92:95], v[64:71], v4\nds_read_b128 v[92:95], v0 offset:27648\nds_read_b128 v[64:67], v1 offset:14336\nds_read_b128 v[68:71], v1 offset:15360\nv_smfmac_f32_16x16x64_bf16 v[48:51], v[80:83], a[208:215], v4\n" \
"v_smfmac_f32_16x16x64_bf16 v[52:55], v[80:83], a[216:223], v4\nv_smfmac_f32_16x16x64_bf16 v[56:59], v[80:83], a[224:231], v4\nv_smfmac_f32_16x16x64_bf16 v[60:63], v[80:83], a[232:239], v4\ns_waitcnt lgkmcnt(6)\nv_smfmac_f32_16x16x64_bf16 v[48:51], v[84:87], a[240:247], v4\ns_waitcnt lgkmcnt(4)\nv_smfmac_f32_16x16x64_bf16 v[52:55], v[84:87], v[72:79], v4\nds_read_b128 v[72:75], v1 offset:16384\n" \
"ds_read_b128 v[76:79], v1 offset:17408\nv_smfmac_f32_16x16x64_bf16 v[56:59], v[84:87], a[248:255], v4\nv_smfmac_f32_16x16x64_bf16 v[60:63], v[84:87], v[96:103], v4\ns_waitcnt lgkmcnt(5)\nv_smfmac_f32_16x16x64_bf16 v[48:51], v[88:91], v[104:111], v4\nv_smfmac_f32_16x16x64_bf16 v[52:55], v[88:91], v[112:119], v4\ns_waitcnt lgkmcnt(4)\nv_smfmac_f32_16x16x64_bf16 v[48:51], v[92:95], v[128:135], v4\n" \
"v_smfmac_f32_16x16x64_bf16 v[52:55], v[92:95], v[136:143], v4\ns_waitcnt lgkmcnt(2)\nv_smfmac_f32_16x16x64_bf16 v[56:59], v[88:91], v[64:71], v4\nv_add_f32 v48, v48, v50\nv_smfmac_f32_16x16x64_bf16 v[60:63], v[88:91], v[120:127], v4\nv_add_f32 v52, v52, v54\nv_add_f32 v49, v49, v51\nv_smfmac_f32_16x16x64_bf16 v[56:59], v[92:95], v[144:151], v4\n" \
"s_waitcnt lgkmcnt(0)\nv_add_f32 v53, v53, v55\nv_cvt_pk_bf16_f32 v50, v48, v52\nv_smfmac_f32_16x16x64_bf16 v[60:63], v[92:95], v[72:79], v4\nv_cvt_pk_bf16_f32 v51, v49, v53\nds_write2_b32 v6, v50, v51 offset0:0 offset1:4\ns_nop 1\nv_add_f32 v56, v56, v58\n" \
"v_add_f32 v57, v57, v59\ns_nop 1\nv_add_f32 v60, v60, v62\nv_add_f32 v61, v61, v63\nv_cvt_pk_bf16_f32 v62, v56, v60\nv_cvt_pk_bf16_f32 v63, v57, v61\nds_write2_b32 v6, v62, v63 offset0:36 offset1:40\ns_waitcnt vmcnt(0)\n" \
"s_waitcnt lgkmcnt(0)\ns_barrier\ns_cbranch_vccnz .Lqk_slow%=\ns_add_u32 m0, %[l0], 16384\ns_add_u32 s60, s41, 32768\nbuffer_load_dwordx4 v0, s[36:39], s60 offen offset:0 sc1 lds\nbuffer_load_dwordx4 v0, s[36:39], s60 offen offset:1024 sc1 lds\nbuffer_load_dwordx4 v0, s[36:39], s60 offen offset:2048 sc1 lds\n" \
"buffer_load_dwordx4 v0, s[36:39], s60 offen offset:3072 sc1 lds\ns_add_u32 m0, %[l0], 20480\ns_add_u32 s60, s41, 36864\nbuffer_load_dwordx4 v0, s[36:39], s60 offen offset:0 sc1 lds\nbuffer_load_dwordx4 v0, s[36:39], s60 offen offset:1024 sc1 lds\nbuffer_load_dwordx4 v0, s[36:39], s60 offen offset:2048 sc1 lds\nbuffer_load_dwordx4 v0, s[36:39], s60 offen offset:3072 sc1 lds\ns_add_u32 m0, %[l0], 24576\n" \
"s_add_u32 s60, s41, 49152\nbuffer_load_dwordx4 v0, s[36:39], s60 offen offset:0 sc1 lds\nbuffer_load_dwordx4 v0, s[36:39], s60 offen offset:1024 sc1 lds\nbuffer_load_dwordx4 v0, s[36:39], s60 offen offset:2048 sc1 lds\nbuffer_load_dwordx4 v0, s[36:39], s60 offen offset:3072 sc1 lds\ns_add_u32 m0, %[l0], 28672\ns_add_u32 s60, s41, 53248\nbuffer_load_dwordx4 v0, s[36:39], s60 offen offset:0 sc1 lds\n" \
"buffer_load_dwordx4 v0, s[36:39], s60 offen offset:1024 sc1 lds\nbuffer_load_dwordx4 v0, s[36:39], s60 offen offset:2048 sc1 lds\nbuffer_load_dwordx4 v0, s[36:39], s60 offen offset:3072 sc1 lds\nds_read_b128 v[80:83], v2 offset:0\nds_read_b128 v[84:87], v2 offset:256\nds_read_b128 v[88:91], v2 offset:2048\ns_branch .Lqk_proj%=\n.Lqk_slow%=:\n" \
"s_cmp_ge_i32 s40, 63\ns_cbranch_scc1 .Lqk_end%=\n.Lqk_fetch0%=:\nbuffer_load_dword v16, v12, s[36:39], s56 offen sc1\ns_waitcnt vmcnt(0)\nv_cmp_ge_i32 vcc, s40, v16\ns_cbranch_vccz .Lqk_fok%=\ns_add_u32 s43, s43, 1\n" \
"s_cmp_lt_u32 s43, 262144\ns_cbranch_scc1 .Lqk_fetch0%=\ns_branch .Lqk_abort%=\n.Lqk_fok%=:\ns_add_u32 s44, s40, 1\ns_lshl_b32 s44, s44, 17\ns_add_u32 s44, s44, %[xn]\ns_lshr_b32 s45, s44, 8\n" \
"s_and_b32 s45, s45, 0x2000\ns_xor_b32 s41, s44, s45\ns_add_u32 m0, %[l0], 0\ns_add_u32 s60, s41, 0\nbuffer_load_dwordx4 v0, s[36:39], s60 offen offset:0 sc1 lds\nbuffer_load_dwordx4 v0, s[36:39], s60 offen offset:1024 sc1 lds\nbuffer_load_dwordx4 v0, s[36:39], s60 offen offset:2048 sc1 lds\nbuffer_load_dwordx4 v0, s[36:39], s60 offen offset:3072 sc1 lds\n" \
"s_add_u32 m0, %[l0], 4096\ns_add_u32 s60, s41, 4096\nbuffer_load_dwordx4 v0, s[36:39], s60 offen offset:0 sc1 lds\nbuffer_load_dwordx4 v0, s[36:39], s60 offen offset:1024 sc1 lds\nbuffer_load_dwordx4 v0, s[36:39], s60 offen offset:2048 sc1 lds\nbuffer_load_dwordx4 v0, s[36:39], s60 offen offset:3072 sc1 lds\ns_add_u32 m0, %[l0], 8192\ns_add_u32 s60, s41, 16384\n" \
"buffer_load_dwordx4 v0, s[36:39], s60 offen offset:0 sc1 lds\nbuffer_load_dwordx4 v0, s[36:39], s60 offen offset:1024 sc1 lds\nbuffer_load_dwordx4 v0, s[36:39], s60 offen offset:2048 sc1 lds\nbuffer_load_dwordx4 v0, s[36:39], s60 offen offset:3072 sc1 lds\ns_add_u32 m0, %[l0], 12288\ns_add_u32 s60, s41, 20480\nbuffer_load_dwordx4 v0, s[36:39], s60 offen offset:0 sc1 lds\nbuffer_load_dwordx4 v0, s[36:39], s60 offen offset:1024 sc1 lds\n" \
"buffer_load_dwordx4 v0, s[36:39], s60 offen offset:2048 sc1 lds\nbuffer_load_dwordx4 v0, s[36:39], s60 offen offset:3072 sc1 lds\ns_add_u32 m0, %[l0], 16384\ns_add_u32 s60, s41, 32768\nbuffer_load_dwordx4 v0, s[36:39], s60 offen offset:0 sc1 lds\nbuffer_load_dwordx4 v0, s[36:39], s60 offen offset:1024 sc1 lds\nbuffer_load_dwordx4 v0, s[36:39], s60 offen offset:2048 sc1 lds\nbuffer_load_dwordx4 v0, s[36:39], s60 offen offset:3072 sc1 lds\n" \
"s_add_u32 m0, %[l0], 20480\ns_add_u32 s60, s41, 36864\nbuffer_load_dwordx4 v0, s[36:39], s60 offen offset:0 sc1 lds\nbuffer_load_dwordx4 v0, s[36:39], s60 offen offset:1024 sc1 lds\nbuffer_load_dwordx4 v0, s[36:39], s60 offen offset:2048 sc1 lds\nbuffer_load_dwordx4 v0, s[36:39], s60 offen offset:3072 sc1 lds\ns_add_u32 m0, %[l0], 24576\ns_add_u32 s60, s41, 49152\n" \
"buffer_load_dwordx4 v0, s[36:39], s60 offen offset:0 sc1 lds\nbuffer_load_dwordx4 v0, s[36:39], s60 offen offset:1024 sc1 lds\nbuffer_load_dwordx4 v0, s[36:39], s60 offen offset:2048 sc1 lds\nbuffer_load_dwordx4 v0, s[36:39], s60 offen offset:3072 sc1 lds\ns_add_u32 m0, %[l0], 28672\ns_add_u32 s60, s41, 53248\nbuffer_load_dwordx4 v0, s[36:39], s60 offen offset:0 sc1 lds\nbuffer_load_dwordx4 v0, s[36:39], s60 offen offset:1024 sc1 lds\n" \
"buffer_load_dwordx4 v0, s[36:39], s60 offen offset:2048 sc1 lds\nbuffer_load_dwordx4 v0, s[36:39], s60 offen offset:3072 sc1 lds\ns_waitcnt vmcnt(16)\ns_branch .Lqk_pwait%=\n.Lqk_re3%=:\nv_max_u32 v40, v80, v82\nv_cmp_eq_u32 vcc, -1, v40\ns_cbranch_vccz .Lqk_sk2445%=\n" \
"s_mov_b64 exec, vcc\nbuffer_load_dwordx4 v[80:83], v3, s[36:39], s42 offen offset:0 sc1\ns_mov_b64 exec, -1\n.Lqk_sk2445%=:\nv_max_u32 v40, v84, v86\nv_cmp_eq_u32 vcc, -1, v40\ns_cbranch_vccz .Lqk_sk2452%=\ns_mov_b64 exec, vcc\n" \
"buffer_load_dwordx4 v[84:87], v3, s[36:39], s42 offen offset:1024 sc1\ns_mov_b64 exec, -1\n.Lqk_sk2452%=:\nv_max_u32 v40, v88, v90\nv_cmp_eq_u32 vcc, -1, v40\ns_cbranch_vccz .Lqk_sk2459%=\ns_mov_b64 exec, vcc\nbuffer_load_dwordx4 v[88:91], v3, s[36:39], s42 offen offset:2048 sc1\n" \
"s_mov_b64 exec, -1\n.Lqk_sk2459%=:\nv_max_u32 v40, v92, v94\nv_cmp_eq_u32 vcc, -1, v40\ns_cbranch_vccz .Lqk_sk2466%=\ns_mov_b64 exec, vcc\nbuffer_load_dwordx4 v[92:95], v3, s[36:39], s42 offen offset:3072 sc1\ns_mov_b64 exec, -1\n" \
".Lqk_sk2466%=:\nbuffer_load_dword v16, v12, s[36:39], s56 offen sc1\nv_mov_b64 v[48:49], 0\nv_mov_b64 v[50:51], 0\nv_mov_b64 v[52:53], 0\nv_mov_b64 v[54:55], 0\nv_mov_b64 v[56:57], 0\nv_mov_b64 v[58:59], 0\n" \
"v_mov_b64 v[60:61], 0\nv_mov_b64 v[62:63], 0\ns_add_u32 s43, s43, 1\ns_cmp_lt_u32 s43, 262144\ns_cbranch_scc1 .Lqk_poll3%=\ns_branch .Lqk_abort%=\n.Lqk_abort%=:\ns_mov_b64 exec, -1\n" \
"v_mov_b32 v16, 0\ns_movk_i32 s44, 6400\nbuffer_store_dword v16, off, s[36:39], s44 sc1\n.Lqk_end%=:\ns_waitcnt vmcnt(0) lgkmcnt(0)\n" \
""
#define VU_ASM \
"v_mbcnt_lo_u32_b32 v255, -1, 0\nv_mbcnt_hi_u32_b32 v255, -1, v255\nv_lshlrev_b32 v255, 2, v255\nv_add_u32 v255, %[tb], v255\nds_read_b32 v0, v255 offset:0\nds_read_b32 v1, v255 offset:256\nds_read_b32 v2, v255 offset:512\nds_read_b32 v3, v255 offset:768\n" \
"ds_read_b32 v4, v255 offset:1024\nds_read_b32 v5, v255 offset:1280\nds_read_b32 v6, v255 offset:1536\nds_read_b32 v7, v255 offset:1792\nds_read_b32 v8, v255 offset:2048\nds_read_b32 v9, v255 offset:2304\nds_read_b32 v10, v255 offset:2560\nds_read_b32 v11, v255 offset:2816\n" \
"ds_read_b32 v90, v255 offset:3072\nds_read_b32 v91, v255 offset:3328\nds_read_b32 v92, v255 offset:3584\nds_read_b32 v93, v255 offset:3840\nds_read_b32 v94, v255 offset:4096\nds_read_b32 v95, v255 offset:4352\ns_waitcnt lgkmcnt(0)\ns_mov_b64 s[36:37], %[ws]\n" \
"s_mov_b32 s38, %[sz]\ns_mov_b32 s39, 0x20000\ns_mov_b32 s45, 2096896\nbuffer_load_dword v15, off, s[36:39], s45 sc1\ns_add_u32 m0, %[lw], 16384\ns_add_u32 s44, %[wo], 0\nbuffer_load_dwordx4 v90, %[rw], s44 offen nt lds\ns_add_u32 m0, %[lw], 17408\n" \
"s_add_u32 s44, %[wo], 65536\nbuffer_load_dwordx4 v90, %[rw], s44 offen nt lds\ns_add_u32 m0, %[lw], 18432\ns_add_u32 s44, %[wo], 131072\nbuffer_load_dwordx4 v90, %[rw], s44 offen nt lds\ns_add_u32 m0, %[lw], 19456\ns_add_u32 s44, %[wo], 196608\nbuffer_load_dwordx4 v90, %[rw], s44 offen nt lds\n" \
"s_add_u32 m0, %[lw], 20480\ns_add_u32 s44, %[wo], 524288\nbuffer_load_dwordx4 v90, %[rw], s44 offen nt lds\ns_add_u32 m0, %[lw], 21504\ns_add_u32 s44, %[wo], 589824\nbuffer_load_dwordx4 v90, %[rw], s44 offen nt lds\ns_add_u32 m0, %[lw], 22528\ns_add_u32 s44, %[wo], 655360\n" \
"buffer_load_dwordx4 v90, %[rw], s44 offen nt lds\ns_add_u32 m0, %[lw], 23552\ns_add_u32 s44, %[wo], 720896\nbuffer_load_dwordx4 v90, %[rw], s44 offen nt lds\ns_add_u32 m0, %[tb], 0\ns_add_u32 s44, %[wo], 1048576\nbuffer_load_dwordx4 v90, %[rw], s44 offen nt lds\ns_add_u32 m0, %[tb], 1024\n" \
"s_add_u32 s44, %[wo], 1114112\nbuffer_load_dwordx4 v90, %[rw], s44 offen nt lds\ns_add_u32 m0, %[tb], 2048\ns_add_u32 s44, %[wo], 1179648\nbuffer_load_dwordx4 v90, %[rw], s44 offen nt lds\ns_add_u32 m0, %[tb], 3072\ns_add_u32 s44, %[wo], 1245184\nbuffer_load_dwordx4 v90, %[rw], s44 offen nt lds\n" \
"s_add_u32 m0, %[tb], 4096\ns_add_u32 s44, %[wo], 1572864\nbuffer_load_dwordx4 v90, %[rw], s44 offen nt lds\ns_add_u32 m0, %[tb], 5120\ns_add_u32 s44, %[wo], 1638400\nbuffer_load_dwordx4 v90, %[rw], s44 offen nt lds\ns_add_u32 m0, %[tb], 6144\ns_add_u32 s44, %[wo], 1703936\n" \
"buffer_load_dwordx4 v90, %[rw], s44 offen nt lds\ns_add_u32 m0, %[tb], 7168\ns_add_u32 s44, %[wo], 1769472\nbuffer_load_dwordx4 v90, %[rw], s44 offen nt lds\ns_add_u32 m0, %[tb], 8192\ns_add_u32 s44, %[wo], 2097152\nbuffer_load_dwordx4 v90, %[rw], s44 offen nt lds\ns_add_u32 m0, %[tb], 9216\n" \
"s_add_u32 s44, %[wo], 2162688\nbuffer_load_dwordx4 v90, %[rw], s44 offen nt lds\ns_add_u32 m0, %[tb], 10240\ns_add_u32 s44, %[wo], 2228224\nbuffer_load_dwordx4 v90, %[rw], s44 offen nt lds\ns_add_u32 m0, %[tb], 11264\ns_add_u32 s44, %[wo], 2293760\nbuffer_load_dwordx4 v90, %[rw], s44 offen nt lds\n" \
"s_add_u32 m0, %[lw], 0\ns_add_u32 s44, %[wo], 2621440\nbuffer_load_dwordx4 v90, %[rw], s44 offen nt lds\ns_add_u32 m0, %[lw], 1024\ns_add_u32 s44, %[wo], 2686976\nbuffer_load_dwordx4 v90, %[rw], s44 offen nt lds\ns_add_u32 m0, %[lw], 2048\ns_add_u32 s44, %[wo], 2752512\n" \
"buffer_load_dwordx4 v90, %[rw], s44 offen nt lds\ns_add_u32 m0, %[lw], 3072\ns_add_u32 s44, %[wo], 2818048\nbuffer_load_dwordx4 v90, %[rw], s44 offen nt lds\ns_add_u32 m0, %[lw], 4096\ns_add_u32 s44, %[wo], 3145728\nbuffer_load_dwordx4 v90, %[rw], s44 offen nt lds\ns_add_u32 m0, %[lw], 5120\n" \
"s_add_u32 s44, %[wo], 3211264\nbuffer_load_dwordx4 v90, %[rw], s44 offen nt lds\ns_add_u32 m0, %[lw], 6144\ns_add_u32 s44, %[wo], 3276800\nbuffer_load_dwordx4 v90, %[rw], s44 offen nt lds\ns_add_u32 m0, %[lw], 7168\ns_add_u32 s44, %[wo], 3342336\nbuffer_load_dwordx4 v90, %[rw], s44 offen nt lds\n" \
"s_add_u32 m0, %[lw], 8192\ns_add_u32 s44, %[wo], 3670016\nbuffer_load_dwordx4 v90, %[rw], s44 offen nt lds\ns_add_u32 m0, %[lw], 9216\ns_add_u32 s44, %[wo], 3735552\nbuffer_load_dwordx4 v90, %[rw], s44 offen nt lds\ns_add_u32 m0, %[lw], 10240\ns_add_u32 s44, %[wo], 3801088\n" \
"buffer_load_dwordx4 v90, %[rw], s44 offen nt lds\ns_add_u32 m0, %[lw], 11264\ns_add_u32 s44, %[wo], 3866624\nbuffer_load_dwordx4 v90, %[rw], s44 offen nt lds\ns_waitcnt vmcnt(28)\ns_barrier\ns_add_u32 m0, %[lw], 12288\ns_add_u32 s44, %[wo], 4194304\n" \
"buffer_load_dwordx4 v90, %[rw], s44 offen nt lds\ns_add_u32 m0, %[lw], 13312\ns_add_u32 s44, %[wo], 4259840\nbuffer_load_dwordx4 v90, %[rw], s44 offen nt lds\ns_add_u32 m0, %[lw], 14336\ns_add_u32 s44, %[wo], 4325376\nbuffer_load_dwordx4 v90, %[rw], s44 offen nt lds\ns_add_u32 m0, %[lw], 15360\n" \
"s_add_u32 s44, %[wo], 4390912\nbuffer_load_dwordx4 v90, %[rw], s44 offen nt lds\nds_read_b64_tr_b16 a[0:1], v93 offset:16384\nds_read_b64_tr_b16 a[2:3], v93 offset:16896\nds_read_b64_tr_b16 a[16:17], v93 offset:16392\nds_read_b64_tr_b16 a[18:19], v93 offset:16904\nds_read_b64_tr_b16 a[4:5], v93 offset:40960\nds_read_b64_tr_b16 a[6:7], v93 offset:41472\n" \
"ds_read_b64_tr_b16 a[20:21], v93 offset:40968\nds_read_b64_tr_b16 a[22:23], v93 offset:41480\ns_waitcnt lgkmcnt(0)\ns_waitcnt vmcnt(28)\ns_barrier\ns_add_u32 m0, %[lw], 16384\ns_add_u32 s44, %[wo], 4718592\nbuffer_load_dwordx4 v90, %[rw], s44 offen nt lds\n" \
"s_add_u32 m0, %[lw], 17408\ns_add_u32 s44, %[wo], 4784128\nbuffer_load_dwordx4 v90, %[rw], s44 offen nt lds\ns_add_u32 m0, %[lw], 18432\ns_add_u32 s44, %[wo], 4849664\nbuffer_load_dwordx4 v90, %[rw], s44 offen nt lds\ns_add_u32 m0, %[lw], 19456\ns_add_u32 s44, %[wo], 4915200\n" \
"buffer_load_dwordx4 v90, %[rw], s44 offen nt lds\nds_read_b64_tr_b16 a[32:33], v93 offset:20480\nds_read_b64_tr_b16 a[34:35], v93 offset:20992\nds_read_b64_tr_b16 a[40:41], v93 offset:20488\nds_read_b64_tr_b16 a[42:43], v93 offset:21000\nds_read_b64_tr_b16 a[36:37], v93 offset:45056\nds_read_b64_tr_b16 a[38:39], v93 offset:45568\nds_read_b64_tr_b16 a[44:45], v93 offset:45064\n" \
"ds_read_b64_tr_b16 a[46:47], v93 offset:45576\ns_waitcnt lgkmcnt(0)\ns_waitcnt vmcnt(28)\ns_barrier\ns_add_u32 m0, %[lw], 20480\ns_add_u32 s44, %[wo], 5242880\nbuffer_load_dwordx4 v90, %[rw], s44 offen nt lds\ns_add_u32 m0, %[lw], 21504\n" \
"s_add_u32 s44, %[wo], 5308416\nbuffer_load_dwordx4 v90, %[rw], s44 offen nt lds\ns_add_u32 m0, %[lw], 22528\ns_add_u32 s44, %[wo], 5373952\nbuffer_load_dwordx4 v90, %[rw], s44 offen nt lds\ns_add_u32 m0, %[lw], 23552\ns_add_u32 s44, %[wo], 5439488\nbuffer_load_dwordx4 v90, %[rw], s44 offen nt lds\n" \
"ds_read_b64_tr_b16 a[56:57], v92 offset:0\nds_read_b64_tr_b16 a[58:59], v92 offset:512\nds_read_b64_tr_b16 v[84:85], v92 offset:8\nds_read_b64_tr_b16 v[86:87], v92 offset:520\nds_read_b64_tr_b16 a[60:61], v92 offset:12288\nds_read_b64_tr_b16 a[62:63], v92 offset:12800\nds_read_b64_tr_b16 v[80:81], v92 offset:12296\nds_read_b64_tr_b16 v[82:83], v92 offset:12808\n" \
"s_waitcnt lgkmcnt(0)\ns_waitcnt vmcnt(28)\ns_barrier\ns_add_u32 m0, %[tb], 0\ns_add_u32 s44, %[wo], 5767168\nbuffer_load_dwordx4 v90, %[rw], s44 offen nt lds\ns_add_u32 m0, %[tb], 1024\ns_add_u32 s44, %[wo], 5832704\n" \
"buffer_load_dwordx4 v90, %[rw], s44 offen nt lds\ns_add_u32 m0, %[tb], 2048\ns_add_u32 s44, %[wo], 5898240\nbuffer_load_dwordx4 v90, %[rw], s44 offen nt lds\ns_add_u32 m0, %[tb], 3072\ns_add_u32 s44, %[wo], 5963776\nbuffer_load_dwordx4 v90, %[rw], s44 offen nt lds\nds_read_b64_tr_b16 a[80:81], v92 offset:4096\n" \
"ds_read_b64_tr_b16 a[82:83], v92 offset:4608\nds_read_b64_tr_b16 a[88:89], v92 offset:4104\nds_read_b64_tr_b16 a[90:91], v92 offset:4616\nds_read_b64_tr_b16 a[84:85], v92 offset:16384\nds_read_b64_tr_b16 a[86:87], v92 offset:16896\nds_read_b64_tr_b16 a[92:93], v92 offset:16392\nds_read_b64_tr_b16 a[94:95], v92 offset:16904\ns_waitcnt lgkmcnt(0)\n" \
"s_waitcnt vmcnt(28)\ns_barrier\ns_add_u32 m0, %[tb], 4096\ns_add_u32 s44, %[wo], 6291456\nbuffer_load_dwordx4 v90, %[rw], s44 offen nt lds\ns_add_u32 m0, %[tb], 5120\ns_add_u32 s44, %[wo], 6356992\nbuffer_load_dwordx4 v90, %[rw], s44 offen nt lds\n" \
"s_add_u32 m0, %[tb], 6144\ns_add_u32 s44, %[wo], 6422528\nbuffer_load_dwordx4 v90, %[rw], s44 offen nt lds\ns_add_u32 m0, %[tb], 7168\ns_add_u32 s44, %[wo], 6488064\nbuffer_load_dwordx4 v90, %[rw], s44 offen nt lds\nds_read_b64_tr_b16 a[104:105], v92 offset:8192\nds_read_b64_tr_b16 a[106:107], v92 offset:8704\n" \
"ds_read_b64_tr_b16 a[120:121], v92 offset:8200\nds_read_b64_tr_b16 a[122:123], v92 offset:8712\nds_read_b64_tr_b16 a[108:109], v92 offset:20480\nds_read_b64_tr_b16 a[110:111], v92 offset:20992\nds_read_b64_tr_b16 a[124:125], v92 offset:20488\nds_read_b64_tr_b16 a[126:127], v92 offset:21000\ns_waitcnt lgkmcnt(0)\ns_waitcnt vmcnt(28)\n" \
"s_barrier\ns_add_u32 m0, %[tb], 8192\ns_add_u32 s44, %[wo], 6815744\nbuffer_load_dwordx4 v90, %[rw], s44 offen nt lds\ns_add_u32 m0, %[tb], 9216\ns_add_u32 s44, %[wo], 6881280\nbuffer_load_dwordx4 v90, %[rw], s44 offen nt lds\ns_add_u32 m0, %[tb], 10240\n" \
"s_add_u32 s44, %[wo], 6946816\nbuffer_load_dwordx4 v90, %[rw], s44 offen nt lds\ns_add_u32 m0, %[tb], 11264\ns_add_u32 s44, %[wo], 7012352\nbuffer_load_dwordx4 v90, %[rw], s44 offen nt lds\nds_read_b64_tr_b16 a[136:137], v93 offset:0\nds_read_b64_tr_b16 a[138:139], v93 offset:512\nds_read_b64_tr_b16 a[144:145], v93 offset:8\n" \
"ds_read_b64_tr_b16 a[146:147], v93 offset:520\nds_read_b64_tr_b16 a[140:141], v93 offset:24576\nds_read_b64_tr_b16 a[142:143], v93 offset:25088\nds_read_b64_tr_b16 a[148:149], v93 offset:24584\nds_read_b64_tr_b16 a[150:151], v93 offset:25096\ns_waitcnt lgkmcnt(0)\ns_waitcnt vmcnt(28)\ns_barrier\n" \
"s_add_u32 m0, %[lw], 0\ns_add_u32 s44, %[wo], 7340032\nbuffer_load_dwordx4 v90, %[rw], s44 offen nt lds\ns_add_u32 m0, %[lw], 1024\ns_add_u32 s44, %[wo], 7405568\nbuffer_load_dwordx4 v90, %[rw], s44 offen nt lds\ns_add_u32 m0, %[lw], 2048\ns_add_u32 s44, %[wo], 7471104\n" \
"buffer_load_dwordx4 v90, %[rw], s44 offen nt lds\ns_add_u32 m0, %[lw], 3072\ns_add_u32 s44, %[wo], 7536640\nbuffer_load_dwordx4 v90, %[rw], s44 offen nt lds\nds_read_b64_tr_b16 a[160:161], v93 offset:4096\nds_read_b64_tr_b16 a[162:163], v93 offset:4608\nds_read_b64_tr_b16 v[76:77], v93 offset:4104\nds_read_b64_tr_b16 v[78:79], v93 offset:4616\n" \
"ds_read_b64_tr_b16 a[164:165], v93 offset:28672\nds_read_b64_tr_b16 a[166:167], v93 offset:29184\nds_read_b64_tr_b16 v[72:73], v93 offset:28680\nds_read_b64_tr_b16 v[74:75], v93 offset:29192\ns_waitcnt lgkmcnt(0)\ns_waitcnt vmcnt(28)\ns_barrier\ns_add_u32 m0, %[lw], 4096\n" \
"s_add_u32 s44, %[wo], 7864320\nbuffer_load_dwordx4 v90, %[rw], s44 offen nt lds\ns_add_u32 m0, %[lw], 5120\ns_add_u32 s44, %[wo], 7929856\nbuffer_load_dwordx4 v90, %[rw], s44 offen nt lds\ns_add_u32 m0, %[lw], 6144\ns_add_u32 s44, %[wo], 7995392\nbuffer_load_dwordx4 v90, %[rw], s44 offen nt lds\n" \
"s_add_u32 m0, %[lw], 7168\ns_add_u32 s44, %[wo], 8060928\nbuffer_load_dwordx4 v90, %[rw], s44 offen nt lds\nds_read_b64_tr_b16 a[184:185], v93 offset:8192\nds_read_b64_tr_b16 a[186:187], v93 offset:8704\nds_read_b64_tr_b16 a[192:193], v93 offset:8200\nds_read_b64_tr_b16 a[194:195], v93 offset:8712\nds_read_b64_tr_b16 a[188:189], v93 offset:32768\n" \
"ds_read_b64_tr_b16 a[190:191], v93 offset:33280\nds_read_b64_tr_b16 a[196:197], v93 offset:32776\nds_read_b64_tr_b16 a[198:199], v93 offset:33288\ns_waitcnt lgkmcnt(0)\ns_waitcnt vmcnt(28)\ns_barrier\ns_add_u32 m0, %[lw], 8192\ns_add_u32 s44, %[w2], 0\n" \
"buffer_load_dwordx4 v91, %[ro], s44 offen nt lds\ns_add_u32 m0, %[lw], 9216\ns_add_u32 s44, %[w2], 16384\nbuffer_load_dwordx4 v91, %[ro], s44 offen nt lds\ns_add_u32 m0, %[lw], 10240\ns_add_u32 s44, %[w2], 32768\nbuffer_load_dwordx4 v91, %[ro], s44 offen nt lds\ns_add_u32 m0, %[lw], 11264\n" \
"s_add_u32 s44, %[w2], 49152\nbuffer_load_dwordx4 v91, %[ro], s44 offen nt lds\nds_read_b64_tr_b16 a[208:209], v93 offset:12288\nds_read_b64_tr_b16 a[210:211], v93 offset:12800\nds_read_b64_tr_b16 a[224:225], v93 offset:12296\nds_read_b64_tr_b16 a[226:227], v93 offset:12808\nds_read_b64_tr_b16 a[212:213], v93 offset:36864\nds_read_b64_tr_b16 a[214:215], v93 offset:37376\n" \
"ds_read_b64_tr_b16 a[228:229], v93 offset:36872\nds_read_b64_tr_b16 a[230:231], v93 offset:37384\ns_waitcnt lgkmcnt(0)\ns_waitcnt vmcnt(28)\ns_barrier\ns_add_u32 m0, %[lw], 12288\ns_add_u32 s44, %[w2], 128\nbuffer_load_dwordx4 v91, %[ro], s44 offen nt lds\n" \
"s_add_u32 m0, %[lw], 13312\ns_add_u32 s44, %[w2], 16512\nbuffer_load_dwordx4 v91, %[ro], s44 offen nt lds\ns_add_u32 m0, %[lw], 14336\ns_add_u32 s44, %[w2], 32896\nbuffer_load_dwordx4 v91, %[ro], s44 offen nt lds\ns_add_u32 m0, %[lw], 15360\ns_add_u32 s44, %[w2], 49280\n" \
"buffer_load_dwordx4 v91, %[ro], s44 offen nt lds\nds_read_b64_tr_b16 a[240:241], v93 offset:16384\nds_read_b64_tr_b16 a[242:243], v93 offset:16896\nds_read_b64_tr_b16 a[248:249], v93 offset:16392\nds_read_b64_tr_b16 a[250:251], v93 offset:16904\nds_read_b64_tr_b16 a[244:245], v93 offset:40960\nds_read_b64_tr_b16 a[246:247], v93 offset:41472\nds_read_b64_tr_b16 a[252:253], v93 offset:40968\n" \
"ds_read_b64_tr_b16 a[254:255], v93 offset:41480\ns_waitcnt lgkmcnt(0)\ns_waitcnt vmcnt(28)\ns_barrier\ns_add_u32 m0, %[lw], 16384\ns_add_u32 s44, %[w2], 256\nbuffer_load_dwordx4 v91, %[ro], s44 offen nt lds\ns_add_u32 m0, %[lw], 17408\n" \
"s_add_u32 s44, %[w2], 16640\nbuffer_load_dwordx4 v91, %[ro], s44 offen nt lds\ns_add_u32 m0, %[lw], 18432\ns_add_u32 s44, %[w2], 33024\nbuffer_load_dwordx4 v91, %[ro], s44 offen nt lds\ns_add_u32 m0, %[lw], 19456\ns_add_u32 s44, %[w2], 49408\nbuffer_load_dwordx4 v91, %[ro], s44 offen nt lds\n" \
"ds_read_b64_tr_b16 v[104:105], v93 offset:20480\nds_read_b64_tr_b16 v[106:107], v93 offset:20992\nds_read_b64_tr_b16 v[68:69], v93 offset:20488\nds_read_b64_tr_b16 v[70:71], v93 offset:21000\nds_read_b64_tr_b16 v[108:109], v93 offset:45056\nds_read_b64_tr_b16 v[110:111], v93 offset:45568\nds_read_b64_tr_b16 v[64:65], v93 offset:45064\nds_read_b64_tr_b16 v[66:67], v93 offset:45576\n" \
"s_waitcnt lgkmcnt(0)\ns_waitcnt vmcnt(28)\ns_barrier\ns_add_u32 m0, %[lw], 20480\ns_add_u32 s44, %[w2], 384\nbuffer_load_dwordx4 v91, %[ro], s44 offen nt lds\ns_add_u32 m0, %[lw], 21504\ns_add_u32 s44, %[w2], 16768\n" \
"buffer_load_dwordx4 v91, %[ro], s44 offen nt lds\ns_add_u32 m0, %[lw], 22528\ns_add_u32 s44, %[w2], 33152\nbuffer_load_dwordx4 v91, %[ro], s44 offen nt lds\ns_add_u32 m0, %[lw], 23552\ns_add_u32 s44, %[w2], 49536\nbuffer_load_dwordx4 v91, %[ro], s44 offen nt lds\nds_read_b64_tr_b16 v[128:129], v92 offset:0\n" \
"ds_read_b64_tr_b16 v[130:131], v92 offset:512\nds_read_b64_tr_b16 v[136:137], v92 offset:8\nds_read_b64_tr_b16 v[138:139], v92 offset:520\nds_read_b64_tr_b16 v[132:133], v92 offset:12288\nds_read_b64_tr_b16 v[134:135], v92 offset:12800\nds_read_b64_tr_b16 v[140:141], v92 offset:12296\nds_read_b64_tr_b16 v[142:143], v92 offset:12808\ns_waitcnt lgkmcnt(0)\n" \
"s_waitcnt vmcnt(28)\ns_barrier\ns_add_u32 m0, %[tb], 0\ns_add_u32 s44, %[w2], 65536\nbuffer_load_dwordx4 v91, %[ro], s44 offen nt lds\ns_add_u32 m0, %[tb], 1024\ns_add_u32 s44, %[w2], 81920\nbuffer_load_dwordx4 v91, %[ro], s44 offen nt lds\n" \
"s_add_u32 m0, %[tb], 2048\ns_add_u32 s44, %[w2], 98304\nbuffer_load_dwordx4 v91, %[ro], s44 offen nt lds\ns_add_u32 m0, %[tb], 3072\ns_add_u32 s44, %[w2], 114688\nbuffer_load_dwordx4 v91, %[ro], s44 offen nt lds\nds_read_b64_tr_b16 v[152:153], v92 offset:4096\nds_read_b64_tr_b16 v[154:155], v92 offset:4608\n" \
"ds_read_b64_tr_b16 v[168:169], v92 offset:4104\nds_read_b64_tr_b16 v[170:171], v92 offset:4616\nds_read_b64_tr_b16 v[156:157], v92 offset:16384\nds_read_b64_tr_b16 v[158:159], v92 offset:16896\nds_read_b64_tr_b16 v[172:173], v92 offset:16392\nds_read_b64_tr_b16 v[174:175], v92 offset:16904\ns_waitcnt lgkmcnt(0)\ns_waitcnt vmcnt(28)\n" \
"s_barrier\ns_add_u32 m0, %[tb], 4096\ns_add_u32 s44, %[w2], 65664\nbuffer_load_dwordx4 v91, %[ro], s44 offen nt lds\ns_add_u32 m0, %[tb], 5120\ns_add_u32 s44, %[w2], 82048\nbuffer_load_dwordx4 v91, %[ro], s44 offen nt lds\ns_add_u32 m0, %[tb], 6144\n" \
"s_add_u32 s44, %[w2], 98432\nbuffer_load_dwordx4 v91, %[ro], s44 offen nt lds\ns_add_u32 m0, %[tb], 7168\ns_add_u32 s44, %[w2], 114816\nbuffer_load_dwordx4 v91, %[ro], s44 offen nt lds\nds_read_b64_tr_b16 v[184:185], v92 offset:8192\nds_read_b64_tr_b16 v[186:187], v92 offset:8704\nds_read_b64_tr_b16 v[192:193], v92 offset:8200\n" \
"ds_read_b64_tr_b16 v[194:195], v92 offset:8712\nds_read_b64_tr_b16 v[188:189], v92 offset:20480\nds_read_b64_tr_b16 v[190:191], v92 offset:20992\nds_read_b64_tr_b16 v[196:197], v92 offset:20488\nds_read_b64_tr_b16 v[198:199], v92 offset:21000\ns_waitcnt lgkmcnt(0)\ns_waitcnt vmcnt(28)\ns_barrier\n" \
"s_add_u32 m0, %[tb], 8192\ns_add_u32 s44, %[w2], 65792\nbuffer_load_dwordx4 v91, %[ro], s44 offen nt lds\ns_add_u32 m0, %[tb], 9216\ns_add_u32 s44, %[w2], 82176\nbuffer_load_dwordx4 v91, %[ro], s44 offen nt lds\ns_add_u32 m0, %[tb], 10240\ns_add_u32 s44, %[w2], 98560\n" \
"buffer_load_dwordx4 v91, %[ro], s44 offen nt lds\ns_add_u32 m0, %[tb], 11264\ns_add_u32 s44, %[w2], 114944\nbuffer_load_dwordx4 v91, %[ro], s44 offen nt lds\nds_read_b64_tr_b16 v[208:209], v93 offset:0\nds_read_b64_tr_b16 v[210:211], v93 offset:512\nds_read_b64_tr_b16 v[60:61], v93 offset:8\nds_read_b64_tr_b16 v[62:63], v93 offset:520\n" \
"ds_read_b64_tr_b16 v[212:213], v93 offset:24576\nds_read_b64_tr_b16 v[214:215], v93 offset:25088\nds_read_b64_tr_b16 v[56:57], v93 offset:24584\nds_read_b64_tr_b16 v[58:59], v93 offset:25096\ns_waitcnt lgkmcnt(0)\ns_waitcnt vmcnt(28)\ns_barrier\ns_add_u32 m0, %[lw], 0\n" \
"s_add_u32 s44, %[w2], 65920\nbuffer_load_dwordx4 v91, %[ro], s44 offen nt lds\ns_add_u32 m0, %[lw], 1024\ns_add_u32 s44, %[w2], 82304\nbuffer_load_dwordx4 v91, %[ro], s44 offen nt lds\ns_add_u32 m0, %[lw], 2048\ns_add_u32 s44, %[w2], 98688\nbuffer_load_dwordx4 v91, %[ro], s44 offen nt lds\n" \
"s_add_u32 m0, %[lw], 3072\ns_add_u32 s44, %[w2], 115072\nbuffer_load_dwordx4 v91, %[ro], s44 offen nt lds\nds_read_b64_tr_b16 v[232:233], v93 offset:4096\nds_read_b64_tr_b16 v[234:235], v93 offset:4608\nds_read_b64_tr_b16 v[240:241], v93 offset:4104\nds_read_b64_tr_b16 v[242:243], v93 offset:4616\nds_read_b64_tr_b16 v[236:237], v93 offset:28672\n" \
"ds_read_b64_tr_b16 v[238:239], v93 offset:29184\nds_read_b64_tr_b16 v[244:245], v93 offset:28680\nds_read_b64_tr_b16 v[246:247], v93 offset:29192\ns_waitcnt lgkmcnt(0)\ns_waitcnt vmcnt(28)\ns_barrier\ns_add_u32 m0, %[lw], 4096\ns_add_u32 s44, %[w2], 196608\n" \
"buffer_load_dwordx4 v91, %[ro], s44 offen nt lds\ns_add_u32 m0, %[lw], 5120\ns_add_u32 s44, %[w2], 212992\nbuffer_load_dwordx4 v91, %[ro], s44 offen nt lds\ns_add_u32 m0, %[lw], 6144\ns_add_u32 s44, %[w2], 229376\nbuffer_load_dwordx4 v91, %[ro], s44 offen nt lds\ns_add_u32 m0, %[lw], 7168\n" \
"s_add_u32 s44, %[w2], 245760\nbuffer_load_dwordx4 v91, %[ro], s44 offen nt lds\nds_read_b64_tr_b16 a[8:9], v95 offset:8192\nds_read_b64_tr_b16 a[10:11], v95 offset:8320\nds_read_b64_tr_b16 a[12:13], v95 offset:10240\nds_read_b64_tr_b16 a[14:15], v95 offset:10368\nds_read_b64_tr_b16 a[216:217], v95 offset:8256\nds_read_b64_tr_b16 a[218:219], v95 offset:8384\n" \
"ds_read_b64_tr_b16 a[220:221], v95 offset:10304\nds_read_b64_tr_b16 a[222:223], v95 offset:10432\ns_waitcnt lgkmcnt(0)\ns_waitcnt vmcnt(28)\ns_barrier\ns_add_u32 m0, %[lw], 8192\ns_add_u32 s44, %[w2], 196736\nbuffer_load_dwordx4 v91, %[ro], s44 offen nt lds\n" \
"s_add_u32 m0, %[lw], 9216\ns_add_u32 s44, %[w2], 213120\nbuffer_load_dwordx4 v91, %[ro], s44 offen nt lds\ns_add_u32 m0, %[lw], 10240\ns_add_u32 s44, %[w2], 229504\nbuffer_load_dwordx4 v91, %[ro], s44 offen nt lds\ns_add_u32 m0, %[lw], 11264\ns_add_u32 s44, %[w2], 245888\n" \
"buffer_load_dwordx4 v91, %[ro], s44 offen nt lds\nds_read_b64_tr_b16 a[64:65], v95 offset:12288\nds_read_b64_tr_b16 a[66:67], v95 offset:12416\nds_read_b64_tr_b16 a[68:69], v95 offset:14336\nds_read_b64_tr_b16 a[70:71], v95 offset:14464\nds_read_b64_tr_b16 v[112:113], v95 offset:12352\nds_read_b64_tr_b16 v[114:115], v95 offset:12480\nds_read_b64_tr_b16 v[116:117], v95 offset:14400\n" \
"ds_read_b64_tr_b16 v[118:119], v95 offset:14528\ns_waitcnt lgkmcnt(0)\ns_waitcnt vmcnt(28)\ns_barrier\ns_add_u32 m0, %[lw], 12288\ns_add_u32 s44, %[w2], 196864\nbuffer_load_dwordx4 v91, %[ro], s44 offen nt lds\ns_add_u32 m0, %[lw], 13312\n" \
"s_add_u32 s44, %[w2], 213248\nbuffer_load_dwordx4 v91, %[ro], s44 offen nt lds\ns_add_u32 m0, %[lw], 14336\ns_add_u32 s44, %[w2], 229632\nbuffer_load_dwordx4 v91, %[ro], s44 offen nt lds\ns_add_u32 m0, %[lw], 15360\ns_add_u32 s44, %[w2], 246016\nbuffer_load_dwordx4 v91, %[ro], s44 offen nt lds\n" \
"ds_read_b64_tr_b16 a[112:113], v95 offset:16384\nds_read_b64_tr_b16 a[114:115], v95 offset:16512\nds_read_b64_tr_b16 a[116:117], v95 offset:18432\nds_read_b64_tr_b16 a[118:119], v95 offset:18560\nds_read_b64_tr_b16 v[160:161], v95 offset:16448\nds_read_b64_tr_b16 v[162:163], v95 offset:16576\nds_read_b64_tr_b16 v[164:165], v95 offset:18496\nds_read_b64_tr_b16 v[166:167], v95 offset:18624\n" \
"s_waitcnt lgkmcnt(0)\ns_waitcnt vmcnt(28)\ns_barrier\ns_add_u32 m0, %[lw], 16384\ns_add_u32 s44, %[w2], 196992\nbuffer_load_dwordx4 v91, %[ro], s44 offen nt lds\ns_add_u32 m0, %[lw], 17408\ns_add_u32 s44, %[w2], 213376\n" \
"buffer_load_dwordx4 v91, %[ro], s44 offen nt lds\ns_add_u32 m0, %[lw], 18432\ns_add_u32 s44, %[w2], 229760\nbuffer_load_dwordx4 v91, %[ro], s44 offen nt lds\ns_add_u32 m0, %[lw], 19456\ns_add_u32 s44, %[w2], 246144\nbuffer_load_dwordx4 v91, %[ro], s44 offen nt lds\nds_read_b64_tr_b16 a[168:169], v95 offset:20480\n" \
"ds_read_b64_tr_b16 a[170:171], v95 offset:20608\nds_read_b64_tr_b16 a[172:173], v95 offset:22528\nds_read_b64_tr_b16 a[174:175], v95 offset:22656\nds_read_b64_tr_b16 v[216:217], v95 offset:20544\nds_read_b64_tr_b16 v[218:219], v95 offset:20672\nds_read_b64_tr_b16 v[220:221], v95 offset:22592\nds_read_b64_tr_b16 v[222:223], v95 offset:22720\ns_waitcnt lgkmcnt(0)\n" \
"s_waitcnt vmcnt(28)\ns_barrier\ns_add_u32 m0, %[lw], 20480\ns_add_u32 s44, %[w2], 131072\nbuffer_load_dwordx4 v91, %[ro], s44 offen nt lds\ns_add_u32 m0, %[lw], 21504\ns_add_u32 s44, %[w2], 147456\nbuffer_load_dwordx4 v91, %[ro], s44 offen nt lds\n" \
"s_add_u32 m0, %[lw], 22528\ns_add_u32 s44, %[w2], 163840\nbuffer_load_dwordx4 v91, %[ro], s44 offen nt lds\ns_add_u32 m0, %[lw], 23552\ns_add_u32 s44, %[w2], 180224\nbuffer_load_dwordx4 v91, %[ro], s44 offen nt lds\nds_read_b64_tr_b16 a[24:25], v94 offset:0\nds_read_b64_tr_b16 a[26:27], v94 offset:128\n" \
"ds_read_b64_tr_b16 a[28:29], v94 offset:2048\nds_read_b64_tr_b16 a[30:31], v94 offset:2176\nds_read_b64_tr_b16 a[232:233], v94 offset:64\nds_read_b64_tr_b16 a[234:235], v94 offset:192\nds_read_b64_tr_b16 a[236:237], v94 offset:2112\nds_read_b64_tr_b16 a[238:239], v94 offset:2240\ns_waitcnt lgkmcnt(0)\ns_waitcnt vmcnt(28)\n" \
"s_barrier\ns_add_u32 m0, %[tb], 0\ns_add_u32 s44, %[w2], 131200\nbuffer_load_dwordx4 v91, %[ro], s44 offen nt lds\ns_add_u32 m0, %[tb], 1024\ns_add_u32 s44, %[w2], 147584\nbuffer_load_dwordx4 v91, %[ro], s44 offen nt lds\ns_add_u32 m0, %[tb], 2048\n" \
"s_add_u32 s44, %[w2], 163968\nbuffer_load_dwordx4 v91, %[ro], s44 offen nt lds\ns_add_u32 m0, %[tb], 3072\ns_add_u32 s44, %[w2], 180352\nbuffer_load_dwordx4 v91, %[ro], s44 offen nt lds\nds_read_b64_tr_b16 a[72:73], v94 offset:4096\nds_read_b64_tr_b16 a[74:75], v94 offset:4224\nds_read_b64_tr_b16 a[76:77], v94 offset:6144\n" \
"ds_read_b64_tr_b16 a[78:79], v94 offset:6272\nds_read_b64_tr_b16 v[120:121], v94 offset:4160\nds_read_b64_tr_b16 v[122:123], v94 offset:4288\nds_read_b64_tr_b16 v[124:125], v94 offset:6208\nds_read_b64_tr_b16 v[126:127], v94 offset:6336\ns_waitcnt lgkmcnt(0)\ns_waitcnt vmcnt(28)\ns_barrier\n" \
"s_add_u32 m0, %[tb], 4096\ns_add_u32 s44, %[w2], 131328\nbuffer_load_dwordx4 v91, %[ro], s44 offen nt lds\ns_add_u32 m0, %[tb], 5120\ns_add_u32 s44, %[w2], 147712\nbuffer_load_dwordx4 v91, %[ro], s44 offen nt lds\ns_add_u32 m0, %[tb], 6144\ns_add_u32 s44, %[w2], 164096\n" \
"buffer_load_dwordx4 v91, %[ro], s44 offen nt lds\ns_add_u32 m0, %[tb], 7168\ns_add_u32 s44, %[w2], 180480\nbuffer_load_dwordx4 v91, %[ro], s44 offen nt lds\nds_read_b64_tr_b16 a[128:129], v94 offset:8192\nds_read_b64_tr_b16 a[130:131], v94 offset:8320\nds_read_b64_tr_b16 a[132:133], v94 offset:10240\nds_read_b64_tr_b16 a[134:135], v94 offset:10368\n" \
"ds_read_b64_tr_b16 v[176:177], v94 offset:8256\nds_read_b64_tr_b16 v[178:179], v94 offset:8384\nds_read_b64_tr_b16 v[180:181], v94 offset:10304\nds_read_b64_tr_b16 v[182:183], v94 offset:10432\ns_waitcnt lgkmcnt(0)\ns_waitcnt vmcnt(28)\ns_barrier\ns_add_u32 m0, %[tb], 8192\n" \
"s_add_u32 s44, %[w2], 131456\nbuffer_load_dwordx4 v91, %[ro], s44 offen nt lds\ns_add_u32 m0, %[tb], 9216\ns_add_u32 s44, %[w2], 147840\nbuffer_load_dwordx4 v91, %[ro], s44 offen nt lds\ns_add_u32 m0, %[tb], 10240\ns_add_u32 s44, %[w2], 164224\nbuffer_load_dwordx4 v91, %[ro], s44 offen nt lds\n" \
"s_add_u32 m0, %[tb], 11264\ns_add_u32 s44, %[w2], 180608\nbuffer_load_dwordx4 v91, %[ro], s44 offen nt lds\nds_read_b64_tr_b16 a[176:177], v95 offset:0\nds_read_b64_tr_b16 a[178:179], v95 offset:128\nds_read_b64_tr_b16 a[180:181], v95 offset:2048\nds_read_b64_tr_b16 a[182:183], v95 offset:2176\nds_read_b64_tr_b16 v[224:225], v95 offset:64\n" \
"ds_read_b64_tr_b16 v[226:227], v95 offset:192\nds_read_b64_tr_b16 v[228:229], v95 offset:2112\nds_read_b64_tr_b16 v[230:231], v95 offset:2240\ns_waitcnt lgkmcnt(0)\ns_waitcnt vmcnt(28)\ns_barrier\nds_read_b64_tr_b16 a[48:49], v95 offset:4096\nds_read_b64_tr_b16 a[50:51], v95 offset:4224\n" \
"ds_read_b64_tr_b16 a[52:53], v95 offset:6144\nds_read_b64_tr_b16 a[54:55], v95 offset:6272\nds_read_b64_tr_b16 v[96:97], v95 offset:4160\nds_read_b64_tr_b16 v[98:99], v95 offset:4288\nds_read_b64_tr_b16 v[100:101], v95 offset:6208\nds_read_b64_tr_b16 v[102:103], v95 offset:6336\ns_waitcnt lgkmcnt(0)\nds_write_b128 v1, v[84:87] offset:0\n" \
"ds_write_b128 v1, v[80:83] offset:1024\nds_write_b128 v1, v[76:79] offset:2048\nds_write_b128 v1, v[72:75] offset:3072\ns_waitcnt vmcnt(24)\ns_barrier\nds_read_b64_tr_b16 a[96:97], v95 offset:8192\nds_read_b64_tr_b16 a[98:99], v95 offset:8320\nds_read_b64_tr_b16 a[100:101], v95 offset:10240\n" \
"ds_read_b64_tr_b16 a[102:103], v95 offset:10368\nds_read_b64_tr_b16 v[144:145], v95 offset:8256\nds_read_b64_tr_b16 v[146:147], v95 offset:8384\nds_read_b64_tr_b16 v[148:149], v95 offset:10304\nds_read_b64_tr_b16 v[150:151], v95 offset:10432\ns_waitcnt lgkmcnt(0)\nds_write_b128 v1, v[68:71] offset:4096\nds_write_b128 v1, v[64:67] offset:5120\n" \
"ds_write_b128 v1, v[60:63] offset:6144\nds_write_b128 v1, v[56:59] offset:7168\ns_waitcnt vmcnt(20)\ns_barrier\nds_read_b64_tr_b16 a[152:153], v95 offset:12288\nds_read_b64_tr_b16 a[154:155], v95 offset:12416\nds_read_b64_tr_b16 a[156:157], v95 offset:14336\nds_read_b64_tr_b16 a[158:159], v95 offset:14464\n" \
"ds_read_b64_tr_b16 v[200:201], v95 offset:12352\nds_read_b64_tr_b16 v[202:203], v95 offset:12480\nds_read_b64_tr_b16 v[204:205], v95 offset:14400\nds_read_b64_tr_b16 v[206:207], v95 offset:14528\ns_waitcnt lgkmcnt(0)\ns_waitcnt vmcnt(16)\ns_barrier\nds_read_b64_tr_b16 a[200:201], v95 offset:16384\n" \
"ds_read_b64_tr_b16 a[202:203], v95 offset:16512\nds_read_b64_tr_b16 a[204:205], v95 offset:18432\nds_read_b64_tr_b16 a[206:207], v95 offset:18560\nds_read_b64_tr_b16 v[248:249], v95 offset:16448\nds_read_b64_tr_b16 v[250:251], v95 offset:16576\nds_read_b64_tr_b16 v[252:253], v95 offset:18496\nds_read_b64_tr_b16 v[254:255], v95 offset:18624\ns_waitcnt lgkmcnt(0)\n" \
"s_waitcnt vmcnt(12)\ns_barrier\nds_read_b64_tr_b16 v[56:57], v95 offset:20480\nds_read_b64_tr_b16 v[58:59], v95 offset:20608\nds_read_b64_tr_b16 v[60:61], v95 offset:22528\nds_read_b64_tr_b16 v[62:63], v95 offset:22656\nds_read_b64_tr_b16 v[64:65], v95 offset:20544\nds_read_b64_tr_b16 v[66:67], v95 offset:20672\n" \
"ds_read_b64_tr_b16 v[68:69], v95 offset:22592\nds_read_b64_tr_b16 v[70:71], v95 offset:22720\ns_waitcnt lgkmcnt(0)\nds_write_b128 v1, v[56:59] offset:8192\nds_write_b128 v1, v[60:63] offset:9216\nds_write_b128 v1, v[64:67] offset:16384\nds_write_b128 v1, v[68:71] offset:17408\ns_waitcnt vmcnt(8)\n" \
"s_barrier\nds_read_b64_tr_b16 v[68:69], v94 offset:0\nds_read_b64_tr_b16 v[70:71], v94 offset:128\nds_read_b64_tr_b16 v[64:65], v94 offset:2048\nds_read_b64_tr_b16 v[66:67], v94 offset:2176\nds_read_b64_tr_b16 v[60:61], v94 offset:64\nds_read_b64_tr_b16 v[62:63], v94 offset:192\nds_read_b64_tr_b16 v[56:57], v94 offset:2112\n" \
"ds_read_b64_tr_b16 v[58:59], v94 offset:2240\ns_waitcnt lgkmcnt(0)\nds_write_b128 v1, v[68:71] offset:10240\nds_write_b128 v1, v[64:67] offset:11264\nds_write_b128 v1, v[60:63] offset:18432\nds_write_b128 v1, v[56:59] offset:19456\ns_waitcnt vmcnt(4)\ns_barrier\n" \
"ds_read_b64_tr_b16 v[56:57], v94 offset:4096\nds_read_b64_tr_b16 v[58:59], v94 offset:4224\nds_read_b64_tr_b16 v[60:61], v94 offset:6144\nds_read_b64_tr_b16 v[62:63], v94 offset:6272\nds_read_b64_tr_b16 v[64:65], v94 offset:4160\nds_read_b64_tr_b16 v[66:67], v94 offset:4288\nds_read_b64_tr_b16 v[68:69], v94 offset:6208\nds_read_b64_tr_b16 v[70:71], v94 offset:6336\n" \
"s_waitcnt lgkmcnt(0)\nds_write_b128 v1, v[56:59] offset:12288\nds_write_b128 v1, v[60:63] offset:13312\nds_write_b128 v1, v[64:67] offset:20480\nds_write_b128 v1, v[68:71] offset:21504\ns_waitcnt vmcnt(0)\ns_barrier\nds_read_b64_tr_b16 v[68:69], v94 offset:8192\n" \
"ds_read_b64_tr_b16 v[70:71], v94 offset:8320\nds_read_b64_tr_b16 v[64:65], v94 offset:10240\nds_read_b64_tr_b16 v[66:67], v94 offset:10368\nds_read_b64_tr_b16 v[60:61], v94 offset:8256\nds_read_b64_tr_b16 v[62:63], v94 offset:8384\nds_read_b64_tr_b16 v[56:57], v94 offset:10304\nds_read_b64_tr_b16 v[58:59], v94 offset:10432\ns_waitcnt lgkmcnt(0)\n" \
"ds_write_b128 v1, v[68:71] offset:14336\nds_write_b128 v1, v[64:67] offset:15360\nds_write_b128 v1, v[60:63] offset:22528\nds_write_b128 v1, v[56:59] offset:23552\ns_waitcnt lgkmcnt(0)\ns_barrier\ns_mov_b32 s43, 0\n.Lvu_hb%=:\n" \
"buffer_load_dwordx2 v[16:17], off, s[36:39], s45 sc1\ns_waitcnt vmcnt(0)\nv_cmp_ne_u32 vcc, v15, v16\ns_cbranch_vccnz .Lvu_hb1%=\ns_sleep 4\ns_add_u32 s43, s43, 1\ns_cmp_lt_u32 s43, 262144\ns_cbranch_scc1 .Lvu_hb%=\n" \
"s_branch .Lvu_abort%=\n.Lvu_hb1%=:\nv_readfirstlane_b32 s44, v17\ns_and_b32 s44, s44, 0x200000\ns_add_u32 s36, s36, s44\ns_addc_u32 s37, s37, 0\ns_mov_b32 s40, -1\ns_mov_b32 s43, 0\n" \
"s_lshl_b32 s57, %[w], 12\ns_mov_b32 s56, 0\ns_mov_b32 s42, 0\ns_mov_b32 s60, -2147483648\ns_mov_b32 s54, -2147483648\ns_add_u32 s44, s40, 1\ns_lshl_b32 s44, s44, 17\ns_add_u32 s44, s44, %[xn]\n" \
"s_lshr_b32 s45, s44, 8\ns_and_b32 s45, s45, 0x2000\ns_xor_b32 s41, s44, s45\ns_add_u32 s41, s41, %[xw]\n.Lvu_f0%=:\nbuffer_load_dword v14, v8, s[36:39], s56 offen sc1\ns_waitcnt vmcnt(0)\nv_cmp_ge_i32 vcc, s40, v14\n" \
"s_cbranch_vccz .Lvu_f0fok%=\ns_add_u32 s43, s43, 1\ns_cmp_lt_u32 s43, 262144\ns_cbranch_scc1 .Lvu_f0%=\ns_branch .Lvu_abort%=\n.Lvu_f0fok%=:\ns_add_u32 m0, s57, 0\ns_add_u32 s58, s41, 0\n" \
"buffer_load_dwordx4 v0, s[36:39], s58 offen offset:0 sc1 lds\nbuffer_load_dwordx4 v0, s[36:39], s58 offen offset:1024 sc1 lds\nbuffer_load_dwordx4 v0, s[36:39], s58 offen offset:2048 sc1 lds\nbuffer_load_dwordx4 v0, s[36:39], s58 offen offset:3072 sc1 lds\ns_add_u32 m0, s57, 32768\ns_add_u32 s58, s41, 32768\nbuffer_load_dwordx4 v0, s[36:39], s58 offen offset:0 sc1 lds\nbuffer_load_dwordx4 v0, s[36:39], s58 offen offset:1024 sc1 lds\n" \
"buffer_load_dwordx4 v0, s[36:39], s58 offen offset:2048 sc1 lds\nbuffer_load_dwordx4 v0, s[36:39], s58 offen offset:3072 sc1 lds\ns_waitcnt vmcnt(0)\ns_barrier\nds_read_b128 v[88:91], v2 offset:0\nds_read_b128 v[92:95], v2 offset:256\nds_read_b128 v[40:43], v2 offset:2048\nds_read_b128 v[44:47], v2 offset:2304\n" \
".Lvu_tok%=:\nds_read_b128 v[64:67], v2 offset:4096\nds_read_b128 v[68:71], v2 offset:4352\nds_read_b128 v[56:59], v1 offset:0\nds_read_b128 v[60:63], v1 offset:1024\ns_waitcnt lgkmcnt(7)\nv_mfma_f32_16x16x32_bf16 v[16:19], a[0:3], v[88:91], 0\nv_cvt_pk_bf16_f32 v76, v80, v81\n" \
"v_cvt_pk_bf16_f32 v77, v82, v83\nv_mfma_f32_16x16x32_bf16 v[20:23], a[16:19], v[88:91], 0\ns_waitcnt lgkmcnt(6)\nds_read_b128 v[32:35], v2 offset:6144\nv_cvt_pk_bf16_f32 v78, v84, v85\nv_cvt_pk_bf16_f32 v79, v86, v87\nv_mfma_f32_16x16x32_bf16 v[24:27], a[0:3], v[92:95], 0\nv_and_or_b32 v76, v76, -2, v13\n" \
"v_and_or_b32 v78, v78, -2, v13\nv_mfma_f32_16x16x32_bf16 v[28:31], a[16:19], v[92:95], 0\ns_waitcnt lgkmcnt(6)\nbuffer_store_dwordx4 v[76:79], v7, s[36:39], s60 offen offset:2560 sc1\nds_read_b128 v[36:39], v2 offset:6400\nv_mfma_f32_16x16x32_bf16 v[16:19], a[4:7], v[40:43], v[16:19]\nv_mfma_f32_16x16x32_bf16 v[20:23], a[20:23], v[40:43], v[20:23]\ns_waitcnt lgkmcnt(6)\n" \
"ds_read_b128 v[40:43], v2 offset:8192\nv_mfma_f32_16x16x32_bf16 v[24:27], a[4:7], v[44:47], v[24:27]\ns_add_u32 m0, s57, 16384\ns_add_u32 s60, s41, 65536\nbuffer_load_dwordx4 v0, s[36:39], s60 offen offset:0 sc1 lds\nv_mfma_f32_16x16x32_bf16 v[28:31], a[20:23], v[44:47], v[28:31]\ns_waitcnt lgkmcnt(6)\nds_read_b128 v[44:47], v2 offset:8448\n" \
"v_mfma_f32_16x16x32_bf16 v[16:19], a[32:35], v[64:67], v[16:19]\nv_mfma_f32_16x16x32_bf16 v[20:23], a[40:43], v[64:67], v[20:23]\ns_waitcnt lgkmcnt(6)\nds_read_b128 v[64:67], v2 offset:10240\nv_mfma_f32_16x16x32_bf16 v[24:27], a[32:35], v[68:71], v[24:27]\nbuffer_load_dwordx4 v0, s[36:39], s60 offen offset:1024 sc1 lds\nv_mfma_f32_16x16x32_bf16 v[28:31], a[40:43], v[68:71], v[28:31]\ns_waitcnt lgkmcnt(4)\n" \
"ds_read_b128 v[68:71], v2 offset:10496\nv_mfma_f32_16x16x32_bf16 v[16:19], a[36:39], v[32:35], v[16:19]\nv_mfma_f32_16x16x32_bf16 v[20:23], a[44:47], v[32:35], v[20:23]\ns_waitcnt lgkmcnt(4)\nds_read_b128 v[32:35], v2 offset:12288\nv_mfma_f32_16x16x32_bf16 v[24:27], a[36:39], v[36:39], v[24:27]\nbuffer_load_dwordx4 v0, s[36:39], s60 offen offset:2048 sc1 lds\nv_mfma_f32_16x16x32_bf16 v[28:31], a[44:47], v[36:39], v[28:31]\n" \
"s_waitcnt lgkmcnt(4)\nds_read_b128 v[36:39], v2 offset:12544\nv_mfma_f32_16x16x32_bf16 v[16:19], a[56:59], v[40:43], v[16:19]\nv_mfma_f32_16x16x32_bf16 v[20:23], v[56:59], v[40:43], v[20:23]\ns_waitcnt lgkmcnt(4)\nds_read_b128 v[40:43], v2 offset:14336\nv_mfma_f32_16x16x32_bf16 v[24:27], a[56:59], v[44:47], v[24:27]\nbuffer_load_dwordx4 v0, s[36:39], s60 offen offset:3072 sc1 lds\n" \
"v_mfma_f32_16x16x32_bf16 v[28:31], v[56:59], v[44:47], v[28:31]\ns_waitcnt lgkmcnt(4)\nds_read_b128 v[44:47], v2 offset:14592\nv_mfma_f32_16x16x32_bf16 v[16:19], a[60:63], v[64:67], v[16:19]\nv_mfma_f32_16x16x32_bf16 v[20:23], v[60:63], v[64:67], v[20:23]\ns_waitcnt lgkmcnt(4)\nds_read_b128 v[64:67], v2 offset:32768\nv_mfma_f32_16x16x32_bf16 v[24:27], a[60:63], v[68:71], v[24:27]\n" \
"v_mfma_f32_16x16x32_bf16 v[28:31], v[60:63], v[68:71], v[28:31]\ns_waitcnt lgkmcnt(4)\nds_read_b128 v[68:71], v2 offset:33024\nv_mfma_f32_16x16x32_bf16 v[16:19], a[80:83], v[32:35], v[16:19]\nv_mfma_f32_16x16x32_bf16 v[20:23], a[88:91], v[32:35], v[20:23]\ns_waitcnt lgkmcnt(4)\nds_read_b128 v[32:35], v2 offset:34816\nv_mfma_f32_16x16x32_bf16 v[24:27], a[80:83], v[36:39], v[24:27]\n" \
"ds_read_b128 v[56:59], v1 offset:2048\nds_read_b128 v[60:63], v1 offset:3072\nv_mfma_f32_16x16x32_bf16 v[28:31], a[88:91], v[36:39], v[28:31]\ns_waitcnt lgkmcnt(6)\nds_read_b128 v[36:39], v2 offset:35072\nv_mfma_f32_16x16x32_bf16 v[16:19], a[84:87], v[40:43], v[16:19]\nv_mfma_f32_16x16x32_bf16 v[20:23], a[92:95], v[40:43], v[20:23]\ns_waitcnt lgkmcnt(6)\n" \
"ds_read_b128 v[40:43], v2 offset:36864\nv_mfma_f32_16x16x32_bf16 v[24:27], a[84:87], v[44:47], v[24:27]\nv_mfma_f32_16x16x32_bf16 v[28:31], a[92:95], v[44:47], v[28:31]\ns_waitcnt lgkmcnt(6)\nds_read_b128 v[44:47], v2 offset:37120\nv_mfma_f32_16x16x32_bf16 v[16:19], a[104:107], v[64:67], v[16:19]\nv_mfma_f32_16x16x32_bf16 v[20:23], a[120:123], v[64:67], v[20:23]\ns_waitcnt lgkmcnt(6)\n" \
"ds_read_b128 v[64:67], v2 offset:38912\nv_mfma_f32_16x16x32_bf16 v[24:27], a[104:107], v[68:71], v[24:27]\ns_barrier\nv_mfma_f32_16x16x32_bf16 v[28:31], a[120:123], v[68:71], v[28:31]\ns_waitcnt lgkmcnt(6)\nds_read_b128 v[68:71], v2 offset:39168\nv_mfma_f32_16x16x32_bf16 v[16:19], a[108:111], v[32:35], v[16:19]\ns_add_u32 m0, s57, 0\n" \
"s_add_u32 s60, s41, 98304\nbuffer_load_dwordx4 v0, s[36:39], s60 offen offset:0 sc1 lds\nv_mfma_f32_16x16x32_bf16 v[20:23], a[124:127], v[32:35], v[20:23]\ns_waitcnt lgkmcnt(4)\nds_read_b128 v[32:35], v2 offset:40960\nv_mfma_f32_16x16x32_bf16 v[24:27], a[108:111], v[36:39], v[24:27]\nv_mfma_f32_16x16x32_bf16 v[28:31], a[124:127], v[36:39], v[28:31]\ns_waitcnt lgkmcnt(4)\n" \
"ds_read_b128 v[36:39], v2 offset:41216\nv_mfma_f32_16x16x32_bf16 v[16:19], a[136:139], v[40:43], v[16:19]\nbuffer_load_dwordx4 v0, s[36:39], s60 offen offset:1024 sc1 lds\nv_mfma_f32_16x16x32_bf16 v[20:23], a[144:147], v[40:43], v[20:23]\ns_waitcnt lgkmcnt(4)\nds_read_b128 v[40:43], v2 offset:43008\nv_mfma_f32_16x16x32_bf16 v[24:27], a[136:139], v[44:47], v[24:27]\nv_mfma_f32_16x16x32_bf16 v[28:31], a[144:147], v[44:47], v[28:31]\n" \
"s_waitcnt lgkmcnt(4)\nds_read_b128 v[44:47], v2 offset:43264\nv_mfma_f32_16x16x32_bf16 v[16:19], a[140:143], v[64:67], v[16:19]\nbuffer_load_dwordx4 v0, s[36:39], s60 offen offset:2048 sc1 lds\nv_mfma_f32_16x16x32_bf16 v[20:23], a[148:151], v[64:67], v[20:23]\ns_waitcnt lgkmcnt(4)\nds_read_b128 v[64:67], v2 offset:45056\nv_mfma_f32_16x16x32_bf16 v[24:27], a[140:143], v[68:71], v[24:27]\n" \
"v_mfma_f32_16x16x32_bf16 v[28:31], a[148:151], v[68:71], v[28:31]\ns_waitcnt lgkmcnt(4)\nds_read_b128 v[68:71], v2 offset:45312\nv_mfma_f32_16x16x32_bf16 v[16:19], a[160:163], v[32:35], v[16:19]\nbuffer_load_dwordx4 v0, s[36:39], s60 offen offset:3072 sc1 lds\nv_mfma_f32_16x16x32_bf16 v[20:23], v[56:59], v[32:35], v[20:23]\ns_waitcnt lgkmcnt(4)\nds_read_b128 v[32:35], v2 offset:47104\n" \
"v_mfma_f32_16x16x32_bf16 v[24:27], a[160:163], v[36:39], v[24:27]\nv_mfma_f32_16x16x32_bf16 v[28:31], v[56:59], v[36:39], v[28:31]\ns_waitcnt lgkmcnt(4)\nds_read_b128 v[36:39], v2 offset:47360\nv_mfma_f32_16x16x32_bf16 v[16:19], a[164:167], v[40:43], v[16:19]\nv_mfma_f32_16x16x32_bf16 v[20:23], v[60:63], v[40:43], v[20:23]\ns_waitcnt vmcnt(4)\ns_barrier\n" \
"s_waitcnt lgkmcnt(4)\nds_read_b128 v[40:43], v2 offset:16384\nv_mfma_f32_16x16x32_bf16 v[24:27], a[164:167], v[44:47], v[24:27]\ns_waitcnt vmcnt(4)\nbuffer_store_dword v15, off, s[36:39], s54 sc1\nv_mfma_f32_16x16x32_bf16 v[28:31], v[60:63], v[44:47], v[28:31]\ns_waitcnt lgkmcnt(4)\nds_read_b128 v[44:47], v2 offset:16640\n" \
"v_mfma_f32_16x16x32_bf16 v[16:19], a[184:187], v[64:67], v[16:19]\ns_add_u32 s56, s56, %[fs]\ns_add_u32 s62, s40, 1\nv_mfma_f32_16x16x32_bf16 v[20:23], a[192:195], v[64:67], v[20:23]\ns_waitcnt lgkmcnt(4)\nds_read_b128 v[64:67], v2 offset:18432\nv_mfma_f32_16x16x32_bf16 v[24:27], a[184:187], v[68:71], v[24:27]\ns_add_u32 s44, s40, 2\n" \
"s_lshl_b32 s44, s44, 17\nds_read_b128 v[56:59], v1 offset:4096\nds_read_b128 v[60:63], v1 offset:5120\nv_mfma_f32_16x16x32_bf16 v[28:31], a[192:195], v[68:71], v[28:31]\ns_waitcnt lgkmcnt(6)\nds_read_b128 v[68:71], v2 offset:18688\nv_mfma_f32_16x16x32_bf16 v[16:19], a[188:191], v[32:35], v[16:19]\ns_add_u32 s44, s44, %[xn]\n" \
"s_lshr_b32 s45, s44, 8\nv_mfma_f32_16x16x32_bf16 v[20:23], a[196:199], v[32:35], v[20:23]\ns_waitcnt lgkmcnt(6)\nds_read_b128 v[32:35], v2 offset:20480\nv_mfma_f32_16x16x32_bf16 v[24:27], a[188:191], v[36:39], v[24:27]\ns_and_b32 s45, s45, 0x2000\ns_xor_b32 s41, s44, s45\nv_mfma_f32_16x16x32_bf16 v[28:31], a[196:199], v[36:39], v[28:31]\n" \
"s_waitcnt lgkmcnt(6)\nds_read_b128 v[36:39], v2 offset:20736\nv_mfma_f32_16x16x32_bf16 v[16:19], a[208:211], v[40:43], v[16:19]\ns_add_u32 s41, s41, %[xw]\nv_mfma_f32_16x16x32_bf16 v[20:23], a[224:227], v[40:43], v[20:23]\ns_waitcnt lgkmcnt(6)\nds_read_b128 v[40:43], v2 offset:22528\nv_mfma_f32_16x16x32_bf16 v[24:27], a[208:211], v[44:47], v[24:27]\n" \
"v_mfma_f32_16x16x32_bf16 v[28:31], a[224:227], v[44:47], v[28:31]\ns_waitcnt lgkmcnt(6)\nds_read_b128 v[44:47], v2 offset:22784\nv_mfma_f32_16x16x32_bf16 v[16:19], a[212:215], v[64:67], v[16:19]\nv_mfma_f32_16x16x32_bf16 v[20:23], a[228:231], v[64:67], v[20:23]\ns_waitcnt lgkmcnt(4)\nds_read_b128 v[64:67], v2 offset:24576\nv_mfma_f32_16x16x32_bf16 v[24:27], a[212:215], v[68:71], v[24:27]\n" \
"v_mfma_f32_16x16x32_bf16 v[28:31], a[228:231], v[68:71], v[28:31]\ns_waitcnt lgkmcnt(4)\nds_read_b128 v[68:71], v2 offset:24832\nv_mfma_f32_16x16x32_bf16 v[16:19], a[240:243], v[32:35], v[16:19]\nv_mfma_f32_16x16x32_bf16 v[20:23], a[248:251], v[32:35], v[20:23]\ns_waitcnt lgkmcnt(4)\nds_read_b128 v[32:35], v2 offset:26624\nv_mfma_f32_16x16x32_bf16 v[24:27], a[240:243], v[36:39], v[24:27]\n" \
"v_mfma_f32_16x16x32_bf16 v[28:31], a[248:251], v[36:39], v[28:31]\ns_waitcnt lgkmcnt(4)\nds_read_b128 v[36:39], v2 offset:26880\nv_mfma_f32_16x16x32_bf16 v[16:19], a[244:247], v[40:43], v[16:19]\nv_mfma_f32_16x16x32_bf16 v[20:23], a[252:255], v[40:43], v[20:23]\ns_waitcnt lgkmcnt(4)\nds_read_b128 v[40:43], v2 offset:28672\nv_mfma_f32_16x16x32_bf16 v[24:27], a[244:247], v[44:47], v[24:27]\n" \
"v_mfma_f32_16x16x32_bf16 v[28:31], a[252:255], v[44:47], v[28:31]\ns_waitcnt lgkmcnt(4)\nds_read_b128 v[44:47], v2 offset:28928\nv_mfma_f32_16x16x32_bf16 v[16:19], v[104:107], v[64:67], v[16:19]\nv_mfma_f32_16x16x32_bf16 v[20:23], v[56:59], v[64:67], v[20:23]\ns_waitcnt lgkmcnt(4)\nds_read_b128 v[64:67], v2 offset:30720\nv_mfma_f32_16x16x32_bf16 v[24:27], v[104:107], v[68:71], v[24:27]\n" \
"v_mfma_f32_16x16x32_bf16 v[28:31], v[56:59], v[68:71], v[28:31]\ns_waitcnt lgkmcnt(4)\nds_read_b128 v[68:71], v2 offset:30976\nv_mfma_f32_16x16x32_bf16 v[16:19], v[108:111], v[32:35], v[16:19]\nv_mfma_f32_16x16x32_bf16 v[20:23], v[60:63], v[32:35], v[20:23]\ns_waitcnt vmcnt(1)\ns_barrier\ns_waitcnt lgkmcnt(4)\n" \
"ds_read_b128 v[32:35], v2 offset:0\nv_mfma_f32_16x16x32_bf16 v[24:27], v[108:111], v[36:39], v[24:27]\nv_mfma_f32_16x16x32_bf16 v[28:31], v[60:63], v[36:39], v[28:31]\ns_waitcnt lgkmcnt(4)\nds_read_b128 v[36:39], v2 offset:256\nv_mfma_f32_16x16x32_bf16 v[16:19], v[128:131], v[40:43], v[16:19]\nv_mfma_f32_16x16x32_bf16 v[20:23], v[136:139], v[40:43], v[20:23]\ns_waitcnt lgkmcnt(4)\n" \
"ds_read_b128 v[40:43], v2 offset:2048\nv_mfma_f32_16x16x32_bf16 v[24:27], v[128:131], v[44:47], v[24:27]\nds_read_b128 v[56:59], v1 offset:6144\nds_read_b128 v[60:63], v1 offset:7168\nv_mfma_f32_16x16x32_bf16 v[28:31], v[136:139], v[44:47], v[28:31]\ns_waitcnt lgkmcnt(6)\nds_read_b128 v[44:47], v2 offset:2304\nv_mfma_f32_16x16x32_bf16 v[16:19], v[132:135], v[64:67], v[16:19]\n" \
"v_mfma_f32_16x16x32_bf16 v[20:23], v[140:143], v[64:67], v[20:23]\ns_waitcnt lgkmcnt(6)\nds_read_b128 v[64:67], v2 offset:4096\nv_mfma_f32_16x16x32_bf16 v[24:27], v[132:135], v[68:71], v[24:27]\nv_mfma_f32_16x16x32_bf16 v[28:31], v[140:143], v[68:71], v[28:31]\ns_waitcnt lgkmcnt(6)\nds_read_b128 v[68:71], v2 offset:4352\nv_mfma_f32_16x16x32_bf16 v[16:19], v[152:155], v[32:35], v[16:19]\n" \
"v_mfma_f32_16x16x32_bf16 v[20:23], v[168:171], v[32:35], v[20:23]\ns_lshr_b32 m0, s57, 4\ns_add_u32 m0, m0, 159744\ns_waitcnt lgkmcnt(6)\nds_read_b128 v[32:35], v2 offset:6144\nv_mfma_f32_16x16x32_bf16 v[24:27], v[152:155], v[36:39], v[24:27]\nbuffer_load_dword v8, s[36:39], s56 offen sc1 lds\nv_lshrrev_b32 v15, 2, v0\n" \
"v_mfma_f32_16x16x32_bf16 v[28:31], v[168:171], v[36:39], v[28:31]\ns_waitcnt lgkmcnt(6)\nds_read_b128 v[36:39], v2 offset:6400\nv_mfma_f32_16x16x32_bf16 v[16:19], v[156:159], v[40:43], v[16:19]\nbuffer_load_dwordx4 v[80:83], v3, s[36:39], s42 offen offset:0 sc1\nv_mfma_f32_16x16x32_bf16 v[20:23], v[172:175], v[40:43], v[20:23]\ns_waitcnt lgkmcnt(4)\nds_read_b128 v[40:43], v2 offset:8192\n" \
"v_mfma_f32_16x16x32_bf16 v[24:27], v[156:159], v[44:47], v[24:27]\nv_add_u32 v15, 159744, v15\nv_mfma_f32_16x16x32_bf16 v[28:31], v[172:175], v[44:47], v[28:31]\ns_waitcnt lgkmcnt(4)\nds_read_b128 v[44:47], v2 offset:8448\nv_mfma_f32_16x16x32_bf16 v[16:19], v[184:187], v[64:67], v[16:19]\nbuffer_load_dwordx4 v[84:87], v3, s[36:39], s42 offen offset:1024 sc1\nv_mfma_f32_16x16x32_bf16 v[20:23], v[192:195], v[64:67], v[20:23]\n" \
"s_waitcnt lgkmcnt(4)\nds_read_b128 v[64:67], v2 offset:10240\nv_mfma_f32_16x16x32_bf16 v[24:27], v[184:187], v[68:71], v[24:27]\nv_mfma_f32_16x16x32_bf16 v[28:31], v[192:195], v[68:71], v[28:31]\ns_waitcnt lgkmcnt(4)\nds_read_b128 v[68:71], v2 offset:10496\nv_mfma_f32_16x16x32_bf16 v[16:19], v[188:191], v[32:35], v[16:19]\nbuffer_load_dwordx4 v[88:91], v3, s[36:39], s42 offen offset:2048 sc1\n" \
"v_mfma_f32_16x16x32_bf16 v[20:23], v[196:199], v[32:35], v[20:23]\ns_waitcnt lgkmcnt(4)\nds_read_b128 v[32:35], v2 offset:12288\nv_mfma_f32_16x16x32_bf16 v[24:27], v[188:191], v[36:39], v[24:27]\nv_mfma_f32_16x16x32_bf16 v[28:31], v[196:199], v[36:39], v[28:31]\ns_waitcnt lgkmcnt(4)\nds_read_b128 v[36:39], v2 offset:14336\nv_mfma_f32_16x16x32_bf16 v[16:19], v[208:211], v[40:43], v[16:19]\n" \
"buffer_load_dwordx4 v[92:95], v3, s[36:39], s42 offen offset:3072 sc1\nv_mfma_f32_16x16x32_bf16 v[20:23], v[56:59], v[40:43], v[20:23]\ns_waitcnt lgkmcnt(4)\nds_read_b128 v[40:43], v2 offset:12544\nv_mfma_f32_16x16x32_bf16 v[24:27], v[208:211], v[44:47], v[24:27]\nv_mfma_f32_16x16x32_bf16 v[28:31], v[56:59], v[44:47], v[28:31]\ns_waitcnt lgkmcnt(4)\nds_read_b128 v[44:47], v2 offset:14592\n" \
"v_mfma_f32_16x16x32_bf16 v[16:19], v[212:215], v[64:67], v[16:19]\nv_mfma_f32_16x16x32_bf16 v[20:23], v[60:63], v[64:67], v[20:23]\ns_waitcnt lgkmcnt(4)\nv_mfma_f32_16x16x32_bf16 v[24:27], v[212:215], v[68:71], v[24:27]\nv_mfma_f32_16x16x32_bf16 v[28:31], v[60:63], v[68:71], v[28:31]\ns_waitcnt lgkmcnt(3)\nv_mfma_f32_16x16x32_bf16 v[16:19], v[232:235], v[32:35], v[16:19]\nv_mov_b64 v[48:49], 0\n" \
"v_mfma_f32_16x16x32_bf16 v[20:23], v[240:243], v[32:35], v[20:23]\ns_waitcnt lgkmcnt(2)\nv_mov_b64 v[50:51], 0\nv_mfma_f32_16x16x32_bf16 v[16:19], v[236:239], v[36:39], v[16:19]\nv_mov_b64 v[52:53], 0\nv_mfma_f32_16x16x32_bf16 v[20:23], v[244:247], v[36:39], v[20:23]\ns_waitcnt lgkmcnt(1)\nv_mov_b64 v[54:55], 0\n" \
"v_mfma_f32_16x16x32_bf16 v[24:27], v[232:235], v[40:43], v[24:27]\nv_mfma_f32_16x16x32_bf16 v[28:31], v[240:243], v[40:43], v[28:31]\ns_waitcnt lgkmcnt(0)\nv_mfma_f32_16x16x32_bf16 v[24:27], v[236:239], v[44:47], v[24:27]\nv_cvt_pk_bf16_f32 v72, v16, v20\nv_cvt_pk_bf16_f32 v73, v17, v21\nv_mfma_f32_16x16x32_bf16 v[28:31], v[244:247], v[44:47], v[28:31]\nv_cvt_pk_bf16_f32 v74, v18, v22\n" \
"v_cvt_pk_bf16_f32 v75, v19, v23\nds_write_b128 v9, v[72:75]\ns_nop 4\nv_cvt_pk_bf16_f32 v76, v24, v28\nv_cvt_pk_bf16_f32 v77, v25, v29\nv_cvt_pk_bf16_f32 v78, v26, v30\nv_cvt_pk_bf16_f32 v79, v27, v31\nds_write_b128 v9, v[76:79] offset:1024\n" \
"s_cmp_lt_i32 s40, 0\ns_cbranch_scc1 .Lvu_first%=\n.Lvu_crit%=:\ns_cmp_eq_u32 %[w], 1\ns_cbranch_scc1 .Lvu_c1%=\ns_cmp_eq_u32 %[w], 2\ns_cbranch_scc1 .Lvu_c2%=\ns_cmp_eq_u32 %[w], 3\n" \
"s_cbranch_scc1 .Lvu_c3%=\n.Lvu_c0%=:\nds_read_b128 v[56:59], v1 offset:0\nds_read_b128 v[60:63], v1 offset:1024\nds_read_b128 v[64:67], v1 offset:2048\nds_read_b128 v[68:71], v1 offset:3072\n.Lvu_poll0%=:\ns_waitcnt vmcnt(0)\n" \
"v_smfmac_f32_16x16x64_bf16 v[48:51], v[80:83], a[0:7], v4\nds_write_b128 v0, v[80:83] offset:16384\nv_max3_u32 v14, v80, v82, v84\nv_smfmac_f32_16x16x64_bf16 v[52:55], v[80:83], a[16:23], v4\nds_write_b128 v0, v[84:87] offset:17408\nv_max3_u32 v14, v14, v86, v88\nv_smfmac_f32_16x16x64_bf16 v[48:51], v[84:87], a[32:39], v4\nds_write_b128 v0, v[88:91] offset:18432\n" \
"ds_write_b128 v0, v[92:95] offset:19456\nv_max3_u32 v14, v14, v90, v92\nv_max_u32 v14, v14, v94\nv_smfmac_f32_16x16x64_bf16 v[52:55], v[84:87], a[40:47], v4\nv_cmp_eq_u32 vcc, -1, v14\nv_smfmac_f32_16x16x64_bf16 v[48:51], v[88:91], a[56:63], v4\ns_cbranch_vccnz .Lvu_re0%=\ns_waitcnt lgkmcnt(0)\n" \
"s_barrier\nds_read_b128 v[16:19], v0 offset:20480\nds_read_b128 v[20:23], v0 offset:21504\nv_smfmac_f32_16x16x64_bf16 v[52:55], v[88:91], v[56:63], v4\nds_read_b128 v[24:27], v0 offset:22528\nds_read_b128 v[28:31], v0 offset:23552\nds_read_b32 v15, v15\nv_smfmac_f32_16x16x64_bf16 v[48:51], v[92:95], a[80:87], v4\n" \
"ds_read_b128 v[32:35], v0 offset:24576\nds_read_b128 v[36:39], v0 offset:25600\nv_smfmac_f32_16x16x64_bf16 v[52:55], v[92:95], a[88:95], v4\ns_waitcnt lgkmcnt(6)\nds_read_b128 v[40:43], v0 offset:26624\nds_read_b128 v[44:47], v0 offset:27648\nv_smfmac_f32_16x16x64_bf16 v[48:51], v[16:19], a[104:111], v4\nds_read_b128 v[80:83], v0 offset:28672\n" \
"ds_read_b128 v[84:87], v0 offset:29696\nv_smfmac_f32_16x16x64_bf16 v[52:55], v[16:19], a[120:127], v4\ns_waitcnt lgkmcnt(9)\nds_read_b128 v[88:91], v0 offset:30720\nds_read_b128 v[92:95], v0 offset:31744\nv_mov_b64 v[16:17], 0\nv_smfmac_f32_16x16x64_bf16 v[48:51], v[20:23], a[136:143], v4\nv_mov_b64 v[18:19], 0\n" \
"v_smfmac_f32_16x16x64_bf16 v[52:55], v[20:23], a[144:151], v4\ns_waitcnt lgkmcnt(10)\nds_read_b128 v[56:59], v1 offset:4096\nds_read_b128 v[60:63], v1 offset:5120\nv_mov_b64 v[20:21], 0\nv_smfmac_f32_16x16x64_bf16 v[48:51], v[24:27], a[160:167], v4\ns_waitcnt lgkmcnt(10)\nv_cmp_ge_i32 s[64:65], s62, v15\n" \
"v_smfmac_f32_16x16x64_bf16 v[52:55], v[24:27], v[64:71], v4\ns_cmp_eq_u64 s[64:65], 0\ns_cselect_b32 s61, s41, -2147483648\nv_mov_b64 v[22:23], 0\nv_smfmac_f32_16x16x64_bf16 v[48:51], v[28:31], a[184:191], v4\nds_read_b128 v[64:67], v1 offset:6144\nds_read_b128 v[68:71], v1 offset:7168\nv_mov_b64 v[24:25], 0\n" \
"v_smfmac_f32_16x16x64_bf16 v[52:55], v[28:31], a[192:199], v4\ns_add_u32 m0, s57, 0\ns_add_u32 s58, s61, 0\ns_waitcnt lgkmcnt(11)\nbuffer_load_dwordx4 v0, s[36:39], s58 offen offset:0 sc1 lds\nv_smfmac_f32_16x16x64_bf16 v[48:51], v[32:35], a[208:215], v4\nv_mov_b64 v[26:27], 0\nv_smfmac_f32_16x16x64_bf16 v[52:55], v[32:35], a[224:231], v4\n" \
"s_waitcnt lgkmcnt(10)\nv_mov_b64 v[28:29], 0\nv_smfmac_f32_16x16x64_bf16 v[48:51], v[36:39], a[240:247], v4\nv_mov_b64 v[30:31], 0\nv_smfmac_f32_16x16x64_bf16 v[52:55], v[36:39], a[248:255], v4\ns_waitcnt lgkmcnt(9)\nbuffer_load_dwordx4 v0, s[36:39], s58 offen offset:1024 sc1 lds\nv_smfmac_f32_16x16x64_bf16 v[48:51], v[40:43], v[104:111], v4\n" \
"s_waitcnt lgkmcnt(2)\nv_mov_b64 v[32:33], 0\nv_smfmac_f32_16x16x64_bf16 v[52:55], v[40:43], v[56:63], v4\nv_mov_b64 v[34:35], 0\nv_smfmac_f32_16x16x64_bf16 v[48:51], v[44:47], v[128:135], v4\nv_mov_b64 v[36:37], 0\nv_smfmac_f32_16x16x64_bf16 v[52:55], v[44:47], v[136:143], v4\nds_read_b128 v[56:59], v1 offset:8192\n" \
"ds_read_b128 v[60:63], v1 offset:9216\nv_mov_b64 v[38:39], 0\nv_smfmac_f32_16x16x64_bf16 v[48:51], v[80:83], v[152:159], v4\nbuffer_load_dwordx4 v0, s[36:39], s58 offen offset:2048 sc1 lds\nv_smfmac_f32_16x16x64_bf16 v[52:55], v[80:83], v[168:175], v4\nv_mov_b64 v[40:41], 0\nv_smfmac_f32_16x16x64_bf16 v[48:51], v[84:87], v[184:191], v4\nv_mov_b64 v[42:43], 0\n" \
"v_smfmac_f32_16x16x64_bf16 v[52:55], v[84:87], v[192:199], v4\nv_mov_b64 v[44:45], 0\nv_smfmac_f32_16x16x64_bf16 v[48:51], v[88:91], v[208:215], v4\ns_waitcnt lgkmcnt(2)\nbuffer_load_dwordx4 v0, s[36:39], s58 offen offset:3072 sc1 lds\nv_smfmac_f32_16x16x64_bf16 v[52:55], v[88:91], v[64:71], v4\nv_mov_b64 v[46:47], 0\nv_smfmac_f32_16x16x64_bf16 v[48:51], v[92:95], v[232:239], v4\n" \
"ds_read_b128 v[64:67], v1 offset:10240\nds_read_b128 v[68:71], v1 offset:11264\nv_smfmac_f32_16x16x64_bf16 v[52:55], v[92:95], v[240:247], v4\ns_nop 3\nv_add_f32 v48, v48, v50\nv_add_f32 v49, v49, v51\ns_nop 1\nv_add_f32 v52, v52, v54\n" \
"v_add_f32 v53, v53, v55\nv_cvt_pk_bf16_f32 v14, v48, v52\nv_cvt_pk_bf16_f32 v15, v49, v53\nds_write2_b32 v5, v14, v15 offset0:0 offset1:4\ns_branch .Lvu_u%=\n.Lvu_re0%=:\nv_max_u32 v14, v80, v82\nv_cmp_eq_u32 vcc, -1, v14\n" \
"s_cbranch_vccz .Lvu_sk1293%=\ns_mov_b64 exec, vcc\nbuffer_load_dwordx4 v[80:83], v3, s[36:39], s42 offen offset:0 sc1\ns_mov_b64 exec, -1\n.Lvu_sk1293%=:\nv_max_u32 v14, v84, v86\nv_cmp_eq_u32 vcc, -1, v14\ns_cbranch_vccz .Lvu_sk1300%=\n" \
"s_mov_b64 exec, vcc\nbuffer_load_dwordx4 v[84:87], v3, s[36:39], s42 offen offset:1024 sc1\ns_mov_b64 exec, -1\n.Lvu_sk1300%=:\nv_max_u32 v14, v88, v90\nv_cmp_eq_u32 vcc, -1, v14\ns_cbranch_vccz .Lvu_sk1307%=\ns_mov_b64 exec, vcc\n" \
"buffer_load_dwordx4 v[88:91], v3, s[36:39], s42 offen offset:2048 sc1\ns_mov_b64 exec, -1\n.Lvu_sk1307%=:\nv_max_u32 v14, v92, v94\nv_cmp_eq_u32 vcc, -1, v14\ns_cbranch_vccz .Lvu_sk1314%=\ns_mov_b64 exec, vcc\nbuffer_load_dwordx4 v[92:95], v3, s[36:39], s42 offen offset:3072 sc1\n" \
"s_mov_b64 exec, -1\n.Lvu_sk1314%=:\nbuffer_load_dword v8, s[36:39], s56 offen sc1 lds\nv_mov_b64 v[48:49], 0\nv_mov_b64 v[50:51], 0\nv_mov_b64 v[52:53], 0\nv_mov_b64 v[54:55], 0\ns_add_u32 s43, s43, 1\n" \
"s_cmp_lt_u32 s43, 262144\ns_cbranch_scc1 .Lvu_poll0%=\ns_branch .Lvu_abort%=\n.Lvu_c1%=:\nds_read_b128 v[56:59], v1 offset:2048\nds_read_b128 v[60:63], v1 offset:3072\nds_read_b128 v[64:67], v1 offset:4096\nds_read_b128 v[68:71], v1 offset:5120\n" \
".Lvu_poll1%=:\ns_waitcnt vmcnt(0)\nv_smfmac_f32_16x16x64_bf16 v[48:51], v[80:83], a[104:111], v4\nds_write_b128 v0, v[80:83] offset:20480\nv_max3_u32 v14, v80, v82, v84\nv_smfmac_f32_16x16x64_bf16 v[52:55], v[80:83], a[120:127], v4\nds_write_b128 v0, v[84:87] offset:21504\nv_max3_u32 v14, v14, v86, v88\n" \
"v_smfmac_f32_16x16x64_bf16 v[48:51], v[84:87], a[136:143], v4\nds_write_b128 v0, v[88:91] offset:22528\nds_write_b128 v0, v[92:95] offset:23552\nv_max3_u32 v14, v14, v90, v92\nv_max_u32 v14, v14, v94\nv_smfmac_f32_16x16x64_bf16 v[52:55], v[84:87], a[144:151], v4\nv_cmp_eq_u32 vcc, -1, v14\nv_smfmac_f32_16x16x64_bf16 v[48:51], v[88:91], a[160:167], v4\n" \
"s_cbranch_vccnz .Lvu_re1%=\ns_waitcnt lgkmcnt(0)\ns_barrier\nds_read_b128 v[16:19], v0 offset:24576\nds_read_b128 v[20:23], v0 offset:25600\nv_smfmac_f32_16x16x64_bf16 v[52:55], v[88:91], v[56:63], v4\nds_read_b128 v[24:27], v0 offset:26624\nds_read_b128 v[28:31], v0 offset:27648\n" \
"ds_read_b32 v15, v15\nv_smfmac_f32_16x16x64_bf16 v[48:51], v[92:95], a[184:191], v4\nds_read_b128 v[32:35], v0 offset:28672\nds_read_b128 v[36:39], v0 offset:29696\nv_smfmac_f32_16x16x64_bf16 v[52:55], v[92:95], a[192:199], v4\ns_waitcnt lgkmcnt(6)\nds_read_b128 v[40:43], v0 offset:30720\nds_read_b128 v[44:47], v0 offset:31744\n" \
"v_smfmac_f32_16x16x64_bf16 v[48:51], v[16:19], a[208:215], v4\nds_read_b128 v[80:83], v0 offset:16384\nds_read_b128 v[84:87], v0 offset:17408\nv_smfmac_f32_16x16x64_bf16 v[52:55], v[16:19], a[224:231], v4\ns_waitcnt lgkmcnt(9)\nds_read_b128 v[88:91], v0 offset:18432\nds_read_b128 v[92:95], v0 offset:19456\nv_mov_b64 v[16:17], 0\n" \
"v_smfmac_f32_16x16x64_bf16 v[48:51], v[20:23], a[240:247], v4\nv_mov_b64 v[18:19], 0\nv_smfmac_f32_16x16x64_bf16 v[52:55], v[20:23], a[248:255], v4\ns_waitcnt lgkmcnt(10)\nds_read_b128 v[56:59], v1 offset:6144\nds_read_b128 v[60:63], v1 offset:7168\nv_mov_b64 v[20:21], 0\nv_smfmac_f32_16x16x64_bf16 v[48:51], v[24:27], v[104:111], v4\n" \
"s_waitcnt lgkmcnt(10)\nv_cmp_ge_i32 s[64:65], s62, v15\nv_smfmac_f32_16x16x64_bf16 v[52:55], v[24:27], v[64:71], v4\ns_cmp_eq_u64 s[64:65], 0\ns_cselect_b32 s61, s41, -2147483648\nv_mov_b64 v[22:23], 0\nv_smfmac_f32_16x16x64_bf16 v[48:51], v[28:31], v[128:135], v4\nds_read_b128 v[64:67], v1 offset:0\n" \
"ds_read_b128 v[68:71], v1 offset:1024\nv_mov_b64 v[24:25], 0\nv_smfmac_f32_16x16x64_bf16 v[52:55], v[28:31], v[136:143], v4\ns_add_u32 m0, s57, 0\ns_add_u32 s58, s61, 0\ns_waitcnt lgkmcnt(11)\nbuffer_load_dwordx4 v0, s[36:39], s58 offen offset:0 sc1 lds\nv_smfmac_f32_16x16x64_bf16 v[48:51], v[32:35], v[152:159], v4\n" \
"v_mov_b64 v[26:27], 0\nv_smfmac_f32_16x16x64_bf16 v[52:55], v[32:35], v[168:175], v4\ns_waitcnt lgkmcnt(10)\nv_mov_b64 v[28:29], 0\nv_smfmac_f32_16x16x64_bf16 v[48:51], v[36:39], v[184:191], v4\nv_mov_b64 v[30:31], 0\nv_smfmac_f32_16x16x64_bf16 v[52:55], v[36:39], v[192:199], v4\ns_waitcnt lgkmcnt(9)\n" \
"buffer_load_dwordx4 v0, s[36:39], s58 offen offset:1024 sc1 lds\nv_smfmac_f32_16x16x64_bf16 v[48:51], v[40:43], v[208:215], v4\ns_waitcnt lgkmcnt(2)\nv_mov_b64 v[32:33], 0\nv_smfmac_f32_16x16x64_bf16 v[52:55], v[40:43], v[56:63], v4\nv_mov_b64 v[34:35], 0\nv_smfmac_f32_16x16x64_bf16 v[48:51], v[44:47], v[232:239], v4\nv_mov_b64 v[36:37], 0\n" \
"v_smfmac_f32_16x16x64_bf16 v[52:55], v[44:47], v[240:247], v4\nds_read_b128 v[56:59], v1 offset:8192\nds_read_b128 v[60:63], v1 offset:9216\nv_mov_b64 v[38:39], 0\nv_smfmac_f32_16x16x64_bf16 v[48:51], v[80:83], a[0:7], v4\nbuffer_load_dwordx4 v0, s[36:39], s58 offen offset:2048 sc1 lds\nv_smfmac_f32_16x16x64_bf16 v[52:55], v[80:83], a[16:23], v4\nv_mov_b64 v[40:41], 0\n" \
"v_smfmac_f32_16x16x64_bf16 v[48:51], v[84:87], a[32:39], v4\nv_mov_b64 v[42:43], 0\nv_smfmac_f32_16x16x64_bf16 v[52:55], v[84:87], a[40:47], v4\nv_mov_b64 v[44:45], 0\nv_smfmac_f32_16x16x64_bf16 v[48:51], v[88:91], a[56:63], v4\ns_waitcnt lgkmcnt(2)\nbuffer_load_dwordx4 v0, s[36:39], s58 offen offset:3072 sc1 lds\nv_smfmac_f32_16x16x64_bf16 v[52:55], v[88:91], v[64:71], v4\n" \
"v_mov_b64 v[46:47], 0\nv_smfmac_f32_16x16x64_bf16 v[48:51], v[92:95], a[80:87], v4\nds_read_b128 v[64:67], v1 offset:10240\nds_read_b128 v[68:71], v1 offset:11264\nv_smfmac_f32_16x16x64_bf16 v[52:55], v[92:95], a[88:95], v4\ns_nop 3\nv_add_f32 v48, v48, v50\nv_add_f32 v49, v49, v51\n" \
"s_nop 1\nv_add_f32 v52, v52, v54\nv_add_f32 v53, v53, v55\nv_cvt_pk_bf16_f32 v14, v48, v52\nv_cvt_pk_bf16_f32 v15, v49, v53\nds_write2_b32 v5, v14, v15 offset0:0 offset1:4\ns_branch .Lvu_u%=\n.Lvu_re1%=:\n" \
"v_max_u32 v14, v80, v82\nv_cmp_eq_u32 vcc, -1, v14\ns_cbranch_vccz .Lvu_sk1445%=\ns_mov_b64 exec, vcc\nbuffer_load_dwordx4 v[80:83], v3, s[36:39], s42 offen offset:0 sc1\ns_mov_b64 exec, -1\n.Lvu_sk1445%=:\nv_max_u32 v14, v84, v86\n" \
"v_cmp_eq_u32 vcc, -1, v14\ns_cbranch_vccz .Lvu_sk1452%=\ns_mov_b64 exec, vcc\nbuffer_load_dwordx4 v[84:87], v3, s[36:39], s42 offen offset:1024 sc1\ns_mov_b64 exec, -1\n.Lvu_sk1452%=:\nv_max_u32 v14, v88, v90\nv_cmp_eq_u32 vcc, -1, v14\n" \
"s_cbranch_vccz .Lvu_sk1459%=\ns_mov_b64 exec, vcc\nbuffer_load_dwordx4 v[88:91], v3, s[36:39], s42 offen offset:2048 sc1\ns_mov_b64 exec, -1\n.Lvu_sk1459%=:\nv_max_u32 v14, v92, v94\nv_cmp_eq_u32 vcc, -1, v14\ns_cbranch_vccz .Lvu_sk1466%=\n" \
"s_mov_b64 exec, vcc\nbuffer_load_dwordx4 v[92:95], v3, s[36:39], s42 offen offset:3072 sc1\ns_mov_b64 exec, -1\n.Lvu_sk1466%=:\nv_mov_b64 v[48:49], 0\nv_mov_b64 v[50:51], 0\nv_mov_b64 v[52:53], 0\nv_mov_b64 v[54:55], 0\n" \
"s_add_u32 s43, s43, 1\ns_cmp_lt_u32 s43, 262144\ns_cbranch_scc1 .Lvu_poll1%=\ns_branch .Lvu_abort%=\n.Lvu_c2%=:\nds_read_b128 v[56:59], v1 offset:4096\nds_read_b128 v[60:63], v1 offset:5120\nds_read_b128 v[64:67], v1 offset:6144\n" \
"ds_read_b128 v[68:71], v1 offset:7168\n.Lvu_poll2%=:\ns_waitcnt vmcnt(0)\nv_smfmac_f32_16x16x64_bf16 v[48:51], v[80:83], a[208:215], v4\nds_write_b128 v0, v[80:83] offset:24576\nv_max3_u32 v14, v80, v82, v84\nv_smfmac_f32_16x16x64_bf16 v[52:55], v[80:83], a[224:231], v4\nds_write_b128 v0, v[84:87] offset:25600\n" \
"v_max3_u32 v14, v14, v86, v88\nv_smfmac_f32_16x16x64_bf16 v[48:51], v[84:87], a[240:247], v4\nds_write_b128 v0, v[88:91] offset:26624\nds_write_b128 v0, v[92:95] offset:27648\nv_max3_u32 v14, v14, v90, v92\nv_max_u32 v14, v14, v94\nv_smfmac_f32_16x16x64_bf16 v[52:55], v[84:87], a[248:255], v4\nv_cmp_eq_u32 vcc, -1, v14\n" \
"v_smfmac_f32_16x16x64_bf16 v[48:51], v[88:91], v[104:111], v4\ns_cbranch_vccnz .Lvu_re2%=\ns_waitcnt lgkmcnt(0)\ns_barrier\nds_read_b128 v[16:19], v0 offset:28672\nds_read_b128 v[20:23], v0 offset:29696\nv_smfmac_f32_16x16x64_bf16 v[52:55], v[88:91], v[56:63], v4\nds_read_b128 v[24:27], v0 offset:30720\n" \
"ds_read_b128 v[28:31], v0 offset:31744\nds_read_b32 v15, v15\nv_smfmac_f32_16x16x64_bf16 v[48:51], v[92:95], v[128:135], v4\nds_read_b128 v[32:35], v0 offset:16384\nds_read_b128 v[36:39], v0 offset:17408\nv_smfmac_f32_16x16x64_bf16 v[52:55], v[92:95], v[136:143], v4\ns_waitcnt lgkmcnt(6)\nds_read_b128 v[40:43], v0 offset:18432\n" \
"ds_read_b128 v[44:47], v0 offset:19456\nv_smfmac_f32_16x16x64_bf16 v[48:51], v[16:19], v[152:159], v4\nds_read_b128 v[80:83], v0 offset:20480\nds_read_b128 v[84:87], v0 offset:21504\nv_smfmac_f32_16x16x64_bf16 v[52:55], v[16:19], v[168:175], v4\ns_waitcnt lgkmcnt(9)\nds_read_b128 v[88:91], v0 offset:22528\nds_read_b128 v[92:95], v0 offset:23552\n" \
"v_mov_b64 v[16:17], 0\nv_smfmac_f32_16x16x64_bf16 v[48:51], v[20:23], v[184:191], v4\nv_mov_b64 v[18:19], 0\nv_smfmac_f32_16x16x64_bf16 v[52:55], v[20:23], v[192:199], v4\ns_waitcnt lgkmcnt(10)\nds_read_b128 v[56:59], v1 offset:0\nds_read_b128 v[60:63], v1 offset:1024\nv_mov_b64 v[20:21], 0\n" \
"v_smfmac_f32_16x16x64_bf16 v[48:51], v[24:27], v[208:215], v4\ns_waitcnt lgkmcnt(10)\nv_cmp_ge_i32 s[64:65], s62, v15\nv_smfmac_f32_16x16x64_bf16 v[52:55], v[24:27], v[64:71], v4\ns_cmp_eq_u64 s[64:65], 0\ns_cselect_b32 s61, s41, -2147483648\nv_mov_b64 v[22:23], 0\nv_smfmac_f32_16x16x64_bf16 v[48:51], v[28:31], v[232:239], v4\n" \
"ds_read_b128 v[64:67], v1 offset:2048\nds_read_b128 v[68:71], v1 offset:3072\nv_mov_b64 v[24:25], 0\nv_smfmac_f32_16x16x64_bf16 v[52:55], v[28:31], v[240:247], v4\ns_add_u32 m0, s57, 0\ns_add_u32 s58, s61, 0\ns_waitcnt lgkmcnt(11)\nbuffer_load_dwordx4 v0, s[36:39], s58 offen offset:0 sc1 lds\n" \
"v_smfmac_f32_16x16x64_bf16 v[48:51], v[32:35], a[0:7], v4\nv_mov_b64 v[26:27], 0\nv_smfmac_f32_16x16x64_bf16 v[52:55], v[32:35], a[16:23], v4\ns_waitcnt lgkmcnt(10)\nv_mov_b64 v[28:29], 0\nv_smfmac_f32_16x16x64_bf16 v[48:51], v[36:39], a[32:39], v4\nv_mov_b64 v[30:31], 0\nv_smfmac_f32_16x16x64_bf16 v[52:55], v[36:39], a[40:47], v4\n" \
"s_waitcnt lgkmcnt(9)\nbuffer_load_dwordx4 v0, s[36:39], s58 offen offset:1024 sc1 lds\nv_smfmac_f32_16x16x64_bf16 v[48:51], v[40:43], a[56:63], v4\ns_waitcnt lgkmcnt(2)\nv_mov_b64 v[32:33], 0\nv_smfmac_f32_16x16x64_bf16 v[52:55], v[40:43], v[56:63], v4\nv_mov_b64 v[34:35], 0\nv_smfmac_f32_16x16x64_bf16 v[48:51], v[44:47], a[80:87], v4\n" \
"v_mov_b64 v[36:37], 0\nv_smfmac_f32_16x16x64_bf16 v[52:55], v[44:47], a[88:95], v4\nds_read_b128 v[56:59], v1 offset:8192\nds_read_b128 v[60:63], v1 offset:9216\nv_mov_b64 v[38:39], 0\nv_smfmac_f32_16x16x64_bf16 v[48:51], v[80:83], a[104:111], v4\nbuffer_load_dwordx4 v0, s[36:39], s58 offen offset:2048 sc1 lds\nv_smfmac_f32_16x16x64_bf16 v[52:55], v[80:83], a[120:127], v4\n" \
"v_mov_b64 v[40:41], 0\nv_smfmac_f32_16x16x64_bf16 v[48:51], v[84:87], a[136:143], v4\nv_mov_b64 v[42:43], 0\nv_smfmac_f32_16x16x64_bf16 v[52:55], v[84:87], a[144:151], v4\nv_mov_b64 v[44:45], 0\nv_smfmac_f32_16x16x64_bf16 v[48:51], v[88:91], a[160:167], v4\ns_waitcnt lgkmcnt(2)\nbuffer_load_dwordx4 v0, s[36:39], s58 offen offset:3072 sc1 lds\n" \
"v_smfmac_f32_16x16x64_bf16 v[52:55], v[88:91], v[64:71], v4\nv_mov_b64 v[46:47], 0\nv_smfmac_f32_16x16x64_bf16 v[48:51], v[92:95], a[184:191], v4\nds_read_b128 v[64:67], v1 offset:10240\nds_read_b128 v[68:71], v1 offset:11264\nv_smfmac_f32_16x16x64_bf16 v[52:55], v[92:95], a[192:199], v4\ns_nop 3\nv_add_f32 v48, v48, v50\n" \
"v_add_f32 v49, v49, v51\ns_nop 1\nv_add_f32 v52, v52, v54\nv_add_f32 v53, v53, v55\nv_cvt_pk_bf16_f32 v14, v48, v52\nv_cvt_pk_bf16_f32 v15, v49, v53\nds_write2_b32 v5, v14, v15 offset0:0 offset1:4\ns_branch .Lvu_u%=\n" \
".Lvu_re2%=:\nv_max_u32 v14, v80, v82\nv_cmp_eq_u32 vcc, -1, v14\ns_cbranch_vccz .Lvu_sk1596%=\ns_mov_b64 exec, vcc\nbuffer_load_dwordx4 v[80:83], v3, s[36:39], s42 offen offset:0 sc1\ns_mov_b64 exec, -1\n.Lvu_sk1596%=:\n" \
"v_max_u32 v14, v84, v86\nv_cmp_eq_u32 vcc, -1, v14\ns_cbranch_vccz .Lvu_sk1603%=\ns_mov_b64 exec, vcc\nbuffer_load_dwordx4 v[84:87], v3, s[36:39], s42 offen offset:1024 sc1\ns_mov_b64 exec, -1\n.Lvu_sk1603%=:\nv_max_u32 v14, v88, v90\n" \
"v_cmp_eq_u32 vcc, -1, v14\ns_cbranch_vccz .Lvu_sk1610%=\ns_mov_b64 exec, vcc\nbuffer_load_dwordx4 v[88:91], v3, s[36:39], s42 offen offset:2048 sc1\ns_mov_b64 exec, -1\n.Lvu_sk1610%=:\nv_max_u32 v14, v92, v94\nv_cmp_eq_u32 vcc, -1, v14\n" \
"s_cbranch_vccz .Lvu_sk1617%=\ns_mov_b64 exec, vcc\nbuffer_load_dwordx4 v[92:95], v3, s[36:39], s42 offen offset:3072 sc1\ns_mov_b64 exec, -1\n.Lvu_sk1617%=:\nv_mov_b64 v[48:49], 0\nv_mov_b64 v[50:51], 0\nv_mov_b64 v[52:53], 0\n" \
"v_mov_b64 v[54:55], 0\ns_add_u32 s43, s43, 1\ns_cmp_lt_u32 s43, 262144\ns_cbranch_scc1 .Lvu_poll2%=\ns_branch .Lvu_abort%=\n.Lvu_c3%=:\nds_read_b128 v[56:59], v1 offset:6144\nds_read_b128 v[60:63], v1 offset:7168\n" \
"ds_read_b128 v[64:67], v1 offset:0\nds_read_b128 v[68:71], v1 offset:1024\n.Lvu_poll3%=:\ns_waitcnt vmcnt(0)\nv_smfmac_f32_16x16x64_bf16 v[48:51], v[80:83], v[152:159], v4\nds_write_b128 v0, v[80:83] offset:28672\nv_max3_u32 v14, v80, v82, v84\nv_smfmac_f32_16x16x64_bf16 v[52:55], v[80:83], v[168:175], v4\n" \
"ds_write_b128 v0, v[84:87] offset:29696\nv_max3_u32 v14, v14, v86, v88\nv_smfmac_f32_16x16x64_bf16 v[48:51], v[84:87], v[184:191], v4\nds_write_b128 v0, v[88:91] offset:30720\nds_write_b128 v0, v[92:95] offset:31744\nv_max3_u32 v14, v14, v90, v92\nv_max_u32 v14, v14, v94\nv_smfmac_f32_16x16x64_bf16 v[52:55], v[84:87], v[192:199], v4\n" \
"v_cmp_eq_u32 vcc, -1, v14\nv_smfmac_f32_16x16x64_bf16 v[48:51], v[88:91], v[208:215], v4\ns_cbranch_vccnz .Lvu_re3%=\ns_waitcnt lgkmcnt(0)\ns_barrier\nds_read_b128 v[16:19], v0 offset:16384\nds_read_b128 v[20:23], v0 offset:17408\nv_smfmac_f32_16x16x64_bf16 v[52:55], v[88:91], v[56:63], v4\n" \
"ds_read_b128 v[24:27], v0 offset:18432\nds_read_b128 v[28:31], v0 offset:19456\nds_read_b32 v15, v15\nv_smfmac_f32_16x16x64_bf16 v[48:51], v[92:95], v[232:239], v4\nds_read_b128 v[32:35], v0 offset:20480\nds_read_b128 v[36:39], v0 offset:21504\nv_smfmac_f32_16x16x64_bf16 v[52:55], v[92:95], v[240:247], v4\ns_waitcnt lgkmcnt(6)\n" \
"ds_read_b128 v[40:43], v0 offset:22528\nds_read_b128 v[44:47], v0 offset:23552\nv_smfmac_f32_16x16x64_bf16 v[48:51], v[16:19], a[0:7], v4\nds_read_b128 v[80:83], v0 offset:24576\nds_read_b128 v[84:87], v0 offset:25600\nv_smfmac_f32_16x16x64_bf16 v[52:55], v[16:19], a[16:23], v4\ns_waitcnt lgkmcnt(9)\nds_read_b128 v[88:91], v0 offset:26624\n" \
"ds_read_b128 v[92:95], v0 offset:27648\nv_mov_b64 v[16:17], 0\nv_smfmac_f32_16x16x64_bf16 v[48:51], v[20:23], a[32:39], v4\nv_mov_b64 v[18:19], 0\nv_smfmac_f32_16x16x64_bf16 v[52:55], v[20:23], a[40:47], v4\ns_waitcnt lgkmcnt(10)\nds_read_b128 v[56:59], v1 offset:2048\nds_read_b128 v[60:63], v1 offset:3072\n" \
"v_mov_b64 v[20:21], 0\nv_smfmac_f32_16x16x64_bf16 v[48:51], v[24:27], a[56:63], v4\ns_waitcnt lgkmcnt(10)\nv_cmp_ge_i32 s[64:65], s62, v15\nv_smfmac_f32_16x16x64_bf16 v[52:55], v[24:27], v[64:71], v4\ns_cmp_eq_u64 s[64:65], 0\ns_cselect_b32 s61, s41, -2147483648\nv_mov_b64 v[22:23], 0\n" \
"v_smfmac_f32_16x16x64_bf16 v[48:51], v[28:31], a[80:87], v4\nds_read_b128 v[64:67], v1 offset:4096\nds_read_b128 v[68:71], v1 offset:5120\nv_mov_b64 v[24:25], 0\nv_smfmac_f32_16x16x64_bf16 v[52:55], v[28:31], a[88:95], v4\ns_add_u32 m0, s57, 0\ns_add_u32 s58, s61, 0\ns_waitcnt lgkmcnt(11)\n" \
"buffer_load_dwordx4 v0, s[36:39], s58 offen offset:0 sc1 lds\nv_smfmac_f32_16x16x64_bf16 v[48:51], v[32:35], a[104:111], v4\nv_mov_b64 v[26:27], 0\nv_smfmac_f32_16x16x64_bf16 v[52:55], v[32:35], a[120:127], v4\ns_waitcnt lgkmcnt(10)\nv_mov_b64 v[28:29], 0\nv_smfmac_f32_16x16x64_bf16 v[48:51], v[36:39], a[136:143], v4\nv_mov_b64 v[30:31], 0\n" \
"v_smfmac_f32_16x16x64_bf16 v[52:55], v[36:39], a[144:151], v4\ns_waitcnt lgkmcnt(9)\nbuffer_load_dwordx4 v0, s[36:39], s58 offen offset:1024 sc1 lds\nv_smfmac_f32_16x16x64_bf16 v[48:51], v[40:43], a[160:167], v4\ns_waitcnt lgkmcnt(2)\nv_mov_b64 v[32:33], 0\nv_smfmac_f32_16x16x64_bf16 v[52:55], v[40:43], v[56:63], v4\nv_mov_b64 v[34:35], 0\n" \
"v_smfmac_f32_16x16x64_bf16 v[48:51], v[44:47], a[184:191], v4\nv_mov_b64 v[36:37], 0\nv_smfmac_f32_16x16x64_bf16 v[52:55], v[44:47], a[192:199], v4\nds_read_b128 v[56:59], v1 offset:8192\nds_read_b128 v[60:63], v1 offset:9216\nv_mov_b64 v[38:39], 0\nv_smfmac_f32_16x16x64_bf16 v[48:51], v[80:83], a[208:215], v4\nbuffer_load_dwordx4 v0, s[36:39], s58 offen offset:2048 sc1 lds\n" \
"v_smfmac_f32_16x16x64_bf16 v[52:55], v[80:83], a[224:231], v4\nv_mov_b64 v[40:41], 0\nv_smfmac_f32_16x16x64_bf16 v[48:51], v[84:87], a[240:247], v4\nv_mov_b64 v[42:43], 0\nv_smfmac_f32_16x16x64_bf16 v[52:55], v[84:87], a[248:255], v4\nv_mov_b64 v[44:45], 0\nv_smfmac_f32_16x16x64_bf16 v[48:51], v[88:91], v[104:111], v4\ns_waitcnt lgkmcnt(2)\n" \
"buffer_load_dwordx4 v0, s[36:39], s58 offen offset:3072 sc1 lds\nv_smfmac_f32_16x16x64_bf16 v[52:55], v[88:91], v[64:71], v4\nv_mov_b64 v[46:47], 0\nv_smfmac_f32_16x16x64_bf16 v[48:51], v[92:95], v[128:135], v4\nds_read_b128 v[64:67], v1 offset:10240\nds_read_b128 v[68:71], v1 offset:11264\nv_smfmac_f32_16x16x64_bf16 v[52:55], v[92:95], v[136:143], v4\ns_nop 3\n" \
"v_add_f32 v48, v48, v50\nv_add_f32 v49, v49, v51\ns_nop 1\nv_add_f32 v52, v52, v54\nv_add_f32 v53, v53, v55\nv_cvt_pk_bf16_f32 v14, v48, v52\nv_cvt_pk_bf16_f32 v15, v49, v53\nds_write2_b32 v5, v14, v15 offset0:0 offset1:4\n" \
".Lvu_u%=:\nv_mov_b64 v[48:49], 0\nv_mov_b64 v[50:51], 0\nv_mov_b64 v[52:53], 0\nv_mov_b64 v[54:55], 0\ns_mov_b32 s59, 0x21000\ns_waitcnt lgkmcnt(0)\ns_barrier\n" \
"ds_read_b128 v[80:83], v11 offset:57344\nds_read_b128 v[84:87], v11 offset:58368\nds_read_b128 v[88:91], v11 offset:59392\nds_read_b128 v[92:95], v11 offset:60416\ns_add_u32 m0, s57, 32768\ns_add_u32 s60, s61, 32768\nbuffer_load_dwordx4 v0, s[36:39], s60 offen offset:0 sc1 lds\ns_waitcnt lgkmcnt(3)\n" \
"v_smfmac_f32_16x16x64_bf16 v[16:19], v[80:83], a[8:15], v10\ns_waitcnt lgkmcnt(2)\nv_smfmac_f32_16x16x64_bf16 v[16:19], v[84:87], a[24:31], v10\ns_waitcnt lgkmcnt(1)\nbuffer_load_dwordx4 v0, s[36:39], s60 offen offset:1024 sc1 lds\nv_smfmac_f32_16x16x64_bf16 v[16:19], v[88:91], v[56:63], v10\ns_waitcnt lgkmcnt(0)\nds_read_b128 v[56:59], v1 offset:12288\n" \
"v_smfmac_f32_16x16x64_bf16 v[16:19], v[92:95], a[48:55], v10\nds_read_b128 v[60:63], v1 offset:13312\nv_smfmac_f32_16x16x64_bf16 v[20:23], v[80:83], a[64:71], v10\nv_smfmac_f32_16x16x64_bf16 v[20:23], v[84:87], a[72:79], v10\nbuffer_load_dwordx4 v0, s[36:39], s60 offen offset:2048 sc1 lds\nv_smfmac_f32_16x16x64_bf16 v[20:23], v[88:91], v[64:71], v10\nds_read_b128 v[64:67], v1 offset:14336\nv_smfmac_f32_16x16x64_bf16 v[20:23], v[92:95], a[96:103], v10\n" \
"ds_read_b128 v[68:71], v1 offset:15360\nv_cvt_pk_bf16_f32 v72, v16, v17\nv_cvt_pk_bf16_f32 v73, v18, v19\nv_smfmac_f32_16x16x64_bf16 v[24:27], v[80:83], a[112:119], v10\nbuffer_load_dwordx4 v0, s[36:39], s60 offen offset:3072 sc1 lds\nv_and_or_b32 v72, v72, -2, v12\nv_smfmac_f32_16x16x64_bf16 v[24:27], v[84:87], a[128:135], v10\ns_waitcnt lgkmcnt(2)\n" \
"v_cvt_pk_bf16_f32 v74, v20, v21\nv_smfmac_f32_16x16x64_bf16 v[24:27], v[88:91], v[56:63], v10\nds_read_b128 v[56:59], v1 offset:16384\nv_cvt_pk_bf16_f32 v75, v22, v23\nv_smfmac_f32_16x16x64_bf16 v[24:27], v[92:95], a[152:159], v10\nds_read_b128 v[60:63], v1 offset:17408\nv_and_or_b32 v74, v74, -2, v12\nv_smfmac_f32_16x16x64_bf16 v[28:31], v[80:83], a[168:175], v10\n" \
"v_permlane16_swap_b32 v73, v75\nv_smfmac_f32_16x16x64_bf16 v[28:31], v[84:87], a[176:183], v10\ns_waitcnt lgkmcnt(2)\nv_permlane16_swap_b32 v72, v74\nv_smfmac_f32_16x16x64_bf16 v[28:31], v[88:91], v[64:71], v10\nds_read_b128 v[64:67], v1 offset:18432\nv_smfmac_f32_16x16x64_bf16 v[28:31], v[92:95], a[200:207], v10\ns_mov_b32 s44, 0\n" \
"ds_read_b128 v[68:71], v1 offset:19456\nbuffer_store_dwordx4 v[72:75], v6, s[36:39], s44 offen sc1\nv_cvt_pk_bf16_f32 v76, v24, v25\nv_cvt_pk_bf16_f32 v77, v26, v27\nv_smfmac_f32_16x16x64_bf16 v[32:35], v[80:83], a[216:223], v10\nds_read_b128 v[16:19], v11 offset:49152\nv_and_or_b32 v76, v76, -2, v12\nv_smfmac_f32_16x16x64_bf16 v[32:35], v[84:87], a[232:239], v10\n" \
"s_waitcnt lgkmcnt(3)\nds_read_b128 v[20:23], v11 offset:50176\nv_cvt_pk_bf16_f32 v78, v28, v29\nv_smfmac_f32_16x16x64_bf16 v[32:35], v[88:91], v[56:63], v10\nds_read_b128 v[56:59], v1 offset:20480\nv_cvt_pk_bf16_f32 v79, v30, v31\nv_smfmac_f32_16x16x64_bf16 v[32:35], v[92:95], v[96:103], v10\nds_read_b128 v[60:63], v1 offset:21504\n" \
"v_and_or_b32 v78, v78, -2, v12\nv_smfmac_f32_16x16x64_bf16 v[36:39], v[80:83], v[112:119], v10\nds_read_b128 v[24:27], v11 offset:51200\nv_permlane16_swap_b32 v77, v79\nv_smfmac_f32_16x16x64_bf16 v[36:39], v[84:87], v[120:127], v10\ns_waitcnt lgkmcnt(5)\nds_read_b128 v[28:31], v11 offset:52224\nv_permlane16_swap_b32 v76, v78\n" \
"v_smfmac_f32_16x16x64_bf16 v[36:39], v[88:91], v[64:71], v10\nds_read_b128 v[64:67], v1 offset:22528\nv_smfmac_f32_16x16x64_bf16 v[36:39], v[92:95], v[144:151], v10\ns_mov_b32 s44, 131072\nds_read_b128 v[68:71], v1 offset:23552\nbuffer_store_dwordx4 v[76:79], v6, s[36:39], s44 offen sc1\nv_cvt_pk_bf16_f32 v72, v32, v33\nv_cvt_pk_bf16_f32 v73, v34, v35\n" \
"v_smfmac_f32_16x16x64_bf16 v[40:43], v[80:83], v[160:167], v10\nds_read_b128 v[32:35], v11 offset:53248\nv_and_or_b32 v72, v72, -2, v12\nv_smfmac_f32_16x16x64_bf16 v[40:43], v[84:87], v[176:183], v10\ns_waitcnt lgkmcnt(5)\nv_cvt_pk_bf16_f32 v74, v36, v37\nv_smfmac_f32_16x16x64_bf16 v[40:43], v[88:91], v[56:63], v10\nds_read_b128 v[56:59], v1 offset:8192\n" \
"v_cvt_pk_bf16_f32 v75, v38, v39\nv_smfmac_f32_16x16x64_bf16 v[40:43], v[92:95], v[200:207], v10\nds_read_b128 v[60:63], v1 offset:9216\nv_and_or_b32 v74, v74, -2, v12\nv_smfmac_f32_16x16x64_bf16 v[44:47], v[80:83], v[216:223], v10\nds_read_b128 v[36:39], v11 offset:54272\nv_permlane16_swap_b32 v73, v75\nv_smfmac_f32_16x16x64_bf16 v[44:47], v[84:87], v[224:231], v10\n" \
"s_waitcnt lgkmcnt(4)\nv_permlane16_swap_b32 v72, v74\nv_smfmac_f32_16x16x64_bf16 v[44:47], v[88:91], v[64:71], v10\nds_read_b128 v[64:67], v1 offset:10240\nds_read_b128 v[88:91], v11 offset:55296\nv_smfmac_f32_16x16x64_bf16 v[44:47], v[92:95], v[248:255], v10\ns_mov_b32 s44, 4096\ns_cmp_ge_i32 s40, 63\n" \
"ds_read_b128 v[68:71], v1 offset:11264\nds_read_b128 v[92:95], v11 offset:56320\nbuffer_store_dwordx4 v[72:75], v6, s[36:39], s44 offen sc1\nv_cvt_pk_bf16_f32 v76, v40, v41\nv_cvt_pk_bf16_f32 v77, v42, v43\ns_cbranch_scc1 .Lvu_last%=\n.Lvu_y%=:\nv_smfmac_f32_16x16x64_bf16 v[48:51], v[16:19], a[8:15], v10\n" \
"s_add_u32 s40, s40, 1\nv_and_or_b32 v76, v76, -2, v12\nv_smfmac_f32_16x16x64_bf16 v[52:55], v[20:23], a[8:15], v10\ns_and_b32 s44, s40, 1\ns_lshl_b32 s55, s44, 22\nv_cvt_pk_bf16_f32 v78, v44, v45\nv_smfmac_f32_16x16x64_bf16 v[48:51], v[24:27], a[24:31], v10\ns_bfe_u32 s45, s40, 0x10001\n" \
"v_cvt_pk_bf16_f32 v79, v46, v47\nv_smfmac_f32_16x16x64_bf16 v[52:55], v[28:31], a[24:31], v10\ns_waitcnt lgkmcnt(5)\nv_and_or_b32 v78, v78, -2, v12\nv_smfmac_f32_16x16x64_bf16 v[48:51], v[32:35], v[56:63], v10\ns_waitcnt lgkmcnt(4)\nv_permlane16_swap_b32 v77, v79\nv_smfmac_f32_16x16x64_bf16 v[52:55], v[36:39], v[56:63], v10\n" \
"s_waitcnt lgkmcnt(2)\nv_permlane16_swap_b32 v76, v78\nv_smfmac_f32_16x16x64_bf16 v[48:51], v[88:91], a[48:55], v10\ns_waitcnt lgkmcnt(0)\nv_mov_b64 v[80:81], 0\nv_smfmac_f32_16x16x64_bf16 v[52:55], v[92:95], a[48:55], v10\nbuffer_store_dwordx4 v[76:79], v6, s[36:39], s59 offen sc1\nv_mov_b64 v[82:83], 0\n" \
"v_mov_b64 v[84:85], 0\nv_mov_b64 v[86:87], 0\nv_mov_b32 v13, s45\nv_smfmac_f32_16x16x64_bf16 v[80:83], v[16:19], a[64:71], v10\nds_read_b128 v[56:59], v1 offset:12288\nv_cvt_pk_bf16_f32 v72, v48, v49\nv_smfmac_f32_16x16x64_bf16 v[84:87], v[20:23], a[64:71], v10\nds_read_b128 v[60:63], v1 offset:13312\n" \
"v_cvt_pk_bf16_f32 v73, v50, v51\nv_smfmac_f32_16x16x64_bf16 v[80:83], v[24:27], a[72:79], v10\nv_cvt_pk_bf16_f32 v74, v52, v53\nv_smfmac_f32_16x16x64_bf16 v[84:87], v[28:31], a[72:79], v10\ns_add_u32 s60, s55, 0\nv_cvt_pk_bf16_f32 v75, v54, v55\nv_smfmac_f32_16x16x64_bf16 v[80:83], v[32:35], v[64:71], v10\nv_and_or_b32 v72, v72, -2, v13\n" \
"v_smfmac_f32_16x16x64_bf16 v[84:87], v[36:39], v[64:71], v10\ns_and_b32 s42, s40, 3\ns_lshl_b32 s42, s42, 15\nv_and_or_b32 v74, v74, -2, v13\nv_smfmac_f32_16x16x64_bf16 v[80:83], v[88:91], a[96:103], v10\nbuffer_store_dwordx4 v[72:75], v7, s[36:39], s60 offen offset:0 sc1\nv_mov_b64 v[48:49], 0\nv_mov_b64 v[50:51], 0\n" \
"v_mov_b64 v[52:53], 0\nv_mov_b64 v[54:55], 0\nv_smfmac_f32_16x16x64_bf16 v[84:87], v[92:95], a[96:103], v10\nv_mov_b32 v12, s44\nv_smfmac_f32_16x16x64_bf16 v[48:51], v[16:19], a[112:119], v10\nds_read_b128 v[64:67], v1 offset:14336\nv_cvt_pk_bf16_f32 v76, v80, v81\nv_smfmac_f32_16x16x64_bf16 v[52:55], v[20:23], a[112:119], v10\n" \
"ds_read_b128 v[68:71], v1 offset:15360\nv_cvt_pk_bf16_f32 v77, v82, v83\nv_smfmac_f32_16x16x64_bf16 v[48:51], v[24:27], a[128:135], v10\nv_cvt_pk_bf16_f32 v78, v84, v85\nv_smfmac_f32_16x16x64_bf16 v[52:55], v[28:31], a[128:135], v10\ns_add_u32 s60, s55, 4096\ns_waitcnt lgkmcnt(2)\nv_cvt_pk_bf16_f32 v79, v86, v87\n" \
"v_smfmac_f32_16x16x64_bf16 v[48:51], v[32:35], v[56:63], v10\nv_and_or_b32 v76, v76, -2, v13\nv_smfmac_f32_16x16x64_bf16 v[52:55], v[36:39], v[56:63], v10\nv_and_or_b32 v78, v78, -2, v13\nv_smfmac_f32_16x16x64_bf16 v[48:51], v[88:91], a[152:159], v10\nbuffer_store_dwordx4 v[76:79], v7, s[36:39], s60 offen offset:0 sc1\nv_mov_b64 v[80:81], 0\nv_mov_b64 v[82:83], 0\n" \
"v_mov_b64 v[84:85], 0\nv_mov_b64 v[86:87], 0\nv_smfmac_f32_16x16x64_bf16 v[52:55], v[92:95], a[152:159], v10\ns_waitcnt vmcnt(6)\ns_barrier\nv_smfmac_f32_16x16x64_bf16 v[80:83], v[16:19], a[168:175], v10\nds_read_b128 v[56:59], v1 offset:16384\nv_cvt_pk_bf16_f32 v72, v48, v49\n" \
"v_smfmac_f32_16x16x64_bf16 v[84:87], v[20:23], a[168:175], v10\nds_read_b128 v[60:63], v1 offset:17408\nv_cvt_pk_bf16_f32 v73, v50, v51\nv_smfmac_f32_16x16x64_bf16 v[80:83], v[24:27], a[176:183], v10\ns_cmp_lt_u32 s40, 2\ns_cselect_b32 s54, %[fy], -2147483648\nv_cvt_pk_bf16_f32 v74, v52, v53\nv_smfmac_f32_16x16x64_bf16 v[84:87], v[28:31], a[176:183], v10\n" \
"s_add_u32 s60, s55, 16384\ns_waitcnt lgkmcnt(2)\nv_cvt_pk_bf16_f32 v75, v54, v55\nv_smfmac_f32_16x16x64_bf16 v[80:83], v[32:35], v[64:71], v10\nv_and_or_b32 v72, v72, -2, v13\nv_smfmac_f32_16x16x64_bf16 v[84:87], v[36:39], v[64:71], v10\nv_and_or_b32 v74, v74, -2, v13\nv_smfmac_f32_16x16x64_bf16 v[80:83], v[88:91], a[200:207], v10\n" \
"buffer_store_dwordx4 v[72:75], v7, s[36:39], s60 offen offset:0 sc1\nv_mov_b64 v[48:49], 0\nv_mov_b64 v[50:51], 0\nv_mov_b64 v[52:53], 0\nv_mov_b64 v[54:55], 0\nv_smfmac_f32_16x16x64_bf16 v[84:87], v[92:95], a[200:207], v10\nv_smfmac_f32_16x16x64_bf16 v[48:51], v[16:19], a[216:223], v10\nds_read_b128 v[64:67], v1 offset:18432\n" \
"v_cvt_pk_bf16_f32 v76, v80, v81\nv_smfmac_f32_16x16x64_bf16 v[52:55], v[20:23], a[216:223], v10\nds_read_b128 v[68:71], v1 offset:19456\nv_cvt_pk_bf16_f32 v77, v82, v83\nv_smfmac_f32_16x16x64_bf16 v[48:51], v[24:27], a[232:239], v10\nv_cvt_pk_bf16_f32 v78, v84, v85\nv_smfmac_f32_16x16x64_bf16 v[52:55], v[28:31], a[232:239], v10\ns_add_u32 s60, s55, 20480\n" \
"s_waitcnt lgkmcnt(2)\nv_cvt_pk_bf16_f32 v79, v86, v87\nv_smfmac_f32_16x16x64_bf16 v[48:51], v[32:35], v[56:63], v10\nv_and_or_b32 v76, v76, -2, v13\nv_smfmac_f32_16x16x64_bf16 v[52:55], v[36:39], v[56:63], v10\nv_and_or_b32 v78, v78, -2, v13\nv_smfmac_f32_16x16x64_bf16 v[48:51], v[88:91], v[96:103], v10\nbuffer_store_dwordx4 v[76:79], v7, s[36:39], s60 offen offset:0 sc1\n" \
"v_mov_b64 v[80:81], 0\nv_mov_b64 v[82:83], 0\nv_mov_b64 v[84:85], 0\nv_mov_b64 v[86:87], 0\nv_smfmac_f32_16x16x64_bf16 v[52:55], v[92:95], v[96:103], v10\nv_smfmac_f32_16x16x64_bf16 v[80:83], v[16:19], v[112:119], v10\nds_read_b128 v[56:59], v1 offset:20480\nv_cvt_pk_bf16_f32 v72, v48, v49\n" \
"v_smfmac_f32_16x16x64_bf16 v[84:87], v[20:23], v[112:119], v10\nds_read_b128 v[60:63], v1 offset:21504\nv_cvt_pk_bf16_f32 v73, v50, v51\nv_smfmac_f32_16x16x64_bf16 v[80:83], v[24:27], v[120:127], v10\nv_cvt_pk_bf16_f32 v74, v52, v53\nv_smfmac_f32_16x16x64_bf16 v[84:87], v[28:31], v[120:127], v10\ns_add_u32 s60, s55, 0\ns_waitcnt lgkmcnt(2)\n" \
"v_cvt_pk_bf16_f32 v75, v54, v55\nv_smfmac_f32_16x16x64_bf16 v[80:83], v[32:35], v[64:71], v10\nv_and_or_b32 v72, v72, -2, v13\nv_smfmac_f32_16x16x64_bf16 v[84:87], v[36:39], v[64:71], v10\nv_and_or_b32 v74, v74, -2, v13\nv_smfmac_f32_16x16x64_bf16 v[80:83], v[88:91], v[144:151], v10\nbuffer_store_dwordx4 v[72:75], v7, s[36:39], s60 offen offset:2048 sc1\nv_mov_b64 v[48:49], 0\n" \
"v_mov_b64 v[50:51], 0\nv_mov_b64 v[52:53], 0\nv_mov_b64 v[54:55], 0\nv_smfmac_f32_16x16x64_bf16 v[84:87], v[92:95], v[144:151], v10\nv_smfmac_f32_16x16x64_bf16 v[48:51], v[16:19], v[160:167], v10\nds_read_b128 v[64:67], v1 offset:22528\nv_cvt_pk_bf16_f32 v76, v80, v81\nv_smfmac_f32_16x16x64_bf16 v[52:55], v[20:23], v[160:167], v10\n" \
"ds_read_b128 v[68:71], v1 offset:23552\nv_cvt_pk_bf16_f32 v77, v82, v83\nv_smfmac_f32_16x16x64_bf16 v[48:51], v[24:27], v[176:183], v10\nds_read_b128 v[40:43], v11 offset:49280\nv_cvt_pk_bf16_f32 v78, v84, v85\nv_smfmac_f32_16x16x64_bf16 v[52:55], v[28:31], v[176:183], v10\ns_add_u32 s60, s55, 4096\ns_waitcnt lgkmcnt(3)\n" \
"v_cvt_pk_bf16_f32 v79, v86, v87\nv_smfmac_f32_16x16x64_bf16 v[48:51], v[32:35], v[56:63], v10\nds_read_b128 v[44:47], v11 offset:50304\nv_and_or_b32 v76, v76, -2, v13\nv_smfmac_f32_16x16x64_bf16 v[52:55], v[36:39], v[56:63], v10\nv_and_or_b32 v78, v78, -2, v13\nv_smfmac_f32_16x16x64_bf16 v[48:51], v[88:91], v[200:207], v10\nbuffer_store_dwordx4 v[76:79], v7, s[36:39], s60 offen offset:2048 sc1\n" \
"v_mov_b64 v[80:81], 0\nv_mov_b64 v[82:83], 0\nv_mov_b64 v[84:85], 0\nv_mov_b64 v[86:87], 0\nv_smfmac_f32_16x16x64_bf16 v[52:55], v[92:95], v[200:207], v10\nv_smfmac_f32_16x16x64_bf16 v[80:83], v[16:19], v[216:223], v10\nds_read_b128 v[56:59], v1 offset:8192\nds_read_b128 v[16:19], v11 offset:51328\n" \
"v_cvt_pk_bf16_f32 v72, v48, v49\nv_smfmac_f32_16x16x64_bf16 v[84:87], v[20:23], v[216:223], v10\nds_read_b128 v[60:63], v1 offset:9216\nds_read_b128 v[20:23], v11 offset:52352\nv_cvt_pk_bf16_f32 v73, v50, v51\nv_smfmac_f32_16x16x64_bf16 v[80:83], v[24:27], v[224:231], v10\nds_read_b128 v[24:27], v11 offset:53376\nv_cvt_pk_bf16_f32 v74, v52, v53\n" \
"v_smfmac_f32_16x16x64_bf16 v[84:87], v[28:31], v[224:231], v10\ns_add_u32 s60, s55, 16384\ns_waitcnt lgkmcnt(7)\nds_read_b128 v[28:31], v11 offset:54400\nv_cvt_pk_bf16_f32 v75, v54, v55\nv_smfmac_f32_16x16x64_bf16 v[80:83], v[32:35], v[64:71], v10\nds_read_b128 v[32:35], v11 offset:55424\nv_and_or_b32 v72, v72, -2, v13\n" \
"v_smfmac_f32_16x16x64_bf16 v[84:87], v[36:39], v[64:71], v10\nds_read_b128 v[36:39], v11 offset:56448\nv_and_or_b32 v74, v74, -2, v13\nv_smfmac_f32_16x16x64_bf16 v[80:83], v[88:91], v[248:255], v10\nbuffer_store_dwordx4 v[72:75], v7, s[36:39], s60 offen offset:2048 sc1\nv_mov_b64 v[48:49], 0\nv_mov_b64 v[50:51], 0\nv_mov_b64 v[52:53], 0\n" \
"v_mov_b64 v[54:55], 0\nv_smfmac_f32_16x16x64_bf16 v[84:87], v[92:95], v[248:255], v10\ns_waitcnt lgkmcnt(9)\nv_smfmac_f32_16x16x64_bf16 v[48:51], v[40:43], a[8:15], v10\ns_waitcnt lgkmcnt(8)\nds_read_b128 v[64:67], v1 offset:10240\nv_cvt_pk_bf16_f32 v76, v80, v81\nv_smfmac_f32_16x16x64_bf16 v[52:55], v[44:47], a[8:15], v10\n" \
"s_waitcnt lgkmcnt(7)\nds_read_b128 v[68:71], v1 offset:11264\nv_cvt_pk_bf16_f32 v77, v82, v83\nv_smfmac_f32_16x16x64_bf16 v[48:51], v[16:19], a[24:31], v10\ns_waitcnt lgkmcnt(6)\nv_cvt_pk_bf16_f32 v78, v84, v85\nv_smfmac_f32_16x16x64_bf16 v[52:55], v[20:23], a[24:31], v10\ns_add_u32 s60, s55, 20480\n" \
"s_waitcnt lgkmcnt(5)\nv_cvt_pk_bf16_f32 v79, v86, v87\nv_smfmac_f32_16x16x64_bf16 v[48:51], v[24:27], v[56:63], v10\ns_waitcnt lgkmcnt(4)\nv_and_or_b32 v76, v76, -2, v13\nv_smfmac_f32_16x16x64_bf16 v[52:55], v[28:31], v[56:63], v10\ns_waitcnt lgkmcnt(3)\nv_and_or_b32 v78, v78, -2, v13\n" \
"v_smfmac_f32_16x16x64_bf16 v[48:51], v[32:35], a[48:55], v10\ns_waitcnt lgkmcnt(2)\nbuffer_store_dwordx4 v[76:79], v7, s[36:39], s60 offen offset:2048 sc1\nv_mov_b64 v[80:81], 0\nv_mov_b64 v[82:83], 0\nv_mov_b64 v[84:85], 0\nv_mov_b64 v[86:87], 0\nv_smfmac_f32_16x16x64_bf16 v[52:55], v[36:39], a[48:55], v10\n" \
"v_smfmac_f32_16x16x64_bf16 v[80:83], v[40:43], a[64:71], v10\nds_read_b128 v[56:59], v1 offset:12288\nv_cvt_pk_bf16_f32 v72, v48, v49\nv_smfmac_f32_16x16x64_bf16 v[84:87], v[44:47], a[64:71], v10\nds_read_b128 v[60:63], v1 offset:13312\nv_cvt_pk_bf16_f32 v73, v50, v51\nv_smfmac_f32_16x16x64_bf16 v[80:83], v[16:19], a[72:79], v10\nv_cvt_pk_bf16_f32 v74, v52, v53\n" \
"v_smfmac_f32_16x16x64_bf16 v[84:87], v[20:23], a[72:79], v10\ns_add_u32 s60, s55, 0\ns_waitcnt lgkmcnt(2)\nv_cvt_pk_bf16_f32 v75, v54, v55\nv_smfmac_f32_16x16x64_bf16 v[80:83], v[24:27], v[64:71], v10\nv_and_or_b32 v72, v72, -2, v13\nv_smfmac_f32_16x16x64_bf16 v[84:87], v[28:31], v[64:71], v10\nv_and_or_b32 v74, v74, -2, v13\n" \
"v_smfmac_f32_16x16x64_bf16 v[80:83], v[32:35], a[96:103], v10\nbuffer_store_dwordx4 v[72:75], v7, s[36:39], s60 offen offset:512 sc1\nv_mov_b64 v[48:49], 0\nv_mov_b64 v[50:51], 0\nv_mov_b64 v[52:53], 0\nv_mov_b64 v[54:55], 0\nv_smfmac_f32_16x16x64_bf16 v[84:87], v[36:39], a[96:103], v10\nv_smfmac_f32_16x16x64_bf16 v[48:51], v[40:43], a[112:119], v10\n" \
"ds_read_b128 v[64:67], v1 offset:14336\nv_cvt_pk_bf16_f32 v76, v80, v81\nv_smfmac_f32_16x16x64_bf16 v[52:55], v[44:47], a[112:119], v10\nds_read_b128 v[68:71], v1 offset:15360\nv_cvt_pk_bf16_f32 v77, v82, v83\nv_smfmac_f32_16x16x64_bf16 v[48:51], v[16:19], a[128:135], v10\nv_cvt_pk_bf16_f32 v78, v84, v85\nv_smfmac_f32_16x16x64_bf16 v[52:55], v[20:23], a[128:135], v10\n" \
"s_add_u32 s60, s55, 4096\ns_waitcnt lgkmcnt(2)\nv_cvt_pk_bf16_f32 v79, v86, v87\nv_smfmac_f32_16x16x64_bf16 v[48:51], v[24:27], v[56:63], v10\nv_and_or_b32 v76, v76, -2, v13\nv_smfmac_f32_16x16x64_bf16 v[52:55], v[28:31], v[56:63], v10\nv_and_or_b32 v78, v78, -2, v13\nv_smfmac_f32_16x16x64_bf16 v[48:51], v[32:35], a[152:159], v10\n" \
"buffer_store_dwordx4 v[76:79], v7, s[36:39], s60 offen offset:512 sc1\nv_mov_b64 v[80:81], 0\nv_mov_b64 v[82:83], 0\nv_mov_b64 v[84:85], 0\nv_mov_b64 v[86:87], 0\nv_smfmac_f32_16x16x64_bf16 v[52:55], v[36:39], a[152:159], v10\nv_smfmac_f32_16x16x64_bf16 v[80:83], v[40:43], a[168:175], v10\nds_read_b128 v[56:59], v1 offset:16384\n" \
"v_cvt_pk_bf16_f32 v72, v48, v49\nv_smfmac_f32_16x16x64_bf16 v[84:87], v[44:47], a[168:175], v10\nds_read_b128 v[60:63], v1 offset:17408\nv_cvt_pk_bf16_f32 v73, v50, v51\nv_smfmac_f32_16x16x64_bf16 v[80:83], v[16:19], a[176:183], v10\nv_cvt_pk_bf16_f32 v74, v52, v53\nv_smfmac_f32_16x16x64_bf16 v[84:87], v[20:23], a[176:183], v10\ns_add_u32 s60, s55, 16384\n" \
"s_waitcnt lgkmcnt(2)\nv_cvt_pk_bf16_f32 v75, v54, v55\nv_smfmac_f32_16x16x64_bf16 v[80:83], v[24:27], v[64:71], v10\nv_and_or_b32 v72, v72, -2, v13\nv_smfmac_f32_16x16x64_bf16 v[84:87], v[28:31], v[64:71], v10\nv_and_or_b32 v74, v74, -2, v13\nv_smfmac_f32_16x16x64_bf16 v[80:83], v[32:35], a[200:207], v10\nbuffer_store_dwordx4 v[72:75], v7, s[36:39], s60 offen offset:512 sc1\n" \
"v_mov_b64 v[48:49], 0\nv_mov_b64 v[50:51], 0\nv_mov_b64 v[52:53], 0\nv_mov_b64 v[54:55], 0\nv_smfmac_f32_16x16x64_bf16 v[84:87], v[36:39], a[200:207], v10\nv_smfmac_f32_16x16x64_bf16 v[48:51], v[40:43], a[216:223], v10\nds_read_b128 v[64:67], v1 offset:18432\nv_cvt_pk_bf16_f32 v76, v80, v81\n" \
"v_smfmac_f32_16x16x64_bf16 v[52:55], v[44:47], a[216:223], v10\nds_read_b128 v[68:71], v1 offset:19456\nv_cvt_pk_bf16_f32 v77, v82, v83\nv_smfmac_f32_16x16x64_bf16 v[48:51], v[16:19], a[232:239], v10\nv_cvt_pk_bf16_f32 v78, v84, v85\nv_smfmac_f32_16x16x64_bf16 v[52:55], v[20:23], a[232:239], v10\ns_add_u32 s60, s55, 20480\ns_waitcnt lgkmcnt(2)\n" \
"v_cvt_pk_bf16_f32 v79, v86, v87\nv_smfmac_f32_16x16x64_bf16 v[48:51], v[24:27], v[56:63], v10\nv_and_or_b32 v76, v76, -2, v13\nv_smfmac_f32_16x16x64_bf16 v[52:55], v[28:31], v[56:63], v10\nv_and_or_b32 v78, v78, -2, v13\nv_smfmac_f32_16x16x64_bf16 v[48:51], v[32:35], v[96:103], v10\nbuffer_store_dwordx4 v[76:79], v7, s[36:39], s60 offen offset:512 sc1\nv_mov_b64 v[80:81], 0\n" \
"v_mov_b64 v[82:83], 0\nv_mov_b64 v[84:85], 0\nv_mov_b64 v[86:87], 0\nv_smfmac_f32_16x16x64_bf16 v[52:55], v[36:39], v[96:103], v10\nv_smfmac_f32_16x16x64_bf16 v[80:83], v[40:43], v[112:119], v10\nds_read_b128 v[56:59], v1 offset:20480\nv_cvt_pk_bf16_f32 v72, v48, v49\nv_smfmac_f32_16x16x64_bf16 v[84:87], v[44:47], v[112:119], v10\n" \
"ds_read_b128 v[60:63], v1 offset:21504\nv_cvt_pk_bf16_f32 v73, v50, v51\nv_smfmac_f32_16x16x64_bf16 v[80:83], v[16:19], v[120:127], v10\nv_cvt_pk_bf16_f32 v74, v52, v53\nv_smfmac_f32_16x16x64_bf16 v[84:87], v[20:23], v[120:127], v10\ns_add_u32 s60, s55, 0\ns_waitcnt lgkmcnt(2)\nv_cvt_pk_bf16_f32 v75, v54, v55\n" \
"v_smfmac_f32_16x16x64_bf16 v[80:83], v[24:27], v[64:71], v10\nv_and_or_b32 v72, v72, -2, v13\nv_smfmac_f32_16x16x64_bf16 v[84:87], v[28:31], v[64:71], v10\nv_and_or_b32 v74, v74, -2, v13\nv_smfmac_f32_16x16x64_bf16 v[80:83], v[32:35], v[144:151], v10\nbuffer_store_dwordx4 v[72:75], v7, s[36:39], s60 offen offset:2560 sc1\nv_mov_b64 v[48:49], 0\nv_mov_b64 v[50:51], 0\n" \
"v_mov_b64 v[52:53], 0\nv_mov_b64 v[54:55], 0\nv_smfmac_f32_16x16x64_bf16 v[84:87], v[36:39], v[144:151], v10\nv_smfmac_f32_16x16x64_bf16 v[48:51], v[40:43], v[160:167], v10\nds_read_b128 v[64:67], v1 offset:22528\nv_cvt_pk_bf16_f32 v76, v80, v81\nv_smfmac_f32_16x16x64_bf16 v[52:55], v[44:47], v[160:167], v10\nds_read_b128 v[68:71], v1 offset:23552\n" \
"v_cvt_pk_bf16_f32 v77, v82, v83\nv_smfmac_f32_16x16x64_bf16 v[48:51], v[16:19], v[176:183], v10\nv_cvt_pk_bf16_f32 v78, v84, v85\nv_smfmac_f32_16x16x64_bf16 v[52:55], v[20:23], v[176:183], v10\ns_add_u32 s60, s55, 4096\ns_waitcnt lgkmcnt(2)\nv_cvt_pk_bf16_f32 v79, v86, v87\nv_smfmac_f32_16x16x64_bf16 v[48:51], v[24:27], v[56:63], v10\n" \
"v_and_or_b32 v76, v76, -2, v13\nv_smfmac_f32_16x16x64_bf16 v[52:55], v[28:31], v[56:63], v10\nv_and_or_b32 v78, v78, -2, v13\nv_smfmac_f32_16x16x64_bf16 v[48:51], v[32:35], v[200:207], v10\nbuffer_store_dwordx4 v[76:79], v7, s[36:39], s60 offen offset:2560 sc1\nv_mov_b64 v[80:81], 0\nv_mov_b64 v[82:83], 0\nv_mov_b64 v[84:85], 0\n" \
"v_mov_b64 v[86:87], 0\nv_smfmac_f32_16x16x64_bf16 v[52:55], v[36:39], v[200:207], v10\ns_cmp_lg_u64 s[64:65], 0\ns_cbranch_scc1 .Lvu_slow%=\n.Lvu_slowr%=:\nds_read_b128 v[88:91], v2 offset:0\nv_smfmac_f32_16x16x64_bf16 v[80:83], v[40:43], v[216:223], v10\nds_read_b128 v[92:95], v2 offset:256\n" \
"v_cvt_pk_bf16_f32 v72, v48, v49\nv_smfmac_f32_16x16x64_bf16 v[84:87], v[44:47], v[216:223], v10\nv_cvt_pk_bf16_f32 v73, v50, v51\nv_smfmac_f32_16x16x64_bf16 v[80:83], v[16:19], v[224:231], v10\nds_read_b128 v[40:43], v2 offset:2048\nv_cvt_pk_bf16_f32 v74, v52, v53\nv_smfmac_f32_16x16x64_bf16 v[84:87], v[20:23], v[224:231], v10\ns_add_u32 s60, s55, 16384\n" \
"s_waitcnt lgkmcnt(3)\nds_read_b128 v[44:47], v2 offset:2304\nv_cvt_pk_bf16_f32 v75, v54, v55\nv_smfmac_f32_16x16x64_bf16 v[80:83], v[24:27], v[64:71], v10\nv_and_or_b32 v72, v72, -2, v13\nv_smfmac_f32_16x16x64_bf16 v[84:87], v[28:31], v[64:71], v10\nv_and_or_b32 v74, v74, -2, v13\nv_smfmac_f32_16x16x64_bf16 v[80:83], v[32:35], v[248:255], v10\n" \
"buffer_store_dwordx4 v[72:75], v7, s[36:39], s60 offen offset:2560 sc1\nv_mov_b64 v[48:49], 0\nv_mov_b64 v[50:51], 0\nv_mov_b64 v[52:53], 0\nv_mov_b64 v[54:55], 0\nv_smfmac_f32_16x16x64_bf16 v[84:87], v[36:39], v[248:255], v10\ns_add_u32 s60, s55, 20480\ns_cmp_lt_i32 s40, 63\n" \
"v_mov_b32 v15, s40\ns_cbranch_scc1 .Lvu_tok%=\nv_cvt_pk_bf16_f32 v76, v80, v81\nv_cvt_pk_bf16_f32 v77, v82, v83\ns_nop 1\nv_cvt_pk_bf16_f32 v78, v84, v85\nv_cvt_pk_bf16_f32 v79, v86, v87\nv_and_or_b32 v76, v76, -2, v13\n" \
"v_and_or_b32 v78, v78, -2, v13\nbuffer_store_dwordx4 v[76:79], v7, s[36:39], s60 offen offset:2560 sc1\nbuffer_load_dwordx4 v[80:83], v3, s[36:39], s42 offen offset:0 sc1\nbuffer_load_dwordx4 v[84:87], v3, s[36:39], s42 offen offset:1024 sc1\nbuffer_load_dwordx4 v[88:91], v3, s[36:39], s42 offen offset:2048 sc1\nbuffer_load_dwordx4 v[92:95], v3, s[36:39], s42 offen offset:3072 sc1\ns_mov_b32 s41, -2147483648\ns_mov_b32 m0, 159744\n" \
"v_mov_b32 v15, 159744\ns_waitcnt vmcnt(0) lgkmcnt(0)\ns_branch .Lvu_crit%=\n.Lvu_re3%=:\nv_max_u32 v14, v80, v82\nv_cmp_eq_u32 vcc, -1, v14\ns_cbranch_vccz .Lvu_sk2284%=\ns_mov_b64 exec, vcc\n" \
"buffer_load_dwordx4 v[80:83], v3, s[36:39], s42 offen offset:0 sc1\ns_mov_b64 exec, -1\n.Lvu_sk2284%=:\nv_max_u32 v14, v84, v86\nv_cmp_eq_u32 vcc, -1, v14\ns_cbranch_vccz .Lvu_sk2291%=\ns_mov_b64 exec, vcc\nbuffer_load_dwordx4 v[84:87], v3, s[36:39], s42 offen offset:1024 sc1\n" \
"s_mov_b64 exec, -1\n.Lvu_sk2291%=:\nv_max_u32 v14, v88, v90\nv_cmp_eq_u32 vcc, -1, v14\ns_cbranch_vccz .Lvu_sk2298%=\ns_mov_b64 exec, vcc\nbuffer_load_dwordx4 v[88:91], v3, s[36:39], s42 offen offset:2048 sc1\ns_mov_b64 exec, -1\n" \
".Lvu_sk2298%=:\nv_max_u32 v14, v92, v94\nv_cmp_eq_u32 vcc, -1, v14\ns_cbranch_vccz .Lvu_sk2305%=\ns_mov_b64 exec, vcc\nbuffer_load_dwordx4 v[92:95], v3, s[36:39], s42 offen offset:3072 sc1\ns_mov_b64 exec, -1\n.Lvu_sk2305%=:\n" \
"v_mov_b64 v[48:49], 0\nv_mov_b64 v[50:51], 0\nv_mov_b64 v[52:53], 0\nv_mov_b64 v[54:55], 0\ns_add_u32 s43, s43, 1\ns_cmp_lt_u32 s43, 262144\ns_cbranch_scc1 .Lvu_poll3%=\ns_branch .Lvu_abort%=\n" \
".Lvu_slow%=:\ns_cmp_ge_i32 s40, 63\ns_cbranch_scc1 .Lvu_slowr%=\n.Lvu_f1%=:\nbuffer_load_dword v14, v8, s[36:39], s56 offen sc1\ns_waitcnt vmcnt(0)\nv_cmp_ge_i32 vcc, s40, v14\ns_cbranch_vccz .Lvu_f1fok%=\n" \
"s_add_u32 s43, s43, 1\ns_cmp_lt_u32 s43, 262144\ns_cbranch_scc1 .Lvu_f1%=\ns_branch .Lvu_abort%=\n.Lvu_f1fok%=:\ns_add_u32 m0, s57, 0\ns_add_u32 s58, s41, 0\nbuffer_load_dwordx4 v0, s[36:39], s58 offen offset:0 sc1 lds\n" \
"buffer_load_dwordx4 v0, s[36:39], s58 offen offset:1024 sc1 lds\nbuffer_load_dwordx4 v0, s[36:39], s58 offen offset:2048 sc1 lds\nbuffer_load_dwordx4 v0, s[36:39], s58 offen offset:3072 sc1 lds\ns_add_u32 m0, s57, 32768\ns_add_u32 s58, s41, 32768\nbuffer_load_dwordx4 v0, s[36:39], s58 offen offset:0 sc1 lds\nbuffer_load_dwordx4 v0, s[36:39], s58 offen offset:1024 sc1 lds\nbuffer_load_dwordx4 v0, s[36:39], s58 offen offset:2048 sc1 lds\n" \
"buffer_load_dwordx4 v0, s[36:39], s58 offen offset:3072 sc1 lds\ns_waitcnt vmcnt(0)\ns_barrier\ns_branch .Lvu_slowr%=\n.Lvu_first%=:\ns_waitcnt vmcnt(0) lgkmcnt(0)\ns_barrier\nds_read_b128 v[56:59], v1 offset:8192\n" \
"ds_read_b128 v[60:63], v1 offset:9216\nds_read_b128 v[64:67], v1 offset:10240\nds_read_b128 v[68:71], v1 offset:11264\nds_read_b128 v[16:19], v11 offset:49152\nds_read_b128 v[20:23], v11 offset:50176\nds_read_b128 v[24:27], v11 offset:51200\nds_read_b128 v[28:31], v11 offset:52224\nds_read_b128 v[32:35], v11 offset:53248\n" \
"ds_read_b128 v[36:39], v11 offset:54272\nds_read_b128 v[88:91], v11 offset:55296\nds_read_b128 v[92:95], v11 offset:56320\nds_read_b32 v15, v15\ns_waitcnt lgkmcnt(0)\nv_cmp_ge_i32 s[64:65], s62, v15\ns_nop 0\ns_cmp_eq_u64 s[64:65], 0\n" \
"s_cselect_b32 s61, s41, -2147483648\ns_add_u32 m0, s57, 0\ns_add_u32 s58, s61, 0\nbuffer_load_dwordx4 v0, s[36:39], s58 offen offset:0 sc1 lds\nbuffer_load_dwordx4 v0, s[36:39], s58 offen offset:1024 sc1 lds\nbuffer_load_dwordx4 v0, s[36:39], s58 offen offset:2048 sc1 lds\nbuffer_load_dwordx4 v0, s[36:39], s58 offen offset:3072 sc1 lds\ns_add_u32 m0, s57, 32768\n" \
"s_add_u32 s58, s61, 32768\nbuffer_load_dwordx4 v0, s[36:39], s58 offen offset:0 sc1 lds\nbuffer_load_dwordx4 v0, s[36:39], s58 offen offset:1024 sc1 lds\nbuffer_load_dwordx4 v0, s[36:39], s58 offen offset:2048 sc1 lds\nbuffer_load_dwordx4 v0, s[36:39], s58 offen offset:3072 sc1 lds\ns_mov_b32 s59, -2147483648\nbuffer_store_dword v14, off, s[36:39], s59\nbuffer_store_dword v14, off, s[36:39], s59\n" \
"buffer_store_dword v14, off, s[36:39], s59\ns_branch .Lvu_y%=\n.Lvu_last%=:\ns_nop 7\nv_and_or_b32 v76, v76, -2, v12\nv_cvt_pk_bf16_f32 v78, v44, v45\nv_cvt_pk_bf16_f32 v79, v46, v47\nv_and_or_b32 v78, v78, -2, v12\n" \
"s_nop 0\nv_permlane16_swap_b32 v77, v79\nv_permlane16_swap_b32 v76, v78\nbuffer_store_dwordx4 v[76:79], v6, s[36:39], s59 offen sc1\ns_branch .Lvu_end%=\n.Lvu_abort%=:\ns_mov_b64 exec, -1\nv_mov_b32 v16, 0\n" \
"s_movk_i32 s44, 6400\nbuffer_store_dword v16, off, s[36:39], s44 sc1\n.Lvu_end%=:\ns_waitcnt vmcnt(0) lgkmcnt(0)\n" \
""
#define RD_ASM \
"v_mbcnt_lo_u32_b32 v255, -1, 0\nv_mbcnt_hi_u32_b32 v255, -1, v255\nv_lshlrev_b32 v255, 2, v255\nv_add_u32 v255, %[tb], v255\nds_read_b32 v196, v255 offset:0\nds_read_b32 v197, v255 offset:256\nds_read_b32 v198, v255 offset:512\nds_read_b32 v199, v255 offset:768\n" \
"ds_read_b32 v200, v255 offset:1024\nds_read_b32 v201, v255 offset:1280\nds_read_b32 v202, v255 offset:1536\nds_read_b32 v203, v255 offset:1792\nds_read_b32 v204, v255 offset:2048\nds_read_b32 v205, v255 offset:2304\nds_read_b32 v206, v255 offset:2560\ns_waitcnt lgkmcnt(0)\n" \
"v_accvgpr_write_b32 a32, v206\ns_mov_b32 s40, 0\ns_mov_b32 s43, 0\ns_add_u32 s57, %[su], 4096\ns_add_u32 s56, %[sp], 4096\ns_mov_b32 s41, %[sy]\n.Lrd_yflag%=:\nv_accvgpr_read_b32 v206, a32\n" \
"buffer_load_dword v208, v206, %[rs], 0 offen sc1\ns_waitcnt vmcnt(0)\nv_cmp_gt_i32 vcc, s40, v208\ns_cbranch_vccz .Lrd_yld%=\ns_add_u32 s43, s43, 1\ns_cmp_lt_u32 s43, 262144\ns_cbranch_scc1 .Lrd_yflag%=\ns_branch .Lrd_abort%=\n" \
".Lrd_yld%=:\ns_add_u32 s60, s41, 0\nbuffer_load_dwordx4 v[0:3], v196, %[rs], s60 offen offset:0 sc1\ns_add_u32 s60, s41, 0\nbuffer_load_dwordx4 v[32:35], v196, %[rs], s60 offen offset:1024 sc1\ns_add_u32 s60, s41, 0\nbuffer_load_dwordx4 v[64:67], v196, %[rs], s60 offen offset:2048 sc1\ns_add_u32 s60, s41, 131072\n" \
"buffer_load_dwordx4 v[4:7], v196, %[rs], s60 offen offset:0 sc1\ns_add_u32 s60, s41, 131072\nbuffer_load_dwordx4 v[36:39], v196, %[rs], s60 offen offset:1024 sc1\ns_add_u32 s60, s41, 131072\nbuffer_load_dwordx4 v[68:71], v196, %[rs], s60 offen offset:2048 sc1\ns_add_u32 s60, s41, 262144\nbuffer_load_dwordx4 v[8:11], v196, %[rs], s60 offen offset:0 sc1\ns_add_u32 s60, s41, 262144\n" \
"buffer_load_dwordx4 v[40:43], v196, %[rs], s60 offen offset:1024 sc1\ns_add_u32 s60, s41, 262144\nbuffer_load_dwordx4 v[72:75], v196, %[rs], s60 offen offset:2048 sc1\ns_add_u32 s60, s41, 393216\nbuffer_load_dwordx4 v[12:15], v196, %[rs], s60 offen offset:0 sc1\ns_add_u32 s60, s41, 393216\nbuffer_load_dwordx4 v[44:47], v196, %[rs], s60 offen offset:1024 sc1\ns_add_u32 s60, s41, 393216\n" \
"buffer_load_dwordx4 v[76:79], v196, %[rs], s60 offen offset:2048 sc1\ns_add_u32 s60, s41, 524288\nbuffer_load_dwordx4 v[16:19], v196, %[rs], s60 offen offset:0 sc1\ns_add_u32 s60, s41, 524288\nbuffer_load_dwordx4 v[48:51], v196, %[rs], s60 offen offset:1024 sc1\ns_add_u32 s60, s41, 524288\nbuffer_load_dwordx4 v[80:83], v196, %[rs], s60 offen offset:2048 sc1\ns_add_u32 s60, s41, 655360\n" \
"buffer_load_dwordx4 v[20:23], v196, %[rs], s60 offen offset:0 sc1\ns_add_u32 s60, s41, 655360\nbuffer_load_dwordx4 v[52:55], v196, %[rs], s60 offen offset:1024 sc1\ns_add_u32 s60, s41, 655360\nbuffer_load_dwordx4 v[84:87], v196, %[rs], s60 offen offset:2048 sc1\ns_add_u32 s60, s41, 786432\nbuffer_load_dwordx4 v[24:27], v196, %[rs], s60 offen offset:0 sc1\ns_add_u32 s60, s41, 786432\n" \
"buffer_load_dwordx4 v[56:59], v196, %[rs], s60 offen offset:1024 sc1\ns_add_u32 s60, s41, 786432\nbuffer_load_dwordx4 v[88:91], v196, %[rs], s60 offen offset:2048 sc1\ns_add_u32 s60, s41, 917504\nbuffer_load_dwordx4 v[28:31], v196, %[rs], s60 offen offset:0 sc1\ns_add_u32 s60, s41, 917504\nbuffer_load_dwordx4 v[60:63], v196, %[rs], s60 offen offset:1024 sc1\ns_add_u32 s60, s41, 917504\n" \
"buffer_load_dwordx4 v[92:95], v196, %[rs], s60 offen offset:2048 sc1\ns_add_u32 s60, s41, 0\nbuffer_load_dwordx4 v[96:99], v196, %[rs], s60 offen offset:3072 sc1\ns_add_u32 s60, s41, 131072\nbuffer_load_dwordx4 v[100:103], v196, %[rs], s60 offen offset:3072 sc1\ns_add_u32 s60, s41, 262144\nbuffer_load_dwordx4 v[104:107], v196, %[rs], s60 offen offset:3072 sc1\ns_add_u32 s60, s41, 393216\n" \
"buffer_load_dwordx4 v[108:111], v196, %[rs], s60 offen offset:3072 sc1\ns_add_u32 s60, s41, 524288\nbuffer_load_dwordx4 v[112:115], v196, %[rs], s60 offen offset:3072 sc1\ns_add_u32 s60, s41, 655360\nbuffer_load_dwordx4 v[116:119], v196, %[rs], s60 offen offset:3072 sc1\ns_add_u32 s60, s41, 786432\nbuffer_load_dwordx4 v[120:123], v196, %[rs], s60 offen offset:3072 sc1\ns_add_u32 s60, s41, 917504\n" \
"buffer_load_dwordx4 v[124:127], v196, %[rs], s60 offen offset:3072 sc1\ns_mov_b64 exec, -1\nbuffer_load_dwordx4 v[128:131], v196, %[rs], %[su] offen offset:0 sc1\nbuffer_load_dwordx4 v[132:135], v196, %[rs], %[su] offen offset:1024 sc1\nbuffer_load_dwordx4 v[136:139], v196, %[rs], %[su] offen offset:2048 sc1\nbuffer_load_dwordx4 v[140:143], v196, %[rs], %[su] offen offset:3072 sc1\nbuffer_load_dwordx4 v[144:147], v196, %[rs], s57 offen offset:0 sc1\nbuffer_load_dwordx4 v[148:151], v196, %[rs], s57 offen offset:1024 sc1\n" \
"buffer_load_dwordx4 v[152:155], v196, %[rs], s57 offen offset:2048 sc1\nbuffer_load_dwordx4 v[156:159], v196, %[rs], s57 offen offset:3072 sc1\nbuffer_load_dwordx4 v[160:163], v196, %[rs], %[sp] offen offset:0 sc1\nbuffer_load_dwordx4 v[164:167], v196, %[rs], %[sp] offen offset:1024 sc1\nbuffer_load_dwordx4 v[168:171], v196, %[rs], %[sp] offen offset:2048 sc1\nbuffer_load_dwordx4 v[172:175], v196, %[rs], %[sp] offen offset:3072 sc1\nbuffer_load_dwordx4 v[176:179], v196, %[rs], s56 offen offset:0 sc1\ns_waitcnt vmcnt(0)\n" \
"s_branch .Lrd_ychk%=\n.Lrd_u2%=:\nbuffer_load_dwordx4 v[136:139], v196, %[rs], %[su] offen offset:2048 sc1\nbuffer_load_dwordx4 v[140:143], v196, %[rs], %[su] offen offset:3072 sc1\ns_add_u32 s43, s43, 1\ns_cmp_ge_u32 s43, 262144\ns_cbranch_scc1 .Lrd_abort%=\ns_waitcnt vmcnt(0)\n" \
"v_bitop3_b32 v208, v136, v138, s54 bitop3:0x7e\nv_bitop3_b32 v209, v140, v142, s54 bitop3:0x7e\nv_bitop3_b32 v208, v208, 1, v209 bitop3:0x80\nv_cmp_eq_u32 vcc, 0, v208\ns_cbranch_vccz .Lrd_u2%=\n.Lrd_u3%=:\nbuffer_load_dwordx4 v[128:131], v196, %[rs], %[su] offen offset:0 sc1\nbuffer_load_dwordx4 v[132:135], v196, %[rs], %[su] offen offset:1024 sc1\n" \
"buffer_load_dwordx4 v[136:139], v196, %[rs], %[su] offen offset:2048 sc1\nbuffer_load_dwordx4 v[140:143], v196, %[rs], %[su] offen offset:3072 sc1\nbuffer_load_dwordx4 v[144:147], v196, %[rs], s57 offen offset:0 sc1\nbuffer_load_dwordx4 v[148:151], v196, %[rs], s57 offen offset:1024 sc1\nbuffer_load_dwordx4 v[152:155], v196, %[rs], s57 offen offset:2048 sc1\nbuffer_load_dwordx4 v[156:159], v196, %[rs], s57 offen offset:3072 sc1\nbuffer_load_dwordx4 v[160:163], v196, %[rs], %[sp] offen offset:0 sc1\nbuffer_load_dwordx4 v[164:167], v196, %[rs], %[sp] offen offset:1024 sc1\n" \
"buffer_load_dwordx4 v[168:171], v196, %[rs], %[sp] offen offset:2048 sc1\nbuffer_load_dwordx4 v[172:175], v196, %[rs], %[sp] offen offset:3072 sc1\nbuffer_load_dwordx4 v[176:179], v196, %[rs], s56 offen offset:0 sc1\n.Lrd_ra%=:\ns_waitcnt vmcnt(5)\nv_bitop3_b32 v208, v128, v130, s54 bitop3:0x7e\nv_bitop3_b32 v209, v136, v138, s54 bitop3:0x7e\nv_bitop3_b32 v208, v208, 1, v209 bitop3:0xc8\n" \
"v_cmp_ne_u32 s[86:87], 0, v208\nv_bitop3_b32 v208, v144, v146, s54 bitop3:0x7e\nv_bitop3_b32 v209, v152, v154, s54 bitop3:0x7e\nv_bitop3_b32 v208, v208, 1, v209 bitop3:0xc8\nv_cmp_ne_u32 s[88:89], 0, v208\nv_bitop3_b32 v208, v132, v134, s54 bitop3:0x7e\nv_bitop3_b32 v209, v140, v142, s54 bitop3:0x7e\nv_bitop3_b32 v208, v208, 1, v209 bitop3:0xc8\n" \
"v_cmp_ne_u32 s[68:69], 0, v208\nv_bitop3_b32 v208, v148, v150, s54 bitop3:0x7e\nv_bitop3_b32 v209, v156, v158, s54 bitop3:0x7e\nv_bitop3_b32 v208, v208, 1, v209 bitop3:0xc8\nv_cmp_ne_u32 s[62:63], 0, v208\ns_or_b64 vcc, s[86:87], s[88:89]\ns_or_b64 vcc, vcc, s[68:69]\ns_or_b64 vcc, vcc, s[62:63]\n" \
"s_waitcnt vmcnt(4)\nv_smfmac_f32_16x16x64_bf16 v[180:183], v[160:163], v[0:7], v197\nv_bitop3_b32 v208, v160, v162, s54 bitop3:0x7e\nv_smfmac_f32_16x16x64_bf16 v[184:187], v[160:163], v[32:39], v197\nv_cmp_gt_i32 s[46:47], 0, v208\nv_smfmac_f32_16x16x64_bf16 v[188:191], v[160:163], v[64:71], v197\nv_smfmac_f32_16x16x64_bf16 v[192:195], v[160:163], v[96:103], v197\ns_or_b64 vcc, vcc, s[46:47]\n" \
"s_waitcnt vmcnt(3)\nv_smfmac_f32_16x16x64_bf16 v[180:183], v[164:167], v[8:15], v197\nv_bitop3_b32 v208, v164, v166, s54 bitop3:0x7e\nv_smfmac_f32_16x16x64_bf16 v[184:187], v[164:167], v[40:47], v197\nv_cmp_gt_i32 s[48:49], 0, v208\nv_smfmac_f32_16x16x64_bf16 v[188:191], v[164:167], v[72:79], v197\nv_smfmac_f32_16x16x64_bf16 v[192:195], v[164:167], v[104:111], v197\ns_or_b64 vcc, vcc, s[48:49]\n" \
"s_waitcnt vmcnt(2)\nv_smfmac_f32_16x16x64_bf16 v[180:183], v[168:171], v[16:23], v197\nv_bitop3_b32 v208, v168, v170, s54 bitop3:0x7e\nv_smfmac_f32_16x16x64_bf16 v[184:187], v[168:171], v[48:55], v197\nv_cmp_gt_i32 s[50:51], 0, v208\nv_smfmac_f32_16x16x64_bf16 v[188:191], v[168:171], v[80:87], v197\nv_smfmac_f32_16x16x64_bf16 v[192:195], v[168:171], v[112:119], v197\ns_or_b64 vcc, vcc, s[50:51]\n" \
"s_waitcnt vmcnt(1)\nv_smfmac_f32_16x16x64_bf16 v[180:183], v[172:175], v[24:31], v197\ns_waitcnt vmcnt(0)\nv_bitop3_b32 v208, v172, v174, s54 bitop3:0x7e\nv_smfmac_f32_16x16x64_bf16 v[180:183], v[176:179], v[128:135], v197\nv_bitop3_b32 v209, v176, v178, s54 bitop3:0x7e\nv_smfmac_f32_16x16x64_bf16 v[184:187], v[172:175], v[56:63], v197\nv_bitop3_b32 v210, v177, v179, s54 bitop3:0x7e\n" \
"v_smfmac_f32_16x16x64_bf16 v[184:187], v[176:179], v[136:143], v197\nv_cmp_gt_i32 s[52:53], 0, v208\nv_smfmac_f32_16x16x64_bf16 v[188:191], v[172:175], v[88:95], v197\nv_or_b32 v209, v209, v210\nv_smfmac_f32_16x16x64_bf16 v[188:191], v[176:179], v[144:151], v197\ns_or_b64 vcc, vcc, s[52:53]\nv_cmp_gt_i32 s[64:65], 0, v209\nv_smfmac_f32_16x16x64_bf16 v[192:195], v[172:175], v[120:127], v197\n" \
"v_smfmac_f32_16x16x64_bf16 v[192:195], v[176:179], v[152:159], v197\ns_or_b64 vcc, vcc, s[64:65]\ns_cbranch_scc1 .Lrd_rb%=\ns_add_u32 s60, s58, 16384\nv_add_f32 v180, v180, v182\nv_add_f32 v181, v181, v183\nds_write2_b32 v198, v180, v181 offset0:0 offset1:8\nbuffer_load_dwordx4 v[212:215], v196, %[rs], s60 offen offset:0 sc1\n" \
"v_add_f32 v184, v184, v186\nv_add_f32 v185, v185, v187\nds_write2_b32 v198, v184, v185 offset0:32 offset1:40\nv_add_f32 v188, v188, v190\nv_add_f32 v189, v189, v191\nds_write2_b32 v198, v188, v189 offset0:64 offset1:72\ns_add_u32 s60, s58, 0\nbuffer_load_dwordx4 v[216:219], v196, %[rs], s60 offen offset:0 sc1\n" \
"v_add_f32 v192, v192, v194\nv_add_f32 v193, v193, v195\nds_write2_b32 v198, v192, v193 offset0:96 offset1:104\ns_waitcnt lgkmcnt(0)\ns_barrier\nds_read_b128 v[180:183], v199 offset:0\nds_read_b128 v[184:187], v199 offset:4096\nds_read_b128 v[188:191], v199 offset:8192\n" \
"ds_read_b128 v[192:195], v199 offset:12288\ns_and_b32 s44, s54, 0x80008000\ns_add_u32 s60, s58, 0\nbuffer_load_dwordx4 v[220:223], v196, %[rs], s60 offen offset:1024 sc1\ns_waitcnt lgkmcnt(2)\nv_pk_add_f32 v[182:183], v[182:183], v[186:187]\nv_pk_add_f32 v[180:181], v[180:181], v[184:185]\ns_waitcnt lgkmcnt(0)\n" \
"v_pk_add_f32 v[186:187], v[188:189], v[192:193]\nv_pk_add_f32 v[184:185], v[190:191], v[194:195]\nv_pk_add_f32 v[180:181], v[180:181], v[186:187]\nv_pk_add_f32 v[182:183], v[182:183], v[184:185]\nv_cvt_pk_bf16_f32 v180, v180, v181\nv_xor_b32 v180, s44, v180\nv_cvt_pk_bf16_f32 v181, v182, v183\nv_min_u32 v180, -2, v180\n" \
"v_xor_b32 v209, s44, v181\nv_or_b32 v208, v180, v201\nbuffer_store_dwordx2 v[208:209], v206, %[rs], 0 offen sc1\ns_add_u32 s60, s58, 16384\nbuffer_load_dwordx4 v[224:227], v196, %[rs], s60 offen offset:1024 sc1\ns_add_u32 s60, s58, 0\nbuffer_load_dwordx4 v[228:231], v196, %[rs], s60 offen offset:2048 sc1\ns_add_u32 s60, s58, 0\n" \
"buffer_load_dwordx4 v[232:235], v196, %[rs], s60 offen offset:3072 sc1\ns_add_u32 s60, s58, 16384\nbuffer_load_dwordx4 v[236:239], v196, %[rs], s60 offen offset:2048 sc1\ns_add_u32 s60, s58, 4096\nbuffer_load_dwordx4 v[240:243], v196, %[rs], s60 offen offset:0 sc1\ns_add_u32 s60, s58, 4096\nbuffer_load_dwordx4 v[244:247], v196, %[rs], s60 offen offset:1024 sc1\ns_add_u32 s60, s58, 16384\n" \
"buffer_load_dwordx4 v[248:251], v196, %[rs], s60 offen offset:3072 sc1\ns_add_u32 s60, s58, 20480\nbuffer_load_dwordx4 v[252:255], v196, %[rs], s60 offen offset:0 sc1\ns_add_u32 s60, s58, 20480\nbuffer_load_dwordx4 v[160:163], v196, %[rs], s60 offen offset:2048 sc1\ns_xor_b32 s41, s41, 4194304\nv_mov_b32 v207, s40\ns_waitcnt vmcnt(12)\n" \
"v_bitop3_b32 v209, v212, v214, s61 bitop3:0x7e\nv_cmp_gt_i32 vcc, 0, v209\ns_cbranch_vccnz .Lrd_tre0%=\n.Lrd_tl0%=:\nv_mfma_f32_16x16x16_bf16 a[0:3], v[0:1], v[212:213], 0\ns_add_u32 s60, s58, 4096\nbuffer_load_dwordx4 v[164:167], v196, %[rs], s60 offen offset:2048 sc1\nv_mfma_f32_16x16x16_bf16 a[8:11], v[32:33], v[212:213], 0\n" \
"v_mfma_f32_16x16x16_bf16 a[16:19], v[64:65], v[212:213], 0\nv_mfma_f32_16x16x16_bf16 a[24:27], v[96:97], v[212:213], 0\ns_waitcnt vmcnt(12)\nv_mfma_f32_16x16x16_bf16 a[0:3], v[4:5], v[214:215], a[0:3]\ns_add_u32 s60, s58, 4096\nbuffer_load_dwordx4 v[168:171], v196, %[rs], s60 offen offset:3072 sc1\nv_bitop3_b32 v209, v216, v218, s61 bitop3:0x7e\nv_mfma_f32_16x16x16_bf16 a[8:11], v[36:37], v[214:215], a[8:11]\n" \
"s_lshl_b32 s42, s40, 17\ns_add_u32 s42, s42, %[at]\nv_mfma_f32_16x16x16_bf16 a[16:19], v[68:69], v[214:215], a[16:19]\nv_cmp_gt_i32 vcc, 0, v209\nv_mfma_f32_16x16x16_bf16 a[24:27], v[100:101], v[214:215], a[24:27]\ns_cbranch_vccnz .Lrd_tre1%=\n.Lrd_tl1%=:\ns_waitcnt vmcnt(12)\n" \
"v_mfma_f32_16x16x32_bf16 a[4:7], v[0:3], v[216:219], 0\ns_add_u32 s60, s58, 20480\nbuffer_load_dwordx4 v[172:175], v196, %[rs], s60 offen offset:1024 sc1\nv_bitop3_b32 v209, v220, v222, s61 bitop3:0x7e\nv_mfma_f32_16x16x32_bf16 a[12:15], v[32:35], v[216:219], 0\ns_lshr_b32 s45, s42, 8\ns_and_b32 s45, s45, 0x2000\nv_mfma_f32_16x16x32_bf16 a[20:23], v[64:67], v[216:219], 0\n" \
"v_cmp_gt_i32 vcc, 0, v209\nv_mfma_f32_16x16x32_bf16 a[28:31], v[96:99], v[216:219], 0\ns_cbranch_vccnz .Lrd_tre2%=\n.Lrd_tl2%=:\ns_waitcnt vmcnt(11)\nv_mfma_f32_16x16x32_bf16 a[4:7], v[4:7], v[220:223], a[4:7]\ns_add_u32 s60, s58, 20480\nbuffer_load_dwordx4 v[176:179], v196, %[rs], s60 offen offset:3072 sc1\n" \
"v_bitop3_b32 v209, v224, v226, s61 bitop3:0x7e\nv_mfma_f32_16x16x32_bf16 a[12:15], v[36:39], v[220:223], a[12:15]\ns_xor_b32 s42, s42, s45\ns_add_u32 s40, s40, 1\nv_mfma_f32_16x16x32_bf16 a[20:23], v[68:71], v[220:223], a[20:23]\nv_cmp_gt_i32 vcc, 0, v209\nv_mfma_f32_16x16x32_bf16 a[28:31], v[100:103], v[220:223], a[28:31]\ns_cbranch_vccnz .Lrd_tre3%=\n" \
".Lrd_tl3%=:\nv_mfma_f32_16x16x16_bf16 a[0:3], v[8:9], v[224:225], a[0:3]\ns_cmp_eq_u32 s40, 1\ns_cbranch_scc1 .Lrd_y1%=\n.Lrd_y1r%=:\ns_add_u32 s60, s41, 0\nbuffer_load_dwordx4 v[0:3], v196, %[rs], s60 offen offset:0 sc1\nv_mfma_f32_16x16x16_bf16 a[8:11], v[40:41], v[224:225], a[8:11]\n" \
"v_mfma_f32_16x16x16_bf16 a[16:19], v[72:73], v[224:225], a[16:19]\nbuffer_load_dwordx4 v[212:215], v204, %[rs], s42 offen sc1\nv_mfma_f32_16x16x16_bf16 a[24:27], v[104:105], v[224:225], a[24:27]\ns_waitcnt vmcnt(13)\nv_mfma_f32_16x16x16_bf16 a[0:3], v[12:13], v[226:227], a[0:3]\ns_add_u32 s60, s41, 0\nbuffer_load_dwordx4 v[32:35], v196, %[rs], s60 offen offset:1024 sc1\nv_bitop3_b32 v209, v228, v230, s61 bitop3:0x7e\n" \
"v_mfma_f32_16x16x16_bf16 a[8:11], v[44:45], v[226:227], a[8:11]\ns_lshl_b32 s62, s55, 31\ns_or_b32 s62, s62, 1.0\ns_mov_b32 s63, s62\nv_mfma_f32_16x16x16_bf16 a[16:19], v[76:77], v[226:227], a[16:19]\nv_cmp_gt_i32 vcc, 0, v209\nv_mfma_f32_16x16x16_bf16 a[24:27], v[108:109], v[226:227], a[24:27]\ns_cbranch_vccnz .Lrd_tre4%=\n" \
".Lrd_tl4%=:\ns_waitcnt vmcnt(13)\nv_mfma_f32_16x16x32_bf16 a[4:7], v[8:11], v[228:231], a[4:7]\ns_add_u32 s60, s41, 0\nbuffer_load_dwordx4 v[64:67], v196, %[rs], s60 offen offset:2048 sc1\nv_bitop3_b32 v209, v232, v234, s61 bitop3:0x7e\nv_mfma_f32_16x16x32_bf16 a[12:15], v[40:43], v[228:231], a[12:15]\nv_mfma_f32_16x16x32_bf16 a[20:23], v[72:75], v[228:231], a[20:23]\n" \
"v_cmp_gt_i32 vcc, 0, v209\nv_mfma_f32_16x16x32_bf16 a[28:31], v[104:107], v[228:231], a[28:31]\ns_cbranch_vccnz .Lrd_tre5%=\n.Lrd_tl5%=:\ns_waitcnt vmcnt(13)\nv_mfma_f32_16x16x32_bf16 a[4:7], v[12:15], v[232:235], a[4:7]\ns_add_u32 s60, s41, 131072\nbuffer_load_dwordx4 v[4:7], v196, %[rs], s60 offen offset:0 sc1\n" \
"v_bitop3_b32 v209, v236, v238, s61 bitop3:0x7e\nv_mfma_f32_16x16x32_bf16 a[12:15], v[44:47], v[232:235], a[12:15]\nv_mfma_f32_16x16x32_bf16 a[20:23], v[76:79], v[232:235], a[20:23]\nv_cmp_gt_i32 vcc, 0, v209\nv_mfma_f32_16x16x32_bf16 a[28:31], v[108:111], v[232:235], a[28:31]\ns_cbranch_vccnz .Lrd_tre6%=\n.Lrd_tl6%=:\nv_mfma_f32_16x16x16_bf16 a[0:3], v[16:17], v[236:237], a[0:3]\n" \
"s_add_u32 s60, s41, 131072\nbuffer_load_dwordx4 v[36:39], v196, %[rs], s60 offen offset:1024 sc1\nv_mfma_f32_16x16x16_bf16 a[8:11], v[48:49], v[236:237], a[8:11]\nv_mfma_f32_16x16x16_bf16 a[16:19], v[80:81], v[236:237], a[16:19]\nv_mfma_f32_16x16x16_bf16 a[24:27], v[112:113], v[236:237], a[24:27]\ns_waitcnt vmcnt(14)\nv_mfma_f32_16x16x16_bf16 a[0:3], v[20:21], v[238:239], a[0:3]\ns_add_u32 s60, s41, 131072\n" \
"buffer_load_dwordx4 v[68:71], v196, %[rs], s60 offen offset:2048 sc1\nv_bitop3_b32 v209, v240, v242, s61 bitop3:0x7e\nv_mfma_f32_16x16x16_bf16 a[8:11], v[52:53], v[238:239], a[8:11]\nv_mfma_f32_16x16x16_bf16 a[16:19], v[84:85], v[238:239], a[16:19]\nv_cmp_gt_i32 vcc, 0, v209\nv_mfma_f32_16x16x16_bf16 a[24:27], v[116:117], v[238:239], a[24:27]\ns_cbranch_vccnz .Lrd_tre7%=\n.Lrd_tl7%=:\n" \
"s_waitcnt vmcnt(14)\nv_mfma_f32_16x16x32_bf16 a[4:7], v[16:19], v[240:243], a[4:7]\ns_add_u32 s60, s41, 262144\nbuffer_load_dwordx4 v[8:11], v196, %[rs], s60 offen offset:0 sc1\nv_bitop3_b32 v209, v244, v246, s61 bitop3:0x7e\nv_mfma_f32_16x16x32_bf16 a[12:15], v[48:51], v[240:243], a[12:15]\nv_mfma_f32_16x16x32_bf16 a[20:23], v[80:83], v[240:243], a[20:23]\nv_cmp_gt_i32 vcc, 0, v209\n" \
"v_mfma_f32_16x16x32_bf16 a[28:31], v[112:115], v[240:243], a[28:31]\ns_cbranch_vccnz .Lrd_tre8%=\n.Lrd_tl8%=:\ns_waitcnt vmcnt(14)\nv_mfma_f32_16x16x32_bf16 a[4:7], v[20:23], v[244:247], a[4:7]\ns_add_u32 s60, s41, 262144\nbuffer_load_dwordx4 v[40:43], v196, %[rs], s60 offen offset:1024 sc1\nv_bitop3_b32 v209, v248, v250, s61 bitop3:0x7e\n" \
"v_mfma_f32_16x16x32_bf16 a[12:15], v[52:55], v[244:247], a[12:15]\nv_mfma_f32_16x16x32_bf16 a[20:23], v[84:87], v[244:247], a[20:23]\nv_cmp_gt_i32 vcc, 0, v209\nv_mfma_f32_16x16x32_bf16 a[28:31], v[116:119], v[244:247], a[28:31]\ns_cbranch_vccnz .Lrd_tre9%=\n.Lrd_tl9%=:\nv_mfma_f32_16x16x16_bf16 a[0:3], v[24:25], v[248:249], a[0:3]\ns_add_u32 s60, s41, 262144\n" \
"buffer_load_dwordx4 v[72:75], v196, %[rs], s60 offen offset:2048 sc1\nv_mfma_f32_16x16x16_bf16 a[8:11], v[56:57], v[248:249], a[8:11]\nv_mfma_f32_16x16x16_bf16 a[16:19], v[88:89], v[248:249], a[16:19]\nv_mfma_f32_16x16x16_bf16 a[24:27], v[120:121], v[248:249], a[24:27]\ns_waitcnt vmcnt(15)\nv_mfma_f32_16x16x16_bf16 a[0:3], v[28:29], v[250:251], a[0:3]\ns_add_u32 s60, s41, 393216\nbuffer_load_dwordx4 v[12:15], v196, %[rs], s60 offen offset:0 sc1\n" \
"v_bitop3_b32 v209, v252, v254, s61 bitop3:0x7e\nv_mfma_f32_16x16x16_bf16 a[8:11], v[60:61], v[250:251], a[8:11]\nv_bitop3_b32 v210, v253, v255, s61 bitop3:0x7e\nv_or_b32 v209, v209, v210\nv_mfma_f32_16x16x16_bf16 a[16:19], v[92:93], v[250:251], a[16:19]\nv_cmp_gt_i32 vcc, 0, v209\nv_mfma_f32_16x16x16_bf16 a[24:27], v[124:125], v[250:251], a[24:27]\ns_cbranch_vccnz .Lrd_tre10%=\n" \
".Lrd_tl10%=:\ns_waitcnt vmcnt(15)\nv_mfma_f32_16x16x32_bf16 a[0:3], v[128:131], v[252:255], a[0:3]\ns_add_u32 s60, s41, 393216\nbuffer_load_dwordx4 v[44:47], v196, %[rs], s60 offen offset:1024 sc1\nv_bitop3_b32 v209, v160, v162, s61 bitop3:0x7e\nv_mfma_f32_16x16x32_bf16 a[8:11], v[136:139], v[252:255], a[8:11]\nv_bitop3_b32 v210, v161, v163, s61 bitop3:0x7e\n" \
"v_or_b32 v209, v209, v210\nv_mfma_f32_16x16x32_bf16 a[16:19], v[144:147], v[252:255], a[16:19]\nv_cmp_gt_i32 vcc, 0, v209\nv_mfma_f32_16x16x32_bf16 a[24:27], v[152:155], v[252:255], a[24:27]\ns_cbranch_vccnz .Lrd_tre11%=\n.Lrd_tl11%=:\ns_waitcnt vmcnt(15)\nv_mfma_f32_16x16x32_bf16 a[0:3], v[132:135], v[160:163], a[0:3]\n" \
"s_add_u32 s60, s41, 393216\nbuffer_load_dwordx4 v[76:79], v196, %[rs], s60 offen offset:2048 sc1\nv_bitop3_b32 v209, v164, v166, s61 bitop3:0x7e\nv_mfma_f32_16x16x32_bf16 a[8:11], v[140:143], v[160:163], a[8:11]\nv_mfma_f32_16x16x32_bf16 a[16:19], v[148:151], v[160:163], a[16:19]\nv_cmp_gt_i32 vcc, 0, v209\nv_mfma_f32_16x16x32_bf16 a[24:27], v[156:159], v[160:163], a[24:27]\ns_cbranch_vccnz .Lrd_tre12%=\n" \
".Lrd_tl12%=:\ns_waitcnt vmcnt(15)\nv_mfma_f32_16x16x32_bf16 a[4:7], v[24:27], v[164:167], a[4:7]\ns_add_u32 s60, s41, 524288\nbuffer_load_dwordx4 v[16:19], v196, %[rs], s60 offen offset:0 sc1\nv_bitop3_b32 v209, v168, v170, s61 bitop3:0x7e\nv_mfma_f32_16x16x32_bf16 a[12:15], v[56:59], v[164:167], a[12:15]\nv_mfma_f32_16x16x32_bf16 a[20:23], v[88:91], v[164:167], a[20:23]\n" \
"ds_write_b128 v202, a[0:3] offset:0\nv_cmp_gt_i32 vcc, 0, v209\nv_mfma_f32_16x16x32_bf16 a[28:31], v[120:123], v[164:167], a[28:31]\ns_cbranch_vccnz .Lrd_tre13%=\n.Lrd_tl13%=:\ns_waitcnt vmcnt(15)\nv_mfma_f32_16x16x32_bf16 a[4:7], v[28:31], v[168:171], a[4:7]\ns_add_u32 s60, s41, 524288\n" \
"buffer_load_dwordx4 v[48:51], v196, %[rs], s60 offen offset:1024 sc1\nv_bitop3_b32 v209, v172, v174, s61 bitop3:0x7e\nv_mfma_f32_16x16x32_bf16 a[12:15], v[60:63], v[168:171], a[12:15]\nv_bitop3_b32 v210, v173, v175, s61 bitop3:0x7e\nv_or_b32 v209, v209, v210\nv_mfma_f32_16x16x32_bf16 a[20:23], v[92:95], v[168:171], a[20:23]\nds_write_b128 v202, a[8:11] offset:2048\nv_cmp_gt_i32 vcc, 0, v209\n" \
"v_mfma_f32_16x16x32_bf16 a[28:31], v[124:127], v[168:171], a[28:31]\ns_cbranch_vccnz .Lrd_tre14%=\n.Lrd_tl14%=:\ns_waitcnt vmcnt(15)\nv_mfma_f32_16x16x32_bf16 a[4:7], v[128:131], v[172:175], a[4:7]\ns_add_u32 s60, s41, 524288\nbuffer_load_dwordx4 v[80:83], v196, %[rs], s60 offen offset:2048 sc1\nv_bitop3_b32 v209, v176, v178, s61 bitop3:0x7e\n" \
"v_mfma_f32_16x16x32_bf16 a[12:15], v[136:139], v[172:175], a[12:15]\nv_bitop3_b32 v210, v177, v179, s61 bitop3:0x7e\nv_or_b32 v209, v209, v210\nv_mfma_f32_16x16x32_bf16 a[20:23], v[144:147], v[172:175], a[20:23]\nds_write_b128 v202, a[16:19] offset:4096\nv_cmp_gt_i32 vcc, 0, v209\nv_mfma_f32_16x16x32_bf16 a[28:31], v[152:155], v[172:175], a[28:31]\ns_cbranch_vccnz .Lrd_tre15%=\n" \
".Lrd_tl15%=:\nv_mfma_f32_16x16x32_bf16 a[4:7], v[132:135], v[176:179], a[4:7]\ns_add_u32 s60, s41, 655360\ns_waitcnt vmcnt(14)\nbuffer_load_dwordx4 v[20:23], v196, %[rs], s60 offen offset:0 sc1\nv_lshlrev_b32 v216, 16, v212\nv_and_b32 v217, 0xffff0000, v212\nv_mfma_f32_16x16x32_bf16 a[12:15], v[140:143], v[176:179], a[12:15]\n" \
"v_lshlrev_b32 v218, 16, v213\nv_and_b32 v219, 0xffff0000, v213\nv_mfma_f32_16x16x32_bf16 a[20:23], v[148:151], v[176:179], a[20:23]\nds_write_b128 v202, a[24:27] offset:6144\nv_lshlrev_b32 v220, 16, v214\nv_and_b32 v221, 0xffff0000, v214\nv_mfma_f32_16x16x32_bf16 a[28:31], v[156:159], v[176:179], a[28:31]\ns_add_u32 s60, s41, 655360\n" \
"buffer_load_dwordx4 v[52:55], v196, %[rs], s60 offen offset:1024 sc1\ns_add_u32 s60, s41, 655360\nds_write_b128 v202, a[4:7] offset:1024\nbuffer_load_dwordx4 v[84:87], v196, %[rs], s60 offen offset:2048 sc1\nds_write_b128 v202, a[12:15] offset:3072\nv_lshlrev_b32 v222, 16, v215\nv_and_b32 v223, 0xffff0000, v215\ns_add_u32 s60, s41, 786432\n" \
"buffer_load_dwordx4 v[24:27], v196, %[rs], s60 offen offset:0 sc1\nds_write_b128 v202, a[20:23] offset:5120\ns_add_u32 s60, s41, 786432\nbuffer_load_dwordx4 v[56:59], v196, %[rs], s60 offen offset:1024 sc1\nds_write_b128 v202, a[28:31] offset:7168\ns_waitcnt lgkmcnt(0)\ns_barrier\nds_read_b128 v[160:163], v203 offset:0\n" \
"ds_read_b128 v[164:167], v203 offset:256\nds_read_b128 v[168:171], v203 offset:8192\nds_read_b128 v[172:175], v203 offset:8448\nds_read_b128 v[176:179], v203 offset:16384\nds_read_b128 v[180:183], v203 offset:16640\nds_read_b128 v[184:187], v203 offset:24576\nds_read_b128 v[188:191], v203 offset:24832\ns_add_u32 s60, s41, 786432\n" \
"buffer_load_dwordx4 v[88:91], v196, %[rs], s60 offen offset:2048 sc1\ns_waitcnt lgkmcnt(4)\nv_pk_add_f32 v[160:161], v[160:161], v[168:169]\nv_pk_add_f32 v[162:163], v[162:163], v[170:171]\nv_pk_add_f32 v[164:165], v[164:165], v[172:173]\nv_pk_add_f32 v[166:167], v[166:167], v[174:175]\ns_add_u32 s60, s41, 917504\nbuffer_load_dwordx4 v[28:31], v196, %[rs], s60 offen offset:0 sc1\n" \
"s_waitcnt lgkmcnt(2)\nv_pk_add_f32 v[160:161], v[160:161], v[176:177]\nv_pk_add_f32 v[162:163], v[162:163], v[178:179]\nv_pk_add_f32 v[164:165], v[164:165], v[180:181]\nv_pk_add_f32 v[166:167], v[166:167], v[182:183]\ns_add_u32 s60, s41, 917504\nbuffer_load_dwordx4 v[60:63], v196, %[rs], s60 offen offset:1024 sc1\ns_waitcnt lgkmcnt(0)\n" \
"v_pk_add_f32 v[160:161], v[160:161], v[184:185]\nv_pk_add_f32 v[162:163], v[162:163], v[186:187]\nv_pk_add_f32 v[164:165], v[164:165], v[188:189]\nv_pk_add_f32 v[166:167], v[166:167], v[190:191]\nv_pk_fma_f32 v[160:161], v[160:161], s[62:63], v[216:217]\nv_pk_fma_f32 v[162:163], v[162:163], s[62:63], v[218:219]\nv_pk_fma_f32 v[164:165], v[164:165], s[62:63], v[220:221]\nv_pk_fma_f32 v[166:167], v[166:167], s[62:63], v[222:223]\n" \
"v_cvt_pk_bf16_f32 v208, v160, v161\nv_cvt_pk_bf16_f32 v209, v162, v163\nv_cvt_pk_bf16_f32 v210, v164, v165\nv_cvt_pk_bf16_f32 v211, v166, v167\nbuffer_store_dwordx4 v[208:211], v204, %[rs], s42 offen sc1\nv_pk_add_f32 v[168:169], v[160:161], v[162:163]\nv_pk_add_f32 v[170:171], v[164:165], v[166:167]\nv_pk_add_f32 v[168:169], v[168:169], v[170:171]\n" \
"s_add_u32 s60, s41, 917504\nbuffer_load_dwordx4 v[92:95], v196, %[rs], s60 offen offset:2048 sc1\nv_add_f32 v168, v168, v169\ns_nop 1\nv_add_f32_dpp v169, v168, v168 row_ror:8 row_mask:0xf bank_mask:0xf\nv_add_f32_dpp v170, v168, v168 row_ror:8 row_mask:0xf bank_mask:0xf\ns_nop 1\nv_permlane16_swap_b32 v169, v170\n" \
"v_add_f32 v168, v169, v170\nv_add_f32 v169, v169, v170\ns_nop 1\nv_permlane32_swap_b32 v168, v169\nv_add_f32 v168, v168, v169\nv_mul_f32 v168, 0x3c800000, v168\nv_pk_add_f32 v[160:161], v[160:161], v[168:169] op_sel_hi:[1,0] neg_lo:[0,1] neg_hi:[0,1]\nv_pk_add_f32 v[162:163], v[162:163], v[168:169] op_sel_hi:[1,0] neg_lo:[0,1] neg_hi:[0,1]\n" \
"v_pk_add_f32 v[164:165], v[164:165], v[168:169] op_sel_hi:[1,0] neg_lo:[0,1] neg_hi:[0,1]\nv_pk_add_f32 v[166:167], v[166:167], v[168:169] op_sel_hi:[1,0] neg_lo:[0,1] neg_hi:[0,1]\ns_add_u32 s60, s41, 0\nbuffer_load_dwordx4 v[96:99], v196, %[rs], s60 offen offset:3072 sc1\nv_pk_mul_f32 v[172:173], v[160:161], v[160:161]\nv_pk_mul_f32 v[174:175], v[162:163], v[162:163]\nv_pk_fma_f32 v[172:173], v[164:165], v[164:165], v[172:173]\nv_pk_fma_f32 v[174:175], v[166:167], v[166:167], v[174:175]\n" \
"v_pk_add_f32 v[172:173], v[172:173], v[174:175]\nv_add_f32 v172, v172, v173\ns_nop 1\nv_add_f32_dpp v173, v172, v172 row_ror:8 row_mask:0xf bank_mask:0xf\nv_add_f32_dpp v174, v172, v172 row_ror:8 row_mask:0xf bank_mask:0xf\ns_nop 1\nv_permlane16_swap_b32 v173, v174\nv_add_f32 v172, v173, v174\n" \
"v_add_f32 v173, v173, v174\ns_nop 1\nv_permlane32_swap_b32 v172, v173\nv_add_f32 v172, v172, v173\nv_mov_b32 v169, v172\nbuffer_store_dwordx2 v[168:169], v205, %[rs], s42 offen sc1\ns_add_u32 s60, s41, 131072\nbuffer_load_dwordx4 v[100:103], v196, %[rs], s60 offen offset:3072 sc1\n" \
"s_add_u32 s60, s41, 262144\nbuffer_load_dwordx4 v[104:107], v196, %[rs], s60 offen offset:3072 sc1\ns_add_u32 s60, s41, 393216\nbuffer_load_dwordx4 v[108:111], v196, %[rs], s60 offen offset:3072 sc1\ns_add_u32 s60, s41, 524288\nbuffer_load_dwordx4 v[112:115], v196, %[rs], s60 offen offset:3072 sc1\ns_add_u32 s60, s41, 655360\nbuffer_load_dwordx4 v[116:119], v196, %[rs], s60 offen offset:3072 sc1\n" \
"s_add_u32 s60, s41, 786432\nbuffer_load_dwordx4 v[120:123], v196, %[rs], s60 offen offset:3072 sc1\ns_add_u32 s60, s41, 917504\nbuffer_load_dwordx4 v[124:127], v196, %[rs], s60 offen offset:3072 sc1\ns_waitcnt vmcnt(7)\nbuffer_store_dword v207, off, %[rs], %[fa] sc1\nbuffer_load_dwordx4 v[136:139], v196, %[rs], %[su] offen offset:2048 sc1\nbuffer_load_dwordx4 v[140:143], v196, %[rs], %[su] offen offset:3072 sc1\n" \
"buffer_load_dwordx4 v[128:131], v196, %[rs], %[su] offen offset:0 sc1\nbuffer_load_dwordx4 v[132:135], v196, %[rs], %[su] offen offset:1024 sc1\nbuffer_load_dwordx4 v[144:147], v196, %[rs], s57 offen offset:0 sc1\nbuffer_load_dwordx4 v[148:151], v196, %[rs], s57 offen offset:1024 sc1\nbuffer_load_dwordx4 v[152:155], v196, %[rs], s57 offen offset:2048 sc1\nbuffer_load_dwordx4 v[156:159], v196, %[rs], s57 offen offset:3072 sc1\nbuffer_load_dwordx4 v[160:163], v196, %[rs], %[sp] offen offset:0 sc1\nbuffer_load_dwordx4 v[164:167], v196, %[rs], %[sp] offen offset:1024 sc1\n" \
"buffer_load_dwordx4 v[168:171], v196, %[rs], %[sp] offen offset:2048 sc1\nbuffer_load_dwordx4 v[172:175], v196, %[rs], %[sp] offen offset:3072 sc1\nbuffer_load_dwordx4 v[176:179], v196, %[rs], s56 offen offset:0 sc1\ns_cmp_ge_u32 s40, 64\ns_cbranch_scc1 .Lrd_end%=\n.Lrd_ychk%=:\ns_and_b32 s44, s40, 1\ns_bfe_u32 s55, s40, 0x10001\n" \
"s_mul_i32 s54, s44, 0x80008001\ns_mul_i32 s61, s55, 0x80008000\ns_lshl_b32 s58, s44, 17\ns_add_u32 s58, s58, %[st]\ns_add_u32 s44, s40, 1\ns_and_b32 s44, s44, 3\ns_lshl_b32 s44, s44, 15\nv_xor_b32 v206, s44, v200\n" \
"v_mov_b64 v[180:181], 0\nv_mov_b64 v[182:183], 0\nv_mov_b64 v[184:185], 0\nv_mov_b64 v[186:187], 0\nv_mov_b64 v[188:189], 0\nv_mov_b64 v[190:191], 0\nv_mov_b64 v[192:193], 0\nv_mov_b64 v[194:195], 0\n" \
"v_bitop3_b32 v209, v0, v2, s55 bitop3:0x7e\nv_bitop3_b32 v210, v32, v34, s55 bitop3:0x7e\nv_or3_b32 v208, v209, v210, 0\nv_bitop3_b32 v209, v64, v66, s55 bitop3:0x7e\nv_bitop3_b32 v210, v4, v6, s55 bitop3:0x7e\nv_or3_b32 v208, v209, v210, v208\nv_bitop3_b32 v209, v36, v38, s55 bitop3:0x7e\nv_bitop3_b32 v210, v68, v70, s55 bitop3:0x7e\n" \
"v_or3_b32 v208, v209, v210, v208\nv_bitop3_b32 v209, v8, v10, s55 bitop3:0x7e\nv_bitop3_b32 v210, v40, v42, s55 bitop3:0x7e\nv_or3_b32 v208, v209, v210, v208\nv_bitop3_b32 v209, v72, v74, s55 bitop3:0x7e\nv_bitop3_b32 v210, v12, v14, s55 bitop3:0x7e\nv_or3_b32 v208, v209, v210, v208\nv_bitop3_b32 v209, v44, v46, s55 bitop3:0x7e\n" \
"v_bitop3_b32 v210, v76, v78, s55 bitop3:0x7e\nv_or3_b32 v208, v209, v210, v208\nv_bitop3_b32 v209, v16, v18, s55 bitop3:0x7e\nv_bitop3_b32 v210, v48, v50, s55 bitop3:0x7e\nv_or3_b32 v208, v209, v210, v208\nv_bitop3_b32 v209, v80, v82, s55 bitop3:0x7e\nv_bitop3_b32 v210, v20, v22, s55 bitop3:0x7e\nv_or3_b32 v208, v209, v210, v208\n" \
"v_bitop3_b32 v209, v52, v54, s55 bitop3:0x7e\nv_bitop3_b32 v210, v84, v86, s55 bitop3:0x7e\nv_or3_b32 v208, v209, v210, v208\nv_bitop3_b32 v209, v24, v26, s55 bitop3:0x7e\nv_bitop3_b32 v210, v56, v58, s55 bitop3:0x7e\nv_or3_b32 v208, v209, v210, v208\nv_bitop3_b32 v209, v88, v90, s55 bitop3:0x7e\nv_bitop3_b32 v210, v28, v30, s55 bitop3:0x7e\n" \
"v_or3_b32 v208, v209, v210, v208\nv_bitop3_b32 v209, v60, v62, s55 bitop3:0x7e\nv_bitop3_b32 v210, v92, v94, s55 bitop3:0x7e\nv_or3_b32 v208, v209, v210, v208\ns_waitcnt vmcnt(18)\nv_bitop3_b32 v209, v96, v98, s55 bitop3:0x7e\nv_bitop3_b32 v210, v100, v102, s55 bitop3:0x7e\nv_or3_b32 v208, v209, v210, v208\n" \
"v_bitop3_b32 v209, v104, v106, s55 bitop3:0x7e\nv_bitop3_b32 v210, v108, v110, s55 bitop3:0x7e\nv_or3_b32 v208, v209, v210, v208\ns_waitcnt vmcnt(14)\nv_bitop3_b32 v209, v112, v114, s55 bitop3:0x7e\nv_bitop3_b32 v210, v116, v118, s55 bitop3:0x7e\nv_or3_b32 v208, v209, v210, v208\nv_bitop3_b32 v209, v120, v122, s55 bitop3:0x7e\n" \
"v_bitop3_b32 v210, v124, v126, s55 bitop3:0x7e\nv_or3_b32 v208, v209, v210, v208\nv_and_b32 v208, 1, v208\nv_cmp_ne_u32 vcc, 0, v208\ns_cbranch_vccnz .Lrd_yre%=\ns_waitcnt vmcnt(11)\nv_bitop3_b32 v208, v136, v138, s54 bitop3:0x7e\nv_bitop3_b32 v209, v140, v142, s54 bitop3:0x7e\n" \
"v_bitop3_b32 v208, v208, 1, v209 bitop3:0x80\nv_cmp_eq_u32 vcc, 0, v208\ns_cbranch_vccz .Lrd_u2%=\ns_branch .Lrd_ra%=\n.Lrd_yre%=:\ns_mov_b64 exec, vcc\ns_add_u32 s43, s43, 1\ns_cmp_lt_u32 s43, 262144\n" \
"s_cbranch_scc1 .Lrd_yld%=\ns_branch .Lrd_abort%=\n.Lrd_rb%=:\ns_mov_b64 exec, s[46:47]\ns_cbranch_execz .Lrd_rs0%=\nbuffer_load_dwordx4 v[160:163], v196, %[rs], %[sp] offen offset:0 sc1\n.Lrd_rs0%=:\ns_mov_b64 exec, s[48:49]\n" \
"s_cbranch_execz .Lrd_rs1%=\nbuffer_load_dwordx4 v[164:167], v196, %[rs], %[sp] offen offset:1024 sc1\n.Lrd_rs1%=:\ns_mov_b64 exec, s[50:51]\ns_cbranch_execz .Lrd_rs2%=\nbuffer_load_dwordx4 v[168:171], v196, %[rs], %[sp] offen offset:2048 sc1\n.Lrd_rs2%=:\ns_mov_b64 exec, s[52:53]\n" \
"s_cbranch_execz .Lrd_rs3%=\nbuffer_load_dwordx4 v[172:175], v196, %[rs], %[sp] offen offset:3072 sc1\n.Lrd_rs3%=:\ns_mov_b64 exec, s[64:65]\ns_cbranch_execz .Lrd_rs4%=\nbuffer_load_dwordx4 v[176:179], v196, %[rs], s56 offen offset:0 sc1\n.Lrd_rs4%=:\ns_mov_b64 exec, s[86:87]\n" \
"s_cbranch_execz .Lrd_rs5%=\nbuffer_load_dwordx4 v[128:131], v196, %[rs], %[su] offen offset:0 sc1\nbuffer_load_dwordx4 v[136:139], v196, %[rs], %[su] offen offset:2048 sc1\n.Lrd_rs5%=:\ns_mov_b64 exec, s[88:89]\ns_cbranch_execz .Lrd_rs6%=\nbuffer_load_dwordx4 v[144:147], v196, %[rs], s57 offen offset:0 sc1\nbuffer_load_dwordx4 v[152:155], v196, %[rs], s57 offen offset:2048 sc1\n" \
".Lrd_rs6%=:\ns_mov_b64 exec, s[68:69]\ns_cbranch_execz .Lrd_rs7%=\nbuffer_load_dwordx4 v[132:135], v196, %[rs], %[su] offen offset:1024 sc1\nbuffer_load_dwordx4 v[140:143], v196, %[rs], %[su] offen offset:3072 sc1\n.Lrd_rs7%=:\ns_mov_b64 exec, s[62:63]\ns_cbranch_execz .Lrd_rs8%=\n" \
"buffer_load_dwordx4 v[148:151], v196, %[rs], s57 offen offset:1024 sc1\nbuffer_load_dwordx4 v[156:159], v196, %[rs], s57 offen offset:3072 sc1\n.Lrd_rs8%=:\ns_mov_b64 exec, -1\nv_mov_b64 v[180:181], 0\nv_mov_b64 v[182:183], 0\nv_mov_b64 v[184:185], 0\nv_mov_b64 v[186:187], 0\n" \
"v_mov_b64 v[188:189], 0\nv_mov_b64 v[190:191], 0\nv_mov_b64 v[192:193], 0\nv_mov_b64 v[194:195], 0\ns_waitcnt vmcnt(0)\ns_add_u32 s43, s43, 1\ns_cmp_lt_u32 s43, 262144\ns_cbranch_scc1 .Lrd_ra%=\n" \
"s_branch .Lrd_abort%=\n.Lrd_y1%=:\nv_accvgpr_read_b32 v206, a32\nbuffer_load_dword v208, v206, %[rs], 0 offen sc1\ns_waitcnt vmcnt(0)\nv_cmp_gt_i32 vcc, s40, v208\ns_cbranch_vccz .Lrd_y1r%=\ns_add_u32 s43, s43, 1\n" \
"s_cmp_lt_u32 s43, 262144\ns_cbranch_scc1 .Lrd_y1%=\ns_branch .Lrd_abort%=\n.Lrd_tre0%=:\ns_add_u32 s60, s58, 16384\nbuffer_load_dwordx4 v[212:215], v196, %[rs], s60 offen offset:0 sc1\ns_waitcnt vmcnt(0)\nv_bitop3_b32 v209, v212, v214, s61 bitop3:0x7e\n" \
"v_cmp_gt_i32 vcc, 0, v209\ns_cbranch_vccz .Lrd_tl0%=\ns_add_u32 s43, s43, 1\ns_cmp_lt_u32 s43, 262144\ns_cbranch_scc1 .Lrd_tre0%=\ns_branch .Lrd_abort%=\n.Lrd_tre1%=:\ns_add_u32 s60, s58, 0\n" \
"buffer_load_dwordx4 v[216:219], v196, %[rs], s60 offen offset:0 sc1\ns_waitcnt vmcnt(0)\nv_bitop3_b32 v209, v216, v218, s61 bitop3:0x7e\nv_cmp_gt_i32 vcc, 0, v209\ns_cbranch_vccz .Lrd_tl1%=\ns_add_u32 s43, s43, 1\ns_cmp_lt_u32 s43, 262144\ns_cbranch_scc1 .Lrd_tre1%=\n" \
"s_branch .Lrd_abort%=\n.Lrd_tre2%=:\ns_add_u32 s60, s58, 0\nbuffer_load_dwordx4 v[220:223], v196, %[rs], s60 offen offset:1024 sc1\ns_waitcnt vmcnt(0)\nv_bitop3_b32 v209, v220, v222, s61 bitop3:0x7e\nv_cmp_gt_i32 vcc, 0, v209\ns_cbranch_vccz .Lrd_tl2%=\n" \
"s_add_u32 s43, s43, 1\ns_cmp_lt_u32 s43, 262144\ns_cbranch_scc1 .Lrd_tre2%=\ns_branch .Lrd_abort%=\n.Lrd_tre3%=:\ns_add_u32 s60, s58, 16384\nbuffer_load_dwordx4 v[224:227], v196, %[rs], s60 offen offset:1024 sc1\ns_waitcnt vmcnt(0)\n" \
"v_bitop3_b32 v209, v224, v226, s61 bitop3:0x7e\nv_cmp_gt_i32 vcc, 0, v209\ns_cbranch_vccz .Lrd_tl3%=\ns_add_u32 s43, s43, 1\ns_cmp_lt_u32 s43, 262144\ns_cbranch_scc1 .Lrd_tre3%=\ns_branch .Lrd_abort%=\n.Lrd_tre4%=:\n" \
"s_add_u32 s60, s58, 0\nbuffer_load_dwordx4 v[228:231], v196, %[rs], s60 offen offset:2048 sc1\ns_waitcnt vmcnt(0)\nv_bitop3_b32 v209, v228, v230, s61 bitop3:0x7e\nv_cmp_gt_i32 vcc, 0, v209\ns_cbranch_vccz .Lrd_tl4%=\ns_add_u32 s43, s43, 1\ns_cmp_lt_u32 s43, 262144\n" \
"s_cbranch_scc1 .Lrd_tre4%=\ns_branch .Lrd_abort%=\n.Lrd_tre5%=:\ns_add_u32 s60, s58, 0\nbuffer_load_dwordx4 v[232:235], v196, %[rs], s60 offen offset:3072 sc1\ns_waitcnt vmcnt(0)\nv_bitop3_b32 v209, v232, v234, s61 bitop3:0x7e\nv_cmp_gt_i32 vcc, 0, v209\n" \
"s_cbranch_vccz .Lrd_tl5%=\ns_add_u32 s43, s43, 1\ns_cmp_lt_u32 s43, 262144\ns_cbranch_scc1 .Lrd_tre5%=\ns_branch .Lrd_abort%=\n.Lrd_tre6%=:\ns_add_u32 s60, s58, 16384\nbuffer_load_dwordx4 v[236:239], v196, %[rs], s60 offen offset:2048 sc1\n" \
"s_waitcnt vmcnt(0)\nv_bitop3_b32 v209, v236, v238, s61 bitop3:0x7e\nv_cmp_gt_i32 vcc, 0, v209\ns_cbranch_vccz .Lrd_tl6%=\ns_add_u32 s43, s43, 1\ns_cmp_lt_u32 s43, 262144\ns_cbranch_scc1 .Lrd_tre6%=\ns_branch .Lrd_abort%=\n" \
".Lrd_tre7%=:\ns_add_u32 s60, s58, 4096\nbuffer_load_dwordx4 v[240:243], v196, %[rs], s60 offen offset:0 sc1\ns_waitcnt vmcnt(0)\nv_bitop3_b32 v209, v240, v242, s61 bitop3:0x7e\nv_cmp_gt_i32 vcc, 0, v209\ns_cbranch_vccz .Lrd_tl7%=\ns_add_u32 s43, s43, 1\n" \
"s_cmp_lt_u32 s43, 262144\ns_cbranch_scc1 .Lrd_tre7%=\ns_branch .Lrd_abort%=\n.Lrd_tre8%=:\ns_add_u32 s60, s58, 4096\nbuffer_load_dwordx4 v[244:247], v196, %[rs], s60 offen offset:1024 sc1\ns_waitcnt vmcnt(0)\nv_bitop3_b32 v209, v244, v246, s61 bitop3:0x7e\n" \
"v_cmp_gt_i32 vcc, 0, v209\ns_cbranch_vccz .Lrd_tl8%=\ns_add_u32 s43, s43, 1\ns_cmp_lt_u32 s43, 262144\ns_cbranch_scc1 .Lrd_tre8%=\ns_branch .Lrd_abort%=\n.Lrd_tre9%=:\ns_add_u32 s60, s58, 16384\n" \
"buffer_load_dwordx4 v[248:251], v196, %[rs], s60 offen offset:3072 sc1\ns_waitcnt vmcnt(0)\nv_bitop3_b32 v209, v248, v250, s61 bitop3:0x7e\nv_cmp_gt_i32 vcc, 0, v209\ns_cbranch_vccz .Lrd_tl9%=\ns_add_u32 s43, s43, 1\ns_cmp_lt_u32 s43, 262144\ns_cbranch_scc1 .Lrd_tre9%=\n" \
"s_branch .Lrd_abort%=\n.Lrd_tre10%=:\ns_add_u32 s60, s58, 20480\nbuffer_load_dwordx4 v[252:255], v196, %[rs], s60 offen offset:0 sc1\ns_waitcnt vmcnt(0)\nv_bitop3_b32 v209, v252, v254, s61 bitop3:0x7e\nv_bitop3_b32 v210, v253, v255, s61 bitop3:0x7e\nv_or_b32 v209, v209, v210\n" \
"v_cmp_gt_i32 vcc, 0, v209\ns_cbranch_vccz .Lrd_tl10%=\ns_add_u32 s43, s43, 1\ns_cmp_lt_u32 s43, 262144\ns_cbranch_scc1 .Lrd_tre10%=\ns_branch .Lrd_abort%=\n.Lrd_tre11%=:\ns_add_u32 s60, s58, 20480\n" \
"buffer_load_dwordx4 v[160:163], v196, %[rs], s60 offen offset:2048 sc1\ns_waitcnt vmcnt(0)\nv_bitop3_b32 v209, v160, v162, s61 bitop3:0x7e\nv_bitop3_b32 v210, v161, v163, s61 bitop3:0x7e\nv_or_b32 v209, v209, v210\nv_cmp_gt_i32 vcc, 0, v209\ns_cbranch_vccz .Lrd_tl11%=\ns_add_u32 s43, s43, 1\n" \
"s_cmp_lt_u32 s43, 262144\ns_cbranch_scc1 .Lrd_tre11%=\ns_branch .Lrd_abort%=\n.Lrd_tre12%=:\ns_add_u32 s60, s58, 4096\nbuffer_load_dwordx4 v[164:167], v196, %[rs], s60 offen offset:2048 sc1\ns_waitcnt vmcnt(0)\nv_bitop3_b32 v209, v164, v166, s61 bitop3:0x7e\n" \
"v_cmp_gt_i32 vcc, 0, v209\ns_cbranch_vccz .Lrd_tl12%=\ns_add_u32 s43, s43, 1\ns_cmp_lt_u32 s43, 262144\ns_cbranch_scc1 .Lrd_tre12%=\ns_branch .Lrd_abort%=\n.Lrd_tre13%=:\ns_add_u32 s60, s58, 4096\n" \
"buffer_load_dwordx4 v[168:171], v196, %[rs], s60 offen offset:3072 sc1\ns_waitcnt vmcnt(0)\nv_bitop3_b32 v209, v168, v170, s61 bitop3:0x7e\nv_cmp_gt_i32 vcc, 0, v209\ns_cbranch_vccz .Lrd_tl13%=\ns_add_u32 s43, s43, 1\ns_cmp_lt_u32 s43, 262144\ns_cbranch_scc1 .Lrd_tre13%=\n" \
"s_branch .Lrd_abort%=\n.Lrd_tre14%=:\ns_add_u32 s60, s58, 20480\nbuffer_load_dwordx4 v[172:175], v196, %[rs], s60 offen offset:1024 sc1\ns_waitcnt vmcnt(0)\nv_bitop3_b32 v209, v172, v174, s61 bitop3:0x7e\nv_bitop3_b32 v210, v173, v175, s61 bitop3:0x7e\nv_or_b32 v209, v209, v210\n" \
"v_cmp_gt_i32 vcc, 0, v209\ns_cbranch_vccz .Lrd_tl14%=\ns_add_u32 s43, s43, 1\ns_cmp_lt_u32 s43, 262144\ns_cbranch_scc1 .Lrd_tre14%=\ns_branch .Lrd_abort%=\n.Lrd_tre15%=:\ns_add_u32 s60, s58, 20480\n" \
"buffer_load_dwordx4 v[176:179], v196, %[rs], s60 offen offset:3072 sc1\ns_waitcnt vmcnt(0)\nv_bitop3_b32 v209, v176, v178, s61 bitop3:0x7e\nv_bitop3_b32 v210, v177, v179, s61 bitop3:0x7e\nv_or_b32 v209, v209, v210\nv_cmp_gt_i32 vcc, 0, v209\ns_cbranch_vccz .Lrd_tl15%=\ns_add_u32 s43, s43, 1\n" \
"s_cmp_lt_u32 s43, 262144\ns_cbranch_scc1 .Lrd_tre15%=\ns_branch .Lrd_abort%=\n.Lrd_abort%=:\ns_mov_b64 exec, -1\nv_mov_b32 v16, 0\ns_movk_i32 s44, 6400\nbuffer_store_dword v16, off, %[rs], s44 sc1\n" \
".Lrd_end%=:\ns_waitcnt vmcnt(0) lgkmcnt(0)\n" \
""
#define GG_ASM \
"v_mbcnt_lo_u32_b32 v255, -1, 0\nv_mbcnt_hi_u32_b32 v255, -1, v255\nv_lshlrev_b32 v255, 2, v255\nv_add_u32 v255, %[tb], v255\nds_read_b32 v0, v255 offset:0\nds_read_b32 v1, v255 offset:256\nds_read_b32 v2, v255 offset:512\nds_read_b32 v3, v255 offset:768\n" \
"ds_read_b32 v4, v255 offset:1024\nds_read_b32 v5, v255 offset:1280\nds_read_b32 v92, v255 offset:1536\nds_read_b32 v93, v255 offset:1792\nds_read_b32 v94, v255 offset:2048\ns_waitcnt lgkmcnt(0)\nv_mov_b32 v6, 0x3f803f80\nv_mov_b32 v7, 0x3f803f80\n" \
"s_add_u32 m0, %[lw], 16384\ns_add_u32 s44, %[wo], 0\nbuffer_load_dwordx4 v92, %[rw], s44 offen nt lds\ns_add_u32 m0, %[lw], 17408\ns_add_u32 s44, %[wo], 57344\nbuffer_load_dwordx4 v92, %[rw], s44 offen nt lds\ns_add_u32 m0, %[lw], 18432\ns_add_u32 s44, %[wo], 114688\n" \
"buffer_load_dwordx4 v92, %[rw], s44 offen nt lds\ns_add_u32 m0, %[lw], 19456\ns_add_u32 s44, %[wo], 172032\nbuffer_load_dwordx4 v92, %[rw], s44 offen nt lds\ns_add_u32 m0, %[lw], 20480\ns_add_u32 s44, %[wo], 229376\nbuffer_load_dwordx4 v92, %[rw], s44 offen nt lds\ns_add_u32 m0, %[lw], 21504\n" \
"s_add_u32 s44, %[wo], 286720\nbuffer_load_dwordx4 v92, %[rw], s44 offen nt lds\ns_add_u32 m0, %[lw], 22528\ns_add_u32 s44, %[wo], 344064\nbuffer_load_dwordx4 v92, %[rw], s44 offen nt lds\ns_add_u32 m0, %[lw], 23552\ns_add_u32 s44, %[wo], 401408\nbuffer_load_dwordx4 v92, %[rw], s44 offen nt lds\n" \
"s_add_u32 m0, %[tb], 0\ns_add_u32 s44, %[wo], 1835008\nbuffer_load_dwordx4 v92, %[rw], s44 offen nt lds\ns_add_u32 m0, %[tb], 1024\ns_add_u32 s44, %[wo], 1892352\nbuffer_load_dwordx4 v92, %[rw], s44 offen nt lds\ns_add_u32 m0, %[tb], 2048\ns_add_u32 s44, %[wo], 1949696\n" \
"buffer_load_dwordx4 v92, %[rw], s44 offen nt lds\ns_add_u32 m0, %[tb], 3072\ns_add_u32 s44, %[wo], 2007040\nbuffer_load_dwordx4 v92, %[rw], s44 offen nt lds\ns_add_u32 m0, %[tb], 4096\ns_add_u32 s44, %[wo], 2064384\nbuffer_load_dwordx4 v92, %[rw], s44 offen nt lds\ns_add_u32 m0, %[tb], 5120\n" \
"s_add_u32 s44, %[wo], 2121728\nbuffer_load_dwordx4 v92, %[rw], s44 offen nt lds\ns_add_u32 m0, %[tb], 6144\ns_add_u32 s44, %[wo], 2179072\nbuffer_load_dwordx4 v92, %[rw], s44 offen nt lds\ns_add_u32 m0, %[tb], 7168\ns_add_u32 s44, %[wo], 2236416\nbuffer_load_dwordx4 v92, %[rw], s44 offen nt lds\n" \
"s_add_u32 m0, %[tb], 8192\ns_add_u32 s44, %[wo], 3670016\nbuffer_load_dwordx4 v92, %[rw], s44 offen nt lds\ns_add_u32 m0, %[tb], 9216\ns_add_u32 s44, %[wo], 3727360\nbuffer_load_dwordx4 v92, %[rw], s44 offen nt lds\ns_add_u32 m0, %[tb], 10240\ns_add_u32 s44, %[wo], 3784704\n" \
"buffer_load_dwordx4 v92, %[rw], s44 offen nt lds\ns_add_u32 m0, %[tb], 11264\ns_add_u32 s44, %[wo], 3842048\nbuffer_load_dwordx4 v92, %[rw], s44 offen nt lds\ns_add_u32 m0, %[lw], 0\ns_add_u32 s44, %[wo], 3899392\nbuffer_load_dwordx4 v92, %[rw], s44 offen nt lds\ns_add_u32 m0, %[lw], 1024\n" \
"s_add_u32 s44, %[wo], 3956736\nbuffer_load_dwordx4 v92, %[rw], s44 offen nt lds\ns_add_u32 m0, %[lw], 2048\ns_add_u32 s44, %[wo], 4014080\nbuffer_load_dwordx4 v92, %[rw], s44 offen nt lds\ns_add_u32 m0, %[lw], 3072\ns_add_u32 s44, %[wo], 4071424\nbuffer_load_dwordx4 v92, %[rw], s44 offen nt lds\n" \
"s_add_u32 m0, %[lw], 4096\ns_add_u32 s44, %[wo], 5505024\nbuffer_load_dwordx4 v92, %[rw], s44 offen nt lds\ns_add_u32 m0, %[lw], 5120\ns_add_u32 s44, %[wo], 5562368\nbuffer_load_dwordx4 v92, %[rw], s44 offen nt lds\ns_add_u32 m0, %[lw], 6144\ns_add_u32 s44, %[wo], 5619712\n" \
"buffer_load_dwordx4 v92, %[rw], s44 offen nt lds\ns_add_u32 m0, %[lw], 7168\ns_add_u32 s44, %[wo], 5677056\nbuffer_load_dwordx4 v92, %[rw], s44 offen nt lds\ns_add_u32 m0, %[lw], 8192\ns_add_u32 s44, %[wo], 5734400\nbuffer_load_dwordx4 v92, %[rw], s44 offen nt lds\ns_add_u32 m0, %[lw], 9216\n" \
"s_add_u32 s44, %[wo], 5791744\nbuffer_load_dwordx4 v92, %[rw], s44 offen nt lds\ns_add_u32 m0, %[lw], 10240\ns_add_u32 s44, %[wo], 5849088\nbuffer_load_dwordx4 v92, %[rw], s44 offen nt lds\ns_add_u32 m0, %[lw], 11264\ns_add_u32 s44, %[wo], 5906432\nbuffer_load_dwordx4 v92, %[rw], s44 offen nt lds\n" \
"s_waitcnt vmcnt(28)\ns_barrier\ns_add_u32 m0, %[lw], 12288\ns_add_u32 s44, %[wo], 458752\nbuffer_load_dwordx4 v92, %[rw], s44 offen nt lds\ns_add_u32 m0, %[lw], 13312\ns_add_u32 s44, %[wo], 516096\nbuffer_load_dwordx4 v92, %[rw], s44 offen nt lds\n" \
"s_add_u32 m0, %[lw], 14336\ns_add_u32 s44, %[wo], 573440\nbuffer_load_dwordx4 v92, %[rw], s44 offen nt lds\ns_add_u32 m0, %[lw], 15360\ns_add_u32 s44, %[wo], 630784\nbuffer_load_dwordx4 v92, %[rw], s44 offen nt lds\nds_read_b64_tr_b16 a[0:1], v94 offset:16384\nds_read_b64_tr_b16 a[2:3], v94 offset:16896\n" \
"ds_read_b64_tr_b16 a[8:9], v94 offset:16392\nds_read_b64_tr_b16 a[10:11], v94 offset:16904\nds_read_b64_tr_b16 a[16:17], v94 offset:40960\nds_read_b64_tr_b16 a[18:19], v94 offset:41472\nds_read_b64_tr_b16 a[24:25], v94 offset:40968\nds_read_b64_tr_b16 a[26:27], v94 offset:41480\ns_waitcnt lgkmcnt(0)\nv_mfma_f32_16x16x16_bf16 v[8:11], a[0:1], v[6:7], 0\n" \
"v_mfma_f32_16x16x16_bf16 v[12:15], a[8:9], v[6:7], 0\nv_mfma_f32_16x16x16_bf16 v[16:19], a[16:17], v[6:7], 0\nv_mfma_f32_16x16x16_bf16 v[20:23], a[24:25], v[6:7], 0\nv_mfma_f32_16x16x16_bf16 v[8:11], a[2:3], v[6:7], v[8:11]\nv_mfma_f32_16x16x16_bf16 v[12:15], a[10:11], v[6:7], v[12:15]\nv_mfma_f32_16x16x16_bf16 v[16:19], a[18:19], v[6:7], v[16:19]\nv_mfma_f32_16x16x16_bf16 v[20:23], a[26:27], v[6:7], v[20:23]\ns_waitcnt vmcnt(28)\n" \
"s_barrier\ns_add_u32 m0, %[lw], 16384\ns_add_u32 s44, %[wo], 688128\nbuffer_load_dwordx4 v92, %[rw], s44 offen nt lds\ns_add_u32 m0, %[lw], 17408\ns_add_u32 s44, %[wo], 745472\nbuffer_load_dwordx4 v92, %[rw], s44 offen nt lds\ns_add_u32 m0, %[lw], 18432\n" \
"s_add_u32 s44, %[wo], 802816\nbuffer_load_dwordx4 v92, %[rw], s44 offen nt lds\ns_add_u32 m0, %[lw], 19456\ns_add_u32 s44, %[wo], 860160\nbuffer_load_dwordx4 v92, %[rw], s44 offen nt lds\nds_read_b64_tr_b16 a[4:5], v94 offset:20480\nds_read_b64_tr_b16 a[6:7], v94 offset:20992\nds_read_b64_tr_b16 a[12:13], v94 offset:20488\n" \
"ds_read_b64_tr_b16 a[14:15], v94 offset:21000\nds_read_b64_tr_b16 a[20:21], v94 offset:45056\nds_read_b64_tr_b16 a[22:23], v94 offset:45568\nds_read_b64_tr_b16 a[28:29], v94 offset:45064\nds_read_b64_tr_b16 a[30:31], v94 offset:45576\ns_waitcnt lgkmcnt(0)\nv_mfma_f32_16x16x16_bf16 v[8:11], a[4:5], v[6:7], v[8:11]\nv_mfma_f32_16x16x16_bf16 v[12:15], a[12:13], v[6:7], v[12:15]\n" \
"v_mfma_f32_16x16x16_bf16 v[16:19], a[20:21], v[6:7], v[16:19]\nv_mfma_f32_16x16x16_bf16 v[20:23], a[28:29], v[6:7], v[20:23]\nv_mfma_f32_16x16x16_bf16 v[8:11], a[6:7], v[6:7], v[8:11]\nv_mfma_f32_16x16x16_bf16 v[12:15], a[14:15], v[6:7], v[12:15]\nv_mfma_f32_16x16x16_bf16 v[16:19], a[22:23], v[6:7], v[16:19]\nv_mfma_f32_16x16x16_bf16 v[20:23], a[30:31], v[6:7], v[20:23]\ns_waitcnt vmcnt(28)\ns_barrier\n" \
"s_add_u32 m0, %[lw], 20480\ns_add_u32 s44, %[wo], 917504\nbuffer_load_dwordx4 v92, %[rw], s44 offen nt lds\ns_add_u32 m0, %[lw], 21504\ns_add_u32 s44, %[wo], 974848\nbuffer_load_dwordx4 v92, %[rw], s44 offen nt lds\ns_add_u32 m0, %[lw], 22528\ns_add_u32 s44, %[wo], 1032192\n" \
"buffer_load_dwordx4 v92, %[rw], s44 offen nt lds\ns_add_u32 m0, %[lw], 23552\ns_add_u32 s44, %[wo], 1089536\nbuffer_load_dwordx4 v92, %[rw], s44 offen nt lds\nds_read_b64_tr_b16 a[104:105], v93 offset:0\nds_read_b64_tr_b16 a[106:107], v93 offset:512\nds_read_b64_tr_b16 a[112:113], v93 offset:8\nds_read_b64_tr_b16 a[114:115], v93 offset:520\n" \
"ds_read_b64_tr_b16 a[120:121], v93 offset:12288\nds_read_b64_tr_b16 a[122:123], v93 offset:12800\nds_read_b64_tr_b16 a[128:129], v93 offset:12296\nds_read_b64_tr_b16 a[130:131], v93 offset:12808\ns_waitcnt lgkmcnt(0)\nv_mfma_f32_16x16x16_bf16 v[8:11], a[104:105], v[6:7], v[8:11]\nv_mfma_f32_16x16x16_bf16 v[12:15], a[112:113], v[6:7], v[12:15]\nv_mfma_f32_16x16x16_bf16 v[16:19], a[120:121], v[6:7], v[16:19]\n" \
"v_mfma_f32_16x16x16_bf16 v[20:23], a[128:129], v[6:7], v[20:23]\nv_mfma_f32_16x16x16_bf16 v[8:11], a[106:107], v[6:7], v[8:11]\nv_mfma_f32_16x16x16_bf16 v[12:15], a[114:115], v[6:7], v[12:15]\nv_mfma_f32_16x16x16_bf16 v[16:19], a[122:123], v[6:7], v[16:19]\nv_mfma_f32_16x16x16_bf16 v[20:23], a[130:131], v[6:7], v[20:23]\ns_waitcnt vmcnt(28)\ns_barrier\ns_add_u32 m0, %[tb], 0\n" \
"s_add_u32 s44, %[wo], 1146880\nbuffer_load_dwordx4 v92, %[rw], s44 offen nt lds\ns_add_u32 m0, %[tb], 1024\ns_add_u32 s44, %[wo], 1204224\nbuffer_load_dwordx4 v92, %[rw], s44 offen nt lds\ns_add_u32 m0, %[tb], 2048\ns_add_u32 s44, %[wo], 1261568\nbuffer_load_dwordx4 v92, %[rw], s44 offen nt lds\n" \
"s_add_u32 m0, %[tb], 3072\ns_add_u32 s44, %[wo], 1318912\nbuffer_load_dwordx4 v92, %[rw], s44 offen nt lds\nds_read_b64_tr_b16 a[108:109], v93 offset:4096\nds_read_b64_tr_b16 a[110:111], v93 offset:4608\nds_read_b64_tr_b16 a[116:117], v93 offset:4104\nds_read_b64_tr_b16 a[118:119], v93 offset:4616\nds_read_b64_tr_b16 a[124:125], v93 offset:16384\n" \
"ds_read_b64_tr_b16 a[126:127], v93 offset:16896\nds_read_b64_tr_b16 a[132:133], v93 offset:16392\nds_read_b64_tr_b16 a[134:135], v93 offset:16904\ns_waitcnt lgkmcnt(0)\nv_mfma_f32_16x16x16_bf16 v[8:11], a[108:109], v[6:7], v[8:11]\nv_mfma_f32_16x16x16_bf16 v[12:15], a[116:117], v[6:7], v[12:15]\nv_mfma_f32_16x16x16_bf16 v[16:19], a[124:125], v[6:7], v[16:19]\nv_mfma_f32_16x16x16_bf16 v[20:23], a[132:133], v[6:7], v[20:23]\n" \
"v_mfma_f32_16x16x16_bf16 v[8:11], a[110:111], v[6:7], v[8:11]\nv_mfma_f32_16x16x16_bf16 v[12:15], a[118:119], v[6:7], v[12:15]\nv_mfma_f32_16x16x16_bf16 v[16:19], a[126:127], v[6:7], v[16:19]\nv_mfma_f32_16x16x16_bf16 v[20:23], a[134:135], v[6:7], v[20:23]\ns_waitcnt vmcnt(28)\ns_barrier\ns_add_u32 m0, %[tb], 4096\ns_add_u32 s44, %[wo], 1376256\n" \
"buffer_load_dwordx4 v92, %[rw], s44 offen nt lds\ns_add_u32 m0, %[tb], 5120\ns_add_u32 s44, %[wo], 1433600\nbuffer_load_dwordx4 v92, %[rw], s44 offen nt lds\ns_add_u32 m0, %[tb], 6144\ns_add_u32 s44, %[wo], 1490944\nbuffer_load_dwordx4 v92, %[rw], s44 offen nt lds\ns_add_u32 m0, %[tb], 7168\n" \
"s_add_u32 s44, %[wo], 1548288\nbuffer_load_dwordx4 v92, %[rw], s44 offen nt lds\nds_read_b64_tr_b16 a[208:209], v93 offset:8192\nds_read_b64_tr_b16 a[210:211], v93 offset:8704\nds_read_b64_tr_b16 a[216:217], v93 offset:8200\nds_read_b64_tr_b16 a[218:219], v93 offset:8712\nds_read_b64_tr_b16 a[224:225], v93 offset:20480\nds_read_b64_tr_b16 a[226:227], v93 offset:20992\n" \
"ds_read_b64_tr_b16 a[232:233], v93 offset:20488\nds_read_b64_tr_b16 a[234:235], v93 offset:21000\ns_waitcnt lgkmcnt(0)\nv_mfma_f32_16x16x16_bf16 v[8:11], a[208:209], v[6:7], v[8:11]\nv_mfma_f32_16x16x16_bf16 v[12:15], a[216:217], v[6:7], v[12:15]\nv_mfma_f32_16x16x16_bf16 v[16:19], a[224:225], v[6:7], v[16:19]\nv_mfma_f32_16x16x16_bf16 v[20:23], a[232:233], v[6:7], v[20:23]\nv_mfma_f32_16x16x16_bf16 v[8:11], a[210:211], v[6:7], v[8:11]\n" \
"v_mfma_f32_16x16x16_bf16 v[12:15], a[218:219], v[6:7], v[12:15]\nv_mfma_f32_16x16x16_bf16 v[16:19], a[226:227], v[6:7], v[16:19]\nv_mfma_f32_16x16x16_bf16 v[20:23], a[234:235], v[6:7], v[20:23]\ns_waitcnt vmcnt(28)\ns_barrier\ns_add_u32 m0, %[tb], 8192\ns_add_u32 s44, %[wo], 1605632\nbuffer_load_dwordx4 v92, %[rw], s44 offen nt lds\n" \
"s_add_u32 m0, %[tb], 9216\ns_add_u32 s44, %[wo], 1662976\nbuffer_load_dwordx4 v92, %[rw], s44 offen nt lds\ns_add_u32 m0, %[tb], 10240\ns_add_u32 s44, %[wo], 1720320\nbuffer_load_dwordx4 v92, %[rw], s44 offen nt lds\ns_add_u32 m0, %[tb], 11264\ns_add_u32 s44, %[wo], 1777664\n" \
"buffer_load_dwordx4 v92, %[rw], s44 offen nt lds\nds_read_b64_tr_b16 a[212:213], v94 offset:0\nds_read_b64_tr_b16 a[214:215], v94 offset:512\nds_read_b64_tr_b16 a[220:221], v94 offset:8\nds_read_b64_tr_b16 a[222:223], v94 offset:520\nds_read_b64_tr_b16 a[228:229], v94 offset:24576\nds_read_b64_tr_b16 a[230:231], v94 offset:25088\nds_read_b64_tr_b16 a[236:237], v94 offset:24584\n" \
"ds_read_b64_tr_b16 a[238:239], v94 offset:25096\ns_waitcnt lgkmcnt(0)\nv_mfma_f32_16x16x16_bf16 v[8:11], a[212:213], v[6:7], v[8:11]\nv_mfma_f32_16x16x16_bf16 v[12:15], a[220:221], v[6:7], v[12:15]\nv_mfma_f32_16x16x16_bf16 v[16:19], a[228:229], v[6:7], v[16:19]\nv_mfma_f32_16x16x16_bf16 v[20:23], a[236:237], v[6:7], v[20:23]\nv_mfma_f32_16x16x16_bf16 v[8:11], a[214:215], v[6:7], v[8:11]\nv_mfma_f32_16x16x16_bf16 v[12:15], a[222:223], v[6:7], v[12:15]\n" \
"v_mfma_f32_16x16x16_bf16 v[16:19], a[230:231], v[6:7], v[16:19]\nv_mfma_f32_16x16x16_bf16 v[20:23], a[238:239], v[6:7], v[20:23]\ns_waitcnt vmcnt(28)\ns_barrier\ns_add_u32 m0, %[lw], 0\ns_add_u32 s44, %[wo], 2293760\nbuffer_load_dwordx4 v92, %[rw], s44 offen nt lds\ns_add_u32 m0, %[lw], 1024\n" \
"s_add_u32 s44, %[wo], 2351104\nbuffer_load_dwordx4 v92, %[rw], s44 offen nt lds\ns_add_u32 m0, %[lw], 2048\ns_add_u32 s44, %[wo], 2408448\nbuffer_load_dwordx4 v92, %[rw], s44 offen nt lds\ns_add_u32 m0, %[lw], 3072\ns_add_u32 s44, %[wo], 2465792\nbuffer_load_dwordx4 v92, %[rw], s44 offen nt lds\n" \
"ds_read_b64_tr_b16 v[152:153], v94 offset:4096\nds_read_b64_tr_b16 v[154:155], v94 offset:4608\nds_read_b64_tr_b16 v[160:161], v94 offset:4104\nds_read_b64_tr_b16 v[162:163], v94 offset:4616\nds_read_b64_tr_b16 v[168:169], v94 offset:28672\nds_read_b64_tr_b16 v[170:171], v94 offset:29184\nds_read_b64_tr_b16 v[176:177], v94 offset:28680\nds_read_b64_tr_b16 v[178:179], v94 offset:29192\n" \
"s_waitcnt lgkmcnt(0)\nv_mfma_f32_16x16x16_bf16 v[8:11], v[152:153], v[6:7], v[8:11]\nv_mfma_f32_16x16x16_bf16 v[12:15], v[160:161], v[6:7], v[12:15]\nv_mfma_f32_16x16x16_bf16 v[16:19], v[168:169], v[6:7], v[16:19]\nv_mfma_f32_16x16x16_bf16 v[20:23], v[176:177], v[6:7], v[20:23]\nv_mfma_f32_16x16x16_bf16 v[8:11], v[154:155], v[6:7], v[8:11]\nv_mfma_f32_16x16x16_bf16 v[12:15], v[162:163], v[6:7], v[12:15]\nv_mfma_f32_16x16x16_bf16 v[16:19], v[170:171], v[6:7], v[16:19]\n" \
"v_mfma_f32_16x16x16_bf16 v[20:23], v[178:179], v[6:7], v[20:23]\ns_waitcnt vmcnt(28)\ns_barrier\ns_add_u32 m0, %[lw], 4096\ns_add_u32 s44, %[wo], 2523136\nbuffer_load_dwordx4 v92, %[rw], s44 offen nt lds\ns_add_u32 m0, %[lw], 5120\ns_add_u32 s44, %[wo], 2580480\n" \
"buffer_load_dwordx4 v92, %[rw], s44 offen nt lds\ns_add_u32 m0, %[lw], 6144\ns_add_u32 s44, %[wo], 2637824\nbuffer_load_dwordx4 v92, %[rw], s44 offen nt lds\ns_add_u32 m0, %[lw], 7168\ns_add_u32 s44, %[wo], 2695168\nbuffer_load_dwordx4 v92, %[rw], s44 offen nt lds\nds_read_b64_tr_b16 v[156:157], v94 offset:8192\n" \
"ds_read_b64_tr_b16 v[158:159], v94 offset:8704\nds_read_b64_tr_b16 v[164:165], v94 offset:8200\nds_read_b64_tr_b16 v[166:167], v94 offset:8712\nds_read_b64_tr_b16 v[172:173], v94 offset:32768\nds_read_b64_tr_b16 v[174:175], v94 offset:33280\nds_read_b64_tr_b16 v[180:181], v94 offset:32776\nds_read_b64_tr_b16 v[182:183], v94 offset:33288\ns_waitcnt lgkmcnt(0)\n" \
"v_mfma_f32_16x16x16_bf16 v[8:11], v[156:157], v[6:7], v[8:11]\nv_mfma_f32_16x16x16_bf16 v[12:15], v[164:165], v[6:7], v[12:15]\nv_mfma_f32_16x16x16_bf16 v[16:19], v[172:173], v[6:7], v[16:19]\nv_mfma_f32_16x16x16_bf16 v[20:23], v[180:181], v[6:7], v[20:23]\nv_mfma_f32_16x16x16_bf16 v[8:11], v[158:159], v[6:7], v[8:11]\nv_mfma_f32_16x16x16_bf16 v[12:15], v[166:167], v[6:7], v[12:15]\nv_mfma_f32_16x16x16_bf16 v[16:19], v[174:175], v[6:7], v[16:19]\nv_mfma_f32_16x16x16_bf16 v[20:23], v[182:183], v[6:7], v[20:23]\n" \
"s_waitcnt vmcnt(28)\ns_barrier\ns_add_u32 m0, %[lw], 8192\ns_add_u32 s44, %[wo], 2752512\nbuffer_load_dwordx4 v92, %[rw], s44 offen nt lds\ns_add_u32 m0, %[lw], 9216\ns_add_u32 s44, %[wo], 2809856\nbuffer_load_dwordx4 v92, %[rw], s44 offen nt lds\n" \
"s_add_u32 m0, %[lw], 10240\ns_add_u32 s44, %[wo], 2867200\nbuffer_load_dwordx4 v92, %[rw], s44 offen nt lds\ns_add_u32 m0, %[lw], 11264\ns_add_u32 s44, %[wo], 2924544\nbuffer_load_dwordx4 v92, %[rw], s44 offen nt lds\nds_read_b64_tr_b16 a[32:33], v94 offset:12288\nds_read_b64_tr_b16 a[34:35], v94 offset:12800\n" \
"ds_read_b64_tr_b16 v[88:89], v94 offset:12296\nds_read_b64_tr_b16 v[90:91], v94 offset:12808\nds_read_b64_tr_b16 a[40:41], v94 offset:36864\nds_read_b64_tr_b16 a[42:43], v94 offset:37376\nds_read_b64_tr_b16 a[48:49], v94 offset:36872\nds_read_b64_tr_b16 a[50:51], v94 offset:37384\ns_waitcnt lgkmcnt(0)\nv_mfma_f32_16x16x16_bf16 v[8:11], a[32:33], v[6:7], v[8:11]\n" \
"v_mfma_f32_16x16x16_bf16 v[12:15], v[88:89], v[6:7], v[12:15]\nv_mfma_f32_16x16x16_bf16 v[16:19], a[40:41], v[6:7], v[16:19]\nv_mfma_f32_16x16x16_bf16 v[20:23], a[48:49], v[6:7], v[20:23]\nv_mfma_f32_16x16x16_bf16 v[8:11], a[34:35], v[6:7], v[8:11]\nv_mfma_f32_16x16x16_bf16 v[12:15], v[90:91], v[6:7], v[12:15]\nv_mfma_f32_16x16x16_bf16 v[16:19], a[42:43], v[6:7], v[16:19]\nv_mfma_f32_16x16x16_bf16 v[20:23], a[50:51], v[6:7], v[20:23]\ns_waitcnt vmcnt(28)\n" \
"s_barrier\ns_add_u32 m0, %[lw], 12288\ns_add_u32 s44, %[wo], 2981888\nbuffer_load_dwordx4 v92, %[rw], s44 offen nt lds\ns_add_u32 m0, %[lw], 13312\ns_add_u32 s44, %[wo], 3039232\nbuffer_load_dwordx4 v92, %[rw], s44 offen nt lds\ns_add_u32 m0, %[lw], 14336\n" \
"s_add_u32 s44, %[wo], 3096576\nbuffer_load_dwordx4 v92, %[rw], s44 offen nt lds\ns_add_u32 m0, %[lw], 15360\ns_add_u32 s44, %[wo], 3153920\nbuffer_load_dwordx4 v92, %[rw], s44 offen nt lds\nds_read_b64_tr_b16 a[36:37], v94 offset:16384\nds_read_b64_tr_b16 a[38:39], v94 offset:16896\nds_read_b64_tr_b16 v[84:85], v94 offset:16392\n" \
"ds_read_b64_tr_b16 v[86:87], v94 offset:16904\nds_read_b64_tr_b16 a[44:45], v94 offset:40960\nds_read_b64_tr_b16 a[46:47], v94 offset:41472\nds_read_b64_tr_b16 a[52:53], v94 offset:40968\nds_read_b64_tr_b16 a[54:55], v94 offset:41480\ns_waitcnt lgkmcnt(0)\nv_mfma_f32_16x16x16_bf16 v[8:11], a[36:37], v[6:7], v[8:11]\nv_mfma_f32_16x16x16_bf16 v[12:15], v[84:85], v[6:7], v[12:15]\n" \
"v_mfma_f32_16x16x16_bf16 v[16:19], a[44:45], v[6:7], v[16:19]\nv_mfma_f32_16x16x16_bf16 v[20:23], a[52:53], v[6:7], v[20:23]\nv_mfma_f32_16x16x16_bf16 v[8:11], a[38:39], v[6:7], v[8:11]\nv_mfma_f32_16x16x16_bf16 v[12:15], v[86:87], v[6:7], v[12:15]\nv_mfma_f32_16x16x16_bf16 v[16:19], a[46:47], v[6:7], v[16:19]\nv_mfma_f32_16x16x16_bf16 v[20:23], a[54:55], v[6:7], v[20:23]\ns_waitcnt vmcnt(28)\ns_barrier\n" \
"s_add_u32 m0, %[lw], 16384\ns_add_u32 s44, %[wo], 3211264\nbuffer_load_dwordx4 v92, %[rw], s44 offen nt lds\ns_add_u32 m0, %[lw], 17408\ns_add_u32 s44, %[wo], 3268608\nbuffer_load_dwordx4 v92, %[rw], s44 offen nt lds\ns_add_u32 m0, %[lw], 18432\ns_add_u32 s44, %[wo], 3325952\n" \
"buffer_load_dwordx4 v92, %[rw], s44 offen nt lds\ns_add_u32 m0, %[lw], 19456\ns_add_u32 s44, %[wo], 3383296\nbuffer_load_dwordx4 v92, %[rw], s44 offen nt lds\nds_read_b64_tr_b16 a[56:57], v94 offset:20480\nds_read_b64_tr_b16 a[58:59], v94 offset:20992\nds_read_b64_tr_b16 a[64:65], v94 offset:20488\nds_read_b64_tr_b16 a[66:67], v94 offset:21000\n" \
"ds_read_b64_tr_b16 v[80:81], v94 offset:45056\nds_read_b64_tr_b16 v[82:83], v94 offset:45568\nds_read_b64_tr_b16 a[72:73], v94 offset:45064\nds_read_b64_tr_b16 a[74:75], v94 offset:45576\ns_waitcnt lgkmcnt(0)\nv_mfma_f32_16x16x16_bf16 v[8:11], a[56:57], v[6:7], v[8:11]\nv_mfma_f32_16x16x16_bf16 v[12:15], a[64:65], v[6:7], v[12:15]\nv_mfma_f32_16x16x16_bf16 v[16:19], v[80:81], v[6:7], v[16:19]\n" \
"v_mfma_f32_16x16x16_bf16 v[20:23], a[72:73], v[6:7], v[20:23]\nv_mfma_f32_16x16x16_bf16 v[8:11], a[58:59], v[6:7], v[8:11]\nv_mfma_f32_16x16x16_bf16 v[12:15], a[66:67], v[6:7], v[12:15]\nv_mfma_f32_16x16x16_bf16 v[16:19], v[82:83], v[6:7], v[16:19]\nv_mfma_f32_16x16x16_bf16 v[20:23], a[74:75], v[6:7], v[20:23]\ns_waitcnt vmcnt(28)\ns_barrier\ns_add_u32 m0, %[lw], 20480\n" \
"s_add_u32 s44, %[wo], 3440640\nbuffer_load_dwordx4 v92, %[rw], s44 offen nt lds\ns_add_u32 m0, %[lw], 21504\ns_add_u32 s44, %[wo], 3497984\nbuffer_load_dwordx4 v92, %[rw], s44 offen nt lds\ns_add_u32 m0, %[lw], 22528\ns_add_u32 s44, %[wo], 3555328\nbuffer_load_dwordx4 v92, %[rw], s44 offen nt lds\n" \
"s_add_u32 m0, %[lw], 23552\ns_add_u32 s44, %[wo], 3612672\nbuffer_load_dwordx4 v92, %[rw], s44 offen nt lds\nds_read_b64_tr_b16 a[60:61], v93 offset:0\nds_read_b64_tr_b16 a[62:63], v93 offset:512\nds_read_b64_tr_b16 a[68:69], v93 offset:8\nds_read_b64_tr_b16 a[70:71], v93 offset:520\nds_read_b64_tr_b16 v[76:77], v93 offset:12288\n" \
"ds_read_b64_tr_b16 v[78:79], v93 offset:12800\nds_read_b64_tr_b16 a[76:77], v93 offset:12296\nds_read_b64_tr_b16 a[78:79], v93 offset:12808\ns_waitcnt lgkmcnt(0)\nv_mfma_f32_16x16x16_bf16 v[8:11], a[60:61], v[6:7], v[8:11]\nv_mfma_f32_16x16x16_bf16 v[12:15], a[68:69], v[6:7], v[12:15]\nv_mfma_f32_16x16x16_bf16 v[16:19], v[76:77], v[6:7], v[16:19]\nv_mfma_f32_16x16x16_bf16 v[20:23], a[76:77], v[6:7], v[20:23]\n" \
"v_mfma_f32_16x16x16_bf16 v[8:11], a[62:63], v[6:7], v[8:11]\nv_mfma_f32_16x16x16_bf16 v[12:15], a[70:71], v[6:7], v[12:15]\nv_mfma_f32_16x16x16_bf16 v[16:19], v[78:79], v[6:7], v[16:19]\nv_mfma_f32_16x16x16_bf16 v[20:23], a[78:79], v[6:7], v[20:23]\ns_waitcnt vmcnt(28)\ns_barrier\ns_add_u32 m0, %[tb], 0\ns_add_u32 s44, %[wo], 4128768\n" \
"buffer_load_dwordx4 v92, %[rw], s44 offen nt lds\ns_add_u32 m0, %[tb], 1024\ns_add_u32 s44, %[wo], 4186112\nbuffer_load_dwordx4 v92, %[rw], s44 offen nt lds\ns_add_u32 m0, %[tb], 2048\ns_add_u32 s44, %[wo], 4243456\nbuffer_load_dwordx4 v92, %[rw], s44 offen nt lds\ns_add_u32 m0, %[tb], 3072\n" \
"s_add_u32 s44, %[wo], 4300800\nbuffer_load_dwordx4 v92, %[rw], s44 offen nt lds\nds_read_b64_tr_b16 a[80:81], v93 offset:4096\nds_read_b64_tr_b16 a[82:83], v93 offset:4608\nds_read_b64_tr_b16 a[88:89], v93 offset:4104\nds_read_b64_tr_b16 a[90:91], v93 offset:4616\nds_read_b64_tr_b16 a[96:97], v93 offset:16384\nds_read_b64_tr_b16 a[98:99], v93 offset:16896\n" \
"ds_read_b64_tr_b16 v[72:73], v93 offset:16392\nds_read_b64_tr_b16 v[74:75], v93 offset:16904\ns_waitcnt lgkmcnt(0)\nv_mfma_f32_16x16x16_bf16 v[8:11], a[80:81], v[6:7], v[8:11]\nv_mfma_f32_16x16x16_bf16 v[12:15], a[88:89], v[6:7], v[12:15]\nv_mfma_f32_16x16x16_bf16 v[16:19], a[96:97], v[6:7], v[16:19]\nv_mfma_f32_16x16x16_bf16 v[20:23], v[72:73], v[6:7], v[20:23]\nv_mfma_f32_16x16x16_bf16 v[8:11], a[82:83], v[6:7], v[8:11]\n" \
"v_mfma_f32_16x16x16_bf16 v[12:15], a[90:91], v[6:7], v[12:15]\nv_mfma_f32_16x16x16_bf16 v[16:19], a[98:99], v[6:7], v[16:19]\nv_mfma_f32_16x16x16_bf16 v[20:23], v[74:75], v[6:7], v[20:23]\ns_waitcnt vmcnt(28)\ns_barrier\ns_add_u32 m0, %[tb], 4096\ns_add_u32 s44, %[wo], 4358144\nbuffer_load_dwordx4 v92, %[rw], s44 offen nt lds\n" \
"s_add_u32 m0, %[tb], 5120\ns_add_u32 s44, %[wo], 4415488\nbuffer_load_dwordx4 v92, %[rw], s44 offen nt lds\ns_add_u32 m0, %[tb], 6144\ns_add_u32 s44, %[wo], 4472832\nbuffer_load_dwordx4 v92, %[rw], s44 offen nt lds\ns_add_u32 m0, %[tb], 7168\ns_add_u32 s44, %[wo], 4530176\n" \
"buffer_load_dwordx4 v92, %[rw], s44 offen nt lds\nds_read_b64_tr_b16 a[84:85], v93 offset:8192\nds_read_b64_tr_b16 a[86:87], v93 offset:8704\nds_read_b64_tr_b16 a[92:93], v93 offset:8200\nds_read_b64_tr_b16 a[94:95], v93 offset:8712\nds_read_b64_tr_b16 a[100:101], v93 offset:20480\nds_read_b64_tr_b16 a[102:103], v93 offset:20992\nds_read_b64_tr_b16 v[68:69], v93 offset:20488\n" \
"ds_read_b64_tr_b16 v[70:71], v93 offset:21000\ns_waitcnt lgkmcnt(0)\nv_mfma_f32_16x16x16_bf16 v[8:11], a[84:85], v[6:7], v[8:11]\nv_mfma_f32_16x16x16_bf16 v[12:15], a[92:93], v[6:7], v[12:15]\nv_mfma_f32_16x16x16_bf16 v[16:19], a[100:101], v[6:7], v[16:19]\nv_mfma_f32_16x16x16_bf16 v[20:23], v[68:69], v[6:7], v[20:23]\nv_mfma_f32_16x16x16_bf16 v[8:11], a[86:87], v[6:7], v[8:11]\nv_mfma_f32_16x16x16_bf16 v[12:15], a[94:95], v[6:7], v[12:15]\n" \
"v_mfma_f32_16x16x16_bf16 v[16:19], a[102:103], v[6:7], v[16:19]\nv_mfma_f32_16x16x16_bf16 v[20:23], v[70:71], v[6:7], v[20:23]\ns_waitcnt vmcnt(28)\ns_barrier\ns_add_u32 m0, %[tb], 8192\ns_add_u32 s44, %[wo], 4587520\nbuffer_load_dwordx4 v92, %[rw], s44 offen nt lds\ns_add_u32 m0, %[tb], 9216\n" \
"s_add_u32 s44, %[wo], 4644864\nbuffer_load_dwordx4 v92, %[rw], s44 offen nt lds\ns_add_u32 m0, %[tb], 10240\ns_add_u32 s44, %[wo], 4702208\nbuffer_load_dwordx4 v92, %[rw], s44 offen nt lds\ns_add_u32 m0, %[tb], 11264\ns_add_u32 s44, %[wo], 4759552\nbuffer_load_dwordx4 v92, %[rw], s44 offen nt lds\n" \
"ds_read_b64_tr_b16 a[136:137], v94 offset:0\nds_read_b64_tr_b16 a[138:139], v94 offset:512\nds_read_b64_tr_b16 v[64:65], v94 offset:8\nds_read_b64_tr_b16 v[66:67], v94 offset:520\nds_read_b64_tr_b16 a[144:145], v94 offset:24576\nds_read_b64_tr_b16 a[146:147], v94 offset:25088\nds_read_b64_tr_b16 a[152:153], v94 offset:24584\nds_read_b64_tr_b16 a[154:155], v94 offset:25096\n" \
"s_waitcnt lgkmcnt(0)\nv_mfma_f32_16x16x16_bf16 v[8:11], a[136:137], v[6:7], v[8:11]\nv_mfma_f32_16x16x16_bf16 v[12:15], v[64:65], v[6:7], v[12:15]\nv_mfma_f32_16x16x16_bf16 v[16:19], a[144:145], v[6:7], v[16:19]\nv_mfma_f32_16x16x16_bf16 v[20:23], a[152:153], v[6:7], v[20:23]\nv_mfma_f32_16x16x16_bf16 v[8:11], a[138:139], v[6:7], v[8:11]\nv_mfma_f32_16x16x16_bf16 v[12:15], v[66:67], v[6:7], v[12:15]\nv_mfma_f32_16x16x16_bf16 v[16:19], a[146:147], v[6:7], v[16:19]\n" \
"v_mfma_f32_16x16x16_bf16 v[20:23], a[154:155], v[6:7], v[20:23]\ns_waitcnt vmcnt(28)\ns_barrier\ns_add_u32 m0, %[lw], 0\ns_add_u32 s44, %[wo], 4816896\nbuffer_load_dwordx4 v92, %[rw], s44 offen nt lds\ns_add_u32 m0, %[lw], 1024\ns_add_u32 s44, %[wo], 4874240\n" \
"buffer_load_dwordx4 v92, %[rw], s44 offen nt lds\ns_add_u32 m0, %[lw], 2048\ns_add_u32 s44, %[wo], 4931584\nbuffer_load_dwordx4 v92, %[rw], s44 offen nt lds\ns_add_u32 m0, %[lw], 3072\ns_add_u32 s44, %[wo], 4988928\nbuffer_load_dwordx4 v92, %[rw], s44 offen nt lds\nds_read_b64_tr_b16 a[140:141], v94 offset:4096\n" \
"ds_read_b64_tr_b16 a[142:143], v94 offset:4608\nds_read_b64_tr_b16 v[60:61], v94 offset:4104\nds_read_b64_tr_b16 v[62:63], v94 offset:4616\nds_read_b64_tr_b16 a[148:149], v94 offset:28672\nds_read_b64_tr_b16 a[150:151], v94 offset:29184\nds_read_b64_tr_b16 a[156:157], v94 offset:28680\nds_read_b64_tr_b16 a[158:159], v94 offset:29192\ns_waitcnt lgkmcnt(0)\n" \
"v_mfma_f32_16x16x16_bf16 v[8:11], a[140:141], v[6:7], v[8:11]\nv_mfma_f32_16x16x16_bf16 v[12:15], v[60:61], v[6:7], v[12:15]\nv_mfma_f32_16x16x16_bf16 v[16:19], a[148:149], v[6:7], v[16:19]\nv_mfma_f32_16x16x16_bf16 v[20:23], a[156:157], v[6:7], v[20:23]\nv_mfma_f32_16x16x16_bf16 v[8:11], a[142:143], v[6:7], v[8:11]\nv_mfma_f32_16x16x16_bf16 v[12:15], v[62:63], v[6:7], v[12:15]\nv_mfma_f32_16x16x16_bf16 v[16:19], a[150:151], v[6:7], v[16:19]\nv_mfma_f32_16x16x16_bf16 v[20:23], a[158:159], v[6:7], v[20:23]\n" \
"s_waitcnt vmcnt(28)\ns_barrier\ns_add_u32 m0, %[lw], 4096\ns_add_u32 s44, %[wo], 5046272\nbuffer_load_dwordx4 v92, %[rw], s44 offen nt lds\ns_add_u32 m0, %[lw], 5120\ns_add_u32 s44, %[wo], 5103616\nbuffer_load_dwordx4 v92, %[rw], s44 offen nt lds\n" \
"s_add_u32 m0, %[lw], 6144\ns_add_u32 s44, %[wo], 5160960\nbuffer_load_dwordx4 v92, %[rw], s44 offen nt lds\ns_add_u32 m0, %[lw], 7168\ns_add_u32 s44, %[wo], 5218304\nbuffer_load_dwordx4 v92, %[rw], s44 offen nt lds\nds_read_b64_tr_b16 a[160:161], v94 offset:8192\nds_read_b64_tr_b16 a[162:163], v94 offset:8704\n" \
"ds_read_b64_tr_b16 a[168:169], v94 offset:8200\nds_read_b64_tr_b16 a[170:171], v94 offset:8712\nds_read_b64_tr_b16 v[56:57], v94 offset:32768\nds_read_b64_tr_b16 v[58:59], v94 offset:33280\nds_read_b64_tr_b16 a[176:177], v94 offset:32776\nds_read_b64_tr_b16 a[178:179], v94 offset:33288\ns_waitcnt lgkmcnt(0)\nv_mfma_f32_16x16x16_bf16 v[8:11], a[160:161], v[6:7], v[8:11]\n" \
"v_mfma_f32_16x16x16_bf16 v[12:15], a[168:169], v[6:7], v[12:15]\nv_mfma_f32_16x16x16_bf16 v[16:19], v[56:57], v[6:7], v[16:19]\nv_mfma_f32_16x16x16_bf16 v[20:23], a[176:177], v[6:7], v[20:23]\nv_mfma_f32_16x16x16_bf16 v[8:11], a[162:163], v[6:7], v[8:11]\nv_mfma_f32_16x16x16_bf16 v[12:15], a[170:171], v[6:7], v[12:15]\nv_mfma_f32_16x16x16_bf16 v[16:19], v[58:59], v[6:7], v[16:19]\nv_mfma_f32_16x16x16_bf16 v[20:23], a[178:179], v[6:7], v[20:23]\ns_waitcnt vmcnt(28)\n" \
"s_barrier\ns_add_u32 m0, %[lw], 8192\ns_add_u32 s44, %[wo], 5275648\nbuffer_load_dwordx4 v92, %[rw], s44 offen nt lds\ns_add_u32 m0, %[lw], 9216\ns_add_u32 s44, %[wo], 5332992\nbuffer_load_dwordx4 v92, %[rw], s44 offen nt lds\ns_add_u32 m0, %[lw], 10240\n" \
"s_add_u32 s44, %[wo], 5390336\nbuffer_load_dwordx4 v92, %[rw], s44 offen nt lds\ns_add_u32 m0, %[lw], 11264\ns_add_u32 s44, %[wo], 5447680\nbuffer_load_dwordx4 v92, %[rw], s44 offen nt lds\nds_read_b64_tr_b16 a[164:165], v94 offset:12288\nds_read_b64_tr_b16 a[166:167], v94 offset:12800\nds_read_b64_tr_b16 a[172:173], v94 offset:12296\n" \
"ds_read_b64_tr_b16 a[174:175], v94 offset:12808\nds_read_b64_tr_b16 v[52:53], v94 offset:36864\nds_read_b64_tr_b16 v[54:55], v94 offset:37376\nds_read_b64_tr_b16 a[180:181], v94 offset:36872\nds_read_b64_tr_b16 a[182:183], v94 offset:37384\ns_waitcnt lgkmcnt(0)\nv_mfma_f32_16x16x16_bf16 v[8:11], a[164:165], v[6:7], v[8:11]\nv_mfma_f32_16x16x16_bf16 v[12:15], a[172:173], v[6:7], v[12:15]\n" \
"v_mfma_f32_16x16x16_bf16 v[16:19], v[52:53], v[6:7], v[16:19]\nv_mfma_f32_16x16x16_bf16 v[20:23], a[180:181], v[6:7], v[20:23]\nv_mfma_f32_16x16x16_bf16 v[8:11], a[166:167], v[6:7], v[8:11]\nv_mfma_f32_16x16x16_bf16 v[12:15], a[174:175], v[6:7], v[12:15]\nv_mfma_f32_16x16x16_bf16 v[16:19], v[54:55], v[6:7], v[16:19]\nv_mfma_f32_16x16x16_bf16 v[20:23], a[182:183], v[6:7], v[20:23]\ns_waitcnt vmcnt(28)\ns_barrier\n" \
"s_add_u32 m0, %[lw], 12288\ns_add_u32 s44, %[wo], 5963776\nbuffer_load_dwordx4 v92, %[rw], s44 offen nt lds\ns_add_u32 m0, %[lw], 13312\ns_add_u32 s44, %[wo], 6021120\nbuffer_load_dwordx4 v92, %[rw], s44 offen nt lds\ns_add_u32 m0, %[lw], 14336\ns_add_u32 s44, %[wo], 6078464\n" \
"buffer_load_dwordx4 v92, %[rw], s44 offen nt lds\ns_add_u32 m0, %[lw], 15360\ns_add_u32 s44, %[wo], 6135808\nbuffer_load_dwordx4 v92, %[rw], s44 offen nt lds\nds_read_b64_tr_b16 a[184:185], v94 offset:16384\nds_read_b64_tr_b16 a[186:187], v94 offset:16896\nds_read_b64_tr_b16 a[192:193], v94 offset:16392\nds_read_b64_tr_b16 a[194:195], v94 offset:16904\n" \
"ds_read_b64_tr_b16 a[200:201], v94 offset:40960\nds_read_b64_tr_b16 a[202:203], v94 offset:41472\nds_read_b64_tr_b16 v[48:49], v94 offset:40968\nds_read_b64_tr_b16 v[50:51], v94 offset:41480\ns_waitcnt lgkmcnt(0)\nv_mfma_f32_16x16x16_bf16 v[8:11], a[184:185], v[6:7], v[8:11]\nv_mfma_f32_16x16x16_bf16 v[12:15], a[192:193], v[6:7], v[12:15]\nv_mfma_f32_16x16x16_bf16 v[16:19], a[200:201], v[6:7], v[16:19]\n" \
"v_mfma_f32_16x16x16_bf16 v[20:23], v[48:49], v[6:7], v[20:23]\nv_mfma_f32_16x16x16_bf16 v[8:11], a[186:187], v[6:7], v[8:11]\nv_mfma_f32_16x16x16_bf16 v[12:15], a[194:195], v[6:7], v[12:15]\nv_mfma_f32_16x16x16_bf16 v[16:19], a[202:203], v[6:7], v[16:19]\nv_mfma_f32_16x16x16_bf16 v[20:23], v[50:51], v[6:7], v[20:23]\ns_waitcnt vmcnt(28)\ns_barrier\ns_add_u32 m0, %[lw], 16384\n" \
"s_add_u32 s44, %[wo], 6193152\nbuffer_load_dwordx4 v92, %[rw], s44 offen nt lds\ns_add_u32 m0, %[lw], 17408\ns_add_u32 s44, %[wo], 6250496\nbuffer_load_dwordx4 v92, %[rw], s44 offen nt lds\ns_add_u32 m0, %[lw], 18432\ns_add_u32 s44, %[wo], 6307840\nbuffer_load_dwordx4 v92, %[rw], s44 offen nt lds\n" \
"s_add_u32 m0, %[lw], 19456\ns_add_u32 s44, %[wo], 6365184\nbuffer_load_dwordx4 v92, %[rw], s44 offen nt lds\nds_read_b64_tr_b16 a[188:189], v94 offset:20480\nds_read_b64_tr_b16 a[190:191], v94 offset:20992\nds_read_b64_tr_b16 a[196:197], v94 offset:20488\nds_read_b64_tr_b16 a[198:199], v94 offset:21000\nds_read_b64_tr_b16 a[204:205], v94 offset:45056\n" \
"ds_read_b64_tr_b16 a[206:207], v94 offset:45568\nds_read_b64_tr_b16 v[44:45], v94 offset:45064\nds_read_b64_tr_b16 v[46:47], v94 offset:45576\ns_waitcnt lgkmcnt(0)\nv_mfma_f32_16x16x16_bf16 v[8:11], a[188:189], v[6:7], v[8:11]\nv_mfma_f32_16x16x16_bf16 v[12:15], a[196:197], v[6:7], v[12:15]\nv_mfma_f32_16x16x16_bf16 v[16:19], a[204:205], v[6:7], v[16:19]\nv_mfma_f32_16x16x16_bf16 v[20:23], v[44:45], v[6:7], v[20:23]\n" \
"v_mfma_f32_16x16x16_bf16 v[8:11], a[190:191], v[6:7], v[8:11]\nv_mfma_f32_16x16x16_bf16 v[12:15], a[198:199], v[6:7], v[12:15]\nv_mfma_f32_16x16x16_bf16 v[16:19], a[206:207], v[6:7], v[16:19]\nv_mfma_f32_16x16x16_bf16 v[20:23], v[46:47], v[6:7], v[20:23]\ns_waitcnt vmcnt(28)\ns_barrier\ns_add_u32 m0, %[lw], 20480\ns_add_u32 s44, %[wo], 6422528\n" \
"buffer_load_dwordx4 v92, %[rw], s44 offen nt lds\ns_add_u32 m0, %[lw], 21504\ns_add_u32 s44, %[wo], 6479872\nbuffer_load_dwordx4 v92, %[rw], s44 offen nt lds\ns_add_u32 m0, %[lw], 22528\ns_add_u32 s44, %[wo], 6537216\nbuffer_load_dwordx4 v92, %[rw], s44 offen nt lds\ns_add_u32 m0, %[lw], 23552\n" \
"s_add_u32 s44, %[wo], 6594560\nbuffer_load_dwordx4 v92, %[rw], s44 offen nt lds\nds_read_b64_tr_b16 a[240:241], v93 offset:0\nds_read_b64_tr_b16 a[242:243], v93 offset:512\nds_read_b64_tr_b16 v[40:41], v93 offset:8\nds_read_b64_tr_b16 v[42:43], v93 offset:520\nds_read_b64_tr_b16 a[248:249], v93 offset:12288\nds_read_b64_tr_b16 a[250:251], v93 offset:12800\n" \
"ds_read_b64_tr_b16 v[96:97], v93 offset:12296\nds_read_b64_tr_b16 v[98:99], v93 offset:12808\ns_waitcnt lgkmcnt(0)\nv_mfma_f32_16x16x16_bf16 v[8:11], a[240:241], v[6:7], v[8:11]\nv_mfma_f32_16x16x16_bf16 v[12:15], v[40:41], v[6:7], v[12:15]\nv_mfma_f32_16x16x16_bf16 v[16:19], a[248:249], v[6:7], v[16:19]\nv_mfma_f32_16x16x16_bf16 v[20:23], v[96:97], v[6:7], v[20:23]\nv_mfma_f32_16x16x16_bf16 v[8:11], a[242:243], v[6:7], v[8:11]\n" \
"v_mfma_f32_16x16x16_bf16 v[12:15], v[42:43], v[6:7], v[12:15]\nv_mfma_f32_16x16x16_bf16 v[16:19], a[250:251], v[6:7], v[16:19]\nv_mfma_f32_16x16x16_bf16 v[20:23], v[98:99], v[6:7], v[20:23]\ns_waitcnt vmcnt(28)\ns_barrier\ns_add_u32 m0, %[tb], 0\ns_add_u32 s44, %[wo], 6651904\nbuffer_load_dwordx4 v92, %[rw], s44 offen nt lds\n" \
"s_add_u32 m0, %[tb], 1024\ns_add_u32 s44, %[wo], 6709248\nbuffer_load_dwordx4 v92, %[rw], s44 offen nt lds\ns_add_u32 m0, %[tb], 2048\ns_add_u32 s44, %[wo], 6766592\nbuffer_load_dwordx4 v92, %[rw], s44 offen nt lds\ns_add_u32 m0, %[tb], 3072\ns_add_u32 s44, %[wo], 6823936\n" \
"buffer_load_dwordx4 v92, %[rw], s44 offen nt lds\nds_read_b64_tr_b16 a[244:245], v93 offset:4096\nds_read_b64_tr_b16 a[246:247], v93 offset:4608\nds_read_b64_tr_b16 v[36:37], v93 offset:4104\nds_read_b64_tr_b16 v[38:39], v93 offset:4616\nds_read_b64_tr_b16 a[252:253], v93 offset:16384\nds_read_b64_tr_b16 a[254:255], v93 offset:16896\nds_read_b64_tr_b16 v[100:101], v93 offset:16392\n" \
"ds_read_b64_tr_b16 v[102:103], v93 offset:16904\ns_waitcnt lgkmcnt(0)\nv_mfma_f32_16x16x16_bf16 v[8:11], a[244:245], v[6:7], v[8:11]\nv_mfma_f32_16x16x16_bf16 v[12:15], v[36:37], v[6:7], v[12:15]\nv_mfma_f32_16x16x16_bf16 v[16:19], a[252:253], v[6:7], v[16:19]\nv_mfma_f32_16x16x16_bf16 v[20:23], v[100:101], v[6:7], v[20:23]\nv_mfma_f32_16x16x16_bf16 v[8:11], a[246:247], v[6:7], v[8:11]\nv_mfma_f32_16x16x16_bf16 v[12:15], v[38:39], v[6:7], v[12:15]\n" \
"v_mfma_f32_16x16x16_bf16 v[16:19], a[254:255], v[6:7], v[16:19]\nv_mfma_f32_16x16x16_bf16 v[20:23], v[102:103], v[6:7], v[20:23]\ns_waitcnt vmcnt(28)\ns_barrier\ns_add_u32 m0, %[tb], 4096\ns_add_u32 s44, %[wo], 6881280\nbuffer_load_dwordx4 v92, %[rw], s44 offen nt lds\ns_add_u32 m0, %[tb], 5120\n" \
"s_add_u32 s44, %[wo], 6938624\nbuffer_load_dwordx4 v92, %[rw], s44 offen nt lds\ns_add_u32 m0, %[tb], 6144\ns_add_u32 s44, %[wo], 6995968\nbuffer_load_dwordx4 v92, %[rw], s44 offen nt lds\ns_add_u32 m0, %[tb], 7168\ns_add_u32 s44, %[wo], 7053312\nbuffer_load_dwordx4 v92, %[rw], s44 offen nt lds\n" \
"ds_read_b64_tr_b16 v[104:105], v93 offset:8192\nds_read_b64_tr_b16 v[106:107], v93 offset:8704\nds_read_b64_tr_b16 v[112:113], v93 offset:8200\nds_read_b64_tr_b16 v[114:115], v93 offset:8712\nds_read_b64_tr_b16 v[32:33], v93 offset:20480\nds_read_b64_tr_b16 v[34:35], v93 offset:20992\nds_read_b64_tr_b16 v[120:121], v93 offset:20488\nds_read_b64_tr_b16 v[122:123], v93 offset:21000\n" \
"s_waitcnt lgkmcnt(0)\nv_mfma_f32_16x16x16_bf16 v[8:11], v[104:105], v[6:7], v[8:11]\nv_mfma_f32_16x16x16_bf16 v[12:15], v[112:113], v[6:7], v[12:15]\nv_mfma_f32_16x16x16_bf16 v[16:19], v[32:33], v[6:7], v[16:19]\nv_mfma_f32_16x16x16_bf16 v[20:23], v[120:121], v[6:7], v[20:23]\nv_mfma_f32_16x16x16_bf16 v[8:11], v[106:107], v[6:7], v[8:11]\nv_mfma_f32_16x16x16_bf16 v[12:15], v[114:115], v[6:7], v[12:15]\nv_mfma_f32_16x16x16_bf16 v[16:19], v[34:35], v[6:7], v[16:19]\n" \
"v_mfma_f32_16x16x16_bf16 v[20:23], v[122:123], v[6:7], v[20:23]\ns_waitcnt vmcnt(28)\ns_barrier\ns_add_u32 m0, %[tb], 8192\ns_add_u32 s44, %[wo], 7110656\nbuffer_load_dwordx4 v92, %[rw], s44 offen nt lds\ns_add_u32 m0, %[tb], 9216\ns_add_u32 s44, %[wo], 7168000\n" \
"buffer_load_dwordx4 v92, %[rw], s44 offen nt lds\ns_add_u32 m0, %[tb], 10240\ns_add_u32 s44, %[wo], 7225344\nbuffer_load_dwordx4 v92, %[rw], s44 offen nt lds\ns_add_u32 m0, %[tb], 11264\ns_add_u32 s44, %[wo], 7282688\nbuffer_load_dwordx4 v92, %[rw], s44 offen nt lds\nds_read_b64_tr_b16 v[108:109], v94 offset:0\n" \
"ds_read_b64_tr_b16 v[110:111], v94 offset:512\nds_read_b64_tr_b16 v[116:117], v94 offset:8\nds_read_b64_tr_b16 v[118:119], v94 offset:520\nds_read_b64_tr_b16 v[28:29], v94 offset:24576\nds_read_b64_tr_b16 v[30:31], v94 offset:25088\nds_read_b64_tr_b16 v[124:125], v94 offset:24584\nds_read_b64_tr_b16 v[126:127], v94 offset:25096\ns_waitcnt lgkmcnt(0)\n" \
"v_mfma_f32_16x16x16_bf16 v[8:11], v[108:109], v[6:7], v[8:11]\nv_mfma_f32_16x16x16_bf16 v[12:15], v[116:117], v[6:7], v[12:15]\nv_mfma_f32_16x16x16_bf16 v[16:19], v[28:29], v[6:7], v[16:19]\nv_mfma_f32_16x16x16_bf16 v[20:23], v[124:125], v[6:7], v[20:23]\nv_mfma_f32_16x16x16_bf16 v[8:11], v[110:111], v[6:7], v[8:11]\nv_mfma_f32_16x16x16_bf16 v[12:15], v[118:119], v[6:7], v[12:15]\nv_mfma_f32_16x16x16_bf16 v[16:19], v[30:31], v[6:7], v[16:19]\nv_mfma_f32_16x16x16_bf16 v[20:23], v[126:127], v[6:7], v[20:23]\n" \
"s_waitcnt vmcnt(28)\ns_barrier\nds_read_b64_tr_b16 v[128:129], v94 offset:4096\nds_read_b64_tr_b16 v[130:131], v94 offset:4608\nds_read_b64_tr_b16 v[136:137], v94 offset:4104\nds_read_b64_tr_b16 v[138:139], v94 offset:4616\nds_read_b64_tr_b16 v[144:145], v94 offset:28672\nds_read_b64_tr_b16 v[146:147], v94 offset:29184\n" \
"ds_read_b64_tr_b16 v[24:25], v94 offset:28680\nds_read_b64_tr_b16 v[26:27], v94 offset:29192\ns_waitcnt lgkmcnt(0)\nv_mfma_f32_16x16x16_bf16 v[8:11], v[128:129], v[6:7], v[8:11]\nv_mfma_f32_16x16x16_bf16 v[12:15], v[136:137], v[6:7], v[12:15]\nv_mfma_f32_16x16x16_bf16 v[16:19], v[144:145], v[6:7], v[16:19]\nv_mfma_f32_16x16x16_bf16 v[20:23], v[24:25], v[6:7], v[20:23]\nv_mfma_f32_16x16x16_bf16 v[8:11], v[130:131], v[6:7], v[8:11]\n" \
"v_mfma_f32_16x16x16_bf16 v[12:15], v[138:139], v[6:7], v[12:15]\nv_mfma_f32_16x16x16_bf16 v[16:19], v[146:147], v[6:7], v[16:19]\nv_mfma_f32_16x16x16_bf16 v[20:23], v[26:27], v[6:7], v[20:23]\ns_waitcnt vmcnt(24)\nds_write_b128 v1, v[88:91] offset:0\nds_write_b128 v1, v[84:87] offset:1024\nds_write_b128 v1, v[80:83] offset:2048\nds_write_b128 v1, v[76:79] offset:3072\n" \
"s_barrier\nds_read_b64_tr_b16 v[132:133], v94 offset:8192\nds_read_b64_tr_b16 v[134:135], v94 offset:8704\nds_read_b64_tr_b16 v[140:141], v94 offset:8200\nds_read_b64_tr_b16 v[142:143], v94 offset:8712\nds_read_b64_tr_b16 v[148:149], v94 offset:32768\nds_read_b64_tr_b16 v[150:151], v94 offset:33280\nds_read_b64_tr_b16 v[76:77], v94 offset:32776\n" \
"ds_read_b64_tr_b16 v[78:79], v94 offset:33288\ns_waitcnt lgkmcnt(0)\nv_mfma_f32_16x16x16_bf16 v[8:11], v[132:133], v[6:7], v[8:11]\nv_mfma_f32_16x16x16_bf16 v[12:15], v[140:141], v[6:7], v[12:15]\nv_mfma_f32_16x16x16_bf16 v[16:19], v[148:149], v[6:7], v[16:19]\nv_mfma_f32_16x16x16_bf16 v[20:23], v[76:77], v[6:7], v[20:23]\nv_mfma_f32_16x16x16_bf16 v[8:11], v[134:135], v[6:7], v[8:11]\nv_mfma_f32_16x16x16_bf16 v[12:15], v[142:143], v[6:7], v[12:15]\n" \
"v_mfma_f32_16x16x16_bf16 v[16:19], v[150:151], v[6:7], v[16:19]\nv_mfma_f32_16x16x16_bf16 v[20:23], v[78:79], v[6:7], v[20:23]\ns_waitcnt vmcnt(20)\nds_write_b128 v1, v[72:75] offset:4096\nds_write_b128 v1, v[68:71] offset:5120\nds_write_b128 v1, v[64:67] offset:6144\nds_write_b128 v1, v[60:63] offset:7168\ns_barrier\n" \
"ds_read_b64_tr_b16 v[184:185], v94 offset:12288\nds_read_b64_tr_b16 v[186:187], v94 offset:12800\nds_read_b64_tr_b16 v[60:61], v94 offset:12296\nds_read_b64_tr_b16 v[62:63], v94 offset:12808\nds_read_b64_tr_b16 v[192:193], v94 offset:36864\nds_read_b64_tr_b16 v[194:195], v94 offset:37376\nds_read_b64_tr_b16 v[200:201], v94 offset:36872\nds_read_b64_tr_b16 v[202:203], v94 offset:37384\n" \
"s_waitcnt lgkmcnt(0)\nv_mfma_f32_16x16x16_bf16 v[8:11], v[184:185], v[6:7], v[8:11]\nv_mfma_f32_16x16x16_bf16 v[12:15], v[60:61], v[6:7], v[12:15]\nv_mfma_f32_16x16x16_bf16 v[16:19], v[192:193], v[6:7], v[16:19]\nv_mfma_f32_16x16x16_bf16 v[20:23], v[200:201], v[6:7], v[20:23]\nv_mfma_f32_16x16x16_bf16 v[8:11], v[186:187], v[6:7], v[8:11]\nv_mfma_f32_16x16x16_bf16 v[12:15], v[62:63], v[6:7], v[12:15]\nv_mfma_f32_16x16x16_bf16 v[16:19], v[194:195], v[6:7], v[16:19]\n" \
"v_mfma_f32_16x16x16_bf16 v[20:23], v[202:203], v[6:7], v[20:23]\ns_waitcnt vmcnt(16)\nds_write_b128 v1, v[56:59] offset:8192\nds_write_b128 v1, v[52:55] offset:9216\nds_write_b128 v1, v[48:51] offset:10240\nds_write_b128 v1, v[44:47] offset:11264\ns_barrier\nds_read_b64_tr_b16 v[188:189], v94 offset:16384\n" \
"ds_read_b64_tr_b16 v[190:191], v94 offset:16896\nds_read_b64_tr_b16 v[44:45], v94 offset:16392\nds_read_b64_tr_b16 v[46:47], v94 offset:16904\nds_read_b64_tr_b16 v[196:197], v94 offset:40960\nds_read_b64_tr_b16 v[198:199], v94 offset:41472\nds_read_b64_tr_b16 v[204:205], v94 offset:40968\nds_read_b64_tr_b16 v[206:207], v94 offset:41480\ns_waitcnt lgkmcnt(0)\n" \
"v_mfma_f32_16x16x16_bf16 v[8:11], v[188:189], v[6:7], v[8:11]\nv_mfma_f32_16x16x16_bf16 v[12:15], v[44:45], v[6:7], v[12:15]\nv_mfma_f32_16x16x16_bf16 v[16:19], v[196:197], v[6:7], v[16:19]\nv_mfma_f32_16x16x16_bf16 v[20:23], v[204:205], v[6:7], v[20:23]\nv_mfma_f32_16x16x16_bf16 v[8:11], v[190:191], v[6:7], v[8:11]\nv_mfma_f32_16x16x16_bf16 v[12:15], v[46:47], v[6:7], v[12:15]\nv_mfma_f32_16x16x16_bf16 v[16:19], v[198:199], v[6:7], v[16:19]\nv_mfma_f32_16x16x16_bf16 v[20:23], v[206:207], v[6:7], v[20:23]\n" \
"s_waitcnt vmcnt(12)\nds_write_b128 v1, v[40:43] offset:12288\nds_write_b128 v1, v[36:39] offset:13312\nds_write_b128 v1, v[32:35] offset:14336\nds_write_b128 v1, v[28:31] offset:15360\ns_barrier\nds_read_b64_tr_b16 v[208:209], v94 offset:20480\nds_read_b64_tr_b16 v[210:211], v94 offset:20992\n" \
"ds_read_b64_tr_b16 v[216:217], v94 offset:20488\nds_read_b64_tr_b16 v[218:219], v94 offset:21000\nds_read_b64_tr_b16 v[28:29], v94 offset:45056\nds_read_b64_tr_b16 v[30:31], v94 offset:45568\nds_read_b64_tr_b16 v[224:225], v94 offset:45064\nds_read_b64_tr_b16 v[226:227], v94 offset:45576\ns_waitcnt lgkmcnt(0)\nv_mfma_f32_16x16x16_bf16 v[8:11], v[208:209], v[6:7], v[8:11]\n" \
"v_mfma_f32_16x16x16_bf16 v[12:15], v[216:217], v[6:7], v[12:15]\nv_mfma_f32_16x16x16_bf16 v[16:19], v[28:29], v[6:7], v[16:19]\nv_mfma_f32_16x16x16_bf16 v[20:23], v[224:225], v[6:7], v[20:23]\nv_mfma_f32_16x16x16_bf16 v[8:11], v[210:211], v[6:7], v[8:11]\nv_mfma_f32_16x16x16_bf16 v[12:15], v[218:219], v[6:7], v[12:15]\nv_mfma_f32_16x16x16_bf16 v[16:19], v[30:31], v[6:7], v[16:19]\nv_mfma_f32_16x16x16_bf16 v[20:23], v[226:227], v[6:7], v[20:23]\ns_waitcnt vmcnt(8)\n" \
"ds_write_b128 v1, v[24:27] offset:16384\nds_write_b128 v1, v[76:79] offset:17408\nds_write_b128 v1, v[60:63] offset:18432\nds_write_b128 v1, v[44:47] offset:19456\ns_barrier\nds_read_b64_tr_b16 v[212:213], v93 offset:0\nds_read_b64_tr_b16 v[214:215], v93 offset:512\nds_read_b64_tr_b16 v[220:221], v93 offset:8\n" \
"ds_read_b64_tr_b16 v[222:223], v93 offset:520\nds_read_b64_tr_b16 v[44:45], v93 offset:12288\nds_read_b64_tr_b16 v[46:47], v93 offset:12800\nds_read_b64_tr_b16 v[228:229], v93 offset:12296\nds_read_b64_tr_b16 v[230:231], v93 offset:12808\ns_waitcnt lgkmcnt(0)\nv_mfma_f32_16x16x16_bf16 v[8:11], v[212:213], v[6:7], v[8:11]\nv_mfma_f32_16x16x16_bf16 v[12:15], v[220:221], v[6:7], v[12:15]\n" \
"v_mfma_f32_16x16x16_bf16 v[16:19], v[44:45], v[6:7], v[16:19]\nv_mfma_f32_16x16x16_bf16 v[20:23], v[228:229], v[6:7], v[20:23]\nv_mfma_f32_16x16x16_bf16 v[8:11], v[214:215], v[6:7], v[8:11]\nv_mfma_f32_16x16x16_bf16 v[12:15], v[222:223], v[6:7], v[12:15]\nv_mfma_f32_16x16x16_bf16 v[16:19], v[46:47], v[6:7], v[16:19]\nv_mfma_f32_16x16x16_bf16 v[20:23], v[230:231], v[6:7], v[20:23]\ns_waitcnt vmcnt(4)\nds_write_b128 v1, v[28:31] offset:20480\n" \
"ds_write_b128 v1, v[44:47] offset:21504\ns_barrier\nds_read_b64_tr_b16 v[232:233], v93 offset:4096\nds_read_b64_tr_b16 v[234:235], v93 offset:4608\nds_read_b64_tr_b16 v[240:241], v93 offset:4104\nds_read_b64_tr_b16 v[242:243], v93 offset:4616\nds_read_b64_tr_b16 v[248:249], v93 offset:16384\nds_read_b64_tr_b16 v[250:251], v93 offset:16896\n" \
"ds_read_b64_tr_b16 v[44:45], v93 offset:16392\nds_read_b64_tr_b16 v[46:47], v93 offset:16904\ns_waitcnt lgkmcnt(0)\nv_mfma_f32_16x16x16_bf16 v[8:11], v[232:233], v[6:7], v[8:11]\nv_mfma_f32_16x16x16_bf16 v[12:15], v[240:241], v[6:7], v[12:15]\nv_mfma_f32_16x16x16_bf16 v[16:19], v[248:249], v[6:7], v[16:19]\nv_mfma_f32_16x16x16_bf16 v[20:23], v[44:45], v[6:7], v[20:23]\nv_mfma_f32_16x16x16_bf16 v[8:11], v[234:235], v[6:7], v[8:11]\n" \
"v_mfma_f32_16x16x16_bf16 v[12:15], v[242:243], v[6:7], v[12:15]\nv_mfma_f32_16x16x16_bf16 v[16:19], v[250:251], v[6:7], v[16:19]\nv_mfma_f32_16x16x16_bf16 v[20:23], v[46:47], v[6:7], v[20:23]\ns_waitcnt vmcnt(0)\nds_write_b128 v1, v[44:47] offset:22528\ns_barrier\nds_read_b64_tr_b16 v[236:237], v93 offset:8192\nds_read_b64_tr_b16 v[238:239], v93 offset:8704\n" \
"ds_read_b64_tr_b16 v[244:245], v93 offset:8200\nds_read_b64_tr_b16 v[246:247], v93 offset:8712\nds_read_b64_tr_b16 v[252:253], v93 offset:20480\nds_read_b64_tr_b16 v[254:255], v93 offset:20992\nds_read_b64_tr_b16 v[44:45], v93 offset:20488\nds_read_b64_tr_b16 v[46:47], v93 offset:21000\ns_waitcnt lgkmcnt(0)\nv_mfma_f32_16x16x16_bf16 v[8:11], v[236:237], v[6:7], v[8:11]\n" \
"v_mfma_f32_16x16x16_bf16 v[12:15], v[244:245], v[6:7], v[12:15]\nv_mfma_f32_16x16x16_bf16 v[16:19], v[252:253], v[6:7], v[16:19]\nv_mfma_f32_16x16x16_bf16 v[20:23], v[44:45], v[6:7], v[20:23]\nv_mfma_f32_16x16x16_bf16 v[8:11], v[238:239], v[6:7], v[8:11]\nv_mfma_f32_16x16x16_bf16 v[12:15], v[246:247], v[6:7], v[12:15]\nv_mfma_f32_16x16x16_bf16 v[16:19], v[254:255], v[6:7], v[16:19]\nv_mfma_f32_16x16x16_bf16 v[20:23], v[46:47], v[6:7], v[20:23]\nds_write_b128 v1, v[44:47] offset:23552\n" \
"s_waitcnt lgkmcnt(0)\ns_barrier\ns_mov_b32 s40, 0\ns_mov_b32 s43, 0\ns_lshl_b32 s57, %[w], 12\ns_mov_b32 s58, 0xbeeff558\ns_mov_b32 s59, 0x409d30fc\ns_mov_b32 s56, -2147483648\n" \
".Lgg_next%=:\ns_cmp_lg_u32 %[w], 0\ns_cbranch_scc1 .Lgg_w1%=\nbuffer_load_dword v56, v3, %[rs], 0 offen sc1\ns_waitcnt vmcnt(0)\nbuffer_store_dword v90, off, %[rs], s56 sc1\n.Lgg_poll%=:\nv_cmp_gt_i32 vcc, s40, v56\n" \
"s_cbranch_vccz .Lgg_go%=\ns_sleep 12\nbuffer_load_dword v56, v3, %[rs], 0 offen sc1\ns_waitcnt vmcnt(0)\ns_add_u32 s43, s43, 1\ns_cmp_lt_u32 s43, 262144\ns_cbranch_scc1 .Lgg_poll%=\ns_branch .Lgg_abort%=\n" \
".Lgg_w1%=:\ns_waitcnt vmcnt(0)\nbuffer_store_dword v90, off, %[rs], s56 sc1\n.Lgg_go%=:\ns_add_u32 s44, s40, 0\ns_lshl_b32 s44, s44, 17\ns_add_u32 s44, s44, %[xn]\ns_lshr_b32 s45, s44, 8\n" \
"s_and_b32 s45, s45, 0x2000\ns_xor_b32 s41, s44, s45\ns_add_u32 s61, s41, %[xw]\ns_barrier\ns_add_u32 m0, s57, 0\ns_add_u32 s60, s61, 0\nbuffer_load_dwordx4 v0, %[rs], s60 offen offset:0 sc1 lds\nbuffer_load_dwordx4 v0, %[rs], s60 offen offset:1024 sc1 lds\n" \
"buffer_load_dwordx4 v0, %[rs], s60 offen offset:2048 sc1 lds\nbuffer_load_dwordx4 v0, %[rs], s60 offen offset:3072 sc1 lds\ns_add_u32 m0, s57, 16384\ns_add_u32 s60, s61, 32768\nbuffer_load_dwordx4 v0, %[rs], s60 offen offset:0 sc1 lds\nbuffer_load_dwordx4 v0, %[rs], s60 offen offset:1024 sc1 lds\nbuffer_load_dwordx4 v0, %[rs], s60 offen offset:2048 sc1 lds\nbuffer_load_dwordx4 v0, %[rs], s60 offen offset:3072 sc1 lds\n" \
"buffer_load_dwordx4 v[40:43], v5, %[rs], s41 offen offset:0 sc1\nbuffer_load_dwordx4 v[44:47], v5, %[rs], s41 offen offset:256 sc1\ns_lshl_b32 s44, s40, 19\ns_add_u32 s44, s44, %[ho]\ns_lshr_b32 s45, s44, 8\ns_and_b32 s45, s45, 0x2000\ns_xor_b32 s55, s44, s45\ns_waitcnt vmcnt(6)\n" \
"s_barrier\nds_read_b128 v[56:59], v2 offset:0\nds_read_b128 v[60:63], v2 offset:2048\nds_read_b128 v[64:67], v2 offset:4096\ns_waitcnt lgkmcnt(2)\nv_mfma_f32_16x16x32_bf16 v[24:27], a[0:3], v[56:59], 0\nds_read_b128 v[68:71], v2 offset:6144\nds_read_b128 v[80:83], v1 offset:0\n" \
"v_mfma_f32_16x16x32_bf16 v[28:31], a[8:11], v[56:59], 0\nbuffer_load_dwordx4 v[48:51], v5, %[rs], s41 offen offset:512 sc1\nv_mfma_f32_16x16x32_bf16 v[32:35], a[16:19], v[56:59], 0\nv_mfma_f32_16x16x32_bf16 v[36:39], a[24:27], v[56:59], 0\ns_waitcnt lgkmcnt(3)\nds_read_b128 v[72:75], v1 offset:1024\nv_mfma_f32_16x16x32_bf16 v[24:27], a[4:7], v[60:63], v[24:27]\nds_read_b128 v[56:59], v2 offset:8192\n" \
"v_mfma_f32_16x16x32_bf16 v[28:31], a[12:15], v[60:63], v[28:31]\nbuffer_load_dwordx4 v[52:55], v5, %[rs], s41 offen offset:768 sc1\nv_mfma_f32_16x16x32_bf16 v[32:35], a[20:23], v[60:63], v[32:35]\nv_mfma_f32_16x16x32_bf16 v[36:39], a[28:31], v[60:63], v[36:39]\ns_waitcnt lgkmcnt(4)\nds_read_b128 v[76:79], v1 offset:2048\nv_mfma_f32_16x16x32_bf16 v[24:27], a[32:35], v[64:67], v[24:27]\ns_waitcnt lgkmcnt(3)\n" \
"ds_read_b128 v[60:63], v2 offset:10240\nv_mfma_f32_16x16x32_bf16 v[28:31], v[80:83], v[64:67], v[28:31]\ns_add_u32 m0, s57, 32768\ns_add_u32 s60, s61, 65536\nbuffer_load_dwordx4 v0, %[rs], s60 offen offset:0 sc1 lds\nv_mfma_f32_16x16x32_bf16 v[32:35], a[40:43], v[64:67], v[32:35]\nv_mfma_f32_16x16x32_bf16 v[36:39], a[48:51], v[64:67], v[36:39]\nds_read_b128 v[80:83], v1 offset:3072\n" \
"v_mfma_f32_16x16x32_bf16 v[24:27], a[36:39], v[68:71], v[24:27]\ns_waitcnt lgkmcnt(4)\nds_read_b128 v[64:67], v2 offset:12288\nv_mfma_f32_16x16x32_bf16 v[28:31], v[72:75], v[68:71], v[28:31]\nbuffer_load_dwordx4 v0, %[rs], s60 offen offset:1024 sc1 lds\nv_mfma_f32_16x16x32_bf16 v[32:35], a[44:47], v[68:71], v[32:35]\nv_mfma_f32_16x16x32_bf16 v[36:39], a[52:55], v[68:71], v[36:39]\ns_waitcnt lgkmcnt(4)\n" \
"ds_read_b128 v[72:75], v1 offset:4096\nv_mfma_f32_16x16x32_bf16 v[24:27], a[56:59], v[56:59], v[24:27]\nds_read_b128 v[68:71], v2 offset:14336\nv_mfma_f32_16x16x32_bf16 v[28:31], a[64:67], v[56:59], v[28:31]\ns_waitcnt lgkmcnt(5)\nbuffer_load_dwordx4 v0, %[rs], s60 offen offset:2048 sc1 lds\nv_mfma_f32_16x16x32_bf16 v[32:35], v[76:79], v[56:59], v[32:35]\nv_mfma_f32_16x16x32_bf16 v[36:39], a[72:75], v[56:59], v[36:39]\n" \
"s_waitcnt lgkmcnt(4)\nds_read_b128 v[76:79], v1 offset:5120\nv_mfma_f32_16x16x32_bf16 v[24:27], a[60:63], v[60:63], v[24:27]\ns_waitcnt vmcnt(7)\ns_barrier\nds_read_b128 v[56:59], v2 offset:16384\nv_mfma_f32_16x16x32_bf16 v[28:31], a[68:71], v[60:63], v[28:31]\ns_waitcnt lgkmcnt(5)\n" \
"buffer_load_dwordx4 v0, %[rs], s60 offen offset:3072 sc1 lds\nv_mfma_f32_16x16x32_bf16 v[32:35], v[80:83], v[60:63], v[32:35]\nv_mfma_f32_16x16x32_bf16 v[36:39], a[76:79], v[60:63], v[36:39]\ns_waitcnt lgkmcnt(4)\nv_mfma_f32_16x16x32_bf16 v[24:27], a[80:83], v[64:67], v[24:27]\nds_read_b128 v[60:63], v2 offset:18432\nv_mfma_f32_16x16x32_bf16 v[28:31], a[88:91], v[64:67], v[28:31]\ns_add_u32 m0, s57, 49152\n" \
"s_add_u32 s60, s61, 98304\nbuffer_load_dwordx4 v0, %[rs], s60 offen offset:0 sc1 lds\nv_mfma_f32_16x16x32_bf16 v[32:35], a[96:99], v[64:67], v[32:35]\ns_waitcnt lgkmcnt(4)\nv_mfma_f32_16x16x32_bf16 v[36:39], v[72:75], v[64:67], v[36:39]\ns_waitcnt lgkmcnt(3)\nv_mfma_f32_16x16x32_bf16 v[24:27], a[84:87], v[68:71], v[24:27]\nds_read_b128 v[64:67], v2 offset:20480\n" \
"v_mfma_f32_16x16x32_bf16 v[28:31], a[92:95], v[68:71], v[28:31]\nbuffer_load_dwordx4 v0, %[rs], s60 offen offset:1024 sc1 lds\nv_mfma_f32_16x16x32_bf16 v[32:35], a[100:103], v[68:71], v[32:35]\ns_waitcnt lgkmcnt(3)\nv_mfma_f32_16x16x32_bf16 v[36:39], v[76:79], v[68:71], v[36:39]\ns_waitcnt lgkmcnt(2)\nds_read_b128 v[76:79], v1 offset:6144\nv_mfma_f32_16x16x32_bf16 v[24:27], a[104:107], v[56:59], v[24:27]\n" \
"ds_read_b128 v[68:71], v2 offset:22528\nv_mfma_f32_16x16x32_bf16 v[28:31], a[112:115], v[56:59], v[28:31]\nbuffer_load_dwordx4 v0, %[rs], s60 offen offset:2048 sc1 lds\nv_mfma_f32_16x16x32_bf16 v[32:35], a[120:123], v[56:59], v[32:35]\nv_mfma_f32_16x16x32_bf16 v[36:39], a[128:131], v[56:59], v[36:39]\ns_waitcnt lgkmcnt(3)\nds_read_b128 v[80:83], v1 offset:7168\nv_mfma_f32_16x16x32_bf16 v[24:27], a[108:111], v[60:63], v[24:27]\n" \
"ds_read_b128 v[56:59], v2 offset:24576\nv_mfma_f32_16x16x32_bf16 v[28:31], a[116:119], v[60:63], v[28:31]\nbuffer_load_dwordx4 v0, %[rs], s60 offen offset:3072 sc1 lds\nv_mfma_f32_16x16x32_bf16 v[32:35], a[124:127], v[60:63], v[32:35]\nv_mfma_f32_16x16x32_bf16 v[36:39], a[132:135], v[60:63], v[36:39]\ns_waitcnt lgkmcnt(4)\nds_read_b128 v[72:75], v1 offset:8192\nv_mfma_f32_16x16x32_bf16 v[24:27], a[136:139], v[64:67], v[24:27]\n" \
"s_waitcnt vmcnt(8)\ns_waitcnt lgkmcnt(4)\nds_read_b128 v[60:63], v2 offset:26624\nv_add_f32 v84, v40, v44\nv_mfma_f32_16x16x32_bf16 v[28:31], v[76:79], v[64:67], v[28:31]\nv_add_f32 v85, v48, v52\nv_add_f32 v84, v84, v85\nv_mfma_f32_16x16x32_bf16 v[32:35], a[144:147], v[64:67], v[32:35]\n" \
"v_mov_b32 v85, v84\ns_nop 0\nv_mfma_f32_16x16x32_bf16 v[36:39], a[152:155], v[64:67], v[36:39]\ns_waitcnt lgkmcnt(4)\nds_read_b128 v[76:79], v1 offset:9216\nv_mfma_f32_16x16x32_bf16 v[24:27], a[140:143], v[68:71], v[24:27]\ns_waitcnt lgkmcnt(4)\nds_read_b128 v[64:67], v2 offset:28672\n" \
"v_mfma_f32_16x16x32_bf16 v[28:31], v[80:83], v[68:71], v[28:31]\nv_permlane16_swap_b32 v84, v85\nv_mfma_f32_16x16x32_bf16 v[32:35], a[148:151], v[68:71], v[32:35]\nv_add_f32 v84, v84, v85\nv_mov_b32 v85, v84\ns_nop 0\nv_mfma_f32_16x16x32_bf16 v[36:39], a[156:159], v[68:71], v[36:39]\ns_waitcnt lgkmcnt(4)\n" \
"ds_read_b128 v[80:83], v1 offset:10240\nv_mfma_f32_16x16x32_bf16 v[24:27], a[160:163], v[56:59], v[24:27]\nds_read_b128 v[68:71], v2 offset:30720\nv_mfma_f32_16x16x32_bf16 v[28:31], a[168:171], v[56:59], v[28:31]\ns_waitcnt lgkmcnt(5)\nv_permlane32_swap_b32 v84, v85\nv_mfma_f32_16x16x32_bf16 v[32:35], v[72:75], v[56:59], v[32:35]\nv_add_f32 v84, v84, v85\n" \
"v_mul_f32 v84, 0xbd800000, v84\nv_mfma_f32_16x16x32_bf16 v[36:39], a[176:179], v[56:59], v[36:39]\ns_waitcnt lgkmcnt(4)\nds_read_b128 v[72:75], v1 offset:11264\nv_add_f32 v40, v40, v84\nv_mfma_f32_16x16x32_bf16 v[24:27], a[164:167], v[60:63], v[24:27]\ns_waitcnt vmcnt(4)\ns_barrier\n" \
"ds_read_b128 v[56:59], v2 offset:32768\nv_mfma_f32_16x16x32_bf16 v[28:31], a[172:175], v[60:63], v[28:31]\ns_waitcnt lgkmcnt(5)\nv_mul_f32 v40, 0x41000000, v40\nv_fma_f32 v41, v40, v40, v41\nv_mfma_f32_16x16x32_bf16 v[32:35], v[76:79], v[60:63], v[32:35]\nv_add_f32 v44, v44, v84\nv_mul_f32 v44, 0x41000000, v44\n" \
"v_mfma_f32_16x16x32_bf16 v[36:39], a[180:183], v[60:63], v[36:39]\ns_waitcnt lgkmcnt(4)\nv_fma_f32 v45, v44, v44, v45\nv_add_f32 v48, v48, v84\nv_mfma_f32_16x16x32_bf16 v[24:27], a[184:187], v[64:67], v[24:27]\nds_read_b128 v[60:63], v2 offset:34816\nv_mul_f32 v48, 0x41000000, v48\nv_mfma_f32_16x16x32_bf16 v[28:31], a[192:195], v[64:67], v[28:31]\n" \
"v_fma_f32 v49, v48, v48, v49\nv_add_f32 v52, v52, v84\nv_mfma_f32_16x16x32_bf16 v[32:35], a[200:203], v[64:67], v[32:35]\ns_waitcnt lgkmcnt(4)\nv_mul_f32 v52, 0x41000000, v52\nv_fma_f32 v53, v52, v52, v53\nv_mfma_f32_16x16x32_bf16 v[36:39], v[80:83], v[64:67], v[36:39]\ns_waitcnt lgkmcnt(3)\n" \
"v_add_f32 v85, v41, v45\nv_add_f32 v86, v49, v53\nv_mfma_f32_16x16x32_bf16 v[24:27], a[188:191], v[68:71], v[24:27]\nds_read_b128 v[64:67], v2 offset:36864\nv_add_f32 v85, v85, v86\nv_mfma_f32_16x16x32_bf16 v[28:31], a[196:199], v[68:71], v[28:31]\nv_mov_b32 v86, v85\ns_nop 0\n" \
"v_mfma_f32_16x16x32_bf16 v[32:35], a[204:207], v[68:71], v[32:35]\ns_waitcnt lgkmcnt(3)\nv_permlane16_swap_b32 v85, v86\nv_mfma_f32_16x16x32_bf16 v[36:39], v[72:75], v[68:71], v[36:39]\ns_waitcnt lgkmcnt(2)\nds_read_b128 v[72:75], v1 offset:12288\nv_add_f32 v85, v85, v86\nv_mfma_f32_16x16x32_bf16 v[24:27], a[208:211], v[56:59], v[24:27]\n" \
"ds_read_b128 v[68:71], v2 offset:38912\nv_mov_b32 v86, v85\ns_nop 0\nv_mfma_f32_16x16x32_bf16 v[28:31], a[216:219], v[56:59], v[28:31]\nv_permlane32_swap_b32 v85, v86\nv_mfma_f32_16x16x32_bf16 v[32:35], a[224:227], v[56:59], v[32:35]\nv_add_f32 v85, v85, v86\nv_mul_f32 v85, 0x3a800000, v85\n" \
"v_mfma_f32_16x16x32_bf16 v[36:39], a[232:235], v[56:59], v[36:39]\ns_waitcnt lgkmcnt(3)\nds_read_b128 v[76:79], v1 offset:13312\nv_cvt_pk_bf16_f32 v85, v85, v85\nv_mfma_f32_16x16x32_bf16 v[24:27], a[212:215], v[60:63], v[24:27]\nds_read_b128 v[56:59], v2 offset:40960\nv_and_b32 v85, 0xffff0000, v85\nv_mfma_f32_16x16x32_bf16 v[28:31], a[220:223], v[60:63], v[28:31]\n" \
"v_add_f32 v85, 0x3727c5ac, v85\nv_cvt_pk_bf16_f32 v85, v85, v85\nv_mfma_f32_16x16x32_bf16 v[32:35], a[228:231], v[60:63], v[32:35]\nv_and_b32 v85, 0xffff0000, v85\nv_mfma_f32_16x16x32_bf16 v[36:39], a[236:239], v[60:63], v[36:39]\ns_waitcnt lgkmcnt(4)\nds_read_b128 v[80:83], v1 offset:14336\nv_mfma_f32_16x16x32_bf16 v[24:27], a[240:243], v[64:67], v[24:27]\n" \
"s_waitcnt lgkmcnt(4)\nds_read_b128 v[60:63], v2 offset:43008\nv_mfma_f32_16x16x32_bf16 v[28:31], v[72:75], v[64:67], v[28:31]\nv_sqrt_f32 v85, v85\nv_mfma_f32_16x16x32_bf16 v[32:35], a[248:251], v[64:67], v[32:35]\nv_cvt_pk_bf16_f32 v84, v84, v84\nv_and_b32 v84, 0xffff0000, v84\nv_mfma_f32_16x16x32_bf16 v[36:39], v[96:99], v[64:67], v[36:39]\n" \
"s_waitcnt lgkmcnt(4)\nds_read_b128 v[72:75], v1 offset:15360\nv_cvt_pk_bf16_f32 v85, v85, v85\nv_mfma_f32_16x16x32_bf16 v[24:27], a[244:247], v[68:71], v[24:27]\ns_waitcnt lgkmcnt(4)\nds_read_b128 v[64:67], v2 offset:45056\nv_and_b32 v85, 0xffff0000, v85\nv_mfma_f32_16x16x32_bf16 v[28:31], v[76:79], v[68:71], v[28:31]\n" \
"v_mul_f32 v86, s58, v85\nv_mfma_f32_16x16x32_bf16 v[32:35], a[252:255], v[68:71], v[32:35]\nv_rcp_f32 v85, v85\ns_nop 0\nv_mfma_f32_16x16x32_bf16 v[36:39], v[100:103], v[68:71], v[36:39]\ns_waitcnt lgkmcnt(4)\nds_read_b128 v[76:79], v1 offset:16384\nv_mul_f32 v85, s58, v85\n" \
"v_mfma_f32_16x16x32_bf16 v[24:27], v[104:107], v[56:59], v[24:27]\nds_read_b128 v[68:71], v2 offset:47104\nv_add_f32 v87, v42, v46\nv_mfma_f32_16x16x32_bf16 v[28:31], v[112:115], v[56:59], v[28:31]\ns_waitcnt lgkmcnt(5)\nv_add_f32 v88, v50, v54\nv_add_f32 v87, v87, v88\nv_mfma_f32_16x16x32_bf16 v[32:35], v[80:83], v[56:59], v[32:35]\n" \
"v_mov_b32 v88, v87\ns_nop 0\nv_mfma_f32_16x16x32_bf16 v[36:39], v[120:123], v[56:59], v[36:39]\ns_waitcnt lgkmcnt(4)\nds_read_b128 v[80:83], v1 offset:17408\nv_mfma_f32_16x16x32_bf16 v[24:27], v[108:111], v[60:63], v[24:27]\ns_waitcnt vmcnt(0)\ns_barrier\n" \
"ds_read_b128 v[56:59], v2 offset:49152\nv_mfma_f32_16x16x32_bf16 v[28:31], v[116:119], v[60:63], v[28:31]\ns_waitcnt lgkmcnt(5)\nv_permlane16_swap_b32 v87, v88\nv_mfma_f32_16x16x32_bf16 v[32:35], v[72:75], v[60:63], v[32:35]\nv_add_f32 v87, v87, v88\nv_mov_b32 v88, v87\ns_nop 0\n" \
"v_mfma_f32_16x16x32_bf16 v[36:39], v[124:127], v[60:63], v[36:39]\ns_waitcnt lgkmcnt(4)\nv_permlane32_swap_b32 v87, v88\nv_mfma_f32_16x16x32_bf16 v[24:27], v[128:131], v[64:67], v[24:27]\nds_read_b128 v[60:63], v2 offset:51200\nv_add_f32 v87, v87, v88\nv_mfma_f32_16x16x32_bf16 v[28:31], v[136:139], v[64:67], v[28:31]\nv_mul_f32 v87, 0xbd800000, v87\n" \
"v_add_f32 v42, v42, v87\nv_mfma_f32_16x16x32_bf16 v[32:35], v[144:147], v[64:67], v[32:35]\ns_waitcnt lgkmcnt(4)\nv_mul_f32 v42, 0x41000000, v42\nv_fma_f32 v43, v42, v42, v43\nv_mfma_f32_16x16x32_bf16 v[36:39], v[76:79], v[64:67], v[36:39]\ns_waitcnt lgkmcnt(3)\nv_add_f32 v46, v46, v87\n" \
"v_mul_f32 v46, 0x41000000, v46\nv_mfma_f32_16x16x32_bf16 v[24:27], v[132:135], v[68:71], v[24:27]\nds_read_b128 v[64:67], v2 offset:53248\nv_fma_f32 v47, v46, v46, v47\nv_mfma_f32_16x16x32_bf16 v[28:31], v[140:143], v[68:71], v[28:31]\nv_add_f32 v50, v50, v87\nv_mul_f32 v50, 0x41000000, v50\nv_mfma_f32_16x16x32_bf16 v[32:35], v[148:151], v[68:71], v[32:35]\n" \
"s_waitcnt lgkmcnt(3)\nv_fma_f32 v51, v50, v50, v51\nv_add_f32 v54, v54, v87\nv_mfma_f32_16x16x32_bf16 v[36:39], v[80:83], v[68:71], v[36:39]\ns_waitcnt lgkmcnt(2)\nds_read_b128 v[80:83], v1 offset:18432\nv_mul_f32 v54, 0x41000000, v54\nv_mfma_f32_16x16x32_bf16 v[24:27], v[152:155], v[56:59], v[24:27]\n" \
"ds_read_b128 v[68:71], v2 offset:55296\nv_fma_f32 v55, v54, v54, v55\nv_mfma_f32_16x16x32_bf16 v[28:31], v[160:163], v[56:59], v[28:31]\nv_add_f32 v88, v43, v47\nv_add_f32 v89, v51, v55\nv_mfma_f32_16x16x32_bf16 v[32:35], v[168:171], v[56:59], v[32:35]\nv_add_f32 v88, v88, v89\nv_mov_b32 v89, v88\n" \
"s_nop 0\nv_mfma_f32_16x16x32_bf16 v[36:39], v[176:179], v[56:59], v[36:39]\ns_waitcnt lgkmcnt(3)\nds_read_b128 v[72:75], v1 offset:19456\nv_mfma_f32_16x16x32_bf16 v[24:27], v[156:159], v[60:63], v[24:27]\nds_read_b128 v[56:59], v2 offset:57344\nv_mfma_f32_16x16x32_bf16 v[28:31], v[164:167], v[60:63], v[28:31]\nv_permlane16_swap_b32 v88, v89\n" \
"v_mfma_f32_16x16x32_bf16 v[32:35], v[172:175], v[60:63], v[32:35]\nv_add_f32 v88, v88, v89\nv_mov_b32 v89, v88\ns_nop 0\nv_mfma_f32_16x16x32_bf16 v[36:39], v[180:183], v[60:63], v[36:39]\ns_waitcnt lgkmcnt(4)\nds_read_b128 v[76:79], v1 offset:20480\nv_mfma_f32_16x16x32_bf16 v[24:27], v[184:187], v[64:67], v[24:27]\n" \
"s_waitcnt lgkmcnt(4)\nds_read_b128 v[60:63], v2 offset:59392\nv_mfma_f32_16x16x32_bf16 v[28:31], v[80:83], v[64:67], v[28:31]\nv_permlane32_swap_b32 v88, v89\nv_mfma_f32_16x16x32_bf16 v[32:35], v[192:195], v[64:67], v[32:35]\nv_add_f32 v88, v88, v89\nv_mul_f32 v88, 0x3a800000, v88\nv_mfma_f32_16x16x32_bf16 v[36:39], v[200:203], v[64:67], v[36:39]\n" \
"s_waitcnt lgkmcnt(4)\nds_read_b128 v[80:83], v1 offset:21504\nv_cvt_pk_bf16_f32 v88, v88, v88\nv_mfma_f32_16x16x32_bf16 v[24:27], v[188:191], v[68:71], v[24:27]\ns_waitcnt lgkmcnt(4)\nds_read_b128 v[64:67], v2 offset:61440\nv_and_b32 v88, 0xffff0000, v88\nv_mfma_f32_16x16x32_bf16 v[28:31], v[72:75], v[68:71], v[28:31]\n" \
"v_add_f32 v88, 0x3727c5ac, v88\nv_cvt_pk_bf16_f32 v88, v88, v88\nv_mfma_f32_16x16x32_bf16 v[32:35], v[196:199], v[68:71], v[32:35]\nv_and_b32 v88, 0xffff0000, v88\nv_mfma_f32_16x16x32_bf16 v[36:39], v[204:207], v[68:71], v[36:39]\ns_waitcnt lgkmcnt(4)\nds_read_b128 v[72:75], v1 offset:22528\nv_mfma_f32_16x16x32_bf16 v[24:27], v[208:211], v[56:59], v[24:27]\n" \
"ds_read_b128 v[68:71], v2 offset:63488\nv_mfma_f32_16x16x32_bf16 v[28:31], v[216:219], v[56:59], v[28:31]\ns_waitcnt lgkmcnt(5)\nv_sqrt_f32 v88, v88\nv_mfma_f32_16x16x32_bf16 v[32:35], v[76:79], v[56:59], v[32:35]\nv_cvt_pk_bf16_f32 v87, v87, v87\nv_and_b32 v87, 0xffff0000, v87\nv_mfma_f32_16x16x32_bf16 v[36:39], v[224:227], v[56:59], v[36:39]\n" \
"s_waitcnt lgkmcnt(4)\nds_read_b128 v[76:79], v1 offset:23552\nv_cvt_pk_bf16_f32 v88, v88, v88\nv_mfma_f32_16x16x32_bf16 v[24:27], v[212:215], v[60:63], v[24:27]\nds_read_b128 v[56:59], v2 offset:256\nv_and_b32 v88, 0xffff0000, v88\nv_mfma_f32_16x16x32_bf16 v[28:31], v[220:223], v[60:63], v[28:31]\ns_waitcnt lgkmcnt(5)\n" \
"v_mul_f32 v89, s58, v88\nv_mfma_f32_16x16x32_bf16 v[32:35], v[80:83], v[60:63], v[32:35]\nv_rcp_f32 v88, v88\ns_nop 0\nv_mfma_f32_16x16x32_bf16 v[36:39], v[228:231], v[60:63], v[36:39]\ns_waitcnt lgkmcnt(4)\nv_mul_f32 v88, s58, v88\nv_mfma_f32_16x16x32_bf16 v[24:27], v[232:235], v[64:67], v[24:27]\n" \
"ds_read_b128 v[60:63], v2 offset:2304\nv_mfma_f32_16x16x32_bf16 v[28:31], v[240:243], v[64:67], v[28:31]\nv_mfma_f32_16x16x32_bf16 v[32:35], v[248:251], v[64:67], v[32:35]\ns_waitcnt lgkmcnt(4)\nv_mfma_f32_16x16x32_bf16 v[36:39], v[72:75], v[64:67], v[36:39]\ns_waitcnt lgkmcnt(3)\nv_mfma_f32_16x16x32_bf16 v[24:27], v[236:239], v[68:71], v[24:27]\nds_read_b128 v[64:67], v2 offset:4352\n" \
"v_mfma_f32_16x16x32_bf16 v[28:31], v[244:247], v[68:71], v[28:31]\nv_mfma_f32_16x16x32_bf16 v[32:35], v[252:255], v[68:71], v[32:35]\ns_waitcnt lgkmcnt(3)\nv_mfma_f32_16x16x32_bf16 v[36:39], v[76:79], v[68:71], v[36:39]\ns_waitcnt lgkmcnt(2)\nds_read_b128 v[76:79], v1 offset:0\nv_mfma_f32_16x16x32_bf16 v[40:43], a[0:3], v[56:59], 0\nds_read_b128 v[68:71], v2 offset:6400\n" \
"v_mfma_f32_16x16x32_bf16 v[44:47], a[8:11], v[56:59], 0\ns_lshl_b32 s44, s40, 1\nv_mov_b32 v90, s44\nv_mfma_f32_16x16x32_bf16 v[48:51], a[16:19], v[56:59], 0\nv_mfma_f32_16x16x32_bf16 v[52:55], a[24:27], v[56:59], 0\ns_waitcnt lgkmcnt(3)\nds_read_b128 v[80:83], v1 offset:1024\nv_fma_f32 v24, v8, v84, v24\n" \
"v_mfma_f32_16x16x32_bf16 v[40:43], a[4:7], v[60:63], v[40:43]\nds_read_b128 v[56:59], v2 offset:8448\nv_fma_f32 v25, v9, v84, v25\nv_mfma_f32_16x16x32_bf16 v[44:47], a[12:15], v[60:63], v[44:47]\nv_fma_f32 v32, v16, v84, v32\nv_fma_f32 v33, v17, v84, v33\nv_mfma_f32_16x16x32_bf16 v[48:51], a[20:23], v[60:63], v[48:51]\nv_mul_f32 v24, v24, v85\n" \
"v_mul_f32 v25, v25, v85\nv_mfma_f32_16x16x32_bf16 v[52:55], a[28:31], v[60:63], v[52:55]\ns_waitcnt lgkmcnt(4)\nds_read_b128 v[72:75], v1 offset:2048\nv_fma_f32 v91, v24, v24, s59\nv_mfma_f32_16x16x32_bf16 v[40:43], a[32:35], v[64:67], v[40:43]\ns_waitcnt lgkmcnt(4)\nds_read_b128 v[60:63], v2 offset:10496\n" \
"v_fma_f32 v92, v25, v25, s59\nv_mfma_f32_16x16x32_bf16 v[44:47], v[76:79], v[64:67], v[44:47]\nv_mul_f32 v91, v91, v24\nv_mul_f32 v92, v92, v25\nv_mfma_f32_16x16x32_bf16 v[48:51], a[40:43], v[64:67], v[48:51]\nv_exp_f32 v91, v91\nv_mfma_f32_16x16x32_bf16 v[52:55], a[48:51], v[64:67], v[52:55]\ns_waitcnt lgkmcnt(4)\n" \
"ds_read_b128 v[76:79], v1 offset:3072\nv_mfma_f32_16x16x32_bf16 v[40:43], a[36:39], v[68:71], v[40:43]\ns_waitcnt lgkmcnt(4)\nds_read_b128 v[64:67], v2 offset:12544\nv_mfma_f32_16x16x32_bf16 v[44:47], v[80:83], v[68:71], v[44:47]\nv_exp_f32 v92, v92\nv_mfma_f32_16x16x32_bf16 v[48:51], a[44:47], v[68:71], v[48:51]\nv_mul_f32 v24, v24, v32\n" \
"v_mul_f32 v25, v25, v33\nv_mfma_f32_16x16x32_bf16 v[52:55], a[52:55], v[68:71], v[52:55]\ns_waitcnt lgkmcnt(4)\nds_read_b128 v[80:83], v1 offset:4096\nv_fma_f32 v91, v91, v86, v86\nv_mfma_f32_16x16x32_bf16 v[40:43], a[56:59], v[56:59], v[40:43]\nds_read_b128 v[68:71], v2 offset:14592\nv_fma_f32 v92, v92, v86, v86\n" \
"v_mfma_f32_16x16x32_bf16 v[44:47], a[64:67], v[56:59], v[44:47]\ns_waitcnt lgkmcnt(5)\nv_rcp_f32 v91, v91\nv_mfma_f32_16x16x32_bf16 v[48:51], v[72:75], v[56:59], v[48:51]\nv_rcp_f32 v92, v92\nv_mfma_f32_16x16x32_bf16 v[52:55], a[72:75], v[56:59], v[52:55]\ns_waitcnt lgkmcnt(4)\nds_read_b128 v[72:75], v1 offset:5120\n" \
"v_mul_f32 v24, v24, v91\nv_mfma_f32_16x16x32_bf16 v[40:43], a[60:63], v[60:63], v[40:43]\nds_read_b128 v[56:59], v2 offset:16640\nv_mul_f32 v25, v25, v92\nv_mfma_f32_16x16x32_bf16 v[44:47], a[68:71], v[60:63], v[44:47]\ns_waitcnt lgkmcnt(5)\nv_cvt_pk_bf16_f32 v32, v24, v25\nv_fma_f32 v26, v10, v84, v26\n" \
"v_mfma_f32_16x16x32_bf16 v[48:51], v[76:79], v[60:63], v[48:51]\nv_fma_f32 v27, v11, v84, v27\nv_fma_f32 v34, v18, v84, v34\nv_mfma_f32_16x16x32_bf16 v[52:55], a[76:79], v[60:63], v[52:55]\ns_waitcnt lgkmcnt(4)\nv_fma_f32 v35, v19, v84, v35\nv_mul_f32 v26, v26, v85\nv_mfma_f32_16x16x32_bf16 v[40:43], a[80:83], v[64:67], v[40:43]\n" \
"ds_read_b128 v[60:63], v2 offset:18688\nv_mul_f32 v27, v27, v85\nv_mfma_f32_16x16x32_bf16 v[44:47], a[88:91], v[64:67], v[44:47]\nv_fma_f32 v91, v26, v26, s59\nv_fma_f32 v92, v27, v27, s59\nv_mfma_f32_16x16x32_bf16 v[48:51], a[96:99], v[64:67], v[48:51]\ns_waitcnt lgkmcnt(4)\nv_mul_f32 v91, v91, v26\n" \
"v_mul_f32 v92, v92, v27\nv_mfma_f32_16x16x32_bf16 v[52:55], v[80:83], v[64:67], v[52:55]\ns_waitcnt lgkmcnt(3)\nv_exp_f32 v91, v91\nv_mfma_f32_16x16x32_bf16 v[40:43], a[84:87], v[68:71], v[40:43]\nds_read_b128 v[64:67], v2 offset:20736\nv_mfma_f32_16x16x32_bf16 v[44:47], a[92:95], v[68:71], v[44:47]\nv_exp_f32 v92, v92\n" \
"v_mfma_f32_16x16x32_bf16 v[48:51], a[100:103], v[68:71], v[48:51]\ns_waitcnt lgkmcnt(3)\nv_mul_f32 v26, v26, v34\nv_mul_f32 v27, v27, v35\nv_mfma_f32_16x16x32_bf16 v[52:55], v[72:75], v[68:71], v[52:55]\ns_waitcnt lgkmcnt(2)\nds_read_b128 v[72:75], v1 offset:6144\nv_fma_f32 v91, v91, v86, v86\n" \
"v_mfma_f32_16x16x32_bf16 v[40:43], a[104:107], v[56:59], v[40:43]\nds_read_b128 v[68:71], v2 offset:22784\nv_fma_f32 v92, v92, v86, v86\nv_mfma_f32_16x16x32_bf16 v[44:47], a[112:115], v[56:59], v[44:47]\nv_rcp_f32 v91, v91\nv_mfma_f32_16x16x32_bf16 v[48:51], a[120:123], v[56:59], v[48:51]\nv_rcp_f32 v92, v92\nv_mfma_f32_16x16x32_bf16 v[52:55], a[128:131], v[56:59], v[52:55]\n" \
"s_waitcnt lgkmcnt(3)\nds_read_b128 v[76:79], v1 offset:7168\nv_mul_f32 v26, v26, v91\nv_mfma_f32_16x16x32_bf16 v[40:43], a[108:111], v[60:63], v[40:43]\nds_read_b128 v[56:59], v2 offset:24832\nv_mul_f32 v27, v27, v92\nv_mfma_f32_16x16x32_bf16 v[44:47], a[116:119], v[60:63], v[44:47]\nv_cvt_pk_bf16_f32 v33, v26, v27\n" \
"v_fma_f32 v28, v12, v84, v28\nv_mfma_f32_16x16x32_bf16 v[48:51], a[124:127], v[60:63], v[48:51]\nv_fma_f32 v29, v13, v84, v29\nv_fma_f32 v36, v20, v84, v36\nv_mfma_f32_16x16x32_bf16 v[52:55], a[132:135], v[60:63], v[52:55]\ns_waitcnt lgkmcnt(4)\nds_read_b128 v[80:83], v1 offset:8192\nv_fma_f32 v37, v21, v84, v37\n" \
"v_mfma_f32_16x16x32_bf16 v[40:43], a[136:139], v[64:67], v[40:43]\ns_waitcnt lgkmcnt(4)\nds_read_b128 v[60:63], v2 offset:26880\nv_mul_f32 v28, v28, v85\nv_mfma_f32_16x16x32_bf16 v[44:47], v[72:75], v[64:67], v[44:47]\nv_mul_f32 v29, v29, v85\nv_fma_f32 v91, v28, v28, s59\nv_mfma_f32_16x16x32_bf16 v[48:51], a[144:147], v[64:67], v[48:51]\n" \
"v_fma_f32 v92, v29, v29, s59\nv_mul_f32 v91, v91, v28\nv_mfma_f32_16x16x32_bf16 v[52:55], a[152:155], v[64:67], v[52:55]\ns_waitcnt lgkmcnt(4)\nds_read_b128 v[72:75], v1 offset:9216\nv_mul_f32 v92, v92, v29\nv_mfma_f32_16x16x32_bf16 v[40:43], a[140:143], v[68:71], v[40:43]\ns_waitcnt lgkmcnt(4)\n" \
"ds_read_b128 v[64:67], v2 offset:28928\nv_mfma_f32_16x16x32_bf16 v[44:47], v[76:79], v[68:71], v[44:47]\nv_exp_f32 v91, v91\nv_mfma_f32_16x16x32_bf16 v[48:51], a[148:151], v[68:71], v[48:51]\nv_exp_f32 v92, v92\nv_mfma_f32_16x16x32_bf16 v[52:55], a[156:159], v[68:71], v[52:55]\ns_waitcnt lgkmcnt(4)\nds_read_b128 v[76:79], v1 offset:10240\n" \
"v_mul_f32 v28, v28, v36\nv_mfma_f32_16x16x32_bf16 v[40:43], a[160:163], v[56:59], v[40:43]\nds_read_b128 v[68:71], v2 offset:30976\nv_mul_f32 v29, v29, v37\nv_mfma_f32_16x16x32_bf16 v[44:47], a[168:171], v[56:59], v[44:47]\ns_waitcnt lgkmcnt(5)\nv_fma_f32 v91, v91, v86, v86\nv_fma_f32 v92, v92, v86, v86\n" \
"v_mfma_f32_16x16x32_bf16 v[48:51], v[80:83], v[56:59], v[48:51]\nv_rcp_f32 v91, v91\nv_mfma_f32_16x16x32_bf16 v[52:55], a[176:179], v[56:59], v[52:55]\ns_waitcnt lgkmcnt(4)\nds_read_b128 v[80:83], v1 offset:11264\nv_mfma_f32_16x16x32_bf16 v[40:43], a[164:167], v[60:63], v[40:43]\nds_read_b128 v[56:59], v2 offset:33024\nv_mfma_f32_16x16x32_bf16 v[44:47], a[172:175], v[60:63], v[44:47]\n" \
"s_waitcnt lgkmcnt(5)\nv_rcp_f32 v92, v92\nv_mfma_f32_16x16x32_bf16 v[48:51], v[72:75], v[60:63], v[48:51]\nv_mul_f32 v28, v28, v91\nv_mul_f32 v29, v29, v92\nv_mfma_f32_16x16x32_bf16 v[52:55], a[180:183], v[60:63], v[52:55]\ns_waitcnt lgkmcnt(4)\nv_cvt_pk_bf16_f32 v34, v28, v29\n" \
"v_fma_f32 v30, v14, v84, v30\nv_mfma_f32_16x16x32_bf16 v[40:43], a[184:187], v[64:67], v[40:43]\nds_read_b128 v[60:63], v2 offset:35072\nv_fma_f32 v31, v15, v84, v31\nv_mfma_f32_16x16x32_bf16 v[44:47], a[192:195], v[64:67], v[44:47]\nv_fma_f32 v38, v22, v84, v38\nv_fma_f32 v39, v23, v84, v39\nv_mfma_f32_16x16x32_bf16 v[48:51], a[200:203], v[64:67], v[48:51]\n" \
"s_waitcnt lgkmcnt(4)\nv_mul_f32 v30, v30, v85\nv_mul_f32 v31, v31, v85\nv_mfma_f32_16x16x32_bf16 v[52:55], v[76:79], v[64:67], v[52:55]\ns_waitcnt lgkmcnt(3)\nv_fma_f32 v91, v30, v30, s59\nv_fma_f32 v92, v31, v31, s59\nv_mfma_f32_16x16x32_bf16 v[40:43], a[188:191], v[68:71], v[40:43]\n" \
"ds_read_b128 v[64:67], v2 offset:37120\nv_mul_f32 v91, v91, v30\nv_mfma_f32_16x16x32_bf16 v[44:47], a[196:199], v[68:71], v[44:47]\nv_mul_f32 v92, v92, v31\nv_mfma_f32_16x16x32_bf16 v[48:51], a[204:207], v[68:71], v[48:51]\ns_waitcnt lgkmcnt(3)\nv_exp_f32 v91, v91\nv_mfma_f32_16x16x32_bf16 v[52:55], v[80:83], v[68:71], v[52:55]\n" \
"s_waitcnt lgkmcnt(2)\nds_read_b128 v[80:83], v1 offset:12288\nv_mfma_f32_16x16x32_bf16 v[40:43], a[208:211], v[56:59], v[40:43]\nds_read_b128 v[68:71], v2 offset:39168\nv_mfma_f32_16x16x32_bf16 v[44:47], a[216:219], v[56:59], v[44:47]\nv_exp_f32 v92, v92\nv_mfma_f32_16x16x32_bf16 v[48:51], a[224:227], v[56:59], v[48:51]\nv_mul_f32 v30, v30, v38\n" \
"v_mul_f32 v31, v31, v39\nv_mfma_f32_16x16x32_bf16 v[52:55], a[232:235], v[56:59], v[52:55]\ns_waitcnt lgkmcnt(3)\nds_read_b128 v[72:75], v1 offset:13312\nv_fma_f32 v91, v91, v86, v86\nv_mfma_f32_16x16x32_bf16 v[40:43], a[212:215], v[60:63], v[40:43]\nds_read_b128 v[56:59], v2 offset:41216\nv_fma_f32 v92, v92, v86, v86\n" \
"v_mfma_f32_16x16x32_bf16 v[44:47], a[220:223], v[60:63], v[44:47]\nv_rcp_f32 v91, v91\nv_mfma_f32_16x16x32_bf16 v[48:51], a[228:231], v[60:63], v[48:51]\nv_rcp_f32 v92, v92\nv_mfma_f32_16x16x32_bf16 v[52:55], a[236:239], v[60:63], v[52:55]\ns_waitcnt lgkmcnt(4)\nds_read_b128 v[76:79], v1 offset:14336\nv_mul_f32 v30, v30, v91\n" \
"v_mfma_f32_16x16x32_bf16 v[40:43], a[240:243], v[64:67], v[40:43]\ns_waitcnt lgkmcnt(4)\nds_read_b128 v[60:63], v2 offset:43264\nv_mul_f32 v31, v31, v92\nv_mfma_f32_16x16x32_bf16 v[44:47], v[80:83], v[64:67], v[44:47]\nv_cvt_pk_bf16_f32 v35, v30, v31\nv_mfma_f32_16x16x32_bf16 v[48:51], a[248:251], v[64:67], v[48:51]\nbuffer_store_dwordx4 v[32:35], v4, %[rs], s55 offen offset:0 sc1\n" \
"v_mfma_f32_16x16x32_bf16 v[52:55], v[96:99], v[64:67], v[52:55]\ns_waitcnt lgkmcnt(4)\nds_read_b128 v[80:83], v1 offset:15360\nv_mfma_f32_16x16x32_bf16 v[40:43], a[244:247], v[68:71], v[40:43]\ns_waitcnt lgkmcnt(4)\nds_read_b128 v[64:67], v2 offset:45312\nv_mfma_f32_16x16x32_bf16 v[44:47], v[72:75], v[68:71], v[44:47]\nv_mfma_f32_16x16x32_bf16 v[48:51], a[252:255], v[68:71], v[48:51]\n" \
"v_mfma_f32_16x16x32_bf16 v[52:55], v[100:103], v[68:71], v[52:55]\ns_waitcnt lgkmcnt(4)\nds_read_b128 v[72:75], v1 offset:16384\nv_mfma_f32_16x16x32_bf16 v[40:43], v[104:107], v[56:59], v[40:43]\nds_read_b128 v[68:71], v2 offset:47360\nv_mfma_f32_16x16x32_bf16 v[44:47], v[112:115], v[56:59], v[44:47]\ns_waitcnt lgkmcnt(5)\nv_mfma_f32_16x16x32_bf16 v[48:51], v[76:79], v[56:59], v[48:51]\n" \
"v_mfma_f32_16x16x32_bf16 v[52:55], v[120:123], v[56:59], v[52:55]\ns_waitcnt lgkmcnt(4)\nds_read_b128 v[76:79], v1 offset:17408\nv_mfma_f32_16x16x32_bf16 v[40:43], v[108:111], v[60:63], v[40:43]\nds_read_b128 v[56:59], v2 offset:49408\nv_mfma_f32_16x16x32_bf16 v[44:47], v[116:119], v[60:63], v[44:47]\ns_waitcnt lgkmcnt(5)\nv_mfma_f32_16x16x32_bf16 v[48:51], v[80:83], v[60:63], v[48:51]\n" \
"v_mfma_f32_16x16x32_bf16 v[52:55], v[124:127], v[60:63], v[52:55]\ns_waitcnt lgkmcnt(4)\nv_mfma_f32_16x16x32_bf16 v[40:43], v[128:131], v[64:67], v[40:43]\nds_read_b128 v[60:63], v2 offset:51456\nv_mfma_f32_16x16x32_bf16 v[44:47], v[136:139], v[64:67], v[44:47]\nv_mfma_f32_16x16x32_bf16 v[48:51], v[144:147], v[64:67], v[48:51]\ns_waitcnt lgkmcnt(4)\nv_mfma_f32_16x16x32_bf16 v[52:55], v[72:75], v[64:67], v[52:55]\n" \
"s_waitcnt lgkmcnt(3)\nv_mfma_f32_16x16x32_bf16 v[40:43], v[132:135], v[68:71], v[40:43]\nds_read_b128 v[64:67], v2 offset:53504\nv_mfma_f32_16x16x32_bf16 v[44:47], v[140:143], v[68:71], v[44:47]\nv_mfma_f32_16x16x32_bf16 v[48:51], v[148:151], v[68:71], v[48:51]\ns_waitcnt lgkmcnt(3)\nv_mfma_f32_16x16x32_bf16 v[52:55], v[76:79], v[68:71], v[52:55]\ns_waitcnt lgkmcnt(2)\n" \
"ds_read_b128 v[76:79], v1 offset:18432\nv_mfma_f32_16x16x32_bf16 v[40:43], v[152:155], v[56:59], v[40:43]\nds_read_b128 v[68:71], v2 offset:55552\nv_mfma_f32_16x16x32_bf16 v[44:47], v[160:163], v[56:59], v[44:47]\nv_mfma_f32_16x16x32_bf16 v[48:51], v[168:171], v[56:59], v[48:51]\nv_mfma_f32_16x16x32_bf16 v[52:55], v[176:179], v[56:59], v[52:55]\ns_waitcnt lgkmcnt(3)\nds_read_b128 v[80:83], v1 offset:19456\n" \
"v_mfma_f32_16x16x32_bf16 v[40:43], v[156:159], v[60:63], v[40:43]\nds_read_b128 v[56:59], v2 offset:57600\nv_mfma_f32_16x16x32_bf16 v[44:47], v[164:167], v[60:63], v[44:47]\nv_mfma_f32_16x16x32_bf16 v[48:51], v[172:175], v[60:63], v[48:51]\nv_mfma_f32_16x16x32_bf16 v[52:55], v[180:183], v[60:63], v[52:55]\ns_waitcnt lgkmcnt(4)\nds_read_b128 v[72:75], v1 offset:20480\nv_mfma_f32_16x16x32_bf16 v[40:43], v[184:187], v[64:67], v[40:43]\n" \
"s_waitcnt lgkmcnt(4)\nds_read_b128 v[60:63], v2 offset:59648\nv_mfma_f32_16x16x32_bf16 v[44:47], v[76:79], v[64:67], v[44:47]\ns_waitcnt vmcnt(0)\nbuffer_store_dword v90, off, %[rs], %[fh] sc1\nv_mfma_f32_16x16x32_bf16 v[48:51], v[192:195], v[64:67], v[48:51]\nv_mfma_f32_16x16x32_bf16 v[52:55], v[200:203], v[64:67], v[52:55]\ns_waitcnt lgkmcnt(4)\n" \
"ds_read_b128 v[76:79], v1 offset:21504\nv_mfma_f32_16x16x32_bf16 v[40:43], v[188:191], v[68:71], v[40:43]\ns_waitcnt lgkmcnt(4)\nds_read_b128 v[64:67], v2 offset:61696\nv_mfma_f32_16x16x32_bf16 v[44:47], v[80:83], v[68:71], v[44:47]\nv_mfma_f32_16x16x32_bf16 v[48:51], v[196:199], v[68:71], v[48:51]\nv_mfma_f32_16x16x32_bf16 v[52:55], v[204:207], v[68:71], v[52:55]\ns_waitcnt lgkmcnt(4)\n" \
"ds_read_b128 v[80:83], v1 offset:22528\nv_mfma_f32_16x16x32_bf16 v[40:43], v[208:211], v[56:59], v[40:43]\nds_read_b128 v[68:71], v2 offset:63744\nv_mfma_f32_16x16x32_bf16 v[44:47], v[216:219], v[56:59], v[44:47]\ns_waitcnt lgkmcnt(5)\nv_mfma_f32_16x16x32_bf16 v[48:51], v[72:75], v[56:59], v[48:51]\nv_mfma_f32_16x16x32_bf16 v[52:55], v[224:227], v[56:59], v[52:55]\ns_waitcnt lgkmcnt(4)\n" \
"ds_read_b128 v[72:75], v1 offset:23552\nv_mfma_f32_16x16x32_bf16 v[40:43], v[212:215], v[60:63], v[40:43]\nv_mfma_f32_16x16x32_bf16 v[44:47], v[220:223], v[60:63], v[44:47]\ns_waitcnt lgkmcnt(4)\nv_mfma_f32_16x16x32_bf16 v[48:51], v[76:79], v[60:63], v[48:51]\nv_mfma_f32_16x16x32_bf16 v[52:55], v[228:231], v[60:63], v[52:55]\ns_waitcnt lgkmcnt(3)\nv_mfma_f32_16x16x32_bf16 v[40:43], v[232:235], v[64:67], v[40:43]\n" \
"v_mfma_f32_16x16x32_bf16 v[44:47], v[240:243], v[64:67], v[44:47]\nv_mfma_f32_16x16x32_bf16 v[48:51], v[248:251], v[64:67], v[48:51]\ns_waitcnt lgkmcnt(2)\nv_mfma_f32_16x16x32_bf16 v[52:55], v[80:83], v[64:67], v[52:55]\ns_waitcnt lgkmcnt(1)\nv_mfma_f32_16x16x32_bf16 v[40:43], v[236:239], v[68:71], v[40:43]\nv_mfma_f32_16x16x32_bf16 v[44:47], v[244:247], v[68:71], v[44:47]\nv_mfma_f32_16x16x32_bf16 v[48:51], v[252:255], v[68:71], v[48:51]\n" \
"s_waitcnt lgkmcnt(0)\nv_mfma_f32_16x16x32_bf16 v[52:55], v[72:75], v[68:71], v[52:55]\nv_fma_f32 v40, v8, v87, v40\nv_fma_f32 v41, v9, v87, v41\ns_nop 1\nv_fma_f32 v48, v16, v87, v48\nv_fma_f32 v49, v17, v87, v49\nv_mul_f32 v40, v40, v88\n" \
"v_mul_f32 v41, v41, v88\nv_fma_f32 v91, v40, v40, s59\nv_fma_f32 v92, v41, v41, s59\nv_mul_f32 v91, v91, v40\nv_mul_f32 v92, v92, v41\nv_exp_f32 v91, v91\nv_exp_f32 v92, v92\nv_mul_f32 v40, v40, v48\n" \
"v_mul_f32 v41, v41, v49\nv_fma_f32 v91, v91, v89, v89\nv_fma_f32 v92, v92, v89, v89\nv_rcp_f32 v91, v91\nv_rcp_f32 v92, v92\nv_mul_f32 v40, v40, v91\nv_mul_f32 v41, v41, v92\nv_cvt_pk_bf16_f32 v48, v40, v41\n" \
"v_fma_f32 v42, v10, v87, v42\nv_fma_f32 v43, v11, v87, v43\nv_fma_f32 v50, v18, v87, v50\nv_fma_f32 v51, v19, v87, v51\nv_mul_f32 v42, v42, v88\nv_mul_f32 v43, v43, v88\nv_fma_f32 v91, v42, v42, s59\nv_fma_f32 v92, v43, v43, s59\n" \
"v_mul_f32 v91, v91, v42\nv_mul_f32 v92, v92, v43\nv_exp_f32 v91, v91\nv_exp_f32 v92, v92\nv_mul_f32 v42, v42, v50\nv_mul_f32 v43, v43, v51\nv_fma_f32 v91, v91, v89, v89\nv_fma_f32 v92, v92, v89, v89\n" \
"v_rcp_f32 v91, v91\nv_rcp_f32 v92, v92\nv_mul_f32 v42, v42, v91\nv_mul_f32 v43, v43, v92\nv_cvt_pk_bf16_f32 v49, v42, v43\nv_fma_f32 v44, v12, v87, v44\nv_fma_f32 v45, v13, v87, v45\nv_fma_f32 v52, v20, v87, v52\n" \
"v_fma_f32 v53, v21, v87, v53\nv_mul_f32 v44, v44, v88\nv_mul_f32 v45, v45, v88\nv_fma_f32 v91, v44, v44, s59\nv_fma_f32 v92, v45, v45, s59\nv_mul_f32 v91, v91, v44\nv_mul_f32 v92, v92, v45\nv_exp_f32 v91, v91\n" \
"v_exp_f32 v92, v92\nv_mul_f32 v44, v44, v52\nv_mul_f32 v45, v45, v53\nv_fma_f32 v91, v91, v89, v89\nv_fma_f32 v92, v92, v89, v89\nv_rcp_f32 v91, v91\nv_rcp_f32 v92, v92\nv_mul_f32 v44, v44, v91\n" \
"v_mul_f32 v45, v45, v92\nv_cvt_pk_bf16_f32 v50, v44, v45\nv_fma_f32 v46, v14, v87, v46\nv_fma_f32 v47, v15, v87, v47\nv_fma_f32 v54, v22, v87, v54\nv_fma_f32 v55, v23, v87, v55\nv_mul_f32 v46, v46, v88\nv_mul_f32 v47, v47, v88\n" \
"v_fma_f32 v91, v46, v46, s59\nv_fma_f32 v92, v47, v47, s59\nv_mul_f32 v91, v91, v46\nv_mul_f32 v92, v92, v47\nv_exp_f32 v91, v91\nv_exp_f32 v92, v92\nv_mul_f32 v46, v46, v54\nv_mul_f32 v47, v47, v55\n" \
"v_fma_f32 v91, v91, v89, v89\nv_fma_f32 v92, v92, v89, v89\nv_rcp_f32 v91, v91\nv_rcp_f32 v92, v92\nv_mul_f32 v46, v46, v91\nv_mul_f32 v47, v47, v92\nv_cvt_pk_bf16_f32 v51, v46, v47\nbuffer_store_dwordx4 v[48:51], v4, %[rs], s55 offen offset:1024 sc1\n" \
"v_add_u32 v90, 1, v90\ns_mov_b32 s56, %[fh]\ns_add_u32 s40, s40, 1\ns_cmp_lt_u32 s40, 64\ns_cbranch_scc1 .Lgg_next%=\ns_waitcnt vmcnt(0)\nbuffer_store_dword v90, off, %[rs], s56 sc1\ns_branch .Lgg_end%=\n" \
".Lgg_abort%=:\ns_mov_b64 exec, -1\nv_mov_b32 v16, 0\ns_movk_i32 s44, 6400\nbuffer_store_dword v16, off, %[rs], s44 sc1\n.Lgg_end%=:\ns_waitcnt vmcnt(0) lgkmcnt(0)\n" \
""
#define DD_ASM \
"v_mbcnt_lo_u32_b32 v255, -1, 0\nv_mbcnt_hi_u32_b32 v255, -1, v255\nv_lshlrev_b32 v255, 2, v255\nv_add_u32 v255, %[tb], v255\nds_read_b32 v0, v255 offset:0\nds_read_b32 v1, v255 offset:256\nds_read_b32 v2, v255 offset:512\nds_read_b32 v3, v255 offset:768\n" \
"ds_read_b32 v4, v255 offset:1024\nds_read_b32 v5, v255 offset:1280\nds_read_b32 v6, v255 offset:1536\nds_read_b32 v7, v255 offset:1792\nds_read_b32 v125, v255 offset:2048\nds_read_b32 v126, v255 offset:2304\nds_read_b32 v127, v255 offset:2560\ns_waitcnt lgkmcnt(0)\n" \
"s_add_u32 m0, %[tb], 8192\ns_add_u32 s44, %[wo], 0\nbuffer_load_dwordx4 v125, %[rw], s44 offen nt lds\ns_add_u32 m0, %[tb], 9216\ns_add_u32 s44, %[wo], 16384\nbuffer_load_dwordx4 v125, %[rw], s44 offen nt lds\ns_add_u32 m0, %[tb], 10240\ns_add_u32 s44, %[wo], 32768\n" \
"buffer_load_dwordx4 v125, %[rw], s44 offen nt lds\ns_add_u32 m0, %[tb], 11264\ns_add_u32 s44, %[wo], 49152\nbuffer_load_dwordx4 v125, %[rw], s44 offen nt lds\ns_add_u32 m0, %[lw], 0\ns_add_u32 s44, %[wo], 65536\nbuffer_load_dwordx4 v125, %[rw], s44 offen nt lds\ns_add_u32 m0, %[lw], 1024\n" \
"s_add_u32 s44, %[wo], 81920\nbuffer_load_dwordx4 v125, %[rw], s44 offen nt lds\ns_add_u32 m0, %[lw], 2048\ns_add_u32 s44, %[wo], 98304\nbuffer_load_dwordx4 v125, %[rw], s44 offen nt lds\ns_add_u32 m0, %[lw], 3072\ns_add_u32 s44, %[wo], 114688\nbuffer_load_dwordx4 v125, %[rw], s44 offen nt lds\n" \
"s_add_u32 m0, %[lw], 4096\ns_add_u32 s44, %[wo], 131072\nbuffer_load_dwordx4 v125, %[rw], s44 offen nt lds\ns_add_u32 m0, %[lw], 5120\ns_add_u32 s44, %[wo], 147456\nbuffer_load_dwordx4 v125, %[rw], s44 offen nt lds\ns_add_u32 m0, %[lw], 6144\ns_add_u32 s44, %[wo], 163840\n" \
"buffer_load_dwordx4 v125, %[rw], s44 offen nt lds\ns_add_u32 m0, %[lw], 7168\ns_add_u32 s44, %[wo], 180224\nbuffer_load_dwordx4 v125, %[rw], s44 offen nt lds\ns_add_u32 m0, %[lw], 8192\ns_add_u32 s44, %[wo], 196608\nbuffer_load_dwordx4 v125, %[rw], s44 offen nt lds\ns_add_u32 m0, %[lw], 9216\n" \
"s_add_u32 s44, %[wo], 212992\nbuffer_load_dwordx4 v125, %[rw], s44 offen nt lds\ns_add_u32 m0, %[lw], 10240\ns_add_u32 s44, %[wo], 229376\nbuffer_load_dwordx4 v125, %[rw], s44 offen nt lds\ns_add_u32 m0, %[lw], 11264\ns_add_u32 s44, %[wo], 245760\nbuffer_load_dwordx4 v125, %[rw], s44 offen nt lds\n" \
"s_add_u32 m0, %[lw], 12288\ns_add_u32 s44, %[wo], 262144\nbuffer_load_dwordx4 v125, %[rw], s44 offen nt lds\ns_add_u32 m0, %[lw], 13312\ns_add_u32 s44, %[wo], 278528\nbuffer_load_dwordx4 v125, %[rw], s44 offen nt lds\ns_add_u32 m0, %[lw], 14336\ns_add_u32 s44, %[wo], 294912\n" \
"buffer_load_dwordx4 v125, %[rw], s44 offen nt lds\ns_add_u32 m0, %[lw], 15360\ns_add_u32 s44, %[wo], 311296\nbuffer_load_dwordx4 v125, %[rw], s44 offen nt lds\ns_add_u32 m0, %[lw], 16384\ns_add_u32 s44, %[wo], 327680\nbuffer_load_dwordx4 v125, %[rw], s44 offen nt lds\ns_add_u32 m0, %[lw], 17408\n" \
"s_add_u32 s44, %[wo], 344064\nbuffer_load_dwordx4 v125, %[rw], s44 offen nt lds\ns_add_u32 m0, %[lw], 18432\ns_add_u32 s44, %[wo], 360448\nbuffer_load_dwordx4 v125, %[rw], s44 offen nt lds\ns_add_u32 m0, %[lw], 19456\ns_add_u32 s44, %[wo], 376832\nbuffer_load_dwordx4 v125, %[rw], s44 offen nt lds\n" \
"s_add_u32 m0, %[lw], 20480\ns_add_u32 s44, %[wo], 393216\nbuffer_load_dwordx4 v125, %[rw], s44 offen nt lds\ns_add_u32 m0, %[lw], 21504\ns_add_u32 s44, %[wo], 409600\nbuffer_load_dwordx4 v125, %[rw], s44 offen nt lds\ns_add_u32 m0, %[lw], 22528\ns_add_u32 s44, %[wo], 425984\n" \
"buffer_load_dwordx4 v125, %[rw], s44 offen nt lds\ns_add_u32 m0, %[lw], 23552\ns_add_u32 s44, %[wo], 442368\nbuffer_load_dwordx4 v125, %[rw], s44 offen nt lds\ns_add_u32 m0, %[tb], 0\ns_add_u32 s44, %[wo], 458752\nbuffer_load_dwordx4 v125, %[rw], s44 offen nt lds\ns_add_u32 m0, %[tb], 1024\n" \
"s_add_u32 s44, %[wo], 475136\nbuffer_load_dwordx4 v125, %[rw], s44 offen nt lds\ns_add_u32 m0, %[tb], 2048\ns_add_u32 s44, %[wo], 491520\nbuffer_load_dwordx4 v125, %[rw], s44 offen nt lds\ns_add_u32 m0, %[tb], 3072\ns_add_u32 s44, %[wo], 507904\nbuffer_load_dwordx4 v125, %[rw], s44 offen nt lds\n" \
"s_add_u32 m0, %[tb], 4096\ns_add_u32 s44, %[wo], 524288\nbuffer_load_dwordx4 v125, %[rw], s44 offen nt lds\ns_add_u32 m0, %[tb], 5120\ns_add_u32 s44, %[wo], 540672\nbuffer_load_dwordx4 v125, %[rw], s44 offen nt lds\ns_add_u32 m0, %[tb], 6144\ns_add_u32 s44, %[wo], 557056\n" \
"buffer_load_dwordx4 v125, %[rw], s44 offen nt lds\ns_add_u32 m0, %[tb], 7168\ns_add_u32 s44, %[wo], 573440\nbuffer_load_dwordx4 v125, %[rw], s44 offen nt lds\ns_waitcnt vmcnt(32)\nds_read_b64_tr_b16 a[0:1], v126 offset:8192\nds_read_b64_tr_b16 a[2:3], v126 offset:8704\nds_read_b64_tr_b16 a[8:9], v126 offset:8224\n" \
"ds_read_b64_tr_b16 a[10:11], v126 offset:8736\nds_read_b64_tr_b16 a[16:17], v126 offset:8256\nds_read_b64_tr_b16 a[18:19], v126 offset:8768\nds_read_b64_tr_b16 a[24:25], v126 offset:8288\nds_read_b64_tr_b16 a[26:27], v126 offset:8800\ns_waitcnt lgkmcnt(0)\ns_add_u32 m0, %[tb], 8192\ns_add_u32 s44, %[wo], 589824\n" \
"buffer_load_dwordx4 v125, %[rw], s44 offen nt lds\ns_add_u32 m0, %[tb], 9216\ns_add_u32 s44, %[wo], 606208\nbuffer_load_dwordx4 v125, %[rw], s44 offen nt lds\ns_add_u32 m0, %[tb], 10240\ns_add_u32 s44, %[wo], 622592\nbuffer_load_dwordx4 v125, %[rw], s44 offen nt lds\ns_add_u32 m0, %[tb], 11264\n" \
"s_add_u32 s44, %[wo], 638976\nbuffer_load_dwordx4 v125, %[rw], s44 offen nt lds\ns_waitcnt vmcnt(32)\nds_read_b64_tr_b16 a[4:5], v127 offset:0\nds_read_b64_tr_b16 a[6:7], v127 offset:512\nds_read_b64_tr_b16 a[12:13], v127 offset:32\nds_read_b64_tr_b16 a[14:15], v127 offset:544\nds_read_b64_tr_b16 a[20:21], v127 offset:64\n" \
"ds_read_b64_tr_b16 a[22:23], v127 offset:576\nds_read_b64_tr_b16 a[28:29], v127 offset:96\nds_read_b64_tr_b16 a[30:31], v127 offset:608\ns_waitcnt lgkmcnt(0)\ns_add_u32 m0, %[lw], 0\ns_add_u32 s44, %[wo], 655360\nbuffer_load_dwordx4 v125, %[rw], s44 offen nt lds\ns_add_u32 m0, %[lw], 1024\n" \
"s_add_u32 s44, %[wo], 671744\nbuffer_load_dwordx4 v125, %[rw], s44 offen nt lds\ns_add_u32 m0, %[lw], 2048\ns_add_u32 s44, %[wo], 688128\nbuffer_load_dwordx4 v125, %[rw], s44 offen nt lds\ns_add_u32 m0, %[lw], 3072\ns_add_u32 s44, %[wo], 704512\nbuffer_load_dwordx4 v125, %[rw], s44 offen nt lds\n" \
"s_waitcnt vmcnt(32)\nds_read_b64_tr_b16 a[32:33], v127 offset:4096\nds_read_b64_tr_b16 a[34:35], v127 offset:4608\nds_read_b64_tr_b16 v[92:93], v127 offset:4128\nds_read_b64_tr_b16 v[94:95], v127 offset:4640\nds_read_b64_tr_b16 a[40:41], v127 offset:4160\nds_read_b64_tr_b16 a[42:43], v127 offset:4672\nds_read_b64_tr_b16 a[48:49], v127 offset:4192\n" \
"ds_read_b64_tr_b16 a[50:51], v127 offset:4704\ns_waitcnt lgkmcnt(0)\ns_add_u32 m0, %[lw], 4096\ns_add_u32 s44, %[wo], 720896\nbuffer_load_dwordx4 v125, %[rw], s44 offen nt lds\ns_add_u32 m0, %[lw], 5120\ns_add_u32 s44, %[wo], 737280\nbuffer_load_dwordx4 v125, %[rw], s44 offen nt lds\n" \
"s_add_u32 m0, %[lw], 6144\ns_add_u32 s44, %[wo], 753664\nbuffer_load_dwordx4 v125, %[rw], s44 offen nt lds\ns_add_u32 m0, %[lw], 7168\ns_add_u32 s44, %[wo], 770048\nbuffer_load_dwordx4 v125, %[rw], s44 offen nt lds\ns_waitcnt vmcnt(32)\nds_read_b64_tr_b16 a[36:37], v127 offset:8192\n" \
"ds_read_b64_tr_b16 a[38:39], v127 offset:8704\nds_read_b64_tr_b16 v[88:89], v127 offset:8224\nds_read_b64_tr_b16 v[90:91], v127 offset:8736\nds_read_b64_tr_b16 a[44:45], v127 offset:8256\nds_read_b64_tr_b16 a[46:47], v127 offset:8768\nds_read_b64_tr_b16 a[52:53], v127 offset:8288\nds_read_b64_tr_b16 a[54:55], v127 offset:8800\ns_waitcnt lgkmcnt(0)\n" \
"s_add_u32 m0, %[lw], 8192\ns_add_u32 s44, %[wo], 786432\nbuffer_load_dwordx4 v125, %[rw], s44 offen nt lds\ns_add_u32 m0, %[lw], 9216\ns_add_u32 s44, %[wo], 802816\nbuffer_load_dwordx4 v125, %[rw], s44 offen nt lds\ns_add_u32 m0, %[lw], 10240\ns_add_u32 s44, %[wo], 819200\n" \
"buffer_load_dwordx4 v125, %[rw], s44 offen nt lds\ns_add_u32 m0, %[lw], 11264\ns_add_u32 s44, %[wo], 835584\nbuffer_load_dwordx4 v125, %[rw], s44 offen nt lds\ns_waitcnt vmcnt(32)\nds_read_b64_tr_b16 a[56:57], v127 offset:12288\nds_read_b64_tr_b16 a[58:59], v127 offset:12800\nds_read_b64_tr_b16 a[64:65], v127 offset:12320\n" \
"ds_read_b64_tr_b16 a[66:67], v127 offset:12832\nds_read_b64_tr_b16 a[72:73], v127 offset:12352\nds_read_b64_tr_b16 a[74:75], v127 offset:12864\nds_read_b64_tr_b16 a[80:81], v127 offset:12384\nds_read_b64_tr_b16 a[82:83], v127 offset:12896\ns_waitcnt lgkmcnt(0)\ns_add_u32 m0, %[lw], 12288\ns_add_u32 s44, %[wo], 851968\n" \
"buffer_load_dwordx4 v125, %[rw], s44 offen nt lds\ns_add_u32 m0, %[lw], 13312\ns_add_u32 s44, %[wo], 868352\nbuffer_load_dwordx4 v125, %[rw], s44 offen nt lds\ns_add_u32 m0, %[lw], 14336\ns_add_u32 s44, %[wo], 884736\nbuffer_load_dwordx4 v125, %[rw], s44 offen nt lds\ns_add_u32 m0, %[lw], 15360\n" \
"s_add_u32 s44, %[wo], 901120\nbuffer_load_dwordx4 v125, %[rw], s44 offen nt lds\ns_waitcnt vmcnt(32)\nds_read_b64_tr_b16 a[60:61], v127 offset:16384\nds_read_b64_tr_b16 a[62:63], v127 offset:16896\nds_read_b64_tr_b16 a[68:69], v127 offset:16416\nds_read_b64_tr_b16 a[70:71], v127 offset:16928\nds_read_b64_tr_b16 a[76:77], v127 offset:16448\n" \
"ds_read_b64_tr_b16 a[78:79], v127 offset:16960\nds_read_b64_tr_b16 a[84:85], v127 offset:16480\nds_read_b64_tr_b16 a[86:87], v127 offset:16992\ns_waitcnt lgkmcnt(0)\ns_add_u32 m0, %[lw], 16384\ns_add_u32 s44, %[wo], 917504\nbuffer_load_dwordx4 v125, %[rw], s44 offen nt lds\ns_add_u32 m0, %[lw], 17408\n" \
"s_add_u32 s44, %[wo], 933888\nbuffer_load_dwordx4 v125, %[rw], s44 offen nt lds\ns_add_u32 m0, %[lw], 18432\ns_add_u32 s44, %[wo], 950272\nbuffer_load_dwordx4 v125, %[rw], s44 offen nt lds\ns_add_u32 m0, %[lw], 19456\ns_add_u32 s44, %[wo], 966656\nbuffer_load_dwordx4 v125, %[rw], s44 offen nt lds\n" \
"s_waitcnt vmcnt(32)\nds_read_b64_tr_b16 a[88:89], v127 offset:20480\nds_read_b64_tr_b16 a[90:91], v127 offset:20992\nds_read_b64_tr_b16 a[96:97], v127 offset:20512\nds_read_b64_tr_b16 a[98:99], v127 offset:21024\nds_read_b64_tr_b16 a[104:105], v127 offset:20544\nds_read_b64_tr_b16 a[106:107], v127 offset:21056\nds_read_b64_tr_b16 v[84:85], v127 offset:20576\n" \
"ds_read_b64_tr_b16 v[86:87], v127 offset:21088\ns_waitcnt lgkmcnt(0)\ns_add_u32 m0, %[lw], 20480\ns_add_u32 s44, %[wo], 983040\nbuffer_load_dwordx4 v125, %[rw], s44 offen nt lds\ns_add_u32 m0, %[lw], 21504\ns_add_u32 s44, %[wo], 999424\nbuffer_load_dwordx4 v125, %[rw], s44 offen nt lds\n" \
"s_add_u32 m0, %[lw], 22528\ns_add_u32 s44, %[wo], 1015808\nbuffer_load_dwordx4 v125, %[rw], s44 offen nt lds\ns_add_u32 m0, %[lw], 23552\ns_add_u32 s44, %[wo], 1032192\nbuffer_load_dwordx4 v125, %[rw], s44 offen nt lds\ns_waitcnt vmcnt(32)\nds_read_b64_tr_b16 a[92:93], v126 offset:0\n" \
"ds_read_b64_tr_b16 a[94:95], v126 offset:512\nds_read_b64_tr_b16 a[100:101], v126 offset:32\nds_read_b64_tr_b16 a[102:103], v126 offset:544\nds_read_b64_tr_b16 a[108:109], v126 offset:64\nds_read_b64_tr_b16 a[110:111], v126 offset:576\nds_read_b64_tr_b16 v[80:81], v126 offset:96\nds_read_b64_tr_b16 v[82:83], v126 offset:608\ns_waitcnt lgkmcnt(0)\n" \
"s_add_u32 m0, %[tb], 0\ns_add_u32 s44, %[wo], 1048576\nbuffer_load_dwordx4 v125, %[rw], s44 offen nt lds\ns_add_u32 m0, %[tb], 1024\ns_add_u32 s44, %[wo], 1064960\nbuffer_load_dwordx4 v125, %[rw], s44 offen nt lds\ns_add_u32 m0, %[tb], 2048\ns_add_u32 s44, %[wo], 1081344\n" \
"buffer_load_dwordx4 v125, %[rw], s44 offen nt lds\ns_add_u32 m0, %[tb], 3072\ns_add_u32 s44, %[wo], 1097728\nbuffer_load_dwordx4 v125, %[rw], s44 offen nt lds\ns_waitcnt vmcnt(32)\nds_read_b64_tr_b16 a[112:113], v126 offset:4096\nds_read_b64_tr_b16 a[114:115], v126 offset:4608\nds_read_b64_tr_b16 a[120:121], v126 offset:4128\n" \
"ds_read_b64_tr_b16 a[122:123], v126 offset:4640\nds_read_b64_tr_b16 a[128:129], v126 offset:4160\nds_read_b64_tr_b16 a[130:131], v126 offset:4672\nds_read_b64_tr_b16 a[136:137], v126 offset:4192\nds_read_b64_tr_b16 a[138:139], v126 offset:4704\ns_waitcnt lgkmcnt(0)\ns_add_u32 m0, %[tb], 4096\ns_add_u32 s44, %[wo], 1114112\n" \
"buffer_load_dwordx4 v125, %[rw], s44 offen nt lds\ns_add_u32 m0, %[tb], 5120\ns_add_u32 s44, %[wo], 1130496\nbuffer_load_dwordx4 v125, %[rw], s44 offen nt lds\ns_add_u32 m0, %[tb], 6144\ns_add_u32 s44, %[wo], 1146880\nbuffer_load_dwordx4 v125, %[rw], s44 offen nt lds\ns_add_u32 m0, %[tb], 7168\n" \
"s_add_u32 s44, %[wo], 1163264\nbuffer_load_dwordx4 v125, %[rw], s44 offen nt lds\ns_waitcnt vmcnt(32)\nds_read_b64_tr_b16 a[116:117], v126 offset:8192\nds_read_b64_tr_b16 a[118:119], v126 offset:8704\nds_read_b64_tr_b16 a[124:125], v126 offset:8224\nds_read_b64_tr_b16 a[126:127], v126 offset:8736\nds_read_b64_tr_b16 a[132:133], v126 offset:8256\n" \
"ds_read_b64_tr_b16 a[134:135], v126 offset:8768\nds_read_b64_tr_b16 a[140:141], v126 offset:8288\nds_read_b64_tr_b16 a[142:143], v126 offset:8800\ns_waitcnt lgkmcnt(0)\ns_add_u32 m0, %[tb], 8192\ns_add_u32 s44, %[wo], 1179648\nbuffer_load_dwordx4 v125, %[rw], s44 offen nt lds\ns_add_u32 m0, %[tb], 9216\n" \
"s_add_u32 s44, %[wo], 1196032\nbuffer_load_dwordx4 v125, %[rw], s44 offen nt lds\ns_add_u32 m0, %[tb], 10240\ns_add_u32 s44, %[wo], 1212416\nbuffer_load_dwordx4 v125, %[rw], s44 offen nt lds\ns_add_u32 m0, %[tb], 11264\ns_add_u32 s44, %[wo], 1228800\nbuffer_load_dwordx4 v125, %[rw], s44 offen nt lds\n" \
"s_waitcnt vmcnt(32)\nds_read_b64_tr_b16 a[144:145], v127 offset:0\nds_read_b64_tr_b16 a[146:147], v127 offset:512\nds_read_b64_tr_b16 v[76:77], v127 offset:32\nds_read_b64_tr_b16 v[78:79], v127 offset:544\nds_read_b64_tr_b16 a[152:153], v127 offset:64\nds_read_b64_tr_b16 a[154:155], v127 offset:576\nds_read_b64_tr_b16 a[160:161], v127 offset:96\n" \
"ds_read_b64_tr_b16 a[162:163], v127 offset:608\ns_waitcnt lgkmcnt(0)\ns_add_u32 m0, %[lw], 0\ns_add_u32 s44, %[wo], 1245184\nbuffer_load_dwordx4 v125, %[rw], s44 offen nt lds\ns_add_u32 m0, %[lw], 1024\ns_add_u32 s44, %[wo], 1261568\nbuffer_load_dwordx4 v125, %[rw], s44 offen nt lds\n" \
"s_add_u32 m0, %[lw], 2048\ns_add_u32 s44, %[wo], 1277952\nbuffer_load_dwordx4 v125, %[rw], s44 offen nt lds\ns_add_u32 m0, %[lw], 3072\ns_add_u32 s44, %[wo], 1294336\nbuffer_load_dwordx4 v125, %[rw], s44 offen nt lds\ns_waitcnt vmcnt(32)\nds_read_b64_tr_b16 a[148:149], v127 offset:4096\n" \
"ds_read_b64_tr_b16 a[150:151], v127 offset:4608\nds_read_b64_tr_b16 v[72:73], v127 offset:4128\nds_read_b64_tr_b16 v[74:75], v127 offset:4640\nds_read_b64_tr_b16 a[156:157], v127 offset:4160\nds_read_b64_tr_b16 a[158:159], v127 offset:4672\nds_read_b64_tr_b16 a[164:165], v127 offset:4192\nds_read_b64_tr_b16 a[166:167], v127 offset:4704\ns_waitcnt lgkmcnt(0)\n" \
"s_add_u32 m0, %[lw], 4096\ns_add_u32 s44, %[wo], 1310720\nbuffer_load_dwordx4 v125, %[rw], s44 offen nt lds\ns_add_u32 m0, %[lw], 5120\ns_add_u32 s44, %[wo], 1327104\nbuffer_load_dwordx4 v125, %[rw], s44 offen nt lds\ns_add_u32 m0, %[lw], 6144\ns_add_u32 s44, %[wo], 1343488\n" \
"buffer_load_dwordx4 v125, %[rw], s44 offen nt lds\ns_add_u32 m0, %[lw], 7168\ns_add_u32 s44, %[wo], 1359872\nbuffer_load_dwordx4 v125, %[rw], s44 offen nt lds\ns_waitcnt vmcnt(32)\nds_read_b64_tr_b16 a[168:169], v127 offset:8192\nds_read_b64_tr_b16 a[170:171], v127 offset:8704\nds_read_b64_tr_b16 a[176:177], v127 offset:8224\n" \
"ds_read_b64_tr_b16 a[178:179], v127 offset:8736\nds_read_b64_tr_b16 v[68:69], v127 offset:8256\nds_read_b64_tr_b16 v[70:71], v127 offset:8768\nds_read_b64_tr_b16 a[184:185], v127 offset:8288\nds_read_b64_tr_b16 a[186:187], v127 offset:8800\ns_waitcnt lgkmcnt(0)\ns_add_u32 m0, %[lw], 8192\ns_add_u32 s44, %[wo], 1376256\n" \
"buffer_load_dwordx4 v125, %[rw], s44 offen nt lds\ns_add_u32 m0, %[lw], 9216\ns_add_u32 s44, %[wo], 1392640\nbuffer_load_dwordx4 v125, %[rw], s44 offen nt lds\ns_add_u32 m0, %[lw], 10240\ns_add_u32 s44, %[wo], 1409024\nbuffer_load_dwordx4 v125, %[rw], s44 offen nt lds\ns_add_u32 m0, %[lw], 11264\n" \
"s_add_u32 s44, %[wo], 1425408\nbuffer_load_dwordx4 v125, %[rw], s44 offen nt lds\ns_waitcnt vmcnt(32)\nds_read_b64_tr_b16 a[172:173], v127 offset:12288\nds_read_b64_tr_b16 a[174:175], v127 offset:12800\nds_read_b64_tr_b16 a[180:181], v127 offset:12320\nds_read_b64_tr_b16 a[182:183], v127 offset:12832\nds_read_b64_tr_b16 v[64:65], v127 offset:12352\n" \
"ds_read_b64_tr_b16 v[66:67], v127 offset:12864\nds_read_b64_tr_b16 a[188:189], v127 offset:12384\nds_read_b64_tr_b16 a[190:191], v127 offset:12896\ns_waitcnt lgkmcnt(0)\ns_add_u32 m0, %[lw], 12288\ns_add_u32 s44, %[wo], 1441792\nbuffer_load_dwordx4 v125, %[rw], s44 offen nt lds\ns_add_u32 m0, %[lw], 13312\n" \
"s_add_u32 s44, %[wo], 1458176\nbuffer_load_dwordx4 v125, %[rw], s44 offen nt lds\ns_add_u32 m0, %[lw], 14336\ns_add_u32 s44, %[wo], 1474560\nbuffer_load_dwordx4 v125, %[rw], s44 offen nt lds\ns_add_u32 m0, %[lw], 15360\ns_add_u32 s44, %[wo], 1490944\nbuffer_load_dwordx4 v125, %[rw], s44 offen nt lds\n" \
"s_waitcnt vmcnt(32)\nds_read_b64_tr_b16 a[192:193], v127 offset:16384\nds_read_b64_tr_b16 a[194:195], v127 offset:16896\nds_read_b64_tr_b16 a[200:201], v127 offset:16416\nds_read_b64_tr_b16 a[202:203], v127 offset:16928\nds_read_b64_tr_b16 a[208:209], v127 offset:16448\nds_read_b64_tr_b16 a[210:211], v127 offset:16960\nds_read_b64_tr_b16 a[216:217], v127 offset:16480\n" \
"ds_read_b64_tr_b16 a[218:219], v127 offset:16992\ns_waitcnt lgkmcnt(0)\ns_add_u32 m0, %[lw], 16384\ns_add_u32 s44, %[wo], 1507328\nbuffer_load_dwordx4 v125, %[rw], s44 offen nt lds\ns_add_u32 m0, %[lw], 17408\ns_add_u32 s44, %[wo], 1523712\nbuffer_load_dwordx4 v125, %[rw], s44 offen nt lds\n" \
"s_add_u32 m0, %[lw], 18432\ns_add_u32 s44, %[wo], 1540096\nbuffer_load_dwordx4 v125, %[rw], s44 offen nt lds\ns_add_u32 m0, %[lw], 19456\ns_add_u32 s44, %[wo], 1556480\nbuffer_load_dwordx4 v125, %[rw], s44 offen nt lds\ns_waitcnt vmcnt(32)\nds_read_b64_tr_b16 a[196:197], v127 offset:20480\n" \
"ds_read_b64_tr_b16 a[198:199], v127 offset:20992\nds_read_b64_tr_b16 a[204:205], v127 offset:20512\nds_read_b64_tr_b16 a[206:207], v127 offset:21024\nds_read_b64_tr_b16 a[212:213], v127 offset:20544\nds_read_b64_tr_b16 a[214:215], v127 offset:21056\nds_read_b64_tr_b16 a[220:221], v127 offset:20576\nds_read_b64_tr_b16 a[222:223], v127 offset:21088\ns_waitcnt lgkmcnt(0)\n" \
"s_add_u32 m0, %[lw], 20480\ns_add_u32 s44, %[wo], 1572864\nbuffer_load_dwordx4 v125, %[rw], s44 offen nt lds\ns_add_u32 m0, %[lw], 21504\ns_add_u32 s44, %[wo], 1589248\nbuffer_load_dwordx4 v125, %[rw], s44 offen nt lds\ns_add_u32 m0, %[lw], 22528\ns_add_u32 s44, %[wo], 1605632\n" \
"buffer_load_dwordx4 v125, %[rw], s44 offen nt lds\ns_add_u32 m0, %[lw], 23552\ns_add_u32 s44, %[wo], 1622016\nbuffer_load_dwordx4 v125, %[rw], s44 offen nt lds\ns_waitcnt vmcnt(32)\nds_read_b64_tr_b16 v[60:61], v126 offset:0\nds_read_b64_tr_b16 v[62:63], v126 offset:512\nds_read_b64_tr_b16 a[224:225], v126 offset:32\n" \
"ds_read_b64_tr_b16 a[226:227], v126 offset:544\nds_read_b64_tr_b16 a[232:233], v126 offset:64\nds_read_b64_tr_b16 a[234:235], v126 offset:576\nds_read_b64_tr_b16 a[240:241], v126 offset:96\nds_read_b64_tr_b16 a[242:243], v126 offset:608\ns_waitcnt lgkmcnt(0)\ns_add_u32 m0, %[tb], 0\ns_add_u32 s44, %[wo], 1638400\n" \
"buffer_load_dwordx4 v125, %[rw], s44 offen nt lds\ns_add_u32 m0, %[tb], 1024\ns_add_u32 s44, %[wo], 1654784\nbuffer_load_dwordx4 v125, %[rw], s44 offen nt lds\ns_add_u32 m0, %[tb], 2048\ns_add_u32 s44, %[wo], 1671168\nbuffer_load_dwordx4 v125, %[rw], s44 offen nt lds\ns_add_u32 m0, %[tb], 3072\n" \
"s_add_u32 s44, %[wo], 1687552\nbuffer_load_dwordx4 v125, %[rw], s44 offen nt lds\ns_waitcnt vmcnt(32)\nds_read_b64_tr_b16 v[56:57], v126 offset:4096\nds_read_b64_tr_b16 v[58:59], v126 offset:4608\nds_read_b64_tr_b16 a[228:229], v126 offset:4128\nds_read_b64_tr_b16 a[230:231], v126 offset:4640\nds_read_b64_tr_b16 a[236:237], v126 offset:4160\n" \
"ds_read_b64_tr_b16 a[238:239], v126 offset:4672\nds_read_b64_tr_b16 a[244:245], v126 offset:4192\nds_read_b64_tr_b16 a[246:247], v126 offset:4704\ns_waitcnt lgkmcnt(0)\ns_add_u32 m0, %[tb], 4096\ns_add_u32 s44, %[wo], 1703936\nbuffer_load_dwordx4 v125, %[rw], s44 offen nt lds\ns_add_u32 m0, %[tb], 5120\n" \
"s_add_u32 s44, %[wo], 1720320\nbuffer_load_dwordx4 v125, %[rw], s44 offen nt lds\ns_add_u32 m0, %[tb], 6144\ns_add_u32 s44, %[wo], 1736704\nbuffer_load_dwordx4 v125, %[rw], s44 offen nt lds\ns_add_u32 m0, %[tb], 7168\ns_add_u32 s44, %[wo], 1753088\nbuffer_load_dwordx4 v125, %[rw], s44 offen nt lds\n" \
"s_waitcnt vmcnt(32)\nds_read_b64_tr_b16 a[248:249], v126 offset:8192\nds_read_b64_tr_b16 a[250:251], v126 offset:8704\nds_read_b64_tr_b16 v[128:129], v126 offset:8224\nds_read_b64_tr_b16 v[130:131], v126 offset:8736\nds_read_b64_tr_b16 v[136:137], v126 offset:8256\nds_read_b64_tr_b16 v[138:139], v126 offset:8768\nds_read_b64_tr_b16 v[144:145], v126 offset:8288\n" \
"ds_read_b64_tr_b16 v[146:147], v126 offset:8800\ns_waitcnt lgkmcnt(0)\ns_add_u32 m0, %[tb], 8192\ns_add_u32 s44, %[wo], 1769472\nbuffer_load_dwordx4 v125, %[rw], s44 offen nt lds\ns_add_u32 m0, %[tb], 9216\ns_add_u32 s44, %[wo], 1785856\nbuffer_load_dwordx4 v125, %[rw], s44 offen nt lds\n" \
"s_add_u32 m0, %[tb], 10240\ns_add_u32 s44, %[wo], 1802240\nbuffer_load_dwordx4 v125, %[rw], s44 offen nt lds\ns_add_u32 m0, %[tb], 11264\ns_add_u32 s44, %[wo], 1818624\nbuffer_load_dwordx4 v125, %[rw], s44 offen nt lds\ns_waitcnt vmcnt(32)\nds_read_b64_tr_b16 a[252:253], v127 offset:0\n" \
"ds_read_b64_tr_b16 a[254:255], v127 offset:512\nds_read_b64_tr_b16 v[132:133], v127 offset:32\nds_read_b64_tr_b16 v[134:135], v127 offset:544\nds_read_b64_tr_b16 v[140:141], v127 offset:64\nds_read_b64_tr_b16 v[142:143], v127 offset:576\nds_read_b64_tr_b16 v[148:149], v127 offset:96\nds_read_b64_tr_b16 v[150:151], v127 offset:608\ns_waitcnt lgkmcnt(0)\n" \
"ds_write_b128 v1, v[92:95] offset:0\nds_write_b128 v1, v[88:91] offset:1024\nds_write_b128 v1, v[84:87] offset:2048\nds_write_b128 v1, v[80:83] offset:3072\ns_waitcnt vmcnt(28)\nds_read_b64_tr_b16 v[152:153], v127 offset:4096\nds_read_b64_tr_b16 v[154:155], v127 offset:4608\nds_read_b64_tr_b16 v[160:161], v127 offset:4128\n" \
"ds_read_b64_tr_b16 v[162:163], v127 offset:4640\nds_read_b64_tr_b16 v[80:81], v127 offset:4160\nds_read_b64_tr_b16 v[82:83], v127 offset:4672\nds_read_b64_tr_b16 v[168:169], v127 offset:4192\nds_read_b64_tr_b16 v[170:171], v127 offset:4704\ns_waitcnt lgkmcnt(0)\nds_write_b128 v1, v[76:79] offset:4096\nds_write_b128 v1, v[72:75] offset:5120\n" \
"ds_write_b128 v1, v[68:71] offset:6144\nds_write_b128 v1, v[64:67] offset:7168\ns_waitcnt vmcnt(24)\nds_read_b64_tr_b16 v[156:157], v127 offset:8192\nds_read_b64_tr_b16 v[158:159], v127 offset:8704\nds_read_b64_tr_b16 v[164:165], v127 offset:8224\nds_read_b64_tr_b16 v[166:167], v127 offset:8736\nds_read_b64_tr_b16 v[64:65], v127 offset:8256\n" \
"ds_read_b64_tr_b16 v[66:67], v127 offset:8768\nds_read_b64_tr_b16 v[172:173], v127 offset:8288\nds_read_b64_tr_b16 v[174:175], v127 offset:8800\ns_waitcnt lgkmcnt(0)\nds_write_b128 v1, v[60:63] offset:8192\nds_write_b128 v1, v[56:59] offset:9216\nds_write_b128 v1, v[80:83] offset:10240\nds_write_b128 v1, v[64:67] offset:11264\n" \
"s_waitcnt vmcnt(20)\nds_read_b64_tr_b16 v[176:177], v127 offset:12288\nds_read_b64_tr_b16 v[178:179], v127 offset:12800\nds_read_b64_tr_b16 v[184:185], v127 offset:12320\nds_read_b64_tr_b16 v[186:187], v127 offset:12832\nds_read_b64_tr_b16 v[192:193], v127 offset:12352\nds_read_b64_tr_b16 v[194:195], v127 offset:12864\nds_read_b64_tr_b16 v[200:201], v127 offset:12384\n" \
"ds_read_b64_tr_b16 v[202:203], v127 offset:12896\ns_waitcnt lgkmcnt(0)\ns_waitcnt vmcnt(16)\nds_read_b64_tr_b16 v[180:181], v127 offset:16384\nds_read_b64_tr_b16 v[182:183], v127 offset:16896\nds_read_b64_tr_b16 v[188:189], v127 offset:16416\nds_read_b64_tr_b16 v[190:191], v127 offset:16928\nds_read_b64_tr_b16 v[196:197], v127 offset:16448\n" \
"ds_read_b64_tr_b16 v[198:199], v127 offset:16960\nds_read_b64_tr_b16 v[204:205], v127 offset:16480\nds_read_b64_tr_b16 v[206:207], v127 offset:16992\ns_waitcnt lgkmcnt(0)\ns_waitcnt vmcnt(12)\nds_read_b64_tr_b16 v[64:65], v127 offset:20480\nds_read_b64_tr_b16 v[66:67], v127 offset:20992\nds_read_b64_tr_b16 v[208:209], v127 offset:20512\n" \
"ds_read_b64_tr_b16 v[210:211], v127 offset:21024\nds_read_b64_tr_b16 v[216:217], v127 offset:20544\nds_read_b64_tr_b16 v[218:219], v127 offset:21056\nds_read_b64_tr_b16 v[224:225], v127 offset:20576\nds_read_b64_tr_b16 v[226:227], v127 offset:21088\ns_waitcnt lgkmcnt(0)\nds_write_b128 v1, v[64:67] offset:12288\ns_waitcnt vmcnt(8)\n" \
"ds_read_b64_tr_b16 v[64:65], v126 offset:0\nds_read_b64_tr_b16 v[66:67], v126 offset:512\nds_read_b64_tr_b16 v[212:213], v126 offset:32\nds_read_b64_tr_b16 v[214:215], v126 offset:544\nds_read_b64_tr_b16 v[220:221], v126 offset:64\nds_read_b64_tr_b16 v[222:223], v126 offset:576\nds_read_b64_tr_b16 v[228:229], v126 offset:96\nds_read_b64_tr_b16 v[230:231], v126 offset:608\n" \
"s_waitcnt lgkmcnt(0)\nds_write_b128 v1, v[64:67] offset:13312\ns_waitcnt vmcnt(4)\nds_read_b64_tr_b16 v[232:233], v126 offset:4096\nds_read_b64_tr_b16 v[234:235], v126 offset:4608\nds_read_b64_tr_b16 v[64:65], v126 offset:4128\nds_read_b64_tr_b16 v[66:67], v126 offset:4640\nds_read_b64_tr_b16 v[240:241], v126 offset:4160\n" \
"ds_read_b64_tr_b16 v[242:243], v126 offset:4672\nds_read_b64_tr_b16 v[248:249], v126 offset:4192\nds_read_b64_tr_b16 v[250:251], v126 offset:4704\ns_waitcnt lgkmcnt(0)\nds_write_b128 v1, v[64:67] offset:14336\ns_waitcnt vmcnt(0)\nds_read_b64_tr_b16 v[236:237], v126 offset:8192\nds_read_b64_tr_b16 v[238:239], v126 offset:8704\n" \
"ds_read_b64_tr_b16 v[64:65], v126 offset:8224\nds_read_b64_tr_b16 v[66:67], v126 offset:8736\nds_read_b64_tr_b16 v[244:245], v126 offset:8256\nds_read_b64_tr_b16 v[246:247], v126 offset:8768\nds_read_b64_tr_b16 v[252:253], v126 offset:8288\nds_read_b64_tr_b16 v[254:255], v126 offset:8800\ns_waitcnt lgkmcnt(0)\nds_write_b128 v1, v[64:67] offset:15360\n" \
"s_waitcnt lgkmcnt(0)\ns_barrier\ns_mov_b32 s40, 0\ns_mov_b32 s43, 0\ns_mov_b32 s56, -2147483648\n.Ldd_next%=:\nbuffer_load_dword v8, v7, %[rs], 0 offen sc1\ns_lshl_b32 s58, s40, 1\n" \
"s_lshl_b32 s44, s40, 19\ns_add_u32 s44, s44, %[ho]\ns_lshr_b32 s45, s44, 8\ns_and_b32 s45, s45, 0x2000\ns_xor_b32 s55, s44, s45\ns_waitcnt vmcnt(0)\nbuffer_store_dword v9, off, %[rs], s56 sc1\n.Ldd_w0%=:\n" \
"v_cmp_gt_i32 vcc, s58, v8\ns_cbranch_vccz .Ldd_w0go%=\ns_sleep 16\nbuffer_load_dword v8, v7, %[rs], 0 offen sc1\ns_waitcnt vmcnt(0)\ns_add_u32 s43, s43, 1\ns_cmp_lt_u32 s43, 262144\ns_cbranch_scc1 .Ldd_w0%=\n" \
"s_branch .Ldd_abort%=\n.Ldd_w0go%=:\ns_add_u32 s60, s55, 0\nbuffer_load_dwordx4 v[48:51], v0, %[rs], s60 offen offset:0 sc1\nbuffer_load_dwordx4 v[52:55], v0, %[rs], s60 offen offset:2048 sc1\ns_add_u32 s60, s55, 4096\nbuffer_load_dwordx4 v[56:59], v0, %[rs], s60 offen offset:0 sc1\nbuffer_load_dwordx4 v[60:63], v0, %[rs], s60 offen offset:2048 sc1\n" \
"s_add_u32 s60, s55, 16384\nbuffer_load_dwordx4 v[64:67], v0, %[rs], s60 offen offset:0 sc1\nbuffer_load_dwordx4 v[68:71], v0, %[rs], s60 offen offset:2048 sc1\ns_add_u32 s60, s55, 20480\nbuffer_load_dwordx4 v[72:75], v0, %[rs], s60 offen offset:0 sc1\nbuffer_load_dwordx4 v[76:79], v0, %[rs], s60 offen offset:2048 sc1\ns_add_u32 s60, s55, 32768\nbuffer_load_dwordx4 v[80:83], v0, %[rs], s60 offen offset:0 sc1\n" \
"buffer_load_dwordx4 v[84:87], v0, %[rs], s60 offen offset:2048 sc1\ns_add_u32 s60, s55, 36864\nbuffer_load_dwordx4 v[88:91], v0, %[rs], s60 offen offset:0 sc1\nbuffer_load_dwordx4 v[92:95], v0, %[rs], s60 offen offset:2048 sc1\ns_add_u32 s60, s55, 49152\nbuffer_load_dwordx4 v[96:99], v0, %[rs], s60 offen offset:0 sc1\nbuffer_load_dwordx4 v[100:103], v0, %[rs], s60 offen offset:2048 sc1\ns_add_u32 s60, s55, 53248\n" \
"buffer_load_dwordx4 v[104:107], v0, %[rs], s60 offen offset:0 sc1\nbuffer_load_dwordx4 v[108:111], v0, %[rs], s60 offen offset:2048 sc1\ns_add_u32 s44, s40, 0\ns_lshl_b32 s44, s44, 17\ns_add_u32 s44, s44, %[xn]\ns_lshr_b32 s45, s44, 8\ns_and_b32 s45, s45, 0x2000\ns_xor_b32 s41, s44, s45\n" \
"s_lshl_b32 s44, s40, 16\ns_cmp_eq_u32 %[l], 0\ns_cselect_b32 s42, s41, s44\ns_add_u32 s58, s58, 1\nds_read_b128 v[120:123], v1 offset:0\ns_waitcnt vmcnt(15)\nv_mfma_f32_16x16x32_bf16 v[16:19], a[0:3], v[48:51], 0\nds_read_b128 v[124:127], v1 offset:1024\n" \
"v_mfma_f32_16x16x32_bf16 v[24:27], a[8:11], v[48:51], 0\nbuffer_load_dword v8, v7, %[rs], 0 offen sc1\nv_mfma_f32_16x16x32_bf16 v[32:35], a[16:19], v[48:51], 0\nv_mfma_f32_16x16x32_bf16 v[40:43], a[24:27], v[48:51], 0\ns_add_u32 s60, s55, 65536\ns_waitcnt vmcnt(15)\nbuffer_load_dwordx4 v[48:51], v0, %[rs], s60 offen offset:0 sc1\nv_mfma_f32_16x16x32_bf16 v[16:19], a[4:7], v[52:55], v[16:19]\n" \
"v_mfma_f32_16x16x32_bf16 v[24:27], a[12:15], v[52:55], v[24:27]\nv_mfma_f32_16x16x32_bf16 v[32:35], a[20:23], v[52:55], v[32:35]\nv_mfma_f32_16x16x32_bf16 v[40:43], a[28:31], v[52:55], v[40:43]\ns_waitcnt vmcnt(15)\nbuffer_load_dwordx4 v[52:55], v0, %[rs], s60 offen offset:2048 sc1\nv_mfma_f32_16x16x32_bf16 v[16:19], a[32:35], v[56:59], v[16:19]\ns_waitcnt lgkmcnt(1)\nv_mfma_f32_16x16x32_bf16 v[24:27], v[120:123], v[56:59], v[24:27]\n" \
"v_mfma_f32_16x16x32_bf16 v[32:35], a[40:43], v[56:59], v[32:35]\nv_mfma_f32_16x16x32_bf16 v[40:43], a[48:51], v[56:59], v[40:43]\ns_add_u32 s60, s55, 69632\ns_waitcnt vmcnt(15)\nbuffer_load_dwordx4 v[56:59], v0, %[rs], s60 offen offset:0 sc1\nv_mfma_f32_16x16x32_bf16 v[16:19], a[36:39], v[60:63], v[16:19]\ns_waitcnt lgkmcnt(0)\nds_read_b128 v[120:123], v1 offset:2048\n" \
"v_mfma_f32_16x16x32_bf16 v[24:27], v[124:127], v[60:63], v[24:27]\nv_mfma_f32_16x16x32_bf16 v[32:35], a[44:47], v[60:63], v[32:35]\nv_mfma_f32_16x16x32_bf16 v[40:43], a[52:55], v[60:63], v[40:43]\ns_waitcnt vmcnt(15)\nbuffer_load_dwordx4 v[60:63], v0, %[rs], s60 offen offset:2048 sc1\nv_mfma_f32_16x16x32_bf16 v[16:19], a[56:59], v[64:67], v[16:19]\nds_read_b128 v[124:127], v1 offset:3072\nv_mfma_f32_16x16x32_bf16 v[24:27], a[64:67], v[64:67], v[24:27]\n" \
"v_mfma_f32_16x16x32_bf16 v[32:35], a[72:75], v[64:67], v[32:35]\nv_mfma_f32_16x16x32_bf16 v[40:43], a[80:83], v[64:67], v[40:43]\ns_add_u32 s60, s55, 81920\ns_waitcnt vmcnt(15)\nbuffer_load_dwordx4 v[64:67], v0, %[rs], s60 offen offset:0 sc1\nv_mfma_f32_16x16x32_bf16 v[16:19], a[60:63], v[68:71], v[16:19]\nv_mfma_f32_16x16x32_bf16 v[24:27], a[68:71], v[68:71], v[24:27]\nv_mfma_f32_16x16x32_bf16 v[32:35], a[76:79], v[68:71], v[32:35]\n" \
"v_mfma_f32_16x16x32_bf16 v[40:43], a[84:87], v[68:71], v[40:43]\ns_waitcnt vmcnt(15)\nbuffer_load_dwordx4 v[68:71], v0, %[rs], s60 offen offset:2048 sc1\nv_mfma_f32_16x16x32_bf16 v[16:19], a[88:91], v[72:75], v[16:19]\nv_mfma_f32_16x16x32_bf16 v[24:27], a[96:99], v[72:75], v[24:27]\nv_mfma_f32_16x16x32_bf16 v[32:35], a[104:107], v[72:75], v[32:35]\ns_waitcnt lgkmcnt(1)\nv_mfma_f32_16x16x32_bf16 v[40:43], v[120:123], v[72:75], v[40:43]\n" \
"s_add_u32 s60, s55, 86016\ns_waitcnt vmcnt(15)\nbuffer_load_dwordx4 v[72:75], v0, %[rs], s60 offen offset:0 sc1\nv_mfma_f32_16x16x32_bf16 v[16:19], a[92:95], v[76:79], v[16:19]\nds_read_b128 v[120:123], v1 offset:4096\nv_mfma_f32_16x16x32_bf16 v[24:27], a[100:103], v[76:79], v[24:27]\nv_mfma_f32_16x16x32_bf16 v[32:35], a[108:111], v[76:79], v[32:35]\ns_waitcnt lgkmcnt(1)\n" \
"v_mfma_f32_16x16x32_bf16 v[40:43], v[124:127], v[76:79], v[40:43]\ns_waitcnt vmcnt(15)\nbuffer_load_dwordx4 v[76:79], v0, %[rs], s60 offen offset:2048 sc1\nv_mfma_f32_16x16x32_bf16 v[16:19], a[112:115], v[80:83], v[16:19]\nds_read_b128 v[124:127], v1 offset:5120\nv_mfma_f32_16x16x32_bf16 v[24:27], a[120:123], v[80:83], v[24:27]\nv_mfma_f32_16x16x32_bf16 v[32:35], a[128:131], v[80:83], v[32:35]\nv_mfma_f32_16x16x32_bf16 v[40:43], a[136:139], v[80:83], v[40:43]\n" \
"s_add_u32 s60, s55, 98304\ns_waitcnt vmcnt(15)\nbuffer_load_dwordx4 v[80:83], v0, %[rs], s60 offen offset:0 sc1\nv_mfma_f32_16x16x32_bf16 v[16:19], a[116:119], v[84:87], v[16:19]\nds_read_b128 v[112:115], v1 offset:6144\nv_mfma_f32_16x16x32_bf16 v[24:27], a[124:127], v[84:87], v[24:27]\nv_mfma_f32_16x16x32_bf16 v[32:35], a[132:135], v[84:87], v[32:35]\nv_mfma_f32_16x16x32_bf16 v[40:43], a[140:143], v[84:87], v[40:43]\n" \
"s_waitcnt vmcnt(15)\nbuffer_load_dwordx4 v[84:87], v0, %[rs], s60 offen offset:2048 sc1\nv_mfma_f32_16x16x32_bf16 v[16:19], a[144:147], v[88:91], v[16:19]\ns_waitcnt lgkmcnt(2)\nds_read_b128 v[116:119], v1 offset:7168\nv_mfma_f32_16x16x32_bf16 v[24:27], v[120:123], v[88:91], v[24:27]\nv_mfma_f32_16x16x32_bf16 v[32:35], a[152:155], v[88:91], v[32:35]\nv_mfma_f32_16x16x32_bf16 v[40:43], a[160:163], v[88:91], v[40:43]\n" \
"s_add_u32 s60, s55, 102400\ns_waitcnt vmcnt(15)\nbuffer_load_dwordx4 v[88:91], v0, %[rs], s60 offen offset:0 sc1\nv_mfma_f32_16x16x32_bf16 v[16:19], a[148:151], v[92:95], v[16:19]\ns_waitcnt lgkmcnt(2)\nv_mfma_f32_16x16x32_bf16 v[24:27], v[124:127], v[92:95], v[24:27]\ns_waitcnt vmcnt(11)\nv_cmp_gt_i32 vcc, s58, v8\n" \
"v_mfma_f32_16x16x32_bf16 v[32:35], a[156:159], v[92:95], v[32:35]\ns_cmp_eq_u64 vcc, 0\ns_cselect_b32 s61, s55, -2147483648\nv_mfma_f32_16x16x32_bf16 v[40:43], a[164:167], v[92:95], v[40:43]\nbuffer_load_dwordx4 v[92:95], v0, %[rs], s60 offen offset:2048 sc1\nv_mfma_f32_16x16x32_bf16 v[16:19], a[168:171], v[96:99], v[16:19]\nv_mfma_f32_16x16x32_bf16 v[24:27], a[176:179], v[96:99], v[24:27]\ns_waitcnt lgkmcnt(1)\n" \
"v_mfma_f32_16x16x32_bf16 v[32:35], v[112:115], v[96:99], v[32:35]\nv_mfma_f32_16x16x32_bf16 v[40:43], a[184:187], v[96:99], v[40:43]\ns_add_u32 s60, s61, 0\nbuffer_load_dwordx4 v[96:99], v0, %[rs], s60 offen offset:1024 sc1\nv_mfma_f32_16x16x32_bf16 v[16:19], a[172:175], v[100:103], v[16:19]\nds_read_b128 v[112:115], v1 offset:8192\nv_mfma_f32_16x16x32_bf16 v[24:27], a[180:183], v[100:103], v[24:27]\ns_waitcnt lgkmcnt(1)\n" \
"v_mfma_f32_16x16x32_bf16 v[32:35], v[116:119], v[100:103], v[32:35]\nv_mfma_f32_16x16x32_bf16 v[40:43], a[188:191], v[100:103], v[40:43]\nbuffer_load_dwordx4 v[100:103], v0, %[rs], s60 offen offset:3072 sc1\nv_mfma_f32_16x16x32_bf16 v[16:19], a[192:195], v[104:107], v[16:19]\nds_read_b128 v[116:119], v1 offset:9216\nv_mfma_f32_16x16x32_bf16 v[24:27], a[200:203], v[104:107], v[24:27]\nv_mfma_f32_16x16x32_bf16 v[32:35], a[208:211], v[104:107], v[32:35]\nv_mfma_f32_16x16x32_bf16 v[40:43], a[216:219], v[104:107], v[40:43]\n" \
"s_add_u32 s60, s61, 4096\nbuffer_load_dwordx4 v[104:107], v0, %[rs], s60 offen offset:1024 sc1\nv_mfma_f32_16x16x32_bf16 v[16:19], a[196:199], v[108:111], v[16:19]\nv_mfma_f32_16x16x32_bf16 v[24:27], a[204:207], v[108:111], v[24:27]\nv_mfma_f32_16x16x32_bf16 v[32:35], a[212:215], v[108:111], v[32:35]\nv_mfma_f32_16x16x32_bf16 v[40:43], a[220:223], v[108:111], v[40:43]\ns_waitcnt vmcnt(14)\ns_waitcnt lgkmcnt(1)\n" \
"buffer_load_dwordx4 v[108:111], v0, %[rs], s60 offen offset:3072 sc1\nv_mfma_f32_16x16x32_bf16 v[16:19], v[112:115], v[48:51], v[16:19]\nv_mfma_f32_16x16x32_bf16 v[24:27], a[224:227], v[48:51], v[24:27]\nv_mfma_f32_16x16x32_bf16 v[32:35], a[232:235], v[48:51], v[32:35]\nv_mfma_f32_16x16x32_bf16 v[40:43], a[240:243], v[48:51], v[40:43]\ns_add_u32 s60, s61, 16384\ns_waitcnt vmcnt(14)\ns_waitcnt lgkmcnt(0)\n" \
"buffer_load_dwordx4 v[48:51], v0, %[rs], s60 offen offset:1024 sc1\nv_mfma_f32_16x16x32_bf16 v[16:19], v[116:119], v[52:55], v[16:19]\nds_read_b128 v[112:115], v1 offset:10240\nv_mfma_f32_16x16x32_bf16 v[24:27], a[228:231], v[52:55], v[24:27]\nv_mfma_f32_16x16x32_bf16 v[32:35], a[236:239], v[52:55], v[32:35]\nv_mfma_f32_16x16x32_bf16 v[40:43], a[244:247], v[52:55], v[40:43]\ns_waitcnt vmcnt(14)\nbuffer_load_dwordx4 v[52:55], v0, %[rs], s60 offen offset:3072 sc1\n" \
"v_mfma_f32_16x16x32_bf16 v[16:19], a[248:251], v[56:59], v[16:19]\nds_read_b128 v[116:119], v1 offset:11264\nv_mfma_f32_16x16x32_bf16 v[24:27], v[128:131], v[56:59], v[24:27]\nv_mfma_f32_16x16x32_bf16 v[32:35], v[136:139], v[56:59], v[32:35]\nv_mfma_f32_16x16x32_bf16 v[40:43], v[144:147], v[56:59], v[40:43]\ns_add_u32 s60, s61, 20480\ns_waitcnt vmcnt(14)\nbuffer_load_dwordx4 v[56:59], v0, %[rs], s60 offen offset:1024 sc1\n" \
"v_mfma_f32_16x16x32_bf16 v[16:19], a[252:255], v[60:63], v[16:19]\nv_mfma_f32_16x16x32_bf16 v[24:27], v[132:135], v[60:63], v[24:27]\nv_mfma_f32_16x16x32_bf16 v[32:35], v[140:143], v[60:63], v[32:35]\nv_mfma_f32_16x16x32_bf16 v[40:43], v[148:151], v[60:63], v[40:43]\ns_waitcnt vmcnt(14)\nbuffer_load_dwordx4 v[60:63], v0, %[rs], s60 offen offset:3072 sc1\nv_mfma_f32_16x16x32_bf16 v[16:19], v[152:155], v[64:67], v[16:19]\nv_mfma_f32_16x16x32_bf16 v[24:27], v[160:163], v[64:67], v[24:27]\n" \
"s_waitcnt lgkmcnt(1)\nv_mfma_f32_16x16x32_bf16 v[32:35], v[112:115], v[64:67], v[32:35]\nv_mfma_f32_16x16x32_bf16 v[40:43], v[168:171], v[64:67], v[40:43]\ns_add_u32 s60, s61, 32768\ns_waitcnt vmcnt(14)\nbuffer_load_dwordx4 v[64:67], v0, %[rs], s60 offen offset:1024 sc1\nv_mfma_f32_16x16x32_bf16 v[16:19], v[156:159], v[68:71], v[16:19]\nds_read_b128 v[112:115], v1 offset:12288\n" \
"v_mfma_f32_16x16x32_bf16 v[24:27], v[164:167], v[68:71], v[24:27]\ns_waitcnt lgkmcnt(1)\nv_mfma_f32_16x16x32_bf16 v[32:35], v[116:119], v[68:71], v[32:35]\nv_mfma_f32_16x16x32_bf16 v[40:43], v[172:175], v[68:71], v[40:43]\ns_waitcnt vmcnt(14)\nbuffer_load_dwordx4 v[68:71], v0, %[rs], s60 offen offset:3072 sc1\nv_mfma_f32_16x16x32_bf16 v[16:19], v[176:179], v[72:75], v[16:19]\nds_read_b128 v[116:119], v1 offset:13312\n" \
"v_mfma_f32_16x16x32_bf16 v[24:27], v[184:187], v[72:75], v[24:27]\nv_mfma_f32_16x16x32_bf16 v[32:35], v[192:195], v[72:75], v[32:35]\nv_mfma_f32_16x16x32_bf16 v[40:43], v[200:203], v[72:75], v[40:43]\ns_add_u32 s60, s61, 36864\ns_waitcnt vmcnt(14)\nbuffer_load_dwordx4 v[72:75], v0, %[rs], s60 offen offset:1024 sc1\nv_mfma_f32_16x16x32_bf16 v[16:19], v[180:183], v[76:79], v[16:19]\nds_read_b128 v[120:123], v1 offset:14336\n" \
"v_mfma_f32_16x16x32_bf16 v[24:27], v[188:191], v[76:79], v[24:27]\nv_mfma_f32_16x16x32_bf16 v[32:35], v[196:199], v[76:79], v[32:35]\nv_mfma_f32_16x16x32_bf16 v[40:43], v[204:207], v[76:79], v[40:43]\ns_waitcnt vmcnt(14)\ns_waitcnt lgkmcnt(2)\nbuffer_load_dwordx4 v[76:79], v0, %[rs], s60 offen offset:3072 sc1\nv_mfma_f32_16x16x32_bf16 v[16:19], v[112:115], v[80:83], v[16:19]\nds_read_b128 v[124:127], v1 offset:15360\n" \
"v_mfma_f32_16x16x32_bf16 v[24:27], v[208:211], v[80:83], v[24:27]\nv_mfma_f32_16x16x32_bf16 v[32:35], v[216:219], v[80:83], v[32:35]\nv_mfma_f32_16x16x32_bf16 v[40:43], v[224:227], v[80:83], v[40:43]\ns_add_u32 s60, s61, 49152\ns_waitcnt vmcnt(14)\ns_waitcnt lgkmcnt(2)\nbuffer_load_dwordx4 v[80:83], v0, %[rs], s60 offen offset:1024 sc1\nv_mfma_f32_16x16x32_bf16 v[16:19], v[116:119], v[84:87], v[16:19]\n" \
"v_mfma_f32_16x16x32_bf16 v[24:27], v[212:215], v[84:87], v[24:27]\nv_mfma_f32_16x16x32_bf16 v[32:35], v[220:223], v[84:87], v[32:35]\nv_mfma_f32_16x16x32_bf16 v[40:43], v[228:231], v[84:87], v[40:43]\ns_waitcnt vmcnt(14)\nbuffer_load_dwordx4 v[84:87], v0, %[rs], s60 offen offset:3072 sc1\nv_mfma_f32_16x16x32_bf16 v[16:19], v[232:235], v[88:91], v[16:19]\ns_waitcnt lgkmcnt(1)\nv_mfma_f32_16x16x32_bf16 v[24:27], v[120:123], v[88:91], v[24:27]\n" \
"v_mfma_f32_16x16x32_bf16 v[32:35], v[240:243], v[88:91], v[32:35]\nv_mfma_f32_16x16x32_bf16 v[40:43], v[248:251], v[88:91], v[40:43]\ns_add_u32 s60, s61, 53248\ns_waitcnt vmcnt(14)\nbuffer_load_dwordx4 v[88:91], v0, %[rs], s60 offen offset:1024 sc1\nv_mfma_f32_16x16x32_bf16 v[16:19], v[236:239], v[92:95], v[16:19]\ns_waitcnt lgkmcnt(0)\nds_read_b128 v[120:123], v1 offset:0\n" \
"v_mfma_f32_16x16x32_bf16 v[24:27], v[124:127], v[92:95], v[24:27]\nv_mfma_f32_16x16x32_bf16 v[32:35], v[244:247], v[92:95], v[32:35]\nv_mfma_f32_16x16x32_bf16 v[40:43], v[252:255], v[92:95], v[40:43]\nbuffer_load_dwordx4 v[92:95], v0, %[rs], s60 offen offset:3072 sc1\ns_cbranch_vccnz .Ldd_slow%=\n.Ldd_slowr%=:\ns_waitcnt vmcnt(15)\nv_mfma_f32_16x16x32_bf16 v[20:23], a[0:3], v[96:99], 0\n" \
"ds_read_b128 v[124:127], v1 offset:1024\nv_mfma_f32_16x16x32_bf16 v[28:31], a[8:11], v[96:99], 0\nv_mfma_f32_16x16x32_bf16 v[36:39], a[16:19], v[96:99], 0\nv_mfma_f32_16x16x32_bf16 v[44:47], a[24:27], v[96:99], 0\ns_add_u32 s60, s55, 65536\ns_waitcnt vmcnt(14)\nbuffer_load_dwordx4 v[96:99], v0, %[rs], s60 offen offset:1024 sc1\nv_mfma_f32_16x16x32_bf16 v[20:23], a[4:7], v[100:103], v[20:23]\n" \
"v_mfma_f32_16x16x32_bf16 v[28:31], a[12:15], v[100:103], v[28:31]\nv_mfma_f32_16x16x32_bf16 v[36:39], a[20:23], v[100:103], v[36:39]\nv_mfma_f32_16x16x32_bf16 v[44:47], a[28:31], v[100:103], v[44:47]\ns_waitcnt vmcnt(14)\nbuffer_load_dwordx4 v[100:103], v0, %[rs], s60 offen offset:3072 sc1\nv_mfma_f32_16x16x32_bf16 v[20:23], a[32:35], v[104:107], v[20:23]\ns_waitcnt lgkmcnt(1)\nv_mfma_f32_16x16x32_bf16 v[28:31], v[120:123], v[104:107], v[28:31]\n" \
"ds_write_b128 v2, v[16:19] offset:0\nv_mfma_f32_16x16x32_bf16 v[36:39], a[40:43], v[104:107], v[36:39]\nv_mfma_f32_16x16x32_bf16 v[44:47], a[48:51], v[104:107], v[44:47]\ns_add_u32 s60, s55, 69632\ns_waitcnt vmcnt(14)\nbuffer_load_dwordx4 v[104:107], v0, %[rs], s60 offen offset:1024 sc1\nv_mfma_f32_16x16x32_bf16 v[20:23], a[36:39], v[108:111], v[20:23]\ns_waitcnt lgkmcnt(1)\n" \
"ds_read_b128 v[120:123], v1 offset:2048\nv_mfma_f32_16x16x32_bf16 v[28:31], v[124:127], v[108:111], v[28:31]\nv_mfma_f32_16x16x32_bf16 v[36:39], a[44:47], v[108:111], v[36:39]\nv_mfma_f32_16x16x32_bf16 v[44:47], a[52:55], v[108:111], v[44:47]\ns_waitcnt vmcnt(14)\nbuffer_load_dwordx4 v[108:111], v0, %[rs], s60 offen offset:3072 sc1\nv_mfma_f32_16x16x32_bf16 v[20:23], a[56:59], v[48:51], v[20:23]\nds_read_b128 v[124:127], v1 offset:3072\n" \
"v_mfma_f32_16x16x32_bf16 v[28:31], a[64:67], v[48:51], v[28:31]\nds_write_b128 v2, v[24:27] offset:2048\nv_mfma_f32_16x16x32_bf16 v[36:39], a[72:75], v[48:51], v[36:39]\nv_mfma_f32_16x16x32_bf16 v[44:47], a[80:83], v[48:51], v[44:47]\ns_add_u32 s60, s55, 81920\ns_waitcnt vmcnt(14)\nbuffer_load_dwordx4 v[48:51], v0, %[rs], s60 offen offset:1024 sc1\nv_mfma_f32_16x16x32_bf16 v[20:23], a[60:63], v[52:55], v[20:23]\n" \
"v_mfma_f32_16x16x32_bf16 v[28:31], a[68:71], v[52:55], v[28:31]\nv_mfma_f32_16x16x32_bf16 v[36:39], a[76:79], v[52:55], v[36:39]\nv_mfma_f32_16x16x32_bf16 v[44:47], a[84:87], v[52:55], v[44:47]\ns_waitcnt vmcnt(14)\nbuffer_load_dwordx4 v[52:55], v0, %[rs], s60 offen offset:3072 sc1\nv_mfma_f32_16x16x32_bf16 v[20:23], a[88:91], v[56:59], v[20:23]\nv_mfma_f32_16x16x32_bf16 v[28:31], a[96:99], v[56:59], v[28:31]\nds_write_b128 v2, v[32:35] offset:4096\n" \
"v_mfma_f32_16x16x32_bf16 v[36:39], a[104:107], v[56:59], v[36:39]\ns_waitcnt lgkmcnt(3)\nv_mfma_f32_16x16x32_bf16 v[44:47], v[120:123], v[56:59], v[44:47]\ns_add_u32 s60, s55, 86016\ns_waitcnt vmcnt(14)\nbuffer_load_dwordx4 v[56:59], v0, %[rs], s60 offen offset:1024 sc1\nv_mfma_f32_16x16x32_bf16 v[20:23], a[92:95], v[60:63], v[20:23]\nds_read_b128 v[120:123], v1 offset:4096\n" \
"v_mfma_f32_16x16x32_bf16 v[28:31], a[100:103], v[60:63], v[28:31]\nv_mfma_f32_16x16x32_bf16 v[36:39], a[108:111], v[60:63], v[36:39]\ns_waitcnt lgkmcnt(3)\nv_mfma_f32_16x16x32_bf16 v[44:47], v[124:127], v[60:63], v[44:47]\ns_waitcnt vmcnt(14)\nbuffer_load_dwordx4 v[60:63], v0, %[rs], s60 offen offset:3072 sc1\nv_mfma_f32_16x16x32_bf16 v[20:23], a[112:115], v[64:67], v[20:23]\nds_read_b128 v[124:127], v1 offset:5120\n" \
"v_mfma_f32_16x16x32_bf16 v[28:31], a[120:123], v[64:67], v[28:31]\nds_write_b128 v2, v[40:43] offset:6144\nv_mfma_f32_16x16x32_bf16 v[36:39], a[128:131], v[64:67], v[36:39]\nv_mfma_f32_16x16x32_bf16 v[44:47], a[136:139], v[64:67], v[44:47]\ns_add_u32 s60, s55, 98304\ns_waitcnt vmcnt(14)\nbuffer_load_dwordx4 v[64:67], v0, %[rs], s60 offen offset:1024 sc1\nv_mfma_f32_16x16x32_bf16 v[20:23], a[116:119], v[68:71], v[20:23]\n" \
"ds_read_b128 v[112:115], v1 offset:6144\nv_mfma_f32_16x16x32_bf16 v[28:31], a[124:127], v[68:71], v[28:31]\nv_mfma_f32_16x16x32_bf16 v[36:39], a[132:135], v[68:71], v[36:39]\nv_mfma_f32_16x16x32_bf16 v[44:47], a[140:143], v[68:71], v[44:47]\ns_waitcnt vmcnt(14)\nbuffer_load_dwordx4 v[68:71], v0, %[rs], s60 offen offset:3072 sc1\nv_mfma_f32_16x16x32_bf16 v[20:23], a[144:147], v[72:75], v[20:23]\ns_waitcnt lgkmcnt(3)\n" \
"ds_read_b128 v[116:119], v1 offset:7168\nv_mfma_f32_16x16x32_bf16 v[28:31], v[120:123], v[72:75], v[28:31]\nv_mfma_f32_16x16x32_bf16 v[36:39], a[152:155], v[72:75], v[36:39]\nv_mfma_f32_16x16x32_bf16 v[44:47], a[160:163], v[72:75], v[44:47]\ns_add_u32 s60, s55, 102400\ns_waitcnt vmcnt(14)\nbuffer_load_dwordx4 v[72:75], v0, %[rs], s60 offen offset:1024 sc1\nv_mfma_f32_16x16x32_bf16 v[20:23], a[148:151], v[76:79], v[20:23]\n" \
"s_waitcnt lgkmcnt(3)\nv_mfma_f32_16x16x32_bf16 v[28:31], v[124:127], v[76:79], v[28:31]\nv_mfma_f32_16x16x32_bf16 v[36:39], a[156:159], v[76:79], v[36:39]\nv_mfma_f32_16x16x32_bf16 v[44:47], a[164:167], v[76:79], v[44:47]\ns_waitcnt vmcnt(14)\nbuffer_load_dwordx4 v[76:79], v0, %[rs], s60 offen offset:3072 sc1\nv_mfma_f32_16x16x32_bf16 v[20:23], a[168:171], v[80:83], v[20:23]\nv_mfma_f32_16x16x32_bf16 v[28:31], a[176:179], v[80:83], v[28:31]\n" \
"s_waitcnt lgkmcnt(1)\nv_mfma_f32_16x16x32_bf16 v[36:39], v[112:115], v[80:83], v[36:39]\nv_mfma_f32_16x16x32_bf16 v[44:47], a[184:187], v[80:83], v[44:47]\ns_waitcnt vmcnt(14)\nbuffer_load_dwordx4 v[12:15], v4, %[rs], s41 offen sc1\nv_mfma_f32_16x16x32_bf16 v[20:23], a[172:175], v[84:87], v[20:23]\nds_read_b128 v[112:115], v1 offset:8192\nv_mfma_f32_16x16x32_bf16 v[28:31], a[180:183], v[84:87], v[28:31]\n" \
"s_waitcnt lgkmcnt(1)\nv_mfma_f32_16x16x32_bf16 v[36:39], v[116:119], v[84:87], v[36:39]\nv_mfma_f32_16x16x32_bf16 v[44:47], a[188:191], v[84:87], v[44:47]\ns_waitcnt vmcnt(14)\nv_mfma_f32_16x16x32_bf16 v[20:23], a[192:195], v[88:91], v[20:23]\nds_read_b128 v[116:119], v1 offset:9216\nv_mfma_f32_16x16x32_bf16 v[28:31], a[200:203], v[88:91], v[28:31]\nv_mfma_f32_16x16x32_bf16 v[36:39], a[208:211], v[88:91], v[36:39]\n" \
"v_mfma_f32_16x16x32_bf16 v[44:47], a[216:219], v[88:91], v[44:47]\ns_waitcnt vmcnt(13)\nv_mfma_f32_16x16x32_bf16 v[20:23], a[196:199], v[92:95], v[20:23]\nv_mfma_f32_16x16x32_bf16 v[28:31], a[204:207], v[92:95], v[28:31]\nv_mfma_f32_16x16x32_bf16 v[36:39], a[212:215], v[92:95], v[36:39]\nv_mfma_f32_16x16x32_bf16 v[44:47], a[220:223], v[92:95], v[44:47]\ns_waitcnt vmcnt(12)\ns_waitcnt lgkmcnt(1)\n" \
"v_mfma_f32_16x16x32_bf16 v[20:23], v[112:115], v[96:99], v[20:23]\nv_mfma_f32_16x16x32_bf16 v[28:31], a[224:227], v[96:99], v[28:31]\nv_mfma_f32_16x16x32_bf16 v[36:39], a[232:235], v[96:99], v[36:39]\nv_mfma_f32_16x16x32_bf16 v[44:47], a[240:243], v[96:99], v[44:47]\ns_waitcnt vmcnt(11)\ns_waitcnt lgkmcnt(0)\nv_mfma_f32_16x16x32_bf16 v[20:23], v[116:119], v[100:103], v[20:23]\nds_read_b128 v[112:115], v1 offset:10240\n" \
"v_mfma_f32_16x16x32_bf16 v[28:31], a[228:231], v[100:103], v[28:31]\nv_mfma_f32_16x16x32_bf16 v[36:39], a[236:239], v[100:103], v[36:39]\nv_mfma_f32_16x16x32_bf16 v[44:47], a[244:247], v[100:103], v[44:47]\ns_waitcnt vmcnt(10)\nv_mfma_f32_16x16x32_bf16 v[20:23], a[248:251], v[104:107], v[20:23]\nds_read_b128 v[116:119], v1 offset:11264\nv_mfma_f32_16x16x32_bf16 v[28:31], v[128:131], v[104:107], v[28:31]\nv_mfma_f32_16x16x32_bf16 v[36:39], v[136:139], v[104:107], v[36:39]\n" \
"v_mfma_f32_16x16x32_bf16 v[44:47], v[144:147], v[104:107], v[44:47]\ns_waitcnt vmcnt(9)\nv_mfma_f32_16x16x32_bf16 v[20:23], a[252:255], v[108:111], v[20:23]\nv_mfma_f32_16x16x32_bf16 v[28:31], v[132:135], v[108:111], v[28:31]\nv_mfma_f32_16x16x32_bf16 v[36:39], v[140:143], v[108:111], v[36:39]\nv_mfma_f32_16x16x32_bf16 v[44:47], v[148:151], v[108:111], v[44:47]\ns_waitcnt vmcnt(8)\nv_mfma_f32_16x16x32_bf16 v[20:23], v[152:155], v[48:51], v[20:23]\n" \
"v_mfma_f32_16x16x32_bf16 v[28:31], v[160:163], v[48:51], v[28:31]\ns_waitcnt lgkmcnt(1)\nv_mfma_f32_16x16x32_bf16 v[36:39], v[112:115], v[48:51], v[36:39]\nv_mfma_f32_16x16x32_bf16 v[44:47], v[168:171], v[48:51], v[44:47]\ns_waitcnt vmcnt(7)\nv_mfma_f32_16x16x32_bf16 v[20:23], v[156:159], v[52:55], v[20:23]\nds_read_b128 v[112:115], v1 offset:12288\nv_mfma_f32_16x16x32_bf16 v[28:31], v[164:167], v[52:55], v[28:31]\n" \
"s_waitcnt lgkmcnt(1)\nv_mfma_f32_16x16x32_bf16 v[36:39], v[116:119], v[52:55], v[36:39]\nv_mfma_f32_16x16x32_bf16 v[44:47], v[172:175], v[52:55], v[44:47]\ns_waitcnt vmcnt(6)\nv_mfma_f32_16x16x32_bf16 v[20:23], v[176:179], v[56:59], v[20:23]\nds_read_b128 v[116:119], v1 offset:13312\nv_mfma_f32_16x16x32_bf16 v[28:31], v[184:187], v[56:59], v[28:31]\nv_mfma_f32_16x16x32_bf16 v[36:39], v[192:195], v[56:59], v[36:39]\n" \
"v_mfma_f32_16x16x32_bf16 v[44:47], v[200:203], v[56:59], v[44:47]\ns_waitcnt vmcnt(5)\nv_mfma_f32_16x16x32_bf16 v[20:23], v[180:183], v[60:63], v[20:23]\nds_read_b128 v[120:123], v1 offset:14336\nv_mfma_f32_16x16x32_bf16 v[28:31], v[188:191], v[60:63], v[28:31]\nv_mfma_f32_16x16x32_bf16 v[36:39], v[196:199], v[60:63], v[36:39]\nv_mfma_f32_16x16x32_bf16 v[44:47], v[204:207], v[60:63], v[44:47]\ns_waitcnt vmcnt(4)\n" \
"s_waitcnt lgkmcnt(2)\nv_mfma_f32_16x16x32_bf16 v[20:23], v[112:115], v[64:67], v[20:23]\nds_read_b128 v[124:127], v1 offset:15360\nv_mfma_f32_16x16x32_bf16 v[28:31], v[208:211], v[64:67], v[28:31]\nv_mfma_f32_16x16x32_bf16 v[36:39], v[216:219], v[64:67], v[36:39]\nv_mfma_f32_16x16x32_bf16 v[44:47], v[224:227], v[64:67], v[44:47]\ns_waitcnt vmcnt(3)\ns_waitcnt lgkmcnt(2)\n" \
"v_mfma_f32_16x16x32_bf16 v[20:23], v[116:119], v[68:71], v[20:23]\nv_mfma_f32_16x16x32_bf16 v[28:31], v[212:215], v[68:71], v[28:31]\nv_mfma_f32_16x16x32_bf16 v[36:39], v[220:223], v[68:71], v[36:39]\nv_mfma_f32_16x16x32_bf16 v[44:47], v[228:231], v[68:71], v[44:47]\ns_waitcnt vmcnt(2)\nv_mfma_f32_16x16x32_bf16 v[20:23], v[232:235], v[72:75], v[20:23]\ns_waitcnt lgkmcnt(1)\nv_mfma_f32_16x16x32_bf16 v[28:31], v[120:123], v[72:75], v[28:31]\n" \
"v_mfma_f32_16x16x32_bf16 v[36:39], v[240:243], v[72:75], v[36:39]\nv_mfma_f32_16x16x32_bf16 v[44:47], v[248:251], v[72:75], v[44:47]\ns_waitcnt vmcnt(1)\nv_mfma_f32_16x16x32_bf16 v[20:23], v[236:239], v[76:79], v[20:23]\ns_waitcnt vmcnt(0)\ns_waitcnt lgkmcnt(0)\nv_lshlrev_b32 v80, 16, v12\nv_and_b32 v81, 0xffff0000, v12\n" \
"v_mfma_f32_16x16x32_bf16 v[28:31], v[124:127], v[76:79], v[28:31]\nv_lshlrev_b32 v82, 16, v13\nv_and_b32 v83, 0xffff0000, v13\nv_mfma_f32_16x16x32_bf16 v[36:39], v[244:247], v[76:79], v[36:39]\nv_lshlrev_b32 v84, 16, v14\nv_and_b32 v85, 0xffff0000, v14\nv_mfma_f32_16x16x32_bf16 v[44:47], v[252:255], v[76:79], v[44:47]\ns_mul_i32 s44, s40, %[l]\n" \
"ds_write_b128 v2, v[20:23] offset:1024\nds_write_b128 v2, v[28:31] offset:3072\ns_nop 0\nds_write_b128 v2, v[36:39] offset:5120\ns_nop 2\nds_write_b128 v2, v[44:47] offset:7168\nv_lshlrev_b32 v86, 16, v15\nv_and_b32 v87, 0xffff0000, v15\n" \
"v_mov_b32 v9, s40\ns_cmp_eq_u32 s44, 0\ns_cselect_b32 s56, %[fd], -2147483648\ns_add_u32 s40, s40, 1\ns_waitcnt lgkmcnt(0)\ns_barrier\nds_read_b128 v[16:19], v3 offset:0\nds_read_b128 v[20:23], v3 offset:256\n" \
"ds_read_b128 v[24:27], v3 offset:8192\nds_read_b128 v[28:31], v3 offset:8448\nds_read_b128 v[32:35], v3 offset:16384\nds_read_b128 v[36:39], v3 offset:16640\nds_read_b128 v[40:43], v3 offset:24576\nds_read_b128 v[44:47], v3 offset:24832\ns_waitcnt lgkmcnt(6)\nv_pk_add_f32 v[16:17], v[16:17], v[80:81]\n" \
"v_pk_add_f32 v[18:19], v[18:19], v[82:83]\nv_pk_add_f32 v[20:21], v[20:21], v[84:85]\nv_pk_add_f32 v[22:23], v[22:23], v[86:87]\ns_waitcnt lgkmcnt(4)\nv_pk_add_f32 v[16:17], v[16:17], v[24:25]\nv_pk_add_f32 v[18:19], v[18:19], v[26:27]\nv_pk_add_f32 v[20:21], v[20:21], v[28:29]\nv_pk_add_f32 v[22:23], v[22:23], v[30:31]\n" \
"s_waitcnt lgkmcnt(2)\nv_pk_add_f32 v[16:17], v[16:17], v[32:33]\nv_pk_add_f32 v[18:19], v[18:19], v[34:35]\nv_pk_add_f32 v[20:21], v[20:21], v[36:37]\nv_pk_add_f32 v[22:23], v[22:23], v[38:39]\ns_waitcnt lgkmcnt(0)\nv_pk_add_f32 v[16:17], v[16:17], v[40:41]\nv_pk_add_f32 v[18:19], v[18:19], v[42:43]\n" \
"v_pk_add_f32 v[20:21], v[20:21], v[44:45]\nv_pk_add_f32 v[22:23], v[22:23], v[46:47]\nv_cvt_pk_bf16_f32 v12, v16, v17\nv_cvt_pk_bf16_f32 v13, v18, v19\nv_cvt_pk_bf16_f32 v14, v20, v21\nv_cvt_pk_bf16_f32 v15, v22, v23\nbuffer_store_dwordx4 v[12:15], v5, %[ro], s42 offen sc1\nv_pk_add_f32 v[24:25], v[16:17], v[18:19]\n" \
"v_pk_add_f32 v[26:27], v[20:21], v[22:23]\nv_pk_add_f32 v[24:25], v[24:25], v[26:27]\nv_add_f32 v24, v24, v25\ns_nop 1\nv_add_f32_dpp v25, v24, v24 row_ror:8 row_mask:0xf bank_mask:0xf\nv_add_f32_dpp v26, v24, v24 row_ror:8 row_mask:0xf bank_mask:0xf\ns_nop 1\nv_permlane16_swap_b32 v25, v26\n" \
"v_add_f32 v24, v25, v26\nv_add_f32 v25, v25, v26\ns_nop 1\nv_permlane32_swap_b32 v24, v25\nv_add_f32 v24, v24, v25\nv_mul_f32 v24, 0x3c800000, v24\nv_pk_add_f32 v[16:17], v[16:17], v[24:25] op_sel_hi:[1,0] neg_lo:[0,1] neg_hi:[0,1]\nv_pk_add_f32 v[18:19], v[18:19], v[24:25] op_sel_hi:[1,0] neg_lo:[0,1] neg_hi:[0,1]\n" \
"v_pk_add_f32 v[20:21], v[20:21], v[24:25] op_sel_hi:[1,0] neg_lo:[0,1] neg_hi:[0,1]\nv_pk_add_f32 v[22:23], v[22:23], v[24:25] op_sel_hi:[1,0] neg_lo:[0,1] neg_hi:[0,1]\nv_pk_mul_f32 v[28:29], v[16:17], v[16:17]\nv_pk_mul_f32 v[30:31], v[18:19], v[18:19]\nv_pk_fma_f32 v[28:29], v[20:21], v[20:21], v[28:29]\nv_pk_fma_f32 v[30:31], v[22:23], v[22:23], v[30:31]\nv_pk_add_f32 v[28:29], v[28:29], v[30:31]\nv_add_f32 v28, v28, v29\n" \
"s_nop 1\nv_add_f32_dpp v29, v28, v28 row_ror:8 row_mask:0xf bank_mask:0xf\nv_add_f32_dpp v30, v28, v28 row_ror:8 row_mask:0xf bank_mask:0xf\ns_nop 1\nv_permlane16_swap_b32 v29, v30\nv_add_f32 v28, v29, v30\nv_add_f32 v29, v29, v30\ns_nop 1\n" \
"v_permlane32_swap_b32 v28, v29\nv_add_f32 v28, v28, v29\nv_mov_b32 v25, v28\nbuffer_store_dwordx2 v[24:25], v6, %[rs], s41 offen sc1\nv_xor_b32 v2, 0x8000, v2\nv_xor_b32 v3, 0x8000, v3\ns_cmp_lt_u32 s40, 64\ns_cbranch_scc1 .Ldd_next%=\n" \
"s_waitcnt vmcnt(0)\nbuffer_store_dword v9, off, %[rs], s56 sc1\ns_branch .Ldd_end%=\n.Ldd_slow%=:\n.Ldd_w1%=:\nv_cmp_gt_i32 vcc, s58, v8\ns_cbranch_vccz .Ldd_w1go%=\ns_sleep 16\n" \
"buffer_load_dword v8, v7, %[rs], 0 offen sc1\ns_waitcnt vmcnt(0)\ns_add_u32 s43, s43, 1\ns_cmp_lt_u32 s43, 262144\ns_cbranch_scc1 .Ldd_w1%=\ns_branch .Ldd_abort%=\n.Ldd_w1go%=:\ns_add_u32 s60, s55, 0\n" \
"buffer_load_dwordx4 v[96:99], v0, %[rs], s60 offen offset:1024 sc1\nbuffer_load_dwordx4 v[100:103], v0, %[rs], s60 offen offset:3072 sc1\ns_add_u32 s60, s55, 4096\nbuffer_load_dwordx4 v[104:107], v0, %[rs], s60 offen offset:1024 sc1\nbuffer_load_dwordx4 v[108:111], v0, %[rs], s60 offen offset:3072 sc1\ns_add_u32 s60, s55, 16384\nbuffer_load_dwordx4 v[48:51], v0, %[rs], s60 offen offset:1024 sc1\nbuffer_load_dwordx4 v[52:55], v0, %[rs], s60 offen offset:3072 sc1\n" \
"s_add_u32 s60, s55, 20480\nbuffer_load_dwordx4 v[56:59], v0, %[rs], s60 offen offset:1024 sc1\nbuffer_load_dwordx4 v[60:63], v0, %[rs], s60 offen offset:3072 sc1\ns_add_u32 s60, s55, 32768\nbuffer_load_dwordx4 v[64:67], v0, %[rs], s60 offen offset:1024 sc1\nbuffer_load_dwordx4 v[68:71], v0, %[rs], s60 offen offset:3072 sc1\ns_add_u32 s60, s55, 36864\nbuffer_load_dwordx4 v[72:75], v0, %[rs], s60 offen offset:1024 sc1\n" \
"buffer_load_dwordx4 v[76:79], v0, %[rs], s60 offen offset:3072 sc1\ns_add_u32 s60, s55, 49152\nbuffer_load_dwordx4 v[80:83], v0, %[rs], s60 offen offset:1024 sc1\nbuffer_load_dwordx4 v[84:87], v0, %[rs], s60 offen offset:3072 sc1\ns_add_u32 s60, s55, 53248\nbuffer_load_dwordx4 v[88:91], v0, %[rs], s60 offen offset:1024 sc1\nbuffer_load_dwordx4 v[92:95], v0, %[rs], s60 offen offset:3072 sc1\ns_branch .Ldd_slowr%=\n" \
".Ldd_abort%=:\ns_mov_b64 exec, -1\nv_mov_b32 v16, 0\ns_movk_i32 s44, 6400\nbuffer_store_dword v16, off, %[rs], s44 sc1\n.Ldd_end%=:\ns_waitcnt vmcnt(0) lgkmcnt(0)\n" \
""

#define V10(p, x) p #x "0", p #x "1", p #x "2", p #x "3", p #x "4", p #x "5", p #x "6", p #x "7", p #x "8", p #x "9"
#define V256(p) V10(p, ), V10(p, 1), V10(p, 2), V10(p, 3), V10(p, 4), V10(p, 5), V10(p, 6), V10(p, 7), V10(p, 8), V10(p, 9), V10(p, 10), V10(p, 11), V10(p, 12), V10(p, 13), V10(p, 14), V10(p, 15), V10(p, 16), \
    V10(p, 17), V10(p, 18), V10(p, 19), V10(p, 20), V10(p, 21), V10(p, 22), V10(p, 23), V10(p, 24), p "250", p "251", p "252", p "253", p "254", p "255"
#define CLOB "memory", "vcc", "scc", V256("v"), V256("a"), V10("s", 4), V10("s", 5), V10("s", 6), V10("s", 7), V10("s", 8)
template <int N> DEVI void table(lds_char* p, const unsigned (&t)[N]) {
#pragma unroll
    for (int i = 0; i < N; ++i) *(l1*)(p + i * 256) = t[i];
    WL0;
}
DEVI unsigned prof_at(int cu, int w) { return O_PROF + (262144 + cu * 2560 + w * 8) * 4; }
DEVI unsigned sp_idx(int lane) { return (lane & 2) ? 0xEEEEu : 0x4444u; }
DEVI int sp_row(int r, int hf) { return 4 * (r >> 1) + 2 * hf + (r & 1); }

DEVI void qk_role(const Args& a, const int w, lds_char* smem, int l, int h) {
    const int lane = lane_id(), g = lane >> 4, n = lane & 15, b = n & 3, p = n >> 2;
    const unsigned l0 = (unsigned)(size_t)smem, tb = l0 + w * 12288, lw = l0 + 55296 + w * 24576, ls = l * SD, IMG = l0 + 32768, MIM = l0 + 49152;
    const unsigned xw = (w >> 1) * 16384 + (w & 1) * 4096, oc = (h >> 3) * 16384 + ls, tr = (8 * g + p) * 128 + b * 16;
    const unsigned cm[4] = {4 * g > n ? 0xF149F2CAu : 0u, 4 * g + 1 > n ? 0xF149F2CAu : 0u, 4 * g + 2 > n ? 0xF149F2CAu : 0u, 4 * g + 3 > n ? 0xF149F2CAu : 0u};
    const unsigned t[19] = {
        l0 + lane * 16, lw + lane * 16, l0 + (g >> 1) * 1024 + (g & 1) * 512 + n * 16, O_E1 + ls + xw + lane * 16, sp_idx(lane),
        IMG + (w >> 1) * 8192 + ((w & 1) * 8 + 2 * g) * 512 + n * 16,
        MIM + (w * 8 + 2 * p) * 144 + g * 32 + b * 4,
        IMG + g * 512 + (w ? n * 16 : 8192 + (16 * (b >> 1) + 8 * (p >> 1) + 2 * (p & 1) + (b & 1)) * 16),
        MIM + g * 144 + (n & 7) * 16, MIM + 2304 + g * 144 + (2 * p + (b & 1)) * 16,
        w ? O_PT + (h >> 3) * 32768 + ls + lane * 16 + (w == 1 ? 16384 + ((h & 7) >> 1) * 1024 + (h & 1) * 8 : (h & 7) * 1024)
          : O_PM + oc + ((h >> 1) & 3) * 1024 + ((2 * (h & 1) + (g >> 1)) * 16 + sp_row(n & 7, g & 1)) * 16 + (n >> 3) * 8,
        w ? O_PT + (h >> 3) * 32768 + ls + 20480 + ((h >> 2) & 1) * 2048 + (w - 1) * 1024 + (h & 3) * 256 + n * 16 + g * 4
          : n >> 3 ? OOB : O_PM + oc + 4096 + ((h & 7) >> 1) * 256 + sp_row(n & 7, g & 1) * 16 + (h & 1) * 8 + (g >> 1) * 4,
        w == 3 ? (l ? F_X + SD + p * 128 + b * 4 : F_X0 + b * 4) : cm[0], cm[1], cm[2], cm[3],
        (lane >> 3) * 8192u + (lane & 7) * 16, tb + tr, lw + tr};
    table(smem + w * 12288 + lane * 4, t);
    asm volatile(QK_ASM : : [ws] "s"(a.ws), [sz] "n"(WS_RS), [rw] "s"(mkrs(w < 2 ? a.Wq : a.Wk, 8u << 20)), [wo] "s"(h * 256 + (w & 1) * 128), [tb] "s"(tb), [lw] "s"(lw), [l0] "s"(l0), [w] "s"(w),
        [xn] "s"(O_XN + ls), [xw] "s"(xw), [fs] "s"(l ? 0 : 128), [pr] "s"(prof_at(l * 80 + h, w)) : CLOB, "s36", "s37", "s38", "s39");
    __builtin_amdgcn_endpgm();
}
DEVI void vu_role(const Args& a, const int w, lds_char* smem, int l, int h) {
    const int lane = lane_id(), g = lane >> 4, n = lane & 15, b = n & 3, p = n >> 2;
    const unsigned l0 = (unsigned)(size_t)smem, tb = l0 + w * 12288, lw = l0 + 61440 + w * 24576, ls = l * SD;
    const unsigned xw = (w >> 1) * 16384 + (w & 1) * 4096, tv = (8 * g + p) * 128 + b * 16 + (w & 1) * 64, to = (8 * (g >> 1) + 4 * (p & 1) + 2 * (g & 1)) * 128 + (p >> 1) * 32 + b * 8;
    const unsigned t[18] = {
        l0 + lane * 16, lw + lane * 16, l0 + (g >> 1) * 1024 + (g & 1) * 512 + n * 16, O_E1 + ls + xw + lane * 16, sp_idx(lane),
        l0 + 57344 + w * 1024 + p * 256 + g * 32 + b * 4,
        O_UM + ls + (4 * w + (g & 1)) * 65536 + (h >> 3) * 16384 + (g >> 1) * 2048 + (h & 7) * 256 + n * 16,
        sd(O_Y + h * 131072 + ls) + w * 32768 + (g >> 1) * 1024 + (g & 1) * 256 + n * 16,
        l ? F_X + SD + p * 128 + b * 4 : F_X0 + b * 4, l0 + 49152 + w * 2048 + lane * 16, sp_idx(lane >> 2), l0 + g * 256 + (n & 7) * 16,
        (lane >> 3) * 8192u + (lane & 7) * 16, (lane >> 3) * 2048u + (lane & 7) * 16, tb + tv - (w & 1) * 12288, lw + tv - (w & 1) * 24576, tb + to, lw + to};
    table(smem + w * 12288 + lane * 4, t);
    asm volatile(VU_ASM : : [ws] "s"(a.ws), [sz] "n"(WS_RS), [rw] "s"(mkrs(a.Wv, 8u << 20)), [ro] "s"(mkrs(a.Wo, 8u << 20)), [wo] "s"(h * 256 + (w >> 1) * 128 + (w & 1) * 262144), [w2] "s"(h * 262144 + w * 512), [tb] "s"(tb), [lw] "s"(lw), [w] "s"(w),
        [xn] "s"(O_XN + ls), [xw] "s"(xw), [fs] "s"(l ? 0 : 128), [xt] "s"(w * 16384 - xw), [fy] "s"(F_Y0 + ls + h * 128 + w * 4), [pr] "s"(prof_at(l * 80 + 32 + h, w) - 128) : CLOB, "s36", "s37", "s38", "s39");
    __builtin_amdgcn_endpgm();
}
DEVI void rd_role(const Args& a, const int w, lds_char* smem, int l, int c) {
    const int lane = lane_id(), g = lane >> 4, n = lane & 15, r = 2 * (n >> 2) + (n & 1), hf = (n >> 1) & 1;
    const rsrc_t rs = mkrs(a.ws, WS_RS);
    const unsigned l0 = (unsigned)(size_t)smem, tb = l0 + 65536 + w * 4096, ls = l * SD, vp = O_E1 + ls + (c >> 3) * 16384 + (c & 7) * 1024 + w * 256 + n * 16;
    {
        const v4u ff = {SENT, SENT, SENT, SENT};
#pragma unroll
        for (int i = 0; i < 8; ++i) st16(ff, rs, O_UM + ls + c * 65536 + w * 16384 + i * 1024 + lane * 16);
#pragma unroll
        for (int i = 0; i < 2; ++i) st16(ff, rs, O_PT + ls + c * 16384 + w * 2048 + i * 1024 + lane * 16);
        st16(ff, rs, c < 8 ? O_PM + ls + w * 16384 + c * 1024 + lane * 16 : OOB);
        st16(ff, rs, g ? vp + g * 32768 : OOB);
        WV0; BAR;
    }
    st4(g & 1 ? *(const unsigned*)(a.mem0 + r * 1024 + c * 64 + w * 16 + 4 * g + 2 * hf) : nosent(*(const unsigned*)(a.mem0 + r * 1024 + c * 64 + w * 16 + 4 * g + 2 * hf)), rs, g & 1 ? vp + g * 4 : OOB, 0);
    WV0;
    st4(nosent(*(const unsigned*)(a.mem0 + r * 1024 + c * 64 + w * 16 + 4 * g + 2 * hf)), rs, g & 1 ? OOB : vp + g * 4, 0);
    const int gi = lane >> 3, r8 = lane & 7;
    const unsigned t[11] = {
        (unsigned)lane * 16, sp_idx(lane), l0 + w * 4096 + g * 576 + ((n >> 3) * 16 + hf * 4 + 2 * ((n >> 2) & 1) + (n & 1)) * 4,
        l0 + (r >> 1) * 576 + w * 128 + (g >> 1) * 64 + (r & 1) * 32 + hf * 16,
        (vp ^ ((g & 1) * 65536)) + (g >> 1) * 8, g & 1 ? SENT : 0u,
        l0 + 16384 + w * 8192 + lane * 16, l0 + 16384 + ((gi >> 1) * 2 + (w >> 1)) * 1024 + ((gi & 1) * 32 + (w & 1) * 8 + r8) * 16,
        (c >> 1) * 16384u + (c & 1) * 4096 + gi * 512 + (w * 8 + r8) * 16,
        gi ? OOB : O_ST - O_XR + c * 256 + ((w & 1) * 8 + r8) * 16 + (w >> 1) * 8,
        F_Y0 + ls + (w * 8 + (lane & 7)) * 128 + (c >> 2) * 4};
    table(smem + 65536 + w * 4096 + lane * 4, t);
    asm volatile(RD_ASM : : [rs] "s"(rs), [tb] "s"(tb), [su] "s"(O_UM + ls + c * 65536 + w * 16384), [sp] "s"(O_PM + ls + w * 16384), [st] "s"(O_PT + ls + w * 32768),
        [sy] "s"(sd(O_Y + w * (1u << 20) + ls) + (c >> 1) * 16384 + (c & 1) * 4096), [at] "s"(O_XR + ls), [fa] "s"(F_A + ls + c * 128 + w * 4), [pr] "s"(prof_at(l * 80 + 64 + c, w) + 128) : CLOB);
    __builtin_amdgcn_endpgm();
}
DEVI void gg_role(const Args& a, const int w, lds_char* smem, int l, int gi) {
    const int lane = lane_id(), g = lane >> 4, n = lane & 15, b = n & 3, p = n >> 2;
    const unsigned l0 = (unsigned)(size_t)smem, tb = l0 + w * 12288, lw = l0 + 65536 + w * 24576, ls = l * SD, tv = (8 * g + p) * 128 + b * 16 + (w & 1) * 64, fh = (g >> 1) * 1024 + (g & 1) * 512 + n * 16;
    const unsigned t[9] = {l0 + lane * 16, lw + lane * 16, l0 + fh, F_A + ls + (lane >> 2) * 128 + (lane & 3) * 4, w * 2048u + lane * 16, O_ST - O_XR + g * 1024 + n * 16, (lane >> 3) * 7168u + (lane & 7) * 16, tb + tv - (w & 1) * 12288, lw + tv - (w & 1) * 24576};
    table(smem + w * 12288 + lane * 4, t);
    asm volatile(GG_ASM : : [rs] "s"(mkrs(a.ws, WS_RS)), [rw] "s"(mkrs(w & 1 ? a.Wu : a.Wg, 3584 * 2048)), [wo] "s"(gi * 256 + (w >> 1) * 128), [tb] "s"(tb), [lw] "s"(lw), [w] "s"(w),
        [xn] "s"(O_XR + ls), [xw] "s"((w >> 1) * 16384 + (w & 1) * 4096), [ho] "s"(O_H + ls + gi * 16384), [fh] "s"(F_H + ls + (gi / 7) * 1024 + (gi % 7) * 128 + w * 4), [pr] "s"(prof_at(160 + l * 28 + gi, w) + 128) : CLOB);
    __builtin_amdgcn_endpgm();
}
DEVI void dd_role(const Args& a, const int w, lds_char* smem, int l, int c) {
    const int lane = lane_id(), g = lane >> 4, b = lane & 3, p = (lane >> 2) & 3, gi = lane >> 3, r8 = lane & 7;
    const unsigned l0 = (unsigned)(size_t)smem, tb = l0 + w * 12288, lw = l0 + 65536 + w * 24576, ls = l * SD, tr = (8 * g + p) * 128 + b * 8;
    const unsigned ta = (c >> 1) * 16384u + (c & 1) * 4096 + gi * 512 + (w * 8 + r8) * 16;
    const rsrc_t rs = mkrs(a.ws, WS_RS);
    const unsigned t[11] = {(unsigned)lane * 16, lw + lane * 16, l0 + w * 8192 + lane * 16, l0 + ((gi >> 1) * 2 + (w >> 1)) * 1024 + ((gi & 1) * 32 + (w & 1) * 8 + r8) * 16,
        ta, l ? (w * 8 + r8) * 2048 + c * 128 + gi * 16 : ta, gi || l ? OOB : O_ST - O_XR + 4096 + c * 256 + (w * 8 + r8) * 8,
        F_H + ls + w * 1024 + ((lane >> 2) % 7) * 128 + b * 4, gi * 2048u + r8 * 16, tb + tr, lw + tr};
    table(smem + w * 12288 + lane * 4, t);
    asm volatile(DD_ASM : : [rs] "s"(rs), [rw] "s"(mkrs(a.Wd, 3584 * 2048)), [ro] "s"(mkrs(l ? (char*)a.out : a.ws, l ? 2048 * 2048 : WS_RS)), [wo] "s"(w * (896 * 2048) + c * 128), [tb] "s"(tb), [lw] "s"(lw), [l] "s"(l),
        [xn] "s"(O_XR + ls), [ho] "s"(O_H + ls + w * 7 * 16384), [fd] "s"(F_D + ls + c * 128 + w * 4), [pr] "s"(prof_at(216 + l * 16 + c, w) + 128) : CLOB);
    __builtin_amdgcn_endpgm();
}

#define DLS 500
#define SPL 0
#define PLAN 3
#define PFA 0
#define PFB 2
#define PFL 1
#define VUX 1
#define LNS 50
#define LN1 700
#define DLG 800
#define DLD 1300
#define DL1 1700
#define DLG1 2600
#define DLD1 3000
DEVI Args args(unsigned sh) {
    auto p = (const __attribute__((address_space(4))) Args*)__builtin_amdgcn_kernarg_segment_ptr();
    asm volatile("" : "+s"(p));
    Args a;
    a.x = p->x; a.mem0 = p->mem0; a.Wq = p->Wq; a.Wk = p->Wk; a.Wv = p->Wv; a.Wo = p->Wo; a.Wg = p->Wg; a.Wu = p->Wu; a.Wd = p->Wd; a.ws = p->ws + sh; a.out = p->out;
    return a;
}
DEVI unsigned beat_wait(unsigned x0) {
    const rsrc_t r0 = mkrs(args(0).ws, 2u << 20);
    for (unsigned spins = 0; spins < SPIN_CAP; ++spins) {
        CB;
        const v2u t = __builtin_amdgcn_raw_buffer_load_b64(r0, 0, O_HB, AUX_SC1);
        if (RFL(t[0]) != x0) return RFL(t[1]) & (2u << 20);
        __builtin_amdgcn_s_sleep(8);
    }
    __builtin_amdgcn_endpgm();
}
DEVI void leader(const Args& a, const int w, lds_char* smem, unsigned x0) {
    const int lane = lane_id();
    const rsrc_t r0 = mkrs(a.ws, 4u << 20);
    unsigned t[2] = {~0u, ~0u};
    for (int i = 0; i < 2 * NPRB; ++i) {
        const unsigned t0 = (unsigned)__builtin_amdgcn_s_memtime();
        WL0;
        if (lane == 0) st4(0u, r0, 0, O_PRB + (i & 1) * SD + w * 1024 + (i >> 1) * 128);
        WV0;
        const unsigned d = (unsigned)__builtin_amdgcn_s_memtime() - t0;
        if (i & 1) t[1] = min(t[1], d); else t[0] = min(t[0], d);
    }
    *(l2*)(smem + w * 8) = v2u{t[0], t[1]};
    WL0; BAR;
    const v4u p = *(l4*)smem, q = *(l4*)(smem + 16);
    const unsigned sh = RFL(min(min(p[0], p[2]), min(q[0], q[2])) < min(min(p[1], p[3]), min(q[1], q[3])) ? 2u << 20 : 0u);
    const unsigned o = sh + w * 1024 + lane * 16;
    const v4u ff = {SENT, SENT, SENT, SENT};
#pragma unroll
    for (int i = 0; i < 8; ++i) st16(ff, r0, o + O_E1 + i * 4096);
#pragma unroll
    for (int i = 1; i < 15; ++i) if (i != 2 && i != 11 && i != 13) st16(ff, r0, o + i * 4096);
    WV0; BAR;
    if (w) return;
    for (unsigned spins = 0; spins < SPIN_CAP; ++spins) {
        if (lane == 0) st8(v2u{++x0, sh}, r0, 0, O_HB);
        if (__ballot((int)ld4(r0, sh + F_D + SD + (lane >> 2) * 128 + (lane & 3) * 4, 0) < 0) == 0ull) break;
        __builtin_amdgcn_s_sleep(16);
    }
}
__global__ __launch_bounds__(256, 1) void sys_kernel(Args) {
    extern __shared__ __attribute__((aligned(16))) char smem_[];
    lds_char* smem = (lds_char*)smem_;
    const int x = blockIdx.x & 7, j = blockIdx.x >> 3, wv = RFL(threadIdx.x >> 6), l = x >> 2, xx = x & 3;
    const unsigned t0 = (unsigned)__builtin_amdgcn_s_memrealtime();
    auto until = [&](unsigned dl) AI { while ((unsigned)__builtin_amdgcn_s_memrealtime() - t0 < dl) __builtin_amdgcn_s_sleep(8); };
    unsigned x0 = 0;
    if (l || j >= 16) x0 = RFL(ld4(mkrs(args(0).ws, 2u << 20), 0, O_HB));
    if (j < 16) {
        if (l) { until(j ? LN1 + j * LNS : 0); ln0(args(0), wv, j * 4 + xx, [&]() AI { return beat_wait(x0); }); until(DL1); }
        if (j < 8) qk_role(args(0), wv, smem, l, xx * 8 + j); else vu_role(args(0), wv, smem, l, (xx ^ VUX) * 8 + j - 8);
        return;
    }
    {
        const int lane = lane_id(), q = (j - 16) * 4 + wv, m = SPL && !l ? ((xx & 1) != (SPL & 1)) * 2 + (q >> 5) : q >> 4;
        const Args a = args(0);
        const u16 *W0 = a.Wq, *W1 = a.Wk, *W2 = a.Wv, *W3 = a.Wo;
        asm volatile("" : "+s"(W0), "+s"(W1), "+s"(W2), "+s"(W3));
        const u16* W = m == 0 ? W0 : m == 1 ? W1 : m == 2 ? W2 : W3;
        const rsrc_t rw = mkrs(W, 8u << 20);
        if (l) {
#pragma unroll
            for (int i = 0; i < PFA; ++i) __builtin_amdgcn_raw_ptr_buffer_load_lds(rw, smem + LDSB - 1024 + wv * 256, 4, lane * 256, i * (1u << 20) + (xx * 16 + (q & 15)) * 16384, 0, 0);
        } else {
            const unsigned po = (0x100u - (unsigned)(size_t)W) & 0x300u, hx = SPL ? (xx & 2) + ((q >> 4) & 1) : m < 2 ? xx : xx ^ VUX;
            const unsigned vo = m == 3 ? (lane >> 1) * 1024 + (lane & 1) * 128 : (lane >> 2) * 8192 + ((lane >> 1) & 1) * 1024 + (lane & 1) * 128;
            const unsigned so = po + (m == 3 ? hx * (2u << 20) + (q & 15) * 32768 : hx * 2048 + (q & 15) * 131072);
#pragma unroll
            for (int i = 0; i < PFB; ++i) __builtin_amdgcn_raw_ptr_buffer_load_lds(rw, smem + LDSB - 1024 + wv * 256, 4, vo, so + i * (m == 3 ? 524288 : 2u << 20), 0, 0);
            const unsigned pl = (0x1F00u - (unsigned)(size_t)W) & 0x1F00u;
#pragma unroll
            for (int i = 0; i < PFL; ++i) __builtin_amdgcn_raw_ptr_buffer_load_lds(rw, smem + LDSB - 1024 + wv * 256, 4, (lane >> 1) * 8192 + (lane & 1) * 128, (pl >> 11) == hx && m < 3 ? pl + ((q & 15) + 16 * i) * 262144 : OOB, 0, 0);
        }
    }
    int ro, ix;
    if ((xx & 1) == (PLAN & 1)) { if (j < 24) { ro = 0; ix = (xx >> 1) * 8 + j - 16; } else if (j < 26) { ro = 3; ix = (xx >> 1) * 2 + j - 24; } else { ro = 1; ix = (xx >> 1) * 14 + j - 26; } }
    else if (j < 24) { ro = 1; ix = (xx >> 1) * 14 + j - 10; } else { ro = 2; ix = (xx >> 1) * 8 + j - 24; }
    if (ro == 3 && l) { if (ix == 0) leader(args(0), wv, smem, x0); return; }
    const unsigned sh = beat_wait(x0);
    if (ro == 0) { rd_role(args(sh), wv, smem, l, ix); return; }
    if (ro == 1) { const Args a = args(sh); until(l ? DLG1 : ((((unsigned)(size_t)a.Wg >> 8) + ix) & 3) == 1 || ((((unsigned)(size_t)a.Wu >> 8) + ix) & 3) == 1 ? DLG - DLS : DLG); WV0; BAR; gg_role(a, wv, smem, l, ix); return; }
    if (ro == 2) { const Args a = args(sh); until((l ? DLD1 : DLD) - (((((unsigned)(size_t)a.Wd >> 8) + (ix >> 1)) & 3) == 1 ? DLS + 200 : 0)); WV0; BAR; dd_role(a, wv, smem, l, ix); return; }
    r_role(args(sh), wv, ix);
}

extern "C" void kernel_launch(void* const* d_in, const int* in_sizes, int n_in, void* d_out, int out_size, void* d_ws, size_t ws_size, hipStream_t stream) {
    (void)in_sizes; (void)n_in; (void)out_size; (void)ws_size;
    Args a;
    a.x = (const u16*)d_in[0]; a.mem0 = (const u16*)d_in[1]; a.Wq = (const u16*)d_in[2]; a.Wk = (const u16*)d_in[3]; a.Wv = (const u16*)d_in[4]; a.Wo = (const u16*)d_in[5];
    a.Wg = (const u16*)d_in[8]; a.Wu = (const u16*)d_in[9]; a.Wd = (const u16*)d_in[10];
    a.ws = (char*)(((uintptr_t)d_ws + (2u << 20) - 1) & ~(uintptr_t)((2u << 20) - 1)); a.out = (u16*)d_out;
    (void)hipFuncSetAttribute((const void*)sys_kernel, hipFuncAttributeMaxDynamicSharedMemorySize, LDSB);
    hipLaunchKernelGGL(sys_kernel, dim3(256), dim3(256), LDSB, stream, a);
}
```

```cpp
#include <hip/hip_runtime.h>
#include <stdint.h>

typedef unsigned short u16;
typedef unsigned long long u64;
typedef __attribute__((ext_vector_type(8))) short bf16x8;
typedef __attribute__((ext_vector_type(4))) short s16x4;
typedef __attribute__((ext_vector_type(4))) float f32x4;
typedef __attribute__((ext_vector_type(16))) float f32x16;
typedef __attribute__((ext_vector_type(2))) float f32x2;
typedef __attribute__((ext_vector_type(4))) unsigned v4u;
typedef __attribute__((ext_vector_type(2))) unsigned v2u;
typedef __amdgpu_buffer_rsrc_t rsrc_t;
typedef __attribute__((address_space(3))) char lds_char;
typedef __attribute__((address_space(3))) bf16x8 l8;
typedef __attribute__((address_space(3))) v4u l4;
typedef __attribute__((address_space(3))) v2u l2;
typedef __attribute__((address_space(3))) unsigned l1;
typedef __attribute__((address_space(3))) f32x4 lf4;
typedef __attribute__((address_space(3))) float lf1;
#define DEVI __device__ __forceinline__
#define AI __attribute__((always_inline))

constexpr int NSEG = 64;
constexpr float ATT_SCALE = 0.08838834764831845f;
constexpr float LOG2E = 1.4426950408889634f;
#define NPRB 4
constexpr unsigned SD = 8192;
DEVI unsigned sd(unsigned o) { return o ^ ((o >> 8) & SD); }
constexpr unsigned F_X0 = 32768;
constexpr unsigned F_X = 4096;
constexpr unsigned F_A = 16384;
constexpr unsigned F_H = F_A + 2048;
constexpr unsigned F_D = F_H + 4096;
constexpr unsigned F_Y0 = 49152;
constexpr unsigned F_ABORT = 6400;
constexpr unsigned O_PM = 65536;
constexpr unsigned O_E1 = 131072;
constexpr unsigned O_PT = 262144;
constexpr unsigned O_UM = 524288;
constexpr unsigned CTRL_END = O_UM + (1u << 20);
constexpr unsigned O_PRB = 0x1F0000, O_HB = 0x1FFF00;
constexpr unsigned O_XN = 4u << 20;
constexpr unsigned O_XR = O_XN + (8u << 20);
constexpr unsigned O_ST = O_XR + (8u << 20);
constexpr unsigned O_H = O_ST + (8u << 20);
constexpr unsigned O_Y = O_H + (36u << 20);
constexpr unsigned WS_NEED = O_Y + (8u << 20);
constexpr unsigned O_PROF = WS_NEED;
constexpr unsigned WS_RS = WS_NEED;
static_assert(CTRL_END <= O_PRB && CTRL_END % 16384 == 0 && O_Y % (8u << 20) == 0, "workspace");
constexpr unsigned OOB = 0x80000000u, OOB2 = 0x90000000u, SENT = 0xFFFFFFFFu;

struct Args { const u16 *x, *mem0, *Wq, *Wk, *Wv, *Wo, *Wg, *Wu, *Wd; char* ws; u16* out; };

typedef __attribute__((ext_vector_type(2))) __bf16 bf16x2_t;
DEVI unsigned pack2(float lo, float hi) { f32x2 v = {lo, hi}; bf16x2_t b = __builtin_convertvector(v, bf16x2_t); return __builtin_bit_cast(unsigned, b); }
DEVI float bflo(unsigned u) { return __uint_as_float(u << 16); }
DEVI float bfhi(unsigned u) { return __uint_as_float(u & 0xffff0000u); }
DEVI float rbf(float x) { return bflo(pack2(x, x)); }
DEVI bf16x8 as8(v4u v) { return __builtin_bit_cast(bf16x8, v); }
DEVI f32x4 mfma16(bf16x8 a, bf16x8 b, f32x4 c) { return __builtin_amdgcn_mfma_f32_16x16x32_bf16(a, b, c, 0, 0, 0); }
DEVI f32x16 mfma32(bf16x8 a, bf16x8 b, f32x16 c) { return __builtin_amdgcn_mfma_f32_32x32x16_bf16(a, b, c, 0, 0, 0); }
constexpr int AUX_SC1 = 16;
DEVI v4u ld16(rsrc_t r, unsigned vo, unsigned so) { return __builtin_amdgcn_raw_buffer_load_b128(r, vo, so, AUX_SC1); }
DEVI unsigned ld4(rsrc_t r, unsigned vo, unsigned so) { return __builtin_amdgcn_raw_buffer_load_b32(r, vo, so, AUX_SC1); }
DEVI void st16(v4u v, rsrc_t r, unsigned vo) { __builtin_amdgcn_raw_buffer_store_b128(v, r, vo, 0, AUX_SC1); }
DEVI void st8(v2u v, rsrc_t r, unsigned vo, unsigned so) { __builtin_amdgcn_raw_buffer_store_b64(v, r, vo, so, AUX_SC1); }
DEVI void st4(unsigned v, rsrc_t r, unsigned vo, unsigned so) { __builtin_amdgcn_raw_buffer_store_b32(v, r, vo, so, AUX_SC1); }
DEVI v2u trd(lds_char* p) { return __builtin_bit_cast(v2u, __builtin_amdgcn_ds_read_tr16_b64_v4i16((__attribute__((address_space(3))) s16x4*)p)); }
template <int O1, int O2> DEVI void tra(v2u& lo, v2u& hi, unsigned a) { asm volatile("ds_read_b64_tr_b16 %0, %2 offset:%3\n\tds_read_b64_tr_b16 %1, %2 offset:%4" : "=&v"(lo), "=&v"(hi) : "v"(a), "n"(O1), "n"(O2) : "memory"); }
DEVI void trw(v2u& a, v2u& b, v2u& c, v2u& d) { asm volatile("s_waitcnt lgkmcnt(0)" : "+v"(a), "+v"(b), "+v"(c), "+v"(d)::"memory"); }
DEVI rsrc_t mkrs(const void* p, unsigned n) { return __builtin_amdgcn_make_buffer_rsrc((void*)p, 0, (int)n, 0x00020000); }
#define CB asm volatile("" ::: "memory")
#define SB __builtin_amdgcn_sched_barrier(0)
#define WL0 asm volatile("s_waitcnt lgkmcnt(0)" ::: "memory")
#define WV0 asm volatile("s_waitcnt vmcnt(0)" ::: "memory")
#define WV(n) asm volatile("s_waitcnt vmcnt(%0)" ::"n"(n) : "memory")
#define BAR do { __builtin_amdgcn_s_barrier(); CB; } while (0)
#define RFL(x) __builtin_amdgcn_readfirstlane(x)
DEVI int lane_id() { int x; asm volatile("v_mbcnt_lo_u32_b32 %0, -1, 0\n\tv_mbcnt_hi_u32_b32 %0, -1, %0" : "=v"(x)); return x; }
template <int I> struct IC { static constexpr int v = I; };
template <int B, int E_, class F> DEVI void sfor(F&& f) { if constexpr (B < E_) { f(IC<B>{}); sfor<B + 1, E_>(f); } }
DEVI float red_x32(float x, bool mx) { unsigned u = __float_as_uint(x); auto r = __builtin_amdgcn_permlane32_swap(u, u, false, false); float a = __uint_as_float(r[0]), b = __uint_as_float(r[1]); return mx ? fmaxf(a, b) : a + b; }
DEVI float red_x16(float x, bool mx) { unsigned u = __float_as_uint(x); auto r = __builtin_amdgcn_permlane16_swap(u, u, false, false); float a = __uint_as_float(r[0]), b = __uint_as_float(r[1]); return mx ? fmaxf(a, b) : a + b; }
DEVI unsigned vmax4(v4u d) { return max(max(d[0], d[1]), max(d[2], d[3])); }
DEVI unsigned nosent(unsigned v) { return min(v, SENT - 1u); }
constexpr unsigned SPIN_CAP = 1u << 18;
DEVI bool spin_bail(unsigned& spins, rsrc_t rs) {
    if (++spins > SPIN_CAP) { st4(0u, rs, 0, F_ABORT); return true; }
    return (spins & 255u) == 0u && RFL(ld4(rs, 0, F_ABORT)) != SENT;
}
#define TSTAMP(idx)
constexpr int LDSB = 160 * 1024;
DEVI void idle(unsigned dl) { const unsigned t0 = (unsigned)__builtin_amdgcn_s_memrealtime(); while ((unsigned)__builtin_amdgcn_s_memrealtime() - t0 < dl) __builtin_amdgcn_s_sleep(8); }

DEVI float red_rows(float s) {
    s += __uint_as_float(__builtin_amdgcn_update_dpp(0, __float_as_uint(s), 0x128, 0xf, 0xf, false));
    s = red_x16(s, false); return red_x32(s, false);
}
DEVI f32x2 up2(unsigned u) { return f32x2{bflo(u), bfhi(u)}; }
DEVI float ln_inv(float var) { return __builtin_amdgcn_rcpf(rbf(__builtin_amdgcn_sqrtf(rbf(rbf(fmaxf(var, 0.f)) + 1e-5f)))); }
template <class F> DEVI void ln0(const Args& a, const int w, int b, F&& base) {
    const int lane = lane_id(), row = w * 8 + (lane & 7);
    const rsrc_t rx = mkrs(a.x, 2048 * 2048);
    v4u u[16];
#pragma unroll
    for (int i = 0; i < 16; ++i) u[i] = __builtin_amdgcn_raw_buffer_load_b128(rx, row * 2048 + (lane >> 3) * 16 + i * 128, b * 65536, 0);
    const float k0 = __shfl(bflo(u[0][0]), lane & 7);
    f32x2 s1 = {0.f, 0.f}, s2 = {0.f, 0.f};
#pragma unroll
    for (int i = 0; i < 16; ++i)
#pragma unroll
        for (int e = 0; e < 4; ++e) { const f32x2 d = up2(u[i][e]) - k0; s1 += d; s2 += d * d; }
    const float s = red_rows(s1[0] + s1[1]) * (1.0f / 1024.0f), q = red_rows(s2[0] + s2[1]) * (1.0f / 1024.0f);
    float inv = ln_inv(q - s * s), c = -rbf(k0 + s) * inv;
    asm volatile("" : "+v"(inv), "+v"(c));
    const rsrc_t rs = mkrs(a.ws + base(), WS_RS);
    const unsigned o = sd(O_XN + b * 131072) + (lane >> 3) * 512 + row * 16;
#pragma unroll
    for (int i = 0; i < 16; ++i) {
        v4u r;
#pragma unroll
        for (int e = 0; e < 4; ++e) { const f32x2 z = up2(u[i][e]) * inv + c; r[e] = pack2(z[0], z[1]); }
        st16(r, rs, o + (i >> 1) * 16384 + (i & 1) * 4096);
    }
#pragma unroll
    for (int i = 0; i < 16; ++i) st16(u[i], rs, o + (O_XR - O_XN) + (i >> 1) * 16384 + (i & 1) * 4096);
    st16(u[0], rs, sd(O_ST + b * 131072) + w * 1024 + lane * 16); st16(u[0], rs, sd(O_ST + b * 131072) + 4096 + w * 1024 + lane * 16); st16(u[0], rs, sd(O_ST + SD + b * 131072) + w * 1024 + lane * 16);
    WV0;
    st4(b, rs, 0, F_X0 + b * 128 + w * 4);
}
#define RSLP 8
DEVI void r_role(const Args& a, const int w, int rq) {
    const int lane = lane_id(), row = (w >> 1) * 16 + (rq & 1) * 8 + (lane & 7), cq = (rq >> 1) * 2 + (w & 1);
    const rsrc_t rs = mkrs(a.ws, WS_RS);
    const unsigned fo = cq * 32768 + (lane >> 3) * 512 + row * 16, so = O_ST - O_XR + 4096 + (lane >> 3) * 512 + row * 8;
    auto io = [](int i) -> unsigned { return (i >> 1) * 16384 + (i & 1) * 4096; };
    const unsigned fd = F_D + (lane & 15) * 128 + (row >> 3) * 4;
    for (int b = 0; b < NSEG; ++b) {
        unsigned spins = 0;
        for (;;) {
            CB;
            if (__ballot((int)ld4(rs, fd, 0) < b) == 0ull) break;
            if (spin_bail(spins, rs)) return;
            __builtin_amdgcn_s_sleep(RSLP);
        }
        const unsigned sx = sd(O_XR + b * 131072);
        const v2u s0 = __builtin_amdgcn_raw_buffer_load_b64(rs, so, sx, AUX_SC1), s1 = __builtin_amdgcn_raw_buffer_load_b64(rs, so + 256, sx, AUX_SC1);
        v4u u[4];
#pragma unroll
        for (int i = 0; i < 4; ++i) u[i] = ld16(rs, fo + io(i), sx);
        const float m0 = __uint_as_float(s0[0]), m1 = __uint_as_float(s1[0]), mean = red_rows(m0 + m1) * 0.0625f;
        const float var = red_rows(__uint_as_float(s0[1]) + __uint_as_float(s1[1]) + 64.f * ((m0 - mean) * (m0 - mean) + (m1 - mean) * (m1 - mean))) * (1.0f / 1024.0f);
        const float inv = ln_inv(var), c = -rbf(mean) * inv;
#pragma unroll
        for (int i = 0; i < 4; ++i) {
            v4u o;
#pragma unroll
            for (int e = 0; e < 4; ++e) { const f32x2 z = up2(u[i][e]) * inv + c; o[e] = pack2(z[0], z[1]); }
            st16(o, rs, fo + io(i) + ((sx ^ SD) - (O_XR - O_XN)));
        }
#pragma unroll
        for (int i = 0; i < 4; ++i) st16(u[i], rs, fo + io(i) + (sx ^ SD));
        WV0;
        st4(b, rs, 0, F_X + SD + rq * 128 + w * 4);
        if (rq == 0 && w == 0) TSTAMP(200000 + 64 + b);
    }
}
#define QK_ASM \
"v_mbcnt_lo_u32_b32 v255, -1, 0\nv_mbcnt_hi_u32_b32 v255, -1, v255\nv_lshlrev_b32 v255, 2, v255\nv_add_u32 v255, %[tb], v255\nds_read_b32 v0, v255 offset:0\nds_read_b32 v1, v255 offset:256\nds_read_b32 v2, v255 offset:512\nds_read_b32 v3, v255 offset:768\n" \
"ds_read_b32 v4, v255 offset:1024\nds_read_b32 v5, v255 offset:1280\nds_read_b32 v6, v255 offset:1536\nds_read_b32 v7, v255 offset:1792\nds_read_b32 v8, v255 offset:2048\nds_read_b32 v9, v255 offset:2304\nds_read_b32 v10, v255 offset:2560\nds_read_b32 v11, v255 offset:2816\n" \
"ds_read_b32 v12, v255 offset:3072\nds_read_b32 v13, v255 offset:3328\nds_read_b32 v14, v255 offset:3584\nds_read_b32 v15, v255 offset:3840\nds_read_b32 v92, v255 offset:4096\nds_read_b32 v93, v255 offset:4352\nds_read_b32 v94, v255 offset:4608\ns_waitcnt lgkmcnt(0)\n" \
"s_mov_b64 s[36:37], %[ws]\ns_mov_b32 s38, %[sz]\ns_mov_b32 s39, 0x20000\ns_mov_b32 s45, 2096896\nbuffer_load_dword v95, off, s[36:39], s45 sc1\ns_add_u32 m0, %[lw], 16384\ns_add_u32 s44, %[wo], 0\nbuffer_load_dwordx4 v92, %[rw], s44 offen nt lds\n" \
"s_add_u32 m0, %[lw], 17408\ns_add_u32 s44, %[wo], 65536\nbuffer_load_dwordx4 v92, %[rw], s44 offen nt lds\ns_add_u32 m0, %[lw], 18432\ns_add_u32 s44, %[wo], 131072\nbuffer_load_dwordx4 v92, %[rw], s44 offen nt lds\ns_add_u32 m0, %[lw], 19456\ns_add_u32 s44, %[wo], 196608\n" \
"buffer_load_dwordx4 v92, %[rw], s44 offen nt lds\ns_add_u32 m0, %[lw], 20480\ns_add_u32 s44, %[wo], 262144\nbuffer_load_dwordx4 v92, %[rw], s44 offen nt lds\ns_add_u32 m0, %[lw], 21504\ns_add_u32 s44, %[wo], 327680\nbuffer_load_dwordx4 v92, %[rw], s44 offen nt lds\ns_add_u32 m0, %[lw], 22528\n" \
"s_add_u32 s44, %[wo], 393216\nbuffer_load_dwordx4 v92, %[rw], s44 offen nt lds\ns_add_u32 m0, %[lw], 23552\ns_add_u32 s44, %[wo], 458752\nbuffer_load_dwordx4 v92, %[rw], s44 offen nt lds\ns_add_u32 m0, %[tb], 0\ns_add_u32 s44, %[wo], 524288\nbuffer_load_dwordx4 v92, %[rw], s44 offen nt lds\n" \
"s_add_u32 m0, %[tb], 1024\ns_add_u32 s44, %[wo], 589824\nbuffer_load_dwordx4 v92, %[rw], s44 offen nt lds\ns_add_u32 m0, %[tb], 2048\ns_add_u32 s44, %[wo], 655360\nbuffer_load_dwordx4 v92, %[rw], s44 offen nt lds\ns_add_u32 m0, %[tb], 3072\ns_add_u32 s44, %[wo], 720896\n" \
"buffer_load_dwordx4 v92, %[rw], s44 offen nt lds\ns_add_u32 m0, %[tb], 4096\ns_add_u32 s44, %[wo], 786432\nbuffer_load_dwordx4 v92, %[rw], s44 offen nt lds\ns_add_u32 m0, %[tb], 5120\ns_add_u32 s44, %[wo], 851968\nbuffer_load_dwordx4 v92, %[rw], s44 offen nt lds\ns_add_u32 m0, %[tb], 6144\n" \
"s_add_u32 s44, %[wo], 917504\nbuffer_load_dwordx4 v92, %[rw], s44 offen nt lds\ns_add_u32 m0, %[tb], 7168\ns_add_u32 s44, %[wo], 983040\nbuffer_load_dwordx4 v92, %[rw], s44 offen nt lds\ns_add_u32 m0, %[tb], 8192\ns_add_u32 s44, %[wo], 1048576\nbuffer_load_dwordx4 v92, %[rw], s44 offen nt lds\n" \
"s_add_u32 m0, %[tb], 9216\ns_add_u32 s44, %[wo], 1114112\nbuffer_load_dwordx4 v92, %[rw], s44 offen nt lds\ns_add_u32 m0, %[tb], 10240\ns_add_u32 s44, %[wo], 1179648\nbuffer_load_dwordx4 v92, %[rw], s44 offen nt lds\ns_add_u32 m0, %[tb], 11264\ns_add_u32 s44, %[wo], 1245184\n" \
"buffer_load_dwordx4 v92, %[rw], s44 offen nt lds\ns_add_u32 m0, %[lw], 0\ns_add_u32 s44, %[wo], 1310720\nbuffer_load_dwordx4 v92, %[rw], s44 offen nt lds\ns_add_u32 m0, %[lw], 1024\ns_add_u32 s44, %[wo], 1376256\nbuffer_load_dwordx4 v92, %[rw], s44 offen nt lds\ns_add_u32 m0, %[lw], 2048\n" \
"s_add_u32 s44, %[wo], 1441792\nbuffer_load_dwordx4 v92, %[rw], s44 offen nt lds\ns_add_u32 m0, %[lw], 3072\ns_add_u32 s44, %[wo], 1507328\nbuffer_load_dwordx4 v92, %[rw], s44 offen nt lds\ns_add_u32 m0, %[lw], 4096\ns_add_u32 s44, %[wo], 1572864\nbuffer_load_dwordx4 v92, %[rw], s44 offen nt lds\n" \
"s_add_u32 m0, %[lw], 5120\ns_add_u32 s44, %[wo], 1638400\nbuffer_load_dwordx4 v92, %[rw], s44 offen nt lds\ns_add_u32 m0, %[lw], 6144\ns_add_u32 s44, %[wo], 1703936\nbuffer_load_dwordx4 v92, %[rw], s44 offen nt lds\ns_add_u32 m0, %[lw], 7168\ns_add_u32 s44, %[wo], 1769472\n" \
"buffer_load_dwordx4 v92, %[rw], s44 offen nt lds\ns_add_u32 m0, %[lw], 8192\ns_add_u32 s44, %[wo], 1835008\nbuffer_load_dwordx4 v92, %[rw], s44 offen nt lds\ns_add_u32 m0, %[lw], 9216\ns_add_u32 s44, %[wo], 1900544\nbuffer_load_dwordx4 v92, %[rw], s44 offen nt lds\ns_add_u32 m0, %[lw], 10240\n" \
"s_add_u32 s44, %[wo], 1966080\nbuffer_load_dwordx4 v92, %[rw], s44 offen nt lds\ns_add_u32 m0, %[lw], 11264\ns_add_u32 s44, %[wo], 2031616\nbuffer_load_dwordx4 v92, %[rw], s44 offen nt lds\ns_add_u32 m0, %[lw], 12288\ns_add_u32 s44, %[wo], 2097152\nbuffer_load_dwordx4 v92, %[rw], s44 offen nt lds\n" \
"s_add_u32 m0, %[lw], 13312\ns_add_u32 s44, %[wo], 2162688\nbuffer_load_dwordx4 v92, %[rw], s44 offen nt lds\ns_add_u32 m0, %[lw], 14336\ns_add_u32 s44, %[wo], 2228224\nbuffer_load_dwordx4 v92, %[rw], s44 offen nt lds\ns_add_u32 m0, %[lw], 15360\ns_add_u32 s44, %[wo], 2293760\n" \
"buffer_load_dwordx4 v92, %[rw], s44 offen nt lds\ns_waitcnt vmcnt(32)\nds_read_b64_tr_b16 a[0:1], v94 offset:16384\nds_read_b64_tr_b16 a[2:3], v94 offset:16896\nds_read_b64_tr_b16 a[8:9], v94 offset:16392\nds_read_b64_tr_b16 a[10:11], v94 offset:16904\nds_read_b64_tr_b16 a[16:17], v94 offset:16448\nds_read_b64_tr_b16 a[18:19], v94 offset:16960\n" \
"ds_read_b64_tr_b16 a[24:25], v94 offset:16456\nds_read_b64_tr_b16 a[26:27], v94 offset:16968\ns_waitcnt lgkmcnt(0)\ns_add_u32 m0, %[lw], 16384\ns_add_u32 s44, %[wo], 2359296\nbuffer_load_dwordx4 v92, %[rw], s44 offen nt lds\ns_add_u32 m0, %[lw], 17408\ns_add_u32 s44, %[wo], 2424832\n" \
"buffer_load_dwordx4 v92, %[rw], s44 offen nt lds\ns_add_u32 m0, %[lw], 18432\ns_add_u32 s44, %[wo], 2490368\nbuffer_load_dwordx4 v92, %[rw], s44 offen nt lds\ns_add_u32 m0, %[lw], 19456\ns_add_u32 s44, %[wo], 2555904\nbuffer_load_dwordx4 v92, %[rw], s44 offen nt lds\ns_waitcnt vmcnt(32)\n" \
"ds_read_b64_tr_b16 a[4:5], v94 offset:20480\nds_read_b64_tr_b16 a[6:7], v94 offset:20992\nds_read_b64_tr_b16 a[12:13], v94 offset:20488\nds_read_b64_tr_b16 a[14:15], v94 offset:21000\nds_read_b64_tr_b16 a[20:21], v94 offset:20544\nds_read_b64_tr_b16 a[22:23], v94 offset:21056\nds_read_b64_tr_b16 a[28:29], v94 offset:20552\nds_read_b64_tr_b16 a[30:31], v94 offset:21064\n" \
"s_waitcnt lgkmcnt(0)\ns_add_u32 m0, %[lw], 20480\ns_add_u32 s44, %[wo], 2621440\nbuffer_load_dwordx4 v92, %[rw], s44 offen nt lds\ns_add_u32 m0, %[lw], 21504\ns_add_u32 s44, %[wo], 2686976\nbuffer_load_dwordx4 v92, %[rw], s44 offen nt lds\ns_add_u32 m0, %[lw], 22528\n" \
"s_add_u32 s44, %[wo], 2752512\nbuffer_load_dwordx4 v92, %[rw], s44 offen nt lds\ns_add_u32 m0, %[lw], 23552\ns_add_u32 s44, %[wo], 2818048\nbuffer_load_dwordx4 v92, %[rw], s44 offen nt lds\ns_waitcnt vmcnt(32)\nds_read_b64_tr_b16 a[32:33], v93 offset:0\nds_read_b64_tr_b16 a[34:35], v93 offset:512\n" \
"ds_read_b64_tr_b16 v[88:89], v93 offset:8\nds_read_b64_tr_b16 v[90:91], v93 offset:520\nds_read_b64_tr_b16 a[40:41], v93 offset:64\nds_read_b64_tr_b16 a[42:43], v93 offset:576\nds_read_b64_tr_b16 a[48:49], v93 offset:72\nds_read_b64_tr_b16 a[50:51], v93 offset:584\ns_waitcnt lgkmcnt(0)\ns_add_u32 m0, %[tb], 0\n" \
"s_add_u32 s44, %[wo], 2883584\nbuffer_load_dwordx4 v92, %[rw], s44 offen nt lds\ns_add_u32 m0, %[tb], 1024\ns_add_u32 s44, %[wo], 2949120\nbuffer_load_dwordx4 v92, %[rw], s44 offen nt lds\ns_add_u32 m0, %[tb], 2048\ns_add_u32 s44, %[wo], 3014656\nbuffer_load_dwordx4 v92, %[rw], s44 offen nt lds\n" \
"s_add_u32 m0, %[tb], 3072\ns_add_u32 s44, %[wo], 3080192\nbuffer_load_dwordx4 v92, %[rw], s44 offen nt lds\ns_waitcnt vmcnt(32)\nds_read_b64_tr_b16 a[36:37], v93 offset:4096\nds_read_b64_tr_b16 a[38:39], v93 offset:4608\nds_read_b64_tr_b16 v[84:85], v93 offset:4104\nds_read_b64_tr_b16 v[86:87], v93 offset:4616\n" \
"ds_read_b64_tr_b16 a[44:45], v93 offset:4160\nds_read_b64_tr_b16 a[46:47], v93 offset:4672\nds_read_b64_tr_b16 a[52:53], v93 offset:4168\nds_read_b64_tr_b16 a[54:55], v93 offset:4680\ns_waitcnt lgkmcnt(0)\ns_add_u32 m0, %[tb], 4096\ns_add_u32 s44, %[wo], 3145728\nbuffer_load_dwordx4 v92, %[rw], s44 offen nt lds\n" \
"s_add_u32 m0, %[tb], 5120\ns_add_u32 s44, %[wo], 3211264\nbuffer_load_dwordx4 v92, %[rw], s44 offen nt lds\ns_add_u32 m0, %[tb], 6144\ns_add_u32 s44, %[wo], 3276800\nbuffer_load_dwordx4 v92, %[rw], s44 offen nt lds\ns_add_u32 m0, %[tb], 7168\ns_add_u32 s44, %[wo], 3342336\n" \
"buffer_load_dwordx4 v92, %[rw], s44 offen nt lds\ns_waitcnt vmcnt(32)\nds_read_b64_tr_b16 a[56:57], v93 offset:8192\nds_read_b64_tr_b16 a[58:59], v93 offset:8704\nds_read_b64_tr_b16 a[64:65], v93 offset:8200\nds_read_b64_tr_b16 a[66:67], v93 offset:8712\nds_read_b64_tr_b16 v[80:81], v93 offset:8256\nds_read_b64_tr_b16 v[82:83], v93 offset:8768\n" \
"ds_read_b64_tr_b16 a[72:73], v93 offset:8264\nds_read_b64_tr_b16 a[74:75], v93 offset:8776\ns_waitcnt lgkmcnt(0)\ns_add_u32 m0, %[tb], 8192\ns_add_u32 s44, %[wo], 3407872\nbuffer_load_dwordx4 v92, %[rw], s44 offen nt lds\ns_add_u32 m0, %[tb], 9216\ns_add_u32 s44, %[wo], 3473408\n" \
"buffer_load_dwordx4 v92, %[rw], s44 offen nt lds\ns_add_u32 m0, %[tb], 10240\ns_add_u32 s44, %[wo], 3538944\nbuffer_load_dwordx4 v92, %[rw], s44 offen nt lds\ns_add_u32 m0, %[tb], 11264\ns_add_u32 s44, %[wo], 3604480\nbuffer_load_dwordx4 v92, %[rw], s44 offen nt lds\ns_waitcnt vmcnt(32)\n" \
"ds_read_b64_tr_b16 a[60:61], v94 offset:0\nds_read_b64_tr_b16 a[62:63], v94 offset:512\nds_read_b64_tr_b16 a[68:69], v94 offset:8\nds_read_b64_tr_b16 a[70:71], v94 offset:520\nds_read_b64_tr_b16 v[76:77], v94 offset:64\nds_read_b64_tr_b16 v[78:79], v94 offset:576\nds_read_b64_tr_b16 a[76:77], v94 offset:72\nds_read_b64_tr_b16 a[78:79], v94 offset:584\n" \
"s_waitcnt lgkmcnt(0)\ns_add_u32 m0, %[lw], 0\ns_add_u32 s44, %[wo], 3670016\nbuffer_load_dwordx4 v92, %[rw], s44 offen nt lds\ns_add_u32 m0, %[lw], 1024\ns_add_u32 s44, %[wo], 3735552\nbuffer_load_dwordx4 v92, %[rw], s44 offen nt lds\ns_add_u32 m0, %[lw], 2048\n" \
"s_add_u32 s44, %[wo], 3801088\nbuffer_load_dwordx4 v92, %[rw], s44 offen nt lds\ns_add_u32 m0, %[lw], 3072\ns_add_u32 s44, %[wo], 3866624\nbuffer_load_dwordx4 v92, %[rw], s44 offen nt lds\ns_waitcnt vmcnt(32)\nds_read_b64_tr_b16 a[80:81], v94 offset:4096\nds_read_b64_tr_b16 a[82:83], v94 offset:4608\n" \
"ds_read_b64_tr_b16 a[88:89], v94 offset:4104\nds_read_b64_tr_b16 a[90:91], v94 offset:4616\nds_read_b64_tr_b16 a[96:97], v94 offset:4160\nds_read_b64_tr_b16 a[98:99], v94 offset:4672\nds_read_b64_tr_b16 v[72:73], v94 offset:4168\nds_read_b64_tr_b16 v[74:75], v94 offset:4680\ns_waitcnt lgkmcnt(0)\ns_add_u32 m0, %[lw], 4096\n" \
"s_add_u32 s44, %[wo], 3932160\nbuffer_load_dwordx4 v92, %[rw], s44 offen nt lds\ns_add_u32 m0, %[lw], 5120\ns_add_u32 s44, %[wo], 3997696\nbuffer_load_dwordx4 v92, %[rw], s44 offen nt lds\ns_add_u32 m0, %[lw], 6144\ns_add_u32 s44, %[wo], 4063232\nbuffer_load_dwordx4 v92, %[rw], s44 offen nt lds\n" \
"s_add_u32 m0, %[lw], 7168\ns_add_u32 s44, %[wo], 4128768\nbuffer_load_dwordx4 v92, %[rw], s44 offen nt lds\ns_waitcnt vmcnt(32)\nds_read_b64_tr_b16 a[84:85], v94 offset:8192\nds_read_b64_tr_b16 a[86:87], v94 offset:8704\nds_read_b64_tr_b16 a[92:93], v94 offset:8200\nds_read_b64_tr_b16 a[94:95], v94 offset:8712\n" \
"ds_read_b64_tr_b16 a[100:101], v94 offset:8256\nds_read_b64_tr_b16 a[102:103], v94 offset:8768\nds_read_b64_tr_b16 v[68:69], v94 offset:8264\nds_read_b64_tr_b16 v[70:71], v94 offset:8776\ns_waitcnt lgkmcnt(0)\ns_add_u32 m0, %[lw], 8192\ns_add_u32 s44, %[wo], 4194304\nbuffer_load_dwordx4 v92, %[rw], s44 offen nt lds\n" \
"s_add_u32 m0, %[lw], 9216\ns_add_u32 s44, %[wo], 4259840\nbuffer_load_dwordx4 v92, %[rw], s44 offen nt lds\ns_add_u32 m0, %[lw], 10240\ns_add_u32 s44, %[wo], 4325376\nbuffer_load_dwordx4 v92, %[rw], s44 offen nt lds\ns_add_u32 m0, %[lw], 11264\ns_add_u32 s44, %[wo], 4390912\n" \
"buffer_load_dwordx4 v92, %[rw], s44 offen nt lds\ns_waitcnt vmcnt(32)\nds_read_b64_tr_b16 a[104:105], v94 offset:12288\nds_read_b64_tr_b16 a[106:107], v94 offset:12800\nds_read_b64_tr_b16 a[112:113], v94 offset:12296\nds_read_b64_tr_b16 a[114:115], v94 offset:12808\nds_read_b64_tr_b16 a[120:121], v94 offset:12352\nds_read_b64_tr_b16 a[122:123], v94 offset:12864\n" \
"ds_read_b64_tr_b16 a[128:129], v94 offset:12360\nds_read_b64_tr_b16 a[130:131], v94 offset:12872\ns_waitcnt lgkmcnt(0)\ns_add_u32 m0, %[lw], 12288\ns_add_u32 s44, %[wo], 4456448\nbuffer_load_dwordx4 v92, %[rw], s44 offen nt lds\ns_add_u32 m0, %[lw], 13312\ns_add_u32 s44, %[wo], 4521984\n" \
"buffer_load_dwordx4 v92, %[rw], s44 offen nt lds\ns_add_u32 m0, %[lw], 14336\ns_add_u32 s44, %[wo], 4587520\nbuffer_load_dwordx4 v92, %[rw], s44 offen nt lds\ns_add_u32 m0, %[lw], 15360\ns_add_u32 s44, %[wo], 4653056\nbuffer_load_dwordx4 v92, %[rw], s44 offen nt lds\ns_waitcnt vmcnt(32)\n" \
"ds_read_b64_tr_b16 a[108:109], v94 offset:16384\nds_read_b64_tr_b16 a[110:111], v94 offset:16896\nds_read_b64_tr_b16 a[116:117], v94 offset:16392\nds_read_b64_tr_b16 a[118:119], v94 offset:16904\nds_read_b64_tr_b16 a[124:125], v94 offset:16448\nds_read_b64_tr_b16 a[126:127], v94 offset:16960\nds_read_b64_tr_b16 a[132:133], v94 offset:16456\nds_read_b64_tr_b16 a[134:135], v94 offset:16968\n" \
"s_waitcnt lgkmcnt(0)\ns_add_u32 m0, %[lw], 16384\ns_add_u32 s44, %[wo], 4718592\nbuffer_load_dwordx4 v92, %[rw], s44 offen nt lds\ns_add_u32 m0, %[lw], 17408\ns_add_u32 s44, %[wo], 4784128\nbuffer_load_dwordx4 v92, %[rw], s44 offen nt lds\ns_add_u32 m0, %[lw], 18432\n" \
"s_add_u32 s44, %[wo], 4849664\nbuffer_load_dwordx4 v92, %[rw], s44 offen nt lds\ns_add_u32 m0, %[lw], 19456\ns_add_u32 s44, %[wo], 4915200\nbuffer_load_dwordx4 v92, %[rw], s44 offen nt lds\ns_waitcnt vmcnt(32)\nds_read_b64_tr_b16 a[136:137], v94 offset:20480\nds_read_b64_tr_b16 a[138:139], v94 offset:20992\n" \
"ds_read_b64_tr_b16 v[64:65], v94 offset:20488\nds_read_b64_tr_b16 v[66:67], v94 offset:21000\nds_read_b64_tr_b16 a[144:145], v94 offset:20544\nds_read_b64_tr_b16 a[146:147], v94 offset:21056\nds_read_b64_tr_b16 a[152:153], v94 offset:20552\nds_read_b64_tr_b16 a[154:155], v94 offset:21064\ns_waitcnt lgkmcnt(0)\ns_add_u32 m0, %[lw], 20480\n" \
"s_add_u32 s44, %[wo], 4980736\nbuffer_load_dwordx4 v92, %[rw], s44 offen nt lds\ns_add_u32 m0, %[lw], 21504\ns_add_u32 s44, %[wo], 5046272\nbuffer_load_dwordx4 v92, %[rw], s44 offen nt lds\ns_add_u32 m0, %[lw], 22528\ns_add_u32 s44, %[wo], 5111808\nbuffer_load_dwordx4 v92, %[rw], s44 offen nt lds\n" \
"s_add_u32 m0, %[lw], 23552\ns_add_u32 s44, %[wo], 5177344\nbuffer_load_dwordx4 v92, %[rw], s44 offen nt lds\ns_waitcnt vmcnt(32)\nds_read_b64_tr_b16 a[140:141], v93 offset:0\nds_read_b64_tr_b16 a[142:143], v93 offset:512\nds_read_b64_tr_b16 v[60:61], v93 offset:8\nds_read_b64_tr_b16 v[62:63], v93 offset:520\n" \
"ds_read_b64_tr_b16 a[148:149], v93 offset:64\nds_read_b64_tr_b16 a[150:151], v93 offset:576\nds_read_b64_tr_b16 a[156:157], v93 offset:72\nds_read_b64_tr_b16 a[158:159], v93 offset:584\ns_waitcnt lgkmcnt(0)\ns_add_u32 m0, %[tb], 0\ns_add_u32 s44, %[wo], 5242880\nbuffer_load_dwordx4 v92, %[rw], s44 offen nt lds\n" \
"s_add_u32 m0, %[tb], 1024\ns_add_u32 s44, %[wo], 5308416\nbuffer_load_dwordx4 v92, %[rw], s44 offen nt lds\ns_add_u32 m0, %[tb], 2048\ns_add_u32 s44, %[wo], 5373952\nbuffer_load_dwordx4 v92, %[rw], s44 offen nt lds\ns_add_u32 m0, %[tb], 3072\ns_add_u32 s44, %[wo], 5439488\n" \
"buffer_load_dwordx4 v92, %[rw], s44 offen nt lds\ns_waitcnt vmcnt(32)\nds_read_b64_tr_b16 a[160:161], v93 offset:4096\nds_read_b64_tr_b16 a[162:163], v93 offset:4608\nds_read_b64_tr_b16 a[168:169], v93 offset:4104\nds_read_b64_tr_b16 a[170:171], v93 offset:4616\nds_read_b64_tr_b16 v[56:57], v93 offset:4160\nds_read_b64_tr_b16 v[58:59], v93 offset:4672\n" \
"ds_read_b64_tr_b16 a[176:177], v93 offset:4168\nds_read_b64_tr_b16 a[178:179], v93 offset:4680\ns_waitcnt lgkmcnt(0)\ns_add_u32 m0, %[tb], 4096\ns_add_u32 s44, %[wo], 5505024\nbuffer_load_dwordx4 v92, %[rw], s44 offen nt lds\ns_add_u32 m0, %[tb], 5120\ns_add_u32 s44, %[wo], 5570560\n" \
"buffer_load_dwordx4 v92, %[rw], s44 offen nt lds\ns_add_u32 m0, %[tb], 6144\ns_add_u32 s44, %[wo], 5636096\nbuffer_load_dwordx4 v92, %[rw], s44 offen nt lds\ns_add_u32 m0, %[tb], 7168\ns_add_u32 s44, %[wo], 5701632\nbuffer_load_dwordx4 v92, %[rw], s44 offen nt lds\ns_waitcnt vmcnt(32)\n" \
"ds_read_b64_tr_b16 a[164:165], v93 offset:8192\nds_read_b64_tr_b16 a[166:167], v93 offset:8704\nds_read_b64_tr_b16 a[172:173], v93 offset:8200\nds_read_b64_tr_b16 a[174:175], v93 offset:8712\nds_read_b64_tr_b16 v[52:53], v93 offset:8256\nds_read_b64_tr_b16 v[54:55], v93 offset:8768\nds_read_b64_tr_b16 a[180:181], v93 offset:8264\nds_read_b64_tr_b16 a[182:183], v93 offset:8776\n" \
"s_waitcnt lgkmcnt(0)\ns_add_u32 m0, %[tb], 8192\ns_add_u32 s44, %[wo], 5767168\nbuffer_load_dwordx4 v92, %[rw], s44 offen nt lds\ns_add_u32 m0, %[tb], 9216\ns_add_u32 s44, %[wo], 5832704\nbuffer_load_dwordx4 v92, %[rw], s44 offen nt lds\ns_add_u32 m0, %[tb], 10240\n" \
"s_add_u32 s44, %[wo], 5898240\nbuffer_load_dwordx4 v92, %[rw], s44 offen nt lds\ns_add_u32 m0, %[tb], 11264\ns_add_u32 s44, %[wo], 5963776\nbuffer_load_dwordx4 v92, %[rw], s44 offen nt lds\ns_waitcnt vmcnt(32)\nds_read_b64_tr_b16 a[184:185], v94 offset:0\nds_read_b64_tr_b16 a[186:187], v94 offset:512\n" \
"ds_read_b64_tr_b16 a[192:193], v94 offset:8\nds_read_b64_tr_b16 a[194:195], v94 offset:520\nds_read_b64_tr_b16 a[200:201], v94 offset:64\nds_read_b64_tr_b16 a[202:203], v94 offset:576\nds_read_b64_tr_b16 v[48:49], v94 offset:72\nds_read_b64_tr_b16 v[50:51], v94 offset:584\ns_waitcnt lgkmcnt(0)\ns_add_u32 m0, %[lw], 0\n" \
"s_add_u32 s44, %[wo], 6029312\nbuffer_load_dwordx4 v92, %[rw], s44 offen nt lds\ns_add_u32 m0, %[lw], 1024\ns_add_u32 s44, %[wo], 6094848\nbuffer_load_dwordx4 v92, %[rw], s44 offen nt lds\ns_add_u32 m0, %[lw], 2048\ns_add_u32 s44, %[wo], 6160384\nbuffer_load_dwordx4 v92, %[rw], s44 offen nt lds\n" \
"s_add_u32 m0, %[lw], 3072\ns_add_u32 s44, %[wo], 6225920\nbuffer_load_dwordx4 v92, %[rw], s44 offen nt lds\ns_waitcnt vmcnt(32)\nds_read_b64_tr_b16 a[188:189], v94 offset:4096\nds_read_b64_tr_b16 a[190:191], v94 offset:4608\nds_read_b64_tr_b16 a[196:197], v94 offset:4104\nds_read_b64_tr_b16 a[198:199], v94 offset:4616\n" \
"ds_read_b64_tr_b16 a[204:205], v94 offset:4160\nds_read_b64_tr_b16 a[206:207], v94 offset:4672\nds_read_b64_tr_b16 v[44:45], v94 offset:4168\nds_read_b64_tr_b16 v[46:47], v94 offset:4680\ns_waitcnt lgkmcnt(0)\ns_add_u32 m0, %[lw], 4096\ns_add_u32 s44, %[wo], 6291456\nbuffer_load_dwordx4 v92, %[rw], s44 offen nt lds\n" \
"s_add_u32 m0, %[lw], 5120\ns_add_u32 s44, %[wo], 6356992\nbuffer_load_dwordx4 v92, %[rw], s44 offen nt lds\ns_add_u32 m0, %[lw], 6144\ns_add_u32 s44, %[wo], 6422528\nbuffer_load_dwordx4 v92, %[rw], s44 offen nt lds\ns_add_u32 m0, %[lw], 7168\ns_add_u32 s44, %[wo], 6488064\n" \
"buffer_load_dwordx4 v92, %[rw], s44 offen nt lds\ns_waitcnt vmcnt(32)\nds_read_b64_tr_b16 a[208:209], v94 offset:8192\nds_read_b64_tr_b16 a[210:211], v94 offset:8704\nds_read_b64_tr_b16 a[216:217], v94 offset:8200\nds_read_b64_tr_b16 a[218:219], v94 offset:8712\nds_read_b64_tr_b16 a[224:225], v94 offset:8256\nds_read_b64_tr_b16 a[226:227], v94 offset:8768\n" \
"ds_read_b64_tr_b16 a[232:233], v94 offset:8264\nds_read_b64_tr_b16 a[234:235], v94 offset:8776\ns_waitcnt lgkmcnt(0)\ns_add_u32 m0, %[lw], 8192\ns_add_u32 s44, %[wo], 6553600\nbuffer_load_dwordx4 v92, %[rw], s44 offen nt lds\ns_add_u32 m0, %[lw], 9216\ns_add_u32 s44, %[wo], 6619136\n" \
"buffer_load_dwordx4 v92, %[rw], s44 offen nt lds\ns_add_u32 m0, %[lw], 10240\ns_add_u32 s44, %[wo], 6684672\nbuffer_load_dwordx4 v92, %[rw], s44 offen nt lds\ns_add_u32 m0, %[lw], 11264\ns_add_u32 s44, %[wo], 6750208\nbuffer_load_dwordx4 v92, %[rw], s44 offen nt lds\ns_waitcnt vmcnt(32)\n" \
"ds_read_b64_tr_b16 a[212:213], v94 offset:12288\nds_read_b64_tr_b16 a[214:215], v94 offset:12800\nds_read_b64_tr_b16 a[220:221], v94 offset:12296\nds_read_b64_tr_b16 a[222:223], v94 offset:12808\nds_read_b64_tr_b16 a[228:229], v94 offset:12352\nds_read_b64_tr_b16 a[230:231], v94 offset:12864\nds_read_b64_tr_b16 a[236:237], v94 offset:12360\nds_read_b64_tr_b16 a[238:239], v94 offset:12872\n" \
"s_waitcnt lgkmcnt(0)\ns_add_u32 m0, %[lw], 12288\ns_add_u32 s44, %[wo], 6815744\nbuffer_load_dwordx4 v92, %[rw], s44 offen nt lds\ns_add_u32 m0, %[lw], 13312\ns_add_u32 s44, %[wo], 6881280\nbuffer_load_dwordx4 v92, %[rw], s44 offen nt lds\ns_add_u32 m0, %[lw], 14336\n" \
"s_add_u32 s44, %[wo], 6946816\nbuffer_load_dwordx4 v92, %[rw], s44 offen nt lds\ns_add_u32 m0, %[lw], 15360\ns_add_u32 s44, %[wo], 7012352\nbuffer_load_dwordx4 v92, %[rw], s44 offen nt lds\ns_waitcnt vmcnt(32)\nds_read_b64_tr_b16 a[240:241], v94 offset:16384\nds_read_b64_tr_b16 a[242:243], v94 offset:16896\n" \
"ds_read_b64_tr_b16 v[40:41], v94 offset:16392\nds_read_b64_tr_b16 v[42:43], v94 offset:16904\nds_read_b64_tr_b16 a[248:249], v94 offset:16448\nds_read_b64_tr_b16 a[250:251], v94 offset:16960\nds_read_b64_tr_b16 v[96:97], v94 offset:16456\nds_read_b64_tr_b16 v[98:99], v94 offset:16968\ns_waitcnt lgkmcnt(0)\ns_add_u32 m0, %[lw], 16384\n" \
"s_add_u32 s44, %[wo], 7077888\nbuffer_load_dwordx4 v92, %[rw], s44 offen nt lds\ns_add_u32 m0, %[lw], 17408\ns_add_u32 s44, %[wo], 7143424\nbuffer_load_dwordx4 v92, %[rw], s44 offen nt lds\ns_add_u32 m0, %[lw], 18432\ns_add_u32 s44, %[wo], 7208960\nbuffer_load_dwordx4 v92, %[rw], s44 offen nt lds\n" \
"s_add_u32 m0, %[lw], 19456\ns_add_u32 s44, %[wo], 7274496\nbuffer_load_dwordx4 v92, %[rw], s44 offen nt lds\ns_waitcnt vmcnt(32)\nds_read_b64_tr_b16 a[244:245], v94 offset:20480\nds_read_b64_tr_b16 a[246:247], v94 offset:20992\nds_read_b64_tr_b16 v[36:37], v94 offset:20488\nds_read_b64_tr_b16 v[38:39], v94 offset:21000\n" \
"ds_read_b64_tr_b16 a[252:253], v94 offset:20544\nds_read_b64_tr_b16 a[254:255], v94 offset:21056\nds_read_b64_tr_b16 v[100:101], v94 offset:20552\nds_read_b64_tr_b16 v[102:103], v94 offset:21064\ns_waitcnt lgkmcnt(0)\ns_add_u32 m0, %[lw], 20480\ns_add_u32 s44, %[wo], 7340032\nbuffer_load_dwordx4 v92, %[rw], s44 offen nt lds\n" \
"s_add_u32 m0, %[lw], 21504\ns_add_u32 s44, %[wo], 7405568\nbuffer_load_dwordx4 v92, %[rw], s44 offen nt lds\ns_add_u32 m0, %[lw], 22528\ns_add_u32 s44, %[wo], 7471104\nbuffer_load_dwordx4 v92, %[rw], s44 offen nt lds\ns_add_u32 m0, %[lw], 23552\ns_add_u32 s44, %[wo], 7536640\n" \
"buffer_load_dwordx4 v92, %[rw], s44 offen nt lds\ns_waitcnt vmcnt(32)\nds_read_b64_tr_b16 v[104:105], v93 offset:0\nds_read_b64_tr_b16 v[106:107], v93 offset:512\nds_read_b64_tr_b16 v[112:113], v93 offset:8\nds_read_b64_tr_b16 v[114:115], v93 offset:520\nds_read_b64_tr_b16 v[32:33], v93 offset:64\nds_read_b64_tr_b16 v[34:35], v93 offset:576\n" \
"ds_read_b64_tr_b16 v[120:121], v93 offset:72\nds_read_b64_tr_b16 v[122:123], v93 offset:584\ns_waitcnt lgkmcnt(0)\ns_add_u32 m0, %[tb], 0\ns_add_u32 s44, %[wo], 7602176\nbuffer_load_dwordx4 v92, %[rw], s44 offen nt lds\ns_add_u32 m0, %[tb], 1024\ns_add_u32 s44, %[wo], 7667712\n" \
"buffer_load_dwordx4 v92, %[rw], s44 offen nt lds\ns_add_u32 m0, %[tb], 2048\ns_add_u32 s44, %[wo], 7733248\nbuffer_load_dwordx4 v92, %[rw], s44 offen nt lds\ns_add_u32 m0, %[tb], 3072\ns_add_u32 s44, %[wo], 7798784\nbuffer_load_dwordx4 v92, %[rw], s44 offen nt lds\ns_waitcnt vmcnt(32)\n" \
"ds_read_b64_tr_b16 v[108:109], v93 offset:4096\nds_read_b64_tr_b16 v[110:111], v93 offset:4608\nds_read_b64_tr_b16 v[116:117], v93 offset:4104\nds_read_b64_tr_b16 v[118:119], v93 offset:4616\nds_read_b64_tr_b16 v[28:29], v93 offset:4160\nds_read_b64_tr_b16 v[30:31], v93 offset:4672\nds_read_b64_tr_b16 v[124:125], v93 offset:4168\nds_read_b64_tr_b16 v[126:127], v93 offset:4680\n" \
"s_waitcnt lgkmcnt(0)\ns_add_u32 m0, %[tb], 4096\ns_add_u32 s44, %[wo], 7864320\nbuffer_load_dwordx4 v92, %[rw], s44 offen nt lds\ns_add_u32 m0, %[tb], 5120\ns_add_u32 s44, %[wo], 7929856\nbuffer_load_dwordx4 v92, %[rw], s44 offen nt lds\ns_add_u32 m0, %[tb], 6144\n" \
"s_add_u32 s44, %[wo], 7995392\nbuffer_load_dwordx4 v92, %[rw], s44 offen nt lds\ns_add_u32 m0, %[tb], 7168\ns_add_u32 s44, %[wo], 8060928\nbuffer_load_dwordx4 v92, %[rw], s44 offen nt lds\ns_waitcnt vmcnt(32)\nds_read_b64_tr_b16 v[128:129], v93 offset:8192\nds_read_b64_tr_b16 v[130:131], v93 offset:8704\n" \
"ds_read_b64_tr_b16 v[136:137], v93 offset:8200\nds_read_b64_tr_b16 v[138:139], v93 offset:8712\nds_read_b64_tr_b16 v[144:145], v93 offset:8256\nds_read_b64_tr_b16 v[146:147], v93 offset:8768\nds_read_b64_tr_b16 v[24:25], v93 offset:8264\nds_read_b64_tr_b16 v[26:27], v93 offset:8776\ns_waitcnt lgkmcnt(0)\ns_add_u32 m0, %[tb], 8192\n" \
"s_add_u32 s44, %[wo], 8126464\nbuffer_load_dwordx4 v92, %[rw], s44 offen nt lds\ns_add_u32 m0, %[tb], 9216\ns_add_u32 s44, %[wo], 8192000\nbuffer_load_dwordx4 v92, %[rw], s44 offen nt lds\ns_add_u32 m0, %[tb], 10240\ns_add_u32 s44, %[wo], 8257536\nbuffer_load_dwordx4 v92, %[rw], s44 offen nt lds\n" \
"s_add_u32 m0, %[tb], 11264\ns_add_u32 s44, %[wo], 8323072\nbuffer_load_dwordx4 v92, %[rw], s44 offen nt lds\ns_waitcnt vmcnt(32)\nds_read_b64_tr_b16 v[132:133], v94 offset:0\nds_read_b64_tr_b16 v[134:135], v94 offset:512\nds_read_b64_tr_b16 v[140:141], v94 offset:8\nds_read_b64_tr_b16 v[142:143], v94 offset:520\n" \
"ds_read_b64_tr_b16 v[148:149], v94 offset:64\nds_read_b64_tr_b16 v[150:151], v94 offset:576\nds_read_b64_tr_b16 v[20:21], v94 offset:72\nds_read_b64_tr_b16 v[22:23], v94 offset:584\ns_waitcnt lgkmcnt(0)\nds_write_b128 v1, v[88:91] offset:0\nds_write_b128 v1, v[84:87] offset:1024\nds_write_b128 v1, v[80:83] offset:2048\n" \
"ds_write_b128 v1, v[76:79] offset:3072\ns_waitcnt vmcnt(28)\nds_read_b64_tr_b16 v[152:153], v94 offset:4096\nds_read_b64_tr_b16 v[154:155], v94 offset:4608\nds_read_b64_tr_b16 v[160:161], v94 offset:4104\nds_read_b64_tr_b16 v[162:163], v94 offset:4616\nds_read_b64_tr_b16 v[168:169], v94 offset:4160\nds_read_b64_tr_b16 v[170:171], v94 offset:4672\n" \
"ds_read_b64_tr_b16 v[176:177], v94 offset:4168\nds_read_b64_tr_b16 v[178:179], v94 offset:4680\ns_waitcnt lgkmcnt(0)\nds_write_b128 v1, v[72:75] offset:4096\nds_write_b128 v1, v[68:71] offset:5120\nds_write_b128 v1, v[64:67] offset:6144\nds_write_b128 v1, v[60:63] offset:7168\ns_waitcnt vmcnt(24)\n" \
"ds_read_b64_tr_b16 v[156:157], v94 offset:8192\nds_read_b64_tr_b16 v[158:159], v94 offset:8704\nds_read_b64_tr_b16 v[164:165], v94 offset:8200\nds_read_b64_tr_b16 v[166:167], v94 offset:8712\nds_read_b64_tr_b16 v[172:173], v94 offset:8256\nds_read_b64_tr_b16 v[174:175], v94 offset:8768\nds_read_b64_tr_b16 v[180:181], v94 offset:8264\nds_read_b64_tr_b16 v[182:183], v94 offset:8776\n" \
"s_waitcnt lgkmcnt(0)\nds_write_b128 v1, v[56:59] offset:8192\nds_write_b128 v1, v[52:55] offset:9216\nds_write_b128 v1, v[48:51] offset:10240\nds_write_b128 v1, v[44:47] offset:11264\ns_waitcnt vmcnt(20)\nds_read_b64_tr_b16 v[184:185], v94 offset:12288\nds_read_b64_tr_b16 v[186:187], v94 offset:12800\n" \
"ds_read_b64_tr_b16 v[44:45], v94 offset:12296\nds_read_b64_tr_b16 v[46:47], v94 offset:12808\nds_read_b64_tr_b16 v[192:193], v94 offset:12352\nds_read_b64_tr_b16 v[194:195], v94 offset:12864\nds_read_b64_tr_b16 v[200:201], v94 offset:12360\nds_read_b64_tr_b16 v[202:203], v94 offset:12872\ns_waitcnt lgkmcnt(0)\nds_write_b128 v1, v[40:43] offset:12288\n" \
"ds_write_b128 v1, v[36:39] offset:13312\nds_write_b128 v1, v[32:35] offset:14336\nds_write_b128 v1, v[28:31] offset:15360\ns_waitcnt vmcnt(16)\nds_read_b64_tr_b16 v[188:189], v94 offset:16384\nds_read_b64_tr_b16 v[190:191], v94 offset:16896\nds_read_b64_tr_b16 v[28:29], v94 offset:16392\nds_read_b64_tr_b16 v[30:31], v94 offset:16904\n" \
"ds_read_b64_tr_b16 v[196:197], v94 offset:16448\nds_read_b64_tr_b16 v[198:199], v94 offset:16960\nds_read_b64_tr_b16 v[204:205], v94 offset:16456\nds_read_b64_tr_b16 v[206:207], v94 offset:16968\ns_waitcnt lgkmcnt(0)\nds_write_b128 v1, v[24:27] offset:16384\nds_write_b128 v1, v[20:23] offset:17408\nds_write_b128 v1, v[44:47] offset:18432\n" \
"ds_write_b128 v1, v[28:31] offset:19456\ns_waitcnt vmcnt(12)\nds_read_b64_tr_b16 v[208:209], v94 offset:20480\nds_read_b64_tr_b16 v[210:211], v94 offset:20992\nds_read_b64_tr_b16 v[216:217], v94 offset:20488\nds_read_b64_tr_b16 v[218:219], v94 offset:21000\nds_read_b64_tr_b16 v[28:29], v94 offset:20544\nds_read_b64_tr_b16 v[30:31], v94 offset:21056\n" \
"ds_read_b64_tr_b16 v[224:225], v94 offset:20552\nds_read_b64_tr_b16 v[226:227], v94 offset:21064\ns_waitcnt lgkmcnt(0)\nds_write_b128 v1, v[28:31] offset:20480\ns_waitcnt vmcnt(8)\nds_read_b64_tr_b16 v[212:213], v93 offset:0\nds_read_b64_tr_b16 v[214:215], v93 offset:512\nds_read_b64_tr_b16 v[220:221], v93 offset:8\n" \
"ds_read_b64_tr_b16 v[222:223], v93 offset:520\nds_read_b64_tr_b16 v[28:29], v93 offset:64\nds_read_b64_tr_b16 v[30:31], v93 offset:576\nds_read_b64_tr_b16 v[228:229], v93 offset:72\nds_read_b64_tr_b16 v[230:231], v93 offset:584\ns_waitcnt lgkmcnt(0)\nds_write_b128 v1, v[28:31] offset:21504\ns_waitcnt vmcnt(4)\n" \
"ds_read_b64_tr_b16 v[232:233], v93 offset:4096\nds_read_b64_tr_b16 v[234:235], v93 offset:4608\nds_read_b64_tr_b16 v[240:241], v93 offset:4104\nds_read_b64_tr_b16 v[242:243], v93 offset:4616\nds_read_b64_tr_b16 v[248:249], v93 offset:4160\nds_read_b64_tr_b16 v[250:251], v93 offset:4672\nds_read_b64_tr_b16 v[28:29], v93 offset:4168\nds_read_b64_tr_b16 v[30:31], v93 offset:4680\n" \
"s_waitcnt lgkmcnt(0)\nds_write_b128 v1, v[28:31] offset:22528\ns_waitcnt vmcnt(0)\nds_read_b64_tr_b16 v[236:237], v93 offset:8192\nds_read_b64_tr_b16 v[238:239], v93 offset:8704\nds_read_b64_tr_b16 v[244:245], v93 offset:8200\nds_read_b64_tr_b16 v[246:247], v93 offset:8712\nds_read_b64_tr_b16 v[252:253], v93 offset:8256\n" \
"ds_read_b64_tr_b16 v[254:255], v93 offset:8768\nds_read_b64_tr_b16 v[28:29], v93 offset:8264\nds_read_b64_tr_b16 v[30:31], v93 offset:8776\ns_waitcnt lgkmcnt(0)\nds_write_b128 v1, v[28:31] offset:23552\ns_waitcnt lgkmcnt(0)\ns_barrier\ns_mov_b32 s43, 0\n" \
".Lqk_hb%=:\nbuffer_load_dwordx2 v[16:17], off, s[36:39], s45 sc1\ns_waitcnt vmcnt(0)\nv_cmp_ne_u32 vcc, v95, v16\ns_cbranch_vccnz .Lqk_hb1%=\ns_sleep 4\ns_add_u32 s43, s43, 1\ns_cmp_lt_u32 s43, 262144\n" \
"s_cbranch_scc1 .Lqk_hb%=\ns_branch .Lqk_abort%=\n.Lqk_hb1%=:\nv_readfirstlane_b32 s44, v17\ns_and_b32 s44, s44, 0x200000\ns_add_u32 s36, s36, s44\ns_addc_u32 s37, s37, 0\ns_mov_b32 s40, -1\n" \
"s_mov_b32 s43, 0\ns_mov_b32 s61, 0x7fff7fff\ns_mov_b32 s58, 0x3e0293ee\ns_mov_b32 s59, 0xbe0293ee\ns_lshl_b32 s57, %[w], 12\ns_mov_b32 s62, 0xff00ff00\ns_mov_b32 s63, 0xff00ff00\ns_mov_b32 s56, 0\n" \
"s_cmp_eq_u32 %[w], 3\ns_cbranch_scc1 .Lqk_fetch0%=\n.Lqk_pwait%=:\ns_barrier\nds_read_b128 v[80:83], v2 offset:0\nds_read_b128 v[84:87], v2 offset:256\nds_read_b128 v[88:91], v2 offset:2048\n.Lqk_proj%=:\n" \
"s_waitcnt lgkmcnt(2)\nv_mfma_f32_16x16x32_bf16 v[16:19], a[0:3], v[80:83], 0\ns_add_u32 s40, s40, 1\nds_read_b128 v[92:95], v2 offset:2304\nv_mfma_f32_16x16x32_bf16 v[20:23], a[8:11], v[80:83], 0\ns_lshl_b32 s44, s40, 17\nds_read_b128 v[72:75], v1 offset:0\nv_mfma_f32_16x16x32_bf16 v[24:27], a[16:19], v[80:83], 0\n" \
"s_add_u32 s44, s44, %[xn]\nv_mfma_f32_16x16x32_bf16 v[28:31], a[24:27], v[80:83], 0\ns_lshr_b32 s45, s44, 8\ns_waitcnt lgkmcnt(3)\nv_mfma_f32_16x16x32_bf16 v[32:35], a[0:3], v[84:87], 0\ns_and_b32 s45, s45, 0x2000\nds_read_b128 v[80:83], v2 offset:4096\nv_mfma_f32_16x16x32_bf16 v[36:39], a[8:11], v[84:87], 0\n" \
"s_xor_b32 s41, s44, s45\nv_mfma_f32_16x16x32_bf16 v[40:43], a[16:19], v[84:87], 0\ns_add_u32 s41, s41, %[xw]\nv_mfma_f32_16x16x32_bf16 v[44:47], a[24:27], v[84:87], 0\ns_and_b32 s42, s40, 3\ns_waitcnt lgkmcnt(3)\nv_mfma_f32_16x16x32_bf16 v[16:19], a[4:7], v[88:91], v[16:19]\ns_lshl_b32 s42, s42, 15\n" \
"ds_read_b128 v[84:87], v2 offset:4352\nv_mfma_f32_16x16x32_bf16 v[20:23], a[12:15], v[88:91], v[20:23]\ns_and_b32 s44, s40, 1\nds_read_b128 v[76:79], v1 offset:1024\nv_mfma_f32_16x16x32_bf16 v[24:27], a[20:23], v[88:91], v[24:27]\ns_lshl_b32 s55, s44, 17\nv_mfma_f32_16x16x32_bf16 v[28:31], a[28:31], v[88:91], v[28:31]\ns_add_u32 s56, s56, %[fs]\n" \
"s_waitcnt lgkmcnt(4)\nv_mfma_f32_16x16x32_bf16 v[32:35], a[4:7], v[92:95], v[32:35]\nds_read_b128 v[88:91], v2 offset:6144\nv_mfma_f32_16x16x32_bf16 v[36:39], a[12:15], v[92:95], v[36:39]\nv_mfma_f32_16x16x32_bf16 v[40:43], a[20:23], v[92:95], v[40:43]\nv_mfma_f32_16x16x32_bf16 v[44:47], a[28:31], v[92:95], v[44:47]\ns_waitcnt lgkmcnt(3)\nv_mfma_f32_16x16x32_bf16 v[16:19], a[32:35], v[80:83], v[16:19]\n" \
"ds_read_b128 v[92:95], v2 offset:6400\nv_mfma_f32_16x16x32_bf16 v[20:23], v[72:75], v[80:83], v[20:23]\nds_read_b128 v[64:67], v1 offset:2048\nv_mfma_f32_16x16x32_bf16 v[24:27], a[40:43], v[80:83], v[24:27]\nv_mfma_f32_16x16x32_bf16 v[28:31], a[48:51], v[80:83], v[28:31]\ns_waitcnt lgkmcnt(4)\nv_mfma_f32_16x16x32_bf16 v[32:35], a[32:35], v[84:87], v[32:35]\nds_read_b128 v[80:83], v2 offset:8192\n" \
"v_mfma_f32_16x16x32_bf16 v[36:39], v[72:75], v[84:87], v[36:39]\nv_mfma_f32_16x16x32_bf16 v[40:43], a[40:43], v[84:87], v[40:43]\ns_add_u32 m0, s57, 32768\ns_add_u32 s60, s41, 65536\nbuffer_load_dwordx4 v0, s[36:39], s60 offen offset:0 sc1 lds\nv_mfma_f32_16x16x32_bf16 v[44:47], a[48:51], v[84:87], v[44:47]\ns_waitcnt lgkmcnt(3)\nv_mfma_f32_16x16x32_bf16 v[16:19], a[36:39], v[88:91], v[16:19]\n" \
"ds_read_b128 v[84:87], v2 offset:8448\nv_mfma_f32_16x16x32_bf16 v[20:23], v[76:79], v[88:91], v[20:23]\nds_read_b128 v[68:71], v1 offset:3072\nv_mfma_f32_16x16x32_bf16 v[24:27], a[44:47], v[88:91], v[24:27]\nv_mfma_f32_16x16x32_bf16 v[28:31], a[52:55], v[88:91], v[28:31]\ns_waitcnt lgkmcnt(4)\nv_mfma_f32_16x16x32_bf16 v[32:35], a[36:39], v[92:95], v[32:35]\nds_read_b128 v[88:91], v2 offset:10240\n" \
"v_mfma_f32_16x16x32_bf16 v[36:39], v[76:79], v[92:95], v[36:39]\nv_mfma_f32_16x16x32_bf16 v[40:43], a[44:47], v[92:95], v[40:43]\nbuffer_load_dwordx4 v0, s[36:39], s60 offen offset:1024 sc1 lds\nv_mfma_f32_16x16x32_bf16 v[44:47], a[52:55], v[92:95], v[44:47]\ns_waitcnt lgkmcnt(3)\nv_mfma_f32_16x16x32_bf16 v[16:19], a[56:59], v[80:83], v[16:19]\nds_read_b128 v[92:95], v2 offset:10496\nv_mfma_f32_16x16x32_bf16 v[20:23], a[64:67], v[80:83], v[20:23]\n" \
"ds_read_b128 v[72:75], v1 offset:4096\nv_mfma_f32_16x16x32_bf16 v[24:27], v[64:67], v[80:83], v[24:27]\nv_mfma_f32_16x16x32_bf16 v[28:31], a[72:75], v[80:83], v[28:31]\ns_waitcnt lgkmcnt(4)\nv_mfma_f32_16x16x32_bf16 v[32:35], a[56:59], v[84:87], v[32:35]\nds_read_b128 v[80:83], v2 offset:12288\nv_mfma_f32_16x16x32_bf16 v[36:39], a[64:67], v[84:87], v[36:39]\nv_mfma_f32_16x16x32_bf16 v[40:43], v[64:67], v[84:87], v[40:43]\n" \
"buffer_load_dwordx4 v0, s[36:39], s60 offen offset:2048 sc1 lds\nv_mfma_f32_16x16x32_bf16 v[44:47], a[72:75], v[84:87], v[44:47]\ns_waitcnt lgkmcnt(3)\nv_mfma_f32_16x16x32_bf16 v[16:19], a[60:63], v[88:91], v[16:19]\nds_read_b128 v[84:87], v2 offset:12544\nv_mfma_f32_16x16x32_bf16 v[20:23], a[68:71], v[88:91], v[20:23]\nds_read_b128 v[76:79], v1 offset:5120\nv_mfma_f32_16x16x32_bf16 v[24:27], v[68:71], v[88:91], v[24:27]\n" \
"v_mfma_f32_16x16x32_bf16 v[28:31], a[76:79], v[88:91], v[28:31]\ns_waitcnt lgkmcnt(4)\nv_mfma_f32_16x16x32_bf16 v[32:35], a[60:63], v[92:95], v[32:35]\nds_read_b128 v[88:91], v2 offset:14336\nv_mfma_f32_16x16x32_bf16 v[36:39], a[68:71], v[92:95], v[36:39]\nv_mfma_f32_16x16x32_bf16 v[40:43], v[68:71], v[92:95], v[40:43]\nbuffer_load_dwordx4 v0, s[36:39], s60 offen offset:3072 sc1 lds\nv_mfma_f32_16x16x32_bf16 v[44:47], a[76:79], v[92:95], v[44:47]\n" \
"s_waitcnt lgkmcnt(3)\nv_mfma_f32_16x16x32_bf16 v[16:19], a[80:83], v[80:83], v[16:19]\nds_read_b128 v[92:95], v2 offset:14592\nv_mfma_f32_16x16x32_bf16 v[20:23], a[88:91], v[80:83], v[20:23]\nv_mfma_f32_16x16x32_bf16 v[24:27], a[96:99], v[80:83], v[24:27]\nv_mfma_f32_16x16x32_bf16 v[28:31], v[72:75], v[80:83], v[28:31]\ns_waitcnt lgkmcnt(3)\nv_mfma_f32_16x16x32_bf16 v[32:35], a[80:83], v[84:87], v[32:35]\n" \
"s_waitcnt vmcnt(4)\ns_barrier\nds_read_b128 v[80:83], v2 offset:16384\nv_mfma_f32_16x16x32_bf16 v[36:39], a[88:91], v[84:87], v[36:39]\nv_mfma_f32_16x16x32_bf16 v[40:43], a[96:99], v[84:87], v[40:43]\nv_mfma_f32_16x16x32_bf16 v[44:47], v[72:75], v[84:87], v[44:47]\ns_waitcnt lgkmcnt(2)\nv_mfma_f32_16x16x32_bf16 v[16:19], a[84:87], v[88:91], v[16:19]\n" \
"ds_read_b128 v[84:87], v2 offset:16640\nv_mfma_f32_16x16x32_bf16 v[20:23], a[92:95], v[88:91], v[20:23]\nv_mfma_f32_16x16x32_bf16 v[24:27], a[100:103], v[88:91], v[24:27]\nv_mfma_f32_16x16x32_bf16 v[28:31], v[76:79], v[88:91], v[28:31]\ns_waitcnt lgkmcnt(2)\nv_mfma_f32_16x16x32_bf16 v[32:35], a[84:87], v[92:95], v[32:35]\nds_read_b128 v[88:91], v2 offset:18432\nv_mfma_f32_16x16x32_bf16 v[36:39], a[92:95], v[92:95], v[36:39]\n" \
"v_mfma_f32_16x16x32_bf16 v[40:43], a[100:103], v[92:95], v[40:43]\nv_mfma_f32_16x16x32_bf16 v[44:47], v[76:79], v[92:95], v[44:47]\ns_waitcnt lgkmcnt(2)\nv_mfma_f32_16x16x32_bf16 v[16:19], a[104:107], v[80:83], v[16:19]\nds_read_b128 v[92:95], v2 offset:18688\nv_mfma_f32_16x16x32_bf16 v[20:23], a[112:115], v[80:83], v[20:23]\nds_read_b128 v[72:75], v1 offset:6144\nv_mfma_f32_16x16x32_bf16 v[24:27], a[120:123], v[80:83], v[24:27]\n" \
"v_mfma_f32_16x16x32_bf16 v[28:31], a[128:131], v[80:83], v[28:31]\ns_waitcnt lgkmcnt(3)\nv_mfma_f32_16x16x32_bf16 v[32:35], a[104:107], v[84:87], v[32:35]\nds_read_b128 v[80:83], v2 offset:20480\nv_mfma_f32_16x16x32_bf16 v[36:39], a[112:115], v[84:87], v[36:39]\nv_mfma_f32_16x16x32_bf16 v[40:43], a[120:123], v[84:87], v[40:43]\nv_mfma_f32_16x16x32_bf16 v[44:47], a[128:131], v[84:87], v[44:47]\ns_waitcnt lgkmcnt(3)\n" \
"v_mfma_f32_16x16x32_bf16 v[16:19], a[108:111], v[88:91], v[16:19]\nds_read_b128 v[84:87], v2 offset:20736\nv_mfma_f32_16x16x32_bf16 v[20:23], a[116:119], v[88:91], v[20:23]\nds_read_b128 v[76:79], v1 offset:7168\nv_mfma_f32_16x16x32_bf16 v[24:27], a[124:127], v[88:91], v[24:27]\nv_mfma_f32_16x16x32_bf16 v[28:31], a[132:135], v[88:91], v[28:31]\ns_waitcnt lgkmcnt(4)\nv_mfma_f32_16x16x32_bf16 v[32:35], a[108:111], v[92:95], v[32:35]\n" \
"ds_read_b128 v[88:91], v2 offset:22528\nv_mfma_f32_16x16x32_bf16 v[36:39], a[116:119], v[92:95], v[36:39]\nv_mfma_f32_16x16x32_bf16 v[40:43], a[124:127], v[92:95], v[40:43]\nv_mfma_f32_16x16x32_bf16 v[44:47], a[132:135], v[92:95], v[44:47]\ns_waitcnt lgkmcnt(3)\nv_mfma_f32_16x16x32_bf16 v[16:19], a[136:139], v[80:83], v[16:19]\nds_read_b128 v[92:95], v2 offset:22784\nv_mfma_f32_16x16x32_bf16 v[20:23], v[72:75], v[80:83], v[20:23]\n" \
"ds_read_b128 v[64:67], v1 offset:8192\nv_mfma_f32_16x16x32_bf16 v[24:27], a[144:147], v[80:83], v[24:27]\nv_mfma_f32_16x16x32_bf16 v[28:31], a[152:155], v[80:83], v[28:31]\ns_waitcnt lgkmcnt(4)\nv_mfma_f32_16x16x32_bf16 v[32:35], a[136:139], v[84:87], v[32:35]\nds_read_b128 v[80:83], v2 offset:24576\nv_mfma_f32_16x16x32_bf16 v[36:39], v[72:75], v[84:87], v[36:39]\nv_mfma_f32_16x16x32_bf16 v[40:43], a[144:147], v[84:87], v[40:43]\n" \
"v_mfma_f32_16x16x32_bf16 v[44:47], a[152:155], v[84:87], v[44:47]\ns_waitcnt lgkmcnt(3)\nv_mfma_f32_16x16x32_bf16 v[16:19], a[140:143], v[88:91], v[16:19]\nds_read_b128 v[84:87], v2 offset:24832\nv_mfma_f32_16x16x32_bf16 v[20:23], v[76:79], v[88:91], v[20:23]\nds_read_b128 v[68:71], v1 offset:9216\nv_mfma_f32_16x16x32_bf16 v[24:27], a[148:151], v[88:91], v[24:27]\nv_mfma_f32_16x16x32_bf16 v[28:31], a[156:159], v[88:91], v[28:31]\n" \
"s_waitcnt lgkmcnt(4)\nv_mfma_f32_16x16x32_bf16 v[32:35], a[140:143], v[92:95], v[32:35]\nds_read_b128 v[88:91], v2 offset:26624\nv_mfma_f32_16x16x32_bf16 v[36:39], v[76:79], v[92:95], v[36:39]\nv_mfma_f32_16x16x32_bf16 v[40:43], a[148:151], v[92:95], v[40:43]\nv_mfma_f32_16x16x32_bf16 v[44:47], a[156:159], v[92:95], v[44:47]\ns_waitcnt lgkmcnt(3)\nv_mfma_f32_16x16x32_bf16 v[16:19], a[160:163], v[80:83], v[16:19]\n" \
"ds_read_b128 v[92:95], v2 offset:26880\nv_mfma_f32_16x16x32_bf16 v[20:23], a[168:171], v[80:83], v[20:23]\nds_read_b128 v[72:75], v1 offset:10240\nv_mfma_f32_16x16x32_bf16 v[24:27], v[64:67], v[80:83], v[24:27]\nv_mfma_f32_16x16x32_bf16 v[28:31], a[176:179], v[80:83], v[28:31]\ns_waitcnt lgkmcnt(4)\nv_mfma_f32_16x16x32_bf16 v[32:35], a[160:163], v[84:87], v[32:35]\nds_read_b128 v[80:83], v2 offset:28672\n" \
"v_mfma_f32_16x16x32_bf16 v[36:39], a[168:171], v[84:87], v[36:39]\nv_mfma_f32_16x16x32_bf16 v[40:43], v[64:67], v[84:87], v[40:43]\nv_mfma_f32_16x16x32_bf16 v[44:47], a[176:179], v[84:87], v[44:47]\ns_waitcnt lgkmcnt(3)\nv_mfma_f32_16x16x32_bf16 v[16:19], a[164:167], v[88:91], v[16:19]\nds_read_b128 v[84:87], v2 offset:28928\nv_mfma_f32_16x16x32_bf16 v[20:23], a[172:175], v[88:91], v[20:23]\nds_read_b128 v[76:79], v1 offset:11264\n" \
"v_mfma_f32_16x16x32_bf16 v[24:27], v[68:71], v[88:91], v[24:27]\nv_mfma_f32_16x16x32_bf16 v[28:31], a[180:183], v[88:91], v[28:31]\ns_waitcnt lgkmcnt(4)\nv_mfma_f32_16x16x32_bf16 v[32:35], a[164:167], v[92:95], v[32:35]\nds_read_b128 v[88:91], v2 offset:30720\nv_mfma_f32_16x16x32_bf16 v[36:39], a[172:175], v[92:95], v[36:39]\nv_mfma_f32_16x16x32_bf16 v[40:43], v[68:71], v[92:95], v[40:43]\nv_mfma_f32_16x16x32_bf16 v[44:47], a[180:183], v[92:95], v[44:47]\n" \
"s_waitcnt lgkmcnt(3)\nv_mfma_f32_16x16x32_bf16 v[16:19], a[184:187], v[80:83], v[16:19]\nds_read_b128 v[92:95], v2 offset:30976\nv_mfma_f32_16x16x32_bf16 v[20:23], a[192:195], v[80:83], v[20:23]\nv_mfma_f32_16x16x32_bf16 v[24:27], a[200:203], v[80:83], v[24:27]\nv_mfma_f32_16x16x32_bf16 v[28:31], v[72:75], v[80:83], v[28:31]\ns_waitcnt lgkmcnt(3)\nv_mfma_f32_16x16x32_bf16 v[32:35], a[184:187], v[84:87], v[32:35]\n" \
"s_waitcnt vmcnt(0)\ns_barrier\nds_read_b128 v[48:51], v2 offset:32768\nv_mfma_f32_16x16x32_bf16 v[36:39], a[192:195], v[84:87], v[36:39]\nv_mfma_f32_16x16x32_bf16 v[40:43], a[200:203], v[84:87], v[40:43]\ns_add_u32 m0, s57, 0\ns_add_u32 s60, s41, 98304\nbuffer_load_dwordx4 v0, s[36:39], s60 offen offset:0 sc1 lds\n" \
"v_mfma_f32_16x16x32_bf16 v[44:47], v[72:75], v[84:87], v[44:47]\ns_waitcnt lgkmcnt(2)\nv_mfma_f32_16x16x32_bf16 v[16:19], a[188:191], v[88:91], v[16:19]\nds_read_b128 v[52:55], v2 offset:33024\nv_mfma_f32_16x16x32_bf16 v[20:23], a[196:199], v[88:91], v[20:23]\nv_mfma_f32_16x16x32_bf16 v[24:27], a[204:207], v[88:91], v[24:27]\nbuffer_load_dwordx4 v0, s[36:39], s60 offen offset:1024 sc1 lds\nv_mfma_f32_16x16x32_bf16 v[28:31], v[76:79], v[88:91], v[28:31]\n" \
"s_waitcnt lgkmcnt(2)\nv_mfma_f32_16x16x32_bf16 v[32:35], a[188:191], v[92:95], v[32:35]\nds_read_b128 v[56:59], v2 offset:34816\nv_mfma_f32_16x16x32_bf16 v[36:39], a[196:199], v[92:95], v[36:39]\nv_mfma_f32_16x16x32_bf16 v[40:43], a[204:207], v[92:95], v[40:43]\nbuffer_load_dwordx4 v0, s[36:39], s60 offen offset:2048 sc1 lds\nv_mfma_f32_16x16x32_bf16 v[44:47], v[76:79], v[92:95], v[44:47]\ns_waitcnt lgkmcnt(2)\n" \
"v_mfma_f32_16x16x32_bf16 v[16:19], a[208:211], v[48:51], v[16:19]\nds_read_b128 v[60:63], v2 offset:35072\nv_mfma_f32_16x16x32_bf16 v[20:23], a[216:219], v[48:51], v[20:23]\nds_read_b128 v[72:75], v1 offset:12288\nv_mfma_f32_16x16x32_bf16 v[24:27], a[224:227], v[48:51], v[24:27]\nbuffer_load_dwordx4 v0, s[36:39], s60 offen offset:3072 sc1 lds\nv_mfma_f32_16x16x32_bf16 v[28:31], a[232:235], v[48:51], v[28:31]\ns_waitcnt lgkmcnt(3)\n" \
"v_mfma_f32_16x16x32_bf16 v[32:35], a[208:211], v[52:55], v[32:35]\nds_read_b128 v[48:51], v2 offset:36864\nv_mfma_f32_16x16x32_bf16 v[36:39], a[216:219], v[52:55], v[36:39]\nv_mfma_f32_16x16x32_bf16 v[40:43], a[224:227], v[52:55], v[40:43]\nv_mfma_f32_16x16x32_bf16 v[44:47], a[232:235], v[52:55], v[44:47]\ns_waitcnt lgkmcnt(3)\nv_mfma_f32_16x16x32_bf16 v[16:19], a[212:215], v[56:59], v[16:19]\nds_read_b128 v[52:55], v2 offset:37120\n" \
"v_mfma_f32_16x16x32_bf16 v[20:23], a[220:223], v[56:59], v[20:23]\nds_read_b128 v[76:79], v1 offset:13312\nv_mfma_f32_16x16x32_bf16 v[24:27], a[228:231], v[56:59], v[24:27]\nv_mfma_f32_16x16x32_bf16 v[28:31], a[236:239], v[56:59], v[28:31]\ns_waitcnt lgkmcnt(4)\nv_mfma_f32_16x16x32_bf16 v[32:35], a[212:215], v[60:63], v[32:35]\nds_read_b128 v[56:59], v2 offset:38912\nv_mfma_f32_16x16x32_bf16 v[36:39], a[220:223], v[60:63], v[36:39]\n" \
"v_mfma_f32_16x16x32_bf16 v[40:43], a[228:231], v[60:63], v[40:43]\nv_mfma_f32_16x16x32_bf16 v[44:47], a[236:239], v[60:63], v[44:47]\ns_waitcnt lgkmcnt(3)\nv_mfma_f32_16x16x32_bf16 v[16:19], a[240:243], v[48:51], v[16:19]\nds_read_b128 v[60:63], v2 offset:39168\nv_mfma_f32_16x16x32_bf16 v[20:23], v[72:75], v[48:51], v[20:23]\nds_read_b128 v[64:67], v1 offset:14336\nv_mfma_f32_16x16x32_bf16 v[24:27], a[248:251], v[48:51], v[24:27]\n" \
"v_mfma_f32_16x16x32_bf16 v[28:31], v[96:99], v[48:51], v[28:31]\ns_waitcnt lgkmcnt(4)\nv_mfma_f32_16x16x32_bf16 v[32:35], a[240:243], v[52:55], v[32:35]\nds_read_b128 v[48:51], v2 offset:40960\nv_mfma_f32_16x16x32_bf16 v[36:39], v[72:75], v[52:55], v[36:39]\nv_mfma_f32_16x16x32_bf16 v[40:43], a[248:251], v[52:55], v[40:43]\nv_mfma_f32_16x16x32_bf16 v[44:47], v[96:99], v[52:55], v[44:47]\ns_waitcnt lgkmcnt(3)\n" \
"v_mfma_f32_16x16x32_bf16 v[16:19], a[244:247], v[56:59], v[16:19]\nds_read_b128 v[52:55], v2 offset:41216\nv_mfma_f32_16x16x32_bf16 v[20:23], v[76:79], v[56:59], v[20:23]\nds_read_b128 v[68:71], v1 offset:15360\nv_mfma_f32_16x16x32_bf16 v[24:27], a[252:255], v[56:59], v[24:27]\nv_mfma_f32_16x16x32_bf16 v[28:31], v[100:103], v[56:59], v[28:31]\ns_waitcnt lgkmcnt(4)\nv_mfma_f32_16x16x32_bf16 v[32:35], a[244:247], v[60:63], v[32:35]\n" \
"ds_read_b128 v[56:59], v2 offset:43008\nv_mfma_f32_16x16x32_bf16 v[36:39], v[76:79], v[60:63], v[36:39]\nv_mfma_f32_16x16x32_bf16 v[40:43], a[252:255], v[60:63], v[40:43]\nv_mfma_f32_16x16x32_bf16 v[44:47], v[100:103], v[60:63], v[44:47]\ns_waitcnt lgkmcnt(3)\nv_mfma_f32_16x16x32_bf16 v[16:19], v[104:107], v[48:51], v[16:19]\nds_read_b128 v[60:63], v2 offset:43264\nv_mfma_f32_16x16x32_bf16 v[20:23], v[112:115], v[48:51], v[20:23]\n" \
"ds_read_b128 v[72:75], v1 offset:16384\nv_mfma_f32_16x16x32_bf16 v[24:27], v[64:67], v[48:51], v[24:27]\nv_mfma_f32_16x16x32_bf16 v[28:31], v[120:123], v[48:51], v[28:31]\ns_waitcnt lgkmcnt(4)\nv_mfma_f32_16x16x32_bf16 v[32:35], v[104:107], v[52:55], v[32:35]\nds_read_b128 v[48:51], v2 offset:45056\nv_mfma_f32_16x16x32_bf16 v[36:39], v[112:115], v[52:55], v[36:39]\nv_mfma_f32_16x16x32_bf16 v[40:43], v[64:67], v[52:55], v[40:43]\n" \
"v_mfma_f32_16x16x32_bf16 v[44:47], v[120:123], v[52:55], v[44:47]\ns_waitcnt lgkmcnt(3)\nv_mfma_f32_16x16x32_bf16 v[16:19], v[108:111], v[56:59], v[16:19]\nds_read_b128 v[52:55], v2 offset:45312\nv_mfma_f32_16x16x32_bf16 v[20:23], v[116:119], v[56:59], v[20:23]\nds_read_b128 v[76:79], v1 offset:17408\nv_mfma_f32_16x16x32_bf16 v[24:27], v[68:71], v[56:59], v[24:27]\nv_mfma_f32_16x16x32_bf16 v[28:31], v[124:127], v[56:59], v[28:31]\n" \
"s_waitcnt lgkmcnt(4)\nv_mfma_f32_16x16x32_bf16 v[32:35], v[108:111], v[60:63], v[32:35]\nds_read_b128 v[56:59], v2 offset:47104\nv_mfma_f32_16x16x32_bf16 v[36:39], v[116:119], v[60:63], v[36:39]\nv_mfma_f32_16x16x32_bf16 v[40:43], v[68:71], v[60:63], v[40:43]\nv_mfma_f32_16x16x32_bf16 v[44:47], v[124:127], v[60:63], v[44:47]\ns_waitcnt lgkmcnt(3)\nv_mfma_f32_16x16x32_bf16 v[16:19], v[128:131], v[48:51], v[16:19]\n" \
"ds_read_b128 v[60:63], v2 offset:47360\nv_mfma_f32_16x16x32_bf16 v[20:23], v[136:139], v[48:51], v[20:23]\nv_mfma_f32_16x16x32_bf16 v[24:27], v[144:147], v[48:51], v[24:27]\nv_mfma_f32_16x16x32_bf16 v[28:31], v[72:75], v[48:51], v[28:31]\ns_waitcnt lgkmcnt(3)\nv_mfma_f32_16x16x32_bf16 v[32:35], v[128:131], v[52:55], v[32:35]\ns_waitcnt lgkmcnt(0)\ns_waitcnt vmcnt(0)\n" \
"s_barrier\nds_read_b128 v[48:51], v2 offset:0\nv_mfma_f32_16x16x32_bf16 v[36:39], v[136:139], v[52:55], v[36:39]\nv_mfma_f32_16x16x32_bf16 v[40:43], v[144:147], v[52:55], v[40:43]\nv_mfma_f32_16x16x32_bf16 v[44:47], v[72:75], v[52:55], v[44:47]\nv_mfma_f32_16x16x32_bf16 v[16:19], v[132:135], v[56:59], v[16:19]\nds_read_b128 v[52:55], v2 offset:256\nv_mfma_f32_16x16x32_bf16 v[20:23], v[140:143], v[56:59], v[20:23]\n" \
"v_mfma_f32_16x16x32_bf16 v[24:27], v[148:151], v[56:59], v[24:27]\nv_mfma_f32_16x16x32_bf16 v[28:31], v[76:79], v[56:59], v[28:31]\nv_mfma_f32_16x16x32_bf16 v[32:35], v[132:135], v[60:63], v[32:35]\nds_read_b128 v[56:59], v2 offset:2048\nv_mfma_f32_16x16x32_bf16 v[36:39], v[140:143], v[60:63], v[36:39]\nv_mfma_f32_16x16x32_bf16 v[40:43], v[148:151], v[60:63], v[40:43]\nv_mfma_f32_16x16x32_bf16 v[44:47], v[76:79], v[60:63], v[44:47]\ns_waitcnt lgkmcnt(2)\n" \
"v_mfma_f32_16x16x32_bf16 v[16:19], v[152:155], v[48:51], v[16:19]\nds_read_b128 v[60:63], v2 offset:2304\nv_mfma_f32_16x16x32_bf16 v[20:23], v[160:163], v[48:51], v[20:23]\nds_read_b128 v[72:75], v1 offset:18432\nv_mfma_f32_16x16x32_bf16 v[24:27], v[168:171], v[48:51], v[24:27]\nv_mfma_f32_16x16x32_bf16 v[28:31], v[176:179], v[48:51], v[28:31]\ns_waitcnt lgkmcnt(3)\nv_mfma_f32_16x16x32_bf16 v[32:35], v[152:155], v[52:55], v[32:35]\n" \
"ds_read_b128 v[48:51], v2 offset:4096\nv_mfma_f32_16x16x32_bf16 v[36:39], v[160:163], v[52:55], v[36:39]\nv_mfma_f32_16x16x32_bf16 v[40:43], v[168:171], v[52:55], v[40:43]\nv_mfma_f32_16x16x32_bf16 v[44:47], v[176:179], v[52:55], v[44:47]\ns_waitcnt lgkmcnt(3)\nv_mfma_f32_16x16x32_bf16 v[16:19], v[156:159], v[56:59], v[16:19]\nds_read_b128 v[52:55], v2 offset:4352\nv_mfma_f32_16x16x32_bf16 v[20:23], v[164:167], v[56:59], v[20:23]\n" \
"ds_read_b128 v[76:79], v1 offset:19456\nv_mfma_f32_16x16x32_bf16 v[24:27], v[172:175], v[56:59], v[24:27]\nv_mfma_f32_16x16x32_bf16 v[28:31], v[180:183], v[56:59], v[28:31]\ns_waitcnt lgkmcnt(4)\nv_mfma_f32_16x16x32_bf16 v[32:35], v[156:159], v[60:63], v[32:35]\nds_read_b128 v[56:59], v2 offset:6144\nv_mfma_f32_16x16x32_bf16 v[36:39], v[164:167], v[60:63], v[36:39]\nv_mfma_f32_16x16x32_bf16 v[40:43], v[172:175], v[60:63], v[40:43]\n" \
"v_mfma_f32_16x16x32_bf16 v[44:47], v[180:183], v[60:63], v[44:47]\ns_waitcnt lgkmcnt(3)\nv_mfma_f32_16x16x32_bf16 v[16:19], v[184:187], v[48:51], v[16:19]\nds_read_b128 v[60:63], v2 offset:6400\nv_mfma_f32_16x16x32_bf16 v[20:23], v[72:75], v[48:51], v[20:23]\nds_read_b128 v[64:67], v1 offset:20480\nv_mfma_f32_16x16x32_bf16 v[24:27], v[192:195], v[48:51], v[24:27]\nv_mfma_f32_16x16x32_bf16 v[28:31], v[200:203], v[48:51], v[28:31]\n" \
"s_waitcnt lgkmcnt(4)\nv_mfma_f32_16x16x32_bf16 v[32:35], v[184:187], v[52:55], v[32:35]\nds_read_b128 v[48:51], v2 offset:8192\nv_mfma_f32_16x16x32_bf16 v[36:39], v[72:75], v[52:55], v[36:39]\nv_mfma_f32_16x16x32_bf16 v[40:43], v[192:195], v[52:55], v[40:43]\nv_mfma_f32_16x16x32_bf16 v[44:47], v[200:203], v[52:55], v[44:47]\ns_waitcnt lgkmcnt(3)\nv_mfma_f32_16x16x32_bf16 v[16:19], v[188:191], v[56:59], v[16:19]\n" \
"ds_read_b128 v[52:55], v2 offset:10240\nv_mfma_f32_16x16x32_bf16 v[20:23], v[76:79], v[56:59], v[20:23]\nds_read_b128 v[68:71], v1 offset:21504\nv_mfma_f32_16x16x32_bf16 v[24:27], v[196:199], v[56:59], v[24:27]\nbuffer_load_dwordx4 v[80:83], v3, s[36:39], s42 offen offset:0 sc1\nv_mfma_f32_16x16x32_bf16 v[28:31], v[204:207], v[56:59], v[28:31]\ns_waitcnt lgkmcnt(4)\nv_mfma_f32_16x16x32_bf16 v[32:35], v[188:191], v[60:63], v[32:35]\n" \
"ds_read_b128 v[56:59], v2 offset:12288\nv_mfma_f32_16x16x32_bf16 v[36:39], v[76:79], v[60:63], v[36:39]\nds_read_b128 v[72:75], v1 offset:22528\nv_mfma_f32_16x16x32_bf16 v[40:43], v[196:199], v[60:63], v[40:43]\nbuffer_load_dwordx4 v[84:87], v3, s[36:39], s42 offen offset:1024 sc1\nv_mfma_f32_16x16x32_bf16 v[44:47], v[204:207], v[60:63], v[44:47]\ns_waitcnt lgkmcnt(4)\nv_mfma_f32_16x16x32_bf16 v[16:19], v[208:211], v[48:51], v[16:19]\n" \
"ds_read_b128 v[60:63], v2 offset:14336\nv_mfma_f32_16x16x32_bf16 v[20:23], v[216:219], v[48:51], v[20:23]\nds_read_b128 v[76:79], v1 offset:23552\nv_mfma_f32_16x16x32_bf16 v[24:27], v[64:67], v[48:51], v[24:27]\nbuffer_load_dwordx4 v[88:91], v3, s[36:39], s42 offen offset:2048 sc1\nv_mfma_f32_16x16x32_bf16 v[28:31], v[224:227], v[48:51], v[28:31]\ns_waitcnt lgkmcnt(5)\nv_mfma_f32_16x16x32_bf16 v[16:19], v[212:215], v[52:55], v[16:19]\n" \
"ds_read_b128 v[48:51], v2 offset:8448\nv_mfma_f32_16x16x32_bf16 v[20:23], v[220:223], v[52:55], v[20:23]\ns_waitcnt lgkmcnt(5)\nv_mfma_f32_16x16x32_bf16 v[24:27], v[68:71], v[52:55], v[24:27]\nbuffer_load_dwordx4 v[92:95], v3, s[36:39], s42 offen offset:3072 sc1\nv_mfma_f32_16x16x32_bf16 v[28:31], v[228:231], v[52:55], v[28:31]\ns_waitcnt lgkmcnt(4)\nv_mfma_f32_16x16x32_bf16 v[16:19], v[232:235], v[56:59], v[16:19]\n" \
"ds_read_b128 v[52:55], v2 offset:10496\nv_mfma_f32_16x16x32_bf16 v[20:23], v[240:243], v[56:59], v[20:23]\nv_mfma_f32_16x16x32_bf16 v[24:27], v[248:251], v[56:59], v[24:27]\ns_waitcnt lgkmcnt(4)\nv_mfma_f32_16x16x32_bf16 v[28:31], v[72:75], v[56:59], v[28:31]\ns_waitcnt lgkmcnt(3)\nv_mfma_f32_16x16x32_bf16 v[16:19], v[236:239], v[60:63], v[16:19]\nds_read_b128 v[56:59], v2 offset:12544\n" \
"v_mfma_f32_16x16x32_bf16 v[20:23], v[244:247], v[60:63], v[20:23]\nv_mfma_f32_16x16x32_bf16 v[24:27], v[252:255], v[60:63], v[24:27]\ns_waitcnt lgkmcnt(3)\nv_mfma_f32_16x16x32_bf16 v[28:31], v[76:79], v[60:63], v[28:31]\ns_waitcnt lgkmcnt(2)\nv_mfma_f32_16x16x32_bf16 v[32:35], v[208:211], v[48:51], v[32:35]\nds_read_b128 v[60:63], v2 offset:14592\nv_mfma_f32_16x16x32_bf16 v[36:39], v[216:219], v[48:51], v[36:39]\n" \
"v_cvt_pk_bf16_f32 v20, v16, v20\nv_cvt_pk_bf16_f32 v21, v17, v21\nv_mfma_f32_16x16x32_bf16 v[40:43], v[64:67], v[48:51], v[40:43]\nv_cvt_pk_bf16_f32 v22, v18, v22\nv_cvt_pk_bf16_f32 v23, v19, v23\nv_mfma_f32_16x16x32_bf16 v[44:47], v[224:227], v[48:51], v[44:47]\ns_waitcnt lgkmcnt(2)\nds_write_b128 v5, v[20:23] offset:0\n" \
"v_mfma_f32_16x16x32_bf16 v[32:35], v[212:215], v[52:55], v[32:35]\nv_cvt_pk_bf16_f32 v28, v24, v28\nv_cvt_pk_bf16_f32 v29, v25, v29\nv_mfma_f32_16x16x32_bf16 v[36:39], v[220:223], v[52:55], v[36:39]\nv_cvt_pk_bf16_f32 v30, v26, v30\nv_cvt_pk_bf16_f32 v31, v27, v31\nv_mfma_f32_16x16x32_bf16 v[40:43], v[68:71], v[52:55], v[40:43]\nv_mov_b64 v[48:49], 0\n" \
"v_mov_b64 v[50:51], 0\nv_mfma_f32_16x16x32_bf16 v[44:47], v[228:231], v[52:55], v[44:47]\ns_waitcnt lgkmcnt(2)\nds_write_b128 v5, v[28:31] offset:512\nv_mfma_f32_16x16x32_bf16 v[32:35], v[232:235], v[56:59], v[32:35]\nv_mov_b64 v[52:53], 0\nv_mov_b64 v[54:55], 0\nv_mfma_f32_16x16x32_bf16 v[36:39], v[240:243], v[56:59], v[36:39]\n" \
"s_waitcnt lgkmcnt(2)\nv_mfma_f32_16x16x32_bf16 v[32:35], v[236:239], v[60:63], v[32:35]\nv_mfma_f32_16x16x32_bf16 v[36:39], v[244:247], v[60:63], v[36:39]\nv_mfma_f32_16x16x32_bf16 v[40:43], v[248:251], v[56:59], v[40:43]\nv_mfma_f32_16x16x32_bf16 v[44:47], v[72:75], v[56:59], v[44:47]\nv_cvt_pk_bf16_f32 v36, v32, v36\nv_cvt_pk_bf16_f32 v37, v33, v37\nv_mfma_f32_16x16x32_bf16 v[40:43], v[252:255], v[60:63], v[40:43]\n" \
"v_cvt_pk_bf16_f32 v38, v34, v38\nv_cvt_pk_bf16_f32 v39, v35, v39\nv_mfma_f32_16x16x32_bf16 v[44:47], v[76:79], v[60:63], v[44:47]\ns_cmp_eq_u32 %[w], 1\nds_write_b128 v5, v[36:39] offset:256\nv_mov_b64 v[56:57], 0\nv_mov_b64 v[58:59], 0\nv_mov_b64 v[60:61], 0\n" \
"v_mov_b64 v[62:63], 0\ns_cbranch_scc1 .Lqk_c1%=\ns_cmp_eq_u32 %[w], 2\ns_cbranch_scc1 .Lqk_c2%=\ns_cmp_eq_u32 %[w], 3\ns_cbranch_scc1 .Lqk_c3%=\n.Lqk_c0%=:\nds_read_b128 v[64:67], v1 offset:0\n" \
"ds_read_b128 v[68:71], v1 offset:1024\nds_read_b128 v[72:75], v1 offset:2048\nds_read_b128 v[76:79], v1 offset:3072\nv_cvt_pk_bf16_f32 v44, v40, v44\nv_cvt_pk_bf16_f32 v45, v41, v45\nv_cvt_pk_bf16_f32 v46, v42, v46\nv_cvt_pk_bf16_f32 v47, v43, v47\nds_write_b128 v5, v[44:47] offset:768\n" \
"s_bfe_i32 s54, s40, 0x10000\n.Lqk_poll0%=:\ns_waitcnt vmcnt(0)\nv_smfmac_f32_16x16x64_bf16 v[48:51], v[80:83], a[0:7], v4\nds_write_b128 v0, v[80:83] offset:16384\nv_max3_u32 v40, v80, v82, v84\nv_smfmac_f32_16x16x64_bf16 v[52:55], v[80:83], a[8:15], v4\nds_write_b128 v0, v[84:87] offset:17408\n" \
"v_max3_u32 v40, v40, v86, v88\nv_smfmac_f32_16x16x64_bf16 v[56:59], v[80:83], a[16:23], v4\nds_write_b128 v0, v[88:91] offset:18432\nv_max3_u32 v40, v40, v90, v92\nv_max_u32 v40, v40, v94\nv_smfmac_f32_16x16x64_bf16 v[60:63], v[80:83], a[24:31], v4\nds_write_b128 v0, v[92:95] offset:19456\nv_cmp_eq_u32 vcc, -1, v40\n" \
"v_smfmac_f32_16x16x64_bf16 v[48:51], v[84:87], a[32:39], v4\ns_cbranch_vccnz .Lqk_re0%=\ns_waitcnt lgkmcnt(7)\nv_smfmac_f32_16x16x64_bf16 v[52:55], v[84:87], v[64:71], v4\ns_waitcnt lgkmcnt(0)\ns_barrier\nds_read_b128 v[80:83], v0 offset:20480\nds_read_b128 v[64:67], v1 offset:4096\n" \
"ds_read_b128 v[68:71], v1 offset:5120\nv_smfmac_f32_16x16x64_bf16 v[56:59], v[84:87], a[40:47], v4\nv_smfmac_f32_16x16x64_bf16 v[60:63], v[84:87], a[48:55], v4\nds_read_b128 v[84:87], v0 offset:21504\nv_smfmac_f32_16x16x64_bf16 v[48:51], v[88:91], a[56:63], v4\nv_smfmac_f32_16x16x64_bf16 v[52:55], v[88:91], a[64:71], v4\nv_smfmac_f32_16x16x64_bf16 v[56:59], v[88:91], v[72:79], v4\nds_read_b128 v[72:75], v1 offset:6144\n" \
"ds_read_b128 v[76:79], v1 offset:7168\nv_smfmac_f32_16x16x64_bf16 v[60:63], v[88:91], a[72:79], v4\nds_read_b128 v[88:91], v0 offset:22528\nv_smfmac_f32_16x16x64_bf16 v[48:51], v[92:95], a[80:87], v4\nds_read_b128 v[16:19], v7 offset:0\nv_smfmac_f32_16x16x64_bf16 v[52:55], v[92:95], a[88:95], v4\nv_smfmac_f32_16x16x64_bf16 v[56:59], v[92:95], a[96:103], v4\ns_waitcnt lgkmcnt(5)\n" \
"v_smfmac_f32_16x16x64_bf16 v[60:63], v[92:95], v[64:71], v4\nds_read_b128 v[92:95], v0 offset:23552\nds_read_b128 v[64:67], v1 offset:8192\nds_read_b128 v[68:71], v1 offset:9216\nv_smfmac_f32_16x16x64_bf16 v[48:51], v[80:83], a[104:111], v4\nds_read_b128 v[20:23], v7 offset:64\nv_smfmac_f32_16x16x64_bf16 v[52:55], v[80:83], a[112:119], v4\nv_smfmac_f32_16x16x64_bf16 v[56:59], v[80:83], a[120:127], v4\n" \
"v_smfmac_f32_16x16x64_bf16 v[60:63], v[80:83], a[128:135], v4\ns_waitcnt lgkmcnt(8)\nds_read_b128 v[80:83], v0 offset:24576\nv_smfmac_f32_16x16x64_bf16 v[48:51], v[84:87], a[136:143], v4\ns_waitcnt lgkmcnt(7)\nds_read_b128 v[24:27], v7 offset:2048\nv_smfmac_f32_16x16x64_bf16 v[52:55], v[84:87], v[72:79], v4\nds_read_b128 v[72:75], v1 offset:10240\n" \
"ds_read_b128 v[76:79], v1 offset:11264\nv_smfmac_f32_16x16x64_bf16 v[56:59], v[84:87], a[144:151], v4\nv_smfmac_f32_16x16x64_bf16 v[60:63], v[84:87], a[152:159], v4\ns_waitcnt lgkmcnt(9)\nds_read_b128 v[84:87], v0 offset:25600\nv_smfmac_f32_16x16x64_bf16 v[48:51], v[88:91], a[160:167], v4\nds_read_b128 v[28:31], v7 offset:2112\nv_smfmac_f32_16x16x64_bf16 v[52:55], v[88:91], a[168:175], v4\n" \
"s_waitcnt lgkmcnt(7)\nv_smfmac_f32_16x16x64_bf16 v[56:59], v[88:91], v[64:71], v4\nds_read_b128 v[64:67], v1 offset:12288\nds_read_b128 v[68:71], v1 offset:13312\nv_smfmac_f32_16x16x64_bf16 v[60:63], v[88:91], a[176:183], v4\nds_read_b128 v[88:91], v0 offset:26624\nv_smfmac_f32_16x16x64_bf16 v[48:51], v[92:95], a[184:191], v4\nds_read_b128 v[32:35], v7 offset:4096\n" \
"v_smfmac_f32_16x16x64_bf16 v[52:55], v[92:95], a[192:199], v4\nv_smfmac_f32_16x16x64_bf16 v[56:59], v[92:95], a[200:207], v4\ns_waitcnt lgkmcnt(6)\nv_smfmac_f32_16x16x64_bf16 v[60:63], v[92:95], v[72:79], v4\nds_read_b128 v[92:95], v0 offset:27648\nds_read_b128 v[72:75], v1 offset:14336\nds_read_b128 v[76:79], v1 offset:15360\nv_smfmac_f32_16x16x64_bf16 v[48:51], v[80:83], a[208:215], v4\n" \
"ds_read_b128 v[36:39], v7 offset:4160\nv_smfmac_f32_16x16x64_bf16 v[52:55], v[80:83], a[216:223], v4\nv_smfmac_f32_16x16x64_bf16 v[56:59], v[80:83], a[224:231], v4\nv_smfmac_f32_16x16x64_bf16 v[60:63], v[80:83], a[232:239], v4\ns_waitcnt lgkmcnt(9)\nds_read_b128 v[80:83], v0 offset:28672\nv_smfmac_f32_16x16x64_bf16 v[48:51], v[84:87], a[240:247], v4\ns_waitcnt lgkmcnt(7)\n" \
"ds_read_b128 v[40:43], v7 offset:6144\nv_smfmac_f32_16x16x64_bf16 v[52:55], v[84:87], v[64:71], v4\nds_read_b128 v[64:67], v1 offset:16384\nds_read_b128 v[68:71], v1 offset:17408\nv_smfmac_f32_16x16x64_bf16 v[56:59], v[84:87], a[248:255], v4\nv_smfmac_f32_16x16x64_bf16 v[60:63], v[84:87], v[96:103], v4\ns_waitcnt lgkmcnt(9)\nds_read_b128 v[84:87], v0 offset:29696\n" \
"v_smfmac_f32_16x16x64_bf16 v[48:51], v[88:91], v[104:111], v4\nds_read_b128 v[44:47], v7 offset:6208\nv_smfmac_f32_16x16x64_bf16 v[52:55], v[88:91], v[112:119], v4\ns_waitcnt lgkmcnt(7)\nv_smfmac_f32_16x16x64_bf16 v[56:59], v[88:91], v[72:79], v4\nds_read_b128 v[72:75], v1 offset:18432\nds_read_b128 v[76:79], v1 offset:19456\nv_smfmac_f32_16x16x64_bf16 v[60:63], v[88:91], v[120:127], v4\n" \
"ds_read_b128 v[88:91], v0 offset:30720\nv_smfmac_f32_16x16x64_bf16 v[48:51], v[92:95], v[128:135], v4\nv_smfmac_f32_16x16x64_bf16 v[52:55], v[92:95], v[136:143], v4\nv_smfmac_f32_16x16x64_bf16 v[56:59], v[92:95], v[144:151], v4\ns_waitcnt lgkmcnt(5)\nv_smfmac_f32_16x16x64_bf16 v[60:63], v[92:95], v[64:71], v4\nds_read_b128 v[92:95], v0 offset:31744\nds_read_b128 v[64:67], v1 offset:20480\n" \
"ds_read_b128 v[68:71], v1 offset:21504\nv_smfmac_f32_16x16x64_bf16 v[48:51], v[80:83], v[152:159], v4\nv_smfmac_f32_16x16x64_bf16 v[52:55], v[80:83], v[160:167], v4\nv_smfmac_f32_16x16x64_bf16 v[56:59], v[80:83], v[168:175], v4\nv_smfmac_f32_16x16x64_bf16 v[60:63], v[80:83], v[176:183], v4\ns_waitcnt lgkmcnt(7)\nv_smfmac_f32_16x16x64_bf16 v[48:51], v[84:87], v[184:191], v4\ns_waitcnt lgkmcnt(4)\n" \
"v_smfmac_f32_16x16x64_bf16 v[52:55], v[84:87], v[72:79], v4\nds_read_b128 v[72:75], v1 offset:22528\nds_read_b128 v[76:79], v1 offset:23552\nv_smfmac_f32_16x16x64_bf16 v[56:59], v[84:87], v[192:199], v4\nv_smfmac_f32_16x16x64_bf16 v[60:63], v[84:87], v[200:207], v4\ns_waitcnt lgkmcnt(5)\nv_smfmac_f32_16x16x64_bf16 v[48:51], v[88:91], v[208:215], v4\nv_smfmac_f32_16x16x64_bf16 v[52:55], v[88:91], v[216:223], v4\n" \
"s_waitcnt lgkmcnt(4)\nv_smfmac_f32_16x16x64_bf16 v[48:51], v[92:95], v[232:239], v4\nv_smfmac_f32_16x16x64_bf16 v[52:55], v[92:95], v[240:247], v4\ns_waitcnt lgkmcnt(2)\nv_smfmac_f32_16x16x64_bf16 v[56:59], v[88:91], v[64:71], v4\nv_add_f32 v48, v48, v50\nv_smfmac_f32_16x16x64_bf16 v[60:63], v[88:91], v[224:231], v4\nv_add_f32 v52, v52, v54\n" \
"v_add_f32 v49, v49, v51\nv_smfmac_f32_16x16x64_bf16 v[56:59], v[92:95], v[248:255], v4\ns_waitcnt lgkmcnt(0)\nv_add_f32 v53, v53, v55\nv_cvt_pk_bf16_f32 v50, v48, v52\nv_smfmac_f32_16x16x64_bf16 v[60:63], v[92:95], v[72:79], v4\nv_cvt_pk_bf16_f32 v51, v49, v53\nds_write2_b32 v6, v50, v51 offset0:0 offset1:4\n" \
"s_nop 1\nv_add_f32 v56, v56, v58\nv_add_f32 v57, v57, v59\ns_nop 1\nv_add_f32 v60, v60, v62\nv_add_f32 v61, v61, v63\nv_cvt_pk_bf16_f32 v62, v56, v60\nv_cvt_pk_bf16_f32 v63, v57, v61\n" \
"ds_write2_b32 v6, v62, v63 offset0:36 offset1:40\nv_mov_b32 v61, s54\ns_waitcnt lgkmcnt(0)\ns_barrier\nds_read_b128 v[80:83], v8 offset:0\nds_read_b128 v[84:87], v8 offset:576\nds_read_b128 v[88:91], v8 offset:1152\nds_read_b128 v[92:95], v8 offset:1728\n" \
"ds_read_b128 v[64:67], v9 offset:0\nds_read_b128 v[68:71], v9 offset:576\nds_read_b128 v[72:75], v9 offset:1152\nds_read_b128 v[76:79], v9 offset:1728\nds_read_b32 v60, v0 offset:53760\ns_waitcnt lgkmcnt(8)\nv_mfma_f32_16x16x32_bf16 v[52:55], v[16:19], v[80:83], 0\nv_mfma_f32_16x16x32_bf16 v[56:59], v[20:23], v[80:83], 0\n" \
"s_waitcnt lgkmcnt(7)\nv_mfma_f32_16x16x32_bf16 v[52:55], v[24:27], v[84:87], v[52:55]\nv_mfma_f32_16x16x32_bf16 v[56:59], v[28:31], v[84:87], v[56:59]\ns_waitcnt lgkmcnt(6)\nv_mfma_f32_16x16x32_bf16 v[52:55], v[32:35], v[88:91], v[52:55]\nv_mfma_f32_16x16x32_bf16 v[56:59], v[36:39], v[88:91], v[56:59]\ns_waitcnt lgkmcnt(5)\nv_mfma_f32_16x16x32_bf16 v[52:55], v[40:43], v[92:95], v[52:55]\n" \
"v_mfma_f32_16x16x32_bf16 v[56:59], v[44:47], v[92:95], v[56:59]\ns_waitcnt lgkmcnt(4)\nv_mfma_f32_16x16x32_bf16 v[48:51], v[64:67], v[80:83], 0\ns_waitcnt lgkmcnt(3)\nv_mfma_f32_16x16x32_bf16 v[48:51], v[68:71], v[84:87], v[48:51]\ns_waitcnt lgkmcnt(2)\nds_read_b128 v[80:83], v2 offset:0\nv_mfma_f32_16x16x32_bf16 v[48:51], v[72:75], v[88:91], v[48:51]\n" \
"s_waitcnt lgkmcnt(2)\nds_read_b128 v[84:87], v2 offset:256\nv_mfma_f32_16x16x32_bf16 v[48:51], v[76:79], v[92:95], v[48:51]\nds_read_b128 v[88:91], v2 offset:2048\nv_max3_f32 v16, v52, v53, v54\nv_max3_f32 v17, v55, v56, v57\nv_max3_f32 v16, v16, v58, v59\nv_cndmask_b32 v52, v52, v56, s[62:63]\n" \
"v_cndmask_b32 v53, v53, v57, s[62:63]\nv_cndmask_b32 v54, v54, v58, s[62:63]\nv_cndmask_b32 v55, v55, v59, s[62:63]\nv_max_f32 v16, v16, v17\nv_max3_f32 v17, v16, v48, v49\nv_max3_f32 v16, v16, v48, v49\ns_nop 1\nv_permlane16_swap_b32 v16, v17\n" \
"v_max_f32 v18, v16, v17\nv_max_f32 v16, v16, v17\ns_nop 1\nv_permlane32_swap_b32 v16, v18\nv_max_f32 v16, v16, v18\nv_mul_f32 v16, s59, v16\nv_fma_f32 v52, v52, s58, v16\nv_fma_f32 v53, v53, s58, v16\n" \
"v_exp_f32 v52, v52\nv_fma_f32 v54, v54, s58, v16\nv_exp_f32 v53, v53\nv_fma_f32 v55, v55, s58, v16\nv_exp_f32 v54, v54\nv_add_f32 v17, v52, v53\nv_fma_f32 v48, v48, s58, v16\nv_exp_f32 v55, v55\n" \
"v_add_f32 v17, v17, v54\nv_fma_f32 v49, v49, s58, v16\nv_exp_f32 v48, v48\nv_add_f32 v17, v17, v55\nv_exp_f32 v49, v49\nv_add_f32 v18, v48, v17\nv_add_f32 v18, v18, v49\ns_nop 1\n" \
"v_add_f32_dpp v16, v17, v18 row_ror:8 row_mask:0xf bank_mask:0xf\nv_add_f32_dpp v18, v17, v18 row_ror:8 row_mask:0xf bank_mask:0xf\ns_nop 1\nv_permlane16_swap_b32 v16, v18\nv_add_f32 v17, v16, v18\nv_add_f32 v18, v16, v18\ns_nop 1\nv_permlane32_swap_b32 v17, v18\n" \
"v_add_f32 v17, v17, v18\nv_rcp_f32 v17, v17\ns_nop 0\nv_mul_f32 v52, v52, v17\nv_mul_f32 v53, v53, v17\nv_mul_f32 v54, v54, v17\nv_mul_f32 v55, v55, v17\nv_mul_f32 v48, v48, v17\n" \
"v_mul_f32 v49, v49, v17\nv_cvt_pk_bf16_f32 v52, v52, v53\nv_cvt_pk_bf16_f32 v53, v54, v55\nv_cvt_pk_bf16_f32 v48, v48, v49\nv_bfi_b32 v52, s61, v52, v61\nv_bfi_b32 v53, s61, v53, v61\nbuffer_store_dwordx2 v[52:53], v10, s[36:39], 0 offen sc1\nv_bfi_b32 v48, s61, v48, v61\n" \
"buffer_store_dword v48, v11, s[36:39], 0 offen sc1\ns_waitcnt lgkmcnt(3)\nv_cmp_ne_u32 vcc, 0, v60\ns_cbranch_vccnz .Lqk_proj%=\ns_cmp_lt_i32 s40, 63\ns_cbranch_scc1 .Lqk_pwait%=\ns_branch .Lqk_end%=\n.Lqk_re0%=:\n" \
"v_max_u32 v40, v80, v82\nv_cmp_eq_u32 vcc, -1, v40\ns_cbranch_vccz .Lqk_sk1522%=\ns_mov_b64 exec, vcc\nbuffer_load_dwordx4 v[80:83], v3, s[36:39], s42 offen offset:0 sc1\ns_mov_b64 exec, -1\n.Lqk_sk1522%=:\nv_max_u32 v40, v84, v86\n" \
"v_cmp_eq_u32 vcc, -1, v40\ns_cbranch_vccz .Lqk_sk1529%=\ns_mov_b64 exec, vcc\nbuffer_load_dwordx4 v[84:87], v3, s[36:39], s42 offen offset:1024 sc1\ns_mov_b64 exec, -1\n.Lqk_sk1529%=:\nv_max_u32 v40, v88, v90\nv_cmp_eq_u32 vcc, -1, v40\n" \
"s_cbranch_vccz .Lqk_sk1536%=\ns_mov_b64 exec, vcc\nbuffer_load_dwordx4 v[88:91], v3, s[36:39], s42 offen offset:2048 sc1\ns_mov_b64 exec, -1\n.Lqk_sk1536%=:\nv_max_u32 v40, v92, v94\nv_cmp_eq_u32 vcc, -1, v40\ns_cbranch_vccz .Lqk_sk1543%=\n" \
"s_mov_b64 exec, vcc\nbuffer_load_dwordx4 v[92:95], v3, s[36:39], s42 offen offset:3072 sc1\ns_mov_b64 exec, -1\n.Lqk_sk1543%=:\nv_mov_b64 v[48:49], 0\nv_mov_b64 v[50:51], 0\nv_mov_b64 v[52:53], 0\nv_mov_b64 v[54:55], 0\n" \
"v_mov_b64 v[56:57], 0\nv_mov_b64 v[58:59], 0\nv_mov_b64 v[60:61], 0\nv_mov_b64 v[62:63], 0\ns_add_u32 s43, s43, 1\ns_cmp_lt_u32 s43, 262144\ns_cbranch_scc1 .Lqk_poll0%=\ns_branch .Lqk_abort%=\n" \
".Lqk_c1%=:\nds_read_b128 v[64:67], v1 offset:6144\nds_read_b128 v[68:71], v1 offset:7168\nds_read_b128 v[72:75], v1 offset:8192\nds_read_b128 v[76:79], v1 offset:9216\ns_nop 1\nv_cvt_pk_bf16_f32 v44, v40, v44\nv_cvt_pk_bf16_f32 v45, v41, v45\n" \
"v_cvt_pk_bf16_f32 v46, v42, v46\nv_cvt_pk_bf16_f32 v47, v43, v47\nds_write_b128 v5, v[44:47] offset:768\ns_bfe_i32 s54, s40, 0x10001\n.Lqk_poll1%=:\ns_waitcnt vmcnt(0)\nv_smfmac_f32_16x16x64_bf16 v[48:51], v[80:83], a[104:111], v4\nds_write_b128 v0, v[80:83] offset:20480\n" \
"v_max3_u32 v40, v80, v82, v84\nv_smfmac_f32_16x16x64_bf16 v[52:55], v[80:83], a[112:119], v4\nds_write_b128 v0, v[84:87] offset:21504\nv_max3_u32 v40, v40, v86, v88\nv_smfmac_f32_16x16x64_bf16 v[56:59], v[80:83], a[120:127], v4\nds_write_b128 v0, v[88:91] offset:22528\nv_max3_u32 v40, v40, v90, v92\nv_max_u32 v40, v40, v94\n" \
"v_smfmac_f32_16x16x64_bf16 v[60:63], v[80:83], a[128:135], v4\nds_write_b128 v0, v[92:95] offset:23552\nv_cmp_eq_u32 vcc, -1, v40\nv_smfmac_f32_16x16x64_bf16 v[48:51], v[84:87], a[136:143], v4\ns_cbranch_vccnz .Lqk_re1%=\ns_waitcnt lgkmcnt(7)\nv_smfmac_f32_16x16x64_bf16 v[52:55], v[84:87], v[64:71], v4\ns_waitcnt lgkmcnt(0)\n" \
"s_barrier\ns_memrealtime s[68:69]\nds_read_b128 v[80:83], v0 offset:24576\nds_read_b128 v[64:67], v1 offset:10240\nds_read_b128 v[68:71], v1 offset:11264\nv_mov_b32 v40, s40\nv_smfmac_f32_16x16x64_bf16 v[56:59], v[84:87], a[144:151], v4\nv_smfmac_f32_16x16x64_bf16 v[60:63], v[84:87], a[152:159], v4\n" \
"ds_read_b128 v[84:87], v0 offset:25600\nv_smfmac_f32_16x16x64_bf16 v[48:51], v[88:91], a[160:167], v4\nv_smfmac_f32_16x16x64_bf16 v[52:55], v[88:91], a[168:175], v4\ns_waitcnt lgkmcnt(0)\nv_mov_b32 v41, s68\nv_smfmac_f32_16x16x64_bf16 v[56:59], v[88:91], v[72:79], v4\ns_mov_b64 exec, 1\nbuffer_store_dwordx2 v[40:41], off, s[36:39], %[sg] sc1\n" \
"s_mov_b64 exec, -1\nds_read_b128 v[72:75], v1 offset:12288\nds_read_b128 v[76:79], v1 offset:13312\nv_smfmac_f32_16x16x64_bf16 v[60:63], v[88:91], a[176:183], v4\nds_read_b128 v[88:91], v0 offset:26624\nv_smfmac_f32_16x16x64_bf16 v[48:51], v[92:95], a[184:191], v4\nds_read_b128 v[16:19], v7 offset:0\nv_smfmac_f32_16x16x64_bf16 v[52:55], v[92:95], a[192:199], v4\n" \
"v_smfmac_f32_16x16x64_bf16 v[56:59], v[92:95], a[200:207], v4\nv_smfmac_f32_16x16x64_bf16 v[60:63], v[92:95], v[64:71], v4\nds_read_b128 v[92:95], v0 offset:27648\nds_read_b128 v[64:67], v1 offset:14336\nds_read_b128 v[68:71], v1 offset:15360\nv_smfmac_f32_16x16x64_bf16 v[48:51], v[80:83], a[208:215], v4\nds_read_b128 v[20:23], v7 offset:2048\nv_smfmac_f32_16x16x64_bf16 v[52:55], v[80:83], a[216:223], v4\n" \
"v_smfmac_f32_16x16x64_bf16 v[56:59], v[80:83], a[224:231], v4\nv_smfmac_f32_16x16x64_bf16 v[60:63], v[80:83], a[232:239], v4\nds_read_b128 v[80:83], v0 offset:28672\nv_smfmac_f32_16x16x64_bf16 v[48:51], v[84:87], a[240:247], v4\ns_waitcnt lgkmcnt(7)\nds_read_b128 v[24:27], v7 offset:4096\nv_smfmac_f32_16x16x64_bf16 v[52:55], v[84:87], v[72:79], v4\nds_read_b128 v[72:75], v1 offset:16384\n" \
"ds_read_b128 v[76:79], v1 offset:17408\nv_smfmac_f32_16x16x64_bf16 v[56:59], v[84:87], a[248:255], v4\nv_smfmac_f32_16x16x64_bf16 v[60:63], v[84:87], v[96:103], v4\ns_waitcnt lgkmcnt(9)\nds_read_b128 v[84:87], v0 offset:29696\nv_smfmac_f32_16x16x64_bf16 v[48:51], v[88:91], v[104:111], v4\nds_read_b128 v[28:31], v7 offset:6144\nv_smfmac_f32_16x16x64_bf16 v[52:55], v[88:91], v[112:119], v4\n" \
"s_waitcnt lgkmcnt(7)\nv_smfmac_f32_16x16x64_bf16 v[56:59], v[88:91], v[64:71], v4\nds_read_b128 v[64:67], v1 offset:18432\nds_read_b128 v[68:71], v1 offset:19456\nv_smfmac_f32_16x16x64_bf16 v[60:63], v[88:91], v[120:127], v4\nds_read_b128 v[88:91], v0 offset:30720\nv_smfmac_f32_16x16x64_bf16 v[48:51], v[92:95], v[128:135], v4\nds_read_b128 v[32:35], v7 offset:8192\n" \
"v_smfmac_f32_16x16x64_bf16 v[52:55], v[92:95], v[136:143], v4\nv_smfmac_f32_16x16x64_bf16 v[56:59], v[92:95], v[144:151], v4\ns_waitcnt lgkmcnt(6)\nv_smfmac_f32_16x16x64_bf16 v[60:63], v[92:95], v[72:79], v4\nds_read_b128 v[92:95], v0 offset:31744\nds_read_b128 v[72:75], v1 offset:20480\nds_read_b128 v[76:79], v1 offset:21504\nv_smfmac_f32_16x16x64_bf16 v[48:51], v[80:83], v[152:159], v4\n" \
"ds_read_b128 v[36:39], v7 offset:10240\nv_smfmac_f32_16x16x64_bf16 v[52:55], v[80:83], v[160:167], v4\nv_smfmac_f32_16x16x64_bf16 v[56:59], v[80:83], v[168:175], v4\nv_smfmac_f32_16x16x64_bf16 v[60:63], v[80:83], v[176:183], v4\ns_waitcnt lgkmcnt(9)\nds_read_b128 v[80:83], v0 offset:16384\nv_smfmac_f32_16x16x64_bf16 v[48:51], v[84:87], v[184:191], v4\ns_waitcnt lgkmcnt(7)\n" \
"ds_read_b128 v[40:43], v7 offset:12288\nv_smfmac_f32_16x16x64_bf16 v[52:55], v[84:87], v[64:71], v4\nds_read_b128 v[64:67], v1 offset:22528\nds_read_b128 v[68:71], v1 offset:23552\nv_smfmac_f32_16x16x64_bf16 v[56:59], v[84:87], v[192:199], v4\nv_smfmac_f32_16x16x64_bf16 v[60:63], v[84:87], v[200:207], v4\ns_waitcnt lgkmcnt(9)\nds_read_b128 v[84:87], v0 offset:17408\n" \
"v_smfmac_f32_16x16x64_bf16 v[48:51], v[88:91], v[208:215], v4\nds_read_b128 v[44:47], v7 offset:14336\nv_smfmac_f32_16x16x64_bf16 v[52:55], v[88:91], v[216:223], v4\ns_waitcnt lgkmcnt(7)\nv_smfmac_f32_16x16x64_bf16 v[56:59], v[88:91], v[72:79], v4\nds_read_b128 v[72:75], v1 offset:0\nds_read_b128 v[76:79], v1 offset:1024\nv_smfmac_f32_16x16x64_bf16 v[60:63], v[88:91], v[224:231], v4\n" \
"ds_read_b128 v[88:91], v0 offset:18432\nv_smfmac_f32_16x16x64_bf16 v[48:51], v[92:95], v[232:239], v4\nv_smfmac_f32_16x16x64_bf16 v[52:55], v[92:95], v[240:247], v4\nv_smfmac_f32_16x16x64_bf16 v[56:59], v[92:95], v[248:255], v4\ns_waitcnt lgkmcnt(5)\nv_smfmac_f32_16x16x64_bf16 v[60:63], v[92:95], v[64:71], v4\nds_read_b128 v[92:95], v0 offset:19456\nds_read_b128 v[64:67], v1 offset:2048\n" \
"ds_read_b128 v[68:71], v1 offset:3072\nv_smfmac_f32_16x16x64_bf16 v[48:51], v[80:83], a[0:7], v4\nv_smfmac_f32_16x16x64_bf16 v[52:55], v[80:83], a[8:15], v4\nv_smfmac_f32_16x16x64_bf16 v[56:59], v[80:83], a[16:23], v4\nv_smfmac_f32_16x16x64_bf16 v[60:63], v[80:83], a[24:31], v4\ns_waitcnt lgkmcnt(7)\nv_smfmac_f32_16x16x64_bf16 v[48:51], v[84:87], a[32:39], v4\ns_waitcnt lgkmcnt(4)\n" \
"v_smfmac_f32_16x16x64_bf16 v[52:55], v[84:87], v[72:79], v4\nds_read_b128 v[72:75], v1 offset:4096\nds_read_b128 v[76:79], v1 offset:5120\nv_smfmac_f32_16x16x64_bf16 v[56:59], v[84:87], a[40:47], v4\nv_smfmac_f32_16x16x64_bf16 v[60:63], v[84:87], a[48:55], v4\ns_waitcnt lgkmcnt(5)\nv_smfmac_f32_16x16x64_bf16 v[48:51], v[88:91], a[56:63], v4\nv_smfmac_f32_16x16x64_bf16 v[52:55], v[88:91], a[64:71], v4\n" \
"s_waitcnt lgkmcnt(4)\nv_smfmac_f32_16x16x64_bf16 v[48:51], v[92:95], a[80:87], v4\nv_smfmac_f32_16x16x64_bf16 v[52:55], v[92:95], a[88:95], v4\ns_waitcnt lgkmcnt(2)\nv_smfmac_f32_16x16x64_bf16 v[56:59], v[88:91], v[64:71], v4\nv_add_f32 v48, v48, v50\nv_smfmac_f32_16x16x64_bf16 v[60:63], v[88:91], a[72:79], v4\nv_add_f32 v52, v52, v54\n" \
"v_add_f32 v49, v49, v51\nv_smfmac_f32_16x16x64_bf16 v[56:59], v[92:95], a[96:103], v4\ns_waitcnt lgkmcnt(0)\nv_add_f32 v53, v53, v55\nv_cvt_pk_bf16_f32 v50, v48, v52\nv_smfmac_f32_16x16x64_bf16 v[60:63], v[92:95], v[72:79], v4\nv_cvt_pk_bf16_f32 v51, v49, v53\nds_write2_b32 v6, v50, v51 offset0:0 offset1:4\n" \
"s_nop 1\nv_add_f32 v56, v56, v58\nv_add_f32 v57, v57, v59\ns_nop 1\nv_add_f32 v60, v60, v62\nv_add_f32 v61, v61, v63\nv_cvt_pk_bf16_f32 v62, v56, v60\nv_cvt_pk_bf16_f32 v63, v57, v61\n" \
"ds_write2_b32 v6, v62, v63 offset0:36 offset1:40\nv_mov_b32 v61, s54\nv_mfma_f32_16x16x32_bf16 v[52:55], v[32:35], v[16:19], v[12:15]\nv_mfma_f32_16x16x32_bf16 v[52:55], v[36:39], v[20:23], v[52:55]\nv_mfma_f32_16x16x32_bf16 v[52:55], v[40:43], v[24:27], v[52:55]\nv_mfma_f32_16x16x32_bf16 v[52:55], v[44:47], v[28:31], v[52:55]\ns_waitcnt lgkmcnt(0)\ns_barrier\n" \
"ds_read_b128 v[32:35], v9 offset:0\nds_read_b128 v[36:39], v9 offset:576\nds_read_b128 v[40:43], v9 offset:1152\nds_read_b128 v[44:47], v9 offset:1728\nds_read_b32 v60, v0 offset:53760\nds_read_b128 v[80:83], v2 offset:0\nds_read_b128 v[84:87], v2 offset:256\nds_read_b128 v[88:91], v2 offset:2048\n" \
"s_waitcnt lgkmcnt(7)\nv_mfma_f32_16x16x32_bf16 v[48:51], v[32:35], v[16:19], 0\ns_waitcnt lgkmcnt(6)\nv_mfma_f32_16x16x32_bf16 v[48:51], v[36:39], v[20:23], v[48:51]\ns_waitcnt lgkmcnt(5)\nv_mfma_f32_16x16x32_bf16 v[48:51], v[40:43], v[24:27], v[48:51]\ns_waitcnt lgkmcnt(4)\nv_mfma_f32_16x16x32_bf16 v[48:51], v[44:47], v[28:31], v[48:51]\n" \
"v_max3_f32 v64, v52, v53, v54\nv_max_f32 v64, v64, v55\ns_nop 5\nv_max3_f32 v65, v64, v48, v49\nv_max3_f32 v64, v64, v48, v49\ns_nop 1\nv_permlane16_swap_b32 v64, v65\nv_max_f32 v66, v64, v65\n" \
"v_max_f32 v64, v64, v65\ns_nop 1\nv_permlane32_swap_b32 v64, v66\nv_max_f32 v64, v64, v66\nv_mul_f32 v64, s59, v64\nv_fma_f32 v52, v52, s58, v64\nv_fma_f32 v53, v53, s58, v64\nv_exp_f32 v52, v52\n" \
"v_fma_f32 v54, v54, s58, v64\nv_exp_f32 v53, v53\nv_fma_f32 v55, v55, s58, v64\nv_exp_f32 v54, v54\nv_add_f32 v65, v52, v53\nv_fma_f32 v48, v48, s58, v64\nv_exp_f32 v55, v55\nv_add_f32 v65, v65, v54\n" \
"v_fma_f32 v49, v49, s58, v64\nv_exp_f32 v48, v48\nv_add_f32 v65, v65, v55\nv_exp_f32 v49, v49\nv_add_f32 v65, v65, v48\nv_add_f32 v64, v65, v49\nv_add_f32 v66, v65, v49\ns_nop 1\n" \
"v_permlane16_swap_b32 v64, v66\nv_add_f32 v65, v64, v66\nv_add_f32 v66, v64, v66\ns_nop 1\nv_permlane32_swap_b32 v65, v66\nv_add_f32 v65, v65, v66\nv_rcp_f32 v65, v65\ns_nop 0\n" \
"v_mul_f32 v52, v52, v65\nv_mul_f32 v53, v53, v65\nv_mul_f32 v54, v54, v65\nv_mul_f32 v55, v55, v65\nv_mul_f32 v48, v48, v65\nv_mul_f32 v49, v49, v65\nv_cvt_pk_bf16_f32 v52, v52, v53\nv_cvt_pk_bf16_f32 v53, v54, v55\n" \
"v_cvt_pk_bf16_f32 v48, v48, v49\nv_bfi_b32 v52, s61, v52, v61\nv_bfi_b32 v53, s61, v53, v61\nv_bfi_b32 v48, s61, v48, v61\nbuffer_store_dwordx2 v[52:53], v10, s[36:39], s55 offen sc1\nbuffer_store_dword v48, v11, s[36:39], s55 offen sc1\ns_waitcnt lgkmcnt(3)\nv_cmp_ne_u32 vcc, 0, v60\n" \
"s_cbranch_vccnz .Lqk_proj%=\ns_cmp_lt_i32 s40, 63\ns_cbranch_scc1 .Lqk_pwait%=\ns_branch .Lqk_end%=\n.Lqk_re1%=:\nv_max_u32 v40, v80, v82\nv_cmp_eq_u32 vcc, -1, v40\ns_cbranch_vccz .Lqk_sk1807%=\n" \
"s_mov_b64 exec, vcc\nbuffer_load_dwordx4 v[80:83], v3, s[36:39], s42 offen offset:0 sc1\ns_mov_b64 exec, -1\n.Lqk_sk1807%=:\nv_max_u32 v40, v84, v86\nv_cmp_eq_u32 vcc, -1, v40\ns_cbranch_vccz .Lqk_sk1814%=\ns_mov_b64 exec, vcc\n" \
"buffer_load_dwordx4 v[84:87], v3, s[36:39], s42 offen offset:1024 sc1\ns_mov_b64 exec, -1\n.Lqk_sk1814%=:\nv_max_u32 v40, v88, v90\nv_cmp_eq_u32 vcc, -1, v40\ns_cbranch_vccz .Lqk_sk1821%=\ns_mov_b64 exec, vcc\nbuffer_load_dwordx4 v[88:91], v3, s[36:39], s42 offen offset:2048 sc1\n" \
"s_mov_b64 exec, -1\n.Lqk_sk1821%=:\nv_max_u32 v40, v92, v94\nv_cmp_eq_u32 vcc, -1, v40\ns_cbranch_vccz .Lqk_sk1828%=\ns_mov_b64 exec, vcc\nbuffer_load_dwordx4 v[92:95], v3, s[36:39], s42 offen offset:3072 sc1\ns_mov_b64 exec, -1\n" \
".Lqk_sk1828%=:\nv_mov_b64 v[48:49], 0\nv_mov_b64 v[50:51], 0\nv_mov_b64 v[52:53], 0\nv_mov_b64 v[54:55], 0\nv_mov_b64 v[56:57], 0\nv_mov_b64 v[58:59], 0\nv_mov_b64 v[60:61], 0\n" \
"v_mov_b64 v[62:63], 0\ns_add_u32 s43, s43, 1\ns_cmp_lt_u32 s43, 262144\ns_cbranch_scc1 .Lqk_poll1%=\ns_branch .Lqk_abort%=\n.Lqk_c2%=:\nds_read_b128 v[64:67], v1 offset:12288\nds_read_b128 v[68:71], v1 offset:13312\n" \
"ds_read_b128 v[72:75], v1 offset:14336\nds_read_b128 v[76:79], v1 offset:15360\ns_nop 1\nv_cvt_pk_bf16_f32 v44, v40, v44\nv_cvt_pk_bf16_f32 v45, v41, v45\nv_cvt_pk_bf16_f32 v46, v42, v46\nv_cvt_pk_bf16_f32 v47, v43, v47\nds_write_b128 v5, v[44:47] offset:768\n" \
"s_bfe_i32 s54, s40, 0x10001\n.Lqk_poll2%=:\ns_waitcnt vmcnt(0)\nv_smfmac_f32_16x16x64_bf16 v[48:51], v[80:83], a[208:215], v4\nds_write_b128 v0, v[80:83] offset:24576\nv_max3_u32 v40, v80, v82, v84\nv_smfmac_f32_16x16x64_bf16 v[52:55], v[80:83], a[216:223], v4\nds_write_b128 v0, v[84:87] offset:25600\n" \
"v_max3_u32 v40, v40, v86, v88\nv_smfmac_f32_16x16x64_bf16 v[56:59], v[80:83], a[224:231], v4\nds_write_b128 v0, v[88:91] offset:26624\nv_max3_u32 v40, v40, v90, v92\nv_max_u32 v40, v40, v94\nv_smfmac_f32_16x16x64_bf16 v[60:63], v[80:83], a[232:239], v4\nds_write_b128 v0, v[92:95] offset:27648\nv_cmp_eq_u32 vcc, -1, v40\n" \
"v_smfmac_f32_16x16x64_bf16 v[48:51], v[84:87], a[240:247], v4\ns_cbranch_vccnz .Lqk_re2%=\ns_waitcnt lgkmcnt(7)\nv_smfmac_f32_16x16x64_bf16 v[52:55], v[84:87], v[64:71], v4\ns_waitcnt lgkmcnt(0)\ns_barrier\nds_read_b128 v[80:83], v0 offset:28672\nds_read_b128 v[64:67], v1 offset:16384\n" \
"ds_read_b128 v[68:71], v1 offset:17408\nv_smfmac_f32_16x16x64_bf16 v[56:59], v[84:87], a[248:255], v4\nv_smfmac_f32_16x16x64_bf16 v[60:63], v[84:87], v[96:103], v4\nds_read_b128 v[84:87], v0 offset:29696\nv_smfmac_f32_16x16x64_bf16 v[48:51], v[88:91], v[104:111], v4\nv_smfmac_f32_16x16x64_bf16 v[52:55], v[88:91], v[112:119], v4\nv_smfmac_f32_16x16x64_bf16 v[56:59], v[88:91], v[72:79], v4\nds_read_b128 v[72:75], v1 offset:18432\n" \
"ds_read_b128 v[76:79], v1 offset:19456\nv_smfmac_f32_16x16x64_bf16 v[60:63], v[88:91], v[120:127], v4\nds_read_b128 v[88:91], v0 offset:30720\nv_smfmac_f32_16x16x64_bf16 v[48:51], v[92:95], v[128:135], v4\nds_read_b128 v[16:19], v7 offset:256\nv_smfmac_f32_16x16x64_bf16 v[52:55], v[92:95], v[136:143], v4\nv_smfmac_f32_16x16x64_bf16 v[56:59], v[92:95], v[144:151], v4\ns_waitcnt lgkmcnt(5)\n" \
"v_smfmac_f32_16x16x64_bf16 v[60:63], v[92:95], v[64:71], v4\nds_read_b128 v[92:95], v0 offset:31744\nds_read_b128 v[64:67], v1 offset:20480\nds_read_b128 v[68:71], v1 offset:21504\nv_smfmac_f32_16x16x64_bf16 v[48:51], v[80:83], v[152:159], v4\nds_read_b128 v[20:23], v7 offset:2304\nv_smfmac_f32_16x16x64_bf16 v[52:55], v[80:83], v[160:167], v4\nv_smfmac_f32_16x16x64_bf16 v[56:59], v[80:83], v[168:175], v4\n" \
"v_smfmac_f32_16x16x64_bf16 v[60:63], v[80:83], v[176:183], v4\ns_waitcnt lgkmcnt(8)\nds_read_b128 v[80:83], v0 offset:16384\nv_smfmac_f32_16x16x64_bf16 v[48:51], v[84:87], v[184:191], v4\ns_waitcnt lgkmcnt(7)\nds_read_b128 v[24:27], v7 offset:4352\nv_smfmac_f32_16x16x64_bf16 v[52:55], v[84:87], v[72:79], v4\nds_read_b128 v[72:75], v1 offset:22528\n" \
"ds_read_b128 v[76:79], v1 offset:23552\nv_smfmac_f32_16x16x64_bf16 v[56:59], v[84:87], v[192:199], v4\nv_smfmac_f32_16x16x64_bf16 v[60:63], v[84:87], v[200:207], v4\ns_waitcnt lgkmcnt(9)\nds_read_b128 v[84:87], v0 offset:17408\nv_smfmac_f32_16x16x64_bf16 v[48:51], v[88:91], v[208:215], v4\nds_read_b128 v[28:31], v7 offset:6400\nv_smfmac_f32_16x16x64_bf16 v[52:55], v[88:91], v[216:223], v4\n" \
"s_waitcnt lgkmcnt(7)\nv_smfmac_f32_16x16x64_bf16 v[56:59], v[88:91], v[64:71], v4\nds_read_b128 v[64:67], v1 offset:0\nds_read_b128 v[68:71], v1 offset:1024\nv_smfmac_f32_16x16x64_bf16 v[60:63], v[88:91], v[224:231], v4\nds_read_b128 v[88:91], v0 offset:18432\nv_smfmac_f32_16x16x64_bf16 v[48:51], v[92:95], v[232:239], v4\nds_read_b128 v[32:35], v7 offset:8192\n" \
"v_smfmac_f32_16x16x64_bf16 v[52:55], v[92:95], v[240:247], v4\nv_smfmac_f32_16x16x64_bf16 v[56:59], v[92:95], v[248:255], v4\ns_waitcnt lgkmcnt(6)\nv_smfmac_f32_16x16x64_bf16 v[60:63], v[92:95], v[72:79], v4\nds_read_b128 v[92:95], v0 offset:19456\nds_read_b128 v[72:75], v1 offset:2048\nds_read_b128 v[76:79], v1 offset:3072\nv_smfmac_f32_16x16x64_bf16 v[48:51], v[80:83], a[0:7], v4\n" \
"ds_read_b128 v[36:39], v7 offset:10240\nv_smfmac_f32_16x16x64_bf16 v[52:55], v[80:83], a[8:15], v4\nv_smfmac_f32_16x16x64_bf16 v[56:59], v[80:83], a[16:23], v4\nv_smfmac_f32_16x16x64_bf16 v[60:63], v[80:83], a[24:31], v4\ns_waitcnt lgkmcnt(9)\nds_read_b128 v[80:83], v0 offset:20480\nv_smfmac_f32_16x16x64_bf16 v[48:51], v[84:87], a[32:39], v4\ns_waitcnt lgkmcnt(7)\n" \
"ds_read_b128 v[40:43], v7 offset:12288\nv_smfmac_f32_16x16x64_bf16 v[52:55], v[84:87], v[64:71], v4\nds_read_b128 v[64:67], v1 offset:4096\nds_read_b128 v[68:71], v1 offset:5120\nv_smfmac_f32_16x16x64_bf16 v[56:59], v[84:87], a[40:47], v4\nv_smfmac_f32_16x16x64_bf16 v[60:63], v[84:87], a[48:55], v4\ns_waitcnt lgkmcnt(9)\nds_read_b128 v[84:87], v0 offset:21504\n" \
"v_smfmac_f32_16x16x64_bf16 v[48:51], v[88:91], a[56:63], v4\nds_read_b128 v[44:47], v7 offset:14336\nv_smfmac_f32_16x16x64_bf16 v[52:55], v[88:91], a[64:71], v4\ns_waitcnt lgkmcnt(7)\nv_smfmac_f32_16x16x64_bf16 v[56:59], v[88:91], v[72:79], v4\nds_read_b128 v[72:75], v1 offset:6144\nds_read_b128 v[76:79], v1 offset:7168\nv_smfmac_f32_16x16x64_bf16 v[60:63], v[88:91], a[72:79], v4\n" \
"ds_read_b128 v[88:91], v0 offset:22528\nv_smfmac_f32_16x16x64_bf16 v[48:51], v[92:95], a[80:87], v4\nv_smfmac_f32_16x16x64_bf16 v[52:55], v[92:95], a[88:95], v4\nv_smfmac_f32_16x16x64_bf16 v[56:59], v[92:95], a[96:103], v4\ns_waitcnt lgkmcnt(5)\nv_smfmac_f32_16x16x64_bf16 v[60:63], v[92:95], v[64:71], v4\nds_read_b128 v[92:95], v0 offset:23552\nds_read_b128 v[64:67], v1 offset:8192\n" \
"ds_read_b128 v[68:71], v1 offset:9216\nv_smfmac_f32_16x16x64_bf16 v[48:51], v[80:83], a[104:111], v4\nv_smfmac_f32_16x16x64_bf16 v[52:55], v[80:83], a[112:119], v4\nv_smfmac_f32_16x16x64_bf16 v[56:59], v[80:83], a[120:127], v4\nv_smfmac_f32_16x16x64_bf16 v[60:63], v[80:83], a[128:135], v4\ns_waitcnt lgkmcnt(7)\nv_smfmac_f32_16x16x64_bf16 v[48:51], v[84:87], a[136:143], v4\ns_waitcnt lgkmcnt(4)\n" \
"ds_read_b128 v[80:83], v7 offset:8448\nv_smfmac_f32_16x16x64_bf16 v[52:55], v[84:87], v[72:79], v4\nds_read_b128 v[72:75], v1 offset:10240\nds_read_b128 v[76:79], v1 offset:11264\nv_smfmac_f32_16x16x64_bf16 v[56:59], v[84:87], a[144:151], v4\nv_smfmac_f32_16x16x64_bf16 v[60:63], v[84:87], a[152:159], v4\ns_waitcnt lgkmcnt(6)\nv_smfmac_f32_16x16x64_bf16 v[48:51], v[88:91], a[160:167], v4\n" \
"ds_read_b128 v[84:87], v7 offset:10496\nv_smfmac_f32_16x16x64_bf16 v[52:55], v[88:91], a[168:175], v4\ns_waitcnt lgkmcnt(6)\nv_smfmac_f32_16x16x64_bf16 v[48:51], v[92:95], a[184:191], v4\nv_smfmac_f32_16x16x64_bf16 v[52:55], v[92:95], a[192:199], v4\ns_waitcnt lgkmcnt(4)\nv_smfmac_f32_16x16x64_bf16 v[56:59], v[88:91], v[64:71], v4\nds_read_b128 v[64:67], v7 offset:12544\n" \
"v_add_f32 v48, v48, v50\nv_smfmac_f32_16x16x64_bf16 v[60:63], v[88:91], a[176:183], v4\nds_read_b128 v[68:71], v7 offset:14592\nv_add_f32 v52, v52, v54\nv_add_f32 v49, v49, v51\nv_smfmac_f32_16x16x64_bf16 v[56:59], v[92:95], a[200:207], v4\ns_waitcnt lgkmcnt(3)\nv_add_f32 v53, v53, v55\n" \
"v_cvt_pk_bf16_f32 v50, v48, v52\nv_smfmac_f32_16x16x64_bf16 v[60:63], v[92:95], v[72:79], v4\nv_cvt_pk_bf16_f32 v51, v49, v53\nds_write2_b32 v6, v50, v51 offset0:0 offset1:4\ns_nop 1\nv_add_f32 v56, v56, v58\nv_add_f32 v57, v57, v59\ns_nop 1\n" \
"v_add_f32 v60, v60, v62\nv_add_f32 v61, v61, v63\nv_cvt_pk_bf16_f32 v62, v56, v60\nv_cvt_pk_bf16_f32 v63, v57, v61\nds_write2_b32 v6, v62, v63 offset0:36 offset1:40\nv_mov_b32 v61, s54\nv_mfma_f32_16x16x32_bf16 v[52:55], v[32:35], v[16:19], 0\nv_mfma_f32_16x16x32_bf16 v[52:55], v[36:39], v[20:23], v[52:55]\n" \
"v_mfma_f32_16x16x32_bf16 v[52:55], v[40:43], v[24:27], v[52:55]\nv_mfma_f32_16x16x32_bf16 v[52:55], v[44:47], v[28:31], v[52:55]\ns_waitcnt lgkmcnt(0)\ns_barrier\nds_read_b128 v[32:35], v9 offset:0\nds_read_b128 v[36:39], v9 offset:576\nds_read_b128 v[40:43], v9 offset:1152\nds_read_b128 v[44:47], v9 offset:1728\n" \
"ds_read_b32 v60, v0 offset:53760\nv_mfma_f32_16x16x32_bf16 v[56:59], v[80:83], v[16:19], v[12:15]\nv_mfma_f32_16x16x32_bf16 v[56:59], v[84:87], v[20:23], v[56:59]\nv_mfma_f32_16x16x32_bf16 v[56:59], v[64:67], v[24:27], v[56:59]\nv_mfma_f32_16x16x32_bf16 v[56:59], v[68:71], v[28:31], v[56:59]\ns_waitcnt lgkmcnt(4)\nds_read_b128 v[80:83], v2 offset:0\nds_read_b128 v[84:87], v2 offset:256\n" \
"ds_read_b128 v[88:91], v2 offset:2048\nv_mfma_f32_16x16x32_bf16 v[48:51], v[32:35], v[16:19], 0\ns_waitcnt lgkmcnt(6)\nv_mfma_f32_16x16x32_bf16 v[48:51], v[36:39], v[20:23], v[48:51]\ns_waitcnt lgkmcnt(5)\nv_mfma_f32_16x16x32_bf16 v[48:51], v[40:43], v[24:27], v[48:51]\ns_waitcnt lgkmcnt(4)\nv_mfma_f32_16x16x32_bf16 v[48:51], v[44:47], v[28:31], v[48:51]\n" \
"v_max3_f32 v64, v52, v53, v54\nv_max3_f32 v64, v64, v56, v57\nv_max3_f32 v64, v64, v58, v59\nv_max_f32 v64, v64, v55\ns_nop 3\nv_max3_f32 v65, v64, v48, v49\nv_max3_f32 v64, v64, v48, v49\ns_nop 1\n" \
"v_permlane16_swap_b32 v64, v65\nv_max_f32 v66, v64, v65\nv_max_f32 v64, v64, v65\ns_nop 1\nv_permlane32_swap_b32 v64, v66\nv_max_f32 v64, v64, v66\nv_mul_f32 v64, s59, v64\nv_fma_f32 v52, v52, s58, v64\n" \
"v_fma_f32 v53, v53, s58, v64\nv_exp_f32 v52, v52\nv_fma_f32 v54, v54, s58, v64\nv_exp_f32 v53, v53\nv_fma_f32 v55, v55, s58, v64\nv_exp_f32 v54, v54\nv_add_f32 v65, v52, v53\nv_fma_f32 v56, v56, s58, v64\n" \
"v_exp_f32 v55, v55\nv_add_f32 v65, v65, v54\nv_fma_f32 v57, v57, s58, v64\nv_exp_f32 v56, v56\nv_add_f32 v65, v65, v55\nv_fma_f32 v58, v58, s58, v64\nv_exp_f32 v57, v57\nv_add_f32 v65, v65, v56\n" \
"v_fma_f32 v59, v59, s58, v64\nv_exp_f32 v58, v58\nv_add_f32 v65, v65, v57\nv_fma_f32 v48, v48, s58, v64\nv_exp_f32 v59, v59\nv_add_f32 v65, v65, v58\nv_fma_f32 v49, v49, s58, v64\nv_exp_f32 v48, v48\n" \
"v_add_f32 v65, v65, v59\nv_exp_f32 v49, v49\nv_add_f32 v65, v65, v48\nv_add_f32 v64, v65, v49\nv_add_f32 v66, v65, v49\ns_nop 1\nv_permlane16_swap_b32 v64, v66\nv_add_f32 v65, v64, v66\n" \
"v_add_f32 v66, v64, v66\ns_nop 1\nv_permlane32_swap_b32 v65, v66\nv_add_f32 v65, v65, v66\nv_rcp_f32 v65, v65\ns_nop 0\nv_mul_f32 v52, v52, v65\nv_mul_f32 v53, v53, v65\n" \
"v_mul_f32 v54, v54, v65\nv_mul_f32 v55, v55, v65\nv_mul_f32 v56, v56, v65\nv_mul_f32 v57, v57, v65\nv_mul_f32 v58, v58, v65\nv_mul_f32 v59, v59, v65\nv_mul_f32 v48, v48, v65\nv_mul_f32 v49, v49, v65\n" \
"v_cvt_pk_bf16_f32 v52, v52, v53\nv_cvt_pk_bf16_f32 v53, v54, v55\nv_cvt_pk_bf16_f32 v48, v48, v49\nv_cvt_pk_bf16_f32 v54, v56, v57\nv_cvt_pk_bf16_f32 v55, v58, v59\nv_bfi_b32 v52, s61, v52, v61\nv_bfi_b32 v53, s61, v53, v61\nv_bfi_b32 v54, s61, v54, v61\n" \
"v_bfi_b32 v55, s61, v55, v61\nv_bfi_b32 v48, s61, v48, v61\nbuffer_store_dwordx4 v[52:55], v10, s[36:39], s55 offen sc1\nbuffer_store_dword v48, v11, s[36:39], s55 offen sc1\ns_waitcnt lgkmcnt(3)\nv_cmp_ne_u32 vcc, 0, v60\ns_cbranch_vccnz .Lqk_proj%=\ns_cmp_lt_i32 s40, 63\n" \
"s_cbranch_scc1 .Lqk_pwait%=\ns_branch .Lqk_end%=\n.Lqk_re2%=:\nv_max_u32 v40, v80, v82\nv_cmp_eq_u32 vcc, -1, v40\ns_cbranch_vccz .Lqk_sk2117%=\ns_mov_b64 exec, vcc\nbuffer_load_dwordx4 v[80:83], v3, s[36:39], s42 offen offset:0 sc1\n" \
"s_mov_b64 exec, -1\n.Lqk_sk2117%=:\nv_max_u32 v40, v84, v86\nv_cmp_eq_u32 vcc, -1, v40\ns_cbranch_vccz .Lqk_sk2124%=\ns_mov_b64 exec, vcc\nbuffer_load_dwordx4 v[84:87], v3, s[36:39], s42 offen offset:1024 sc1\ns_mov_b64 exec, -1\n" \
".Lqk_sk2124%=:\nv_max_u32 v40, v88, v90\nv_cmp_eq_u32 vcc, -1, v40\ns_cbranch_vccz .Lqk_sk2131%=\ns_mov_b64 exec, vcc\nbuffer_load_dwordx4 v[88:91], v3, s[36:39], s42 offen offset:2048 sc1\ns_mov_b64 exec, -1\n.Lqk_sk2131%=:\n" \
"v_max_u32 v40, v92, v94\nv_cmp_eq_u32 vcc, -1, v40\ns_cbranch_vccz .Lqk_sk2138%=\ns_mov_b64 exec, vcc\nbuffer_load_dwordx4 v[92:95], v3, s[36:39], s42 offen offset:3072 sc1\ns_mov_b64 exec, -1\n.Lqk_sk2138%=:\nv_mov_b64 v[48:49], 0\n" \
"v_mov_b64 v[50:51], 0\nv_mov_b64 v[52:53], 0\nv_mov_b64 v[54:55], 0\nv_mov_b64 v[56:57], 0\nv_mov_b64 v[58:59], 0\nv_mov_b64 v[60:61], 0\nv_mov_b64 v[62:63], 0\ns_add_u32 s43, s43, 1\n" \
"s_cmp_lt_u32 s43, 262144\ns_cbranch_scc1 .Lqk_poll2%=\ns_branch .Lqk_abort%=\n.Lqk_c3%=:\nds_read_b128 v[64:67], v1 offset:18432\nds_read_b128 v[68:71], v1 offset:19456\nds_read_b128 v[72:75], v1 offset:20480\nds_read_b128 v[76:79], v1 offset:21504\n" \
"s_nop 1\nv_cvt_pk_bf16_f32 v44, v40, v44\nv_cvt_pk_bf16_f32 v45, v41, v45\nv_cvt_pk_bf16_f32 v46, v42, v46\nv_cvt_pk_bf16_f32 v47, v43, v47\nds_write_b128 v5, v[44:47] offset:768\ns_add_u32 s44, s40, 1\ns_lshl_b32 s44, s44, 17\n" \
"s_add_u32 s44, s44, %[xn]\ns_lshr_b32 s45, s44, 8\ns_and_b32 s45, s45, 0x2000\ns_xor_b32 s41, s44, s45\nbuffer_load_dword v16, v12, s[36:39], s56 offen sc1\n.Lqk_poll3%=:\ns_waitcnt vmcnt(1)\nv_smfmac_f32_16x16x64_bf16 v[48:51], v[80:83], v[152:159], v4\n" \
"ds_write_b128 v0, v[80:83] offset:28672\nv_max3_u32 v40, v80, v82, v84\nv_smfmac_f32_16x16x64_bf16 v[52:55], v[80:83], v[160:167], v4\nds_write_b128 v0, v[84:87] offset:29696\nv_max3_u32 v40, v40, v86, v88\nv_smfmac_f32_16x16x64_bf16 v[56:59], v[80:83], v[168:175], v4\nds_write_b128 v0, v[88:91] offset:30720\nv_max3_u32 v40, v40, v90, v92\n" \
"v_max_u32 v40, v40, v94\nv_smfmac_f32_16x16x64_bf16 v[60:63], v[80:83], v[176:183], v4\nds_write_b128 v0, v[92:95] offset:31744\nv_cmp_eq_u32 vcc, -1, v40\nv_smfmac_f32_16x16x64_bf16 v[48:51], v[84:87], v[184:191], v4\ns_cbranch_vccnz .Lqk_re3%=\ns_waitcnt lgkmcnt(7)\nv_smfmac_f32_16x16x64_bf16 v[52:55], v[84:87], v[64:71], v4\n" \
"s_waitcnt lgkmcnt(0)\ns_barrier\nds_read_b128 v[80:83], v0 offset:16384\nds_read_b128 v[64:67], v1 offset:22528\nds_read_b128 v[68:71], v1 offset:23552\nv_smfmac_f32_16x16x64_bf16 v[56:59], v[84:87], v[192:199], v4\nv_smfmac_f32_16x16x64_bf16 v[60:63], v[84:87], v[200:207], v4\nds_read_b128 v[84:87], v0 offset:17408\n" \
"v_smfmac_f32_16x16x64_bf16 v[48:51], v[88:91], v[208:215], v4\nv_smfmac_f32_16x16x64_bf16 v[52:55], v[88:91], v[216:223], v4\nv_smfmac_f32_16x16x64_bf16 v[56:59], v[88:91], v[72:79], v4\ns_waitcnt vmcnt(0)\nds_read_b128 v[72:75], v1 offset:0\nds_read_b128 v[76:79], v1 offset:1024\nv_cmp_ge_i32 vcc, s40, v16\nv_smfmac_f32_16x16x64_bf16 v[60:63], v[88:91], v[224:231], v4\n" \
"s_cmp_eq_u64 vcc, 0\ns_cselect_b32 s61, s41, -2147483648\ns_cselect_b32 s44, 1, 0\nds_read_b128 v[88:91], v0 offset:18432\nv_smfmac_f32_16x16x64_bf16 v[48:51], v[92:95], v[232:239], v4\ns_add_u32 m0, %[l0], 0\ns_add_u32 s60, s61, 0\nbuffer_load_dwordx4 v0, s[36:39], s60 offen offset:0 sc1 lds\n" \
"v_smfmac_f32_16x16x64_bf16 v[52:55], v[92:95], v[240:247], v4\nv_mov_b32 v16, s44\nv_smfmac_f32_16x16x64_bf16 v[56:59], v[92:95], v[248:255], v4\ns_waitcnt lgkmcnt(4)\nbuffer_load_dwordx4 v0, s[36:39], s60 offen offset:1024 sc1 lds\nv_smfmac_f32_16x16x64_bf16 v[60:63], v[92:95], v[64:71], v4\nds_read_b128 v[92:95], v0 offset:19456\nds_read_b128 v[64:67], v1 offset:2048\n" \
"ds_read_b128 v[68:71], v1 offset:3072\nds_write_b32 v0, v16 offset:53760\nv_smfmac_f32_16x16x64_bf16 v[48:51], v[80:83], a[0:7], v4\nbuffer_load_dwordx4 v0, s[36:39], s60 offen offset:2048 sc1 lds\nv_smfmac_f32_16x16x64_bf16 v[52:55], v[80:83], a[8:15], v4\nv_smfmac_f32_16x16x64_bf16 v[56:59], v[80:83], a[16:23], v4\nbuffer_load_dwordx4 v0, s[36:39], s60 offen offset:3072 sc1 lds\nv_smfmac_f32_16x16x64_bf16 v[60:63], v[80:83], a[24:31], v4\n" \
"s_waitcnt lgkmcnt(7)\nds_read_b128 v[80:83], v0 offset:20480\nv_smfmac_f32_16x16x64_bf16 v[48:51], v[84:87], a[32:39], v4\ns_add_u32 m0, %[l0], 4096\ns_add_u32 s60, s61, 4096\ns_waitcnt lgkmcnt(6)\nbuffer_load_dwordx4 v0, s[36:39], s60 offen offset:0 sc1 lds\nv_smfmac_f32_16x16x64_bf16 v[52:55], v[84:87], v[72:79], v4\n" \
"ds_read_b128 v[72:75], v1 offset:4096\nds_read_b128 v[76:79], v1 offset:5120\nv_smfmac_f32_16x16x64_bf16 v[56:59], v[84:87], a[40:47], v4\nbuffer_load_dwordx4 v0, s[36:39], s60 offen offset:1024 sc1 lds\nv_smfmac_f32_16x16x64_bf16 v[60:63], v[84:87], a[48:55], v4\ns_waitcnt lgkmcnt(7)\nds_read_b128 v[84:87], v0 offset:21504\nv_smfmac_f32_16x16x64_bf16 v[48:51], v[88:91], a[56:63], v4\n" \
"buffer_load_dwordx4 v0, s[36:39], s60 offen offset:2048 sc1 lds\nv_smfmac_f32_16x16x64_bf16 v[52:55], v[88:91], a[64:71], v4\ns_waitcnt lgkmcnt(5)\nv_smfmac_f32_16x16x64_bf16 v[56:59], v[88:91], v[64:71], v4\nds_read_b128 v[64:67], v1 offset:6144\nds_read_b128 v[68:71], v1 offset:7168\nbuffer_load_dwordx4 v0, s[36:39], s60 offen offset:3072 sc1 lds\nv_smfmac_f32_16x16x64_bf16 v[60:63], v[88:91], a[72:79], v4\n" \
"ds_read_b128 v[88:91], v0 offset:22528\nv_smfmac_f32_16x16x64_bf16 v[48:51], v[92:95], a[80:87], v4\ns_add_u32 m0, %[l0], 8192\ns_add_u32 s60, s61, 16384\nbuffer_load_dwordx4 v0, s[36:39], s60 offen offset:0 sc1 lds\nv_smfmac_f32_16x16x64_bf16 v[52:55], v[92:95], a[88:95], v4\nv_smfmac_f32_16x16x64_bf16 v[56:59], v[92:95], a[96:103], v4\ns_waitcnt lgkmcnt(4)\n" \
"buffer_load_dwordx4 v0, s[36:39], s60 offen offset:1024 sc1 lds\nv_smfmac_f32_16x16x64_bf16 v[60:63], v[92:95], v[72:79], v4\nds_read_b128 v[92:95], v0 offset:23552\nds_read_b128 v[72:75], v1 offset:8192\nds_read_b128 v[76:79], v1 offset:9216\nv_smfmac_f32_16x16x64_bf16 v[48:51], v[80:83], a[104:111], v4\nbuffer_load_dwordx4 v0, s[36:39], s60 offen offset:2048 sc1 lds\nv_smfmac_f32_16x16x64_bf16 v[52:55], v[80:83], a[112:119], v4\n" \
"v_smfmac_f32_16x16x64_bf16 v[56:59], v[80:83], a[120:127], v4\nbuffer_load_dwordx4 v0, s[36:39], s60 offen offset:3072 sc1 lds\nv_smfmac_f32_16x16x64_bf16 v[60:63], v[80:83], a[128:135], v4\ns_waitcnt lgkmcnt(6)\nds_read_b128 v[80:83], v0 offset:24576\nv_smfmac_f32_16x16x64_bf16 v[48:51], v[84:87], a[136:143], v4\ns_add_u32 m0, %[l0], 12288\ns_add_u32 s60, s61, 20480\n" \
"s_waitcnt lgkmcnt(5)\nbuffer_load_dwordx4 v0, s[36:39], s60 offen offset:0 sc1 lds\nv_smfmac_f32_16x16x64_bf16 v[52:55], v[84:87], v[64:71], v4\nds_read_b128 v[64:67], v1 offset:10240\nds_read_b128 v[68:71], v1 offset:11264\nv_smfmac_f32_16x16x64_bf16 v[56:59], v[84:87], a[144:151], v4\nbuffer_load_dwordx4 v0, s[36:39], s60 offen offset:1024 sc1 lds\nv_smfmac_f32_16x16x64_bf16 v[60:63], v[84:87], a[152:159], v4\n" \
"s_waitcnt lgkmcnt(6)\nds_read_b128 v[84:87], v0 offset:25600\nv_smfmac_f32_16x16x64_bf16 v[48:51], v[88:91], a[160:167], v4\nbuffer_load_dwordx4 v0, s[36:39], s60 offen offset:2048 sc1 lds\nv_smfmac_f32_16x16x64_bf16 v[52:55], v[88:91], a[168:175], v4\ns_waitcnt lgkmcnt(4)\nv_smfmac_f32_16x16x64_bf16 v[56:59], v[88:91], v[72:79], v4\nds_read_b128 v[72:75], v1 offset:12288\n" \
"ds_read_b128 v[76:79], v1 offset:13312\nbuffer_load_dwordx4 v0, s[36:39], s60 offen offset:3072 sc1 lds\nv_smfmac_f32_16x16x64_bf16 v[60:63], v[88:91], a[176:183], v4\nds_read_b128 v[88:91], v0 offset:26624\nv_smfmac_f32_16x16x64_bf16 v[48:51], v[92:95], a[184:191], v4\nv_smfmac_f32_16x16x64_bf16 v[52:55], v[92:95], a[192:199], v4\nv_smfmac_f32_16x16x64_bf16 v[56:59], v[92:95], a[200:207], v4\ns_waitcnt lgkmcnt(4)\n" \
"v_smfmac_f32_16x16x64_bf16 v[60:63], v[92:95], v[64:71], v4\nds_read_b128 v[92:95], v0 offset:27648\nds_read_b128 v[64:67], v1 offset:14336\nds_read_b128 v[68:71], v1 offset:15360\nv_smfmac_f32_16x16x64_bf16 v[48:51], v[80:83], a[208:215], v4\nv_smfmac_f32_16x16x64_bf16 v[52:55], v[80:83], a[216:223], v4\nv_smfmac_f32_16x16x64_bf16 v[56:59], v[80:83], a[224:231], v4\nv_smfmac_f32_16x16x64_bf16 v[60:63], v[80:83], a[232:239], v4\n" \
"s_waitcnt lgkmcnt(6)\nv_smfmac_f32_16x16x64_bf16 v[48:51], v[84:87], a[240:247], v4\ns_waitcnt lgkmcnt(4)\nv_smfmac_f32_16x16x64_bf16 v[52:55], v[84:87], v[72:79], v4\nds_read_b128 v[72:75], v1 offset:16384\nds_read_b128 v[76:79], v1 offset:17408\nv_smfmac_f32_16x16x64_bf16 v[56:59], v[84:87], a[248:255], v4\nv_smfmac_f32_16x16x64_bf16 v[60:63], v[84:87], v[96:103], v4\n" \
"s_waitcnt lgkmcnt(5)\nv_smfmac_f32_16x16x64_bf16 v[48:51], v[88:91], v[104:111], v4\nv_smfmac_f32_16x16x64_bf16 v[52:55], v[88:91], v[112:119], v4\ns_waitcnt lgkmcnt(4)\nv_smfmac_f32_16x16x64_bf16 v[48:51], v[92:95], v[128:135], v4\nv_smfmac_f32_16x16x64_bf16 v[52:55], v[92:95], v[136:143], v4\ns_waitcnt lgkmcnt(2)\nv_smfmac_f32_16x16x64_bf16 v[56:59], v[88:91], v[64:71], v4\n" \
"v_add_f32 v48, v48, v50\nv_smfmac_f32_16x16x64_bf16 v[60:63], v[88:91], v[120:127], v4\nv_add_f32 v52, v52, v54\nv_add_f32 v49, v49, v51\nv_smfmac_f32_16x16x64_bf16 v[56:59], v[92:95], v[144:151], v4\ns_waitcnt lgkmcnt(0)\nv_add_f32 v53, v53, v55\nv_cvt_pk_bf16_f32 v50, v48, v52\n" \
"v_smfmac_f32_16x16x64_bf16 v[60:63], v[92:95], v[72:79], v4\nv_cvt_pk_bf16_f32 v51, v49, v53\nds_write2_b32 v6, v50, v51 offset0:0 offset1:4\ns_nop 1\nv_add_f32 v56, v56, v58\nv_add_f32 v57, v57, v59\ns_nop 1\nv_add_f32 v60, v60, v62\n" \
"v_add_f32 v61, v61, v63\nv_cvt_pk_bf16_f32 v62, v56, v60\nv_cvt_pk_bf16_f32 v63, v57, v61\nds_write2_b32 v6, v62, v63 offset0:36 offset1:40\ns_waitcnt vmcnt(0)\ns_waitcnt lgkmcnt(0)\ns_barrier\ns_cbranch_vccnz .Lqk_slow%=\n" \
"s_add_u32 m0, %[l0], 16384\ns_add_u32 s60, s41, 32768\nbuffer_load_dwordx4 v0, s[36:39], s60 offen offset:0 sc1 lds\nbuffer_load_dwordx4 v0, s[36:39], s60 offen offset:1024 sc1 lds\nbuffer_load_dwordx4 v0, s[36:39], s60 offen offset:2048 sc1 lds\nbuffer_load_dwordx4 v0, s[36:39], s60 offen offset:3072 sc1 lds\ns_add_u32 m0, %[l0], 20480\ns_add_u32 s60, s41, 36864\n" \
"buffer_load_dwordx4 v0, s[36:39], s60 offen offset:0 sc1 lds\nbuffer_load_dwordx4 v0, s[36:39], s60 offen offset:1024 sc1 lds\nbuffer_load_dwordx4 v0, s[36:39], s60 offen offset:2048 sc1 lds\nbuffer_load_dwordx4 v0, s[36:39], s60 offen offset:3072 sc1 lds\ns_add_u32 m0, %[l0], 24576\ns_add_u32 s60, s41, 49152\nbuffer_load_dwordx4 v0, s[36:39], s60 offen offset:0 sc1 lds\nbuffer_load_dwordx4 v0, s[36:39], s60 offen offset:1024 sc1 lds\n" \
"buffer_load_dwordx4 v0, s[36:39], s60 offen offset:2048 sc1 lds\nbuffer_load_dwordx4 v0, s[36:39], s60 offen offset:3072 sc1 lds\ns_add_u32 m0, %[l0], 28672\ns_add_u32 s60, s41, 53248\nbuffer_load_dwordx4 v0, s[36:39], s60 offen offset:0 sc1 lds\nbuffer_load_dwordx4 v0, s[36:39], s60 offen offset:1024 sc1 lds\nbuffer_load_dwordx4 v0, s[36:39], s60 offen offset:2048 sc1 lds\nbuffer_load_dwordx4 v0, s[36:39], s60 offen offset:3072 sc1 lds\n" \
"ds_read_b128 v[80:83], v2 offset:0\nds_read_b128 v[84:87], v2 offset:256\nds_read_b128 v[88:91], v2 offset:2048\ns_branch .Lqk_proj%=\n.Lqk_slow%=:\ns_cmp_ge_i32 s40, 63\ns_cbranch_scc1 .Lqk_end%=\n.Lqk_fetch0%=:\n" \
"buffer_load_dword v16, v12, s[36:39], s56 offen sc1\ns_waitcnt vmcnt(0)\nv_cmp_ge_i32 vcc, s40, v16\ns_cbranch_vccz .Lqk_fok%=\ns_add_u32 s43, s43, 1\ns_cmp_lt_u32 s43, 262144\ns_cbranch_scc1 .Lqk_fetch0%=\ns_branch .Lqk_abort%=\n" \
".Lqk_fok%=:\ns_add_u32 s44, s40, 1\ns_lshl_b32 s44, s44, 17\ns_add_u32 s44, s44, %[xn]\ns_lshr_b32 s45, s44, 8\ns_and_b32 s45, s45, 0x2000\ns_xor_b32 s41, s44, s45\ns_add_u32 m0, %[l0], 0\n" \
"s_add_u32 s60, s41, 0\nbuffer_load_dwordx4 v0, s[36:39], s60 offen offset:0 sc1 lds\nbuffer_load_dwordx4 v0, s[36:39], s60 offen offset:1024 sc1 lds\nbuffer_load_dwordx4 v0, s[36:39], s60 offen offset:2048 sc1 lds\nbuffer_load_dwordx4 v0, s[36:39], s60 offen offset:3072 sc1 lds\ns_add_u32 m0, %[l0], 4096\ns_add_u32 s60, s41, 4096\nbuffer_load_dwordx4 v0, s[36:39], s60 offen offset:0 sc1 lds\n" \
"buffer_load_dwordx4 v0, s[36:39], s60 offen offset:1024 sc1 lds\nbuffer_load_dwordx4 v0, s[36:39], s60 offen offset:2048 sc1 lds\nbuffer_load_dwordx4 v0, s[36:39], s60 offen offset:3072 sc1 lds\ns_add_u32 m0, %[l0], 8192\ns_add_u32 s60, s41, 16384\nbuffer_load_dwordx4 v0, s[36:39], s60 offen offset:0 sc1 lds\nbuffer_load_dwordx4 v0, s[36:39], s60 offen offset:1024 sc1 lds\nbuffer_load_dwordx4 v0, s[36:39], s60 offen offset:2048 sc1 lds\n" \
"buffer_load_dwordx4 v0, s[36:39], s60 offen offset:3072 sc1 lds\ns_add_u32 m0, %[l0], 12288\ns_add_u32 s60, s41, 20480\nbuffer_load_dwordx4 v0, s[36:39], s60 offen offset:0 sc1 lds\nbuffer_load_dwordx4 v0, s[36:39], s60 offen offset:1024 sc1 lds\nbuffer_load_dwordx4 v0, s[36:39], s60 offen offset:2048 sc1 lds\nbuffer_load_dwordx4 v0, s[36:39], s60 offen offset:3072 sc1 lds\ns_add_u32 m0, %[l0], 16384\n" \
"s_add_u32 s60, s41, 32768\nbuffer_load_dwordx4 v0, s[36:39], s60 offen offset:0 sc1 lds\nbuffer_load_dwordx4 v0, s[36:39], s60 offen offset:1024 sc1 lds\nbuffer_load_dwordx4 v0, s[36:39], s60 offen offset:2048 sc1 lds\nbuffer_load_dwordx4 v0, s[36:39], s60 offen offset:3072 sc1 lds\ns_add_u32 m0, %[l0], 20480\ns_add_u32 s60, s41, 36864\nbuffer_load_dwordx4 v0, s[36:39], s60 offen offset:0 sc1 lds\n" \
"buffer_load_dwordx4 v0, s[36:39], s60 offen offset:1024 sc1 lds\nbuffer_load_dwordx4 v0, s[36:39], s60 offen offset:2048 sc1 lds\nbuffer_load_dwordx4 v0, s[36:39], s60 offen offset:3072 sc1 lds\ns_add_u32 m0, %[l0], 24576\ns_add_u32 s60, s41, 49152\nbuffer_load_dwordx4 v0, s[36:39], s60 offen offset:0 sc1 lds\nbuffer_load_dwordx4 v0, s[36:39], s60 offen offset:1024 sc1 lds\nbuffer_load_dwordx4 v0, s[36:39], s60 offen offset:2048 sc1 lds\n" \
"buffer_load_dwordx4 v0, s[36:39], s60 offen offset:3072 sc1 lds\ns_add_u32 m0, %[l0], 28672\ns_add_u32 s60, s41, 53248\nbuffer_load_dwordx4 v0, s[36:39], s60 offen offset:0 sc1 lds\nbuffer_load_dwordx4 v0, s[36:39], s60 offen offset:1024 sc1 lds\nbuffer_load_dwordx4 v0, s[36:39], s60 offen offset:2048 sc1 lds\nbuffer_load_dwordx4 v0, s[36:39], s60 offen offset:3072 sc1 lds\ns_waitcnt vmcnt(16)\n" \
"s_branch .Lqk_pwait%=\n.Lqk_re3%=:\nv_max_u32 v40, v80, v82\nv_cmp_eq_u32 vcc, -1, v40\ns_cbranch_vccz .Lqk_sk2450%=\ns_mov_b64 exec, vcc\nbuffer_load_dwordx4 v[80:83], v3, s[36:39], s42 offen offset:0 sc1\ns_mov_b64 exec, -1\n" \
".Lqk_sk2450%=:\nv_max_u32 v40, v84, v86\nv_cmp_eq_u32 vcc, -1, v40\ns_cbranch_vccz .Lqk_sk2457%=\ns_mov_b64 exec, vcc\nbuffer_load_dwordx4 v[84:87], v3, s[36:39], s42 offen offset:1024 sc1\ns_mov_b64 exec, -1\n.Lqk_sk2457%=:\n" \
"v_max_u32 v40, v88, v90\nv_cmp_eq_u32 vcc, -1, v40\ns_cbranch_vccz .Lqk_sk2464%=\ns_mov_b64 exec, vcc\nbuffer_load_dwordx4 v[88:91], v3, s[36:39], s42 offen offset:2048 sc1\ns_mov_b64 exec, -1\n.Lqk_sk2464%=:\nv_max_u32 v40, v92, v94\n" \
"v_cmp_eq_u32 vcc, -1, v40\ns_cbranch_vccz .Lqk_sk2471%=\ns_mov_b64 exec, vcc\nbuffer_load_dwordx4 v[92:95], v3, s[36:39], s42 offen offset:3072 sc1\ns_mov_b64 exec, -1\n.Lqk_sk2471%=:\nbuffer_load_dword v16, v12, s[36:39], s56 offen sc1\nv_mov_b64 v[48:49], 0\n" \
"v_mov_b64 v[50:51], 0\nv_mov_b64 v[52:53], 0\nv_mov_b64 v[54:55], 0\nv_mov_b64 v[56:57], 0\nv_mov_b64 v[58:59], 0\nv_mov_b64 v[60:61], 0\nv_mov_b64 v[62:63], 0\ns_add_u32 s43, s43, 1\n" \
"s_cmp_lt_u32 s43, 262144\ns_cbranch_scc1 .Lqk_poll3%=\ns_branch .Lqk_abort%=\n.Lqk_abort%=:\ns_mov_b64 exec, -1\nv_mov_b32 v16, 0\ns_movk_i32 s44, 6400\nbuffer_store_dword v16, off, s[36:39], s44 sc1\n" \
".Lqk_end%=:\ns_waitcnt vmcnt(0) lgkmcnt(0)\n" \
""
#define VU_ASM \
"v_mbcnt_lo_u32_b32 v255, -1, 0\nv_mbcnt_hi_u32_b32 v255, -1, v255\nv_lshlrev_b32 v255, 2, v255\nv_add_u32 v255, %[tb], v255\nds_read_b32 v0, v255 offset:0\nds_read_b32 v1, v255 offset:256\nds_read_b32 v2, v255 offset:512\nds_read_b32 v3, v255 offset:768\n" \
"ds_read_b32 v4, v255 offset:1024\nds_read_b32 v5, v255 offset:1280\nds_read_b32 v6, v255 offset:1536\nds_read_b32 v7, v255 offset:1792\nds_read_b32 v8, v255 offset:2048\nds_read_b32 v9, v255 offset:2304\nds_read_b32 v10, v255 offset:2560\nds_read_b32 v11, v255 offset:2816\n" \
"ds_read_b32 v90, v255 offset:3072\nds_read_b32 v91, v255 offset:3328\nds_read_b32 v92, v255 offset:3584\nds_read_b32 v93, v255 offset:3840\nds_read_b32 v94, v255 offset:4096\nds_read_b32 v95, v255 offset:4352\ns_waitcnt lgkmcnt(0)\ns_mov_b64 s[36:37], %[ws]\n" \
"s_mov_b32 s38, %[sz]\ns_mov_b32 s39, 0x20000\ns_mov_b32 s45, 2096896\nbuffer_load_dword v15, off, s[36:39], s45 sc1\ns_add_u32 m0, %[lw], 16384\ns_add_u32 s44, %[wo], 0\nbuffer_load_dwordx4 v90, %[rw], s44 offen nt lds\ns_add_u32 m0, %[lw], 17408\n" \
"s_add_u32 s44, %[wo], 65536\nbuffer_load_dwordx4 v90, %[rw], s44 offen nt lds\ns_add_u32 m0, %[lw], 18432\ns_add_u32 s44, %[wo], 131072\nbuffer_load_dwordx4 v90, %[rw], s44 offen nt lds\ns_add_u32 m0, %[lw], 19456\ns_add_u32 s44, %[wo], 196608\nbuffer_load_dwordx4 v90, %[rw], s44 offen nt lds\n" \
"s_add_u32 m0, %[lw], 20480\ns_add_u32 s44, %[wo], 524288\nbuffer_load_dwordx4 v90, %[rw], s44 offen nt lds\ns_add_u32 m0, %[lw], 21504\ns_add_u32 s44, %[wo], 589824\nbuffer_load_dwordx4 v90, %[rw], s44 offen nt lds\ns_add_u32 m0, %[lw], 22528\ns_add_u32 s44, %[wo], 655360\n" \
"buffer_load_dwordx4 v90, %[rw], s44 offen nt lds\ns_add_u32 m0, %[lw], 23552\ns_add_u32 s44, %[wo], 720896\nbuffer_load_dwordx4 v90, %[rw], s44 offen nt lds\ns_add_u32 m0, %[tb], 0\ns_add_u32 s44, %[wo], 1048576\nbuffer_load_dwordx4 v90, %[rw], s44 offen nt lds\ns_add_u32 m0, %[tb], 1024\n" \
"s_add_u32 s44, %[wo], 1114112\nbuffer_load_dwordx4 v90, %[rw], s44 offen nt lds\ns_add_u32 m0, %[tb], 2048\ns_add_u32 s44, %[wo], 1179648\nbuffer_load_dwordx4 v90, %[rw], s44 offen nt lds\ns_add_u32 m0, %[tb], 3072\ns_add_u32 s44, %[wo], 1245184\nbuffer_load_dwordx4 v90, %[rw], s44 offen nt lds\n" \
"s_add_u32 m0, %[tb], 4096\ns_add_u32 s44, %[wo], 1572864\nbuffer_load_dwordx4 v90, %[rw], s44 offen nt lds\ns_add_u32 m0, %[tb], 5120\ns_add_u32 s44, %[wo], 1638400\nbuffer_load_dwordx4 v90, %[rw], s44 offen nt lds\ns_add_u32 m0, %[tb], 6144\ns_add_u32 s44, %[wo], 1703936\n" \
"buffer_load_dwordx4 v90, %[rw], s44 offen nt lds\ns_add_u32 m0, %[tb], 7168\ns_add_u32 s44, %[wo], 1769472\nbuffer_load_dwordx4 v90, %[rw], s44 offen nt lds\ns_add_u32 m0, %[tb], 8192\ns_add_u32 s44, %[wo], 2097152\nbuffer_load_dwordx4 v90, %[rw], s44 offen nt lds\ns_add_u32 m0, %[tb], 9216\n" \
"s_add_u32 s44, %[wo], 2162688\nbuffer_load_dwordx4 v90, %[rw], s44 offen nt lds\ns_add_u32 m0, %[tb], 10240\ns_add_u32 s44, %[wo], 2228224\nbuffer_load_dwordx4 v90, %[rw], s44 offen nt lds\ns_add_u32 m0, %[tb], 11264\ns_add_u32 s44, %[wo], 2293760\nbuffer_load_dwordx4 v90, %[rw], s44 offen nt lds\n" \
"s_add_u32 m0, %[lw], 0\ns_add_u32 s44, %[wo], 2621440\nbuffer_load_dwordx4 v90, %[rw], s44 offen nt lds\ns_add_u32 m0, %[lw], 1024\ns_add_u32 s44, %[wo], 2686976\nbuffer_load_dwordx4 v90, %[rw], s44 offen nt lds\ns_add_u32 m0, %[lw], 2048\ns_add_u32 s44, %[wo], 2752512\n" \
"buffer_load_dwordx4 v90, %[rw], s44 offen nt lds\ns_add_u32 m0, %[lw], 3072\ns_add_u32 s44, %[wo], 2818048\nbuffer_load_dwordx4 v90, %[rw], s44 offen nt lds\ns_add_u32 m0, %[lw], 4096\ns_add_u32 s44, %[wo], 3145728\nbuffer_load_dwordx4 v90, %[rw], s44 offen nt lds\ns_add_u32 m0, %[lw], 5120\n" \
"s_add_u32 s44, %[wo], 3211264\nbuffer_load_dwordx4 v90, %[rw], s44 offen nt lds\ns_add_u32 m0, %[lw], 6144\ns_add_u32 s44, %[wo], 3276800\nbuffer_load_dwordx4 v90, %[rw], s44 offen nt lds\ns_add_u32 m0, %[lw], 7168\ns_add_u32 s44, %[wo], 3342336\nbuffer_load_dwordx4 v90, %[rw], s44 offen nt lds\n" \
"s_add_u32 m0, %[lw], 8192\ns_add_u32 s44, %[wo], 3670016\nbuffer_load_dwordx4 v90, %[rw], s44 offen nt lds\ns_add_u32 m0, %[lw], 9216\ns_add_u32 s44, %[wo], 3735552\nbuffer_load_dwordx4 v90, %[rw], s44 offen nt lds\ns_add_u32 m0, %[lw], 10240\ns_add_u32 s44, %[wo], 3801088\n" \
"buffer_load_dwordx4 v90, %[rw], s44 offen nt lds\ns_add_u32 m0, %[lw], 11264\ns_add_u32 s44, %[wo], 3866624\nbuffer_load_dwordx4 v90, %[rw], s44 offen nt lds\ns_waitcnt vmcnt(28)\ns_barrier\ns_add_u32 m0, %[lw], 12288\ns_add_u32 s44, %[wo], 4194304\n" \
"buffer_load_dwordx4 v90, %[rw], s44 offen nt lds\ns_add_u32 m0, %[lw], 13312\ns_add_u32 s44, %[wo], 4259840\nbuffer_load_dwordx4 v90, %[rw], s44 offen nt lds\ns_add_u32 m0, %[lw], 14336\ns_add_u32 s44, %[wo], 4325376\nbuffer_load_dwordx4 v90, %[rw], s44 offen nt lds\ns_add_u32 m0, %[lw], 15360\n" \
"s_add_u32 s44, %[wo], 4390912\nbuffer_load_dwordx4 v90, %[rw], s44 offen nt lds\nds_read_b64_tr_b16 a[0:1], v93 offset:16384\nds_read_b64_tr_b16 a[2:3], v93 offset:16896\nds_read_b64_tr_b16 a[16:17], v93 offset:16392\nds_read_b64_tr_b16 a[18:19], v93 offset:16904\nds_read_b64_tr_b16 a[4:5], v93 offset:40960\nds_read_b64_tr_b16 a[6:7], v93 offset:41472\n" \
"ds_read_b64_tr_b16 a[20:21], v93 offset:40968\nds_read_b64_tr_b16 a[22:23], v93 offset:41480\ns_waitcnt lgkmcnt(0)\ns_waitcnt vmcnt(28)\ns_barrier\ns_add_u32 m0, %[lw], 16384\ns_add_u32 s44, %[wo], 4718592\nbuffer_load_dwordx4 v90, %[rw], s44 offen nt lds\n" \
"s_add_u32 m0, %[lw], 17408\ns_add_u32 s44, %[wo], 4784128\nbuffer_load_dwordx4 v90, %[rw], s44 offen nt lds\ns_add_u32 m0, %[lw], 18432\ns_add_u32 s44, %[wo], 4849664\nbuffer_load_dwordx4 v90, %[rw], s44 offen nt lds\ns_add_u32 m0, %[lw], 19456\ns_add_u32 s44, %[wo], 4915200\n" \
"buffer_load_dwordx4 v90, %[rw], s44 offen nt lds\nds_read_b64_tr_b16 a[32:33], v93 offset:20480\nds_read_b64_tr_b16 a[34:35], v93 offset:20992\nds_read_b64_tr_b16 a[40:41], v93 offset:20488\nds_read_b64_tr_b16 a[42:43], v93 offset:21000\nds_read_b64_tr_b16 a[36:37], v93 offset:45056\nds_read_b64_tr_b16 a[38:39], v93 offset:45568\nds_read_b64_tr_b16 a[44:45], v93 offset:45064\n" \
"ds_read_b64_tr_b16 a[46:47], v93 offset:45576\ns_waitcnt lgkmcnt(0)\ns_waitcnt vmcnt(28)\ns_barrier\ns_add_u32 m0, %[lw], 20480\ns_add_u32 s44, %[wo], 5242880\nbuffer_load_dwordx4 v90, %[rw], s44 offen nt lds\ns_add_u32 m0, %[lw], 21504\n" \
"s_add_u32 s44, %[wo], 5308416\nbuffer_load_dwordx4 v90, %[rw], s44 offen nt lds\ns_add_u32 m0, %[lw], 22528\ns_add_u32 s44, %[wo], 5373952\nbuffer_load_dwordx4 v90, %[rw], s44 offen nt lds\ns_add_u32 m0, %[lw], 23552\ns_add_u32 s44, %[wo], 5439488\nbuffer_load_dwordx4 v90, %[rw], s44 offen nt lds\n" \
"ds_read_b64_tr_b16 a[56:57], v92 offset:0\nds_read_b64_tr_b16 a[58:59], v92 offset:512\nds_read_b64_tr_b16 v[84:85], v92 offset:8\nds_read_b64_tr_b16 v[86:87], v92 offset:520\nds_read_b64_tr_b16 a[60:61], v92 offset:12288\nds_read_b64_tr_b16 a[62:63], v92 offset:12800\nds_read_b64_tr_b16 v[80:81], v92 offset:12296\nds_read_b64_tr_b16 v[82:83], v92 offset:12808\n" \
"s_waitcnt lgkmcnt(0)\ns_waitcnt vmcnt(28)\ns_barrier\ns_add_u32 m0, %[tb], 0\ns_add_u32 s44, %[wo], 5767168\nbuffer_load_dwordx4 v90, %[rw], s44 offen nt lds\ns_add_u32 m0, %[tb], 1024\ns_add_u32 s44, %[wo], 5832704\n" \
"buffer_load_dwordx4 v90, %[rw], s44 offen nt lds\ns_add_u32 m0, %[tb], 2048\ns_add_u32 s44, %[wo], 5898240\nbuffer_load_dwordx4 v90, %[rw], s44 offen nt lds\ns_add_u32 m0, %[tb], 3072\ns_add_u32 s44, %[wo], 5963776\nbuffer_load_dwordx4 v90, %[rw], s44 offen nt lds\nds_read_b64_tr_b16 a[80:81], v92 offset:4096\n" \
"ds_read_b64_tr_b16 a[82:83], v92 offset:4608\nds_read_b64_tr_b16 a[88:89], v92 offset:4104\nds_read_b64_tr_b16 a[90:91], v92 offset:4616\nds_read_b64_tr_b16 a[84:85], v92 offset:16384\nds_read_b64_tr_b16 a[86:87], v92 offset:16896\nds_read_b64_tr_b16 a[92:93], v92 offset:16392\nds_read_b64_tr_b16 a[94:95], v92 offset:16904\ns_waitcnt lgkmcnt(0)\n" \
"s_waitcnt vmcnt(28)\ns_barrier\ns_add_u32 m0, %[tb], 4096\ns_add_u32 s44, %[wo], 6291456\nbuffer_load_dwordx4 v90, %[rw], s44 offen nt lds\ns_add_u32 m0, %[tb], 5120\ns_add_u32 s44, %[wo], 6356992\nbuffer_load_dwordx4 v90, %[rw], s44 offen nt lds\n" \
"s_add_u32 m0, %[tb], 6144\ns_add_u32 s44, %[wo], 6422528\nbuffer_load_dwordx4 v90, %[rw], s44 offen nt lds\ns_add_u32 m0, %[tb], 7168\ns_add_u32 s44, %[wo], 6488064\nbuffer_load_dwordx4 v90, %[rw], s44 offen nt lds\nds_read_b64_tr_b16 a[104:105], v92 offset:8192\nds_read_b64_tr_b16 a[106:107], v92 offset:8704\n" \
"ds_read_b64_tr_b16 a[120:121], v92 offset:8200\nds_read_b64_tr_b16 a[122:123], v92 offset:8712\nds_read_b64_tr_b16 a[108:109], v92 offset:20480\nds_read_b64_tr_b16 a[110:111], v92 offset:20992\nds_read_b64_tr_b16 a[124:125], v92 offset:20488\nds_read_b64_tr_b16 a[126:127], v92 offset:21000\ns_waitcnt lgkmcnt(0)\ns_waitcnt vmcnt(28)\n" \
"s_barrier\ns_add_u32 m0, %[tb], 8192\ns_add_u32 s44, %[wo], 6815744\nbuffer_load_dwordx4 v90, %[rw], s44 offen nt lds\ns_add_u32 m0, %[tb], 9216\ns_add_u32 s44, %[wo], 6881280\nbuffer_load_dwordx4 v90, %[rw], s44 offen nt lds\ns_add_u32 m0, %[tb], 10240\n" \
"s_add_u32 s44, %[wo], 6946816\nbuffer_load_dwordx4 v90, %[rw], s44 offen nt lds\ns_add_u32 m0, %[tb], 11264\ns_add_u32 s44, %[wo], 7012352\nbuffer_load_dwordx4 v90, %[rw], s44 offen nt lds\nds_read_b64_tr_b16 a[136:137], v93 offset:0\nds_read_b64_tr_b16 a[138:139], v93 offset:512\nds_read_b64_tr_b16 a[144:145], v93 offset:8\n" \
"ds_read_b64_tr_b16 a[146:147], v93 offset:520\nds_read_b64_tr_b16 a[140:141], v93 offset:24576\nds_read_b64_tr_b16 a[142:143], v93 offset:25088\nds_read_b64_tr_b16 a[148:149], v93 offset:24584\nds_read_b64_tr_b16 a[150:151], v93 offset:25096\ns_waitcnt lgkmcnt(0)\ns_waitcnt vmcnt(28)\ns_barrier\n" \
"s_add_u32 m0, %[lw], 0\ns_add_u32 s44, %[wo], 7340032\nbuffer_load_dwordx4 v90, %[rw], s44 offen nt lds\ns_add_u32 m0, %[lw], 1024\ns_add_u32 s44, %[wo], 7405568\nbuffer_load_dwordx4 v90, %[rw], s44 offen nt lds\ns_add_u32 m0, %[lw], 2048\ns_add_u32 s44, %[wo], 7471104\n" \
"buffer_load_dwordx4 v90, %[rw], s44 offen nt lds\ns_add_u32 m0, %[lw], 3072\ns_add_u32 s44, %[wo], 7536640\nbuffer_load_dwordx4 v90, %[rw], s44 offen nt lds\nds_read_b64_tr_b16 a[160:161], v93 offset:4096\nds_read_b64_tr_b16 a[162:163], v93 offset:4608\nds_read_b64_tr_b16 v[76:77], v93 offset:4104\nds_read_b64_tr_b16 v[78:79], v93 offset:4616\n" \
"ds_read_b64_tr_b16 a[164:165], v93 offset:28672\nds_read_b64_tr_b16 a[166:167], v93 offset:29184\nds_read_b64_tr_b16 v[72:73], v93 offset:28680\nds_read_b64_tr_b16 v[74:75], v93 offset:29192\ns_waitcnt lgkmcnt(0)\ns_waitcnt vmcnt(28)\ns_barrier\ns_add_u32 m0, %[lw], 4096\n" \
"s_add_u32 s44, %[wo], 7864320\nbuffer_load_dwordx4 v90, %[rw], s44 offen nt lds\ns_add_u32 m0, %[lw], 5120\ns_add_u32 s44, %[wo], 7929856\nbuffer_load_dwordx4 v90, %[rw], s44 offen nt lds\ns_add_u32 m0, %[lw], 6144\ns_add_u32 s44, %[wo], 7995392\nbuffer_load_dwordx4 v90, %[rw], s44 offen nt lds\n" \
"s_add_u32 m0, %[lw], 7168\ns_add_u32 s44, %[wo], 8060928\nbuffer_load_dwordx4 v90, %[rw], s44 offen nt lds\nds_read_b64_tr_b16 a[184:185], v93 offset:8192\nds_read_b64_tr_b16 a[186:187], v93 offset:8704\nds_read_b64_tr_b16 a[192:193], v93 offset:8200\nds_read_b64_tr_b16 a[194:195], v93 offset:8712\nds_read_b64_tr_b16 a[188:189], v93 offset:32768\n" \
"ds_read_b64_tr_b16 a[190:191], v93 offset:33280\nds_read_b64_tr_b16 a[196:197], v93 offset:32776\nds_read_b64_tr_b16 a[198:199], v93 offset:33288\ns_waitcnt lgkmcnt(0)\ns_waitcnt vmcnt(28)\ns_barrier\ns_add_u32 m0, %[lw], 8192\ns_add_u32 s44, %[w2], 0\n" \
"buffer_load_dwordx4 v91, %[ro], s44 offen nt lds\ns_add_u32 m0, %[lw], 9216\ns_add_u32 s44, %[w2], 16384\nbuffer_load_dwordx4 v91, %[ro], s44 offen nt lds\ns_add_u32 m0, %[lw], 10240\ns_add_u32 s44, %[w2], 32768\nbuffer_load_dwordx4 v91, %[ro], s44 offen nt lds\ns_add_u32 m0, %[lw], 11264\n" \
"s_add_u32 s44, %[w2], 49152\nbuffer_load_dwordx4 v91, %[ro], s44 offen nt lds\nds_read_b64_tr_b16 a[208:209], v93 offset:12288\nds_read_b64_tr_b16 a[210:211], v93 offset:12800\nds_read_b64_tr_b16 a[224:225], v93 offset:12296\nds_read_b64_tr_b16 a[226:227], v93 offset:12808\nds_read_b64_tr_b16 a[212:213], v93 offset:36864\nds_read_b64_tr_b16 a[214:215], v93 offset:37376\n" \
"ds_read_b64_tr_b16 a[228:229], v93 offset:36872\nds_read_b64_tr_b16 a[230:231], v93 offset:37384\ns_waitcnt lgkmcnt(0)\ns_waitcnt vmcnt(28)\ns_barrier\ns_add_u32 m0, %[lw], 12288\ns_add_u32 s44, %[w2], 128\nbuffer_load_dwordx4 v91, %[ro], s44 offen nt lds\n" \
"s_add_u32 m0, %[lw], 13312\ns_add_u32 s44, %[w2], 16512\nbuffer_load_dwordx4 v91, %[ro], s44 offen nt lds\ns_add_u32 m0, %[lw], 14336\ns_add_u32 s44, %[w2], 32896\nbuffer_load_dwordx4 v91, %[ro], s44 offen nt lds\ns_add_u32 m0, %[lw], 15360\ns_add_u32 s44, %[w2], 49280\n" \
"buffer_load_dwordx4 v91, %[ro], s44 offen nt lds\nds_read_b64_tr_b16 a[240:241], v93 offset:16384\nds_read_b64_tr_b16 a[242:243], v93 offset:16896\nds_read_b64_tr_b16 a[248:249], v93 offset:16392\nds_read_b64_tr_b16 a[250:251], v93 offset:16904\nds_read_b64_tr_b16 a[244:245], v93 offset:40960\nds_read_b64_tr_b16 a[246:247], v93 offset:41472\nds_read_b64_tr_b16 a[252:253], v93 offset:40968\n" \
"ds_read_b64_tr_b16 a[254:255], v93 offset:41480\ns_waitcnt lgkmcnt(0)\ns_waitcnt vmcnt(28)\ns_barrier\ns_add_u32 m0, %[lw], 16384\ns_add_u32 s44, %[w2], 256\nbuffer_load_dwordx4 v91, %[ro], s44 offen nt lds\ns_add_u32 m0, %[lw], 17408\n" \
"s_add_u32 s44, %[w2], 16640\nbuffer_load_dwordx4 v91, %[ro], s44 offen nt lds\ns_add_u32 m0, %[lw], 18432\ns_add_u32 s44, %[w2], 33024\nbuffer_load_dwordx4 v91, %[ro], s44 offen nt lds\ns_add_u32 m0, %[lw], 19456\ns_add_u32 s44, %[w2], 49408\nbuffer_load_dwordx4 v91, %[ro], s44 offen nt lds\n" \
"ds_read_b64_tr_b16 v[104:105], v93 offset:20480\nds_read_b64_tr_b16 v[106:107], v93 offset:20992\nds_read_b64_tr_b16 v[68:69], v93 offset:20488\nds_read_b64_tr_b16 v[70:71], v93 offset:21000\nds_read_b64_tr_b16 v[108:109], v93 offset:45056\nds_read_b64_tr_b16 v[110:111], v93 offset:45568\nds_read_b64_tr_b16 v[64:65], v93 offset:45064\nds_read_b64_tr_b16 v[66:67], v93 offset:45576\n" \
"s_waitcnt lgkmcnt(0)\ns_waitcnt vmcnt(28)\ns_barrier\ns_add_u32 m0, %[lw], 20480\ns_add_u32 s44, %[w2], 384\nbuffer_load_dwordx4 v91, %[ro], s44 offen nt lds\ns_add_u32 m0, %[lw], 21504\ns_add_u32 s44, %[w2], 16768\n" \
"buffer_load_dwordx4 v91, %[ro], s44 offen nt lds\ns_add_u32 m0, %[lw], 22528\ns_add_u32 s44, %[w2], 33152\nbuffer_load_dwordx4 v91, %[ro], s44 offen nt lds\ns_add_u32 m0, %[lw], 23552\ns_add_u32 s44, %[w2], 49536\nbuffer_load_dwordx4 v91, %[ro], s44 offen nt lds\nds_read_b64_tr_b16 v[128:129], v92 offset:0\n" \
"ds_read_b64_tr_b16 v[130:131], v92 offset:512\nds_read_b64_tr_b16 v[136:137], v92 offset:8\nds_read_b64_tr_b16 v[138:139], v92 offset:520\nds_read_b64_tr_b16 v[132:133], v92 offset:12288\nds_read_b64_tr_b16 v[134:135], v92 offset:12800\nds_read_b64_tr_b16 v[140:141], v92 offset:12296\nds_read_b64_tr_b16 v[142:143], v92 offset:12808\ns_waitcnt lgkmcnt(0)\n" \
"s_waitcnt vmcnt(28)\ns_barrier\ns_add_u32 m0, %[tb], 0\ns_add_u32 s44, %[w2], 65536\nbuffer_load_dwordx4 v91, %[ro], s44 offen nt lds\ns_add_u32 m0, %[tb], 1024\ns_add_u32 s44, %[w2], 81920\nbuffer_load_dwordx4 v91, %[ro], s44 offen nt lds\n" \
"s_add_u32 m0, %[tb], 2048\ns_add_u32 s44, %[w2], 98304\nbuffer_load_dwordx4 v91, %[ro], s44 offen nt lds\ns_add_u32 m0, %[tb], 3072\ns_add_u32 s44, %[w2], 114688\nbuffer_load_dwordx4 v91, %[ro], s44 offen nt lds\nds_read_b64_tr_b16 v[152:153], v92 offset:4096\nds_read_b64_tr_b16 v[154:155], v92 offset:4608\n" \
"ds_read_b64_tr_b16 v[168:169], v92 offset:4104\nds_read_b64_tr_b16 v[170:171], v92 offset:4616\nds_read_b64_tr_b16 v[156:157], v92 offset:16384\nds_read_b64_tr_b16 v[158:159], v92 offset:16896\nds_read_b64_tr_b16 v[172:173], v92 offset:16392\nds_read_b64_tr_b16 v[174:175], v92 offset:16904\ns_waitcnt lgkmcnt(0)\ns_waitcnt vmcnt(28)\n" \
"s_barrier\ns_add_u32 m0, %[tb], 4096\ns_add_u32 s44, %[w2], 65664\nbuffer_load_dwordx4 v91, %[ro], s44 offen nt lds\ns_add_u32 m0, %[tb], 5120\ns_add_u32 s44, %[w2], 82048\nbuffer_load_dwordx4 v91, %[ro], s44 offen nt lds\ns_add_u32 m0, %[tb], 6144\n" \
"s_add_u32 s44, %[w2], 98432\nbuffer_load_dwordx4 v91, %[ro], s44 offen nt lds\ns_add_u32 m0, %[tb], 7168\ns_add_u32 s44, %[w2], 114816\nbuffer_load_dwordx4 v91, %[ro], s44 offen nt lds\nds_read_b64_tr_b16 v[184:185], v92 offset:8192\nds_read_b64_tr_b16 v[186:187], v92 offset:8704\nds_read_b64_tr_b16 v[192:193], v92 offset:8200\n" \
"ds_read_b64_tr_b16 v[194:195], v92 offset:8712\nds_read_b64_tr_b16 v[188:189], v92 offset:20480\nds_read_b64_tr_b16 v[190:191], v92 offset:20992\nds_read_b64_tr_b16 v[196:197], v92 offset:20488\nds_read_b64_tr_b16 v[198:199], v92 offset:21000\ns_waitcnt lgkmcnt(0)\ns_waitcnt vmcnt(28)\ns_barrier\n" \
"s_add_u32 m0, %[tb], 8192\ns_add_u32 s44, %[w2], 65792\nbuffer_load_dwordx4 v91, %[ro], s44 offen nt lds\ns_add_u32 m0, %[tb], 9216\ns_add_u32 s44, %[w2], 82176\nbuffer_load_dwordx4 v91, %[ro], s44 offen nt lds\ns_add_u32 m0, %[tb], 10240\ns_add_u32 s44, %[w2], 98560\n" \
"buffer_load_dwordx4 v91, %[ro], s44 offen nt lds\ns_add_u32 m0, %[tb], 11264\ns_add_u32 s44, %[w2], 114944\nbuffer_load_dwordx4 v91, %[ro], s44 offen nt lds\nds_read_b64_tr_b16 v[208:209], v93 offset:0\nds_read_b64_tr_b16 v[210:211], v93 offset:512\nds_read_b64_tr_b16 v[60:61], v93 offset:8\nds_read_b64_tr_b16 v[62:63], v93 offset:520\n" \
"ds_read_b64_tr_b16 v[212:213], v93 offset:24576\nds_read_b64_tr_b16 v[214:215], v93 offset:25088\nds_read_b64_tr_b16 v[56:57], v93 offset:24584\nds_read_b64_tr_b16 v[58:59], v93 offset:25096\ns_waitcnt lgkmcnt(0)\ns_waitcnt vmcnt(28)\ns_barrier\ns_add_u32 m0, %[lw], 0\n" \
"s_add_u32 s44, %[w2], 65920\nbuffer_load_dwordx4 v91, %[ro], s44 offen nt lds\ns_add_u32 m0, %[lw], 1024\ns_add_u32 s44, %[w2], 82304\nbuffer_load_dwordx4 v91, %[ro], s44 offen nt lds\ns_add_u32 m0, %[lw], 2048\ns_add_u32 s44, %[w2], 98688\nbuffer_load_dwordx4 v91, %[ro], s44 offen nt lds\n" \
"s_add_u32 m0, %[lw], 3072\ns_add_u32 s44, %[w2], 115072\nbuffer_load_dwordx4 v91, %[ro], s44 offen nt lds\nds_read_b64_tr_b16 v[232:233], v93 offset:4096\nds_read_b64_tr_b16 v[234:235], v93 offset:4608\nds_read_b64_tr_b16 v[240:241], v93 offset:4104\nds_read_b64_tr_b16 v[242:243], v93 offset:4616\nds_read_b64_tr_b16 v[236:237], v93 offset:28672\n" \
"ds_read_b64_tr_b16 v[238:239], v93 offset:29184\nds_read_b64_tr_b16 v[244:245], v93 offset:28680\nds_read_b64_tr_b16 v[246:247], v93 offset:29192\ns_waitcnt lgkmcnt(0)\ns_waitcnt vmcnt(28)\ns_barrier\ns_add_u32 m0, %[lw], 4096\ns_add_u32 s44, %[w2], 196608\n" \
"buffer_load_dwordx4 v91, %[ro], s44 offen nt lds\ns_add_u32 m0, %[lw], 5120\ns_add_u32 s44, %[w2], 212992\nbuffer_load_dwordx4 v91, %[ro], s44 offen nt lds\ns_add_u32 m0, %[lw], 6144\ns_add_u32 s44, %[w2], 229376\nbuffer_load_dwordx4 v91, %[ro], s44 offen nt lds\ns_add_u32 m0, %[lw], 7168\n" \
"s_add_u32 s44, %[w2], 245760\nbuffer_load_dwordx4 v91, %[ro], s44 offen nt lds\nds_read_b64_tr_b16 a[8:9], v95 offset:8192\nds_read_b64_tr_b16 a[10:11], v95 offset:8320\nds_read_b64_tr_b16 a[12:13], v95 offset:10240\nds_read_b64_tr_b16 a[14:15], v95 offset:10368\nds_read_b64_tr_b16 a[216:217], v95 offset:8256\nds_read_b64_tr_b16 a[218:219], v95 offset:8384\n" \
"ds_read_b64_tr_b16 a[220:221], v95 offset:10304\nds_read_b64_tr_b16 a[222:223], v95 offset:10432\ns_waitcnt lgkmcnt(0)\ns_waitcnt vmcnt(28)\ns_barrier\ns_add_u32 m0, %[lw], 8192\ns_add_u32 s44, %[w2], 196736\nbuffer_load_dwordx4 v91, %[ro], s44 offen nt lds\n" \
"s_add_u32 m0, %[lw], 9216\ns_add_u32 s44, %[w2], 213120\nbuffer_load_dwordx4 v91, %[ro], s44 offen nt lds\ns_add_u32 m0, %[lw], 10240\ns_add_u32 s44, %[w2], 229504\nbuffer_load_dwordx4 v91, %[ro], s44 offen nt lds\ns_add_u32 m0, %[lw], 11264\ns_add_u32 s44, %[w2], 245888\n" \
"buffer_load_dwordx4 v91, %[ro], s44 offen nt lds\nds_read_b64_tr_b16 a[64:65], v95 offset:12288\nds_read_b64_tr_b16 a[66:67], v95 offset:12416\nds_read_b64_tr_b16 a[68:69], v95 offset:14336\nds_read_b64_tr_b16 a[70:71], v95 offset:14464\nds_read_b64_tr_b16 v[112:113], v95 offset:12352\nds_read_b64_tr_b16 v[114:115], v95 offset:12480\nds_read_b64_tr_b16 v[116:117], v95 offset:14400\n" \
"ds_read_b64_tr_b16 v[118:119], v95 offset:14528\ns_waitcnt lgkmcnt(0)\ns_waitcnt vmcnt(28)\ns_barrier\ns_add_u32 m0, %[lw], 12288\ns_add_u32 s44, %[w2], 196864\nbuffer_load_dwordx4 v91, %[ro], s44 offen nt lds\ns_add_u32 m0, %[lw], 13312\n" \
"s_add_u32 s44, %[w2], 213248\nbuffer_load_dwordx4 v91, %[ro], s44 offen nt lds\ns_add_u32 m0, %[lw], 14336\ns_add_u32 s44, %[w2], 229632\nbuffer_load_dwordx4 v91, %[ro], s44 offen nt lds\ns_add_u32 m0, %[lw], 15360\ns_add_u32 s44, %[w2], 246016\nbuffer_load_dwordx4 v91, %[ro], s44 offen nt lds\n" \
"ds_read_b64_tr_b16 a[112:113], v95 offset:16384\nds_read_b64_tr_b16 a[114:115], v95 offset:16512\nds_read_b64_tr_b16 a[116:117], v95 offset:18432\nds_read_b64_tr_b16 a[118:119], v95 offset:18560\nds_read_b64_tr_b16 v[160:161], v95 offset:16448\nds_read_b64_tr_b16 v[162:163], v95 offset:16576\nds_read_b64_tr_b16 v[164:165], v95 offset:18496\nds_read_b64_tr_b16 v[166:167], v95 offset:18624\n" \
"s_waitcnt lgkmcnt(0)\ns_waitcnt vmcnt(28)\ns_barrier\ns_add_u32 m0, %[lw], 16384\ns_add_u32 s44, %[w2], 196992\nbuffer_load_dwordx4 v91, %[ro], s44 offen nt lds\ns_add_u32 m0, %[lw], 17408\ns_add_u32 s44, %[w2], 213376\n" \
"buffer_load_dwordx4 v91, %[ro], s44 offen nt lds\ns_add_u32 m0, %[lw], 18432\ns_add_u32 s44, %[w2], 229760\nbuffer_load_dwordx4 v91, %[ro], s44 offen nt lds\ns_add_u32 m0, %[lw], 19456\ns_add_u32 s44, %[w2], 246144\nbuffer_load_dwordx4 v91, %[ro], s44 offen nt lds\nds_read_b64_tr_b16 a[168:169], v95 offset:20480\n" \
"ds_read_b64_tr_b16 a[170:171], v95 offset:20608\nds_read_b64_tr_b16 a[172:173], v95 offset:22528\nds_read_b64_tr_b16 a[174:175], v95 offset:22656\nds_read_b64_tr_b16 v[216:217], v95 offset:20544\nds_read_b64_tr_b16 v[218:219], v95 offset:20672\nds_read_b64_tr_b16 v[220:221], v95 offset:22592\nds_read_b64_tr_b16 v[222:223], v95 offset:22720\ns_waitcnt lgkmcnt(0)\n" \
"s_waitcnt vmcnt(28)\ns_barrier\ns_add_u32 m0, %[lw], 20480\ns_add_u32 s44, %[w2], 131072\nbuffer_load_dwordx4 v91, %[ro], s44 offen nt lds\ns_add_u32 m0, %[lw], 21504\ns_add_u32 s44, %[w2], 147456\nbuffer_load_dwordx4 v91, %[ro], s44 offen nt lds\n" \
"s_add_u32 m0, %[lw], 22528\ns_add_u32 s44, %[w2], 163840\nbuffer_load_dwordx4 v91, %[ro], s44 offen nt lds\ns_add_u32 m0, %[lw], 23552\ns_add_u32 s44, %[w2], 180224\nbuffer_load_dwordx4 v91, %[ro], s44 offen nt lds\nds_read_b64_tr_b16 a[24:25], v94 offset:0\nds_read_b64_tr_b16 a[26:27], v94 offset:128\n" \
"ds_read_b64_tr_b16 a[28:29], v94 offset:2048\nds_read_b64_tr_b16 a[30:31], v94 offset:2176\nds_read_b64_tr_b16 a[232:233], v94 offset:64\nds_read_b64_tr_b16 a[234:235], v94 offset:192\nds_read_b64_tr_b16 a[236:237], v94 offset:2112\nds_read_b64_tr_b16 a[238:239], v94 offset:2240\ns_waitcnt lgkmcnt(0)\ns_waitcnt vmcnt(28)\n" \
"s_barrier\ns_add_u32 m0, %[tb], 0\ns_add_u32 s44, %[w2], 131200\nbuffer_load_dwordx4 v91, %[ro], s44 offen nt lds\ns_add_u32 m0, %[tb], 1024\ns_add_u32 s44, %[w2], 147584\nbuffer_load_dwordx4 v91, %[ro], s44 offen nt lds\ns_add_u32 m0, %[tb], 2048\n" \
"s_add_u32 s44, %[w2], 163968\nbuffer_load_dwordx4 v91, %[ro], s44 offen nt lds\ns_add_u32 m0, %[tb], 3072\ns_add_u32 s44, %[w2], 180352\nbuffer_load_dwordx4 v91, %[ro], s44 offen nt lds\nds_read_b64_tr_b16 a[72:73], v94 offset:4096\nds_read_b64_tr_b16 a[74:75], v94 offset:4224\nds_read_b64_tr_b16 a[76:77], v94 offset:6144\n" \
"ds_read_b64_tr_b16 a[78:79], v94 offset:6272\nds_read_b64_tr_b16 v[120:121], v94 offset:4160\nds_read_b64_tr_b16 v[122:123], v94 offset:4288\nds_read_b64_tr_b16 v[124:125], v94 offset:6208\nds_read_b64_tr_b16 v[126:127], v94 offset:6336\ns_waitcnt lgkmcnt(0)\ns_waitcnt vmcnt(28)\ns_barrier\n" \
"s_add_u32 m0, %[tb], 4096\ns_add_u32 s44, %[w2], 131328\nbuffer_load_dwordx4 v91, %[ro], s44 offen nt lds\ns_add_u32 m0, %[tb], 5120\ns_add_u32 s44, %[w2], 147712\nbuffer_load_dwordx4 v91, %[ro], s44 offen nt lds\ns_add_u32 m0, %[tb], 6144\ns_add_u32 s44, %[w2], 164096\n" \
"buffer_load_dwordx4 v91, %[ro], s44 offen nt lds\ns_add_u32 m0, %[tb], 7168\ns_add_u32 s44, %[w2], 180480\nbuffer_load_dwordx4 v91, %[ro], s44 offen nt lds\nds_read_b64_tr_b16 a[128:129], v94 offset:8192\nds_read_b64_tr_b16 a[130:131], v94 offset:8320\nds_read_b64_tr_b16 a[132:133], v94 offset:10240\nds_read_b64_tr_b16 a[134:135], v94 offset:10368\n" \
"ds_read_b64_tr_b16 v[176:177], v94 offset:8256\nds_read_b64_tr_b16 v[178:179], v94 offset:8384\nds_read_b64_tr_b16 v[180:181], v94 offset:10304\nds_read_b64_tr_b16 v[182:183], v94 offset:10432\ns_waitcnt lgkmcnt(0)\ns_waitcnt vmcnt(28)\ns_barrier\ns_add_u32 m0, %[tb], 8192\n" \
"s_add_u32 s44, %[w2], 131456\nbuffer_load_dwordx4 v91, %[ro], s44 offen nt lds\ns_add_u32 m0, %[tb], 9216\ns_add_u32 s44, %[w2], 147840\nbuffer_load_dwordx4 v91, %[ro], s44 offen nt lds\ns_add_u32 m0, %[tb], 10240\ns_add_u32 s44, %[w2], 164224\nbuffer_load_dwordx4 v91, %[ro], s44 offen nt lds\n" \
"s_add_u32 m0, %[tb], 11264\ns_add_u32 s44, %[w2], 180608\nbuffer_load_dwordx4 v91, %[ro], s44 offen nt lds\nds_read_b64_tr_b16 a[176:177], v95 offset:0\nds_read_b64_tr_b16 a[178:179], v95 offset:128\nds_read_b64_tr_b16 a[180:181], v95 offset:2048\nds_read_b64_tr_b16 a[182:183], v95 offset:2176\nds_read_b64_tr_b16 v[224:225], v95 offset:64\n" \
"ds_read_b64_tr_b16 v[226:227], v95 offset:192\nds_read_b64_tr_b16 v[228:229], v95 offset:2112\nds_read_b64_tr_b16 v[230:231], v95 offset:2240\ns_waitcnt lgkmcnt(0)\ns_waitcnt vmcnt(28)\ns_barrier\nds_read_b64_tr_b16 a[48:49], v95 offset:4096\nds_read_b64_tr_b16 a[50:51], v95 offset:4224\n" \
"ds_read_b64_tr_b16 a[52:53], v95 offset:6144\nds_read_b64_tr_b16 a[54:55], v95 offset:6272\nds_read_b64_tr_b16 v[96:97], v95 offset:4160\nds_read_b64_tr_b16 v[98:99], v95 offset:4288\nds_read_b64_tr_b16 v[100:101], v95 offset:6208\nds_read_b64_tr_b16 v[102:103], v95 offset:6336\ns_waitcnt lgkmcnt(0)\nds_write_b128 v1, v[84:87] offset:0\n" \
"ds_write_b128 v1, v[80:83] offset:1024\nds_write_b128 v1, v[76:79] offset:2048\nds_write_b128 v1, v[72:75] offset:3072\ns_waitcnt vmcnt(24)\ns_barrier\nds_read_b64_tr_b16 a[96:97], v95 offset:8192\nds_read_b64_tr_b16 a[98:99], v95 offset:8320\nds_read_b64_tr_b16 a[100:101], v95 offset:10240\n" \
"ds_read_b64_tr_b16 a[102:103], v95 offset:10368\nds_read_b64_tr_b16 v[144:145], v95 offset:8256\nds_read_b64_tr_b16 v[146:147], v95 offset:8384\nds_read_b64_tr_b16 v[148:149], v95 offset:10304\nds_read_b64_tr_b16 v[150:151], v95 offset:10432\ns_waitcnt lgkmcnt(0)\nds_write_b128 v1, v[68:71] offset:4096\nds_write_b128 v1, v[64:67] offset:5120\n" \
"ds_write_b128 v1, v[60:63] offset:6144\nds_write_b128 v1, v[56:59] offset:7168\ns_waitcnt vmcnt(20)\ns_barrier\nds_read_b64_tr_b16 a[152:153], v95 offset:12288\nds_read_b64_tr_b16 a[154:155], v95 offset:12416\nds_read_b64_tr_b16 a[156:157], v95 offset:14336\nds_read_b64_tr_b16 a[158:159], v95 offset:14464\n" \
"ds_read_b64_tr_b16 v[200:201], v95 offset:12352\nds_read_b64_tr_b16 v[202:203], v95 offset:12480\nds_read_b64_tr_b16 v[204:205], v95 offset:14400\nds_read_b64_tr_b16 v[206:207], v95 offset:14528\ns_waitcnt lgkmcnt(0)\ns_waitcnt vmcnt(16)\ns_barrier\nds_read_b64_tr_b16 a[200:201], v95 offset:16384\n" \
"ds_read_b64_tr_b16 a[202:203], v95 offset:16512\nds_read_b64_tr_b16 a[204:205], v95 offset:18432\nds_read_b64_tr_b16 a[206:207], v95 offset:18560\nds_read_b64_tr_b16 v[248:249], v95 offset:16448\nds_read_b64_tr_b16 v[250:251], v95 offset:16576\nds_read_b64_tr_b16 v[252:253], v95 offset:18496\nds_read_b64_tr_b16 v[254:255], v95 offset:18624\ns_waitcnt lgkmcnt(0)\n" \
"s_waitcnt vmcnt(12)\ns_barrier\nds_read_b64_tr_b16 v[56:57], v95 offset:20480\nds_read_b64_tr_b16 v[58:59], v95 offset:20608\nds_read_b64_tr_b16 v[60:61], v95 offset:22528\nds_read_b64_tr_b16 v[62:63], v95 offset:22656\nds_read_b64_tr_b16 v[64:65], v95 offset:20544\nds_read_b64_tr_b16 v[66:67], v95 offset:20672\n" \
"ds_read_b64_tr_b16 v[68:69], v95 offset:22592\nds_read_b64_tr_b16 v[70:71], v95 offset:22720\ns_waitcnt lgkmcnt(0)\nds_write_b128 v1, v[56:59] offset:8192\nds_write_b128 v1, v[60:63] offset:9216\nds_write_b128 v1, v[64:67] offset:16384\nds_write_b128 v1, v[68:71] offset:17408\ns_waitcnt vmcnt(8)\n" \
"s_barrier\nds_read_b64_tr_b16 v[68:69], v94 offset:0\nds_read_b64_tr_b16 v[70:71], v94 offset:128\nds_read_b64_tr_b16 v[64:65], v94 offset:2048\nds_read_b64_tr_b16 v[66:67], v94 offset:2176\nds_read_b64_tr_b16 v[60:61], v94 offset:64\nds_read_b64_tr_b16 v[62:63], v94 offset:192\nds_read_b64_tr_b16 v[56:57], v94 offset:2112\n" \
"ds_read_b64_tr_b16 v[58:59], v94 offset:2240\ns_waitcnt lgkmcnt(0)\nds_write_b128 v1, v[68:71] offset:10240\nds_write_b128 v1, v[64:67] offset:11264\nds_write_b128 v1, v[60:63] offset:18432\nds_write_b128 v1, v[56:59] offset:19456\ns_waitcnt vmcnt(4)\ns_barrier\n" \
"ds_read_b64_tr_b16 v[56:57], v94 offset:4096\nds_read_b64_tr_b16 v[58:59], v94 offset:4224\nds_read_b64_tr_b16 v[60:61], v94 offset:6144\nds_read_b64_tr_b16 v[62:63], v94 offset:6272\nds_read_b64_tr_b16 v[64:65], v94 offset:4160\nds_read_b64_tr_b16 v[66:67], v94 offset:4288\nds_read_b64_tr_b16 v[68:69], v94 offset:6208\nds_read_b64_tr_b16 v[70:71], v94 offset:6336\n" \
"s_waitcnt lgkmcnt(0)\nds_write_b128 v1, v[56:59] offset:12288\nds_write_b128 v1, v[60:63] offset:13312\nds_write_b128 v1, v[64:67] offset:20480\nds_write_b128 v1, v[68:71] offset:21504\ns_waitcnt vmcnt(0)\ns_barrier\nds_read_b64_tr_b16 v[68:69], v94 offset:8192\n" \
"ds_read_b64_tr_b16 v[70:71], v94 offset:8320\nds_read_b64_tr_b16 v[64:65], v94 offset:10240\nds_read_b64_tr_b16 v[66:67], v94 offset:10368\nds_read_b64_tr_b16 v[60:61], v94 offset:8256\nds_read_b64_tr_b16 v[62:63], v94 offset:8384\nds_read_b64_tr_b16 v[56:57], v94 offset:10304\nds_read_b64_tr_b16 v[58:59], v94 offset:10432\ns_waitcnt lgkmcnt(0)\n" \
"ds_write_b128 v1, v[68:71] offset:14336\nds_write_b128 v1, v[64:67] offset:15360\nds_write_b128 v1, v[60:63] offset:22528\nds_write_b128 v1, v[56:59] offset:23552\ns_waitcnt lgkmcnt(0)\ns_barrier\ns_mov_b32 s43, 0\n.Lvu_hb%=:\n" \
"buffer_load_dwordx2 v[16:17], off, s[36:39], s45 sc1\ns_waitcnt vmcnt(0)\nv_cmp_ne_u32 vcc, v15, v16\ns_cbranch_vccnz .Lvu_hb1%=\ns_sleep 4\ns_add_u32 s43, s43, 1\ns_cmp_lt_u32 s43, 262144\ns_cbranch_scc1 .Lvu_hb%=\n" \
"s_branch .Lvu_abort%=\n.Lvu_hb1%=:\nv_readfirstlane_b32 s44, v17\ns_and_b32 s44, s44, 0x200000\ns_add_u32 s36, s36, s44\ns_addc_u32 s37, s37, 0\ns_mov_b32 s40, -1\ns_mov_b32 s43, 0\n" \
"s_lshl_b32 s57, %[w], 12\ns_mov_b32 s56, 0\ns_mov_b32 s42, 0\ns_mov_b32 s60, -2147483648\ns_mov_b32 s54, -2147483648\ns_add_u32 s44, s40, 1\ns_lshl_b32 s44, s44, 17\ns_add_u32 s44, s44, %[xn]\n" \
"s_lshr_b32 s45, s44, 8\ns_and_b32 s45, s45, 0x2000\ns_xor_b32 s41, s44, s45\ns_add_u32 s41, s41, %[xw]\n.Lvu_f0%=:\nbuffer_load_dword v14, v8, s[36:39], s56 offen sc1\ns_waitcnt vmcnt(0)\nv_cmp_ge_i32 vcc, s40, v14\n" \
"s_cbranch_vccz .Lvu_f0fok%=\ns_add_u32 s43, s43, 1\ns_cmp_lt_u32 s43, 262144\ns_cbranch_scc1 .Lvu_f0%=\ns_branch .Lvu_abort%=\n.Lvu_f0fok%=:\ns_add_u32 m0, s57, 0\ns_add_u32 s58, s41, 0\n" \
"buffer_load_dwordx4 v0, s[36:39], s58 offen offset:0 sc1 lds\nbuffer_load_dwordx4 v0, s[36:39], s58 offen offset:1024 sc1 lds\nbuffer_load_dwordx4 v0, s[36:39], s58 offen offset:2048 sc1 lds\nbuffer_load_dwordx4 v0, s[36:39], s58 offen offset:3072 sc1 lds\ns_add_u32 m0, s57, 32768\ns_add_u32 s58, s41, 32768\nbuffer_load_dwordx4 v0, s[36:39], s58 offen offset:0 sc1 lds\nbuffer_load_dwordx4 v0, s[36:39], s58 offen offset:1024 sc1 lds\n" \
"buffer_load_dwordx4 v0, s[36:39], s58 offen offset:2048 sc1 lds\nbuffer_load_dwordx4 v0, s[36:39], s58 offen offset:3072 sc1 lds\ns_waitcnt vmcnt(0)\ns_barrier\nds_read_b128 v[88:91], v2 offset:0\nds_read_b128 v[92:95], v2 offset:256\nds_read_b128 v[40:43], v2 offset:2048\nds_read_b128 v[44:47], v2 offset:2304\n" \
".Lvu_tok%=:\nds_read_b128 v[64:67], v2 offset:4096\nds_read_b128 v[68:71], v2 offset:4352\nds_read_b128 v[56:59], v1 offset:0\nds_read_b128 v[60:63], v1 offset:1024\ns_waitcnt lgkmcnt(7)\nv_mfma_f32_16x16x32_bf16 v[16:19], a[0:3], v[88:91], 0\nv_cvt_pk_bf16_f32 v76, v80, v81\n" \
"v_cvt_pk_bf16_f32 v77, v82, v83\nv_mfma_f32_16x16x32_bf16 v[20:23], a[16:19], v[88:91], 0\ns_waitcnt lgkmcnt(6)\nds_read_b128 v[32:35], v2 offset:6144\nv_cvt_pk_bf16_f32 v78, v84, v85\nv_cvt_pk_bf16_f32 v79, v86, v87\nv_mfma_f32_16x16x32_bf16 v[24:27], a[0:3], v[92:95], 0\nv_and_or_b32 v76, v76, -2, v13\n" \
"v_and_or_b32 v78, v78, -2, v13\nv_mfma_f32_16x16x32_bf16 v[28:31], a[16:19], v[92:95], 0\ns_waitcnt lgkmcnt(6)\nbuffer_store_dwordx4 v[76:79], v7, s[36:39], s60 offen offset:2560 sc1\nds_read_b128 v[36:39], v2 offset:6400\nv_mfma_f32_16x16x32_bf16 v[16:19], a[4:7], v[40:43], v[16:19]\nv_mfma_f32_16x16x32_bf16 v[20:23], a[20:23], v[40:43], v[20:23]\ns_waitcnt lgkmcnt(6)\n" \
"ds_read_b128 v[40:43], v2 offset:8192\nv_mfma_f32_16x16x32_bf16 v[24:27], a[4:7], v[44:47], v[24:27]\ns_add_u32 m0, s57, 16384\ns_add_u32 s60, s41, 65536\nbuffer_load_dwordx4 v0, s[36:39], s60 offen offset:0 sc1 lds\nv_mfma_f32_16x16x32_bf16 v[28:31], a[20:23], v[44:47], v[28:31]\ns_waitcnt lgkmcnt(6)\nds_read_b128 v[44:47], v2 offset:8448\n" \
"v_mfma_f32_16x16x32_bf16 v[16:19], a[32:35], v[64:67], v[16:19]\nv_mfma_f32_16x16x32_bf16 v[20:23], a[40:43], v[64:67], v[20:23]\ns_waitcnt lgkmcnt(6)\nds_read_b128 v[64:67], v2 offset:10240\nv_mfma_f32_16x16x32_bf16 v[24:27], a[32:35], v[68:71], v[24:27]\nbuffer_load_dwordx4 v0, s[36:39], s60 offen offset:1024 sc1 lds\nv_mfma_f32_16x16x32_bf16 v[28:31], a[40:43], v[68:71], v[28:31]\ns_waitcnt lgkmcnt(4)\n" \
"ds_read_b128 v[68:71], v2 offset:10496\nv_mfma_f32_16x16x32_bf16 v[16:19], a[36:39], v[32:35], v[16:19]\nv_mfma_f32_16x16x32_bf16 v[20:23], a[44:47], v[32:35], v[20:23]\ns_waitcnt lgkmcnt(4)\nds_read_b128 v[32:35], v2 offset:12288\nv_mfma_f32_16x16x32_bf16 v[24:27], a[36:39], v[36:39], v[24:27]\nbuffer_load_dwordx4 v0, s[36:39], s60 offen offset:2048 sc1 lds\nv_mfma_f32_16x16x32_bf16 v[28:31], a[44:47], v[36:39], v[28:31]\n" \
"s_waitcnt lgkmcnt(4)\nds_read_b128 v[36:39], v2 offset:12544\nv_mfma_f32_16x16x32_bf16 v[16:19], a[56:59], v[40:43], v[16:19]\nv_mfma_f32_16x16x32_bf16 v[20:23], v[56:59], v[40:43], v[20:23]\ns_waitcnt lgkmcnt(4)\nds_read_b128 v[40:43], v2 offset:14336\nv_mfma_f32_16x16x32_bf16 v[24:27], a[56:59], v[44:47], v[24:27]\nbuffer_load_dwordx4 v0, s[36:39], s60 offen offset:3072 sc1 lds\n" \
"v_mfma_f32_16x16x32_bf16 v[28:31], v[56:59], v[44:47], v[28:31]\ns_waitcnt lgkmcnt(4)\nds_read_b128 v[44:47], v2 offset:14592\nv_mfma_f32_16x16x32_bf16 v[16:19], a[60:63], v[64:67], v[16:19]\nv_mfma_f32_16x16x32_bf16 v[20:23], v[60:63], v[64:67], v[20:23]\ns_waitcnt lgkmcnt(4)\nds_read_b128 v[64:67], v2 offset:32768\nv_mfma_f32_16x16x32_bf16 v[24:27], a[60:63], v[68:71], v[24:27]\n" \
"v_mfma_f32_16x16x32_bf16 v[28:31], v[60:63], v[68:71], v[28:31]\ns_waitcnt lgkmcnt(4)\nds_read_b128 v[68:71], v2 offset:33024\nv_mfma_f32_16x16x32_bf16 v[16:19], a[80:83], v[32:35], v[16:19]\nv_mfma_f32_16x16x32_bf16 v[20:23], a[88:91], v[32:35], v[20:23]\ns_waitcnt lgkmcnt(4)\nds_read_b128 v[32:35], v2 offset:34816\nv_mfma_f32_16x16x32_bf16 v[24:27], a[80:83], v[36:39], v[24:27]\n" \
"ds_read_b128 v[56:59], v1 offset:2048\nds_read_b128 v[60:63], v1 offset:3072\nv_mfma_f32_16x16x32_bf16 v[28:31], a[88:91], v[36:39], v[28:31]\ns_waitcnt lgkmcnt(6)\nds_read_b128 v[36:39], v2 offset:35072\nv_mfma_f32_16x16x32_bf16 v[16:19], a[84:87], v[40:43], v[16:19]\nv_mfma_f32_16x16x32_bf16 v[20:23], a[92:95], v[40:43], v[20:23]\ns_waitcnt lgkmcnt(6)\n" \
"ds_read_b128 v[40:43], v2 offset:36864\nv_mfma_f32_16x16x32_bf16 v[24:27], a[84:87], v[44:47], v[24:27]\nv_mfma_f32_16x16x32_bf16 v[28:31], a[92:95], v[44:47], v[28:31]\ns_waitcnt lgkmcnt(6)\nds_read_b128 v[44:47], v2 offset:37120\nv_mfma_f32_16x16x32_bf16 v[16:19], a[104:107], v[64:67], v[16:19]\nv_mfma_f32_16x16x32_bf16 v[20:23], a[120:123], v[64:67], v[20:23]\ns_waitcnt lgkmcnt(6)\n" \
"ds_read_b128 v[64:67], v2 offset:38912\nv_mfma_f32_16x16x32_bf16 v[24:27], a[104:107], v[68:71], v[24:27]\ns_barrier\nv_mfma_f32_16x16x32_bf16 v[28:31], a[120:123], v[68:71], v[28:31]\ns_waitcnt lgkmcnt(6)\nds_read_b128 v[68:71], v2 offset:39168\nv_mfma_f32_16x16x32_bf16 v[16:19], a[108:111], v[32:35], v[16:19]\ns_add_u32 m0, s57, 0\n" \
"s_add_u32 s60, s41, 98304\nbuffer_load_dwordx4 v0, s[36:39], s60 offen offset:0 sc1 lds\nv_mfma_f32_16x16x32_bf16 v[20:23], a[124:127], v[32:35], v[20:23]\ns_waitcnt lgkmcnt(4)\nds_read_b128 v[32:35], v2 offset:40960\nv_mfma_f32_16x16x32_bf16 v[24:27], a[108:111], v[36:39], v[24:27]\nv_mfma_f32_16x16x32_bf16 v[28:31], a[124:127], v[36:39], v[28:31]\ns_waitcnt lgkmcnt(4)\n" \
"ds_read_b128 v[36:39], v2 offset:41216\nv_mfma_f32_16x16x32_bf16 v[16:19], a[136:139], v[40:43], v[16:19]\nbuffer_load_dwordx4 v0, s[36:39], s60 offen offset:1024 sc1 lds\nv_mfma_f32_16x16x32_bf16 v[20:23], a[144:147], v[40:43], v[20:23]\ns_waitcnt lgkmcnt(4)\nds_read_b128 v[40:43], v2 offset:43008\nv_mfma_f32_16x16x32_bf16 v[24:27], a[136:139], v[44:47], v[24:27]\nv_mfma_f32_16x16x32_bf16 v[28:31], a[144:147], v[44:47], v[28:31]\n" \
"s_waitcnt lgkmcnt(4)\nds_read_b128 v[44:47], v2 offset:43264\nv_mfma_f32_16x16x32_bf16 v[16:19], a[140:143], v[64:67], v[16:19]\nbuffer_load_dwordx4 v0, s[36:39], s60 offen offset:2048 sc1 lds\nv_mfma_f32_16x16x32_bf16 v[20:23], a[148:151], v[64:67], v[20:23]\ns_waitcnt lgkmcnt(4)\nds_read_b128 v[64:67], v2 offset:45056\nv_mfma_f32_16x16x32_bf16 v[24:27], a[140:143], v[68:71], v[24:27]\n" \
"v_mfma_f32_16x16x32_bf16 v[28:31], a[148:151], v[68:71], v[28:31]\ns_waitcnt lgkmcnt(4)\nds_read_b128 v[68:71], v2 offset:45312\nv_mfma_f32_16x16x32_bf16 v[16:19], a[160:163], v[32:35], v[16:19]\nbuffer_load_dwordx4 v0, s[36:39], s60 offen offset:3072 sc1 lds\nv_mfma_f32_16x16x32_bf16 v[20:23], v[56:59], v[32:35], v[20:23]\ns_waitcnt lgkmcnt(4)\nds_read_b128 v[32:35], v2 offset:47104\n" \
"v_mfma_f32_16x16x32_bf16 v[24:27], a[160:163], v[36:39], v[24:27]\nv_mfma_f32_16x16x32_bf16 v[28:31], v[56:59], v[36:39], v[28:31]\ns_waitcnt lgkmcnt(4)\nds_read_b128 v[36:39], v2 offset:47360\nv_mfma_f32_16x16x32_bf16 v[16:19], a[164:167], v[40:43], v[16:19]\nv_mfma_f32_16x16x32_bf16 v[20:23], v[60:63], v[40:43], v[20:23]\ns_waitcnt vmcnt(4)\ns_barrier\n" \
"s_waitcnt lgkmcnt(4)\nds_read_b128 v[40:43], v2 offset:16384\nv_mfma_f32_16x16x32_bf16 v[24:27], a[164:167], v[44:47], v[24:27]\ns_waitcnt vmcnt(4)\nbuffer_store_dword v15, off, s[36:39], s54 sc1\nv_mfma_f32_16x16x32_bf16 v[28:31], v[60:63], v[44:47], v[28:31]\ns_waitcnt lgkmcnt(4)\nds_read_b128 v[44:47], v2 offset:16640\n" \
"v_mfma_f32_16x16x32_bf16 v[16:19], a[184:187], v[64:67], v[16:19]\ns_add_u32 s56, s56, %[fs]\ns_add_u32 s62, s40, 1\nv_mfma_f32_16x16x32_bf16 v[20:23], a[192:195], v[64:67], v[20:23]\ns_waitcnt lgkmcnt(4)\nds_read_b128 v[64:67], v2 offset:18432\nv_mfma_f32_16x16x32_bf16 v[24:27], a[184:187], v[68:71], v[24:27]\ns_add_u32 s44, s40, 2\n" \
"s_lshl_b32 s44, s44, 17\nds_read_b128 v[56:59], v1 offset:4096\nds_read_b128 v[60:63], v1 offset:5120\nv_mfma_f32_16x16x32_bf16 v[28:31], a[192:195], v[68:71], v[28:31]\ns_waitcnt lgkmcnt(6)\nds_read_b128 v[68:71], v2 offset:18688\nv_mfma_f32_16x16x32_bf16 v[16:19], a[188:191], v[32:35], v[16:19]\ns_add_u32 s44, s44, %[xn]\n" \
"s_lshr_b32 s45, s44, 8\nv_mfma_f32_16x16x32_bf16 v[20:23], a[196:199], v[32:35], v[20:23]\ns_waitcnt lgkmcnt(6)\nds_read_b128 v[32:35], v2 offset:20480\nv_mfma_f32_16x16x32_bf16 v[24:27], a[188:191], v[36:39], v[24:27]\ns_and_b32 s45, s45, 0x2000\ns_xor_b32 s41, s44, s45\nv_mfma_f32_16x16x32_bf16 v[28:31], a[196:199], v[36:39], v[28:31]\n" \
"s_waitcnt lgkmcnt(6)\nds_read_b128 v[36:39], v2 offset:20736\nv_mfma_f32_16x16x32_bf16 v[16:19], a[208:211], v[40:43], v[16:19]\ns_add_u32 s41, s41, %[xw]\nv_mfma_f32_16x16x32_bf16 v[20:23], a[224:227], v[40:43], v[20:23]\ns_waitcnt lgkmcnt(6)\nds_read_b128 v[40:43], v2 offset:22528\nv_mfma_f32_16x16x32_bf16 v[24:27], a[208:211], v[44:47], v[24:27]\n" \
"v_mfma_f32_16x16x32_bf16 v[28:31], a[224:227], v[44:47], v[28:31]\ns_waitcnt lgkmcnt(6)\nds_read_b128 v[44:47], v2 offset:22784\nv_mfma_f32_16x16x32_bf16 v[16:19], a[212:215], v[64:67], v[16:19]\nv_mfma_f32_16x16x32_bf16 v[20:23], a[228:231], v[64:67], v[20:23]\ns_waitcnt lgkmcnt(4)\nds_read_b128 v[64:67], v2 offset:24576\nv_mfma_f32_16x16x32_bf16 v[24:27], a[212:215], v[68:71], v[24:27]\n" \
"v_mfma_f32_16x16x32_bf16 v[28:31], a[228:231], v[68:71], v[28:31]\ns_waitcnt lgkmcnt(4)\nds_read_b128 v[68:71], v2 offset:24832\nv_mfma_f32_16x16x32_bf16 v[16:19], a[240:243], v[32:35], v[16:19]\nv_mfma_f32_16x16x32_bf16 v[20:23], a[248:251], v[32:35], v[20:23]\ns_waitcnt lgkmcnt(4)\nds_read_b128 v[32:35], v2 offset:26624\nv_mfma_f32_16x16x32_bf16 v[24:27], a[240:243], v[36:39], v[24:27]\n" \
"v_mfma_f32_16x16x32_bf16 v[28:31], a[248:251], v[36:39], v[28:31]\ns_waitcnt lgkmcnt(4)\nds_read_b128 v[36:39], v2 offset:26880\nv_mfma_f32_16x16x32_bf16 v[16:19], a[244:247], v[40:43], v[16:19]\nv_mfma_f32_16x16x32_bf16 v[20:23], a[252:255], v[40:43], v[20:23]\ns_waitcnt lgkmcnt(4)\nds_read_b128 v[40:43], v2 offset:28672\nv_mfma_f32_16x16x32_bf16 v[24:27], a[244:247], v[44:47], v[24:27]\n" \
"v_mfma_f32_16x16x32_bf16 v[28:31], a[252:255], v[44:47], v[28:31]\ns_waitcnt lgkmcnt(4)\nds_read_b128 v[44:47], v2 offset:28928\nv_mfma_f32_16x16x32_bf16 v[16:19], v[104:107], v[64:67], v[16:19]\nv_mfma_f32_16x16x32_bf16 v[20:23], v[56:59], v[64:67], v[20:23]\ns_waitcnt lgkmcnt(4)\nds_read_b128 v[64:67], v2 offset:30720\nv_mfma_f32_16x16x32_bf16 v[24:27], v[104:107], v[68:71], v[24:27]\n" \
"v_mfma_f32_16x16x32_bf16 v[28:31], v[56:59], v[68:71], v[28:31]\ns_waitcnt lgkmcnt(4)\nds_read_b128 v[68:71], v2 offset:30976\nv_mfma_f32_16x16x32_bf16 v[16:19], v[108:111], v[32:35], v[16:19]\nv_mfma_f32_16x16x32_bf16 v[20:23], v[60:63], v[32:35], v[20:23]\ns_waitcnt vmcnt(1)\ns_barrier\ns_lshr_b32 m0, s57, 4\n" \
"s_add_u32 m0, m0, 159744\ns_waitcnt lgkmcnt(4)\nds_read_b128 v[32:35], v2 offset:0\nv_mfma_f32_16x16x32_bf16 v[24:27], v[108:111], v[36:39], v[24:27]\nbuffer_load_dword v8, s[36:39], s56 offen sc1 lds\nv_lshrrev_b32 v15, 2, v0\nv_mfma_f32_16x16x32_bf16 v[28:31], v[60:63], v[36:39], v[28:31]\ns_waitcnt lgkmcnt(4)\n" \
"ds_read_b128 v[36:39], v2 offset:256\nv_mfma_f32_16x16x32_bf16 v[16:19], v[128:131], v[40:43], v[16:19]\ns_add_u32 m0, s57, 16384\ns_nop 0\nbuffer_load_dwordx4 v3, s[36:39], s42 offen offset:0 sc1 lds\nv_mfma_f32_16x16x32_bf16 v[20:23], v[136:139], v[40:43], v[20:23]\ns_waitcnt lgkmcnt(4)\nds_read_b128 v[40:43], v2 offset:2048\n" \
"v_mfma_f32_16x16x32_bf16 v[24:27], v[128:131], v[44:47], v[24:27]\nds_read_b128 v[56:59], v1 offset:6144\nds_read_b128 v[60:63], v1 offset:7168\nv_add_u32 v15, 159744, v15\nv_mfma_f32_16x16x32_bf16 v[28:31], v[136:139], v[44:47], v[28:31]\ns_waitcnt lgkmcnt(6)\nds_read_b128 v[44:47], v2 offset:2304\nv_mfma_f32_16x16x32_bf16 v[16:19], v[132:135], v[64:67], v[16:19]\n" \
"buffer_load_dwordx4 v3, s[36:39], s42 offen offset:1024 sc1 lds\nv_mfma_f32_16x16x32_bf16 v[20:23], v[140:143], v[64:67], v[20:23]\ns_waitcnt lgkmcnt(6)\nds_read_b128 v[64:67], v2 offset:4096\nv_mfma_f32_16x16x32_bf16 v[24:27], v[132:135], v[68:71], v[24:27]\nv_mfma_f32_16x16x32_bf16 v[28:31], v[140:143], v[68:71], v[28:31]\ns_waitcnt lgkmcnt(6)\nds_read_b128 v[68:71], v2 offset:4352\n" \
"v_mfma_f32_16x16x32_bf16 v[16:19], v[152:155], v[32:35], v[16:19]\nbuffer_load_dwordx4 v3, s[36:39], s42 offen offset:2048 sc1 lds\nv_mfma_f32_16x16x32_bf16 v[20:23], v[168:171], v[32:35], v[20:23]\ns_waitcnt lgkmcnt(6)\nds_read_b128 v[32:35], v2 offset:6144\nv_mfma_f32_16x16x32_bf16 v[24:27], v[152:155], v[36:39], v[24:27]\nv_mfma_f32_16x16x32_bf16 v[28:31], v[168:171], v[36:39], v[28:31]\ns_waitcnt lgkmcnt(6)\n" \
"ds_read_b128 v[36:39], v2 offset:6400\nv_mfma_f32_16x16x32_bf16 v[16:19], v[156:159], v[40:43], v[16:19]\nbuffer_load_dwordx4 v3, s[36:39], s42 offen offset:3072 sc1 lds\nv_mfma_f32_16x16x32_bf16 v[20:23], v[172:175], v[40:43], v[20:23]\ns_waitcnt lgkmcnt(4)\nds_read_b128 v[40:43], v2 offset:8192\nv_mfma_f32_16x16x32_bf16 v[24:27], v[156:159], v[44:47], v[24:27]\nv_mfma_f32_16x16x32_bf16 v[28:31], v[172:175], v[44:47], v[28:31]\n" \
"s_waitcnt lgkmcnt(4)\nds_read_b128 v[44:47], v2 offset:8448\nv_mfma_f32_16x16x32_bf16 v[16:19], v[184:187], v[64:67], v[16:19]\nv_mfma_f32_16x16x32_bf16 v[20:23], v[192:195], v[64:67], v[20:23]\ns_waitcnt lgkmcnt(4)\nds_read_b128 v[64:67], v2 offset:10240\nv_mfma_f32_16x16x32_bf16 v[24:27], v[184:187], v[68:71], v[24:27]\nv_mfma_f32_16x16x32_bf16 v[28:31], v[192:195], v[68:71], v[28:31]\n" \
"s_waitcnt lgkmcnt(4)\nds_read_b128 v[68:71], v2 offset:10496\nv_mfma_f32_16x16x32_bf16 v[16:19], v[188:191], v[32:35], v[16:19]\nv_mfma_f32_16x16x32_bf16 v[20:23], v[196:199], v[32:35], v[20:23]\ns_waitcnt lgkmcnt(4)\nds_read_b128 v[32:35], v2 offset:12288\nv_mfma_f32_16x16x32_bf16 v[24:27], v[188:191], v[36:39], v[24:27]\nv_mfma_f32_16x16x32_bf16 v[28:31], v[196:199], v[36:39], v[28:31]\n" \
"s_waitcnt lgkmcnt(4)\nds_read_b128 v[36:39], v2 offset:14336\nv_mfma_f32_16x16x32_bf16 v[16:19], v[208:211], v[40:43], v[16:19]\nv_mfma_f32_16x16x32_bf16 v[20:23], v[56:59], v[40:43], v[20:23]\ns_waitcnt lgkmcnt(4)\nds_read_b128 v[40:43], v2 offset:12544\nv_mfma_f32_16x16x32_bf16 v[24:27], v[208:211], v[44:47], v[24:27]\nv_mfma_f32_16x16x32_bf16 v[28:31], v[56:59], v[44:47], v[28:31]\n" \
"s_waitcnt lgkmcnt(4)\nds_read_b128 v[44:47], v2 offset:14592\nv_add_u32 v14, s57, v0\nv_mfma_f32_16x16x32_bf16 v[16:19], v[212:215], v[64:67], v[16:19]\nv_mov_b64 v[48:49], 0\nv_mfma_f32_16x16x32_bf16 v[20:23], v[60:63], v[64:67], v[20:23]\ns_waitcnt lgkmcnt(4)\nv_mov_b64 v[50:51], 0\n" \
"v_mfma_f32_16x16x32_bf16 v[24:27], v[212:215], v[68:71], v[24:27]\nv_mov_b64 v[52:53], 0\nv_mfma_f32_16x16x32_bf16 v[28:31], v[60:63], v[68:71], v[28:31]\ns_waitcnt lgkmcnt(3)\nv_mov_b64 v[54:55], 0\nv_mfma_f32_16x16x32_bf16 v[16:19], v[232:235], v[32:35], v[16:19]\ns_waitcnt vmcnt(0)\nds_read_b128 v[80:83], v14 offset:16384\n" \
"v_mfma_f32_16x16x32_bf16 v[20:23], v[240:243], v[32:35], v[20:23]\ns_waitcnt lgkmcnt(3)\nds_read_b128 v[84:87], v14 offset:17408\nv_mfma_f32_16x16x32_bf16 v[16:19], v[236:239], v[36:39], v[16:19]\nds_read_b128 v[88:91], v14 offset:18432\nv_mfma_f32_16x16x32_bf16 v[20:23], v[244:247], v[36:39], v[20:23]\ns_waitcnt lgkmcnt(4)\nds_read_b128 v[92:95], v14 offset:19456\n" \
"v_mfma_f32_16x16x32_bf16 v[24:27], v[232:235], v[40:43], v[24:27]\nv_mfma_f32_16x16x32_bf16 v[28:31], v[240:243], v[40:43], v[28:31]\ns_waitcnt lgkmcnt(4)\nv_mfma_f32_16x16x32_bf16 v[24:27], v[236:239], v[44:47], v[24:27]\nv_cvt_pk_bf16_f32 v72, v16, v20\nv_cvt_pk_bf16_f32 v73, v17, v21\nv_mfma_f32_16x16x32_bf16 v[28:31], v[244:247], v[44:47], v[28:31]\ns_waitcnt lgkmcnt(2)\n" \
"s_waitcnt lgkmcnt(1)\ns_waitcnt lgkmcnt(0)\nv_cvt_pk_bf16_f32 v74, v18, v22\nv_cvt_pk_bf16_f32 v75, v19, v23\nds_write_b128 v9, v[72:75]\nv_max3_u32 v16, v80, v82, v84\nv_max3_u32 v16, v16, v86, v88\nv_max3_u32 v16, v16, v90, v92\n" \
"v_max_u32 v16, v16, v94\nv_cmp_eq_u32 vcc, -1, v16\nv_cvt_pk_bf16_f32 v76, v24, v28\nv_cvt_pk_bf16_f32 v77, v25, v29\nv_cvt_pk_bf16_f32 v78, v26, v30\nv_cvt_pk_bf16_f32 v79, v27, v31\nds_write_b128 v9, v[76:79] offset:1024\ns_cmp_lt_i32 s40, 0\n" \
"s_cbranch_scc1 .Lvu_first%=\n.Lvu_crit%=:\ns_cmp_eq_u32 %[w], 1\ns_cbranch_scc1 .Lvu_c1%=\ns_cmp_eq_u32 %[w], 2\ns_cbranch_scc1 .Lvu_c2%=\ns_cmp_eq_u32 %[w], 3\ns_cbranch_scc1 .Lvu_c3%=\n" \
".Lvu_c0%=:\nds_read_b128 v[56:59], v1 offset:0\nds_read_b128 v[60:63], v1 offset:1024\nds_read_b128 v[64:67], v1 offset:2048\nds_read_b128 v[68:71], v1 offset:3072\n.Lvu_poll0%=:\ns_cbranch_vccnz .Lvu_re0%=\ns_barrier\n" \
"v_smfmac_f32_16x16x64_bf16 v[48:51], v[80:83], a[0:7], v4\nds_read_b128 v[16:19], v0 offset:20480\nv_smfmac_f32_16x16x64_bf16 v[52:55], v[80:83], a[16:23], v4\nds_read_b128 v[20:23], v0 offset:21504\nds_read_b32 v15, v15\nv_smfmac_f32_16x16x64_bf16 v[48:51], v[84:87], a[32:39], v4\nds_read_b128 v[24:27], v0 offset:22528\nv_smfmac_f32_16x16x64_bf16 v[52:55], v[84:87], a[40:47], v4\n" \
"ds_read_b128 v[28:31], v0 offset:23552\nv_smfmac_f32_16x16x64_bf16 v[48:51], v[88:91], a[56:63], v4\ns_waitcnt lgkmcnt(7)\nds_read_b128 v[32:35], v0 offset:24576\nv_smfmac_f32_16x16x64_bf16 v[52:55], v[88:91], v[56:63], v4\nds_read_b128 v[36:39], v0 offset:25600\nv_smfmac_f32_16x16x64_bf16 v[48:51], v[92:95], a[80:87], v4\nds_read_b128 v[40:43], v0 offset:26624\n" \
"v_smfmac_f32_16x16x64_bf16 v[52:55], v[92:95], a[88:95], v4\ns_waitcnt lgkmcnt(7)\nds_read_b128 v[44:47], v0 offset:27648\nv_smfmac_f32_16x16x64_bf16 v[48:51], v[16:19], a[104:111], v4\nds_read_b128 v[80:83], v0 offset:28672\nv_smfmac_f32_16x16x64_bf16 v[52:55], v[16:19], a[120:127], v4\ns_waitcnt lgkmcnt(8)\nds_read_b128 v[84:87], v0 offset:29696\n" \
"v_mov_b64 v[16:17], 0\nv_smfmac_f32_16x16x64_bf16 v[48:51], v[20:23], a[136:143], v4\nds_read_b128 v[88:91], v0 offset:30720\nv_mov_b64 v[18:19], 0\nv_smfmac_f32_16x16x64_bf16 v[52:55], v[20:23], a[144:151], v4\ns_waitcnt lgkmcnt(8)\nds_read_b128 v[92:95], v0 offset:31744\nds_read_b128 v[56:59], v1 offset:4096\n" \
"ds_read_b128 v[60:63], v1 offset:5120\nv_mov_b64 v[20:21], 0\nv_smfmac_f32_16x16x64_bf16 v[48:51], v[24:27], a[160:167], v4\nv_cmp_ge_i32 s[64:65], s62, v15\nv_smfmac_f32_16x16x64_bf16 v[52:55], v[24:27], v[64:71], v4\ns_cmp_eq_u64 s[64:65], 0\ns_cselect_b32 s61, s41, -2147483648\ns_waitcnt lgkmcnt(10)\n" \
"v_mov_b64 v[22:23], 0\nv_smfmac_f32_16x16x64_bf16 v[48:51], v[28:31], a[184:191], v4\nds_read_b128 v[64:67], v1 offset:6144\nds_read_b128 v[68:71], v1 offset:7168\nv_mov_b64 v[24:25], 0\nv_smfmac_f32_16x16x64_bf16 v[52:55], v[28:31], a[192:199], v4\ns_add_u32 m0, s57, 0\ns_add_u32 s58, s61, 0\n" \
"s_waitcnt lgkmcnt(11)\nbuffer_load_dwordx4 v0, s[36:39], s58 offen offset:0 sc1 lds\nv_smfmac_f32_16x16x64_bf16 v[48:51], v[32:35], a[208:215], v4\nv_mov_b64 v[26:27], 0\nv_smfmac_f32_16x16x64_bf16 v[52:55], v[32:35], a[224:231], v4\ns_waitcnt lgkmcnt(10)\nv_mov_b64 v[28:29], 0\nv_smfmac_f32_16x16x64_bf16 v[48:51], v[36:39], a[240:247], v4\n" \
"v_mov_b64 v[30:31], 0\nv_smfmac_f32_16x16x64_bf16 v[52:55], v[36:39], a[248:255], v4\ns_waitcnt lgkmcnt(9)\nbuffer_load_dwordx4 v0, s[36:39], s58 offen offset:1024 sc1 lds\nv_smfmac_f32_16x16x64_bf16 v[48:51], v[40:43], v[104:111], v4\ns_waitcnt lgkmcnt(2)\nv_mov_b64 v[32:33], 0\nv_smfmac_f32_16x16x64_bf16 v[52:55], v[40:43], v[56:63], v4\n" \
"v_mov_b64 v[34:35], 0\nv_smfmac_f32_16x16x64_bf16 v[48:51], v[44:47], v[128:135], v4\nv_mov_b64 v[36:37], 0\nv_smfmac_f32_16x16x64_bf16 v[52:55], v[44:47], v[136:143], v4\nds_read_b128 v[56:59], v1 offset:8192\nds_read_b128 v[60:63], v1 offset:9216\nv_mov_b64 v[38:39], 0\nv_smfmac_f32_16x16x64_bf16 v[48:51], v[80:83], v[152:159], v4\n" \
"buffer_load_dwordx4 v0, s[36:39], s58 offen offset:2048 sc1 lds\nv_smfmac_f32_16x16x64_bf16 v[52:55], v[80:83], v[168:175], v4\nv_mov_b64 v[40:41], 0\nv_smfmac_f32_16x16x64_bf16 v[48:51], v[84:87], v[184:191], v4\nv_mov_b64 v[42:43], 0\nv_smfmac_f32_16x16x64_bf16 v[52:55], v[84:87], v[192:199], v4\nv_mov_b64 v[44:45], 0\nv_smfmac_f32_16x16x64_bf16 v[48:51], v[88:91], v[208:215], v4\n" \
"s_waitcnt lgkmcnt(2)\nbuffer_load_dwordx4 v0, s[36:39], s58 offen offset:3072 sc1 lds\nv_smfmac_f32_16x16x64_bf16 v[52:55], v[88:91], v[64:71], v4\nv_mov_b64 v[46:47], 0\nv_smfmac_f32_16x16x64_bf16 v[48:51], v[92:95], v[232:239], v4\nds_read_b128 v[64:67], v1 offset:10240\nds_read_b128 v[68:71], v1 offset:11264\nv_smfmac_f32_16x16x64_bf16 v[52:55], v[92:95], v[240:247], v4\n" \
"s_nop 3\nv_add_f32 v48, v48, v50\nv_add_f32 v49, v49, v51\ns_nop 1\nv_add_f32 v52, v52, v54\nv_add_f32 v53, v53, v55\nv_cvt_pk_bf16_f32 v14, v48, v52\nv_cvt_pk_bf16_f32 v15, v49, v53\n" \
"ds_write2_b32 v5, v14, v15 offset0:0 offset1:4\ns_branch .Lvu_u%=\n.Lvu_re0%=:\ns_add_u32 m0, s57, 16384\nv_max_u32 v16, v80, v82\nv_cmp_eq_u32 vcc, -1, v16\ns_cbranch_vccz .Lvu_sk1297%=\ns_mov_b64 exec, vcc\n" \
"buffer_load_dwordx4 v3, s[36:39], s42 offen offset:0 sc1 lds\ns_mov_b64 exec, -1\n.Lvu_sk1297%=:\nv_max_u32 v16, v84, v86\nv_cmp_eq_u32 vcc, -1, v16\ns_cbranch_vccz .Lvu_sk1304%=\ns_mov_b64 exec, vcc\nbuffer_load_dwordx4 v3, s[36:39], s42 offen offset:1024 sc1 lds\n" \
"s_mov_b64 exec, -1\n.Lvu_sk1304%=:\nv_max_u32 v16, v88, v90\nv_cmp_eq_u32 vcc, -1, v16\ns_cbranch_vccz .Lvu_sk1311%=\ns_mov_b64 exec, vcc\nbuffer_load_dwordx4 v3, s[36:39], s42 offen offset:2048 sc1 lds\ns_mov_b64 exec, -1\n" \
".Lvu_sk1311%=:\nv_max_u32 v16, v92, v94\nv_cmp_eq_u32 vcc, -1, v16\ns_cbranch_vccz .Lvu_sk1318%=\ns_mov_b64 exec, vcc\nbuffer_load_dwordx4 v3, s[36:39], s42 offen offset:3072 sc1 lds\ns_mov_b64 exec, -1\n.Lvu_sk1318%=:\n" \
"s_mov_b32 m0, 159744\ns_nop 0\nbuffer_load_dword v8, s[36:39], s56 offen sc1 lds\ns_waitcnt vmcnt(0)\nds_read_b128 v[80:83], v14 offset:16384\nds_read_b128 v[84:87], v14 offset:17408\nds_read_b128 v[88:91], v14 offset:18432\nds_read_b128 v[92:95], v14 offset:19456\n" \
"s_waitcnt lgkmcnt(2)\nv_max3_u32 v16, v80, v82, v84\ns_waitcnt lgkmcnt(1)\nv_max3_u32 v16, v16, v86, v88\ns_waitcnt lgkmcnt(0)\nv_max3_u32 v16, v16, v90, v92\nv_max_u32 v16, v16, v94\nv_cmp_eq_u32 vcc, -1, v16\n" \
"s_add_u32 s43, s43, 1\ns_cmp_lt_u32 s43, 262144\ns_cbranch_scc1 .Lvu_poll0%=\ns_branch .Lvu_abort%=\n.Lvu_c1%=:\nds_read_b128 v[56:59], v1 offset:2048\nds_read_b128 v[60:63], v1 offset:3072\nds_read_b128 v[64:67], v1 offset:4096\n" \
"ds_read_b128 v[68:71], v1 offset:5120\n.Lvu_poll1%=:\ns_cbranch_vccnz .Lvu_re1%=\ns_barrier\nv_smfmac_f32_16x16x64_bf16 v[48:51], v[80:83], a[104:111], v4\ns_memrealtime s[68:69]\nds_read_b128 v[16:19], v0 offset:24576\nv_mov_b32 v72, s40\n" \
"v_smfmac_f32_16x16x64_bf16 v[52:55], v[80:83], a[120:127], v4\nds_read_b128 v[20:23], v0 offset:25600\nds_read_b32 v15, v15\nv_smfmac_f32_16x16x64_bf16 v[48:51], v[84:87], a[136:143], v4\nds_read_b128 v[24:27], v0 offset:26624\nv_smfmac_f32_16x16x64_bf16 v[52:55], v[84:87], a[144:151], v4\nds_read_b128 v[28:31], v0 offset:27648\nv_smfmac_f32_16x16x64_bf16 v[48:51], v[88:91], a[160:167], v4\n" \
"s_waitcnt lgkmcnt(7)\nds_read_b128 v[32:35], v0 offset:28672\nv_smfmac_f32_16x16x64_bf16 v[52:55], v[88:91], v[56:63], v4\nds_read_b128 v[36:39], v0 offset:29696\nv_smfmac_f32_16x16x64_bf16 v[48:51], v[92:95], a[184:191], v4\nds_read_b128 v[40:43], v0 offset:30720\nv_smfmac_f32_16x16x64_bf16 v[52:55], v[92:95], a[192:199], v4\ns_waitcnt lgkmcnt(7)\n" \
"ds_read_b128 v[44:47], v0 offset:31744\nv_smfmac_f32_16x16x64_bf16 v[48:51], v[16:19], a[208:215], v4\nds_read_b128 v[80:83], v0 offset:16384\nv_smfmac_f32_16x16x64_bf16 v[52:55], v[16:19], a[224:231], v4\ns_waitcnt lgkmcnt(8)\nds_read_b128 v[84:87], v0 offset:17408\nv_mov_b64 v[16:17], 0\nv_smfmac_f32_16x16x64_bf16 v[48:51], v[20:23], a[240:247], v4\n" \
"ds_read_b128 v[88:91], v0 offset:18432\nv_mov_b64 v[18:19], 0\nv_smfmac_f32_16x16x64_bf16 v[52:55], v[20:23], a[248:255], v4\ns_waitcnt lgkmcnt(8)\nds_read_b128 v[92:95], v0 offset:19456\nds_read_b128 v[56:59], v1 offset:6144\nds_read_b128 v[60:63], v1 offset:7168\nv_mov_b64 v[20:21], 0\n" \
"v_smfmac_f32_16x16x64_bf16 v[48:51], v[24:27], v[104:111], v4\nv_cmp_ge_i32 s[64:65], s62, v15\nv_smfmac_f32_16x16x64_bf16 v[52:55], v[24:27], v[64:71], v4\ns_cmp_eq_u64 s[64:65], 0\ns_cselect_b32 s61, s41, -2147483648\ns_waitcnt lgkmcnt(10)\nv_mov_b64 v[22:23], 0\nv_smfmac_f32_16x16x64_bf16 v[48:51], v[28:31], v[128:135], v4\n" \
"ds_read_b128 v[64:67], v1 offset:0\nds_read_b128 v[68:71], v1 offset:1024\nv_mov_b64 v[24:25], 0\nv_smfmac_f32_16x16x64_bf16 v[52:55], v[28:31], v[136:143], v4\ns_add_u32 m0, s57, 0\ns_add_u32 s58, s61, 0\ns_waitcnt lgkmcnt(11)\nbuffer_load_dwordx4 v0, s[36:39], s58 offen offset:0 sc1 lds\n" \
"v_smfmac_f32_16x16x64_bf16 v[48:51], v[32:35], v[152:159], v4\ns_waitcnt lgkmcnt(0)\nv_mov_b32 v73, s68\nv_mov_b64 v[26:27], 0\nv_smfmac_f32_16x16x64_bf16 v[52:55], v[32:35], v[168:175], v4\ns_mov_b64 exec, 1\nbuffer_store_dwordx2 v[72:73], off, s[36:39], %[sg] sc1\ns_mov_b64 exec, -1\n" \
"v_mov_b64 v[28:29], 0\nv_smfmac_f32_16x16x64_bf16 v[48:51], v[36:39], v[184:191], v4\nv_mov_b64 v[30:31], 0\nv_smfmac_f32_16x16x64_bf16 v[52:55], v[36:39], v[192:199], v4\nbuffer_load_dwordx4 v0, s[36:39], s58 offen offset:1024 sc1 lds\nv_smfmac_f32_16x16x64_bf16 v[48:51], v[40:43], v[208:215], v4\nv_mov_b64 v[32:33], 0\nv_smfmac_f32_16x16x64_bf16 v[52:55], v[40:43], v[56:63], v4\n" \
"v_mov_b64 v[34:35], 0\nv_smfmac_f32_16x16x64_bf16 v[48:51], v[44:47], v[232:239], v4\nv_mov_b64 v[36:37], 0\nv_smfmac_f32_16x16x64_bf16 v[52:55], v[44:47], v[240:247], v4\nds_read_b128 v[56:59], v1 offset:8192\nds_read_b128 v[60:63], v1 offset:9216\nv_mov_b64 v[38:39], 0\nv_smfmac_f32_16x16x64_bf16 v[48:51], v[80:83], a[0:7], v4\n" \
"buffer_load_dwordx4 v0, s[36:39], s58 offen offset:2048 sc1 lds\nv_smfmac_f32_16x16x64_bf16 v[52:55], v[80:83], a[16:23], v4\nv_mov_b64 v[40:41], 0\nv_smfmac_f32_16x16x64_bf16 v[48:51], v[84:87], a[32:39], v4\nv_mov_b64 v[42:43], 0\nv_smfmac_f32_16x16x64_bf16 v[52:55], v[84:87], a[40:47], v4\nv_mov_b64 v[44:45], 0\nv_smfmac_f32_16x16x64_bf16 v[48:51], v[88:91], a[56:63], v4\n" \
"buffer_load_dwordx4 v0, s[36:39], s58 offen offset:3072 sc1 lds\nv_smfmac_f32_16x16x64_bf16 v[52:55], v[88:91], v[64:71], v4\nv_mov_b64 v[46:47], 0\nv_smfmac_f32_16x16x64_bf16 v[48:51], v[92:95], a[80:87], v4\nds_read_b128 v[64:67], v1 offset:10240\nds_read_b128 v[68:71], v1 offset:11264\nv_smfmac_f32_16x16x64_bf16 v[52:55], v[92:95], a[88:95], v4\ns_nop 3\n" \
"v_add_f32 v48, v48, v50\nv_add_f32 v49, v49, v51\ns_nop 1\nv_add_f32 v52, v52, v54\nv_add_f32 v53, v53, v55\nv_cvt_pk_bf16_f32 v14, v48, v52\nv_cvt_pk_bf16_f32 v15, v49, v53\nds_write2_b32 v5, v14, v15 offset0:0 offset1:4\n" \
"s_branch .Lvu_u%=\n.Lvu_re1%=:\ns_add_u32 m0, s57, 16384\nv_max_u32 v16, v80, v82\nv_cmp_eq_u32 vcc, -1, v16\ns_cbranch_vccz .Lvu_sk1453%=\ns_mov_b64 exec, vcc\nbuffer_load_dwordx4 v3, s[36:39], s42 offen offset:0 sc1 lds\n" \
"s_mov_b64 exec, -1\n.Lvu_sk1453%=:\nv_max_u32 v16, v84, v86\nv_cmp_eq_u32 vcc, -1, v16\ns_cbranch_vccz .Lvu_sk1460%=\ns_mov_b64 exec, vcc\nbuffer_load_dwordx4 v3, s[36:39], s42 offen offset:1024 sc1 lds\ns_mov_b64 exec, -1\n" \
".Lvu_sk1460%=:\nv_max_u32 v16, v88, v90\nv_cmp_eq_u32 vcc, -1, v16\ns_cbranch_vccz .Lvu_sk1467%=\ns_mov_b64 exec, vcc\nbuffer_load_dwordx4 v3, s[36:39], s42 offen offset:2048 sc1 lds\ns_mov_b64 exec, -1\n.Lvu_sk1467%=:\n" \
"v_max_u32 v16, v92, v94\nv_cmp_eq_u32 vcc, -1, v16\ns_cbranch_vccz .Lvu_sk1474%=\ns_mov_b64 exec, vcc\nbuffer_load_dwordx4 v3, s[36:39], s42 offen offset:3072 sc1 lds\ns_mov_b64 exec, -1\n.Lvu_sk1474%=:\ns_waitcnt vmcnt(0)\n" \
"ds_read_b128 v[80:83], v14 offset:16384\nds_read_b128 v[84:87], v14 offset:17408\nds_read_b128 v[88:91], v14 offset:18432\nds_read_b128 v[92:95], v14 offset:19456\ns_waitcnt lgkmcnt(2)\nv_max3_u32 v16, v80, v82, v84\ns_waitcnt lgkmcnt(1)\nv_max3_u32 v16, v16, v86, v88\n" \
"s_waitcnt lgkmcnt(0)\nv_max3_u32 v16, v16, v90, v92\nv_max_u32 v16, v16, v94\nv_cmp_eq_u32 vcc, -1, v16\ns_add_u32 s43, s43, 1\ns_cmp_lt_u32 s43, 262144\ns_cbranch_scc1 .Lvu_poll1%=\ns_branch .Lvu_abort%=\n" \
".Lvu_c2%=:\nds_read_b128 v[56:59], v1 offset:4096\nds_read_b128 v[60:63], v1 offset:5120\nds_read_b128 v[64:67], v1 offset:6144\nds_read_b128 v[68:71], v1 offset:7168\n.Lvu_poll2%=:\ns_cbranch_vccnz .Lvu_re2%=\ns_barrier\n" \
"v_smfmac_f32_16x16x64_bf16 v[48:51], v[80:83], a[208:215], v4\nds_read_b128 v[16:19], v0 offset:28672\nv_smfmac_f32_16x16x64_bf16 v[52:55], v[80:83], a[224:231], v4\nds_read_b128 v[20:23], v0 offset:29696\nds_read_b32 v15, v15\nv_smfmac_f32_16x16x64_bf16 v[48:51], v[84:87], a[240:247], v4\nds_read_b128 v[24:27], v0 offset:30720\nv_smfmac_f32_16x16x64_bf16 v[52:55], v[84:87], a[248:255], v4\n" \
"ds_read_b128 v[28:31], v0 offset:31744\nv_smfmac_f32_16x16x64_bf16 v[48:51], v[88:91], v[104:111], v4\ns_waitcnt lgkmcnt(7)\nds_read_b128 v[32:35], v0 offset:16384\nv_smfmac_f32_16x16x64_bf16 v[52:55], v[88:91], v[56:63], v4\nds_read_b128 v[36:39], v0 offset:17408\nv_smfmac_f32_16x16x64_bf16 v[48:51], v[92:95], v[128:135], v4\nds_read_b128 v[40:43], v0 offset:18432\n" \
"v_smfmac_f32_16x16x64_bf16 v[52:55], v[92:95], v[136:143], v4\ns_waitcnt lgkmcnt(7)\nds_read_b128 v[44:47], v0 offset:19456\nv_smfmac_f32_16x16x64_bf16 v[48:51], v[16:19], v[152:159], v4\nds_read_b128 v[80:83], v0 offset:20480\nv_smfmac_f32_16x16x64_bf16 v[52:55], v[16:19], v[168:175], v4\ns_waitcnt lgkmcnt(8)\nds_read_b128 v[84:87], v0 offset:21504\n" \
"v_mov_b64 v[16:17], 0\nv_smfmac_f32_16x16x64_bf16 v[48:51], v[20:23], v[184:191], v4\nds_read_b128 v[88:91], v0 offset:22528\nv_mov_b64 v[18:19], 0\nv_smfmac_f32_16x16x64_bf16 v[52:55], v[20:23], v[192:199], v4\ns_waitcnt lgkmcnt(8)\nds_read_b128 v[92:95], v0 offset:23552\nds_read_b128 v[56:59], v1 offset:0\n" \
"ds_read_b128 v[60:63], v1 offset:1024\nv_mov_b64 v[20:21], 0\nv_smfmac_f32_16x16x64_bf16 v[48:51], v[24:27], v[208:215], v4\nv_cmp_ge_i32 s[64:65], s62, v15\nv_smfmac_f32_16x16x64_bf16 v[52:55], v[24:27], v[64:71], v4\ns_cmp_eq_u64 s[64:65], 0\ns_cselect_b32 s61, s41, -2147483648\ns_waitcnt lgkmcnt(10)\n" \
"v_mov_b64 v[22:23], 0\nv_smfmac_f32_16x16x64_bf16 v[48:51], v[28:31], v[232:239], v4\nds_read_b128 v[64:67], v1 offset:2048\nds_read_b128 v[68:71], v1 offset:3072\nv_mov_b64 v[24:25], 0\nv_smfmac_f32_16x16x64_bf16 v[52:55], v[28:31], v[240:247], v4\ns_add_u32 m0, s57, 0\ns_add_u32 s58, s61, 0\n" \
"s_waitcnt lgkmcnt(11)\nbuffer_load_dwordx4 v0, s[36:39], s58 offen offset:0 sc1 lds\nv_smfmac_f32_16x16x64_bf16 v[48:51], v[32:35], a[0:7], v4\nv_mov_b64 v[26:27], 0\nv_smfmac_f32_16x16x64_bf16 v[52:55], v[32:35], a[16:23], v4\ns_waitcnt lgkmcnt(10)\nv_mov_b64 v[28:29], 0\nv_smfmac_f32_16x16x64_bf16 v[48:51], v[36:39], a[32:39], v4\n" \
"v_mov_b64 v[30:31], 0\nv_smfmac_f32_16x16x64_bf16 v[52:55], v[36:39], a[40:47], v4\ns_waitcnt lgkmcnt(9)\nbuffer_load_dwordx4 v0, s[36:39], s58 offen offset:1024 sc1 lds\nv_smfmac_f32_16x16x64_bf16 v[48:51], v[40:43], a[56:63], v4\ns_waitcnt lgkmcnt(2)\nv_mov_b64 v[32:33], 0\nv_smfmac_f32_16x16x64_bf16 v[52:55], v[40:43], v[56:63], v4\n" \
"v_mov_b64 v[34:35], 0\nv_smfmac_f32_16x16x64_bf16 v[48:51], v[44:47], a[80:87], v4\nv_mov_b64 v[36:37], 0\nv_smfmac_f32_16x16x64_bf16 v[52:55], v[44:47], a[88:95], v4\nds_read_b128 v[56:59], v1 offset:8192\nds_read_b128 v[60:63], v1 offset:9216\nv_mov_b64 v[38:39], 0\nv_smfmac_f32_16x16x64_bf16 v[48:51], v[80:83], a[104:111], v4\n" \
"buffer_load_dwordx4 v0, s[36:39], s58 offen offset:2048 sc1 lds\nv_smfmac_f32_16x16x64_bf16 v[52:55], v[80:83], a[120:127], v4\nv_mov_b64 v[40:41], 0\nv_smfmac_f32_16x16x64_bf16 v[48:51], v[84:87], a[136:143], v4\nv_mov_b64 v[42:43], 0\nv_smfmac_f32_16x16x64_bf16 v[52:55], v[84:87], a[144:151], v4\nv_mov_b64 v[44:45], 0\nv_smfmac_f32_16x16x64_bf16 v[48:51], v[88:91], a[160:167], v4\n" \
"s_waitcnt lgkmcnt(2)\nbuffer_load_dwordx4 v0, s[36:39], s58 offen offset:3072 sc1 lds\nv_smfmac_f32_16x16x64_bf16 v[52:55], v[88:91], v[64:71], v4\nv_mov_b64 v[46:47], 0\nv_smfmac_f32_16x16x64_bf16 v[48:51], v[92:95], a[184:191], v4\nds_read_b128 v[64:67], v1 offset:10240\nds_read_b128 v[68:71], v1 offset:11264\nv_smfmac_f32_16x16x64_bf16 v[52:55], v[92:95], a[192:199], v4\n" \
"s_nop 3\nv_add_f32 v48, v48, v50\nv_add_f32 v49, v49, v51\ns_nop 1\nv_add_f32 v52, v52, v54\nv_add_f32 v53, v53, v55\nv_cvt_pk_bf16_f32 v14, v48, v52\nv_cvt_pk_bf16_f32 v15, v49, v53\n" \
"ds_write2_b32 v5, v14, v15 offset0:0 offset1:4\ns_branch .Lvu_u%=\n.Lvu_re2%=:\ns_add_u32 m0, s57, 16384\nv_max_u32 v16, v80, v82\nv_cmp_eq_u32 vcc, -1, v16\ns_cbranch_vccz .Lvu_sk1604%=\ns_mov_b64 exec, vcc\n" \
"buffer_load_dwordx4 v3, s[36:39], s42 offen offset:0 sc1 lds\ns_mov_b64 exec, -1\n.Lvu_sk1604%=:\nv_max_u32 v16, v84, v86\nv_cmp_eq_u32 vcc, -1, v16\ns_cbranch_vccz .Lvu_sk1611%=\ns_mov_b64 exec, vcc\nbuffer_load_dwordx4 v3, s[36:39], s42 offen offset:1024 sc1 lds\n" \
"s_mov_b64 exec, -1\n.Lvu_sk1611%=:\nv_max_u32 v16, v88, v90\nv_cmp_eq_u32 vcc, -1, v16\ns_cbranch_vccz .Lvu_sk1618%=\ns_mov_b64 exec, vcc\nbuffer_load_dwordx4 v3, s[36:39], s42 offen offset:2048 sc1 lds\ns_mov_b64 exec, -1\n" \
".Lvu_sk1618%=:\nv_max_u32 v16, v92, v94\nv_cmp_eq_u32 vcc, -1, v16\ns_cbranch_vccz .Lvu_sk1625%=\ns_mov_b64 exec, vcc\nbuffer_load_dwordx4 v3, s[36:39], s42 offen offset:3072 sc1 lds\ns_mov_b64 exec, -1\n.Lvu_sk1625%=:\n" \
"s_waitcnt vmcnt(0)\nds_read_b128 v[80:83], v14 offset:16384\nds_read_b128 v[84:87], v14 offset:17408\nds_read_b128 v[88:91], v14 offset:18432\nds_read_b128 v[92:95], v14 offset:19456\ns_waitcnt lgkmcnt(2)\nv_max3_u32 v16, v80, v82, v84\ns_waitcnt lgkmcnt(1)\n" \
"v_max3_u32 v16, v16, v86, v88\ns_waitcnt lgkmcnt(0)\nv_max3_u32 v16, v16, v90, v92\nv_max_u32 v16, v16, v94\nv_cmp_eq_u32 vcc, -1, v16\ns_add_u32 s43, s43, 1\ns_cmp_lt_u32 s43, 262144\ns_cbranch_scc1 .Lvu_poll2%=\n" \
"s_branch .Lvu_abort%=\n.Lvu_c3%=:\nds_read_b128 v[56:59], v1 offset:6144\nds_read_b128 v[60:63], v1 offset:7168\nds_read_b128 v[64:67], v1 offset:0\nds_read_b128 v[68:71], v1 offset:1024\n.Lvu_poll3%=:\ns_cbranch_vccnz .Lvu_re3%=\n" \
"s_barrier\nv_smfmac_f32_16x16x64_bf16 v[48:51], v[80:83], v[152:159], v4\nds_read_b128 v[16:19], v0 offset:16384\nv_smfmac_f32_16x16x64_bf16 v[52:55], v[80:83], v[168:175], v4\nds_read_b128 v[20:23], v0 offset:17408\nds_read_b32 v15, v15\nv_smfmac_f32_16x16x64_bf16 v[48:51], v[84:87], v[184:191], v4\nds_read_b128 v[24:27], v0 offset:18432\n" \
"v_smfmac_f32_16x16x64_bf16 v[52:55], v[84:87], v[192:199], v4\nds_read_b128 v[28:31], v0 offset:19456\nv_smfmac_f32_16x16x64_bf16 v[48:51], v[88:91], v[208:215], v4\ns_waitcnt lgkmcnt(7)\nds_read_b128 v[32:35], v0 offset:20480\nv_smfmac_f32_16x16x64_bf16 v[52:55], v[88:91], v[56:63], v4\nds_read_b128 v[36:39], v0 offset:21504\nv_smfmac_f32_16x16x64_bf16 v[48:51], v[92:95], v[232:239], v4\n" \
"ds_read_b128 v[40:43], v0 offset:22528\nv_smfmac_f32_16x16x64_bf16 v[52:55], v[92:95], v[240:247], v4\ns_waitcnt lgkmcnt(7)\nds_read_b128 v[44:47], v0 offset:23552\nv_smfmac_f32_16x16x64_bf16 v[48:51], v[16:19], a[0:7], v4\nds_read_b128 v[80:83], v0 offset:24576\nv_smfmac_f32_16x16x64_bf16 v[52:55], v[16:19], a[16:23], v4\ns_waitcnt lgkmcnt(8)\n" \
"ds_read_b128 v[84:87], v0 offset:25600\nv_mov_b64 v[16:17], 0\nv_smfmac_f32_16x16x64_bf16 v[48:51], v[20:23], a[32:39], v4\nds_read_b128 v[88:91], v0 offset:26624\nv_mov_b64 v[18:19], 0\nv_smfmac_f32_16x16x64_bf16 v[52:55], v[20:23], a[40:47], v4\ns_waitcnt lgkmcnt(8)\nds_read_b128 v[92:95], v0 offset:27648\n" \
"ds_read_b128 v[56:59], v1 offset:2048\nds_read_b128 v[60:63], v1 offset:3072\nv_mov_b64 v[20:21], 0\nv_smfmac_f32_16x16x64_bf16 v[48:51], v[24:27], a[56:63], v4\nv_cmp_ge_i32 s[64:65], s62, v15\nv_smfmac_f32_16x16x64_bf16 v[52:55], v[24:27], v[64:71], v4\ns_cmp_eq_u64 s[64:65], 0\ns_cselect_b32 s61, s41, -2147483648\n" \
"s_waitcnt lgkmcnt(10)\nv_mov_b64 v[22:23], 0\nv_smfmac_f32_16x16x64_bf16 v[48:51], v[28:31], a[80:87], v4\nds_read_b128 v[64:67], v1 offset:4096\nds_read_b128 v[68:71], v1 offset:5120\nv_mov_b64 v[24:25], 0\nv_smfmac_f32_16x16x64_bf16 v[52:55], v[28:31], a[88:95], v4\ns_add_u32 m0, s57, 0\n" \
"s_add_u32 s58, s61, 0\ns_waitcnt lgkmcnt(11)\nbuffer_load_dwordx4 v0, s[36:39], s58 offen offset:0 sc1 lds\nv_smfmac_f32_16x16x64_bf16 v[48:51], v[32:35], a[104:111], v4\nv_mov_b64 v[26:27], 0\nv_smfmac_f32_16x16x64_bf16 v[52:55], v[32:35], a[120:127], v4\ns_waitcnt lgkmcnt(10)\nv_mov_b64 v[28:29], 0\n" \
"v_smfmac_f32_16x16x64_bf16 v[48:51], v[36:39], a[136:143], v4\nv_mov_b64 v[30:31], 0\nv_smfmac_f32_16x16x64_bf16 v[52:55], v[36:39], a[144:151], v4\ns_waitcnt lgkmcnt(9)\nbuffer_load_dwordx4 v0, s[36:39], s58 offen offset:1024 sc1 lds\nv_smfmac_f32_16x16x64_bf16 v[48:51], v[40:43], a[160:167], v4\ns_waitcnt lgkmcnt(2)\nv_mov_b64 v[32:33], 0\n" \
"v_smfmac_f32_16x16x64_bf16 v[52:55], v[40:43], v[56:63], v4\nv_mov_b64 v[34:35], 0\nv_smfmac_f32_16x16x64_bf16 v[48:51], v[44:47], a[184:191], v4\nv_mov_b64 v[36:37], 0\nv_smfmac_f32_16x16x64_bf16 v[52:55], v[44:47], a[192:199], v4\nds_read_b128 v[56:59], v1 offset:8192\nds_read_b128 v[60:63], v1 offset:9216\nv_mov_b64 v[38:39], 0\n" \
"v_smfmac_f32_16x16x64_bf16 v[48:51], v[80:83], a[208:215], v4\nbuffer_load_dwordx4 v0, s[36:39], s58 offen offset:2048 sc1 lds\nv_smfmac_f32_16x16x64_bf16 v[52:55], v[80:83], a[224:231], v4\nv_mov_b64 v[40:41], 0\nv_smfmac_f32_16x16x64_bf16 v[48:51], v[84:87], a[240:247], v4\nv_mov_b64 v[42:43], 0\nv_smfmac_f32_16x16x64_bf16 v[52:55], v[84:87], a[248:255], v4\nv_mov_b64 v[44:45], 0\n" \
"v_smfmac_f32_16x16x64_bf16 v[48:51], v[88:91], v[104:111], v4\ns_waitcnt lgkmcnt(2)\nbuffer_load_dwordx4 v0, s[36:39], s58 offen offset:3072 sc1 lds\nv_smfmac_f32_16x16x64_bf16 v[52:55], v[88:91], v[64:71], v4\nv_mov_b64 v[46:47], 0\nv_smfmac_f32_16x16x64_bf16 v[48:51], v[92:95], v[128:135], v4\nds_read_b128 v[64:67], v1 offset:10240\nds_read_b128 v[68:71], v1 offset:11264\n" \
"v_smfmac_f32_16x16x64_bf16 v[52:55], v[92:95], v[136:143], v4\ns_nop 3\nv_add_f32 v48, v48, v50\nv_add_f32 v49, v49, v51\ns_nop 1\nv_add_f32 v52, v52, v54\nv_add_f32 v53, v53, v55\nv_cvt_pk_bf16_f32 v14, v48, v52\n" \
"v_cvt_pk_bf16_f32 v15, v49, v53\nds_write2_b32 v5, v14, v15 offset0:0 offset1:4\n.Lvu_u%=:\nv_mov_b64 v[48:49], 0\nv_mov_b64 v[50:51], 0\nv_mov_b64 v[52:53], 0\nv_mov_b64 v[54:55], 0\ns_mov_b32 s59, 0x21000\n" \
"s_waitcnt lgkmcnt(0)\ns_barrier\nds_read_b128 v[80:83], v11 offset:57344\nds_read_b128 v[84:87], v11 offset:58368\nds_read_b128 v[88:91], v11 offset:59392\nds_read_b128 v[92:95], v11 offset:60416\ns_add_u32 m0, s57, 32768\ns_add_u32 s60, s61, 32768\n" \
"buffer_load_dwordx4 v0, s[36:39], s60 offen offset:0 sc1 lds\ns_waitcnt lgkmcnt(3)\nv_smfmac_f32_16x16x64_bf16 v[16:19], v[80:83], a[8:15], v10\ns_waitcnt lgkmcnt(2)\nv_smfmac_f32_16x16x64_bf16 v[16:19], v[84:87], a[24:31], v10\ns_waitcnt lgkmcnt(1)\nbuffer_load_dwordx4 v0, s[36:39], s60 offen offset:1024 sc1 lds\nv_smfmac_f32_16x16x64_bf16 v[16:19], v[88:91], v[56:63], v10\n" \
"s_waitcnt lgkmcnt(0)\nds_read_b128 v[56:59], v1 offset:12288\nv_smfmac_f32_16x16x64_bf16 v[16:19], v[92:95], a[48:55], v10\nds_read_b128 v[60:63], v1 offset:13312\nv_smfmac_f32_16x16x64_bf16 v[20:23], v[80:83], a[64:71], v10\nv_smfmac_f32_16x16x64_bf16 v[20:23], v[84:87], a[72:79], v10\nbuffer_load_dwordx4 v0, s[36:39], s60 offen offset:2048 sc1 lds\nv_smfmac_f32_16x16x64_bf16 v[20:23], v[88:91], v[64:71], v10\n" \
"ds_read_b128 v[64:67], v1 offset:14336\nv_smfmac_f32_16x16x64_bf16 v[20:23], v[92:95], a[96:103], v10\nds_read_b128 v[68:71], v1 offset:15360\nv_cvt_pk_bf16_f32 v72, v16, v17\nv_cvt_pk_bf16_f32 v73, v18, v19\nv_smfmac_f32_16x16x64_bf16 v[24:27], v[80:83], a[112:119], v10\nbuffer_load_dwordx4 v0, s[36:39], s60 offen offset:3072 sc1 lds\nv_and_or_b32 v72, v72, -2, v12\n" \
"v_smfmac_f32_16x16x64_bf16 v[24:27], v[84:87], a[128:135], v10\ns_waitcnt lgkmcnt(2)\nv_cvt_pk_bf16_f32 v74, v20, v21\nv_smfmac_f32_16x16x64_bf16 v[24:27], v[88:91], v[56:63], v10\nds_read_b128 v[56:59], v1 offset:16384\nv_cvt_pk_bf16_f32 v75, v22, v23\nv_smfmac_f32_16x16x64_bf16 v[24:27], v[92:95], a[152:159], v10\nds_read_b128 v[60:63], v1 offset:17408\n" \
"v_and_or_b32 v74, v74, -2, v12\nv_smfmac_f32_16x16x64_bf16 v[28:31], v[80:83], a[168:175], v10\nv_permlane16_swap_b32 v73, v75\nv_smfmac_f32_16x16x64_bf16 v[28:31], v[84:87], a[176:183], v10\ns_waitcnt lgkmcnt(2)\nv_permlane16_swap_b32 v72, v74\nv_smfmac_f32_16x16x64_bf16 v[28:31], v[88:91], v[64:71], v10\nds_read_b128 v[64:67], v1 offset:18432\n" \
"v_smfmac_f32_16x16x64_bf16 v[28:31], v[92:95], a[200:207], v10\ns_mov_b32 s44, 0\nds_read_b128 v[68:71], v1 offset:19456\nbuffer_store_dwordx4 v[72:75], v6, s[36:39], s44 offen sc1\nv_cvt_pk_bf16_f32 v76, v24, v25\nv_cvt_pk_bf16_f32 v77, v26, v27\nv_smfmac_f32_16x16x64_bf16 v[32:35], v[80:83], a[216:223], v10\nds_read_b128 v[16:19], v11 offset:49152\n" \
"v_and_or_b32 v76, v76, -2, v12\nv_smfmac_f32_16x16x64_bf16 v[32:35], v[84:87], a[232:239], v10\ns_waitcnt lgkmcnt(3)\nds_read_b128 v[20:23], v11 offset:50176\nv_cvt_pk_bf16_f32 v78, v28, v29\nv_smfmac_f32_16x16x64_bf16 v[32:35], v[88:91], v[56:63], v10\nds_read_b128 v[56:59], v1 offset:20480\nv_cvt_pk_bf16_f32 v79, v30, v31\n" \
"v_smfmac_f32_16x16x64_bf16 v[32:35], v[92:95], v[96:103], v10\nds_read_b128 v[60:63], v1 offset:21504\nv_and_or_b32 v78, v78, -2, v12\nv_smfmac_f32_16x16x64_bf16 v[36:39], v[80:83], v[112:119], v10\nds_read_b128 v[24:27], v11 offset:51200\nv_permlane16_swap_b32 v77, v79\nv_smfmac_f32_16x16x64_bf16 v[36:39], v[84:87], v[120:127], v10\ns_waitcnt lgkmcnt(5)\n" \
"ds_read_b128 v[28:31], v11 offset:52224\nv_permlane16_swap_b32 v76, v78\nv_smfmac_f32_16x16x64_bf16 v[36:39], v[88:91], v[64:71], v10\nds_read_b128 v[64:67], v1 offset:22528\nv_smfmac_f32_16x16x64_bf16 v[36:39], v[92:95], v[144:151], v10\ns_mov_b32 s44, 131072\nds_read_b128 v[68:71], v1 offset:23552\nbuffer_store_dwordx4 v[76:79], v6, s[36:39], s44 offen sc1\n" \
"v_cvt_pk_bf16_f32 v72, v32, v33\nv_cvt_pk_bf16_f32 v73, v34, v35\nv_smfmac_f32_16x16x64_bf16 v[40:43], v[80:83], v[160:167], v10\nds_read_b128 v[32:35], v11 offset:53248\nv_and_or_b32 v72, v72, -2, v12\nv_smfmac_f32_16x16x64_bf16 v[40:43], v[84:87], v[176:183], v10\ns_waitcnt lgkmcnt(5)\nv_cvt_pk_bf16_f32 v74, v36, v37\n" \
"v_smfmac_f32_16x16x64_bf16 v[40:43], v[88:91], v[56:63], v10\nds_read_b128 v[56:59], v1 offset:8192\nv_cvt_pk_bf16_f32 v75, v38, v39\nv_smfmac_f32_16x16x64_bf16 v[40:43], v[92:95], v[200:207], v10\nds_read_b128 v[60:63], v1 offset:9216\nv_and_or_b32 v74, v74, -2, v12\nv_smfmac_f32_16x16x64_bf16 v[44:47], v[80:83], v[216:223], v10\nds_read_b128 v[36:39], v11 offset:54272\n" \
"v_permlane16_swap_b32 v73, v75\nv_smfmac_f32_16x16x64_bf16 v[44:47], v[84:87], v[224:231], v10\ns_waitcnt lgkmcnt(4)\nv_permlane16_swap_b32 v72, v74\nv_smfmac_f32_16x16x64_bf16 v[44:47], v[88:91], v[64:71], v10\nds_read_b128 v[64:67], v1 offset:10240\nds_read_b128 v[88:91], v11 offset:55296\nv_smfmac_f32_16x16x64_bf16 v[44:47], v[92:95], v[248:255], v10\n" \
"s_mov_b32 s44, 4096\ns_cmp_ge_i32 s40, 63\nds_read_b128 v[68:71], v1 offset:11264\nds_read_b128 v[92:95], v11 offset:56320\nbuffer_store_dwordx4 v[72:75], v6, s[36:39], s44 offen sc1\nv_cvt_pk_bf16_f32 v76, v40, v41\nv_cvt_pk_bf16_f32 v77, v42, v43\ns_cbranch_scc1 .Lvu_last%=\n" \
".Lvu_y%=:\nv_smfmac_f32_16x16x64_bf16 v[48:51], v[16:19], a[8:15], v10\ns_add_u32 s40, s40, 1\nv_and_or_b32 v76, v76, -2, v12\nv_smfmac_f32_16x16x64_bf16 v[52:55], v[20:23], a[8:15], v10\ns_and_b32 s44, s40, 1\ns_lshl_b32 s55, s44, 22\nv_cvt_pk_bf16_f32 v78, v44, v45\n" \
"v_smfmac_f32_16x16x64_bf16 v[48:51], v[24:27], a[24:31], v10\ns_bfe_u32 s45, s40, 0x10001\nv_cvt_pk_bf16_f32 v79, v46, v47\nv_smfmac_f32_16x16x64_bf16 v[52:55], v[28:31], a[24:31], v10\ns_waitcnt lgkmcnt(5)\nv_and_or_b32 v78, v78, -2, v12\nv_smfmac_f32_16x16x64_bf16 v[48:51], v[32:35], v[56:63], v10\ns_waitcnt lgkmcnt(4)\n" \
"v_permlane16_swap_b32 v77, v79\nv_smfmac_f32_16x16x64_bf16 v[52:55], v[36:39], v[56:63], v10\ns_waitcnt lgkmcnt(2)\nv_permlane16_swap_b32 v76, v78\nv_smfmac_f32_16x16x64_bf16 v[48:51], v[88:91], a[48:55], v10\ns_waitcnt lgkmcnt(0)\nv_mov_b64 v[80:81], 0\nv_smfmac_f32_16x16x64_bf16 v[52:55], v[92:95], a[48:55], v10\n" \
"buffer_store_dwordx4 v[76:79], v6, s[36:39], s59 offen sc1\nv_mov_b64 v[82:83], 0\nv_mov_b64 v[84:85], 0\nv_mov_b64 v[86:87], 0\nv_mov_b32 v13, s45\nv_smfmac_f32_16x16x64_bf16 v[80:83], v[16:19], a[64:71], v10\nds_read_b128 v[56:59], v1 offset:12288\nv_cvt_pk_bf16_f32 v72, v48, v49\n" \
"v_smfmac_f32_16x16x64_bf16 v[84:87], v[20:23], a[64:71], v10\nds_read_b128 v[60:63], v1 offset:13312\nv_cvt_pk_bf16_f32 v73, v50, v51\nv_smfmac_f32_16x16x64_bf16 v[80:83], v[24:27], a[72:79], v10\nv_cvt_pk_bf16_f32 v74, v52, v53\nv_smfmac_f32_16x16x64_bf16 v[84:87], v[28:31], a[72:79], v10\ns_add_u32 s60, s55, 0\nv_cvt_pk_bf16_f32 v75, v54, v55\n" \
"v_smfmac_f32_16x16x64_bf16 v[80:83], v[32:35], v[64:71], v10\nv_and_or_b32 v72, v72, -2, v13\nv_smfmac_f32_16x16x64_bf16 v[84:87], v[36:39], v[64:71], v10\ns_and_b32 s42, s40, 3\ns_lshl_b32 s42, s42, 15\nv_and_or_b32 v74, v74, -2, v13\nv_smfmac_f32_16x16x64_bf16 v[80:83], v[88:91], a[96:103], v10\nbuffer_store_dwordx4 v[72:75], v7, s[36:39], s60 offen offset:0 sc1\n" \
"v_mov_b64 v[48:49], 0\nv_mov_b64 v[50:51], 0\nv_mov_b64 v[52:53], 0\nv_mov_b64 v[54:55], 0\nv_smfmac_f32_16x16x64_bf16 v[84:87], v[92:95], a[96:103], v10\nv_mov_b32 v12, s44\nv_smfmac_f32_16x16x64_bf16 v[48:51], v[16:19], a[112:119], v10\nds_read_b128 v[64:67], v1 offset:14336\n" \
"v_cvt_pk_bf16_f32 v76, v80, v81\nv_smfmac_f32_16x16x64_bf16 v[52:55], v[20:23], a[112:119], v10\nds_read_b128 v[68:71], v1 offset:15360\nv_cvt_pk_bf16_f32 v77, v82, v83\nv_smfmac_f32_16x16x64_bf16 v[48:51], v[24:27], a[128:135], v10\nv_cvt_pk_bf16_f32 v78, v84, v85\nv_smfmac_f32_16x16x64_bf16 v[52:55], v[28:31], a[128:135], v10\ns_add_u32 s60, s55, 4096\n" \
"s_waitcnt lgkmcnt(2)\nv_cvt_pk_bf16_f32 v79, v86, v87\nv_smfmac_f32_16x16x64_bf16 v[48:51], v[32:35], v[56:63], v10\nv_and_or_b32 v76, v76, -2, v13\nv_smfmac_f32_16x16x64_bf16 v[52:55], v[36:39], v[56:63], v10\nv_and_or_b32 v78, v78, -2, v13\nv_smfmac_f32_16x16x64_bf16 v[48:51], v[88:91], a[152:159], v10\nbuffer_store_dwordx4 v[76:79], v7, s[36:39], s60 offen offset:0 sc1\n" \
"v_mov_b64 v[80:81], 0\nv_mov_b64 v[82:83], 0\nv_mov_b64 v[84:85], 0\nv_mov_b64 v[86:87], 0\nv_smfmac_f32_16x16x64_bf16 v[52:55], v[92:95], a[152:159], v10\ns_waitcnt vmcnt(6)\ns_barrier\nv_smfmac_f32_16x16x64_bf16 v[80:83], v[16:19], a[168:175], v10\n" \
"ds_read_b128 v[56:59], v1 offset:16384\nv_cvt_pk_bf16_f32 v72, v48, v49\nv_smfmac_f32_16x16x64_bf16 v[84:87], v[20:23], a[168:175], v10\nds_read_b128 v[60:63], v1 offset:17408\nv_cvt_pk_bf16_f32 v73, v50, v51\nv_smfmac_f32_16x16x64_bf16 v[80:83], v[24:27], a[176:183], v10\ns_cmp_lt_u32 s40, 2\ns_cselect_b32 s54, %[fy], -2147483648\n" \
"v_cvt_pk_bf16_f32 v74, v52, v53\nv_smfmac_f32_16x16x64_bf16 v[84:87], v[28:31], a[176:183], v10\ns_add_u32 s60, s55, 16384\ns_waitcnt lgkmcnt(2)\nv_cvt_pk_bf16_f32 v75, v54, v55\nv_smfmac_f32_16x16x64_bf16 v[80:83], v[32:35], v[64:71], v10\nv_and_or_b32 v72, v72, -2, v13\nv_smfmac_f32_16x16x64_bf16 v[84:87], v[36:39], v[64:71], v10\n" \
"v_and_or_b32 v74, v74, -2, v13\nv_smfmac_f32_16x16x64_bf16 v[80:83], v[88:91], a[200:207], v10\nbuffer_store_dwordx4 v[72:75], v7, s[36:39], s60 offen offset:0 sc1\nv_mov_b64 v[48:49], 0\nv_mov_b64 v[50:51], 0\nv_mov_b64 v[52:53], 0\nv_mov_b64 v[54:55], 0\nv_smfmac_f32_16x16x64_bf16 v[84:87], v[92:95], a[200:207], v10\n" \
"v_smfmac_f32_16x16x64_bf16 v[48:51], v[16:19], a[216:223], v10\nds_read_b128 v[64:67], v1 offset:18432\nv_cvt_pk_bf16_f32 v76, v80, v81\nv_smfmac_f32_16x16x64_bf16 v[52:55], v[20:23], a[216:223], v10\nds_read_b128 v[68:71], v1 offset:19456\nv_cvt_pk_bf16_f32 v77, v82, v83\nv_smfmac_f32_16x16x64_bf16 v[48:51], v[24:27], a[232:239], v10\nv_cvt_pk_bf16_f32 v78, v84, v85\n" \
"v_smfmac_f32_16x16x64_bf16 v[52:55], v[28:31], a[232:239], v10\ns_add_u32 s60, s55, 20480\ns_waitcnt lgkmcnt(2)\nv_cvt_pk_bf16_f32 v79, v86, v87\nv_smfmac_f32_16x16x64_bf16 v[48:51], v[32:35], v[56:63], v10\nv_and_or_b32 v76, v76, -2, v13\nv_smfmac_f32_16x16x64_bf16 v[52:55], v[36:39], v[56:63], v10\nv_and_or_b32 v78, v78, -2, v13\n" \
"v_smfmac_f32_16x16x64_bf16 v[48:51], v[88:91], v[96:103], v10\nbuffer_store_dwordx4 v[76:79], v7, s[36:39], s60 offen offset:0 sc1\nv_mov_b64 v[80:81], 0\nv_mov_b64 v[82:83], 0\nv_mov_b64 v[84:85], 0\nv_mov_b64 v[86:87], 0\nv_smfmac_f32_16x16x64_bf16 v[52:55], v[92:95], v[96:103], v10\nv_smfmac_f32_16x16x64_bf16 v[80:83], v[16:19], v[112:119], v10\n" \
"ds_read_b128 v[56:59], v1 offset:20480\nv_cvt_pk_bf16_f32 v72, v48, v49\nv_smfmac_f32_16x16x64_bf16 v[84:87], v[20:23], v[112:119], v10\nds_read_b128 v[60:63], v1 offset:21504\nv_cvt_pk_bf16_f32 v73, v50, v51\nv_smfmac_f32_16x16x64_bf16 v[80:83], v[24:27], v[120:127], v10\nv_cvt_pk_bf16_f32 v74, v52, v53\nv_smfmac_f32_16x16x64_bf16 v[84:87], v[28:31], v[120:127], v10\n" \
"s_add_u32 s60, s55, 0\ns_waitcnt lgkmcnt(2)\nv_cvt_pk_bf16_f32 v75, v54, v55\nv_smfmac_f32_16x16x64_bf16 v[80:83], v[32:35], v[64:71], v10\nv_and_or_b32 v72, v72, -2, v13\nv_smfmac_f32_16x16x64_bf16 v[84:87], v[36:39], v[64:71], v10\nv_and_or_b32 v74, v74, -2, v13\nv_smfmac_f32_16x16x64_bf16 v[80:83], v[88:91], v[144:151], v10\n" \
"buffer_store_dwordx4 v[72:75], v7, s[36:39], s60 offen offset:2048 sc1\nv_mov_b64 v[48:49], 0\nv_mov_b64 v[50:51], 0\nv_mov_b64 v[52:53], 0\nv_mov_b64 v[54:55], 0\nv_smfmac_f32_16x16x64_bf16 v[84:87], v[92:95], v[144:151], v10\nv_smfmac_f32_16x16x64_bf16 v[48:51], v[16:19], v[160:167], v10\nds_read_b128 v[64:67], v1 offset:22528\n" \
"v_cvt_pk_bf16_f32 v76, v80, v81\nv_smfmac_f32_16x16x64_bf16 v[52:55], v[20:23], v[160:167], v10\nds_read_b128 v[68:71], v1 offset:23552\nv_cvt_pk_bf16_f32 v77, v82, v83\nv_smfmac_f32_16x16x64_bf16 v[48:51], v[24:27], v[176:183], v10\nds_read_b128 v[40:43], v11 offset:49280\nv_cvt_pk_bf16_f32 v78, v84, v85\nv_smfmac_f32_16x16x64_bf16 v[52:55], v[28:31], v[176:183], v10\n" \
"s_add_u32 s60, s55, 4096\ns_waitcnt lgkmcnt(3)\nv_cvt_pk_bf16_f32 v79, v86, v87\nv_smfmac_f32_16x16x64_bf16 v[48:51], v[32:35], v[56:63], v10\nds_read_b128 v[44:47], v11 offset:50304\nv_and_or_b32 v76, v76, -2, v13\nv_smfmac_f32_16x16x64_bf16 v[52:55], v[36:39], v[56:63], v10\nv_and_or_b32 v78, v78, -2, v13\n" \
"v_smfmac_f32_16x16x64_bf16 v[48:51], v[88:91], v[200:207], v10\nbuffer_store_dwordx4 v[76:79], v7, s[36:39], s60 offen offset:2048 sc1\nv_mov_b64 v[80:81], 0\nv_mov_b64 v[82:83], 0\nv_mov_b64 v[84:85], 0\nv_mov_b64 v[86:87], 0\nv_smfmac_f32_16x16x64_bf16 v[52:55], v[92:95], v[200:207], v10\nv_smfmac_f32_16x16x64_bf16 v[80:83], v[16:19], v[216:223], v10\n" \
"ds_read_b128 v[56:59], v1 offset:8192\nds_read_b128 v[16:19], v11 offset:51328\nv_cvt_pk_bf16_f32 v72, v48, v49\nv_smfmac_f32_16x16x64_bf16 v[84:87], v[20:23], v[216:223], v10\nds_read_b128 v[60:63], v1 offset:9216\nds_read_b128 v[20:23], v11 offset:52352\nv_cvt_pk_bf16_f32 v73, v50, v51\nv_smfmac_f32_16x16x64_bf16 v[80:83], v[24:27], v[224:231], v10\n" \
"ds_read_b128 v[24:27], v11 offset:53376\nv_cvt_pk_bf16_f32 v74, v52, v53\nv_smfmac_f32_16x16x64_bf16 v[84:87], v[28:31], v[224:231], v10\ns_add_u32 s60, s55, 16384\ns_waitcnt lgkmcnt(7)\nds_read_b128 v[28:31], v11 offset:54400\nv_cvt_pk_bf16_f32 v75, v54, v55\nv_smfmac_f32_16x16x64_bf16 v[80:83], v[32:35], v[64:71], v10\n" \
"ds_read_b128 v[32:35], v11 offset:55424\nv_and_or_b32 v72, v72, -2, v13\nv_smfmac_f32_16x16x64_bf16 v[84:87], v[36:39], v[64:71], v10\nds_read_b128 v[36:39], v11 offset:56448\nv_and_or_b32 v74, v74, -2, v13\nv_smfmac_f32_16x16x64_bf16 v[80:83], v[88:91], v[248:255], v10\nbuffer_store_dwordx4 v[72:75], v7, s[36:39], s60 offen offset:2048 sc1\nv_mov_b64 v[48:49], 0\n" \
"v_mov_b64 v[50:51], 0\nv_mov_b64 v[52:53], 0\nv_mov_b64 v[54:55], 0\nv_smfmac_f32_16x16x64_bf16 v[84:87], v[92:95], v[248:255], v10\ns_waitcnt lgkmcnt(9)\nv_smfmac_f32_16x16x64_bf16 v[48:51], v[40:43], a[8:15], v10\ns_waitcnt lgkmcnt(8)\nds_read_b128 v[64:67], v1 offset:10240\n" \
"v_cvt_pk_bf16_f32 v76, v80, v81\nv_smfmac_f32_16x16x64_bf16 v[52:55], v[44:47], a[8:15], v10\ns_waitcnt lgkmcnt(7)\nds_read_b128 v[68:71], v1 offset:11264\nv_cvt_pk_bf16_f32 v77, v82, v83\nv_smfmac_f32_16x16x64_bf16 v[48:51], v[16:19], a[24:31], v10\ns_waitcnt lgkmcnt(6)\nv_cvt_pk_bf16_f32 v78, v84, v85\n" \
"v_smfmac_f32_16x16x64_bf16 v[52:55], v[20:23], a[24:31], v10\ns_add_u32 s60, s55, 20480\ns_waitcnt lgkmcnt(5)\nv_cvt_pk_bf16_f32 v79, v86, v87\nv_smfmac_f32_16x16x64_bf16 v[48:51], v[24:27], v[56:63], v10\ns_waitcnt lgkmcnt(4)\nv_and_or_b32 v76, v76, -2, v13\nv_smfmac_f32_16x16x64_bf16 v[52:55], v[28:31], v[56:63], v10\n" \
"s_waitcnt lgkmcnt(3)\nv_and_or_b32 v78, v78, -2, v13\nv_smfmac_f32_16x16x64_bf16 v[48:51], v[32:35], a[48:55], v10\ns_waitcnt lgkmcnt(2)\nbuffer_store_dwordx4 v[76:79], v7, s[36:39], s60 offen offset:2048 sc1\nv_mov_b64 v[80:81], 0\nv_mov_b64 v[82:83], 0\nv_mov_b64 v[84:85], 0\n" \
"v_mov_b64 v[86:87], 0\nv_smfmac_f32_16x16x64_bf16 v[52:55], v[36:39], a[48:55], v10\nv_smfmac_f32_16x16x64_bf16 v[80:83], v[40:43], a[64:71], v10\nds_read_b128 v[56:59], v1 offset:12288\nv_cvt_pk_bf16_f32 v72, v48, v49\nv_smfmac_f32_16x16x64_bf16 v[84:87], v[44:47], a[64:71], v10\nds_read_b128 v[60:63], v1 offset:13312\nv_cvt_pk_bf16_f32 v73, v50, v51\n" \
"v_smfmac_f32_16x16x64_bf16 v[80:83], v[16:19], a[72:79], v10\nv_cvt_pk_bf16_f32 v74, v52, v53\nv_smfmac_f32_16x16x64_bf16 v[84:87], v[20:23], a[72:79], v10\ns_add_u32 s60, s55, 0\ns_waitcnt lgkmcnt(2)\nv_cvt_pk_bf16_f32 v75, v54, v55\nv_smfmac_f32_16x16x64_bf16 v[80:83], v[24:27], v[64:71], v10\nv_and_or_b32 v72, v72, -2, v13\n" \
"v_smfmac_f32_16x16x64_bf16 v[84:87], v[28:31], v[64:71], v10\nv_and_or_b32 v74, v74, -2, v13\nv_smfmac_f32_16x16x64_bf16 v[80:83], v[32:35], a[96:103], v10\nbuffer_store_dwordx4 v[72:75], v7, s[36:39], s60 offen offset:512 sc1\nv_mov_b64 v[48:49], 0\nv_mov_b64 v[50:51], 0\nv_mov_b64 v[52:53], 0\nv_mov_b64 v[54:55], 0\n" \
"v_smfmac_f32_16x16x64_bf16 v[84:87], v[36:39], a[96:103], v10\nv_smfmac_f32_16x16x64_bf16 v[48:51], v[40:43], a[112:119], v10\nds_read_b128 v[64:67], v1 offset:14336\nv_cvt_pk_bf16_f32 v76, v80, v81\nv_smfmac_f32_16x16x64_bf16 v[52:55], v[44:47], a[112:119], v10\nds_read_b128 v[68:71], v1 offset:15360\nv_cvt_pk_bf16_f32 v77, v82, v83\nv_smfmac_f32_16x16x64_bf16 v[48:51], v[16:19], a[128:135], v10\n" \
"v_cvt_pk_bf16_f32 v78, v84, v85\nv_smfmac_f32_16x16x64_bf16 v[52:55], v[20:23], a[128:135], v10\ns_add_u32 s60, s55, 4096\ns_waitcnt lgkmcnt(2)\nv_cvt_pk_bf16_f32 v79, v86, v87\nv_smfmac_f32_16x16x64_bf16 v[48:51], v[24:27], v[56:63], v10\nv_and_or_b32 v76, v76, -2, v13\nv_smfmac_f32_16x16x64_bf16 v[52:55], v[28:31], v[56:63], v10\n" \
"v_and_or_b32 v78, v78, -2, v13\nv_smfmac_f32_16x16x64_bf16 v[48:51], v[32:35], a[152:159], v10\nbuffer_store_dwordx4 v[76:79], v7, s[36:39], s60 offen offset:512 sc1\nv_mov_b64 v[80:81], 0\nv_mov_b64 v[82:83], 0\nv_mov_b64 v[84:85], 0\nv_mov_b64 v[86:87], 0\nv_smfmac_f32_16x16x64_bf16 v[52:55], v[36:39], a[152:159], v10\n" \
"v_smfmac_f32_16x16x64_bf16 v[80:83], v[40:43], a[168:175], v10\nds_read_b128 v[56:59], v1 offset:16384\nv_cvt_pk_bf16_f32 v72, v48, v49\nv_smfmac_f32_16x16x64_bf16 v[84:87], v[44:47], a[168:175], v10\nds_read_b128 v[60:63], v1 offset:17408\nv_cvt_pk_bf16_f32 v73, v50, v51\nv_smfmac_f32_16x16x64_bf16 v[80:83], v[16:19], a[176:183], v10\nv_cvt_pk_bf16_f32 v74, v52, v53\n" \
"v_smfmac_f32_16x16x64_bf16 v[84:87], v[20:23], a[176:183], v10\ns_add_u32 s60, s55, 16384\ns_waitcnt lgkmcnt(2)\nv_cvt_pk_bf16_f32 v75, v54, v55\nv_smfmac_f32_16x16x64_bf16 v[80:83], v[24:27], v[64:71], v10\nv_and_or_b32 v72, v72, -2, v13\nv_smfmac_f32_16x16x64_bf16 v[84:87], v[28:31], v[64:71], v10\nv_and_or_b32 v74, v74, -2, v13\n" \
"v_smfmac_f32_16x16x64_bf16 v[80:83], v[32:35], a[200:207], v10\nbuffer_store_dwordx4 v[72:75], v7, s[36:39], s60 offen offset:512 sc1\nv_mov_b64 v[48:49], 0\nv_mov_b64 v[50:51], 0\nv_mov_b64 v[52:53], 0\nv_mov_b64 v[54:55], 0\nv_smfmac_f32_16x16x64_bf16 v[84:87], v[36:39], a[200:207], v10\nv_smfmac_f32_16x16x64_bf16 v[48:51], v[40:43], a[216:223], v10\n" \
"ds_read_b128 v[64:67], v1 offset:18432\nv_cvt_pk_bf16_f32 v76, v80, v81\nv_smfmac_f32_16x16x64_bf16 v[52:55], v[44:47], a[216:223], v10\nds_read_b128 v[68:71], v1 offset:19456\nv_cvt_pk_bf16_f32 v77, v82, v83\nv_smfmac_f32_16x16x64_bf16 v[48:51], v[16:19], a[232:239], v10\nv_cvt_pk_bf16_f32 v78, v84, v85\nv_smfmac_f32_16x16x64_bf16 v[52:55], v[20:23], a[232:239], v10\n" \
"s_add_u32 s60, s55, 20480\ns_waitcnt lgkmcnt(2)\nv_cvt_pk_bf16_f32 v79, v86, v87\nv_smfmac_f32_16x16x64_bf16 v[48:51], v[24:27], v[56:63], v10\nv_and_or_b32 v76, v76, -2, v13\nv_smfmac_f32_16x16x64_bf16 v[52:55], v[28:31], v[56:63], v10\nv_and_or_b32 v78, v78, -2, v13\nv_smfmac_f32_16x16x64_bf16 v[48:51], v[32:35], v[96:103], v10\n" \
"buffer_store_dwordx4 v[76:79], v7, s[36:39], s60 offen offset:512 sc1\nv_mov_b64 v[80:81], 0\nv_mov_b64 v[82:83], 0\nv_mov_b64 v[84:85], 0\nv_mov_b64 v[86:87], 0\nv_smfmac_f32_16x16x64_bf16 v[52:55], v[36:39], v[96:103], v10\nv_smfmac_f32_16x16x64_bf16 v[80:83], v[40:43], v[112:119], v10\nds_read_b128 v[56:59], v1 offset:20480\n" \
"v_cvt_pk_bf16_f32 v72, v48, v49\nv_smfmac_f32_16x16x64_bf16 v[84:87], v[44:47], v[112:119], v10\nds_read_b128 v[60:63], v1 offset:21504\nv_cvt_pk_bf16_f32 v73, v50, v51\nv_smfmac_f32_16x16x64_bf16 v[80:83], v[16:19], v[120:127], v10\nv_cvt_pk_bf16_f32 v74, v52, v53\nv_smfmac_f32_16x16x64_bf16 v[84:87], v[20:23], v[120:127], v10\ns_add_u32 s60, s55, 0\n" \
"s_waitcnt lgkmcnt(2)\nv_cvt_pk_bf16_f32 v75, v54, v55\nv_smfmac_f32_16x16x64_bf16 v[80:83], v[24:27], v[64:71], v10\nv_and_or_b32 v72, v72, -2, v13\nv_smfmac_f32_16x16x64_bf16 v[84:87], v[28:31], v[64:71], v10\nv_and_or_b32 v74, v74, -2, v13\nv_smfmac_f32_16x16x64_bf16 v[80:83], v[32:35], v[144:151], v10\nbuffer_store_dwordx4 v[72:75], v7, s[36:39], s60 offen offset:2560 sc1\n" \
"v_mov_b64 v[48:49], 0\nv_mov_b64 v[50:51], 0\nv_mov_b64 v[52:53], 0\nv_mov_b64 v[54:55], 0\nv_smfmac_f32_16x16x64_bf16 v[84:87], v[36:39], v[144:151], v10\nv_smfmac_f32_16x16x64_bf16 v[48:51], v[40:43], v[160:167], v10\nds_read_b128 v[64:67], v1 offset:22528\nv_cvt_pk_bf16_f32 v76, v80, v81\n" \
"v_smfmac_f32_16x16x64_bf16 v[52:55], v[44:47], v[160:167], v10\nds_read_b128 v[68:71], v1 offset:23552\nv_cvt_pk_bf16_f32 v77, v82, v83\nv_smfmac_f32_16x16x64_bf16 v[48:51], v[16:19], v[176:183], v10\nv_cvt_pk_bf16_f32 v78, v84, v85\nv_smfmac_f32_16x16x64_bf16 v[52:55], v[20:23], v[176:183], v10\ns_add_u32 s60, s55, 4096\ns_waitcnt lgkmcnt(2)\n" \
"v_cvt_pk_bf16_f32 v79, v86, v87\nv_smfmac_f32_16x16x64_bf16 v[48:51], v[24:27], v[56:63], v10\nv_and_or_b32 v76, v76, -2, v13\nv_smfmac_f32_16x16x64_bf16 v[52:55], v[28:31], v[56:63], v10\nv_and_or_b32 v78, v78, -2, v13\nv_smfmac_f32_16x16x64_bf16 v[48:51], v[32:35], v[200:207], v10\nbuffer_store_dwordx4 v[76:79], v7, s[36:39], s60 offen offset:2560 sc1\nv_mov_b64 v[80:81], 0\n" \
"v_mov_b64 v[82:83], 0\nv_mov_b64 v[84:85], 0\nv_mov_b64 v[86:87], 0\nv_smfmac_f32_16x16x64_bf16 v[52:55], v[36:39], v[200:207], v10\ns_cmp_lg_u64 s[64:65], 0\ns_cbranch_scc1 .Lvu_slow%=\n.Lvu_slowr%=:\nds_read_b128 v[88:91], v2 offset:0\n" \
"v_smfmac_f32_16x16x64_bf16 v[80:83], v[40:43], v[216:223], v10\nds_read_b128 v[92:95], v2 offset:256\nv_cvt_pk_bf16_f32 v72, v48, v49\nv_smfmac_f32_16x16x64_bf16 v[84:87], v[44:47], v[216:223], v10\nv_cvt_pk_bf16_f32 v73, v50, v51\nv_smfmac_f32_16x16x64_bf16 v[80:83], v[16:19], v[224:231], v10\nds_read_b128 v[40:43], v2 offset:2048\nv_cvt_pk_bf16_f32 v74, v52, v53\n" \
"v_smfmac_f32_16x16x64_bf16 v[84:87], v[20:23], v[224:231], v10\ns_add_u32 s60, s55, 16384\ns_waitcnt lgkmcnt(3)\nds_read_b128 v[44:47], v2 offset:2304\nv_cvt_pk_bf16_f32 v75, v54, v55\nv_smfmac_f32_16x16x64_bf16 v[80:83], v[24:27], v[64:71], v10\nv_and_or_b32 v72, v72, -2, v13\nv_smfmac_f32_16x16x64_bf16 v[84:87], v[28:31], v[64:71], v10\n" \
"v_and_or_b32 v74, v74, -2, v13\nv_smfmac_f32_16x16x64_bf16 v[80:83], v[32:35], v[248:255], v10\nbuffer_store_dwordx4 v[72:75], v7, s[36:39], s60 offen offset:2560 sc1\nv_mov_b64 v[48:49], 0\nv_mov_b64 v[50:51], 0\nv_mov_b64 v[52:53], 0\nv_mov_b64 v[54:55], 0\nv_smfmac_f32_16x16x64_bf16 v[84:87], v[36:39], v[248:255], v10\n" \
"s_add_u32 s60, s55, 20480\ns_cmp_lt_i32 s40, 63\nv_mov_b32 v15, s40\ns_cbranch_scc1 .Lvu_tok%=\nv_cvt_pk_bf16_f32 v76, v80, v81\nv_cvt_pk_bf16_f32 v77, v82, v83\ns_nop 1\nv_cvt_pk_bf16_f32 v78, v84, v85\n" \
"v_cvt_pk_bf16_f32 v79, v86, v87\nv_and_or_b32 v76, v76, -2, v13\nv_and_or_b32 v78, v78, -2, v13\nbuffer_store_dwordx4 v[76:79], v7, s[36:39], s60 offen offset:2560 sc1\ns_add_u32 m0, s57, 16384\ns_nop 0\nbuffer_load_dwordx4 v3, s[36:39], s42 offen offset:0 sc1 lds\nbuffer_load_dwordx4 v3, s[36:39], s42 offen offset:1024 sc1 lds\n" \
"buffer_load_dwordx4 v3, s[36:39], s42 offen offset:2048 sc1 lds\nbuffer_load_dwordx4 v3, s[36:39], s42 offen offset:3072 sc1 lds\ns_mov_b32 s41, -2147483648\ns_mov_b32 m0, 159744\nv_mov_b32 v15, 159744\ns_waitcnt vmcnt(0) lgkmcnt(0)\nv_add_u32 v14, s57, v0\nds_read_b128 v[80:83], v14 offset:16384\n" \
"ds_read_b128 v[84:87], v14 offset:17408\nds_read_b128 v[88:91], v14 offset:18432\nds_read_b128 v[92:95], v14 offset:19456\ns_waitcnt lgkmcnt(2)\nv_max3_u32 v16, v80, v82, v84\ns_waitcnt lgkmcnt(1)\nv_max3_u32 v16, v16, v86, v88\ns_waitcnt lgkmcnt(0)\n" \
"v_max3_u32 v16, v16, v90, v92\nv_max_u32 v16, v16, v94\nv_cmp_eq_u32 vcc, -1, v16\ns_branch .Lvu_crit%=\n.Lvu_re3%=:\ns_add_u32 m0, s57, 16384\nv_max_u32 v16, v80, v82\nv_cmp_eq_u32 vcc, -1, v16\n" \
"s_cbranch_vccz .Lvu_sk2306%=\ns_mov_b64 exec, vcc\nbuffer_load_dwordx4 v3, s[36:39], s42 offen offset:0 sc1 lds\ns_mov_b64 exec, -1\n.Lvu_sk2306%=:\nv_max_u32 v16, v84, v86\nv_cmp_eq_u32 vcc, -1, v16\ns_cbranch_vccz .Lvu_sk2313%=\n" \
"s_mov_b64 exec, vcc\nbuffer_load_dwordx4 v3, s[36:39], s42 offen offset:1024 sc1 lds\ns_mov_b64 exec, -1\n.Lvu_sk2313%=:\nv_max_u32 v16, v88, v90\nv_cmp_eq_u32 vcc, -1, v16\ns_cbranch_vccz .Lvu_sk2320%=\ns_mov_b64 exec, vcc\n" \
"buffer_load_dwordx4 v3, s[36:39], s42 offen offset:2048 sc1 lds\ns_mov_b64 exec, -1\n.Lvu_sk2320%=:\nv_max_u32 v16, v92, v94\nv_cmp_eq_u32 vcc, -1, v16\ns_cbranch_vccz .Lvu_sk2327%=\ns_mov_b64 exec, vcc\nbuffer_load_dwordx4 v3, s[36:39], s42 offen offset:3072 sc1 lds\n" \
"s_mov_b64 exec, -1\n.Lvu_sk2327%=:\ns_waitcnt vmcnt(0)\nds_read_b128 v[80:83], v14 offset:16384\nds_read_b128 v[84:87], v14 offset:17408\nds_read_b128 v[88:91], v14 offset:18432\nds_read_b128 v[92:95], v14 offset:19456\ns_waitcnt lgkmcnt(2)\n" \
"v_max3_u32 v16, v80, v82, v84\ns_waitcnt lgkmcnt(1)\nv_max3_u32 v16, v16, v86, v88\ns_waitcnt lgkmcnt(0)\nv_max3_u32 v16, v16, v90, v92\nv_max_u32 v16, v16, v94\nv_cmp_eq_u32 vcc, -1, v16\ns_add_u32 s43, s43, 1\n" \
"s_cmp_lt_u32 s43, 262144\ns_cbranch_scc1 .Lvu_poll3%=\ns_branch .Lvu_abort%=\n.Lvu_slow%=:\ns_cmp_ge_i32 s40, 63\ns_cbranch_scc1 .Lvu_slowr%=\n.Lvu_f1%=:\nbuffer_load_dword v14, v8, s[36:39], s56 offen sc1\n" \
"s_waitcnt vmcnt(0)\nv_cmp_ge_i32 vcc, s40, v14\ns_cbranch_vccz .Lvu_f1fok%=\ns_add_u32 s43, s43, 1\ns_cmp_lt_u32 s43, 262144\ns_cbranch_scc1 .Lvu_f1%=\ns_branch .Lvu_abort%=\n.Lvu_f1fok%=:\n" \
"s_add_u32 m0, s57, 0\ns_add_u32 s58, s41, 0\nbuffer_load_dwordx4 v0, s[36:39], s58 offen offset:0 sc1 lds\nbuffer_load_dwordx4 v0, s[36:39], s58 offen offset:1024 sc1 lds\nbuffer_load_dwordx4 v0, s[36:39], s58 offen offset:2048 sc1 lds\nbuffer_load_dwordx4 v0, s[36:39], s58 offen offset:3072 sc1 lds\ns_add_u32 m0, s57, 32768\ns_add_u32 s58, s41, 32768\n" \
"buffer_load_dwordx4 v0, s[36:39], s58 offen offset:0 sc1 lds\nbuffer_load_dwordx4 v0, s[36:39], s58 offen offset:1024 sc1 lds\nbuffer_load_dwordx4 v0, s[36:39], s58 offen offset:2048 sc1 lds\nbuffer_load_dwordx4 v0, s[36:39], s58 offen offset:3072 sc1 lds\ns_waitcnt vmcnt(0)\ns_barrier\ns_branch .Lvu_slowr%=\n.Lvu_first%=:\n" \
"s_waitcnt vmcnt(0) lgkmcnt(0)\ns_barrier\nds_read_b128 v[56:59], v1 offset:8192\nds_read_b128 v[60:63], v1 offset:9216\nds_read_b128 v[64:67], v1 offset:10240\nds_read_b128 v[68:71], v1 offset:11264\nds_read_b128 v[16:19], v11 offset:49152\nds_read_b128 v[20:23], v11 offset:50176\n" \
"ds_read_b128 v[24:27], v11 offset:51200\nds_read_b128 v[28:31], v11 offset:52224\nds_read_b128 v[32:35], v11 offset:53248\nds_read_b128 v[36:39], v11 offset:54272\nds_read_b128 v[88:91], v11 offset:55296\nds_read_b128 v[92:95], v11 offset:56320\nds_read_b32 v15, v15\ns_waitcnt lgkmcnt(0)\n" \
"v_cmp_ge_i32 s[64:65], s62, v15\ns_nop 0\ns_cmp_eq_u64 s[64:65], 0\ns_cselect_b32 s61, s41, -2147483648\ns_add_u32 m0, s57, 0\ns_add_u32 s58, s61, 0\nbuffer_load_dwordx4 v0, s[36:39], s58 offen offset:0 sc1 lds\nbuffer_load_dwordx4 v0, s[36:39], s58 offen offset:1024 sc1 lds\n" \
"buffer_load_dwordx4 v0, s[36:39], s58 offen offset:2048 sc1 lds\nbuffer_load_dwordx4 v0, s[36:39], s58 offen offset:3072 sc1 lds\ns_add_u32 m0, s57, 32768\ns_add_u32 s58, s61, 32768\nbuffer_load_dwordx4 v0, s[36:39], s58 offen offset:0 sc1 lds\nbuffer_load_dwordx4 v0, s[36:39], s58 offen offset:1024 sc1 lds\nbuffer_load_dwordx4 v0, s[36:39], s58 offen offset:2048 sc1 lds\nbuffer_load_dwordx4 v0, s[36:39], s58 offen offset:3072 sc1 lds\n" \
"s_mov_b32 s59, -2147483648\nbuffer_store_dword v14, off, s[36:39], s59\nbuffer_store_dword v14, off, s[36:39], s59\nbuffer_store_dword v14, off, s[36:39], s59\ns_branch .Lvu_y%=\n.Lvu_last%=:\ns_nop 7\nv_and_or_b32 v76, v76, -2, v12\n" \
"v_cvt_pk_bf16_f32 v78, v44, v45\nv_cvt_pk_bf16_f32 v79, v46, v47\nv_and_or_b32 v78, v78, -2, v12\ns_nop 0\nv_permlane16_swap_b32 v77, v79\nv_permlane16_swap_b32 v76, v78\nbuffer_store_dwordx4 v[76:79], v6, s[36:39], s59 offen sc1\ns_branch .Lvu_end%=\n" \
".Lvu_abort%=:\ns_mov_b64 exec, -1\nv_mov_b32 v16, 0\ns_movk_i32 s44, 6400\nbuffer_store_dword v16, off, s[36:39], s44 sc1\n.Lvu_end%=:\ns_waitcnt vmcnt(0) lgkmcnt(0)\n" \
""
#define RD_ASM \
"v_mbcnt_lo_u32_b32 v255, -1, 0\nv_mbcnt_hi_u32_b32 v255, -1, v255\nv_lshlrev_b32 v255, 2, v255\nv_add_u32 v255, %[tb], v255\nds_read_b32 v196, v255 offset:0\nds_read_b32 v197, v255 offset:256\nds_read_b32 v198, v255 offset:512\nds_read_b32 v199, v255 offset:768\n" \
"ds_read_b32 v200, v255 offset:1024\nds_read_b32 v201, v255 offset:1280\nds_read_b32 v202, v255 offset:1536\nds_read_b32 v203, v255 offset:1792\nds_read_b32 v204, v255 offset:2048\nds_read_b32 v205, v255 offset:2304\nds_read_b32 v206, v255 offset:2560\nds_read_b32 v207, v255 offset:2816\n" \
"s_waitcnt lgkmcnt(0)\nv_accvgpr_write_b32 a32, v206\nv_accvgpr_write_b32 a33, v207\ns_mov_b32 s40, 0\ns_mov_b32 s43, 0\ns_add_u32 s57, %[su], 4096\ns_add_u32 s56, %[sp], 4096\ns_mov_b32 s41, %[sy]\n" \
".Lrd_yflag%=:\nv_accvgpr_read_b32 v206, a32\nbuffer_load_dword v208, v206, %[rs], 0 offen sc1\ns_waitcnt vmcnt(0)\nv_cmp_gt_i32 vcc, s40, v208\ns_cbranch_vccz .Lrd_yld%=\ns_add_u32 s43, s43, 1\ns_cmp_lt_u32 s43, 262144\n" \
"s_cbranch_scc1 .Lrd_yflag%=\ns_branch .Lrd_abort%=\n.Lrd_yld%=:\ns_add_u32 s60, s41, 0\nbuffer_load_dwordx4 v[0:3], v196, %[rs], s60 offen offset:0 sc1\ns_add_u32 s60, s41, 0\nbuffer_load_dwordx4 v[32:35], v196, %[rs], s60 offen offset:1024 sc1\ns_add_u32 s60, s41, 0\n" \
"buffer_load_dwordx4 v[64:67], v196, %[rs], s60 offen offset:2048 sc1\ns_add_u32 s60, s41, 131072\nbuffer_load_dwordx4 v[4:7], v196, %[rs], s60 offen offset:0 sc1\ns_add_u32 s60, s41, 131072\nbuffer_load_dwordx4 v[36:39], v196, %[rs], s60 offen offset:1024 sc1\ns_add_u32 s60, s41, 131072\nbuffer_load_dwordx4 v[68:71], v196, %[rs], s60 offen offset:2048 sc1\ns_add_u32 s60, s41, 262144\n" \
"buffer_load_dwordx4 v[8:11], v196, %[rs], s60 offen offset:0 sc1\ns_add_u32 s60, s41, 262144\nbuffer_load_dwordx4 v[40:43], v196, %[rs], s60 offen offset:1024 sc1\ns_add_u32 s60, s41, 262144\nbuffer_load_dwordx4 v[72:75], v196, %[rs], s60 offen offset:2048 sc1\ns_add_u32 s60, s41, 393216\nbuffer_load_dwordx4 v[12:15], v196, %[rs], s60 offen offset:0 sc1\ns_add_u32 s60, s41, 393216\n" \
"buffer_load_dwordx4 v[44:47], v196, %[rs], s60 offen offset:1024 sc1\ns_add_u32 s60, s41, 393216\nbuffer_load_dwordx4 v[76:79], v196, %[rs], s60 offen offset:2048 sc1\ns_add_u32 s60, s41, 524288\nbuffer_load_dwordx4 v[16:19], v196, %[rs], s60 offen offset:0 sc1\ns_add_u32 s60, s41, 524288\nbuffer_load_dwordx4 v[48:51], v196, %[rs], s60 offen offset:1024 sc1\ns_add_u32 s60, s41, 524288\n" \
"buffer_load_dwordx4 v[80:83], v196, %[rs], s60 offen offset:2048 sc1\ns_add_u32 s60, s41, 655360\nbuffer_load_dwordx4 v[20:23], v196, %[rs], s60 offen offset:0 sc1\ns_add_u32 s60, s41, 655360\nbuffer_load_dwordx4 v[52:55], v196, %[rs], s60 offen offset:1024 sc1\ns_add_u32 s60, s41, 655360\nbuffer_load_dwordx4 v[84:87], v196, %[rs], s60 offen offset:2048 sc1\ns_add_u32 s60, s41, 786432\n" \
"buffer_load_dwordx4 v[24:27], v196, %[rs], s60 offen offset:0 sc1\ns_add_u32 s60, s41, 786432\nbuffer_load_dwordx4 v[56:59], v196, %[rs], s60 offen offset:1024 sc1\ns_add_u32 s60, s41, 786432\nbuffer_load_dwordx4 v[88:91], v196, %[rs], s60 offen offset:2048 sc1\ns_add_u32 s60, s41, 917504\nbuffer_load_dwordx4 v[28:31], v196, %[rs], s60 offen offset:0 sc1\ns_add_u32 s60, s41, 917504\n" \
"buffer_load_dwordx4 v[60:63], v196, %[rs], s60 offen offset:1024 sc1\ns_add_u32 s60, s41, 917504\nbuffer_load_dwordx4 v[92:95], v196, %[rs], s60 offen offset:2048 sc1\ns_add_u32 s60, s41, 0\nbuffer_load_dwordx4 v[96:99], v196, %[rs], s60 offen offset:3072 sc1\ns_add_u32 s60, s41, 131072\nbuffer_load_dwordx4 v[100:103], v196, %[rs], s60 offen offset:3072 sc1\ns_add_u32 s60, s41, 262144\n" \
"buffer_load_dwordx4 v[104:107], v196, %[rs], s60 offen offset:3072 sc1\ns_add_u32 s60, s41, 393216\nbuffer_load_dwordx4 v[108:111], v196, %[rs], s60 offen offset:3072 sc1\ns_add_u32 s60, s41, 524288\nbuffer_load_dwordx4 v[112:115], v196, %[rs], s60 offen offset:3072 sc1\ns_add_u32 s60, s41, 655360\nbuffer_load_dwordx4 v[116:119], v196, %[rs], s60 offen offset:3072 sc1\ns_add_u32 s60, s41, 786432\n" \
"buffer_load_dwordx4 v[120:123], v196, %[rs], s60 offen offset:3072 sc1\ns_add_u32 s60, s41, 917504\nbuffer_load_dwordx4 v[124:127], v196, %[rs], s60 offen offset:3072 sc1\ns_mov_b64 exec, -1\nv_accvgpr_read_b32 v212, a33\nbuffer_load_dwordx2 v[214:215], v212, %[rs], 0 offen sc1\ns_waitcnt vmcnt(0)\ns_branch .Lrd_ychk%=\n" \
".Lrd_u2%=:\nbuffer_load_dwordx4 v[136:139], v196, %[rs], %[su] offen offset:2048 sc1\nbuffer_load_dwordx4 v[140:143], v196, %[rs], %[su] offen offset:3072 sc1\ns_add_u32 s43, s43, 1\ns_cmp_ge_u32 s43, 262144\ns_cbranch_scc1 .Lrd_abort%=\ns_waitcnt vmcnt(0)\nv_bitop3_b32 v208, v136, v138, s54 bitop3:0x7e\n" \
"v_bitop3_b32 v209, v140, v142, s54 bitop3:0x7e\nv_bitop3_b32 v208, v208, 1, v209 bitop3:0xc8\nv_cmp_ne_u32 vcc, 0, v208\ns_cbranch_vccnz .Lrd_u2%=\n.Lrd_u3%=:\nbuffer_load_dwordx4 v[128:131], v196, %[rs], %[su] offen offset:0 sc1\nbuffer_load_dwordx4 v[132:135], v196, %[rs], %[su] offen offset:1024 sc1\nbuffer_load_dwordx4 v[144:147], v196, %[rs], s57 offen offset:0 sc1\n" \
"buffer_load_dwordx4 v[148:151], v196, %[rs], s57 offen offset:1024 sc1\nbuffer_load_dwordx4 v[152:155], v196, %[rs], s57 offen offset:2048 sc1\nbuffer_load_dwordx4 v[156:159], v196, %[rs], s57 offen offset:3072 sc1\nbuffer_load_dwordx4 v[160:163], v196, %[rs], %[sp] offen offset:0 sc1\nbuffer_load_dwordx4 v[164:167], v196, %[rs], %[sp] offen offset:1024 sc1\nbuffer_load_dwordx4 v[168:171], v196, %[rs], %[sp] offen offset:2048 sc1\nbuffer_load_dwordx4 v[172:175], v196, %[rs], %[sp] offen offset:3072 sc1\nbuffer_load_dwordx4 v[176:179], v196, %[rs], s56 offen offset:0 sc1\n" \
".Lrd_ra%=:\ns_waitcnt vmcnt(5)\nv_bitop3_b32 v208, v128, v130, s54 bitop3:0x7e\nv_bitop3_b32 v209, v136, v138, s54 bitop3:0x7e\nv_bitop3_b32 v208, v208, 1, v209 bitop3:0xc8\nv_cmp_ne_u32 s[86:87], 0, v208\nv_bitop3_b32 v208, v144, v146, s54 bitop3:0x7e\nv_bitop3_b32 v209, v152, v154, s54 bitop3:0x7e\n" \
"v_bitop3_b32 v208, v208, 1, v209 bitop3:0xc8\nv_cmp_ne_u32 s[88:89], 0, v208\nv_bitop3_b32 v208, v132, v134, s54 bitop3:0x7e\nv_bitop3_b32 v209, v140, v142, s54 bitop3:0x7e\nv_bitop3_b32 v208, v208, 1, v209 bitop3:0xc8\nv_cmp_ne_u32 s[68:69], 0, v208\nv_bitop3_b32 v208, v148, v150, s54 bitop3:0x7e\nv_bitop3_b32 v209, v156, v158, s54 bitop3:0x7e\n" \
"v_bitop3_b32 v208, v208, 1, v209 bitop3:0xc8\nv_cmp_ne_u32 s[62:63], 0, v208\ns_or_b64 vcc, s[86:87], s[88:89]\ns_or_b64 vcc, vcc, s[68:69]\ns_or_b64 vcc, vcc, s[62:63]\ns_waitcnt vmcnt(4)\nv_smfmac_f32_16x16x64_bf16 v[180:183], v[160:163], v[0:7], v197\nv_bitop3_b32 v208, v160, v162, s54 bitop3:0x7e\n" \
"v_smfmac_f32_16x16x64_bf16 v[184:187], v[160:163], v[32:39], v197\nv_cmp_gt_i32 s[46:47], 0, v208\nv_smfmac_f32_16x16x64_bf16 v[188:191], v[160:163], v[64:71], v197\nv_smfmac_f32_16x16x64_bf16 v[192:195], v[160:163], v[96:103], v197\ns_or_b64 vcc, vcc, s[46:47]\ns_waitcnt vmcnt(3)\nv_smfmac_f32_16x16x64_bf16 v[180:183], v[164:167], v[8:15], v197\nv_bitop3_b32 v208, v164, v166, s54 bitop3:0x7e\n" \
"v_smfmac_f32_16x16x64_bf16 v[184:187], v[164:167], v[40:47], v197\nv_cmp_gt_i32 s[48:49], 0, v208\nv_smfmac_f32_16x16x64_bf16 v[188:191], v[164:167], v[72:79], v197\nv_smfmac_f32_16x16x64_bf16 v[192:195], v[164:167], v[104:111], v197\ns_or_b64 vcc, vcc, s[48:49]\ns_waitcnt vmcnt(2)\nv_smfmac_f32_16x16x64_bf16 v[180:183], v[168:171], v[16:23], v197\nv_bitop3_b32 v208, v168, v170, s54 bitop3:0x7e\n" \
"v_smfmac_f32_16x16x64_bf16 v[184:187], v[168:171], v[48:55], v197\nv_cmp_gt_i32 s[50:51], 0, v208\nv_smfmac_f32_16x16x64_bf16 v[188:191], v[168:171], v[80:87], v197\nv_smfmac_f32_16x16x64_bf16 v[192:195], v[168:171], v[112:119], v197\ns_or_b64 vcc, vcc, s[50:51]\ns_waitcnt vmcnt(1)\nv_smfmac_f32_16x16x64_bf16 v[180:183], v[172:175], v[24:31], v197\ns_waitcnt vmcnt(0)\n" \
"v_bitop3_b32 v208, v172, v174, s54 bitop3:0x7e\nv_smfmac_f32_16x16x64_bf16 v[180:183], v[176:179], v[128:135], v197\nv_bitop3_b32 v209, v176, v178, s54 bitop3:0x7e\nv_smfmac_f32_16x16x64_bf16 v[184:187], v[172:175], v[56:63], v197\nv_bitop3_b32 v210, v177, v179, s54 bitop3:0x7e\nv_smfmac_f32_16x16x64_bf16 v[184:187], v[176:179], v[136:143], v197\nv_cmp_gt_i32 s[52:53], 0, v208\nv_smfmac_f32_16x16x64_bf16 v[188:191], v[172:175], v[88:95], v197\n" \
"v_or_b32 v209, v209, v210\nv_smfmac_f32_16x16x64_bf16 v[188:191], v[176:179], v[144:151], v197\ns_or_b64 vcc, vcc, s[52:53]\nv_cmp_gt_i32 s[64:65], 0, v209\nv_smfmac_f32_16x16x64_bf16 v[192:195], v[172:175], v[120:127], v197\nv_smfmac_f32_16x16x64_bf16 v[192:195], v[176:179], v[152:159], v197\ns_or_b64 vcc, vcc, s[64:65]\ns_cbranch_scc1 .Lrd_rb%=\n" \
"s_add_u32 s60, s58, 16384\nv_add_f32 v180, v180, v182\nv_add_f32 v181, v181, v183\nds_write2_b32 v198, v180, v181 offset0:0 offset1:8\nbuffer_load_dwordx4 v[212:215], v196, %[rs], s60 offen offset:0 sc1\nv_add_f32 v184, v184, v186\nv_add_f32 v185, v185, v187\nds_write2_b32 v198, v184, v185 offset0:32 offset1:40\n" \
"s_add_u32 s60, s58, 0\nbuffer_load_dwordx4 v[216:219], v196, %[rs], s60 offen offset:0 sc1\nv_add_f32 v188, v188, v190\nv_add_f32 v189, v189, v191\nds_write2_b32 v198, v188, v189 offset0:64 offset1:72\ns_add_u32 s60, s58, 0\nbuffer_load_dwordx4 v[220:223], v196, %[rs], s60 offen offset:1024 sc1\nv_add_f32 v192, v192, v194\n" \
"v_add_f32 v193, v193, v195\nds_write2_b32 v198, v192, v193 offset0:96 offset1:104\ns_add_u32 s60, s58, 16384\nbuffer_load_dwordx4 v[224:227], v196, %[rs], s60 offen offset:1024 sc1\ns_waitcnt lgkmcnt(0)\ns_barrier\nds_read_b128 v[180:183], v199 offset:0\nds_read_b128 v[184:187], v199 offset:4096\n" \
"ds_read_b128 v[188:191], v199 offset:8192\nds_read_b128 v[192:195], v199 offset:12288\ns_and_b32 s44, s54, 0x80008000\ns_add_u32 s60, s58, 0\nbuffer_load_dwordx4 v[228:231], v196, %[rs], s60 offen offset:2048 sc1\ns_waitcnt lgkmcnt(2)\nv_pk_add_f32 v[182:183], v[182:183], v[186:187]\nv_pk_add_f32 v[180:181], v[180:181], v[184:185]\n" \
"s_waitcnt lgkmcnt(0)\nv_pk_add_f32 v[186:187], v[188:189], v[192:193]\nv_pk_add_f32 v[184:185], v[190:191], v[194:195]\nv_pk_add_f32 v[180:181], v[180:181], v[186:187]\nv_pk_add_f32 v[182:183], v[182:183], v[184:185]\nv_cvt_pk_bf16_f32 v180, v180, v181\nv_xor_b32 v180, s44, v180\nv_cvt_pk_bf16_f32 v181, v182, v183\n" \
"v_min_u32 v180, -2, v180\nv_xor_b32 v209, s44, v181\nv_or_b32 v208, v180, v201\nbuffer_store_dwordx2 v[208:209], v206, %[rs], 0 offen sc1\ns_add_u32 s60, s58, 0\nbuffer_load_dwordx4 v[232:235], v196, %[rs], s60 offen offset:3072 sc1\ns_add_u32 s60, s58, 16384\nbuffer_load_dwordx4 v[236:239], v196, %[rs], s60 offen offset:2048 sc1\n" \
"s_add_u32 s60, s58, 4096\nbuffer_load_dwordx4 v[240:243], v196, %[rs], s60 offen offset:0 sc1\ns_add_u32 s60, s58, 4096\nbuffer_load_dwordx4 v[244:247], v196, %[rs], s60 offen offset:1024 sc1\ns_add_u32 s60, s58, 16384\nbuffer_load_dwordx4 v[248:251], v196, %[rs], s60 offen offset:3072 sc1\ns_add_u32 s60, s58, 20480\nbuffer_load_dwordx4 v[252:255], v196, %[rs], s60 offen offset:0 sc1\n" \
"s_add_u32 s60, s58, 20480\nbuffer_load_dwordx4 v[160:163], v196, %[rs], s60 offen offset:2048 sc1\ns_xor_b32 s41, s41, 4194304\nv_mov_b32 v207, s40\ns_waitcnt vmcnt(12)\nv_bitop3_b32 v209, v212, v214, s61 bitop3:0x7e\nv_cmp_gt_i32 vcc, 0, v209\ns_cbranch_vccnz .Lrd_tre0%=\n" \
".Lrd_tl0%=:\nv_mfma_f32_16x16x16_bf16 a[0:3], v[0:1], v[212:213], 0\ns_add_u32 s60, s58, 4096\nbuffer_load_dwordx4 v[164:167], v196, %[rs], s60 offen offset:2048 sc1\nv_mfma_f32_16x16x16_bf16 a[8:11], v[32:33], v[212:213], 0\nv_mfma_f32_16x16x16_bf16 a[16:19], v[64:65], v[212:213], 0\nv_mfma_f32_16x16x16_bf16 a[24:27], v[96:97], v[212:213], 0\ns_waitcnt vmcnt(12)\n" \
"v_mfma_f32_16x16x16_bf16 a[0:3], v[4:5], v[214:215], a[0:3]\ns_add_u32 s60, s58, 4096\nbuffer_load_dwordx4 v[168:171], v196, %[rs], s60 offen offset:3072 sc1\nv_bitop3_b32 v209, v216, v218, s61 bitop3:0x7e\nv_mfma_f32_16x16x16_bf16 a[8:11], v[36:37], v[214:215], a[8:11]\ns_lshl_b32 s42, s40, 17\ns_add_u32 s42, s42, %[at]\nv_mfma_f32_16x16x16_bf16 a[16:19], v[68:69], v[214:215], a[16:19]\n" \
"v_cmp_gt_i32 vcc, 0, v209\nv_mfma_f32_16x16x16_bf16 a[24:27], v[100:101], v[214:215], a[24:27]\ns_cbranch_vccnz .Lrd_tre1%=\n.Lrd_tl1%=:\ns_waitcnt vmcnt(12)\nv_mfma_f32_16x16x32_bf16 a[4:7], v[0:3], v[216:219], 0\ns_add_u32 s60, s58, 20480\nbuffer_load_dwordx4 v[172:175], v196, %[rs], s60 offen offset:1024 sc1\n" \
"v_bitop3_b32 v209, v220, v222, s61 bitop3:0x7e\nv_mfma_f32_16x16x32_bf16 a[12:15], v[32:35], v[216:219], 0\ns_lshr_b32 s45, s42, 8\ns_and_b32 s45, s45, 0x2000\nv_mfma_f32_16x16x32_bf16 a[20:23], v[64:67], v[216:219], 0\nv_cmp_gt_i32 vcc, 0, v209\nv_mfma_f32_16x16x32_bf16 a[28:31], v[96:99], v[216:219], 0\ns_cbranch_vccnz .Lrd_tre2%=\n" \
".Lrd_tl2%=:\ns_waitcnt vmcnt(12)\nv_mfma_f32_16x16x32_bf16 a[4:7], v[4:7], v[220:223], a[4:7]\ns_add_u32 s60, s58, 20480\nbuffer_load_dwordx4 v[176:179], v196, %[rs], s60 offen offset:3072 sc1\nv_bitop3_b32 v209, v224, v226, s61 bitop3:0x7e\nv_mfma_f32_16x16x32_bf16 a[12:15], v[36:39], v[220:223], a[12:15]\ns_xor_b32 s42, s42, s45\n" \
"s_add_u32 s40, s40, 1\nv_mfma_f32_16x16x32_bf16 a[20:23], v[68:71], v[220:223], a[20:23]\nv_cmp_gt_i32 vcc, 0, v209\nv_mfma_f32_16x16x32_bf16 a[28:31], v[100:103], v[220:223], a[28:31]\ns_cbranch_vccnz .Lrd_tre3%=\n.Lrd_tl3%=:\nv_mfma_f32_16x16x16_bf16 a[0:3], v[8:9], v[224:225], a[0:3]\ns_cmp_eq_u32 s40, 1\n" \
"s_cbranch_scc1 .Lrd_y1%=\n.Lrd_y1r%=:\ns_add_u32 s60, s41, 0\nbuffer_load_dwordx4 v[0:3], v196, %[rs], s60 offen offset:0 sc1\nv_mfma_f32_16x16x16_bf16 a[8:11], v[40:41], v[224:225], a[8:11]\nv_mfma_f32_16x16x16_bf16 a[16:19], v[72:73], v[224:225], a[16:19]\nbuffer_load_dwordx4 v[212:215], v204, %[rs], s42 offen sc1\nv_mfma_f32_16x16x16_bf16 a[24:27], v[104:105], v[224:225], a[24:27]\n" \
"s_waitcnt vmcnt(14)\nv_mfma_f32_16x16x16_bf16 a[0:3], v[12:13], v[226:227], a[0:3]\ns_add_u32 s60, s41, 0\nbuffer_load_dwordx4 v[32:35], v196, %[rs], s60 offen offset:1024 sc1\nv_bitop3_b32 v209, v228, v230, s61 bitop3:0x7e\nv_mfma_f32_16x16x16_bf16 a[8:11], v[44:45], v[226:227], a[8:11]\ns_lshl_b32 s62, s55, 31\ns_or_b32 s62, s62, 1.0\n" \
"s_mov_b32 s63, s62\nv_mfma_f32_16x16x16_bf16 a[16:19], v[76:77], v[226:227], a[16:19]\nv_cmp_gt_i32 vcc, 0, v209\nv_mfma_f32_16x16x16_bf16 a[24:27], v[108:109], v[226:227], a[24:27]\ns_cbranch_vccnz .Lrd_tre4%=\n.Lrd_tl4%=:\ns_waitcnt vmcnt(13)\nv_mfma_f32_16x16x32_bf16 a[4:7], v[8:11], v[228:231], a[4:7]\n" \
"s_add_u32 s60, s41, 0\nbuffer_load_dwordx4 v[64:67], v196, %[rs], s60 offen offset:2048 sc1\nv_bitop3_b32 v209, v232, v234, s61 bitop3:0x7e\nv_mfma_f32_16x16x32_bf16 a[12:15], v[40:43], v[228:231], a[12:15]\nv_mfma_f32_16x16x32_bf16 a[20:23], v[72:75], v[228:231], a[20:23]\nv_cmp_gt_i32 vcc, 0, v209\nv_mfma_f32_16x16x32_bf16 a[28:31], v[104:107], v[228:231], a[28:31]\ns_cbranch_vccnz .Lrd_tre5%=\n" \
".Lrd_tl5%=:\ns_waitcnt vmcnt(13)\nv_mfma_f32_16x16x32_bf16 a[4:7], v[12:15], v[232:235], a[4:7]\ns_add_u32 s60, s41, 131072\nbuffer_load_dwordx4 v[4:7], v196, %[rs], s60 offen offset:0 sc1\nv_bitop3_b32 v209, v236, v238, s61 bitop3:0x7e\nv_mfma_f32_16x16x32_bf16 a[12:15], v[44:47], v[232:235], a[12:15]\nv_mfma_f32_16x16x32_bf16 a[20:23], v[76:79], v[232:235], a[20:23]\n" \
"v_cmp_gt_i32 vcc, 0, v209\nv_mfma_f32_16x16x32_bf16 a[28:31], v[108:111], v[232:235], a[28:31]\ns_cbranch_vccnz .Lrd_tre6%=\n.Lrd_tl6%=:\nv_mfma_f32_16x16x16_bf16 a[0:3], v[16:17], v[236:237], a[0:3]\ns_add_u32 s60, s41, 131072\nbuffer_load_dwordx4 v[36:39], v196, %[rs], s60 offen offset:1024 sc1\nv_mfma_f32_16x16x16_bf16 a[8:11], v[48:49], v[236:237], a[8:11]\n" \
"v_mfma_f32_16x16x16_bf16 a[16:19], v[80:81], v[236:237], a[16:19]\nv_mfma_f32_16x16x16_bf16 a[24:27], v[112:113], v[236:237], a[24:27]\ns_waitcnt vmcnt(14)\nv_mfma_f32_16x16x16_bf16 a[0:3], v[20:21], v[238:239], a[0:3]\ns_add_u32 s60, s41, 131072\nbuffer_load_dwordx4 v[68:71], v196, %[rs], s60 offen offset:2048 sc1\nv_bitop3_b32 v209, v240, v242, s61 bitop3:0x7e\nv_mfma_f32_16x16x16_bf16 a[8:11], v[52:53], v[238:239], a[8:11]\n" \
"v_mfma_f32_16x16x16_bf16 a[16:19], v[84:85], v[238:239], a[16:19]\nv_cmp_gt_i32 vcc, 0, v209\nv_mfma_f32_16x16x16_bf16 a[24:27], v[116:117], v[238:239], a[24:27]\ns_cbranch_vccnz .Lrd_tre7%=\n.Lrd_tl7%=:\ns_waitcnt vmcnt(14)\nv_mfma_f32_16x16x32_bf16 a[4:7], v[16:19], v[240:243], a[4:7]\ns_add_u32 s60, s41, 262144\n" \
"buffer_load_dwordx4 v[8:11], v196, %[rs], s60 offen offset:0 sc1\nv_bitop3_b32 v209, v244, v246, s61 bitop3:0x7e\nv_mfma_f32_16x16x32_bf16 a[12:15], v[48:51], v[240:243], a[12:15]\nv_mfma_f32_16x16x32_bf16 a[20:23], v[80:83], v[240:243], a[20:23]\nv_cmp_gt_i32 vcc, 0, v209\nv_mfma_f32_16x16x32_bf16 a[28:31], v[112:115], v[240:243], a[28:31]\ns_cbranch_vccnz .Lrd_tre8%=\n.Lrd_tl8%=:\n" \
"s_waitcnt vmcnt(14)\nv_mfma_f32_16x16x32_bf16 a[4:7], v[20:23], v[244:247], a[4:7]\ns_add_u32 s60, s41, 262144\nbuffer_load_dwordx4 v[40:43], v196, %[rs], s60 offen offset:1024 sc1\nv_bitop3_b32 v209, v248, v250, s61 bitop3:0x7e\nv_mfma_f32_16x16x32_bf16 a[12:15], v[52:55], v[244:247], a[12:15]\nv_mfma_f32_16x16x32_bf16 a[20:23], v[84:87], v[244:247], a[20:23]\nv_cmp_gt_i32 vcc, 0, v209\n" \
"v_mfma_f32_16x16x32_bf16 a[28:31], v[116:119], v[244:247], a[28:31]\ns_cbranch_vccnz .Lrd_tre9%=\n.Lrd_tl9%=:\nv_mfma_f32_16x16x16_bf16 a[0:3], v[24:25], v[248:249], a[0:3]\ns_add_u32 s60, s41, 262144\nbuffer_load_dwordx4 v[72:75], v196, %[rs], s60 offen offset:2048 sc1\nv_mfma_f32_16x16x16_bf16 a[8:11], v[56:57], v[248:249], a[8:11]\nv_mfma_f32_16x16x16_bf16 a[16:19], v[88:89], v[248:249], a[16:19]\n" \
"v_mfma_f32_16x16x16_bf16 a[24:27], v[120:121], v[248:249], a[24:27]\ns_waitcnt vmcnt(15)\nv_mfma_f32_16x16x16_bf16 a[0:3], v[28:29], v[250:251], a[0:3]\ns_add_u32 s60, s41, 393216\nbuffer_load_dwordx4 v[12:15], v196, %[rs], s60 offen offset:0 sc1\nv_bitop3_b32 v209, v252, v254, s61 bitop3:0x7e\nv_mfma_f32_16x16x16_bf16 a[8:11], v[60:61], v[250:251], a[8:11]\nv_bitop3_b32 v210, v253, v255, s61 bitop3:0x7e\n" \
"v_or_b32 v209, v209, v210\nv_mfma_f32_16x16x16_bf16 a[16:19], v[92:93], v[250:251], a[16:19]\nv_cmp_gt_i32 vcc, 0, v209\nv_mfma_f32_16x16x16_bf16 a[24:27], v[124:125], v[250:251], a[24:27]\ns_cbranch_vccnz .Lrd_tre10%=\n.Lrd_tl10%=:\ns_waitcnt vmcnt(15)\nv_mfma_f32_16x16x32_bf16 a[0:3], v[128:131], v[252:255], a[0:3]\n" \
"s_add_u32 s60, s41, 393216\nbuffer_load_dwordx4 v[44:47], v196, %[rs], s60 offen offset:1024 sc1\nv_bitop3_b32 v209, v160, v162, s61 bitop3:0x7e\nv_mfma_f32_16x16x32_bf16 a[8:11], v[136:139], v[252:255], a[8:11]\nv_bitop3_b32 v210, v161, v163, s61 bitop3:0x7e\nv_or_b32 v209, v209, v210\nv_mfma_f32_16x16x32_bf16 a[16:19], v[144:147], v[252:255], a[16:19]\nv_cmp_gt_i32 vcc, 0, v209\n" \
"v_mfma_f32_16x16x32_bf16 a[24:27], v[152:155], v[252:255], a[24:27]\ns_cbranch_vccnz .Lrd_tre11%=\n.Lrd_tl11%=:\ns_waitcnt vmcnt(15)\nv_mfma_f32_16x16x32_bf16 a[0:3], v[132:135], v[160:163], a[0:3]\ns_add_u32 s60, s41, 393216\nbuffer_load_dwordx4 v[76:79], v196, %[rs], s60 offen offset:2048 sc1\nv_bitop3_b32 v209, v164, v166, s61 bitop3:0x7e\n" \
"v_mfma_f32_16x16x32_bf16 a[8:11], v[140:143], v[160:163], a[8:11]\nv_mfma_f32_16x16x32_bf16 a[16:19], v[148:151], v[160:163], a[16:19]\nv_cmp_gt_i32 vcc, 0, v209\nv_mfma_f32_16x16x32_bf16 a[24:27], v[156:159], v[160:163], a[24:27]\ns_cbranch_vccnz .Lrd_tre12%=\n.Lrd_tl12%=:\ns_waitcnt vmcnt(15)\nv_mfma_f32_16x16x32_bf16 a[4:7], v[24:27], v[164:167], a[4:7]\n" \
"s_add_u32 s60, s41, 524288\nbuffer_load_dwordx4 v[16:19], v196, %[rs], s60 offen offset:0 sc1\nv_bitop3_b32 v209, v168, v170, s61 bitop3:0x7e\nv_mfma_f32_16x16x32_bf16 a[12:15], v[56:59], v[164:167], a[12:15]\nv_mfma_f32_16x16x32_bf16 a[20:23], v[88:91], v[164:167], a[20:23]\nds_write_b128 v202, a[0:3] offset:0\nv_cmp_gt_i32 vcc, 0, v209\nv_mfma_f32_16x16x32_bf16 a[28:31], v[120:123], v[164:167], a[28:31]\n" \
"s_cbranch_vccnz .Lrd_tre13%=\n.Lrd_tl13%=:\ns_waitcnt vmcnt(15)\nv_mfma_f32_16x16x32_bf16 a[4:7], v[28:31], v[168:171], a[4:7]\ns_add_u32 s60, s41, 524288\nbuffer_load_dwordx4 v[48:51], v196, %[rs], s60 offen offset:1024 sc1\nv_bitop3_b32 v209, v172, v174, s61 bitop3:0x7e\nv_mfma_f32_16x16x32_bf16 a[12:15], v[60:63], v[168:171], a[12:15]\n" \
"v_bitop3_b32 v210, v173, v175, s61 bitop3:0x7e\nv_or_b32 v209, v209, v210\nv_mfma_f32_16x16x32_bf16 a[20:23], v[92:95], v[168:171], a[20:23]\nds_write_b128 v202, a[8:11] offset:2048\nv_cmp_gt_i32 vcc, 0, v209\nv_mfma_f32_16x16x32_bf16 a[28:31], v[124:127], v[168:171], a[28:31]\ns_cbranch_vccnz .Lrd_tre14%=\n.Lrd_tl14%=:\n" \
"s_waitcnt vmcnt(15)\nv_mfma_f32_16x16x32_bf16 a[4:7], v[128:131], v[172:175], a[4:7]\ns_add_u32 s60, s41, 524288\nbuffer_load_dwordx4 v[80:83], v196, %[rs], s60 offen offset:2048 sc1\nv_bitop3_b32 v209, v176, v178, s61 bitop3:0x7e\nv_mfma_f32_16x16x32_bf16 a[12:15], v[136:139], v[172:175], a[12:15]\nv_bitop3_b32 v210, v177, v179, s61 bitop3:0x7e\nv_or_b32 v209, v209, v210\n" \
"v_mfma_f32_16x16x32_bf16 a[20:23], v[144:147], v[172:175], a[20:23]\nds_write_b128 v202, a[16:19] offset:4096\nv_cmp_gt_i32 vcc, 0, v209\nv_mfma_f32_16x16x32_bf16 a[28:31], v[152:155], v[172:175], a[28:31]\ns_cbranch_vccnz .Lrd_tre15%=\n.Lrd_tl15%=:\nv_mfma_f32_16x16x32_bf16 a[4:7], v[132:135], v[176:179], a[4:7]\ns_add_u32 s60, s41, 655360\n" \
"s_waitcnt vmcnt(14)\nbuffer_load_dwordx4 v[20:23], v196, %[rs], s60 offen offset:0 sc1\nv_lshlrev_b32 v216, 16, v212\nv_and_b32 v217, 0xffff0000, v212\nv_mfma_f32_16x16x32_bf16 a[12:15], v[140:143], v[176:179], a[12:15]\nv_lshlrev_b32 v218, 16, v213\nv_and_b32 v219, 0xffff0000, v213\nv_mfma_f32_16x16x32_bf16 a[20:23], v[148:151], v[176:179], a[20:23]\n" \
"ds_write_b128 v202, a[24:27] offset:6144\nv_lshlrev_b32 v220, 16, v214\nv_and_b32 v221, 0xffff0000, v214\nv_mfma_f32_16x16x32_bf16 a[28:31], v[156:159], v[176:179], a[28:31]\ns_add_u32 s60, s41, 655360\nbuffer_load_dwordx4 v[52:55], v196, %[rs], s60 offen offset:1024 sc1\ns_add_u32 s60, s41, 655360\nds_write_b128 v202, a[4:7] offset:1024\n" \
"buffer_load_dwordx4 v[84:87], v196, %[rs], s60 offen offset:2048 sc1\nds_write_b128 v202, a[12:15] offset:3072\nv_lshlrev_b32 v222, 16, v215\nv_and_b32 v223, 0xffff0000, v215\ns_add_u32 s60, s41, 786432\nbuffer_load_dwordx4 v[24:27], v196, %[rs], s60 offen offset:0 sc1\nds_write_b128 v202, a[20:23] offset:5120\ns_add_u32 s60, s41, 786432\n" \
"buffer_load_dwordx4 v[56:59], v196, %[rs], s60 offen offset:1024 sc1\nds_write_b128 v202, a[28:31] offset:7168\ns_waitcnt lgkmcnt(0)\ns_barrier\nds_read_b128 v[224:227], v203 offset:0\nds_read_b128 v[228:231], v203 offset:256\nds_read_b128 v[232:235], v203 offset:8192\nds_read_b128 v[236:239], v203 offset:8448\n" \
"ds_read_b128 v[240:243], v203 offset:16384\nds_read_b128 v[244:247], v203 offset:16640\nds_read_b128 v[248:251], v203 offset:24576\nds_read_b128 v[252:255], v203 offset:24832\ns_add_u32 s60, s41, 786432\nbuffer_load_dwordx4 v[88:91], v196, %[rs], s60 offen offset:2048 sc1\ns_waitcnt lgkmcnt(4)\nv_pk_add_f32 v[224:225], v[224:225], v[232:233]\n" \
"v_pk_add_f32 v[226:227], v[226:227], v[234:235]\nv_pk_add_f32 v[228:229], v[228:229], v[236:237]\nv_pk_add_f32 v[230:231], v[230:231], v[238:239]\ns_add_u32 s60, s41, 917504\nbuffer_load_dwordx4 v[28:31], v196, %[rs], s60 offen offset:0 sc1\ns_waitcnt lgkmcnt(2)\nv_pk_add_f32 v[224:225], v[224:225], v[240:241]\nv_pk_add_f32 v[226:227], v[226:227], v[242:243]\n" \
"v_pk_add_f32 v[228:229], v[228:229], v[244:245]\nv_pk_add_f32 v[230:231], v[230:231], v[246:247]\ns_add_u32 s60, s41, 917504\nbuffer_load_dwordx4 v[60:63], v196, %[rs], s60 offen offset:1024 sc1\ns_waitcnt lgkmcnt(0)\nv_pk_add_f32 v[224:225], v[224:225], v[248:249]\nv_pk_add_f32 v[226:227], v[226:227], v[250:251]\nv_pk_add_f32 v[228:229], v[228:229], v[252:253]\n" \
"v_pk_add_f32 v[230:231], v[230:231], v[254:255]\nv_pk_fma_f32 v[224:225], v[224:225], s[62:63], v[216:217]\nv_pk_fma_f32 v[226:227], v[226:227], s[62:63], v[218:219]\nv_pk_fma_f32 v[228:229], v[228:229], s[62:63], v[220:221]\nv_pk_fma_f32 v[230:231], v[230:231], s[62:63], v[222:223]\nv_cvt_pk_bf16_f32 v208, v224, v225\nv_cvt_pk_bf16_f32 v209, v226, v227\nv_cvt_pk_bf16_f32 v210, v228, v229\n" \
"v_cvt_pk_bf16_f32 v211, v230, v231\nbuffer_store_dwordx4 v[208:211], v204, %[rs], s42 offen sc1\nv_pk_add_f32 v[232:233], v[224:225], v[226:227]\nv_pk_add_f32 v[234:235], v[228:229], v[230:231]\nv_pk_add_f32 v[232:233], v[232:233], v[234:235]\ns_add_u32 s60, s41, 917504\nbuffer_load_dwordx4 v[92:95], v196, %[rs], s60 offen offset:2048 sc1\ns_add_u32 s60, s41, 0\n" \
"buffer_load_dwordx4 v[96:99], v196, %[rs], s60 offen offset:3072 sc1\nv_add_f32 v232, v232, v233\ns_nop 1\nv_add_f32_dpp v233, v232, v232 row_ror:8 row_mask:0xf bank_mask:0xf\nv_add_f32_dpp v234, v232, v232 row_ror:8 row_mask:0xf bank_mask:0xf\ns_nop 1\nv_permlane16_swap_b32 v233, v234\nv_add_f32 v232, v233, v234\n" \
"v_add_f32 v233, v233, v234\ns_nop 1\nv_permlane32_swap_b32 v232, v233\nv_add_f32 v232, v232, v233\nv_mul_f32 v232, 0x3c800000, v232\nv_pk_add_f32 v[224:225], v[224:225], v[232:233] op_sel_hi:[1,0] neg_lo:[0,1] neg_hi:[0,1]\nv_pk_add_f32 v[226:227], v[226:227], v[232:233] op_sel_hi:[1,0] neg_lo:[0,1] neg_hi:[0,1]\nv_pk_add_f32 v[228:229], v[228:229], v[232:233] op_sel_hi:[1,0] neg_lo:[0,1] neg_hi:[0,1]\n" \
"v_pk_add_f32 v[230:231], v[230:231], v[232:233] op_sel_hi:[1,0] neg_lo:[0,1] neg_hi:[0,1]\ns_add_u32 s60, s41, 131072\nbuffer_load_dwordx4 v[100:103], v196, %[rs], s60 offen offset:3072 sc1\ns_add_u32 s60, s41, 262144\nbuffer_load_dwordx4 v[104:107], v196, %[rs], s60 offen offset:3072 sc1\nv_pk_mul_f32 v[236:237], v[224:225], v[224:225]\nv_pk_mul_f32 v[238:239], v[226:227], v[226:227]\nv_pk_fma_f32 v[236:237], v[228:229], v[228:229], v[236:237]\n" \
"v_pk_fma_f32 v[238:239], v[230:231], v[230:231], v[238:239]\nv_pk_add_f32 v[236:237], v[236:237], v[238:239]\nv_add_f32 v236, v236, v237\ns_nop 1\nv_add_f32_dpp v237, v236, v236 row_ror:8 row_mask:0xf bank_mask:0xf\nv_add_f32_dpp v238, v236, v236 row_ror:8 row_mask:0xf bank_mask:0xf\ns_nop 1\nv_permlane16_swap_b32 v237, v238\n" \
"v_add_f32 v236, v237, v238\nv_add_f32 v237, v237, v238\ns_nop 1\nv_permlane32_swap_b32 v236, v237\nv_add_f32 v236, v236, v237\nv_mov_b32 v233, v236\nbuffer_store_dwordx2 v[232:233], v205, %[rs], s42 offen sc1\ns_add_u32 s60, s41, 393216\n" \
"buffer_load_dwordx4 v[108:111], v196, %[rs], s60 offen offset:3072 sc1\nv_accvgpr_read_b32 v212, a33\nbuffer_load_dwordx2 v[214:215], v212, %[rs], 0 offen sc1\ns_add_u32 s60, s41, 524288\nbuffer_load_dwordx4 v[112:115], v196, %[rs], s60 offen offset:3072 sc1\ns_add_u32 s60, s41, 655360\nbuffer_load_dwordx4 v[116:119], v196, %[rs], s60 offen offset:3072 sc1\ns_add_u32 s60, s41, 786432\n" \
"buffer_load_dwordx4 v[120:123], v196, %[rs], s60 offen offset:3072 sc1\ns_add_u32 s60, s41, 917504\nbuffer_load_dwordx4 v[124:127], v196, %[rs], s60 offen offset:3072 sc1\ns_waitcnt vmcnt(6)\nbuffer_store_dword v207, off, %[rs], %[fa] sc1\ns_cmp_ge_u32 s40, 64\ns_cbranch_scc1 .Lrd_end%=\n.Lrd_ychk%=:\n" \
"s_and_b32 s44, s40, 1\ns_bfe_u32 s55, s40, 0x10001\ns_mul_i32 s54, s44, 0x80008001\ns_mul_i32 s61, s55, 0x80008000\ns_lshl_b32 s58, s44, 17\ns_add_u32 s58, s58, %[st]\ns_add_u32 s44, s40, 1\ns_and_b32 s44, s44, 3\n" \
"s_lshl_b32 s44, s44, 15\nv_xor_b32 v206, s44, v200\nv_mov_b64 v[180:181], 0\nv_mov_b64 v[182:183], 0\nv_mov_b64 v[184:185], 0\nv_mov_b64 v[186:187], 0\nv_mov_b64 v[188:189], 0\nv_mov_b64 v[190:191], 0\n" \
"v_mov_b64 v[192:193], 0\nv_mov_b64 v[194:195], 0\ns_waitcnt vmcnt(5)\n.Lrd_sg0%=:\nv_cmp_ne_u32 vcc, s40, v214\ns_cbranch_vccnz .Lrd_sg1%=\nv_max_u32_dpp v215, v215, v215 quad_perm:[1,0,3,2] row_mask:0xf bank_mask:0xf\ns_nop 1\n" \
"v_max_u32_dpp v215, v215, v215 quad_perm:[2,3,0,1] row_mask:0xf bank_mask:0xf\ns_nop 1\nv_max_u32_dpp v215, v215, v215 row_half_mirror row_mask:0xf bank_mask:0xf\ns_nop 0\nv_readlane_b32 s86, v215, 0\nv_readlane_b32 s87, v215, 8\ns_add_u32 s86, s86, %[du]\n.Lrd_tw622%=:\n" \
"s_memrealtime s[68:69]\ns_waitcnt lgkmcnt(0)\ns_sub_u32 s44, s86, s68\ns_add_u32 s44, s44, 79\ns_cmp_lt_u32 s44, 250\ns_cbranch_scc1 .Lrd_tw622%=\nbuffer_load_dwordx4 v[128:131], v196, %[rs], %[su] offen offset:0 sc1\nbuffer_load_dwordx4 v[132:135], v196, %[rs], %[su] offen offset:1024 sc1\n" \
"buffer_load_dwordx4 v[136:139], v196, %[rs], %[su] offen offset:2048 sc1\nbuffer_load_dwordx4 v[140:143], v196, %[rs], %[su] offen offset:3072 sc1\n.Lrd_tw633%=:\ns_memrealtime s[68:69]\ns_waitcnt lgkmcnt(0)\ns_sub_u32 s44, s86, s68\ns_add_u32 s44, s44, 94\ns_cmp_lt_u32 s44, 250\n" \
"s_cbranch_scc1 .Lrd_tw633%=\nbuffer_load_dwordx4 v[144:147], v196, %[rs], s57 offen offset:0 sc1\nbuffer_load_dwordx4 v[148:151], v196, %[rs], s57 offen offset:1024 sc1\nbuffer_load_dwordx4 v[152:155], v196, %[rs], s57 offen offset:2048 sc1\nbuffer_load_dwordx4 v[156:159], v196, %[rs], s57 offen offset:3072 sc1\n.Lrd_tw644%=:\ns_memrealtime s[68:69]\ns_waitcnt lgkmcnt(0)\n" \
"s_sub_u32 s44, s87, s68\ns_add_u32 s44, s44, 124\ns_cmp_lt_u32 s44, 250\ns_cbranch_scc1 .Lrd_tw644%=\nbuffer_load_dwordx4 v[160:163], v196, %[rs], %[sp] offen offset:0 sc1\nbuffer_load_dwordx4 v[164:167], v196, %[rs], %[sp] offen offset:1024 sc1\nbuffer_load_dwordx4 v[168:171], v196, %[rs], %[sp] offen offset:2048 sc1\nbuffer_load_dwordx4 v[172:175], v196, %[rs], %[sp] offen offset:3072 sc1\n" \
"buffer_load_dwordx4 v[176:179], v196, %[rs], s56 offen offset:0 sc1\nv_bitop3_b32 v209, v0, v2, s55 bitop3:0x7e\nv_bitop3_b32 v210, v32, v34, s55 bitop3:0x7e\nv_or3_b32 v208, v209, v210, 0\nv_bitop3_b32 v209, v64, v66, s55 bitop3:0x7e\nv_bitop3_b32 v210, v4, v6, s55 bitop3:0x7e\nv_or3_b32 v208, v209, v210, v208\nv_bitop3_b32 v209, v36, v38, s55 bitop3:0x7e\n" \
"v_bitop3_b32 v210, v68, v70, s55 bitop3:0x7e\nv_or3_b32 v208, v209, v210, v208\nv_bitop3_b32 v209, v8, v10, s55 bitop3:0x7e\nv_bitop3_b32 v210, v40, v42, s55 bitop3:0x7e\nv_or3_b32 v208, v209, v210, v208\nv_bitop3_b32 v209, v72, v74, s55 bitop3:0x7e\nv_bitop3_b32 v210, v12, v14, s55 bitop3:0x7e\nv_or3_b32 v208, v209, v210, v208\n" \
"v_bitop3_b32 v209, v44, v46, s55 bitop3:0x7e\nv_bitop3_b32 v210, v76, v78, s55 bitop3:0x7e\nv_or3_b32 v208, v209, v210, v208\nv_bitop3_b32 v209, v16, v18, s55 bitop3:0x7e\nv_bitop3_b32 v210, v48, v50, s55 bitop3:0x7e\nv_or3_b32 v208, v209, v210, v208\nv_bitop3_b32 v209, v80, v82, s55 bitop3:0x7e\nv_bitop3_b32 v210, v20, v22, s55 bitop3:0x7e\n" \
"v_or3_b32 v208, v209, v210, v208\nv_bitop3_b32 v209, v52, v54, s55 bitop3:0x7e\nv_bitop3_b32 v210, v84, v86, s55 bitop3:0x7e\nv_or3_b32 v208, v209, v210, v208\nv_bitop3_b32 v209, v24, v26, s55 bitop3:0x7e\nv_bitop3_b32 v210, v56, v58, s55 bitop3:0x7e\nv_or3_b32 v208, v209, v210, v208\nv_bitop3_b32 v209, v88, v90, s55 bitop3:0x7e\n" \
"v_bitop3_b32 v210, v28, v30, s55 bitop3:0x7e\nv_or3_b32 v208, v209, v210, v208\nv_bitop3_b32 v209, v60, v62, s55 bitop3:0x7e\nv_bitop3_b32 v210, v92, v94, s55 bitop3:0x7e\nv_or3_b32 v208, v209, v210, v208\nv_bitop3_b32 v209, v96, v98, s55 bitop3:0x7e\nv_bitop3_b32 v210, v100, v102, s55 bitop3:0x7e\nv_or3_b32 v208, v209, v210, v208\n" \
"v_bitop3_b32 v209, v104, v106, s55 bitop3:0x7e\nv_bitop3_b32 v210, v108, v110, s55 bitop3:0x7e\nv_or3_b32 v208, v209, v210, v208\ns_waitcnt vmcnt(14)\nv_bitop3_b32 v209, v112, v114, s55 bitop3:0x7e\nv_bitop3_b32 v210, v116, v118, s55 bitop3:0x7e\nv_or3_b32 v208, v209, v210, v208\nv_bitop3_b32 v209, v120, v122, s55 bitop3:0x7e\n" \
"v_bitop3_b32 v210, v124, v126, s55 bitop3:0x7e\nv_or3_b32 v208, v209, v210, v208\nv_and_b32 v208, 1, v208\nv_cmp_ne_u32 vcc, 0, v208\ns_cbranch_vccnz .Lrd_yre%=\ns_branch .Lrd_ra%=\n.Lrd_sg1%=:\nbuffer_load_dwordx2 v[214:215], v212, %[rs], 0 offen sc1\n" \
"s_waitcnt vmcnt(0)\ns_add_u32 s43, s43, 1\ns_cmp_lt_u32 s43, 262144\ns_cbranch_scc1 .Lrd_sg0%=\ns_branch .Lrd_abort%=\n.Lrd_yre%=:\ns_mov_b64 exec, vcc\ns_add_u32 s43, s43, 1\n" \
"s_cmp_lt_u32 s43, 262144\ns_cbranch_scc1 .Lrd_yld%=\ns_branch .Lrd_abort%=\n.Lrd_rb%=:\ns_mov_b64 exec, s[46:47]\ns_cbranch_execz .Lrd_rs0%=\nbuffer_load_dwordx4 v[160:163], v196, %[rs], %[sp] offen offset:0 sc1\n.Lrd_rs0%=:\n" \
"s_mov_b64 exec, s[48:49]\ns_cbranch_execz .Lrd_rs1%=\nbuffer_load_dwordx4 v[164:167], v196, %[rs], %[sp] offen offset:1024 sc1\n.Lrd_rs1%=:\ns_mov_b64 exec, s[50:51]\ns_cbranch_execz .Lrd_rs2%=\nbuffer_load_dwordx4 v[168:171], v196, %[rs], %[sp] offen offset:2048 sc1\n.Lrd_rs2%=:\n" \
"s_mov_b64 exec, s[52:53]\ns_cbranch_execz .Lrd_rs3%=\nbuffer_load_dwordx4 v[172:175], v196, %[rs], %[sp] offen offset:3072 sc1\n.Lrd_rs3%=:\ns_mov_b64 exec, s[64:65]\ns_cbranch_execz .Lrd_rs4%=\nbuffer_load_dwordx4 v[176:179], v196, %[rs], s56 offen offset:0 sc1\n.Lrd_rs4%=:\n" \
"s_mov_b64 exec, s[86:87]\ns_cbranch_execz .Lrd_rs5%=\nbuffer_load_dwordx4 v[128:131], v196, %[rs], %[su] offen offset:0 sc1\nbuffer_load_dwordx4 v[136:139], v196, %[rs], %[su] offen offset:2048 sc1\n.Lrd_rs5%=:\ns_mov_b64 exec, s[88:89]\ns_cbranch_execz .Lrd_rs6%=\nbuffer_load_dwordx4 v[144:147], v196, %[rs], s57 offen offset:0 sc1\n" \
"buffer_load_dwordx4 v[152:155], v196, %[rs], s57 offen offset:2048 sc1\n.Lrd_rs6%=:\ns_mov_b64 exec, s[68:69]\ns_cbranch_execz .Lrd_rs7%=\nbuffer_load_dwordx4 v[132:135], v196, %[rs], %[su] offen offset:1024 sc1\nbuffer_load_dwordx4 v[140:143], v196, %[rs], %[su] offen offset:3072 sc1\n.Lrd_rs7%=:\ns_mov_b64 exec, s[62:63]\n" \
"s_cbranch_execz .Lrd_rs8%=\nbuffer_load_dwordx4 v[148:151], v196, %[rs], s57 offen offset:1024 sc1\nbuffer_load_dwordx4 v[156:159], v196, %[rs], s57 offen offset:3072 sc1\n.Lrd_rs8%=:\ns_mov_b64 exec, -1\nv_mov_b64 v[180:181], 0\nv_mov_b64 v[182:183], 0\nv_mov_b64 v[184:185], 0\n" \
"v_mov_b64 v[186:187], 0\nv_mov_b64 v[188:189], 0\nv_mov_b64 v[190:191], 0\nv_mov_b64 v[192:193], 0\nv_mov_b64 v[194:195], 0\ns_waitcnt vmcnt(0)\ns_add_u32 s43, s43, 1\ns_cmp_lt_u32 s43, 262144\n" \
"s_cbranch_scc1 .Lrd_ra%=\ns_branch .Lrd_abort%=\n.Lrd_y1%=:\nv_accvgpr_read_b32 v206, a32\nbuffer_load_dword v208, v206, %[rs], 0 offen sc1\ns_waitcnt vmcnt(0)\nv_cmp_gt_i32 vcc, s40, v208\ns_cbranch_vccz .Lrd_y1r%=\n" \
"s_add_u32 s43, s43, 1\ns_cmp_lt_u32 s43, 262144\ns_cbranch_scc1 .Lrd_y1%=\ns_branch .Lrd_abort%=\n.Lrd_tre0%=:\ns_add_u32 s60, s58, 16384\nbuffer_load_dwordx4 v[212:215], v196, %[rs], s60 offen offset:0 sc1\ns_waitcnt vmcnt(0)\n" \
"v_bitop3_b32 v209, v212, v214, s61 bitop3:0x7e\nv_cmp_gt_i32 vcc, 0, v209\ns_cbranch_vccz .Lrd_tl0%=\ns_add_u32 s43, s43, 1\ns_cmp_lt_u32 s43, 262144\ns_cbranch_scc1 .Lrd_tre0%=\ns_branch .Lrd_abort%=\n.Lrd_tre1%=:\n" \
"s_add_u32 s60, s58, 0\nbuffer_load_dwordx4 v[216:219], v196, %[rs], s60 offen offset:0 sc1\ns_waitcnt vmcnt(0)\nv_bitop3_b32 v209, v216, v218, s61 bitop3:0x7e\nv_cmp_gt_i32 vcc, 0, v209\ns_cbranch_vccz .Lrd_tl1%=\ns_add_u32 s43, s43, 1\ns_cmp_lt_u32 s43, 262144\n" \
"s_cbranch_scc1 .Lrd_tre1%=\ns_branch .Lrd_abort%=\n.Lrd_tre2%=:\ns_add_u32 s60, s58, 0\nbuffer_load_dwordx4 v[220:223], v196, %[rs], s60 offen offset:1024 sc1\ns_waitcnt vmcnt(0)\nv_bitop3_b32 v209, v220, v222, s61 bitop3:0x7e\nv_cmp_gt_i32 vcc, 0, v209\n" \
"s_cbranch_vccz .Lrd_tl2%=\ns_add_u32 s43, s43, 1\ns_cmp_lt_u32 s43, 262144\ns_cbranch_scc1 .Lrd_tre2%=\ns_branch .Lrd_abort%=\n.Lrd_tre3%=:\ns_add_u32 s60, s58, 16384\nbuffer_load_dwordx4 v[224:227], v196, %[rs], s60 offen offset:1024 sc1\n" \
"s_waitcnt vmcnt(0)\nv_bitop3_b32 v209, v224, v226, s61 bitop3:0x7e\nv_cmp_gt_i32 vcc, 0, v209\ns_cbranch_vccz .Lrd_tl3%=\ns_add_u32 s43, s43, 1\ns_cmp_lt_u32 s43, 262144\ns_cbranch_scc1 .Lrd_tre3%=\ns_branch .Lrd_abort%=\n" \
".Lrd_tre4%=:\ns_add_u32 s60, s58, 0\nbuffer_load_dwordx4 v[228:231], v196, %[rs], s60 offen offset:2048 sc1\ns_waitcnt vmcnt(0)\nv_bitop3_b32 v209, v228, v230, s61 bitop3:0x7e\nv_cmp_gt_i32 vcc, 0, v209\ns_cbranch_vccz .Lrd_tl4%=\ns_add_u32 s43, s43, 1\n" \
"s_cmp_lt_u32 s43, 262144\ns_cbranch_scc1 .Lrd_tre4%=\ns_branch .Lrd_abort%=\n.Lrd_tre5%=:\ns_add_u32 s60, s58, 0\nbuffer_load_dwordx4 v[232:235], v196, %[rs], s60 offen offset:3072 sc1\ns_waitcnt vmcnt(0)\nv_bitop3_b32 v209, v232, v234, s61 bitop3:0x7e\n" \
"v_cmp_gt_i32 vcc, 0, v209\ns_cbranch_vccz .Lrd_tl5%=\ns_add_u32 s43, s43, 1\ns_cmp_lt_u32 s43, 262144\ns_cbranch_scc1 .Lrd_tre5%=\ns_branch .Lrd_abort%=\n.Lrd_tre6%=:\ns_add_u32 s60, s58, 16384\n" \
"buffer_load_dwordx4 v[236:239], v196, %[rs], s60 offen offset:2048 sc1\ns_waitcnt vmcnt(0)\nv_bitop3_b32 v209, v236, v238, s61 bitop3:0x7e\nv_cmp_gt_i32 vcc, 0, v209\ns_cbranch_vccz .Lrd_tl6%=\ns_add_u32 s43, s43, 1\ns_cmp_lt_u32 s43, 262144\ns_cbranch_scc1 .Lrd_tre6%=\n" \
"s_branch .Lrd_abort%=\n.Lrd_tre7%=:\ns_add_u32 s60, s58, 4096\nbuffer_load_dwordx4 v[240:243], v196, %[rs], s60 offen offset:0 sc1\ns_waitcnt vmcnt(0)\nv_bitop3_b32 v209, v240, v242, s61 bitop3:0x7e\nv_cmp_gt_i32 vcc, 0, v209\ns_cbranch_vccz .Lrd_tl7%=\n" \
"s_add_u32 s43, s43, 1\ns_cmp_lt_u32 s43, 262144\ns_cbranch_scc1 .Lrd_tre7%=\ns_branch .Lrd_abort%=\n.Lrd_tre8%=:\ns_add_u32 s60, s58, 4096\nbuffer_load_dwordx4 v[244:247], v196, %[rs], s60 offen offset:1024 sc1\ns_waitcnt vmcnt(0)\n" \
"v_bitop3_b32 v209, v244, v246, s61 bitop3:0x7e\nv_cmp_gt_i32 vcc, 0, v209\ns_cbranch_vccz .Lrd_tl8%=\ns_add_u32 s43, s43, 1\ns_cmp_lt_u32 s43, 262144\ns_cbranch_scc1 .Lrd_tre8%=\ns_branch .Lrd_abort%=\n.Lrd_tre9%=:\n" \
"s_add_u32 s60, s58, 16384\nbuffer_load_dwordx4 v[248:251], v196, %[rs], s60 offen offset:3072 sc1\ns_waitcnt vmcnt(0)\nv_bitop3_b32 v209, v248, v250, s61 bitop3:0x7e\nv_cmp_gt_i32 vcc, 0, v209\ns_cbranch_vccz .Lrd_tl9%=\ns_add_u32 s43, s43, 1\ns_cmp_lt_u32 s43, 262144\n" \
"s_cbranch_scc1 .Lrd_tre9%=\ns_branch .Lrd_abort%=\n.Lrd_tre10%=:\ns_add_u32 s60, s58, 20480\nbuffer_load_dwordx4 v[252:255], v196, %[rs], s60 offen offset:0 sc1\ns_waitcnt vmcnt(0)\nv_bitop3_b32 v209, v252, v254, s61 bitop3:0x7e\nv_bitop3_b32 v210, v253, v255, s61 bitop3:0x7e\n" \
"v_or_b32 v209, v209, v210\nv_cmp_gt_i32 vcc, 0, v209\ns_cbranch_vccz .Lrd_tl10%=\ns_add_u32 s43, s43, 1\ns_cmp_lt_u32 s43, 262144\ns_cbranch_scc1 .Lrd_tre10%=\ns_branch .Lrd_abort%=\n.Lrd_tre11%=:\n" \
"s_add_u32 s60, s58, 20480\nbuffer_load_dwordx4 v[160:163], v196, %[rs], s60 offen offset:2048 sc1\ns_waitcnt vmcnt(0)\nv_bitop3_b32 v209, v160, v162, s61 bitop3:0x7e\nv_bitop3_b32 v210, v161, v163, s61 bitop3:0x7e\nv_or_b32 v209, v209, v210\nv_cmp_gt_i32 vcc, 0, v209\ns_cbranch_vccz .Lrd_tl11%=\n" \
"s_add_u32 s43, s43, 1\ns_cmp_lt_u32 s43, 262144\ns_cbranch_scc1 .Lrd_tre11%=\ns_branch .Lrd_abort%=\n.Lrd_tre12%=:\ns_add_u32 s60, s58, 4096\nbuffer_load_dwordx4 v[164:167], v196, %[rs], s60 offen offset:2048 sc1\ns_waitcnt vmcnt(0)\n" \
"v_bitop3_b32 v209, v164, v166, s61 bitop3:0x7e\nv_cmp_gt_i32 vcc, 0, v209\ns_cbranch_vccz .Lrd_tl12%=\ns_add_u32 s43, s43, 1\ns_cmp_lt_u32 s43, 262144\ns_cbranch_scc1 .Lrd_tre12%=\ns_branch .Lrd_abort%=\n.Lrd_tre13%=:\n" \
"s_add_u32 s60, s58, 4096\nbuffer_load_dwordx4 v[168:171], v196, %[rs], s60 offen offset:3072 sc1\ns_waitcnt vmcnt(0)\nv_bitop3_b32 v209, v168, v170, s61 bitop3:0x7e\nv_cmp_gt_i32 vcc, 0, v209\ns_cbranch_vccz .Lrd_tl13%=\ns_add_u32 s43, s43, 1\ns_cmp_lt_u32 s43, 262144\n" \
"s_cbranch_scc1 .Lrd_tre13%=\ns_branch .Lrd_abort%=\n.Lrd_tre14%=:\ns_add_u32 s60, s58, 20480\nbuffer_load_dwordx4 v[172:175], v196, %[rs], s60 offen offset:1024 sc1\ns_waitcnt vmcnt(0)\nv_bitop3_b32 v209, v172, v174, s61 bitop3:0x7e\nv_bitop3_b32 v210, v173, v175, s61 bitop3:0x7e\n" \
"v_or_b32 v209, v209, v210\nv_cmp_gt_i32 vcc, 0, v209\ns_cbranch_vccz .Lrd_tl14%=\ns_add_u32 s43, s43, 1\ns_cmp_lt_u32 s43, 262144\ns_cbranch_scc1 .Lrd_tre14%=\ns_branch .Lrd_abort%=\n.Lrd_tre15%=:\n" \
"s_add_u32 s60, s58, 20480\nbuffer_load_dwordx4 v[176:179], v196, %[rs], s60 offen offset:3072 sc1\ns_waitcnt vmcnt(0)\nv_bitop3_b32 v209, v176, v178, s61 bitop3:0x7e\nv_bitop3_b32 v210, v177, v179, s61 bitop3:0x7e\nv_or_b32 v209, v209, v210\nv_cmp_gt_i32 vcc, 0, v209\ns_cbranch_vccz .Lrd_tl15%=\n" \
"s_add_u32 s43, s43, 1\ns_cmp_lt_u32 s43, 262144\ns_cbranch_scc1 .Lrd_tre15%=\ns_branch .Lrd_abort%=\n.Lrd_abort%=:\ns_mov_b64 exec, -1\nv_mov_b32 v16, 0\ns_movk_i32 s44, 6400\n" \
"buffer_store_dword v16, off, %[rs], s44 sc1\n.Lrd_end%=:\ns_waitcnt vmcnt(0) lgkmcnt(0)\n" \
""
#define GG_ASM \
"v_mbcnt_lo_u32_b32 v255, -1, 0\nv_mbcnt_hi_u32_b32 v255, -1, v255\nv_lshlrev_b32 v255, 2, v255\nv_add_u32 v255, %[tb], v255\nds_read_b32 v0, v255 offset:0\nds_read_b32 v1, v255 offset:256\nds_read_b32 v2, v255 offset:512\nds_read_b32 v3, v255 offset:768\n" \
"ds_read_b32 v4, v255 offset:1024\nds_read_b32 v5, v255 offset:1280\nds_read_b32 v92, v255 offset:1536\nds_read_b32 v93, v255 offset:1792\nds_read_b32 v94, v255 offset:2048\ns_waitcnt lgkmcnt(0)\nv_mov_b32 v6, 0x3f803f80\nv_mov_b32 v7, 0x3f803f80\n" \
"s_add_u32 m0, %[lw], 16384\ns_add_u32 s44, %[wo], 0\nbuffer_load_dwordx4 v92, %[rw], s44 offen nt lds\ns_add_u32 m0, %[lw], 17408\ns_add_u32 s44, %[wo], 57344\nbuffer_load_dwordx4 v92, %[rw], s44 offen nt lds\ns_add_u32 m0, %[lw], 18432\ns_add_u32 s44, %[wo], 114688\n" \
"buffer_load_dwordx4 v92, %[rw], s44 offen nt lds\ns_add_u32 m0, %[lw], 19456\ns_add_u32 s44, %[wo], 172032\nbuffer_load_dwordx4 v92, %[rw], s44 offen nt lds\ns_add_u32 m0, %[lw], 20480\ns_add_u32 s44, %[wo], 229376\nbuffer_load_dwordx4 v92, %[rw], s44 offen nt lds\ns_add_u32 m0, %[lw], 21504\n" \
"s_add_u32 s44, %[wo], 286720\nbuffer_load_dwordx4 v92, %[rw], s44 offen nt lds\ns_add_u32 m0, %[lw], 22528\ns_add_u32 s44, %[wo], 344064\nbuffer_load_dwordx4 v92, %[rw], s44 offen nt lds\ns_add_u32 m0, %[lw], 23552\ns_add_u32 s44, %[wo], 401408\nbuffer_load_dwordx4 v92, %[rw], s44 offen nt lds\n" \
"s_add_u32 m0, %[tb], 0\ns_add_u32 s44, %[wo], 1835008\nbuffer_load_dwordx4 v92, %[rw], s44 offen nt lds\ns_add_u32 m0, %[tb], 1024\ns_add_u32 s44, %[wo], 1892352\nbuffer_load_dwordx4 v92, %[rw], s44 offen nt lds\ns_add_u32 m0, %[tb], 2048\ns_add_u32 s44, %[wo], 1949696\n" \
"buffer_load_dwordx4 v92, %[rw], s44 offen nt lds\ns_add_u32 m0, %[tb], 3072\ns_add_u32 s44, %[wo], 2007040\nbuffer_load_dwordx4 v92, %[rw], s44 offen nt lds\ns_add_u32 m0, %[tb], 4096\ns_add_u32 s44, %[wo], 2064384\nbuffer_load_dwordx4 v92, %[rw], s44 offen nt lds\ns_add_u32 m0, %[tb], 5120\n" \
"s_add_u32 s44, %[wo], 2121728\nbuffer_load_dwordx4 v92, %[rw], s44 offen nt lds\ns_add_u32 m0, %[tb], 6144\ns_add_u32 s44, %[wo], 2179072\nbuffer_load_dwordx4 v92, %[rw], s44 offen nt lds\ns_add_u32 m0, %[tb], 7168\ns_add_u32 s44, %[wo], 2236416\nbuffer_load_dwordx4 v92, %[rw], s44 offen nt lds\n" \
"s_add_u32 m0, %[tb], 8192\ns_add_u32 s44, %[wo], 3670016\nbuffer_load_dwordx4 v92, %[rw], s44 offen nt lds\ns_add_u32 m0, %[tb], 9216\ns_add_u32 s44, %[wo], 3727360\nbuffer_load_dwordx4 v92, %[rw], s44 offen nt lds\ns_add_u32 m0, %[tb], 10240\ns_add_u32 s44, %[wo], 3784704\n" \
"buffer_load_dwordx4 v92, %[rw], s44 offen nt lds\ns_add_u32 m0, %[tb], 11264\ns_add_u32 s44, %[wo], 3842048\nbuffer_load_dwordx4 v92, %[rw], s44 offen nt lds\ns_add_u32 m0, %[lw], 0\ns_add_u32 s44, %[wo], 3899392\nbuffer_load_dwordx4 v92, %[rw], s44 offen nt lds\ns_add_u32 m0, %[lw], 1024\n" \
"s_add_u32 s44, %[wo], 3956736\nbuffer_load_dwordx4 v92, %[rw], s44 offen nt lds\ns_add_u32 m0, %[lw], 2048\ns_add_u32 s44, %[wo], 4014080\nbuffer_load_dwordx4 v92, %[rw], s44 offen nt lds\ns_add_u32 m0, %[lw], 3072\ns_add_u32 s44, %[wo], 4071424\nbuffer_load_dwordx4 v92, %[rw], s44 offen nt lds\n" \
"s_add_u32 m0, %[lw], 4096\ns_add_u32 s44, %[wo], 5505024\nbuffer_load_dwordx4 v92, %[rw], s44 offen nt lds\ns_add_u32 m0, %[lw], 5120\ns_add_u32 s44, %[wo], 5562368\nbuffer_load_dwordx4 v92, %[rw], s44 offen nt lds\ns_add_u32 m0, %[lw], 6144\ns_add_u32 s44, %[wo], 5619712\n" \
"buffer_load_dwordx4 v92, %[rw], s44 offen nt lds\ns_add_u32 m0, %[lw], 7168\ns_add_u32 s44, %[wo], 5677056\nbuffer_load_dwordx4 v92, %[rw], s44 offen nt lds\ns_add_u32 m0, %[lw], 8192\ns_add_u32 s44, %[wo], 5734400\nbuffer_load_dwordx4 v92, %[rw], s44 offen nt lds\ns_add_u32 m0, %[lw], 9216\n" \
"s_add_u32 s44, %[wo], 5791744\nbuffer_load_dwordx4 v92, %[rw], s44 offen nt lds\ns_add_u32 m0, %[lw], 10240\ns_add_u32 s44, %[wo], 5849088\nbuffer_load_dwordx4 v92, %[rw], s44 offen nt lds\ns_add_u32 m0, %[lw], 11264\ns_add_u32 s44, %[wo], 5906432\nbuffer_load_dwordx4 v92, %[rw], s44 offen nt lds\n" \
"s_waitcnt vmcnt(28)\ns_barrier\ns_add_u32 m0, %[lw], 12288\ns_add_u32 s44, %[wo], 458752\nbuffer_load_dwordx4 v92, %[rw], s44 offen nt lds\ns_add_u32 m0, %[lw], 13312\ns_add_u32 s44, %[wo], 516096\nbuffer_load_dwordx4 v92, %[rw], s44 offen nt lds\n" \
"s_add_u32 m0, %[lw], 14336\ns_add_u32 s44, %[wo], 573440\nbuffer_load_dwordx4 v92, %[rw], s44 offen nt lds\ns_add_u32 m0, %[lw], 15360\ns_add_u32 s44, %[wo], 630784\nbuffer_load_dwordx4 v92, %[rw], s44 offen nt lds\nds_read_b64_tr_b16 a[0:1], v94 offset:16384\nds_read_b64_tr_b16 a[2:3], v94 offset:16896\n" \
"ds_read_b64_tr_b16 a[8:9], v94 offset:16392\nds_read_b64_tr_b16 a[10:11], v94 offset:16904\nds_read_b64_tr_b16 a[16:17], v94 offset:40960\nds_read_b64_tr_b16 a[18:19], v94 offset:41472\nds_read_b64_tr_b16 a[24:25], v94 offset:40968\nds_read_b64_tr_b16 a[26:27], v94 offset:41480\ns_waitcnt lgkmcnt(0)\nv_mfma_f32_16x16x16_bf16 v[8:11], a[0:1], v[6:7], 0\n" \
"v_mfma_f32_16x16x16_bf16 v[12:15], a[8:9], v[6:7], 0\nv_mfma_f32_16x16x16_bf16 v[16:19], a[16:17], v[6:7], 0\nv_mfma_f32_16x16x16_bf16 v[20:23], a[24:25], v[6:7], 0\nv_mfma_f32_16x16x16_bf16 v[8:11], a[2:3], v[6:7], v[8:11]\nv_mfma_f32_16x16x16_bf16 v[12:15], a[10:11], v[6:7], v[12:15]\nv_mfma_f32_16x16x16_bf16 v[16:19], a[18:19], v[6:7], v[16:19]\nv_mfma_f32_16x16x16_bf16 v[20:23], a[26:27], v[6:7], v[20:23]\ns_waitcnt vmcnt(28)\n" \
"s_barrier\ns_add_u32 m0, %[lw], 16384\ns_add_u32 s44, %[wo], 688128\nbuffer_load_dwordx4 v92, %[rw], s44 offen nt lds\ns_add_u32 m0, %[lw], 17408\ns_add_u32 s44, %[wo], 745472\nbuffer_load_dwordx4 v92, %[rw], s44 offen nt lds\ns_add_u32 m0, %[lw], 18432\n" \
"s_add_u32 s44, %[wo], 802816\nbuffer_load_dwordx4 v92, %[rw], s44 offen nt lds\ns_add_u32 m0, %[lw], 19456\ns_add_u32 s44, %[wo], 860160\nbuffer_load_dwordx4 v92, %[rw], s44 offen nt lds\nds_read_b64_tr_b16 a[4:5], v94 offset:20480\nds_read_b64_tr_b16 a[6:7], v94 offset:20992\nds_read_b64_tr_b16 a[12:13], v94 offset:20488\n" \
"ds_read_b64_tr_b16 a[14:15], v94 offset:21000\nds_read_b64_tr_b16 a[20:21], v94 offset:45056\nds_read_b64_tr_b16 a[22:23], v94 offset:45568\nds_read_b64_tr_b16 a[28:29], v94 offset:45064\nds_read_b64_tr_b16 a[30:31], v94 offset:45576\ns_waitcnt lgkmcnt(0)\nv_mfma_f32_16x16x16_bf16 v[8:11], a[4:5], v[6:7], v[8:11]\nv_mfma_f32_16x16x16_bf16 v[12:15], a[12:13], v[6:7], v[12:15]\n" \
"v_mfma_f32_16x16x16_bf16 v[16:19], a[20:21], v[6:7], v[16:19]\nv_mfma_f32_16x16x16_bf16 v[20:23], a[28:29], v[6:7], v[20:23]\nv_mfma_f32_16x16x16_bf16 v[8:11], a[6:7], v[6:7], v[8:11]\nv_mfma_f32_16x16x16_bf16 v[12:15], a[14:15], v[6:7], v[12:15]\nv_mfma_f32_16x16x16_bf16 v[16:19], a[22:23], v[6:7], v[16:19]\nv_mfma_f32_16x16x16_bf16 v[20:23], a[30:31], v[6:7], v[20:23]\ns_waitcnt vmcnt(28)\ns_barrier\n" \
"s_add_u32 m0, %[lw], 20480\ns_add_u32 s44, %[wo], 917504\nbuffer_load_dwordx4 v92, %[rw], s44 offen nt lds\ns_add_u32 m0, %[lw], 21504\ns_add_u32 s44, %[wo], 974848\nbuffer_load_dwordx4 v92, %[rw], s44 offen nt lds\ns_add_u32 m0, %[lw], 22528\ns_add_u32 s44, %[wo], 1032192\n" \
"buffer_load_dwordx4 v92, %[rw], s44 offen nt lds\ns_add_u32 m0, %[lw], 23552\ns_add_u32 s44, %[wo], 1089536\nbuffer_load_dwordx4 v92, %[rw], s44 offen nt lds\nds_read_b64_tr_b16 a[104:105], v93 offset:0\nds_read_b64_tr_b16 a[106:107], v93 offset:512\nds_read_b64_tr_b16 a[112:113], v93 offset:8\nds_read_b64_tr_b16 a[114:115], v93 offset:520\n" \
"ds_read_b64_tr_b16 a[120:121], v93 offset:12288\nds_read_b64_tr_b16 a[122:123], v93 offset:12800\nds_read_b64_tr_b16 a[128:129], v93 offset:12296\nds_read_b64_tr_b16 a[130:131], v93 offset:12808\ns_waitcnt lgkmcnt(0)\nv_mfma_f32_16x16x16_bf16 v[8:11], a[104:105], v[6:7], v[8:11]\nv_mfma_f32_16x16x16_bf16 v[12:15], a[112:113], v[6:7], v[12:15]\nv_mfma_f32_16x16x16_bf16 v[16:19], a[120:121], v[6:7], v[16:19]\n" \
"v_mfma_f32_16x16x16_bf16 v[20:23], a[128:129], v[6:7], v[20:23]\nv_mfma_f32_16x16x16_bf16 v[8:11], a[106:107], v[6:7], v[8:11]\nv_mfma_f32_16x16x16_bf16 v[12:15], a[114:115], v[6:7], v[12:15]\nv_mfma_f32_16x16x16_bf16 v[16:19], a[122:123], v[6:7], v[16:19]\nv_mfma_f32_16x16x16_bf16 v[20:23], a[130:131], v[6:7], v[20:23]\ns_waitcnt vmcnt(28)\ns_barrier\ns_add_u32 m0, %[tb], 0\n" \
"s_add_u32 s44, %[wo], 1146880\nbuffer_load_dwordx4 v92, %[rw], s44 offen nt lds\ns_add_u32 m0, %[tb], 1024\ns_add_u32 s44, %[wo], 1204224\nbuffer_load_dwordx4 v92, %[rw], s44 offen nt lds\ns_add_u32 m0, %[tb], 2048\ns_add_u32 s44, %[wo], 1261568\nbuffer_load_dwordx4 v92, %[rw], s44 offen nt lds\n" \
"s_add_u32 m0, %[tb], 3072\ns_add_u32 s44, %[wo], 1318912\nbuffer_load_dwordx4 v92, %[rw], s44 offen nt lds\nds_read_b64_tr_b16 a[108:109], v93 offset:4096\nds_read_b64_tr_b16 a[110:111], v93 offset:4608\nds_read_b64_tr_b16 a[116:117], v93 offset:4104\nds_read_b64_tr_b16 a[118:119], v93 offset:4616\nds_read_b64_tr_b16 a[124:125], v93 offset:16384\n" \
"ds_read_b64_tr_b16 a[126:127], v93 offset:16896\nds_read_b64_tr_b16 a[132:133], v93 offset:16392\nds_read_b64_tr_b16 a[134:135], v93 offset:16904\ns_waitcnt lgkmcnt(0)\nv_mfma_f32_16x16x16_bf16 v[8:11], a[108:109], v[6:7], v[8:11]\nv_mfma_f32_16x16x16_bf16 v[12:15], a[116:117], v[6:7], v[12:15]\nv_mfma_f32_16x16x16_bf16 v[16:19], a[124:125], v[6:7], v[16:19]\nv_mfma_f32_16x16x16_bf16 v[20:23], a[132:133], v[6:7], v[20:23]\n" \
"v_mfma_f32_16x16x16_bf16 v[8:11], a[110:111], v[6:7], v[8:11]\nv_mfma_f32_16x16x16_bf16 v[12:15], a[118:119], v[6:7], v[12:15]\nv_mfma_f32_16x16x16_bf16 v[16:19], a[126:127], v[6:7], v[16:19]\nv_mfma_f32_16x16x16_bf16 v[20:23], a[134:135], v[6:7], v[20:23]\ns_waitcnt vmcnt(28)\ns_barrier\ns_add_u32 m0, %[tb], 4096\ns_add_u32 s44, %[wo], 1376256\n" \
"buffer_load_dwordx4 v92, %[rw], s44 offen nt lds\ns_add_u32 m0, %[tb], 5120\ns_add_u32 s44, %[wo], 1433600\nbuffer_load_dwordx4 v92, %[rw], s44 offen nt lds\ns_add_u32 m0, %[tb], 6144\ns_add_u32 s44, %[wo], 1490944\nbuffer_load_dwordx4 v92, %[rw], s44 offen nt lds\ns_add_u32 m0, %[tb], 7168\n" \
"s_add_u32 s44, %[wo], 1548288\nbuffer_load_dwordx4 v92, %[rw], s44 offen nt lds\nds_read_b64_tr_b16 a[208:209], v93 offset:8192\nds_read_b64_tr_b16 a[210:211], v93 offset:8704\nds_read_b64_tr_b16 a[216:217], v93 offset:8200\nds_read_b64_tr_b16 a[218:219], v93 offset:8712\nds_read_b64_tr_b16 a[224:225], v93 offset:20480\nds_read_b64_tr_b16 a[226:227], v93 offset:20992\n" \
"ds_read_b64_tr_b16 a[232:233], v93 offset:20488\nds_read_b64_tr_b16 a[234:235], v93 offset:21000\ns_waitcnt lgkmcnt(0)\nv_mfma_f32_16x16x16_bf16 v[8:11], a[208:209], v[6:7], v[8:11]\nv_mfma_f32_16x16x16_bf16 v[12:15], a[216:217], v[6:7], v[12:15]\nv_mfma_f32_16x16x16_bf16 v[16:19], a[224:225], v[6:7], v[16:19]\nv_mfma_f32_16x16x16_bf16 v[20:23], a[232:233], v[6:7], v[20:23]\nv_mfma_f32_16x16x16_bf16 v[8:11], a[210:211], v[6:7], v[8:11]\n" \
"v_mfma_f32_16x16x16_bf16 v[12:15], a[218:219], v[6:7], v[12:15]\nv_mfma_f32_16x16x16_bf16 v[16:19], a[226:227], v[6:7], v[16:19]\nv_mfma_f32_16x16x16_bf16 v[20:23], a[234:235], v[6:7], v[20:23]\ns_waitcnt vmcnt(28)\ns_barrier\ns_add_u32 m0, %[tb], 8192\ns_add_u32 s44, %[wo], 1605632\nbuffer_load_dwordx4 v92, %[rw], s44 offen nt lds\n" \
"s_add_u32 m0, %[tb], 9216\ns_add_u32 s44, %[wo], 1662976\nbuffer_load_dwordx4 v92, %[rw], s44 offen nt lds\ns_add_u32 m0, %[tb], 10240\ns_add_u32 s44, %[wo], 1720320\nbuffer_load_dwordx4 v92, %[rw], s44 offen nt lds\ns_add_u32 m0, %[tb], 11264\ns_add_u32 s44, %[wo], 1777664\n" \
"buffer_load_dwordx4 v92, %[rw], s44 offen nt lds\nds_read_b64_tr_b16 a[212:213], v94 offset:0\nds_read_b64_tr_b16 a[214:215], v94 offset:512\nds_read_b64_tr_b16 a[220:221], v94 offset:8\nds_read_b64_tr_b16 a[222:223], v94 offset:520\nds_read_b64_tr_b16 a[228:229], v94 offset:24576\nds_read_b64_tr_b16 a[230:231], v94 offset:25088\nds_read_b64_tr_b16 a[236:237], v94 offset:24584\n" \
"ds_read_b64_tr_b16 a[238:239], v94 offset:25096\ns_waitcnt lgkmcnt(0)\nv_mfma_f32_16x16x16_bf16 v[8:11], a[212:213], v[6:7], v[8:11]\nv_mfma_f32_16x16x16_bf16 v[12:15], a[220:221], v[6:7], v[12:15]\nv_mfma_f32_16x16x16_bf16 v[16:19], a[228:229], v[6:7], v[16:19]\nv_mfma_f32_16x16x16_bf16 v[20:23], a[236:237], v[6:7], v[20:23]\nv_mfma_f32_16x16x16_bf16 v[8:11], a[214:215], v[6:7], v[8:11]\nv_mfma_f32_16x16x16_bf16 v[12:15], a[222:223], v[6:7], v[12:15]\n" \
"v_mfma_f32_16x16x16_bf16 v[16:19], a[230:231], v[6:7], v[16:19]\nv_mfma_f32_16x16x16_bf16 v[20:23], a[238:239], v[6:7], v[20:23]\ns_waitcnt vmcnt(28)\ns_barrier\ns_add_u32 m0, %[lw], 0\ns_add_u32 s44, %[wo], 2293760\nbuffer_load_dwordx4 v92, %[rw], s44 offen nt lds\ns_add_u32 m0, %[lw], 1024\n" \
"s_add_u32 s44, %[wo], 2351104\nbuffer_load_dwordx4 v92, %[rw], s44 offen nt lds\ns_add_u32 m0, %[lw], 2048\ns_add_u32 s44, %[wo], 2408448\nbuffer_load_dwordx4 v92, %[rw], s44 offen nt lds\ns_add_u32 m0, %[lw], 3072\ns_add_u32 s44, %[wo], 2465792\nbuffer_load_dwordx4 v92, %[rw], s44 offen nt lds\n" \
"ds_read_b64_tr_b16 v[152:153], v94 offset:4096\nds_read_b64_tr_b16 v[154:155], v94 offset:4608\nds_read_b64_tr_b16 v[160:161], v94 offset:4104\nds_read_b64_tr_b16 v[162:163], v94 offset:4616\nds_read_b64_tr_b16 v[168:169], v94 offset:28672\nds_read_b64_tr_b16 v[170:171], v94 offset:29184\nds_read_b64_tr_b16 v[176:177], v94 offset:28680\nds_read_b64_tr_b16 v[178:179], v94 offset:29192\n" \
"s_waitcnt lgkmcnt(0)\nv_mfma_f32_16x16x16_bf16 v[8:11], v[152:153], v[6:7], v[8:11]\nv_mfma_f32_16x16x16_bf16 v[12:15], v[160:161], v[6:7], v[12:15]\nv_mfma_f32_16x16x16_bf16 v[16:19], v[168:169], v[6:7], v[16:19]\nv_mfma_f32_16x16x16_bf16 v[20:23], v[176:177], v[6:7], v[20:23]\nv_mfma_f32_16x16x16_bf16 v[8:11], v[154:155], v[6:7], v[8:11]\nv_mfma_f32_16x16x16_bf16 v[12:15], v[162:163], v[6:7], v[12:15]\nv_mfma_f32_16x16x16_bf16 v[16:19], v[170:171], v[6:7], v[16:19]\n" \
"v_mfma_f32_16x16x16_bf16 v[20:23], v[178:179], v[6:7], v[20:23]\ns_waitcnt vmcnt(28)\ns_barrier\ns_add_u32 m0, %[lw], 4096\ns_add_u32 s44, %[wo], 2523136\nbuffer_load_dwordx4 v92, %[rw], s44 offen nt lds\ns_add_u32 m0, %[lw], 5120\ns_add_u32 s44, %[wo], 2580480\n" \
"buffer_load_dwordx4 v92, %[rw], s44 offen nt lds\ns_add_u32 m0, %[lw], 6144\ns_add_u32 s44, %[wo], 2637824\nbuffer_load_dwordx4 v92, %[rw], s44 offen nt lds\ns_add_u32 m0, %[lw], 7168\ns_add_u32 s44, %[wo], 2695168\nbuffer_load_dwordx4 v92, %[rw], s44 offen nt lds\nds_read_b64_tr_b16 v[156:157], v94 offset:8192\n" \
"ds_read_b64_tr_b16 v[158:159], v94 offset:8704\nds_read_b64_tr_b16 v[164:165], v94 offset:8200\nds_read_b64_tr_b16 v[166:167], v94 offset:8712\nds_read_b64_tr_b16 v[172:173], v94 offset:32768\nds_read_b64_tr_b16 v[174:175], v94 offset:33280\nds_read_b64_tr_b16 v[180:181], v94 offset:32776\nds_read_b64_tr_b16 v[182:183], v94 offset:33288\ns_waitcnt lgkmcnt(0)\n" \
"v_mfma_f32_16x16x16_bf16 v[8:11], v[156:157], v[6:7], v[8:11]\nv_mfma_f32_16x16x16_bf16 v[12:15], v[164:165], v[6:7], v[12:15]\nv_mfma_f32_16x16x16_bf16 v[16:19], v[172:173], v[6:7], v[16:19]\nv_mfma_f32_16x16x16_bf16 v[20:23], v[180:181], v[6:7], v[20:23]\nv_mfma_f32_16x16x16_bf16 v[8:11], v[158:159], v[6:7], v[8:11]\nv_mfma_f32_16x16x16_bf16 v[12:15], v[166:167], v[6:7], v[12:15]\nv_mfma_f32_16x16x16_bf16 v[16:19], v[174:175], v[6:7], v[16:19]\nv_mfma_f32_16x16x16_bf16 v[20:23], v[182:183], v[6:7], v[20:23]\n" \
"s_waitcnt vmcnt(28)\ns_barrier\ns_add_u32 m0, %[lw], 8192\ns_add_u32 s44, %[wo], 2752512\nbuffer_load_dwordx4 v92, %[rw], s44 offen nt lds\ns_add_u32 m0, %[lw], 9216\ns_add_u32 s44, %[wo], 2809856\nbuffer_load_dwordx4 v92, %[rw], s44 offen nt lds\n" \
"s_add_u32 m0, %[lw], 10240\ns_add_u32 s44, %[wo], 2867200\nbuffer_load_dwordx4 v92, %[rw], s44 offen nt lds\ns_add_u32 m0, %[lw], 11264\ns_add_u32 s44, %[wo], 2924544\nbuffer_load_dwordx4 v92, %[rw], s44 offen nt lds\nds_read_b64_tr_b16 a[32:33], v94 offset:12288\nds_read_b64_tr_b16 a[34:35], v94 offset:12800\n" \
"ds_read_b64_tr_b16 v[88:89], v94 offset:12296\nds_read_b64_tr_b16 v[90:91], v94 offset:12808\nds_read_b64_tr_b16 a[40:41], v94 offset:36864\nds_read_b64_tr_b16 a[42:43], v94 offset:37376\nds_read_b64_tr_b16 a[48:49], v94 offset:36872\nds_read_b64_tr_b16 a[50:51], v94 offset:37384\ns_waitcnt lgkmcnt(0)\nv_mfma_f32_16x16x16_bf16 v[8:11], a[32:33], v[6:7], v[8:11]\n" \
"v_mfma_f32_16x16x16_bf16 v[12:15], v[88:89], v[6:7], v[12:15]\nv_mfma_f32_16x16x16_bf16 v[16:19], a[40:41], v[6:7], v[16:19]\nv_mfma_f32_16x16x16_bf16 v[20:23], a[48:49], v[6:7], v[20:23]\nv_mfma_f32_16x16x16_bf16 v[8:11], a[34:35], v[6:7], v[8:11]\nv_mfma_f32_16x16x16_bf16 v[12:15], v[90:91], v[6:7], v[12:15]\nv_mfma_f32_16x16x16_bf16 v[16:19], a[42:43], v[6:7], v[16:19]\nv_mfma_f32_16x16x16_bf16 v[20:23], a[50:51], v[6:7], v[20:23]\ns_waitcnt vmcnt(28)\n" \
"s_barrier\ns_add_u32 m0, %[lw], 12288\ns_add_u32 s44, %[wo], 2981888\nbuffer_load_dwordx4 v92, %[rw], s44 offen nt lds\ns_add_u32 m0, %[lw], 13312\ns_add_u32 s44, %[wo], 3039232\nbuffer_load_dwordx4 v92, %[rw], s44 offen nt lds\ns_add_u32 m0, %[lw], 14336\n" \
"s_add_u32 s44, %[wo], 3096576\nbuffer_load_dwordx4 v92, %[rw], s44 offen nt lds\ns_add_u32 m0, %[lw], 15360\ns_add_u32 s44, %[wo], 3153920\nbuffer_load_dwordx4 v92, %[rw], s44 offen nt lds\nds_read_b64_tr_b16 a[36:37], v94 offset:16384\nds_read_b64_tr_b16 a[38:39], v94 offset:16896\nds_read_b64_tr_b16 v[84:85], v94 offset:16392\n" \
"ds_read_b64_tr_b16 v[86:87], v94 offset:16904\nds_read_b64_tr_b16 a[44:45], v94 offset:40960\nds_read_b64_tr_b16 a[46:47], v94 offset:41472\nds_read_b64_tr_b16 a[52:53], v94 offset:40968\nds_read_b64_tr_b16 a[54:55], v94 offset:41480\ns_waitcnt lgkmcnt(0)\nv_mfma_f32_16x16x16_bf16 v[8:11], a[36:37], v[6:7], v[8:11]\nv_mfma_f32_16x16x16_bf16 v[12:15], v[84:85], v[6:7], v[12:15]\n" \
"v_mfma_f32_16x16x16_bf16 v[16:19], a[44:45], v[6:7], v[16:19]\nv_mfma_f32_16x16x16_bf16 v[20:23], a[52:53], v[6:7], v[20:23]\nv_mfma_f32_16x16x16_bf16 v[8:11], a[38:39], v[6:7], v[8:11]\nv_mfma_f32_16x16x16_bf16 v[12:15], v[86:87], v[6:7], v[12:15]\nv_mfma_f32_16x16x16_bf16 v[16:19], a[46:47], v[6:7], v[16:19]\nv_mfma_f32_16x16x16_bf16 v[20:23], a[54:55], v[6:7], v[20:23]\ns_waitcnt vmcnt(28)\ns_barrier\n" \
"s_add_u32 m0, %[lw], 16384\ns_add_u32 s44, %[wo], 3211264\nbuffer_load_dwordx4 v92, %[rw], s44 offen nt lds\ns_add_u32 m0, %[lw], 17408\ns_add_u32 s44, %[wo], 3268608\nbuffer_load_dwordx4 v92, %[rw], s44 offen nt lds\ns_add_u32 m0, %[lw], 18432\ns_add_u32 s44, %[wo], 3325952\n" \
"buffer_load_dwordx4 v92, %[rw], s44 offen nt lds\ns_add_u32 m0, %[lw], 19456\ns_add_u32 s44, %[wo], 3383296\nbuffer_load_dwordx4 v92, %[rw], s44 offen nt lds\nds_read_b64_tr_b16 a[56:57], v94 offset:20480\nds_read_b64_tr_b16 a[58:59], v94 offset:20992\nds_read_b64_tr_b16 a[64:65], v94 offset:20488\nds_read_b64_tr_b16 a[66:67], v94 offset:21000\n" \
"ds_read_b64_tr_b16 v[80:81], v94 offset:45056\nds_read_b64_tr_b16 v[82:83], v94 offset:45568\nds_read_b64_tr_b16 a[72:73], v94 offset:45064\nds_read_b64_tr_b16 a[74:75], v94 offset:45576\ns_waitcnt lgkmcnt(0)\nv_mfma_f32_16x16x16_bf16 v[8:11], a[56:57], v[6:7], v[8:11]\nv_mfma_f32_16x16x16_bf16 v[12:15], a[64:65], v[6:7], v[12:15]\nv_mfma_f32_16x16x16_bf16 v[16:19], v[80:81], v[6:7], v[16:19]\n" \
"v_mfma_f32_16x16x16_bf16 v[20:23], a[72:73], v[6:7], v[20:23]\nv_mfma_f32_16x16x16_bf16 v[8:11], a[58:59], v[6:7], v[8:11]\nv_mfma_f32_16x16x16_bf16 v[12:15], a[66:67], v[6:7], v[12:15]\nv_mfma_f32_16x16x16_bf16 v[16:19], v[82:83], v[6:7], v[16:19]\nv_mfma_f32_16x16x16_bf16 v[20:23], a[74:75], v[6:7], v[20:23]\ns_waitcnt vmcnt(28)\ns_barrier\ns_add_u32 m0, %[lw], 20480\n" \
"s_add_u32 s44, %[wo], 3440640\nbuffer_load_dwordx4 v92, %[rw], s44 offen nt lds\ns_add_u32 m0, %[lw], 21504\ns_add_u32 s44, %[wo], 3497984\nbuffer_load_dwordx4 v92, %[rw], s44 offen nt lds\ns_add_u32 m0, %[lw], 22528\ns_add_u32 s44, %[wo], 3555328\nbuffer_load_dwordx4 v92, %[rw], s44 offen nt lds\n" \
"s_add_u32 m0, %[lw], 23552\ns_add_u32 s44, %[wo], 3612672\nbuffer_load_dwordx4 v92, %[rw], s44 offen nt lds\nds_read_b64_tr_b16 a[60:61], v93 offset:0\nds_read_b64_tr_b16 a[62:63], v93 offset:512\nds_read_b64_tr_b16 a[68:69], v93 offset:8\nds_read_b64_tr_b16 a[70:71], v93 offset:520\nds_read_b64_tr_b16 v[76:77], v93 offset:12288\n" \
"ds_read_b64_tr_b16 v[78:79], v93 offset:12800\nds_read_b64_tr_b16 a[76:77], v93 offset:12296\nds_read_b64_tr_b16 a[78:79], v93 offset:12808\ns_waitcnt lgkmcnt(0)\nv_mfma_f32_16x16x16_bf16 v[8:11], a[60:61], v[6:7], v[8:11]\nv_mfma_f32_16x16x16_bf16 v[12:15], a[68:69], v[6:7], v[12:15]\nv_mfma_f32_16x16x16_bf16 v[16:19], v[76:77], v[6:7], v[16:19]\nv_mfma_f32_16x16x16_bf16 v[20:23], a[76:77], v[6:7], v[20:23]\n" \
"v_mfma_f32_16x16x16_bf16 v[8:11], a[62:63], v[6:7], v[8:11]\nv_mfma_f32_16x16x16_bf16 v[12:15], a[70:71], v[6:7], v[12:15]\nv_mfma_f32_16x16x16_bf16 v[16:19], v[78:79], v[6:7], v[16:19]\nv_mfma_f32_16x16x16_bf16 v[20:23], a[78:79], v[6:7], v[20:23]\ns_waitcnt vmcnt(28)\ns_barrier\ns_add_u32 m0, %[tb], 0\ns_add_u32 s44, %[wo], 4128768\n" \
"buffer_load_dwordx4 v92, %[rw], s44 offen nt lds\ns_add_u32 m0, %[tb], 1024\ns_add_u32 s44, %[wo], 4186112\nbuffer_load_dwordx4 v92, %[rw], s44 offen nt lds\ns_add_u32 m0, %[tb], 2048\ns_add_u32 s44, %[wo], 4243456\nbuffer_load_dwordx4 v92, %[rw], s44 offen nt lds\ns_add_u32 m0, %[tb], 3072\n" \
"s_add_u32 s44, %[wo], 4300800\nbuffer_load_dwordx4 v92, %[rw], s44 offen nt lds\nds_read_b64_tr_b16 a[80:81], v93 offset:4096\nds_read_b64_tr_b16 a[82:83], v93 offset:4608\nds_read_b64_tr_b16 a[88:89], v93 offset:4104\nds_read_b64_tr_b16 a[90:91], v93 offset:4616\nds_read_b64_tr_b16 a[96:97], v93 offset:16384\nds_read_b64_tr_b16 a[98:99], v93 offset:16896\n" \
"ds_read_b64_tr_b16 v[72:73], v93 offset:16392\nds_read_b64_tr_b16 v[74:75], v93 offset:16904\ns_waitcnt lgkmcnt(0)\nv_mfma_f32_16x16x16_bf16 v[8:11], a[80:81], v[6:7], v[8:11]\nv_mfma_f32_16x16x16_bf16 v[12:15], a[88:89], v[6:7], v[12:15]\nv_mfma_f32_16x16x16_bf16 v[16:19], a[96:97], v[6:7], v[16:19]\nv_mfma_f32_16x16x16_bf16 v[20:23], v[72:73], v[6:7], v[20:23]\nv_mfma_f32_16x16x16_bf16 v[8:11], a[82:83], v[6:7], v[8:11]\n" \
"v_mfma_f32_16x16x16_bf16 v[12:15], a[90:91], v[6:7], v[12:15]\nv_mfma_f32_16x16x16_bf16 v[16:19], a[98:99], v[6:7], v[16:19]\nv_mfma_f32_16x16x16_bf16 v[20:23], v[74:75], v[6:7], v[20:23]\ns_waitcnt vmcnt(28)\ns_barrier\ns_add_u32 m0, %[tb], 4096\ns_add_u32 s44, %[wo], 4358144\nbuffer_load_dwordx4 v92, %[rw], s44 offen nt lds\n" \
"s_add_u32 m0, %[tb], 5120\ns_add_u32 s44, %[wo], 4415488\nbuffer_load_dwordx4 v92, %[rw], s44 offen nt lds\ns_add_u32 m0, %[tb], 6144\ns_add_u32 s44, %[wo], 4472832\nbuffer_load_dwordx4 v92, %[rw], s44 offen nt lds\ns_add_u32 m0, %[tb], 7168\ns_add_u32 s44, %[wo], 4530176\n" \
"buffer_load_dwordx4 v92, %[rw], s44 offen nt lds\nds_read_b64_tr_b16 a[84:85], v93 offset:8192\nds_read_b64_tr_b16 a[86:87], v93 offset:8704\nds_read_b64_tr_b16 a[92:93], v93 offset:8200\nds_read_b64_tr_b16 a[94:95], v93 offset:8712\nds_read_b64_tr_b16 a[100:101], v93 offset:20480\nds_read_b64_tr_b16 a[102:103], v93 offset:20992\nds_read_b64_tr_b16 v[68:69], v93 offset:20488\n" \
"ds_read_b64_tr_b16 v[70:71], v93 offset:21000\ns_waitcnt lgkmcnt(0)\nv_mfma_f32_16x16x16_bf16 v[8:11], a[84:85], v[6:7], v[8:11]\nv_mfma_f32_16x16x16_bf16 v[12:15], a[92:93], v[6:7], v[12:15]\nv_mfma_f32_16x16x16_bf16 v[16:19], a[100:101], v[6:7], v[16:19]\nv_mfma_f32_16x16x16_bf16 v[20:23], v[68:69], v[6:7], v[20:23]\nv_mfma_f32_16x16x16_bf16 v[8:11], a[86:87], v[6:7], v[8:11]\nv_mfma_f32_16x16x16_bf16 v[12:15], a[94:95], v[6:7], v[12:15]\n" \
"v_mfma_f32_16x16x16_bf16 v[16:19], a[102:103], v[6:7], v[16:19]\nv_mfma_f32_16x16x16_bf16 v[20:23], v[70:71], v[6:7], v[20:23]\ns_waitcnt vmcnt(28)\ns_barrier\ns_add_u32 m0, %[tb], 8192\ns_add_u32 s44, %[wo], 4587520\nbuffer_load_dwordx4 v92, %[rw], s44 offen nt lds\ns_add_u32 m0, %[tb], 9216\n" \
"s_add_u32 s44, %[wo], 4644864\nbuffer_load_dwordx4 v92, %[rw], s44 offen nt lds\ns_add_u32 m0, %[tb], 10240\ns_add_u32 s44, %[wo], 4702208\nbuffer_load_dwordx4 v92, %[rw], s44 offen nt lds\ns_add_u32 m0, %[tb], 11264\ns_add_u32 s44, %[wo], 4759552\nbuffer_load_dwordx4 v92, %[rw], s44 offen nt lds\n" \
"ds_read_b64_tr_b16 a[136:137], v94 offset:0\nds_read_b64_tr_b16 a[138:139], v94 offset:512\nds_read_b64_tr_b16 v[64:65], v94 offset:8\nds_read_b64_tr_b16 v[66:67], v94 offset:520\nds_read_b64_tr_b16 a[144:145], v94 offset:24576\nds_read_b64_tr_b16 a[146:147], v94 offset:25088\nds_read_b64_tr_b16 a[152:153], v94 offset:24584\nds_read_b64_tr_b16 a[154:155], v94 offset:25096\n" \
"s_waitcnt lgkmcnt(0)\nv_mfma_f32_16x16x16_bf16 v[8:11], a[136:137], v[6:7], v[8:11]\nv_mfma_f32_16x16x16_bf16 v[12:15], v[64:65], v[6:7], v[12:15]\nv_mfma_f32_16x16x16_bf16 v[16:19], a[144:145], v[6:7], v[16:19]\nv_mfma_f32_16x16x16_bf16 v[20:23], a[152:153], v[6:7], v[20:23]\nv_mfma_f32_16x16x16_bf16 v[8:11], a[138:139], v[6:7], v[8:11]\nv_mfma_f32_16x16x16_bf16 v[12:15], v[66:67], v[6:7], v[12:15]\nv_mfma_f32_16x16x16_bf16 v[16:19], a[146:147], v[6:7], v[16:19]\n" \
"v_mfma_f32_16x16x16_bf16 v[20:23], a[154:155], v[6:7], v[20:23]\ns_waitcnt vmcnt(28)\ns_barrier\ns_add_u32 m0, %[lw], 0\ns_add_u32 s44, %[wo], 4816896\nbuffer_load_dwordx4 v92, %[rw], s44 offen nt lds\ns_add_u32 m0, %[lw], 1024\ns_add_u32 s44, %[wo], 4874240\n" \
"buffer_load_dwordx4 v92, %[rw], s44 offen nt lds\ns_add_u32 m0, %[lw], 2048\ns_add_u32 s44, %[wo], 4931584\nbuffer_load_dwordx4 v92, %[rw], s44 offen nt lds\ns_add_u32 m0, %[lw], 3072\ns_add_u32 s44, %[wo], 4988928\nbuffer_load_dwordx4 v92, %[rw], s44 offen nt lds\nds_read_b64_tr_b16 a[140:141], v94 offset:4096\n" \
"ds_read_b64_tr_b16 a[142:143], v94 offset:4608\nds_read_b64_tr_b16 v[60:61], v94 offset:4104\nds_read_b64_tr_b16 v[62:63], v94 offset:4616\nds_read_b64_tr_b16 a[148:149], v94 offset:28672\nds_read_b64_tr_b16 a[150:151], v94 offset:29184\nds_read_b64_tr_b16 a[156:157], v94 offset:28680\nds_read_b64_tr_b16 a[158:159], v94 offset:29192\ns_waitcnt lgkmcnt(0)\n" \
"v_mfma_f32_16x16x16_bf16 v[8:11], a[140:141], v[6:7], v[8:11]\nv_mfma_f32_16x16x16_bf16 v[12:15], v[60:61], v[6:7], v[12:15]\nv_mfma_f32_16x16x16_bf16 v[16:19], a[148:149], v[6:7], v[16:19]\nv_mfma_f32_16x16x16_bf16 v[20:23], a[156:157], v[6:7], v[20:23]\nv_mfma_f32_16x16x16_bf16 v[8:11], a[142:143], v[6:7], v[8:11]\nv_mfma_f32_16x16x16_bf16 v[12:15], v[62:63], v[6:7], v[12:15]\nv_mfma_f32_16x16x16_bf16 v[16:19], a[150:151], v[6:7], v[16:19]\nv_mfma_f32_16x16x16_bf16 v[20:23], a[158:159], v[6:7], v[20:23]\n" \
"s_waitcnt vmcnt(28)\ns_barrier\ns_add_u32 m0, %[lw], 4096\ns_add_u32 s44, %[wo], 5046272\nbuffer_load_dwordx4 v92, %[rw], s44 offen nt lds\ns_add_u32 m0, %[lw], 5120\ns_add_u32 s44, %[wo], 5103616\nbuffer_load_dwordx4 v92, %[rw], s44 offen nt lds\n" \
"s_add_u32 m0, %[lw], 6144\ns_add_u32 s44, %[wo], 5160960\nbuffer_load_dwordx4 v92, %[rw], s44 offen nt lds\ns_add_u32 m0, %[lw], 7168\ns_add_u32 s44, %[wo], 5218304\nbuffer_load_dwordx4 v92, %[rw], s44 offen nt lds\nds_read_b64_tr_b16 a[160:161], v94 offset:8192\nds_read_b64_tr_b16 a[162:163], v94 offset:8704\n" \
"ds_read_b64_tr_b16 a[168:169], v94 offset:8200\nds_read_b64_tr_b16 a[170:171], v94 offset:8712\nds_read_b64_tr_b16 v[56:57], v94 offset:32768\nds_read_b64_tr_b16 v[58:59], v94 offset:33280\nds_read_b64_tr_b16 a[176:177], v94 offset:32776\nds_read_b64_tr_b16 a[178:179], v94 offset:33288\ns_waitcnt lgkmcnt(0)\nv_mfma_f32_16x16x16_bf16 v[8:11], a[160:161], v[6:7], v[8:11]\n" \
"v_mfma_f32_16x16x16_bf16 v[12:15], a[168:169], v[6:7], v[12:15]\nv_mfma_f32_16x16x16_bf16 v[16:19], v[56:57], v[6:7], v[16:19]\nv_mfma_f32_16x16x16_bf16 v[20:23], a[176:177], v[6:7], v[20:23]\nv_mfma_f32_16x16x16_bf16 v[8:11], a[162:163], v[6:7], v[8:11]\nv_mfma_f32_16x16x16_bf16 v[12:15], a[170:171], v[6:7], v[12:15]\nv_mfma_f32_16x16x16_bf16 v[16:19], v[58:59], v[6:7], v[16:19]\nv_mfma_f32_16x16x16_bf16 v[20:23], a[178:179], v[6:7], v[20:23]\ns_waitcnt vmcnt(28)\n" \
"s_barrier\ns_add_u32 m0, %[lw], 8192\ns_add_u32 s44, %[wo], 5275648\nbuffer_load_dwordx4 v92, %[rw], s44 offen nt lds\ns_add_u32 m0, %[lw], 9216\ns_add_u32 s44, %[wo], 5332992\nbuffer_load_dwordx4 v92, %[rw], s44 offen nt lds\ns_add_u32 m0, %[lw], 10240\n" \
"s_add_u32 s44, %[wo], 5390336\nbuffer_load_dwordx4 v92, %[rw], s44 offen nt lds\ns_add_u32 m0, %[lw], 11264\ns_add_u32 s44, %[wo], 5447680\nbuffer_load_dwordx4 v92, %[rw], s44 offen nt lds\nds_read_b64_tr_b16 a[164:165], v94 offset:12288\nds_read_b64_tr_b16 a[166:167], v94 offset:12800\nds_read_b64_tr_b16 a[172:173], v94 offset:12296\n" \
"ds_read_b64_tr_b16 a[174:175], v94 offset:12808\nds_read_b64_tr_b16 v[52:53], v94 offset:36864\nds_read_b64_tr_b16 v[54:55], v94 offset:37376\nds_read_b64_tr_b16 a[180:181], v94 offset:36872\nds_read_b64_tr_b16 a[182:183], v94 offset:37384\ns_waitcnt lgkmcnt(0)\nv_mfma_f32_16x16x16_bf16 v[8:11], a[164:165], v[6:7], v[8:11]\nv_mfma_f32_16x16x16_bf16 v[12:15], a[172:173], v[6:7], v[12:15]\n" \
"v_mfma_f32_16x16x16_bf16 v[16:19], v[52:53], v[6:7], v[16:19]\nv_mfma_f32_16x16x16_bf16 v[20:23], a[180:181], v[6:7], v[20:23]\nv_mfma_f32_16x16x16_bf16 v[8:11], a[166:167], v[6:7], v[8:11]\nv_mfma_f32_16x16x16_bf16 v[12:15], a[174:175], v[6:7], v[12:15]\nv_mfma_f32_16x16x16_bf16 v[16:19], v[54:55], v[6:7], v[16:19]\nv_mfma_f32_16x16x16_bf16 v[20:23], a[182:183], v[6:7], v[20:23]\ns_waitcnt vmcnt(28)\ns_barrier\n" \
"s_add_u32 m0, %[lw], 12288\ns_add_u32 s44, %[wo], 5963776\nbuffer_load_dwordx4 v92, %[rw], s44 offen nt lds\ns_add_u32 m0, %[lw], 13312\ns_add_u32 s44, %[wo], 6021120\nbuffer_load_dwordx4 v92, %[rw], s44 offen nt lds\ns_add_u32 m0, %[lw], 14336\ns_add_u32 s44, %[wo], 6078464\n" \
"buffer_load_dwordx4 v92, %[rw], s44 offen nt lds\ns_add_u32 m0, %[lw], 15360\ns_add_u32 s44, %[wo], 6135808\nbuffer_load_dwordx4 v92, %[rw], s44 offen nt lds\nds_read_b64_tr_b16 a[184:185], v94 offset:16384\nds_read_b64_tr_b16 a[186:187], v94 offset:16896\nds_read_b64_tr_b16 a[192:193], v94 offset:16392\nds_read_b64_tr_b16 a[194:195], v94 offset:16904\n" \
"ds_read_b64_tr_b16 a[200:201], v94 offset:40960\nds_read_b64_tr_b16 a[202:203], v94 offset:41472\nds_read_b64_tr_b16 v[48:49], v94 offset:40968\nds_read_b64_tr_b16 v[50:51], v94 offset:41480\ns_waitcnt lgkmcnt(0)\nv_mfma_f32_16x16x16_bf16 v[8:11], a[184:185], v[6:7], v[8:11]\nv_mfma_f32_16x16x16_bf16 v[12:15], a[192:193], v[6:7], v[12:15]\nv_mfma_f32_16x16x16_bf16 v[16:19], a[200:201], v[6:7], v[16:19]\n" \
"v_mfma_f32_16x16x16_bf16 v[20:23], v[48:49], v[6:7], v[20:23]\nv_mfma_f32_16x16x16_bf16 v[8:11], a[186:187], v[6:7], v[8:11]\nv_mfma_f32_16x16x16_bf16 v[12:15], a[194:195], v[6:7], v[12:15]\nv_mfma_f32_16x16x16_bf16 v[16:19], a[202:203], v[6:7], v[16:19]\nv_mfma_f32_16x16x16_bf16 v[20:23], v[50:51], v[6:7], v[20:23]\ns_waitcnt vmcnt(28)\ns_barrier\ns_add_u32 m0, %[lw], 16384\n" \
"s_add_u32 s44, %[wo], 6193152\nbuffer_load_dwordx4 v92, %[rw], s44 offen nt lds\ns_add_u32 m0, %[lw], 17408\ns_add_u32 s44, %[wo], 6250496\nbuffer_load_dwordx4 v92, %[rw], s44 offen nt lds\ns_add_u32 m0, %[lw], 18432\ns_add_u32 s44, %[wo], 6307840\nbuffer_load_dwordx4 v92, %[rw], s44 offen nt lds\n" \
"s_add_u32 m0, %[lw], 19456\ns_add_u32 s44, %[wo], 6365184\nbuffer_load_dwordx4 v92, %[rw], s44 offen nt lds\nds_read_b64_tr_b16 a[188:189], v94 offset:20480\nds_read_b64_tr_b16 a[190:191], v94 offset:20992\nds_read_b64_tr_b16 a[196:197], v94 offset:20488\nds_read_b64_tr_b16 a[198:199], v94 offset:21000\nds_read_b64_tr_b16 a[204:205], v94 offset:45056\n" \
"ds_read_b64_tr_b16 a[206:207], v94 offset:45568\nds_read_b64_tr_b16 v[44:45], v94 offset:45064\nds_read_b64_tr_b16 v[46:47], v94 offset:45576\ns_waitcnt lgkmcnt(0)\nv_mfma_f32_16x16x16_bf16 v[8:11], a[188:189], v[6:7], v[8:11]\nv_mfma_f32_16x16x16_bf16 v[12:15], a[196:197], v[6:7], v[12:15]\nv_mfma_f32_16x16x16_bf16 v[16:19], a[204:205], v[6:7], v[16:19]\nv_mfma_f32_16x16x16_bf16 v[20:23], v[44:45], v[6:7], v[20:23]\n" \
"v_mfma_f32_16x16x16_bf16 v[8:11], a[190:191], v[6:7], v[8:11]\nv_mfma_f32_16x16x16_bf16 v[12:15], a[198:199], v[6:7], v[12:15]\nv_mfma_f32_16x16x16_bf16 v[16:19], a[206:207], v[6:7], v[16:19]\nv_mfma_f32_16x16x16_bf16 v[20:23], v[46:47], v[6:7], v[20:23]\ns_waitcnt vmcnt(28)\ns_barrier\ns_add_u32 m0, %[lw], 20480\ns_add_u32 s44, %[wo], 6422528\n" \
"buffer_load_dwordx4 v92, %[rw], s44 offen nt lds\ns_add_u32 m0, %[lw], 21504\ns_add_u32 s44, %[wo], 6479872\nbuffer_load_dwordx4 v92, %[rw], s44 offen nt lds\ns_add_u32 m0, %[lw], 22528\ns_add_u32 s44, %[wo], 6537216\nbuffer_load_dwordx4 v92, %[rw], s44 offen nt lds\ns_add_u32 m0, %[lw], 23552\n" \
"s_add_u32 s44, %[wo], 6594560\nbuffer_load_dwordx4 v92, %[rw], s44 offen nt lds\nds_read_b64_tr_b16 a[240:241], v93 offset:0\nds_read_b64_tr_b16 a[242:243], v93 offset:512\nds_read_b64_tr_b16 v[40:41], v93 offset:8\nds_read_b64_tr_b16 v[42:43], v93 offset:520\nds_read_b64_tr_b16 a[248:249], v93 offset:12288\nds_read_b64_tr_b16 a[250:251], v93 offset:12800\n" \
"ds_read_b64_tr_b16 v[96:97], v93 offset:12296\nds_read_b64_tr_b16 v[98:99], v93 offset:12808\ns_waitcnt lgkmcnt(0)\nv_mfma_f32_16x16x16_bf16 v[8:11], a[240:241], v[6:7], v[8:11]\nv_mfma_f32_16x16x16_bf16 v[12:15], v[40:41], v[6:7], v[12:15]\nv_mfma_f32_16x16x16_bf16 v[16:19], a[248:249], v[6:7], v[16:19]\nv_mfma_f32_16x16x16_bf16 v[20:23], v[96:97], v[6:7], v[20:23]\nv_mfma_f32_16x16x16_bf16 v[8:11], a[242:243], v[6:7], v[8:11]\n" \
"v_mfma_f32_16x16x16_bf16 v[12:15], v[42:43], v[6:7], v[12:15]\nv_mfma_f32_16x16x16_bf16 v[16:19], a[250:251], v[6:7], v[16:19]\nv_mfma_f32_16x16x16_bf16 v[20:23], v[98:99], v[6:7], v[20:23]\ns_waitcnt vmcnt(28)\ns_barrier\ns_add_u32 m0, %[tb], 0\ns_add_u32 s44, %[wo], 6651904\nbuffer_load_dwordx4 v92, %[rw], s44 offen nt lds\n" \
"s_add_u32 m0, %[tb], 1024\ns_add_u32 s44, %[wo], 6709248\nbuffer_load_dwordx4 v92, %[rw], s44 offen nt lds\ns_add_u32 m0, %[tb], 2048\ns_add_u32 s44, %[wo], 6766592\nbuffer_load_dwordx4 v92, %[rw], s44 offen nt lds\ns_add_u32 m0, %[tb], 3072\ns_add_u32 s44, %[wo], 6823936\n" \
"buffer_load_dwordx4 v92, %[rw], s44 offen nt lds\nds_read_b64_tr_b16 a[244:245], v93 offset:4096\nds_read_b64_tr_b16 a[246:247], v93 offset:4608\nds_read_b64_tr_b16 v[36:37], v93 offset:4104\nds_read_b64_tr_b16 v[38:39], v93 offset:4616\nds_read_b64_tr_b16 a[252:253], v93 offset:16384\nds_read_b64_tr_b16 a[254:255], v93 offset:16896\nds_read_b64_tr_b16 v[100:101], v93 offset:16392\n" \
"ds_read_b64_tr_b16 v[102:103], v93 offset:16904\ns_waitcnt lgkmcnt(0)\nv_mfma_f32_16x16x16_bf16 v[8:11], a[244:245], v[6:7], v[8:11]\nv_mfma_f32_16x16x16_bf16 v[12:15], v[36:37], v[6:7], v[12:15]\nv_mfma_f32_16x16x16_bf16 v[16:19], a[252:253], v[6:7], v[16:19]\nv_mfma_f32_16x16x16_bf16 v[20:23], v[100:101], v[6:7], v[20:23]\nv_mfma_f32_16x16x16_bf16 v[8:11], a[246:247], v[6:7], v[8:11]\nv_mfma_f32_16x16x16_bf16 v[12:15], v[38:39], v[6:7], v[12:15]\n" \
"v_mfma_f32_16x16x16_bf16 v[16:19], a[254:255], v[6:7], v[16:19]\nv_mfma_f32_16x16x16_bf16 v[20:23], v[102:103], v[6:7], v[20:23]\ns_waitcnt vmcnt(28)\ns_barrier\ns_add_u32 m0, %[tb], 4096\ns_add_u32 s44, %[wo], 6881280\nbuffer_load_dwordx4 v92, %[rw], s44 offen nt lds\ns_add_u32 m0, %[tb], 5120\n" \
"s_add_u32 s44, %[wo], 6938624\nbuffer_load_dwordx4 v92, %[rw], s44 offen nt lds\ns_add_u32 m0, %[tb], 6144\ns_add_u32 s44, %[wo], 6995968\nbuffer_load_dwordx4 v92, %[rw], s44 offen nt lds\ns_add_u32 m0, %[tb], 7168\ns_add_u32 s44, %[wo], 7053312\nbuffer_load_dwordx4 v92, %[rw], s44 offen nt lds\n" \
"ds_read_b64_tr_b16 v[104:105], v93 offset:8192\nds_read_b64_tr_b16 v[106:107], v93 offset:8704\nds_read_b64_tr_b16 v[112:113], v93 offset:8200\nds_read_b64_tr_b16 v[114:115], v93 offset:8712\nds_read_b64_tr_b16 v[32:33], v93 offset:20480\nds_read_b64_tr_b16 v[34:35], v93 offset:20992\nds_read_b64_tr_b16 v[120:121], v93 offset:20488\nds_read_b64_tr_b16 v[122:123], v93 offset:21000\n" \
"s_waitcnt lgkmcnt(0)\nv_mfma_f32_16x16x16_bf16 v[8:11], v[104:105], v[6:7], v[8:11]\nv_mfma_f32_16x16x16_bf16 v[12:15], v[112:113], v[6:7], v[12:15]\nv_mfma_f32_16x16x16_bf16 v[16:19], v[32:33], v[6:7], v[16:19]\nv_mfma_f32_16x16x16_bf16 v[20:23], v[120:121], v[6:7], v[20:23]\nv_mfma_f32_16x16x16_bf16 v[8:11], v[106:107], v[6:7], v[8:11]\nv_mfma_f32_16x16x16_bf16 v[12:15], v[114:115], v[6:7], v[12:15]\nv_mfma_f32_16x16x16_bf16 v[16:19], v[34:35], v[6:7], v[16:19]\n" \
"v_mfma_f32_16x16x16_bf16 v[20:23], v[122:123], v[6:7], v[20:23]\ns_waitcnt vmcnt(28)\ns_barrier\ns_add_u32 m0, %[tb], 8192\ns_add_u32 s44, %[wo], 7110656\nbuffer_load_dwordx4 v92, %[rw], s44 offen nt lds\ns_add_u32 m0, %[tb], 9216\ns_add_u32 s44, %[wo], 7168000\n" \
"buffer_load_dwordx4 v92, %[rw], s44 offen nt lds\ns_add_u32 m0, %[tb], 10240\ns_add_u32 s44, %[wo], 7225344\nbuffer_load_dwordx4 v92, %[rw], s44 offen nt lds\ns_add_u32 m0, %[tb], 11264\ns_add_u32 s44, %[wo], 7282688\nbuffer_load_dwordx4 v92, %[rw], s44 offen nt lds\nds_read_b64_tr_b16 v[108:109], v94 offset:0\n" \
"ds_read_b64_tr_b16 v[110:111], v94 offset:512\nds_read_b64_tr_b16 v[116:117], v94 offset:8\nds_read_b64_tr_b16 v[118:119], v94 offset:520\nds_read_b64_tr_b16 v[28:29], v94 offset:24576\nds_read_b64_tr_b16 v[30:31], v94 offset:25088\nds_read_b64_tr_b16 v[124:125], v94 offset:24584\nds_read_b64_tr_b16 v[126:127], v94 offset:25096\ns_waitcnt lgkmcnt(0)\n" \
"v_mfma_f32_16x16x16_bf16 v[8:11], v[108:109], v[6:7], v[8:11]\nv_mfma_f32_16x16x16_bf16 v[12:15], v[116:117], v[6:7], v[12:15]\nv_mfma_f32_16x16x16_bf16 v[16:19], v[28:29], v[6:7], v[16:19]\nv_mfma_f32_16x16x16_bf16 v[20:23], v[124:125], v[6:7], v[20:23]\nv_mfma_f32_16x16x16_bf16 v[8:11], v[110:111], v[6:7], v[8:11]\nv_mfma_f32_16x16x16_bf16 v[12:15], v[118:119], v[6:7], v[12:15]\nv_mfma_f32_16x16x16_bf16 v[16:19], v[30:31], v[6:7], v[16:19]\nv_mfma_f32_16x16x16_bf16 v[20:23], v[126:127], v[6:7], v[20:23]\n" \
"s_waitcnt vmcnt(28)\ns_barrier\nds_read_b64_tr_b16 v[128:129], v94 offset:4096\nds_read_b64_tr_b16 v[130:131], v94 offset:4608\nds_read_b64_tr_b16 v[136:137], v94 offset:4104\nds_read_b64_tr_b16 v[138:139], v94 offset:4616\nds_read_b64_tr_b16 v[144:145], v94 offset:28672\nds_read_b64_tr_b16 v[146:147], v94 offset:29184\n" \
"ds_read_b64_tr_b16 v[24:25], v94 offset:28680\nds_read_b64_tr_b16 v[26:27], v94 offset:29192\ns_waitcnt lgkmcnt(0)\nv_mfma_f32_16x16x16_bf16 v[8:11], v[128:129], v[6:7], v[8:11]\nv_mfma_f32_16x16x16_bf16 v[12:15], v[136:137], v[6:7], v[12:15]\nv_mfma_f32_16x16x16_bf16 v[16:19], v[144:145], v[6:7], v[16:19]\nv_mfma_f32_16x16x16_bf16 v[20:23], v[24:25], v[6:7], v[20:23]\nv_mfma_f32_16x16x16_bf16 v[8:11], v[130:131], v[6:7], v[8:11]\n" \
"v_mfma_f32_16x16x16_bf16 v[12:15], v[138:139], v[6:7], v[12:15]\nv_mfma_f32_16x16x16_bf16 v[16:19], v[146:147], v[6:7], v[16:19]\nv_mfma_f32_16x16x16_bf16 v[20:23], v[26:27], v[6:7], v[20:23]\ns_waitcnt vmcnt(24)\nds_write_b128 v1, v[88:91] offset:0\nds_write_b128 v1, v[84:87] offset:1024\nds_write_b128 v1, v[80:83] offset:2048\nds_write_b128 v1, v[76:79] offset:3072\n" \
"s_barrier\nds_read_b64_tr_b16 v[132:133], v94 offset:8192\nds_read_b64_tr_b16 v[134:135], v94 offset:8704\nds_read_b64_tr_b16 v[140:141], v94 offset:8200\nds_read_b64_tr_b16 v[142:143], v94 offset:8712\nds_read_b64_tr_b16 v[148:149], v94 offset:32768\nds_read_b64_tr_b16 v[150:151], v94 offset:33280\nds_read_b64_tr_b16 v[76:77], v94 offset:32776\n" \
"ds_read_b64_tr_b16 v[78:79], v94 offset:33288\ns_waitcnt lgkmcnt(0)\nv_mfma_f32_16x16x16_bf16 v[8:11], v[132:133], v[6:7], v[8:11]\nv_mfma_f32_16x16x16_bf16 v[12:15], v[140:141], v[6:7], v[12:15]\nv_mfma_f32_16x16x16_bf16 v[16:19], v[148:149], v[6:7], v[16:19]\nv_mfma_f32_16x16x16_bf16 v[20:23], v[76:77], v[6:7], v[20:23]\nv_mfma_f32_16x16x16_bf16 v[8:11], v[134:135], v[6:7], v[8:11]\nv_mfma_f32_16x16x16_bf16 v[12:15], v[142:143], v[6:7], v[12:15]\n" \
"v_mfma_f32_16x16x16_bf16 v[16:19], v[150:151], v[6:7], v[16:19]\nv_mfma_f32_16x16x16_bf16 v[20:23], v[78:79], v[6:7], v[20:23]\ns_waitcnt vmcnt(20)\nds_write_b128 v1, v[72:75] offset:4096\nds_write_b128 v1, v[68:71] offset:5120\nds_write_b128 v1, v[64:67] offset:6144\nds_write_b128 v1, v[60:63] offset:7168\ns_barrier\n" \
"ds_read_b64_tr_b16 v[184:185], v94 offset:12288\nds_read_b64_tr_b16 v[186:187], v94 offset:12800\nds_read_b64_tr_b16 v[60:61], v94 offset:12296\nds_read_b64_tr_b16 v[62:63], v94 offset:12808\nds_read_b64_tr_b16 v[192:193], v94 offset:36864\nds_read_b64_tr_b16 v[194:195], v94 offset:37376\nds_read_b64_tr_b16 v[200:201], v94 offset:36872\nds_read_b64_tr_b16 v[202:203], v94 offset:37384\n" \
"s_waitcnt lgkmcnt(0)\nv_mfma_f32_16x16x16_bf16 v[8:11], v[184:185], v[6:7], v[8:11]\nv_mfma_f32_16x16x16_bf16 v[12:15], v[60:61], v[6:7], v[12:15]\nv_mfma_f32_16x16x16_bf16 v[16:19], v[192:193], v[6:7], v[16:19]\nv_mfma_f32_16x16x16_bf16 v[20:23], v[200:201], v[6:7], v[20:23]\nv_mfma_f32_16x16x16_bf16 v[8:11], v[186:187], v[6:7], v[8:11]\nv_mfma_f32_16x16x16_bf16 v[12:15], v[62:63], v[6:7], v[12:15]\nv_mfma_f32_16x16x16_bf16 v[16:19], v[194:195], v[6:7], v[16:19]\n" \
"v_mfma_f32_16x16x16_bf16 v[20:23], v[202:203], v[6:7], v[20:23]\ns_waitcnt vmcnt(16)\nds_write_b128 v1, v[56:59] offset:8192\nds_write_b128 v1, v[52:55] offset:9216\nds_write_b128 v1, v[48:51] offset:10240\nds_write_b128 v1, v[44:47] offset:11264\ns_barrier\nds_read_b64_tr_b16 v[188:189], v94 offset:16384\n" \
"ds_read_b64_tr_b16 v[190:191], v94 offset:16896\nds_read_b64_tr_b16 v[44:45], v94 offset:16392\nds_read_b64_tr_b16 v[46:47], v94 offset:16904\nds_read_b64_tr_b16 v[196:197], v94 offset:40960\nds_read_b64_tr_b16 v[198:199], v94 offset:41472\nds_read_b64_tr_b16 v[204:205], v94 offset:40968\nds_read_b64_tr_b16 v[206:207], v94 offset:41480\ns_waitcnt lgkmcnt(0)\n" \
"v_mfma_f32_16x16x16_bf16 v[8:11], v[188:189], v[6:7], v[8:11]\nv_mfma_f32_16x16x16_bf16 v[12:15], v[44:45], v[6:7], v[12:15]\nv_mfma_f32_16x16x16_bf16 v[16:19], v[196:197], v[6:7], v[16:19]\nv_mfma_f32_16x16x16_bf16 v[20:23], v[204:205], v[6:7], v[20:23]\nv_mfma_f32_16x16x16_bf16 v[8:11], v[190:191], v[6:7], v[8:11]\nv_mfma_f32_16x16x16_bf16 v[12:15], v[46:47], v[6:7], v[12:15]\nv_mfma_f32_16x16x16_bf16 v[16:19], v[198:199], v[6:7], v[16:19]\nv_mfma_f32_16x16x16_bf16 v[20:23], v[206:207], v[6:7], v[20:23]\n" \
"s_waitcnt vmcnt(12)\nds_write_b128 v1, v[40:43] offset:12288\nds_write_b128 v1, v[36:39] offset:13312\nds_write_b128 v1, v[32:35] offset:14336\nds_write_b128 v1, v[28:31] offset:15360\ns_barrier\nds_read_b64_tr_b16 v[208:209], v94 offset:20480\nds_read_b64_tr_b16 v[210:211], v94 offset:20992\n" \
"ds_read_b64_tr_b16 v[216:217], v94 offset:20488\nds_read_b64_tr_b16 v[218:219], v94 offset:21000\nds_read_b64_tr_b16 v[28:29], v94 offset:45056\nds_read_b64_tr_b16 v[30:31], v94 offset:45568\nds_read_b64_tr_b16 v[224:225], v94 offset:45064\nds_read_b64_tr_b16 v[226:227], v94 offset:45576\ns_waitcnt lgkmcnt(0)\nv_mfma_f32_16x16x16_bf16 v[8:11], v[208:209], v[6:7], v[8:11]\n" \
"v_mfma_f32_16x16x16_bf16 v[12:15], v[216:217], v[6:7], v[12:15]\nv_mfma_f32_16x16x16_bf16 v[16:19], v[28:29], v[6:7], v[16:19]\nv_mfma_f32_16x16x16_bf16 v[20:23], v[224:225], v[6:7], v[20:23]\nv_mfma_f32_16x16x16_bf16 v[8:11], v[210:211], v[6:7], v[8:11]\nv_mfma_f32_16x16x16_bf16 v[12:15], v[218:219], v[6:7], v[12:15]\nv_mfma_f32_16x16x16_bf16 v[16:19], v[30:31], v[6:7], v[16:19]\nv_mfma_f32_16x16x16_bf16 v[20:23], v[226:227], v[6:7], v[20:23]\ns_waitcnt vmcnt(8)\n" \
"ds_write_b128 v1, v[24:27] offset:16384\nds_write_b128 v1, v[76:79] offset:17408\nds_write_b128 v1, v[60:63] offset:18432\nds_write_b128 v1, v[44:47] offset:19456\ns_barrier\nds_read_b64_tr_b16 v[212:213], v93 offset:0\nds_read_b64_tr_b16 v[214:215], v93 offset:512\nds_read_b64_tr_b16 v[220:221], v93 offset:8\n" \
"ds_read_b64_tr_b16 v[222:223], v93 offset:520\nds_read_b64_tr_b16 v[44:45], v93 offset:12288\nds_read_b64_tr_b16 v[46:47], v93 offset:12800\nds_read_b64_tr_b16 v[228:229], v93 offset:12296\nds_read_b64_tr_b16 v[230:231], v93 offset:12808\ns_waitcnt lgkmcnt(0)\nv_mfma_f32_16x16x16_bf16 v[8:11], v[212:213], v[6:7], v[8:11]\nv_mfma_f32_16x16x16_bf16 v[12:15], v[220:221], v[6:7], v[12:15]\n" \
"v_mfma_f32_16x16x16_bf16 v[16:19], v[44:45], v[6:7], v[16:19]\nv_mfma_f32_16x16x16_bf16 v[20:23], v[228:229], v[6:7], v[20:23]\nv_mfma_f32_16x16x16_bf16 v[8:11], v[214:215], v[6:7], v[8:11]\nv_mfma_f32_16x16x16_bf16 v[12:15], v[222:223], v[6:7], v[12:15]\nv_mfma_f32_16x16x16_bf16 v[16:19], v[46:47], v[6:7], v[16:19]\nv_mfma_f32_16x16x16_bf16 v[20:23], v[230:231], v[6:7], v[20:23]\ns_waitcnt vmcnt(4)\nds_write_b128 v1, v[28:31] offset:20480\n" \
"ds_write_b128 v1, v[44:47] offset:21504\ns_barrier\nds_read_b64_tr_b16 v[232:233], v93 offset:4096\nds_read_b64_tr_b16 v[234:235], v93 offset:4608\nds_read_b64_tr_b16 v[240:241], v93 offset:4104\nds_read_b64_tr_b16 v[242:243], v93 offset:4616\nds_read_b64_tr_b16 v[248:249], v93 offset:16384\nds_read_b64_tr_b16 v[250:251], v93 offset:16896\n" \
"ds_read_b64_tr_b16 v[44:45], v93 offset:16392\nds_read_b64_tr_b16 v[46:47], v93 offset:16904\ns_waitcnt lgkmcnt(0)\nv_mfma_f32_16x16x16_bf16 v[8:11], v[232:233], v[6:7], v[8:11]\nv_mfma_f32_16x16x16_bf16 v[12:15], v[240:241], v[6:7], v[12:15]\nv_mfma_f32_16x16x16_bf16 v[16:19], v[248:249], v[6:7], v[16:19]\nv_mfma_f32_16x16x16_bf16 v[20:23], v[44:45], v[6:7], v[20:23]\nv_mfma_f32_16x16x16_bf16 v[8:11], v[234:235], v[6:7], v[8:11]\n" \
"v_mfma_f32_16x16x16_bf16 v[12:15], v[242:243], v[6:7], v[12:15]\nv_mfma_f32_16x16x16_bf16 v[16:19], v[250:251], v[6:7], v[16:19]\nv_mfma_f32_16x16x16_bf16 v[20:23], v[46:47], v[6:7], v[20:23]\ns_waitcnt vmcnt(0)\nds_write_b128 v1, v[44:47] offset:22528\ns_barrier\nds_read_b64_tr_b16 v[236:237], v93 offset:8192\nds_read_b64_tr_b16 v[238:239], v93 offset:8704\n" \
"ds_read_b64_tr_b16 v[244:245], v93 offset:8200\nds_read_b64_tr_b16 v[246:247], v93 offset:8712\nds_read_b64_tr_b16 v[252:253], v93 offset:20480\nds_read_b64_tr_b16 v[254:255], v93 offset:20992\nds_read_b64_tr_b16 v[44:45], v93 offset:20488\nds_read_b64_tr_b16 v[46:47], v93 offset:21000\ns_waitcnt lgkmcnt(0)\nv_mfma_f32_16x16x16_bf16 v[8:11], v[236:237], v[6:7], v[8:11]\n" \
"v_mfma_f32_16x16x16_bf16 v[12:15], v[244:245], v[6:7], v[12:15]\nv_mfma_f32_16x16x16_bf16 v[16:19], v[252:253], v[6:7], v[16:19]\nv_mfma_f32_16x16x16_bf16 v[20:23], v[44:45], v[6:7], v[20:23]\nv_mfma_f32_16x16x16_bf16 v[8:11], v[238:239], v[6:7], v[8:11]\nv_mfma_f32_16x16x16_bf16 v[12:15], v[246:247], v[6:7], v[12:15]\nv_mfma_f32_16x16x16_bf16 v[16:19], v[254:255], v[6:7], v[16:19]\nv_mfma_f32_16x16x16_bf16 v[20:23], v[46:47], v[6:7], v[20:23]\nds_write_b128 v1, v[44:47] offset:23552\n" \
"s_waitcnt lgkmcnt(0)\ns_barrier\ns_mov_b32 s40, 0\ns_mov_b32 s43, 0\ns_lshl_b32 s57, %[w], 12\ns_mov_b32 s58, 0xbeeff558\ns_mov_b32 s59, 0x409d30fc\ns_mov_b32 s56, -2147483648\n" \
".Lgg_next%=:\ns_cmp_lg_u32 %[w], 0\ns_cbranch_scc1 .Lgg_w1%=\nbuffer_load_dword v56, v3, %[rs], 0 offen sc1\ns_waitcnt vmcnt(0)\nbuffer_store_dword v90, off, %[rs], s56 sc1\n.Lgg_poll%=:\nv_cmp_gt_i32 vcc, s40, v56\n" \
"s_cbranch_vccz .Lgg_go%=\ns_sleep 12\nbuffer_load_dword v56, v3, %[rs], 0 offen sc1\ns_waitcnt vmcnt(0)\ns_add_u32 s43, s43, 1\ns_cmp_lt_u32 s43, 262144\ns_cbranch_scc1 .Lgg_poll%=\ns_branch .Lgg_abort%=\n" \
".Lgg_w1%=:\ns_waitcnt vmcnt(0)\nbuffer_store_dword v90, off, %[rs], s56 sc1\n.Lgg_go%=:\ns_add_u32 s44, s40, 0\ns_lshl_b32 s44, s44, 17\ns_add_u32 s44, s44, %[xn]\ns_lshr_b32 s45, s44, 8\n" \
"s_and_b32 s45, s45, 0x2000\ns_xor_b32 s41, s44, s45\ns_add_u32 s61, s41, %[xw]\ns_barrier\ns_add_u32 m0, s57, 0\ns_add_u32 s60, s61, 0\nbuffer_load_dwordx4 v0, %[rs], s60 offen offset:0 sc1 lds\nbuffer_load_dwordx4 v0, %[rs], s60 offen offset:1024 sc1 lds\n" \
"buffer_load_dwordx4 v0, %[rs], s60 offen offset:2048 sc1 lds\nbuffer_load_dwordx4 v0, %[rs], s60 offen offset:3072 sc1 lds\ns_add_u32 m0, s57, 16384\ns_add_u32 s60, s61, 32768\nbuffer_load_dwordx4 v0, %[rs], s60 offen offset:0 sc1 lds\nbuffer_load_dwordx4 v0, %[rs], s60 offen offset:1024 sc1 lds\nbuffer_load_dwordx4 v0, %[rs], s60 offen offset:2048 sc1 lds\nbuffer_load_dwordx4 v0, %[rs], s60 offen offset:3072 sc1 lds\n" \
"buffer_load_dwordx4 v[40:43], v5, %[rs], s41 offen offset:0 sc1\nbuffer_load_dwordx4 v[44:47], v5, %[rs], s41 offen offset:256 sc1\ns_lshl_b32 s44, s40, 19\ns_add_u32 s44, s44, %[ho]\ns_lshr_b32 s45, s44, 8\ns_and_b32 s45, s45, 0x2000\ns_xor_b32 s55, s44, s45\ns_waitcnt vmcnt(6)\n" \
"s_barrier\nds_read_b128 v[56:59], v2 offset:0\nds_read_b128 v[60:63], v2 offset:2048\nds_read_b128 v[64:67], v2 offset:4096\ns_waitcnt lgkmcnt(2)\nv_mfma_f32_16x16x32_bf16 v[24:27], a[0:3], v[56:59], 0\nds_read_b128 v[68:71], v2 offset:6144\nds_read_b128 v[80:83], v1 offset:0\n" \
"v_mfma_f32_16x16x32_bf16 v[28:31], a[8:11], v[56:59], 0\nbuffer_load_dwordx4 v[48:51], v5, %[rs], s41 offen offset:512 sc1\nv_mfma_f32_16x16x32_bf16 v[32:35], a[16:19], v[56:59], 0\nv_mfma_f32_16x16x32_bf16 v[36:39], a[24:27], v[56:59], 0\ns_waitcnt lgkmcnt(3)\nds_read_b128 v[72:75], v1 offset:1024\nv_mfma_f32_16x16x32_bf16 v[24:27], a[4:7], v[60:63], v[24:27]\nds_read_b128 v[56:59], v2 offset:8192\n" \
"v_mfma_f32_16x16x32_bf16 v[28:31], a[12:15], v[60:63], v[28:31]\nbuffer_load_dwordx4 v[52:55], v5, %[rs], s41 offen offset:768 sc1\nv_mfma_f32_16x16x32_bf16 v[32:35], a[20:23], v[60:63], v[32:35]\nv_mfma_f32_16x16x32_bf16 v[36:39], a[28:31], v[60:63], v[36:39]\ns_waitcnt lgkmcnt(4)\nds_read_b128 v[76:79], v1 offset:2048\nv_mfma_f32_16x16x32_bf16 v[24:27], a[32:35], v[64:67], v[24:27]\ns_waitcnt lgkmcnt(3)\n" \
"ds_read_b128 v[60:63], v2 offset:10240\nv_mfma_f32_16x16x32_bf16 v[28:31], v[80:83], v[64:67], v[28:31]\ns_add_u32 m0, s57, 32768\ns_add_u32 s60, s61, 65536\nbuffer_load_dwordx4 v0, %[rs], s60 offen offset:0 sc1 lds\nv_mfma_f32_16x16x32_bf16 v[32:35], a[40:43], v[64:67], v[32:35]\nv_mfma_f32_16x16x32_bf16 v[36:39], a[48:51], v[64:67], v[36:39]\nds_read_b128 v[80:83], v1 offset:3072\n" \
"v_mfma_f32_16x16x32_bf16 v[24:27], a[36:39], v[68:71], v[24:27]\ns_waitcnt lgkmcnt(4)\nds_read_b128 v[64:67], v2 offset:12288\nv_mfma_f32_16x16x32_bf16 v[28:31], v[72:75], v[68:71], v[28:31]\nbuffer_load_dwordx4 v0, %[rs], s60 offen offset:1024 sc1 lds\nv_mfma_f32_16x16x32_bf16 v[32:35], a[44:47], v[68:71], v[32:35]\nv_mfma_f32_16x16x32_bf16 v[36:39], a[52:55], v[68:71], v[36:39]\ns_waitcnt lgkmcnt(4)\n" \
"ds_read_b128 v[72:75], v1 offset:4096\nv_mfma_f32_16x16x32_bf16 v[24:27], a[56:59], v[56:59], v[24:27]\nds_read_b128 v[68:71], v2 offset:14336\nv_mfma_f32_16x16x32_bf16 v[28:31], a[64:67], v[56:59], v[28:31]\ns_waitcnt lgkmcnt(5)\nbuffer_load_dwordx4 v0, %[rs], s60 offen offset:2048 sc1 lds\nv_mfma_f32_16x16x32_bf16 v[32:35], v[76:79], v[56:59], v[32:35]\nv_mfma_f32_16x16x32_bf16 v[36:39], a[72:75], v[56:59], v[36:39]\n" \
"s_waitcnt lgkmcnt(4)\nds_read_b128 v[76:79], v1 offset:5120\nv_mfma_f32_16x16x32_bf16 v[24:27], a[60:63], v[60:63], v[24:27]\ns_waitcnt vmcnt(7)\ns_barrier\nds_read_b128 v[56:59], v2 offset:16384\nv_mfma_f32_16x16x32_bf16 v[28:31], a[68:71], v[60:63], v[28:31]\ns_waitcnt lgkmcnt(5)\n" \
"buffer_load_dwordx4 v0, %[rs], s60 offen offset:3072 sc1 lds\nv_mfma_f32_16x16x32_bf16 v[32:35], v[80:83], v[60:63], v[32:35]\nv_mfma_f32_16x16x32_bf16 v[36:39], a[76:79], v[60:63], v[36:39]\ns_waitcnt lgkmcnt(4)\nv_mfma_f32_16x16x32_bf16 v[24:27], a[80:83], v[64:67], v[24:27]\nds_read_b128 v[60:63], v2 offset:18432\nv_mfma_f32_16x16x32_bf16 v[28:31], a[88:91], v[64:67], v[28:31]\ns_add_u32 m0, s57, 49152\n" \
"s_add_u32 s60, s61, 98304\nbuffer_load_dwordx4 v0, %[rs], s60 offen offset:0 sc1 lds\nv_mfma_f32_16x16x32_bf16 v[32:35], a[96:99], v[64:67], v[32:35]\ns_waitcnt lgkmcnt(4)\nv_mfma_f32_16x16x32_bf16 v[36:39], v[72:75], v[64:67], v[36:39]\ns_waitcnt lgkmcnt(3)\nv_mfma_f32_16x16x32_bf16 v[24:27], a[84:87], v[68:71], v[24:27]\nds_read_b128 v[64:67], v2 offset:20480\n" \
"v_mfma_f32_16x16x32_bf16 v[28:31], a[92:95], v[68:71], v[28:31]\nbuffer_load_dwordx4 v0, %[rs], s60 offen offset:1024 sc1 lds\nv_mfma_f32_16x16x32_bf16 v[32:35], a[100:103], v[68:71], v[32:35]\ns_waitcnt lgkmcnt(3)\nv_mfma_f32_16x16x32_bf16 v[36:39], v[76:79], v[68:71], v[36:39]\ns_waitcnt lgkmcnt(2)\nds_read_b128 v[76:79], v1 offset:6144\nv_mfma_f32_16x16x32_bf16 v[24:27], a[104:107], v[56:59], v[24:27]\n" \
"ds_read_b128 v[68:71], v2 offset:22528\nv_mfma_f32_16x16x32_bf16 v[28:31], a[112:115], v[56:59], v[28:31]\nbuffer_load_dwordx4 v0, %[rs], s60 offen offset:2048 sc1 lds\nv_mfma_f32_16x16x32_bf16 v[32:35], a[120:123], v[56:59], v[32:35]\nv_mfma_f32_16x16x32_bf16 v[36:39], a[128:131], v[56:59], v[36:39]\ns_waitcnt lgkmcnt(3)\nds_read_b128 v[80:83], v1 offset:7168\nv_mfma_f32_16x16x32_bf16 v[24:27], a[108:111], v[60:63], v[24:27]\n" \
"ds_read_b128 v[56:59], v2 offset:24576\nv_mfma_f32_16x16x32_bf16 v[28:31], a[116:119], v[60:63], v[28:31]\nbuffer_load_dwordx4 v0, %[rs], s60 offen offset:3072 sc1 lds\nv_mfma_f32_16x16x32_bf16 v[32:35], a[124:127], v[60:63], v[32:35]\nv_mfma_f32_16x16x32_bf16 v[36:39], a[132:135], v[60:63], v[36:39]\ns_waitcnt lgkmcnt(4)\nds_read_b128 v[72:75], v1 offset:8192\nv_mfma_f32_16x16x32_bf16 v[24:27], a[136:139], v[64:67], v[24:27]\n" \
"s_waitcnt vmcnt(8)\ns_waitcnt lgkmcnt(4)\nds_read_b128 v[60:63], v2 offset:26624\nv_add_f32 v84, v40, v44\nv_mfma_f32_16x16x32_bf16 v[28:31], v[76:79], v[64:67], v[28:31]\nv_add_f32 v85, v48, v52\nv_add_f32 v84, v84, v85\nv_mfma_f32_16x16x32_bf16 v[32:35], a[144:147], v[64:67], v[32:35]\n" \
"v_mov_b32 v85, v84\ns_nop 0\nv_mfma_f32_16x16x32_bf16 v[36:39], a[152:155], v[64:67], v[36:39]\ns_waitcnt lgkmcnt(4)\nds_read_b128 v[76:79], v1 offset:9216\nv_mfma_f32_16x16x32_bf16 v[24:27], a[140:143], v[68:71], v[24:27]\ns_waitcnt lgkmcnt(4)\nds_read_b128 v[64:67], v2 offset:28672\n" \
"v_mfma_f32_16x16x32_bf16 v[28:31], v[80:83], v[68:71], v[28:31]\nv_permlane16_swap_b32 v84, v85\nv_mfma_f32_16x16x32_bf16 v[32:35], a[148:151], v[68:71], v[32:35]\nv_add_f32 v84, v84, v85\nv_mov_b32 v85, v84\ns_nop 0\nv_mfma_f32_16x16x32_bf16 v[36:39], a[156:159], v[68:71], v[36:39]\ns_waitcnt lgkmcnt(4)\n" \
"ds_read_b128 v[80:83], v1 offset:10240\nv_mfma_f32_16x16x32_bf16 v[24:27], a[160:163], v[56:59], v[24:27]\nds_read_b128 v[68:71], v2 offset:30720\nv_mfma_f32_16x16x32_bf16 v[28:31], a[168:171], v[56:59], v[28:31]\ns_waitcnt lgkmcnt(5)\nv_permlane32_swap_b32 v84, v85\nv_mfma_f32_16x16x32_bf16 v[32:35], v[72:75], v[56:59], v[32:35]\nv_add_f32 v84, v84, v85\n" \
"v_mul_f32 v84, 0xbd800000, v84\nv_mfma_f32_16x16x32_bf16 v[36:39], a[176:179], v[56:59], v[36:39]\ns_waitcnt lgkmcnt(4)\nds_read_b128 v[72:75], v1 offset:11264\nv_add_f32 v40, v40, v84\nv_mfma_f32_16x16x32_bf16 v[24:27], a[164:167], v[60:63], v[24:27]\ns_waitcnt vmcnt(4)\ns_barrier\n" \
"ds_read_b128 v[56:59], v2 offset:32768\nv_mfma_f32_16x16x32_bf16 v[28:31], a[172:175], v[60:63], v[28:31]\ns_waitcnt lgkmcnt(5)\nv_mul_f32 v40, 0x41000000, v40\nv_fma_f32 v41, v40, v40, v41\nv_mfma_f32_16x16x32_bf16 v[32:35], v[76:79], v[60:63], v[32:35]\nv_add_f32 v44, v44, v84\nv_mul_f32 v44, 0x41000000, v44\n" \
"v_mfma_f32_16x16x32_bf16 v[36:39], a[180:183], v[60:63], v[36:39]\ns_waitcnt lgkmcnt(4)\nv_fma_f32 v45, v44, v44, v45\nv_add_f32 v48, v48, v84\nv_mfma_f32_16x16x32_bf16 v[24:27], a[184:187], v[64:67], v[24:27]\nds_read_b128 v[60:63], v2 offset:34816\nv_mul_f32 v48, 0x41000000, v48\nv_mfma_f32_16x16x32_bf16 v[28:31], a[192:195], v[64:67], v[28:31]\n" \
"v_fma_f32 v49, v48, v48, v49\nv_add_f32 v52, v52, v84\nv_mfma_f32_16x16x32_bf16 v[32:35], a[200:203], v[64:67], v[32:35]\ns_waitcnt lgkmcnt(4)\nv_mul_f32 v52, 0x41000000, v52\nv_fma_f32 v53, v52, v52, v53\nv_mfma_f32_16x16x32_bf16 v[36:39], v[80:83], v[64:67], v[36:39]\ns_waitcnt lgkmcnt(3)\n" \
"v_add_f32 v85, v41, v45\nv_add_f32 v86, v49, v53\nv_mfma_f32_16x16x32_bf16 v[24:27], a[188:191], v[68:71], v[24:27]\nds_read_b128 v[64:67], v2 offset:36864\nv_add_f32 v85, v85, v86\nv_mfma_f32_16x16x32_bf16 v[28:31], a[196:199], v[68:71], v[28:31]\nv_mov_b32 v86, v85\ns_nop 0\n" \
"v_mfma_f32_16x16x32_bf16 v[32:35], a[204:207], v[68:71], v[32:35]\ns_waitcnt lgkmcnt(3)\nv_permlane16_swap_b32 v85, v86\nv_mfma_f32_16x16x32_bf16 v[36:39], v[72:75], v[68:71], v[36:39]\ns_waitcnt lgkmcnt(2)\nds_read_b128 v[72:75], v1 offset:12288\nv_add_f32 v85, v85, v86\nv_mfma_f32_16x16x32_bf16 v[24:27], a[208:211], v[56:59], v[24:27]\n" \
"ds_read_b128 v[68:71], v2 offset:38912\nv_mov_b32 v86, v85\ns_nop 0\nv_mfma_f32_16x16x32_bf16 v[28:31], a[216:219], v[56:59], v[28:31]\nv_permlane32_swap_b32 v85, v86\nv_mfma_f32_16x16x32_bf16 v[32:35], a[224:227], v[56:59], v[32:35]\nv_add_f32 v85, v85, v86\nv_mul_f32 v85, 0x3a800000, v85\n" \
"v_mfma_f32_16x16x32_bf16 v[36:39], a[232:235], v[56:59], v[36:39]\ns_waitcnt lgkmcnt(3)\nds_read_b128 v[76:79], v1 offset:13312\nv_cvt_pk_bf16_f32 v85, v85, v85\nv_mfma_f32_16x16x32_bf16 v[24:27], a[212:215], v[60:63], v[24:27]\nds_read_b128 v[56:59], v2 offset:40960\nv_and_b32 v85, 0xffff0000, v85\nv_mfma_f32_16x16x32_bf16 v[28:31], a[220:223], v[60:63], v[28:31]\n" \
"v_add_f32 v85, 0x3727c5ac, v85\nv_cvt_pk_bf16_f32 v85, v85, v85\nv_mfma_f32_16x16x32_bf16 v[32:35], a[228:231], v[60:63], v[32:35]\nv_and_b32 v85, 0xffff0000, v85\nv_mfma_f32_16x16x32_bf16 v[36:39], a[236:239], v[60:63], v[36:39]\ns_waitcnt lgkmcnt(4)\nds_read_b128 v[80:83], v1 offset:14336\nv_mfma_f32_16x16x32_bf16 v[24:27], a[240:243], v[64:67], v[24:27]\n" \
"s_waitcnt lgkmcnt(4)\nds_read_b128 v[60:63], v2 offset:43008\nv_mfma_f32_16x16x32_bf16 v[28:31], v[72:75], v[64:67], v[28:31]\nv_sqrt_f32 v85, v85\nv_mfma_f32_16x16x32_bf16 v[32:35], a[248:251], v[64:67], v[32:35]\nv_cvt_pk_bf16_f32 v84, v84, v84\nv_and_b32 v84, 0xffff0000, v84\nv_mfma_f32_16x16x32_bf16 v[36:39], v[96:99], v[64:67], v[36:39]\n" \
"s_waitcnt lgkmcnt(4)\nds_read_b128 v[72:75], v1 offset:15360\nv_cvt_pk_bf16_f32 v85, v85, v85\nv_mfma_f32_16x16x32_bf16 v[24:27], a[244:247], v[68:71], v[24:27]\ns_waitcnt lgkmcnt(4)\nds_read_b128 v[64:67], v2 offset:45056\nv_and_b32 v85, 0xffff0000, v85\nv_mfma_f32_16x16x32_bf16 v[28:31], v[76:79], v[68:71], v[28:31]\n" \
"v_mul_f32 v86, s58, v85\nv_mfma_f32_16x16x32_bf16 v[32:35], a[252:255], v[68:71], v[32:35]\nv_rcp_f32 v85, v85\ns_nop 0\nv_mfma_f32_16x16x32_bf16 v[36:39], v[100:103], v[68:71], v[36:39]\ns_waitcnt lgkmcnt(4)\nds_read_b128 v[76:79], v1 offset:16384\nv_mul_f32 v85, s58, v85\n" \
"v_mfma_f32_16x16x32_bf16 v[24:27], v[104:107], v[56:59], v[24:27]\nds_read_b128 v[68:71], v2 offset:47104\nv_add_f32 v87, v42, v46\nv_mfma_f32_16x16x32_bf16 v[28:31], v[112:115], v[56:59], v[28:31]\ns_waitcnt lgkmcnt(5)\nv_add_f32 v88, v50, v54\nv_add_f32 v87, v87, v88\nv_mfma_f32_16x16x32_bf16 v[32:35], v[80:83], v[56:59], v[32:35]\n" \
"v_mov_b32 v88, v87\ns_nop 0\nv_mfma_f32_16x16x32_bf16 v[36:39], v[120:123], v[56:59], v[36:39]\ns_waitcnt lgkmcnt(4)\nds_read_b128 v[80:83], v1 offset:17408\nv_mfma_f32_16x16x32_bf16 v[24:27], v[108:111], v[60:63], v[24:27]\ns_waitcnt vmcnt(0)\ns_barrier\n" \
"ds_read_b128 v[56:59], v2 offset:49152\nv_mfma_f32_16x16x32_bf16 v[28:31], v[116:119], v[60:63], v[28:31]\ns_waitcnt lgkmcnt(5)\nv_permlane16_swap_b32 v87, v88\nv_mfma_f32_16x16x32_bf16 v[32:35], v[72:75], v[60:63], v[32:35]\nv_add_f32 v87, v87, v88\nv_mov_b32 v88, v87\ns_nop 0\n" \
"v_mfma_f32_16x16x32_bf16 v[36:39], v[124:127], v[60:63], v[36:39]\ns_waitcnt lgkmcnt(4)\nv_permlane32_swap_b32 v87, v88\nv_mfma_f32_16x16x32_bf16 v[24:27], v[128:131], v[64:67], v[24:27]\nds_read_b128 v[60:63], v2 offset:51200\nv_add_f32 v87, v87, v88\nv_mfma_f32_16x16x32_bf16 v[28:31], v[136:139], v[64:67], v[28:31]\nv_mul_f32 v87, 0xbd800000, v87\n" \
"v_add_f32 v42, v42, v87\nv_mfma_f32_16x16x32_bf16 v[32:35], v[144:147], v[64:67], v[32:35]\ns_waitcnt lgkmcnt(4)\nv_mul_f32 v42, 0x41000000, v42\nv_fma_f32 v43, v42, v42, v43\nv_mfma_f32_16x16x32_bf16 v[36:39], v[76:79], v[64:67], v[36:39]\ns_waitcnt lgkmcnt(3)\nv_add_f32 v46, v46, v87\n" \
"v_mul_f32 v46, 0x41000000, v46\nv_mfma_f32_16x16x32_bf16 v[24:27], v[132:135], v[68:71], v[24:27]\nds_read_b128 v[64:67], v2 offset:53248\nv_fma_f32 v47, v46, v46, v47\nv_mfma_f32_16x16x32_bf16 v[28:31], v[140:143], v[68:71], v[28:31]\nv_add_f32 v50, v50, v87\nv_mul_f32 v50, 0x41000000, v50\nv_mfma_f32_16x16x32_bf16 v[32:35], v[148:151], v[68:71], v[32:35]\n" \
"s_waitcnt lgkmcnt(3)\nv_fma_f32 v51, v50, v50, v51\nv_add_f32 v54, v54, v87\nv_mfma_f32_16x16x32_bf16 v[36:39], v[80:83], v[68:71], v[36:39]\ns_waitcnt lgkmcnt(2)\nds_read_b128 v[80:83], v1 offset:18432\nv_mul_f32 v54, 0x41000000, v54\nv_mfma_f32_16x16x32_bf16 v[24:27], v[152:155], v[56:59], v[24:27]\n" \
"ds_read_b128 v[68:71], v2 offset:55296\nv_fma_f32 v55, v54, v54, v55\nv_mfma_f32_16x16x32_bf16 v[28:31], v[160:163], v[56:59], v[28:31]\nv_add_f32 v88, v43, v47\nv_add_f32 v89, v51, v55\nv_mfma_f32_16x16x32_bf16 v[32:35], v[168:171], v[56:59], v[32:35]\nv_add_f32 v88, v88, v89\nv_mov_b32 v89, v88\n" \
"s_nop 0\nv_mfma_f32_16x16x32_bf16 v[36:39], v[176:179], v[56:59], v[36:39]\ns_waitcnt lgkmcnt(3)\nds_read_b128 v[72:75], v1 offset:19456\nv_mfma_f32_16x16x32_bf16 v[24:27], v[156:159], v[60:63], v[24:27]\nds_read_b128 v[56:59], v2 offset:57344\nv_mfma_f32_16x16x32_bf16 v[28:31], v[164:167], v[60:63], v[28:31]\nv_permlane16_swap_b32 v88, v89\n" \
"v_mfma_f32_16x16x32_bf16 v[32:35], v[172:175], v[60:63], v[32:35]\nv_add_f32 v88, v88, v89\nv_mov_b32 v89, v88\ns_nop 0\nv_mfma_f32_16x16x32_bf16 v[36:39], v[180:183], v[60:63], v[36:39]\ns_waitcnt lgkmcnt(4)\nds_read_b128 v[76:79], v1 offset:20480\nv_mfma_f32_16x16x32_bf16 v[24:27], v[184:187], v[64:67], v[24:27]\n" \
"s_waitcnt lgkmcnt(4)\nds_read_b128 v[60:63], v2 offset:59392\nv_mfma_f32_16x16x32_bf16 v[28:31], v[80:83], v[64:67], v[28:31]\nv_permlane32_swap_b32 v88, v89\nv_mfma_f32_16x16x32_bf16 v[32:35], v[192:195], v[64:67], v[32:35]\nv_add_f32 v88, v88, v89\nv_mul_f32 v88, 0x3a800000, v88\nv_mfma_f32_16x16x32_bf16 v[36:39], v[200:203], v[64:67], v[36:39]\n" \
"s_waitcnt lgkmcnt(4)\nds_read_b128 v[80:83], v1 offset:21504\nv_cvt_pk_bf16_f32 v88, v88, v88\nv_mfma_f32_16x16x32_bf16 v[24:27], v[188:191], v[68:71], v[24:27]\ns_waitcnt lgkmcnt(4)\nds_read_b128 v[64:67], v2 offset:61440\nv_and_b32 v88, 0xffff0000, v88\nv_mfma_f32_16x16x32_bf16 v[28:31], v[72:75], v[68:71], v[28:31]\n" \
"v_add_f32 v88, 0x3727c5ac, v88\nv_cvt_pk_bf16_f32 v88, v88, v88\nv_mfma_f32_16x16x32_bf16 v[32:35], v[196:199], v[68:71], v[32:35]\nv_and_b32 v88, 0xffff0000, v88\nv_mfma_f32_16x16x32_bf16 v[36:39], v[204:207], v[68:71], v[36:39]\ns_waitcnt lgkmcnt(4)\nds_read_b128 v[72:75], v1 offset:22528\nv_mfma_f32_16x16x32_bf16 v[24:27], v[208:211], v[56:59], v[24:27]\n" \
"ds_read_b128 v[68:71], v2 offset:63488\nv_mfma_f32_16x16x32_bf16 v[28:31], v[216:219], v[56:59], v[28:31]\ns_waitcnt lgkmcnt(5)\nv_sqrt_f32 v88, v88\nv_mfma_f32_16x16x32_bf16 v[32:35], v[76:79], v[56:59], v[32:35]\nv_cvt_pk_bf16_f32 v87, v87, v87\nv_and_b32 v87, 0xffff0000, v87\nv_mfma_f32_16x16x32_bf16 v[36:39], v[224:227], v[56:59], v[36:39]\n" \
"s_waitcnt lgkmcnt(4)\nds_read_b128 v[76:79], v1 offset:23552\nv_cvt_pk_bf16_f32 v88, v88, v88\nv_mfma_f32_16x16x32_bf16 v[24:27], v[212:215], v[60:63], v[24:27]\nds_read_b128 v[56:59], v2 offset:256\nv_and_b32 v88, 0xffff0000, v88\nv_mfma_f32_16x16x32_bf16 v[28:31], v[220:223], v[60:63], v[28:31]\ns_waitcnt lgkmcnt(5)\n" \
"v_mul_f32 v89, s58, v88\nv_mfma_f32_16x16x32_bf16 v[32:35], v[80:83], v[60:63], v[32:35]\nv_rcp_f32 v88, v88\ns_nop 0\nv_mfma_f32_16x16x32_bf16 v[36:39], v[228:231], v[60:63], v[36:39]\ns_waitcnt lgkmcnt(4)\nv_mul_f32 v88, s58, v88\nv_mfma_f32_16x16x32_bf16 v[24:27], v[232:235], v[64:67], v[24:27]\n" \
"ds_read_b128 v[60:63], v2 offset:2304\nv_mfma_f32_16x16x32_bf16 v[28:31], v[240:243], v[64:67], v[28:31]\nv_mfma_f32_16x16x32_bf16 v[32:35], v[248:251], v[64:67], v[32:35]\ns_waitcnt lgkmcnt(4)\nv_mfma_f32_16x16x32_bf16 v[36:39], v[72:75], v[64:67], v[36:39]\ns_waitcnt lgkmcnt(3)\nv_mfma_f32_16x16x32_bf16 v[24:27], v[236:239], v[68:71], v[24:27]\nds_read_b128 v[64:67], v2 offset:4352\n" \
"v_mfma_f32_16x16x32_bf16 v[28:31], v[244:247], v[68:71], v[28:31]\nv_mfma_f32_16x16x32_bf16 v[32:35], v[252:255], v[68:71], v[32:35]\ns_waitcnt lgkmcnt(3)\nv_mfma_f32_16x16x32_bf16 v[36:39], v[76:79], v[68:71], v[36:39]\ns_waitcnt lgkmcnt(2)\nds_read_b128 v[76:79], v1 offset:0\nv_mfma_f32_16x16x32_bf16 v[40:43], a[0:3], v[56:59], 0\nds_read_b128 v[68:71], v2 offset:6400\n" \
"v_mfma_f32_16x16x32_bf16 v[44:47], a[8:11], v[56:59], 0\ns_lshl_b32 s44, s40, 1\nv_mov_b32 v90, s44\nv_mfma_f32_16x16x32_bf16 v[48:51], a[16:19], v[56:59], 0\nv_mfma_f32_16x16x32_bf16 v[52:55], a[24:27], v[56:59], 0\ns_waitcnt lgkmcnt(3)\nds_read_b128 v[80:83], v1 offset:1024\nv_fma_f32 v24, v8, v84, v24\n" \
"v_mfma_f32_16x16x32_bf16 v[40:43], a[4:7], v[60:63], v[40:43]\nds_read_b128 v[56:59], v2 offset:8448\nv_fma_f32 v25, v9, v84, v25\nv_mfma_f32_16x16x32_bf16 v[44:47], a[12:15], v[60:63], v[44:47]\nv_fma_f32 v32, v16, v84, v32\nv_fma_f32 v33, v17, v84, v33\nv_mfma_f32_16x16x32_bf16 v[48:51], a[20:23], v[60:63], v[48:51]\nv_mul_f32 v24, v24, v85\n" \
"v_mul_f32 v25, v25, v85\nv_mfma_f32_16x16x32_bf16 v[52:55], a[28:31], v[60:63], v[52:55]\ns_waitcnt lgkmcnt(4)\nds_read_b128 v[72:75], v1 offset:2048\nv_fma_f32 v91, v24, v24, s59\nv_mfma_f32_16x16x32_bf16 v[40:43], a[32:35], v[64:67], v[40:43]\ns_waitcnt lgkmcnt(4)\nds_read_b128 v[60:63], v2 offset:10496\n" \
"v_fma_f32 v92, v25, v25, s59\nv_mfma_f32_16x16x32_bf16 v[44:47], v[76:79], v[64:67], v[44:47]\nv_mul_f32 v91, v91, v24\nv_mul_f32 v92, v92, v25\nv_mfma_f32_16x16x32_bf16 v[48:51], a[40:43], v[64:67], v[48:51]\nv_exp_f32 v91, v91\nv_mfma_f32_16x16x32_bf16 v[52:55], a[48:51], v[64:67], v[52:55]\ns_waitcnt lgkmcnt(4)\n" \
"ds_read_b128 v[76:79], v1 offset:3072\nv_mfma_f32_16x16x32_bf16 v[40:43], a[36:39], v[68:71], v[40:43]\ns_waitcnt lgkmcnt(4)\nds_read_b128 v[64:67], v2 offset:12544\nv_mfma_f32_16x16x32_bf16 v[44:47], v[80:83], v[68:71], v[44:47]\nv_exp_f32 v92, v92\nv_mfma_f32_16x16x32_bf16 v[48:51], a[44:47], v[68:71], v[48:51]\nv_mul_f32 v24, v24, v32\n" \
"v_mul_f32 v25, v25, v33\nv_mfma_f32_16x16x32_bf16 v[52:55], a[52:55], v[68:71], v[52:55]\ns_waitcnt lgkmcnt(4)\nds_read_b128 v[80:83], v1 offset:4096\nv_fma_f32 v91, v91, v86, v86\nv_mfma_f32_16x16x32_bf16 v[40:43], a[56:59], v[56:59], v[40:43]\nds_read_b128 v[68:71], v2 offset:14592\nv_fma_f32 v92, v92, v86, v86\n" \
"v_mfma_f32_16x16x32_bf16 v[44:47], a[64:67], v[56:59], v[44:47]\ns_waitcnt lgkmcnt(5)\nv_rcp_f32 v91, v91\nv_mfma_f32_16x16x32_bf16 v[48:51], v[72:75], v[56:59], v[48:51]\nv_rcp_f32 v92, v92\nv_mfma_f32_16x16x32_bf16 v[52:55], a[72:75], v[56:59], v[52:55]\ns_waitcnt lgkmcnt(4)\nds_read_b128 v[72:75], v1 offset:5120\n" \
"v_mul_f32 v24, v24, v91\nv_mfma_f32_16x16x32_bf16 v[40:43], a[60:63], v[60:63], v[40:43]\nds_read_b128 v[56:59], v2 offset:16640\nv_mul_f32 v25, v25, v92\nv_mfma_f32_16x16x32_bf16 v[44:47], a[68:71], v[60:63], v[44:47]\ns_waitcnt lgkmcnt(5)\nv_cvt_pk_bf16_f32 v32, v24, v25\nv_fma_f32 v26, v10, v84, v26\n" \
"v_mfma_f32_16x16x32_bf16 v[48:51], v[76:79], v[60:63], v[48:51]\nv_fma_f32 v27, v11, v84, v27\nv_fma_f32 v34, v18, v84, v34\nv_mfma_f32_16x16x32_bf16 v[52:55], a[76:79], v[60:63], v[52:55]\ns_waitcnt lgkmcnt(4)\nv_fma_f32 v35, v19, v84, v35\nv_mul_f32 v26, v26, v85\nv_mfma_f32_16x16x32_bf16 v[40:43], a[80:83], v[64:67], v[40:43]\n" \
"ds_read_b128 v[60:63], v2 offset:18688\nv_mul_f32 v27, v27, v85\nv_mfma_f32_16x16x32_bf16 v[44:47], a[88:91], v[64:67], v[44:47]\nv_fma_f32 v91, v26, v26, s59\nv_fma_f32 v92, v27, v27, s59\nv_mfma_f32_16x16x32_bf16 v[48:51], a[96:99], v[64:67], v[48:51]\ns_waitcnt lgkmcnt(4)\nv_mul_f32 v91, v91, v26\n" \
"v_mul_f32 v92, v92, v27\nv_mfma_f32_16x16x32_bf16 v[52:55], v[80:83], v[64:67], v[52:55]\ns_waitcnt lgkmcnt(3)\nv_exp_f32 v91, v91\nv_mfma_f32_16x16x32_bf16 v[40:43], a[84:87], v[68:71], v[40:43]\nds_read_b128 v[64:67], v2 offset:20736\nv_mfma_f32_16x16x32_bf16 v[44:47], a[92:95], v[68:71], v[44:47]\nv_exp_f32 v92, v92\n" \
"v_mfma_f32_16x16x32_bf16 v[48:51], a[100:103], v[68:71], v[48:51]\ns_waitcnt lgkmcnt(3)\nv_mul_f32 v26, v26, v34\nv_mul_f32 v27, v27, v35\nv_mfma_f32_16x16x32_bf16 v[52:55], v[72:75], v[68:71], v[52:55]\ns_waitcnt lgkmcnt(2)\nds_read_b128 v[72:75], v1 offset:6144\nv_fma_f32 v91, v91, v86, v86\n" \
"v_mfma_f32_16x16x32_bf16 v[40:43], a[104:107], v[56:59], v[40:43]\nds_read_b128 v[68:71], v2 offset:22784\nv_fma_f32 v92, v92, v86, v86\nv_mfma_f32_16x16x32_bf16 v[44:47], a[112:115], v[56:59], v[44:47]\nv_rcp_f32 v91, v91\nv_mfma_f32_16x16x32_bf16 v[48:51], a[120:123], v[56:59], v[48:51]\nv_rcp_f32 v92, v92\nv_mfma_f32_16x16x32_bf16 v[52:55], a[128:131], v[56:59], v[52:55]\n" \
"s_waitcnt lgkmcnt(3)\nds_read_b128 v[76:79], v1 offset:7168\nv_mul_f32 v26, v26, v91\nv_mfma_f32_16x16x32_bf16 v[40:43], a[108:111], v[60:63], v[40:43]\nds_read_b128 v[56:59], v2 offset:24832\nv_mul_f32 v27, v27, v92\nv_mfma_f32_16x16x32_bf16 v[44:47], a[116:119], v[60:63], v[44:47]\nv_cvt_pk_bf16_f32 v33, v26, v27\n" \
"v_fma_f32 v28, v12, v84, v28\nv_mfma_f32_16x16x32_bf16 v[48:51], a[124:127], v[60:63], v[48:51]\nv_fma_f32 v29, v13, v84, v29\nv_fma_f32 v36, v20, v84, v36\nv_mfma_f32_16x16x32_bf16 v[52:55], a[132:135], v[60:63], v[52:55]\ns_waitcnt lgkmcnt(4)\nds_read_b128 v[80:83], v1 offset:8192\nv_fma_f32 v37, v21, v84, v37\n" \
"v_mfma_f32_16x16x32_bf16 v[40:43], a[136:139], v[64:67], v[40:43]\ns_waitcnt lgkmcnt(4)\nds_read_b128 v[60:63], v2 offset:26880\nv_mul_f32 v28, v28, v85\nv_mfma_f32_16x16x32_bf16 v[44:47], v[72:75], v[64:67], v[44:47]\nv_mul_f32 v29, v29, v85\nv_fma_f32 v91, v28, v28, s59\nv_mfma_f32_16x16x32_bf16 v[48:51], a[144:147], v[64:67], v[48:51]\n" \
"v_fma_f32 v92, v29, v29, s59\nv_mul_f32 v91, v91, v28\nv_mfma_f32_16x16x32_bf16 v[52:55], a[152:155], v[64:67], v[52:55]\ns_waitcnt lgkmcnt(4)\nds_read_b128 v[72:75], v1 offset:9216\nv_mul_f32 v92, v92, v29\nv_mfma_f32_16x16x32_bf16 v[40:43], a[140:143], v[68:71], v[40:43]\ns_waitcnt lgkmcnt(4)\n" \
"ds_read_b128 v[64:67], v2 offset:28928\nv_mfma_f32_16x16x32_bf16 v[44:47], v[76:79], v[68:71], v[44:47]\nv_exp_f32 v91, v91\nv_mfma_f32_16x16x32_bf16 v[48:51], a[148:151], v[68:71], v[48:51]\nv_exp_f32 v92, v92\nv_mfma_f32_16x16x32_bf16 v[52:55], a[156:159], v[68:71], v[52:55]\ns_waitcnt lgkmcnt(4)\nds_read_b128 v[76:79], v1 offset:10240\n" \
"v_mul_f32 v28, v28, v36\nv_mfma_f32_16x16x32_bf16 v[40:43], a[160:163], v[56:59], v[40:43]\nds_read_b128 v[68:71], v2 offset:30976\nv_mul_f32 v29, v29, v37\nv_mfma_f32_16x16x32_bf16 v[44:47], a[168:171], v[56:59], v[44:47]\ns_waitcnt lgkmcnt(5)\nv_fma_f32 v91, v91, v86, v86\nv_fma_f32 v92, v92, v86, v86\n" \
"v_mfma_f32_16x16x32_bf16 v[48:51], v[80:83], v[56:59], v[48:51]\nv_rcp_f32 v91, v91\nv_mfma_f32_16x16x32_bf16 v[52:55], a[176:179], v[56:59], v[52:55]\ns_waitcnt lgkmcnt(4)\nds_read_b128 v[80:83], v1 offset:11264\nv_mfma_f32_16x16x32_bf16 v[40:43], a[164:167], v[60:63], v[40:43]\nds_read_b128 v[56:59], v2 offset:33024\nv_mfma_f32_16x16x32_bf16 v[44:47], a[172:175], v[60:63], v[44:47]\n" \
"s_waitcnt lgkmcnt(5)\nv_rcp_f32 v92, v92\nv_mfma_f32_16x16x32_bf16 v[48:51], v[72:75], v[60:63], v[48:51]\nv_mul_f32 v28, v28, v91\nv_mul_f32 v29, v29, v92\nv_mfma_f32_16x16x32_bf16 v[52:55], a[180:183], v[60:63], v[52:55]\ns_waitcnt lgkmcnt(4)\nv_cvt_pk_bf16_f32 v34, v28, v29\n" \
"v_fma_f32 v30, v14, v84, v30\nv_mfma_f32_16x16x32_bf16 v[40:43], a[184:187], v[64:67], v[40:43]\nds_read_b128 v[60:63], v2 offset:35072\nv_fma_f32 v31, v15, v84, v31\nv_mfma_f32_16x16x32_bf16 v[44:47], a[192:195], v[64:67], v[44:47]\nv_fma_f32 v38, v22, v84, v38\nv_fma_f32 v39, v23, v84, v39\nv_mfma_f32_16x16x32_bf16 v[48:51], a[200:203], v[64:67], v[48:51]\n" \
"s_waitcnt lgkmcnt(4)\nv_mul_f32 v30, v30, v85\nv_mul_f32 v31, v31, v85\nv_mfma_f32_16x16x32_bf16 v[52:55], v[76:79], v[64:67], v[52:55]\ns_waitcnt lgkmcnt(3)\nv_fma_f32 v91, v30, v30, s59\nv_fma_f32 v92, v31, v31, s59\nv_mfma_f32_16x16x32_bf16 v[40:43], a[188:191], v[68:71], v[40:43]\n" \
"ds_read_b128 v[64:67], v2 offset:37120\nv_mul_f32 v91, v91, v30\nv_mfma_f32_16x16x32_bf16 v[44:47], a[196:199], v[68:71], v[44:47]\nv_mul_f32 v92, v92, v31\nv_mfma_f32_16x16x32_bf16 v[48:51], a[204:207], v[68:71], v[48:51]\ns_waitcnt lgkmcnt(3)\nv_exp_f32 v91, v91\nv_mfma_f32_16x16x32_bf16 v[52:55], v[80:83], v[68:71], v[52:55]\n" \
"s_waitcnt lgkmcnt(2)\nds_read_b128 v[80:83], v1 offset:12288\nv_mfma_f32_16x16x32_bf16 v[40:43], a[208:211], v[56:59], v[40:43]\nds_read_b128 v[68:71], v2 offset:39168\nv_mfma_f32_16x16x32_bf16 v[44:47], a[216:219], v[56:59], v[44:47]\nv_exp_f32 v92, v92\nv_mfma_f32_16x16x32_bf16 v[48:51], a[224:227], v[56:59], v[48:51]\nv_mul_f32 v30, v30, v38\n" \
"v_mul_f32 v31, v31, v39\nv_mfma_f32_16x16x32_bf16 v[52:55], a[232:235], v[56:59], v[52:55]\ns_waitcnt lgkmcnt(3)\nds_read_b128 v[72:75], v1 offset:13312\nv_fma_f32 v91, v91, v86, v86\nv_mfma_f32_16x16x32_bf16 v[40:43], a[212:215], v[60:63], v[40:43]\nds_read_b128 v[56:59], v2 offset:41216\nv_fma_f32 v92, v92, v86, v86\n" \
"v_mfma_f32_16x16x32_bf16 v[44:47], a[220:223], v[60:63], v[44:47]\nv_rcp_f32 v91, v91\nv_mfma_f32_16x16x32_bf16 v[48:51], a[228:231], v[60:63], v[48:51]\nv_rcp_f32 v92, v92\nv_mfma_f32_16x16x32_bf16 v[52:55], a[236:239], v[60:63], v[52:55]\ns_waitcnt lgkmcnt(4)\nds_read_b128 v[76:79], v1 offset:14336\nv_mul_f32 v30, v30, v91\n" \
"v_mfma_f32_16x16x32_bf16 v[40:43], a[240:243], v[64:67], v[40:43]\ns_waitcnt lgkmcnt(4)\nds_read_b128 v[60:63], v2 offset:43264\nv_mul_f32 v31, v31, v92\nv_mfma_f32_16x16x32_bf16 v[44:47], v[80:83], v[64:67], v[44:47]\nv_cvt_pk_bf16_f32 v35, v30, v31\nv_mfma_f32_16x16x32_bf16 v[48:51], a[248:251], v[64:67], v[48:51]\nbuffer_store_dwordx4 v[32:35], v4, %[rs], s55 offen offset:0 sc1\n" \
"v_mfma_f32_16x16x32_bf16 v[52:55], v[96:99], v[64:67], v[52:55]\ns_waitcnt lgkmcnt(4)\nds_read_b128 v[80:83], v1 offset:15360\nv_mfma_f32_16x16x32_bf16 v[40:43], a[244:247], v[68:71], v[40:43]\ns_waitcnt lgkmcnt(4)\nds_read_b128 v[64:67], v2 offset:45312\nv_mfma_f32_16x16x32_bf16 v[44:47], v[72:75], v[68:71], v[44:47]\nv_mfma_f32_16x16x32_bf16 v[48:51], a[252:255], v[68:71], v[48:51]\n" \
"v_mfma_f32_16x16x32_bf16 v[52:55], v[100:103], v[68:71], v[52:55]\ns_waitcnt lgkmcnt(4)\nds_read_b128 v[72:75], v1 offset:16384\nv_mfma_f32_16x16x32_bf16 v[40:43], v[104:107], v[56:59], v[40:43]\nds_read_b128 v[68:71], v2 offset:47360\nv_mfma_f32_16x16x32_bf16 v[44:47], v[112:115], v[56:59], v[44:47]\ns_waitcnt lgkmcnt(5)\nv_mfma_f32_16x16x32_bf16 v[48:51], v[76:79], v[56:59], v[48:51]\n" \
"v_mfma_f32_16x16x32_bf16 v[52:55], v[120:123], v[56:59], v[52:55]\ns_waitcnt lgkmcnt(4)\nds_read_b128 v[76:79], v1 offset:17408\nv_mfma_f32_16x16x32_bf16 v[40:43], v[108:111], v[60:63], v[40:43]\nds_read_b128 v[56:59], v2 offset:49408\nv_mfma_f32_16x16x32_bf16 v[44:47], v[116:119], v[60:63], v[44:47]\ns_waitcnt lgkmcnt(5)\nv_mfma_f32_16x16x32_bf16 v[48:51], v[80:83], v[60:63], v[48:51]\n" \
"v_mfma_f32_16x16x32_bf16 v[52:55], v[124:127], v[60:63], v[52:55]\ns_waitcnt lgkmcnt(4)\nv_mfma_f32_16x16x32_bf16 v[40:43], v[128:131], v[64:67], v[40:43]\nds_read_b128 v[60:63], v2 offset:51456\nv_mfma_f32_16x16x32_bf16 v[44:47], v[136:139], v[64:67], v[44:47]\nv_mfma_f32_16x16x32_bf16 v[48:51], v[144:147], v[64:67], v[48:51]\ns_waitcnt lgkmcnt(4)\nv_mfma_f32_16x16x32_bf16 v[52:55], v[72:75], v[64:67], v[52:55]\n" \
"s_waitcnt lgkmcnt(3)\nv_mfma_f32_16x16x32_bf16 v[40:43], v[132:135], v[68:71], v[40:43]\nds_read_b128 v[64:67], v2 offset:53504\nv_mfma_f32_16x16x32_bf16 v[44:47], v[140:143], v[68:71], v[44:47]\nv_mfma_f32_16x16x32_bf16 v[48:51], v[148:151], v[68:71], v[48:51]\ns_waitcnt lgkmcnt(3)\nv_mfma_f32_16x16x32_bf16 v[52:55], v[76:79], v[68:71], v[52:55]\ns_waitcnt lgkmcnt(2)\n" \
"ds_read_b128 v[76:79], v1 offset:18432\nv_mfma_f32_16x16x32_bf16 v[40:43], v[152:155], v[56:59], v[40:43]\nds_read_b128 v[68:71], v2 offset:55552\nv_mfma_f32_16x16x32_bf16 v[44:47], v[160:163], v[56:59], v[44:47]\nv_mfma_f32_16x16x32_bf16 v[48:51], v[168:171], v[56:59], v[48:51]\nv_mfma_f32_16x16x32_bf16 v[52:55], v[176:179], v[56:59], v[52:55]\ns_waitcnt lgkmcnt(3)\nds_read_b128 v[80:83], v1 offset:19456\n" \
"v_mfma_f32_16x16x32_bf16 v[40:43], v[156:159], v[60:63], v[40:43]\nds_read_b128 v[56:59], v2 offset:57600\nv_mfma_f32_16x16x32_bf16 v[44:47], v[164:167], v[60:63], v[44:47]\nv_mfma_f32_16x16x32_bf16 v[48:51], v[172:175], v[60:63], v[48:51]\nv_mfma_f32_16x16x32_bf16 v[52:55], v[180:183], v[60:63], v[52:55]\ns_waitcnt lgkmcnt(4)\nds_read_b128 v[72:75], v1 offset:20480\nv_mfma_f32_16x16x32_bf16 v[40:43], v[184:187], v[64:67], v[40:43]\n" \
"s_waitcnt lgkmcnt(4)\nds_read_b128 v[60:63], v2 offset:59648\nv_mfma_f32_16x16x32_bf16 v[44:47], v[76:79], v[64:67], v[44:47]\ns_waitcnt vmcnt(0)\nbuffer_store_dword v90, off, %[rs], %[fh] sc1\nv_mfma_f32_16x16x32_bf16 v[48:51], v[192:195], v[64:67], v[48:51]\nv_mfma_f32_16x16x32_bf16 v[52:55], v[200:203], v[64:67], v[52:55]\ns_waitcnt lgkmcnt(4)\n" \
"ds_read_b128 v[76:79], v1 offset:21504\nv_mfma_f32_16x16x32_bf16 v[40:43], v[188:191], v[68:71], v[40:43]\ns_waitcnt lgkmcnt(4)\nds_read_b128 v[64:67], v2 offset:61696\nv_mfma_f32_16x16x32_bf16 v[44:47], v[80:83], v[68:71], v[44:47]\nv_mfma_f32_16x16x32_bf16 v[48:51], v[196:199], v[68:71], v[48:51]\nv_mfma_f32_16x16x32_bf16 v[52:55], v[204:207], v[68:71], v[52:55]\ns_waitcnt lgkmcnt(4)\n" \
"ds_read_b128 v[80:83], v1 offset:22528\nv_mfma_f32_16x16x32_bf16 v[40:43], v[208:211], v[56:59], v[40:43]\nds_read_b128 v[68:71], v2 offset:63744\nv_mfma_f32_16x16x32_bf16 v[44:47], v[216:219], v[56:59], v[44:47]\ns_waitcnt lgkmcnt(5)\nv_mfma_f32_16x16x32_bf16 v[48:51], v[72:75], v[56:59], v[48:51]\nv_mfma_f32_16x16x32_bf16 v[52:55], v[224:227], v[56:59], v[52:55]\ns_waitcnt lgkmcnt(4)\n" \
"ds_read_b128 v[72:75], v1 offset:23552\nv_mfma_f32_16x16x32_bf16 v[40:43], v[212:215], v[60:63], v[40:43]\nv_mfma_f32_16x16x32_bf16 v[44:47], v[220:223], v[60:63], v[44:47]\ns_waitcnt lgkmcnt(4)\nv_mfma_f32_16x16x32_bf16 v[48:51], v[76:79], v[60:63], v[48:51]\nv_mfma_f32_16x16x32_bf16 v[52:55], v[228:231], v[60:63], v[52:55]\ns_waitcnt lgkmcnt(3)\nv_mfma_f32_16x16x32_bf16 v[40:43], v[232:235], v[64:67], v[40:43]\n" \
"v_mfma_f32_16x16x32_bf16 v[44:47], v[240:243], v[64:67], v[44:47]\nv_mfma_f32_16x16x32_bf16 v[48:51], v[248:251], v[64:67], v[48:51]\ns_waitcnt lgkmcnt(2)\nv_mfma_f32_16x16x32_bf16 v[52:55], v[80:83], v[64:67], v[52:55]\ns_waitcnt lgkmcnt(1)\nv_mfma_f32_16x16x32_bf16 v[40:43], v[236:239], v[68:71], v[40:43]\nv_mfma_f32_16x16x32_bf16 v[44:47], v[244:247], v[68:71], v[44:47]\nv_mfma_f32_16x16x32_bf16 v[48:51], v[252:255], v[68:71], v[48:51]\n" \
"s_waitcnt lgkmcnt(0)\nv_mfma_f32_16x16x32_bf16 v[52:55], v[72:75], v[68:71], v[52:55]\nv_fma_f32 v40, v8, v87, v40\nv_fma_f32 v41, v9, v87, v41\ns_nop 1\nv_fma_f32 v48, v16, v87, v48\nv_fma_f32 v49, v17, v87, v49\nv_mul_f32 v40, v40, v88\n" \
"v_mul_f32 v41, v41, v88\nv_fma_f32 v91, v40, v40, s59\nv_fma_f32 v92, v41, v41, s59\nv_mul_f32 v91, v91, v40\nv_mul_f32 v92, v92, v41\nv_exp_f32 v91, v91\nv_exp_f32 v92, v92\nv_mul_f32 v40, v40, v48\n" \
"v_mul_f32 v41, v41, v49\nv_fma_f32 v91, v91, v89, v89\nv_fma_f32 v92, v92, v89, v89\nv_rcp_f32 v91, v91\nv_rcp_f32 v92, v92\nv_mul_f32 v40, v40, v91\nv_mul_f32 v41, v41, v92\nv_cvt_pk_bf16_f32 v48, v40, v41\n" \
"v_fma_f32 v42, v10, v87, v42\nv_fma_f32 v43, v11, v87, v43\nv_fma_f32 v50, v18, v87, v50\nv_fma_f32 v51, v19, v87, v51\nv_mul_f32 v42, v42, v88\nv_mul_f32 v43, v43, v88\nv_fma_f32 v91, v42, v42, s59\nv_fma_f32 v92, v43, v43, s59\n" \
"v_mul_f32 v91, v91, v42\nv_mul_f32 v92, v92, v43\nv_exp_f32 v91, v91\nv_exp_f32 v92, v92\nv_mul_f32 v42, v42, v50\nv_mul_f32 v43, v43, v51\nv_fma_f32 v91, v91, v89, v89\nv_fma_f32 v92, v92, v89, v89\n" \
"v_rcp_f32 v91, v91\nv_rcp_f32 v92, v92\nv_mul_f32 v42, v42, v91\nv_mul_f32 v43, v43, v92\nv_cvt_pk_bf16_f32 v49, v42, v43\nv_fma_f32 v44, v12, v87, v44\nv_fma_f32 v45, v13, v87, v45\nv_fma_f32 v52, v20, v87, v52\n" \
"v_fma_f32 v53, v21, v87, v53\nv_mul_f32 v44, v44, v88\nv_mul_f32 v45, v45, v88\nv_fma_f32 v91, v44, v44, s59\nv_fma_f32 v92, v45, v45, s59\nv_mul_f32 v91, v91, v44\nv_mul_f32 v92, v92, v45\nv_exp_f32 v91, v91\n" \
"v_exp_f32 v92, v92\nv_mul_f32 v44, v44, v52\nv_mul_f32 v45, v45, v53\nv_fma_f32 v91, v91, v89, v89\nv_fma_f32 v92, v92, v89, v89\nv_rcp_f32 v91, v91\nv_rcp_f32 v92, v92\nv_mul_f32 v44, v44, v91\n" \
"v_mul_f32 v45, v45, v92\nv_cvt_pk_bf16_f32 v50, v44, v45\nv_fma_f32 v46, v14, v87, v46\nv_fma_f32 v47, v15, v87, v47\nv_fma_f32 v54, v22, v87, v54\nv_fma_f32 v55, v23, v87, v55\nv_mul_f32 v46, v46, v88\nv_mul_f32 v47, v47, v88\n" \
"v_fma_f32 v91, v46, v46, s59\nv_fma_f32 v92, v47, v47, s59\nv_mul_f32 v91, v91, v46\nv_mul_f32 v92, v92, v47\nv_exp_f32 v91, v91\nv_exp_f32 v92, v92\nv_mul_f32 v46, v46, v54\nv_mul_f32 v47, v47, v55\n" \
"v_fma_f32 v91, v91, v89, v89\nv_fma_f32 v92, v92, v89, v89\nv_rcp_f32 v91, v91\nv_rcp_f32 v92, v92\nv_mul_f32 v46, v46, v91\nv_mul_f32 v47, v47, v92\nv_cvt_pk_bf16_f32 v51, v46, v47\nbuffer_store_dwordx4 v[48:51], v4, %[rs], s55 offen offset:1024 sc1\n" \
"v_add_u32 v90, 1, v90\ns_mov_b32 s56, %[fh]\ns_add_u32 s40, s40, 1\ns_cmp_lt_u32 s40, 64\ns_cbranch_scc1 .Lgg_next%=\ns_waitcnt vmcnt(0)\nbuffer_store_dword v90, off, %[rs], s56 sc1\ns_branch .Lgg_end%=\n" \
".Lgg_abort%=:\ns_mov_b64 exec, -1\nv_mov_b32 v16, 0\ns_movk_i32 s44, 6400\nbuffer_store_dword v16, off, %[rs], s44 sc1\n.Lgg_end%=:\ns_waitcnt vmcnt(0) lgkmcnt(0)\n" \
""
#define DD_ASM \
"v_mbcnt_lo_u32_b32 v255, -1, 0\nv_mbcnt_hi_u32_b32 v255, -1, v255\nv_lshlrev_b32 v255, 2, v255\nv_add_u32 v255, %[tb], v255\nds_read_b32 v0, v255 offset:0\nds_read_b32 v1, v255 offset:256\nds_read_b32 v2, v255 offset:512\nds_read_b32 v3, v255 offset:768\n" \
"ds_read_b32 v4, v255 offset:1024\nds_read_b32 v5, v255 offset:1280\nds_read_b32 v6, v255 offset:1536\nds_read_b32 v7, v255 offset:1792\nds_read_b32 v125, v255 offset:2048\nds_read_b32 v126, v255 offset:2304\nds_read_b32 v127, v255 offset:2560\ns_waitcnt lgkmcnt(0)\n" \
"s_add_u32 m0, %[tb], 8192\ns_add_u32 s44, %[wo], 0\nbuffer_load_dwordx4 v125, %[rw], s44 offen nt lds\ns_add_u32 m0, %[tb], 9216\ns_add_u32 s44, %[wo], 16384\nbuffer_load_dwordx4 v125, %[rw], s44 offen nt lds\ns_add_u32 m0, %[tb], 10240\ns_add_u32 s44, %[wo], 32768\n" \
"buffer_load_dwordx4 v125, %[rw], s44 offen nt lds\ns_add_u32 m0, %[tb], 11264\ns_add_u32 s44, %[wo], 49152\nbuffer_load_dwordx4 v125, %[rw], s44 offen nt lds\ns_add_u32 m0, %[lw], 0\ns_add_u32 s44, %[wo], 65536\nbuffer_load_dwordx4 v125, %[rw], s44 offen nt lds\ns_add_u32 m0, %[lw], 1024\n" \
"s_add_u32 s44, %[wo], 81920\nbuffer_load_dwordx4 v125, %[rw], s44 offen nt lds\ns_add_u32 m0, %[lw], 2048\ns_add_u32 s44, %[wo], 98304\nbuffer_load_dwordx4 v125, %[rw], s44 offen nt lds\ns_add_u32 m0, %[lw], 3072\ns_add_u32 s44, %[wo], 114688\nbuffer_load_dwordx4 v125, %[rw], s44 offen nt lds\n" \
"s_add_u32 m0, %[lw], 4096\ns_add_u32 s44, %[wo], 131072\nbuffer_load_dwordx4 v125, %[rw], s44 offen nt lds\ns_add_u32 m0, %[lw], 5120\ns_add_u32 s44, %[wo], 147456\nbuffer_load_dwordx4 v125, %[rw], s44 offen nt lds\ns_add_u32 m0, %[lw], 6144\ns_add_u32 s44, %[wo], 163840\n" \
"buffer_load_dwordx4 v125, %[rw], s44 offen nt lds\ns_add_u32 m0, %[lw], 7168\ns_add_u32 s44, %[wo], 180224\nbuffer_load_dwordx4 v125, %[rw], s44 offen nt lds\ns_add_u32 m0, %[lw], 8192\ns_add_u32 s44, %[wo], 196608\nbuffer_load_dwordx4 v125, %[rw], s44 offen nt lds\ns_add_u32 m0, %[lw], 9216\n" \
"s_add_u32 s44, %[wo], 212992\nbuffer_load_dwordx4 v125, %[rw], s44 offen nt lds\ns_add_u32 m0, %[lw], 10240\ns_add_u32 s44, %[wo], 229376\nbuffer_load_dwordx4 v125, %[rw], s44 offen nt lds\ns_add_u32 m0, %[lw], 11264\ns_add_u32 s44, %[wo], 245760\nbuffer_load_dwordx4 v125, %[rw], s44 offen nt lds\n" \
"s_add_u32 m0, %[lw], 12288\ns_add_u32 s44, %[wo], 262144\nbuffer_load_dwordx4 v125, %[rw], s44 offen nt lds\ns_add_u32 m0, %[lw], 13312\ns_add_u32 s44, %[wo], 278528\nbuffer_load_dwordx4 v125, %[rw], s44 offen nt lds\ns_add_u32 m0, %[lw], 14336\ns_add_u32 s44, %[wo], 294912\n" \
"buffer_load_dwordx4 v125, %[rw], s44 offen nt lds\ns_add_u32 m0, %[lw], 15360\ns_add_u32 s44, %[wo], 311296\nbuffer_load_dwordx4 v125, %[rw], s44 offen nt lds\ns_add_u32 m0, %[lw], 16384\ns_add_u32 s44, %[wo], 327680\nbuffer_load_dwordx4 v125, %[rw], s44 offen nt lds\ns_add_u32 m0, %[lw], 17408\n" \
"s_add_u32 s44, %[wo], 344064\nbuffer_load_dwordx4 v125, %[rw], s44 offen nt lds\ns_add_u32 m0, %[lw], 18432\ns_add_u32 s44, %[wo], 360448\nbuffer_load_dwordx4 v125, %[rw], s44 offen nt lds\ns_add_u32 m0, %[lw], 19456\ns_add_u32 s44, %[wo], 376832\nbuffer_load_dwordx4 v125, %[rw], s44 offen nt lds\n" \
"s_add_u32 m0, %[lw], 20480\ns_add_u32 s44, %[wo], 393216\nbuffer_load_dwordx4 v125, %[rw], s44 offen nt lds\ns_add_u32 m0, %[lw], 21504\ns_add_u32 s44, %[wo], 409600\nbuffer_load_dwordx4 v125, %[rw], s44 offen nt lds\ns_add_u32 m0, %[lw], 22528\ns_add_u32 s44, %[wo], 425984\n" \
"buffer_load_dwordx4 v125, %[rw], s44 offen nt lds\ns_add_u32 m0, %[lw], 23552\ns_add_u32 s44, %[wo], 442368\nbuffer_load_dwordx4 v125, %[rw], s44 offen nt lds\ns_add_u32 m0, %[tb], 0\ns_add_u32 s44, %[wo], 458752\nbuffer_load_dwordx4 v125, %[rw], s44 offen nt lds\ns_add_u32 m0, %[tb], 1024\n" \
"s_add_u32 s44, %[wo], 475136\nbuffer_load_dwordx4 v125, %[rw], s44 offen nt lds\ns_add_u32 m0, %[tb], 2048\ns_add_u32 s44, %[wo], 491520\nbuffer_load_dwordx4 v125, %[rw], s44 offen nt lds\ns_add_u32 m0, %[tb], 3072\ns_add_u32 s44, %[wo], 507904\nbuffer_load_dwordx4 v125, %[rw], s44 offen nt lds\n" \
"s_add_u32 m0, %[tb], 4096\ns_add_u32 s44, %[wo], 524288\nbuffer_load_dwordx4 v125, %[rw], s44 offen nt lds\ns_add_u32 m0, %[tb], 5120\ns_add_u32 s44, %[wo], 540672\nbuffer_load_dwordx4 v125, %[rw], s44 offen nt lds\ns_add_u32 m0, %[tb], 6144\ns_add_u32 s44, %[wo], 557056\n" \
"buffer_load_dwordx4 v125, %[rw], s44 offen nt lds\ns_add_u32 m0, %[tb], 7168\ns_add_u32 s44, %[wo], 573440\nbuffer_load_dwordx4 v125, %[rw], s44 offen nt lds\ns_waitcnt vmcnt(32)\nds_read_b64_tr_b16 a[0:1], v126 offset:8192\nds_read_b64_tr_b16 a[2:3], v126 offset:8704\nds_read_b64_tr_b16 a[8:9], v126 offset:8224\n" \
"ds_read_b64_tr_b16 a[10:11], v126 offset:8736\nds_read_b64_tr_b16 a[16:17], v126 offset:8256\nds_read_b64_tr_b16 a[18:19], v126 offset:8768\nds_read_b64_tr_b16 a[24:25], v126 offset:8288\nds_read_b64_tr_b16 a[26:27], v126 offset:8800\ns_waitcnt lgkmcnt(0)\ns_add_u32 m0, %[tb], 8192\ns_add_u32 s44, %[wo], 589824\n" \
"buffer_load_dwordx4 v125, %[rw], s44 offen nt lds\ns_add_u32 m0, %[tb], 9216\ns_add_u32 s44, %[wo], 606208\nbuffer_load_dwordx4 v125, %[rw], s44 offen nt lds\ns_add_u32 m0, %[tb], 10240\ns_add_u32 s44, %[wo], 622592\nbuffer_load_dwordx4 v125, %[rw], s44 offen nt lds\ns_add_u32 m0, %[tb], 11264\n" \
"s_add_u32 s44, %[wo], 638976\nbuffer_load_dwordx4 v125, %[rw], s44 offen nt lds\ns_waitcnt vmcnt(32)\nds_read_b64_tr_b16 a[4:5], v127 offset:0\nds_read_b64_tr_b16 a[6:7], v127 offset:512\nds_read_b64_tr_b16 a[12:13], v127 offset:32\nds_read_b64_tr_b16 a[14:15], v127 offset:544\nds_read_b64_tr_b16 a[20:21], v127 offset:64\n" \
"ds_read_b64_tr_b16 a[22:23], v127 offset:576\nds_read_b64_tr_b16 a[28:29], v127 offset:96\nds_read_b64_tr_b16 a[30:31], v127 offset:608\ns_waitcnt lgkmcnt(0)\ns_add_u32 m0, %[lw], 0\ns_add_u32 s44, %[wo], 655360\nbuffer_load_dwordx4 v125, %[rw], s44 offen nt lds\ns_add_u32 m0, %[lw], 1024\n" \
"s_add_u32 s44, %[wo], 671744\nbuffer_load_dwordx4 v125, %[rw], s44 offen nt lds\ns_add_u32 m0, %[lw], 2048\ns_add_u32 s44, %[wo], 688128\nbuffer_load_dwordx4 v125, %[rw], s44 offen nt lds\ns_add_u32 m0, %[lw], 3072\ns_add_u32 s44, %[wo], 704512\nbuffer_load_dwordx4 v125, %[rw], s44 offen nt lds\n" \
"s_waitcnt vmcnt(32)\nds_read_b64_tr_b16 a[32:33], v127 offset:4096\nds_read_b64_tr_b16 a[34:35], v127 offset:4608\nds_read_b64_tr_b16 v[92:93], v127 offset:4128\nds_read_b64_tr_b16 v[94:95], v127 offset:4640\nds_read_b64_tr_b16 a[40:41], v127 offset:4160\nds_read_b64_tr_b16 a[42:43], v127 offset:4672\nds_read_b64_tr_b16 a[48:49], v127 offset:4192\n" \
"ds_read_b64_tr_b16 a[50:51], v127 offset:4704\ns_waitcnt lgkmcnt(0)\ns_add_u32 m0, %[lw], 4096\ns_add_u32 s44, %[wo], 720896\nbuffer_load_dwordx4 v125, %[rw], s44 offen nt lds\ns_add_u32 m0, %[lw], 5120\ns_add_u32 s44, %[wo], 737280\nbuffer_load_dwordx4 v125, %[rw], s44 offen nt lds\n" \
"s_add_u32 m0, %[lw], 6144\ns_add_u32 s44, %[wo], 753664\nbuffer_load_dwordx4 v125, %[rw], s44 offen nt lds\ns_add_u32 m0, %[lw], 7168\ns_add_u32 s44, %[wo], 770048\nbuffer_load_dwordx4 v125, %[rw], s44 offen nt lds\ns_waitcnt vmcnt(32)\nds_read_b64_tr_b16 a[36:37], v127 offset:8192\n" \
"ds_read_b64_tr_b16 a[38:39], v127 offset:8704\nds_read_b64_tr_b16 v[88:89], v127 offset:8224\nds_read_b64_tr_b16 v[90:91], v127 offset:8736\nds_read_b64_tr_b16 a[44:45], v127 offset:8256\nds_read_b64_tr_b16 a[46:47], v127 offset:8768\nds_read_b64_tr_b16 a[52:53], v127 offset:8288\nds_read_b64_tr_b16 a[54:55], v127 offset:8800\ns_waitcnt lgkmcnt(0)\n" \
"s_add_u32 m0, %[lw], 8192\ns_add_u32 s44, %[wo], 786432\nbuffer_load_dwordx4 v125, %[rw], s44 offen nt lds\ns_add_u32 m0, %[lw], 9216\ns_add_u32 s44, %[wo], 802816\nbuffer_load_dwordx4 v125, %[rw], s44 offen nt lds\ns_add_u32 m0, %[lw], 10240\ns_add_u32 s44, %[wo], 819200\n" \
"buffer_load_dwordx4 v125, %[rw], s44 offen nt lds\ns_add_u32 m0, %[lw], 11264\ns_add_u32 s44, %[wo], 835584\nbuffer_load_dwordx4 v125, %[rw], s44 offen nt lds\ns_waitcnt vmcnt(32)\nds_read_b64_tr_b16 a[56:57], v127 offset:12288\nds_read_b64_tr_b16 a[58:59], v127 offset:12800\nds_read_b64_tr_b16 a[64:65], v127 offset:12320\n" \
"ds_read_b64_tr_b16 a[66:67], v127 offset:12832\nds_read_b64_tr_b16 a[72:73], v127 offset:12352\nds_read_b64_tr_b16 a[74:75], v127 offset:12864\nds_read_b64_tr_b16 a[80:81], v127 offset:12384\nds_read_b64_tr_b16 a[82:83], v127 offset:12896\ns_waitcnt lgkmcnt(0)\ns_add_u32 m0, %[lw], 12288\ns_add_u32 s44, %[wo], 851968\n" \
"buffer_load_dwordx4 v125, %[rw], s44 offen nt lds\ns_add_u32 m0, %[lw], 13312\ns_add_u32 s44, %[wo], 868352\nbuffer_load_dwordx4 v125, %[rw], s44 offen nt lds\ns_add_u32 m0, %[lw], 14336\ns_add_u32 s44, %[wo], 884736\nbuffer_load_dwordx4 v125, %[rw], s44 offen nt lds\ns_add_u32 m0, %[lw], 15360\n" \
"s_add_u32 s44, %[wo], 901120\nbuffer_load_dwordx4 v125, %[rw], s44 offen nt lds\ns_waitcnt vmcnt(32)\nds_read_b64_tr_b16 a[60:61], v127 offset:16384\nds_read_b64_tr_b16 a[62:63], v127 offset:16896\nds_read_b64_tr_b16 a[68:69], v127 offset:16416\nds_read_b64_tr_b16 a[70:71], v127 offset:16928\nds_read_b64_tr_b16 a[76:77], v127 offset:16448\n" \
"ds_read_b64_tr_b16 a[78:79], v127 offset:16960\nds_read_b64_tr_b16 a[84:85], v127 offset:16480\nds_read_b64_tr_b16 a[86:87], v127 offset:16992\ns_waitcnt lgkmcnt(0)\ns_add_u32 m0, %[lw], 16384\ns_add_u32 s44, %[wo], 917504\nbuffer_load_dwordx4 v125, %[rw], s44 offen nt lds\ns_add_u32 m0, %[lw], 17408\n" \
"s_add_u32 s44, %[wo], 933888\nbuffer_load_dwordx4 v125, %[rw], s44 offen nt lds\ns_add_u32 m0, %[lw], 18432\ns_add_u32 s44, %[wo], 950272\nbuffer_load_dwordx4 v125, %[rw], s44 offen nt lds\ns_add_u32 m0, %[lw], 19456\ns_add_u32 s44, %[wo], 966656\nbuffer_load_dwordx4 v125, %[rw], s44 offen nt lds\n" \
"s_waitcnt vmcnt(32)\nds_read_b64_tr_b16 a[88:89], v127 offset:20480\nds_read_b64_tr_b16 a[90:91], v127 offset:20992\nds_read_b64_tr_b16 a[96:97], v127 offset:20512\nds_read_b64_tr_b16 a[98:99], v127 offset:21024\nds_read_b64_tr_b16 a[104:105], v127 offset:20544\nds_read_b64_tr_b16 a[106:107], v127 offset:21056\nds_read_b64_tr_b16 v[84:85], v127 offset:20576\n" \
"ds_read_b64_tr_b16 v[86:87], v127 offset:21088\ns_waitcnt lgkmcnt(0)\ns_add_u32 m0, %[lw], 20480\ns_add_u32 s44, %[wo], 983040\nbuffer_load_dwordx4 v125, %[rw], s44 offen nt lds\ns_add_u32 m0, %[lw], 21504\ns_add_u32 s44, %[wo], 999424\nbuffer_load_dwordx4 v125, %[rw], s44 offen nt lds\n" \
"s_add_u32 m0, %[lw], 22528\ns_add_u32 s44, %[wo], 1015808\nbuffer_load_dwordx4 v125, %[rw], s44 offen nt lds\ns_add_u32 m0, %[lw], 23552\ns_add_u32 s44, %[wo], 1032192\nbuffer_load_dwordx4 v125, %[rw], s44 offen nt lds\ns_waitcnt vmcnt(32)\nds_read_b64_tr_b16 a[92:93], v126 offset:0\n" \
"ds_read_b64_tr_b16 a[94:95], v126 offset:512\nds_read_b64_tr_b16 a[100:101], v126 offset:32\nds_read_b64_tr_b16 a[102:103], v126 offset:544\nds_read_b64_tr_b16 a[108:109], v126 offset:64\nds_read_b64_tr_b16 a[110:111], v126 offset:576\nds_read_b64_tr_b16 v[80:81], v126 offset:96\nds_read_b64_tr_b16 v[82:83], v126 offset:608\ns_waitcnt lgkmcnt(0)\n" \
"s_add_u32 m0, %[tb], 0\ns_add_u32 s44, %[wo], 1048576\nbuffer_load_dwordx4 v125, %[rw], s44 offen nt lds\ns_add_u32 m0, %[tb], 1024\ns_add_u32 s44, %[wo], 1064960\nbuffer_load_dwordx4 v125, %[rw], s44 offen nt lds\ns_add_u32 m0, %[tb], 2048\ns_add_u32 s44, %[wo], 1081344\n" \
"buffer_load_dwordx4 v125, %[rw], s44 offen nt lds\ns_add_u32 m0, %[tb], 3072\ns_add_u32 s44, %[wo], 1097728\nbuffer_load_dwordx4 v125, %[rw], s44 offen nt lds\ns_waitcnt vmcnt(32)\nds_read_b64_tr_b16 a[112:113], v126 offset:4096\nds_read_b64_tr_b16 a[114:115], v126 offset:4608\nds_read_b64_tr_b16 a[120:121], v126 offset:4128\n" \
"ds_read_b64_tr_b16 a[122:123], v126 offset:4640\nds_read_b64_tr_b16 a[128:129], v126 offset:4160\nds_read_b64_tr_b16 a[130:131], v126 offset:4672\nds_read_b64_tr_b16 a[136:137], v126 offset:4192\nds_read_b64_tr_b16 a[138:139], v126 offset:4704\ns_waitcnt lgkmcnt(0)\ns_add_u32 m0, %[tb], 4096\ns_add_u32 s44, %[wo], 1114112\n" \
"buffer_load_dwordx4 v125, %[rw], s44 offen nt lds\ns_add_u32 m0, %[tb], 5120\ns_add_u32 s44, %[wo], 1130496\nbuffer_load_dwordx4 v125, %[rw], s44 offen nt lds\ns_add_u32 m0, %[tb], 6144\ns_add_u32 s44, %[wo], 1146880\nbuffer_load_dwordx4 v125, %[rw], s44 offen nt lds\ns_add_u32 m0, %[tb], 7168\n" \
"s_add_u32 s44, %[wo], 1163264\nbuffer_load_dwordx4 v125, %[rw], s44 offen nt lds\ns_waitcnt vmcnt(32)\nds_read_b64_tr_b16 a[116:117], v126 offset:8192\nds_read_b64_tr_b16 a[118:119], v126 offset:8704\nds_read_b64_tr_b16 a[124:125], v126 offset:8224\nds_read_b64_tr_b16 a[126:127], v126 offset:8736\nds_read_b64_tr_b16 a[132:133], v126 offset:8256\n" \
"ds_read_b64_tr_b16 a[134:135], v126 offset:8768\nds_read_b64_tr_b16 a[140:141], v126 offset:8288\nds_read_b64_tr_b16 a[142:143], v126 offset:8800\ns_waitcnt lgkmcnt(0)\ns_add_u32 m0, %[tb], 8192\ns_add_u32 s44, %[wo], 1179648\nbuffer_load_dwordx4 v125, %[rw], s44 offen nt lds\ns_add_u32 m0, %[tb], 9216\n" \
"s_add_u32 s44, %[wo], 1196032\nbuffer_load_dwordx4 v125, %[rw], s44 offen nt lds\ns_add_u32 m0, %[tb], 10240\ns_add_u32 s44, %[wo], 1212416\nbuffer_load_dwordx4 v125, %[rw], s44 offen nt lds\ns_add_u32 m0, %[tb], 11264\ns_add_u32 s44, %[wo], 1228800\nbuffer_load_dwordx4 v125, %[rw], s44 offen nt lds\n" \
"s_waitcnt vmcnt(32)\nds_read_b64_tr_b16 a[144:145], v127 offset:0\nds_read_b64_tr_b16 a[146:147], v127 offset:512\nds_read_b64_tr_b16 v[76:77], v127 offset:32\nds_read_b64_tr_b16 v[78:79], v127 offset:544\nds_read_b64_tr_b16 a[152:153], v127 offset:64\nds_read_b64_tr_b16 a[154:155], v127 offset:576\nds_read_b64_tr_b16 a[160:161], v127 offset:96\n" \
"ds_read_b64_tr_b16 a[162:163], v127 offset:608\ns_waitcnt lgkmcnt(0)\ns_add_u32 m0, %[lw], 0\ns_add_u32 s44, %[wo], 1245184\nbuffer_load_dwordx4 v125, %[rw], s44 offen nt lds\ns_add_u32 m0, %[lw], 1024\ns_add_u32 s44, %[wo], 1261568\nbuffer_load_dwordx4 v125, %[rw], s44 offen nt lds\n" \
"s_add_u32 m0, %[lw], 2048\ns_add_u32 s44, %[wo], 1277952\nbuffer_load_dwordx4 v125, %[rw], s44 offen nt lds\ns_add_u32 m0, %[lw], 3072\ns_add_u32 s44, %[wo], 1294336\nbuffer_load_dwordx4 v125, %[rw], s44 offen nt lds\ns_waitcnt vmcnt(32)\nds_read_b64_tr_b16 a[148:149], v127 offset:4096\n" \
"ds_read_b64_tr_b16 a[150:151], v127 offset:4608\nds_read_b64_tr_b16 v[72:73], v127 offset:4128\nds_read_b64_tr_b16 v[74:75], v127 offset:4640\nds_read_b64_tr_b16 a[156:157], v127 offset:4160\nds_read_b64_tr_b16 a[158:159], v127 offset:4672\nds_read_b64_tr_b16 a[164:165], v127 offset:4192\nds_read_b64_tr_b16 a[166:167], v127 offset:4704\ns_waitcnt lgkmcnt(0)\n" \
"s_add_u32 m0, %[lw], 4096\ns_add_u32 s44, %[wo], 1310720\nbuffer_load_dwordx4 v125, %[rw], s44 offen nt lds\ns_add_u32 m0, %[lw], 5120\ns_add_u32 s44, %[wo], 1327104\nbuffer_load_dwordx4 v125, %[rw], s44 offen nt lds\ns_add_u32 m0, %[lw], 6144\ns_add_u32 s44, %[wo], 1343488\n" \
"buffer_load_dwordx4 v125, %[rw], s44 offen nt lds\ns_add_u32 m0, %[lw], 7168\ns_add_u32 s44, %[wo], 1359872\nbuffer_load_dwordx4 v125, %[rw], s44 offen nt lds\ns_waitcnt vmcnt(32)\nds_read_b64_tr_b16 a[168:169], v127 offset:8192\nds_read_b64_tr_b16 a[170:171], v127 offset:8704\nds_read_b64_tr_b16 a[176:177], v127 offset:8224\n" \
"ds_read_b64_tr_b16 a[178:179], v127 offset:8736\nds_read_b64_tr_b16 v[68:69], v127 offset:8256\nds_read_b64_tr_b16 v[70:71], v127 offset:8768\nds_read_b64_tr_b16 a[184:185], v127 offset:8288\nds_read_b64_tr_b16 a[186:187], v127 offset:8800\ns_waitcnt lgkmcnt(0)\ns_add_u32 m0, %[lw], 8192\ns_add_u32 s44, %[wo], 1376256\n" \
"buffer_load_dwordx4 v125, %[rw], s44 offen nt lds\ns_add_u32 m0, %[lw], 9216\ns_add_u32 s44, %[wo], 1392640\nbuffer_load_dwordx4 v125, %[rw], s44 offen nt lds\ns_add_u32 m0, %[lw], 10240\ns_add_u32 s44, %[wo], 1409024\nbuffer_load_dwordx4 v125, %[rw], s44 offen nt lds\ns_add_u32 m0, %[lw], 11264\n" \
"s_add_u32 s44, %[wo], 1425408\nbuffer_load_dwordx4 v125, %[rw], s44 offen nt lds\ns_waitcnt vmcnt(32)\nds_read_b64_tr_b16 a[172:173], v127 offset:12288\nds_read_b64_tr_b16 a[174:175], v127 offset:12800\nds_read_b64_tr_b16 a[180:181], v127 offset:12320\nds_read_b64_tr_b16 a[182:183], v127 offset:12832\nds_read_b64_tr_b16 v[64:65], v127 offset:12352\n" \
"ds_read_b64_tr_b16 v[66:67], v127 offset:12864\nds_read_b64_tr_b16 a[188:189], v127 offset:12384\nds_read_b64_tr_b16 a[190:191], v127 offset:12896\ns_waitcnt lgkmcnt(0)\ns_add_u32 m0, %[lw], 12288\ns_add_u32 s44, %[wo], 1441792\nbuffer_load_dwordx4 v125, %[rw], s44 offen nt lds\ns_add_u32 m0, %[lw], 13312\n" \
"s_add_u32 s44, %[wo], 1458176\nbuffer_load_dwordx4 v125, %[rw], s44 offen nt lds\ns_add_u32 m0, %[lw], 14336\ns_add_u32 s44, %[wo], 1474560\nbuffer_load_dwordx4 v125, %[rw], s44 offen nt lds\ns_add_u32 m0, %[lw], 15360\ns_add_u32 s44, %[wo], 1490944\nbuffer_load_dwordx4 v125, %[rw], s44 offen nt lds\n" \
"s_waitcnt vmcnt(32)\nds_read_b64_tr_b16 a[192:193], v127 offset:16384\nds_read_b64_tr_b16 a[194:195], v127 offset:16896\nds_read_b64_tr_b16 a[200:201], v127 offset:16416\nds_read_b64_tr_b16 a[202:203], v127 offset:16928\nds_read_b64_tr_b16 a[208:209], v127 offset:16448\nds_read_b64_tr_b16 a[210:211], v127 offset:16960\nds_read_b64_tr_b16 a[216:217], v127 offset:16480\n" \
"ds_read_b64_tr_b16 a[218:219], v127 offset:16992\ns_waitcnt lgkmcnt(0)\ns_add_u32 m0, %[lw], 16384\ns_add_u32 s44, %[wo], 1507328\nbuffer_load_dwordx4 v125, %[rw], s44 offen nt lds\ns_add_u32 m0, %[lw], 17408\ns_add_u32 s44, %[wo], 1523712\nbuffer_load_dwordx4 v125, %[rw], s44 offen nt lds\n" \
"s_add_u32 m0, %[lw], 18432\ns_add_u32 s44, %[wo], 1540096\nbuffer_load_dwordx4 v125, %[rw], s44 offen nt lds\ns_add_u32 m0, %[lw], 19456\ns_add_u32 s44, %[wo], 1556480\nbuffer_load_dwordx4 v125, %[rw], s44 offen nt lds\ns_waitcnt vmcnt(32)\nds_read_b64_tr_b16 a[196:197], v127 offset:20480\n" \
"ds_read_b64_tr_b16 a[198:199], v127 offset:20992\nds_read_b64_tr_b16 a[204:205], v127 offset:20512\nds_read_b64_tr_b16 a[206:207], v127 offset:21024\nds_read_b64_tr_b16 a[212:213], v127 offset:20544\nds_read_b64_tr_b16 a[214:215], v127 offset:21056\nds_read_b64_tr_b16 a[220:221], v127 offset:20576\nds_read_b64_tr_b16 a[222:223], v127 offset:21088\ns_waitcnt lgkmcnt(0)\n" \
"s_add_u32 m0, %[lw], 20480\ns_add_u32 s44, %[wo], 1572864\nbuffer_load_dwordx4 v125, %[rw], s44 offen nt lds\ns_add_u32 m0, %[lw], 21504\ns_add_u32 s44, %[wo], 1589248\nbuffer_load_dwordx4 v125, %[rw], s44 offen nt lds\ns_add_u32 m0, %[lw], 22528\ns_add_u32 s44, %[wo], 1605632\n" \
"buffer_load_dwordx4 v125, %[rw], s44 offen nt lds\ns_add_u32 m0, %[lw], 23552\ns_add_u32 s44, %[wo], 1622016\nbuffer_load_dwordx4 v125, %[rw], s44 offen nt lds\ns_waitcnt vmcnt(32)\nds_read_b64_tr_b16 v[60:61], v126 offset:0\nds_read_b64_tr_b16 v[62:63], v126 offset:512\nds_read_b64_tr_b16 a[224:225], v126 offset:32\n" \
"ds_read_b64_tr_b16 a[226:227], v126 offset:544\nds_read_b64_tr_b16 a[232:233], v126 offset:64\nds_read_b64_tr_b16 a[234:235], v126 offset:576\nds_read_b64_tr_b16 a[240:241], v126 offset:96\nds_read_b64_tr_b16 a[242:243], v126 offset:608\ns_waitcnt lgkmcnt(0)\ns_add_u32 m0, %[tb], 0\ns_add_u32 s44, %[wo], 1638400\n" \
"buffer_load_dwordx4 v125, %[rw], s44 offen nt lds\ns_add_u32 m0, %[tb], 1024\ns_add_u32 s44, %[wo], 1654784\nbuffer_load_dwordx4 v125, %[rw], s44 offen nt lds\ns_add_u32 m0, %[tb], 2048\ns_add_u32 s44, %[wo], 1671168\nbuffer_load_dwordx4 v125, %[rw], s44 offen nt lds\ns_add_u32 m0, %[tb], 3072\n" \
"s_add_u32 s44, %[wo], 1687552\nbuffer_load_dwordx4 v125, %[rw], s44 offen nt lds\ns_waitcnt vmcnt(32)\nds_read_b64_tr_b16 v[56:57], v126 offset:4096\nds_read_b64_tr_b16 v[58:59], v126 offset:4608\nds_read_b64_tr_b16 a[228:229], v126 offset:4128\nds_read_b64_tr_b16 a[230:231], v126 offset:4640\nds_read_b64_tr_b16 a[236:237], v126 offset:4160\n" \
"ds_read_b64_tr_b16 a[238:239], v126 offset:4672\nds_read_b64_tr_b16 a[244:245], v126 offset:4192\nds_read_b64_tr_b16 a[246:247], v126 offset:4704\ns_waitcnt lgkmcnt(0)\ns_add_u32 m0, %[tb], 4096\ns_add_u32 s44, %[wo], 1703936\nbuffer_load_dwordx4 v125, %[rw], s44 offen nt lds\ns_add_u32 m0, %[tb], 5120\n" \
"s_add_u32 s44, %[wo], 1720320\nbuffer_load_dwordx4 v125, %[rw], s44 offen nt lds\ns_add_u32 m0, %[tb], 6144\ns_add_u32 s44, %[wo], 1736704\nbuffer_load_dwordx4 v125, %[rw], s44 offen nt lds\ns_add_u32 m0, %[tb], 7168\ns_add_u32 s44, %[wo], 1753088\nbuffer_load_dwordx4 v125, %[rw], s44 offen nt lds\n" \
"s_waitcnt vmcnt(32)\nds_read_b64_tr_b16 a[248:249], v126 offset:8192\nds_read_b64_tr_b16 a[250:251], v126 offset:8704\nds_read_b64_tr_b16 v[128:129], v126 offset:8224\nds_read_b64_tr_b16 v[130:131], v126 offset:8736\nds_read_b64_tr_b16 v[136:137], v126 offset:8256\nds_read_b64_tr_b16 v[138:139], v126 offset:8768\nds_read_b64_tr_b16 v[144:145], v126 offset:8288\n" \
"ds_read_b64_tr_b16 v[146:147], v126 offset:8800\ns_waitcnt lgkmcnt(0)\ns_add_u32 m0, %[tb], 8192\ns_add_u32 s44, %[wo], 1769472\nbuffer_load_dwordx4 v125, %[rw], s44 offen nt lds\ns_add_u32 m0, %[tb], 9216\ns_add_u32 s44, %[wo], 1785856\nbuffer_load_dwordx4 v125, %[rw], s44 offen nt lds\n" \
"s_add_u32 m0, %[tb], 10240\ns_add_u32 s44, %[wo], 1802240\nbuffer_load_dwordx4 v125, %[rw], s44 offen nt lds\ns_add_u32 m0, %[tb], 11264\ns_add_u32 s44, %[wo], 1818624\nbuffer_load_dwordx4 v125, %[rw], s44 offen nt lds\ns_waitcnt vmcnt(32)\nds_read_b64_tr_b16 a[252:253], v127 offset:0\n" \
"ds_read_b64_tr_b16 a[254:255], v127 offset:512\nds_read_b64_tr_b16 v[132:133], v127 offset:32\nds_read_b64_tr_b16 v[134:135], v127 offset:544\nds_read_b64_tr_b16 v[140:141], v127 offset:64\nds_read_b64_tr_b16 v[142:143], v127 offset:576\nds_read_b64_tr_b16 v[148:149], v127 offset:96\nds_read_b64_tr_b16 v[150:151], v127 offset:608\ns_waitcnt lgkmcnt(0)\n" \
"ds_write_b128 v1, v[92:95] offset:0\nds_write_b128 v1, v[88:91] offset:1024\nds_write_b128 v1, v[84:87] offset:2048\nds_write_b128 v1, v[80:83] offset:3072\ns_waitcnt vmcnt(28)\nds_read_b64_tr_b16 v[152:153], v127 offset:4096\nds_read_b64_tr_b16 v[154:155], v127 offset:4608\nds_read_b64_tr_b16 v[160:161], v127 offset:4128\n" \
"ds_read_b64_tr_b16 v[162:163], v127 offset:4640\nds_read_b64_tr_b16 v[80:81], v127 offset:4160\nds_read_b64_tr_b16 v[82:83], v127 offset:4672\nds_read_b64_tr_b16 v[168:169], v127 offset:4192\nds_read_b64_tr_b16 v[170:171], v127 offset:4704\ns_waitcnt lgkmcnt(0)\nds_write_b128 v1, v[76:79] offset:4096\nds_write_b128 v1, v[72:75] offset:5120\n" \
"ds_write_b128 v1, v[68:71] offset:6144\nds_write_b128 v1, v[64:67] offset:7168\ns_waitcnt vmcnt(24)\nds_read_b64_tr_b16 v[156:157], v127 offset:8192\nds_read_b64_tr_b16 v[158:159], v127 offset:8704\nds_read_b64_tr_b16 v[164:165], v127 offset:8224\nds_read_b64_tr_b16 v[166:167], v127 offset:8736\nds_read_b64_tr_b16 v[64:65], v127 offset:8256\n" \
"ds_read_b64_tr_b16 v[66:67], v127 offset:8768\nds_read_b64_tr_b16 v[172:173], v127 offset:8288\nds_read_b64_tr_b16 v[174:175], v127 offset:8800\ns_waitcnt lgkmcnt(0)\nds_write_b128 v1, v[60:63] offset:8192\nds_write_b128 v1, v[56:59] offset:9216\nds_write_b128 v1, v[80:83] offset:10240\nds_write_b128 v1, v[64:67] offset:11264\n" \
"s_waitcnt vmcnt(20)\nds_read_b64_tr_b16 v[176:177], v127 offset:12288\nds_read_b64_tr_b16 v[178:179], v127 offset:12800\nds_read_b64_tr_b16 v[184:185], v127 offset:12320\nds_read_b64_tr_b16 v[186:187], v127 offset:12832\nds_read_b64_tr_b16 v[192:193], v127 offset:12352\nds_read_b64_tr_b16 v[194:195], v127 offset:12864\nds_read_b64_tr_b16 v[200:201], v127 offset:12384\n" \
"ds_read_b64_tr_b16 v[202:203], v127 offset:12896\ns_waitcnt lgkmcnt(0)\ns_waitcnt vmcnt(16)\nds_read_b64_tr_b16 v[180:181], v127 offset:16384\nds_read_b64_tr_b16 v[182:183], v127 offset:16896\nds_read_b64_tr_b16 v[188:189], v127 offset:16416\nds_read_b64_tr_b16 v[190:191], v127 offset:16928\nds_read_b64_tr_b16 v[196:197], v127 offset:16448\n" \
"ds_read_b64_tr_b16 v[198:199], v127 offset:16960\nds_read_b64_tr_b16 v[204:205], v127 offset:16480\nds_read_b64_tr_b16 v[206:207], v127 offset:16992\ns_waitcnt lgkmcnt(0)\ns_waitcnt vmcnt(12)\nds_read_b64_tr_b16 v[64:65], v127 offset:20480\nds_read_b64_tr_b16 v[66:67], v127 offset:20992\nds_read_b64_tr_b16 v[208:209], v127 offset:20512\n" \
"ds_read_b64_tr_b16 v[210:211], v127 offset:21024\nds_read_b64_tr_b16 v[216:217], v127 offset:20544\nds_read_b64_tr_b16 v[218:219], v127 offset:21056\nds_read_b64_tr_b16 v[224:225], v127 offset:20576\nds_read_b64_tr_b16 v[226:227], v127 offset:21088\ns_waitcnt lgkmcnt(0)\nds_write_b128 v1, v[64:67] offset:12288\ns_waitcnt vmcnt(8)\n" \
"ds_read_b64_tr_b16 v[64:65], v126 offset:0\nds_read_b64_tr_b16 v[66:67], v126 offset:512\nds_read_b64_tr_b16 v[212:213], v126 offset:32\nds_read_b64_tr_b16 v[214:215], v126 offset:544\nds_read_b64_tr_b16 v[220:221], v126 offset:64\nds_read_b64_tr_b16 v[222:223], v126 offset:576\nds_read_b64_tr_b16 v[228:229], v126 offset:96\nds_read_b64_tr_b16 v[230:231], v126 offset:608\n" \
"s_waitcnt lgkmcnt(0)\nds_write_b128 v1, v[64:67] offset:13312\ns_waitcnt vmcnt(4)\nds_read_b64_tr_b16 v[232:233], v126 offset:4096\nds_read_b64_tr_b16 v[234:235], v126 offset:4608\nds_read_b64_tr_b16 v[64:65], v126 offset:4128\nds_read_b64_tr_b16 v[66:67], v126 offset:4640\nds_read_b64_tr_b16 v[240:241], v126 offset:4160\n" \
"ds_read_b64_tr_b16 v[242:243], v126 offset:4672\nds_read_b64_tr_b16 v[248:249], v126 offset:4192\nds_read_b64_tr_b16 v[250:251], v126 offset:4704\ns_waitcnt lgkmcnt(0)\nds_write_b128 v1, v[64:67] offset:14336\ns_waitcnt vmcnt(0)\nds_read_b64_tr_b16 v[236:237], v126 offset:8192\nds_read_b64_tr_b16 v[238:239], v126 offset:8704\n" \
"ds_read_b64_tr_b16 v[64:65], v126 offset:8224\nds_read_b64_tr_b16 v[66:67], v126 offset:8736\nds_read_b64_tr_b16 v[244:245], v126 offset:8256\nds_read_b64_tr_b16 v[246:247], v126 offset:8768\nds_read_b64_tr_b16 v[252:253], v126 offset:8288\nds_read_b64_tr_b16 v[254:255], v126 offset:8800\ns_waitcnt lgkmcnt(0)\nds_write_b128 v1, v[64:67] offset:15360\n" \
"s_waitcnt lgkmcnt(0)\ns_barrier\ns_mov_b32 s40, 0\ns_mov_b32 s43, 0\ns_mov_b32 s56, -2147483648\n.Ldd_next%=:\nbuffer_load_dword v8, v7, %[rs], 0 offen sc1\ns_lshl_b32 s58, s40, 1\n" \
"s_lshl_b32 s44, s40, 19\ns_add_u32 s44, s44, %[ho]\ns_lshr_b32 s45, s44, 8\ns_and_b32 s45, s45, 0x2000\ns_xor_b32 s55, s44, s45\ns_waitcnt vmcnt(0)\nbuffer_store_dword v9, off, %[rs], s56 sc1\n.Ldd_w0%=:\n" \
"v_cmp_gt_i32 vcc, s58, v8\ns_cbranch_vccz .Ldd_w0go%=\ns_sleep 16\nbuffer_load_dword v8, v7, %[rs], 0 offen sc1\ns_waitcnt vmcnt(0)\ns_add_u32 s43, s43, 1\ns_cmp_lt_u32 s43, 262144\ns_cbranch_scc1 .Ldd_w0%=\n" \
"s_branch .Ldd_abort%=\n.Ldd_w0go%=:\ns_add_u32 s60, s55, 0\nbuffer_load_dwordx4 v[48:51], v0, %[rs], s60 offen offset:0 sc1\nbuffer_load_dwordx4 v[52:55], v0, %[rs], s60 offen offset:2048 sc1\ns_add_u32 s60, s55, 4096\nbuffer_load_dwordx4 v[56:59], v0, %[rs], s60 offen offset:0 sc1\nbuffer_load_dwordx4 v[60:63], v0, %[rs], s60 offen offset:2048 sc1\n" \
"s_add_u32 s60, s55, 16384\nbuffer_load_dwordx4 v[64:67], v0, %[rs], s60 offen offset:0 sc1\nbuffer_load_dwordx4 v[68:71], v0, %[rs], s60 offen offset:2048 sc1\ns_add_u32 s60, s55, 20480\nbuffer_load_dwordx4 v[72:75], v0, %[rs], s60 offen offset:0 sc1\nbuffer_load_dwordx4 v[76:79], v0, %[rs], s60 offen offset:2048 sc1\ns_add_u32 s60, s55, 32768\nbuffer_load_dwordx4 v[80:83], v0, %[rs], s60 offen offset:0 sc1\n" \
"buffer_load_dwordx4 v[84:87], v0, %[rs], s60 offen offset:2048 sc1\ns_add_u32 s60, s55, 36864\nbuffer_load_dwordx4 v[88:91], v0, %[rs], s60 offen offset:0 sc1\nbuffer_load_dwordx4 v[92:95], v0, %[rs], s60 offen offset:2048 sc1\ns_add_u32 s60, s55, 49152\nbuffer_load_dwordx4 v[96:99], v0, %[rs], s60 offen offset:0 sc1\nbuffer_load_dwordx4 v[100:103], v0, %[rs], s60 offen offset:2048 sc1\ns_add_u32 s60, s55, 53248\n" \
"buffer_load_dwordx4 v[104:107], v0, %[rs], s60 offen offset:0 sc1\nbuffer_load_dwordx4 v[108:111], v0, %[rs], s60 offen offset:2048 sc1\ns_add_u32 s44, s40, 0\ns_lshl_b32 s44, s44, 17\ns_add_u32 s44, s44, %[xn]\ns_lshr_b32 s45, s44, 8\ns_and_b32 s45, s45, 0x2000\ns_xor_b32 s41, s44, s45\n" \
"s_lshl_b32 s44, s40, 16\ns_cmp_eq_u32 %[l], 0\ns_cselect_b32 s42, s41, s44\ns_add_u32 s58, s58, 1\nds_read_b128 v[120:123], v1 offset:0\ns_waitcnt vmcnt(15)\nv_mfma_f32_16x16x32_bf16 v[16:19], a[0:3], v[48:51], 0\nds_read_b128 v[124:127], v1 offset:1024\n" \
"v_mfma_f32_16x16x32_bf16 v[24:27], a[8:11], v[48:51], 0\nbuffer_load_dword v8, v7, %[rs], 0 offen sc1\nv_mfma_f32_16x16x32_bf16 v[32:35], a[16:19], v[48:51], 0\nv_mfma_f32_16x16x32_bf16 v[40:43], a[24:27], v[48:51], 0\ns_add_u32 s60, s55, 65536\ns_waitcnt vmcnt(15)\nbuffer_load_dwordx4 v[48:51], v0, %[rs], s60 offen offset:0 sc1\nv_mfma_f32_16x16x32_bf16 v[16:19], a[4:7], v[52:55], v[16:19]\n" \
"v_mfma_f32_16x16x32_bf16 v[24:27], a[12:15], v[52:55], v[24:27]\nv_mfma_f32_16x16x32_bf16 v[32:35], a[20:23], v[52:55], v[32:35]\nv_mfma_f32_16x16x32_bf16 v[40:43], a[28:31], v[52:55], v[40:43]\ns_waitcnt vmcnt(15)\nbuffer_load_dwordx4 v[52:55], v0, %[rs], s60 offen offset:2048 sc1\nv_mfma_f32_16x16x32_bf16 v[16:19], a[32:35], v[56:59], v[16:19]\ns_waitcnt lgkmcnt(1)\nv_mfma_f32_16x16x32_bf16 v[24:27], v[120:123], v[56:59], v[24:27]\n" \
"v_mfma_f32_16x16x32_bf16 v[32:35], a[40:43], v[56:59], v[32:35]\nv_mfma_f32_16x16x32_bf16 v[40:43], a[48:51], v[56:59], v[40:43]\ns_add_u32 s60, s55, 69632\ns_waitcnt vmcnt(15)\nbuffer_load_dwordx4 v[56:59], v0, %[rs], s60 offen offset:0 sc1\nv_mfma_f32_16x16x32_bf16 v[16:19], a[36:39], v[60:63], v[16:19]\ns_waitcnt lgkmcnt(0)\nds_read_b128 v[120:123], v1 offset:2048\n" \
"v_mfma_f32_16x16x32_bf16 v[24:27], v[124:127], v[60:63], v[24:27]\nv_mfma_f32_16x16x32_bf16 v[32:35], a[44:47], v[60:63], v[32:35]\nv_mfma_f32_16x16x32_bf16 v[40:43], a[52:55], v[60:63], v[40:43]\ns_waitcnt vmcnt(15)\nbuffer_load_dwordx4 v[60:63], v0, %[rs], s60 offen offset:2048 sc1\nv_mfma_f32_16x16x32_bf16 v[16:19], a[56:59], v[64:67], v[16:19]\nds_read_b128 v[124:127], v1 offset:3072\nv_mfma_f32_16x16x32_bf16 v[24:27], a[64:67], v[64:67], v[24:27]\n" \
"v_mfma_f32_16x16x32_bf16 v[32:35], a[72:75], v[64:67], v[32:35]\nv_mfma_f32_16x16x32_bf16 v[40:43], a[80:83], v[64:67], v[40:43]\ns_add_u32 s60, s55, 81920\ns_waitcnt vmcnt(15)\nbuffer_load_dwordx4 v[64:67], v0, %[rs], s60 offen offset:0 sc1\nv_mfma_f32_16x16x32_bf16 v[16:19], a[60:63], v[68:71], v[16:19]\nv_mfma_f32_16x16x32_bf16 v[24:27], a[68:71], v[68:71], v[24:27]\nv_mfma_f32_16x16x32_bf16 v[32:35], a[76:79], v[68:71], v[32:35]\n" \
"v_mfma_f32_16x16x32_bf16 v[40:43], a[84:87], v[68:71], v[40:43]\ns_waitcnt vmcnt(15)\nbuffer_load_dwordx4 v[68:71], v0, %[rs], s60 offen offset:2048 sc1\nv_mfma_f32_16x16x32_bf16 v[16:19], a[88:91], v[72:75], v[16:19]\nv_mfma_f32_16x16x32_bf16 v[24:27], a[96:99], v[72:75], v[24:27]\nv_mfma_f32_16x16x32_bf16 v[32:35], a[104:107], v[72:75], v[32:35]\ns_waitcnt lgkmcnt(1)\nv_mfma_f32_16x16x32_bf16 v[40:43], v[120:123], v[72:75], v[40:43]\n" \
"s_add_u32 s60, s55, 86016\ns_waitcnt vmcnt(15)\nbuffer_load_dwordx4 v[72:75], v0, %[rs], s60 offen offset:0 sc1\nv_mfma_f32_16x16x32_bf16 v[16:19], a[92:95], v[76:79], v[16:19]\nds_read_b128 v[120:123], v1 offset:4096\nv_mfma_f32_16x16x32_bf16 v[24:27], a[100:103], v[76:79], v[24:27]\nv_mfma_f32_16x16x32_bf16 v[32:35], a[108:111], v[76:79], v[32:35]\ns_waitcnt lgkmcnt(1)\n" \
"v_mfma_f32_16x16x32_bf16 v[40:43], v[124:127], v[76:79], v[40:43]\ns_waitcnt vmcnt(15)\nbuffer_load_dwordx4 v[76:79], v0, %[rs], s60 offen offset:2048 sc1\nv_mfma_f32_16x16x32_bf16 v[16:19], a[112:115], v[80:83], v[16:19]\nds_read_b128 v[124:127], v1 offset:5120\nv_mfma_f32_16x16x32_bf16 v[24:27], a[120:123], v[80:83], v[24:27]\nv_mfma_f32_16x16x32_bf16 v[32:35], a[128:131], v[80:83], v[32:35]\nv_mfma_f32_16x16x32_bf16 v[40:43], a[136:139], v[80:83], v[40:43]\n" \
"s_add_u32 s60, s55, 98304\ns_waitcnt vmcnt(15)\nbuffer_load_dwordx4 v[80:83], v0, %[rs], s60 offen offset:0 sc1\nv_mfma_f32_16x16x32_bf16 v[16:19], a[116:119], v[84:87], v[16:19]\nds_read_b128 v[112:115], v1 offset:6144\nv_mfma_f32_16x16x32_bf16 v[24:27], a[124:127], v[84:87], v[24:27]\nv_mfma_f32_16x16x32_bf16 v[32:35], a[132:135], v[84:87], v[32:35]\nv_mfma_f32_16x16x32_bf16 v[40:43], a[140:143], v[84:87], v[40:43]\n" \
"s_waitcnt vmcnt(15)\nbuffer_load_dwordx4 v[84:87], v0, %[rs], s60 offen offset:2048 sc1\nv_mfma_f32_16x16x32_bf16 v[16:19], a[144:147], v[88:91], v[16:19]\ns_waitcnt lgkmcnt(2)\nds_read_b128 v[116:119], v1 offset:7168\nv_mfma_f32_16x16x32_bf16 v[24:27], v[120:123], v[88:91], v[24:27]\nv_mfma_f32_16x16x32_bf16 v[32:35], a[152:155], v[88:91], v[32:35]\nv_mfma_f32_16x16x32_bf16 v[40:43], a[160:163], v[88:91], v[40:43]\n" \
"s_add_u32 s60, s55, 102400\ns_waitcnt vmcnt(15)\nbuffer_load_dwordx4 v[88:91], v0, %[rs], s60 offen offset:0 sc1\nv_mfma_f32_16x16x32_bf16 v[16:19], a[148:151], v[92:95], v[16:19]\ns_waitcnt lgkmcnt(2)\nv_mfma_f32_16x16x32_bf16 v[24:27], v[124:127], v[92:95], v[24:27]\ns_waitcnt vmcnt(11)\nv_cmp_gt_i32 vcc, s58, v8\n" \
"v_mfma_f32_16x16x32_bf16 v[32:35], a[156:159], v[92:95], v[32:35]\ns_cmp_eq_u64 vcc, 0\ns_cselect_b32 s61, s55, -2147483648\nv_mfma_f32_16x16x32_bf16 v[40:43], a[164:167], v[92:95], v[40:43]\nbuffer_load_dwordx4 v[92:95], v0, %[rs], s60 offen offset:2048 sc1\nv_mfma_f32_16x16x32_bf16 v[16:19], a[168:171], v[96:99], v[16:19]\nv_mfma_f32_16x16x32_bf16 v[24:27], a[176:179], v[96:99], v[24:27]\ns_waitcnt lgkmcnt(1)\n" \
"v_mfma_f32_16x16x32_bf16 v[32:35], v[112:115], v[96:99], v[32:35]\nv_mfma_f32_16x16x32_bf16 v[40:43], a[184:187], v[96:99], v[40:43]\ns_add_u32 s60, s61, 0\nbuffer_load_dwordx4 v[96:99], v0, %[rs], s60 offen offset:1024 sc1\nv_mfma_f32_16x16x32_bf16 v[16:19], a[172:175], v[100:103], v[16:19]\nds_read_b128 v[112:115], v1 offset:8192\nv_mfma_f32_16x16x32_bf16 v[24:27], a[180:183], v[100:103], v[24:27]\ns_waitcnt lgkmcnt(1)\n" \
"v_mfma_f32_16x16x32_bf16 v[32:35], v[116:119], v[100:103], v[32:35]\nv_mfma_f32_16x16x32_bf16 v[40:43], a[188:191], v[100:103], v[40:43]\nbuffer_load_dwordx4 v[100:103], v0, %[rs], s60 offen offset:3072 sc1\nv_mfma_f32_16x16x32_bf16 v[16:19], a[192:195], v[104:107], v[16:19]\nds_read_b128 v[116:119], v1 offset:9216\nv_mfma_f32_16x16x32_bf16 v[24:27], a[200:203], v[104:107], v[24:27]\nv_mfma_f32_16x16x32_bf16 v[32:35], a[208:211], v[104:107], v[32:35]\nv_mfma_f32_16x16x32_bf16 v[40:43], a[216:219], v[104:107], v[40:43]\n" \
"s_add_u32 s60, s61, 4096\nbuffer_load_dwordx4 v[104:107], v0, %[rs], s60 offen offset:1024 sc1\nv_mfma_f32_16x16x32_bf16 v[16:19], a[196:199], v[108:111], v[16:19]\nv_mfma_f32_16x16x32_bf16 v[24:27], a[204:207], v[108:111], v[24:27]\nv_mfma_f32_16x16x32_bf16 v[32:35], a[212:215], v[108:111], v[32:35]\nv_mfma_f32_16x16x32_bf16 v[40:43], a[220:223], v[108:111], v[40:43]\ns_waitcnt vmcnt(14)\ns_waitcnt lgkmcnt(1)\n" \
"buffer_load_dwordx4 v[108:111], v0, %[rs], s60 offen offset:3072 sc1\nv_mfma_f32_16x16x32_bf16 v[16:19], v[112:115], v[48:51], v[16:19]\nv_mfma_f32_16x16x32_bf16 v[24:27], a[224:227], v[48:51], v[24:27]\nv_mfma_f32_16x16x32_bf16 v[32:35], a[232:235], v[48:51], v[32:35]\nv_mfma_f32_16x16x32_bf16 v[40:43], a[240:243], v[48:51], v[40:43]\ns_add_u32 s60, s61, 16384\ns_waitcnt vmcnt(14)\ns_waitcnt lgkmcnt(0)\n" \
"buffer_load_dwordx4 v[48:51], v0, %[rs], s60 offen offset:1024 sc1\nv_mfma_f32_16x16x32_bf16 v[16:19], v[116:119], v[52:55], v[16:19]\nds_read_b128 v[112:115], v1 offset:10240\nv_mfma_f32_16x16x32_bf16 v[24:27], a[228:231], v[52:55], v[24:27]\nv_mfma_f32_16x16x32_bf16 v[32:35], a[236:239], v[52:55], v[32:35]\nv_mfma_f32_16x16x32_bf16 v[40:43], a[244:247], v[52:55], v[40:43]\ns_waitcnt vmcnt(14)\nbuffer_load_dwordx4 v[52:55], v0, %[rs], s60 offen offset:3072 sc1\n" \
"v_mfma_f32_16x16x32_bf16 v[16:19], a[248:251], v[56:59], v[16:19]\nds_read_b128 v[116:119], v1 offset:11264\nv_mfma_f32_16x16x32_bf16 v[24:27], v[128:131], v[56:59], v[24:27]\nv_mfma_f32_16x16x32_bf16 v[32:35], v[136:139], v[56:59], v[32:35]\nv_mfma_f32_16x16x32_bf16 v[40:43], v[144:147], v[56:59], v[40:43]\ns_add_u32 s60, s61, 20480\ns_waitcnt vmcnt(14)\nbuffer_load_dwordx4 v[56:59], v0, %[rs], s60 offen offset:1024 sc1\n" \
"v_mfma_f32_16x16x32_bf16 v[16:19], a[252:255], v[60:63], v[16:19]\nv_mfma_f32_16x16x32_bf16 v[24:27], v[132:135], v[60:63], v[24:27]\nv_mfma_f32_16x16x32_bf16 v[32:35], v[140:143], v[60:63], v[32:35]\nv_mfma_f32_16x16x32_bf16 v[40:43], v[148:151], v[60:63], v[40:43]\ns_waitcnt vmcnt(14)\nbuffer_load_dwordx4 v[60:63], v0, %[rs], s60 offen offset:3072 sc1\nv_mfma_f32_16x16x32_bf16 v[16:19], v[152:155], v[64:67], v[16:19]\nv_mfma_f32_16x16x32_bf16 v[24:27], v[160:163], v[64:67], v[24:27]\n" \
"s_waitcnt lgkmcnt(1)\nv_mfma_f32_16x16x32_bf16 v[32:35], v[112:115], v[64:67], v[32:35]\nv_mfma_f32_16x16x32_bf16 v[40:43], v[168:171], v[64:67], v[40:43]\ns_add_u32 s60, s61, 32768\ns_waitcnt vmcnt(14)\nbuffer_load_dwordx4 v[64:67], v0, %[rs], s60 offen offset:1024 sc1\nv_mfma_f32_16x16x32_bf16 v[16:19], v[156:159], v[68:71], v[16:19]\nds_read_b128 v[112:115], v1 offset:12288\n" \
"v_mfma_f32_16x16x32_bf16 v[24:27], v[164:167], v[68:71], v[24:27]\ns_waitcnt lgkmcnt(1)\nv_mfma_f32_16x16x32_bf16 v[32:35], v[116:119], v[68:71], v[32:35]\nv_mfma_f32_16x16x32_bf16 v[40:43], v[172:175], v[68:71], v[40:43]\ns_waitcnt vmcnt(14)\nbuffer_load_dwordx4 v[68:71], v0, %[rs], s60 offen offset:3072 sc1\nv_mfma_f32_16x16x32_bf16 v[16:19], v[176:179], v[72:75], v[16:19]\nds_read_b128 v[116:119], v1 offset:13312\n" \
"v_mfma_f32_16x16x32_bf16 v[24:27], v[184:187], v[72:75], v[24:27]\nv_mfma_f32_16x16x32_bf16 v[32:35], v[192:195], v[72:75], v[32:35]\nv_mfma_f32_16x16x32_bf16 v[40:43], v[200:203], v[72:75], v[40:43]\ns_add_u32 s60, s61, 36864\ns_waitcnt vmcnt(14)\nbuffer_load_dwordx4 v[72:75], v0, %[rs], s60 offen offset:1024 sc1\nv_mfma_f32_16x16x32_bf16 v[16:19], v[180:183], v[76:79], v[16:19]\nds_read_b128 v[120:123], v1 offset:14336\n" \
"v_mfma_f32_16x16x32_bf16 v[24:27], v[188:191], v[76:79], v[24:27]\nv_mfma_f32_16x16x32_bf16 v[32:35], v[196:199], v[76:79], v[32:35]\nv_mfma_f32_16x16x32_bf16 v[40:43], v[204:207], v[76:79], v[40:43]\ns_waitcnt vmcnt(14)\ns_waitcnt lgkmcnt(2)\nbuffer_load_dwordx4 v[76:79], v0, %[rs], s60 offen offset:3072 sc1\nv_mfma_f32_16x16x32_bf16 v[16:19], v[112:115], v[80:83], v[16:19]\nds_read_b128 v[124:127], v1 offset:15360\n" \
"v_mfma_f32_16x16x32_bf16 v[24:27], v[208:211], v[80:83], v[24:27]\nv_mfma_f32_16x16x32_bf16 v[32:35], v[216:219], v[80:83], v[32:35]\nv_mfma_f32_16x16x32_bf16 v[40:43], v[224:227], v[80:83], v[40:43]\ns_add_u32 s60, s61, 49152\ns_waitcnt vmcnt(14)\ns_waitcnt lgkmcnt(2)\nbuffer_load_dwordx4 v[80:83], v0, %[rs], s60 offen offset:1024 sc1\nv_mfma_f32_16x16x32_bf16 v[16:19], v[116:119], v[84:87], v[16:19]\n" \
"v_mfma_f32_16x16x32_bf16 v[24:27], v[212:215], v[84:87], v[24:27]\nv_mfma_f32_16x16x32_bf16 v[32:35], v[220:223], v[84:87], v[32:35]\nv_mfma_f32_16x16x32_bf16 v[40:43], v[228:231], v[84:87], v[40:43]\ns_waitcnt vmcnt(14)\nbuffer_load_dwordx4 v[84:87], v0, %[rs], s60 offen offset:3072 sc1\nv_mfma_f32_16x16x32_bf16 v[16:19], v[232:235], v[88:91], v[16:19]\ns_waitcnt lgkmcnt(1)\nv_mfma_f32_16x16x32_bf16 v[24:27], v[120:123], v[88:91], v[24:27]\n" \
"v_mfma_f32_16x16x32_bf16 v[32:35], v[240:243], v[88:91], v[32:35]\nv_mfma_f32_16x16x32_bf16 v[40:43], v[248:251], v[88:91], v[40:43]\ns_add_u32 s60, s61, 53248\ns_waitcnt vmcnt(14)\nbuffer_load_dwordx4 v[88:91], v0, %[rs], s60 offen offset:1024 sc1\nv_mfma_f32_16x16x32_bf16 v[16:19], v[236:239], v[92:95], v[16:19]\ns_waitcnt lgkmcnt(0)\nds_read_b128 v[120:123], v1 offset:0\n" \
"v_mfma_f32_16x16x32_bf16 v[24:27], v[124:127], v[92:95], v[24:27]\nv_mfma_f32_16x16x32_bf16 v[32:35], v[244:247], v[92:95], v[32:35]\nv_mfma_f32_16x16x32_bf16 v[40:43], v[252:255], v[92:95], v[40:43]\nbuffer_load_dwordx4 v[92:95], v0, %[rs], s60 offen offset:3072 sc1\ns_cbranch_vccnz .Ldd_slow%=\n.Ldd_slowr%=:\ns_waitcnt vmcnt(15)\nv_mfma_f32_16x16x32_bf16 v[20:23], a[0:3], v[96:99], 0\n" \
"ds_read_b128 v[124:127], v1 offset:1024\nv_mfma_f32_16x16x32_bf16 v[28:31], a[8:11], v[96:99], 0\nv_mfma_f32_16x16x32_bf16 v[36:39], a[16:19], v[96:99], 0\nv_mfma_f32_16x16x32_bf16 v[44:47], a[24:27], v[96:99], 0\ns_add_u32 s60, s55, 65536\ns_waitcnt vmcnt(14)\nbuffer_load_dwordx4 v[96:99], v0, %[rs], s60 offen offset:1024 sc1\nv_mfma_f32_16x16x32_bf16 v[20:23], a[4:7], v[100:103], v[20:23]\n" \
"v_mfma_f32_16x16x32_bf16 v[28:31], a[12:15], v[100:103], v[28:31]\nv_mfma_f32_16x16x32_bf16 v[36:39], a[20:23], v[100:103], v[36:39]\nv_mfma_f32_16x16x32_bf16 v[44:47], a[28:31], v[100:103], v[44:47]\ns_waitcnt vmcnt(14)\nbuffer_load_dwordx4 v[100:103], v0, %[rs], s60 offen offset:3072 sc1\nv_mfma_f32_16x16x32_bf16 v[20:23], a[32:35], v[104:107], v[20:23]\ns_waitcnt lgkmcnt(1)\nv_mfma_f32_16x16x32_bf16 v[28:31], v[120:123], v[104:107], v[28:31]\n" \
"ds_write_b128 v2, v[16:19] offset:0\nv_mfma_f32_16x16x32_bf16 v[36:39], a[40:43], v[104:107], v[36:39]\nv_mfma_f32_16x16x32_bf16 v[44:47], a[48:51], v[104:107], v[44:47]\ns_add_u32 s60, s55, 69632\ns_waitcnt vmcnt(14)\nbuffer_load_dwordx4 v[104:107], v0, %[rs], s60 offen offset:1024 sc1\nv_mfma_f32_16x16x32_bf16 v[20:23], a[36:39], v[108:111], v[20:23]\ns_waitcnt lgkmcnt(1)\n" \
"ds_read_b128 v[120:123], v1 offset:2048\nv_mfma_f32_16x16x32_bf16 v[28:31], v[124:127], v[108:111], v[28:31]\nv_mfma_f32_16x16x32_bf16 v[36:39], a[44:47], v[108:111], v[36:39]\nv_mfma_f32_16x16x32_bf16 v[44:47], a[52:55], v[108:111], v[44:47]\ns_waitcnt vmcnt(14)\nbuffer_load_dwordx4 v[108:111], v0, %[rs], s60 offen offset:3072 sc1\nv_mfma_f32_16x16x32_bf16 v[20:23], a[56:59], v[48:51], v[20:23]\nds_read_b128 v[124:127], v1 offset:3072\n" \
"v_mfma_f32_16x16x32_bf16 v[28:31], a[64:67], v[48:51], v[28:31]\nds_write_b128 v2, v[24:27] offset:2048\nv_mfma_f32_16x16x32_bf16 v[36:39], a[72:75], v[48:51], v[36:39]\nv_mfma_f32_16x16x32_bf16 v[44:47], a[80:83], v[48:51], v[44:47]\ns_add_u32 s60, s55, 81920\ns_waitcnt vmcnt(14)\nbuffer_load_dwordx4 v[48:51], v0, %[rs], s60 offen offset:1024 sc1\nv_mfma_f32_16x16x32_bf16 v[20:23], a[60:63], v[52:55], v[20:23]\n" \
"v_mfma_f32_16x16x32_bf16 v[28:31], a[68:71], v[52:55], v[28:31]\nv_mfma_f32_16x16x32_bf16 v[36:39], a[76:79], v[52:55], v[36:39]\nv_mfma_f32_16x16x32_bf16 v[44:47], a[84:87], v[52:55], v[44:47]\ns_waitcnt vmcnt(14)\nbuffer_load_dwordx4 v[52:55], v0, %[rs], s60 offen offset:3072 sc1\nv_mfma_f32_16x16x32_bf16 v[20:23], a[88:91], v[56:59], v[20:23]\nv_mfma_f32_16x16x32_bf16 v[28:31], a[96:99], v[56:59], v[28:31]\nds_write_b128 v2, v[32:35] offset:4096\n" \
"v_mfma_f32_16x16x32_bf16 v[36:39], a[104:107], v[56:59], v[36:39]\ns_waitcnt lgkmcnt(3)\nv_mfma_f32_16x16x32_bf16 v[44:47], v[120:123], v[56:59], v[44:47]\ns_add_u32 s60, s55, 86016\ns_waitcnt vmcnt(14)\nbuffer_load_dwordx4 v[56:59], v0, %[rs], s60 offen offset:1024 sc1\nv_mfma_f32_16x16x32_bf16 v[20:23], a[92:95], v[60:63], v[20:23]\nds_read_b128 v[120:123], v1 offset:4096\n" \
"v_mfma_f32_16x16x32_bf16 v[28:31], a[100:103], v[60:63], v[28:31]\nv_mfma_f32_16x16x32_bf16 v[36:39], a[108:111], v[60:63], v[36:39]\ns_waitcnt lgkmcnt(3)\nv_mfma_f32_16x16x32_bf16 v[44:47], v[124:127], v[60:63], v[44:47]\ns_waitcnt vmcnt(14)\nbuffer_load_dwordx4 v[60:63], v0, %[rs], s60 offen offset:3072 sc1\nv_mfma_f32_16x16x32_bf16 v[20:23], a[112:115], v[64:67], v[20:23]\nds_read_b128 v[124:127], v1 offset:5120\n" \
"v_mfma_f32_16x16x32_bf16 v[28:31], a[120:123], v[64:67], v[28:31]\nds_write_b128 v2, v[40:43] offset:6144\nv_mfma_f32_16x16x32_bf16 v[36:39], a[128:131], v[64:67], v[36:39]\nv_mfma_f32_16x16x32_bf16 v[44:47], a[136:139], v[64:67], v[44:47]\ns_add_u32 s60, s55, 98304\ns_waitcnt vmcnt(14)\nbuffer_load_dwordx4 v[64:67], v0, %[rs], s60 offen offset:1024 sc1\nv_mfma_f32_16x16x32_bf16 v[20:23], a[116:119], v[68:71], v[20:23]\n" \
"ds_read_b128 v[112:115], v1 offset:6144\nv_mfma_f32_16x16x32_bf16 v[28:31], a[124:127], v[68:71], v[28:31]\nv_mfma_f32_16x16x32_bf16 v[36:39], a[132:135], v[68:71], v[36:39]\nv_mfma_f32_16x16x32_bf16 v[44:47], a[140:143], v[68:71], v[44:47]\ns_waitcnt vmcnt(14)\nbuffer_load_dwordx4 v[68:71], v0, %[rs], s60 offen offset:3072 sc1\nv_mfma_f32_16x16x32_bf16 v[20:23], a[144:147], v[72:75], v[20:23]\ns_waitcnt lgkmcnt(3)\n" \
"ds_read_b128 v[116:119], v1 offset:7168\nv_mfma_f32_16x16x32_bf16 v[28:31], v[120:123], v[72:75], v[28:31]\nv_mfma_f32_16x16x32_bf16 v[36:39], a[152:155], v[72:75], v[36:39]\nv_mfma_f32_16x16x32_bf16 v[44:47], a[160:163], v[72:75], v[44:47]\ns_add_u32 s60, s55, 102400\ns_waitcnt vmcnt(14)\nbuffer_load_dwordx4 v[72:75], v0, %[rs], s60 offen offset:1024 sc1\nv_mfma_f32_16x16x32_bf16 v[20:23], a[148:151], v[76:79], v[20:23]\n" \
"s_waitcnt lgkmcnt(3)\nv_mfma_f32_16x16x32_bf16 v[28:31], v[124:127], v[76:79], v[28:31]\nv_mfma_f32_16x16x32_bf16 v[36:39], a[156:159], v[76:79], v[36:39]\nv_mfma_f32_16x16x32_bf16 v[44:47], a[164:167], v[76:79], v[44:47]\ns_waitcnt vmcnt(14)\nbuffer_load_dwordx4 v[76:79], v0, %[rs], s60 offen offset:3072 sc1\nv_mfma_f32_16x16x32_bf16 v[20:23], a[168:171], v[80:83], v[20:23]\nv_mfma_f32_16x16x32_bf16 v[28:31], a[176:179], v[80:83], v[28:31]\n" \
"s_waitcnt lgkmcnt(1)\nv_mfma_f32_16x16x32_bf16 v[36:39], v[112:115], v[80:83], v[36:39]\nv_mfma_f32_16x16x32_bf16 v[44:47], a[184:187], v[80:83], v[44:47]\ns_waitcnt vmcnt(14)\nbuffer_load_dwordx4 v[12:15], v4, %[rs], s41 offen sc1\nv_mfma_f32_16x16x32_bf16 v[20:23], a[172:175], v[84:87], v[20:23]\nds_read_b128 v[112:115], v1 offset:8192\nv_mfma_f32_16x16x32_bf16 v[28:31], a[180:183], v[84:87], v[28:31]\n" \
"s_waitcnt lgkmcnt(1)\nv_mfma_f32_16x16x32_bf16 v[36:39], v[116:119], v[84:87], v[36:39]\nv_mfma_f32_16x16x32_bf16 v[44:47], a[188:191], v[84:87], v[44:47]\ns_waitcnt vmcnt(14)\nv_mfma_f32_16x16x32_bf16 v[20:23], a[192:195], v[88:91], v[20:23]\nds_read_b128 v[116:119], v1 offset:9216\nv_mfma_f32_16x16x32_bf16 v[28:31], a[200:203], v[88:91], v[28:31]\nv_mfma_f32_16x16x32_bf16 v[36:39], a[208:211], v[88:91], v[36:39]\n" \
"v_mfma_f32_16x16x32_bf16 v[44:47], a[216:219], v[88:91], v[44:47]\ns_waitcnt vmcnt(13)\nv_mfma_f32_16x16x32_bf16 v[20:23], a[196:199], v[92:95], v[20:23]\nv_mfma_f32_16x16x32_bf16 v[28:31], a[204:207], v[92:95], v[28:31]\nv_mfma_f32_16x16x32_bf16 v[36:39], a[212:215], v[92:95], v[36:39]\nv_mfma_f32_16x16x32_bf16 v[44:47], a[220:223], v[92:95], v[44:47]\ns_waitcnt vmcnt(12)\ns_waitcnt lgkmcnt(1)\n" \
"v_mfma_f32_16x16x32_bf16 v[20:23], v[112:115], v[96:99], v[20:23]\nv_mfma_f32_16x16x32_bf16 v[28:31], a[224:227], v[96:99], v[28:31]\nv_mfma_f32_16x16x32_bf16 v[36:39], a[232:235], v[96:99], v[36:39]\nv_mfma_f32_16x16x32_bf16 v[44:47], a[240:243], v[96:99], v[44:47]\ns_waitcnt vmcnt(11)\ns_waitcnt lgkmcnt(0)\nv_mfma_f32_16x16x32_bf16 v[20:23], v[116:119], v[100:103], v[20:23]\nds_read_b128 v[112:115], v1 offset:10240\n" \
"v_mfma_f32_16x16x32_bf16 v[28:31], a[228:231], v[100:103], v[28:31]\nv_mfma_f32_16x16x32_bf16 v[36:39], a[236:239], v[100:103], v[36:39]\nv_mfma_f32_16x16x32_bf16 v[44:47], a[244:247], v[100:103], v[44:47]\ns_waitcnt vmcnt(10)\nv_mfma_f32_16x16x32_bf16 v[20:23], a[248:251], v[104:107], v[20:23]\nds_read_b128 v[116:119], v1 offset:11264\nv_mfma_f32_16x16x32_bf16 v[28:31], v[128:131], v[104:107], v[28:31]\nv_mfma_f32_16x16x32_bf16 v[36:39], v[136:139], v[104:107], v[36:39]\n" \
"v_mfma_f32_16x16x32_bf16 v[44:47], v[144:147], v[104:107], v[44:47]\ns_waitcnt vmcnt(9)\nv_mfma_f32_16x16x32_bf16 v[20:23], a[252:255], v[108:111], v[20:23]\nv_mfma_f32_16x16x32_bf16 v[28:31], v[132:135], v[108:111], v[28:31]\nv_mfma_f32_16x16x32_bf16 v[36:39], v[140:143], v[108:111], v[36:39]\nv_mfma_f32_16x16x32_bf16 v[44:47], v[148:151], v[108:111], v[44:47]\ns_waitcnt vmcnt(8)\nv_mfma_f32_16x16x32_bf16 v[20:23], v[152:155], v[48:51], v[20:23]\n" \
"v_mfma_f32_16x16x32_bf16 v[28:31], v[160:163], v[48:51], v[28:31]\ns_waitcnt lgkmcnt(1)\nv_mfma_f32_16x16x32_bf16 v[36:39], v[112:115], v[48:51], v[36:39]\nv_mfma_f32_16x16x32_bf16 v[44:47], v[168:171], v[48:51], v[44:47]\ns_waitcnt vmcnt(7)\nv_mfma_f32_16x16x32_bf16 v[20:23], v[156:159], v[52:55], v[20:23]\nds_read_b128 v[112:115], v1 offset:12288\nv_mfma_f32_16x16x32_bf16 v[28:31], v[164:167], v[52:55], v[28:31]\n" \
"s_waitcnt lgkmcnt(1)\nv_mfma_f32_16x16x32_bf16 v[36:39], v[116:119], v[52:55], v[36:39]\nv_mfma_f32_16x16x32_bf16 v[44:47], v[172:175], v[52:55], v[44:47]\ns_waitcnt vmcnt(6)\nv_mfma_f32_16x16x32_bf16 v[20:23], v[176:179], v[56:59], v[20:23]\nds_read_b128 v[116:119], v1 offset:13312\nv_mfma_f32_16x16x32_bf16 v[28:31], v[184:187], v[56:59], v[28:31]\nv_mfma_f32_16x16x32_bf16 v[36:39], v[192:195], v[56:59], v[36:39]\n" \
"v_mfma_f32_16x16x32_bf16 v[44:47], v[200:203], v[56:59], v[44:47]\ns_waitcnt vmcnt(5)\nv_mfma_f32_16x16x32_bf16 v[20:23], v[180:183], v[60:63], v[20:23]\nds_read_b128 v[120:123], v1 offset:14336\nv_mfma_f32_16x16x32_bf16 v[28:31], v[188:191], v[60:63], v[28:31]\nv_mfma_f32_16x16x32_bf16 v[36:39], v[196:199], v[60:63], v[36:39]\nv_mfma_f32_16x16x32_bf16 v[44:47], v[204:207], v[60:63], v[44:47]\ns_waitcnt vmcnt(4)\n" \
"s_waitcnt lgkmcnt(2)\nv_mfma_f32_16x16x32_bf16 v[20:23], v[112:115], v[64:67], v[20:23]\nds_read_b128 v[124:127], v1 offset:15360\nv_mfma_f32_16x16x32_bf16 v[28:31], v[208:211], v[64:67], v[28:31]\nv_mfma_f32_16x16x32_bf16 v[36:39], v[216:219], v[64:67], v[36:39]\nv_mfma_f32_16x16x32_bf16 v[44:47], v[224:227], v[64:67], v[44:47]\ns_waitcnt vmcnt(3)\ns_waitcnt lgkmcnt(2)\n" \
"v_mfma_f32_16x16x32_bf16 v[20:23], v[116:119], v[68:71], v[20:23]\nv_mfma_f32_16x16x32_bf16 v[28:31], v[212:215], v[68:71], v[28:31]\nv_mfma_f32_16x16x32_bf16 v[36:39], v[220:223], v[68:71], v[36:39]\nv_mfma_f32_16x16x32_bf16 v[44:47], v[228:231], v[68:71], v[44:47]\ns_waitcnt vmcnt(2)\nv_mfma_f32_16x16x32_bf16 v[20:23], v[232:235], v[72:75], v[20:23]\ns_waitcnt lgkmcnt(1)\nv_mfma_f32_16x16x32_bf16 v[28:31], v[120:123], v[72:75], v[28:31]\n" \
"v_mfma_f32_16x16x32_bf16 v[36:39], v[240:243], v[72:75], v[36:39]\nv_mfma_f32_16x16x32_bf16 v[44:47], v[248:251], v[72:75], v[44:47]\ns_waitcnt vmcnt(1)\nv_mfma_f32_16x16x32_bf16 v[20:23], v[236:239], v[76:79], v[20:23]\ns_waitcnt vmcnt(0)\ns_waitcnt lgkmcnt(0)\nv_lshlrev_b32 v80, 16, v12\nv_and_b32 v81, 0xffff0000, v12\n" \
"v_mfma_f32_16x16x32_bf16 v[28:31], v[124:127], v[76:79], v[28:31]\nv_lshlrev_b32 v82, 16, v13\nv_and_b32 v83, 0xffff0000, v13\nv_mfma_f32_16x16x32_bf16 v[36:39], v[244:247], v[76:79], v[36:39]\nv_lshlrev_b32 v84, 16, v14\nv_and_b32 v85, 0xffff0000, v14\nv_mfma_f32_16x16x32_bf16 v[44:47], v[252:255], v[76:79], v[44:47]\ns_mul_i32 s44, s40, %[l]\n" \
"ds_write_b128 v2, v[20:23] offset:1024\nds_write_b128 v2, v[28:31] offset:3072\ns_nop 0\nds_write_b128 v2, v[36:39] offset:5120\ns_nop 2\nds_write_b128 v2, v[44:47] offset:7168\nv_lshlrev_b32 v86, 16, v15\nv_and_b32 v87, 0xffff0000, v15\n" \
"v_mov_b32 v9, s40\ns_cmp_eq_u32 s44, 0\ns_cselect_b32 s56, %[fd], -2147483648\ns_add_u32 s40, s40, 1\ns_waitcnt lgkmcnt(0)\ns_barrier\nds_read_b128 v[16:19], v3 offset:0\nds_read_b128 v[20:23], v3 offset:256\n" \
"ds_read_b128 v[24:27], v3 offset:8192\nds_read_b128 v[28:31], v3 offset:8448\nds_read_b128 v[32:35], v3 offset:16384\nds_read_b128 v[36:39], v3 offset:16640\nds_read_b128 v[40:43], v3 offset:24576\nds_read_b128 v[44:47], v3 offset:24832\ns_waitcnt lgkmcnt(6)\nv_pk_add_f32 v[16:17], v[16:17], v[80:81]\n" \
"v_pk_add_f32 v[18:19], v[18:19], v[82:83]\nv_pk_add_f32 v[20:21], v[20:21], v[84:85]\nv_pk_add_f32 v[22:23], v[22:23], v[86:87]\ns_waitcnt lgkmcnt(4)\nv_pk_add_f32 v[16:17], v[16:17], v[24:25]\nv_pk_add_f32 v[18:19], v[18:19], v[26:27]\nv_pk_add_f32 v[20:21], v[20:21], v[28:29]\nv_pk_add_f32 v[22:23], v[22:23], v[30:31]\n" \
"s_waitcnt lgkmcnt(2)\nv_pk_add_f32 v[16:17], v[16:17], v[32:33]\nv_pk_add_f32 v[18:19], v[18:19], v[34:35]\nv_pk_add_f32 v[20:21], v[20:21], v[36:37]\nv_pk_add_f32 v[22:23], v[22:23], v[38:39]\ns_waitcnt lgkmcnt(0)\nv_pk_add_f32 v[16:17], v[16:17], v[40:41]\nv_pk_add_f32 v[18:19], v[18:19], v[42:43]\n" \
"v_pk_add_f32 v[20:21], v[20:21], v[44:45]\nv_pk_add_f32 v[22:23], v[22:23], v[46:47]\nv_cvt_pk_bf16_f32 v12, v16, v17\nv_cvt_pk_bf16_f32 v13, v18, v19\nv_cvt_pk_bf16_f32 v14, v20, v21\nv_cvt_pk_bf16_f32 v15, v22, v23\nbuffer_store_dwordx4 v[12:15], v5, %[ro], s42 offen sc1\nv_pk_add_f32 v[24:25], v[16:17], v[18:19]\n" \
"v_pk_add_f32 v[26:27], v[20:21], v[22:23]\nv_pk_add_f32 v[24:25], v[24:25], v[26:27]\nv_add_f32 v24, v24, v25\ns_nop 1\nv_add_f32_dpp v25, v24, v24 row_ror:8 row_mask:0xf bank_mask:0xf\nv_add_f32_dpp v26, v24, v24 row_ror:8 row_mask:0xf bank_mask:0xf\ns_nop 1\nv_permlane16_swap_b32 v25, v26\n" \
"v_add_f32 v24, v25, v26\nv_add_f32 v25, v25, v26\ns_nop 1\nv_permlane32_swap_b32 v24, v25\nv_add_f32 v24, v24, v25\nv_mul_f32 v24, 0x3c800000, v24\nv_pk_add_f32 v[16:17], v[16:17], v[24:25] op_sel_hi:[1,0] neg_lo:[0,1] neg_hi:[0,1]\nv_pk_add_f32 v[18:19], v[18:19], v[24:25] op_sel_hi:[1,0] neg_lo:[0,1] neg_hi:[0,1]\n" \
"v_pk_add_f32 v[20:21], v[20:21], v[24:25] op_sel_hi:[1,0] neg_lo:[0,1] neg_hi:[0,1]\nv_pk_add_f32 v[22:23], v[22:23], v[24:25] op_sel_hi:[1,0] neg_lo:[0,1] neg_hi:[0,1]\nv_pk_mul_f32 v[28:29], v[16:17], v[16:17]\nv_pk_mul_f32 v[30:31], v[18:19], v[18:19]\nv_pk_fma_f32 v[28:29], v[20:21], v[20:21], v[28:29]\nv_pk_fma_f32 v[30:31], v[22:23], v[22:23], v[30:31]\nv_pk_add_f32 v[28:29], v[28:29], v[30:31]\nv_add_f32 v28, v28, v29\n" \
"s_nop 1\nv_add_f32_dpp v29, v28, v28 row_ror:8 row_mask:0xf bank_mask:0xf\nv_add_f32_dpp v30, v28, v28 row_ror:8 row_mask:0xf bank_mask:0xf\ns_nop 1\nv_permlane16_swap_b32 v29, v30\nv_add_f32 v28, v29, v30\nv_add_f32 v29, v29, v30\ns_nop 1\n" \
"v_permlane32_swap_b32 v28, v29\nv_add_f32 v28, v28, v29\nv_mov_b32 v25, v28\nbuffer_store_dwordx2 v[24:25], v6, %[rs], s41 offen sc1\nv_xor_b32 v2, 0x8000, v2\nv_xor_b32 v3, 0x8000, v3\ns_cmp_lt_u32 s40, 64\ns_cbranch_scc1 .Ldd_next%=\n" \
"s_waitcnt vmcnt(0)\nbuffer_store_dword v9, off, %[rs], s56 sc1\ns_branch .Ldd_end%=\n.Ldd_slow%=:\n.Ldd_w1%=:\nv_cmp_gt_i32 vcc, s58, v8\ns_cbranch_vccz .Ldd_w1go%=\ns_sleep 16\n" \
"buffer_load_dword v8, v7, %[rs], 0 offen sc1\ns_waitcnt vmcnt(0)\ns_add_u32 s43, s43, 1\ns_cmp_lt_u32 s43, 262144\ns_cbranch_scc1 .Ldd_w1%=\ns_branch .Ldd_abort%=\n.Ldd_w1go%=:\ns_add_u32 s60, s55, 0\n" \
"buffer_load_dwordx4 v[96:99], v0, %[rs], s60 offen offset:1024 sc1\nbuffer_load_dwordx4 v[100:103], v0, %[rs], s60 offen offset:3072 sc1\ns_add_u32 s60, s55, 4096\nbuffer_load_dwordx4 v[104:107], v0, %[rs], s60 offen offset:1024 sc1\nbuffer_load_dwordx4 v[108:111], v0, %[rs], s60 offen offset:3072 sc1\ns_add_u32 s60, s55, 16384\nbuffer_load_dwordx4 v[48:51], v0, %[rs], s60 offen offset:1024 sc1\nbuffer_load_dwordx4 v[52:55], v0, %[rs], s60 offen offset:3072 sc1\n" \
"s_add_u32 s60, s55, 20480\nbuffer_load_dwordx4 v[56:59], v0, %[rs], s60 offen offset:1024 sc1\nbuffer_load_dwordx4 v[60:63], v0, %[rs], s60 offen offset:3072 sc1\ns_add_u32 s60, s55, 32768\nbuffer_load_dwordx4 v[64:67], v0, %[rs], s60 offen offset:1024 sc1\nbuffer_load_dwordx4 v[68:71], v0, %[rs], s60 offen offset:3072 sc1\ns_add_u32 s60, s55, 36864\nbuffer_load_dwordx4 v[72:75], v0, %[rs], s60 offen offset:1024 sc1\n" \
"buffer_load_dwordx4 v[76:79], v0, %[rs], s60 offen offset:3072 sc1\ns_add_u32 s60, s55, 49152\nbuffer_load_dwordx4 v[80:83], v0, %[rs], s60 offen offset:1024 sc1\nbuffer_load_dwordx4 v[84:87], v0, %[rs], s60 offen offset:3072 sc1\ns_add_u32 s60, s55, 53248\nbuffer_load_dwordx4 v[88:91], v0, %[rs], s60 offen offset:1024 sc1\nbuffer_load_dwordx4 v[92:95], v0, %[rs], s60 offen offset:3072 sc1\ns_branch .Ldd_slowr%=\n" \
".Ldd_abort%=:\ns_mov_b64 exec, -1\nv_mov_b32 v16, 0\ns_movk_i32 s44, 6400\nbuffer_store_dword v16, off, %[rs], s44 sc1\n.Ldd_end%=:\ns_waitcnt vmcnt(0) lgkmcnt(0)\n" \
""

#define V10(p, x) p #x "0", p #x "1", p #x "2", p #x "3", p #x "4", p #x "5", p #x "6", p #x "7", p #x "8", p #x "9"
#define V256(p) V10(p, ), V10(p, 1), V10(p, 2), V10(p, 3), V10(p, 4), V10(p, 5), V10(p, 6), V10(p, 7), V10(p, 8), V10(p, 9), V10(p, 10), V10(p, 11), V10(p, 12), V10(p, 13), V10(p, 14), V10(p, 15), V10(p, 16), \
    V10(p, 17), V10(p, 18), V10(p, 19), V10(p, 20), V10(p, 21), V10(p, 22), V10(p, 23), V10(p, 24), p "250", p "251", p "252", p "253", p "254", p "255"
#define CLOB "memory", "vcc", "scc", V256("v"), V256("a"), V10("s", 4), V10("s", 5), V10("s", 6), V10("s", 7), V10("s", 8)
template <int N> DEVI void table(lds_char* p, const unsigned (&t)[N]) {
#pragma unroll
    for (int i = 0; i < N; ++i) *(l1*)(p + i * 256) = t[i];
    WL0;
}
DEVI unsigned prof_at(int cu, int w) { return O_PROF + (262144 + cu * 2560 + w * 8) * 4; }
DEVI unsigned sp_idx(int lane) { return (lane & 2) ? 0xEEEEu : 0x4444u; }
DEVI int sp_row(int r, int hf) { return 4 * (r >> 1) + 2 * hf + (r & 1); }

DEVI void qk_role(const Args& a, const int w, lds_char* smem, int l, int h) {
    const int lane = lane_id(), g = lane >> 4, n = lane & 15, b = n & 3, p = n >> 2;
    const unsigned l0 = (unsigned)(size_t)smem, tb = l0 + w * 12288, lw = l0 + 55296 + w * 24576, ls = l * SD, IMG = l0 + 32768, MIM = l0 + 49152;
    const unsigned xw = (w >> 1) * 16384 + (w & 1) * 4096, oc = (h >> 3) * 16384 + ls, tr = (8 * g + p) * 128 + b * 16;
    const unsigned cm[4] = {4 * g > n ? 0xF149F2CAu : 0u, 4 * g + 1 > n ? 0xF149F2CAu : 0u, 4 * g + 2 > n ? 0xF149F2CAu : 0u, 4 * g + 3 > n ? 0xF149F2CAu : 0u};
    const unsigned t[19] = {
        l0 + lane * 16, lw + lane * 16, l0 + (g >> 1) * 1024 + (g & 1) * 512 + n * 16, O_E1 + ls + xw + lane * 16, sp_idx(lane),
        IMG + (w >> 1) * 8192 + ((w & 1) * 8 + 2 * g) * 512 + n * 16,
        MIM + (w * 8 + 2 * p) * 144 + g * 32 + b * 4,
        IMG + g * 512 + (w ? n * 16 : 8192 + (16 * (b >> 1) + 8 * (p >> 1) + 2 * (p & 1) + (b & 1)) * 16),
        MIM + g * 144 + (n & 7) * 16, MIM + 2304 + g * 144 + (2 * p + (b & 1)) * 16,
        w ? O_PT + (h >> 3) * 32768 + ls + lane * 16 + (w == 1 ? 16384 + ((h & 7) >> 1) * 1024 + (h & 1) * 8 : (h & 7) * 1024)
          : O_PM + oc + ((h >> 1) & 3) * 1024 + ((2 * (h & 1) + (g >> 1)) * 16 + sp_row(n & 7, g & 1)) * 16 + (n >> 3) * 8,
        w ? O_PT + (h >> 3) * 32768 + ls + 20480 + ((h >> 2) & 1) * 2048 + (w - 1) * 1024 + (h & 3) * 256 + n * 16 + g * 4
          : n >> 3 ? OOB : O_PM + oc + 4096 + ((h & 7) >> 1) * 256 + sp_row(n & 7, g & 1) * 16 + (h & 1) * 8 + (g >> 1) * 4,
        w == 3 ? (l ? F_X + SD + p * 128 + b * 4 : F_X0 + b * 4) : cm[0], cm[1], cm[2], cm[3],
        (lane >> 3) * 8192u + (lane & 7) * 16, tb + tr, lw + tr};
    table(smem + w * 12288 + lane * 4, t);
    asm volatile(QK_ASM : : [ws] "s"(a.ws), [sz] "n"(WS_RS), [rw] "s"(mkrs(w < 2 ? a.Wq : a.Wk, 8u << 20)), [wo] "s"(h * 256 + (w & 1) * 128), [tb] "s"(tb), [lw] "s"(lw), [l0] "s"(l0), [w] "s"(w),
        [xn] "s"(O_XN + ls), [xw] "s"(xw), [fs] "s"(l ? 0 : 128), [sg] "s"(F_Y0 + 4096 + ls + h * 128 + 16), [pr] "s"(prof_at(l * 80 + h, w)) : CLOB, "s36", "s37", "s38", "s39");
    __builtin_amdgcn_endpgm();
}
DEVI void vu_role(const Args& a, const int w, lds_char* smem, int l, int h) {
    const int lane = lane_id(), g = lane >> 4, n = lane & 15, b = n & 3, p = n >> 2;
    const unsigned l0 = (unsigned)(size_t)smem, tb = l0 + w * 12288, lw = l0 + 61440 + w * 24576, ls = l * SD;
    const unsigned xw = (w >> 1) * 16384 + (w & 1) * 4096, tv = (8 * g + p) * 128 + b * 16 + (w & 1) * 64, to = (8 * (g >> 1) + 4 * (p & 1) + 2 * (g & 1)) * 128 + (p >> 1) * 32 + b * 8;
    const unsigned t[18] = {
        l0 + lane * 16, lw + lane * 16, l0 + (g >> 1) * 1024 + (g & 1) * 512 + n * 16, O_E1 + ls + xw + lane * 16, sp_idx(lane),
        l0 + 57344 + w * 1024 + p * 256 + g * 32 + b * 4,
        O_UM + ls + (4 * w + (g & 1)) * 65536 + (h >> 3) * 16384 + (g >> 1) * 2048 + (h & 7) * 256 + n * 16,
        sd(O_Y + h * 131072 + ls) + w * 32768 + (g >> 1) * 1024 + (g & 1) * 256 + n * 16,
        l ? F_X + SD + p * 128 + b * 4 : F_X0 + b * 4, l0 + 49152 + w * 2048 + lane * 16, sp_idx(lane >> 2), l0 + g * 256 + (n & 7) * 16,
        (lane >> 3) * 8192u + (lane & 7) * 16, (lane >> 3) * 2048u + (lane & 7) * 16, tb + tv - (w & 1) * 12288, lw + tv - (w & 1) * 24576, tb + to, lw + to};
    table(smem + w * 12288 + lane * 4, t);
    asm volatile(VU_ASM : : [ws] "s"(a.ws), [sz] "n"(WS_RS), [rw] "s"(mkrs(a.Wv, 8u << 20)), [ro] "s"(mkrs(a.Wo, 8u << 20)), [wo] "s"(h * 256 + (w >> 1) * 128 + (w & 1) * 262144), [w2] "s"(h * 262144 + w * 512), [tb] "s"(tb), [lw] "s"(lw), [w] "s"(w),
        [xn] "s"(O_XN + ls), [xw] "s"(xw), [fs] "s"(l ? 0 : 128), [xt] "s"(w * 16384 - xw), [fy] "s"(F_Y0 + ls + h * 128 + w * 4), [sg] "s"(F_Y0 + ls + h * 128 + 16), [pr] "s"(prof_at(l * 80 + 32 + h, w) - 128) : CLOB, "s36", "s37", "s38", "s39");
    __builtin_amdgcn_endpgm();
}
DEVI void rd_role(const Args& a, const int w, lds_char* smem, int l, int c) {
    const int lane = lane_id(), g = lane >> 4, n = lane & 15, r = 2 * (n >> 2) + (n & 1), hf = (n >> 1) & 1;
    const rsrc_t rs = mkrs(a.ws, WS_RS);
    const unsigned l0 = (unsigned)(size_t)smem, tb = l0 + 65536 + w * 4096, ls = l * SD, vp = O_E1 + ls + (c >> 3) * 16384 + (c & 7) * 1024 + w * 256 + n * 16;
    {
        const v4u ff = {SENT, SENT, SENT, SENT};
#pragma unroll
        for (int i = 0; i < 8; ++i) st16(ff, rs, O_UM + ls + c * 65536 + w * 16384 + i * 1024 + lane * 16);
#pragma unroll
        for (int i = 0; i < 2; ++i) st16(ff, rs, O_PT + ls + c * 16384 + w * 2048 + i * 1024 + lane * 16);
        st16(ff, rs, c < 8 ? O_PM + ls + w * 16384 + c * 1024 + lane * 16 : OOB);
        st16(ff, rs, g ? vp + g * 32768 : OOB);
        WV0; BAR;
    }
    st4(g & 1 ? *(const unsigned*)(a.mem0 + r * 1024 + c * 64 + w * 16 + 4 * g + 2 * hf) : nosent(*(const unsigned*)(a.mem0 + r * 1024 + c * 64 + w * 16 + 4 * g + 2 * hf)), rs, g & 1 ? vp + g * 4 : OOB, 0);
    WV0;
    st4(nosent(*(const unsigned*)(a.mem0 + r * 1024 + c * 64 + w * 16 + 4 * g + 2 * hf)), rs, g & 1 ? OOB : vp + g * 4, 0);
    const int gi = lane >> 3, r8 = lane & 7;
    const unsigned t[12] = {
        (unsigned)lane * 16, sp_idx(lane), l0 + w * 4096 + g * 576 + ((n >> 3) * 16 + hf * 4 + 2 * ((n >> 2) & 1) + (n & 1)) * 4,
        l0 + (r >> 1) * 576 + w * 128 + (g >> 1) * 64 + (r & 1) * 32 + hf * 16,
        (vp ^ ((g & 1) * 65536)) + (g >> 1) * 8, g & 1 ? SENT : 0u,
        l0 + 16384 + w * 8192 + lane * 16, l0 + 16384 + ((gi >> 1) * 2 + (w >> 1)) * 1024 + ((gi & 1) * 32 + (w & 1) * 8 + r8) * 16,
        (c >> 1) * 16384u + (c & 1) * 4096 + gi * 512 + (w * 8 + r8) * 16,
        gi ? OOB : O_ST - O_XR + c * 256 + ((w & 1) * 8 + r8) * 16 + (w >> 1) * 8,
        F_Y0 + ls + (w * 8 + r8) * 128 + (c >> 2) * 4, F_Y0 + ls + (gi & 1) * 4096 + (w * 8 + r8) * 128 + 16};
    table(smem + 65536 + w * 4096 + lane * 4, t);
    asm volatile(RD_ASM : : [rs] "s"(rs), [tb] "s"(tb), [su] "s"(O_UM + ls + c * 65536 + w * 16384), [sp] "s"(O_PM + ls + w * 16384), [st] "s"(O_PT + ls + w * 32768),
        [sy] "s"(sd(O_Y + w * (1u << 20) + ls) + (c >> 1) * 16384 + (c & 1) * 4096), [at] "s"(O_XR + ls), [du] "s"((c & 2) * 7 / 2), [fa] "s"(F_A + ls + c * 128 + w * 4), [pr] "s"(prof_at(l * 80 + 64 + c, w) + 128) : CLOB);
    __builtin_amdgcn_endpgm();
}
DEVI void gg_role(const Args& a, const int w, lds_char* smem, int l, int gi) {
    const int lane = lane_id(), g = lane >> 4, n = lane & 15, b = n & 3, p = n >> 2;
    const unsigned l0 = (unsigned)(size_t)smem, tb = l0 + w * 12288, lw = l0 + 65536 + w * 24576, ls = l * SD, tv = (8 * g + p) * 128 + b * 16 + (w & 1) * 64, fh = (g >> 1) * 1024 + (g & 1) * 512 + n * 16;
    const unsigned t[9] = {l0 + lane * 16, lw + lane * 16, l0 + fh, F_A + ls + (lane >> 2) * 128 + (lane & 3) * 4, w * 2048u + lane * 16, O_ST - O_XR + g * 1024 + n * 16, (lane >> 3) * 7168u + (lane & 7) * 16, tb + tv - (w & 1) * 12288, lw + tv - (w & 1) * 24576};
    table(smem + w * 12288 + lane * 4, t);
    asm volatile(GG_ASM : : [rs] "s"(mkrs(a.ws, WS_RS)), [rw] "s"(mkrs(w & 1 ? a.Wu : a.Wg, 3584 * 2048)), [wo] "s"(gi * 256 + (w >> 1) * 128), [tb] "s"(tb), [lw] "s"(lw), [w] "s"(w),
        [xn] "s"(O_XR + ls), [xw] "s"((w >> 1) * 16384 + (w & 1) * 4096), [ho] "s"(O_H + ls + gi * 16384), [fh] "s"(F_H + ls + (gi / 7) * 1024 + (gi % 7) * 128 + w * 4), [pr] "s"(prof_at(160 + l * 28 + gi, w) + 128) : CLOB);
    __builtin_amdgcn_endpgm();
}
DEVI void dd_role(const Args& a, const int w, lds_char* smem, int l, int c) {
    const int lane = lane_id(), g = lane >> 4, b = lane & 3, p = (lane >> 2) & 3, gi = lane >> 3, r8 = lane & 7;
    const unsigned l0 = (unsigned)(size_t)smem, tb = l0 + w * 12288, lw = l0 + 65536 + w * 24576, ls = l * SD, tr = (8 * g + p) * 128 + b * 8;
    const unsigned ta = (c >> 1) * 16384u + (c & 1) * 4096 + gi * 512 + (w * 8 + r8) * 16;
    const rsrc_t rs = mkrs(a.ws, WS_RS);
    const unsigned t[11] = {(unsigned)lane * 16, lw + lane * 16, l0 + w * 8192 + lane * 16, l0 + ((gi >> 1) * 2 + (w >> 1)) * 1024 + ((gi & 1) * 32 + (w & 1) * 8 + r8) * 16,
        ta, l ? (w * 8 + r8) * 2048 + c * 128 + gi * 16 : ta, gi || l ? OOB : O_ST - O_XR + 4096 + c * 256 + (w * 8 + r8) * 8,
        F_H + ls + w * 1024 + ((lane >> 2) % 7) * 128 + b * 4, gi * 2048u + r8 * 16, tb + tr, lw + tr};
    table(smem + w * 12288 + lane * 4, t);
    asm volatile(DD_ASM : : [rs] "s"(rs), [rw] "s"(mkrs(a.Wd, 3584 * 2048)), [ro] "s"(mkrs(l ? (char*)a.out : a.ws, l ? 2048 * 2048 : WS_RS)), [wo] "s"(w * (896 * 2048) + c * 128), [tb] "s"(tb), [lw] "s"(lw), [l] "s"(l),
        [xn] "s"(O_XR + ls), [ho] "s"(O_H + ls + w * 7 * 16384), [fd] "s"(F_D + ls + c * 128 + w * 4), [pr] "s"(prof_at(216 + l * 16 + c, w) + 128) : CLOB);
    __builtin_amdgcn_endpgm();
}

#define DLS 500
#define SPL 0
#define PLAN 3
#define PFA 0
#define PFB 2
#define PFL 1
#define VUX 1
#define LNS 50
#define LN1 700
#define DLG 800
#define DLD 1300
#define DL1 1700
#define DLG1 2600
#define DLD1 3000
DEVI Args args(unsigned sh) {
    auto p = (const __attribute__((address_space(4))) Args*)__builtin_amdgcn_kernarg_segment_ptr();
    asm volatile("" : "+s"(p));
    Args a;
    a.x = p->x; a.mem0 = p->mem0; a.Wq = p->Wq; a.Wk = p->Wk; a.Wv = p->Wv; a.Wo = p->Wo; a.Wg = p->Wg; a.Wu = p->Wu; a.Wd = p->Wd; a.ws = p->ws + sh; a.out = p->out;
    return a;
}
DEVI unsigned beat_wait(unsigned x0) {
    const rsrc_t r0 = mkrs(args(0).ws, 2u << 20);
    for (unsigned spins = 0; spins < SPIN_CAP; ++spins) {
        CB;
        const v2u t = __builtin_amdgcn_raw_buffer_load_b64(r0, 0, O_HB, AUX_SC1);
        if (RFL(t[0]) != x0) return RFL(t[1]) & (2u << 20);
        __builtin_amdgcn_s_sleep(8);
    }
    __builtin_amdgcn_endpgm();
}
DEVI void leader(const Args& a, const int w, lds_char* smem, unsigned x0) {
    const int lane = lane_id();
    const rsrc_t r0 = mkrs(a.ws, 4u << 20);
    unsigned t[2] = {~0u, ~0u};
    for (int i = 0; i < 2 * NPRB; ++i) {
        const unsigned t0 = (unsigned)__builtin_amdgcn_s_memtime();
        WL0;
        if (lane == 0) st4(0u, r0, 0, O_PRB + (i & 1) * SD + w * 1024 + (i >> 1) * 128);
        WV0;
        const unsigned d = (unsigned)__builtin_amdgcn_s_memtime() - t0;
        if (i & 1) t[1] = min(t[1], d); else t[0] = min(t[0], d);
    }
    *(l2*)(smem + w * 8) = v2u{t[0], t[1]};
    WL0; BAR;
    const v4u p = *(l4*)smem, q = *(l4*)(smem + 16);
    const unsigned sh = RFL(min(min(p[0], p[2]), min(q[0], q[2])) < min(min(p[1], p[3]), min(q[1], q[3])) ? 2u << 20 : 0u);
    const unsigned o = sh + w * 1024 + lane * 16;
    const v4u ff = {SENT, SENT, SENT, SENT};
#pragma unroll
    for (int i = 0; i < 8; ++i) st16(ff, r0, o + O_E1 + i * 4096);
#pragma unroll
    for (int i = 1; i < 16; ++i) if (i != 2 && i != 11) st16(ff, r0, o + i * 4096);
    WV0; BAR;
    if (w) return;
    for (unsigned spins = 0; spins < SPIN_CAP; ++spins) {
        if (lane == 0) st8(v2u{++x0, sh}, r0, 0, O_HB);
        if (__ballot((int)ld4(r0, sh + F_D + SD + (lane >> 2) * 128 + (lane & 3) * 4, 0) < 0) == 0ull) break;
        __builtin_amdgcn_s_sleep(16);
    }
}
__global__ __launch_bounds__(256, 1) void sys_kernel(Args) {
    extern __shared__ __attribute__((aligned(16))) char smem_[];
    lds_char* smem = (lds_char*)smem_;
    const int x = blockIdx.x & 7, j = blockIdx.x >> 3, wv = RFL(threadIdx.x >> 6), l = x >> 2, xx = x & 3;
    const unsigned t0 = (unsigned)__builtin_amdgcn_s_memrealtime();
    auto until = [&](unsigned dl) AI { while ((unsigned)__builtin_amdgcn_s_memrealtime() - t0 < dl) __builtin_amdgcn_s_sleep(8); };
    unsigned x0 = 0;
    if (l || j >= 16) x0 = RFL(ld4(mkrs(args(0).ws, 2u << 20), 0, O_HB));
    if (j < 16) {
        if (l) { until(j ? LN1 + j * LNS : 0); ln0(args(0), wv, j * 4 + xx, [&]() AI { return beat_wait(x0); }); until(DL1); }
        if (j < 8) qk_role(args(0), wv, smem, l, xx * 8 + j); else vu_role(args(0), wv, smem, l, (xx ^ VUX) * 8 + j - 8);
        return;
    }
    {
        const int lane = lane_id(), q = (j - 16) * 4 + wv, m = SPL && !l ? ((xx & 1) != (SPL & 1)) * 2 + (q >> 5) : q >> 4;
        const Args a = args(0);
        const u16 *W0 = a.Wq, *W1 = a.Wk, *W2 = a.Wv, *W3 = a.Wo;
        asm volatile("" : "+s"(W0), "+s"(W1), "+s"(W2), "+s"(W3));
        const u16* W = m == 0 ? W0 : m == 1 ? W1 : m == 2 ? W2 : W3;
        const rsrc_t rw = mkrs(W, 8u << 20);
        if (l) {
#pragma unroll
            for (int i = 0; i < PFA; ++i) __builtin_amdgcn_raw_ptr_buffer_load_lds(rw, smem + LDSB - 1024 + wv * 256, 4, lane * 256, i * (1u << 20) + (xx * 16 + (q & 15)) * 16384, 0, 0);
        } else {
            const unsigned po = (0x100u - (unsigned)(size_t)W) & 0x300u, hx = SPL ? (xx & 2) + ((q >> 4) & 1) : m < 2 ? xx : xx ^ VUX;
            const unsigned vo = m == 3 ? (lane >> 1) * 1024 + (lane & 1) * 128 : (lane >> 2) * 8192 + ((lane >> 1) & 1) * 1024 + (lane & 1) * 128;
            const unsigned so = po + (m == 3 ? hx * (2u << 20) + (q & 15) * 32768 : hx * 2048 + (q & 15) * 131072);
#pragma unroll
            for (int i = 0; i < PFB; ++i) __builtin_amdgcn_raw_ptr_buffer_load_lds(rw, smem + LDSB - 1024 + wv * 256, 4, vo, so + i * (m == 3 ? 524288 : 2u << 20), 0, 0);
            const unsigned pl = (0x1F00u - (unsigned)(size_t)W) & 0x1F00u;
#pragma unroll
            for (int i = 0; i < PFL; ++i) __builtin_amdgcn_raw_ptr_buffer_load_lds(rw, smem + LDSB - 1024 + wv * 256, 4, (lane >> 1) * 8192 + (lane & 1) * 128, (pl >> 11) == hx && m < 3 ? pl + ((q & 15) + 16 * i) * 262144 : OOB, 0, 0);
        }
    }
    int ro, ix;
    if ((xx & 1) == (PLAN & 1)) { if (j < 24) { ro = 0; ix = (xx >> 1) * 8 + j - 16; } else if (j < 26) { ro = 3; ix = (xx >> 1) * 2 + j - 24; } else { ro = 1; ix = (xx >> 1) * 14 + j - 26; } }
    else if (j < 24) { ro = 1; ix = (xx >> 1) * 14 + j - 10; } else { ro = 2; ix = (xx >> 1) * 8 + j - 24; }
    if (ro == 3 && l) { if (ix == 0) leader(args(0), wv, smem, x0); return; }
    const unsigned sh = beat_wait(x0);
    if (ro == 0) { rd_role(args(sh), wv, smem, l, ix); return; }
    if (ro == 1) { const Args a = args(sh); until(l ? DLG1 : ((((unsigned)(size_t)a.Wg >> 8) + ix) & 3) == 1 || ((((unsigned)(size_t)a.Wu >> 8) + ix) & 3) == 1 ? DLG - DLS : DLG); WV0; BAR; gg_role(a, wv, smem, l, ix); return; }
    if (ro == 2) { const Args a = args(sh); until((l ? DLD1 : DLD) - (((((unsigned)(size_t)a.Wd >> 8) + (ix >> 1)) & 3) == 1 ? DLS + 200 : 0)); WV0; BAR; dd_role(a, wv, smem, l, ix); return; }
    r_role(args(sh), wv, ix);
}

extern "C" void kernel_launch(void* const* d_in, const int* in_sizes, int n_in, void* d_out, int out_size, void* d_ws, size_t ws_size, hipStream_t stream) {
    (void)in_sizes; (void)n_in; (void)out_size; (void)ws_size;
    Args a;
    a.x = (const u16*)d_in[0]; a.mem0 = (const u16*)d_in[1]; a.Wq = (const u16*)d_in[2]; a.Wk = (const u16*)d_in[3]; a.Wv = (const u16*)d_in[4]; a.Wo = (const u16*)d_in[5];
    a.Wg = (const u16*)d_in[8]; a.Wu = (const u16*)d_in[9]; a.Wd = (const u16*)d_in[10];
    a.ws = (char*)(((uintptr_t)d_ws + (2u << 20) - 1) & ~(uintptr_t)((2u << 20) - 1)); a.out = (u16*)d_out;
    (void)hipFuncSetAttribute((const void*)sys_kernel, hipFuncAttributeMaxDynamicSharedMemorySize, LDSB);
    hipLaunchKernelGGL(sys_kernel, dim3(256), dim3(256), LDSB, stream, a);
}
```

```cpp
#include <hip/hip_runtime.h>
#include <stdint.h>

typedef unsigned short u16;
typedef unsigned long long u64;
typedef __attribute__((ext_vector_type(8))) short bf16x8;
typedef __attribute__((ext_vector_type(4))) short s16x4;
typedef __attribute__((ext_vector_type(4))) float f32x4;
typedef __attribute__((ext_vector_type(16))) float f32x16;
typedef __attribute__((ext_vector_type(2))) float f32x2;
typedef __attribute__((ext_vector_type(4))) unsigned v4u;
typedef __attribute__((ext_vector_type(2))) unsigned v2u;
typedef __amdgpu_buffer_rsrc_t rsrc_t;
typedef __attribute__((address_space(3))) char lds_char;
typedef __attribute__((address_space(3))) bf16x8 l8;
typedef __attribute__((address_space(3))) v4u l4;
typedef __attribute__((address_space(3))) v2u l2;
typedef __attribute__((address_space(3))) unsigned l1;
typedef __attribute__((address_space(3))) f32x4 lf4;
typedef __attribute__((address_space(3))) float lf1;
#define DEVI __device__ __forceinline__
#define AI __attribute__((always_inline))

constexpr int NSEG = 64;
constexpr float ATT_SCALE = 0.08838834764831845f;
constexpr float LOG2E = 1.4426950408889634f;
#define NPRB 4
constexpr unsigned SD = 8192;
DEVI unsigned sd(unsigned o) { return o ^ ((o >> 8) & SD); }
constexpr unsigned F_X0 = 32768;
constexpr unsigned F_X = 4096;
constexpr unsigned F_A = 16384;
constexpr unsigned F_H = F_A + 2048;
constexpr unsigned F_D = F_H + 4096;
constexpr unsigned F_Y0 = 49152;
constexpr unsigned F_ABORT = 6400;
constexpr unsigned O_PM = 65536;
constexpr unsigned O_E1 = 131072;
constexpr unsigned O_PT = 262144;
constexpr unsigned O_UM = 524288;
constexpr unsigned CTRL_END = O_UM + (1u << 20);
constexpr unsigned O_PRB = 0x1F0000, O_HB = 0x1FFF00;
constexpr unsigned O_XN = 4u << 20;
constexpr unsigned O_XR = O_XN + (8u << 20);
constexpr unsigned O_ST = O_XR + (8u << 20);
constexpr unsigned O_H = O_ST + (8u << 20);
constexpr unsigned O_Y = O_H + (36u << 20);
constexpr unsigned WS_NEED = O_Y + (8u << 20);
constexpr unsigned O_PROF = WS_NEED;
constexpr unsigned WS_RS = WS_NEED;
static_assert(CTRL_END <= O_PRB && CTRL_END % 16384 == 0 && O_Y % (8u << 20) == 0, "workspace");
constexpr unsigned OOB = 0x80000000u, OOB2 = 0x90000000u, SENT = 0xFFFFFFFFu;

struct Args { const u16 *x, *mem0, *Wq, *Wk, *Wv, *Wo, *Wg, *Wu, *Wd; char* ws; u16* out; };

typedef __attribute__((ext_vector_type(2))) __bf16 bf16x2_t;
DEVI unsigned pack2(float lo, float hi) { f32x2 v = {lo, hi}; bf16x2_t b = __builtin_convertvector(v, bf16x2_t); return __builtin_bit_cast(unsigned, b); }
DEVI float bflo(unsigned u) { return __uint_as_float(u << 16); }
DEVI float bfhi(unsigned u) { return __uint_as_float(u & 0xffff0000u); }
DEVI float rbf(float x) { return bflo(pack2(x, x)); }
DEVI bf16x8 as8(v4u v) { return __builtin_bit_cast(bf16x8, v); }
DEVI f32x4 mfma16(bf16x8 a, bf16x8 b, f32x4 c) { return __builtin_amdgcn_mfma_f32_16x16x32_bf16(a, b, c, 0, 0, 0); }
DEVI f32x16 mfma32(bf16x8 a, bf16x8 b, f32x16 c) { return __builtin_amdgcn_mfma_f32_32x32x16_bf16(a, b, c, 0, 0, 0); }
constexpr int AUX_SC1 = 16;
DEVI v4u ld16(rsrc_t r, unsigned vo, unsigned so) { return __builtin_amdgcn_raw_buffer_load_b128(r, vo, so, AUX_SC1); }
DEVI unsigned ld4(rsrc_t r, unsigned vo, unsigned so) { return __builtin_amdgcn_raw_buffer_load_b32(r, vo, so, AUX_SC1); }
DEVI void st16(v4u v, rsrc_t r, unsigned vo) { __builtin_amdgcn_raw_buffer_store_b128(v, r, vo, 0, AUX_SC1); }
DEVI void st8(v2u v, rsrc_t r, unsigned vo, unsigned so) { __builtin_amdgcn_raw_buffer_store_b64(v, r, vo, so, AUX_SC1); }
DEVI void st4(unsigned v, rsrc_t r, unsigned vo, unsigned so) { __builtin_amdgcn_raw_buffer_store_b32(v, r, vo, so, AUX_SC1); }
DEVI v2u trd(lds_char* p) { return __builtin_bit_cast(v2u, __builtin_amdgcn_ds_read_tr16_b64_v4i16((__attribute__((address_space(3))) s16x4*)p)); }
template <int O1, int O2> DEVI void tra(v2u& lo, v2u& hi, unsigned a) { asm volatile("ds_read_b64_tr_b16 %0, %2 offset:%3\n\tds_read_b64_tr_b16 %1, %2 offset:%4" : "=&v"(lo), "=&v"(hi) : "v"(a), "n"(O1), "n"(O2) : "memory"); }
DEVI void trw(v2u& a, v2u& b, v2u& c, v2u& d) { asm volatile("s_waitcnt lgkmcnt(0)" : "+v"(a), "+v"(b), "+v"(c), "+v"(d)::"memory"); }
DEVI rsrc_t mkrs(const void* p, unsigned n) { return __builtin_amdgcn_make_buffer_rsrc((void*)p, 0, (int)n, 0x00020000); }
#define CB asm volatile("" ::: "memory")
#define SB __builtin_amdgcn_sched_barrier(0)
#define WL0 asm volatile("s_waitcnt lgkmcnt(0)" ::: "memory")
#define WV0 asm volatile("s_waitcnt vmcnt(0)" ::: "memory")
#define WV(n) asm volatile("s_waitcnt vmcnt(%0)" ::"n"(n) : "memory")
#define BAR do { __builtin_amdgcn_s_barrier(); CB; } while (0)
#define RFL(x) __builtin_amdgcn_readfirstlane(x)
DEVI int lane_id() { int x; asm volatile("v_mbcnt_lo_u32_b32 %0, -1, 0\n\tv_mbcnt_hi_u32_b32 %0, -1, %0" : "=v"(x)); return x; }
template <int I> struct IC { static constexpr int v = I; };
template <int B, int E_, class F> DEVI void sfor(F&& f) { if constexpr (B < E_) { f(IC<B>{}); sfor<B + 1, E_>(f); } }
DEVI float red_x32(float x, bool mx) { unsigned u = __float_as_uint(x); auto r = __builtin_amdgcn_permlane32_swap(u, u, false, false); float a = __uint_as_float(r[0]), b = __uint_as_float(r[1]); return mx ? fmaxf(a, b) : a + b; }
DEVI float red_x16(float x, bool mx) { unsigned u = __float_as_uint(x); auto r = __builtin_amdgcn_permlane16_swap(u, u, false, false); float a = __uint_as_float(r[0]), b = __uint_as_float(r[1]); return mx ? fmaxf(a, b) : a + b; }
DEVI unsigned vmax4(v4u d) { return max(max(d[0], d[1]), max(d[2], d[3])); }
DEVI unsigned nosent(unsigned v) { return min(v, SENT - 1u); }
constexpr unsigned SPIN_CAP = 1u << 18;
DEVI bool spin_bail(unsigned& spins, rsrc_t rs) {
    if (++spins > SPIN_CAP) { st4(0u, rs, 0, F_ABORT); return true; }
    return (spins & 255u) == 0u && RFL(ld4(rs, 0, F_ABORT)) != SENT;
}
#define TSTAMP(idx)
constexpr int LDSB = 160 * 1024;
DEVI void idle(unsigned dl) { const unsigned t0 = (unsigned)__builtin_amdgcn_s_memrealtime(); while ((unsigned)__builtin_amdgcn_s_memrealtime() - t0 < dl) __builtin_amdgcn_s_sleep(8); }

DEVI float red_rows(float s) {
    s += __uint_as_float(__builtin_amdgcn_update_dpp(0, __float_as_uint(s), 0x128, 0xf, 0xf, false));
    s = red_x16(s, false); return red_x32(s, false);
}
DEVI f32x2 up2(unsigned u) { return f32x2{bflo(u), bfhi(u)}; }
DEVI float ln_inv(float var) { return __builtin_amdgcn_rcpf(rbf(__builtin_amdgcn_sqrtf(rbf(rbf(fmaxf(var, 0.f)) + 1e-5f)))); }
template <class F> DEVI void ln0(const Args& a, const int w, int b, F&& base) {
    const int lane = lane_id(), row = w * 8 + (lane & 7);
    const rsrc_t rx = mkrs(a.x, 2048 * 2048);
    v4u u[16];
#pragma unroll
    for (int i = 0; i < 16; ++i) u[i] = __builtin_amdgcn_raw_buffer_load_b128(rx, row * 2048 + (lane >> 3) * 16 + i * 128, b * 65536, 0);
    const float k0 = __shfl(bflo(u[0][0]), lane & 7);
    f32x2 s1 = {0.f, 0.f}, s2 = {0.f, 0.f};
#pragma unroll
    for (int i = 0; i < 16; ++i)
#pragma unroll
        for (int e = 0; e < 4; ++e) { const f32x2 d = up2(u[i][e]) - k0; s1 += d; s2 += d * d; }
    const float s = red_rows(s1[0] + s1[1]) * (1.0f / 1024.0f), q = red_rows(s2[0] + s2[1]) * (1.0f / 1024.0f);
    float inv = ln_inv(q - s * s), c = -rbf(k0 + s) * inv;
    asm volatile("" : "+v"(inv), "+v"(c));
    const rsrc_t rs = mkrs(a.ws + base(), WS_RS);
    const unsigned o = sd(O_XN + b * 131072) + (lane >> 3) * 512 + row * 16;
#pragma unroll
    for (int i = 0; i < 16; ++i) {
        v4u r;
#pragma unroll
        for (int e = 0; e < 4; ++e) { const f32x2 z = up2(u[i][e]) * inv + c; r[e] = pack2(z[0], z[1]); }
        st16(r, rs, o + (i >> 1) * 16384 + (i & 1) * 4096);
    }
#pragma unroll
    for (int i = 0; i < 16; ++i) st16(u[i], rs, o + (O_XR - O_XN) + (i >> 1) * 16384 + (i & 1) * 4096);
    st16(u[0], rs, sd(O_ST + b * 131072) + w * 1024 + lane * 16); st16(u[0], rs, sd(O_ST + b * 131072) + 4096 + w * 1024 + lane * 16); st16(u[0], rs, sd(O_ST + SD + b * 131072) + w * 1024 + lane * 16);
    WV0;
    st4(b, rs, 0, F_X0 + b * 128 + w * 4);
}
#define RSLP 8
DEVI void r_role(const Args& a, const int w, int rq) {
    const int lane = lane_id(), row = (w >> 1) * 16 + (rq & 1) * 8 + (lane & 7), cq = (rq >> 1) * 2 + (w & 1);
    const rsrc_t rs = mkrs(a.ws, WS_RS);
    const unsigned fo = cq * 32768 + (lane >> 3) * 512 + row * 16, so = O_ST - O_XR + 4096 + (lane >> 3) * 512 + row * 8;
    auto io = [](int i) -> unsigned { return (i >> 1) * 16384 + (i & 1) * 4096; };
    const unsigned fd = F_D + (lane & 15) * 128 + (row >> 3) * 4;
    for (int b = 0; b < NSEG; ++b) {
        unsigned spins = 0;
        for (;;) {
            CB;
            if (__ballot((int)ld4(rs, fd, 0) < b) == 0ull) break;
            if (spin_bail(spins, rs)) return;
            __builtin_amdgcn_s_sleep(RSLP);
        }
        const unsigned sx = sd(O_XR + b * 131072);
        const v2u s0 = __builtin_amdgcn_raw_buffer_load_b64(rs, so, sx, AUX_SC1), s1 = __builtin_amdgcn_raw_buffer_load_b64(rs, so + 256, sx, AUX_SC1);
        v4u u[4];
#pragma unroll
        for (int i = 0; i < 4; ++i) u[i] = ld16(rs, fo + io(i), sx);
        const float m0 = __uint_as_float(s0[0]), m1 = __uint_as_float(s1[0]), mean = red_rows(m0 + m1) * 0.0625f;
        const float var = red_rows(__uint_as_float(s0[1]) + __uint_as_float(s1[1]) + 64.f * ((m0 - mean) * (m0 - mean) + (m1 - mean) * (m1 - mean))) * (1.0f / 1024.0f);
        const float inv = ln_inv(var), c = -rbf(mean) * inv;
#pragma unroll
        for (int i = 0; i < 4; ++i) {
            v4u o;
#pragma unroll
            for (int e = 0; e < 4; ++e) { const f32x2 z = up2(u[i][e]) * inv + c; o[e] = pack2(z[0], z[1]); }
            st16(o, rs, fo + io(i) + ((sx ^ SD) - (O_XR - O_XN)));
        }
#pragma unroll
        for (int i = 0; i < 4; ++i) st16(u[i], rs, fo + io(i) + (sx ^ SD));
        WV0;
        st4(b, rs, 0, F_X + SD + rq * 128 + w * 4);
        if (rq == 0 && w == 0) TSTAMP(200000 + 64 + b);
    }
}
#define QK_ASM \
"v_mbcnt_lo_u32_b32 v255, -1, 0\nv_mbcnt_hi_u32_b32 v255, -1, v255\nv_lshlrev_b32 v255, 2, v255\nv_add_u32 v255, %[tb], v255\nds_read_b32 v0, v255 offset:0\nds_read_b32 v1, v255 offset:256\nds_read_b32 v2, v255 offset:512\nds_read_b32 v3, v255 offset:768\n" \
"ds_read_b32 v4, v255 offset:1024\nds_read_b32 v5, v255 offset:1280\nds_read_b32 v6, v255 offset:1536\nds_read_b32 v7, v255 offset:1792\nds_read_b32 v8, v255 offset:2048\nds_read_b32 v9, v255 offset:2304\nds_read_b32 v10, v255 offset:2560\nds_read_b32 v11, v255 offset:2816\n" \
"ds_read_b32 v12, v255 offset:3072\nds_read_b32 v13, v255 offset:3328\nds_read_b32 v14, v255 offset:3584\nds_read_b32 v15, v255 offset:3840\nds_read_b32 v92, v255 offset:4096\nds_read_b32 v93, v255 offset:4352\nds_read_b32 v94, v255 offset:4608\ns_waitcnt lgkmcnt(0)\n" \
"s_mov_b64 s[36:37], %[ws]\ns_mov_b32 s38, %[sz]\ns_mov_b32 s39, 0x20000\ns_mov_b32 s45, 2096896\nbuffer_load_dword v95, off, s[36:39], s45 sc1\ns_add_u32 m0, %[lw], 16384\ns_add_u32 s44, %[wo], 0\nbuffer_load_dwordx4 v92, %[rw], s44 offen nt lds\n" \
"s_add_u32 m0, %[lw], 17408\ns_add_u32 s44, %[wo], 65536\nbuffer_load_dwordx4 v92, %[rw], s44 offen nt lds\ns_add_u32 m0, %[lw], 18432\ns_add_u32 s44, %[wo], 131072\nbuffer_load_dwordx4 v92, %[rw], s44 offen nt lds\ns_add_u32 m0, %[lw], 19456\ns_add_u32 s44, %[wo], 196608\n" \
"buffer_load_dwordx4 v92, %[rw], s44 offen nt lds\ns_add_u32 m0, %[lw], 20480\ns_add_u32 s44, %[wo], 262144\nbuffer_load_dwordx4 v92, %[rw], s44 offen nt lds\ns_add_u32 m0, %[lw], 21504\ns_add_u32 s44, %[wo], 327680\nbuffer_load_dwordx4 v92, %[rw], s44 offen nt lds\ns_add_u32 m0, %[lw], 22528\n" \
"s_add_u32 s44, %[wo], 393216\nbuffer_load_dwordx4 v92, %[rw], s44 offen nt lds\ns_add_u32 m0, %[lw], 23552\ns_add_u32 s44, %[wo], 458752\nbuffer_load_dwordx4 v92, %[rw], s44 offen nt lds\ns_add_u32 m0, %[tb], 0\ns_add_u32 s44, %[wo], 524288\nbuffer_load_dwordx4 v92, %[rw], s44 offen nt lds\n" \
"s_add_u32 m0, %[tb], 1024\ns_add_u32 s44, %[wo], 589824\nbuffer_load_dwordx4 v92, %[rw], s44 offen nt lds\ns_add_u32 m0, %[tb], 2048\ns_add_u32 s44, %[wo], 655360\nbuffer_load_dwordx4 v92, %[rw], s44 offen nt lds\ns_add_u32 m0, %[tb], 3072\ns_add_u32 s44, %[wo], 720896\n" \
"buffer_load_dwordx4 v92, %[rw], s44 offen nt lds\ns_add_u32 m0, %[tb], 4096\ns_add_u32 s44, %[wo], 786432\nbuffer_load_dwordx4 v92, %[rw], s44 offen nt lds\ns_add_u32 m0, %[tb], 5120\ns_add_u32 s44, %[wo], 851968\nbuffer_load_dwordx4 v92, %[rw], s44 offen nt lds\ns_add_u32 m0, %[tb], 6144\n" \
"s_add_u32 s44, %[wo], 917504\nbuffer_load_dwordx4 v92, %[rw], s44 offen nt lds\ns_add_u32 m0, %[tb], 7168\ns_add_u32 s44, %[wo], 983040\nbuffer_load_dwordx4 v92, %[rw], s44 offen nt lds\ns_add_u32 m0, %[tb], 8192\ns_add_u32 s44, %[wo], 1048576\nbuffer_load_dwordx4 v92, %[rw], s44 offen nt lds\n" \
"s_add_u32 m0, %[tb], 9216\ns_add_u32 s44, %[wo], 1114112\nbuffer_load_dwordx4 v92, %[rw], s44 offen nt lds\ns_add_u32 m0, %[tb], 10240\ns_add_u32 s44, %[wo], 1179648\nbuffer_load_dwordx4 v92, %[rw], s44 offen nt lds\ns_add_u32 m0, %[tb], 11264\ns_add_u32 s44, %[wo], 1245184\n" \
"buffer_load_dwordx4 v92, %[rw], s44 offen nt lds\ns_add_u32 m0, %[lw], 0\ns_add_u32 s44, %[wo], 1310720\nbuffer_load_dwordx4 v92, %[rw], s44 offen nt lds\ns_add_u32 m0, %[lw], 1024\ns_add_u32 s44, %[wo], 1376256\nbuffer_load_dwordx4 v92, %[rw], s44 offen nt lds\ns_add_u32 m0, %[lw], 2048\n" \
"s_add_u32 s44, %[wo], 1441792\nbuffer_load_dwordx4 v92, %[rw], s44 offen nt lds\ns_add_u32 m0, %[lw], 3072\ns_add_u32 s44, %[wo], 1507328\nbuffer_load_dwordx4 v92, %[rw], s44 offen nt lds\ns_add_u32 m0, %[lw], 4096\ns_add_u32 s44, %[wo], 1572864\nbuffer_load_dwordx4 v92, %[rw], s44 offen nt lds\n" \
"s_add_u32 m0, %[lw], 5120\ns_add_u32 s44, %[wo], 1638400\nbuffer_load_dwordx4 v92, %[rw], s44 offen nt lds\ns_add_u32 m0, %[lw], 6144\ns_add_u32 s44, %[wo], 1703936\nbuffer_load_dwordx4 v92, %[rw], s44 offen nt lds\ns_add_u32 m0, %[lw], 7168\ns_add_u32 s44, %[wo], 1769472\n" \
"buffer_load_dwordx4 v92, %[rw], s44 offen nt lds\ns_add_u32 m0, %[lw], 8192\ns_add_u32 s44, %[wo], 1835008\nbuffer_load_dwordx4 v92, %[rw], s44 offen nt lds\ns_add_u32 m0, %[lw], 9216\ns_add_u32 s44, %[wo], 1900544\nbuffer_load_dwordx4 v92, %[rw], s44 offen nt lds\ns_add_u32 m0, %[lw], 10240\n" \
"s_add_u32 s44, %[wo], 1966080\nbuffer_load_dwordx4 v92, %[rw], s44 offen nt lds\ns_add_u32 m0, %[lw], 11264\ns_add_u32 s44, %[wo], 2031616\nbuffer_load_dwordx4 v92, %[rw], s44 offen nt lds\ns_add_u32 m0, %[lw], 12288\ns_add_u32 s44, %[wo], 2097152\nbuffer_load_dwordx4 v92, %[rw], s44 offen nt lds\n" \
"s_add_u32 m0, %[lw], 13312\ns_add_u32 s44, %[wo], 2162688\nbuffer_load_dwordx4 v92, %[rw], s44 offen nt lds\ns_add_u32 m0, %[lw], 14336\ns_add_u32 s44, %[wo], 2228224\nbuffer_load_dwordx4 v92, %[rw], s44 offen nt lds\ns_add_u32 m0, %[lw], 15360\ns_add_u32 s44, %[wo], 2293760\n" \
"buffer_load_dwordx4 v92, %[rw], s44 offen nt lds\ns_waitcnt vmcnt(32)\nds_read_b64_tr_b16 a[0:1], v94 offset:16384\nds_read_b64_tr_b16 a[2:3], v94 offset:16896\nds_read_b64_tr_b16 a[8:9], v94 offset:16392\nds_read_b64_tr_b16 a[10:11], v94 offset:16904\nds_read_b64_tr_b16 a[16:17], v94 offset:16448\nds_read_b64_tr_b16 a[18:19], v94 offset:16960\n" \
"ds_read_b64_tr_b16 a[24:25], v94 offset:16456\nds_read_b64_tr_b16 a[26:27], v94 offset:16968\ns_waitcnt lgkmcnt(0)\ns_add_u32 m0, %[lw], 16384\ns_add_u32 s44, %[wo], 2359296\nbuffer_load_dwordx4 v92, %[rw], s44 offen nt lds\ns_add_u32 m0, %[lw], 17408\ns_add_u32 s44, %[wo], 2424832\n" \
"buffer_load_dwordx4 v92, %[rw], s44 offen nt lds\ns_add_u32 m0, %[lw], 18432\ns_add_u32 s44, %[wo], 2490368\nbuffer_load_dwordx4 v92, %[rw], s44 offen nt lds\ns_add_u32 m0, %[lw], 19456\ns_add_u32 s44, %[wo], 2555904\nbuffer_load_dwordx4 v92, %[rw], s44 offen nt lds\ns_waitcnt vmcnt(32)\n" \
"ds_read_b64_tr_b16 a[4:5], v94 offset:20480\nds_read_b64_tr_b16 a[6:7], v94 offset:20992\nds_read_b64_tr_b16 a[12:13], v94 offset:20488\nds_read_b64_tr_b16 a[14:15], v94 offset:21000\nds_read_b64_tr_b16 a[20:21], v94 offset:20544\nds_read_b64_tr_b16 a[22:23], v94 offset:21056\nds_read_b64_tr_b16 a[28:29], v94 offset:20552\nds_read_b64_tr_b16 a[30:31], v94 offset:21064\n" \
"s_waitcnt lgkmcnt(0)\ns_add_u32 m0, %[lw], 20480\ns_add_u32 s44, %[wo], 2621440\nbuffer_load_dwordx4 v92, %[rw], s44 offen nt lds\ns_add_u32 m0, %[lw], 21504\ns_add_u32 s44, %[wo], 2686976\nbuffer_load_dwordx4 v92, %[rw], s44 offen nt lds\ns_add_u32 m0, %[lw], 22528\n" \
"s_add_u32 s44, %[wo], 2752512\nbuffer_load_dwordx4 v92, %[rw], s44 offen nt lds\ns_add_u32 m0, %[lw], 23552\ns_add_u32 s44, %[wo], 2818048\nbuffer_load_dwordx4 v92, %[rw], s44 offen nt lds\ns_waitcnt vmcnt(32)\nds_read_b64_tr_b16 a[32:33], v93 offset:0\nds_read_b64_tr_b16 a[34:35], v93 offset:512\n" \
"ds_read_b64_tr_b16 v[88:89], v93 offset:8\nds_read_b64_tr_b16 v[90:91], v93 offset:520\nds_read_b64_tr_b16 a[40:41], v93 offset:64\nds_read_b64_tr_b16 a[42:43], v93 offset:576\nds_read_b64_tr_b16 a[48:49], v93 offset:72\nds_read_b64_tr_b16 a[50:51], v93 offset:584\ns_waitcnt lgkmcnt(0)\ns_add_u32 m0, %[tb], 0\n" \
"s_add_u32 s44, %[wo], 2883584\nbuffer_load_dwordx4 v92, %[rw], s44 offen nt lds\ns_add_u32 m0, %[tb], 1024\ns_add_u32 s44, %[wo], 2949120\nbuffer_load_dwordx4 v92, %[rw], s44 offen nt lds\ns_add_u32 m0, %[tb], 2048\ns_add_u32 s44, %[wo], 3014656\nbuffer_load_dwordx4 v92, %[rw], s44 offen nt lds\n" \
"s_add_u32 m0, %[tb], 3072\ns_add_u32 s44, %[wo], 3080192\nbuffer_load_dwordx4 v92, %[rw], s44 offen nt lds\ns_waitcnt vmcnt(32)\nds_read_b64_tr_b16 a[36:37], v93 offset:4096\nds_read_b64_tr_b16 a[38:39], v93 offset:4608\nds_read_b64_tr_b16 v[84:85], v93 offset:4104\nds_read_b64_tr_b16 v[86:87], v93 offset:4616\n" \
"ds_read_b64_tr_b16 a[44:45], v93 offset:4160\nds_read_b64_tr_b16 a[46:47], v93 offset:4672\nds_read_b64_tr_b16 a[52:53], v93 offset:4168\nds_read_b64_tr_b16 a[54:55], v93 offset:4680\ns_waitcnt lgkmcnt(0)\ns_add_u32 m0, %[tb], 4096\ns_add_u32 s44, %[wo], 3145728\nbuffer_load_dwordx4 v92, %[rw], s44 offen nt lds\n" \
"s_add_u32 m0, %[tb], 5120\ns_add_u32 s44, %[wo], 3211264\nbuffer_load_dwordx4 v92, %[rw], s44 offen nt lds\ns_add_u32 m0, %[tb], 6144\ns_add_u32 s44, %[wo], 3276800\nbuffer_load_dwordx4 v92, %[rw], s44 offen nt lds\ns_add_u32 m0, %[tb], 7168\ns_add_u32 s44, %[wo], 3342336\n" \
"buffer_load_dwordx4 v92, %[rw], s44 offen nt lds\ns_waitcnt vmcnt(32)\nds_read_b64_tr_b16 a[56:57], v93 offset:8192\nds_read_b64_tr_b16 a[58:59], v93 offset:8704\nds_read_b64_tr_b16 a[64:65], v93 offset:8200\nds_read_b64_tr_b16 a[66:67], v93 offset:8712\nds_read_b64_tr_b16 v[80:81], v93 offset:8256\nds_read_b64_tr_b16 v[82:83], v93 offset:8768\n" \
"ds_read_b64_tr_b16 a[72:73], v93 offset:8264\nds_read_b64_tr_b16 a[74:75], v93 offset:8776\ns_waitcnt lgkmcnt(0)\ns_add_u32 m0, %[tb], 8192\ns_add_u32 s44, %[wo], 3407872\nbuffer_load_dwordx4 v92, %[rw], s44 offen nt lds\ns_add_u32 m0, %[tb], 9216\ns_add_u32 s44, %[wo], 3473408\n" \
"buffer_load_dwordx4 v92, %[rw], s44 offen nt lds\ns_add_u32 m0, %[tb], 10240\ns_add_u32 s44, %[wo], 3538944\nbuffer_load_dwordx4 v92, %[rw], s44 offen nt lds\ns_add_u32 m0, %[tb], 11264\ns_add_u32 s44, %[wo], 3604480\nbuffer_load_dwordx4 v92, %[rw], s44 offen nt lds\ns_waitcnt vmcnt(32)\n" \
"ds_read_b64_tr_b16 a[60:61], v94 offset:0\nds_read_b64_tr_b16 a[62:63], v94 offset:512\nds_read_b64_tr_b16 a[68:69], v94 offset:8\nds_read_b64_tr_b16 a[70:71], v94 offset:520\nds_read_b64_tr_b16 v[76:77], v94 offset:64\nds_read_b64_tr_b16 v[78:79], v94 offset:576\nds_read_b64_tr_b16 a[76:77], v94 offset:72\nds_read_b64_tr_b16 a[78:79], v94 offset:584\n" \
"s_waitcnt lgkmcnt(0)\ns_add_u32 m0, %[lw], 0\ns_add_u32 s44, %[wo], 3670016\nbuffer_load_dwordx4 v92, %[rw], s44 offen nt lds\ns_add_u32 m0, %[lw], 1024\ns_add_u32 s44, %[wo], 3735552\nbuffer_load_dwordx4 v92, %[rw], s44 offen nt lds\ns_add_u32 m0, %[lw], 2048\n" \
"s_add_u32 s44, %[wo], 3801088\nbuffer_load_dwordx4 v92, %[rw], s44 offen nt lds\ns_add_u32 m0, %[lw], 3072\ns_add_u32 s44, %[wo], 3866624\nbuffer_load_dwordx4 v92, %[rw], s44 offen nt lds\ns_waitcnt vmcnt(32)\nds_read_b64_tr_b16 a[80:81], v94 offset:4096\nds_read_b64_tr_b16 a[82:83], v94 offset:4608\n" \
"ds_read_b64_tr_b16 a[88:89], v94 offset:4104\nds_read_b64_tr_b16 a[90:91], v94 offset:4616\nds_read_b64_tr_b16 a[96:97], v94 offset:4160\nds_read_b64_tr_b16 a[98:99], v94 offset:4672\nds_read_b64_tr_b16 v[72:73], v94 offset:4168\nds_read_b64_tr_b16 v[74:75], v94 offset:4680\ns_waitcnt lgkmcnt(0)\ns_add_u32 m0, %[lw], 4096\n" \
"s_add_u32 s44, %[wo], 3932160\nbuffer_load_dwordx4 v92, %[rw], s44 offen nt lds\ns_add_u32 m0, %[lw], 5120\ns_add_u32 s44, %[wo], 3997696\nbuffer_load_dwordx4 v92, %[rw], s44 offen nt lds\ns_add_u32 m0, %[lw], 6144\ns_add_u32 s44, %[wo], 4063232\nbuffer_load_dwordx4 v92, %[rw], s44 offen nt lds\n" \
"s_add_u32 m0, %[lw], 7168\ns_add_u32 s44, %[wo], 4128768\nbuffer_load_dwordx4 v92, %[rw], s44 offen nt lds\ns_waitcnt vmcnt(32)\nds_read_b64_tr_b16 a[84:85], v94 offset:8192\nds_read_b64_tr_b16 a[86:87], v94 offset:8704\nds_read_b64_tr_b16 a[92:93], v94 offset:8200\nds_read_b64_tr_b16 a[94:95], v94 offset:8712\n" \
"ds_read_b64_tr_b16 a[100:101], v94 offset:8256\nds_read_b64_tr_b16 a[102:103], v94 offset:8768\nds_read_b64_tr_b16 v[68:69], v94 offset:8264\nds_read_b64_tr_b16 v[70:71], v94 offset:8776\ns_waitcnt lgkmcnt(0)\ns_add_u32 m0, %[lw], 8192\ns_add_u32 s44, %[wo], 4194304\nbuffer_load_dwordx4 v92, %[rw], s44 offen nt lds\n" \
"s_add_u32 m0, %[lw], 9216\ns_add_u32 s44, %[wo], 4259840\nbuffer_load_dwordx4 v92, %[rw], s44 offen nt lds\ns_add_u32 m0, %[lw], 10240\ns_add_u32 s44, %[wo], 4325376\nbuffer_load_dwordx4 v92, %[rw], s44 offen nt lds\ns_add_u32 m0, %[lw], 11264\ns_add_u32 s44, %[wo], 4390912\n" \
"buffer_load_dwordx4 v92, %[rw], s44 offen nt lds\ns_waitcnt vmcnt(32)\nds_read_b64_tr_b16 a[104:105], v94 offset:12288\nds_read_b64_tr_b16 a[106:107], v94 offset:12800\nds_read_b64_tr_b16 a[112:113], v94 offset:12296\nds_read_b64_tr_b16 a[114:115], v94 offset:12808\nds_read_b64_tr_b16 a[120:121], v94 offset:12352\nds_read_b64_tr_b16 a[122:123], v94 offset:12864\n" \
"ds_read_b64_tr_b16 a[128:129], v94 offset:12360\nds_read_b64_tr_b16 a[130:131], v94 offset:12872\ns_waitcnt lgkmcnt(0)\ns_add_u32 m0, %[lw], 12288\ns_add_u32 s44, %[wo], 4456448\nbuffer_load_dwordx4 v92, %[rw], s44 offen nt lds\ns_add_u32 m0, %[lw], 13312\ns_add_u32 s44, %[wo], 4521984\n" \
"buffer_load_dwordx4 v92, %[rw], s44 offen nt lds\ns_add_u32 m0, %[lw], 14336\ns_add_u32 s44, %[wo], 4587520\nbuffer_load_dwordx4 v92, %[rw], s44 offen nt lds\ns_add_u32 m0, %[lw], 15360\ns_add_u32 s44, %[wo], 4653056\nbuffer_load_dwordx4 v92, %[rw], s44 offen nt lds\ns_waitcnt vmcnt(32)\n" \
"ds_read_b64_tr_b16 a[108:109], v94 offset:16384\nds_read_b64_tr_b16 a[110:111], v94 offset:16896\nds_read_b64_tr_b16 a[116:117], v94 offset:16392\nds_read_b64_tr_b16 a[118:119], v94 offset:16904\nds_read_b64_tr_b16 a[124:125], v94 offset:16448\nds_read_b64_tr_b16 a[126:127], v94 offset:16960\nds_read_b64_tr_b16 a[132:133], v94 offset:16456\nds_read_b64_tr_b16 a[134:135], v94 offset:16968\n" \
"s_waitcnt lgkmcnt(0)\ns_add_u32 m0, %[lw], 16384\ns_add_u32 s44, %[wo], 4718592\nbuffer_load_dwordx4 v92, %[rw], s44 offen nt lds\ns_add_u32 m0, %[lw], 17408\ns_add_u32 s44, %[wo], 4784128\nbuffer_load_dwordx4 v92, %[rw], s44 offen nt lds\ns_add_u32 m0, %[lw], 18432\n" \
"s_add_u32 s44, %[wo], 4849664\nbuffer_load_dwordx4 v92, %[rw], s44 offen nt lds\ns_add_u32 m0, %[lw], 19456\ns_add_u32 s44, %[wo], 4915200\nbuffer_load_dwordx4 v92, %[rw], s44 offen nt lds\ns_waitcnt vmcnt(32)\nds_read_b64_tr_b16 a[136:137], v94 offset:20480\nds_read_b64_tr_b16 a[138:139], v94 offset:20992\n" \
"ds_read_b64_tr_b16 v[64:65], v94 offset:20488\nds_read_b64_tr_b16 v[66:67], v94 offset:21000\nds_read_b64_tr_b16 a[144:145], v94 offset:20544\nds_read_b64_tr_b16 a[146:147], v94 offset:21056\nds_read_b64_tr_b16 a[152:153], v94 offset:20552\nds_read_b64_tr_b16 a[154:155], v94 offset:21064\ns_waitcnt lgkmcnt(0)\ns_add_u32 m0, %[lw], 20480\n" \
"s_add_u32 s44, %[wo], 4980736\nbuffer_load_dwordx4 v92, %[rw], s44 offen nt lds\ns_add_u32 m0, %[lw], 21504\ns_add_u32 s44, %[wo], 5046272\nbuffer_load_dwordx4 v92, %[rw], s44 offen nt lds\ns_add_u32 m0, %[lw], 22528\ns_add_u32 s44, %[wo], 5111808\nbuffer_load_dwordx4 v92, %[rw], s44 offen nt lds\n" \
"s_add_u32 m0, %[lw], 23552\ns_add_u32 s44, %[wo], 5177344\nbuffer_load_dwordx4 v92, %[rw], s44 offen nt lds\ns_waitcnt vmcnt(32)\nds_read_b64_tr_b16 a[140:141], v93 offset:0\nds_read_b64_tr_b16 a[142:143], v93 offset:512\nds_read_b64_tr_b16 v[60:61], v93 offset:8\nds_read_b64_tr_b16 v[62:63], v93 offset:520\n" \
"ds_read_b64_tr_b16 a[148:149], v93 offset:64\nds_read_b64_tr_b16 a[150:151], v93 offset:576\nds_read_b64_tr_b16 a[156:157], v93 offset:72\nds_read_b64_tr_b16 a[158:159], v93 offset:584\ns_waitcnt lgkmcnt(0)\ns_add_u32 m0, %[tb], 0\ns_add_u32 s44, %[wo], 5242880\nbuffer_load_dwordx4 v92, %[rw], s44 offen nt lds\n" \
"s_add_u32 m0, %[tb], 1024\ns_add_u32 s44, %[wo], 5308416\nbuffer_load_dwordx4 v92, %[rw], s44 offen nt lds\ns_add_u32 m0, %[tb], 2048\ns_add_u32 s44, %[wo], 5373952\nbuffer_load_dwordx4 v92, %[rw], s44 offen nt lds\ns_add_u32 m0, %[tb], 3072\ns_add_u32 s44, %[wo], 5439488\n" \
"buffer_load_dwordx4 v92, %[rw], s44 offen nt lds\ns_waitcnt vmcnt(32)\nds_read_b64_tr_b16 a[160:161], v93 offset:4096\nds_read_b64_tr_b16 a[162:163], v93 offset:4608\nds_read_b64_tr_b16 a[168:169], v93 offset:4104\nds_read_b64_tr_b16 a[170:171], v93 offset:4616\nds_read_b64_tr_b16 v[56:57], v93 offset:4160\nds_read_b64_tr_b16 v[58:59], v93 offset:4672\n" \
"ds_read_b64_tr_b16 a[176:177], v93 offset:4168\nds_read_b64_tr_b16 a[178:179], v93 offset:4680\ns_waitcnt lgkmcnt(0)\ns_add_u32 m0, %[tb], 4096\ns_add_u32 s44, %[wo], 5505024\nbuffer_load_dwordx4 v92, %[rw], s44 offen nt lds\ns_add_u32 m0, %[tb], 5120\ns_add_u32 s44, %[wo], 5570560\n" \
"buffer_load_dwordx4 v92, %[rw], s44 offen nt lds\ns_add_u32 m0, %[tb], 6144\ns_add_u32 s44, %[wo], 5636096\nbuffer_load_dwordx4 v92, %[rw], s44 offen nt lds\ns_add_u32 m0, %[tb], 7168\ns_add_u32 s44, %[wo], 5701632\nbuffer_load_dwordx4 v92, %[rw], s44 offen nt lds\ns_waitcnt vmcnt(32)\n" \
"ds_read_b64_tr_b16 a[164:165], v93 offset:8192\nds_read_b64_tr_b16 a[166:167], v93 offset:8704\nds_read_b64_tr_b16 a[172:173], v93 offset:8200\nds_read_b64_tr_b16 a[174:175], v93 offset:8712\nds_read_b64_tr_b16 v[52:53], v93 offset:8256\nds_read_b64_tr_b16 v[54:55], v93 offset:8768\nds_read_b64_tr_b16 a[180:181], v93 offset:8264\nds_read_b64_tr_b16 a[182:183], v93 offset:8776\n" \
"s_waitcnt lgkmcnt(0)\ns_add_u32 m0, %[tb], 8192\ns_add_u32 s44, %[wo], 5767168\nbuffer_load_dwordx4 v92, %[rw], s44 offen nt lds\ns_add_u32 m0, %[tb], 9216\ns_add_u32 s44, %[wo], 5832704\nbuffer_load_dwordx4 v92, %[rw], s44 offen nt lds\ns_add_u32 m0, %[tb], 10240\n" \
"s_add_u32 s44, %[wo], 5898240\nbuffer_load_dwordx4 v92, %[rw], s44 offen nt lds\ns_add_u32 m0, %[tb], 11264\ns_add_u32 s44, %[wo], 5963776\nbuffer_load_dwordx4 v92, %[rw], s44 offen nt lds\ns_waitcnt vmcnt(32)\nds_read_b64_tr_b16 a[184:185], v94 offset:0\nds_read_b64_tr_b16 a[186:187], v94 offset:512\n" \
"ds_read_b64_tr_b16 a[192:193], v94 offset:8\nds_read_b64_tr_b16 a[194:195], v94 offset:520\nds_read_b64_tr_b16 a[200:201], v94 offset:64\nds_read_b64_tr_b16 a[202:203], v94 offset:576\nds_read_b64_tr_b16 v[48:49], v94 offset:72\nds_read_b64_tr_b16 v[50:51], v94 offset:584\ns_waitcnt lgkmcnt(0)\ns_add_u32 m0, %[lw], 0\n" \
"s_add_u32 s44, %[wo], 6029312\nbuffer_load_dwordx4 v92, %[rw], s44 offen nt lds\ns_add_u32 m0, %[lw], 1024\ns_add_u32 s44, %[wo], 6094848\nbuffer_load_dwordx4 v92, %[rw], s44 offen nt lds\ns_add_u32 m0, %[lw], 2048\ns_add_u32 s44, %[wo], 6160384\nbuffer_load_dwordx4 v92, %[rw], s44 offen nt lds\n" \
"s_add_u32 m0, %[lw], 3072\ns_add_u32 s44, %[wo], 6225920\nbuffer_load_dwordx4 v92, %[rw], s44 offen nt lds\ns_waitcnt vmcnt(32)\nds_read_b64_tr_b16 a[188:189], v94 offset:4096\nds_read_b64_tr_b16 a[190:191], v94 offset:4608\nds_read_b64_tr_b16 a[196:197], v94 offset:4104\nds_read_b64_tr_b16 a[198:199], v94 offset:4616\n" \
"ds_read_b64_tr_b16 a[204:205], v94 offset:4160\nds_read_b64_tr_b16 a[206:207], v94 offset:4672\nds_read_b64_tr_b16 v[44:45], v94 offset:4168\nds_read_b64_tr_b16 v[46:47], v94 offset:4680\ns_waitcnt lgkmcnt(0)\ns_add_u32 m0, %[lw], 4096\ns_add_u32 s44, %[wo], 6291456\nbuffer_load_dwordx4 v92, %[rw], s44 offen nt lds\n" \
"s_add_u32 m0, %[lw], 5120\ns_add_u32 s44, %[wo], 6356992\nbuffer_load_dwordx4 v92, %[rw], s44 offen nt lds\ns_add_u32 m0, %[lw], 6144\ns_add_u32 s44, %[wo], 6422528\nbuffer_load_dwordx4 v92, %[rw], s44 offen nt lds\ns_add_u32 m0, %[lw], 7168\ns_add_u32 s44, %[wo], 6488064\n" \
"buffer_load_dwordx4 v92, %[rw], s44 offen nt lds\ns_waitcnt vmcnt(32)\nds_read_b64_tr_b16 a[208:209], v94 offset:8192\nds_read_b64_tr_b16 a[210:211], v94 offset:8704\nds_read_b64_tr_b16 a[216:217], v94 offset:8200\nds_read_b64_tr_b16 a[218:219], v94 offset:8712\nds_read_b64_tr_b16 a[224:225], v94 offset:8256\nds_read_b64_tr_b16 a[226:227], v94 offset:8768\n" \
"ds_read_b64_tr_b16 a[232:233], v94 offset:8264\nds_read_b64_tr_b16 a[234:235], v94 offset:8776\ns_waitcnt lgkmcnt(0)\ns_add_u32 m0, %[lw], 8192\ns_add_u32 s44, %[wo], 6553600\nbuffer_load_dwordx4 v92, %[rw], s44 offen nt lds\ns_add_u32 m0, %[lw], 9216\ns_add_u32 s44, %[wo], 6619136\n" \
"buffer_load_dwordx4 v92, %[rw], s44 offen nt lds\ns_add_u32 m0, %[lw], 10240\ns_add_u32 s44, %[wo], 6684672\nbuffer_load_dwordx4 v92, %[rw], s44 offen nt lds\ns_add_u32 m0, %[lw], 11264\ns_add_u32 s44, %[wo], 6750208\nbuffer_load_dwordx4 v92, %[rw], s44 offen nt lds\ns_waitcnt vmcnt(32)\n" \
"ds_read_b64_tr_b16 a[212:213], v94 offset:12288\nds_read_b64_tr_b16 a[214:215], v94 offset:12800\nds_read_b64_tr_b16 a[220:221], v94 offset:12296\nds_read_b64_tr_b16 a[222:223], v94 offset:12808\nds_read_b64_tr_b16 a[228:229], v94 offset:12352\nds_read_b64_tr_b16 a[230:231], v94 offset:12864\nds_read_b64_tr_b16 a[236:237], v94 offset:12360\nds_read_b64_tr_b16 a[238:239], v94 offset:12872\n" \
"s_waitcnt lgkmcnt(0)\ns_add_u32 m0, %[lw], 12288\ns_add_u32 s44, %[wo], 6815744\nbuffer_load_dwordx4 v92, %[rw], s44 offen nt lds\ns_add_u32 m0, %[lw], 13312\ns_add_u32 s44, %[wo], 6881280\nbuffer_load_dwordx4 v92, %[rw], s44 offen nt lds\ns_add_u32 m0, %[lw], 14336\n" \
"s_add_u32 s44, %[wo], 6946816\nbuffer_load_dwordx4 v92, %[rw], s44 offen nt lds\ns_add_u32 m0, %[lw], 15360\ns_add_u32 s44, %[wo], 7012352\nbuffer_load_dwordx4 v92, %[rw], s44 offen nt lds\ns_waitcnt vmcnt(32)\nds_read_b64_tr_b16 a[240:241], v94 offset:16384\nds_read_b64_tr_b16 a[242:243], v94 offset:16896\n" \
"ds_read_b64_tr_b16 v[40:41], v94 offset:16392\nds_read_b64_tr_b16 v[42:43], v94 offset:16904\nds_read_b64_tr_b16 a[248:249], v94 offset:16448\nds_read_b64_tr_b16 a[250:251], v94 offset:16960\nds_read_b64_tr_b16 v[96:97], v94 offset:16456\nds_read_b64_tr_b16 v[98:99], v94 offset:16968\ns_waitcnt lgkmcnt(0)\ns_add_u32 m0, %[lw], 16384\n" \
"s_add_u32 s44, %[wo], 7077888\nbuffer_load_dwordx4 v92, %[rw], s44 offen nt lds\ns_add_u32 m0, %[lw], 17408\ns_add_u32 s44, %[wo], 7143424\nbuffer_load_dwordx4 v92, %[rw], s44 offen nt lds\ns_add_u32 m0, %[lw], 18432\ns_add_u32 s44, %[wo], 7208960\nbuffer_load_dwordx4 v92, %[rw], s44 offen nt lds\n" \
"s_add_u32 m0, %[lw], 19456\ns_add_u32 s44, %[wo], 7274496\nbuffer_load_dwordx4 v92, %[rw], s44 offen nt lds\ns_waitcnt vmcnt(32)\nds_read_b64_tr_b16 a[244:245], v94 offset:20480\nds_read_b64_tr_b16 a[246:247], v94 offset:20992\nds_read_b64_tr_b16 v[36:37], v94 offset:20488\nds_read_b64_tr_b16 v[38:39], v94 offset:21000\n" \
"ds_read_b64_tr_b16 a[252:253], v94 offset:20544\nds_read_b64_tr_b16 a[254:255], v94 offset:21056\nds_read_b64_tr_b16 v[100:101], v94 offset:20552\nds_read_b64_tr_b16 v[102:103], v94 offset:21064\ns_waitcnt lgkmcnt(0)\ns_add_u32 m0, %[lw], 20480\ns_add_u32 s44, %[wo], 7340032\nbuffer_load_dwordx4 v92, %[rw], s44 offen nt lds\n" \
"s_add_u32 m0, %[lw], 21504\ns_add_u32 s44, %[wo], 7405568\nbuffer_load_dwordx4 v92, %[rw], s44 offen nt lds\ns_add_u32 m0, %[lw], 22528\ns_add_u32 s44, %[wo], 7471104\nbuffer_load_dwordx4 v92, %[rw], s44 offen nt lds\ns_add_u32 m0, %[lw], 23552\ns_add_u32 s44, %[wo], 7536640\n" \
"buffer_load_dwordx4 v92, %[rw], s44 offen nt lds\ns_waitcnt vmcnt(32)\nds_read_b64_tr_b16 v[104:105], v93 offset:0\nds_read_b64_tr_b16 v[106:107], v93 offset:512\nds_read_b64_tr_b16 v[112:113], v93 offset:8\nds_read_b64_tr_b16 v[114:115], v93 offset:520\nds_read_b64_tr_b16 v[32:33], v93 offset:64\nds_read_b64_tr_b16 v[34:35], v93 offset:576\n" \
"ds_read_b64_tr_b16 v[120:121], v93 offset:72\nds_read_b64_tr_b16 v[122:123], v93 offset:584\ns_waitcnt lgkmcnt(0)\ns_add_u32 m0, %[tb], 0\ns_add_u32 s44, %[wo], 7602176\nbuffer_load_dwordx4 v92, %[rw], s44 offen nt lds\ns_add_u32 m0, %[tb], 1024\ns_add_u32 s44, %[wo], 7667712\n" \
"buffer_load_dwordx4 v92, %[rw], s44 offen nt lds\ns_add_u32 m0, %[tb], 2048\ns_add_u32 s44, %[wo], 7733248\nbuffer_load_dwordx4 v92, %[rw], s44 offen nt lds\ns_add_u32 m0, %[tb], 3072\ns_add_u32 s44, %[wo], 7798784\nbuffer_load_dwordx4 v92, %[rw], s44 offen nt lds\ns_waitcnt vmcnt(32)\n" \
"ds_read_b64_tr_b16 v[108:109], v93 offset:4096\nds_read_b64_tr_b16 v[110:111], v93 offset:4608\nds_read_b64_tr_b16 v[116:117], v93 offset:4104\nds_read_b64_tr_b16 v[118:119], v93 offset:4616\nds_read_b64_tr_b16 v[28:29], v93 offset:4160\nds_read_b64_tr_b16 v[30:31], v93 offset:4672\nds_read_b64_tr_b16 v[124:125], v93 offset:4168\nds_read_b64_tr_b16 v[126:127], v93 offset:4680\n" \
"s_waitcnt lgkmcnt(0)\ns_add_u32 m0, %[tb], 4096\ns_add_u32 s44, %[wo], 7864320\nbuffer_load_dwordx4 v92, %[rw], s44 offen nt lds\ns_add_u32 m0, %[tb], 5120\ns_add_u32 s44, %[wo], 7929856\nbuffer_load_dwordx4 v92, %[rw], s44 offen nt lds\ns_add_u32 m0, %[tb], 6144\n" \
"s_add_u32 s44, %[wo], 7995392\nbuffer_load_dwordx4 v92, %[rw], s44 offen nt lds\ns_add_u32 m0, %[tb], 7168\ns_add_u32 s44, %[wo], 8060928\nbuffer_load_dwordx4 v92, %[rw], s44 offen nt lds\ns_waitcnt vmcnt(32)\nds_read_b64_tr_b16 v[128:129], v93 offset:8192\nds_read_b64_tr_b16 v[130:131], v93 offset:8704\n" \
"ds_read_b64_tr_b16 v[136:137], v93 offset:8200\nds_read_b64_tr_b16 v[138:139], v93 offset:8712\nds_read_b64_tr_b16 v[144:145], v93 offset:8256\nds_read_b64_tr_b16 v[146:147], v93 offset:8768\nds_read_b64_tr_b16 v[24:25], v93 offset:8264\nds_read_b64_tr_b16 v[26:27], v93 offset:8776\ns_waitcnt lgkmcnt(0)\ns_add_u32 m0, %[tb], 8192\n" \
"s_add_u32 s44, %[wo], 8126464\nbuffer_load_dwordx4 v92, %[rw], s44 offen nt lds\ns_add_u32 m0, %[tb], 9216\ns_add_u32 s44, %[wo], 8192000\nbuffer_load_dwordx4 v92, %[rw], s44 offen nt lds\ns_add_u32 m0, %[tb], 10240\ns_add_u32 s44, %[wo], 8257536\nbuffer_load_dwordx4 v92, %[rw], s44 offen nt lds\n" \
"s_add_u32 m0, %[tb], 11264\ns_add_u32 s44, %[wo], 8323072\nbuffer_load_dwordx4 v92, %[rw], s44 offen nt lds\ns_waitcnt vmcnt(32)\nds_read_b64_tr_b16 v[132:133], v94 offset:0\nds_read_b64_tr_b16 v[134:135], v94 offset:512\nds_read_b64_tr_b16 v[140:141], v94 offset:8\nds_read_b64_tr_b16 v[142:143], v94 offset:520\n" \
"ds_read_b64_tr_b16 v[148:149], v94 offset:64\nds_read_b64_tr_b16 v[150:151], v94 offset:576\nds_read_b64_tr_b16 v[20:21], v94 offset:72\nds_read_b64_tr_b16 v[22:23], v94 offset:584\ns_waitcnt lgkmcnt(0)\nds_write_b128 v1, v[88:91] offset:0\nds_write_b128 v1, v[84:87] offset:1024\nds_write_b128 v1, v[80:83] offset:2048\n" \
"ds_write_b128 v1, v[76:79] offset:3072\ns_waitcnt vmcnt(28)\nds_read_b64_tr_b16 v[152:153], v94 offset:4096\nds_read_b64_tr_b16 v[154:155], v94 offset:4608\nds_read_b64_tr_b16 v[160:161], v94 offset:4104\nds_read_b64_tr_b16 v[162:163], v94 offset:4616\nds_read_b64_tr_b16 v[168:169], v94 offset:4160\nds_read_b64_tr_b16 v[170:171], v94 offset:4672\n" \
"ds_read_b64_tr_b16 v[176:177], v94 offset:4168\nds_read_b64_tr_b16 v[178:179], v94 offset:4680\ns_waitcnt lgkmcnt(0)\nds_write_b128 v1, v[72:75] offset:4096\nds_write_b128 v1, v[68:71] offset:5120\nds_write_b128 v1, v[64:67] offset:6144\nds_write_b128 v1, v[60:63] offset:7168\ns_waitcnt vmcnt(24)\n" \
"ds_read_b64_tr_b16 v[156:157], v94 offset:8192\nds_read_b64_tr_b16 v[158:159], v94 offset:8704\nds_read_b64_tr_b16 v[164:165], v94 offset:8200\nds_read_b64_tr_b16 v[166:167], v94 offset:8712\nds_read_b64_tr_b16 v[172:173], v94 offset:8256\nds_read_b64_tr_b16 v[174:175], v94 offset:8768\nds_read_b64_tr_b16 v[180:181], v94 offset:8264\nds_read_b64_tr_b16 v[182:183], v94 offset:8776\n" \
"s_waitcnt lgkmcnt(0)\nds_write_b128 v1, v[56:59] offset:8192\nds_write_b128 v1, v[52:55] offset:9216\nds_write_b128 v1, v[48:51] offset:10240\nds_write_b128 v1, v[44:47] offset:11264\ns_waitcnt vmcnt(20)\nds_read_b64_tr_b16 v[184:185], v94 offset:12288\nds_read_b64_tr_b16 v[186:187], v94 offset:12800\n" \
"ds_read_b64_tr_b16 v[44:45], v94 offset:12296\nds_read_b64_tr_b16 v[46:47], v94 offset:12808\nds_read_b64_tr_b16 v[192:193], v94 offset:12352\nds_read_b64_tr_b16 v[194:195], v94 offset:12864\nds_read_b64_tr_b16 v[200:201], v94 offset:12360\nds_read_b64_tr_b16 v[202:203], v94 offset:12872\ns_waitcnt lgkmcnt(0)\nds_write_b128 v1, v[40:43] offset:12288\n" \
"ds_write_b128 v1, v[36:39] offset:13312\nds_write_b128 v1, v[32:35] offset:14336\nds_write_b128 v1, v[28:31] offset:15360\ns_waitcnt vmcnt(16)\nds_read_b64_tr_b16 v[188:189], v94 offset:16384\nds_read_b64_tr_b16 v[190:191], v94 offset:16896\nds_read_b64_tr_b16 v[28:29], v94 offset:16392\nds_read_b64_tr_b16 v[30:31], v94 offset:16904\n" \
"ds_read_b64_tr_b16 v[196:197], v94 offset:16448\nds_read_b64_tr_b16 v[198:199], v94 offset:16960\nds_read_b64_tr_b16 v[204:205], v94 offset:16456\nds_read_b64_tr_b16 v[206:207], v94 offset:16968\ns_waitcnt lgkmcnt(0)\nds_write_b128 v1, v[24:27] offset:16384\nds_write_b128 v1, v[20:23] offset:17408\nds_write_b128 v1, v[44:47] offset:18432\n" \
"ds_write_b128 v1, v[28:31] offset:19456\ns_waitcnt vmcnt(12)\nds_read_b64_tr_b16 v[208:209], v94 offset:20480\nds_read_b64_tr_b16 v[210:211], v94 offset:20992\nds_read_b64_tr_b16 v[216:217], v94 offset:20488\nds_read_b64_tr_b16 v[218:219], v94 offset:21000\nds_read_b64_tr_b16 v[28:29], v94 offset:20544\nds_read_b64_tr_b16 v[30:31], v94 offset:21056\n" \
"ds_read_b64_tr_b16 v[224:225], v94 offset:20552\nds_read_b64_tr_b16 v[226:227], v94 offset:21064\ns_waitcnt lgkmcnt(0)\nds_write_b128 v1, v[28:31] offset:20480\ns_waitcnt vmcnt(8)\nds_read_b64_tr_b16 v[212:213], v93 offset:0\nds_read_b64_tr_b16 v[214:215], v93 offset:512\nds_read_b64_tr_b16 v[220:221], v93 offset:8\n" \
"ds_read_b64_tr_b16 v[222:223], v93 offset:520\nds_read_b64_tr_b16 v[28:29], v93 offset:64\nds_read_b64_tr_b16 v[30:31], v93 offset:576\nds_read_b64_tr_b16 v[228:229], v93 offset:72\nds_read_b64_tr_b16 v[230:231], v93 offset:584\ns_waitcnt lgkmcnt(0)\nds_write_b128 v1, v[28:31] offset:21504\ns_waitcnt vmcnt(4)\n" \
"ds_read_b64_tr_b16 v[232:233], v93 offset:4096\nds_read_b64_tr_b16 v[234:235], v93 offset:4608\nds_read_b64_tr_b16 v[240:241], v93 offset:4104\nds_read_b64_tr_b16 v[242:243], v93 offset:4616\nds_read_b64_tr_b16 v[248:249], v93 offset:4160\nds_read_b64_tr_b16 v[250:251], v93 offset:4672\nds_read_b64_tr_b16 v[28:29], v93 offset:4168\nds_read_b64_tr_b16 v[30:31], v93 offset:4680\n" \
"s_waitcnt lgkmcnt(0)\nds_write_b128 v1, v[28:31] offset:22528\ns_waitcnt vmcnt(0)\nds_read_b64_tr_b16 v[236:237], v93 offset:8192\nds_read_b64_tr_b16 v[238:239], v93 offset:8704\nds_read_b64_tr_b16 v[244:245], v93 offset:8200\nds_read_b64_tr_b16 v[246:247], v93 offset:8712\nds_read_b64_tr_b16 v[252:253], v93 offset:8256\n" \
"ds_read_b64_tr_b16 v[254:255], v93 offset:8768\nds_read_b64_tr_b16 v[28:29], v93 offset:8264\nds_read_b64_tr_b16 v[30:31], v93 offset:8776\ns_waitcnt lgkmcnt(0)\nds_write_b128 v1, v[28:31] offset:23552\ns_waitcnt lgkmcnt(0)\ns_barrier\ns_mov_b32 s43, 0\n" \
".Lqk_hb%=:\nbuffer_load_dwordx2 v[16:17], off, s[36:39], s45 sc1\ns_waitcnt vmcnt(0)\nv_cmp_ne_u32 vcc, v95, v16\ns_cbranch_vccnz .Lqk_hb1%=\ns_sleep 4\ns_add_u32 s43, s43, 1\ns_cmp_lt_u32 s43, 262144\n" \
"s_cbranch_scc1 .Lqk_hb%=\ns_branch .Lqk_abort%=\n.Lqk_hb1%=:\nv_readfirstlane_b32 s44, v17\ns_and_b32 s44, s44, 0x200000\ns_add_u32 s36, s36, s44\ns_addc_u32 s37, s37, 0\ns_mov_b32 s40, -1\n" \
"s_mov_b32 s43, 0\ns_mov_b32 s61, 0x7fff7fff\ns_mov_b32 s58, 0x3e0293ee\ns_mov_b32 s59, 0xbe0293ee\ns_lshl_b32 s57, %[w], 12\ns_mov_b32 s62, 0xff00ff00\ns_mov_b32 s63, 0xff00ff00\ns_mov_b32 s56, 0\n" \
"s_cmp_eq_u32 %[w], 3\ns_cbranch_scc1 .Lqk_fetch0%=\n.Lqk_pwait%=:\ns_barrier\nds_read_b128 v[80:83], v2 offset:0\nds_read_b128 v[84:87], v2 offset:256\nds_read_b128 v[88:91], v2 offset:2048\n.Lqk_proj%=:\n" \
"s_waitcnt lgkmcnt(2)\nv_mfma_f32_16x16x32_bf16 v[16:19], a[0:3], v[80:83], 0\nds_read_b128 v[92:95], v2 offset:2304\nv_mfma_f32_16x16x32_bf16 v[20:23], a[8:11], v[80:83], 0\nds_read_b128 v[72:75], v1 offset:0\nv_mfma_f32_16x16x32_bf16 v[24:27], a[16:19], v[80:83], 0\nv_mfma_f32_16x16x32_bf16 v[28:31], a[24:27], v[80:83], 0\ns_waitcnt lgkmcnt(3)\n" \
"v_mfma_f32_16x16x32_bf16 v[32:35], a[0:3], v[84:87], 0\nds_read_b128 v[80:83], v2 offset:4096\nv_mfma_f32_16x16x32_bf16 v[36:39], a[8:11], v[84:87], 0\nv_mfma_f32_16x16x32_bf16 v[40:43], a[16:19], v[84:87], 0\nv_mfma_f32_16x16x32_bf16 v[44:47], a[24:27], v[84:87], 0\ns_waitcnt lgkmcnt(3)\nv_mfma_f32_16x16x32_bf16 v[16:19], a[4:7], v[88:91], v[16:19]\nds_read_b128 v[84:87], v2 offset:4352\n" \
"v_mfma_f32_16x16x32_bf16 v[20:23], a[12:15], v[88:91], v[20:23]\nds_read_b128 v[76:79], v1 offset:1024\nv_mfma_f32_16x16x32_bf16 v[24:27], a[20:23], v[88:91], v[24:27]\nv_mfma_f32_16x16x32_bf16 v[28:31], a[28:31], v[88:91], v[28:31]\ns_waitcnt lgkmcnt(4)\nv_mfma_f32_16x16x32_bf16 v[32:35], a[4:7], v[92:95], v[32:35]\nds_read_b128 v[88:91], v2 offset:6144\nv_mfma_f32_16x16x32_bf16 v[36:39], a[12:15], v[92:95], v[36:39]\n" \
"v_mfma_f32_16x16x32_bf16 v[40:43], a[20:23], v[92:95], v[40:43]\nv_mfma_f32_16x16x32_bf16 v[44:47], a[28:31], v[92:95], v[44:47]\ns_waitcnt lgkmcnt(3)\nv_mfma_f32_16x16x32_bf16 v[16:19], a[32:35], v[80:83], v[16:19]\nds_read_b128 v[92:95], v2 offset:6400\nv_mfma_f32_16x16x32_bf16 v[20:23], v[72:75], v[80:83], v[20:23]\nds_read_b128 v[64:67], v1 offset:2048\nv_mfma_f32_16x16x32_bf16 v[24:27], a[40:43], v[80:83], v[24:27]\n" \
"v_mfma_f32_16x16x32_bf16 v[28:31], a[48:51], v[80:83], v[28:31]\ns_waitcnt lgkmcnt(4)\nv_mfma_f32_16x16x32_bf16 v[32:35], a[32:35], v[84:87], v[32:35]\nds_read_b128 v[80:83], v2 offset:8192\nv_mfma_f32_16x16x32_bf16 v[36:39], v[72:75], v[84:87], v[36:39]\nv_mfma_f32_16x16x32_bf16 v[40:43], a[40:43], v[84:87], v[40:43]\nv_mfma_f32_16x16x32_bf16 v[44:47], a[48:51], v[84:87], v[44:47]\ns_waitcnt lgkmcnt(3)\n" \
"v_mfma_f32_16x16x32_bf16 v[16:19], a[36:39], v[88:91], v[16:19]\nds_read_b128 v[84:87], v2 offset:8448\nv_mfma_f32_16x16x32_bf16 v[20:23], v[76:79], v[88:91], v[20:23]\nds_read_b128 v[68:71], v1 offset:3072\nv_mfma_f32_16x16x32_bf16 v[24:27], a[44:47], v[88:91], v[24:27]\nv_mfma_f32_16x16x32_bf16 v[28:31], a[52:55], v[88:91], v[28:31]\ns_waitcnt lgkmcnt(4)\nv_mfma_f32_16x16x32_bf16 v[32:35], a[36:39], v[92:95], v[32:35]\n" \
"ds_read_b128 v[88:91], v2 offset:10240\nv_mfma_f32_16x16x32_bf16 v[36:39], v[76:79], v[92:95], v[36:39]\nv_mfma_f32_16x16x32_bf16 v[40:43], a[44:47], v[92:95], v[40:43]\nv_mfma_f32_16x16x32_bf16 v[44:47], a[52:55], v[92:95], v[44:47]\ns_waitcnt lgkmcnt(3)\nv_mfma_f32_16x16x32_bf16 v[16:19], a[56:59], v[80:83], v[16:19]\nds_read_b128 v[92:95], v2 offset:10496\nv_mfma_f32_16x16x32_bf16 v[20:23], a[64:67], v[80:83], v[20:23]\n" \
"ds_read_b128 v[72:75], v1 offset:4096\nv_mfma_f32_16x16x32_bf16 v[24:27], v[64:67], v[80:83], v[24:27]\nv_mfma_f32_16x16x32_bf16 v[28:31], a[72:75], v[80:83], v[28:31]\ns_waitcnt lgkmcnt(4)\nv_mfma_f32_16x16x32_bf16 v[32:35], a[56:59], v[84:87], v[32:35]\nds_read_b128 v[80:83], v2 offset:12288\nv_mfma_f32_16x16x32_bf16 v[36:39], a[64:67], v[84:87], v[36:39]\nv_mfma_f32_16x16x32_bf16 v[40:43], v[64:67], v[84:87], v[40:43]\n" \
"v_mfma_f32_16x16x32_bf16 v[44:47], a[72:75], v[84:87], v[44:47]\ns_waitcnt lgkmcnt(3)\nv_mfma_f32_16x16x32_bf16 v[16:19], a[60:63], v[88:91], v[16:19]\nds_read_b128 v[84:87], v2 offset:12544\nv_mfma_f32_16x16x32_bf16 v[20:23], a[68:71], v[88:91], v[20:23]\nds_read_b128 v[76:79], v1 offset:5120\nv_mfma_f32_16x16x32_bf16 v[24:27], v[68:71], v[88:91], v[24:27]\nv_mfma_f32_16x16x32_bf16 v[28:31], a[76:79], v[88:91], v[28:31]\n" \
"s_waitcnt lgkmcnt(4)\nv_mfma_f32_16x16x32_bf16 v[32:35], a[60:63], v[92:95], v[32:35]\nds_read_b128 v[88:91], v2 offset:14336\nv_mfma_f32_16x16x32_bf16 v[36:39], a[68:71], v[92:95], v[36:39]\nv_mfma_f32_16x16x32_bf16 v[40:43], v[68:71], v[92:95], v[40:43]\nv_mfma_f32_16x16x32_bf16 v[44:47], a[76:79], v[92:95], v[44:47]\n.Lqk_pj%=:\ns_waitcnt lgkmcnt(3)\n" \
"v_mfma_f32_16x16x32_bf16 v[16:19], a[80:83], v[80:83], v[16:19]\nds_read_b128 v[92:95], v2 offset:14592\nv_mfma_f32_16x16x32_bf16 v[20:23], a[88:91], v[80:83], v[20:23]\ns_add_u32 s40, s40, 1\nv_mfma_f32_16x16x32_bf16 v[24:27], a[96:99], v[80:83], v[24:27]\ns_lshl_b32 s44, s40, 17\nv_mfma_f32_16x16x32_bf16 v[28:31], v[72:75], v[80:83], v[28:31]\ns_add_u32 s44, s44, %[xn]\n" \
"s_waitcnt lgkmcnt(3)\nv_mfma_f32_16x16x32_bf16 v[32:35], a[80:83], v[84:87], v[32:35]\ns_barrier\nds_read_b128 v[80:83], v2 offset:16384\nv_mfma_f32_16x16x32_bf16 v[36:39], a[88:91], v[84:87], v[36:39]\ns_lshr_b32 s45, s44, 8\nv_mfma_f32_16x16x32_bf16 v[40:43], a[96:99], v[84:87], v[40:43]\ns_and_b32 s45, s45, 0x2000\n" \
"v_mfma_f32_16x16x32_bf16 v[44:47], v[72:75], v[84:87], v[44:47]\ns_xor_b32 s41, s44, s45\ns_waitcnt lgkmcnt(2)\nv_mfma_f32_16x16x32_bf16 v[16:19], a[84:87], v[88:91], v[16:19]\nds_read_b128 v[84:87], v2 offset:16640\nv_mfma_f32_16x16x32_bf16 v[20:23], a[92:95], v[88:91], v[20:23]\ns_add_u32 s41, s41, %[xw]\nv_mfma_f32_16x16x32_bf16 v[24:27], a[100:103], v[88:91], v[24:27]\n" \
"s_and_b32 s42, s40, 3\ns_add_u32 m0, s57, 32768\ns_add_u32 s60, s41, 65536\nbuffer_load_dwordx4 v0, s[36:39], s60 offen offset:0 sc1 lds\nv_mfma_f32_16x16x32_bf16 v[28:31], v[76:79], v[88:91], v[28:31]\ns_lshl_b32 s42, s42, 15\ns_waitcnt lgkmcnt(2)\nv_mfma_f32_16x16x32_bf16 v[32:35], a[84:87], v[92:95], v[32:35]\n" \
"ds_read_b128 v[88:91], v2 offset:18432\nv_mfma_f32_16x16x32_bf16 v[36:39], a[92:95], v[92:95], v[36:39]\ns_and_b32 s44, s40, 1\nv_mfma_f32_16x16x32_bf16 v[40:43], a[100:103], v[92:95], v[40:43]\ns_lshl_b32 s55, s44, 17\nbuffer_load_dwordx4 v0, s[36:39], s60 offen offset:1024 sc1 lds\nv_mfma_f32_16x16x32_bf16 v[44:47], v[76:79], v[92:95], v[44:47]\ns_add_u32 s56, s56, %[fs]\n" \
"s_waitcnt lgkmcnt(2)\nv_mfma_f32_16x16x32_bf16 v[16:19], a[104:107], v[80:83], v[16:19]\nds_read_b128 v[92:95], v2 offset:18688\nv_mfma_f32_16x16x32_bf16 v[20:23], a[112:115], v[80:83], v[20:23]\nds_read_b128 v[72:75], v1 offset:6144\nv_mfma_f32_16x16x32_bf16 v[24:27], a[120:123], v[80:83], v[24:27]\nbuffer_load_dwordx4 v0, s[36:39], s60 offen offset:2048 sc1 lds\nv_mfma_f32_16x16x32_bf16 v[28:31], a[128:131], v[80:83], v[28:31]\n" \
"s_waitcnt lgkmcnt(3)\nv_mfma_f32_16x16x32_bf16 v[32:35], a[104:107], v[84:87], v[32:35]\nds_read_b128 v[80:83], v2 offset:20480\nv_mfma_f32_16x16x32_bf16 v[36:39], a[112:115], v[84:87], v[36:39]\nv_mfma_f32_16x16x32_bf16 v[40:43], a[120:123], v[84:87], v[40:43]\nbuffer_load_dwordx4 v0, s[36:39], s60 offen offset:3072 sc1 lds\nv_mfma_f32_16x16x32_bf16 v[44:47], a[128:131], v[84:87], v[44:47]\ns_waitcnt lgkmcnt(3)\n" \
"v_mfma_f32_16x16x32_bf16 v[16:19], a[108:111], v[88:91], v[16:19]\nds_read_b128 v[84:87], v2 offset:20736\nv_mfma_f32_16x16x32_bf16 v[20:23], a[116:119], v[88:91], v[20:23]\nds_read_b128 v[76:79], v1 offset:7168\nv_mfma_f32_16x16x32_bf16 v[24:27], a[124:127], v[88:91], v[24:27]\nv_mfma_f32_16x16x32_bf16 v[28:31], a[132:135], v[88:91], v[28:31]\ns_waitcnt lgkmcnt(4)\nv_mfma_f32_16x16x32_bf16 v[32:35], a[108:111], v[92:95], v[32:35]\n" \
"ds_read_b128 v[88:91], v2 offset:22528\nv_mfma_f32_16x16x32_bf16 v[36:39], a[116:119], v[92:95], v[36:39]\nv_mfma_f32_16x16x32_bf16 v[40:43], a[124:127], v[92:95], v[40:43]\nv_mfma_f32_16x16x32_bf16 v[44:47], a[132:135], v[92:95], v[44:47]\ns_waitcnt lgkmcnt(3)\nv_mfma_f32_16x16x32_bf16 v[16:19], a[136:139], v[80:83], v[16:19]\nds_read_b128 v[92:95], v2 offset:22784\nv_mfma_f32_16x16x32_bf16 v[20:23], v[72:75], v[80:83], v[20:23]\n" \
"ds_read_b128 v[64:67], v1 offset:8192\nv_mfma_f32_16x16x32_bf16 v[24:27], a[144:147], v[80:83], v[24:27]\nv_mfma_f32_16x16x32_bf16 v[28:31], a[152:155], v[80:83], v[28:31]\ns_waitcnt lgkmcnt(4)\nv_mfma_f32_16x16x32_bf16 v[32:35], a[136:139], v[84:87], v[32:35]\nds_read_b128 v[80:83], v2 offset:24576\nv_mfma_f32_16x16x32_bf16 v[36:39], v[72:75], v[84:87], v[36:39]\nv_mfma_f32_16x16x32_bf16 v[40:43], a[144:147], v[84:87], v[40:43]\n" \
"v_mfma_f32_16x16x32_bf16 v[44:47], a[152:155], v[84:87], v[44:47]\ns_waitcnt lgkmcnt(3)\nv_mfma_f32_16x16x32_bf16 v[16:19], a[140:143], v[88:91], v[16:19]\nds_read_b128 v[84:87], v2 offset:24832\nv_mfma_f32_16x16x32_bf16 v[20:23], v[76:79], v[88:91], v[20:23]\nds_read_b128 v[68:71], v1 offset:9216\nv_mfma_f32_16x16x32_bf16 v[24:27], a[148:151], v[88:91], v[24:27]\nv_mfma_f32_16x16x32_bf16 v[28:31], a[156:159], v[88:91], v[28:31]\n" \
"s_waitcnt lgkmcnt(4)\nv_mfma_f32_16x16x32_bf16 v[32:35], a[140:143], v[92:95], v[32:35]\nds_read_b128 v[88:91], v2 offset:26624\nv_mfma_f32_16x16x32_bf16 v[36:39], v[76:79], v[92:95], v[36:39]\nv_mfma_f32_16x16x32_bf16 v[40:43], a[148:151], v[92:95], v[40:43]\nv_mfma_f32_16x16x32_bf16 v[44:47], a[156:159], v[92:95], v[44:47]\ns_waitcnt lgkmcnt(3)\nv_mfma_f32_16x16x32_bf16 v[16:19], a[160:163], v[80:83], v[16:19]\n" \
"ds_read_b128 v[92:95], v2 offset:26880\nv_mfma_f32_16x16x32_bf16 v[20:23], a[168:171], v[80:83], v[20:23]\nds_read_b128 v[72:75], v1 offset:10240\nv_mfma_f32_16x16x32_bf16 v[24:27], v[64:67], v[80:83], v[24:27]\nv_mfma_f32_16x16x32_bf16 v[28:31], a[176:179], v[80:83], v[28:31]\ns_waitcnt lgkmcnt(4)\nv_mfma_f32_16x16x32_bf16 v[32:35], a[160:163], v[84:87], v[32:35]\nds_read_b128 v[80:83], v2 offset:28672\n" \
"v_mfma_f32_16x16x32_bf16 v[36:39], a[168:171], v[84:87], v[36:39]\nv_mfma_f32_16x16x32_bf16 v[40:43], v[64:67], v[84:87], v[40:43]\nv_mfma_f32_16x16x32_bf16 v[44:47], a[176:179], v[84:87], v[44:47]\ns_waitcnt lgkmcnt(3)\nv_mfma_f32_16x16x32_bf16 v[16:19], a[164:167], v[88:91], v[16:19]\nds_read_b128 v[84:87], v2 offset:28928\nv_mfma_f32_16x16x32_bf16 v[20:23], a[172:175], v[88:91], v[20:23]\nds_read_b128 v[76:79], v1 offset:11264\n" \
"v_mfma_f32_16x16x32_bf16 v[24:27], v[68:71], v[88:91], v[24:27]\nv_mfma_f32_16x16x32_bf16 v[28:31], a[180:183], v[88:91], v[28:31]\ns_waitcnt lgkmcnt(4)\nv_mfma_f32_16x16x32_bf16 v[32:35], a[164:167], v[92:95], v[32:35]\nds_read_b128 v[88:91], v2 offset:30720\nv_mfma_f32_16x16x32_bf16 v[36:39], a[172:175], v[92:95], v[36:39]\nv_mfma_f32_16x16x32_bf16 v[40:43], v[68:71], v[92:95], v[40:43]\nv_mfma_f32_16x16x32_bf16 v[44:47], a[180:183], v[92:95], v[44:47]\n" \
"s_waitcnt lgkmcnt(3)\nv_mfma_f32_16x16x32_bf16 v[16:19], a[184:187], v[80:83], v[16:19]\nds_read_b128 v[92:95], v2 offset:30976\nv_mfma_f32_16x16x32_bf16 v[20:23], a[192:195], v[80:83], v[20:23]\nv_mfma_f32_16x16x32_bf16 v[24:27], a[200:203], v[80:83], v[24:27]\nv_mfma_f32_16x16x32_bf16 v[28:31], v[72:75], v[80:83], v[28:31]\ns_waitcnt lgkmcnt(3)\nv_mfma_f32_16x16x32_bf16 v[32:35], a[184:187], v[84:87], v[32:35]\n" \
"s_waitcnt vmcnt(0)\ns_barrier\nds_read_b128 v[48:51], v2 offset:32768\nv_mfma_f32_16x16x32_bf16 v[36:39], a[192:195], v[84:87], v[36:39]\nv_mfma_f32_16x16x32_bf16 v[40:43], a[200:203], v[84:87], v[40:43]\ns_add_u32 m0, s57, 0\ns_add_u32 s60, s41, 98304\nbuffer_load_dwordx4 v0, s[36:39], s60 offen offset:0 sc1 lds\n" \
"v_mfma_f32_16x16x32_bf16 v[44:47], v[72:75], v[84:87], v[44:47]\ns_waitcnt lgkmcnt(2)\nv_mfma_f32_16x16x32_bf16 v[16:19], a[188:191], v[88:91], v[16:19]\nds_read_b128 v[52:55], v2 offset:33024\nv_mfma_f32_16x16x32_bf16 v[20:23], a[196:199], v[88:91], v[20:23]\nv_mfma_f32_16x16x32_bf16 v[24:27], a[204:207], v[88:91], v[24:27]\nbuffer_load_dwordx4 v0, s[36:39], s60 offen offset:1024 sc1 lds\nv_mfma_f32_16x16x32_bf16 v[28:31], v[76:79], v[88:91], v[28:31]\n" \
"s_waitcnt lgkmcnt(2)\nv_mfma_f32_16x16x32_bf16 v[32:35], a[188:191], v[92:95], v[32:35]\nds_read_b128 v[56:59], v2 offset:34816\nv_mfma_f32_16x16x32_bf16 v[36:39], a[196:199], v[92:95], v[36:39]\nv_mfma_f32_16x16x32_bf16 v[40:43], a[204:207], v[92:95], v[40:43]\nbuffer_load_dwordx4 v0, s[36:39], s60 offen offset:2048 sc1 lds\nv_mfma_f32_16x16x32_bf16 v[44:47], v[76:79], v[92:95], v[44:47]\ns_waitcnt lgkmcnt(2)\n" \
"v_mfma_f32_16x16x32_bf16 v[16:19], a[208:211], v[48:51], v[16:19]\nds_read_b128 v[60:63], v2 offset:35072\nv_mfma_f32_16x16x32_bf16 v[20:23], a[216:219], v[48:51], v[20:23]\nds_read_b128 v[72:75], v1 offset:12288\nv_mfma_f32_16x16x32_bf16 v[24:27], a[224:227], v[48:51], v[24:27]\nbuffer_load_dwordx4 v0, s[36:39], s60 offen offset:3072 sc1 lds\nv_mfma_f32_16x16x32_bf16 v[28:31], a[232:235], v[48:51], v[28:31]\ns_waitcnt lgkmcnt(3)\n" \
"v_mfma_f32_16x16x32_bf16 v[32:35], a[208:211], v[52:55], v[32:35]\nds_read_b128 v[48:51], v2 offset:36864\nv_mfma_f32_16x16x32_bf16 v[36:39], a[216:219], v[52:55], v[36:39]\nv_mfma_f32_16x16x32_bf16 v[40:43], a[224:227], v[52:55], v[40:43]\nv_mfma_f32_16x16x32_bf16 v[44:47], a[232:235], v[52:55], v[44:47]\ns_waitcnt lgkmcnt(3)\nv_mfma_f32_16x16x32_bf16 v[16:19], a[212:215], v[56:59], v[16:19]\nds_read_b128 v[52:55], v2 offset:37120\n" \
"v_mfma_f32_16x16x32_bf16 v[20:23], a[220:223], v[56:59], v[20:23]\nds_read_b128 v[76:79], v1 offset:13312\nv_mfma_f32_16x16x32_bf16 v[24:27], a[228:231], v[56:59], v[24:27]\nv_mfma_f32_16x16x32_bf16 v[28:31], a[236:239], v[56:59], v[28:31]\ns_waitcnt lgkmcnt(4)\nv_mfma_f32_16x16x32_bf16 v[32:35], a[212:215], v[60:63], v[32:35]\nds_read_b128 v[56:59], v2 offset:38912\nv_mfma_f32_16x16x32_bf16 v[36:39], a[220:223], v[60:63], v[36:39]\n" \
"v_mfma_f32_16x16x32_bf16 v[40:43], a[228:231], v[60:63], v[40:43]\nv_mfma_f32_16x16x32_bf16 v[44:47], a[236:239], v[60:63], v[44:47]\ns_waitcnt lgkmcnt(3)\nv_mfma_f32_16x16x32_bf16 v[16:19], a[240:243], v[48:51], v[16:19]\nds_read_b128 v[60:63], v2 offset:39168\nv_mfma_f32_16x16x32_bf16 v[20:23], v[72:75], v[48:51], v[20:23]\nds_read_b128 v[64:67], v1 offset:14336\nv_mfma_f32_16x16x32_bf16 v[24:27], a[248:251], v[48:51], v[24:27]\n" \
"v_mfma_f32_16x16x32_bf16 v[28:31], v[96:99], v[48:51], v[28:31]\ns_waitcnt lgkmcnt(4)\nv_mfma_f32_16x16x32_bf16 v[32:35], a[240:243], v[52:55], v[32:35]\nds_read_b128 v[48:51], v2 offset:40960\nv_mfma_f32_16x16x32_bf16 v[36:39], v[72:75], v[52:55], v[36:39]\nv_mfma_f32_16x16x32_bf16 v[40:43], a[248:251], v[52:55], v[40:43]\nv_mfma_f32_16x16x32_bf16 v[44:47], v[96:99], v[52:55], v[44:47]\ns_waitcnt lgkmcnt(3)\n" \
"v_mfma_f32_16x16x32_bf16 v[16:19], a[244:247], v[56:59], v[16:19]\nds_read_b128 v[52:55], v2 offset:41216\nv_mfma_f32_16x16x32_bf16 v[20:23], v[76:79], v[56:59], v[20:23]\nds_read_b128 v[68:71], v1 offset:15360\nv_mfma_f32_16x16x32_bf16 v[24:27], a[252:255], v[56:59], v[24:27]\nv_mfma_f32_16x16x32_bf16 v[28:31], v[100:103], v[56:59], v[28:31]\ns_waitcnt lgkmcnt(4)\nv_mfma_f32_16x16x32_bf16 v[32:35], a[244:247], v[60:63], v[32:35]\n" \
"ds_read_b128 v[56:59], v2 offset:43008\nv_mfma_f32_16x16x32_bf16 v[36:39], v[76:79], v[60:63], v[36:39]\nv_mfma_f32_16x16x32_bf16 v[40:43], a[252:255], v[60:63], v[40:43]\nv_mfma_f32_16x16x32_bf16 v[44:47], v[100:103], v[60:63], v[44:47]\ns_waitcnt lgkmcnt(3)\nv_mfma_f32_16x16x32_bf16 v[16:19], v[104:107], v[48:51], v[16:19]\nds_read_b128 v[60:63], v2 offset:43264\nv_mfma_f32_16x16x32_bf16 v[20:23], v[112:115], v[48:51], v[20:23]\n" \
"ds_read_b128 v[72:75], v1 offset:16384\nv_mfma_f32_16x16x32_bf16 v[24:27], v[64:67], v[48:51], v[24:27]\nv_mfma_f32_16x16x32_bf16 v[28:31], v[120:123], v[48:51], v[28:31]\ns_waitcnt lgkmcnt(4)\nv_mfma_f32_16x16x32_bf16 v[32:35], v[104:107], v[52:55], v[32:35]\nds_read_b128 v[48:51], v2 offset:45056\nv_mfma_f32_16x16x32_bf16 v[36:39], v[112:115], v[52:55], v[36:39]\nv_mfma_f32_16x16x32_bf16 v[40:43], v[64:67], v[52:55], v[40:43]\n" \
"v_mfma_f32_16x16x32_bf16 v[44:47], v[120:123], v[52:55], v[44:47]\ns_waitcnt lgkmcnt(3)\nv_mfma_f32_16x16x32_bf16 v[16:19], v[108:111], v[56:59], v[16:19]\nds_read_b128 v[52:55], v2 offset:45312\nv_mfma_f32_16x16x32_bf16 v[20:23], v[116:119], v[56:59], v[20:23]\nds_read_b128 v[76:79], v1 offset:17408\nv_mfma_f32_16x16x32_bf16 v[24:27], v[68:71], v[56:59], v[24:27]\nv_mfma_f32_16x16x32_bf16 v[28:31], v[124:127], v[56:59], v[28:31]\n" \
"s_waitcnt lgkmcnt(4)\nv_mfma_f32_16x16x32_bf16 v[32:35], v[108:111], v[60:63], v[32:35]\nds_read_b128 v[56:59], v2 offset:47104\nv_mfma_f32_16x16x32_bf16 v[36:39], v[116:119], v[60:63], v[36:39]\nv_mfma_f32_16x16x32_bf16 v[40:43], v[68:71], v[60:63], v[40:43]\nv_mfma_f32_16x16x32_bf16 v[44:47], v[124:127], v[60:63], v[44:47]\ns_waitcnt lgkmcnt(3)\nv_mfma_f32_16x16x32_bf16 v[16:19], v[128:131], v[48:51], v[16:19]\n" \
"ds_read_b128 v[60:63], v2 offset:47360\nv_mfma_f32_16x16x32_bf16 v[20:23], v[136:139], v[48:51], v[20:23]\nv_mfma_f32_16x16x32_bf16 v[24:27], v[144:147], v[48:51], v[24:27]\nv_mfma_f32_16x16x32_bf16 v[28:31], v[72:75], v[48:51], v[28:31]\ns_waitcnt lgkmcnt(3)\nv_mfma_f32_16x16x32_bf16 v[32:35], v[128:131], v[52:55], v[32:35]\ns_waitcnt lgkmcnt(0)\ns_waitcnt vmcnt(0)\n" \
"s_barrier\nds_read_b128 v[48:51], v2 offset:0\nv_mfma_f32_16x16x32_bf16 v[36:39], v[136:139], v[52:55], v[36:39]\nv_mfma_f32_16x16x32_bf16 v[40:43], v[144:147], v[52:55], v[40:43]\nv_mfma_f32_16x16x32_bf16 v[44:47], v[72:75], v[52:55], v[44:47]\nv_mfma_f32_16x16x32_bf16 v[16:19], v[132:135], v[56:59], v[16:19]\nds_read_b128 v[52:55], v2 offset:256\nv_mfma_f32_16x16x32_bf16 v[20:23], v[140:143], v[56:59], v[20:23]\n" \
"v_mfma_f32_16x16x32_bf16 v[24:27], v[148:151], v[56:59], v[24:27]\nv_mfma_f32_16x16x32_bf16 v[28:31], v[76:79], v[56:59], v[28:31]\nv_mfma_f32_16x16x32_bf16 v[32:35], v[132:135], v[60:63], v[32:35]\nds_read_b128 v[56:59], v2 offset:2048\nv_mfma_f32_16x16x32_bf16 v[36:39], v[140:143], v[60:63], v[36:39]\nv_mfma_f32_16x16x32_bf16 v[40:43], v[148:151], v[60:63], v[40:43]\nv_mfma_f32_16x16x32_bf16 v[44:47], v[76:79], v[60:63], v[44:47]\ns_waitcnt lgkmcnt(2)\n" \
"v_mfma_f32_16x16x32_bf16 v[16:19], v[152:155], v[48:51], v[16:19]\nds_read_b128 v[60:63], v2 offset:2304\nv_mfma_f32_16x16x32_bf16 v[20:23], v[160:163], v[48:51], v[20:23]\nds_read_b128 v[72:75], v1 offset:18432\nv_mfma_f32_16x16x32_bf16 v[24:27], v[168:171], v[48:51], v[24:27]\nv_mfma_f32_16x16x32_bf16 v[28:31], v[176:179], v[48:51], v[28:31]\ns_waitcnt lgkmcnt(3)\nv_mfma_f32_16x16x32_bf16 v[32:35], v[152:155], v[52:55], v[32:35]\n" \
"ds_read_b128 v[48:51], v2 offset:4096\nv_mfma_f32_16x16x32_bf16 v[36:39], v[160:163], v[52:55], v[36:39]\nv_mfma_f32_16x16x32_bf16 v[40:43], v[168:171], v[52:55], v[40:43]\nv_mfma_f32_16x16x32_bf16 v[44:47], v[176:179], v[52:55], v[44:47]\ns_waitcnt lgkmcnt(3)\nv_mfma_f32_16x16x32_bf16 v[16:19], v[156:159], v[56:59], v[16:19]\nds_read_b128 v[52:55], v2 offset:4352\nv_mfma_f32_16x16x32_bf16 v[20:23], v[164:167], v[56:59], v[20:23]\n" \
"ds_read_b128 v[76:79], v1 offset:19456\nv_mfma_f32_16x16x32_bf16 v[24:27], v[172:175], v[56:59], v[24:27]\nv_mfma_f32_16x16x32_bf16 v[28:31], v[180:183], v[56:59], v[28:31]\ns_waitcnt lgkmcnt(4)\nv_mfma_f32_16x16x32_bf16 v[32:35], v[156:159], v[60:63], v[32:35]\nds_read_b128 v[56:59], v2 offset:6144\nv_mfma_f32_16x16x32_bf16 v[36:39], v[164:167], v[60:63], v[36:39]\nv_mfma_f32_16x16x32_bf16 v[40:43], v[172:175], v[60:63], v[40:43]\n" \
"v_mfma_f32_16x16x32_bf16 v[44:47], v[180:183], v[60:63], v[44:47]\ns_waitcnt lgkmcnt(3)\nv_mfma_f32_16x16x32_bf16 v[16:19], v[184:187], v[48:51], v[16:19]\nds_read_b128 v[60:63], v2 offset:6400\nv_mfma_f32_16x16x32_bf16 v[20:23], v[72:75], v[48:51], v[20:23]\nds_read_b128 v[64:67], v1 offset:20480\nv_mfma_f32_16x16x32_bf16 v[24:27], v[192:195], v[48:51], v[24:27]\nv_mfma_f32_16x16x32_bf16 v[28:31], v[200:203], v[48:51], v[28:31]\n" \
"s_waitcnt lgkmcnt(4)\nv_mfma_f32_16x16x32_bf16 v[32:35], v[184:187], v[52:55], v[32:35]\nds_read_b128 v[48:51], v2 offset:8192\nv_mfma_f32_16x16x32_bf16 v[36:39], v[72:75], v[52:55], v[36:39]\nv_mfma_f32_16x16x32_bf16 v[40:43], v[192:195], v[52:55], v[40:43]\nv_mfma_f32_16x16x32_bf16 v[44:47], v[200:203], v[52:55], v[44:47]\ns_waitcnt lgkmcnt(3)\nv_mfma_f32_16x16x32_bf16 v[16:19], v[188:191], v[56:59], v[16:19]\n" \
"ds_read_b128 v[52:55], v2 offset:10240\nv_mfma_f32_16x16x32_bf16 v[20:23], v[76:79], v[56:59], v[20:23]\nds_read_b128 v[68:71], v1 offset:21504\nv_mfma_f32_16x16x32_bf16 v[24:27], v[196:199], v[56:59], v[24:27]\nbuffer_load_dwordx4 v[80:83], v3, s[36:39], s42 offen offset:0 sc1\nv_mfma_f32_16x16x32_bf16 v[28:31], v[204:207], v[56:59], v[28:31]\ns_waitcnt lgkmcnt(4)\nv_mfma_f32_16x16x32_bf16 v[32:35], v[188:191], v[60:63], v[32:35]\n" \
"ds_read_b128 v[56:59], v2 offset:12288\nv_mfma_f32_16x16x32_bf16 v[36:39], v[76:79], v[60:63], v[36:39]\nds_read_b128 v[72:75], v1 offset:22528\nv_mfma_f32_16x16x32_bf16 v[40:43], v[196:199], v[60:63], v[40:43]\nbuffer_load_dwordx4 v[84:87], v3, s[36:39], s42 offen offset:1024 sc1\nv_mfma_f32_16x16x32_bf16 v[44:47], v[204:207], v[60:63], v[44:47]\ns_waitcnt lgkmcnt(4)\nv_mfma_f32_16x16x32_bf16 v[16:19], v[208:211], v[48:51], v[16:19]\n" \
"ds_read_b128 v[60:63], v2 offset:14336\nv_mfma_f32_16x16x32_bf16 v[20:23], v[216:219], v[48:51], v[20:23]\nds_read_b128 v[76:79], v1 offset:23552\nv_mfma_f32_16x16x32_bf16 v[24:27], v[64:67], v[48:51], v[24:27]\nbuffer_load_dwordx4 v[88:91], v3, s[36:39], s42 offen offset:2048 sc1\nv_mfma_f32_16x16x32_bf16 v[28:31], v[224:227], v[48:51], v[28:31]\ns_waitcnt lgkmcnt(5)\nv_mfma_f32_16x16x32_bf16 v[16:19], v[212:215], v[52:55], v[16:19]\n" \
"ds_read_b128 v[48:51], v2 offset:8448\nv_mfma_f32_16x16x32_bf16 v[20:23], v[220:223], v[52:55], v[20:23]\ns_waitcnt lgkmcnt(5)\nv_mfma_f32_16x16x32_bf16 v[24:27], v[68:71], v[52:55], v[24:27]\nbuffer_load_dwordx4 v[92:95], v3, s[36:39], s42 offen offset:3072 sc1\nv_mfma_f32_16x16x32_bf16 v[28:31], v[228:231], v[52:55], v[28:31]\ns_waitcnt lgkmcnt(4)\nv_mfma_f32_16x16x32_bf16 v[16:19], v[232:235], v[56:59], v[16:19]\n" \
"ds_read_b128 v[52:55], v2 offset:10496\nv_mfma_f32_16x16x32_bf16 v[20:23], v[240:243], v[56:59], v[20:23]\nv_mfma_f32_16x16x32_bf16 v[24:27], v[248:251], v[56:59], v[24:27]\ns_waitcnt lgkmcnt(4)\nv_mfma_f32_16x16x32_bf16 v[28:31], v[72:75], v[56:59], v[28:31]\ns_waitcnt lgkmcnt(3)\nv_mfma_f32_16x16x32_bf16 v[16:19], v[236:239], v[60:63], v[16:19]\nds_read_b128 v[56:59], v2 offset:12544\n" \
"v_mfma_f32_16x16x32_bf16 v[20:23], v[244:247], v[60:63], v[20:23]\nv_mfma_f32_16x16x32_bf16 v[24:27], v[252:255], v[60:63], v[24:27]\ns_waitcnt lgkmcnt(3)\nv_mfma_f32_16x16x32_bf16 v[28:31], v[76:79], v[60:63], v[28:31]\ns_waitcnt lgkmcnt(2)\nv_mfma_f32_16x16x32_bf16 v[32:35], v[208:211], v[48:51], v[32:35]\nds_read_b128 v[60:63], v2 offset:14592\nv_mfma_f32_16x16x32_bf16 v[36:39], v[216:219], v[48:51], v[36:39]\n" \
"v_cvt_pk_bf16_f32 v20, v16, v20\nv_cvt_pk_bf16_f32 v21, v17, v21\nv_mfma_f32_16x16x32_bf16 v[40:43], v[64:67], v[48:51], v[40:43]\nv_cvt_pk_bf16_f32 v22, v18, v22\nv_cvt_pk_bf16_f32 v23, v19, v23\nv_mfma_f32_16x16x32_bf16 v[44:47], v[224:227], v[48:51], v[44:47]\ns_waitcnt lgkmcnt(2)\nds_write_b128 v5, v[20:23] offset:0\n" \
"v_mfma_f32_16x16x32_bf16 v[32:35], v[212:215], v[52:55], v[32:35]\nv_cvt_pk_bf16_f32 v28, v24, v28\nv_cvt_pk_bf16_f32 v29, v25, v29\nv_mfma_f32_16x16x32_bf16 v[36:39], v[220:223], v[52:55], v[36:39]\nv_cvt_pk_bf16_f32 v30, v26, v30\nv_cvt_pk_bf16_f32 v31, v27, v31\nv_mfma_f32_16x16x32_bf16 v[40:43], v[68:71], v[52:55], v[40:43]\nv_mov_b64 v[48:49], 0\n" \
"v_mov_b64 v[50:51], 0\nv_mfma_f32_16x16x32_bf16 v[44:47], v[228:231], v[52:55], v[44:47]\ns_waitcnt lgkmcnt(2)\nds_write_b128 v5, v[28:31] offset:512\nv_mfma_f32_16x16x32_bf16 v[32:35], v[232:235], v[56:59], v[32:35]\nv_mov_b64 v[52:53], 0\nv_mov_b64 v[54:55], 0\nv_mfma_f32_16x16x32_bf16 v[36:39], v[240:243], v[56:59], v[36:39]\n" \
"s_waitcnt lgkmcnt(2)\nv_mfma_f32_16x16x32_bf16 v[32:35], v[236:239], v[60:63], v[32:35]\nv_mfma_f32_16x16x32_bf16 v[36:39], v[244:247], v[60:63], v[36:39]\nv_mfma_f32_16x16x32_bf16 v[40:43], v[248:251], v[56:59], v[40:43]\nv_mfma_f32_16x16x32_bf16 v[44:47], v[72:75], v[56:59], v[44:47]\nv_cvt_pk_bf16_f32 v36, v32, v36\nv_cvt_pk_bf16_f32 v37, v33, v37\nv_mfma_f32_16x16x32_bf16 v[40:43], v[252:255], v[60:63], v[40:43]\n" \
"v_cvt_pk_bf16_f32 v38, v34, v38\nv_cvt_pk_bf16_f32 v39, v35, v39\nv_mfma_f32_16x16x32_bf16 v[44:47], v[76:79], v[60:63], v[44:47]\ns_cmp_eq_u32 %[w], 1\nds_write_b128 v5, v[36:39] offset:256\nv_mov_b64 v[56:57], 0\nv_mov_b64 v[58:59], 0\nv_mov_b64 v[60:61], 0\n" \
"v_mov_b64 v[62:63], 0\ns_cbranch_scc1 .Lqk_c1%=\ns_cmp_eq_u32 %[w], 2\ns_cbranch_scc1 .Lqk_c2%=\ns_cmp_eq_u32 %[w], 3\ns_cbranch_scc1 .Lqk_c3%=\n.Lqk_c0%=:\nds_read_b128 v[64:67], v1 offset:0\n" \
"ds_read_b128 v[68:71], v1 offset:1024\nds_read_b128 v[72:75], v1 offset:2048\nds_read_b128 v[76:79], v1 offset:3072\nv_cvt_pk_bf16_f32 v44, v40, v44\nv_cvt_pk_bf16_f32 v45, v41, v45\nv_cvt_pk_bf16_f32 v46, v42, v46\nv_cvt_pk_bf16_f32 v47, v43, v47\nds_write_b128 v5, v[44:47] offset:768\n" \
"s_bfe_i32 s54, s40, 0x10000\n.Lqk_poll0%=:\ns_waitcnt vmcnt(0)\nv_smfmac_f32_16x16x64_bf16 v[48:51], v[80:83], a[0:7], v4\nds_write_b128 v0, v[80:83] offset:16384\nv_max3_u32 v40, v80, v82, v84\nv_smfmac_f32_16x16x64_bf16 v[52:55], v[80:83], a[8:15], v4\nds_write_b128 v0, v[84:87] offset:17408\n" \
"v_max3_u32 v40, v40, v86, v88\nv_smfmac_f32_16x16x64_bf16 v[56:59], v[80:83], a[16:23], v4\nds_write_b128 v0, v[88:91] offset:18432\nv_max3_u32 v40, v40, v90, v92\nv_max_u32 v40, v40, v94\nv_smfmac_f32_16x16x64_bf16 v[60:63], v[80:83], a[24:31], v4\nds_write_b128 v0, v[92:95] offset:19456\nv_cmp_eq_u32 vcc, -1, v40\n" \
"v_smfmac_f32_16x16x64_bf16 v[48:51], v[84:87], a[32:39], v4\ns_cbranch_vccnz .Lqk_re0%=\ns_waitcnt lgkmcnt(7)\nv_smfmac_f32_16x16x64_bf16 v[52:55], v[84:87], v[64:71], v4\ns_waitcnt lgkmcnt(0)\ns_barrier\nds_read_b128 v[80:83], v0 offset:20480\nds_read_b128 v[64:67], v1 offset:4096\n" \
"ds_read_b128 v[68:71], v1 offset:5120\nv_smfmac_f32_16x16x64_bf16 v[56:59], v[84:87], a[40:47], v4\nv_smfmac_f32_16x16x64_bf16 v[60:63], v[84:87], a[48:55], v4\nds_read_b128 v[84:87], v0 offset:21504\nv_smfmac_f32_16x16x64_bf16 v[48:51], v[88:91], a[56:63], v4\nv_smfmac_f32_16x16x64_bf16 v[52:55], v[88:91], a[64:71], v4\nv_smfmac_f32_16x16x64_bf16 v[56:59], v[88:91], v[72:79], v4\nds_read_b128 v[72:75], v1 offset:6144\n" \
"ds_read_b128 v[76:79], v1 offset:7168\nv_smfmac_f32_16x16x64_bf16 v[60:63], v[88:91], a[72:79], v4\nds_read_b128 v[88:91], v0 offset:22528\nv_smfmac_f32_16x16x64_bf16 v[48:51], v[92:95], a[80:87], v4\nds_read_b128 v[16:19], v7 offset:0\nv_smfmac_f32_16x16x64_bf16 v[52:55], v[92:95], a[88:95], v4\nv_smfmac_f32_16x16x64_bf16 v[56:59], v[92:95], a[96:103], v4\ns_waitcnt lgkmcnt(5)\n" \
"v_smfmac_f32_16x16x64_bf16 v[60:63], v[92:95], v[64:71], v4\nds_read_b128 v[92:95], v0 offset:23552\nds_read_b128 v[64:67], v1 offset:8192\nds_read_b128 v[68:71], v1 offset:9216\nv_smfmac_f32_16x16x64_bf16 v[48:51], v[80:83], a[104:111], v4\nds_read_b128 v[20:23], v7 offset:64\nv_smfmac_f32_16x16x64_bf16 v[52:55], v[80:83], a[112:119], v4\nv_smfmac_f32_16x16x64_bf16 v[56:59], v[80:83], a[120:127], v4\n" \
"v_smfmac_f32_16x16x64_bf16 v[60:63], v[80:83], a[128:135], v4\ns_waitcnt lgkmcnt(8)\nds_read_b128 v[80:83], v0 offset:24576\nv_smfmac_f32_16x16x64_bf16 v[48:51], v[84:87], a[136:143], v4\ns_waitcnt lgkmcnt(7)\nds_read_b128 v[24:27], v7 offset:2048\nv_smfmac_f32_16x16x64_bf16 v[52:55], v[84:87], v[72:79], v4\nds_read_b128 v[72:75], v1 offset:10240\n" \
"ds_read_b128 v[76:79], v1 offset:11264\nv_smfmac_f32_16x16x64_bf16 v[56:59], v[84:87], a[144:151], v4\nv_smfmac_f32_16x16x64_bf16 v[60:63], v[84:87], a[152:159], v4\ns_waitcnt lgkmcnt(9)\nds_read_b128 v[84:87], v0 offset:25600\nv_smfmac_f32_16x16x64_bf16 v[48:51], v[88:91], a[160:167], v4\nds_read_b128 v[28:31], v7 offset:2112\nv_smfmac_f32_16x16x64_bf16 v[52:55], v[88:91], a[168:175], v4\n" \
"s_waitcnt lgkmcnt(7)\nv_smfmac_f32_16x16x64_bf16 v[56:59], v[88:91], v[64:71], v4\nds_read_b128 v[64:67], v1 offset:12288\nds_read_b128 v[68:71], v1 offset:13312\nv_smfmac_f32_16x16x64_bf16 v[60:63], v[88:91], a[176:183], v4\nds_read_b128 v[88:91], v0 offset:26624\nv_smfmac_f32_16x16x64_bf16 v[48:51], v[92:95], a[184:191], v4\nds_read_b128 v[32:35], v7 offset:4096\n" \
"v_smfmac_f32_16x16x64_bf16 v[52:55], v[92:95], a[192:199], v4\nv_smfmac_f32_16x16x64_bf16 v[56:59], v[92:95], a[200:207], v4\ns_waitcnt lgkmcnt(6)\nv_smfmac_f32_16x16x64_bf16 v[60:63], v[92:95], v[72:79], v4\nds_read_b128 v[92:95], v0 offset:27648\nds_read_b128 v[72:75], v1 offset:14336\nds_read_b128 v[76:79], v1 offset:15360\nv_smfmac_f32_16x16x64_bf16 v[48:51], v[80:83], a[208:215], v4\n" \
"ds_read_b128 v[36:39], v7 offset:4160\nv_smfmac_f32_16x16x64_bf16 v[52:55], v[80:83], a[216:223], v4\nv_smfmac_f32_16x16x64_bf16 v[56:59], v[80:83], a[224:231], v4\nv_smfmac_f32_16x16x64_bf16 v[60:63], v[80:83], a[232:239], v4\ns_waitcnt lgkmcnt(9)\nds_read_b128 v[80:83], v0 offset:28672\nv_smfmac_f32_16x16x64_bf16 v[48:51], v[84:87], a[240:247], v4\ns_waitcnt lgkmcnt(7)\n" \
"ds_read_b128 v[40:43], v7 offset:6144\nv_smfmac_f32_16x16x64_bf16 v[52:55], v[84:87], v[64:71], v4\nds_read_b128 v[64:67], v1 offset:16384\nds_read_b128 v[68:71], v1 offset:17408\nv_smfmac_f32_16x16x64_bf16 v[56:59], v[84:87], a[248:255], v4\nv_smfmac_f32_16x16x64_bf16 v[60:63], v[84:87], v[96:103], v4\ns_waitcnt lgkmcnt(9)\nds_read_b128 v[84:87], v0 offset:29696\n" \
"v_smfmac_f32_16x16x64_bf16 v[48:51], v[88:91], v[104:111], v4\nds_read_b128 v[44:47], v7 offset:6208\nv_smfmac_f32_16x16x64_bf16 v[52:55], v[88:91], v[112:119], v4\ns_waitcnt lgkmcnt(7)\nv_smfmac_f32_16x16x64_bf16 v[56:59], v[88:91], v[72:79], v4\nds_read_b128 v[72:75], v1 offset:18432\nds_read_b128 v[76:79], v1 offset:19456\nv_smfmac_f32_16x16x64_bf16 v[60:63], v[88:91], v[120:127], v4\n" \
"ds_read_b128 v[88:91], v0 offset:30720\nv_smfmac_f32_16x16x64_bf16 v[48:51], v[92:95], v[128:135], v4\nv_smfmac_f32_16x16x64_bf16 v[52:55], v[92:95], v[136:143], v4\nv_smfmac_f32_16x16x64_bf16 v[56:59], v[92:95], v[144:151], v4\ns_waitcnt lgkmcnt(5)\nv_smfmac_f32_16x16x64_bf16 v[60:63], v[92:95], v[64:71], v4\nds_read_b128 v[92:95], v0 offset:31744\nds_read_b128 v[64:67], v1 offset:20480\n" \
"ds_read_b128 v[68:71], v1 offset:21504\nv_smfmac_f32_16x16x64_bf16 v[48:51], v[80:83], v[152:159], v4\nv_smfmac_f32_16x16x64_bf16 v[52:55], v[80:83], v[160:167], v4\nv_smfmac_f32_16x16x64_bf16 v[56:59], v[80:83], v[168:175], v4\nv_smfmac_f32_16x16x64_bf16 v[60:63], v[80:83], v[176:183], v4\ns_waitcnt lgkmcnt(7)\nv_smfmac_f32_16x16x64_bf16 v[48:51], v[84:87], v[184:191], v4\ns_waitcnt lgkmcnt(4)\n" \
"v_smfmac_f32_16x16x64_bf16 v[52:55], v[84:87], v[72:79], v4\nds_read_b128 v[72:75], v1 offset:22528\nds_read_b128 v[76:79], v1 offset:23552\nv_smfmac_f32_16x16x64_bf16 v[56:59], v[84:87], v[192:199], v4\nv_smfmac_f32_16x16x64_bf16 v[60:63], v[84:87], v[200:207], v4\ns_waitcnt lgkmcnt(5)\nv_smfmac_f32_16x16x64_bf16 v[48:51], v[88:91], v[208:215], v4\nv_smfmac_f32_16x16x64_bf16 v[52:55], v[88:91], v[216:223], v4\n" \
"s_waitcnt lgkmcnt(4)\nv_smfmac_f32_16x16x64_bf16 v[48:51], v[92:95], v[232:239], v4\nv_smfmac_f32_16x16x64_bf16 v[52:55], v[92:95], v[240:247], v4\ns_waitcnt lgkmcnt(2)\nv_smfmac_f32_16x16x64_bf16 v[56:59], v[88:91], v[64:71], v4\nv_add_f32 v48, v48, v50\nv_smfmac_f32_16x16x64_bf16 v[60:63], v[88:91], v[224:231], v4\nv_add_f32 v52, v52, v54\n" \
"v_add_f32 v49, v49, v51\nv_smfmac_f32_16x16x64_bf16 v[56:59], v[92:95], v[248:255], v4\ns_waitcnt lgkmcnt(0)\nv_add_f32 v53, v53, v55\nv_cvt_pk_bf16_f32 v50, v48, v52\nv_smfmac_f32_16x16x64_bf16 v[60:63], v[92:95], v[72:79], v4\nv_cvt_pk_bf16_f32 v51, v49, v53\nds_write2_b32 v6, v50, v51 offset0:0 offset1:4\n" \
"s_nop 1\nv_add_f32 v56, v56, v58\nv_add_f32 v57, v57, v59\ns_nop 1\nv_add_f32 v60, v60, v62\nv_add_f32 v61, v61, v63\nv_cvt_pk_bf16_f32 v62, v56, v60\nv_cvt_pk_bf16_f32 v63, v57, v61\n" \
"ds_write2_b32 v6, v62, v63 offset0:36 offset1:40\nv_mov_b32 v61, s54\ns_waitcnt lgkmcnt(0)\ns_barrier\nds_read_b128 v[80:83], v8 offset:0\nds_read_b128 v[84:87], v8 offset:576\nds_read_b128 v[88:91], v8 offset:1152\nds_read_b128 v[92:95], v8 offset:1728\n" \
"ds_read_b128 v[64:67], v9 offset:0\nds_read_b128 v[68:71], v9 offset:576\nds_read_b128 v[72:75], v9 offset:1152\nds_read_b128 v[76:79], v9 offset:1728\nds_read_b32 v60, v0 offset:53760\ns_waitcnt lgkmcnt(8)\nv_mfma_f32_16x16x32_bf16 v[52:55], v[16:19], v[80:83], 0\nv_mfma_f32_16x16x32_bf16 v[56:59], v[20:23], v[80:83], 0\n" \
"s_waitcnt lgkmcnt(7)\nv_mfma_f32_16x16x32_bf16 v[52:55], v[24:27], v[84:87], v[52:55]\nv_mfma_f32_16x16x32_bf16 v[56:59], v[28:31], v[84:87], v[56:59]\ns_waitcnt lgkmcnt(6)\nv_mfma_f32_16x16x32_bf16 v[52:55], v[32:35], v[88:91], v[52:55]\nv_mfma_f32_16x16x32_bf16 v[56:59], v[36:39], v[88:91], v[56:59]\ns_waitcnt lgkmcnt(5)\nv_mfma_f32_16x16x32_bf16 v[52:55], v[40:43], v[92:95], v[52:55]\n" \
"v_mfma_f32_16x16x32_bf16 v[56:59], v[44:47], v[92:95], v[56:59]\ns_waitcnt lgkmcnt(4)\nv_mfma_f32_16x16x32_bf16 v[48:51], v[64:67], v[80:83], 0\ns_waitcnt lgkmcnt(3)\nv_mfma_f32_16x16x32_bf16 v[48:51], v[68:71], v[84:87], v[48:51]\ns_waitcnt lgkmcnt(2)\nds_read_b128 v[80:83], v2 offset:0\nv_mfma_f32_16x16x32_bf16 v[48:51], v[72:75], v[88:91], v[48:51]\n" \
"s_waitcnt lgkmcnt(2)\nds_read_b128 v[84:87], v2 offset:256\nv_mfma_f32_16x16x32_bf16 v[48:51], v[76:79], v[92:95], v[48:51]\nds_read_b128 v[88:91], v2 offset:2048\nv_max3_f32 v62, v52, v53, v54\nv_max3_f32 v63, v55, v56, v57\nv_max3_f32 v62, v62, v58, v59\nv_cndmask_b32 v52, v52, v56, s[62:63]\n" \
"v_cndmask_b32 v53, v53, v57, s[62:63]\nv_cndmask_b32 v54, v54, v58, s[62:63]\nv_cndmask_b32 v55, v55, v59, s[62:63]\nv_max_f32 v62, v62, v63\nv_max3_f32 v63, v62, v48, v49\nv_max3_f32 v62, v62, v48, v49\ns_waitcnt lgkmcnt(2)\nv_mfma_f32_16x16x32_bf16 v[16:19], a[0:3], v[80:83], 0\n" \
"ds_read_b128 v[92:95], v2 offset:2304\nv_permlane16_swap_b32 v62, v63\nv_max_f32 v50, v62, v63\nv_mfma_f32_16x16x32_bf16 v[20:23], a[8:11], v[80:83], 0\nds_read_b128 v[72:75], v1 offset:0\nv_max_f32 v62, v62, v63\nv_mfma_f32_16x16x32_bf16 v[24:27], a[16:19], v[80:83], 0\ns_nop 0\n" \
"v_permlane32_swap_b32 v62, v50\nv_max_f32 v62, v62, v50\nv_mul_f32 v62, s59, v62\nv_mfma_f32_16x16x32_bf16 v[28:31], a[24:27], v[80:83], 0\ns_waitcnt lgkmcnt(3)\nv_fma_f32 v52, v52, s58, v62\nv_fma_f32 v53, v53, s58, v62\nv_exp_f32 v52, v52\n" \
"v_mfma_f32_16x16x32_bf16 v[32:35], a[0:3], v[84:87], 0\nds_read_b128 v[80:83], v2 offset:4096\nv_fma_f32 v54, v54, s58, v62\nv_exp_f32 v53, v53\nv_mfma_f32_16x16x32_bf16 v[36:39], a[8:11], v[84:87], 0\nv_fma_f32 v55, v55, s58, v62\nv_exp_f32 v54, v54\nv_add_f32 v63, v52, v53\n" \
"v_mfma_f32_16x16x32_bf16 v[40:43], a[16:19], v[84:87], 0\nv_fma_f32 v48, v48, s58, v62\nv_exp_f32 v55, v55\nv_add_f32 v63, v63, v54\nv_mfma_f32_16x16x32_bf16 v[44:47], a[24:27], v[84:87], 0\ns_waitcnt lgkmcnt(3)\nv_fma_f32 v49, v49, s58, v62\nv_exp_f32 v48, v48\n" \
"v_add_f32 v63, v63, v55\nv_mfma_f32_16x16x32_bf16 v[16:19], a[4:7], v[88:91], v[16:19]\nds_read_b128 v[84:87], v2 offset:4352\nv_exp_f32 v49, v49\nv_add_f32 v50, v48, v63\nv_mfma_f32_16x16x32_bf16 v[20:23], a[12:15], v[88:91], v[20:23]\nds_read_b128 v[76:79], v1 offset:1024\nv_add_f32 v50, v50, v49\n" \
"v_mfma_f32_16x16x32_bf16 v[24:27], a[20:23], v[88:91], v[24:27]\ns_nop 0\nv_add_f32_dpp v62, v63, v50 row_ror:8 row_mask:0xf bank_mask:0xf\nv_add_f32_dpp v50, v63, v50 row_ror:8 row_mask:0xf bank_mask:0xf\nv_mfma_f32_16x16x32_bf16 v[28:31], a[28:31], v[88:91], v[28:31]\ns_waitcnt lgkmcnt(4)\nv_permlane16_swap_b32 v62, v50\nv_add_f32 v63, v62, v50\n" \
"v_add_f32 v50, v62, v50\nv_mfma_f32_16x16x32_bf16 v[32:35], a[4:7], v[92:95], v[32:35]\nds_read_b128 v[88:91], v2 offset:6144\nv_permlane32_swap_b32 v63, v50\nv_add_f32 v63, v63, v50\nv_mfma_f32_16x16x32_bf16 v[36:39], a[12:15], v[92:95], v[36:39]\nv_rcp_f32 v63, v63\nv_mfma_f32_16x16x32_bf16 v[40:43], a[20:23], v[92:95], v[40:43]\n" \
"v_mul_f32 v52, v52, v63\nv_mul_f32 v53, v53, v63\nv_mul_f32 v54, v54, v63\nv_mul_f32 v55, v55, v63\nv_mfma_f32_16x16x32_bf16 v[44:47], a[28:31], v[92:95], v[44:47]\ns_waitcnt lgkmcnt(3)\nv_mul_f32 v48, v48, v63\nv_mul_f32 v49, v49, v63\n" \
"v_cvt_pk_bf16_f32 v52, v52, v53\nv_cvt_pk_bf16_f32 v53, v54, v55\nv_mfma_f32_16x16x32_bf16 v[16:19], a[32:35], v[80:83], v[16:19]\nds_read_b128 v[92:95], v2 offset:6400\nv_cvt_pk_bf16_f32 v48, v48, v49\nv_bfi_b32 v52, s61, v52, v61\nv_bfi_b32 v53, s61, v53, v61\nv_mfma_f32_16x16x32_bf16 v[20:23], v[72:75], v[80:83], v[20:23]\n" \
"ds_read_b128 v[64:67], v1 offset:2048\nv_bfi_b32 v48, s61, v48, v61\nv_mfma_f32_16x16x32_bf16 v[24:27], a[40:43], v[80:83], v[24:27]\nbuffer_store_dwordx2 v[52:53], v10, s[36:39], 0 offen sc1\nbuffer_store_dword v48, v11, s[36:39], 0 offen sc1\nv_mfma_f32_16x16x32_bf16 v[28:31], a[48:51], v[80:83], v[28:31]\ns_waitcnt lgkmcnt(4)\nv_cmp_ne_u32 vcc, 0, v60\n" \
"v_mfma_f32_16x16x32_bf16 v[32:35], a[32:35], v[84:87], v[32:35]\nds_read_b128 v[80:83], v2 offset:8192\nv_mfma_f32_16x16x32_bf16 v[36:39], v[72:75], v[84:87], v[36:39]\nv_mfma_f32_16x16x32_bf16 v[40:43], a[40:43], v[84:87], v[40:43]\nv_mfma_f32_16x16x32_bf16 v[44:47], a[48:51], v[84:87], v[44:47]\ns_waitcnt lgkmcnt(3)\nv_mfma_f32_16x16x32_bf16 v[16:19], a[36:39], v[88:91], v[16:19]\nds_read_b128 v[84:87], v2 offset:8448\n" \
"v_mfma_f32_16x16x32_bf16 v[20:23], v[76:79], v[88:91], v[20:23]\nds_read_b128 v[68:71], v1 offset:3072\nv_mfma_f32_16x16x32_bf16 v[24:27], a[44:47], v[88:91], v[24:27]\nv_mfma_f32_16x16x32_bf16 v[28:31], a[52:55], v[88:91], v[28:31]\ns_waitcnt lgkmcnt(4)\nv_mfma_f32_16x16x32_bf16 v[32:35], a[36:39], v[92:95], v[32:35]\nds_read_b128 v[88:91], v2 offset:10240\nv_mfma_f32_16x16x32_bf16 v[36:39], v[76:79], v[92:95], v[36:39]\n" \
"v_mfma_f32_16x16x32_bf16 v[40:43], a[44:47], v[92:95], v[40:43]\nv_mfma_f32_16x16x32_bf16 v[44:47], a[52:55], v[92:95], v[44:47]\ns_waitcnt lgkmcnt(3)\nv_mfma_f32_16x16x32_bf16 v[16:19], a[56:59], v[80:83], v[16:19]\nds_read_b128 v[92:95], v2 offset:10496\nv_mfma_f32_16x16x32_bf16 v[20:23], a[64:67], v[80:83], v[20:23]\nds_read_b128 v[72:75], v1 offset:4096\nv_mfma_f32_16x16x32_bf16 v[24:27], v[64:67], v[80:83], v[24:27]\n" \
"v_mfma_f32_16x16x32_bf16 v[28:31], a[72:75], v[80:83], v[28:31]\ns_waitcnt lgkmcnt(4)\nv_mfma_f32_16x16x32_bf16 v[32:35], a[56:59], v[84:87], v[32:35]\nds_read_b128 v[80:83], v2 offset:12288\nv_mfma_f32_16x16x32_bf16 v[36:39], a[64:67], v[84:87], v[36:39]\nv_mfma_f32_16x16x32_bf16 v[40:43], v[64:67], v[84:87], v[40:43]\nv_mfma_f32_16x16x32_bf16 v[44:47], a[72:75], v[84:87], v[44:47]\ns_waitcnt lgkmcnt(3)\n" \
"v_mfma_f32_16x16x32_bf16 v[16:19], a[60:63], v[88:91], v[16:19]\nds_read_b128 v[84:87], v2 offset:12544\nv_mfma_f32_16x16x32_bf16 v[20:23], a[68:71], v[88:91], v[20:23]\nds_read_b128 v[76:79], v1 offset:5120\nv_mfma_f32_16x16x32_bf16 v[24:27], v[68:71], v[88:91], v[24:27]\nv_mfma_f32_16x16x32_bf16 v[28:31], a[76:79], v[88:91], v[28:31]\ns_waitcnt lgkmcnt(4)\nv_mfma_f32_16x16x32_bf16 v[32:35], a[60:63], v[92:95], v[32:35]\n" \
"ds_read_b128 v[88:91], v2 offset:14336\nv_mfma_f32_16x16x32_bf16 v[36:39], a[68:71], v[92:95], v[36:39]\nv_mfma_f32_16x16x32_bf16 v[40:43], v[68:71], v[92:95], v[40:43]\nv_mfma_f32_16x16x32_bf16 v[44:47], a[76:79], v[92:95], v[44:47]\ns_cbranch_vccnz .Lqk_pj%=\ns_cmp_lt_i32 s40, 63\ns_waitcnt lgkmcnt(0)\ns_cbranch_scc1 .Lqk_pwait%=\n" \
"s_branch .Lqk_end%=\n.Lqk_re0%=:\nv_max_u32 v40, v80, v82\nv_cmp_eq_u32 vcc, -1, v40\ns_cbranch_vccz .Lqk_sk1600%=\ns_mov_b64 exec, vcc\nbuffer_load_dwordx4 v[80:83], v3, s[36:39], s42 offen offset:0 sc1\ns_mov_b64 exec, -1\n" \
".Lqk_sk1600%=:\nv_max_u32 v40, v84, v86\nv_cmp_eq_u32 vcc, -1, v40\ns_cbranch_vccz .Lqk_sk1607%=\ns_mov_b64 exec, vcc\nbuffer_load_dwordx4 v[84:87], v3, s[36:39], s42 offen offset:1024 sc1\ns_mov_b64 exec, -1\n.Lqk_sk1607%=:\n" \
"v_max_u32 v40, v88, v90\nv_cmp_eq_u32 vcc, -1, v40\ns_cbranch_vccz .Lqk_sk1614%=\ns_mov_b64 exec, vcc\nbuffer_load_dwordx4 v[88:91], v3, s[36:39], s42 offen offset:2048 sc1\ns_mov_b64 exec, -1\n.Lqk_sk1614%=:\nv_max_u32 v40, v92, v94\n" \
"v_cmp_eq_u32 vcc, -1, v40\ns_cbranch_vccz .Lqk_sk1621%=\ns_mov_b64 exec, vcc\nbuffer_load_dwordx4 v[92:95], v3, s[36:39], s42 offen offset:3072 sc1\ns_mov_b64 exec, -1\n.Lqk_sk1621%=:\nv_mov_b64 v[48:49], 0\nv_mov_b64 v[50:51], 0\n" \
"v_mov_b64 v[52:53], 0\nv_mov_b64 v[54:55], 0\nv_mov_b64 v[56:57], 0\nv_mov_b64 v[58:59], 0\nv_mov_b64 v[60:61], 0\nv_mov_b64 v[62:63], 0\ns_add_u32 s43, s43, 1\ns_cmp_lt_u32 s43, 262144\n" \
"s_cbranch_scc1 .Lqk_poll0%=\ns_branch .Lqk_abort%=\n.Lqk_c1%=:\nds_read_b128 v[64:67], v1 offset:6144\nds_read_b128 v[68:71], v1 offset:7168\nds_read_b128 v[72:75], v1 offset:8192\nds_read_b128 v[76:79], v1 offset:9216\ns_nop 1\n" \
"v_cvt_pk_bf16_f32 v44, v40, v44\nv_cvt_pk_bf16_f32 v45, v41, v45\nv_cvt_pk_bf16_f32 v46, v42, v46\nv_cvt_pk_bf16_f32 v47, v43, v47\nds_write_b128 v5, v[44:47] offset:768\ns_bfe_i32 s54, s40, 0x10001\n.Lqk_poll1%=:\ns_waitcnt vmcnt(0)\n" \
"v_smfmac_f32_16x16x64_bf16 v[48:51], v[80:83], a[104:111], v4\nds_write_b128 v0, v[80:83] offset:20480\nv_max3_u32 v40, v80, v82, v84\nv_smfmac_f32_16x16x64_bf16 v[52:55], v[80:83], a[112:119], v4\nds_write_b128 v0, v[84:87] offset:21504\nv_max3_u32 v40, v40, v86, v88\nv_smfmac_f32_16x16x64_bf16 v[56:59], v[80:83], a[120:127], v4\nds_write_b128 v0, v[88:91] offset:22528\n" \
"v_max3_u32 v40, v40, v90, v92\nv_max_u32 v40, v40, v94\nv_smfmac_f32_16x16x64_bf16 v[60:63], v[80:83], a[128:135], v4\nds_write_b128 v0, v[92:95] offset:23552\nv_cmp_eq_u32 vcc, -1, v40\nv_smfmac_f32_16x16x64_bf16 v[48:51], v[84:87], a[136:143], v4\ns_cbranch_vccnz .Lqk_re1%=\ns_waitcnt lgkmcnt(7)\n" \
"v_smfmac_f32_16x16x64_bf16 v[52:55], v[84:87], v[64:71], v4\ns_waitcnt lgkmcnt(0)\ns_barrier\ns_memrealtime s[68:69]\nds_read_b128 v[80:83], v0 offset:24576\nds_read_b128 v[64:67], v1 offset:10240\nds_read_b128 v[68:71], v1 offset:11264\nv_mov_b32 v40, s40\n" \
"v_smfmac_f32_16x16x64_bf16 v[56:59], v[84:87], a[144:151], v4\nv_smfmac_f32_16x16x64_bf16 v[60:63], v[84:87], a[152:159], v4\nds_read_b128 v[84:87], v0 offset:25600\nv_smfmac_f32_16x16x64_bf16 v[48:51], v[88:91], a[160:167], v4\nv_smfmac_f32_16x16x64_bf16 v[52:55], v[88:91], a[168:175], v4\nv_smfmac_f32_16x16x64_bf16 v[56:59], v[88:91], v[72:79], v4\nds_read_b128 v[72:75], v1 offset:12288\nds_read_b128 v[76:79], v1 offset:13312\n" \
"v_smfmac_f32_16x16x64_bf16 v[60:63], v[88:91], a[176:183], v4\nds_read_b128 v[88:91], v0 offset:26624\nv_smfmac_f32_16x16x64_bf16 v[48:51], v[92:95], a[184:191], v4\nds_read_b128 v[16:19], v7 offset:0\nv_smfmac_f32_16x16x64_bf16 v[52:55], v[92:95], a[192:199], v4\nv_smfmac_f32_16x16x64_bf16 v[56:59], v[92:95], a[200:207], v4\ns_waitcnt lgkmcnt(5)\nv_smfmac_f32_16x16x64_bf16 v[60:63], v[92:95], v[64:71], v4\n" \
"ds_read_b128 v[92:95], v0 offset:27648\nds_read_b128 v[64:67], v1 offset:14336\nds_read_b128 v[68:71], v1 offset:15360\nv_smfmac_f32_16x16x64_bf16 v[48:51], v[80:83], a[208:215], v4\nds_read_b128 v[20:23], v7 offset:2048\nv_smfmac_f32_16x16x64_bf16 v[52:55], v[80:83], a[216:223], v4\nv_mov_b32 v41, s68\nv_smfmac_f32_16x16x64_bf16 v[56:59], v[80:83], a[224:231], v4\n" \
"s_mov_b64 exec, 1\nbuffer_store_dwordx2 v[40:41], off, s[36:39], %[sg] sc1\ns_mov_b64 exec, -1\nv_smfmac_f32_16x16x64_bf16 v[60:63], v[80:83], a[232:239], v4\ns_waitcnt lgkmcnt(8)\nds_read_b128 v[80:83], v0 offset:28672\nv_smfmac_f32_16x16x64_bf16 v[48:51], v[84:87], a[240:247], v4\ns_waitcnt lgkmcnt(7)\n" \
"ds_read_b128 v[24:27], v7 offset:4096\nv_smfmac_f32_16x16x64_bf16 v[52:55], v[84:87], v[72:79], v4\nds_read_b128 v[72:75], v1 offset:16384\nds_read_b128 v[76:79], v1 offset:17408\nv_smfmac_f32_16x16x64_bf16 v[56:59], v[84:87], a[248:255], v4\nv_smfmac_f32_16x16x64_bf16 v[60:63], v[84:87], v[96:103], v4\ns_waitcnt lgkmcnt(9)\nds_read_b128 v[84:87], v0 offset:29696\n" \
"v_smfmac_f32_16x16x64_bf16 v[48:51], v[88:91], v[104:111], v4\nds_read_b128 v[28:31], v7 offset:6144\nv_smfmac_f32_16x16x64_bf16 v[52:55], v[88:91], v[112:119], v4\ns_waitcnt lgkmcnt(7)\nv_smfmac_f32_16x16x64_bf16 v[56:59], v[88:91], v[64:71], v4\nds_read_b128 v[64:67], v1 offset:18432\nds_read_b128 v[68:71], v1 offset:19456\nv_smfmac_f32_16x16x64_bf16 v[60:63], v[88:91], v[120:127], v4\n" \
"ds_read_b128 v[88:91], v0 offset:30720\nv_smfmac_f32_16x16x64_bf16 v[48:51], v[92:95], v[128:135], v4\nds_read_b128 v[32:35], v7 offset:8192\nv_smfmac_f32_16x16x64_bf16 v[52:55], v[92:95], v[136:143], v4\nv_smfmac_f32_16x16x64_bf16 v[56:59], v[92:95], v[144:151], v4\ns_waitcnt lgkmcnt(6)\nv_smfmac_f32_16x16x64_bf16 v[60:63], v[92:95], v[72:79], v4\nds_read_b128 v[92:95], v0 offset:31744\n" \
"ds_read_b128 v[72:75], v1 offset:20480\nds_read_b128 v[76:79], v1 offset:21504\nv_smfmac_f32_16x16x64_bf16 v[48:51], v[80:83], v[152:159], v4\nds_read_b128 v[36:39], v7 offset:10240\nv_smfmac_f32_16x16x64_bf16 v[52:55], v[80:83], v[160:167], v4\nv_smfmac_f32_16x16x64_bf16 v[56:59], v[80:83], v[168:175], v4\nv_smfmac_f32_16x16x64_bf16 v[60:63], v[80:83], v[176:183], v4\ns_waitcnt lgkmcnt(9)\n" \
"ds_read_b128 v[80:83], v0 offset:16384\nv_smfmac_f32_16x16x64_bf16 v[48:51], v[84:87], v[184:191], v4\ns_waitcnt lgkmcnt(7)\nds_read_b128 v[40:43], v7 offset:12288\nv_smfmac_f32_16x16x64_bf16 v[52:55], v[84:87], v[64:71], v4\nds_read_b128 v[64:67], v1 offset:22528\nds_read_b128 v[68:71], v1 offset:23552\nv_smfmac_f32_16x16x64_bf16 v[56:59], v[84:87], v[192:199], v4\n" \
"v_smfmac_f32_16x16x64_bf16 v[60:63], v[84:87], v[200:207], v4\ns_waitcnt lgkmcnt(9)\nds_read_b128 v[84:87], v0 offset:17408\nv_smfmac_f32_16x16x64_bf16 v[48:51], v[88:91], v[208:215], v4\nds_read_b128 v[44:47], v7 offset:14336\nv_smfmac_f32_16x16x64_bf16 v[52:55], v[88:91], v[216:223], v4\ns_waitcnt lgkmcnt(7)\nv_smfmac_f32_16x16x64_bf16 v[56:59], v[88:91], v[72:79], v4\n" \
"ds_read_b128 v[72:75], v1 offset:0\nds_read_b128 v[76:79], v1 offset:1024\nv_smfmac_f32_16x16x64_bf16 v[60:63], v[88:91], v[224:231], v4\nds_read_b128 v[88:91], v0 offset:18432\nv_smfmac_f32_16x16x64_bf16 v[48:51], v[92:95], v[232:239], v4\nv_smfmac_f32_16x16x64_bf16 v[52:55], v[92:95], v[240:247], v4\nv_smfmac_f32_16x16x64_bf16 v[56:59], v[92:95], v[248:255], v4\ns_waitcnt lgkmcnt(5)\n" \
"v_smfmac_f32_16x16x64_bf16 v[60:63], v[92:95], v[64:71], v4\nds_read_b128 v[92:95], v0 offset:19456\nds_read_b128 v[64:67], v1 offset:2048\nds_read_b128 v[68:71], v1 offset:3072\nv_smfmac_f32_16x16x64_bf16 v[48:51], v[80:83], a[0:7], v4\nv_smfmac_f32_16x16x64_bf16 v[52:55], v[80:83], a[8:15], v4\nv_smfmac_f32_16x16x64_bf16 v[56:59], v[80:83], a[16:23], v4\nv_smfmac_f32_16x16x64_bf16 v[60:63], v[80:83], a[24:31], v4\n" \
"s_waitcnt lgkmcnt(7)\nv_smfmac_f32_16x16x64_bf16 v[48:51], v[84:87], a[32:39], v4\ns_waitcnt lgkmcnt(4)\nv_smfmac_f32_16x16x64_bf16 v[52:55], v[84:87], v[72:79], v4\nds_read_b128 v[72:75], v1 offset:4096\nds_read_b128 v[76:79], v1 offset:5120\nv_smfmac_f32_16x16x64_bf16 v[56:59], v[84:87], a[40:47], v4\nv_smfmac_f32_16x16x64_bf16 v[60:63], v[84:87], a[48:55], v4\n" \
"s_waitcnt lgkmcnt(5)\nv_smfmac_f32_16x16x64_bf16 v[48:51], v[88:91], a[56:63], v4\nv_smfmac_f32_16x16x64_bf16 v[52:55], v[88:91], a[64:71], v4\ns_waitcnt lgkmcnt(4)\nv_smfmac_f32_16x16x64_bf16 v[48:51], v[92:95], a[80:87], v4\nv_smfmac_f32_16x16x64_bf16 v[52:55], v[92:95], a[88:95], v4\ns_waitcnt lgkmcnt(2)\nv_smfmac_f32_16x16x64_bf16 v[56:59], v[88:91], v[64:71], v4\n" \
"v_add_f32 v48, v48, v50\nv_smfmac_f32_16x16x64_bf16 v[60:63], v[88:91], a[72:79], v4\nv_add_f32 v52, v52, v54\nv_add_f32 v49, v49, v51\nv_smfmac_f32_16x16x64_bf16 v[56:59], v[92:95], a[96:103], v4\ns_waitcnt lgkmcnt(0)\nv_add_f32 v53, v53, v55\nv_cvt_pk_bf16_f32 v50, v48, v52\n" \
"v_smfmac_f32_16x16x64_bf16 v[60:63], v[92:95], v[72:79], v4\nv_cvt_pk_bf16_f32 v51, v49, v53\nds_write2_b32 v6, v50, v51 offset0:0 offset1:4\ns_nop 1\nv_add_f32 v56, v56, v58\nv_add_f32 v57, v57, v59\ns_nop 1\nv_add_f32 v60, v60, v62\n" \
"v_add_f32 v61, v61, v63\nv_cvt_pk_bf16_f32 v62, v56, v60\nv_cvt_pk_bf16_f32 v63, v57, v61\nds_write2_b32 v6, v62, v63 offset0:36 offset1:40\nv_mov_b32 v61, s54\nv_mfma_f32_16x16x32_bf16 v[52:55], v[32:35], v[16:19], v[12:15]\nv_mfma_f32_16x16x32_bf16 v[52:55], v[36:39], v[20:23], v[52:55]\nv_mfma_f32_16x16x32_bf16 v[52:55], v[40:43], v[24:27], v[52:55]\n" \
"v_mfma_f32_16x16x32_bf16 v[52:55], v[44:47], v[28:31], v[52:55]\ns_waitcnt lgkmcnt(0)\ns_barrier\nds_read_b128 v[32:35], v9 offset:0\nds_read_b128 v[36:39], v9 offset:576\nds_read_b128 v[40:43], v9 offset:1152\nds_read_b128 v[44:47], v9 offset:1728\nds_read_b32 v60, v0 offset:53760\n" \
"ds_read_b128 v[80:83], v2 offset:0\nds_read_b128 v[84:87], v2 offset:256\nds_read_b128 v[88:91], v2 offset:2048\ns_waitcnt lgkmcnt(7)\nv_mfma_f32_16x16x32_bf16 v[48:51], v[32:35], v[16:19], 0\ns_waitcnt lgkmcnt(6)\nv_mfma_f32_16x16x32_bf16 v[48:51], v[36:39], v[20:23], v[48:51]\ns_waitcnt lgkmcnt(5)\n" \
"v_mfma_f32_16x16x32_bf16 v[48:51], v[40:43], v[24:27], v[48:51]\ns_waitcnt lgkmcnt(4)\nv_mfma_f32_16x16x32_bf16 v[48:51], v[44:47], v[28:31], v[48:51]\ns_waitcnt lgkmcnt(2)\nv_mfma_f32_16x16x32_bf16 v[16:19], a[0:3], v[80:83], 0\nds_read_b128 v[92:95], v2 offset:2304\nv_max3_f32 v62, v52, v53, v54\nv_mfma_f32_16x16x32_bf16 v[20:23], a[8:11], v[80:83], 0\n" \
"ds_read_b128 v[72:75], v1 offset:0\nv_max_f32 v62, v62, v55\nv_mfma_f32_16x16x32_bf16 v[24:27], a[16:19], v[80:83], 0\nv_max3_f32 v63, v62, v48, v49\nv_max3_f32 v62, v62, v48, v49\nv_mfma_f32_16x16x32_bf16 v[28:31], a[24:27], v[80:83], 0\ns_waitcnt lgkmcnt(3)\nv_permlane16_swap_b32 v62, v63\n" \
"v_mfma_f32_16x16x32_bf16 v[32:35], a[0:3], v[84:87], 0\nds_read_b128 v[80:83], v2 offset:4096\nv_max_f32 v50, v62, v63\nv_mfma_f32_16x16x32_bf16 v[36:39], a[8:11], v[84:87], 0\nv_max_f32 v62, v62, v63\nv_mfma_f32_16x16x32_bf16 v[40:43], a[16:19], v[84:87], 0\ns_nop 0\nv_permlane32_swap_b32 v62, v50\n" \
"v_mfma_f32_16x16x32_bf16 v[44:47], a[24:27], v[84:87], 0\ns_waitcnt lgkmcnt(3)\nv_max_f32 v62, v62, v50\nv_mul_f32 v62, s59, v62\nv_mfma_f32_16x16x32_bf16 v[16:19], a[4:7], v[88:91], v[16:19]\nds_read_b128 v[84:87], v2 offset:4352\nv_fma_f32 v52, v52, s58, v62\nv_mfma_f32_16x16x32_bf16 v[20:23], a[12:15], v[88:91], v[20:23]\n" \
"ds_read_b128 v[76:79], v1 offset:1024\nv_fma_f32 v53, v53, s58, v62\nv_mfma_f32_16x16x32_bf16 v[24:27], a[20:23], v[88:91], v[24:27]\nv_exp_f32 v52, v52\nv_mfma_f32_16x16x32_bf16 v[28:31], a[28:31], v[88:91], v[28:31]\ns_waitcnt lgkmcnt(4)\nv_fma_f32 v54, v54, s58, v62\nv_fma_f32 v55, v55, s58, v62\n" \
"v_mfma_f32_16x16x32_bf16 v[32:35], a[4:7], v[92:95], v[32:35]\nds_read_b128 v[88:91], v2 offset:6144\nv_fma_f32 v48, v48, s58, v62\nv_mfma_f32_16x16x32_bf16 v[36:39], a[12:15], v[92:95], v[36:39]\nv_exp_f32 v53, v53\nv_mfma_f32_16x16x32_bf16 v[40:43], a[20:23], v[92:95], v[40:43]\nv_exp_f32 v54, v54\nv_mfma_f32_16x16x32_bf16 v[44:47], a[28:31], v[92:95], v[44:47]\n" \
"s_waitcnt lgkmcnt(3)\nv_add_f32 v63, v52, v53\nv_add_f32 v63, v63, v54\nv_mfma_f32_16x16x32_bf16 v[16:19], a[32:35], v[80:83], v[16:19]\nds_read_b128 v[92:95], v2 offset:6400\nv_fma_f32 v49, v49, s58, v62\nv_mfma_f32_16x16x32_bf16 v[20:23], v[72:75], v[80:83], v[20:23]\nds_read_b128 v[64:67], v1 offset:2048\n" \
"v_mfma_f32_16x16x32_bf16 v[24:27], a[40:43], v[80:83], v[24:27]\nv_exp_f32 v55, v55\nv_mfma_f32_16x16x32_bf16 v[28:31], a[48:51], v[80:83], v[28:31]\ns_waitcnt lgkmcnt(4)\nv_exp_f32 v48, v48\nv_mfma_f32_16x16x32_bf16 v[32:35], a[32:35], v[84:87], v[32:35]\nds_read_b128 v[80:83], v2 offset:8192\nv_add_f32 v63, v63, v55\n" \
"v_mfma_f32_16x16x32_bf16 v[36:39], v[72:75], v[84:87], v[36:39]\nv_exp_f32 v49, v49\nv_mfma_f32_16x16x32_bf16 v[40:43], a[40:43], v[84:87], v[40:43]\nv_add_f32 v63, v63, v48\nv_add_f32 v62, v63, v49\nv_mfma_f32_16x16x32_bf16 v[44:47], a[48:51], v[84:87], v[44:47]\ns_waitcnt lgkmcnt(3)\nv_add_f32 v50, v63, v49\n" \
"v_mfma_f32_16x16x32_bf16 v[16:19], a[36:39], v[88:91], v[16:19]\nds_read_b128 v[84:87], v2 offset:8448\nv_mfma_f32_16x16x32_bf16 v[20:23], v[76:79], v[88:91], v[20:23]\nds_read_b128 v[68:71], v1 offset:3072\nv_mfma_f32_16x16x32_bf16 v[24:27], a[44:47], v[88:91], v[24:27]\nv_permlane16_swap_b32 v62, v50\nv_mfma_f32_16x16x32_bf16 v[28:31], a[52:55], v[88:91], v[28:31]\ns_waitcnt lgkmcnt(4)\n" \
"v_add_f32 v63, v62, v50\nv_add_f32 v50, v62, v50\nv_mfma_f32_16x16x32_bf16 v[32:35], a[36:39], v[92:95], v[32:35]\nds_read_b128 v[88:91], v2 offset:10240\nv_mfma_f32_16x16x32_bf16 v[36:39], v[76:79], v[92:95], v[36:39]\nv_permlane32_swap_b32 v63, v50\nv_mfma_f32_16x16x32_bf16 v[40:43], a[44:47], v[92:95], v[40:43]\nv_add_f32 v63, v63, v50\n" \
"v_mfma_f32_16x16x32_bf16 v[44:47], a[52:55], v[92:95], v[44:47]\ns_waitcnt lgkmcnt(3)\nv_rcp_f32 v63, v63\nv_mfma_f32_16x16x32_bf16 v[16:19], a[56:59], v[80:83], v[16:19]\nds_read_b128 v[92:95], v2 offset:10496\nv_mul_f32 v52, v52, v63\nv_mfma_f32_16x16x32_bf16 v[20:23], a[64:67], v[80:83], v[20:23]\nds_read_b128 v[72:75], v1 offset:4096\n" \
"v_mul_f32 v53, v53, v63\nv_mfma_f32_16x16x32_bf16 v[24:27], v[64:67], v[80:83], v[24:27]\nv_mul_f32 v54, v54, v63\nv_mul_f32 v55, v55, v63\nv_mfma_f32_16x16x32_bf16 v[28:31], a[72:75], v[80:83], v[28:31]\ns_waitcnt lgkmcnt(4)\nv_mul_f32 v48, v48, v63\nv_mul_f32 v49, v49, v63\n" \
"v_mfma_f32_16x16x32_bf16 v[32:35], a[56:59], v[84:87], v[32:35]\nds_read_b128 v[80:83], v2 offset:12288\nv_cvt_pk_bf16_f32 v52, v52, v53\nv_mfma_f32_16x16x32_bf16 v[36:39], a[64:67], v[84:87], v[36:39]\nv_cvt_pk_bf16_f32 v53, v54, v55\nv_cvt_pk_bf16_f32 v48, v48, v49\nv_mfma_f32_16x16x32_bf16 v[40:43], v[64:67], v[84:87], v[40:43]\nv_bfi_b32 v52, s61, v52, v61\n" \
"v_bfi_b32 v53, s61, v53, v61\nv_mfma_f32_16x16x32_bf16 v[44:47], a[72:75], v[84:87], v[44:47]\ns_waitcnt lgkmcnt(3)\nv_bfi_b32 v48, s61, v48, v61\nv_mfma_f32_16x16x32_bf16 v[16:19], a[60:63], v[88:91], v[16:19]\nds_read_b128 v[84:87], v2 offset:12544\nv_mfma_f32_16x16x32_bf16 v[20:23], a[68:71], v[88:91], v[20:23]\nds_read_b128 v[76:79], v1 offset:5120\n" \
"v_mfma_f32_16x16x32_bf16 v[24:27], v[68:71], v[88:91], v[24:27]\nbuffer_store_dwordx2 v[52:53], v10, s[36:39], s55 offen sc1\nv_mfma_f32_16x16x32_bf16 v[28:31], a[76:79], v[88:91], v[28:31]\ns_waitcnt lgkmcnt(4)\nbuffer_store_dword v48, v11, s[36:39], s55 offen sc1\nv_mfma_f32_16x16x32_bf16 v[32:35], a[60:63], v[92:95], v[32:35]\nds_read_b128 v[88:91], v2 offset:14336\nv_cmp_ne_u32 vcc, 0, v60\n" \
"v_mfma_f32_16x16x32_bf16 v[36:39], a[68:71], v[92:95], v[36:39]\nv_mfma_f32_16x16x32_bf16 v[40:43], v[68:71], v[92:95], v[40:43]\nv_mfma_f32_16x16x32_bf16 v[44:47], a[76:79], v[92:95], v[44:47]\ns_cbranch_vccnz .Lqk_pj%=\ns_cmp_lt_i32 s40, 63\ns_waitcnt lgkmcnt(0)\ns_cbranch_scc1 .Lqk_pwait%=\ns_branch .Lqk_end%=\n" \
".Lqk_re1%=:\nv_max_u32 v40, v80, v82\nv_cmp_eq_u32 vcc, -1, v40\ns_cbranch_vccz .Lqk_sk1964%=\ns_mov_b64 exec, vcc\nbuffer_load_dwordx4 v[80:83], v3, s[36:39], s42 offen offset:0 sc1\ns_mov_b64 exec, -1\n.Lqk_sk1964%=:\n" \
"v_max_u32 v40, v84, v86\nv_cmp_eq_u32 vcc, -1, v40\ns_cbranch_vccz .Lqk_sk1971%=\ns_mov_b64 exec, vcc\nbuffer_load_dwordx4 v[84:87], v3, s[36:39], s42 offen offset:1024 sc1\ns_mov_b64 exec, -1\n.Lqk_sk1971%=:\nv_max_u32 v40, v88, v90\n" \
"v_cmp_eq_u32 vcc, -1, v40\ns_cbranch_vccz .Lqk_sk1978%=\ns_mov_b64 exec, vcc\nbuffer_load_dwordx4 v[88:91], v3, s[36:39], s42 offen offset:2048 sc1\ns_mov_b64 exec, -1\n.Lqk_sk1978%=:\nv_max_u32 v40, v92, v94\nv_cmp_eq_u32 vcc, -1, v40\n" \
"s_cbranch_vccz .Lqk_sk1985%=\ns_mov_b64 exec, vcc\nbuffer_load_dwordx4 v[92:95], v3, s[36:39], s42 offen offset:3072 sc1\ns_mov_b64 exec, -1\n.Lqk_sk1985%=:\nv_mov_b64 v[48:49], 0\nv_mov_b64 v[50:51], 0\nv_mov_b64 v[52:53], 0\n" \
"v_mov_b64 v[54:55], 0\nv_mov_b64 v[56:57], 0\nv_mov_b64 v[58:59], 0\nv_mov_b64 v[60:61], 0\nv_mov_b64 v[62:63], 0\ns_add_u32 s43, s43, 1\ns_cmp_lt_u32 s43, 262144\ns_cbranch_scc1 .Lqk_poll1%=\n" \
"s_branch .Lqk_abort%=\n.Lqk_c2%=:\nds_read_b128 v[64:67], v1 offset:12288\nds_read_b128 v[68:71], v1 offset:13312\nds_read_b128 v[72:75], v1 offset:14336\nds_read_b128 v[76:79], v1 offset:15360\ns_nop 1\nv_cvt_pk_bf16_f32 v44, v40, v44\n" \
"v_cvt_pk_bf16_f32 v45, v41, v45\nv_cvt_pk_bf16_f32 v46, v42, v46\nv_cvt_pk_bf16_f32 v47, v43, v47\nds_write_b128 v5, v[44:47] offset:768\ns_bfe_i32 s54, s40, 0x10001\n.Lqk_poll2%=:\ns_waitcnt vmcnt(0)\nv_smfmac_f32_16x16x64_bf16 v[48:51], v[80:83], a[208:215], v4\n" \
"ds_write_b128 v0, v[80:83] offset:24576\nv_max3_u32 v40, v80, v82, v84\nv_smfmac_f32_16x16x64_bf16 v[52:55], v[80:83], a[216:223], v4\nds_write_b128 v0, v[84:87] offset:25600\nv_max3_u32 v40, v40, v86, v88\nv_smfmac_f32_16x16x64_bf16 v[56:59], v[80:83], a[224:231], v4\nds_write_b128 v0, v[88:91] offset:26624\nv_max3_u32 v40, v40, v90, v92\n" \
"v_max_u32 v40, v40, v94\nv_smfmac_f32_16x16x64_bf16 v[60:63], v[80:83], a[232:239], v4\nds_write_b128 v0, v[92:95] offset:27648\nv_cmp_eq_u32 vcc, -1, v40\nv_smfmac_f32_16x16x64_bf16 v[48:51], v[84:87], a[240:247], v4\ns_cbranch_vccnz .Lqk_re2%=\ns_waitcnt lgkmcnt(7)\nv_smfmac_f32_16x16x64_bf16 v[52:55], v[84:87], v[64:71], v4\n" \
"s_waitcnt lgkmcnt(0)\ns_barrier\nds_read_b128 v[80:83], v0 offset:28672\nds_read_b128 v[64:67], v1 offset:16384\nds_read_b128 v[68:71], v1 offset:17408\nv_smfmac_f32_16x16x64_bf16 v[56:59], v[84:87], a[248:255], v4\nv_smfmac_f32_16x16x64_bf16 v[60:63], v[84:87], v[96:103], v4\nds_read_b128 v[84:87], v0 offset:29696\n" \
"v_smfmac_f32_16x16x64_bf16 v[48:51], v[88:91], v[104:111], v4\nv_smfmac_f32_16x16x64_bf16 v[52:55], v[88:91], v[112:119], v4\nv_smfmac_f32_16x16x64_bf16 v[56:59], v[88:91], v[72:79], v4\nds_read_b128 v[72:75], v1 offset:18432\nds_read_b128 v[76:79], v1 offset:19456\nv_smfmac_f32_16x16x64_bf16 v[60:63], v[88:91], v[120:127], v4\nds_read_b128 v[88:91], v0 offset:30720\nv_smfmac_f32_16x16x64_bf16 v[48:51], v[92:95], v[128:135], v4\n" \
"ds_read_b128 v[16:19], v7 offset:256\nv_smfmac_f32_16x16x64_bf16 v[52:55], v[92:95], v[136:143], v4\nv_smfmac_f32_16x16x64_bf16 v[56:59], v[92:95], v[144:151], v4\ns_waitcnt lgkmcnt(5)\nv_smfmac_f32_16x16x64_bf16 v[60:63], v[92:95], v[64:71], v4\nds_read_b128 v[92:95], v0 offset:31744\nds_read_b128 v[64:67], v1 offset:20480\nds_read_b128 v[68:71], v1 offset:21504\n" \
"v_smfmac_f32_16x16x64_bf16 v[48:51], v[80:83], v[152:159], v4\nds_read_b128 v[20:23], v7 offset:2304\nv_smfmac_f32_16x16x64_bf16 v[52:55], v[80:83], v[160:167], v4\nv_smfmac_f32_16x16x64_bf16 v[56:59], v[80:83], v[168:175], v4\nv_smfmac_f32_16x16x64_bf16 v[60:63], v[80:83], v[176:183], v4\ns_waitcnt lgkmcnt(8)\nds_read_b128 v[80:83], v0 offset:16384\nv_smfmac_f32_16x16x64_bf16 v[48:51], v[84:87], v[184:191], v4\n" \
"s_waitcnt lgkmcnt(7)\nds_read_b128 v[24:27], v7 offset:4352\nv_smfmac_f32_16x16x64_bf16 v[52:55], v[84:87], v[72:79], v4\nds_read_b128 v[72:75], v1 offset:22528\nds_read_b128 v[76:79], v1 offset:23552\nv_smfmac_f32_16x16x64_bf16 v[56:59], v[84:87], v[192:199], v4\nv_smfmac_f32_16x16x64_bf16 v[60:63], v[84:87], v[200:207], v4\ns_waitcnt lgkmcnt(9)\n" \
"ds_read_b128 v[84:87], v0 offset:17408\nv_smfmac_f32_16x16x64_bf16 v[48:51], v[88:91], v[208:215], v4\nds_read_b128 v[28:31], v7 offset:6400\nv_smfmac_f32_16x16x64_bf16 v[52:55], v[88:91], v[216:223], v4\ns_waitcnt lgkmcnt(7)\nv_smfmac_f32_16x16x64_bf16 v[56:59], v[88:91], v[64:71], v4\nds_read_b128 v[64:67], v1 offset:0\nds_read_b128 v[68:71], v1 offset:1024\n" \
"v_smfmac_f32_16x16x64_bf16 v[60:63], v[88:91], v[224:231], v4\nds_read_b128 v[88:91], v0 offset:18432\nv_smfmac_f32_16x16x64_bf16 v[48:51], v[92:95], v[232:239], v4\nds_read_b128 v[32:35], v7 offset:8192\nv_smfmac_f32_16x16x64_bf16 v[52:55], v[92:95], v[240:247], v4\nv_smfmac_f32_16x16x64_bf16 v[56:59], v[92:95], v[248:255], v4\ns_waitcnt lgkmcnt(6)\nv_smfmac_f32_16x16x64_bf16 v[60:63], v[92:95], v[72:79], v4\n" \
"ds_read_b128 v[92:95], v0 offset:19456\nds_read_b128 v[72:75], v1 offset:2048\nds_read_b128 v[76:79], v1 offset:3072\nv_smfmac_f32_16x16x64_bf16 v[48:51], v[80:83], a[0:7], v4\nds_read_b128 v[36:39], v7 offset:10240\nv_smfmac_f32_16x16x64_bf16 v[52:55], v[80:83], a[8:15], v4\nv_smfmac_f32_16x16x64_bf16 v[56:59], v[80:83], a[16:23], v4\nv_smfmac_f32_16x16x64_bf16 v[60:63], v[80:83], a[24:31], v4\n" \
"s_waitcnt lgkmcnt(9)\nds_read_b128 v[80:83], v0 offset:20480\nv_smfmac_f32_16x16x64_bf16 v[48:51], v[84:87], a[32:39], v4\ns_waitcnt lgkmcnt(7)\nds_read_b128 v[40:43], v7 offset:12288\nv_smfmac_f32_16x16x64_bf16 v[52:55], v[84:87], v[64:71], v4\nds_read_b128 v[64:67], v1 offset:4096\nds_read_b128 v[68:71], v1 offset:5120\n" \
"v_smfmac_f32_16x16x64_bf16 v[56:59], v[84:87], a[40:47], v4\nv_smfmac_f32_16x16x64_bf16 v[60:63], v[84:87], a[48:55], v4\ns_waitcnt lgkmcnt(9)\nds_read_b128 v[84:87], v0 offset:21504\nv_smfmac_f32_16x16x64_bf16 v[48:51], v[88:91], a[56:63], v4\nds_read_b128 v[44:47], v7 offset:14336\nv_smfmac_f32_16x16x64_bf16 v[52:55], v[88:91], a[64:71], v4\ns_waitcnt lgkmcnt(7)\n" \
"v_smfmac_f32_16x16x64_bf16 v[56:59], v[88:91], v[72:79], v4\nds_read_b128 v[72:75], v1 offset:6144\nds_read_b128 v[76:79], v1 offset:7168\nv_smfmac_f32_16x16x64_bf16 v[60:63], v[88:91], a[72:79], v4\nds_read_b128 v[88:91], v0 offset:22528\nv_smfmac_f32_16x16x64_bf16 v[48:51], v[92:95], a[80:87], v4\nv_smfmac_f32_16x16x64_bf16 v[52:55], v[92:95], a[88:95], v4\nv_smfmac_f32_16x16x64_bf16 v[56:59], v[92:95], a[96:103], v4\n" \
"s_waitcnt lgkmcnt(5)\nv_smfmac_f32_16x16x64_bf16 v[60:63], v[92:95], v[64:71], v4\nds_read_b128 v[92:95], v0 offset:23552\nds_read_b128 v[64:67], v1 offset:8192\nds_read_b128 v[68:71], v1 offset:9216\nv_smfmac_f32_16x16x64_bf16 v[48:51], v[80:83], a[104:111], v4\nv_smfmac_f32_16x16x64_bf16 v[52:55], v[80:83], a[112:119], v4\nv_smfmac_f32_16x16x64_bf16 v[56:59], v[80:83], a[120:127], v4\n" \
"v_smfmac_f32_16x16x64_bf16 v[60:63], v[80:83], a[128:135], v4\ns_waitcnt lgkmcnt(7)\nv_smfmac_f32_16x16x64_bf16 v[48:51], v[84:87], a[136:143], v4\ns_waitcnt lgkmcnt(4)\nds_read_b128 v[80:83], v7 offset:8448\nv_smfmac_f32_16x16x64_bf16 v[52:55], v[84:87], v[72:79], v4\nds_read_b128 v[72:75], v1 offset:10240\nds_read_b128 v[76:79], v1 offset:11264\n" \
"v_smfmac_f32_16x16x64_bf16 v[56:59], v[84:87], a[144:151], v4\nv_smfmac_f32_16x16x64_bf16 v[60:63], v[84:87], a[152:159], v4\ns_waitcnt lgkmcnt(6)\nv_smfmac_f32_16x16x64_bf16 v[48:51], v[88:91], a[160:167], v4\nds_read_b128 v[84:87], v7 offset:10496\nv_smfmac_f32_16x16x64_bf16 v[52:55], v[88:91], a[168:175], v4\ns_waitcnt lgkmcnt(6)\nv_smfmac_f32_16x16x64_bf16 v[48:51], v[92:95], a[184:191], v4\n" \
"v_smfmac_f32_16x16x64_bf16 v[52:55], v[92:95], a[192:199], v4\ns_waitcnt lgkmcnt(4)\nv_smfmac_f32_16x16x64_bf16 v[56:59], v[88:91], v[64:71], v4\nds_read_b128 v[64:67], v7 offset:12544\nv_add_f32 v48, v48, v50\nv_smfmac_f32_16x16x64_bf16 v[60:63], v[88:91], a[176:183], v4\nds_read_b128 v[68:71], v7 offset:14592\nv_add_f32 v52, v52, v54\n" \
"v_add_f32 v49, v49, v51\nv_smfmac_f32_16x16x64_bf16 v[56:59], v[92:95], a[200:207], v4\ns_waitcnt lgkmcnt(3)\nv_add_f32 v53, v53, v55\nv_cvt_pk_bf16_f32 v50, v48, v52\nv_smfmac_f32_16x16x64_bf16 v[60:63], v[92:95], v[72:79], v4\nv_cvt_pk_bf16_f32 v51, v49, v53\nds_write2_b32 v6, v50, v51 offset0:0 offset1:4\n" \
"s_nop 1\nv_add_f32 v56, v56, v58\nv_add_f32 v57, v57, v59\ns_nop 1\nv_add_f32 v60, v60, v62\nv_add_f32 v61, v61, v63\nv_cvt_pk_bf16_f32 v62, v56, v60\nv_cvt_pk_bf16_f32 v63, v57, v61\n" \
"ds_write2_b32 v6, v62, v63 offset0:36 offset1:40\nv_mov_b32 v61, s54\nv_mfma_f32_16x16x32_bf16 v[52:55], v[32:35], v[16:19], 0\nv_mfma_f32_16x16x32_bf16 v[52:55], v[36:39], v[20:23], v[52:55]\nv_mfma_f32_16x16x32_bf16 v[52:55], v[40:43], v[24:27], v[52:55]\nv_mfma_f32_16x16x32_bf16 v[52:55], v[44:47], v[28:31], v[52:55]\ns_waitcnt lgkmcnt(0)\ns_barrier\n" \
"ds_read_b128 v[32:35], v9 offset:0\nds_read_b128 v[36:39], v9 offset:576\nds_read_b128 v[40:43], v9 offset:1152\nds_read_b128 v[44:47], v9 offset:1728\nds_read_b32 v60, v0 offset:53760\nv_mfma_f32_16x16x32_bf16 v[56:59], v[80:83], v[16:19], v[12:15]\nv_mfma_f32_16x16x32_bf16 v[56:59], v[84:87], v[20:23], v[56:59]\nv_mfma_f32_16x16x32_bf16 v[56:59], v[64:67], v[24:27], v[56:59]\n" \
"v_mfma_f32_16x16x32_bf16 v[56:59], v[68:71], v[28:31], v[56:59]\ns_waitcnt lgkmcnt(4)\nds_read_b128 v[80:83], v2 offset:0\nds_read_b128 v[84:87], v2 offset:256\nds_read_b128 v[88:91], v2 offset:2048\nv_mfma_f32_16x16x32_bf16 v[48:51], v[32:35], v[16:19], 0\ns_waitcnt lgkmcnt(6)\nv_mfma_f32_16x16x32_bf16 v[48:51], v[36:39], v[20:23], v[48:51]\n" \
"s_waitcnt lgkmcnt(5)\nv_mfma_f32_16x16x32_bf16 v[48:51], v[40:43], v[24:27], v[48:51]\ns_waitcnt lgkmcnt(4)\nv_mfma_f32_16x16x32_bf16 v[48:51], v[44:47], v[28:31], v[48:51]\ns_waitcnt lgkmcnt(2)\nv_mfma_f32_16x16x32_bf16 v[16:19], a[0:3], v[80:83], 0\nds_read_b128 v[92:95], v2 offset:2304\nv_max3_f32 v62, v52, v53, v54\n" \
"v_max3_f32 v62, v62, v56, v57\nv_mfma_f32_16x16x32_bf16 v[20:23], a[8:11], v[80:83], 0\nds_read_b128 v[72:75], v1 offset:0\nv_max3_f32 v62, v62, v58, v59\nv_max_f32 v62, v62, v55\nv_mfma_f32_16x16x32_bf16 v[24:27], a[16:19], v[80:83], 0\nv_max3_f32 v63, v62, v48, v49\nv_max3_f32 v62, v62, v48, v49\n" \
"v_mfma_f32_16x16x32_bf16 v[28:31], a[24:27], v[80:83], 0\ns_waitcnt lgkmcnt(3)\nv_permlane16_swap_b32 v62, v63\nv_max_f32 v50, v62, v63\nv_mfma_f32_16x16x32_bf16 v[32:35], a[0:3], v[84:87], 0\nds_read_b128 v[80:83], v2 offset:4096\nv_max_f32 v62, v62, v63\nv_mfma_f32_16x16x32_bf16 v[36:39], a[8:11], v[84:87], 0\n" \
"s_nop 0\nv_permlane32_swap_b32 v62, v50\nv_max_f32 v62, v62, v50\nv_mfma_f32_16x16x32_bf16 v[40:43], a[16:19], v[84:87], 0\nv_mul_f32 v62, s59, v62\nv_fma_f32 v52, v52, s58, v62\nv_fma_f32 v53, v53, s58, v62\nv_mfma_f32_16x16x32_bf16 v[44:47], a[24:27], v[84:87], 0\n" \
"s_waitcnt lgkmcnt(3)\nv_exp_f32 v52, v52\nv_fma_f32 v54, v54, s58, v62\nv_mfma_f32_16x16x32_bf16 v[16:19], a[4:7], v[88:91], v[16:19]\nds_read_b128 v[84:87], v2 offset:4352\nv_exp_f32 v53, v53\nv_mfma_f32_16x16x32_bf16 v[20:23], a[12:15], v[88:91], v[20:23]\nds_read_b128 v[76:79], v1 offset:1024\n" \
"v_fma_f32 v55, v55, s58, v62\nv_add_f32 v63, v52, v53\nv_mfma_f32_16x16x32_bf16 v[24:27], a[20:23], v[88:91], v[24:27]\nv_exp_f32 v54, v54\nv_fma_f32 v56, v56, s58, v62\nv_mfma_f32_16x16x32_bf16 v[28:31], a[28:31], v[88:91], v[28:31]\ns_waitcnt lgkmcnt(4)\nv_exp_f32 v55, v55\n" \
"v_add_f32 v63, v63, v54\nv_mfma_f32_16x16x32_bf16 v[32:35], a[4:7], v[92:95], v[32:35]\nds_read_b128 v[88:91], v2 offset:6144\nv_fma_f32 v57, v57, s58, v62\nv_add_f32 v63, v63, v55\nv_mfma_f32_16x16x32_bf16 v[36:39], a[12:15], v[92:95], v[36:39]\nv_exp_f32 v56, v56\nv_fma_f32 v58, v58, s58, v62\n" \
"v_mfma_f32_16x16x32_bf16 v[40:43], a[20:23], v[92:95], v[40:43]\nv_exp_f32 v57, v57\nv_add_f32 v63, v63, v56\nv_mfma_f32_16x16x32_bf16 v[44:47], a[28:31], v[92:95], v[44:47]\ns_waitcnt lgkmcnt(3)\nv_fma_f32 v59, v59, s58, v62\nv_exp_f32 v58, v58\nv_mfma_f32_16x16x32_bf16 v[16:19], a[32:35], v[80:83], v[16:19]\n" \
"ds_read_b128 v[92:95], v2 offset:6400\nv_add_f32 v63, v63, v57\nv_fma_f32 v48, v48, s58, v62\nv_mfma_f32_16x16x32_bf16 v[20:23], v[72:75], v[80:83], v[20:23]\nds_read_b128 v[64:67], v1 offset:2048\nv_exp_f32 v59, v59\nv_mfma_f32_16x16x32_bf16 v[24:27], a[40:43], v[80:83], v[24:27]\nv_add_f32 v63, v63, v58\n" \
"v_fma_f32 v49, v49, s58, v62\nv_add_f32 v63, v63, v59\nv_mfma_f32_16x16x32_bf16 v[28:31], a[48:51], v[80:83], v[28:31]\ns_waitcnt lgkmcnt(4)\nv_exp_f32 v48, v48\nv_mfma_f32_16x16x32_bf16 v[32:35], a[32:35], v[84:87], v[32:35]\nds_read_b128 v[80:83], v2 offset:8192\nv_exp_f32 v49, v49\n" \
"v_mfma_f32_16x16x32_bf16 v[36:39], v[72:75], v[84:87], v[36:39]\nv_add_f32 v63, v63, v48\nv_add_f32 v62, v63, v49\nv_add_f32 v50, v63, v49\nv_mfma_f32_16x16x32_bf16 v[40:43], a[40:43], v[84:87], v[40:43]\ns_nop 0\nv_permlane16_swap_b32 v62, v50\nv_add_f32 v63, v62, v50\n" \
"v_mfma_f32_16x16x32_bf16 v[44:47], a[48:51], v[84:87], v[44:47]\ns_waitcnt lgkmcnt(3)\nv_add_f32 v50, v62, v50\nv_mfma_f32_16x16x32_bf16 v[16:19], a[36:39], v[88:91], v[16:19]\nds_read_b128 v[84:87], v2 offset:8448\nv_permlane32_swap_b32 v63, v50\nv_mfma_f32_16x16x32_bf16 v[20:23], v[76:79], v[88:91], v[20:23]\nds_read_b128 v[68:71], v1 offset:3072\n" \
"v_add_f32 v63, v63, v50\nv_mfma_f32_16x16x32_bf16 v[24:27], a[44:47], v[88:91], v[24:27]\nv_rcp_f32 v63, v63\nv_mfma_f32_16x16x32_bf16 v[28:31], a[52:55], v[88:91], v[28:31]\ns_waitcnt lgkmcnt(4)\nv_mul_f32 v52, v52, v63\nv_mul_f32 v53, v53, v63\nv_mul_f32 v54, v54, v63\n" \
"v_mfma_f32_16x16x32_bf16 v[32:35], a[36:39], v[92:95], v[32:35]\nds_read_b128 v[88:91], v2 offset:10240\nv_mul_f32 v55, v55, v63\nv_mul_f32 v56, v56, v63\nv_mfma_f32_16x16x32_bf16 v[36:39], v[76:79], v[92:95], v[36:39]\nv_mul_f32 v57, v57, v63\nv_mul_f32 v58, v58, v63\nv_mul_f32 v59, v59, v63\n" \
"v_mfma_f32_16x16x32_bf16 v[40:43], a[44:47], v[92:95], v[40:43]\nv_mul_f32 v48, v48, v63\nv_mul_f32 v49, v49, v63\nv_cvt_pk_bf16_f32 v52, v52, v53\nv_mfma_f32_16x16x32_bf16 v[44:47], a[52:55], v[92:95], v[44:47]\ns_waitcnt lgkmcnt(3)\nv_cvt_pk_bf16_f32 v53, v54, v55\nv_cvt_pk_bf16_f32 v48, v48, v49\n" \
"v_cvt_pk_bf16_f32 v54, v56, v57\nv_mfma_f32_16x16x32_bf16 v[16:19], a[56:59], v[80:83], v[16:19]\nds_read_b128 v[92:95], v2 offset:10496\nv_cvt_pk_bf16_f32 v55, v58, v59\nv_bfi_b32 v52, s61, v52, v61\nv_mfma_f32_16x16x32_bf16 v[20:23], a[64:67], v[80:83], v[20:23]\nds_read_b128 v[72:75], v1 offset:4096\nv_bfi_b32 v53, s61, v53, v61\n" \
"v_bfi_b32 v54, s61, v54, v61\nv_mfma_f32_16x16x32_bf16 v[24:27], v[64:67], v[80:83], v[24:27]\nv_bfi_b32 v55, s61, v55, v61\nv_bfi_b32 v48, s61, v48, v61\nv_mfma_f32_16x16x32_bf16 v[28:31], a[72:75], v[80:83], v[28:31]\ns_waitcnt lgkmcnt(4)\nbuffer_store_dwordx4 v[52:55], v10, s[36:39], s55 offen sc1\nv_mfma_f32_16x16x32_bf16 v[32:35], a[56:59], v[84:87], v[32:35]\n" \
"ds_read_b128 v[80:83], v2 offset:12288\nv_mfma_f32_16x16x32_bf16 v[36:39], a[64:67], v[84:87], v[36:39]\nbuffer_store_dword v48, v11, s[36:39], s55 offen sc1\nv_cmp_ne_u32 vcc, 0, v60\nv_mfma_f32_16x16x32_bf16 v[40:43], v[64:67], v[84:87], v[40:43]\nv_mfma_f32_16x16x32_bf16 v[44:47], a[72:75], v[84:87], v[44:47]\ns_waitcnt lgkmcnt(3)\nv_mfma_f32_16x16x32_bf16 v[16:19], a[60:63], v[88:91], v[16:19]\n" \
"ds_read_b128 v[84:87], v2 offset:12544\nv_mfma_f32_16x16x32_bf16 v[20:23], a[68:71], v[88:91], v[20:23]\nds_read_b128 v[76:79], v1 offset:5120\nv_mfma_f32_16x16x32_bf16 v[24:27], v[68:71], v[88:91], v[24:27]\nv_mfma_f32_16x16x32_bf16 v[28:31], a[76:79], v[88:91], v[28:31]\ns_waitcnt lgkmcnt(4)\nv_mfma_f32_16x16x32_bf16 v[32:35], a[60:63], v[92:95], v[32:35]\nds_read_b128 v[88:91], v2 offset:14336\n" \
"v_mfma_f32_16x16x32_bf16 v[36:39], a[68:71], v[92:95], v[36:39]\nv_mfma_f32_16x16x32_bf16 v[40:43], v[68:71], v[92:95], v[40:43]\nv_mfma_f32_16x16x32_bf16 v[44:47], a[76:79], v[92:95], v[44:47]\ns_cbranch_vccnz .Lqk_pj%=\ns_cmp_lt_i32 s40, 63\ns_waitcnt lgkmcnt(0)\ns_cbranch_scc1 .Lqk_pwait%=\ns_branch .Lqk_end%=\n" \
".Lqk_re2%=:\nv_max_u32 v40, v80, v82\nv_cmp_eq_u32 vcc, -1, v40\ns_cbranch_vccz .Lqk_sk2352%=\ns_mov_b64 exec, vcc\nbuffer_load_dwordx4 v[80:83], v3, s[36:39], s42 offen offset:0 sc1\ns_mov_b64 exec, -1\n.Lqk_sk2352%=:\n" \
"v_max_u32 v40, v84, v86\nv_cmp_eq_u32 vcc, -1, v40\ns_cbranch_vccz .Lqk_sk2359%=\ns_mov_b64 exec, vcc\nbuffer_load_dwordx4 v[84:87], v3, s[36:39], s42 offen offset:1024 sc1\ns_mov_b64 exec, -1\n.Lqk_sk2359%=:\nv_max_u32 v40, v88, v90\n" \
"v_cmp_eq_u32 vcc, -1, v40\ns_cbranch_vccz .Lqk_sk2366%=\ns_mov_b64 exec, vcc\nbuffer_load_dwordx4 v[88:91], v3, s[36:39], s42 offen offset:2048 sc1\ns_mov_b64 exec, -1\n.Lqk_sk2366%=:\nv_max_u32 v40, v92, v94\nv_cmp_eq_u32 vcc, -1, v40\n" \
"s_cbranch_vccz .Lqk_sk2373%=\ns_mov_b64 exec, vcc\nbuffer_load_dwordx4 v[92:95], v3, s[36:39], s42 offen offset:3072 sc1\ns_mov_b64 exec, -1\n.Lqk_sk2373%=:\nv_mov_b64 v[48:49], 0\nv_mov_b64 v[50:51], 0\nv_mov_b64 v[52:53], 0\n" \
"v_mov_b64 v[54:55], 0\nv_mov_b64 v[56:57], 0\nv_mov_b64 v[58:59], 0\nv_mov_b64 v[60:61], 0\nv_mov_b64 v[62:63], 0\ns_add_u32 s43, s43, 1\ns_cmp_lt_u32 s43, 262144\ns_cbranch_scc1 .Lqk_poll2%=\n" \
"s_branch .Lqk_abort%=\n.Lqk_c3%=:\nds_read_b128 v[64:67], v1 offset:18432\nds_read_b128 v[68:71], v1 offset:19456\nds_read_b128 v[72:75], v1 offset:20480\nds_read_b128 v[76:79], v1 offset:21504\ns_nop 1\nv_cvt_pk_bf16_f32 v44, v40, v44\n" \
"v_cvt_pk_bf16_f32 v45, v41, v45\nv_cvt_pk_bf16_f32 v46, v42, v46\nv_cvt_pk_bf16_f32 v47, v43, v47\nds_write_b128 v5, v[44:47] offset:768\ns_add_u32 s44, s40, 1\ns_lshl_b32 s44, s44, 17\ns_add_u32 s44, s44, %[xn]\ns_lshr_b32 s45, s44, 8\n" \
"s_and_b32 s45, s45, 0x2000\ns_xor_b32 s41, s44, s45\nbuffer_load_dword v16, v12, s[36:39], s56 offen sc1\n.Lqk_poll3%=:\ns_waitcnt vmcnt(1)\nv_smfmac_f32_16x16x64_bf16 v[48:51], v[80:83], v[152:159], v4\nds_write_b128 v0, v[80:83] offset:28672\nv_max3_u32 v40, v80, v82, v84\n" \
"v_smfmac_f32_16x16x64_bf16 v[52:55], v[80:83], v[160:167], v4\nds_write_b128 v0, v[84:87] offset:29696\nv_max3_u32 v40, v40, v86, v88\nv_smfmac_f32_16x16x64_bf16 v[56:59], v[80:83], v[168:175], v4\nds_write_b128 v0, v[88:91] offset:30720\nv_max3_u32 v40, v40, v90, v92\nv_max_u32 v40, v40, v94\nv_smfmac_f32_16x16x64_bf16 v[60:63], v[80:83], v[176:183], v4\n" \
"ds_write_b128 v0, v[92:95] offset:31744\nv_cmp_eq_u32 vcc, -1, v40\nv_smfmac_f32_16x16x64_bf16 v[48:51], v[84:87], v[184:191], v4\ns_cbranch_vccnz .Lqk_re3%=\ns_waitcnt lgkmcnt(7)\nv_smfmac_f32_16x16x64_bf16 v[52:55], v[84:87], v[64:71], v4\ns_waitcnt lgkmcnt(0)\ns_barrier\n" \
"ds_read_b128 v[80:83], v0 offset:16384\nds_read_b128 v[64:67], v1 offset:22528\nds_read_b128 v[68:71], v1 offset:23552\nv_smfmac_f32_16x16x64_bf16 v[56:59], v[84:87], v[192:199], v4\nv_smfmac_f32_16x16x64_bf16 v[60:63], v[84:87], v[200:207], v4\nds_read_b128 v[84:87], v0 offset:17408\nv_smfmac_f32_16x16x64_bf16 v[48:51], v[88:91], v[208:215], v4\nv_smfmac_f32_16x16x64_bf16 v[52:55], v[88:91], v[216:223], v4\n" \
"v_smfmac_f32_16x16x64_bf16 v[56:59], v[88:91], v[72:79], v4\ns_waitcnt vmcnt(0)\nds_read_b128 v[72:75], v1 offset:0\nds_read_b128 v[76:79], v1 offset:1024\nv_cmp_ge_i32 vcc, s40, v16\nv_smfmac_f32_16x16x64_bf16 v[60:63], v[88:91], v[224:231], v4\ns_cmp_eq_u64 vcc, 0\ns_cselect_b32 s61, s41, -2147483648\n" \
"s_cselect_b32 s44, 1, 0\nds_read_b128 v[88:91], v0 offset:18432\nv_smfmac_f32_16x16x64_bf16 v[48:51], v[92:95], v[232:239], v4\ns_add_u32 m0, %[l0], 0\ns_add_u32 s60, s61, 0\nbuffer_load_dwordx4 v0, s[36:39], s60 offen offset:0 sc1 lds\nv_smfmac_f32_16x16x64_bf16 v[52:55], v[92:95], v[240:247], v4\nv_mov_b32 v16, s44\n" \
"v_smfmac_f32_16x16x64_bf16 v[56:59], v[92:95], v[248:255], v4\ns_waitcnt lgkmcnt(4)\nbuffer_load_dwordx4 v0, s[36:39], s60 offen offset:1024 sc1 lds\nv_smfmac_f32_16x16x64_bf16 v[60:63], v[92:95], v[64:71], v4\nds_read_b128 v[92:95], v0 offset:19456\nds_read_b128 v[64:67], v1 offset:2048\nds_read_b128 v[68:71], v1 offset:3072\nds_write_b32 v0, v16 offset:53760\n" \
"v_smfmac_f32_16x16x64_bf16 v[48:51], v[80:83], a[0:7], v4\nbuffer_load_dwordx4 v0, s[36:39], s60 offen offset:2048 sc1 lds\nv_smfmac_f32_16x16x64_bf16 v[52:55], v[80:83], a[8:15], v4\nv_smfmac_f32_16x16x64_bf16 v[56:59], v[80:83], a[16:23], v4\nbuffer_load_dwordx4 v0, s[36:39], s60 offen offset:3072 sc1 lds\nv_smfmac_f32_16x16x64_bf16 v[60:63], v[80:83], a[24:31], v4\ns_waitcnt lgkmcnt(7)\nds_read_b128 v[80:83], v0 offset:20480\n" \
"v_smfmac_f32_16x16x64_bf16 v[48:51], v[84:87], a[32:39], v4\ns_add_u32 m0, %[l0], 4096\ns_add_u32 s60, s61, 4096\ns_waitcnt lgkmcnt(6)\nbuffer_load_dwordx4 v0, s[36:39], s60 offen offset:0 sc1 lds\nv_smfmac_f32_16x16x64_bf16 v[52:55], v[84:87], v[72:79], v4\nds_read_b128 v[72:75], v1 offset:4096\nds_read_b128 v[76:79], v1 offset:5120\n" \
"v_smfmac_f32_16x16x64_bf16 v[56:59], v[84:87], a[40:47], v4\nbuffer_load_dwordx4 v0, s[36:39], s60 offen offset:1024 sc1 lds\nv_smfmac_f32_16x16x64_bf16 v[60:63], v[84:87], a[48:55], v4\ns_waitcnt lgkmcnt(7)\nds_read_b128 v[84:87], v0 offset:21504\nv_smfmac_f32_16x16x64_bf16 v[48:51], v[88:91], a[56:63], v4\nbuffer_load_dwordx4 v0, s[36:39], s60 offen offset:2048 sc1 lds\nv_smfmac_f32_16x16x64_bf16 v[52:55], v[88:91], a[64:71], v4\n" \
"s_waitcnt lgkmcnt(5)\nv_smfmac_f32_16x16x64_bf16 v[56:59], v[88:91], v[64:71], v4\nds_read_b128 v[64:67], v1 offset:6144\nds_read_b128 v[68:71], v1 offset:7168\nbuffer_load_dwordx4 v0, s[36:39], s60 offen offset:3072 sc1 lds\nv_smfmac_f32_16x16x64_bf16 v[60:63], v[88:91], a[72:79], v4\nds_read_b128 v[88:91], v0 offset:22528\nv_smfmac_f32_16x16x64_bf16 v[48:51], v[92:95], a[80:87], v4\n" \
"s_add_u32 m0, %[l0], 8192\ns_add_u32 s60, s61, 16384\nbuffer_load_dwordx4 v0, s[36:39], s60 offen offset:0 sc1 lds\nv_smfmac_f32_16x16x64_bf16 v[52:55], v[92:95], a[88:95], v4\nv_smfmac_f32_16x16x64_bf16 v[56:59], v[92:95], a[96:103], v4\ns_waitcnt lgkmcnt(4)\nbuffer_load_dwordx4 v0, s[36:39], s60 offen offset:1024 sc1 lds\nv_smfmac_f32_16x16x64_bf16 v[60:63], v[92:95], v[72:79], v4\n" \
"ds_read_b128 v[92:95], v0 offset:23552\nds_read_b128 v[72:75], v1 offset:8192\nds_read_b128 v[76:79], v1 offset:9216\nv_smfmac_f32_16x16x64_bf16 v[48:51], v[80:83], a[104:111], v4\nbuffer_load_dwordx4 v0, s[36:39], s60 offen offset:2048 sc1 lds\nv_smfmac_f32_16x16x64_bf16 v[52:55], v[80:83], a[112:119], v4\nv_smfmac_f32_16x16x64_bf16 v[56:59], v[80:83], a[120:127], v4\nbuffer_load_dwordx4 v0, s[36:39], s60 offen offset:3072 sc1 lds\n" \
"v_smfmac_f32_16x16x64_bf16 v[60:63], v[80:83], a[128:135], v4\ns_waitcnt lgkmcnt(6)\nds_read_b128 v[80:83], v0 offset:24576\nv_smfmac_f32_16x16x64_bf16 v[48:51], v[84:87], a[136:143], v4\ns_add_u32 m0, %[l0], 12288\ns_add_u32 s60, s61, 20480\ns_waitcnt lgkmcnt(5)\nbuffer_load_dwordx4 v0, s[36:39], s60 offen offset:0 sc1 lds\n" \
"v_smfmac_f32_16x16x64_bf16 v[52:55], v[84:87], v[64:71], v4\nds_read_b128 v[64:67], v1 offset:10240\nds_read_b128 v[68:71], v1 offset:11264\nv_smfmac_f32_16x16x64_bf16 v[56:59], v[84:87], a[144:151], v4\nbuffer_load_dwordx4 v0, s[36:39], s60 offen offset:1024 sc1 lds\nv_smfmac_f32_16x16x64_bf16 v[60:63], v[84:87], a[152:159], v4\ns_waitcnt lgkmcnt(6)\nds_read_b128 v[84:87], v0 offset:25600\n" \
"v_smfmac_f32_16x16x64_bf16 v[48:51], v[88:91], a[160:167], v4\nbuffer_load_dwordx4 v0, s[36:39], s60 offen offset:2048 sc1 lds\nv_smfmac_f32_16x16x64_bf16 v[52:55], v[88:91], a[168:175], v4\ns_waitcnt lgkmcnt(4)\nv_smfmac_f32_16x16x64_bf16 v[56:59], v[88:91], v[72:79], v4\nds_read_b128 v[72:75], v1 offset:12288\nds_read_b128 v[76:79], v1 offset:13312\nbuffer_load_dwordx4 v0, s[36:39], s60 offen offset:3072 sc1 lds\n" \
"v_smfmac_f32_16x16x64_bf16 v[60:63], v[88:91], a[176:183], v4\nds_read_b128 v[88:91], v0 offset:26624\nv_smfmac_f32_16x16x64_bf16 v[48:51], v[92:95], a[184:191], v4\nv_smfmac_f32_16x16x64_bf16 v[52:55], v[92:95], a[192:199], v4\nv_smfmac_f32_16x16x64_bf16 v[56:59], v[92:95], a[200:207], v4\ns_waitcnt lgkmcnt(4)\nv_smfmac_f32_16x16x64_bf16 v[60:63], v[92:95], v[64:71], v4\nds_read_b128 v[92:95], v0 offset:27648\n" \
"ds_read_b128 v[64:67], v1 offset:14336\nds_read_b128 v[68:71], v1 offset:15360\nv_smfmac_f32_16x16x64_bf16 v[48:51], v[80:83], a[208:215], v4\nv_smfmac_f32_16x16x64_bf16 v[52:55], v[80:83], a[216:223], v4\nv_smfmac_f32_16x16x64_bf16 v[56:59], v[80:83], a[224:231], v4\nv_smfmac_f32_16x16x64_bf16 v[60:63], v[80:83], a[232:239], v4\ns_waitcnt lgkmcnt(6)\nv_smfmac_f32_16x16x64_bf16 v[48:51], v[84:87], a[240:247], v4\n" \
"s_waitcnt lgkmcnt(4)\nv_smfmac_f32_16x16x64_bf16 v[52:55], v[84:87], v[72:79], v4\nds_read_b128 v[72:75], v1 offset:16384\nds_read_b128 v[76:79], v1 offset:17408\nv_smfmac_f32_16x16x64_bf16 v[56:59], v[84:87], a[248:255], v4\nv_smfmac_f32_16x16x64_bf16 v[60:63], v[84:87], v[96:103], v4\ns_waitcnt lgkmcnt(5)\nv_smfmac_f32_16x16x64_bf16 v[48:51], v[88:91], v[104:111], v4\n" \
"v_smfmac_f32_16x16x64_bf16 v[52:55], v[88:91], v[112:119], v4\ns_waitcnt lgkmcnt(4)\nv_smfmac_f32_16x16x64_bf16 v[48:51], v[92:95], v[128:135], v4\nv_smfmac_f32_16x16x64_bf16 v[52:55], v[92:95], v[136:143], v4\ns_waitcnt lgkmcnt(2)\nv_smfmac_f32_16x16x64_bf16 v[56:59], v[88:91], v[64:71], v4\nv_add_f32 v48, v48, v50\nv_smfmac_f32_16x16x64_bf16 v[60:63], v[88:91], v[120:127], v4\n" \
"v_add_f32 v52, v52, v54\nv_add_f32 v49, v49, v51\nv_smfmac_f32_16x16x64_bf16 v[56:59], v[92:95], v[144:151], v4\ns_waitcnt lgkmcnt(0)\nv_add_f32 v53, v53, v55\nv_cvt_pk_bf16_f32 v50, v48, v52\nv_smfmac_f32_16x16x64_bf16 v[60:63], v[92:95], v[72:79], v4\nv_cvt_pk_bf16_f32 v51, v49, v53\n" \
"ds_write2_b32 v6, v50, v51 offset0:0 offset1:4\ns_nop 1\nv_add_f32 v56, v56, v58\nv_add_f32 v57, v57, v59\ns_nop 1\nv_add_f32 v60, v60, v62\nv_add_f32 v61, v61, v63\nv_cvt_pk_bf16_f32 v62, v56, v60\n" \
"v_cvt_pk_bf16_f32 v63, v57, v61\nds_write2_b32 v6, v62, v63 offset0:36 offset1:40\ns_waitcnt vmcnt(0)\ns_waitcnt lgkmcnt(0)\ns_barrier\ns_cbranch_vccnz .Lqk_slow%=\nds_read_b128 v[80:83], v2 offset:0\nds_read_b128 v[84:87], v2 offset:256\n" \
"ds_read_b128 v[88:91], v2 offset:2048\ns_add_u32 m0, %[l0], 16384\ns_add_u32 s60, s41, 32768\nbuffer_load_dwordx4 v0, s[36:39], s60 offen offset:0 sc1 lds\nbuffer_load_dwordx4 v0, s[36:39], s60 offen offset:1024 sc1 lds\nbuffer_load_dwordx4 v0, s[36:39], s60 offen offset:2048 sc1 lds\nbuffer_load_dwordx4 v0, s[36:39], s60 offen offset:3072 sc1 lds\ns_add_u32 m0, %[l0], 20480\n" \
"s_add_u32 s60, s41, 36864\nbuffer_load_dwordx4 v0, s[36:39], s60 offen offset:0 sc1 lds\nbuffer_load_dwordx4 v0, s[36:39], s60 offen offset:1024 sc1 lds\nbuffer_load_dwordx4 v0, s[36:39], s60 offen offset:2048 sc1 lds\nbuffer_load_dwordx4 v0, s[36:39], s60 offen offset:3072 sc1 lds\ns_add_u32 m0, %[l0], 24576\ns_add_u32 s60, s41, 49152\nbuffer_load_dwordx4 v0, s[36:39], s60 offen offset:0 sc1 lds\n" \
"buffer_load_dwordx4 v0, s[36:39], s60 offen offset:1024 sc1 lds\nbuffer_load_dwordx4 v0, s[36:39], s60 offen offset:2048 sc1 lds\nbuffer_load_dwordx4 v0, s[36:39], s60 offen offset:3072 sc1 lds\ns_add_u32 m0, %[l0], 28672\ns_add_u32 s60, s41, 53248\nbuffer_load_dwordx4 v0, s[36:39], s60 offen offset:0 sc1 lds\nbuffer_load_dwordx4 v0, s[36:39], s60 offen offset:1024 sc1 lds\nbuffer_load_dwordx4 v0, s[36:39], s60 offen offset:2048 sc1 lds\n" \
"buffer_load_dwordx4 v0, s[36:39], s60 offen offset:3072 sc1 lds\ns_waitcnt lgkmcnt(2)\nv_mfma_f32_16x16x32_bf16 v[16:19], a[0:3], v[80:83], 0\nds_read_b128 v[92:95], v2 offset:2304\nv_mfma_f32_16x16x32_bf16 v[20:23], a[8:11], v[80:83], 0\nds_read_b128 v[72:75], v1 offset:0\nv_mfma_f32_16x16x32_bf16 v[24:27], a[16:19], v[80:83], 0\nv_mfma_f32_16x16x32_bf16 v[28:31], a[24:27], v[80:83], 0\n" \
"s_waitcnt lgkmcnt(3)\nv_mfma_f32_16x16x32_bf16 v[32:35], a[0:3], v[84:87], 0\nds_read_b128 v[80:83], v2 offset:4096\nv_mfma_f32_16x16x32_bf16 v[36:39], a[8:11], v[84:87], 0\nv_mfma_f32_16x16x32_bf16 v[40:43], a[16:19], v[84:87], 0\nv_mfma_f32_16x16x32_bf16 v[44:47], a[24:27], v[84:87], 0\ns_waitcnt lgkmcnt(3)\nv_mfma_f32_16x16x32_bf16 v[16:19], a[4:7], v[88:91], v[16:19]\n" \
"ds_read_b128 v[84:87], v2 offset:4352\nv_mfma_f32_16x16x32_bf16 v[20:23], a[12:15], v[88:91], v[20:23]\nds_read_b128 v[76:79], v1 offset:1024\nv_mfma_f32_16x16x32_bf16 v[24:27], a[20:23], v[88:91], v[24:27]\nv_mfma_f32_16x16x32_bf16 v[28:31], a[28:31], v[88:91], v[28:31]\ns_waitcnt lgkmcnt(4)\nv_mfma_f32_16x16x32_bf16 v[32:35], a[4:7], v[92:95], v[32:35]\nds_read_b128 v[88:91], v2 offset:6144\n" \
"v_mfma_f32_16x16x32_bf16 v[36:39], a[12:15], v[92:95], v[36:39]\nv_mfma_f32_16x16x32_bf16 v[40:43], a[20:23], v[92:95], v[40:43]\nv_mfma_f32_16x16x32_bf16 v[44:47], a[28:31], v[92:95], v[44:47]\ns_waitcnt lgkmcnt(3)\nv_mfma_f32_16x16x32_bf16 v[16:19], a[32:35], v[80:83], v[16:19]\nds_read_b128 v[92:95], v2 offset:6400\nv_mfma_f32_16x16x32_bf16 v[20:23], v[72:75], v[80:83], v[20:23]\nds_read_b128 v[64:67], v1 offset:2048\n" \
"v_mfma_f32_16x16x32_bf16 v[24:27], a[40:43], v[80:83], v[24:27]\nv_mfma_f32_16x16x32_bf16 v[28:31], a[48:51], v[80:83], v[28:31]\ns_waitcnt lgkmcnt(4)\nv_mfma_f32_16x16x32_bf16 v[32:35], a[32:35], v[84:87], v[32:35]\nds_read_b128 v[80:83], v2 offset:8192\nv_mfma_f32_16x16x32_bf16 v[36:39], v[72:75], v[84:87], v[36:39]\nv_mfma_f32_16x16x32_bf16 v[40:43], a[40:43], v[84:87], v[40:43]\nv_mfma_f32_16x16x32_bf16 v[44:47], a[48:51], v[84:87], v[44:47]\n" \
"s_waitcnt lgkmcnt(3)\nv_mfma_f32_16x16x32_bf16 v[16:19], a[36:39], v[88:91], v[16:19]\nds_read_b128 v[84:87], v2 offset:8448\nv_mfma_f32_16x16x32_bf16 v[20:23], v[76:79], v[88:91], v[20:23]\nds_read_b128 v[68:71], v1 offset:3072\nv_mfma_f32_16x16x32_bf16 v[24:27], a[44:47], v[88:91], v[24:27]\nv_mfma_f32_16x16x32_bf16 v[28:31], a[52:55], v[88:91], v[28:31]\ns_waitcnt lgkmcnt(4)\n" \
"v_mfma_f32_16x16x32_bf16 v[32:35], a[36:39], v[92:95], v[32:35]\nds_read_b128 v[88:91], v2 offset:10240\nv_mfma_f32_16x16x32_bf16 v[36:39], v[76:79], v[92:95], v[36:39]\nv_mfma_f32_16x16x32_bf16 v[40:43], a[44:47], v[92:95], v[40:43]\nv_mfma_f32_16x16x32_bf16 v[44:47], a[52:55], v[92:95], v[44:47]\ns_waitcnt lgkmcnt(3)\nv_mfma_f32_16x16x32_bf16 v[16:19], a[56:59], v[80:83], v[16:19]\nds_read_b128 v[92:95], v2 offset:10496\n" \
"v_mfma_f32_16x16x32_bf16 v[20:23], a[64:67], v[80:83], v[20:23]\nds_read_b128 v[72:75], v1 offset:4096\nv_mfma_f32_16x16x32_bf16 v[24:27], v[64:67], v[80:83], v[24:27]\nv_mfma_f32_16x16x32_bf16 v[28:31], a[72:75], v[80:83], v[28:31]\ns_waitcnt lgkmcnt(4)\nv_mfma_f32_16x16x32_bf16 v[32:35], a[56:59], v[84:87], v[32:35]\nds_read_b128 v[80:83], v2 offset:12288\nv_mfma_f32_16x16x32_bf16 v[36:39], a[64:67], v[84:87], v[36:39]\n" \
"v_mfma_f32_16x16x32_bf16 v[40:43], v[64:67], v[84:87], v[40:43]\nv_mfma_f32_16x16x32_bf16 v[44:47], a[72:75], v[84:87], v[44:47]\ns_waitcnt lgkmcnt(3)\nv_mfma_f32_16x16x32_bf16 v[16:19], a[60:63], v[88:91], v[16:19]\nds_read_b128 v[84:87], v2 offset:12544\nv_mfma_f32_16x16x32_bf16 v[20:23], a[68:71], v[88:91], v[20:23]\nds_read_b128 v[76:79], v1 offset:5120\nv_mfma_f32_16x16x32_bf16 v[24:27], v[68:71], v[88:91], v[24:27]\n" \
"v_mfma_f32_16x16x32_bf16 v[28:31], a[76:79], v[88:91], v[28:31]\ns_waitcnt lgkmcnt(4)\nv_mfma_f32_16x16x32_bf16 v[32:35], a[60:63], v[92:95], v[32:35]\nds_read_b128 v[88:91], v2 offset:14336\nv_mfma_f32_16x16x32_bf16 v[36:39], a[68:71], v[92:95], v[36:39]\nv_mfma_f32_16x16x32_bf16 v[40:43], v[68:71], v[92:95], v[40:43]\nv_mfma_f32_16x16x32_bf16 v[44:47], a[76:79], v[92:95], v[44:47]\ns_waitcnt vmcnt(0)\n" \
"s_branch .Lqk_pj%=\n.Lqk_slow%=:\ns_cmp_ge_i32 s40, 63\ns_cbranch_scc1 .Lqk_end%=\n.Lqk_fetch0%=:\nbuffer_load_dword v16, v12, s[36:39], s56 offen sc1\ns_waitcnt vmcnt(0)\nv_cmp_ge_i32 vcc, s40, v16\n" \
"s_cbranch_vccz .Lqk_fok%=\ns_add_u32 s43, s43, 1\ns_cmp_lt_u32 s43, 262144\ns_cbranch_scc1 .Lqk_fetch0%=\ns_branch .Lqk_abort%=\n.Lqk_fok%=:\ns_add_u32 s44, s40, 1\ns_lshl_b32 s44, s44, 17\n" \
"s_add_u32 s44, s44, %[xn]\ns_lshr_b32 s45, s44, 8\ns_and_b32 s45, s45, 0x2000\ns_xor_b32 s41, s44, s45\ns_add_u32 m0, %[l0], 0\ns_add_u32 s60, s41, 0\nbuffer_load_dwordx4 v0, s[36:39], s60 offen offset:0 sc1 lds\nbuffer_load_dwordx4 v0, s[36:39], s60 offen offset:1024 sc1 lds\n" \
"buffer_load_dwordx4 v0, s[36:39], s60 offen offset:2048 sc1 lds\nbuffer_load_dwordx4 v0, s[36:39], s60 offen offset:3072 sc1 lds\ns_add_u32 m0, %[l0], 4096\ns_add_u32 s60, s41, 4096\nbuffer_load_dwordx4 v0, s[36:39], s60 offen offset:0 sc1 lds\nbuffer_load_dwordx4 v0, s[36:39], s60 offen offset:1024 sc1 lds\nbuffer_load_dwordx4 v0, s[36:39], s60 offen offset:2048 sc1 lds\nbuffer_load_dwordx4 v0, s[36:39], s60 offen offset:3072 sc1 lds\n" \
"s_add_u32 m0, %[l0], 8192\ns_add_u32 s60, s41, 16384\nbuffer_load_dwordx4 v0, s[36:39], s60 offen offset:0 sc1 lds\nbuffer_load_dwordx4 v0, s[36:39], s60 offen offset:1024 sc1 lds\nbuffer_load_dwordx4 v0, s[36:39], s60 offen offset:2048 sc1 lds\nbuffer_load_dwordx4 v0, s[36:39], s60 offen offset:3072 sc1 lds\ns_add_u32 m0, %[l0], 12288\ns_add_u32 s60, s41, 20480\n" \
"buffer_load_dwordx4 v0, s[36:39], s60 offen offset:0 sc1 lds\nbuffer_load_dwordx4 v0, s[36:39], s60 offen offset:1024 sc1 lds\nbuffer_load_dwordx4 v0, s[36:39], s60 offen offset:2048 sc1 lds\nbuffer_load_dwordx4 v0, s[36:39], s60 offen offset:3072 sc1 lds\ns_add_u32 m0, %[l0], 16384\ns_add_u32 s60, s41, 32768\nbuffer_load_dwordx4 v0, s[36:39], s60 offen offset:0 sc1 lds\nbuffer_load_dwordx4 v0, s[36:39], s60 offen offset:1024 sc1 lds\n" \
"buffer_load_dwordx4 v0, s[36:39], s60 offen offset:2048 sc1 lds\nbuffer_load_dwordx4 v0, s[36:39], s60 offen offset:3072 sc1 lds\ns_add_u32 m0, %[l0], 20480\ns_add_u32 s60, s41, 36864\nbuffer_load_dwordx4 v0, s[36:39], s60 offen offset:0 sc1 lds\nbuffer_load_dwordx4 v0, s[36:39], s60 offen offset:1024 sc1 lds\nbuffer_load_dwordx4 v0, s[36:39], s60 offen offset:2048 sc1 lds\nbuffer_load_dwordx4 v0, s[36:39], s60 offen offset:3072 sc1 lds\n" \
"s_add_u32 m0, %[l0], 24576\ns_add_u32 s60, s41, 49152\nbuffer_load_dwordx4 v0, s[36:39], s60 offen offset:0 sc1 lds\nbuffer_load_dwordx4 v0, s[36:39], s60 offen offset:1024 sc1 lds\nbuffer_load_dwordx4 v0, s[36:39], s60 offen offset:2048 sc1 lds\nbuffer_load_dwordx4 v0, s[36:39], s60 offen offset:3072 sc1 lds\ns_add_u32 m0, %[l0], 28672\ns_add_u32 s60, s41, 53248\n" \
"buffer_load_dwordx4 v0, s[36:39], s60 offen offset:0 sc1 lds\nbuffer_load_dwordx4 v0, s[36:39], s60 offen offset:1024 sc1 lds\nbuffer_load_dwordx4 v0, s[36:39], s60 offen offset:2048 sc1 lds\nbuffer_load_dwordx4 v0, s[36:39], s60 offen offset:3072 sc1 lds\ns_waitcnt vmcnt(0)\ns_branch .Lqk_pwait%=\n.Lqk_re3%=:\nv_max_u32 v40, v80, v82\n" \
"v_cmp_eq_u32 vcc, -1, v40\ns_cbranch_vccz .Lqk_sk2764%=\ns_mov_b64 exec, vcc\nbuffer_load_dwordx4 v[80:83], v3, s[36:39], s42 offen offset:0 sc1\ns_mov_b64 exec, -1\n.Lqk_sk2764%=:\nv_max_u32 v40, v84, v86\nv_cmp_eq_u32 vcc, -1, v40\n" \
"s_cbranch_vccz .Lqk_sk2771%=\ns_mov_b64 exec, vcc\nbuffer_load_dwordx4 v[84:87], v3, s[36:39], s42 offen offset:1024 sc1\ns_mov_b64 exec, -1\n.Lqk_sk2771%=:\nv_max_u32 v40, v88, v90\nv_cmp_eq_u32 vcc, -1, v40\ns_cbranch_vccz .Lqk_sk2778%=\n" \
"s_mov_b64 exec, vcc\nbuffer_load_dwordx4 v[88:91], v3, s[36:39], s42 offen offset:2048 sc1\ns_mov_b64 exec, -1\n.Lqk_sk2778%=:\nv_max_u32 v40, v92, v94\nv_cmp_eq_u32 vcc, -1, v40\ns_cbranch_vccz .Lqk_sk2785%=\ns_mov_b64 exec, vcc\n" \
"buffer_load_dwordx4 v[92:95], v3, s[36:39], s42 offen offset:3072 sc1\ns_mov_b64 exec, -1\n.Lqk_sk2785%=:\nbuffer_load_dword v16, v12, s[36:39], s56 offen sc1\nv_mov_b64 v[48:49], 0\nv_mov_b64 v[50:51], 0\nv_mov_b64 v[52:53], 0\nv_mov_b64 v[54:55], 0\n" \
"v_mov_b64 v[56:57], 0\nv_mov_b64 v[58:59], 0\nv_mov_b64 v[60:61], 0\nv_mov_b64 v[62:63], 0\ns_add_u32 s43, s43, 1\ns_cmp_lt_u32 s43, 262144\ns_cbranch_scc1 .Lqk_poll3%=\ns_branch .Lqk_abort%=\n" \
".Lqk_abort%=:\ns_mov_b64 exec, -1\nv_mov_b32 v16, 0\ns_movk_i32 s44, 6400\nbuffer_store_dword v16, off, s[36:39], s44 sc1\n.Lqk_end%=:\ns_waitcnt vmcnt(0) lgkmcnt(0)\n" \
""
#define VU_ASM \
"v_mbcnt_lo_u32_b32 v255, -1, 0\nv_mbcnt_hi_u32_b32 v255, -1, v255\nv_lshlrev_b32 v255, 2, v255\nv_add_u32 v255, %[tb], v255\nds_read_b32 v0, v255 offset:0\nds_read_b32 v1, v255 offset:256\nds_read_b32 v2, v255 offset:512\nds_read_b32 v3, v255 offset:768\n" \
"ds_read_b32 v4, v255 offset:1024\nds_read_b32 v5, v255 offset:1280\nds_read_b32 v6, v255 offset:1536\nds_read_b32 v7, v255 offset:1792\nds_read_b32 v8, v255 offset:2048\nds_read_b32 v9, v255 offset:2304\nds_read_b32 v10, v255 offset:2560\nds_read_b32 v11, v255 offset:2816\n" \
"ds_read_b32 v90, v255 offset:3072\nds_read_b32 v91, v255 offset:3328\nds_read_b32 v92, v255 offset:3584\nds_read_b32 v93, v255 offset:3840\nds_read_b32 v94, v255 offset:4096\nds_read_b32 v95, v255 offset:4352\ns_waitcnt lgkmcnt(0)\ns_mov_b64 s[36:37], %[ws]\n" \
"s_mov_b32 s38, %[sz]\ns_mov_b32 s39, 0x20000\ns_mov_b32 s45, 2096896\nbuffer_load_dword v15, off, s[36:39], s45 sc1\ns_add_u32 m0, %[lw], 16384\ns_add_u32 s44, %[wo], 0\nbuffer_load_dwordx4 v90, %[rw], s44 offen nt lds\ns_add_u32 m0, %[lw], 17408\n" \
"s_add_u32 s44, %[wo], 65536\nbuffer_load_dwordx4 v90, %[rw], s44 offen nt lds\ns_add_u32 m0, %[lw], 18432\ns_add_u32 s44, %[wo], 131072\nbuffer_load_dwordx4 v90, %[rw], s44 offen nt lds\ns_add_u32 m0, %[lw], 19456\ns_add_u32 s44, %[wo], 196608\nbuffer_load_dwordx4 v90, %[rw], s44 offen nt lds\n" \
"s_add_u32 m0, %[lw], 20480\ns_add_u32 s44, %[wo], 524288\nbuffer_load_dwordx4 v90, %[rw], s44 offen nt lds\ns_add_u32 m0, %[lw], 21504\ns_add_u32 s44, %[wo], 589824\nbuffer_load_dwordx4 v90, %[rw], s44 offen nt lds\ns_add_u32 m0, %[lw], 22528\ns_add_u32 s44, %[wo], 655360\n" \
"buffer_load_dwordx4 v90, %[rw], s44 offen nt lds\ns_add_u32 m0, %[lw], 23552\ns_add_u32 s44, %[wo], 720896\nbuffer_load_dwordx4 v90, %[rw], s44 offen nt lds\ns_add_u32 m0, %[tb], 0\ns_add_u32 s44, %[wo], 1048576\nbuffer_load_dwordx4 v90, %[rw], s44 offen nt lds\ns_add_u32 m0, %[tb], 1024\n" \
"s_add_u32 s44, %[wo], 1114112\nbuffer_load_dwordx4 v90, %[rw], s44 offen nt lds\ns_add_u32 m0, %[tb], 2048\ns_add_u32 s44, %[wo], 1179648\nbuffer_load_dwordx4 v90, %[rw], s44 offen nt lds\ns_add_u32 m0, %[tb], 3072\ns_add_u32 s44, %[wo], 1245184\nbuffer_load_dwordx4 v90, %[rw], s44 offen nt lds\n" \
"s_add_u32 m0, %[tb], 4096\ns_add_u32 s44, %[wo], 1572864\nbuffer_load_dwordx4 v90, %[rw], s44 offen nt lds\ns_add_u32 m0, %[tb], 5120\ns_add_u32 s44, %[wo], 1638400\nbuffer_load_dwordx4 v90, %[rw], s44 offen nt lds\ns_add_u32 m0, %[tb], 6144\ns_add_u32 s44, %[wo], 1703936\n" \
"buffer_load_dwordx4 v90, %[rw], s44 offen nt lds\ns_add_u32 m0, %[tb], 7168\ns_add_u32 s44, %[wo], 1769472\nbuffer_load_dwordx4 v90, %[rw], s44 offen nt lds\ns_add_u32 m0, %[tb], 8192\ns_add_u32 s44, %[wo], 2097152\nbuffer_load_dwordx4 v90, %[rw], s44 offen nt lds\ns_add_u32 m0, %[tb], 9216\n" \
"s_add_u32 s44, %[wo], 2162688\nbuffer_load_dwordx4 v90, %[rw], s44 offen nt lds\ns_add_u32 m0, %[tb], 10240\ns_add_u32 s44, %[wo], 2228224\nbuffer_load_dwordx4 v90, %[rw], s44 offen nt lds\ns_add_u32 m0, %[tb], 11264\ns_add_u32 s44, %[wo], 2293760\nbuffer_load_dwordx4 v90, %[rw], s44 offen nt lds\n" \
"s_add_u32 m0, %[lw], 0\ns_add_u32 s44, %[wo], 2621440\nbuffer_load_dwordx4 v90, %[rw], s44 offen nt lds\ns_add_u32 m0, %[lw], 1024\ns_add_u32 s44, %[wo], 2686976\nbuffer_load_dwordx4 v90, %[rw], s44 offen nt lds\ns_add_u32 m0, %[lw], 2048\ns_add_u32 s44, %[wo], 2752512\n" \
"buffer_load_dwordx4 v90, %[rw], s44 offen nt lds\ns_add_u32 m0, %[lw], 3072\ns_add_u32 s44, %[wo], 2818048\nbuffer_load_dwordx4 v90, %[rw], s44 offen nt lds\ns_add_u32 m0, %[lw], 4096\ns_add_u32 s44, %[wo], 3145728\nbuffer_load_dwordx4 v90, %[rw], s44 offen nt lds\ns_add_u32 m0, %[lw], 5120\n" \
"s_add_u32 s44, %[wo], 3211264\nbuffer_load_dwordx4 v90, %[rw], s44 offen nt lds\ns_add_u32 m0, %[lw], 6144\ns_add_u32 s44, %[wo], 3276800\nbuffer_load_dwordx4 v90, %[rw], s44 offen nt lds\ns_add_u32 m0, %[lw], 7168\ns_add_u32 s44, %[wo], 3342336\nbuffer_load_dwordx4 v90, %[rw], s44 offen nt lds\n" \
"s_add_u32 m0, %[lw], 8192\ns_add_u32 s44, %[wo], 3670016\nbuffer_load_dwordx4 v90, %[rw], s44 offen nt lds\ns_add_u32 m0, %[lw], 9216\ns_add_u32 s44, %[wo], 3735552\nbuffer_load_dwordx4 v90, %[rw], s44 offen nt lds\ns_add_u32 m0, %[lw], 10240\ns_add_u32 s44, %[wo], 3801088\n" \
"buffer_load_dwordx4 v90, %[rw], s44 offen nt lds\ns_add_u32 m0, %[lw], 11264\ns_add_u32 s44, %[wo], 3866624\nbuffer_load_dwordx4 v90, %[rw], s44 offen nt lds\ns_waitcnt vmcnt(28)\ns_barrier\ns_add_u32 m0, %[lw], 12288\ns_add_u32 s44, %[wo], 4194304\n" \
"buffer_load_dwordx4 v90, %[rw], s44 offen nt lds\ns_add_u32 m0, %[lw], 13312\ns_add_u32 s44, %[wo], 4259840\nbuffer_load_dwordx4 v90, %[rw], s44 offen nt lds\ns_add_u32 m0, %[lw], 14336\ns_add_u32 s44, %[wo], 4325376\nbuffer_load_dwordx4 v90, %[rw], s44 offen nt lds\ns_add_u32 m0, %[lw], 15360\n" \
"s_add_u32 s44, %[wo], 4390912\nbuffer_load_dwordx4 v90, %[rw], s44 offen nt lds\nds_read_b64_tr_b16 a[0:1], v93 offset:16384\nds_read_b64_tr_b16 a[2:3], v93 offset:16896\nds_read_b64_tr_b16 a[16:17], v93 offset:16392\nds_read_b64_tr_b16 a[18:19], v93 offset:16904\nds_read_b64_tr_b16 a[4:5], v93 offset:40960\nds_read_b64_tr_b16 a[6:7], v93 offset:41472\n" \
"ds_read_b64_tr_b16 a[20:21], v93 offset:40968\nds_read_b64_tr_b16 a[22:23], v93 offset:41480\ns_waitcnt lgkmcnt(0)\ns_waitcnt vmcnt(28)\ns_barrier\ns_add_u32 m0, %[lw], 16384\ns_add_u32 s44, %[wo], 4718592\nbuffer_load_dwordx4 v90, %[rw], s44 offen nt lds\n" \
"s_add_u32 m0, %[lw], 17408\ns_add_u32 s44, %[wo], 4784128\nbuffer_load_dwordx4 v90, %[rw], s44 offen nt lds\ns_add_u32 m0, %[lw], 18432\ns_add_u32 s44, %[wo], 4849664\nbuffer_load_dwordx4 v90, %[rw], s44 offen nt lds\ns_add_u32 m0, %[lw], 19456\ns_add_u32 s44, %[wo], 4915200\n" \
"buffer_load_dwordx4 v90, %[rw], s44 offen nt lds\nds_read_b64_tr_b16 a[24:25], v93 offset:20480\nds_read_b64_tr_b16 a[26:27], v93 offset:20992\nds_read_b64_tr_b16 a[40:41], v93 offset:20488\nds_read_b64_tr_b16 a[42:43], v93 offset:21000\nds_read_b64_tr_b16 a[28:29], v93 offset:45056\nds_read_b64_tr_b16 a[30:31], v93 offset:45568\nds_read_b64_tr_b16 a[44:45], v93 offset:45064\n" \
"ds_read_b64_tr_b16 a[46:47], v93 offset:45576\ns_waitcnt lgkmcnt(0)\ns_waitcnt vmcnt(28)\ns_barrier\ns_add_u32 m0, %[lw], 20480\ns_add_u32 s44, %[wo], 5242880\nbuffer_load_dwordx4 v90, %[rw], s44 offen nt lds\ns_add_u32 m0, %[lw], 21504\n" \
"s_add_u32 s44, %[wo], 5308416\nbuffer_load_dwordx4 v90, %[rw], s44 offen nt lds\ns_add_u32 m0, %[lw], 22528\ns_add_u32 s44, %[wo], 5373952\nbuffer_load_dwordx4 v90, %[rw], s44 offen nt lds\ns_add_u32 m0, %[lw], 23552\ns_add_u32 s44, %[wo], 5439488\nbuffer_load_dwordx4 v90, %[rw], s44 offen nt lds\n" \
"ds_read_b64_tr_b16 a[56:57], v92 offset:0\nds_read_b64_tr_b16 a[58:59], v92 offset:512\nds_read_b64_tr_b16 a[72:73], v92 offset:8\nds_read_b64_tr_b16 a[74:75], v92 offset:520\nds_read_b64_tr_b16 a[60:61], v92 offset:12288\nds_read_b64_tr_b16 a[62:63], v92 offset:12800\nds_read_b64_tr_b16 a[76:77], v92 offset:12296\nds_read_b64_tr_b16 a[78:79], v92 offset:12808\n" \
"s_waitcnt lgkmcnt(0)\ns_waitcnt vmcnt(28)\ns_barrier\ns_add_u32 m0, %[tb], 0\ns_add_u32 s44, %[wo], 5767168\nbuffer_load_dwordx4 v90, %[rw], s44 offen nt lds\ns_add_u32 m0, %[tb], 1024\ns_add_u32 s44, %[wo], 5832704\n" \
"buffer_load_dwordx4 v90, %[rw], s44 offen nt lds\ns_add_u32 m0, %[tb], 2048\ns_add_u32 s44, %[wo], 5898240\nbuffer_load_dwordx4 v90, %[rw], s44 offen nt lds\ns_add_u32 m0, %[tb], 3072\ns_add_u32 s44, %[wo], 5963776\nbuffer_load_dwordx4 v90, %[rw], s44 offen nt lds\nds_read_b64_tr_b16 v[84:85], v92 offset:4096\n" \
"ds_read_b64_tr_b16 v[86:87], v92 offset:4608\nds_read_b64_tr_b16 a[88:89], v92 offset:4104\nds_read_b64_tr_b16 a[90:91], v92 offset:4616\nds_read_b64_tr_b16 v[80:81], v92 offset:16384\nds_read_b64_tr_b16 v[82:83], v92 offset:16896\nds_read_b64_tr_b16 a[92:93], v92 offset:16392\nds_read_b64_tr_b16 a[94:95], v92 offset:16904\ns_waitcnt lgkmcnt(0)\n" \
"s_waitcnt vmcnt(28)\ns_barrier\ns_add_u32 m0, %[tb], 4096\ns_add_u32 s44, %[wo], 6291456\nbuffer_load_dwordx4 v90, %[rw], s44 offen nt lds\ns_add_u32 m0, %[tb], 5120\ns_add_u32 s44, %[wo], 6356992\nbuffer_load_dwordx4 v90, %[rw], s44 offen nt lds\n" \
"s_add_u32 m0, %[tb], 6144\ns_add_u32 s44, %[wo], 6422528\nbuffer_load_dwordx4 v90, %[rw], s44 offen nt lds\ns_add_u32 m0, %[tb], 7168\ns_add_u32 s44, %[wo], 6488064\nbuffer_load_dwordx4 v90, %[rw], s44 offen nt lds\nds_read_b64_tr_b16 a[104:105], v92 offset:8192\nds_read_b64_tr_b16 a[106:107], v92 offset:8704\n" \
"ds_read_b64_tr_b16 a[120:121], v92 offset:8200\nds_read_b64_tr_b16 a[122:123], v92 offset:8712\nds_read_b64_tr_b16 a[108:109], v92 offset:20480\nds_read_b64_tr_b16 a[110:111], v92 offset:20992\nds_read_b64_tr_b16 a[124:125], v92 offset:20488\nds_read_b64_tr_b16 a[126:127], v92 offset:21000\ns_waitcnt lgkmcnt(0)\ns_waitcnt vmcnt(28)\n" \
"s_barrier\ns_add_u32 m0, %[tb], 8192\ns_add_u32 s44, %[wo], 6815744\nbuffer_load_dwordx4 v90, %[rw], s44 offen nt lds\ns_add_u32 m0, %[tb], 9216\ns_add_u32 s44, %[wo], 6881280\nbuffer_load_dwordx4 v90, %[rw], s44 offen nt lds\ns_add_u32 m0, %[tb], 10240\n" \
"s_add_u32 s44, %[wo], 6946816\nbuffer_load_dwordx4 v90, %[rw], s44 offen nt lds\ns_add_u32 m0, %[tb], 11264\ns_add_u32 s44, %[wo], 7012352\nbuffer_load_dwordx4 v90, %[rw], s44 offen nt lds\nds_read_b64_tr_b16 a[128:129], v93 offset:0\nds_read_b64_tr_b16 a[130:131], v93 offset:512\nds_read_b64_tr_b16 a[144:145], v93 offset:8\n" \
"ds_read_b64_tr_b16 a[146:147], v93 offset:520\nds_read_b64_tr_b16 a[132:133], v93 offset:24576\nds_read_b64_tr_b16 a[134:135], v93 offset:25088\nds_read_b64_tr_b16 a[148:149], v93 offset:24584\nds_read_b64_tr_b16 a[150:151], v93 offset:25096\ns_waitcnt lgkmcnt(0)\ns_waitcnt vmcnt(28)\ns_barrier\n" \
"s_add_u32 m0, %[lw], 0\ns_add_u32 s44, %[wo], 7340032\nbuffer_load_dwordx4 v90, %[rw], s44 offen nt lds\ns_add_u32 m0, %[lw], 1024\ns_add_u32 s44, %[wo], 7405568\nbuffer_load_dwordx4 v90, %[rw], s44 offen nt lds\ns_add_u32 m0, %[lw], 2048\ns_add_u32 s44, %[wo], 7471104\n" \
"buffer_load_dwordx4 v90, %[rw], s44 offen nt lds\ns_add_u32 m0, %[lw], 3072\ns_add_u32 s44, %[wo], 7536640\nbuffer_load_dwordx4 v90, %[rw], s44 offen nt lds\nds_read_b64_tr_b16 a[160:161], v93 offset:4096\nds_read_b64_tr_b16 a[162:163], v93 offset:4608\nds_read_b64_tr_b16 v[76:77], v93 offset:4104\nds_read_b64_tr_b16 v[78:79], v93 offset:4616\n" \
"ds_read_b64_tr_b16 a[164:165], v93 offset:28672\nds_read_b64_tr_b16 a[166:167], v93 offset:29184\nds_read_b64_tr_b16 v[72:73], v93 offset:28680\nds_read_b64_tr_b16 v[74:75], v93 offset:29192\ns_waitcnt lgkmcnt(0)\ns_waitcnt vmcnt(28)\ns_barrier\ns_add_u32 m0, %[lw], 4096\n" \
"s_add_u32 s44, %[wo], 7864320\nbuffer_load_dwordx4 v90, %[rw], s44 offen nt lds\ns_add_u32 m0, %[lw], 5120\ns_add_u32 s44, %[wo], 7929856\nbuffer_load_dwordx4 v90, %[rw], s44 offen nt lds\ns_add_u32 m0, %[lw], 6144\ns_add_u32 s44, %[wo], 7995392\nbuffer_load_dwordx4 v90, %[rw], s44 offen nt lds\n" \
"s_add_u32 m0, %[lw], 7168\ns_add_u32 s44, %[wo], 8060928\nbuffer_load_dwordx4 v90, %[rw], s44 offen nt lds\nds_read_b64_tr_b16 a[184:185], v93 offset:8192\nds_read_b64_tr_b16 a[186:187], v93 offset:8704\nds_read_b64_tr_b16 a[192:193], v93 offset:8200\nds_read_b64_tr_b16 a[194:195], v93 offset:8712\nds_read_b64_tr_b16 a[188:189], v93 offset:32768\n" \
"ds_read_b64_tr_b16 a[190:191], v93 offset:33280\nds_read_b64_tr_b16 a[196:197], v93 offset:32776\nds_read_b64_tr_b16 a[198:199], v93 offset:33288\ns_waitcnt lgkmcnt(0)\ns_waitcnt vmcnt(28)\ns_barrier\ns_add_u32 m0, %[lw], 8192\ns_add_u32 s44, %[w2], 0\n" \
"buffer_load_dwordx4 v91, %[ro], s44 offen nt lds\ns_add_u32 m0, %[lw], 9216\ns_add_u32 s44, %[w2], 32768\nbuffer_load_dwordx4 v91, %[ro], s44 offen nt lds\ns_add_u32 m0, %[lw], 10240\ns_add_u32 s44, %[w2], 65536\nbuffer_load_dwordx4 v91, %[ro], s44 offen nt lds\ns_add_u32 m0, %[lw], 11264\n" \
"s_add_u32 s44, %[w2], 98304\nbuffer_load_dwordx4 v91, %[ro], s44 offen nt lds\nds_read_b64_tr_b16 a[208:209], v93 offset:12288\nds_read_b64_tr_b16 a[210:211], v93 offset:12800\nds_read_b64_tr_b16 a[224:225], v93 offset:12296\nds_read_b64_tr_b16 a[226:227], v93 offset:12808\nds_read_b64_tr_b16 a[212:213], v93 offset:36864\nds_read_b64_tr_b16 a[214:215], v93 offset:37376\n" \
"ds_read_b64_tr_b16 a[228:229], v93 offset:36872\nds_read_b64_tr_b16 a[230:231], v93 offset:37384\ns_waitcnt lgkmcnt(0)\ns_waitcnt vmcnt(28)\ns_barrier\ns_add_u32 m0, %[lw], 12288\ns_add_u32 s44, %[w2], 128\nbuffer_load_dwordx4 v91, %[ro], s44 offen nt lds\n" \
"s_add_u32 m0, %[lw], 13312\ns_add_u32 s44, %[w2], 32896\nbuffer_load_dwordx4 v91, %[ro], s44 offen nt lds\ns_add_u32 m0, %[lw], 14336\ns_add_u32 s44, %[w2], 65664\nbuffer_load_dwordx4 v91, %[ro], s44 offen nt lds\ns_add_u32 m0, %[lw], 15360\ns_add_u32 s44, %[w2], 98432\n" \
"buffer_load_dwordx4 v91, %[ro], s44 offen nt lds\nds_read_b64_tr_b16 v[68:69], v93 offset:16384\nds_read_b64_tr_b16 v[70:71], v93 offset:16896\nds_read_b64_tr_b16 a[240:241], v93 offset:16392\nds_read_b64_tr_b16 a[242:243], v93 offset:16904\nds_read_b64_tr_b16 v[64:65], v93 offset:40960\nds_read_b64_tr_b16 v[66:67], v93 offset:41472\nds_read_b64_tr_b16 a[244:245], v93 offset:40968\n" \
"ds_read_b64_tr_b16 a[246:247], v93 offset:41480\ns_waitcnt lgkmcnt(0)\ns_waitcnt vmcnt(28)\ns_barrier\ns_add_u32 m0, %[lw], 16384\ns_add_u32 s44, %[w2], 256\nbuffer_load_dwordx4 v91, %[ro], s44 offen nt lds\ns_add_u32 m0, %[lw], 17408\n" \
"s_add_u32 s44, %[w2], 33024\nbuffer_load_dwordx4 v91, %[ro], s44 offen nt lds\ns_add_u32 m0, %[lw], 18432\ns_add_u32 s44, %[w2], 65792\nbuffer_load_dwordx4 v91, %[ro], s44 offen nt lds\ns_add_u32 m0, %[lw], 19456\ns_add_u32 s44, %[w2], 98560\nbuffer_load_dwordx4 v91, %[ro], s44 offen nt lds\n" \
"ds_read_b64_tr_b16 v[96:97], v93 offset:20480\nds_read_b64_tr_b16 v[98:99], v93 offset:20992\nds_read_b64_tr_b16 v[112:113], v93 offset:20488\nds_read_b64_tr_b16 v[114:115], v93 offset:21000\nds_read_b64_tr_b16 v[100:101], v93 offset:45056\nds_read_b64_tr_b16 v[102:103], v93 offset:45568\nds_read_b64_tr_b16 v[116:117], v93 offset:45064\nds_read_b64_tr_b16 v[118:119], v93 offset:45576\n" \
"s_waitcnt lgkmcnt(0)\ns_waitcnt vmcnt(28)\ns_barrier\ns_add_u32 m0, %[lw], 20480\ns_add_u32 s44, %[w2], 384\nbuffer_load_dwordx4 v91, %[ro], s44 offen nt lds\ns_add_u32 m0, %[lw], 21504\ns_add_u32 s44, %[w2], 33152\n" \
"buffer_load_dwordx4 v91, %[ro], s44 offen nt lds\ns_add_u32 m0, %[lw], 22528\ns_add_u32 s44, %[w2], 65920\nbuffer_load_dwordx4 v91, %[ro], s44 offen nt lds\ns_add_u32 m0, %[lw], 23552\ns_add_u32 s44, %[w2], 98688\nbuffer_load_dwordx4 v91, %[ro], s44 offen nt lds\nds_read_b64_tr_b16 v[128:129], v92 offset:0\n" \
"ds_read_b64_tr_b16 v[130:131], v92 offset:512\nds_read_b64_tr_b16 v[136:137], v92 offset:8\nds_read_b64_tr_b16 v[138:139], v92 offset:520\nds_read_b64_tr_b16 v[132:133], v92 offset:12288\nds_read_b64_tr_b16 v[134:135], v92 offset:12800\nds_read_b64_tr_b16 v[140:141], v92 offset:12296\nds_read_b64_tr_b16 v[142:143], v92 offset:12808\ns_waitcnt lgkmcnt(0)\n" \
"s_waitcnt vmcnt(28)\ns_barrier\ns_add_u32 m0, %[tb], 0\ns_add_u32 s44, %[w2], 139264\nbuffer_load_dwordx4 v91, %[ro], s44 offen nt lds\ns_add_u32 m0, %[tb], 1024\ns_add_u32 s44, %[w2], 172032\nbuffer_load_dwordx4 v91, %[ro], s44 offen nt lds\n" \
"s_add_u32 m0, %[tb], 2048\ns_add_u32 s44, %[w2], 204800\nbuffer_load_dwordx4 v91, %[ro], s44 offen nt lds\ns_add_u32 m0, %[tb], 3072\ns_add_u32 s44, %[w2], 237568\nbuffer_load_dwordx4 v91, %[ro], s44 offen nt lds\nds_read_b64_tr_b16 v[152:153], v92 offset:4096\nds_read_b64_tr_b16 v[154:155], v92 offset:4608\n" \
"ds_read_b64_tr_b16 v[60:61], v92 offset:4104\nds_read_b64_tr_b16 v[62:63], v92 offset:4616\nds_read_b64_tr_b16 v[156:157], v92 offset:16384\nds_read_b64_tr_b16 v[158:159], v92 offset:16896\nds_read_b64_tr_b16 v[56:57], v92 offset:16392\nds_read_b64_tr_b16 v[58:59], v92 offset:16904\ns_waitcnt lgkmcnt(0)\ns_waitcnt vmcnt(28)\n" \
"s_barrier\ns_add_u32 m0, %[tb], 4096\ns_add_u32 s44, %[w2], 139392\nbuffer_load_dwordx4 v91, %[ro], s44 offen nt lds\ns_add_u32 m0, %[tb], 5120\ns_add_u32 s44, %[w2], 172160\nbuffer_load_dwordx4 v91, %[ro], s44 offen nt lds\ns_add_u32 m0, %[tb], 6144\n" \
"s_add_u32 s44, %[w2], 204928\nbuffer_load_dwordx4 v91, %[ro], s44 offen nt lds\ns_add_u32 m0, %[tb], 7168\ns_add_u32 s44, %[w2], 237696\nbuffer_load_dwordx4 v91, %[ro], s44 offen nt lds\nds_read_b64_tr_b16 v[168:169], v92 offset:8192\nds_read_b64_tr_b16 v[170:171], v92 offset:8704\nds_read_b64_tr_b16 v[184:185], v92 offset:8200\n" \
"ds_read_b64_tr_b16 v[186:187], v92 offset:8712\nds_read_b64_tr_b16 v[172:173], v92 offset:20480\nds_read_b64_tr_b16 v[174:175], v92 offset:20992\nds_read_b64_tr_b16 v[188:189], v92 offset:20488\nds_read_b64_tr_b16 v[190:191], v92 offset:21000\ns_waitcnt lgkmcnt(0)\ns_waitcnt vmcnt(28)\ns_barrier\n" \
"s_add_u32 m0, %[tb], 8192\ns_add_u32 s44, %[w2], 139520\nbuffer_load_dwordx4 v91, %[ro], s44 offen nt lds\ns_add_u32 m0, %[tb], 9216\ns_add_u32 s44, %[w2], 172288\nbuffer_load_dwordx4 v91, %[ro], s44 offen nt lds\ns_add_u32 m0, %[tb], 10240\ns_add_u32 s44, %[w2], 205056\n" \
"buffer_load_dwordx4 v91, %[ro], s44 offen nt lds\ns_add_u32 m0, %[tb], 11264\ns_add_u32 s44, %[w2], 237824\nbuffer_load_dwordx4 v91, %[ro], s44 offen nt lds\nds_read_b64_tr_b16 v[200:201], v93 offset:0\nds_read_b64_tr_b16 v[202:203], v93 offset:512\nds_read_b64_tr_b16 v[216:217], v93 offset:8\nds_read_b64_tr_b16 v[218:219], v93 offset:520\n" \
"ds_read_b64_tr_b16 v[204:205], v93 offset:24576\nds_read_b64_tr_b16 v[206:207], v93 offset:25088\nds_read_b64_tr_b16 v[220:221], v93 offset:24584\nds_read_b64_tr_b16 v[222:223], v93 offset:25096\ns_waitcnt lgkmcnt(0)\ns_waitcnt vmcnt(28)\ns_barrier\ns_add_u32 m0, %[lw], 0\n" \
"s_add_u32 s44, %[w2], 139648\nbuffer_load_dwordx4 v91, %[ro], s44 offen nt lds\ns_add_u32 m0, %[lw], 1024\ns_add_u32 s44, %[w2], 172416\nbuffer_load_dwordx4 v91, %[ro], s44 offen nt lds\ns_add_u32 m0, %[lw], 2048\ns_add_u32 s44, %[w2], 205184\nbuffer_load_dwordx4 v91, %[ro], s44 offen nt lds\n" \
"s_add_u32 m0, %[lw], 3072\ns_add_u32 s44, %[w2], 237952\nbuffer_load_dwordx4 v91, %[ro], s44 offen nt lds\nds_read_b64_tr_b16 v[232:233], v93 offset:4096\nds_read_b64_tr_b16 v[234:235], v93 offset:4608\nds_read_b64_tr_b16 v[240:241], v93 offset:4104\nds_read_b64_tr_b16 v[242:243], v93 offset:4616\nds_read_b64_tr_b16 v[236:237], v93 offset:28672\n" \
"ds_read_b64_tr_b16 v[238:239], v93 offset:29184\nds_read_b64_tr_b16 v[244:245], v93 offset:28680\nds_read_b64_tr_b16 v[246:247], v93 offset:29192\ns_waitcnt lgkmcnt(0)\ns_waitcnt vmcnt(28)\ns_barrier\ns_add_u32 m0, %[lw], 4096\ns_add_u32 s44, %[w2], 131072\n" \
"buffer_load_dwordx4 v91, %[ro], s44 offen nt lds\ns_add_u32 m0, %[lw], 5120\ns_add_u32 s44, %[w2], 163840\nbuffer_load_dwordx4 v91, %[ro], s44 offen nt lds\ns_add_u32 m0, %[lw], 6144\ns_add_u32 s44, %[w2], 196608\nbuffer_load_dwordx4 v91, %[ro], s44 offen nt lds\ns_add_u32 m0, %[lw], 7168\n" \
"s_add_u32 s44, %[w2], 229376\nbuffer_load_dwordx4 v91, %[ro], s44 offen nt lds\nds_read_b64_tr_b16 a[8:9], v95 offset:8192\nds_read_b64_tr_b16 a[10:11], v95 offset:8448\nds_read_b64_tr_b16 a[12:13], v95 offset:10240\nds_read_b64_tr_b16 a[14:15], v95 offset:10496\nds_read_b64_tr_b16 a[216:217], v95 offset:8256\nds_read_b64_tr_b16 a[218:219], v95 offset:8512\n" \
"ds_read_b64_tr_b16 a[220:221], v95 offset:10304\nds_read_b64_tr_b16 a[222:223], v95 offset:10560\ns_waitcnt lgkmcnt(0)\ns_waitcnt vmcnt(28)\ns_barrier\ns_add_u32 m0, %[lw], 8192\ns_add_u32 s44, %[w2], 131200\nbuffer_load_dwordx4 v91, %[ro], s44 offen nt lds\n" \
"s_add_u32 m0, %[lw], 9216\ns_add_u32 s44, %[w2], 163968\nbuffer_load_dwordx4 v91, %[ro], s44 offen nt lds\ns_add_u32 m0, %[lw], 10240\ns_add_u32 s44, %[w2], 196736\nbuffer_load_dwordx4 v91, %[ro], s44 offen nt lds\ns_add_u32 m0, %[lw], 11264\ns_add_u32 s44, %[w2], 229504\n" \
"buffer_load_dwordx4 v91, %[ro], s44 offen nt lds\nds_read_b64_tr_b16 a[64:65], v95 offset:12288\nds_read_b64_tr_b16 a[66:67], v95 offset:12544\nds_read_b64_tr_b16 a[68:69], v95 offset:14336\nds_read_b64_tr_b16 a[70:71], v95 offset:14592\nds_read_b64_tr_b16 v[104:105], v95 offset:12352\nds_read_b64_tr_b16 v[106:107], v95 offset:12608\nds_read_b64_tr_b16 v[108:109], v95 offset:14400\n" \
"ds_read_b64_tr_b16 v[110:111], v95 offset:14656\ns_waitcnt lgkmcnt(0)\ns_waitcnt vmcnt(28)\ns_barrier\ns_add_u32 m0, %[lw], 12288\ns_add_u32 s44, %[w2], 131328\nbuffer_load_dwordx4 v91, %[ro], s44 offen nt lds\ns_add_u32 m0, %[lw], 13312\n" \
"s_add_u32 s44, %[w2], 164096\nbuffer_load_dwordx4 v91, %[ro], s44 offen nt lds\ns_add_u32 m0, %[lw], 14336\ns_add_u32 s44, %[w2], 196864\nbuffer_load_dwordx4 v91, %[ro], s44 offen nt lds\ns_add_u32 m0, %[lw], 15360\ns_add_u32 s44, %[w2], 229632\nbuffer_load_dwordx4 v91, %[ro], s44 offen nt lds\n" \
"ds_read_b64_tr_b16 a[112:113], v95 offset:16384\nds_read_b64_tr_b16 a[114:115], v95 offset:16640\nds_read_b64_tr_b16 a[116:117], v95 offset:18432\nds_read_b64_tr_b16 a[118:119], v95 offset:18688\nds_read_b64_tr_b16 v[160:161], v95 offset:16448\nds_read_b64_tr_b16 v[162:163], v95 offset:16704\nds_read_b64_tr_b16 v[164:165], v95 offset:18496\nds_read_b64_tr_b16 v[166:167], v95 offset:18752\n" \
"s_waitcnt lgkmcnt(0)\ns_waitcnt vmcnt(28)\ns_barrier\ns_add_u32 m0, %[lw], 16384\ns_add_u32 s44, %[w2], 131456\nbuffer_load_dwordx4 v91, %[ro], s44 offen nt lds\ns_add_u32 m0, %[lw], 17408\ns_add_u32 s44, %[w2], 164224\n" \
"buffer_load_dwordx4 v91, %[ro], s44 offen nt lds\ns_add_u32 m0, %[lw], 18432\ns_add_u32 s44, %[w2], 196992\nbuffer_load_dwordx4 v91, %[ro], s44 offen nt lds\ns_add_u32 m0, %[lw], 19456\ns_add_u32 s44, %[w2], 229760\nbuffer_load_dwordx4 v91, %[ro], s44 offen nt lds\nds_read_b64_tr_b16 a[168:169], v95 offset:20480\n" \
"ds_read_b64_tr_b16 a[170:171], v95 offset:20736\nds_read_b64_tr_b16 a[172:173], v95 offset:22528\nds_read_b64_tr_b16 a[174:175], v95 offset:22784\nds_read_b64_tr_b16 v[208:209], v95 offset:20544\nds_read_b64_tr_b16 v[210:211], v95 offset:20800\nds_read_b64_tr_b16 v[212:213], v95 offset:22592\nds_read_b64_tr_b16 v[214:215], v95 offset:22848\ns_waitcnt lgkmcnt(0)\n" \
"s_waitcnt vmcnt(28)\ns_barrier\ns_add_u32 m0, %[lw], 20480\ns_add_u32 s44, %[w2], 8192\nbuffer_load_dwordx4 v91, %[ro], s44 offen nt lds\ns_add_u32 m0, %[lw], 21504\ns_add_u32 s44, %[w2], 40960\nbuffer_load_dwordx4 v91, %[ro], s44 offen nt lds\n" \
"s_add_u32 m0, %[lw], 22528\ns_add_u32 s44, %[w2], 73728\nbuffer_load_dwordx4 v91, %[ro], s44 offen nt lds\ns_add_u32 m0, %[lw], 23552\ns_add_u32 s44, %[w2], 106496\nbuffer_load_dwordx4 v91, %[ro], s44 offen nt lds\nds_read_b64_tr_b16 a[48:49], v94 offset:0\nds_read_b64_tr_b16 a[50:51], v94 offset:256\n" \
"ds_read_b64_tr_b16 a[52:53], v94 offset:2048\nds_read_b64_tr_b16 a[54:55], v94 offset:2304\nds_read_b64_tr_b16 a[248:249], v94 offset:64\nds_read_b64_tr_b16 a[250:251], v94 offset:320\nds_read_b64_tr_b16 a[252:253], v94 offset:2112\nds_read_b64_tr_b16 a[254:255], v94 offset:2368\ns_waitcnt lgkmcnt(0)\ns_waitcnt vmcnt(28)\n" \
"s_barrier\ns_add_u32 m0, %[tb], 0\ns_add_u32 s44, %[w2], 8320\nbuffer_load_dwordx4 v91, %[ro], s44 offen nt lds\ns_add_u32 m0, %[tb], 1024\ns_add_u32 s44, %[w2], 41088\nbuffer_load_dwordx4 v91, %[ro], s44 offen nt lds\ns_add_u32 m0, %[tb], 2048\n" \
"s_add_u32 s44, %[w2], 73856\nbuffer_load_dwordx4 v91, %[ro], s44 offen nt lds\ns_add_u32 m0, %[tb], 3072\ns_add_u32 s44, %[w2], 106624\nbuffer_load_dwordx4 v91, %[ro], s44 offen nt lds\nds_read_b64_tr_b16 a[96:97], v94 offset:4096\nds_read_b64_tr_b16 a[98:99], v94 offset:4352\nds_read_b64_tr_b16 a[100:101], v94 offset:6144\n" \
"ds_read_b64_tr_b16 a[102:103], v94 offset:6400\nds_read_b64_tr_b16 v[144:145], v94 offset:4160\nds_read_b64_tr_b16 v[146:147], v94 offset:4416\nds_read_b64_tr_b16 v[148:149], v94 offset:6208\nds_read_b64_tr_b16 v[150:151], v94 offset:6464\ns_waitcnt lgkmcnt(0)\ns_waitcnt vmcnt(28)\ns_barrier\n" \
"s_add_u32 m0, %[tb], 4096\ns_add_u32 s44, %[w2], 8448\nbuffer_load_dwordx4 v91, %[ro], s44 offen nt lds\ns_add_u32 m0, %[tb], 5120\ns_add_u32 s44, %[w2], 41216\nbuffer_load_dwordx4 v91, %[ro], s44 offen nt lds\ns_add_u32 m0, %[tb], 6144\ns_add_u32 s44, %[w2], 73984\n" \
"buffer_load_dwordx4 v91, %[ro], s44 offen nt lds\ns_add_u32 m0, %[tb], 7168\ns_add_u32 s44, %[w2], 106752\nbuffer_load_dwordx4 v91, %[ro], s44 offen nt lds\nds_read_b64_tr_b16 a[152:153], v94 offset:8192\nds_read_b64_tr_b16 a[154:155], v94 offset:8448\nds_read_b64_tr_b16 a[156:157], v94 offset:10240\nds_read_b64_tr_b16 a[158:159], v94 offset:10496\n" \
"ds_read_b64_tr_b16 v[192:193], v94 offset:8256\nds_read_b64_tr_b16 v[194:195], v94 offset:8512\nds_read_b64_tr_b16 v[196:197], v94 offset:10304\nds_read_b64_tr_b16 v[198:199], v94 offset:10560\ns_waitcnt lgkmcnt(0)\ns_waitcnt vmcnt(28)\ns_barrier\ns_add_u32 m0, %[tb], 8192\n" \
"s_add_u32 s44, %[w2], 8576\nbuffer_load_dwordx4 v91, %[ro], s44 offen nt lds\ns_add_u32 m0, %[tb], 9216\ns_add_u32 s44, %[w2], 41344\nbuffer_load_dwordx4 v91, %[ro], s44 offen nt lds\ns_add_u32 m0, %[tb], 10240\ns_add_u32 s44, %[w2], 74112\nbuffer_load_dwordx4 v91, %[ro], s44 offen nt lds\n" \
"s_add_u32 m0, %[tb], 11264\ns_add_u32 s44, %[w2], 106880\nbuffer_load_dwordx4 v91, %[ro], s44 offen nt lds\nds_read_b64_tr_b16 a[200:201], v95 offset:0\nds_read_b64_tr_b16 a[202:203], v95 offset:256\nds_read_b64_tr_b16 a[204:205], v95 offset:2048\nds_read_b64_tr_b16 a[206:207], v95 offset:2304\nds_read_b64_tr_b16 v[248:249], v95 offset:64\n" \
"ds_read_b64_tr_b16 v[250:251], v95 offset:320\nds_read_b64_tr_b16 v[252:253], v95 offset:2112\nds_read_b64_tr_b16 v[254:255], v95 offset:2368\ns_waitcnt lgkmcnt(0)\ns_waitcnt vmcnt(28)\ns_barrier\nds_read_b64_tr_b16 v[52:53], v95 offset:4096\nds_read_b64_tr_b16 v[54:55], v95 offset:4352\n" \
"ds_read_b64_tr_b16 v[48:49], v95 offset:6144\nds_read_b64_tr_b16 v[50:51], v95 offset:6400\nds_read_b64_tr_b16 v[44:45], v95 offset:4160\nds_read_b64_tr_b16 v[46:47], v95 offset:4416\nds_read_b64_tr_b16 v[40:41], v95 offset:6208\nds_read_b64_tr_b16 v[42:43], v95 offset:6464\ns_waitcnt lgkmcnt(0)\nds_write_b128 v1, v[84:87] offset:0\n" \
"ds_write_b128 v1, v[80:83] offset:1024\nds_write_b128 v1, v[76:79] offset:2048\nds_write_b128 v1, v[72:75] offset:3072\ns_waitcnt vmcnt(24)\ns_barrier\nds_read_b64_tr_b16 a[80:81], v95 offset:8192\nds_read_b64_tr_b16 a[82:83], v95 offset:8448\nds_read_b64_tr_b16 a[84:85], v95 offset:10240\n" \
"ds_read_b64_tr_b16 a[86:87], v95 offset:10496\nds_read_b64_tr_b16 v[120:121], v95 offset:8256\nds_read_b64_tr_b16 v[122:123], v95 offset:8512\nds_read_b64_tr_b16 v[124:125], v95 offset:10304\nds_read_b64_tr_b16 v[126:127], v95 offset:10560\ns_waitcnt lgkmcnt(0)\nds_write_b128 v1, v[68:71] offset:4096\nds_write_b128 v1, v[64:67] offset:5120\n" \
"ds_write_b128 v1, v[60:63] offset:6144\nds_write_b128 v1, v[56:59] offset:7168\ns_waitcnt vmcnt(20)\ns_barrier\nds_read_b64_tr_b16 v[56:57], v95 offset:12288\nds_read_b64_tr_b16 v[58:59], v95 offset:12544\nds_read_b64_tr_b16 v[60:61], v95 offset:14336\nds_read_b64_tr_b16 v[62:63], v95 offset:14592\n" \
"ds_read_b64_tr_b16 v[64:65], v95 offset:12352\nds_read_b64_tr_b16 v[66:67], v95 offset:12608\nds_read_b64_tr_b16 v[68:69], v95 offset:14400\nds_read_b64_tr_b16 v[70:71], v95 offset:14656\ns_waitcnt lgkmcnt(0)\nds_write_b128 v1, v[52:55] offset:8192\nds_write_b128 v1, v[48:51] offset:9216\nds_write_b128 v1, v[44:47] offset:10240\n" \
"ds_write_b128 v1, v[40:43] offset:11264\ns_waitcnt vmcnt(16)\ns_barrier\nds_read_b64_tr_b16 a[176:177], v95 offset:16384\nds_read_b64_tr_b16 a[178:179], v95 offset:16640\nds_read_b64_tr_b16 a[180:181], v95 offset:18432\nds_read_b64_tr_b16 a[182:183], v95 offset:18688\nds_read_b64_tr_b16 v[224:225], v95 offset:16448\n" \
"ds_read_b64_tr_b16 v[226:227], v95 offset:16704\nds_read_b64_tr_b16 v[228:229], v95 offset:18496\nds_read_b64_tr_b16 v[230:231], v95 offset:18752\ns_waitcnt lgkmcnt(0)\nds_write_b128 v1, v[56:59] offset:12288\nds_write_b128 v1, v[60:63] offset:13312\nds_write_b128 v1, v[64:67] offset:14336\nds_write_b128 v1, v[68:71] offset:15360\n" \
"s_waitcnt vmcnt(12)\ns_barrier\nds_read_b64_tr_b16 a[32:33], v95 offset:20480\nds_read_b64_tr_b16 a[34:35], v95 offset:20736\nds_read_b64_tr_b16 a[36:37], v95 offset:22528\nds_read_b64_tr_b16 a[38:39], v95 offset:22784\nds_read_b64_tr_b16 a[232:233], v95 offset:20544\nds_read_b64_tr_b16 a[234:235], v95 offset:20800\n" \
"ds_read_b64_tr_b16 a[236:237], v95 offset:22592\nds_read_b64_tr_b16 a[238:239], v95 offset:22848\ns_waitcnt lgkmcnt(0)\ns_waitcnt vmcnt(8)\ns_barrier\nds_read_b64_tr_b16 v[68:69], v94 offset:0\nds_read_b64_tr_b16 v[70:71], v94 offset:256\nds_read_b64_tr_b16 v[64:65], v94 offset:2048\n" \
"ds_read_b64_tr_b16 v[66:67], v94 offset:2304\nds_read_b64_tr_b16 v[60:61], v94 offset:64\nds_read_b64_tr_b16 v[62:63], v94 offset:320\nds_read_b64_tr_b16 v[56:57], v94 offset:2112\nds_read_b64_tr_b16 v[58:59], v94 offset:2368\ns_waitcnt lgkmcnt(0)\nds_write_b128 v1, v[68:71] offset:16384\nds_write_b128 v1, v[64:67] offset:17408\n" \
"ds_write_b128 v1, v[60:63] offset:18432\nds_write_b128 v1, v[56:59] offset:19456\ns_waitcnt vmcnt(4)\ns_barrier\nds_read_b64_tr_b16 a[136:137], v94 offset:4096\nds_read_b64_tr_b16 a[138:139], v94 offset:4352\nds_read_b64_tr_b16 a[140:141], v94 offset:6144\nds_read_b64_tr_b16 a[142:143], v94 offset:6400\n" \
"ds_read_b64_tr_b16 v[176:177], v94 offset:4160\nds_read_b64_tr_b16 v[178:179], v94 offset:4416\nds_read_b64_tr_b16 v[180:181], v94 offset:6208\nds_read_b64_tr_b16 v[182:183], v94 offset:6464\ns_waitcnt lgkmcnt(0)\ns_waitcnt vmcnt(0)\ns_barrier\nds_read_b64_tr_b16 v[56:57], v94 offset:8192\n" \
"ds_read_b64_tr_b16 v[58:59], v94 offset:8448\nds_read_b64_tr_b16 v[60:61], v94 offset:10240\nds_read_b64_tr_b16 v[62:63], v94 offset:10496\nds_read_b64_tr_b16 v[64:65], v94 offset:8256\nds_read_b64_tr_b16 v[66:67], v94 offset:8512\nds_read_b64_tr_b16 v[68:69], v94 offset:10304\nds_read_b64_tr_b16 v[70:71], v94 offset:10560\ns_waitcnt lgkmcnt(0)\n" \
"ds_write_b128 v1, v[56:59] offset:20480\nds_write_b128 v1, v[60:63] offset:21504\nds_write_b128 v1, v[64:67] offset:22528\nds_write_b128 v1, v[68:71] offset:23552\ns_waitcnt lgkmcnt(0)\ns_barrier\ns_mov_b32 s43, 0\n.Lvu_hb%=:\n" \
"buffer_load_dwordx2 v[16:17], off, s[36:39], s45 sc1\ns_waitcnt vmcnt(0)\nv_cmp_ne_u32 vcc, v15, v16\ns_cbranch_vccnz .Lvu_hb1%=\ns_sleep 4\ns_add_u32 s43, s43, 1\ns_cmp_lt_u32 s43, 262144\ns_cbranch_scc1 .Lvu_hb%=\n" \
"s_branch .Lvu_abort%=\n.Lvu_hb1%=:\nv_readfirstlane_b32 s44, v17\ns_and_b32 s44, s44, 0x200000\ns_add_u32 s36, s36, s44\ns_addc_u32 s37, s37, 0\ns_mov_b32 s40, -1\ns_mov_b32 s43, 0\n" \
"s_lshl_b32 s57, %[w], 12\ns_mov_b32 s56, 0\ns_mov_b32 s42, 0\ns_mov_b32 s60, -2147483648\ns_mov_b32 s54, -2147483648\ns_add_u32 s44, s40, 1\ns_lshl_b32 s44, s44, 17\ns_add_u32 s44, s44, %[xn]\n" \
"s_lshr_b32 s45, s44, 8\ns_and_b32 s45, s45, 0x2000\ns_xor_b32 s41, s44, s45\ns_add_u32 s41, s41, %[xw]\n.Lvu_f0%=:\nbuffer_load_dword v14, v8, s[36:39], s56 offen sc1\ns_waitcnt vmcnt(0)\nv_cmp_ge_i32 vcc, s40, v14\n" \
"s_cbranch_vccz .Lvu_f0fok%=\ns_add_u32 s43, s43, 1\ns_cmp_lt_u32 s43, 262144\ns_cbranch_scc1 .Lvu_f0%=\ns_branch .Lvu_abort%=\n.Lvu_f0fok%=:\ns_add_u32 m0, s57, 0\ns_add_u32 s58, s41, 0\n" \
"buffer_load_dwordx4 v0, s[36:39], s58 offen offset:0 sc1 lds\nbuffer_load_dwordx4 v0, s[36:39], s58 offen offset:1024 sc1 lds\nbuffer_load_dwordx4 v0, s[36:39], s58 offen offset:2048 sc1 lds\nbuffer_load_dwordx4 v0, s[36:39], s58 offen offset:3072 sc1 lds\ns_add_u32 m0, s57, 32768\ns_add_u32 s58, s41, 32768\nbuffer_load_dwordx4 v0, s[36:39], s58 offen offset:0 sc1 lds\nbuffer_load_dwordx4 v0, s[36:39], s58 offen offset:1024 sc1 lds\n" \
"buffer_load_dwordx4 v0, s[36:39], s58 offen offset:2048 sc1 lds\nbuffer_load_dwordx4 v0, s[36:39], s58 offen offset:3072 sc1 lds\ns_waitcnt vmcnt(0)\ns_barrier\nds_read_b128 v[32:35], v2 offset:0\nds_read_b128 v[36:39], v2 offset:256\nds_read_b128 v[40:43], v2 offset:2048\nds_read_b128 v[44:47], v2 offset:2304\n" \
".Lvu_tok%=:\nds_read_b128 v[64:67], v2 offset:4096\nds_read_b128 v[68:71], v2 offset:4352\ns_waitcnt lgkmcnt(5)\nv_mfma_f32_16x16x32_bf16 v[16:19], a[0:3], v[32:35], 0\nv_cvt_pk_bf16_f32 v92, v80, v81\nv_cvt_pk_bf16_f32 v93, v82, v83\nv_mfma_f32_16x16x32_bf16 v[20:23], a[16:19], v[32:35], 0\n" \
"s_waitcnt lgkmcnt(4)\nds_read_b128 v[32:35], v2 offset:6144\nv_cvt_pk_bf16_f32 v94, v84, v85\nv_cvt_pk_bf16_f32 v95, v86, v87\nv_mfma_f32_16x16x32_bf16 v[24:27], a[0:3], v[36:39], 0\nv_and_or_b32 v92, v92, -2, v13\nv_and_or_b32 v94, v94, -2, v13\nv_mfma_f32_16x16x32_bf16 v[28:31], a[16:19], v[36:39], 0\n" \
"s_waitcnt lgkmcnt(4)\nbuffer_store_dwordx4 v[92:95], v7, s[36:39], s60 offen offset:2560 sc1\nds_read_b128 v[36:39], v2 offset:6400\nv_mfma_f32_16x16x32_bf16 v[16:19], a[4:7], v[40:43], v[16:19]\nv_mfma_f32_16x16x32_bf16 v[20:23], a[20:23], v[40:43], v[20:23]\ns_waitcnt lgkmcnt(4)\nds_read_b128 v[40:43], v2 offset:8192\nv_mfma_f32_16x16x32_bf16 v[24:27], a[4:7], v[44:47], v[24:27]\n" \
"s_add_u32 m0, s57, 16384\ns_add_u32 s60, s41, 65536\nbuffer_load_dwordx4 v0, s[36:39], s60 offen offset:0 sc1 lds\nv_mfma_f32_16x16x32_bf16 v[28:31], a[20:23], v[44:47], v[28:31]\ns_waitcnt lgkmcnt(4)\nds_read_b128 v[44:47], v2 offset:8448\nv_mfma_f32_16x16x32_bf16 v[16:19], a[24:27], v[64:67], v[16:19]\nds_read_b128 v[56:59], v1 offset:0\n" \
"ds_read_b128 v[60:63], v1 offset:1024\nv_mfma_f32_16x16x32_bf16 v[20:23], a[40:43], v[64:67], v[20:23]\ns_waitcnt lgkmcnt(6)\nds_read_b128 v[64:67], v2 offset:10240\nv_mfma_f32_16x16x32_bf16 v[24:27], a[24:27], v[68:71], v[24:27]\nbuffer_load_dwordx4 v0, s[36:39], s60 offen offset:1024 sc1 lds\nv_mfma_f32_16x16x32_bf16 v[28:31], a[40:43], v[68:71], v[28:31]\ns_waitcnt lgkmcnt(6)\n" \
"ds_read_b128 v[68:71], v2 offset:10496\nv_mfma_f32_16x16x32_bf16 v[16:19], a[28:31], v[32:35], v[16:19]\nv_mfma_f32_16x16x32_bf16 v[20:23], a[44:47], v[32:35], v[20:23]\ns_waitcnt lgkmcnt(6)\nds_read_b128 v[32:35], v2 offset:12288\nv_mfma_f32_16x16x32_bf16 v[24:27], a[28:31], v[36:39], v[24:27]\nbuffer_load_dwordx4 v0, s[36:39], s60 offen offset:2048 sc1 lds\nv_mfma_f32_16x16x32_bf16 v[28:31], a[44:47], v[36:39], v[28:31]\n" \
"s_waitcnt lgkmcnt(6)\nds_read_b128 v[36:39], v2 offset:12544\nv_mfma_f32_16x16x32_bf16 v[16:19], a[56:59], v[40:43], v[16:19]\nv_mfma_f32_16x16x32_bf16 v[20:23], a[72:75], v[40:43], v[20:23]\ns_waitcnt lgkmcnt(6)\nds_read_b128 v[40:43], v2 offset:14336\nv_mfma_f32_16x16x32_bf16 v[24:27], a[56:59], v[44:47], v[24:27]\nbuffer_load_dwordx4 v0, s[36:39], s60 offen offset:3072 sc1 lds\n" \
"v_mfma_f32_16x16x32_bf16 v[28:31], a[72:75], v[44:47], v[28:31]\ns_waitcnt lgkmcnt(4)\nds_read_b128 v[44:47], v2 offset:14592\nv_mfma_f32_16x16x32_bf16 v[16:19], a[60:63], v[64:67], v[16:19]\ns_add_u32 s56, s56, %[fs]\ns_add_u32 s62, s40, 1\nv_mfma_f32_16x16x32_bf16 v[20:23], a[76:79], v[64:67], v[20:23]\ns_lshr_b32 m0, s57, 4\n" \
"s_add_u32 m0, m0, 159744\ns_waitcnt lgkmcnt(4)\nds_read_b128 v[64:67], v2 offset:32768\nv_mfma_f32_16x16x32_bf16 v[24:27], a[60:63], v[68:71], v[24:27]\nbuffer_load_dword v8, s[36:39], s56 offen sc1 lds\nv_lshrrev_b32 v14, 2, v0\nv_mfma_f32_16x16x32_bf16 v[28:31], a[76:79], v[68:71], v[28:31]\ns_waitcnt lgkmcnt(4)\n" \
"ds_read_b128 v[68:71], v2 offset:33024\nv_mfma_f32_16x16x32_bf16 v[16:19], v[56:59], v[32:35], v[16:19]\nv_add_u32 v14, 159744, v14\nv_mfma_f32_16x16x32_bf16 v[20:23], a[88:91], v[32:35], v[20:23]\ns_waitcnt lgkmcnt(4)\nds_read_b128 v[32:35], v2 offset:34816\nv_mfma_f32_16x16x32_bf16 v[24:27], v[56:59], v[36:39], v[24:27]\nv_mfma_f32_16x16x32_bf16 v[28:31], a[88:91], v[36:39], v[28:31]\n" \
"s_waitcnt lgkmcnt(4)\nds_read_b128 v[36:39], v2 offset:35072\nv_mfma_f32_16x16x32_bf16 v[16:19], v[60:63], v[40:43], v[16:19]\nv_mfma_f32_16x16x32_bf16 v[20:23], a[92:95], v[40:43], v[20:23]\ns_waitcnt lgkmcnt(4)\nds_read_b128 v[40:43], v2 offset:36864\nv_mfma_f32_16x16x32_bf16 v[24:27], v[60:63], v[44:47], v[24:27]\nv_mfma_f32_16x16x32_bf16 v[28:31], a[92:95], v[44:47], v[28:31]\n" \
"s_waitcnt lgkmcnt(4)\nds_read_b128 v[44:47], v2 offset:37120\nv_mfma_f32_16x16x32_bf16 v[16:19], a[104:107], v[64:67], v[16:19]\nds_read_b128 v[56:59], v1 offset:2048\nds_read_b128 v[60:63], v1 offset:3072\nv_mfma_f32_16x16x32_bf16 v[20:23], a[120:123], v[64:67], v[20:23]\ns_waitcnt lgkmcnt(6)\nds_read_b128 v[64:67], v2 offset:38912\n" \
"v_mfma_f32_16x16x32_bf16 v[24:27], a[104:107], v[68:71], v[24:27]\ns_barrier\nv_mfma_f32_16x16x32_bf16 v[28:31], a[120:123], v[68:71], v[28:31]\ns_waitcnt lgkmcnt(6)\nds_read_b128 v[68:71], v2 offset:39168\nv_mfma_f32_16x16x32_bf16 v[16:19], a[108:111], v[32:35], v[16:19]\ns_add_u32 m0, s57, 0\ns_add_u32 s60, s41, 98304\n" \
"buffer_load_dwordx4 v0, s[36:39], s60 offen offset:0 sc1 lds\nv_mfma_f32_16x16x32_bf16 v[20:23], a[124:127], v[32:35], v[20:23]\ns_waitcnt lgkmcnt(6)\nds_read_b128 v[32:35], v2 offset:40960\nv_mfma_f32_16x16x32_bf16 v[24:27], a[108:111], v[36:39], v[24:27]\nv_mfma_f32_16x16x32_bf16 v[28:31], a[124:127], v[36:39], v[28:31]\ns_waitcnt lgkmcnt(6)\nds_read_b128 v[36:39], v2 offset:41216\n" \
"v_mfma_f32_16x16x32_bf16 v[16:19], a[128:131], v[40:43], v[16:19]\nbuffer_load_dwordx4 v0, s[36:39], s60 offen offset:1024 sc1 lds\nv_mfma_f32_16x16x32_bf16 v[20:23], a[144:147], v[40:43], v[20:23]\ns_waitcnt lgkmcnt(6)\nds_read_b128 v[40:43], v2 offset:43008\nv_mfma_f32_16x16x32_bf16 v[24:27], a[128:131], v[44:47], v[24:27]\nv_mfma_f32_16x16x32_bf16 v[28:31], a[144:147], v[44:47], v[28:31]\ns_waitcnt lgkmcnt(4)\n" \
"ds_read_b128 v[44:47], v2 offset:43264\nv_mfma_f32_16x16x32_bf16 v[16:19], a[132:135], v[64:67], v[16:19]\nbuffer_load_dwordx4 v0, s[36:39], s60 offen offset:2048 sc1 lds\nv_mfma_f32_16x16x32_bf16 v[20:23], a[148:151], v[64:67], v[20:23]\ns_waitcnt lgkmcnt(4)\nds_read_b128 v[64:67], v2 offset:45056\nv_mfma_f32_16x16x32_bf16 v[24:27], a[132:135], v[68:71], v[24:27]\nv_mfma_f32_16x16x32_bf16 v[28:31], a[148:151], v[68:71], v[28:31]\n" \
"s_waitcnt lgkmcnt(4)\nds_read_b128 v[68:71], v2 offset:45312\nv_mfma_f32_16x16x32_bf16 v[16:19], a[160:163], v[32:35], v[16:19]\nbuffer_load_dwordx4 v0, s[36:39], s60 offen offset:3072 sc1 lds\nv_mfma_f32_16x16x32_bf16 v[20:23], v[56:59], v[32:35], v[20:23]\ns_waitcnt lgkmcnt(4)\nds_read_b128 v[32:35], v2 offset:47104\nv_mfma_f32_16x16x32_bf16 v[24:27], a[160:163], v[36:39], v[24:27]\n" \
"s_add_u32 s44, s40, 2\ns_lshl_b32 s44, s44, 17\nv_mfma_f32_16x16x32_bf16 v[28:31], v[56:59], v[36:39], v[28:31]\ns_waitcnt lgkmcnt(4)\nds_read_b128 v[36:39], v2 offset:47360\nv_mfma_f32_16x16x32_bf16 v[16:19], a[164:167], v[40:43], v[16:19]\ns_add_u32 s44, s44, %[xn]\ns_lshr_b32 s45, s44, 8\n" \
"v_mfma_f32_16x16x32_bf16 v[20:23], v[60:63], v[40:43], v[20:23]\ns_waitcnt vmcnt(4)\ns_barrier\ns_waitcnt lgkmcnt(4)\nds_read_b128 v[40:43], v2 offset:16384\nv_mfma_f32_16x16x32_bf16 v[24:27], a[164:167], v[44:47], v[24:27]\ns_and_b32 s45, s45, 0x2000\ns_xor_b32 s41, s44, s45\n" \
"ds_read_b32 v14, v14\nv_mfma_f32_16x16x32_bf16 v[28:31], v[60:63], v[44:47], v[28:31]\ns_waitcnt lgkmcnt(5)\nds_read_b128 v[44:47], v2 offset:16640\nv_mfma_f32_16x16x32_bf16 v[16:19], a[184:187], v[64:67], v[16:19]\nds_read_b128 v[56:59], v1 offset:4096\nds_read_b128 v[60:63], v1 offset:5120\nv_mfma_f32_16x16x32_bf16 v[20:23], a[192:195], v[64:67], v[20:23]\n" \
"s_waitcnt lgkmcnt(7)\nds_read_b128 v[64:67], v2 offset:18432\nv_mfma_f32_16x16x32_bf16 v[24:27], a[184:187], v[68:71], v[24:27]\ns_add_u32 s41, s41, %[xw]\nv_mfma_f32_16x16x32_bf16 v[28:31], a[192:195], v[68:71], v[28:31]\ns_waitcnt lgkmcnt(7)\nds_read_b128 v[68:71], v2 offset:18688\nv_mfma_f32_16x16x32_bf16 v[16:19], a[188:191], v[32:35], v[16:19]\n" \
"s_waitcnt lgkmcnt(5)\nv_cmp_ge_i32 s[64:65], s62, v14\nv_mfma_f32_16x16x32_bf16 v[20:23], a[196:199], v[32:35], v[20:23]\nds_read_b128 v[32:35], v2 offset:20480\nv_mfma_f32_16x16x32_bf16 v[24:27], a[188:191], v[36:39], v[24:27]\ns_cmp_eq_u64 s[64:65], 0\ns_cselect_b32 s61, s41, -2147483648\nv_mfma_f32_16x16x32_bf16 v[28:31], a[196:199], v[36:39], v[28:31]\n" \
"ds_read_b128 v[36:39], v2 offset:20736\nv_mfma_f32_16x16x32_bf16 v[16:19], a[208:211], v[40:43], v[16:19]\ns_waitcnt vmcnt(4)\nbuffer_store_dword v15, off, s[36:39], s54 sc1\nv_mfma_f32_16x16x32_bf16 v[20:23], a[224:227], v[40:43], v[20:23]\ns_waitcnt lgkmcnt(6)\nds_read_b128 v[40:43], v2 offset:22528\nv_mfma_f32_16x16x32_bf16 v[24:27], a[208:211], v[44:47], v[24:27]\n" \
"s_add_u32 m0, s57, 32768\ns_add_u32 s58, s61, 32768\nbuffer_load_dwordx4 v0, s[36:39], s58 offen offset:0 sc1 lds\nv_mfma_f32_16x16x32_bf16 v[28:31], a[224:227], v[44:47], v[28:31]\ns_waitcnt lgkmcnt(4)\nds_read_b128 v[44:47], v2 offset:22784\nv_mfma_f32_16x16x32_bf16 v[16:19], a[212:215], v[64:67], v[16:19]\nv_mfma_f32_16x16x32_bf16 v[20:23], a[228:231], v[64:67], v[20:23]\n" \
"s_waitcnt lgkmcnt(4)\nds_read_b128 v[64:67], v2 offset:24576\nv_mfma_f32_16x16x32_bf16 v[24:27], a[212:215], v[68:71], v[24:27]\nbuffer_load_dwordx4 v0, s[36:39], s58 offen offset:1024 sc1 lds\nv_mfma_f32_16x16x32_bf16 v[28:31], a[228:231], v[68:71], v[28:31]\ns_waitcnt lgkmcnt(4)\nds_read_b128 v[68:71], v2 offset:24832\nv_mfma_f32_16x16x32_bf16 v[16:19], v[56:59], v[32:35], v[16:19]\n" \
"v_mfma_f32_16x16x32_bf16 v[20:23], a[240:243], v[32:35], v[20:23]\ns_waitcnt lgkmcnt(4)\nds_read_b128 v[32:35], v2 offset:26624\nv_mfma_f32_16x16x32_bf16 v[24:27], v[56:59], v[36:39], v[24:27]\nbuffer_load_dwordx4 v0, s[36:39], s58 offen offset:2048 sc1 lds\nv_mfma_f32_16x16x32_bf16 v[28:31], a[240:243], v[36:39], v[28:31]\ns_waitcnt lgkmcnt(4)\nds_read_b128 v[36:39], v2 offset:26880\n" \
"v_mfma_f32_16x16x32_bf16 v[16:19], v[60:63], v[40:43], v[16:19]\nv_mfma_f32_16x16x32_bf16 v[20:23], a[244:247], v[40:43], v[20:23]\ns_waitcnt lgkmcnt(4)\nds_read_b128 v[40:43], v2 offset:28672\nv_mfma_f32_16x16x32_bf16 v[24:27], v[60:63], v[44:47], v[24:27]\nbuffer_load_dwordx4 v0, s[36:39], s58 offen offset:3072 sc1 lds\nv_mfma_f32_16x16x32_bf16 v[28:31], a[244:247], v[44:47], v[28:31]\ns_waitcnt lgkmcnt(4)\n" \
"ds_read_b128 v[44:47], v2 offset:28928\nv_mfma_f32_16x16x32_bf16 v[16:19], v[96:99], v[64:67], v[16:19]\nds_read_b128 v[56:59], v1 offset:6144\nds_read_b128 v[60:63], v1 offset:7168\nv_mfma_f32_16x16x32_bf16 v[20:23], v[112:115], v[64:67], v[20:23]\ns_waitcnt lgkmcnt(6)\nds_read_b128 v[64:67], v2 offset:30720\nv_mfma_f32_16x16x32_bf16 v[24:27], v[96:99], v[68:71], v[24:27]\n" \
"v_mfma_f32_16x16x32_bf16 v[28:31], v[112:115], v[68:71], v[28:31]\ns_waitcnt lgkmcnt(6)\nds_read_b128 v[68:71], v2 offset:30976\nv_mfma_f32_16x16x32_bf16 v[16:19], v[100:103], v[32:35], v[16:19]\nv_mfma_f32_16x16x32_bf16 v[20:23], v[116:119], v[32:35], v[20:23]\ns_waitcnt vmcnt(5)\ns_barrier\ns_waitcnt lgkmcnt(6)\n" \
"ds_read_b128 v[32:35], v2 offset:0\nv_mfma_f32_16x16x32_bf16 v[24:27], v[100:103], v[36:39], v[24:27]\nv_mfma_f32_16x16x32_bf16 v[28:31], v[116:119], v[36:39], v[28:31]\ns_waitcnt lgkmcnt(6)\nds_read_b128 v[36:39], v2 offset:256\nv_mfma_f32_16x16x32_bf16 v[16:19], v[128:131], v[40:43], v[16:19]\ns_add_u32 m0, s57, 16384\ns_nop 0\n" \
"buffer_load_dwordx4 v3, s[36:39], s42 offen offset:0 sc1 lds\nv_mfma_f32_16x16x32_bf16 v[20:23], v[136:139], v[40:43], v[20:23]\ns_waitcnt lgkmcnt(6)\nds_read_b128 v[40:43], v2 offset:2048\nv_mfma_f32_16x16x32_bf16 v[24:27], v[128:131], v[44:47], v[24:27]\nv_mfma_f32_16x16x32_bf16 v[28:31], v[136:139], v[44:47], v[28:31]\ns_waitcnt lgkmcnt(4)\nds_read_b128 v[44:47], v2 offset:2304\n" \
"v_mfma_f32_16x16x32_bf16 v[16:19], v[132:135], v[64:67], v[16:19]\nbuffer_load_dwordx4 v3, s[36:39], s42 offen offset:1024 sc1 lds\nv_mfma_f32_16x16x32_bf16 v[20:23], v[140:143], v[64:67], v[20:23]\ns_waitcnt lgkmcnt(4)\nds_read_b128 v[64:67], v2 offset:4096\nv_mfma_f32_16x16x32_bf16 v[24:27], v[132:135], v[68:71], v[24:27]\nv_mfma_f32_16x16x32_bf16 v[28:31], v[140:143], v[68:71], v[28:31]\ns_waitcnt lgkmcnt(4)\n" \
"ds_read_b128 v[68:71], v2 offset:4352\nv_mfma_f32_16x16x32_bf16 v[16:19], v[152:155], v[32:35], v[16:19]\nbuffer_load_dwordx4 v3, s[36:39], s42 offen offset:2048 sc1 lds\nv_mfma_f32_16x16x32_bf16 v[20:23], v[56:59], v[32:35], v[20:23]\ns_waitcnt lgkmcnt(4)\nds_read_b128 v[32:35], v2 offset:6144\nv_mfma_f32_16x16x32_bf16 v[24:27], v[152:155], v[36:39], v[24:27]\nv_mfma_f32_16x16x32_bf16 v[28:31], v[56:59], v[36:39], v[28:31]\n" \
"s_waitcnt lgkmcnt(4)\nds_read_b128 v[36:39], v2 offset:6400\nv_mfma_f32_16x16x32_bf16 v[16:19], v[156:159], v[40:43], v[16:19]\nbuffer_load_dwordx4 v3, s[36:39], s42 offen offset:3072 sc1 lds\nv_add_u32 v14, s57, v0\nv_mfma_f32_16x16x32_bf16 v[20:23], v[60:63], v[40:43], v[20:23]\ns_waitcnt lgkmcnt(4)\nds_read_b128 v[40:43], v2 offset:8192\n" \
"v_mfma_f32_16x16x32_bf16 v[24:27], v[156:159], v[44:47], v[24:27]\nv_mov_b64 v[48:49], 0\nv_mfma_f32_16x16x32_bf16 v[28:31], v[60:63], v[44:47], v[28:31]\ns_waitcnt lgkmcnt(4)\nds_read_b128 v[44:47], v2 offset:10240\nv_mov_b64 v[50:51], 0\nv_mfma_f32_16x16x32_bf16 v[16:19], v[168:171], v[64:67], v[16:19]\nv_mov_b64 v[52:53], 0\n" \
"v_mfma_f32_16x16x32_bf16 v[20:23], v[184:187], v[64:67], v[20:23]\ns_waitcnt lgkmcnt(4)\nds_read_b128 v[64:67], v2 offset:12288\nv_mov_b64 v[54:55], 0\nv_mfma_f32_16x16x32_bf16 v[24:27], v[168:171], v[68:71], v[24:27]\nds_read_b128 v[56:59], v1 offset:0\nds_read_b128 v[60:63], v1 offset:1024\nv_mfma_f32_16x16x32_bf16 v[28:31], v[184:187], v[68:71], v[28:31]\n" \
"s_waitcnt lgkmcnt(6)\nds_read_b128 v[68:71], v2 offset:14336\nv_mfma_f32_16x16x32_bf16 v[16:19], v[172:175], v[32:35], v[16:19]\nv_mfma_f32_16x16x32_bf16 v[20:23], v[188:191], v[32:35], v[20:23]\ns_waitcnt lgkmcnt(6)\nds_read_b128 v[32:35], v2 offset:8448\nv_mfma_f32_16x16x32_bf16 v[24:27], v[172:175], v[36:39], v[24:27]\nv_mfma_f32_16x16x32_bf16 v[28:31], v[188:191], v[36:39], v[28:31]\n" \
"s_waitcnt lgkmcnt(6)\nds_read_b128 v[36:39], v2 offset:10496\nv_mfma_f32_16x16x32_bf16 v[16:19], v[200:203], v[40:43], v[16:19]\ns_waitcnt vmcnt(0)\nds_read_b128 v[80:83], v14 offset:16384\nds_read_b128 v[84:87], v14 offset:17408\nv_mfma_f32_16x16x32_bf16 v[20:23], v[216:219], v[40:43], v[20:23]\ns_waitcnt lgkmcnt(8)\n" \
"ds_read_b128 v[40:43], v2 offset:12544\nv_mfma_f32_16x16x32_bf16 v[16:19], v[204:207], v[44:47], v[16:19]\nds_read_b128 v[88:91], v14 offset:18432\nds_read_b128 v[92:95], v14 offset:19456\nv_mfma_f32_16x16x32_bf16 v[20:23], v[220:223], v[44:47], v[20:23]\ns_waitcnt lgkmcnt(10)\nds_read_b128 v[44:47], v2 offset:14592\nv_mfma_f32_16x16x32_bf16 v[16:19], v[232:235], v[64:67], v[16:19]\n" \
"s_waitcnt lgkmcnt(4)\nv_max3_u32 v72, v80, v82, v84\nv_mfma_f32_16x16x32_bf16 v[20:23], v[240:243], v[64:67], v[20:23]\ns_waitcnt lgkmcnt(2)\nv_max3_u32 v72, v72, v86, v88\nv_mfma_f32_16x16x32_bf16 v[16:19], v[236:239], v[68:71], v[16:19]\ns_waitcnt lgkmcnt(1)\nv_max3_u32 v72, v72, v90, v92\n" \
"v_max_u32 v72, v72, v94\nv_mfma_f32_16x16x32_bf16 v[20:23], v[244:247], v[68:71], v[20:23]\nv_cmp_eq_u32 vcc, -1, v72\nv_mfma_f32_16x16x32_bf16 v[24:27], v[200:203], v[32:35], v[24:27]\ns_cbranch_vccnz .Lvu_re%=\n.Lvu_rer%=:\ns_barrier\ns_memrealtime s[68:69]\n" \
"ds_read_b128 v[80:83], v0 offset:16384\nv_mfma_f32_16x16x32_bf16 v[28:31], v[216:219], v[32:35], v[28:31]\nds_read_b128 v[84:87], v0 offset:17408\nv_mfma_f32_16x16x32_bf16 v[24:27], v[204:207], v[36:39], v[24:27]\nds_read_b128 v[88:91], v0 offset:18432\nv_cvt_pk_bf16_f32 v72, v16, v17\nv_cvt_pk_bf16_f32 v73, v18, v19\nv_mfma_f32_16x16x32_bf16 v[28:31], v[220:223], v[36:39], v[28:31]\n" \
"ds_read_b128 v[92:95], v0 offset:19456\nv_cvt_pk_bf16_f32 v74, v20, v21\nv_cvt_pk_bf16_f32 v75, v22, v23\nv_mfma_f32_16x16x32_bf16 v[24:27], v[232:235], v[40:43], v[24:27]\nds_read_b128 v[16:19], v0 offset:20480\nv_mfma_f32_16x16x32_bf16 v[28:31], v[240:243], v[40:43], v[28:31]\ns_waitcnt lgkmcnt(5)\nds_read_b128 v[20:23], v0 offset:21504\n" \
"v_mfma_f32_16x16x32_bf16 v[24:27], v[236:239], v[44:47], v[24:27]\nds_read_b128 v[32:35], v0 offset:22528\nv_mfma_f32_16x16x32_bf16 v[28:31], v[244:247], v[44:47], v[28:31]\ns_cmp_lt_i32 s40, 0\nds_read_b128 v[36:39], v0 offset:23552\nds_write2st64_b64 v9, v[72:73], v[74:75] offset0:0 offset1:8\ns_cbranch_scc1 .Lvu_first%=\n.Lvu_crit%=:\n" \
"s_waitcnt lgkmcnt(8)\nv_smfmac_f32_16x16x64_bf16 v[48:51], v[80:83], a[0:7], v4\ns_waitcnt lgkmcnt(7)\nds_read_b128 v[64:67], v1 offset:4096\nds_read_b128 v[40:43], v0 offset:24576\nv_smfmac_f32_16x16x64_bf16 v[48:51], v[84:87], a[24:31], v4\ns_waitcnt lgkmcnt(8)\nds_read_b128 v[68:71], v1 offset:5120\n" \
"ds_read_b128 v[44:47], v0 offset:25600\nv_cvt_pk_bf16_f32 v76, v24, v25\nv_smfmac_f32_16x16x64_bf16 v[48:51], v[88:91], a[56:63], v4\ns_waitcnt lgkmcnt(9)\nds_read_b128 v[80:83], v0 offset:28672\nv_cvt_pk_bf16_f32 v77, v26, v27\nv_smfmac_f32_16x16x64_bf16 v[48:51], v[92:95], v[56:63], v4\ns_waitcnt lgkmcnt(9)\n" \
"ds_read_b128 v[84:87], v0 offset:29696\nv_cvt_pk_bf16_f32 v78, v28, v29\nv_smfmac_f32_16x16x64_bf16 v[48:51], v[16:19], a[104:111], v4\ns_waitcnt lgkmcnt(9)\nds_read_b128 v[88:91], v0 offset:30720\nv_cvt_pk_bf16_f32 v79, v30, v31\nv_smfmac_f32_16x16x64_bf16 v[48:51], v[20:23], a[128:135], v4\ns_waitcnt lgkmcnt(9)\n" \
"ds_read_b128 v[92:95], v0 offset:31744\nv_mov_b32 v14, s40\nv_smfmac_f32_16x16x64_bf16 v[48:51], v[32:35], a[160:167], v4\ns_waitcnt lgkmcnt(9)\nds_read_b128 v[24:27], v0 offset:26624\nv_smfmac_f32_16x16x64_bf16 v[48:51], v[36:39], a[184:191], v4\ns_waitcnt lgkmcnt(7)\nds_read_b128 v[28:31], v0 offset:27648\n" \
"v_mov_b32 v15, s68\nv_smfmac_f32_16x16x64_bf16 v[48:51], v[40:43], a[208:215], v4\ns_waitcnt lgkmcnt(6)\nds_read_b128 v[56:59], v1 offset:6144\nv_smfmac_f32_16x16x64_bf16 v[48:51], v[44:47], v[64:71], v4\ns_waitcnt lgkmcnt(6)\nds_read_b128 v[60:63], v1 offset:7168\nv_smfmac_f32_16x16x64_bf16 v[48:51], v[80:83], v[152:159], v4\n" \
"s_waitcnt lgkmcnt(6)\nds_write2st64_b64 v9, v[76:77], v[78:79] offset0:2 offset1:10\nv_smfmac_f32_16x16x64_bf16 v[48:51], v[84:87], v[168:175], v4\ns_mov_b64 exec, 1\nbuffer_store_dwordx2 v[14:15], off, s[36:39], %[sg] sc1\ns_mov_b64 exec, -1\ns_waitcnt lgkmcnt(6)\nv_smfmac_f32_16x16x64_bf16 v[48:51], v[88:91], v[200:207], v4\n" \
"s_waitcnt lgkmcnt(5)\nv_smfmac_f32_16x16x64_bf16 v[48:51], v[92:95], v[232:239], v4\ns_waitcnt lgkmcnt(4)\nv_smfmac_f32_16x16x64_bf16 v[48:51], v[24:27], v[96:103], v4\ns_waitcnt lgkmcnt(3)\nds_read_b128 v[64:67], v1 offset:2048\nv_smfmac_f32_16x16x64_bf16 v[48:51], v[28:31], v[128:135], v4\ns_waitcnt lgkmcnt(2)\n" \
"ds_read_b128 v[68:71], v1 offset:3072\nv_smfmac_f32_16x16x64_bf16 v[52:55], v[80:83], v[56:63], v4\nds_read_b128 v[56:59], v1 offset:8192\nv_smfmac_f32_16x16x64_bf16 v[52:55], v[84:87], v[184:191], v4\nds_read_b128 v[60:63], v1 offset:9216\nv_add_f32 v48, v48, v50\nv_add_f32 v49, v49, v51\nv_smfmac_f32_16x16x64_bf16 v[52:55], v[88:91], v[216:223], v4\n" \
"ds_read_b128 v[80:83], v0 offset:16384\nv_cvt_pk_bf16_f32 v14, v48, v49\nv_smfmac_f32_16x16x64_bf16 v[52:55], v[92:95], v[240:247], v4\nds_write_b16 v5, v14 offset:0\nds_write_b16_d16_hi v5, v14 offset:16\nv_smfmac_f32_16x16x64_bf16 v[52:55], v[16:19], a[120:127], v4\nds_read_b128 v[84:87], v0 offset:17408\nv_mov_b64 v[16:17], 0\n" \
"v_smfmac_f32_16x16x64_bf16 v[52:55], v[20:23], a[144:151], v4\nds_read_b128 v[88:91], v0 offset:18432\nv_mov_b64 v[18:19], 0\nv_smfmac_f32_16x16x64_bf16 v[52:55], v[24:27], v[112:119], v4\nds_read_b128 v[92:95], v0 offset:19456\nv_mov_b64 v[20:21], 0\nv_smfmac_f32_16x16x64_bf16 v[52:55], v[28:31], v[136:143], v4\ns_waitcnt lgkmcnt(8)\n" \
"v_mov_b64 v[22:23], 0\nv_smfmac_f32_16x16x64_bf16 v[52:55], v[32:35], v[64:71], v4\ns_waitcnt lgkmcnt(3)\nds_read_b128 v[64:67], v1 offset:12288\ns_barrier\nds_read_b128 v[72:75], v11 offset:57344\nv_mov_b64 v[24:25], 0\nv_smfmac_f32_16x16x64_bf16 v[52:55], v[36:39], a[192:199], v4\n" \
"ds_read_b128 v[68:71], v1 offset:13312\nds_read_b128 v[76:79], v11 offset:58368\nv_mov_b64 v[26:27], 0\nv_smfmac_f32_16x16x64_bf16 v[52:55], v[40:43], a[224:231], v4\nv_mov_b64 v[28:29], 0\nv_smfmac_f32_16x16x64_bf16 v[52:55], v[44:47], a[240:247], v4\nv_mov_b64 v[30:31], 0\nv_smfmac_f32_16x16x64_bf16 v[52:55], v[80:83], a[16:23], v4\n" \
"s_waitcnt lgkmcnt(6)\nv_mov_b64 v[32:33], 0\nv_smfmac_f32_16x16x64_bf16 v[52:55], v[84:87], a[40:47], v4\ns_waitcnt lgkmcnt(5)\nv_mov_b64 v[34:35], 0\nv_smfmac_f32_16x16x64_bf16 v[52:55], v[88:91], a[72:79], v4\ns_waitcnt lgkmcnt(4)\nv_mov_b64 v[36:37], 0\n" \
"v_smfmac_f32_16x16x64_bf16 v[52:55], v[92:95], a[88:95], v4\ns_waitcnt lgkmcnt(2)\nv_mov_b64 v[38:39], 0\nv_smfmac_f32_16x16x64_bf16 v[16:19], v[72:75], a[8:15], v10\ns_waitcnt lgkmcnt(0)\nv_mov_b64 v[40:41], 0\nv_smfmac_f32_16x16x64_bf16 v[16:19], v[76:79], v[56:63], v10\nds_read_b128 v[56:59], v1 offset:10240\n" \
"v_add_f32 v52, v52, v54\nv_add_f32 v53, v53, v55\nv_smfmac_f32_16x16x64_bf16 v[20:23], v[72:75], a[64:71], v10\nds_read_b128 v[60:63], v1 offset:11264\nv_cvt_pk_bf16_f32 v14, v52, v53\nv_smfmac_f32_16x16x64_bf16 v[20:23], v[76:79], a[80:87], v10\nds_write_b16 v5, v14 offset:2048\nds_write_b16_d16_hi v5, v14 offset:2064\n" \
"v_mov_b64 v[42:43], 0\nv_smfmac_f32_16x16x64_bf16 v[24:27], v[72:75], a[112:119], v10\nv_mov_b64 v[44:45], 0\nv_smfmac_f32_16x16x64_bf16 v[24:27], v[76:79], v[64:71], v10\nds_read_b128 v[64:67], v1 offset:14336\nv_mov_b64 v[46:47], 0\nv_smfmac_f32_16x16x64_bf16 v[28:31], v[72:75], a[168:175], v10\nds_read_b128 v[68:71], v1 offset:15360\n" \
"v_mov_b64 v[48:49], 0\nv_smfmac_f32_16x16x64_bf16 v[28:31], v[76:79], a[176:183], v10\nv_mov_b64 v[50:51], 0\nv_smfmac_f32_16x16x64_bf16 v[32:35], v[72:75], a[216:223], v10\ns_waitcnt lgkmcnt(2)\ns_barrier\ns_mov_b32 s59, 0\nds_read_b128 v[80:83], v11 offset:59392\n" \
"v_mov_b64 v[52:53], 0\nv_smfmac_f32_16x16x64_bf16 v[32:35], v[76:79], v[56:63], v10\nds_read_b128 v[56:59], v1 offset:16384\nds_read_b128 v[84:87], v11 offset:60416\nv_mov_b64 v[54:55], 0\nv_smfmac_f32_16x16x64_bf16 v[36:39], v[72:75], v[104:111], v10\ns_add_u32 m0, s57, 0\ns_add_u32 s58, s61, 0\n" \
"ds_read_b128 v[60:63], v1 offset:17408\nbuffer_load_dwordx4 v0, s[36:39], s58 offen offset:0 sc1 lds\nv_smfmac_f32_16x16x64_bf16 v[36:39], v[76:79], v[120:127], v10\nv_smfmac_f32_16x16x64_bf16 v[40:43], v[72:75], v[160:167], v10\ns_waitcnt lgkmcnt(4)\nv_smfmac_f32_16x16x64_bf16 v[40:43], v[76:79], v[64:71], v10\nds_read_b128 v[64:67], v1 offset:20480\nv_smfmac_f32_16x16x64_bf16 v[44:47], v[72:75], v[208:215], v10\n" \
"ds_read_b128 v[68:71], v1 offset:21504\nbuffer_load_dwordx4 v0, s[36:39], s58 offen offset:1024 sc1 lds\nv_smfmac_f32_16x16x64_bf16 v[44:47], v[76:79], v[224:231], v10\ns_waitcnt lgkmcnt(5)\nv_smfmac_f32_16x16x64_bf16 v[16:19], v[80:83], a[32:39], v10\ns_waitcnt lgkmcnt(3)\nds_read_b128 v[72:75], v11 offset:49152\nv_smfmac_f32_16x16x64_bf16 v[16:19], v[84:87], a[48:55], v10\n" \
"s_waitcnt lgkmcnt(3)\nbuffer_load_dwordx4 v0, s[36:39], s58 offen offset:2048 sc1 lds\nds_read_b128 v[76:79], v11 offset:50176\nv_smfmac_f32_16x16x64_bf16 v[20:23], v[80:83], v[56:63], v10\nds_read_b128 v[56:59], v1 offset:18432\nv_smfmac_f32_16x16x64_bf16 v[20:23], v[84:87], a[96:103], v10\nds_read_b128 v[60:63], v1 offset:19456\nv_cvt_pk_bf16_f32 v88, v16, v17\n" \
"v_cvt_pk_bf16_f32 v89, v18, v19\nv_and_or_b32 v88, v88, -2, v12\nv_smfmac_f32_16x16x64_bf16 v[24:27], v[80:83], a[136:143], v10\nbuffer_load_dwordx4 v0, s[36:39], s58 offen offset:3072 sc1 lds\nds_read_b128 v[16:19], v11 offset:51200\nv_smfmac_f32_16x16x64_bf16 v[24:27], v[84:87], a[152:159], v10\ns_waitcnt lgkmcnt(5)\nv_cvt_pk_bf16_f32 v90, v20, v21\n" \
"v_cvt_pk_bf16_f32 v91, v22, v23\nv_and_or_b32 v90, v90, -2, v12\nv_smfmac_f32_16x16x64_bf16 v[28:31], v[80:83], v[64:71], v10\nds_read_b128 v[64:67], v1 offset:22528\nv_permlane16_swap_b32 v89, v91\nv_permlane16_swap_b32 v88, v90\nv_smfmac_f32_16x16x64_bf16 v[28:31], v[84:87], a[200:207], v10\ns_add_u32 s63, s59, 0\n" \
"ds_read_b128 v[68:71], v1 offset:23552\nbuffer_store_dwordx4 v[88:91], v6, s[36:39], s63 offen sc1\nv_cvt_pk_bf16_f32 v92, v24, v25\nv_cvt_pk_bf16_f32 v93, v26, v27\nv_and_or_b32 v92, v92, -2, v12\nv_smfmac_f32_16x16x64_bf16 v[32:35], v[80:83], a[232:239], v10\nds_read_b128 v[20:23], v11 offset:52224\nv_smfmac_f32_16x16x64_bf16 v[32:35], v[84:87], a[248:255], v10\n" \
"s_waitcnt lgkmcnt(4)\nds_read_b128 v[24:27], v11 offset:53248\nv_cvt_pk_bf16_f32 v94, v28, v29\nv_cvt_pk_bf16_f32 v95, v30, v31\nv_and_or_b32 v94, v94, -2, v12\nv_smfmac_f32_16x16x64_bf16 v[36:39], v[80:83], v[56:63], v10\nds_read_b128 v[56:59], v1 offset:8192\nv_permlane16_swap_b32 v93, v95\n" \
"v_permlane16_swap_b32 v92, v94\nv_smfmac_f32_16x16x64_bf16 v[36:39], v[84:87], v[144:151], v10\ns_add_u32 s63, s59, 131072\nds_read_b128 v[60:63], v1 offset:9216\nbuffer_store_dwordx4 v[92:95], v6, s[36:39], s63 offen sc1\nv_cvt_pk_bf16_f32 v88, v32, v33\nv_cvt_pk_bf16_f32 v89, v34, v35\nv_and_or_b32 v88, v88, -2, v12\n" \
"v_smfmac_f32_16x16x64_bf16 v[40:43], v[80:83], v[176:183], v10\nds_read_b128 v[28:31], v11 offset:54272\nv_smfmac_f32_16x16x64_bf16 v[40:43], v[84:87], v[192:199], v10\ns_waitcnt lgkmcnt(5)\nds_read_b128 v[32:35], v11 offset:55296\nv_cvt_pk_bf16_f32 v90, v36, v37\nv_cvt_pk_bf16_f32 v91, v38, v39\nv_and_or_b32 v90, v90, -2, v12\n" \
"v_smfmac_f32_16x16x64_bf16 v[44:47], v[80:83], v[64:71], v10\nds_read_b128 v[64:67], v1 offset:16384\nv_permlane16_swap_b32 v89, v91\nv_permlane16_swap_b32 v88, v90\nv_smfmac_f32_16x16x64_bf16 v[44:47], v[84:87], v[248:255], v10\ns_add_u32 s63, s59, 4096\ns_cmp_ge_i32 s40, 63\nds_read_b128 v[68:71], v1 offset:17408\n" \
"buffer_store_dwordx4 v[88:91], v6, s[36:39], s63 offen sc1\nv_cvt_pk_bf16_f32 v92, v40, v41\nv_cvt_pk_bf16_f32 v93, v42, v43\nv_and_or_b32 v92, v92, -2, v12\ns_cbranch_scc1 .Lvu_last%=\n.Lvu_y%=:\nv_smfmac_f32_16x16x64_bf16 v[48:51], v[72:75], a[8:15], v10\ns_add_u32 s40, s40, 1\n" \
"ds_read_b128 v[36:39], v11 offset:56320\nv_smfmac_f32_16x16x64_bf16 v[52:55], v[76:79], a[8:15], v10\ns_and_b32 s44, s40, 1\ns_lshl_b32 s55, s44, 22\ns_waitcnt lgkmcnt(5)\nv_cvt_pk_bf16_f32 v94, v44, v45\nv_cvt_pk_bf16_f32 v95, v46, v47\nv_and_or_b32 v94, v94, -2, v12\n" \
"v_smfmac_f32_16x16x64_bf16 v[48:51], v[16:19], v[56:63], v10\ns_bfe_u32 s45, s40, 0x10001\nv_permlane16_swap_b32 v93, v95\nv_permlane16_swap_b32 v92, v94\nv_smfmac_f32_16x16x64_bf16 v[52:55], v[20:23], v[56:63], v10\ns_add_u32 s63, s59, 135168\nbuffer_store_dwordx4 v[92:95], v6, s[36:39], s63 offen sc1\nv_mov_b64 v[80:81], 0\n" \
"v_mov_b64 v[82:83], 0\nv_mov_b64 v[84:85], 0\nv_mov_b64 v[86:87], 0\nv_smfmac_f32_16x16x64_bf16 v[48:51], v[24:27], a[32:39], v10\ns_waitcnt lgkmcnt(4)\nds_read_b128 v[56:59], v1 offset:12288\nv_smfmac_f32_16x16x64_bf16 v[52:55], v[28:31], a[32:39], v10\ns_waitcnt lgkmcnt(4)\n" \
"ds_read_b128 v[60:63], v1 offset:13312\nv_smfmac_f32_16x16x64_bf16 v[48:51], v[32:35], a[48:55], v10\ns_waitcnt lgkmcnt(2)\nv_smfmac_f32_16x16x64_bf16 v[52:55], v[36:39], a[48:55], v10\nv_mov_b32 v13, s45\nv_smfmac_f32_16x16x64_bf16 v[80:83], v[72:75], a[64:71], v10\nv_cvt_pk_bf16_f32 v88, v48, v49\nv_smfmac_f32_16x16x64_bf16 v[84:87], v[76:79], a[64:71], v10\n" \
"v_cvt_pk_bf16_f32 v89, v50, v51\nv_smfmac_f32_16x16x64_bf16 v[80:83], v[16:19], a[80:87], v10\nv_cvt_pk_bf16_f32 v90, v52, v53\nv_smfmac_f32_16x16x64_bf16 v[84:87], v[20:23], a[80:87], v10\ns_add_u32 s60, s55, 0\nv_cvt_pk_bf16_f32 v91, v54, v55\nv_smfmac_f32_16x16x64_bf16 v[80:83], v[24:27], v[64:71], v10\nv_and_or_b32 v88, v88, -2, v13\n" \
"v_smfmac_f32_16x16x64_bf16 v[84:87], v[28:31], v[64:71], v10\ns_and_b32 s42, s40, 3\ns_lshl_b32 s42, s42, 15\nv_and_or_b32 v90, v90, -2, v13\nv_smfmac_f32_16x16x64_bf16 v[80:83], v[32:35], a[96:103], v10\nbuffer_store_dwordx4 v[88:91], v7, s[36:39], s60 offen offset:0 sc1\nv_mov_b64 v[48:49], 0\nv_mov_b64 v[50:51], 0\n" \
"v_mov_b64 v[52:53], 0\nv_mov_b64 v[54:55], 0\nv_smfmac_f32_16x16x64_bf16 v[84:87], v[36:39], a[96:103], v10\nds_read_b128 v[64:67], v1 offset:20480\nv_mov_b32 v12, s44\nv_smfmac_f32_16x16x64_bf16 v[48:51], v[72:75], a[112:119], v10\nds_read_b128 v[68:71], v1 offset:21504\nv_cvt_pk_bf16_f32 v92, v80, v81\n" \
"v_smfmac_f32_16x16x64_bf16 v[52:55], v[76:79], a[112:119], v10\ns_waitcnt lgkmcnt(2)\nv_cvt_pk_bf16_f32 v93, v82, v83\nv_smfmac_f32_16x16x64_bf16 v[48:51], v[16:19], v[56:63], v10\nv_cvt_pk_bf16_f32 v94, v84, v85\nv_smfmac_f32_16x16x64_bf16 v[52:55], v[20:23], v[56:63], v10\ns_add_u32 s60, s55, 4096\nv_cvt_pk_bf16_f32 v95, v86, v87\n" \
"v_smfmac_f32_16x16x64_bf16 v[48:51], v[24:27], a[136:143], v10\nds_read_b128 v[56:59], v1 offset:10240\nv_and_or_b32 v92, v92, -2, v13\nv_smfmac_f32_16x16x64_bf16 v[52:55], v[28:31], a[136:143], v10\nds_read_b128 v[60:63], v1 offset:11264\nv_and_or_b32 v94, v94, -2, v13\nv_smfmac_f32_16x16x64_bf16 v[48:51], v[32:35], a[152:159], v10\nbuffer_store_dwordx4 v[92:95], v7, s[36:39], s60 offen offset:0 sc1\n" \
"v_mov_b64 v[80:81], 0\nv_mov_b64 v[82:83], 0\nv_mov_b64 v[84:85], 0\nv_mov_b64 v[86:87], 0\nv_smfmac_f32_16x16x64_bf16 v[52:55], v[36:39], a[152:159], v10\ns_waitcnt vmcnt(6)\ns_barrier\nv_smfmac_f32_16x16x64_bf16 v[80:83], v[72:75], a[168:175], v10\n" \
"v_cvt_pk_bf16_f32 v88, v48, v49\nv_smfmac_f32_16x16x64_bf16 v[84:87], v[76:79], a[168:175], v10\nv_cvt_pk_bf16_f32 v89, v50, v51\nv_smfmac_f32_16x16x64_bf16 v[80:83], v[16:19], a[176:183], v10\ns_cmp_lt_u32 s40, 2\ns_cselect_b32 s54, %[fy], -2147483648\nv_cvt_pk_bf16_f32 v90, v52, v53\nv_smfmac_f32_16x16x64_bf16 v[84:87], v[20:23], a[176:183], v10\n" \
"s_add_u32 s60, s55, 16384\ns_waitcnt lgkmcnt(2)\nv_cvt_pk_bf16_f32 v91, v54, v55\nv_smfmac_f32_16x16x64_bf16 v[80:83], v[24:27], v[64:71], v10\nv_and_or_b32 v88, v88, -2, v13\nv_smfmac_f32_16x16x64_bf16 v[84:87], v[28:31], v[64:71], v10\nv_and_or_b32 v90, v90, -2, v13\nv_smfmac_f32_16x16x64_bf16 v[80:83], v[32:35], a[200:207], v10\n" \
"buffer_store_dwordx4 v[88:91], v7, s[36:39], s60 offen offset:0 sc1\nv_mov_b64 v[48:49], 0\nv_mov_b64 v[50:51], 0\nv_mov_b64 v[52:53], 0\nv_mov_b64 v[54:55], 0\nv_smfmac_f32_16x16x64_bf16 v[84:87], v[36:39], a[200:207], v10\nds_read_b128 v[64:67], v1 offset:18432\nv_smfmac_f32_16x16x64_bf16 v[48:51], v[72:75], a[216:223], v10\n" \
"ds_read_b128 v[68:71], v1 offset:19456\nv_cvt_pk_bf16_f32 v92, v80, v81\nv_smfmac_f32_16x16x64_bf16 v[52:55], v[76:79], a[216:223], v10\ns_waitcnt lgkmcnt(2)\nv_cvt_pk_bf16_f32 v93, v82, v83\nv_smfmac_f32_16x16x64_bf16 v[48:51], v[16:19], v[56:63], v10\nv_cvt_pk_bf16_f32 v94, v84, v85\nv_smfmac_f32_16x16x64_bf16 v[52:55], v[20:23], v[56:63], v10\n" \
"s_add_u32 s60, s55, 20480\nv_cvt_pk_bf16_f32 v95, v86, v87\nv_smfmac_f32_16x16x64_bf16 v[48:51], v[24:27], a[232:239], v10\nds_read_b128 v[56:59], v1 offset:14336\nv_and_or_b32 v92, v92, -2, v13\nv_smfmac_f32_16x16x64_bf16 v[52:55], v[28:31], a[232:239], v10\nds_read_b128 v[60:63], v1 offset:15360\nv_and_or_b32 v94, v94, -2, v13\n" \
"v_smfmac_f32_16x16x64_bf16 v[48:51], v[32:35], a[248:255], v10\nbuffer_store_dwordx4 v[92:95], v7, s[36:39], s60 offen offset:0 sc1\nv_mov_b64 v[80:81], 0\nv_mov_b64 v[82:83], 0\nv_mov_b64 v[84:85], 0\nv_mov_b64 v[86:87], 0\nv_smfmac_f32_16x16x64_bf16 v[52:55], v[36:39], a[248:255], v10\nv_smfmac_f32_16x16x64_bf16 v[80:83], v[72:75], v[104:111], v10\n" \
"v_cvt_pk_bf16_f32 v88, v48, v49\nv_smfmac_f32_16x16x64_bf16 v[84:87], v[76:79], v[104:111], v10\nv_cvt_pk_bf16_f32 v89, v50, v51\nv_smfmac_f32_16x16x64_bf16 v[80:83], v[16:19], v[120:127], v10\nv_cvt_pk_bf16_f32 v90, v52, v53\nv_smfmac_f32_16x16x64_bf16 v[84:87], v[20:23], v[120:127], v10\ns_add_u32 s60, s55, 0\ns_waitcnt lgkmcnt(2)\n" \
"v_cvt_pk_bf16_f32 v91, v54, v55\nv_smfmac_f32_16x16x64_bf16 v[80:83], v[24:27], v[64:71], v10\nv_and_or_b32 v88, v88, -2, v13\nv_smfmac_f32_16x16x64_bf16 v[84:87], v[28:31], v[64:71], v10\nv_and_or_b32 v90, v90, -2, v13\nv_smfmac_f32_16x16x64_bf16 v[80:83], v[32:35], v[144:151], v10\nbuffer_store_dwordx4 v[88:91], v7, s[36:39], s60 offen offset:2048 sc1\nv_mov_b64 v[48:49], 0\n" \
"v_mov_b64 v[50:51], 0\nv_mov_b64 v[52:53], 0\nv_mov_b64 v[54:55], 0\nv_smfmac_f32_16x16x64_bf16 v[84:87], v[36:39], v[144:151], v10\nds_read_b128 v[64:67], v1 offset:22528\nv_smfmac_f32_16x16x64_bf16 v[48:51], v[72:75], v[160:167], v10\nds_read_b128 v[68:71], v1 offset:23552\nv_cvt_pk_bf16_f32 v92, v80, v81\n" \
"v_smfmac_f32_16x16x64_bf16 v[52:55], v[76:79], v[160:167], v10\ns_waitcnt lgkmcnt(2)\nv_cvt_pk_bf16_f32 v93, v82, v83\nv_smfmac_f32_16x16x64_bf16 v[48:51], v[16:19], v[56:63], v10\nds_read_b128 v[40:43], v11 offset:49280\nv_cvt_pk_bf16_f32 v94, v84, v85\nv_smfmac_f32_16x16x64_bf16 v[52:55], v[20:23], v[56:63], v10\ns_add_u32 s60, s55, 4096\n" \
"v_cvt_pk_bf16_f32 v95, v86, v87\nv_smfmac_f32_16x16x64_bf16 v[48:51], v[24:27], v[176:183], v10\nds_read_b128 v[56:59], v1 offset:8192\nds_read_b128 v[44:47], v11 offset:50304\nv_and_or_b32 v92, v92, -2, v13\nv_smfmac_f32_16x16x64_bf16 v[52:55], v[28:31], v[176:183], v10\nds_read_b128 v[60:63], v1 offset:9216\nv_and_or_b32 v94, v94, -2, v13\n" \
"v_smfmac_f32_16x16x64_bf16 v[48:51], v[32:35], v[192:199], v10\nbuffer_store_dwordx4 v[92:95], v7, s[36:39], s60 offen offset:2048 sc1\nv_mov_b64 v[80:81], 0\nv_mov_b64 v[82:83], 0\nv_mov_b64 v[84:85], 0\nv_mov_b64 v[86:87], 0\nv_smfmac_f32_16x16x64_bf16 v[52:55], v[36:39], v[192:199], v10\nv_smfmac_f32_16x16x64_bf16 v[80:83], v[72:75], v[208:215], v10\n" \
"ds_read_b128 v[72:75], v11 offset:51328\nv_cvt_pk_bf16_f32 v88, v48, v49\nv_smfmac_f32_16x16x64_bf16 v[84:87], v[76:79], v[208:215], v10\nds_read_b128 v[76:79], v11 offset:52352\nv_cvt_pk_bf16_f32 v89, v50, v51\nv_smfmac_f32_16x16x64_bf16 v[80:83], v[16:19], v[224:231], v10\nds_read_b128 v[16:19], v11 offset:53376\nv_cvt_pk_bf16_f32 v90, v52, v53\n" \
"v_smfmac_f32_16x16x64_bf16 v[84:87], v[20:23], v[224:231], v10\ns_add_u32 s60, s55, 16384\ns_waitcnt lgkmcnt(7)\nds_read_b128 v[20:23], v11 offset:54400\nv_cvt_pk_bf16_f32 v91, v54, v55\nv_smfmac_f32_16x16x64_bf16 v[80:83], v[24:27], v[64:71], v10\nds_read_b128 v[24:27], v11 offset:55424\nv_and_or_b32 v88, v88, -2, v13\n" \
"v_smfmac_f32_16x16x64_bf16 v[84:87], v[28:31], v[64:71], v10\nds_read_b128 v[28:31], v11 offset:56448\nv_and_or_b32 v90, v90, -2, v13\nv_smfmac_f32_16x16x64_bf16 v[80:83], v[32:35], v[248:255], v10\nbuffer_store_dwordx4 v[88:91], v7, s[36:39], s60 offen offset:2048 sc1\nv_mov_b64 v[48:49], 0\nv_mov_b64 v[50:51], 0\nv_mov_b64 v[52:53], 0\n" \
"v_mov_b64 v[54:55], 0\nv_smfmac_f32_16x16x64_bf16 v[84:87], v[36:39], v[248:255], v10\ns_waitcnt lgkmcnt(9)\nds_read_b128 v[64:67], v1 offset:16384\nv_smfmac_f32_16x16x64_bf16 v[48:51], v[40:43], a[8:15], v10\ns_waitcnt lgkmcnt(8)\nds_read_b128 v[68:71], v1 offset:17408\nv_cvt_pk_bf16_f32 v92, v80, v81\n" \
"v_smfmac_f32_16x16x64_bf16 v[52:55], v[44:47], a[8:15], v10\ns_waitcnt lgkmcnt(7)\nv_cvt_pk_bf16_f32 v93, v82, v83\nv_smfmac_f32_16x16x64_bf16 v[48:51], v[72:75], v[56:63], v10\ns_waitcnt lgkmcnt(6)\nv_cvt_pk_bf16_f32 v94, v84, v85\nv_smfmac_f32_16x16x64_bf16 v[52:55], v[76:79], v[56:63], v10\ns_add_u32 s60, s55, 20480\n" \
"s_waitcnt lgkmcnt(5)\nv_cvt_pk_bf16_f32 v95, v86, v87\nv_smfmac_f32_16x16x64_bf16 v[48:51], v[16:19], a[32:39], v10\ns_waitcnt lgkmcnt(4)\nds_read_b128 v[56:59], v1 offset:12288\nv_and_or_b32 v92, v92, -2, v13\nv_smfmac_f32_16x16x64_bf16 v[52:55], v[20:23], a[32:39], v10\ns_waitcnt lgkmcnt(4)\n" \
"ds_read_b128 v[60:63], v1 offset:13312\nv_and_or_b32 v94, v94, -2, v13\nv_smfmac_f32_16x16x64_bf16 v[48:51], v[24:27], a[48:55], v10\ns_waitcnt lgkmcnt(4)\nbuffer_store_dwordx4 v[92:95], v7, s[36:39], s60 offen offset:2048 sc1\nv_mov_b64 v[80:81], 0\nv_mov_b64 v[82:83], 0\nv_mov_b64 v[84:85], 0\n" \
"v_mov_b64 v[86:87], 0\nv_smfmac_f32_16x16x64_bf16 v[52:55], v[28:31], a[48:55], v10\nv_smfmac_f32_16x16x64_bf16 v[80:83], v[40:43], a[64:71], v10\nv_cvt_pk_bf16_f32 v88, v48, v49\nv_smfmac_f32_16x16x64_bf16 v[84:87], v[44:47], a[64:71], v10\nv_cvt_pk_bf16_f32 v89, v50, v51\nv_smfmac_f32_16x16x64_bf16 v[80:83], v[72:75], a[80:87], v10\nv_cvt_pk_bf16_f32 v90, v52, v53\n" \
"v_smfmac_f32_16x16x64_bf16 v[84:87], v[76:79], a[80:87], v10\ns_add_u32 s60, s55, 0\ns_waitcnt lgkmcnt(2)\nv_cvt_pk_bf16_f32 v91, v54, v55\nv_smfmac_f32_16x16x64_bf16 v[80:83], v[16:19], v[64:71], v10\nv_and_or_b32 v88, v88, -2, v13\nv_smfmac_f32_16x16x64_bf16 v[84:87], v[20:23], v[64:71], v10\nv_and_or_b32 v90, v90, -2, v13\n" \
"v_smfmac_f32_16x16x64_bf16 v[80:83], v[24:27], a[96:103], v10\nbuffer_store_dwordx4 v[88:91], v7, s[36:39], s60 offen offset:512 sc1\nv_mov_b64 v[48:49], 0\nv_mov_b64 v[50:51], 0\nv_mov_b64 v[52:53], 0\nv_mov_b64 v[54:55], 0\nv_smfmac_f32_16x16x64_bf16 v[84:87], v[28:31], a[96:103], v10\nds_read_b128 v[64:67], v1 offset:20480\n" \
"v_smfmac_f32_16x16x64_bf16 v[48:51], v[40:43], a[112:119], v10\nds_read_b128 v[68:71], v1 offset:21504\nv_cvt_pk_bf16_f32 v92, v80, v81\nv_smfmac_f32_16x16x64_bf16 v[52:55], v[44:47], a[112:119], v10\ns_waitcnt lgkmcnt(2)\nv_cvt_pk_bf16_f32 v93, v82, v83\nv_smfmac_f32_16x16x64_bf16 v[48:51], v[72:75], v[56:63], v10\nv_cvt_pk_bf16_f32 v94, v84, v85\n" \
"v_smfmac_f32_16x16x64_bf16 v[52:55], v[76:79], v[56:63], v10\ns_add_u32 s60, s55, 4096\nv_cvt_pk_bf16_f32 v95, v86, v87\nv_smfmac_f32_16x16x64_bf16 v[48:51], v[16:19], a[136:143], v10\nds_read_b128 v[56:59], v1 offset:10240\nv_and_or_b32 v92, v92, -2, v13\nv_smfmac_f32_16x16x64_bf16 v[52:55], v[20:23], a[136:143], v10\nds_read_b128 v[60:63], v1 offset:11264\n" \
"v_and_or_b32 v94, v94, -2, v13\nv_smfmac_f32_16x16x64_bf16 v[48:51], v[24:27], a[152:159], v10\nbuffer_store_dwordx4 v[92:95], v7, s[36:39], s60 offen offset:512 sc1\nv_mov_b64 v[80:81], 0\nv_mov_b64 v[82:83], 0\nv_mov_b64 v[84:85], 0\nv_mov_b64 v[86:87], 0\nv_smfmac_f32_16x16x64_bf16 v[52:55], v[28:31], a[152:159], v10\n" \
"v_smfmac_f32_16x16x64_bf16 v[80:83], v[40:43], a[168:175], v10\nv_cvt_pk_bf16_f32 v88, v48, v49\nv_smfmac_f32_16x16x64_bf16 v[84:87], v[44:47], a[168:175], v10\nv_cvt_pk_bf16_f32 v89, v50, v51\nv_smfmac_f32_16x16x64_bf16 v[80:83], v[72:75], a[176:183], v10\nv_cvt_pk_bf16_f32 v90, v52, v53\nv_smfmac_f32_16x16x64_bf16 v[84:87], v[76:79], a[176:183], v10\ns_add_u32 s60, s55, 16384\n" \
"s_waitcnt lgkmcnt(2)\nv_cvt_pk_bf16_f32 v91, v54, v55\nv_smfmac_f32_16x16x64_bf16 v[80:83], v[16:19], v[64:71], v10\nv_and_or_b32 v88, v88, -2, v13\nv_smfmac_f32_16x16x64_bf16 v[84:87], v[20:23], v[64:71], v10\nv_and_or_b32 v90, v90, -2, v13\nv_smfmac_f32_16x16x64_bf16 v[80:83], v[24:27], a[200:207], v10\nbuffer_store_dwordx4 v[88:91], v7, s[36:39], s60 offen offset:512 sc1\n" \
"v_mov_b64 v[48:49], 0\nv_mov_b64 v[50:51], 0\nv_mov_b64 v[52:53], 0\nv_mov_b64 v[54:55], 0\nv_smfmac_f32_16x16x64_bf16 v[84:87], v[28:31], a[200:207], v10\nds_read_b128 v[64:67], v1 offset:18432\nv_smfmac_f32_16x16x64_bf16 v[48:51], v[40:43], a[216:223], v10\nds_read_b128 v[68:71], v1 offset:19456\n" \
"v_cvt_pk_bf16_f32 v92, v80, v81\nv_smfmac_f32_16x16x64_bf16 v[52:55], v[44:47], a[216:223], v10\ns_waitcnt lgkmcnt(2)\nv_cvt_pk_bf16_f32 v93, v82, v83\nv_smfmac_f32_16x16x64_bf16 v[48:51], v[72:75], v[56:63], v10\nv_cvt_pk_bf16_f32 v94, v84, v85\nv_smfmac_f32_16x16x64_bf16 v[52:55], v[76:79], v[56:63], v10\ns_add_u32 s60, s55, 20480\n" \
"v_cvt_pk_bf16_f32 v95, v86, v87\nv_smfmac_f32_16x16x64_bf16 v[48:51], v[16:19], a[232:239], v10\nds_read_b128 v[56:59], v1 offset:14336\nv_and_or_b32 v92, v92, -2, v13\nv_smfmac_f32_16x16x64_bf16 v[52:55], v[20:23], a[232:239], v10\nds_read_b128 v[60:63], v1 offset:15360\nv_and_or_b32 v94, v94, -2, v13\nv_smfmac_f32_16x16x64_bf16 v[48:51], v[24:27], a[248:255], v10\n" \
"buffer_store_dwordx4 v[92:95], v7, s[36:39], s60 offen offset:512 sc1\nv_mov_b64 v[80:81], 0\nv_mov_b64 v[82:83], 0\nv_mov_b64 v[84:85], 0\nv_mov_b64 v[86:87], 0\nv_smfmac_f32_16x16x64_bf16 v[52:55], v[28:31], a[248:255], v10\nv_smfmac_f32_16x16x64_bf16 v[80:83], v[40:43], v[104:111], v10\nv_cvt_pk_bf16_f32 v88, v48, v49\n" \
"v_smfmac_f32_16x16x64_bf16 v[84:87], v[44:47], v[104:111], v10\nv_cvt_pk_bf16_f32 v89, v50, v51\nv_smfmac_f32_16x16x64_bf16 v[80:83], v[72:75], v[120:127], v10\nv_cvt_pk_bf16_f32 v90, v52, v53\nv_smfmac_f32_16x16x64_bf16 v[84:87], v[76:79], v[120:127], v10\ns_add_u32 s60, s55, 0\ns_waitcnt lgkmcnt(2)\nv_cvt_pk_bf16_f32 v91, v54, v55\n" \
"v_smfmac_f32_16x16x64_bf16 v[80:83], v[16:19], v[64:71], v10\nv_and_or_b32 v88, v88, -2, v13\nv_smfmac_f32_16x16x64_bf16 v[84:87], v[20:23], v[64:71], v10\nv_and_or_b32 v90, v90, -2, v13\nv_smfmac_f32_16x16x64_bf16 v[80:83], v[24:27], v[144:151], v10\nbuffer_store_dwordx4 v[88:91], v7, s[36:39], s60 offen offset:2560 sc1\nv_mov_b64 v[48:49], 0\nv_mov_b64 v[50:51], 0\n" \
"v_mov_b64 v[52:53], 0\nv_mov_b64 v[54:55], 0\nv_smfmac_f32_16x16x64_bf16 v[84:87], v[28:31], v[144:151], v10\nds_read_b128 v[64:67], v1 offset:22528\nv_smfmac_f32_16x16x64_bf16 v[48:51], v[40:43], v[160:167], v10\nds_read_b128 v[68:71], v1 offset:23552\nv_cvt_pk_bf16_f32 v92, v80, v81\nv_smfmac_f32_16x16x64_bf16 v[52:55], v[44:47], v[160:167], v10\n" \
"s_waitcnt lgkmcnt(2)\nv_cvt_pk_bf16_f32 v93, v82, v83\nv_smfmac_f32_16x16x64_bf16 v[48:51], v[72:75], v[56:63], v10\nv_cvt_pk_bf16_f32 v94, v84, v85\nv_smfmac_f32_16x16x64_bf16 v[52:55], v[76:79], v[56:63], v10\ns_add_u32 s60, s55, 4096\nv_cvt_pk_bf16_f32 v95, v86, v87\nv_smfmac_f32_16x16x64_bf16 v[48:51], v[16:19], v[176:183], v10\n" \
"v_and_or_b32 v92, v92, -2, v13\nv_smfmac_f32_16x16x64_bf16 v[52:55], v[20:23], v[176:183], v10\nv_and_or_b32 v94, v94, -2, v13\nv_smfmac_f32_16x16x64_bf16 v[48:51], v[24:27], v[192:199], v10\nbuffer_store_dwordx4 v[92:95], v7, s[36:39], s60 offen offset:2560 sc1\nv_mov_b64 v[80:81], 0\nv_mov_b64 v[82:83], 0\nv_mov_b64 v[84:85], 0\n" \
"v_mov_b64 v[86:87], 0\nv_smfmac_f32_16x16x64_bf16 v[52:55], v[28:31], v[192:199], v10\ns_cmp_lg_u64 s[64:65], 0\ns_cbranch_scc1 .Lvu_slow%=\n.Lvu_slowr%=:\nds_read_b128 v[32:35], v2 offset:0\nv_smfmac_f32_16x16x64_bf16 v[80:83], v[40:43], v[208:215], v10\nds_read_b128 v[36:39], v2 offset:256\n" \
"v_cvt_pk_bf16_f32 v88, v48, v49\nv_smfmac_f32_16x16x64_bf16 v[84:87], v[44:47], v[208:215], v10\nv_cvt_pk_bf16_f32 v89, v50, v51\nv_smfmac_f32_16x16x64_bf16 v[80:83], v[72:75], v[224:231], v10\nds_read_b128 v[40:43], v2 offset:2048\nv_cvt_pk_bf16_f32 v90, v52, v53\nv_smfmac_f32_16x16x64_bf16 v[84:87], v[76:79], v[224:231], v10\ns_add_u32 s60, s55, 16384\n" \
"s_waitcnt lgkmcnt(3)\nds_read_b128 v[44:47], v2 offset:2304\nv_cvt_pk_bf16_f32 v91, v54, v55\nv_smfmac_f32_16x16x64_bf16 v[80:83], v[16:19], v[64:71], v10\nv_and_or_b32 v88, v88, -2, v13\nv_smfmac_f32_16x16x64_bf16 v[84:87], v[20:23], v[64:71], v10\nv_and_or_b32 v90, v90, -2, v13\nv_smfmac_f32_16x16x64_bf16 v[80:83], v[24:27], v[248:255], v10\n" \
"buffer_store_dwordx4 v[88:91], v7, s[36:39], s60 offen offset:2560 sc1\nv_mov_b64 v[48:49], 0\nv_mov_b64 v[50:51], 0\nv_mov_b64 v[52:53], 0\nv_mov_b64 v[54:55], 0\nv_smfmac_f32_16x16x64_bf16 v[84:87], v[28:31], v[248:255], v10\ns_add_u32 s60, s55, 20480\ns_cmp_lt_i32 s40, 63\n" \
"v_mov_b32 v15, s40\ns_cbranch_scc1 .Lvu_tok%=\nv_cvt_pk_bf16_f32 v92, v80, v81\nv_cvt_pk_bf16_f32 v93, v82, v83\ns_nop 1\nv_cvt_pk_bf16_f32 v94, v84, v85\nv_cvt_pk_bf16_f32 v95, v86, v87\nv_and_or_b32 v92, v92, -2, v13\n" \
"v_and_or_b32 v94, v94, -2, v13\nbuffer_store_dwordx4 v[92:95], v7, s[36:39], s60 offen offset:2560 sc1\ns_mov_b32 s61, -2147483648\nv_add_u32 v14, s57, v0\ns_waitcnt lgkmcnt(0)\n.Lvu_nov%=:\ns_add_u32 m0, s57, 16384\ns_nop 0\n" \
"buffer_load_dwordx4 v3, s[36:39], s42 offen offset:0 sc1 lds\nbuffer_load_dwordx4 v3, s[36:39], s42 offen offset:1024 sc1 lds\nbuffer_load_dwordx4 v3, s[36:39], s42 offen offset:2048 sc1 lds\nbuffer_load_dwordx4 v3, s[36:39], s42 offen offset:3072 sc1 lds\ns_waitcnt vmcnt(0)\nds_read_b128 v[80:83], v14 offset:16384\nds_read_b128 v[84:87], v14 offset:17408\nds_read_b128 v[88:91], v14 offset:18432\n" \
"ds_read_b128 v[92:95], v14 offset:19456\ns_waitcnt lgkmcnt(2)\nv_max3_u32 v72, v80, v82, v84\ns_waitcnt lgkmcnt(1)\nv_max3_u32 v72, v72, v86, v88\ns_waitcnt lgkmcnt(0)\nv_max3_u32 v72, v72, v90, v92\nv_max_u32 v72, v72, v94\n" \
"v_cmp_eq_u32 vcc, -1, v72\ns_cbranch_vccz .Lvu_novfok%=\ns_add_u32 s43, s43, 1\ns_cmp_lt_u32 s43, 262144\ns_cbranch_scc1 .Lvu_nov%=\ns_branch .Lvu_abort%=\n.Lvu_novfok%=:\ns_waitcnt lgkmcnt(0)\n" \
"s_barrier\ns_memrealtime s[68:69]\nds_read_b128 v[56:59], v1 offset:0\nds_read_b128 v[60:63], v1 offset:1024\nds_read_b128 v[80:83], v0 offset:16384\nds_read_b128 v[84:87], v0 offset:17408\nds_read_b128 v[88:91], v0 offset:18432\nds_read_b128 v[92:95], v0 offset:19456\n" \
"ds_read_b128 v[16:19], v0 offset:20480\nds_read_b128 v[20:23], v0 offset:21504\nds_read_b128 v[32:35], v0 offset:22528\nds_read_b128 v[36:39], v0 offset:23552\ns_waitcnt lgkmcnt(0)\ns_branch .Lvu_crit%=\n.Lvu_re%=:\n.Lvu_re2%=:\n" \
"s_add_u32 m0, s57, 16384\nv_max_u32 v72, v80, v82\nv_cmp_eq_u32 vcc, -1, v72\ns_cbranch_vccz .Lvu_sk1886%=\ns_mov_b64 exec, vcc\nbuffer_load_dwordx4 v3, s[36:39], s42 offen offset:0 sc1 lds\ns_mov_b64 exec, -1\n.Lvu_sk1886%=:\n" \
"v_max_u32 v72, v84, v86\nv_cmp_eq_u32 vcc, -1, v72\ns_cbranch_vccz .Lvu_sk1893%=\ns_mov_b64 exec, vcc\nbuffer_load_dwordx4 v3, s[36:39], s42 offen offset:1024 sc1 lds\ns_mov_b64 exec, -1\n.Lvu_sk1893%=:\nv_max_u32 v72, v88, v90\n" \
"v_cmp_eq_u32 vcc, -1, v72\ns_cbranch_vccz .Lvu_sk1900%=\ns_mov_b64 exec, vcc\nbuffer_load_dwordx4 v3, s[36:39], s42 offen offset:2048 sc1 lds\ns_mov_b64 exec, -1\n.Lvu_sk1900%=:\nv_max_u32 v72, v92, v94\nv_cmp_eq_u32 vcc, -1, v72\n" \
"s_cbranch_vccz .Lvu_sk1907%=\ns_mov_b64 exec, vcc\nbuffer_load_dwordx4 v3, s[36:39], s42 offen offset:3072 sc1 lds\ns_mov_b64 exec, -1\n.Lvu_sk1907%=:\n.Lvu_skf%=:\ns_waitcnt vmcnt(0)\nds_read_b128 v[80:83], v14 offset:16384\n" \
"ds_read_b128 v[84:87], v14 offset:17408\nds_read_b128 v[88:91], v14 offset:18432\nds_read_b128 v[92:95], v14 offset:19456\ns_waitcnt lgkmcnt(2)\nv_max3_u32 v72, v80, v82, v84\ns_waitcnt lgkmcnt(1)\nv_max3_u32 v72, v72, v86, v88\ns_waitcnt lgkmcnt(0)\n" \
"v_max3_u32 v72, v72, v90, v92\nv_max_u32 v72, v72, v94\nv_cmp_eq_u32 vcc, -1, v72\ns_cbranch_vccz .Lvu_rer%=\ns_add_u32 s43, s43, 1\ns_cmp_lt_u32 s43, 262144\ns_cbranch_scc1 .Lvu_re2%=\ns_branch .Lvu_abort%=\n" \
".Lvu_rew%=:\nds_read_b128 v[80:83], v14 offset:16384\nds_read_b128 v[84:87], v14 offset:17408\nds_read_b128 v[88:91], v14 offset:18432\nds_read_b128 v[92:95], v14 offset:19456\ns_waitcnt lgkmcnt(0)\ns_branch .Lvu_rer%=\n.Lvu_slow%=:\n" \
"s_cmp_ge_i32 s40, 63\ns_cbranch_scc1 .Lvu_slowr%=\n.Lvu_f1%=:\nbuffer_load_dword v14, v8, s[36:39], s56 offen sc1\ns_waitcnt vmcnt(0)\nv_cmp_ge_i32 vcc, s40, v14\ns_cbranch_vccz .Lvu_f1fok%=\ns_add_u32 s43, s43, 1\n" \
"s_cmp_lt_u32 s43, 262144\ns_cbranch_scc1 .Lvu_f1%=\ns_branch .Lvu_abort%=\n.Lvu_f1fok%=:\ns_add_u32 m0, s57, 0\ns_add_u32 s58, s41, 0\nbuffer_load_dwordx4 v0, s[36:39], s58 offen offset:0 sc1 lds\nbuffer_load_dwordx4 v0, s[36:39], s58 offen offset:1024 sc1 lds\n" \
"buffer_load_dwordx4 v0, s[36:39], s58 offen offset:2048 sc1 lds\nbuffer_load_dwordx4 v0, s[36:39], s58 offen offset:3072 sc1 lds\ns_add_u32 m0, s57, 32768\ns_add_u32 s58, s41, 32768\nbuffer_load_dwordx4 v0, s[36:39], s58 offen offset:0 sc1 lds\nbuffer_load_dwordx4 v0, s[36:39], s58 offen offset:1024 sc1 lds\nbuffer_load_dwordx4 v0, s[36:39], s58 offen offset:2048 sc1 lds\nbuffer_load_dwordx4 v0, s[36:39], s58 offen offset:3072 sc1 lds\n" \
"s_waitcnt vmcnt(0)\ns_barrier\ns_branch .Lvu_slowr%=\n.Lvu_first%=:\ns_nop 7\nv_cvt_pk_bf16_f32 v76, v24, v25\nv_cvt_pk_bf16_f32 v77, v26, v27\nv_cvt_pk_bf16_f32 v78, v28, v29\n" \
"v_cvt_pk_bf16_f32 v79, v30, v31\nds_write2st64_b64 v9, v[76:77], v[78:79] offset0:2 offset1:10\ns_waitcnt vmcnt(0) lgkmcnt(0)\ns_barrier\nds_read_b128 v[56:59], v1 offset:8192\nds_read_b128 v[60:63], v1 offset:9216\nds_read_b128 v[64:67], v1 offset:16384\nds_read_b128 v[68:71], v1 offset:17408\n" \
"ds_read_b128 v[72:75], v11 offset:49152\nds_read_b128 v[76:79], v11 offset:50176\nds_read_b128 v[16:19], v11 offset:51200\nds_read_b128 v[20:23], v11 offset:52224\nds_read_b128 v[24:27], v11 offset:53248\nds_read_b128 v[28:31], v11 offset:54272\nds_read_b128 v[32:35], v11 offset:55296\nds_read_b128 v[36:39], v11 offset:56320\n" \
"s_waitcnt lgkmcnt(0)\ns_add_u32 m0, s57, 0\ns_add_u32 s58, s61, 0\nbuffer_load_dwordx4 v0, s[36:39], s58 offen offset:0 sc1 lds\nbuffer_load_dwordx4 v0, s[36:39], s58 offen offset:1024 sc1 lds\nbuffer_load_dwordx4 v0, s[36:39], s58 offen offset:2048 sc1 lds\nbuffer_load_dwordx4 v0, s[36:39], s58 offen offset:3072 sc1 lds\ns_mov_b32 s59, -2147483648\n" \
"buffer_store_dword v14, off, s[36:39], s59\nbuffer_store_dword v14, off, s[36:39], s59\nbuffer_store_dword v14, off, s[36:39], s59\ns_branch .Lvu_y%=\n.Lvu_last%=:\ns_nop 7\nv_cvt_pk_bf16_f32 v94, v44, v45\nv_cvt_pk_bf16_f32 v95, v46, v47\n" \
"v_and_or_b32 v94, v94, -2, v12\ns_nop 0\nv_permlane16_swap_b32 v93, v95\nv_permlane16_swap_b32 v92, v94\ns_add_u32 s63, s59, 135168\nbuffer_store_dwordx4 v[92:95], v6, s[36:39], s63 offen sc1\ns_branch .Lvu_end%=\n.Lvu_abort%=:\n" \
"s_mov_b64 exec, -1\nv_mov_b32 v16, 0\ns_movk_i32 s44, 6400\nbuffer_store_dword v16, off, s[36:39], s44 sc1\n.Lvu_end%=:\ns_waitcnt vmcnt(0) lgkmcnt(0)\n" \
""
#define RD_ASM \
"v_mbcnt_lo_u32_b32 v255, -1, 0\nv_mbcnt_hi_u32_b32 v255, -1, v255\nv_lshlrev_b32 v255, 2, v255\nv_add_u32 v255, %[tb], v255\nds_read_b32 v196, v255 offset:0\nds_read_b32 v197, v255 offset:256\nds_read_b32 v198, v255 offset:512\nds_read_b32 v199, v255 offset:768\n" \
"ds_read_b32 v200, v255 offset:1024\nds_read_b32 v201, v255 offset:1280\nds_read_b32 v202, v255 offset:1536\nds_read_b32 v203, v255 offset:1792\nds_read_b32 v204, v255 offset:2048\nds_read_b32 v205, v255 offset:2304\nds_read_b32 v206, v255 offset:2560\nds_read_b32 v207, v255 offset:2816\n" \
"s_waitcnt lgkmcnt(0)\nv_accvgpr_write_b32 a32, v206\nv_accvgpr_write_b32 a33, v207\ns_mov_b32 s40, 0\ns_mov_b32 s43, 0\ns_add_u32 s57, %[su], 4096\ns_add_u32 s56, %[sp], 4096\ns_mov_b32 s41, %[sy]\n" \
".Lrd_yflag%=:\nv_accvgpr_read_b32 v206, a32\nbuffer_load_dword v208, v206, %[rs], 0 offen sc1\ns_waitcnt vmcnt(0)\nv_cmp_gt_i32 vcc, s40, v208\ns_cbranch_vccz .Lrd_yld%=\ns_add_u32 s43, s43, 1\ns_cmp_lt_u32 s43, 262144\n" \
"s_cbranch_scc1 .Lrd_yflag%=\ns_branch .Lrd_abort%=\n.Lrd_yld%=:\ns_add_u32 s60, s41, 0\nbuffer_load_dwordx4 v[0:3], v196, %[rs], s60 offen offset:0 sc1\ns_add_u32 s60, s41, 0\nbuffer_load_dwordx4 v[32:35], v196, %[rs], s60 offen offset:1024 sc1\ns_add_u32 s60, s41, 0\n" \
"buffer_load_dwordx4 v[64:67], v196, %[rs], s60 offen offset:2048 sc1\ns_add_u32 s60, s41, 131072\nbuffer_load_dwordx4 v[4:7], v196, %[rs], s60 offen offset:0 sc1\ns_add_u32 s60, s41, 131072\nbuffer_load_dwordx4 v[36:39], v196, %[rs], s60 offen offset:1024 sc1\ns_add_u32 s60, s41, 131072\nbuffer_load_dwordx4 v[68:71], v196, %[rs], s60 offen offset:2048 sc1\ns_add_u32 s60, s41, 262144\n" \
"buffer_load_dwordx4 v[8:11], v196, %[rs], s60 offen offset:0 sc1\ns_add_u32 s60, s41, 262144\nbuffer_load_dwordx4 v[40:43], v196, %[rs], s60 offen offset:1024 sc1\ns_add_u32 s60, s41, 262144\nbuffer_load_dwordx4 v[72:75], v196, %[rs], s60 offen offset:2048 sc1\ns_add_u32 s60, s41, 393216\nbuffer_load_dwordx4 v[12:15], v196, %[rs], s60 offen offset:0 sc1\ns_add_u32 s60, s41, 393216\n" \
"buffer_load_dwordx4 v[44:47], v196, %[rs], s60 offen offset:1024 sc1\ns_add_u32 s60, s41, 393216\nbuffer_load_dwordx4 v[76:79], v196, %[rs], s60 offen offset:2048 sc1\ns_add_u32 s60, s41, 524288\nbuffer_load_dwordx4 v[16:19], v196, %[rs], s60 offen offset:0 sc1\ns_add_u32 s60, s41, 524288\nbuffer_load_dwordx4 v[48:51], v196, %[rs], s60 offen offset:1024 sc1\ns_add_u32 s60, s41, 524288\n" \
"buffer_load_dwordx4 v[80:83], v196, %[rs], s60 offen offset:2048 sc1\ns_add_u32 s60, s41, 655360\nbuffer_load_dwordx4 v[20:23], v196, %[rs], s60 offen offset:0 sc1\ns_add_u32 s60, s41, 655360\nbuffer_load_dwordx4 v[52:55], v196, %[rs], s60 offen offset:1024 sc1\ns_add_u32 s60, s41, 655360\nbuffer_load_dwordx4 v[84:87], v196, %[rs], s60 offen offset:2048 sc1\ns_add_u32 s60, s41, 786432\n" \
"buffer_load_dwordx4 v[24:27], v196, %[rs], s60 offen offset:0 sc1\ns_add_u32 s60, s41, 786432\nbuffer_load_dwordx4 v[56:59], v196, %[rs], s60 offen offset:1024 sc1\ns_add_u32 s60, s41, 786432\nbuffer_load_dwordx4 v[88:91], v196, %[rs], s60 offen offset:2048 sc1\ns_add_u32 s60, s41, 917504\nbuffer_load_dwordx4 v[28:31], v196, %[rs], s60 offen offset:0 sc1\ns_add_u32 s60, s41, 917504\n" \
"buffer_load_dwordx4 v[60:63], v196, %[rs], s60 offen offset:1024 sc1\ns_add_u32 s60, s41, 917504\nbuffer_load_dwordx4 v[92:95], v196, %[rs], s60 offen offset:2048 sc1\ns_add_u32 s60, s41, 0\nbuffer_load_dwordx4 v[96:99], v196, %[rs], s60 offen offset:3072 sc1\ns_add_u32 s60, s41, 131072\nbuffer_load_dwordx4 v[100:103], v196, %[rs], s60 offen offset:3072 sc1\ns_add_u32 s60, s41, 262144\n" \
"buffer_load_dwordx4 v[104:107], v196, %[rs], s60 offen offset:3072 sc1\ns_add_u32 s60, s41, 393216\nbuffer_load_dwordx4 v[108:111], v196, %[rs], s60 offen offset:3072 sc1\ns_add_u32 s60, s41, 524288\nbuffer_load_dwordx4 v[112:115], v196, %[rs], s60 offen offset:3072 sc1\ns_add_u32 s60, s41, 655360\nbuffer_load_dwordx4 v[116:119], v196, %[rs], s60 offen offset:3072 sc1\ns_add_u32 s60, s41, 786432\n" \
"buffer_load_dwordx4 v[120:123], v196, %[rs], s60 offen offset:3072 sc1\ns_add_u32 s60, s41, 917504\nbuffer_load_dwordx4 v[124:127], v196, %[rs], s60 offen offset:3072 sc1\ns_mov_b64 exec, -1\nv_accvgpr_read_b32 v212, a33\nbuffer_load_dwordx2 v[214:215], v212, %[rs], 0 offen sc1\ns_waitcnt vmcnt(0)\ns_branch .Lrd_ychk%=\n" \
".Lrd_u2%=:\nbuffer_load_dwordx4 v[136:139], v196, %[rs], %[su] offen offset:2048 sc1\nbuffer_load_dwordx4 v[140:143], v196, %[rs], %[su] offen offset:3072 sc1\ns_add_u32 s43, s43, 1\ns_cmp_ge_u32 s43, 262144\ns_cbranch_scc1 .Lrd_abort%=\ns_waitcnt vmcnt(0)\nv_bitop3_b32 v208, v136, v138, s54 bitop3:0x7e\n" \
"v_bitop3_b32 v209, v140, v142, s54 bitop3:0x7e\nv_bitop3_b32 v208, v208, 1, v209 bitop3:0xc8\nv_cmp_ne_u32 vcc, 0, v208\ns_cbranch_vccnz .Lrd_u2%=\n.Lrd_u3%=:\nbuffer_load_dwordx4 v[128:131], v196, %[rs], %[su] offen offset:0 sc1\nbuffer_load_dwordx4 v[132:135], v196, %[rs], %[su] offen offset:1024 sc1\nbuffer_load_dwordx4 v[144:147], v196, %[rs], s57 offen offset:0 sc1\n" \
"buffer_load_dwordx4 v[148:151], v196, %[rs], s57 offen offset:1024 sc1\nbuffer_load_dwordx4 v[152:155], v196, %[rs], s57 offen offset:2048 sc1\nbuffer_load_dwordx4 v[156:159], v196, %[rs], s57 offen offset:3072 sc1\nbuffer_load_dwordx4 v[160:163], v196, %[rs], %[sp] offen offset:0 sc1\nbuffer_load_dwordx4 v[164:167], v196, %[rs], %[sp] offen offset:1024 sc1\nbuffer_load_dwordx4 v[168:171], v196, %[rs], %[sp] offen offset:2048 sc1\nbuffer_load_dwordx4 v[172:175], v196, %[rs], %[sp] offen offset:3072 sc1\nbuffer_load_dwordx4 v[176:179], v196, %[rs], s56 offen offset:0 sc1\n" \
".Lrd_ra%=:\ns_waitcnt vmcnt(5)\nv_bitop3_b32 v208, v128, v130, s54 bitop3:0x7e\nv_bitop3_b32 v209, v136, v138, s54 bitop3:0x7e\nv_bitop3_b32 v208, v208, 1, v209 bitop3:0xc8\nv_cmp_ne_u32 s[86:87], 0, v208\nv_bitop3_b32 v208, v144, v146, s54 bitop3:0x7e\nv_bitop3_b32 v209, v152, v154, s54 bitop3:0x7e\n" \
"v_bitop3_b32 v208, v208, 1, v209 bitop3:0xc8\nv_cmp_ne_u32 s[88:89], 0, v208\nv_bitop3_b32 v208, v132, v134, s54 bitop3:0x7e\nv_bitop3_b32 v209, v140, v142, s54 bitop3:0x7e\nv_bitop3_b32 v208, v208, 1, v209 bitop3:0xc8\nv_cmp_ne_u32 s[68:69], 0, v208\nv_bitop3_b32 v208, v148, v150, s54 bitop3:0x7e\nv_bitop3_b32 v209, v156, v158, s54 bitop3:0x7e\n" \
"v_bitop3_b32 v208, v208, 1, v209 bitop3:0xc8\nv_cmp_ne_u32 s[62:63], 0, v208\ns_or_b64 vcc, s[86:87], s[88:89]\ns_or_b64 vcc, vcc, s[68:69]\ns_or_b64 vcc, vcc, s[62:63]\ns_waitcnt vmcnt(4)\nv_smfmac_f32_16x16x64_bf16 v[180:183], v[160:163], v[0:7], v197\nv_bitop3_b32 v208, v160, v162, s54 bitop3:0x7e\n" \
"v_smfmac_f32_16x16x64_bf16 v[184:187], v[160:163], v[32:39], v197\nv_cmp_gt_i32 s[46:47], 0, v208\nv_smfmac_f32_16x16x64_bf16 v[188:191], v[160:163], v[64:71], v197\nv_smfmac_f32_16x16x64_bf16 v[192:195], v[160:163], v[96:103], v197\ns_or_b64 vcc, vcc, s[46:47]\ns_waitcnt vmcnt(3)\nv_smfmac_f32_16x16x64_bf16 v[180:183], v[164:167], v[8:15], v197\nv_bitop3_b32 v208, v164, v166, s54 bitop3:0x7e\n" \
"v_smfmac_f32_16x16x64_bf16 v[184:187], v[164:167], v[40:47], v197\nv_cmp_gt_i32 s[48:49], 0, v208\nv_smfmac_f32_16x16x64_bf16 v[188:191], v[164:167], v[72:79], v197\nv_smfmac_f32_16x16x64_bf16 v[192:195], v[164:167], v[104:111], v197\ns_or_b64 vcc, vcc, s[48:49]\ns_waitcnt vmcnt(2)\nv_smfmac_f32_16x16x64_bf16 v[180:183], v[168:171], v[16:23], v197\nv_bitop3_b32 v208, v168, v170, s54 bitop3:0x7e\n" \
"v_smfmac_f32_16x16x64_bf16 v[184:187], v[168:171], v[48:55], v197\nv_cmp_gt_i32 s[50:51], 0, v208\nv_smfmac_f32_16x16x64_bf16 v[188:191], v[168:171], v[80:87], v197\nv_smfmac_f32_16x16x64_bf16 v[192:195], v[168:171], v[112:119], v197\ns_or_b64 vcc, vcc, s[50:51]\ns_waitcnt vmcnt(1)\nv_smfmac_f32_16x16x64_bf16 v[180:183], v[172:175], v[24:31], v197\ns_waitcnt vmcnt(0)\n" \
"v_bitop3_b32 v208, v172, v174, s54 bitop3:0x7e\nv_smfmac_f32_16x16x64_bf16 v[180:183], v[176:179], v[128:135], v197\nv_bitop3_b32 v209, v176, v178, s54 bitop3:0x7e\nv_smfmac_f32_16x16x64_bf16 v[184:187], v[172:175], v[56:63], v197\nv_bitop3_b32 v210, v177, v179, s54 bitop3:0x7e\nv_smfmac_f32_16x16x64_bf16 v[184:187], v[176:179], v[136:143], v197\nv_cmp_gt_i32 s[52:53], 0, v208\nv_smfmac_f32_16x16x64_bf16 v[188:191], v[172:175], v[88:95], v197\n" \
"v_or_b32 v209, v209, v210\nv_smfmac_f32_16x16x64_bf16 v[188:191], v[176:179], v[144:151], v197\ns_or_b64 vcc, vcc, s[52:53]\nv_cmp_gt_i32 s[64:65], 0, v209\nv_smfmac_f32_16x16x64_bf16 v[192:195], v[172:175], v[120:127], v197\nv_smfmac_f32_16x16x64_bf16 v[192:195], v[176:179], v[152:159], v197\ns_or_b64 vcc, vcc, s[64:65]\ns_cbranch_scc1 .Lrd_rb%=\n" \
"s_add_u32 s60, s58, 16384\nv_add_f32 v180, v180, v182\nv_add_f32 v181, v181, v183\nds_write2_b32 v198, v180, v181 offset0:0 offset1:8\nbuffer_load_dwordx4 v[212:215], v196, %[rs], s60 offen offset:0 sc1\nv_add_f32 v184, v184, v186\nv_add_f32 v185, v185, v187\nds_write2_b32 v198, v184, v185 offset0:32 offset1:40\n" \
"s_add_u32 s60, s58, 0\nbuffer_load_dwordx4 v[216:219], v196, %[rs], s60 offen offset:0 sc1\nv_add_f32 v188, v188, v190\nv_add_f32 v189, v189, v191\nds_write2_b32 v198, v188, v189 offset0:64 offset1:72\ns_add_u32 s60, s58, 0\nbuffer_load_dwordx4 v[220:223], v196, %[rs], s60 offen offset:1024 sc1\nv_add_f32 v192, v192, v194\n" \
"v_add_f32 v193, v193, v195\nds_write2_b32 v198, v192, v193 offset0:96 offset1:104\ns_add_u32 s60, s58, 16384\nbuffer_load_dwordx4 v[224:227], v196, %[rs], s60 offen offset:1024 sc1\ns_waitcnt lgkmcnt(0)\ns_barrier\nds_read_b128 v[180:183], v199 offset:0\nds_read_b128 v[184:187], v199 offset:4096\n" \
"ds_read_b128 v[188:191], v199 offset:8192\nds_read_b128 v[192:195], v199 offset:12288\ns_and_b32 s44, s54, 0x80008000\ns_add_u32 s60, s58, 0\nbuffer_load_dwordx4 v[228:231], v196, %[rs], s60 offen offset:2048 sc1\ns_waitcnt lgkmcnt(2)\nv_pk_add_f32 v[182:183], v[182:183], v[186:187]\nv_pk_add_f32 v[180:181], v[180:181], v[184:185]\n" \
"s_waitcnt lgkmcnt(0)\nv_pk_add_f32 v[186:187], v[188:189], v[192:193]\nv_pk_add_f32 v[184:185], v[190:191], v[194:195]\nv_pk_add_f32 v[180:181], v[180:181], v[186:187]\nv_pk_add_f32 v[182:183], v[182:183], v[184:185]\nv_cvt_pk_bf16_f32 v180, v180, v181\nv_xor_b32 v180, s44, v180\nv_cvt_pk_bf16_f32 v181, v182, v183\n" \
"v_min_u32 v180, -2, v180\nv_xor_b32 v209, s44, v181\nv_or_b32 v208, v180, v201\nbuffer_store_dwordx2 v[208:209], v206, %[rs], 0 offen sc1\ns_add_u32 s60, s58, 0\nbuffer_load_dwordx4 v[232:235], v196, %[rs], s60 offen offset:3072 sc1\ns_add_u32 s60, s58, 16384\nbuffer_load_dwordx4 v[236:239], v196, %[rs], s60 offen offset:2048 sc1\n" \
"s_add_u32 s60, s58, 4096\nbuffer_load_dwordx4 v[240:243], v196, %[rs], s60 offen offset:0 sc1\ns_add_u32 s60, s58, 4096\nbuffer_load_dwordx4 v[244:247], v196, %[rs], s60 offen offset:1024 sc1\ns_add_u32 s60, s58, 16384\nbuffer_load_dwordx4 v[248:251], v196, %[rs], s60 offen offset:3072 sc1\ns_add_u32 s60, s58, 20480\nbuffer_load_dwordx4 v[252:255], v196, %[rs], s60 offen offset:0 sc1\n" \
"s_add_u32 s60, s58, 20480\nbuffer_load_dwordx4 v[160:163], v196, %[rs], s60 offen offset:2048 sc1\ns_xor_b32 s41, s41, 4194304\nv_mov_b32 v207, s40\ns_waitcnt vmcnt(12)\nv_bitop3_b32 v209, v212, v214, s61 bitop3:0x7e\nv_cmp_gt_i32 vcc, 0, v209\ns_cbranch_vccnz .Lrd_tre0%=\n" \
".Lrd_tl0%=:\nv_mfma_f32_16x16x16_bf16 a[0:3], v[0:1], v[212:213], 0\ns_add_u32 s60, s58, 4096\nbuffer_load_dwordx4 v[164:167], v196, %[rs], s60 offen offset:2048 sc1\nv_mfma_f32_16x16x16_bf16 a[8:11], v[32:33], v[212:213], 0\nv_mfma_f32_16x16x16_bf16 a[16:19], v[64:65], v[212:213], 0\nv_mfma_f32_16x16x16_bf16 a[24:27], v[96:97], v[212:213], 0\ns_waitcnt vmcnt(12)\n" \
"v_mfma_f32_16x16x16_bf16 a[0:3], v[4:5], v[214:215], a[0:3]\ns_add_u32 s60, s58, 4096\nbuffer_load_dwordx4 v[168:171], v196, %[rs], s60 offen offset:3072 sc1\nv_bitop3_b32 v209, v216, v218, s61 bitop3:0x7e\nv_mfma_f32_16x16x16_bf16 a[8:11], v[36:37], v[214:215], a[8:11]\ns_lshl_b32 s42, s40, 17\ns_add_u32 s42, s42, %[at]\nv_mfma_f32_16x16x16_bf16 a[16:19], v[68:69], v[214:215], a[16:19]\n" \
"v_cmp_gt_i32 vcc, 0, v209\nv_mfma_f32_16x16x16_bf16 a[24:27], v[100:101], v[214:215], a[24:27]\ns_cbranch_vccnz .Lrd_tre1%=\n.Lrd_tl1%=:\ns_waitcnt vmcnt(12)\nv_mfma_f32_16x16x32_bf16 a[4:7], v[0:3], v[216:219], 0\ns_add_u32 s60, s58, 20480\nbuffer_load_dwordx4 v[172:175], v196, %[rs], s60 offen offset:1024 sc1\n" \
"v_bitop3_b32 v209, v220, v222, s61 bitop3:0x7e\nv_mfma_f32_16x16x32_bf16 a[12:15], v[32:35], v[216:219], 0\ns_lshr_b32 s45, s42, 8\ns_and_b32 s45, s45, 0x2000\nv_mfma_f32_16x16x32_bf16 a[20:23], v[64:67], v[216:219], 0\nv_cmp_gt_i32 vcc, 0, v209\nv_mfma_f32_16x16x32_bf16 a[28:31], v[96:99], v[216:219], 0\ns_cbranch_vccnz .Lrd_tre2%=\n" \
".Lrd_tl2%=:\ns_waitcnt vmcnt(12)\nv_mfma_f32_16x16x32_bf16 a[4:7], v[4:7], v[220:223], a[4:7]\ns_add_u32 s60, s58, 20480\nbuffer_load_dwordx4 v[176:179], v196, %[rs], s60 offen offset:3072 sc1\nv_bitop3_b32 v209, v224, v226, s61 bitop3:0x7e\nv_mfma_f32_16x16x32_bf16 a[12:15], v[36:39], v[220:223], a[12:15]\ns_xor_b32 s42, s42, s45\n" \
"s_add_u32 s40, s40, 1\nv_mfma_f32_16x16x32_bf16 a[20:23], v[68:71], v[220:223], a[20:23]\nv_cmp_gt_i32 vcc, 0, v209\nv_mfma_f32_16x16x32_bf16 a[28:31], v[100:103], v[220:223], a[28:31]\ns_cbranch_vccnz .Lrd_tre3%=\n.Lrd_tl3%=:\nv_mfma_f32_16x16x16_bf16 a[0:3], v[8:9], v[224:225], a[0:3]\ns_cmp_eq_u32 s40, 1\n" \
"s_cbranch_scc1 .Lrd_y1%=\n.Lrd_y1r%=:\ns_add_u32 s60, s41, 0\nbuffer_load_dwordx4 v[0:3], v196, %[rs], s60 offen offset:0 sc1\nv_mfma_f32_16x16x16_bf16 a[8:11], v[40:41], v[224:225], a[8:11]\nv_mfma_f32_16x16x16_bf16 a[16:19], v[72:73], v[224:225], a[16:19]\nbuffer_load_dwordx4 v[212:215], v204, %[rs], s42 offen sc1\nv_mfma_f32_16x16x16_bf16 a[24:27], v[104:105], v[224:225], a[24:27]\n" \
"s_waitcnt vmcnt(14)\nv_mfma_f32_16x16x16_bf16 a[0:3], v[12:13], v[226:227], a[0:3]\ns_add_u32 s60, s41, 0\nbuffer_load_dwordx4 v[32:35], v196, %[rs], s60 offen offset:1024 sc1\nv_bitop3_b32 v209, v228, v230, s61 bitop3:0x7e\nv_mfma_f32_16x16x16_bf16 a[8:11], v[44:45], v[226:227], a[8:11]\ns_lshl_b32 s62, s55, 31\ns_or_b32 s62, s62, 1.0\n" \
"s_mov_b32 s63, s62\nv_mfma_f32_16x16x16_bf16 a[16:19], v[76:77], v[226:227], a[16:19]\nv_cmp_gt_i32 vcc, 0, v209\nv_mfma_f32_16x16x16_bf16 a[24:27], v[108:109], v[226:227], a[24:27]\ns_cbranch_vccnz .Lrd_tre4%=\n.Lrd_tl4%=:\ns_waitcnt vmcnt(13)\nv_mfma_f32_16x16x32_bf16 a[4:7], v[8:11], v[228:231], a[4:7]\n" \
"s_add_u32 s60, s41, 0\nbuffer_load_dwordx4 v[64:67], v196, %[rs], s60 offen offset:2048 sc1\nv_bitop3_b32 v209, v232, v234, s61 bitop3:0x7e\nv_mfma_f32_16x16x32_bf16 a[12:15], v[40:43], v[228:231], a[12:15]\nv_mfma_f32_16x16x32_bf16 a[20:23], v[72:75], v[228:231], a[20:23]\nv_cmp_gt_i32 vcc, 0, v209\nv_mfma_f32_16x16x32_bf16 a[28:31], v[104:107], v[228:231], a[28:31]\ns_cbranch_vccnz .Lrd_tre5%=\n" \
".Lrd_tl5%=:\ns_waitcnt vmcnt(13)\nv_mfma_f32_16x16x32_bf16 a[4:7], v[12:15], v[232:235], a[4:7]\ns_add_u32 s60, s41, 131072\nbuffer_load_dwordx4 v[4:7], v196, %[rs], s60 offen offset:0 sc1\nv_bitop3_b32 v209, v236, v238, s61 bitop3:0x7e\nv_mfma_f32_16x16x32_bf16 a[12:15], v[44:47], v[232:235], a[12:15]\nv_mfma_f32_16x16x32_bf16 a[20:23], v[76:79], v[232:235], a[20:23]\n" \
"v_cmp_gt_i32 vcc, 0, v209\nv_mfma_f32_16x16x32_bf16 a[28:31], v[108:111], v[232:235], a[28:31]\ns_cbranch_vccnz .Lrd_tre6%=\n.Lrd_tl6%=:\nv_mfma_f32_16x16x16_bf16 a[0:3], v[16:17], v[236:237], a[0:3]\ns_add_u32 s60, s41, 131072\nbuffer_load_dwordx4 v[36:39], v196, %[rs], s60 offen offset:1024 sc1\nv_mfma_f32_16x16x16_bf16 a[8:11], v[48:49], v[236:237], a[8:11]\n" \
"v_mfma_f32_16x16x16_bf16 a[16:19], v[80:81], v[236:237], a[16:19]\nv_mfma_f32_16x16x16_bf16 a[24:27], v[112:113], v[236:237], a[24:27]\ns_waitcnt vmcnt(14)\nv_mfma_f32_16x16x16_bf16 a[0:3], v[20:21], v[238:239], a[0:3]\ns_add_u32 s60, s41, 131072\nbuffer_load_dwordx4 v[68:71], v196, %[rs], s60 offen offset:2048 sc1\nv_bitop3_b32 v209, v240, v242, s61 bitop3:0x7e\nv_mfma_f32_16x16x16_bf16 a[8:11], v[52:53], v[238:239], a[8:11]\n" \
"v_mfma_f32_16x16x16_bf16 a[16:19], v[84:85], v[238:239], a[16:19]\nv_cmp_gt_i32 vcc, 0, v209\nv_mfma_f32_16x16x16_bf16 a[24:27], v[116:117], v[238:239], a[24:27]\ns_cbranch_vccnz .Lrd_tre7%=\n.Lrd_tl7%=:\ns_waitcnt vmcnt(14)\nv_mfma_f32_16x16x32_bf16 a[4:7], v[16:19], v[240:243], a[4:7]\ns_add_u32 s60, s41, 262144\n" \
"buffer_load_dwordx4 v[8:11], v196, %[rs], s60 offen offset:0 sc1\nv_bitop3_b32 v209, v244, v246, s61 bitop3:0x7e\nv_mfma_f32_16x16x32_bf16 a[12:15], v[48:51], v[240:243], a[12:15]\nv_mfma_f32_16x16x32_bf16 a[20:23], v[80:83], v[240:243], a[20:23]\nv_cmp_gt_i32 vcc, 0, v209\nv_mfma_f32_16x16x32_bf16 a[28:31], v[112:115], v[240:243], a[28:31]\ns_cbranch_vccnz .Lrd_tre8%=\n.Lrd_tl8%=:\n" \
"s_waitcnt vmcnt(14)\nv_mfma_f32_16x16x32_bf16 a[4:7], v[20:23], v[244:247], a[4:7]\ns_add_u32 s60, s41, 262144\nbuffer_load_dwordx4 v[40:43], v196, %[rs], s60 offen offset:1024 sc1\nv_bitop3_b32 v209, v248, v250, s61 bitop3:0x7e\nv_mfma_f32_16x16x32_bf16 a[12:15], v[52:55], v[244:247], a[12:15]\nv_mfma_f32_16x16x32_bf16 a[20:23], v[84:87], v[244:247], a[20:23]\nv_cmp_gt_i32 vcc, 0, v209\n" \
"v_mfma_f32_16x16x32_bf16 a[28:31], v[116:119], v[244:247], a[28:31]\ns_cbranch_vccnz .Lrd_tre9%=\n.Lrd_tl9%=:\nv_mfma_f32_16x16x16_bf16 a[0:3], v[24:25], v[248:249], a[0:3]\ns_add_u32 s60, s41, 262144\nbuffer_load_dwordx4 v[72:75], v196, %[rs], s60 offen offset:2048 sc1\nv_mfma_f32_16x16x16_bf16 a[8:11], v[56:57], v[248:249], a[8:11]\nv_mfma_f32_16x16x16_bf16 a[16:19], v[88:89], v[248:249], a[16:19]\n" \
"v_mfma_f32_16x16x16_bf16 a[24:27], v[120:121], v[248:249], a[24:27]\ns_waitcnt vmcnt(15)\nv_mfma_f32_16x16x16_bf16 a[0:3], v[28:29], v[250:251], a[0:3]\ns_add_u32 s60, s41, 393216\nbuffer_load_dwordx4 v[12:15], v196, %[rs], s60 offen offset:0 sc1\nv_bitop3_b32 v209, v252, v254, s61 bitop3:0x7e\nv_mfma_f32_16x16x16_bf16 a[8:11], v[60:61], v[250:251], a[8:11]\nv_bitop3_b32 v210, v253, v255, s61 bitop3:0x7e\n" \
"v_or_b32 v209, v209, v210\nv_mfma_f32_16x16x16_bf16 a[16:19], v[92:93], v[250:251], a[16:19]\nv_cmp_gt_i32 vcc, 0, v209\nv_mfma_f32_16x16x16_bf16 a[24:27], v[124:125], v[250:251], a[24:27]\ns_cbranch_vccnz .Lrd_tre10%=\n.Lrd_tl10%=:\ns_waitcnt vmcnt(15)\nv_mfma_f32_16x16x32_bf16 a[0:3], v[128:131], v[252:255], a[0:3]\n" \
"s_add_u32 s60, s41, 393216\nbuffer_load_dwordx4 v[44:47], v196, %[rs], s60 offen offset:1024 sc1\nv_bitop3_b32 v209, v160, v162, s61 bitop3:0x7e\nv_mfma_f32_16x16x32_bf16 a[8:11], v[136:139], v[252:255], a[8:11]\nv_bitop3_b32 v210, v161, v163, s61 bitop3:0x7e\nv_or_b32 v209, v209, v210\nv_mfma_f32_16x16x32_bf16 a[16:19], v[144:147], v[252:255], a[16:19]\nv_cmp_gt_i32 vcc, 0, v209\n" \
"v_mfma_f32_16x16x32_bf16 a[24:27], v[152:155], v[252:255], a[24:27]\ns_cbranch_vccnz .Lrd_tre11%=\n.Lrd_tl11%=:\ns_waitcnt vmcnt(15)\nv_mfma_f32_16x16x32_bf16 a[0:3], v[132:135], v[160:163], a[0:3]\ns_add_u32 s60, s41, 393216\nbuffer_load_dwordx4 v[76:79], v196, %[rs], s60 offen offset:2048 sc1\nv_bitop3_b32 v209, v164, v166, s61 bitop3:0x7e\n" \
"v_mfma_f32_16x16x32_bf16 a[8:11], v[140:143], v[160:163], a[8:11]\nv_mfma_f32_16x16x32_bf16 a[16:19], v[148:151], v[160:163], a[16:19]\nv_cmp_gt_i32 vcc, 0, v209\nv_mfma_f32_16x16x32_bf16 a[24:27], v[156:159], v[160:163], a[24:27]\ns_cbranch_vccnz .Lrd_tre12%=\n.Lrd_tl12%=:\ns_waitcnt vmcnt(15)\nv_mfma_f32_16x16x32_bf16 a[4:7], v[24:27], v[164:167], a[4:7]\n" \
"s_add_u32 s60, s41, 524288\nbuffer_load_dwordx4 v[16:19], v196, %[rs], s60 offen offset:0 sc1\nv_bitop3_b32 v209, v168, v170, s61 bitop3:0x7e\nv_mfma_f32_16x16x32_bf16 a[12:15], v[56:59], v[164:167], a[12:15]\nv_mfma_f32_16x16x32_bf16 a[20:23], v[88:91], v[164:167], a[20:23]\nds_write_b128 v202, a[0:3] offset:0\nv_cmp_gt_i32 vcc, 0, v209\nv_mfma_f32_16x16x32_bf16 a[28:31], v[120:123], v[164:167], a[28:31]\n" \
"s_cbranch_vccnz .Lrd_tre13%=\n.Lrd_tl13%=:\ns_waitcnt vmcnt(15)\nv_mfma_f32_16x16x32_bf16 a[4:7], v[28:31], v[168:171], a[4:7]\ns_add_u32 s60, s41, 524288\nbuffer_load_dwordx4 v[48:51], v196, %[rs], s60 offen offset:1024 sc1\nv_bitop3_b32 v209, v172, v174, s61 bitop3:0x7e\nv_mfma_f32_16x16x32_bf16 a[12:15], v[60:63], v[168:171], a[12:15]\n" \
"v_bitop3_b32 v210, v173, v175, s61 bitop3:0x7e\nv_or_b32 v209, v209, v210\nv_mfma_f32_16x16x32_bf16 a[20:23], v[92:95], v[168:171], a[20:23]\nds_write_b128 v202, a[8:11] offset:2048\nv_cmp_gt_i32 vcc, 0, v209\nv_mfma_f32_16x16x32_bf16 a[28:31], v[124:127], v[168:171], a[28:31]\ns_cbranch_vccnz .Lrd_tre14%=\n.Lrd_tl14%=:\n" \
"s_waitcnt vmcnt(15)\nv_mfma_f32_16x16x32_bf16 a[4:7], v[128:131], v[172:175], a[4:7]\ns_add_u32 s60, s41, 524288\nbuffer_load_dwordx4 v[80:83], v196, %[rs], s60 offen offset:2048 sc1\nv_bitop3_b32 v209, v176, v178, s61 bitop3:0x7e\nv_mfma_f32_16x16x32_bf16 a[12:15], v[136:139], v[172:175], a[12:15]\nv_bitop3_b32 v210, v177, v179, s61 bitop3:0x7e\nv_or_b32 v209, v209, v210\n" \
"v_mfma_f32_16x16x32_bf16 a[20:23], v[144:147], v[172:175], a[20:23]\nds_write_b128 v202, a[16:19] offset:4096\nv_cmp_gt_i32 vcc, 0, v209\nv_mfma_f32_16x16x32_bf16 a[28:31], v[152:155], v[172:175], a[28:31]\ns_cbranch_vccnz .Lrd_tre15%=\n.Lrd_tl15%=:\nv_mfma_f32_16x16x32_bf16 a[4:7], v[132:135], v[176:179], a[4:7]\ns_add_u32 s60, s41, 655360\n" \
"s_waitcnt vmcnt(14)\nbuffer_load_dwordx4 v[20:23], v196, %[rs], s60 offen offset:0 sc1\nv_lshlrev_b32 v216, 16, v212\nv_and_b32 v217, 0xffff0000, v212\nv_mfma_f32_16x16x32_bf16 a[12:15], v[140:143], v[176:179], a[12:15]\nv_lshlrev_b32 v218, 16, v213\nv_and_b32 v219, 0xffff0000, v213\nv_mfma_f32_16x16x32_bf16 a[20:23], v[148:151], v[176:179], a[20:23]\n" \
"ds_write_b128 v202, a[24:27] offset:6144\nv_lshlrev_b32 v220, 16, v214\nv_and_b32 v221, 0xffff0000, v214\nv_mfma_f32_16x16x32_bf16 a[28:31], v[156:159], v[176:179], a[28:31]\ns_add_u32 s60, s41, 655360\nbuffer_load_dwordx4 v[52:55], v196, %[rs], s60 offen offset:1024 sc1\ns_add_u32 s60, s41, 655360\nds_write_b128 v202, a[4:7] offset:1024\n" \
"buffer_load_dwordx4 v[84:87], v196, %[rs], s60 offen offset:2048 sc1\nds_write_b128 v202, a[12:15] offset:3072\nv_lshlrev_b32 v222, 16, v215\nv_and_b32 v223, 0xffff0000, v215\ns_add_u32 s60, s41, 786432\nbuffer_load_dwordx4 v[24:27], v196, %[rs], s60 offen offset:0 sc1\nds_write_b128 v202, a[20:23] offset:5120\ns_add_u32 s60, s41, 786432\n" \
"buffer_load_dwordx4 v[56:59], v196, %[rs], s60 offen offset:1024 sc1\nds_write_b128 v202, a[28:31] offset:7168\ns_waitcnt lgkmcnt(0)\ns_barrier\nds_read_b128 v[224:227], v203 offset:0\nds_read_b128 v[228:231], v203 offset:256\nds_read_b128 v[232:235], v203 offset:8192\nds_read_b128 v[236:239], v203 offset:8448\n" \
"ds_read_b128 v[240:243], v203 offset:16384\nds_read_b128 v[244:247], v203 offset:16640\nds_read_b128 v[248:251], v203 offset:24576\nds_read_b128 v[252:255], v203 offset:24832\ns_add_u32 s60, s41, 786432\nbuffer_load_dwordx4 v[88:91], v196, %[rs], s60 offen offset:2048 sc1\ns_waitcnt lgkmcnt(4)\nv_pk_add_f32 v[224:225], v[224:225], v[232:233]\n" \
"v_pk_add_f32 v[226:227], v[226:227], v[234:235]\nv_pk_add_f32 v[228:229], v[228:229], v[236:237]\nv_pk_add_f32 v[230:231], v[230:231], v[238:239]\ns_add_u32 s60, s41, 917504\nbuffer_load_dwordx4 v[28:31], v196, %[rs], s60 offen offset:0 sc1\ns_waitcnt lgkmcnt(2)\nv_pk_add_f32 v[224:225], v[224:225], v[240:241]\nv_pk_add_f32 v[226:227], v[226:227], v[242:243]\n" \
"v_pk_add_f32 v[228:229], v[228:229], v[244:245]\nv_pk_add_f32 v[230:231], v[230:231], v[246:247]\ns_add_u32 s60, s41, 917504\nbuffer_load_dwordx4 v[60:63], v196, %[rs], s60 offen offset:1024 sc1\ns_waitcnt lgkmcnt(0)\nv_pk_add_f32 v[224:225], v[224:225], v[248:249]\nv_pk_add_f32 v[226:227], v[226:227], v[250:251]\nv_pk_add_f32 v[228:229], v[228:229], v[252:253]\n" \
"v_pk_add_f32 v[230:231], v[230:231], v[254:255]\nv_pk_fma_f32 v[224:225], v[224:225], s[62:63], v[216:217]\nv_pk_fma_f32 v[226:227], v[226:227], s[62:63], v[218:219]\nv_pk_fma_f32 v[228:229], v[228:229], s[62:63], v[220:221]\nv_pk_fma_f32 v[230:231], v[230:231], s[62:63], v[222:223]\nv_cvt_pk_bf16_f32 v208, v224, v225\nv_cvt_pk_bf16_f32 v209, v226, v227\nv_cvt_pk_bf16_f32 v210, v228, v229\n" \
"v_cvt_pk_bf16_f32 v211, v230, v231\nbuffer_store_dwordx4 v[208:211], v204, %[rs], s42 offen sc1\nv_pk_add_f32 v[232:233], v[224:225], v[226:227]\nv_pk_add_f32 v[234:235], v[228:229], v[230:231]\nv_pk_add_f32 v[232:233], v[232:233], v[234:235]\ns_add_u32 s60, s41, 917504\nbuffer_load_dwordx4 v[92:95], v196, %[rs], s60 offen offset:2048 sc1\ns_add_u32 s60, s41, 0\n" \
"buffer_load_dwordx4 v[96:99], v196, %[rs], s60 offen offset:3072 sc1\nv_add_f32 v232, v232, v233\ns_nop 1\nv_add_f32_dpp v233, v232, v232 row_ror:8 row_mask:0xf bank_mask:0xf\nv_add_f32_dpp v234, v232, v232 row_ror:8 row_mask:0xf bank_mask:0xf\ns_nop 1\nv_permlane16_swap_b32 v233, v234\nv_add_f32 v232, v233, v234\n" \
"v_add_f32 v233, v233, v234\ns_nop 1\nv_permlane32_swap_b32 v232, v233\nv_add_f32 v232, v232, v233\nv_mul_f32 v232, 0x3c800000, v232\nv_pk_add_f32 v[224:225], v[224:225], v[232:233] op_sel_hi:[1,0] neg_lo:[0,1] neg_hi:[0,1]\nv_pk_add_f32 v[226:227], v[226:227], v[232:233] op_sel_hi:[1,0] neg_lo:[0,1] neg_hi:[0,1]\nv_pk_add_f32 v[228:229], v[228:229], v[232:233] op_sel_hi:[1,0] neg_lo:[0,1] neg_hi:[0,1]\n" \
"v_pk_add_f32 v[230:231], v[230:231], v[232:233] op_sel_hi:[1,0] neg_lo:[0,1] neg_hi:[0,1]\ns_add_u32 s60, s41, 131072\nbuffer_load_dwordx4 v[100:103], v196, %[rs], s60 offen offset:3072 sc1\ns_add_u32 s60, s41, 262144\nbuffer_load_dwordx4 v[104:107], v196, %[rs], s60 offen offset:3072 sc1\nv_pk_mul_f32 v[236:237], v[224:225], v[224:225]\nv_pk_mul_f32 v[238:239], v[226:227], v[226:227]\nv_pk_fma_f32 v[236:237], v[228:229], v[228:229], v[236:237]\n" \
"v_pk_fma_f32 v[238:239], v[230:231], v[230:231], v[238:239]\nv_pk_add_f32 v[236:237], v[236:237], v[238:239]\nv_add_f32 v236, v236, v237\ns_nop 1\nv_add_f32_dpp v237, v236, v236 row_ror:8 row_mask:0xf bank_mask:0xf\nv_add_f32_dpp v238, v236, v236 row_ror:8 row_mask:0xf bank_mask:0xf\ns_nop 1\nv_permlane16_swap_b32 v237, v238\n" \
"v_add_f32 v236, v237, v238\nv_add_f32 v237, v237, v238\ns_nop 1\nv_permlane32_swap_b32 v236, v237\nv_add_f32 v236, v236, v237\nv_mov_b32 v233, v236\nbuffer_store_dwordx2 v[232:233], v205, %[rs], s42 offen sc1\ns_add_u32 s60, s41, 393216\n" \
"buffer_load_dwordx4 v[108:111], v196, %[rs], s60 offen offset:3072 sc1\nv_accvgpr_read_b32 v212, a33\nbuffer_load_dwordx2 v[214:215], v212, %[rs], 0 offen sc1\ns_add_u32 s60, s41, 524288\nbuffer_load_dwordx4 v[112:115], v196, %[rs], s60 offen offset:3072 sc1\ns_add_u32 s60, s41, 655360\nbuffer_load_dwordx4 v[116:119], v196, %[rs], s60 offen offset:3072 sc1\ns_add_u32 s60, s41, 786432\n" \
"buffer_load_dwordx4 v[120:123], v196, %[rs], s60 offen offset:3072 sc1\ns_add_u32 s60, s41, 917504\nbuffer_load_dwordx4 v[124:127], v196, %[rs], s60 offen offset:3072 sc1\ns_waitcnt vmcnt(6)\nbuffer_store_dword v207, off, %[rs], %[fa] sc1\ns_cmp_ge_u32 s40, 64\ns_cbranch_scc1 .Lrd_end%=\n.Lrd_ychk%=:\n" \
"s_and_b32 s44, s40, 1\ns_bfe_u32 s55, s40, 0x10001\ns_mul_i32 s54, s44, 0x80008001\ns_mul_i32 s61, s55, 0x80008000\ns_lshl_b32 s58, s44, 17\ns_add_u32 s58, s58, %[st]\ns_add_u32 s44, s40, 1\ns_and_b32 s44, s44, 3\n" \
"s_lshl_b32 s44, s44, 15\nv_xor_b32 v206, s44, v200\nv_mov_b64 v[180:181], 0\nv_mov_b64 v[182:183], 0\nv_mov_b64 v[184:185], 0\nv_mov_b64 v[186:187], 0\nv_mov_b64 v[188:189], 0\nv_mov_b64 v[190:191], 0\n" \
"v_mov_b64 v[192:193], 0\nv_mov_b64 v[194:195], 0\ns_waitcnt vmcnt(5)\n.Lrd_sg0%=:\nv_cmp_ne_u32 vcc, s40, v214\ns_cbranch_vccnz .Lrd_sg1%=\nv_max_u32_dpp v215, v215, v215 quad_perm:[1,0,3,2] row_mask:0xf bank_mask:0xf\ns_nop 1\n" \
"v_max_u32_dpp v215, v215, v215 quad_perm:[2,3,0,1] row_mask:0xf bank_mask:0xf\ns_nop 1\nv_max_u32_dpp v215, v215, v215 row_half_mirror row_mask:0xf bank_mask:0xf\ns_nop 0\nv_readlane_b32 s86, v215, 0\nv_readlane_b32 s87, v215, 8\ns_add_u32 s86, s86, %[du]\n.Lrd_tw622%=:\n" \
"s_memrealtime s[68:69]\ns_waitcnt lgkmcnt(0)\ns_sub_u32 s44, s86, s68\ns_add_u32 s44, s44, 109\ns_cmp_lt_u32 s44, 250\ns_cbranch_scc1 .Lrd_tw622%=\nbuffer_load_dwordx4 v[128:131], v196, %[rs], %[su] offen offset:0 sc1\nbuffer_load_dwordx4 v[132:135], v196, %[rs], %[su] offen offset:1024 sc1\n" \
"buffer_load_dwordx4 v[136:139], v196, %[rs], %[su] offen offset:2048 sc1\nbuffer_load_dwordx4 v[140:143], v196, %[rs], %[su] offen offset:3072 sc1\n.Lrd_tw633%=:\ns_memrealtime s[68:69]\ns_waitcnt lgkmcnt(0)\ns_sub_u32 s44, s86, s68\ns_add_u32 s44, s44, 119\ns_cmp_lt_u32 s44, 250\n" \
"s_cbranch_scc1 .Lrd_tw633%=\nbuffer_load_dwordx4 v[144:147], v196, %[rs], s57 offen offset:0 sc1\nbuffer_load_dwordx4 v[148:151], v196, %[rs], s57 offen offset:1024 sc1\nbuffer_load_dwordx4 v[152:155], v196, %[rs], s57 offen offset:2048 sc1\nbuffer_load_dwordx4 v[156:159], v196, %[rs], s57 offen offset:3072 sc1\n.Lrd_tw644%=:\ns_memrealtime s[68:69]\ns_waitcnt lgkmcnt(0)\n" \
"s_sub_u32 s44, s87, s68\ns_add_u32 s44, s44, 131\ns_cmp_lt_u32 s44, 250\ns_cbranch_scc1 .Lrd_tw644%=\nbuffer_load_dwordx4 v[160:163], v196, %[rs], %[sp] offen offset:0 sc1\nbuffer_load_dwordx4 v[164:167], v196, %[rs], %[sp] offen offset:1024 sc1\nbuffer_load_dwordx4 v[168:171], v196, %[rs], %[sp] offen offset:2048 sc1\nbuffer_load_dwordx4 v[172:175], v196, %[rs], %[sp] offen offset:3072 sc1\n" \
"buffer_load_dwordx4 v[176:179], v196, %[rs], s56 offen offset:0 sc1\nv_bitop3_b32 v209, v0, v2, s55 bitop3:0x7e\nv_bitop3_b32 v210, v32, v34, s55 bitop3:0x7e\nv_or3_b32 v208, v209, v210, 0\nv_bitop3_b32 v209, v64, v66, s55 bitop3:0x7e\nv_bitop3_b32 v210, v4, v6, s55 bitop3:0x7e\nv_or3_b32 v208, v209, v210, v208\nv_bitop3_b32 v209, v36, v38, s55 bitop3:0x7e\n" \
"v_bitop3_b32 v210, v68, v70, s55 bitop3:0x7e\nv_or3_b32 v208, v209, v210, v208\nv_bitop3_b32 v209, v8, v10, s55 bitop3:0x7e\nv_bitop3_b32 v210, v40, v42, s55 bitop3:0x7e\nv_or3_b32 v208, v209, v210, v208\nv_bitop3_b32 v209, v72, v74, s55 bitop3:0x7e\nv_bitop3_b32 v210, v12, v14, s55 bitop3:0x7e\nv_or3_b32 v208, v209, v210, v208\n" \
"v_bitop3_b32 v209, v44, v46, s55 bitop3:0x7e\nv_bitop3_b32 v210, v76, v78, s55 bitop3:0x7e\nv_or3_b32 v208, v209, v210, v208\nv_bitop3_b32 v209, v16, v18, s55 bitop3:0x7e\nv_bitop3_b32 v210, v48, v50, s55 bitop3:0x7e\nv_or3_b32 v208, v209, v210, v208\nv_bitop3_b32 v209, v80, v82, s55 bitop3:0x7e\nv_bitop3_b32 v210, v20, v22, s55 bitop3:0x7e\n" \
"v_or3_b32 v208, v209, v210, v208\nv_bitop3_b32 v209, v52, v54, s55 bitop3:0x7e\nv_bitop3_b32 v210, v84, v86, s55 bitop3:0x7e\nv_or3_b32 v208, v209, v210, v208\nv_bitop3_b32 v209, v24, v26, s55 bitop3:0x7e\nv_bitop3_b32 v210, v56, v58, s55 bitop3:0x7e\nv_or3_b32 v208, v209, v210, v208\nv_bitop3_b32 v209, v88, v90, s55 bitop3:0x7e\n" \
"v_bitop3_b32 v210, v28, v30, s55 bitop3:0x7e\nv_or3_b32 v208, v209, v210, v208\nv_bitop3_b32 v209, v60, v62, s55 bitop3:0x7e\nv_bitop3_b32 v210, v92, v94, s55 bitop3:0x7e\nv_or3_b32 v208, v209, v210, v208\nv_bitop3_b32 v209, v96, v98, s55 bitop3:0x7e\nv_bitop3_b32 v210, v100, v102, s55 bitop3:0x7e\nv_or3_b32 v208, v209, v210, v208\n" \
"v_bitop3_b32 v209, v104, v106, s55 bitop3:0x7e\nv_bitop3_b32 v210, v108, v110, s55 bitop3:0x7e\nv_or3_b32 v208, v209, v210, v208\ns_waitcnt vmcnt(14)\nv_bitop3_b32 v209, v112, v114, s55 bitop3:0x7e\nv_bitop3_b32 v210, v116, v118, s55 bitop3:0x7e\nv_or3_b32 v208, v209, v210, v208\nv_bitop3_b32 v209, v120, v122, s55 bitop3:0x7e\n" \
"v_bitop3_b32 v210, v124, v126, s55 bitop3:0x7e\nv_or3_b32 v208, v209, v210, v208\nv_and_b32 v208, 1, v208\nv_cmp_ne_u32 vcc, 0, v208\ns_cbranch_vccnz .Lrd_yre%=\ns_branch .Lrd_ra%=\n.Lrd_sg1%=:\nbuffer_load_dwordx2 v[214:215], v212, %[rs], 0 offen sc1\n" \
"s_waitcnt vmcnt(0)\ns_add_u32 s43, s43, 1\ns_cmp_lt_u32 s43, 262144\ns_cbranch_scc1 .Lrd_sg0%=\ns_branch .Lrd_abort%=\n.Lrd_yre%=:\ns_mov_b64 exec, vcc\ns_add_u32 s43, s43, 1\n" \
"s_cmp_lt_u32 s43, 262144\ns_cbranch_scc1 .Lrd_yld%=\ns_branch .Lrd_abort%=\n.Lrd_rb%=:\ns_mov_b64 exec, s[46:47]\ns_cbranch_execz .Lrd_rs0%=\nbuffer_load_dwordx4 v[160:163], v196, %[rs], %[sp] offen offset:0 sc1\n.Lrd_rs0%=:\n" \
"s_mov_b64 exec, s[48:49]\ns_cbranch_execz .Lrd_rs1%=\nbuffer_load_dwordx4 v[164:167], v196, %[rs], %[sp] offen offset:1024 sc1\n.Lrd_rs1%=:\ns_mov_b64 exec, s[50:51]\ns_cbranch_execz .Lrd_rs2%=\nbuffer_load_dwordx4 v[168:171], v196, %[rs], %[sp] offen offset:2048 sc1\n.Lrd_rs2%=:\n" \
"s_mov_b64 exec, s[52:53]\ns_cbranch_execz .Lrd_rs3%=\nbuffer_load_dwordx4 v[172:175], v196, %[rs], %[sp] offen offset:3072 sc1\n.Lrd_rs3%=:\ns_mov_b64 exec, s[64:65]\ns_cbranch_execz .Lrd_rs4%=\nbuffer_load_dwordx4 v[176:179], v196, %[rs], s56 offen offset:0 sc1\n.Lrd_rs4%=:\n" \
"s_mov_b64 exec, s[86:87]\ns_cbranch_execz .Lrd_rs5%=\nbuffer_load_dwordx4 v[128:131], v196, %[rs], %[su] offen offset:0 sc1\nbuffer_load_dwordx4 v[136:139], v196, %[rs], %[su] offen offset:2048 sc1\n.Lrd_rs5%=:\ns_mov_b64 exec, s[88:89]\ns_cbranch_execz .Lrd_rs6%=\nbuffer_load_dwordx4 v[144:147], v196, %[rs], s57 offen offset:0 sc1\n" \
"buffer_load_dwordx4 v[152:155], v196, %[rs], s57 offen offset:2048 sc1\n.Lrd_rs6%=:\ns_mov_b64 exec, s[68:69]\ns_cbranch_execz .Lrd_rs7%=\nbuffer_load_dwordx4 v[132:135], v196, %[rs], %[su] offen offset:1024 sc1\nbuffer_load_dwordx4 v[140:143], v196, %[rs], %[su] offen offset:3072 sc1\n.Lrd_rs7%=:\ns_mov_b64 exec, s[62:63]\n" \
"s_cbranch_execz .Lrd_rs8%=\nbuffer_load_dwordx4 v[148:151], v196, %[rs], s57 offen offset:1024 sc1\nbuffer_load_dwordx4 v[156:159], v196, %[rs], s57 offen offset:3072 sc1\n.Lrd_rs8%=:\ns_mov_b64 exec, -1\nv_mov_b64 v[180:181], 0\nv_mov_b64 v[182:183], 0\nv_mov_b64 v[184:185], 0\n" \
"v_mov_b64 v[186:187], 0\nv_mov_b64 v[188:189], 0\nv_mov_b64 v[190:191], 0\nv_mov_b64 v[192:193], 0\nv_mov_b64 v[194:195], 0\ns_waitcnt vmcnt(0)\ns_add_u32 s43, s43, 1\ns_cmp_lt_u32 s43, 262144\n" \
"s_cbranch_scc1 .Lrd_ra%=\ns_branch .Lrd_abort%=\n.Lrd_y1%=:\nv_accvgpr_read_b32 v206, a32\nbuffer_load_dword v208, v206, %[rs], 0 offen sc1\ns_waitcnt vmcnt(0)\nv_cmp_gt_i32 vcc, s40, v208\ns_cbranch_vccz .Lrd_y1r%=\n" \
"s_add_u32 s43, s43, 1\ns_cmp_lt_u32 s43, 262144\ns_cbranch_scc1 .Lrd_y1%=\ns_branch .Lrd_abort%=\n.Lrd_tre0%=:\ns_add_u32 s60, s58, 16384\nbuffer_load_dwordx4 v[212:215], v196, %[rs], s60 offen offset:0 sc1\ns_waitcnt vmcnt(0)\n" \
"v_bitop3_b32 v209, v212, v214, s61 bitop3:0x7e\nv_cmp_gt_i32 vcc, 0, v209\ns_cbranch_vccz .Lrd_tl0%=\ns_add_u32 s43, s43, 1\ns_cmp_lt_u32 s43, 262144\ns_cbranch_scc1 .Lrd_tre0%=\ns_branch .Lrd_abort%=\n.Lrd_tre1%=:\n" \
"s_add_u32 s60, s58, 0\nbuffer_load_dwordx4 v[216:219], v196, %[rs], s60 offen offset:0 sc1\ns_waitcnt vmcnt(0)\nv_bitop3_b32 v209, v216, v218, s61 bitop3:0x7e\nv_cmp_gt_i32 vcc, 0, v209\ns_cbranch_vccz .Lrd_tl1%=\ns_add_u32 s43, s43, 1\ns_cmp_lt_u32 s43, 262144\n" \
"s_cbranch_scc1 .Lrd_tre1%=\ns_branch .Lrd_abort%=\n.Lrd_tre2%=:\ns_add_u32 s60, s58, 0\nbuffer_load_dwordx4 v[220:223], v196, %[rs], s60 offen offset:1024 sc1\ns_waitcnt vmcnt(0)\nv_bitop3_b32 v209, v220, v222, s61 bitop3:0x7e\nv_cmp_gt_i32 vcc, 0, v209\n" \
"s_cbranch_vccz .Lrd_tl2%=\ns_add_u32 s43, s43, 1\ns_cmp_lt_u32 s43, 262144\ns_cbranch_scc1 .Lrd_tre2%=\ns_branch .Lrd_abort%=\n.Lrd_tre3%=:\ns_add_u32 s60, s58, 16384\nbuffer_load_dwordx4 v[224:227], v196, %[rs], s60 offen offset:1024 sc1\n" \
"s_waitcnt vmcnt(0)\nv_bitop3_b32 v209, v224, v226, s61 bitop3:0x7e\nv_cmp_gt_i32 vcc, 0, v209\ns_cbranch_vccz .Lrd_tl3%=\ns_add_u32 s43, s43, 1\ns_cmp_lt_u32 s43, 262144\ns_cbranch_scc1 .Lrd_tre3%=\ns_branch .Lrd_abort%=\n" \
".Lrd_tre4%=:\ns_add_u32 s60, s58, 0\nbuffer_load_dwordx4 v[228:231], v196, %[rs], s60 offen offset:2048 sc1\ns_waitcnt vmcnt(0)\nv_bitop3_b32 v209, v228, v230, s61 bitop3:0x7e\nv_cmp_gt_i32 vcc, 0, v209\ns_cbranch_vccz .Lrd_tl4%=\ns_add_u32 s43, s43, 1\n" \
"s_cmp_lt_u32 s43, 262144\ns_cbranch_scc1 .Lrd_tre4%=\ns_branch .Lrd_abort%=\n.Lrd_tre5%=:\ns_add_u32 s60, s58, 0\nbuffer_load_dwordx4 v[232:235], v196, %[rs], s60 offen offset:3072 sc1\ns_waitcnt vmcnt(0)\nv_bitop3_b32 v209, v232, v234, s61 bitop3:0x7e\n" \
"v_cmp_gt_i32 vcc, 0, v209\ns_cbranch_vccz .Lrd_tl5%=\ns_add_u32 s43, s43, 1\ns_cmp_lt_u32 s43, 262144\ns_cbranch_scc1 .Lrd_tre5%=\ns_branch .Lrd_abort%=\n.Lrd_tre6%=:\ns_add_u32 s60, s58, 16384\n" \
"buffer_load_dwordx4 v[236:239], v196, %[rs], s60 offen offset:2048 sc1\ns_waitcnt vmcnt(0)\nv_bitop3_b32 v209, v236, v238, s61 bitop3:0x7e\nv_cmp_gt_i32 vcc, 0, v209\ns_cbranch_vccz .Lrd_tl6%=\ns_add_u32 s43, s43, 1\ns_cmp_lt_u32 s43, 262144\ns_cbranch_scc1 .Lrd_tre6%=\n" \
"s_branch .Lrd_abort%=\n.Lrd_tre7%=:\ns_add_u32 s60, s58, 4096\nbuffer_load_dwordx4 v[240:243], v196, %[rs], s60 offen offset:0 sc1\ns_waitcnt vmcnt(0)\nv_bitop3_b32 v209, v240, v242, s61 bitop3:0x7e\nv_cmp_gt_i32 vcc, 0, v209\ns_cbranch_vccz .Lrd_tl7%=\n" \
"s_add_u32 s43, s43, 1\ns_cmp_lt_u32 s43, 262144\ns_cbranch_scc1 .Lrd_tre7%=\ns_branch .Lrd_abort%=\n.Lrd_tre8%=:\ns_add_u32 s60, s58, 4096\nbuffer_load_dwordx4 v[244:247], v196, %[rs], s60 offen offset:1024 sc1\ns_waitcnt vmcnt(0)\n" \
"v_bitop3_b32 v209, v244, v246, s61 bitop3:0x7e\nv_cmp_gt_i32 vcc, 0, v209\ns_cbranch_vccz .Lrd_tl8%=\ns_add_u32 s43, s43, 1\ns_cmp_lt_u32 s43, 262144\ns_cbranch_scc1 .Lrd_tre8%=\ns_branch .Lrd_abort%=\n.Lrd_tre9%=:\n" \
"s_add_u32 s60, s58, 16384\nbuffer_load_dwordx4 v[248:251], v196, %[rs], s60 offen offset:3072 sc1\ns_waitcnt vmcnt(0)\nv_bitop3_b32 v209, v248, v250, s61 bitop3:0x7e\nv_cmp_gt_i32 vcc, 0, v209\ns_cbranch_vccz .Lrd_tl9%=\ns_add_u32 s43, s43, 1\ns_cmp_lt_u32 s43, 262144\n" \
"s_cbranch_scc1 .Lrd_tre9%=\ns_branch .Lrd_abort%=\n.Lrd_tre10%=:\ns_add_u32 s60, s58, 20480\nbuffer_load_dwordx4 v[252:255], v196, %[rs], s60 offen offset:0 sc1\ns_waitcnt vmcnt(0)\nv_bitop3_b32 v209, v252, v254, s61 bitop3:0x7e\nv_bitop3_b32 v210, v253, v255, s61 bitop3:0x7e\n" \
"v_or_b32 v209, v209, v210\nv_cmp_gt_i32 vcc, 0, v209\ns_cbranch_vccz .Lrd_tl10%=\ns_add_u32 s43, s43, 1\ns_cmp_lt_u32 s43, 262144\ns_cbranch_scc1 .Lrd_tre10%=\ns_branch .Lrd_abort%=\n.Lrd_tre11%=:\n" \
"s_add_u32 s60, s58, 20480\nbuffer_load_dwordx4 v[160:163], v196, %[rs], s60 offen offset:2048 sc1\ns_waitcnt vmcnt(0)\nv_bitop3_b32 v209, v160, v162, s61 bitop3:0x7e\nv_bitop3_b32 v210, v161, v163, s61 bitop3:0x7e\nv_or_b32 v209, v209, v210\nv_cmp_gt_i32 vcc, 0, v209\ns_cbranch_vccz .Lrd_tl11%=\n" \
"s_add_u32 s43, s43, 1\ns_cmp_lt_u32 s43, 262144\ns_cbranch_scc1 .Lrd_tre11%=\ns_branch .Lrd_abort%=\n.Lrd_tre12%=:\ns_add_u32 s60, s58, 4096\nbuffer_load_dwordx4 v[164:167], v196, %[rs], s60 offen offset:2048 sc1\ns_waitcnt vmcnt(0)\n" \
"v_bitop3_b32 v209, v164, v166, s61 bitop3:0x7e\nv_cmp_gt_i32 vcc, 0, v209\ns_cbranch_vccz .Lrd_tl12%=\ns_add_u32 s43, s43, 1\ns_cmp_lt_u32 s43, 262144\ns_cbranch_scc1 .Lrd_tre12%=\ns_branch .Lrd_abort%=\n.Lrd_tre13%=:\n" \
"s_add_u32 s60, s58, 4096\nbuffer_load_dwordx4 v[168:171], v196, %[rs], s60 offen offset:3072 sc1\ns_waitcnt vmcnt(0)\nv_bitop3_b32 v209, v168, v170, s61 bitop3:0x7e\nv_cmp_gt_i32 vcc, 0, v209\ns_cbranch_vccz .Lrd_tl13%=\ns_add_u32 s43, s43, 1\ns_cmp_lt_u32 s43, 262144\n" \
"s_cbranch_scc1 .Lrd_tre13%=\ns_branch .Lrd_abort%=\n.Lrd_tre14%=:\ns_add_u32 s60, s58, 20480\nbuffer_load_dwordx4 v[172:175], v196, %[rs], s60 offen offset:1024 sc1\ns_waitcnt vmcnt(0)\nv_bitop3_b32 v209, v172, v174, s61 bitop3:0x7e\nv_bitop3_b32 v210, v173, v175, s61 bitop3:0x7e\n" \
"v_or_b32 v209, v209, v210\nv_cmp_gt_i32 vcc, 0, v209\ns_cbranch_vccz .Lrd_tl14%=\ns_add_u32 s43, s43, 1\ns_cmp_lt_u32 s43, 262144\ns_cbranch_scc1 .Lrd_tre14%=\ns_branch .Lrd_abort%=\n.Lrd_tre15%=:\n" \
"s_add_u32 s60, s58, 20480\nbuffer_load_dwordx4 v[176:179], v196, %[rs], s60 offen offset:3072 sc1\ns_waitcnt vmcnt(0)\nv_bitop3_b32 v209, v176, v178, s61 bitop3:0x7e\nv_bitop3_b32 v210, v177, v179, s61 bitop3:0x7e\nv_or_b32 v209, v209, v210\nv_cmp_gt_i32 vcc, 0, v209\ns_cbranch_vccz .Lrd_tl15%=\n" \
"s_add_u32 s43, s43, 1\ns_cmp_lt_u32 s43, 262144\ns_cbranch_scc1 .Lrd_tre15%=\ns_branch .Lrd_abort%=\n.Lrd_abort%=:\ns_mov_b64 exec, -1\nv_mov_b32 v16, 0\ns_movk_i32 s44, 6400\n" \
"buffer_store_dword v16, off, %[rs], s44 sc1\n.Lrd_end%=:\ns_waitcnt vmcnt(0) lgkmcnt(0)\n" \
""
#define GG_ASM \
"v_mbcnt_lo_u32_b32 v255, -1, 0\nv_mbcnt_hi_u32_b32 v255, -1, v255\nv_lshlrev_b32 v255, 2, v255\nv_add_u32 v255, %[tb], v255\nds_read_b32 v0, v255 offset:0\nds_read_b32 v1, v255 offset:256\nds_read_b32 v2, v255 offset:512\nds_read_b32 v3, v255 offset:768\n" \
"ds_read_b32 v4, v255 offset:1024\nds_read_b32 v5, v255 offset:1280\nds_read_b32 v92, v255 offset:1536\nds_read_b32 v93, v255 offset:1792\nds_read_b32 v94, v255 offset:2048\ns_waitcnt lgkmcnt(0)\nv_mov_b32 v6, 0x3f803f80\nv_mov_b32 v7, 0x3f803f80\n" \
"s_add_u32 m0, %[lw], 16384\ns_add_u32 s44, %[wo], 0\nbuffer_load_dwordx4 v92, %[rw], s44 offen nt lds\ns_add_u32 m0, %[lw], 17408\ns_add_u32 s44, %[wo], 57344\nbuffer_load_dwordx4 v92, %[rw], s44 offen nt lds\ns_add_u32 m0, %[lw], 18432\ns_add_u32 s44, %[wo], 114688\n" \
"buffer_load_dwordx4 v92, %[rw], s44 offen nt lds\ns_add_u32 m0, %[lw], 19456\ns_add_u32 s44, %[wo], 172032\nbuffer_load_dwordx4 v92, %[rw], s44 offen nt lds\ns_add_u32 m0, %[lw], 20480\ns_add_u32 s44, %[wo], 229376\nbuffer_load_dwordx4 v92, %[rw], s44 offen nt lds\ns_add_u32 m0, %[lw], 21504\n" \
"s_add_u32 s44, %[wo], 286720\nbuffer_load_dwordx4 v92, %[rw], s44 offen nt lds\ns_add_u32 m0, %[lw], 22528\ns_add_u32 s44, %[wo], 344064\nbuffer_load_dwordx4 v92, %[rw], s44 offen nt lds\ns_add_u32 m0, %[lw], 23552\ns_add_u32 s44, %[wo], 401408\nbuffer_load_dwordx4 v92, %[rw], s44 offen nt lds\n" \
"s_add_u32 m0, %[tb], 0\ns_add_u32 s44, %[wo], 1835008\nbuffer_load_dwordx4 v92, %[rw], s44 offen nt lds\ns_add_u32 m0, %[tb], 1024\ns_add_u32 s44, %[wo], 1892352\nbuffer_load_dwordx4 v92, %[rw], s44 offen nt lds\ns_add_u32 m0, %[tb], 2048\ns_add_u32 s44, %[wo], 1949696\n" \
"buffer_load_dwordx4 v92, %[rw], s44 offen nt lds\ns_add_u32 m0, %[tb], 3072\ns_add_u32 s44, %[wo], 2007040\nbuffer_load_dwordx4 v92, %[rw], s44 offen nt lds\ns_add_u32 m0, %[tb], 4096\ns_add_u32 s44, %[wo], 2064384\nbuffer_load_dwordx4 v92, %[rw], s44 offen nt lds\ns_add_u32 m0, %[tb], 5120\n" \
"s_add_u32 s44, %[wo], 2121728\nbuffer_load_dwordx4 v92, %[rw], s44 offen nt lds\ns_add_u32 m0, %[tb], 6144\ns_add_u32 s44, %[wo], 2179072\nbuffer_load_dwordx4 v92, %[rw], s44 offen nt lds\ns_add_u32 m0, %[tb], 7168\ns_add_u32 s44, %[wo], 2236416\nbuffer_load_dwordx4 v92, %[rw], s44 offen nt lds\n" \
"s_add_u32 m0, %[tb], 8192\ns_add_u32 s44, %[wo], 3670016\nbuffer_load_dwordx4 v92, %[rw], s44 offen nt lds\ns_add_u32 m0, %[tb], 9216\ns_add_u32 s44, %[wo], 3727360\nbuffer_load_dwordx4 v92, %[rw], s44 offen nt lds\ns_add_u32 m0, %[tb], 10240\ns_add_u32 s44, %[wo], 3784704\n" \
"buffer_load_dwordx4 v92, %[rw], s44 offen nt lds\ns_add_u32 m0, %[tb], 11264\ns_add_u32 s44, %[wo], 3842048\nbuffer_load_dwordx4 v92, %[rw], s44 offen nt lds\ns_add_u32 m0, %[lw], 0\ns_add_u32 s44, %[wo], 3899392\nbuffer_load_dwordx4 v92, %[rw], s44 offen nt lds\ns_add_u32 m0, %[lw], 1024\n" \
"s_add_u32 s44, %[wo], 3956736\nbuffer_load_dwordx4 v92, %[rw], s44 offen nt lds\ns_add_u32 m0, %[lw], 2048\ns_add_u32 s44, %[wo], 4014080\nbuffer_load_dwordx4 v92, %[rw], s44 offen nt lds\ns_add_u32 m0, %[lw], 3072\ns_add_u32 s44, %[wo], 4071424\nbuffer_load_dwordx4 v92, %[rw], s44 offen nt lds\n" \
"s_add_u32 m0, %[lw], 4096\ns_add_u32 s44, %[wo], 5505024\nbuffer_load_dwordx4 v92, %[rw], s44 offen nt lds\ns_add_u32 m0, %[lw], 5120\ns_add_u32 s44, %[wo], 5562368\nbuffer_load_dwordx4 v92, %[rw], s44 offen nt lds\ns_add_u32 m0, %[lw], 6144\ns_add_u32 s44, %[wo], 5619712\n" \
"buffer_load_dwordx4 v92, %[rw], s44 offen nt lds\ns_add_u32 m0, %[lw], 7168\ns_add_u32 s44, %[wo], 5677056\nbuffer_load_dwordx4 v92, %[rw], s44 offen nt lds\ns_add_u32 m0, %[lw], 8192\ns_add_u32 s44, %[wo], 5734400\nbuffer_load_dwordx4 v92, %[rw], s44 offen nt lds\ns_add_u32 m0, %[lw], 9216\n" \
"s_add_u32 s44, %[wo], 5791744\nbuffer_load_dwordx4 v92, %[rw], s44 offen nt lds\ns_add_u32 m0, %[lw], 10240\ns_add_u32 s44, %[wo], 5849088\nbuffer_load_dwordx4 v92, %[rw], s44 offen nt lds\ns_add_u32 m0, %[lw], 11264\ns_add_u32 s44, %[wo], 5906432\nbuffer_load_dwordx4 v92, %[rw], s44 offen nt lds\n" \
"s_waitcnt vmcnt(28)\ns_barrier\ns_add_u32 m0, %[lw], 12288\ns_add_u32 s44, %[wo], 458752\nbuffer_load_dwordx4 v92, %[rw], s44 offen nt lds\ns_add_u32 m0, %[lw], 13312\ns_add_u32 s44, %[wo], 516096\nbuffer_load_dwordx4 v92, %[rw], s44 offen nt lds\n" \
"s_add_u32 m0, %[lw], 14336\ns_add_u32 s44, %[wo], 573440\nbuffer_load_dwordx4 v92, %[rw], s44 offen nt lds\ns_add_u32 m0, %[lw], 15360\ns_add_u32 s44, %[wo], 630784\nbuffer_load_dwordx4 v92, %[rw], s44 offen nt lds\nds_read_b64_tr_b16 a[0:1], v94 offset:16384\nds_read_b64_tr_b16 a[2:3], v94 offset:16896\n" \
"ds_read_b64_tr_b16 a[8:9], v94 offset:16392\nds_read_b64_tr_b16 a[10:11], v94 offset:16904\nds_read_b64_tr_b16 a[16:17], v94 offset:40960\nds_read_b64_tr_b16 a[18:19], v94 offset:41472\nds_read_b64_tr_b16 a[24:25], v94 offset:40968\nds_read_b64_tr_b16 a[26:27], v94 offset:41480\ns_waitcnt lgkmcnt(0)\nv_mfma_f32_16x16x16_bf16 v[8:11], a[0:1], v[6:7], 0\n" \
"v_mfma_f32_16x16x16_bf16 v[12:15], a[8:9], v[6:7], 0\nv_mfma_f32_16x16x16_bf16 v[16:19], a[16:17], v[6:7], 0\nv_mfma_f32_16x16x16_bf16 v[20:23], a[24:25], v[6:7], 0\nv_mfma_f32_16x16x16_bf16 v[8:11], a[2:3], v[6:7], v[8:11]\nv_mfma_f32_16x16x16_bf16 v[12:15], a[10:11], v[6:7], v[12:15]\nv_mfma_f32_16x16x16_bf16 v[16:19], a[18:19], v[6:7], v[16:19]\nv_mfma_f32_16x16x16_bf16 v[20:23], a[26:27], v[6:7], v[20:23]\ns_waitcnt vmcnt(28)\n" \
"s_barrier\ns_add_u32 m0, %[lw], 16384\ns_add_u32 s44, %[wo], 688128\nbuffer_load_dwordx4 v92, %[rw], s44 offen nt lds\ns_add_u32 m0, %[lw], 17408\ns_add_u32 s44, %[wo], 745472\nbuffer_load_dwordx4 v92, %[rw], s44 offen nt lds\ns_add_u32 m0, %[lw], 18432\n" \
"s_add_u32 s44, %[wo], 802816\nbuffer_load_dwordx4 v92, %[rw], s44 offen nt lds\ns_add_u32 m0, %[lw], 19456\ns_add_u32 s44, %[wo], 860160\nbuffer_load_dwordx4 v92, %[rw], s44 offen nt lds\nds_read_b64_tr_b16 a[4:5], v94 offset:20480\nds_read_b64_tr_b16 a[6:7], v94 offset:20992\nds_read_b64_tr_b16 a[12:13], v94 offset:20488\n" \
"ds_read_b64_tr_b16 a[14:15], v94 offset:21000\nds_read_b64_tr_b16 a[20:21], v94 offset:45056\nds_read_b64_tr_b16 a[22:23], v94 offset:45568\nds_read_b64_tr_b16 a[28:29], v94 offset:45064\nds_read_b64_tr_b16 a[30:31], v94 offset:45576\ns_waitcnt lgkmcnt(0)\nv_mfma_f32_16x16x16_bf16 v[8:11], a[4:5], v[6:7], v[8:11]\nv_mfma_f32_16x16x16_bf16 v[12:15], a[12:13], v[6:7], v[12:15]\n" \
"v_mfma_f32_16x16x16_bf16 v[16:19], a[20:21], v[6:7], v[16:19]\nv_mfma_f32_16x16x16_bf16 v[20:23], a[28:29], v[6:7], v[20:23]\nv_mfma_f32_16x16x16_bf16 v[8:11], a[6:7], v[6:7], v[8:11]\nv_mfma_f32_16x16x16_bf16 v[12:15], a[14:15], v[6:7], v[12:15]\nv_mfma_f32_16x16x16_bf16 v[16:19], a[22:23], v[6:7], v[16:19]\nv_mfma_f32_16x16x16_bf16 v[20:23], a[30:31], v[6:7], v[20:23]\ns_waitcnt vmcnt(28)\ns_barrier\n" \
"s_add_u32 m0, %[lw], 20480\ns_add_u32 s44, %[wo], 917504\nbuffer_load_dwordx4 v92, %[rw], s44 offen nt lds\ns_add_u32 m0, %[lw], 21504\ns_add_u32 s44, %[wo], 974848\nbuffer_load_dwordx4 v92, %[rw], s44 offen nt lds\ns_add_u32 m0, %[lw], 22528\ns_add_u32 s44, %[wo], 1032192\n" \
"buffer_load_dwordx4 v92, %[rw], s44 offen nt lds\ns_add_u32 m0, %[lw], 23552\ns_add_u32 s44, %[wo], 1089536\nbuffer_load_dwordx4 v92, %[rw], s44 offen nt lds\nds_read_b64_tr_b16 a[104:105], v93 offset:0\nds_read_b64_tr_b16 a[106:107], v93 offset:512\nds_read_b64_tr_b16 a[112:113], v93 offset:8\nds_read_b64_tr_b16 a[114:115], v93 offset:520\n" \
"ds_read_b64_tr_b16 a[120:121], v93 offset:12288\nds_read_b64_tr_b16 a[122:123], v93 offset:12800\nds_read_b64_tr_b16 a[128:129], v93 offset:12296\nds_read_b64_tr_b16 a[130:131], v93 offset:12808\ns_waitcnt lgkmcnt(0)\nv_mfma_f32_16x16x16_bf16 v[8:11], a[104:105], v[6:7], v[8:11]\nv_mfma_f32_16x16x16_bf16 v[12:15], a[112:113], v[6:7], v[12:15]\nv_mfma_f32_16x16x16_bf16 v[16:19], a[120:121], v[6:7], v[16:19]\n" \
"v_mfma_f32_16x16x16_bf16 v[20:23], a[128:129], v[6:7], v[20:23]\nv_mfma_f32_16x16x16_bf16 v[8:11], a[106:107], v[6:7], v[8:11]\nv_mfma_f32_16x16x16_bf16 v[12:15], a[114:115], v[6:7], v[12:15]\nv_mfma_f32_16x16x16_bf16 v[16:19], a[122:123], v[6:7], v[16:19]\nv_mfma_f32_16x16x16_bf16 v[20:23], a[130:131], v[6:7], v[20:23]\ns_waitcnt vmcnt(28)\ns_barrier\ns_add_u32 m0, %[tb], 0\n" \
"s_add_u32 s44, %[wo], 1146880\nbuffer_load_dwordx4 v92, %[rw], s44 offen nt lds\ns_add_u32 m0, %[tb], 1024\ns_add_u32 s44, %[wo], 1204224\nbuffer_load_dwordx4 v92, %[rw], s44 offen nt lds\ns_add_u32 m0, %[tb], 2048\ns_add_u32 s44, %[wo], 1261568\nbuffer_load_dwordx4 v92, %[rw], s44 offen nt lds\n" \
"s_add_u32 m0, %[tb], 3072\ns_add_u32 s44, %[wo], 1318912\nbuffer_load_dwordx4 v92, %[rw], s44 offen nt lds\nds_read_b64_tr_b16 a[108:109], v93 offset:4096\nds_read_b64_tr_b16 a[110:111], v93 offset:4608\nds_read_b64_tr_b16 a[116:117], v93 offset:4104\nds_read_b64_tr_b16 a[118:119], v93 offset:4616\nds_read_b64_tr_b16 a[124:125], v93 offset:16384\n" \
"ds_read_b64_tr_b16 a[126:127], v93 offset:16896\nds_read_b64_tr_b16 a[132:133], v93 offset:16392\nds_read_b64_tr_b16 a[134:135], v93 offset:16904\ns_waitcnt lgkmcnt(0)\nv_mfma_f32_16x16x16_bf16 v[8:11], a[108:109], v[6:7], v[8:11]\nv_mfma_f32_16x16x16_bf16 v[12:15], a[116:117], v[6:7], v[12:15]\nv_mfma_f32_16x16x16_bf16 v[16:19], a[124:125], v[6:7], v[16:19]\nv_mfma_f32_16x16x16_bf16 v[20:23], a[132:133], v[6:7], v[20:23]\n" \
"v_mfma_f32_16x16x16_bf16 v[8:11], a[110:111], v[6:7], v[8:11]\nv_mfma_f32_16x16x16_bf16 v[12:15], a[118:119], v[6:7], v[12:15]\nv_mfma_f32_16x16x16_bf16 v[16:19], a[126:127], v[6:7], v[16:19]\nv_mfma_f32_16x16x16_bf16 v[20:23], a[134:135], v[6:7], v[20:23]\ns_waitcnt vmcnt(28)\ns_barrier\ns_add_u32 m0, %[tb], 4096\ns_add_u32 s44, %[wo], 1376256\n" \
"buffer_load_dwordx4 v92, %[rw], s44 offen nt lds\ns_add_u32 m0, %[tb], 5120\ns_add_u32 s44, %[wo], 1433600\nbuffer_load_dwordx4 v92, %[rw], s44 offen nt lds\ns_add_u32 m0, %[tb], 6144\ns_add_u32 s44, %[wo], 1490944\nbuffer_load_dwordx4 v92, %[rw], s44 offen nt lds\ns_add_u32 m0, %[tb], 7168\n" \
"s_add_u32 s44, %[wo], 1548288\nbuffer_load_dwordx4 v92, %[rw], s44 offen nt lds\nds_read_b64_tr_b16 a[208:209], v93 offset:8192\nds_read_b64_tr_b16 a[210:211], v93 offset:8704\nds_read_b64_tr_b16 a[216:217], v93 offset:8200\nds_read_b64_tr_b16 a[218:219], v93 offset:8712\nds_read_b64_tr_b16 a[224:225], v93 offset:20480\nds_read_b64_tr_b16 a[226:227], v93 offset:20992\n" \
"ds_read_b64_tr_b16 a[232:233], v93 offset:20488\nds_read_b64_tr_b16 a[234:235], v93 offset:21000\ns_waitcnt lgkmcnt(0)\nv_mfma_f32_16x16x16_bf16 v[8:11], a[208:209], v[6:7], v[8:11]\nv_mfma_f32_16x16x16_bf16 v[12:15], a[216:217], v[6:7], v[12:15]\nv_mfma_f32_16x16x16_bf16 v[16:19], a[224:225], v[6:7], v[16:19]\nv_mfma_f32_16x16x16_bf16 v[20:23], a[232:233], v[6:7], v[20:23]\nv_mfma_f32_16x16x16_bf16 v[8:11], a[210:211], v[6:7], v[8:11]\n" \
"v_mfma_f32_16x16x16_bf16 v[12:15], a[218:219], v[6:7], v[12:15]\nv_mfma_f32_16x16x16_bf16 v[16:19], a[226:227], v[6:7], v[16:19]\nv_mfma_f32_16x16x16_bf16 v[20:23], a[234:235], v[6:7], v[20:23]\ns_waitcnt vmcnt(28)\ns_barrier\ns_add_u32 m0, %[tb], 8192\ns_add_u32 s44, %[wo], 1605632\nbuffer_load_dwordx4 v92, %[rw], s44 offen nt lds\n" \
"s_add_u32 m0, %[tb], 9216\ns_add_u32 s44, %[wo], 1662976\nbuffer_load_dwordx4 v92, %[rw], s44 offen nt lds\ns_add_u32 m0, %[tb], 10240\ns_add_u32 s44, %[wo], 1720320\nbuffer_load_dwordx4 v92, %[rw], s44 offen nt lds\ns_add_u32 m0, %[tb], 11264\ns_add_u32 s44, %[wo], 1777664\n" \
"buffer_load_dwordx4 v92, %[rw], s44 offen nt lds\nds_read_b64_tr_b16 a[212:213], v94 offset:0\nds_read_b64_tr_b16 a[214:215], v94 offset:512\nds_read_b64_tr_b16 a[220:221], v94 offset:8\nds_read_b64_tr_b16 a[222:223], v94 offset:520\nds_read_b64_tr_b16 a[228:229], v94 offset:24576\nds_read_b64_tr_b16 a[230:231], v94 offset:25088\nds_read_b64_tr_b16 a[236:237], v94 offset:24584\n" \
"ds_read_b64_tr_b16 a[238:239], v94 offset:25096\ns_waitcnt lgkmcnt(0)\nv_mfma_f32_16x16x16_bf16 v[8:11], a[212:213], v[6:7], v[8:11]\nv_mfma_f32_16x16x16_bf16 v[12:15], a[220:221], v[6:7], v[12:15]\nv_mfma_f32_16x16x16_bf16 v[16:19], a[228:229], v[6:7], v[16:19]\nv_mfma_f32_16x16x16_bf16 v[20:23], a[236:237], v[6:7], v[20:23]\nv_mfma_f32_16x16x16_bf16 v[8:11], a[214:215], v[6:7], v[8:11]\nv_mfma_f32_16x16x16_bf16 v[12:15], a[222:223], v[6:7], v[12:15]\n" \
"v_mfma_f32_16x16x16_bf16 v[16:19], a[230:231], v[6:7], v[16:19]\nv_mfma_f32_16x16x16_bf16 v[20:23], a[238:239], v[6:7], v[20:23]\ns_waitcnt vmcnt(28)\ns_barrier\ns_add_u32 m0, %[lw], 0\ns_add_u32 s44, %[wo], 2293760\nbuffer_load_dwordx4 v92, %[rw], s44 offen nt lds\ns_add_u32 m0, %[lw], 1024\n" \
"s_add_u32 s44, %[wo], 2351104\nbuffer_load_dwordx4 v92, %[rw], s44 offen nt lds\ns_add_u32 m0, %[lw], 2048\ns_add_u32 s44, %[wo], 2408448\nbuffer_load_dwordx4 v92, %[rw], s44 offen nt lds\ns_add_u32 m0, %[lw], 3072\ns_add_u32 s44, %[wo], 2465792\nbuffer_load_dwordx4 v92, %[rw], s44 offen nt lds\n" \
"ds_read_b64_tr_b16 v[152:153], v94 offset:4096\nds_read_b64_tr_b16 v[154:155], v94 offset:4608\nds_read_b64_tr_b16 v[160:161], v94 offset:4104\nds_read_b64_tr_b16 v[162:163], v94 offset:4616\nds_read_b64_tr_b16 v[168:169], v94 offset:28672\nds_read_b64_tr_b16 v[170:171], v94 offset:29184\nds_read_b64_tr_b16 v[176:177], v94 offset:28680\nds_read_b64_tr_b16 v[178:179], v94 offset:29192\n" \
"s_waitcnt lgkmcnt(0)\nv_mfma_f32_16x16x16_bf16 v[8:11], v[152:153], v[6:7], v[8:11]\nv_mfma_f32_16x16x16_bf16 v[12:15], v[160:161], v[6:7], v[12:15]\nv_mfma_f32_16x16x16_bf16 v[16:19], v[168:169], v[6:7], v[16:19]\nv_mfma_f32_16x16x16_bf16 v[20:23], v[176:177], v[6:7], v[20:23]\nv_mfma_f32_16x16x16_bf16 v[8:11], v[154:155], v[6:7], v[8:11]\nv_mfma_f32_16x16x16_bf16 v[12:15], v[162:163], v[6:7], v[12:15]\nv_mfma_f32_16x16x16_bf16 v[16:19], v[170:171], v[6:7], v[16:19]\n" \
"v_mfma_f32_16x16x16_bf16 v[20:23], v[178:179], v[6:7], v[20:23]\ns_waitcnt vmcnt(28)\ns_barrier\ns_add_u32 m0, %[lw], 4096\ns_add_u32 s44, %[wo], 2523136\nbuffer_load_dwordx4 v92, %[rw], s44 offen nt lds\ns_add_u32 m0, %[lw], 5120\ns_add_u32 s44, %[wo], 2580480\n" \
"buffer_load_dwordx4 v92, %[rw], s44 offen nt lds\ns_add_u32 m0, %[lw], 6144\ns_add_u32 s44, %[wo], 2637824\nbuffer_load_dwordx4 v92, %[rw], s44 offen nt lds\ns_add_u32 m0, %[lw], 7168\ns_add_u32 s44, %[wo], 2695168\nbuffer_load_dwordx4 v92, %[rw], s44 offen nt lds\nds_read_b64_tr_b16 v[156:157], v94 offset:8192\n" \
"ds_read_b64_tr_b16 v[158:159], v94 offset:8704\nds_read_b64_tr_b16 v[164:165], v94 offset:8200\nds_read_b64_tr_b16 v[166:167], v94 offset:8712\nds_read_b64_tr_b16 v[172:173], v94 offset:32768\nds_read_b64_tr_b16 v[174:175], v94 offset:33280\nds_read_b64_tr_b16 v[180:181], v94 offset:32776\nds_read_b64_tr_b16 v[182:183], v94 offset:33288\ns_waitcnt lgkmcnt(0)\n" \
"v_mfma_f32_16x16x16_bf16 v[8:11], v[156:157], v[6:7], v[8:11]\nv_mfma_f32_16x16x16_bf16 v[12:15], v[164:165], v[6:7], v[12:15]\nv_mfma_f32_16x16x16_bf16 v[16:19], v[172:173], v[6:7], v[16:19]\nv_mfma_f32_16x16x16_bf16 v[20:23], v[180:181], v[6:7], v[20:23]\nv_mfma_f32_16x16x16_bf16 v[8:11], v[158:159], v[6:7], v[8:11]\nv_mfma_f32_16x16x16_bf16 v[12:15], v[166:167], v[6:7], v[12:15]\nv_mfma_f32_16x16x16_bf16 v[16:19], v[174:175], v[6:7], v[16:19]\nv_mfma_f32_16x16x16_bf16 v[20:23], v[182:183], v[6:7], v[20:23]\n" \
"s_waitcnt vmcnt(28)\ns_barrier\ns_add_u32 m0, %[lw], 8192\ns_add_u32 s44, %[wo], 2752512\nbuffer_load_dwordx4 v92, %[rw], s44 offen nt lds\ns_add_u32 m0, %[lw], 9216\ns_add_u32 s44, %[wo], 2809856\nbuffer_load_dwordx4 v92, %[rw], s44 offen nt lds\n" \
"s_add_u32 m0, %[lw], 10240\ns_add_u32 s44, %[wo], 2867200\nbuffer_load_dwordx4 v92, %[rw], s44 offen nt lds\ns_add_u32 m0, %[lw], 11264\ns_add_u32 s44, %[wo], 2924544\nbuffer_load_dwordx4 v92, %[rw], s44 offen nt lds\nds_read_b64_tr_b16 a[32:33], v94 offset:12288\nds_read_b64_tr_b16 a[34:35], v94 offset:12800\n" \
"ds_read_b64_tr_b16 v[88:89], v94 offset:12296\nds_read_b64_tr_b16 v[90:91], v94 offset:12808\nds_read_b64_tr_b16 a[40:41], v94 offset:36864\nds_read_b64_tr_b16 a[42:43], v94 offset:37376\nds_read_b64_tr_b16 a[48:49], v94 offset:36872\nds_read_b64_tr_b16 a[50:51], v94 offset:37384\ns_waitcnt lgkmcnt(0)\nv_mfma_f32_16x16x16_bf16 v[8:11], a[32:33], v[6:7], v[8:11]\n" \
"v_mfma_f32_16x16x16_bf16 v[12:15], v[88:89], v[6:7], v[12:15]\nv_mfma_f32_16x16x16_bf16 v[16:19], a[40:41], v[6:7], v[16:19]\nv_mfma_f32_16x16x16_bf16 v[20:23], a[48:49], v[6:7], v[20:23]\nv_mfma_f32_16x16x16_bf16 v[8:11], a[34:35], v[6:7], v[8:11]\nv_mfma_f32_16x16x16_bf16 v[12:15], v[90:91], v[6:7], v[12:15]\nv_mfma_f32_16x16x16_bf16 v[16:19], a[42:43], v[6:7], v[16:19]\nv_mfma_f32_16x16x16_bf16 v[20:23], a[50:51], v[6:7], v[20:23]\ns_waitcnt vmcnt(28)\n" \
"s_barrier\ns_add_u32 m0, %[lw], 12288\ns_add_u32 s44, %[wo], 2981888\nbuffer_load_dwordx4 v92, %[rw], s44 offen nt lds\ns_add_u32 m0, %[lw], 13312\ns_add_u32 s44, %[wo], 3039232\nbuffer_load_dwordx4 v92, %[rw], s44 offen nt lds\ns_add_u32 m0, %[lw], 14336\n" \
"s_add_u32 s44, %[wo], 3096576\nbuffer_load_dwordx4 v92, %[rw], s44 offen nt lds\ns_add_u32 m0, %[lw], 15360\ns_add_u32 s44, %[wo], 3153920\nbuffer_load_dwordx4 v92, %[rw], s44 offen nt lds\nds_read_b64_tr_b16 a[36:37], v94 offset:16384\nds_read_b64_tr_b16 a[38:39], v94 offset:16896\nds_read_b64_tr_b16 v[84:85], v94 offset:16392\n" \
"ds_read_b64_tr_b16 v[86:87], v94 offset:16904\nds_read_b64_tr_b16 a[44:45], v94 offset:40960\nds_read_b64_tr_b16 a[46:47], v94 offset:41472\nds_read_b64_tr_b16 a[52:53], v94 offset:40968\nds_read_b64_tr_b16 a[54:55], v94 offset:41480\ns_waitcnt lgkmcnt(0)\nv_mfma_f32_16x16x16_bf16 v[8:11], a[36:37], v[6:7], v[8:11]\nv_mfma_f32_16x16x16_bf16 v[12:15], v[84:85], v[6:7], v[12:15]\n" \
"v_mfma_f32_16x16x16_bf16 v[16:19], a[44:45], v[6:7], v[16:19]\nv_mfma_f32_16x16x16_bf16 v[20:23], a[52:53], v[6:7], v[20:23]\nv_mfma_f32_16x16x16_bf16 v[8:11], a[38:39], v[6:7], v[8:11]\nv_mfma_f32_16x16x16_bf16 v[12:15], v[86:87], v[6:7], v[12:15]\nv_mfma_f32_16x16x16_bf16 v[16:19], a[46:47], v[6:7], v[16:19]\nv_mfma_f32_16x16x16_bf16 v[20:23], a[54:55], v[6:7], v[20:23]\ns_waitcnt vmcnt(28)\ns_barrier\n" \
"s_add_u32 m0, %[lw], 16384\ns_add_u32 s44, %[wo], 3211264\nbuffer_load_dwordx4 v92, %[rw], s44 offen nt lds\ns_add_u32 m0, %[lw], 17408\ns_add_u32 s44, %[wo], 3268608\nbuffer_load_dwordx4 v92, %[rw], s44 offen nt lds\ns_add_u32 m0, %[lw], 18432\ns_add_u32 s44, %[wo], 3325952\n" \
"buffer_load_dwordx4 v92, %[rw], s44 offen nt lds\ns_add_u32 m0, %[lw], 19456\ns_add_u32 s44, %[wo], 3383296\nbuffer_load_dwordx4 v92, %[rw], s44 offen nt lds\nds_read_b64_tr_b16 a[56:57], v94 offset:20480\nds_read_b64_tr_b16 a[58:59], v94 offset:20992\nds_read_b64_tr_b16 a[64:65], v94 offset:20488\nds_read_b64_tr_b16 a[66:67], v94 offset:21000\n" \
"ds_read_b64_tr_b16 v[80:81], v94 offset:45056\nds_read_b64_tr_b16 v[82:83], v94 offset:45568\nds_read_b64_tr_b16 a[72:73], v94 offset:45064\nds_read_b64_tr_b16 a[74:75], v94 offset:45576\ns_waitcnt lgkmcnt(0)\nv_mfma_f32_16x16x16_bf16 v[8:11], a[56:57], v[6:7], v[8:11]\nv_mfma_f32_16x16x16_bf16 v[12:15], a[64:65], v[6:7], v[12:15]\nv_mfma_f32_16x16x16_bf16 v[16:19], v[80:81], v[6:7], v[16:19]\n" \
"v_mfma_f32_16x16x16_bf16 v[20:23], a[72:73], v[6:7], v[20:23]\nv_mfma_f32_16x16x16_bf16 v[8:11], a[58:59], v[6:7], v[8:11]\nv_mfma_f32_16x16x16_bf16 v[12:15], a[66:67], v[6:7], v[12:15]\nv_mfma_f32_16x16x16_bf16 v[16:19], v[82:83], v[6:7], v[16:19]\nv_mfma_f32_16x16x16_bf16 v[20:23], a[74:75], v[6:7], v[20:23]\ns_waitcnt vmcnt(28)\ns_barrier\ns_add_u32 m0, %[lw], 20480\n" \
"s_add_u32 s44, %[wo], 3440640\nbuffer_load_dwordx4 v92, %[rw], s44 offen nt lds\ns_add_u32 m0, %[lw], 21504\ns_add_u32 s44, %[wo], 3497984\nbuffer_load_dwordx4 v92, %[rw], s44 offen nt lds\ns_add_u32 m0, %[lw], 22528\ns_add_u32 s44, %[wo], 3555328\nbuffer_load_dwordx4 v92, %[rw], s44 offen nt lds\n" \
"s_add_u32 m0, %[lw], 23552\ns_add_u32 s44, %[wo], 3612672\nbuffer_load_dwordx4 v92, %[rw], s44 offen nt lds\nds_read_b64_tr_b16 a[60:61], v93 offset:0\nds_read_b64_tr_b16 a[62:63], v93 offset:512\nds_read_b64_tr_b16 a[68:69], v93 offset:8\nds_read_b64_tr_b16 a[70:71], v93 offset:520\nds_read_b64_tr_b16 v[76:77], v93 offset:12288\n" \
"ds_read_b64_tr_b16 v[78:79], v93 offset:12800\nds_read_b64_tr_b16 a[76:77], v93 offset:12296\nds_read_b64_tr_b16 a[78:79], v93 offset:12808\ns_waitcnt lgkmcnt(0)\nv_mfma_f32_16x16x16_bf16 v[8:11], a[60:61], v[6:7], v[8:11]\nv_mfma_f32_16x16x16_bf16 v[12:15], a[68:69], v[6:7], v[12:15]\nv_mfma_f32_16x16x16_bf16 v[16:19], v[76:77], v[6:7], v[16:19]\nv_mfma_f32_16x16x16_bf16 v[20:23], a[76:77], v[6:7], v[20:23]\n" \
"v_mfma_f32_16x16x16_bf16 v[8:11], a[62:63], v[6:7], v[8:11]\nv_mfma_f32_16x16x16_bf16 v[12:15], a[70:71], v[6:7], v[12:15]\nv_mfma_f32_16x16x16_bf16 v[16:19], v[78:79], v[6:7], v[16:19]\nv_mfma_f32_16x16x16_bf16 v[20:23], a[78:79], v[6:7], v[20:23]\ns_waitcnt vmcnt(28)\ns_barrier\ns_add_u32 m0, %[tb], 0\ns_add_u32 s44, %[wo], 4128768\n" \
"buffer_load_dwordx4 v92, %[rw], s44 offen nt lds\ns_add_u32 m0, %[tb], 1024\ns_add_u32 s44, %[wo], 4186112\nbuffer_load_dwordx4 v92, %[rw], s44 offen nt lds\ns_add_u32 m0, %[tb], 2048\ns_add_u32 s44, %[wo], 4243456\nbuffer_load_dwordx4 v92, %[rw], s44 offen nt lds\ns_add_u32 m0, %[tb], 3072\n" \
"s_add_u32 s44, %[wo], 4300800\nbuffer_load_dwordx4 v92, %[rw], s44 offen nt lds\nds_read_b64_tr_b16 a[80:81], v93 offset:4096\nds_read_b64_tr_b16 a[82:83], v93 offset:4608\nds_read_b64_tr_b16 a[88:89], v93 offset:4104\nds_read_b64_tr_b16 a[90:91], v93 offset:4616\nds_read_b64_tr_b16 a[96:97], v93 offset:16384\nds_read_b64_tr_b16 a[98:99], v93 offset:16896\n" \
"ds_read_b64_tr_b16 v[72:73], v93 offset:16392\nds_read_b64_tr_b16 v[74:75], v93 offset:16904\ns_waitcnt lgkmcnt(0)\nv_mfma_f32_16x16x16_bf16 v[8:11], a[80:81], v[6:7], v[8:11]\nv_mfma_f32_16x16x16_bf16 v[12:15], a[88:89], v[6:7], v[12:15]\nv_mfma_f32_16x16x16_bf16 v[16:19], a[96:97], v[6:7], v[16:19]\nv_mfma_f32_16x16x16_bf16 v[20:23], v[72:73], v[6:7], v[20:23]\nv_mfma_f32_16x16x16_bf16 v[8:11], a[82:83], v[6:7], v[8:11]\n" \
"v_mfma_f32_16x16x16_bf16 v[12:15], a[90:91], v[6:7], v[12:15]\nv_mfma_f32_16x16x16_bf16 v[16:19], a[98:99], v[6:7], v[16:19]\nv_mfma_f32_16x16x16_bf16 v[20:23], v[74:75], v[6:7], v[20:23]\ns_waitcnt vmcnt(28)\ns_barrier\ns_add_u32 m0, %[tb], 4096\ns_add_u32 s44, %[wo], 4358144\nbuffer_load_dwordx4 v92, %[rw], s44 offen nt lds\n" \
"s_add_u32 m0, %[tb], 5120\ns_add_u32 s44, %[wo], 4415488\nbuffer_load_dwordx4 v92, %[rw], s44 offen nt lds\ns_add_u32 m0, %[tb], 6144\ns_add_u32 s44, %[wo], 4472832\nbuffer_load_dwordx4 v92, %[rw], s44 offen nt lds\ns_add_u32 m0, %[tb], 7168\ns_add_u32 s44, %[wo], 4530176\n" \
"buffer_load_dwordx4 v92, %[rw], s44 offen nt lds\nds_read_b64_tr_b16 a[84:85], v93 offset:8192\nds_read_b64_tr_b16 a[86:87], v93 offset:8704\nds_read_b64_tr_b16 a[92:93], v93 offset:8200\nds_read_b64_tr_b16 a[94:95], v93 offset:8712\nds_read_b64_tr_b16 a[100:101], v93 offset:20480\nds_read_b64_tr_b16 a[102:103], v93 offset:20992\nds_read_b64_tr_b16 v[68:69], v93 offset:20488\n" \
"ds_read_b64_tr_b16 v[70:71], v93 offset:21000\ns_waitcnt lgkmcnt(0)\nv_mfma_f32_16x16x16_bf16 v[8:11], a[84:85], v[6:7], v[8:11]\nv_mfma_f32_16x16x16_bf16 v[12:15], a[92:93], v[6:7], v[12:15]\nv_mfma_f32_16x16x16_bf16 v[16:19], a[100:101], v[6:7], v[16:19]\nv_mfma_f32_16x16x16_bf16 v[20:23], v[68:69], v[6:7], v[20:23]\nv_mfma_f32_16x16x16_bf16 v[8:11], a[86:87], v[6:7], v[8:11]\nv_mfma_f32_16x16x16_bf16 v[12:15], a[94:95], v[6:7], v[12:15]\n" \
"v_mfma_f32_16x16x16_bf16 v[16:19], a[102:103], v[6:7], v[16:19]\nv_mfma_f32_16x16x16_bf16 v[20:23], v[70:71], v[6:7], v[20:23]\ns_waitcnt vmcnt(28)\ns_barrier\ns_add_u32 m0, %[tb], 8192\ns_add_u32 s44, %[wo], 4587520\nbuffer_load_dwordx4 v92, %[rw], s44 offen nt lds\ns_add_u32 m0, %[tb], 9216\n" \
"s_add_u32 s44, %[wo], 4644864\nbuffer_load_dwordx4 v92, %[rw], s44 offen nt lds\ns_add_u32 m0, %[tb], 10240\ns_add_u32 s44, %[wo], 4702208\nbuffer_load_dwordx4 v92, %[rw], s44 offen nt lds\ns_add_u32 m0, %[tb], 11264\ns_add_u32 s44, %[wo], 4759552\nbuffer_load_dwordx4 v92, %[rw], s44 offen nt lds\n" \
"ds_read_b64_tr_b16 a[136:137], v94 offset:0\nds_read_b64_tr_b16 a[138:139], v94 offset:512\nds_read_b64_tr_b16 v[64:65], v94 offset:8\nds_read_b64_tr_b16 v[66:67], v94 offset:520\nds_read_b64_tr_b16 a[144:145], v94 offset:24576\nds_read_b64_tr_b16 a[146:147], v94 offset:25088\nds_read_b64_tr_b16 a[152:153], v94 offset:24584\nds_read_b64_tr_b16 a[154:155], v94 offset:25096\n" \
"s_waitcnt lgkmcnt(0)\nv_mfma_f32_16x16x16_bf16 v[8:11], a[136:137], v[6:7], v[8:11]\nv_mfma_f32_16x16x16_bf16 v[12:15], v[64:65], v[6:7], v[12:15]\nv_mfma_f32_16x16x16_bf16 v[16:19], a[144:145], v[6:7], v[16:19]\nv_mfma_f32_16x16x16_bf16 v[20:23], a[152:153], v[6:7], v[20:23]\nv_mfma_f32_16x16x16_bf16 v[8:11], a[138:139], v[6:7], v[8:11]\nv_mfma_f32_16x16x16_bf16 v[12:15], v[66:67], v[6:7], v[12:15]\nv_mfma_f32_16x16x16_bf16 v[16:19], a[146:147], v[6:7], v[16:19]\n" \
"v_mfma_f32_16x16x16_bf16 v[20:23], a[154:155], v[6:7], v[20:23]\ns_waitcnt vmcnt(28)\ns_barrier\ns_add_u32 m0, %[lw], 0\ns_add_u32 s44, %[wo], 4816896\nbuffer_load_dwordx4 v92, %[rw], s44 offen nt lds\ns_add_u32 m0, %[lw], 1024\ns_add_u32 s44, %[wo], 4874240\n" \
"buffer_load_dwordx4 v92, %[rw], s44 offen nt lds\ns_add_u32 m0, %[lw], 2048\ns_add_u32 s44, %[wo], 4931584\nbuffer_load_dwordx4 v92, %[rw], s44 offen nt lds\ns_add_u32 m0, %[lw], 3072\ns_add_u32 s44, %[wo], 4988928\nbuffer_load_dwordx4 v92, %[rw], s44 offen nt lds\nds_read_b64_tr_b16 a[140:141], v94 offset:4096\n" \
"ds_read_b64_tr_b16 a[142:143], v94 offset:4608\nds_read_b64_tr_b16 v[60:61], v94 offset:4104\nds_read_b64_tr_b16 v[62:63], v94 offset:4616\nds_read_b64_tr_b16 a[148:149], v94 offset:28672\nds_read_b64_tr_b16 a[150:151], v94 offset:29184\nds_read_b64_tr_b16 a[156:157], v94 offset:28680\nds_read_b64_tr_b16 a[158:159], v94 offset:29192\ns_waitcnt lgkmcnt(0)\n" \
"v_mfma_f32_16x16x16_bf16 v[8:11], a[140:141], v[6:7], v[8:11]\nv_mfma_f32_16x16x16_bf16 v[12:15], v[60:61], v[6:7], v[12:15]\nv_mfma_f32_16x16x16_bf16 v[16:19], a[148:149], v[6:7], v[16:19]\nv_mfma_f32_16x16x16_bf16 v[20:23], a[156:157], v[6:7], v[20:23]\nv_mfma_f32_16x16x16_bf16 v[8:11], a[142:143], v[6:7], v[8:11]\nv_mfma_f32_16x16x16_bf16 v[12:15], v[62:63], v[6:7], v[12:15]\nv_mfma_f32_16x16x16_bf16 v[16:19], a[150:151], v[6:7], v[16:19]\nv_mfma_f32_16x16x16_bf16 v[20:23], a[158:159], v[6:7], v[20:23]\n" \
"s_waitcnt vmcnt(28)\ns_barrier\ns_add_u32 m0, %[lw], 4096\ns_add_u32 s44, %[wo], 5046272\nbuffer_load_dwordx4 v92, %[rw], s44 offen nt lds\ns_add_u32 m0, %[lw], 5120\ns_add_u32 s44, %[wo], 5103616\nbuffer_load_dwordx4 v92, %[rw], s44 offen nt lds\n" \
"s_add_u32 m0, %[lw], 6144\ns_add_u32 s44, %[wo], 5160960\nbuffer_load_dwordx4 v92, %[rw], s44 offen nt lds\ns_add_u32 m0, %[lw], 7168\ns_add_u32 s44, %[wo], 5218304\nbuffer_load_dwordx4 v92, %[rw], s44 offen nt lds\nds_read_b64_tr_b16 a[160:161], v94 offset:8192\nds_read_b64_tr_b16 a[162:163], v94 offset:8704\n" \
"ds_read_b64_tr_b16 a[168:169], v94 offset:8200\nds_read_b64_tr_b16 a[170:171], v94 offset:8712\nds_read_b64_tr_b16 v[56:57], v94 offset:32768\nds_read_b64_tr_b16 v[58:59], v94 offset:33280\nds_read_b64_tr_b16 a[176:177], v94 offset:32776\nds_read_b64_tr_b16 a[178:179], v94 offset:33288\ns_waitcnt lgkmcnt(0)\nv_mfma_f32_16x16x16_bf16 v[8:11], a[160:161], v[6:7], v[8:11]\n" \
"v_mfma_f32_16x16x16_bf16 v[12:15], a[168:169], v[6:7], v[12:15]\nv_mfma_f32_16x16x16_bf16 v[16:19], v[56:57], v[6:7], v[16:19]\nv_mfma_f32_16x16x16_bf16 v[20:23], a[176:177], v[6:7], v[20:23]\nv_mfma_f32_16x16x16_bf16 v[8:11], a[162:163], v[6:7], v[8:11]\nv_mfma_f32_16x16x16_bf16 v[12:15], a[170:171], v[6:7], v[12:15]\nv_mfma_f32_16x16x16_bf16 v[16:19], v[58:59], v[6:7], v[16:19]\nv_mfma_f32_16x16x16_bf16 v[20:23], a[178:179], v[6:7], v[20:23]\ns_waitcnt vmcnt(28)\n" \
"s_barrier\ns_add_u32 m0, %[lw], 8192\ns_add_u32 s44, %[wo], 5275648\nbuffer_load_dwordx4 v92, %[rw], s44 offen nt lds\ns_add_u32 m0, %[lw], 9216\ns_add_u32 s44, %[wo], 5332992\nbuffer_load_dwordx4 v92, %[rw], s44 offen nt lds\ns_add_u32 m0, %[lw], 10240\n" \
"s_add_u32 s44, %[wo], 5390336\nbuffer_load_dwordx4 v92, %[rw], s44 offen nt lds\ns_add_u32 m0, %[lw], 11264\ns_add_u32 s44, %[wo], 5447680\nbuffer_load_dwordx4 v92, %[rw], s44 offen nt lds\nds_read_b64_tr_b16 a[164:165], v94 offset:12288\nds_read_b64_tr_b16 a[166:167], v94 offset:12800\nds_read_b64_tr_b16 a[172:173], v94 offset:12296\n" \
"ds_read_b64_tr_b16 a[174:175], v94 offset:12808\nds_read_b64_tr_b16 v[52:53], v94 offset:36864\nds_read_b64_tr_b16 v[54:55], v94 offset:37376\nds_read_b64_tr_b16 a[180:181], v94 offset:36872\nds_read_b64_tr_b16 a[182:183], v94 offset:37384\ns_waitcnt lgkmcnt(0)\nv_mfma_f32_16x16x16_bf16 v[8:11], a[164:165], v[6:7], v[8:11]\nv_mfma_f32_16x16x16_bf16 v[12:15], a[172:173], v[6:7], v[12:15]\n" \
"v_mfma_f32_16x16x16_bf16 v[16:19], v[52:53], v[6:7], v[16:19]\nv_mfma_f32_16x16x16_bf16 v[20:23], a[180:181], v[6:7], v[20:23]\nv_mfma_f32_16x16x16_bf16 v[8:11], a[166:167], v[6:7], v[8:11]\nv_mfma_f32_16x16x16_bf16 v[12:15], a[174:175], v[6:7], v[12:15]\nv_mfma_f32_16x16x16_bf16 v[16:19], v[54:55], v[6:7], v[16:19]\nv_mfma_f32_16x16x16_bf16 v[20:23], a[182:183], v[6:7], v[20:23]\ns_waitcnt vmcnt(28)\ns_barrier\n" \
"s_add_u32 m0, %[lw], 12288\ns_add_u32 s44, %[wo], 5963776\nbuffer_load_dwordx4 v92, %[rw], s44 offen nt lds\ns_add_u32 m0, %[lw], 13312\ns_add_u32 s44, %[wo], 6021120\nbuffer_load_dwordx4 v92, %[rw], s44 offen nt lds\ns_add_u32 m0, %[lw], 14336\ns_add_u32 s44, %[wo], 6078464\n" \
"buffer_load_dwordx4 v92, %[rw], s44 offen nt lds\ns_add_u32 m0, %[lw], 15360\ns_add_u32 s44, %[wo], 6135808\nbuffer_load_dwordx4 v92, %[rw], s44 offen nt lds\nds_read_b64_tr_b16 a[184:185], v94 offset:16384\nds_read_b64_tr_b16 a[186:187], v94 offset:16896\nds_read_b64_tr_b16 a[192:193], v94 offset:16392\nds_read_b64_tr_b16 a[194:195], v94 offset:16904\n" \
"ds_read_b64_tr_b16 a[200:201], v94 offset:40960\nds_read_b64_tr_b16 a[202:203], v94 offset:41472\nds_read_b64_tr_b16 v[48:49], v94 offset:40968\nds_read_b64_tr_b16 v[50:51], v94 offset:41480\ns_waitcnt lgkmcnt(0)\nv_mfma_f32_16x16x16_bf16 v[8:11], a[184:185], v[6:7], v[8:11]\nv_mfma_f32_16x16x16_bf16 v[12:15], a[192:193], v[6:7], v[12:15]\nv_mfma_f32_16x16x16_bf16 v[16:19], a[200:201], v[6:7], v[16:19]\n" \
"v_mfma_f32_16x16x16_bf16 v[20:23], v[48:49], v[6:7], v[20:23]\nv_mfma_f32_16x16x16_bf16 v[8:11], a[186:187], v[6:7], v[8:11]\nv_mfma_f32_16x16x16_bf16 v[12:15], a[194:195], v[6:7], v[12:15]\nv_mfma_f32_16x16x16_bf16 v[16:19], a[202:203], v[6:7], v[16:19]\nv_mfma_f32_16x16x16_bf16 v[20:23], v[50:51], v[6:7], v[20:23]\ns_waitcnt vmcnt(28)\ns_barrier\ns_add_u32 m0, %[lw], 16384\n" \
"s_add_u32 s44, %[wo], 6193152\nbuffer_load_dwordx4 v92, %[rw], s44 offen nt lds\ns_add_u32 m0, %[lw], 17408\ns_add_u32 s44, %[wo], 6250496\nbuffer_load_dwordx4 v92, %[rw], s44 offen nt lds\ns_add_u32 m0, %[lw], 18432\ns_add_u32 s44, %[wo], 6307840\nbuffer_load_dwordx4 v92, %[rw], s44 offen nt lds\n" \
"s_add_u32 m0, %[lw], 19456\ns_add_u32 s44, %[wo], 6365184\nbuffer_load_dwordx4 v92, %[rw], s44 offen nt lds\nds_read_b64_tr_b16 a[188:189], v94 offset:20480\nds_read_b64_tr_b16 a[190:191], v94 offset:20992\nds_read_b64_tr_b16 a[196:197], v94 offset:20488\nds_read_b64_tr_b16 a[198:199], v94 offset:21000\nds_read_b64_tr_b16 a[204:205], v94 offset:45056\n" \
"ds_read_b64_tr_b16 a[206:207], v94 offset:45568\nds_read_b64_tr_b16 v[44:45], v94 offset:45064\nds_read_b64_tr_b16 v[46:47], v94 offset:45576\ns_waitcnt lgkmcnt(0)\nv_mfma_f32_16x16x16_bf16 v[8:11], a[188:189], v[6:7], v[8:11]\nv_mfma_f32_16x16x16_bf16 v[12:15], a[196:197], v[6:7], v[12:15]\nv_mfma_f32_16x16x16_bf16 v[16:19], a[204:205], v[6:7], v[16:19]\nv_mfma_f32_16x16x16_bf16 v[20:23], v[44:45], v[6:7], v[20:23]\n" \
"v_mfma_f32_16x16x16_bf16 v[8:11], a[190:191], v[6:7], v[8:11]\nv_mfma_f32_16x16x16_bf16 v[12:15], a[198:199], v[6:7], v[12:15]\nv_mfma_f32_16x16x16_bf16 v[16:19], a[206:207], v[6:7], v[16:19]\nv_mfma_f32_16x16x16_bf16 v[20:23], v[46:47], v[6:7], v[20:23]\ns_waitcnt vmcnt(28)\ns_barrier\ns_add_u32 m0, %[lw], 20480\ns_add_u32 s44, %[wo], 6422528\n" \
"buffer_load_dwordx4 v92, %[rw], s44 offen nt lds\ns_add_u32 m0, %[lw], 21504\ns_add_u32 s44, %[wo], 6479872\nbuffer_load_dwordx4 v92, %[rw], s44 offen nt lds\ns_add_u32 m0, %[lw], 22528\ns_add_u32 s44, %[wo], 6537216\nbuffer_load_dwordx4 v92, %[rw], s44 offen nt lds\ns_add_u32 m0, %[lw], 23552\n" \
"s_add_u32 s44, %[wo], 6594560\nbuffer_load_dwordx4 v92, %[rw], s44 offen nt lds\nds_read_b64_tr_b16 a[240:241], v93 offset:0\nds_read_b64_tr_b16 a[242:243], v93 offset:512\nds_read_b64_tr_b16 v[40:41], v93 offset:8\nds_read_b64_tr_b16 v[42:43], v93 offset:520\nds_read_b64_tr_b16 a[248:249], v93 offset:12288\nds_read_b64_tr_b16 a[250:251], v93 offset:12800\n" \
"ds_read_b64_tr_b16 v[96:97], v93 offset:12296\nds_read_b64_tr_b16 v[98:99], v93 offset:12808\ns_waitcnt lgkmcnt(0)\nv_mfma_f32_16x16x16_bf16 v[8:11], a[240:241], v[6:7], v[8:11]\nv_mfma_f32_16x16x16_bf16 v[12:15], v[40:41], v[6:7], v[12:15]\nv_mfma_f32_16x16x16_bf16 v[16:19], a[248:249], v[6:7], v[16:19]\nv_mfma_f32_16x16x16_bf16 v[20:23], v[96:97], v[6:7], v[20:23]\nv_mfma_f32_16x16x16_bf16 v[8:11], a[242:243], v[6:7], v[8:11]\n" \
"v_mfma_f32_16x16x16_bf16 v[12:15], v[42:43], v[6:7], v[12:15]\nv_mfma_f32_16x16x16_bf16 v[16:19], a[250:251], v[6:7], v[16:19]\nv_mfma_f32_16x16x16_bf16 v[20:23], v[98:99], v[6:7], v[20:23]\ns_waitcnt vmcnt(28)\ns_barrier\ns_add_u32 m0, %[tb], 0\ns_add_u32 s44, %[wo], 6651904\nbuffer_load_dwordx4 v92, %[rw], s44 offen nt lds\n" \
"s_add_u32 m0, %[tb], 1024\ns_add_u32 s44, %[wo], 6709248\nbuffer_load_dwordx4 v92, %[rw], s44 offen nt lds\ns_add_u32 m0, %[tb], 2048\ns_add_u32 s44, %[wo], 6766592\nbuffer_load_dwordx4 v92, %[rw], s44 offen nt lds\ns_add_u32 m0, %[tb], 3072\ns_add_u32 s44, %[wo], 6823936\n" \
"buffer_load_dwordx4 v92, %[rw], s44 offen nt lds\nds_read_b64_tr_b16 a[244:245], v93 offset:4096\nds_read_b64_tr_b16 a[246:247], v93 offset:4608\nds_read_b64_tr_b16 v[36:37], v93 offset:4104\nds_read_b64_tr_b16 v[38:39], v93 offset:4616\nds_read_b64_tr_b16 a[252:253], v93 offset:16384\nds_read_b64_tr_b16 a[254:255], v93 offset:16896\nds_read_b64_tr_b16 v[100:101], v93 offset:16392\n" \
"ds_read_b64_tr_b16 v[102:103], v93 offset:16904\ns_waitcnt lgkmcnt(0)\nv_mfma_f32_16x16x16_bf16 v[8:11], a[244:245], v[6:7], v[8:11]\nv_mfma_f32_16x16x16_bf16 v[12:15], v[36:37], v[6:7], v[12:15]\nv_mfma_f32_16x16x16_bf16 v[16:19], a[252:253], v[6:7], v[16:19]\nv_mfma_f32_16x16x16_bf16 v[20:23], v[100:101], v[6:7], v[20:23]\nv_mfma_f32_16x16x16_bf16 v[8:11], a[246:247], v[6:7], v[8:11]\nv_mfma_f32_16x16x16_bf16 v[12:15], v[38:39], v[6:7], v[12:15]\n" \
"v_mfma_f32_16x16x16_bf16 v[16:19], a[254:255], v[6:7], v[16:19]\nv_mfma_f32_16x16x16_bf16 v[20:23], v[102:103], v[6:7], v[20:23]\ns_waitcnt vmcnt(28)\ns_barrier\ns_add_u32 m0, %[tb], 4096\ns_add_u32 s44, %[wo], 6881280\nbuffer_load_dwordx4 v92, %[rw], s44 offen nt lds\ns_add_u32 m0, %[tb], 5120\n" \
"s_add_u32 s44, %[wo], 6938624\nbuffer_load_dwordx4 v92, %[rw], s44 offen nt lds\ns_add_u32 m0, %[tb], 6144\ns_add_u32 s44, %[wo], 6995968\nbuffer_load_dwordx4 v92, %[rw], s44 offen nt lds\ns_add_u32 m0, %[tb], 7168\ns_add_u32 s44, %[wo], 7053312\nbuffer_load_dwordx4 v92, %[rw], s44 offen nt lds\n" \
"ds_read_b64_tr_b16 v[104:105], v93 offset:8192\nds_read_b64_tr_b16 v[106:107], v93 offset:8704\nds_read_b64_tr_b16 v[112:113], v93 offset:8200\nds_read_b64_tr_b16 v[114:115], v93 offset:8712\nds_read_b64_tr_b16 v[32:33], v93 offset:20480\nds_read_b64_tr_b16 v[34:35], v93 offset:20992\nds_read_b64_tr_b16 v[120:121], v93 offset:20488\nds_read_b64_tr_b16 v[122:123], v93 offset:21000\n" \
"s_waitcnt lgkmcnt(0)\nv_mfma_f32_16x16x16_bf16 v[8:11], v[104:105], v[6:7], v[8:11]\nv_mfma_f32_16x16x16_bf16 v[12:15], v[112:113], v[6:7], v[12:15]\nv_mfma_f32_16x16x16_bf16 v[16:19], v[32:33], v[6:7], v[16:19]\nv_mfma_f32_16x16x16_bf16 v[20:23], v[120:121], v[6:7], v[20:23]\nv_mfma_f32_16x16x16_bf16 v[8:11], v[106:107], v[6:7], v[8:11]\nv_mfma_f32_16x16x16_bf16 v[12:15], v[114:115], v[6:7], v[12:15]\nv_mfma_f32_16x16x16_bf16 v[16:19], v[34:35], v[6:7], v[16:19]\n" \
"v_mfma_f32_16x16x16_bf16 v[20:23], v[122:123], v[6:7], v[20:23]\ns_waitcnt vmcnt(28)\ns_barrier\ns_add_u32 m0, %[tb], 8192\ns_add_u32 s44, %[wo], 7110656\nbuffer_load_dwordx4 v92, %[rw], s44 offen nt lds\ns_add_u32 m0, %[tb], 9216\ns_add_u32 s44, %[wo], 7168000\n" \
"buffer_load_dwordx4 v92, %[rw], s44 offen nt lds\ns_add_u32 m0, %[tb], 10240\ns_add_u32 s44, %[wo], 7225344\nbuffer_load_dwordx4 v92, %[rw], s44 offen nt lds\ns_add_u32 m0, %[tb], 11264\ns_add_u32 s44, %[wo], 7282688\nbuffer_load_dwordx4 v92, %[rw], s44 offen nt lds\nds_read_b64_tr_b16 v[108:109], v94 offset:0\n" \
"ds_read_b64_tr_b16 v[110:111], v94 offset:512\nds_read_b64_tr_b16 v[116:117], v94 offset:8\nds_read_b64_tr_b16 v[118:119], v94 offset:520\nds_read_b64_tr_b16 v[28:29], v94 offset:24576\nds_read_b64_tr_b16 v[30:31], v94 offset:25088\nds_read_b64_tr_b16 v[124:125], v94 offset:24584\nds_read_b64_tr_b16 v[126:127], v94 offset:25096\ns_waitcnt lgkmcnt(0)\n" \
"v_mfma_f32_16x16x16_bf16 v[8:11], v[108:109], v[6:7], v[8:11]\nv_mfma_f32_16x16x16_bf16 v[12:15], v[116:117], v[6:7], v[12:15]\nv_mfma_f32_16x16x16_bf16 v[16:19], v[28:29], v[6:7], v[16:19]\nv_mfma_f32_16x16x16_bf16 v[20:23], v[124:125], v[6:7], v[20:23]\nv_mfma_f32_16x16x16_bf16 v[8:11], v[110:111], v[6:7], v[8:11]\nv_mfma_f32_16x16x16_bf16 v[12:15], v[118:119], v[6:7], v[12:15]\nv_mfma_f32_16x16x16_bf16 v[16:19], v[30:31], v[6:7], v[16:19]\nv_mfma_f32_16x16x16_bf16 v[20:23], v[126:127], v[6:7], v[20:23]\n" \
"s_waitcnt vmcnt(28)\ns_barrier\nds_read_b64_tr_b16 v[128:129], v94 offset:4096\nds_read_b64_tr_b16 v[130:131], v94 offset:4608\nds_read_b64_tr_b16 v[136:137], v94 offset:4104\nds_read_b64_tr_b16 v[138:139], v94 offset:4616\nds_read_b64_tr_b16 v[144:145], v94 offset:28672\nds_read_b64_tr_b16 v[146:147], v94 offset:29184\n" \
"ds_read_b64_tr_b16 v[24:25], v94 offset:28680\nds_read_b64_tr_b16 v[26:27], v94 offset:29192\ns_waitcnt lgkmcnt(0)\nv_mfma_f32_16x16x16_bf16 v[8:11], v[128:129], v[6:7], v[8:11]\nv_mfma_f32_16x16x16_bf16 v[12:15], v[136:137], v[6:7], v[12:15]\nv_mfma_f32_16x16x16_bf16 v[16:19], v[144:145], v[6:7], v[16:19]\nv_mfma_f32_16x16x16_bf16 v[20:23], v[24:25], v[6:7], v[20:23]\nv_mfma_f32_16x16x16_bf16 v[8:11], v[130:131], v[6:7], v[8:11]\n" \
"v_mfma_f32_16x16x16_bf16 v[12:15], v[138:139], v[6:7], v[12:15]\nv_mfma_f32_16x16x16_bf16 v[16:19], v[146:147], v[6:7], v[16:19]\nv_mfma_f32_16x16x16_bf16 v[20:23], v[26:27], v[6:7], v[20:23]\ns_waitcnt vmcnt(24)\nds_write_b128 v1, v[88:91] offset:0\nds_write_b128 v1, v[84:87] offset:1024\nds_write_b128 v1, v[80:83] offset:2048\nds_write_b128 v1, v[76:79] offset:3072\n" \
"s_barrier\nds_read_b64_tr_b16 v[132:133], v94 offset:8192\nds_read_b64_tr_b16 v[134:135], v94 offset:8704\nds_read_b64_tr_b16 v[140:141], v94 offset:8200\nds_read_b64_tr_b16 v[142:143], v94 offset:8712\nds_read_b64_tr_b16 v[148:149], v94 offset:32768\nds_read_b64_tr_b16 v[150:151], v94 offset:33280\nds_read_b64_tr_b16 v[76:77], v94 offset:32776\n" \
"ds_read_b64_tr_b16 v[78:79], v94 offset:33288\ns_waitcnt lgkmcnt(0)\nv_mfma_f32_16x16x16_bf16 v[8:11], v[132:133], v[6:7], v[8:11]\nv_mfma_f32_16x16x16_bf16 v[12:15], v[140:141], v[6:7], v[12:15]\nv_mfma_f32_16x16x16_bf16 v[16:19], v[148:149], v[6:7], v[16:19]\nv_mfma_f32_16x16x16_bf16 v[20:23], v[76:77], v[6:7], v[20:23]\nv_mfma_f32_16x16x16_bf16 v[8:11], v[134:135], v[6:7], v[8:11]\nv_mfma_f32_16x16x16_bf16 v[12:15], v[142:143], v[6:7], v[12:15]\n" \
"v_mfma_f32_16x16x16_bf16 v[16:19], v[150:151], v[6:7], v[16:19]\nv_mfma_f32_16x16x16_bf16 v[20:23], v[78:79], v[6:7], v[20:23]\ns_waitcnt vmcnt(20)\nds_write_b128 v1, v[72:75] offset:4096\nds_write_b128 v1, v[68:71] offset:5120\nds_write_b128 v1, v[64:67] offset:6144\nds_write_b128 v1, v[60:63] offset:7168\ns_barrier\n" \
"ds_read_b64_tr_b16 v[184:185], v94 offset:12288\nds_read_b64_tr_b16 v[186:187], v94 offset:12800\nds_read_b64_tr_b16 v[60:61], v94 offset:12296\nds_read_b64_tr_b16 v[62:63], v94 offset:12808\nds_read_b64_tr_b16 v[192:193], v94 offset:36864\nds_read_b64_tr_b16 v[194:195], v94 offset:37376\nds_read_b64_tr_b16 v[200:201], v94 offset:36872\nds_read_b64_tr_b16 v[202:203], v94 offset:37384\n" \
"s_waitcnt lgkmcnt(0)\nv_mfma_f32_16x16x16_bf16 v[8:11], v[184:185], v[6:7], v[8:11]\nv_mfma_f32_16x16x16_bf16 v[12:15], v[60:61], v[6:7], v[12:15]\nv_mfma_f32_16x16x16_bf16 v[16:19], v[192:193], v[6:7], v[16:19]\nv_mfma_f32_16x16x16_bf16 v[20:23], v[200:201], v[6:7], v[20:23]\nv_mfma_f32_16x16x16_bf16 v[8:11], v[186:187], v[6:7], v[8:11]\nv_mfma_f32_16x16x16_bf16 v[12:15], v[62:63], v[6:7], v[12:15]\nv_mfma_f32_16x16x16_bf16 v[16:19], v[194:195], v[6:7], v[16:19]\n" \
"v_mfma_f32_16x16x16_bf16 v[20:23], v[202:203], v[6:7], v[20:23]\ns_waitcnt vmcnt(16)\nds_write_b128 v1, v[56:59] offset:8192\nds_write_b128 v1, v[52:55] offset:9216\nds_write_b128 v1, v[48:51] offset:10240\nds_write_b128 v1, v[44:47] offset:11264\ns_barrier\nds_read_b64_tr_b16 v[188:189], v94 offset:16384\n" \
"ds_read_b64_tr_b16 v[190:191], v94 offset:16896\nds_read_b64_tr_b16 v[44:45], v94 offset:16392\nds_read_b64_tr_b16 v[46:47], v94 offset:16904\nds_read_b64_tr_b16 v[196:197], v94 offset:40960\nds_read_b64_tr_b16 v[198:199], v94 offset:41472\nds_read_b64_tr_b16 v[204:205], v94 offset:40968\nds_read_b64_tr_b16 v[206:207], v94 offset:41480\ns_waitcnt lgkmcnt(0)\n" \
"v_mfma_f32_16x16x16_bf16 v[8:11], v[188:189], v[6:7], v[8:11]\nv_mfma_f32_16x16x16_bf16 v[12:15], v[44:45], v[6:7], v[12:15]\nv_mfma_f32_16x16x16_bf16 v[16:19], v[196:197], v[6:7], v[16:19]\nv_mfma_f32_16x16x16_bf16 v[20:23], v[204:205], v[6:7], v[20:23]\nv_mfma_f32_16x16x16_bf16 v[8:11], v[190:191], v[6:7], v[8:11]\nv_mfma_f32_16x16x16_bf16 v[12:15], v[46:47], v[6:7], v[12:15]\nv_mfma_f32_16x16x16_bf16 v[16:19], v[198:199], v[6:7], v[16:19]\nv_mfma_f32_16x16x16_bf16 v[20:23], v[206:207], v[6:7], v[20:23]\n" \
"s_waitcnt vmcnt(12)\nds_write_b128 v1, v[40:43] offset:12288\nds_write_b128 v1, v[36:39] offset:13312\nds_write_b128 v1, v[32:35] offset:14336\nds_write_b128 v1, v[28:31] offset:15360\ns_barrier\nds_read_b64_tr_b16 v[208:209], v94 offset:20480\nds_read_b64_tr_b16 v[210:211], v94 offset:20992\n" \
"ds_read_b64_tr_b16 v[216:217], v94 offset:20488\nds_read_b64_tr_b16 v[218:219], v94 offset:21000\nds_read_b64_tr_b16 v[28:29], v94 offset:45056\nds_read_b64_tr_b16 v[30:31], v94 offset:45568\nds_read_b64_tr_b16 v[224:225], v94 offset:45064\nds_read_b64_tr_b16 v[226:227], v94 offset:45576\ns_waitcnt lgkmcnt(0)\nv_mfma_f32_16x16x16_bf16 v[8:11], v[208:209], v[6:7], v[8:11]\n" \
"v_mfma_f32_16x16x16_bf16 v[12:15], v[216:217], v[6:7], v[12:15]\nv_mfma_f32_16x16x16_bf16 v[16:19], v[28:29], v[6:7], v[16:19]\nv_mfma_f32_16x16x16_bf16 v[20:23], v[224:225], v[6:7], v[20:23]\nv_mfma_f32_16x16x16_bf16 v[8:11], v[210:211], v[6:7], v[8:11]\nv_mfma_f32_16x16x16_bf16 v[12:15], v[218:219], v[6:7], v[12:15]\nv_mfma_f32_16x16x16_bf16 v[16:19], v[30:31], v[6:7], v[16:19]\nv_mfma_f32_16x16x16_bf16 v[20:23], v[226:227], v[6:7], v[20:23]\ns_waitcnt vmcnt(8)\n" \
"ds_write_b128 v1, v[24:27] offset:16384\nds_write_b128 v1, v[76:79] offset:17408\nds_write_b128 v1, v[60:63] offset:18432\nds_write_b128 v1, v[44:47] offset:19456\ns_barrier\nds_read_b64_tr_b16 v[212:213], v93 offset:0\nds_read_b64_tr_b16 v[214:215], v93 offset:512\nds_read_b64_tr_b16 v[220:221], v93 offset:8\n" \
"ds_read_b64_tr_b16 v[222:223], v93 offset:520\nds_read_b64_tr_b16 v[44:45], v93 offset:12288\nds_read_b64_tr_b16 v[46:47], v93 offset:12800\nds_read_b64_tr_b16 v[228:229], v93 offset:12296\nds_read_b64_tr_b16 v[230:231], v93 offset:12808\ns_waitcnt lgkmcnt(0)\nv_mfma_f32_16x16x16_bf16 v[8:11], v[212:213], v[6:7], v[8:11]\nv_mfma_f32_16x16x16_bf16 v[12:15], v[220:221], v[6:7], v[12:15]\n" \
"v_mfma_f32_16x16x16_bf16 v[16:19], v[44:45], v[6:7], v[16:19]\nv_mfma_f32_16x16x16_bf16 v[20:23], v[228:229], v[6:7], v[20:23]\nv_mfma_f32_16x16x16_bf16 v[8:11], v[214:215], v[6:7], v[8:11]\nv_mfma_f32_16x16x16_bf16 v[12:15], v[222:223], v[6:7], v[12:15]\nv_mfma_f32_16x16x16_bf16 v[16:19], v[46:47], v[6:7], v[16:19]\nv_mfma_f32_16x16x16_bf16 v[20:23], v[230:231], v[6:7], v[20:23]\ns_waitcnt vmcnt(4)\nds_write_b128 v1, v[28:31] offset:20480\n" \
"ds_write_b128 v1, v[44:47] offset:21504\ns_barrier\nds_read_b64_tr_b16 v[232:233], v93 offset:4096\nds_read_b64_tr_b16 v[234:235], v93 offset:4608\nds_read_b64_tr_b16 v[240:241], v93 offset:4104\nds_read_b64_tr_b16 v[242:243], v93 offset:4616\nds_read_b64_tr_b16 v[248:249], v93 offset:16384\nds_read_b64_tr_b16 v[250:251], v93 offset:16896\n" \
"ds_read_b64_tr_b16 v[44:45], v93 offset:16392\nds_read_b64_tr_b16 v[46:47], v93 offset:16904\ns_waitcnt lgkmcnt(0)\nv_mfma_f32_16x16x16_bf16 v[8:11], v[232:233], v[6:7], v[8:11]\nv_mfma_f32_16x16x16_bf16 v[12:15], v[240:241], v[6:7], v[12:15]\nv_mfma_f32_16x16x16_bf16 v[16:19], v[248:249], v[6:7], v[16:19]\nv_mfma_f32_16x16x16_bf16 v[20:23], v[44:45], v[6:7], v[20:23]\nv_mfma_f32_16x16x16_bf16 v[8:11], v[234:235], v[6:7], v[8:11]\n" \
"v_mfma_f32_16x16x16_bf16 v[12:15], v[242:243], v[6:7], v[12:15]\nv_mfma_f32_16x16x16_bf16 v[16:19], v[250:251], v[6:7], v[16:19]\nv_mfma_f32_16x16x16_bf16 v[20:23], v[46:47], v[6:7], v[20:23]\ns_waitcnt vmcnt(0)\nds_write_b128 v1, v[44:47] offset:22528\ns_barrier\nds_read_b64_tr_b16 v[236:237], v93 offset:8192\nds_read_b64_tr_b16 v[238:239], v93 offset:8704\n" \
"ds_read_b64_tr_b16 v[244:245], v93 offset:8200\nds_read_b64_tr_b16 v[246:247], v93 offset:8712\nds_read_b64_tr_b16 v[252:253], v93 offset:20480\nds_read_b64_tr_b16 v[254:255], v93 offset:20992\nds_read_b64_tr_b16 v[44:45], v93 offset:20488\nds_read_b64_tr_b16 v[46:47], v93 offset:21000\ns_waitcnt lgkmcnt(0)\nv_mfma_f32_16x16x16_bf16 v[8:11], v[236:237], v[6:7], v[8:11]\n" \
"v_mfma_f32_16x16x16_bf16 v[12:15], v[244:245], v[6:7], v[12:15]\nv_mfma_f32_16x16x16_bf16 v[16:19], v[252:253], v[6:7], v[16:19]\nv_mfma_f32_16x16x16_bf16 v[20:23], v[44:45], v[6:7], v[20:23]\nv_mfma_f32_16x16x16_bf16 v[8:11], v[238:239], v[6:7], v[8:11]\nv_mfma_f32_16x16x16_bf16 v[12:15], v[246:247], v[6:7], v[12:15]\nv_mfma_f32_16x16x16_bf16 v[16:19], v[254:255], v[6:7], v[16:19]\nv_mfma_f32_16x16x16_bf16 v[20:23], v[46:47], v[6:7], v[20:23]\nds_write_b128 v1, v[44:47] offset:23552\n" \
"s_waitcnt lgkmcnt(0)\ns_barrier\ns_mov_b32 s40, 0\ns_mov_b32 s43, 0\ns_lshl_b32 s57, %[w], 12\ns_mov_b32 s58, 0xbeeff558\ns_mov_b32 s59, 0x409d30fc\ns_mov_b32 s56, -2147483648\n" \
".Lgg_next%=:\ns_cmp_lg_u32 %[w], 0\ns_cbranch_scc1 .Lgg_w1%=\nbuffer_load_dword v56, v3, %[rs], 0 offen sc1\ns_waitcnt vmcnt(0)\nbuffer_store_dword v90, off, %[rs], s56 sc1\n.Lgg_poll%=:\nv_cmp_gt_i32 vcc, s40, v56\n" \
"s_cbranch_vccz .Lgg_go%=\ns_sleep 12\nbuffer_load_dword v56, v3, %[rs], 0 offen sc1\ns_waitcnt vmcnt(0)\ns_add_u32 s43, s43, 1\ns_cmp_lt_u32 s43, 262144\ns_cbranch_scc1 .Lgg_poll%=\ns_branch .Lgg_abort%=\n" \
".Lgg_w1%=:\ns_waitcnt vmcnt(0)\nbuffer_store_dword v90, off, %[rs], s56 sc1\n.Lgg_go%=:\ns_add_u32 s44, s40, 0\ns_lshl_b32 s44, s44, 17\ns_add_u32 s44, s44, %[xn]\ns_lshr_b32 s45, s44, 8\n" \
"s_and_b32 s45, s45, 0x2000\ns_xor_b32 s41, s44, s45\ns_add_u32 s61, s41, %[xw]\ns_barrier\ns_add_u32 m0, s57, 0\ns_add_u32 s60, s61, 0\nbuffer_load_dwordx4 v0, %[rs], s60 offen offset:0 sc1 lds\nbuffer_load_dwordx4 v0, %[rs], s60 offen offset:1024 sc1 lds\n" \
"buffer_load_dwordx4 v0, %[rs], s60 offen offset:2048 sc1 lds\nbuffer_load_dwordx4 v0, %[rs], s60 offen offset:3072 sc1 lds\ns_add_u32 m0, s57, 16384\ns_add_u32 s60, s61, 32768\nbuffer_load_dwordx4 v0, %[rs], s60 offen offset:0 sc1 lds\nbuffer_load_dwordx4 v0, %[rs], s60 offen offset:1024 sc1 lds\nbuffer_load_dwordx4 v0, %[rs], s60 offen offset:2048 sc1 lds\nbuffer_load_dwordx4 v0, %[rs], s60 offen offset:3072 sc1 lds\n" \
"buffer_load_dwordx4 v[40:43], v5, %[rs], s41 offen offset:0 sc1\nbuffer_load_dwordx4 v[44:47], v5, %[rs], s41 offen offset:256 sc1\ns_lshl_b32 s44, s40, 19\ns_add_u32 s44, s44, %[ho]\ns_lshr_b32 s45, s44, 8\ns_and_b32 s45, s45, 0x2000\ns_xor_b32 s55, s44, s45\ns_waitcnt vmcnt(6)\n" \
"s_barrier\nds_read_b128 v[56:59], v2 offset:0\nds_read_b128 v[60:63], v2 offset:2048\nds_read_b128 v[64:67], v2 offset:4096\ns_waitcnt lgkmcnt(2)\nv_mfma_f32_16x16x32_bf16 v[24:27], a[0:3], v[56:59], 0\nds_read_b128 v[68:71], v2 offset:6144\nds_read_b128 v[80:83], v1 offset:0\n" \
"v_mfma_f32_16x16x32_bf16 v[28:31], a[8:11], v[56:59], 0\nbuffer_load_dwordx4 v[48:51], v5, %[rs], s41 offen offset:512 sc1\nv_mfma_f32_16x16x32_bf16 v[32:35], a[16:19], v[56:59], 0\nv_mfma_f32_16x16x32_bf16 v[36:39], a[24:27], v[56:59], 0\ns_waitcnt lgkmcnt(3)\nds_read_b128 v[72:75], v1 offset:1024\nv_mfma_f32_16x16x32_bf16 v[24:27], a[4:7], v[60:63], v[24:27]\nds_read_b128 v[56:59], v2 offset:8192\n" \
"v_mfma_f32_16x16x32_bf16 v[28:31], a[12:15], v[60:63], v[28:31]\nbuffer_load_dwordx4 v[52:55], v5, %[rs], s41 offen offset:768 sc1\nv_mfma_f32_16x16x32_bf16 v[32:35], a[20:23], v[60:63], v[32:35]\nv_mfma_f32_16x16x32_bf16 v[36:39], a[28:31], v[60:63], v[36:39]\ns_waitcnt lgkmcnt(4)\nds_read_b128 v[76:79], v1 offset:2048\nv_mfma_f32_16x16x32_bf16 v[24:27], a[32:35], v[64:67], v[24:27]\ns_waitcnt lgkmcnt(3)\n" \
"ds_read_b128 v[60:63], v2 offset:10240\nv_mfma_f32_16x16x32_bf16 v[28:31], v[80:83], v[64:67], v[28:31]\ns_add_u32 m0, s57, 32768\ns_add_u32 s60, s61, 65536\nbuffer_load_dwordx4 v0, %[rs], s60 offen offset:0 sc1 lds\nv_mfma_f32_16x16x32_bf16 v[32:35], a[40:43], v[64:67], v[32:35]\nv_mfma_f32_16x16x32_bf16 v[36:39], a[48:51], v[64:67], v[36:39]\nds_read_b128 v[80:83], v1 offset:3072\n" \
"v_mfma_f32_16x16x32_bf16 v[24:27], a[36:39], v[68:71], v[24:27]\ns_waitcnt lgkmcnt(4)\nds_read_b128 v[64:67], v2 offset:12288\nv_mfma_f32_16x16x32_bf16 v[28:31], v[72:75], v[68:71], v[28:31]\nbuffer_load_dwordx4 v0, %[rs], s60 offen offset:1024 sc1 lds\nv_mfma_f32_16x16x32_bf16 v[32:35], a[44:47], v[68:71], v[32:35]\nv_mfma_f32_16x16x32_bf16 v[36:39], a[52:55], v[68:71], v[36:39]\ns_waitcnt lgkmcnt(4)\n" \
"ds_read_b128 v[72:75], v1 offset:4096\nv_mfma_f32_16x16x32_bf16 v[24:27], a[56:59], v[56:59], v[24:27]\nds_read_b128 v[68:71], v2 offset:14336\nv_mfma_f32_16x16x32_bf16 v[28:31], a[64:67], v[56:59], v[28:31]\ns_waitcnt lgkmcnt(5)\nbuffer_load_dwordx4 v0, %[rs], s60 offen offset:2048 sc1 lds\nv_mfma_f32_16x16x32_bf16 v[32:35], v[76:79], v[56:59], v[32:35]\nv_mfma_f32_16x16x32_bf16 v[36:39], a[72:75], v[56:59], v[36:39]\n" \
"s_waitcnt lgkmcnt(4)\nds_read_b128 v[76:79], v1 offset:5120\nv_mfma_f32_16x16x32_bf16 v[24:27], a[60:63], v[60:63], v[24:27]\ns_waitcnt vmcnt(7)\ns_barrier\nds_read_b128 v[56:59], v2 offset:16384\nv_mfma_f32_16x16x32_bf16 v[28:31], a[68:71], v[60:63], v[28:31]\ns_waitcnt lgkmcnt(5)\n" \
"buffer_load_dwordx4 v0, %[rs], s60 offen offset:3072 sc1 lds\nv_mfma_f32_16x16x32_bf16 v[32:35], v[80:83], v[60:63], v[32:35]\nv_mfma_f32_16x16x32_bf16 v[36:39], a[76:79], v[60:63], v[36:39]\ns_waitcnt lgkmcnt(4)\nv_mfma_f32_16x16x32_bf16 v[24:27], a[80:83], v[64:67], v[24:27]\nds_read_b128 v[60:63], v2 offset:18432\nv_mfma_f32_16x16x32_bf16 v[28:31], a[88:91], v[64:67], v[28:31]\ns_add_u32 m0, s57, 49152\n" \
"s_add_u32 s60, s61, 98304\nbuffer_load_dwordx4 v0, %[rs], s60 offen offset:0 sc1 lds\nv_mfma_f32_16x16x32_bf16 v[32:35], a[96:99], v[64:67], v[32:35]\ns_waitcnt lgkmcnt(4)\nv_mfma_f32_16x16x32_bf16 v[36:39], v[72:75], v[64:67], v[36:39]\ns_waitcnt lgkmcnt(3)\nv_mfma_f32_16x16x32_bf16 v[24:27], a[84:87], v[68:71], v[24:27]\nds_read_b128 v[64:67], v2 offset:20480\n" \
"v_mfma_f32_16x16x32_bf16 v[28:31], a[92:95], v[68:71], v[28:31]\nbuffer_load_dwordx4 v0, %[rs], s60 offen offset:1024 sc1 lds\nv_mfma_f32_16x16x32_bf16 v[32:35], a[100:103], v[68:71], v[32:35]\ns_waitcnt lgkmcnt(3)\nv_mfma_f32_16x16x32_bf16 v[36:39], v[76:79], v[68:71], v[36:39]\ns_waitcnt lgkmcnt(2)\nds_read_b128 v[76:79], v1 offset:6144\nv_mfma_f32_16x16x32_bf16 v[24:27], a[104:107], v[56:59], v[24:27]\n" \
"ds_read_b128 v[68:71], v2 offset:22528\nv_mfma_f32_16x16x32_bf16 v[28:31], a[112:115], v[56:59], v[28:31]\nbuffer_load_dwordx4 v0, %[rs], s60 offen offset:2048 sc1 lds\nv_mfma_f32_16x16x32_bf16 v[32:35], a[120:123], v[56:59], v[32:35]\nv_mfma_f32_16x16x32_bf16 v[36:39], a[128:131], v[56:59], v[36:39]\ns_waitcnt lgkmcnt(3)\nds_read_b128 v[80:83], v1 offset:7168\nv_mfma_f32_16x16x32_bf16 v[24:27], a[108:111], v[60:63], v[24:27]\n" \
"ds_read_b128 v[56:59], v2 offset:24576\nv_mfma_f32_16x16x32_bf16 v[28:31], a[116:119], v[60:63], v[28:31]\nbuffer_load_dwordx4 v0, %[rs], s60 offen offset:3072 sc1 lds\nv_mfma_f32_16x16x32_bf16 v[32:35], a[124:127], v[60:63], v[32:35]\nv_mfma_f32_16x16x32_bf16 v[36:39], a[132:135], v[60:63], v[36:39]\ns_waitcnt lgkmcnt(4)\nds_read_b128 v[72:75], v1 offset:8192\nv_mfma_f32_16x16x32_bf16 v[24:27], a[136:139], v[64:67], v[24:27]\n" \
"s_waitcnt vmcnt(8)\ns_waitcnt lgkmcnt(4)\nds_read_b128 v[60:63], v2 offset:26624\nv_add_f32 v84, v40, v44\nv_mfma_f32_16x16x32_bf16 v[28:31], v[76:79], v[64:67], v[28:31]\nv_add_f32 v85, v48, v52\nv_add_f32 v84, v84, v85\nv_mfma_f32_16x16x32_bf16 v[32:35], a[144:147], v[64:67], v[32:35]\n" \
"v_mov_b32 v85, v84\nv_mfma_f32_16x16x32_bf16 v[36:39], a[152:155], v[64:67], v[36:39]\ns_waitcnt lgkmcnt(4)\nds_read_b128 v[76:79], v1 offset:9216\nv_mfma_f32_16x16x32_bf16 v[24:27], a[140:143], v[68:71], v[24:27]\ns_waitcnt lgkmcnt(4)\nds_read_b128 v[64:67], v2 offset:28672\nv_mfma_f32_16x16x32_bf16 v[28:31], v[80:83], v[68:71], v[28:31]\n" \
"v_permlane16_swap_b32 v84, v85\nv_mfma_f32_16x16x32_bf16 v[32:35], a[148:151], v[68:71], v[32:35]\nv_add_f32 v84, v84, v85\nv_mov_b32 v85, v84\nv_mfma_f32_16x16x32_bf16 v[36:39], a[156:159], v[68:71], v[36:39]\ns_waitcnt lgkmcnt(4)\nds_read_b128 v[80:83], v1 offset:10240\nv_mfma_f32_16x16x32_bf16 v[24:27], a[160:163], v[56:59], v[24:27]\n" \
"ds_read_b128 v[68:71], v2 offset:30720\nv_mfma_f32_16x16x32_bf16 v[28:31], a[168:171], v[56:59], v[28:31]\ns_waitcnt lgkmcnt(5)\nv_permlane32_swap_b32 v84, v85\nv_mfma_f32_16x16x32_bf16 v[32:35], v[72:75], v[56:59], v[32:35]\nv_add_f32 v84, v84, v85\nv_mul_f32 v84, 0xbd800000, v84\nv_mfma_f32_16x16x32_bf16 v[36:39], a[176:179], v[56:59], v[36:39]\n" \
"s_waitcnt lgkmcnt(4)\nds_read_b128 v[72:75], v1 offset:11264\nv_add_f32 v40, v40, v84\nv_mfma_f32_16x16x32_bf16 v[24:27], a[164:167], v[60:63], v[24:27]\ns_waitcnt vmcnt(4)\ns_barrier\nds_read_b128 v[56:59], v2 offset:32768\nv_mfma_f32_16x16x32_bf16 v[28:31], a[172:175], v[60:63], v[28:31]\n" \
"s_waitcnt lgkmcnt(5)\nv_mul_f32 v40, 0x41000000, v40\nv_fma_f32 v41, v40, v40, v41\nv_mfma_f32_16x16x32_bf16 v[32:35], v[76:79], v[60:63], v[32:35]\nv_add_f32 v44, v44, v84\nv_mul_f32 v44, 0x41000000, v44\nv_mfma_f32_16x16x32_bf16 v[36:39], a[180:183], v[60:63], v[36:39]\ns_waitcnt lgkmcnt(4)\n" \
"v_fma_f32 v45, v44, v44, v45\nv_add_f32 v48, v48, v84\nv_mfma_f32_16x16x32_bf16 v[24:27], a[184:187], v[64:67], v[24:27]\nds_read_b128 v[60:63], v2 offset:34816\nv_mul_f32 v48, 0x41000000, v48\nv_mfma_f32_16x16x32_bf16 v[28:31], a[192:195], v[64:67], v[28:31]\nv_fma_f32 v49, v48, v48, v49\nv_add_f32 v52, v52, v84\n" \
"v_mfma_f32_16x16x32_bf16 v[32:35], a[200:203], v[64:67], v[32:35]\ns_waitcnt lgkmcnt(4)\nv_mul_f32 v52, 0x41000000, v52\nv_fma_f32 v53, v52, v52, v53\nv_mfma_f32_16x16x32_bf16 v[36:39], v[80:83], v[64:67], v[36:39]\ns_waitcnt lgkmcnt(3)\nv_add_f32 v85, v41, v45\nv_add_f32 v86, v49, v53\n" \
"v_mfma_f32_16x16x32_bf16 v[24:27], a[188:191], v[68:71], v[24:27]\nds_read_b128 v[64:67], v2 offset:36864\nv_add_f32 v85, v85, v86\nv_mfma_f32_16x16x32_bf16 v[28:31], a[196:199], v[68:71], v[28:31]\nv_mov_b32 v86, v85\nv_mfma_f32_16x16x32_bf16 v[32:35], a[204:207], v[68:71], v[32:35]\ns_waitcnt lgkmcnt(3)\nv_permlane16_swap_b32 v85, v86\n" \
"v_mfma_f32_16x16x32_bf16 v[36:39], v[72:75], v[68:71], v[36:39]\ns_waitcnt lgkmcnt(2)\nds_read_b128 v[72:75], v1 offset:12288\nv_add_f32 v85, v85, v86\nv_mfma_f32_16x16x32_bf16 v[24:27], a[208:211], v[56:59], v[24:27]\nds_read_b128 v[68:71], v2 offset:38912\nv_mov_b32 v86, v85\nv_mfma_f32_16x16x32_bf16 v[28:31], a[216:219], v[56:59], v[28:31]\n" \
"s_nop 0\nv_permlane32_swap_b32 v85, v86\nv_mfma_f32_16x16x32_bf16 v[32:35], a[224:227], v[56:59], v[32:35]\nv_add_f32 v85, v85, v86\nv_mul_f32 v85, 0x3a800000, v85\nv_mfma_f32_16x16x32_bf16 v[36:39], a[232:235], v[56:59], v[36:39]\ns_waitcnt lgkmcnt(3)\nds_read_b128 v[76:79], v1 offset:13312\n" \
"v_cvt_pk_bf16_f32 v85, v85, v85\nv_mfma_f32_16x16x32_bf16 v[24:27], a[212:215], v[60:63], v[24:27]\nds_read_b128 v[56:59], v2 offset:40960\nv_and_b32 v85, 0xffff0000, v85\nv_mfma_f32_16x16x32_bf16 v[28:31], a[220:223], v[60:63], v[28:31]\nv_add_f32 v85, 0x3727c5ac, v85\nv_cvt_pk_bf16_f32 v85, v85, v85\nv_mfma_f32_16x16x32_bf16 v[32:35], a[228:231], v[60:63], v[32:35]\n" \
"v_and_b32 v85, 0xffff0000, v85\nv_cvt_pk_bf16_f32 v84, v84, v84\nv_mfma_f32_16x16x32_bf16 v[36:39], a[236:239], v[60:63], v[36:39]\ns_waitcnt lgkmcnt(4)\nds_read_b128 v[80:83], v1 offset:14336\nv_and_b32 v84, 0xffff0000, v84\nv_mfma_f32_16x16x32_bf16 v[24:27], a[240:243], v[64:67], v[24:27]\ns_waitcnt lgkmcnt(4)\n" \
"ds_read_b128 v[60:63], v2 offset:43008\nv_mfma_f32_16x16x32_bf16 v[28:31], v[72:75], v[64:67], v[28:31]\nv_sqrt_f32 v85, v85\nv_mfma_f32_16x16x32_bf16 v[32:35], a[248:251], v[64:67], v[32:35]\nv_cvt_pk_bf16_f32 v85, v85, v85\nv_and_b32 v85, 0xffff0000, v85\nv_mfma_f32_16x16x32_bf16 v[36:39], v[96:99], v[64:67], v[36:39]\ns_waitcnt lgkmcnt(4)\n" \
"ds_read_b128 v[72:75], v1 offset:15360\nv_mul_f32 v86, s58, v85\nv_mfma_f32_16x16x32_bf16 v[24:27], a[244:247], v[68:71], v[24:27]\ns_waitcnt lgkmcnt(4)\nds_read_b128 v[64:67], v2 offset:45056\nv_add_f32 v87, v42, v46\nv_mfma_f32_16x16x32_bf16 v[28:31], v[76:79], v[68:71], v[28:31]\nv_rcp_f32 v85, v85\n" \
"v_mfma_f32_16x16x32_bf16 v[32:35], a[252:255], v[68:71], v[32:35]\nv_mul_f32 v85, s58, v85\nv_add_f32 v88, v50, v54\nv_mfma_f32_16x16x32_bf16 v[36:39], v[100:103], v[68:71], v[36:39]\ns_waitcnt lgkmcnt(4)\nds_read_b128 v[76:79], v1 offset:16384\nv_add_f32 v87, v87, v88\nv_mfma_f32_16x16x32_bf16 v[24:27], v[104:107], v[56:59], v[24:27]\n" \
"ds_read_b128 v[68:71], v2 offset:47104\nv_mov_b32 v88, v87\nv_mfma_f32_16x16x32_bf16 v[28:31], v[112:115], v[56:59], v[28:31]\ns_waitcnt lgkmcnt(5)\nv_permlane16_swap_b32 v87, v88\nv_mfma_f32_16x16x32_bf16 v[32:35], v[80:83], v[56:59], v[32:35]\nv_add_f32 v87, v87, v88\nv_mov_b32 v88, v87\n" \
"v_mfma_f32_16x16x32_bf16 v[36:39], v[120:123], v[56:59], v[36:39]\ns_waitcnt lgkmcnt(4)\nds_read_b128 v[80:83], v1 offset:17408\nv_mfma_f32_16x16x32_bf16 v[24:27], v[108:111], v[60:63], v[24:27]\ns_waitcnt vmcnt(0)\ns_barrier\nds_read_b128 v[56:59], v2 offset:49152\nv_mfma_f32_16x16x32_bf16 v[28:31], v[116:119], v[60:63], v[28:31]\n" \
"s_waitcnt lgkmcnt(5)\nv_permlane32_swap_b32 v87, v88\nv_mfma_f32_16x16x32_bf16 v[32:35], v[72:75], v[60:63], v[32:35]\nv_add_f32 v87, v87, v88\nv_mul_f32 v87, 0xbd800000, v87\nv_mfma_f32_16x16x32_bf16 v[36:39], v[124:127], v[60:63], v[36:39]\ns_waitcnt lgkmcnt(4)\nv_add_f32 v42, v42, v87\n" \
"v_mul_f32 v42, 0x41000000, v42\nv_mfma_f32_16x16x32_bf16 v[24:27], v[128:131], v[64:67], v[24:27]\nds_read_b128 v[60:63], v2 offset:51200\nv_fma_f32 v43, v42, v42, v43\nv_mfma_f32_16x16x32_bf16 v[28:31], v[136:139], v[64:67], v[28:31]\nv_add_f32 v46, v46, v87\nv_mul_f32 v46, 0x41000000, v46\nv_mfma_f32_16x16x32_bf16 v[32:35], v[144:147], v[64:67], v[32:35]\n" \
"s_waitcnt lgkmcnt(4)\nv_fma_f32 v47, v46, v46, v47\nv_add_f32 v50, v50, v87\nv_mfma_f32_16x16x32_bf16 v[36:39], v[76:79], v[64:67], v[36:39]\ns_waitcnt lgkmcnt(3)\nv_mul_f32 v50, 0x41000000, v50\nv_fma_f32 v51, v50, v50, v51\nv_mfma_f32_16x16x32_bf16 v[24:27], v[132:135], v[68:71], v[24:27]\n" \
"ds_read_b128 v[64:67], v2 offset:53248\nv_add_f32 v54, v54, v87\nv_mfma_f32_16x16x32_bf16 v[28:31], v[140:143], v[68:71], v[28:31]\nv_mul_f32 v54, 0x41000000, v54\nv_fma_f32 v55, v54, v54, v55\nv_mfma_f32_16x16x32_bf16 v[32:35], v[148:151], v[68:71], v[32:35]\ns_waitcnt lgkmcnt(3)\nv_add_f32 v88, v43, v47\n" \
"v_add_f32 v89, v51, v55\nv_mfma_f32_16x16x32_bf16 v[36:39], v[80:83], v[68:71], v[36:39]\ns_waitcnt lgkmcnt(2)\nds_read_b128 v[80:83], v1 offset:18432\nv_add_f32 v88, v88, v89\nv_mfma_f32_16x16x32_bf16 v[24:27], v[152:155], v[56:59], v[24:27]\nds_read_b128 v[68:71], v2 offset:55296\nv_mov_b32 v89, v88\n" \
"v_mfma_f32_16x16x32_bf16 v[28:31], v[160:163], v[56:59], v[28:31]\ns_nop 0\nv_permlane16_swap_b32 v88, v89\nv_mfma_f32_16x16x32_bf16 v[32:35], v[168:171], v[56:59], v[32:35]\nv_add_f32 v88, v88, v89\nv_mov_b32 v89, v88\nv_mfma_f32_16x16x32_bf16 v[36:39], v[176:179], v[56:59], v[36:39]\ns_waitcnt lgkmcnt(3)\n" \
"ds_read_b128 v[72:75], v1 offset:19456\nv_mfma_f32_16x16x32_bf16 v[24:27], v[156:159], v[60:63], v[24:27]\nds_read_b128 v[56:59], v2 offset:57344\nv_mfma_f32_16x16x32_bf16 v[28:31], v[164:167], v[60:63], v[28:31]\nv_permlane32_swap_b32 v88, v89\nv_mfma_f32_16x16x32_bf16 v[32:35], v[172:175], v[60:63], v[32:35]\nv_add_f32 v88, v88, v89\nv_mul_f32 v88, 0x3a800000, v88\n" \
"v_mfma_f32_16x16x32_bf16 v[36:39], v[180:183], v[60:63], v[36:39]\ns_waitcnt lgkmcnt(4)\nds_read_b128 v[76:79], v1 offset:20480\nv_cvt_pk_bf16_f32 v88, v88, v88\nv_mfma_f32_16x16x32_bf16 v[24:27], v[184:187], v[64:67], v[24:27]\ns_waitcnt lgkmcnt(4)\nds_read_b128 v[60:63], v2 offset:59392\nv_and_b32 v88, 0xffff0000, v88\n" \
"v_mfma_f32_16x16x32_bf16 v[28:31], v[80:83], v[64:67], v[28:31]\nv_add_f32 v88, 0x3727c5ac, v88\nv_cvt_pk_bf16_f32 v88, v88, v88\nv_mfma_f32_16x16x32_bf16 v[32:35], v[192:195], v[64:67], v[32:35]\nv_and_b32 v88, 0xffff0000, v88\nv_cvt_pk_bf16_f32 v87, v87, v87\nv_mfma_f32_16x16x32_bf16 v[36:39], v[200:203], v[64:67], v[36:39]\ns_waitcnt lgkmcnt(4)\n" \
"ds_read_b128 v[80:83], v1 offset:21504\nv_and_b32 v87, 0xffff0000, v87\nv_mfma_f32_16x16x32_bf16 v[24:27], v[188:191], v[68:71], v[24:27]\ns_waitcnt lgkmcnt(4)\nds_read_b128 v[64:67], v2 offset:61440\nv_mfma_f32_16x16x32_bf16 v[28:31], v[72:75], v[68:71], v[28:31]\nv_sqrt_f32 v88, v88\nv_mfma_f32_16x16x32_bf16 v[32:35], v[196:199], v[68:71], v[32:35]\n" \
"v_cvt_pk_bf16_f32 v88, v88, v88\nv_and_b32 v88, 0xffff0000, v88\nv_mfma_f32_16x16x32_bf16 v[36:39], v[204:207], v[68:71], v[36:39]\ns_waitcnt lgkmcnt(4)\nds_read_b128 v[72:75], v1 offset:22528\nv_mul_f32 v89, s58, v88\nv_mfma_f32_16x16x32_bf16 v[24:27], v[208:211], v[56:59], v[24:27]\nds_read_b128 v[68:71], v2 offset:63488\n" \
"v_mfma_f32_16x16x32_bf16 v[28:31], v[216:219], v[56:59], v[28:31]\ns_waitcnt lgkmcnt(5)\nv_rcp_f32 v88, v88\nv_mfma_f32_16x16x32_bf16 v[32:35], v[76:79], v[56:59], v[32:35]\nv_mul_f32 v88, s58, v88\nv_mfma_f32_16x16x32_bf16 v[36:39], v[224:227], v[56:59], v[36:39]\ns_waitcnt lgkmcnt(4)\nds_read_b128 v[76:79], v1 offset:23552\n" \
"v_mfma_f32_16x16x32_bf16 v[24:27], v[212:215], v[60:63], v[24:27]\nds_read_b128 v[56:59], v2 offset:256\nv_mfma_f32_16x16x32_bf16 v[28:31], v[220:223], v[60:63], v[28:31]\ns_waitcnt lgkmcnt(5)\nv_mfma_f32_16x16x32_bf16 v[32:35], v[80:83], v[60:63], v[32:35]\nv_mfma_f32_16x16x32_bf16 v[36:39], v[228:231], v[60:63], v[36:39]\ns_waitcnt lgkmcnt(4)\nv_mfma_f32_16x16x32_bf16 v[24:27], v[232:235], v[64:67], v[24:27]\n" \
"ds_read_b128 v[60:63], v2 offset:2304\nv_mfma_f32_16x16x32_bf16 v[28:31], v[240:243], v[64:67], v[28:31]\nv_mfma_f32_16x16x32_bf16 v[32:35], v[248:251], v[64:67], v[32:35]\ns_waitcnt lgkmcnt(4)\nv_mfma_f32_16x16x32_bf16 v[36:39], v[72:75], v[64:67], v[36:39]\ns_waitcnt lgkmcnt(3)\nv_mfma_f32_16x16x32_bf16 v[24:27], v[236:239], v[68:71], v[24:27]\nds_read_b128 v[64:67], v2 offset:4352\n" \
"v_mfma_f32_16x16x32_bf16 v[28:31], v[244:247], v[68:71], v[28:31]\nv_mfma_f32_16x16x32_bf16 v[32:35], v[252:255], v[68:71], v[32:35]\ns_waitcnt lgkmcnt(3)\nv_mfma_f32_16x16x32_bf16 v[36:39], v[76:79], v[68:71], v[36:39]\ns_waitcnt lgkmcnt(2)\nds_read_b128 v[76:79], v1 offset:0\nv_mfma_f32_16x16x32_bf16 v[40:43], a[0:3], v[56:59], 0\nds_read_b128 v[68:71], v2 offset:6400\n" \
"v_mfma_f32_16x16x32_bf16 v[44:47], a[8:11], v[56:59], 0\ns_lshl_b32 s44, s40, 1\nv_mov_b32 v90, s44\nv_mfma_f32_16x16x32_bf16 v[48:51], a[16:19], v[56:59], 0\nv_mfma_f32_16x16x32_bf16 v[52:55], a[24:27], v[56:59], 0\ns_waitcnt lgkmcnt(3)\nds_read_b128 v[80:83], v1 offset:1024\nv_fma_f32 v24, v8, v84, v24\n" \
"v_mfma_f32_16x16x32_bf16 v[40:43], a[4:7], v[60:63], v[40:43]\nds_read_b128 v[56:59], v2 offset:8448\nv_fma_f32 v25, v9, v84, v25\nv_mfma_f32_16x16x32_bf16 v[44:47], a[12:15], v[60:63], v[44:47]\nv_fma_f32 v32, v16, v84, v32\nv_fma_f32 v33, v17, v84, v33\nv_mfma_f32_16x16x32_bf16 v[48:51], a[20:23], v[60:63], v[48:51]\nv_mul_f32 v24, v24, v85\n" \
"v_mul_f32 v25, v25, v85\nv_mfma_f32_16x16x32_bf16 v[52:55], a[28:31], v[60:63], v[52:55]\ns_waitcnt lgkmcnt(4)\nds_read_b128 v[72:75], v1 offset:2048\nv_fma_f32 v91, v24, v24, s59\nv_mfma_f32_16x16x32_bf16 v[40:43], a[32:35], v[64:67], v[40:43]\ns_waitcnt lgkmcnt(4)\nds_read_b128 v[60:63], v2 offset:10496\n" \
"v_fma_f32 v92, v25, v25, s59\nv_mfma_f32_16x16x32_bf16 v[44:47], v[76:79], v[64:67], v[44:47]\nv_mul_f32 v91, v91, v24\nv_mul_f32 v92, v92, v25\nv_mfma_f32_16x16x32_bf16 v[48:51], a[40:43], v[64:67], v[48:51]\nv_exp_f32 v91, v91\nv_mfma_f32_16x16x32_bf16 v[52:55], a[48:51], v[64:67], v[52:55]\ns_waitcnt lgkmcnt(4)\n" \
"ds_read_b128 v[76:79], v1 offset:3072\nv_mul_f32 v24, v24, v32\nv_mfma_f32_16x16x32_bf16 v[40:43], a[36:39], v[68:71], v[40:43]\ns_waitcnt lgkmcnt(4)\nds_read_b128 v[64:67], v2 offset:12544\nv_mul_f32 v25, v25, v33\nv_mfma_f32_16x16x32_bf16 v[44:47], v[80:83], v[68:71], v[44:47]\nv_exp_f32 v92, v92\n" \
"v_mfma_f32_16x16x32_bf16 v[48:51], a[44:47], v[68:71], v[48:51]\nv_fma_f32 v91, v91, v86, v86\nv_fma_f32 v92, v92, v86, v86\nv_mfma_f32_16x16x32_bf16 v[52:55], a[52:55], v[68:71], v[52:55]\ns_waitcnt lgkmcnt(4)\nds_read_b128 v[80:83], v1 offset:4096\nv_fma_f32 v26, v10, v84, v26\nv_mfma_f32_16x16x32_bf16 v[40:43], a[56:59], v[56:59], v[40:43]\n" \
"ds_read_b128 v[68:71], v2 offset:14592\nv_fma_f32 v27, v11, v84, v27\nv_mfma_f32_16x16x32_bf16 v[44:47], a[64:67], v[56:59], v[44:47]\ns_waitcnt lgkmcnt(5)\nv_rcp_f32 v91, v91\nv_mfma_f32_16x16x32_bf16 v[48:51], v[72:75], v[56:59], v[48:51]\nv_rcp_f32 v92, v92\nv_mfma_f32_16x16x32_bf16 v[52:55], a[72:75], v[56:59], v[52:55]\n" \
"s_waitcnt lgkmcnt(4)\nds_read_b128 v[72:75], v1 offset:5120\nv_mul_f32 v24, v24, v91\nv_mfma_f32_16x16x32_bf16 v[40:43], a[60:63], v[60:63], v[40:43]\nds_read_b128 v[56:59], v2 offset:16640\nv_mul_f32 v25, v25, v92\nv_mfma_f32_16x16x32_bf16 v[44:47], a[68:71], v[60:63], v[44:47]\ns_waitcnt lgkmcnt(5)\n" \
"v_cvt_pk_bf16_f32 v32, v24, v25\nv_fma_f32 v34, v18, v84, v34\nv_mfma_f32_16x16x32_bf16 v[48:51], v[76:79], v[60:63], v[48:51]\nv_fma_f32 v35, v19, v84, v35\nv_mul_f32 v26, v26, v85\nv_mfma_f32_16x16x32_bf16 v[52:55], a[76:79], v[60:63], v[52:55]\ns_waitcnt lgkmcnt(4)\nv_mul_f32 v27, v27, v85\n" \
"v_fma_f32 v91, v26, v26, s59\nv_mfma_f32_16x16x32_bf16 v[40:43], a[80:83], v[64:67], v[40:43]\nds_read_b128 v[60:63], v2 offset:18688\nv_fma_f32 v92, v27, v27, s59\nv_mfma_f32_16x16x32_bf16 v[44:47], a[88:91], v[64:67], v[44:47]\nv_mul_f32 v91, v91, v26\nv_mul_f32 v92, v92, v27\nv_mfma_f32_16x16x32_bf16 v[48:51], a[96:99], v[64:67], v[48:51]\n" \
"s_waitcnt lgkmcnt(4)\nv_exp_f32 v91, v91\nv_mfma_f32_16x16x32_bf16 v[52:55], v[80:83], v[64:67], v[52:55]\ns_waitcnt lgkmcnt(3)\nv_exp_f32 v92, v92\nv_mfma_f32_16x16x32_bf16 v[40:43], a[84:87], v[68:71], v[40:43]\nds_read_b128 v[64:67], v2 offset:20736\nv_mul_f32 v26, v26, v34\n" \
"v_mfma_f32_16x16x32_bf16 v[44:47], a[92:95], v[68:71], v[44:47]\nv_mul_f32 v27, v27, v35\nv_fma_f32 v91, v91, v86, v86\nv_mfma_f32_16x16x32_bf16 v[48:51], a[100:103], v[68:71], v[48:51]\ns_waitcnt lgkmcnt(3)\nv_fma_f32 v92, v92, v86, v86\nv_fma_f32 v28, v12, v84, v28\nv_mfma_f32_16x16x32_bf16 v[52:55], v[72:75], v[68:71], v[52:55]\n" \
"s_waitcnt lgkmcnt(2)\nds_read_b128 v[72:75], v1 offset:6144\nv_fma_f32 v29, v13, v84, v29\nv_mfma_f32_16x16x32_bf16 v[40:43], a[104:107], v[56:59], v[40:43]\nds_read_b128 v[68:71], v2 offset:22784\nv_fma_f32 v36, v20, v84, v36\nv_mfma_f32_16x16x32_bf16 v[44:47], a[112:115], v[56:59], v[44:47]\nv_rcp_f32 v91, v91\n" \
"v_mfma_f32_16x16x32_bf16 v[48:51], a[120:123], v[56:59], v[48:51]\nv_rcp_f32 v92, v92\nv_mfma_f32_16x16x32_bf16 v[52:55], a[128:131], v[56:59], v[52:55]\ns_waitcnt lgkmcnt(3)\nds_read_b128 v[76:79], v1 offset:7168\nv_mul_f32 v26, v26, v91\nv_mfma_f32_16x16x32_bf16 v[40:43], a[108:111], v[60:63], v[40:43]\nds_read_b128 v[56:59], v2 offset:24832\n" \
"v_mul_f32 v27, v27, v92\nv_mfma_f32_16x16x32_bf16 v[44:47], a[116:119], v[60:63], v[44:47]\nv_cvt_pk_bf16_f32 v33, v26, v27\nv_fma_f32 v37, v21, v84, v37\nv_mfma_f32_16x16x32_bf16 v[48:51], a[124:127], v[60:63], v[48:51]\nv_mul_f32 v28, v28, v85\nv_mul_f32 v29, v29, v85\nv_mfma_f32_16x16x32_bf16 v[52:55], a[132:135], v[60:63], v[52:55]\n" \
"s_waitcnt lgkmcnt(4)\nds_read_b128 v[80:83], v1 offset:8192\nv_fma_f32 v91, v28, v28, s59\nv_mfma_f32_16x16x32_bf16 v[40:43], a[136:139], v[64:67], v[40:43]\ns_waitcnt lgkmcnt(4)\nds_read_b128 v[60:63], v2 offset:26880\nv_fma_f32 v92, v29, v29, s59\nv_mfma_f32_16x16x32_bf16 v[44:47], v[72:75], v[64:67], v[44:47]\n" \
"v_mul_f32 v91, v91, v28\nv_mul_f32 v92, v92, v29\nv_mfma_f32_16x16x32_bf16 v[48:51], a[144:147], v[64:67], v[48:51]\nv_exp_f32 v91, v91\nv_mfma_f32_16x16x32_bf16 v[52:55], a[152:155], v[64:67], v[52:55]\ns_waitcnt lgkmcnt(4)\nds_read_b128 v[72:75], v1 offset:9216\nv_mul_f32 v28, v28, v36\n" \
"v_mfma_f32_16x16x32_bf16 v[40:43], a[140:143], v[68:71], v[40:43]\ns_waitcnt lgkmcnt(4)\nds_read_b128 v[64:67], v2 offset:28928\nv_mul_f32 v29, v29, v37\nv_mfma_f32_16x16x32_bf16 v[44:47], v[76:79], v[68:71], v[44:47]\nv_exp_f32 v92, v92\nv_mfma_f32_16x16x32_bf16 v[48:51], a[148:151], v[68:71], v[48:51]\nv_fma_f32 v91, v91, v86, v86\n" \
"v_fma_f32 v92, v92, v86, v86\nv_mfma_f32_16x16x32_bf16 v[52:55], a[156:159], v[68:71], v[52:55]\ns_waitcnt lgkmcnt(4)\nds_read_b128 v[76:79], v1 offset:10240\nv_fma_f32 v30, v14, v84, v30\nv_mfma_f32_16x16x32_bf16 v[40:43], a[160:163], v[56:59], v[40:43]\nds_read_b128 v[68:71], v2 offset:30976\nv_fma_f32 v31, v15, v84, v31\n" \
"v_mfma_f32_16x16x32_bf16 v[44:47], a[168:171], v[56:59], v[44:47]\ns_waitcnt lgkmcnt(5)\nv_rcp_f32 v91, v91\nv_mfma_f32_16x16x32_bf16 v[48:51], v[80:83], v[56:59], v[48:51]\nv_rcp_f32 v92, v92\nv_mfma_f32_16x16x32_bf16 v[52:55], a[176:179], v[56:59], v[52:55]\ns_waitcnt lgkmcnt(4)\nds_read_b128 v[80:83], v1 offset:11264\n" \
"v_mul_f32 v28, v28, v91\nv_mfma_f32_16x16x32_bf16 v[40:43], a[164:167], v[60:63], v[40:43]\nds_read_b128 v[56:59], v2 offset:33024\nv_mul_f32 v29, v29, v92\nv_mfma_f32_16x16x32_bf16 v[44:47], a[172:175], v[60:63], v[44:47]\ns_waitcnt lgkmcnt(5)\nv_cvt_pk_bf16_f32 v34, v28, v29\nv_fma_f32 v38, v22, v84, v38\n" \
"v_mfma_f32_16x16x32_bf16 v[48:51], v[72:75], v[60:63], v[48:51]\nv_fma_f32 v39, v23, v84, v39\nv_mul_f32 v30, v30, v85\nv_mfma_f32_16x16x32_bf16 v[52:55], a[180:183], v[60:63], v[52:55]\ns_waitcnt lgkmcnt(4)\nv_mul_f32 v31, v31, v85\nv_fma_f32 v91, v30, v30, s59\nv_mfma_f32_16x16x32_bf16 v[40:43], a[184:187], v[64:67], v[40:43]\n" \
"ds_read_b128 v[60:63], v2 offset:35072\nv_fma_f32 v92, v31, v31, s59\nv_mfma_f32_16x16x32_bf16 v[44:47], a[192:195], v[64:67], v[44:47]\nv_mul_f32 v91, v91, v30\nv_mul_f32 v92, v92, v31\nv_mfma_f32_16x16x32_bf16 v[48:51], a[200:203], v[64:67], v[48:51]\ns_waitcnt lgkmcnt(4)\nv_exp_f32 v91, v91\n" \
"v_mfma_f32_16x16x32_bf16 v[52:55], v[76:79], v[64:67], v[52:55]\ns_waitcnt lgkmcnt(3)\nv_exp_f32 v92, v92\nv_mfma_f32_16x16x32_bf16 v[40:43], a[188:191], v[68:71], v[40:43]\nds_read_b128 v[64:67], v2 offset:37120\nv_mul_f32 v30, v30, v38\nv_mfma_f32_16x16x32_bf16 v[44:47], a[196:199], v[68:71], v[44:47]\nv_mul_f32 v31, v31, v39\n" \
"v_fma_f32 v91, v91, v86, v86\nv_mfma_f32_16x16x32_bf16 v[48:51], a[204:207], v[68:71], v[48:51]\ns_waitcnt lgkmcnt(3)\nv_fma_f32 v92, v92, v86, v86\nv_mfma_f32_16x16x32_bf16 v[52:55], v[80:83], v[68:71], v[52:55]\ns_waitcnt lgkmcnt(2)\nds_read_b128 v[80:83], v1 offset:12288\nv_mfma_f32_16x16x32_bf16 v[40:43], a[208:211], v[56:59], v[40:43]\n" \
"ds_read_b128 v[68:71], v2 offset:39168\nv_mfma_f32_16x16x32_bf16 v[44:47], a[216:219], v[56:59], v[44:47]\nv_rcp_f32 v91, v91\nv_mfma_f32_16x16x32_bf16 v[48:51], a[224:227], v[56:59], v[48:51]\nv_rcp_f32 v92, v92\nv_mfma_f32_16x16x32_bf16 v[52:55], a[232:235], v[56:59], v[52:55]\ns_waitcnt lgkmcnt(3)\nds_read_b128 v[72:75], v1 offset:13312\n" \
"v_mul_f32 v30, v30, v91\nv_mfma_f32_16x16x32_bf16 v[40:43], a[212:215], v[60:63], v[40:43]\nds_read_b128 v[56:59], v2 offset:41216\nv_mul_f32 v31, v31, v92\nv_mfma_f32_16x16x32_bf16 v[44:47], a[220:223], v[60:63], v[44:47]\nv_cvt_pk_bf16_f32 v35, v30, v31\nv_mfma_f32_16x16x32_bf16 v[48:51], a[228:231], v[60:63], v[48:51]\nbuffer_store_dwordx4 v[32:35], v4, %[rs], s55 offen offset:0 sc1\n" \
"v_mfma_f32_16x16x32_bf16 v[52:55], a[236:239], v[60:63], v[52:55]\ns_waitcnt lgkmcnt(4)\nds_read_b128 v[76:79], v1 offset:14336\nv_mfma_f32_16x16x32_bf16 v[40:43], a[240:243], v[64:67], v[40:43]\ns_waitcnt lgkmcnt(4)\nds_read_b128 v[60:63], v2 offset:43264\nv_mfma_f32_16x16x32_bf16 v[44:47], v[80:83], v[64:67], v[44:47]\nv_mfma_f32_16x16x32_bf16 v[48:51], a[248:251], v[64:67], v[48:51]\n" \
"v_mfma_f32_16x16x32_bf16 v[52:55], v[96:99], v[64:67], v[52:55]\ns_waitcnt lgkmcnt(4)\nds_read_b128 v[80:83], v1 offset:15360\nv_mfma_f32_16x16x32_bf16 v[40:43], a[244:247], v[68:71], v[40:43]\ns_waitcnt lgkmcnt(4)\nds_read_b128 v[64:67], v2 offset:45312\nv_mfma_f32_16x16x32_bf16 v[44:47], v[72:75], v[68:71], v[44:47]\nv_mfma_f32_16x16x32_bf16 v[48:51], a[252:255], v[68:71], v[48:51]\n" \
"v_mfma_f32_16x16x32_bf16 v[52:55], v[100:103], v[68:71], v[52:55]\ns_waitcnt lgkmcnt(4)\nds_read_b128 v[72:75], v1 offset:16384\nv_mfma_f32_16x16x32_bf16 v[40:43], v[104:107], v[56:59], v[40:43]\nds_read_b128 v[68:71], v2 offset:47360\nv_mfma_f32_16x16x32_bf16 v[44:47], v[112:115], v[56:59], v[44:47]\ns_waitcnt lgkmcnt(5)\nv_mfma_f32_16x16x32_bf16 v[48:51], v[76:79], v[56:59], v[48:51]\n" \
"v_mfma_f32_16x16x32_bf16 v[52:55], v[120:123], v[56:59], v[52:55]\ns_waitcnt lgkmcnt(4)\nds_read_b128 v[76:79], v1 offset:17408\nv_mfma_f32_16x16x32_bf16 v[40:43], v[108:111], v[60:63], v[40:43]\nds_read_b128 v[56:59], v2 offset:49408\nv_mfma_f32_16x16x32_bf16 v[44:47], v[116:119], v[60:63], v[44:47]\ns_waitcnt lgkmcnt(5)\nv_mfma_f32_16x16x32_bf16 v[48:51], v[80:83], v[60:63], v[48:51]\n" \
"v_mfma_f32_16x16x32_bf16 v[52:55], v[124:127], v[60:63], v[52:55]\ns_waitcnt lgkmcnt(4)\nv_mfma_f32_16x16x32_bf16 v[40:43], v[128:131], v[64:67], v[40:43]\nds_read_b128 v[60:63], v2 offset:51456\nv_mfma_f32_16x16x32_bf16 v[44:47], v[136:139], v[64:67], v[44:47]\nv_mfma_f32_16x16x32_bf16 v[48:51], v[144:147], v[64:67], v[48:51]\ns_waitcnt lgkmcnt(4)\nv_mfma_f32_16x16x32_bf16 v[52:55], v[72:75], v[64:67], v[52:55]\n" \
"s_waitcnt lgkmcnt(3)\nv_mfma_f32_16x16x32_bf16 v[40:43], v[132:135], v[68:71], v[40:43]\nds_read_b128 v[64:67], v2 offset:53504\nv_mfma_f32_16x16x32_bf16 v[44:47], v[140:143], v[68:71], v[44:47]\nv_mfma_f32_16x16x32_bf16 v[48:51], v[148:151], v[68:71], v[48:51]\ns_waitcnt lgkmcnt(3)\nv_mfma_f32_16x16x32_bf16 v[52:55], v[76:79], v[68:71], v[52:55]\ns_waitcnt lgkmcnt(2)\n" \
"ds_read_b128 v[76:79], v1 offset:18432\nv_mfma_f32_16x16x32_bf16 v[40:43], v[152:155], v[56:59], v[40:43]\nds_read_b128 v[68:71], v2 offset:55552\nv_mfma_f32_16x16x32_bf16 v[44:47], v[160:163], v[56:59], v[44:47]\nv_mfma_f32_16x16x32_bf16 v[48:51], v[168:171], v[56:59], v[48:51]\nv_mfma_f32_16x16x32_bf16 v[52:55], v[176:179], v[56:59], v[52:55]\ns_waitcnt lgkmcnt(3)\nds_read_b128 v[80:83], v1 offset:19456\n" \
"v_mfma_f32_16x16x32_bf16 v[40:43], v[156:159], v[60:63], v[40:43]\nds_read_b128 v[56:59], v2 offset:57600\nv_mfma_f32_16x16x32_bf16 v[44:47], v[164:167], v[60:63], v[44:47]\nv_mfma_f32_16x16x32_bf16 v[48:51], v[172:175], v[60:63], v[48:51]\nv_mfma_f32_16x16x32_bf16 v[52:55], v[180:183], v[60:63], v[52:55]\ns_waitcnt lgkmcnt(4)\nds_read_b128 v[72:75], v1 offset:20480\nv_mfma_f32_16x16x32_bf16 v[40:43], v[184:187], v[64:67], v[40:43]\n" \
"s_waitcnt lgkmcnt(4)\nds_read_b128 v[60:63], v2 offset:59648\nv_mfma_f32_16x16x32_bf16 v[44:47], v[76:79], v[64:67], v[44:47]\ns_waitcnt vmcnt(0)\nbuffer_store_dword v90, off, %[rs], %[fh] sc1\nv_mfma_f32_16x16x32_bf16 v[48:51], v[192:195], v[64:67], v[48:51]\nv_mfma_f32_16x16x32_bf16 v[52:55], v[200:203], v[64:67], v[52:55]\ns_waitcnt lgkmcnt(4)\n" \
"ds_read_b128 v[76:79], v1 offset:21504\nv_mfma_f32_16x16x32_bf16 v[40:43], v[188:191], v[68:71], v[40:43]\ns_waitcnt lgkmcnt(4)\nds_read_b128 v[64:67], v2 offset:61696\nv_mfma_f32_16x16x32_bf16 v[44:47], v[80:83], v[68:71], v[44:47]\nv_mfma_f32_16x16x32_bf16 v[48:51], v[196:199], v[68:71], v[48:51]\nv_mfma_f32_16x16x32_bf16 v[52:55], v[204:207], v[68:71], v[52:55]\ns_waitcnt lgkmcnt(4)\n" \
"ds_read_b128 v[80:83], v1 offset:22528\nv_mfma_f32_16x16x32_bf16 v[40:43], v[208:211], v[56:59], v[40:43]\nds_read_b128 v[68:71], v2 offset:63744\nv_mfma_f32_16x16x32_bf16 v[44:47], v[216:219], v[56:59], v[44:47]\ns_waitcnt lgkmcnt(5)\nv_mfma_f32_16x16x32_bf16 v[48:51], v[72:75], v[56:59], v[48:51]\nv_mfma_f32_16x16x32_bf16 v[52:55], v[224:227], v[56:59], v[52:55]\ns_waitcnt lgkmcnt(4)\n" \
"ds_read_b128 v[72:75], v1 offset:23552\nv_mfma_f32_16x16x32_bf16 v[40:43], v[212:215], v[60:63], v[40:43]\nv_mfma_f32_16x16x32_bf16 v[44:47], v[220:223], v[60:63], v[44:47]\ns_waitcnt lgkmcnt(4)\nv_mfma_f32_16x16x32_bf16 v[48:51], v[76:79], v[60:63], v[48:51]\nv_mfma_f32_16x16x32_bf16 v[52:55], v[228:231], v[60:63], v[52:55]\ns_waitcnt lgkmcnt(3)\nv_mfma_f32_16x16x32_bf16 v[40:43], v[232:235], v[64:67], v[40:43]\n" \
"v_mfma_f32_16x16x32_bf16 v[44:47], v[240:243], v[64:67], v[44:47]\nv_mfma_f32_16x16x32_bf16 v[48:51], v[248:251], v[64:67], v[48:51]\ns_waitcnt lgkmcnt(2)\nv_mfma_f32_16x16x32_bf16 v[52:55], v[80:83], v[64:67], v[52:55]\ns_waitcnt lgkmcnt(1)\nv_mfma_f32_16x16x32_bf16 v[40:43], v[236:239], v[68:71], v[40:43]\nv_mfma_f32_16x16x32_bf16 v[44:47], v[244:247], v[68:71], v[44:47]\nv_mfma_f32_16x16x32_bf16 v[48:51], v[252:255], v[68:71], v[48:51]\n" \
"s_waitcnt lgkmcnt(0)\nv_mfma_f32_16x16x32_bf16 v[52:55], v[72:75], v[68:71], v[52:55]\nv_fma_f32 v40, v8, v87, v40\nv_fma_f32 v41, v9, v87, v41\ns_nop 1\nv_fma_f32 v48, v16, v87, v48\nv_fma_f32 v49, v17, v87, v49\nv_mul_f32 v40, v40, v88\n" \
"v_mul_f32 v41, v41, v88\nv_fma_f32 v91, v40, v40, s59\nv_fma_f32 v92, v41, v41, s59\nv_mul_f32 v91, v91, v40\nv_mul_f32 v92, v92, v41\nv_exp_f32 v91, v91\nv_exp_f32 v92, v92\nv_mul_f32 v40, v40, v48\n" \
"v_mul_f32 v41, v41, v49\nv_fma_f32 v91, v91, v89, v89\nv_fma_f32 v92, v92, v89, v89\nv_rcp_f32 v91, v91\nv_rcp_f32 v92, v92\nv_mul_f32 v40, v40, v91\nv_mul_f32 v41, v41, v92\nv_cvt_pk_bf16_f32 v48, v40, v41\n" \
"v_fma_f32 v42, v10, v87, v42\nv_fma_f32 v43, v11, v87, v43\nv_fma_f32 v50, v18, v87, v50\nv_fma_f32 v51, v19, v87, v51\nv_mul_f32 v42, v42, v88\nv_mul_f32 v43, v43, v88\nv_fma_f32 v91, v42, v42, s59\nv_fma_f32 v92, v43, v43, s59\n" \
"v_mul_f32 v91, v91, v42\nv_mul_f32 v92, v92, v43\nv_exp_f32 v91, v91\nv_exp_f32 v92, v92\nv_mul_f32 v42, v42, v50\nv_mul_f32 v43, v43, v51\nv_fma_f32 v91, v91, v89, v89\nv_fma_f32 v92, v92, v89, v89\n" \
"v_rcp_f32 v91, v91\nv_rcp_f32 v92, v92\nv_mul_f32 v42, v42, v91\nv_mul_f32 v43, v43, v92\nv_cvt_pk_bf16_f32 v49, v42, v43\nv_fma_f32 v44, v12, v87, v44\nv_fma_f32 v45, v13, v87, v45\nv_fma_f32 v52, v20, v87, v52\n" \
"v_fma_f32 v53, v21, v87, v53\nv_mul_f32 v44, v44, v88\nv_mul_f32 v45, v45, v88\nv_fma_f32 v91, v44, v44, s59\nv_fma_f32 v92, v45, v45, s59\nv_mul_f32 v91, v91, v44\nv_mul_f32 v92, v92, v45\nv_exp_f32 v91, v91\n" \
"v_exp_f32 v92, v92\nv_mul_f32 v44, v44, v52\nv_mul_f32 v45, v45, v53\nv_fma_f32 v91, v91, v89, v89\nv_fma_f32 v92, v92, v89, v89\nv_rcp_f32 v91, v91\nv_rcp_f32 v92, v92\nv_mul_f32 v44, v44, v91\n" \
"v_mul_f32 v45, v45, v92\nv_cvt_pk_bf16_f32 v50, v44, v45\nv_fma_f32 v46, v14, v87, v46\nv_fma_f32 v47, v15, v87, v47\nv_fma_f32 v54, v22, v87, v54\nv_fma_f32 v55, v23, v87, v55\nv_mul_f32 v46, v46, v88\nv_mul_f32 v47, v47, v88\n" \
"v_fma_f32 v91, v46, v46, s59\nv_fma_f32 v92, v47, v47, s59\nv_mul_f32 v91, v91, v46\nv_mul_f32 v92, v92, v47\nv_exp_f32 v91, v91\nv_exp_f32 v92, v92\nv_mul_f32 v46, v46, v54\nv_mul_f32 v47, v47, v55\n" \
"v_fma_f32 v91, v91, v89, v89\nv_fma_f32 v92, v92, v89, v89\nv_rcp_f32 v91, v91\nv_rcp_f32 v92, v92\nv_mul_f32 v46, v46, v91\nv_mul_f32 v47, v47, v92\nv_cvt_pk_bf16_f32 v51, v46, v47\nbuffer_store_dwordx4 v[48:51], v4, %[rs], s55 offen offset:1024 sc1\n" \
"v_add_u32 v90, 1, v90\ns_mov_b32 s56, %[fh]\ns_add_u32 s40, s40, 1\ns_cmp_lt_u32 s40, 64\ns_cbranch_scc1 .Lgg_next%=\ns_waitcnt vmcnt(0)\nbuffer_store_dword v90, off, %[rs], s56 sc1\ns_branch .Lgg_end%=\n" \
".Lgg_abort%=:\ns_mov_b64 exec, -1\nv_mov_b32 v16, 0\ns_movk_i32 s44, 6400\nbuffer_store_dword v16, off, %[rs], s44 sc1\n.Lgg_end%=:\ns_waitcnt vmcnt(0) lgkmcnt(0)\n" \
""
#define DD_ASM \
"v_mbcnt_lo_u32_b32 v255, -1, 0\nv_mbcnt_hi_u32_b32 v255, -1, v255\nv_lshlrev_b32 v255, 2, v255\nv_add_u32 v255, %[tb], v255\nds_read_b32 v0, v255 offset:0\nds_read_b32 v1, v255 offset:256\nds_read_b32 v2, v255 offset:512\nds_read_b32 v3, v255 offset:768\n" \
"ds_read_b32 v4, v255 offset:1024\nds_read_b32 v5, v255 offset:1280\nds_read_b32 v6, v255 offset:1536\nds_read_b32 v7, v255 offset:1792\nds_read_b32 v125, v255 offset:2048\nds_read_b32 v126, v255 offset:2304\nds_read_b32 v127, v255 offset:2560\ns_waitcnt lgkmcnt(0)\n" \
"s_add_u32 m0, %[tb], 8192\ns_add_u32 s44, %[wo], 0\nbuffer_load_dwordx4 v125, %[rw], s44 offen nt lds\ns_add_u32 m0, %[tb], 9216\ns_add_u32 s44, %[wo], 16384\nbuffer_load_dwordx4 v125, %[rw], s44 offen nt lds\ns_add_u32 m0, %[tb], 10240\ns_add_u32 s44, %[wo], 32768\n" \
"buffer_load_dwordx4 v125, %[rw], s44 offen nt lds\ns_add_u32 m0, %[tb], 11264\ns_add_u32 s44, %[wo], 49152\nbuffer_load_dwordx4 v125, %[rw], s44 offen nt lds\ns_add_u32 m0, %[lw], 0\ns_add_u32 s44, %[wo], 65536\nbuffer_load_dwordx4 v125, %[rw], s44 offen nt lds\ns_add_u32 m0, %[lw], 1024\n" \
"s_add_u32 s44, %[wo], 81920\nbuffer_load_dwordx4 v125, %[rw], s44 offen nt lds\ns_add_u32 m0, %[lw], 2048\ns_add_u32 s44, %[wo], 98304\nbuffer_load_dwordx4 v125, %[rw], s44 offen nt lds\ns_add_u32 m0, %[lw], 3072\ns_add_u32 s44, %[wo], 114688\nbuffer_load_dwordx4 v125, %[rw], s44 offen nt lds\n" \
"s_add_u32 m0, %[lw], 4096\ns_add_u32 s44, %[wo], 131072\nbuffer_load_dwordx4 v125, %[rw], s44 offen nt lds\ns_add_u32 m0, %[lw], 5120\ns_add_u32 s44, %[wo], 147456\nbuffer_load_dwordx4 v125, %[rw], s44 offen nt lds\ns_add_u32 m0, %[lw], 6144\ns_add_u32 s44, %[wo], 163840\n" \
"buffer_load_dwordx4 v125, %[rw], s44 offen nt lds\ns_add_u32 m0, %[lw], 7168\ns_add_u32 s44, %[wo], 180224\nbuffer_load_dwordx4 v125, %[rw], s44 offen nt lds\ns_add_u32 m0, %[lw], 8192\ns_add_u32 s44, %[wo], 196608\nbuffer_load_dwordx4 v125, %[rw], s44 offen nt lds\ns_add_u32 m0, %[lw], 9216\n" \
"s_add_u32 s44, %[wo], 212992\nbuffer_load_dwordx4 v125, %[rw], s44 offen nt lds\ns_add_u32 m0, %[lw], 10240\ns_add_u32 s44, %[wo], 229376\nbuffer_load_dwordx4 v125, %[rw], s44 offen nt lds\ns_add_u32 m0, %[lw], 11264\ns_add_u32 s44, %[wo], 245760\nbuffer_load_dwordx4 v125, %[rw], s44 offen nt lds\n" \
"s_add_u32 m0, %[lw], 12288\ns_add_u32 s44, %[wo], 262144\nbuffer_load_dwordx4 v125, %[rw], s44 offen nt lds\ns_add_u32 m0, %[lw], 13312\ns_add_u32 s44, %[wo], 278528\nbuffer_load_dwordx4 v125, %[rw], s44 offen nt lds\ns_add_u32 m0, %[lw], 14336\ns_add_u32 s44, %[wo], 294912\n" \
"buffer_load_dwordx4 v125, %[rw], s44 offen nt lds\ns_add_u32 m0, %[lw], 15360\ns_add_u32 s44, %[wo], 311296\nbuffer_load_dwordx4 v125, %[rw], s44 offen nt lds\ns_add_u32 m0, %[lw], 16384\ns_add_u32 s44, %[wo], 327680\nbuffer_load_dwordx4 v125, %[rw], s44 offen nt lds\ns_add_u32 m0, %[lw], 17408\n" \
"s_add_u32 s44, %[wo], 344064\nbuffer_load_dwordx4 v125, %[rw], s44 offen nt lds\ns_add_u32 m0, %[lw], 18432\ns_add_u32 s44, %[wo], 360448\nbuffer_load_dwordx4 v125, %[rw], s44 offen nt lds\ns_add_u32 m0, %[lw], 19456\ns_add_u32 s44, %[wo], 376832\nbuffer_load_dwordx4 v125, %[rw], s44 offen nt lds\n" \
"s_add_u32 m0, %[lw], 20480\ns_add_u32 s44, %[wo], 393216\nbuffer_load_dwordx4 v125, %[rw], s44 offen nt lds\ns_add_u32 m0, %[lw], 21504\ns_add_u32 s44, %[wo], 409600\nbuffer_load_dwordx4 v125, %[rw], s44 offen nt lds\ns_add_u32 m0, %[lw], 22528\ns_add_u32 s44, %[wo], 425984\n" \
"buffer_load_dwordx4 v125, %[rw], s44 offen nt lds\ns_add_u32 m0, %[lw], 23552\ns_add_u32 s44, %[wo], 442368\nbuffer_load_dwordx4 v125, %[rw], s44 offen nt lds\ns_add_u32 m0, %[tb], 0\ns_add_u32 s44, %[wo], 458752\nbuffer_load_dwordx4 v125, %[rw], s44 offen nt lds\ns_add_u32 m0, %[tb], 1024\n" \
"s_add_u32 s44, %[wo], 475136\nbuffer_load_dwordx4 v125, %[rw], s44 offen nt lds\ns_add_u32 m0, %[tb], 2048\ns_add_u32 s44, %[wo], 491520\nbuffer_load_dwordx4 v125, %[rw], s44 offen nt lds\ns_add_u32 m0, %[tb], 3072\ns_add_u32 s44, %[wo], 507904\nbuffer_load_dwordx4 v125, %[rw], s44 offen nt lds\n" \
"s_add_u32 m0, %[tb], 4096\ns_add_u32 s44, %[wo], 524288\nbuffer_load_dwordx4 v125, %[rw], s44 offen nt lds\ns_add_u32 m0, %[tb], 5120\ns_add_u32 s44, %[wo], 540672\nbuffer_load_dwordx4 v125, %[rw], s44 offen nt lds\ns_add_u32 m0, %[tb], 6144\ns_add_u32 s44, %[wo], 557056\n" \
"buffer_load_dwordx4 v125, %[rw], s44 offen nt lds\ns_add_u32 m0, %[tb], 7168\ns_add_u32 s44, %[wo], 573440\nbuffer_load_dwordx4 v125, %[rw], s44 offen nt lds\ns_waitcnt vmcnt(32)\nds_read_b64_tr_b16 a[0:1], v126 offset:8192\nds_read_b64_tr_b16 a[2:3], v126 offset:8704\nds_read_b64_tr_b16 a[8:9], v126 offset:8224\n" \
"ds_read_b64_tr_b16 a[10:11], v126 offset:8736\nds_read_b64_tr_b16 a[16:17], v126 offset:8256\nds_read_b64_tr_b16 a[18:19], v126 offset:8768\nds_read_b64_tr_b16 a[24:25], v126 offset:8288\nds_read_b64_tr_b16 a[26:27], v126 offset:8800\ns_waitcnt lgkmcnt(0)\ns_add_u32 m0, %[tb], 8192\ns_add_u32 s44, %[wo], 589824\n" \
"buffer_load_dwordx4 v125, %[rw], s44 offen nt lds\ns_add_u32 m0, %[tb], 9216\ns_add_u32 s44, %[wo], 606208\nbuffer_load_dwordx4 v125, %[rw], s44 offen nt lds\ns_add_u32 m0, %[tb], 10240\ns_add_u32 s44, %[wo], 622592\nbuffer_load_dwordx4 v125, %[rw], s44 offen nt lds\ns_add_u32 m0, %[tb], 11264\n" \
"s_add_u32 s44, %[wo], 638976\nbuffer_load_dwordx4 v125, %[rw], s44 offen nt lds\ns_waitcnt vmcnt(32)\nds_read_b64_tr_b16 a[4:5], v127 offset:0\nds_read_b64_tr_b16 a[6:7], v127 offset:512\nds_read_b64_tr_b16 a[12:13], v127 offset:32\nds_read_b64_tr_b16 a[14:15], v127 offset:544\nds_read_b64_tr_b16 a[20:21], v127 offset:64\n" \
"ds_read_b64_tr_b16 a[22:23], v127 offset:576\nds_read_b64_tr_b16 a[28:29], v127 offset:96\nds_read_b64_tr_b16 a[30:31], v127 offset:608\ns_waitcnt lgkmcnt(0)\ns_add_u32 m0, %[lw], 0\ns_add_u32 s44, %[wo], 655360\nbuffer_load_dwordx4 v125, %[rw], s44 offen nt lds\ns_add_u32 m0, %[lw], 1024\n" \
"s_add_u32 s44, %[wo], 671744\nbuffer_load_dwordx4 v125, %[rw], s44 offen nt lds\ns_add_u32 m0, %[lw], 2048\ns_add_u32 s44, %[wo], 688128\nbuffer_load_dwordx4 v125, %[rw], s44 offen nt lds\ns_add_u32 m0, %[lw], 3072\ns_add_u32 s44, %[wo], 704512\nbuffer_load_dwordx4 v125, %[rw], s44 offen nt lds\n" \
"s_waitcnt vmcnt(32)\nds_read_b64_tr_b16 a[32:33], v127 offset:4096\nds_read_b64_tr_b16 a[34:35], v127 offset:4608\nds_read_b64_tr_b16 v[92:93], v127 offset:4128\nds_read_b64_tr_b16 v[94:95], v127 offset:4640\nds_read_b64_tr_b16 a[40:41], v127 offset:4160\nds_read_b64_tr_b16 a[42:43], v127 offset:4672\nds_read_b64_tr_b16 a[48:49], v127 offset:4192\n" \
"ds_read_b64_tr_b16 a[50:51], v127 offset:4704\ns_waitcnt lgkmcnt(0)\ns_add_u32 m0, %[lw], 4096\ns_add_u32 s44, %[wo], 720896\nbuffer_load_dwordx4 v125, %[rw], s44 offen nt lds\ns_add_u32 m0, %[lw], 5120\ns_add_u32 s44, %[wo], 737280\nbuffer_load_dwordx4 v125, %[rw], s44 offen nt lds\n" \
"s_add_u32 m0, %[lw], 6144\ns_add_u32 s44, %[wo], 753664\nbuffer_load_dwordx4 v125, %[rw], s44 offen nt lds\ns_add_u32 m0, %[lw], 7168\ns_add_u32 s44, %[wo], 770048\nbuffer_load_dwordx4 v125, %[rw], s44 offen nt lds\ns_waitcnt vmcnt(32)\nds_read_b64_tr_b16 a[36:37], v127 offset:8192\n" \
"ds_read_b64_tr_b16 a[38:39], v127 offset:8704\nds_read_b64_tr_b16 v[88:89], v127 offset:8224\nds_read_b64_tr_b16 v[90:91], v127 offset:8736\nds_read_b64_tr_b16 a[44:45], v127 offset:8256\nds_read_b64_tr_b16 a[46:47], v127 offset:8768\nds_read_b64_tr_b16 a[52:53], v127 offset:8288\nds_read_b64_tr_b16 a[54:55], v127 offset:8800\ns_waitcnt lgkmcnt(0)\n" \
"s_add_u32 m0, %[lw], 8192\ns_add_u32 s44, %[wo], 786432\nbuffer_load_dwordx4 v125, %[rw], s44 offen nt lds\ns_add_u32 m0, %[lw], 9216\ns_add_u32 s44, %[wo], 802816\nbuffer_load_dwordx4 v125, %[rw], s44 offen nt lds\ns_add_u32 m0, %[lw], 10240\ns_add_u32 s44, %[wo], 819200\n" \
"buffer_load_dwordx4 v125, %[rw], s44 offen nt lds\ns_add_u32 m0, %[lw], 11264\ns_add_u32 s44, %[wo], 835584\nbuffer_load_dwordx4 v125, %[rw], s44 offen nt lds\ns_waitcnt vmcnt(32)\nds_read_b64_tr_b16 a[56:57], v127 offset:12288\nds_read_b64_tr_b16 a[58:59], v127 offset:12800\nds_read_b64_tr_b16 a[64:65], v127 offset:12320\n" \
"ds_read_b64_tr_b16 a[66:67], v127 offset:12832\nds_read_b64_tr_b16 a[72:73], v127 offset:12352\nds_read_b64_tr_b16 a[74:75], v127 offset:12864\nds_read_b64_tr_b16 a[80:81], v127 offset:12384\nds_read_b64_tr_b16 a[82:83], v127 offset:12896\ns_waitcnt lgkmcnt(0)\ns_add_u32 m0, %[lw], 12288\ns_add_u32 s44, %[wo], 851968\n" \
"buffer_load_dwordx4 v125, %[rw], s44 offen nt lds\ns_add_u32 m0, %[lw], 13312\ns_add_u32 s44, %[wo], 868352\nbuffer_load_dwordx4 v125, %[rw], s44 offen nt lds\ns_add_u32 m0, %[lw], 14336\ns_add_u32 s44, %[wo], 884736\nbuffer_load_dwordx4 v125, %[rw], s44 offen nt lds\ns_add_u32 m0, %[lw], 15360\n" \
"s_add_u32 s44, %[wo], 901120\nbuffer_load_dwordx4 v125, %[rw], s44 offen nt lds\ns_waitcnt vmcnt(32)\nds_read_b64_tr_b16 a[60:61], v127 offset:16384\nds_read_b64_tr_b16 a[62:63], v127 offset:16896\nds_read_b64_tr_b16 a[68:69], v127 offset:16416\nds_read_b64_tr_b16 a[70:71], v127 offset:16928\nds_read_b64_tr_b16 a[76:77], v127 offset:16448\n" \
"ds_read_b64_tr_b16 a[78:79], v127 offset:16960\nds_read_b64_tr_b16 a[84:85], v127 offset:16480\nds_read_b64_tr_b16 a[86:87], v127 offset:16992\ns_waitcnt lgkmcnt(0)\ns_add_u32 m0, %[lw], 16384\ns_add_u32 s44, %[wo], 917504\nbuffer_load_dwordx4 v125, %[rw], s44 offen nt lds\ns_add_u32 m0, %[lw], 17408\n" \
"s_add_u32 s44, %[wo], 933888\nbuffer_load_dwordx4 v125, %[rw], s44 offen nt lds\ns_add_u32 m0, %[lw], 18432\ns_add_u32 s44, %[wo], 950272\nbuffer_load_dwordx4 v125, %[rw], s44 offen nt lds\ns_add_u32 m0, %[lw], 19456\ns_add_u32 s44, %[wo], 966656\nbuffer_load_dwordx4 v125, %[rw], s44 offen nt lds\n" \
"s_waitcnt vmcnt(32)\nds_read_b64_tr_b16 a[88:89], v127 offset:20480\nds_read_b64_tr_b16 a[90:91], v127 offset:20992\nds_read_b64_tr_b16 a[96:97], v127 offset:20512\nds_read_b64_tr_b16 a[98:99], v127 offset:21024\nds_read_b64_tr_b16 a[104:105], v127 offset:20544\nds_read_b64_tr_b16 a[106:107], v127 offset:21056\nds_read_b64_tr_b16 v[84:85], v127 offset:20576\n" \
"ds_read_b64_tr_b16 v[86:87], v127 offset:21088\ns_waitcnt lgkmcnt(0)\ns_add_u32 m0, %[lw], 20480\ns_add_u32 s44, %[wo], 983040\nbuffer_load_dwordx4 v125, %[rw], s44 offen nt lds\ns_add_u32 m0, %[lw], 21504\ns_add_u32 s44, %[wo], 999424\nbuffer_load_dwordx4 v125, %[rw], s44 offen nt lds\n" \
"s_add_u32 m0, %[lw], 22528\ns_add_u32 s44, %[wo], 1015808\nbuffer_load_dwordx4 v125, %[rw], s44 offen nt lds\ns_add_u32 m0, %[lw], 23552\ns_add_u32 s44, %[wo], 1032192\nbuffer_load_dwordx4 v125, %[rw], s44 offen nt lds\ns_waitcnt vmcnt(32)\nds_read_b64_tr_b16 a[92:93], v126 offset:0\n" \
"ds_read_b64_tr_b16 a[94:95], v126 offset:512\nds_read_b64_tr_b16 a[100:101], v126 offset:32\nds_read_b64_tr_b16 a[102:103], v126 offset:544\nds_read_b64_tr_b16 a[108:109], v126 offset:64\nds_read_b64_tr_b16 a[110:111], v126 offset:576\nds_read_b64_tr_b16 v[80:81], v126 offset:96\nds_read_b64_tr_b16 v[82:83], v126 offset:608\ns_waitcnt lgkmcnt(0)\n" \
"s_add_u32 m0, %[tb], 0\ns_add_u32 s44, %[wo], 1048576\nbuffer_load_dwordx4 v125, %[rw], s44 offen nt lds\ns_add_u32 m0, %[tb], 1024\ns_add_u32 s44, %[wo], 1064960\nbuffer_load_dwordx4 v125, %[rw], s44 offen nt lds\ns_add_u32 m0, %[tb], 2048\ns_add_u32 s44, %[wo], 1081344\n" \
"buffer_load_dwordx4 v125, %[rw], s44 offen nt lds\ns_add_u32 m0, %[tb], 3072\ns_add_u32 s44, %[wo], 1097728\nbuffer_load_dwordx4 v125, %[rw], s44 offen nt lds\ns_waitcnt vmcnt(32)\nds_read_b64_tr_b16 a[112:113], v126 offset:4096\nds_read_b64_tr_b16 a[114:115], v126 offset:4608\nds_read_b64_tr_b16 a[120:121], v126 offset:4128\n" \
"ds_read_b64_tr_b16 a[122:123], v126 offset:4640\nds_read_b64_tr_b16 a[128:129], v126 offset:4160\nds_read_b64_tr_b16 a[130:131], v126 offset:4672\nds_read_b64_tr_b16 a[136:137], v126 offset:4192\nds_read_b64_tr_b16 a[138:139], v126 offset:4704\ns_waitcnt lgkmcnt(0)\ns_add_u32 m0, %[tb], 4096\ns_add_u32 s44, %[wo], 1114112\n" \
"buffer_load_dwordx4 v125, %[rw], s44 offen nt lds\ns_add_u32 m0, %[tb], 5120\ns_add_u32 s44, %[wo], 1130496\nbuffer_load_dwordx4 v125, %[rw], s44 offen nt lds\ns_add_u32 m0, %[tb], 6144\ns_add_u32 s44, %[wo], 1146880\nbuffer_load_dwordx4 v125, %[rw], s44 offen nt lds\ns_add_u32 m0, %[tb], 7168\n" \
"s_add_u32 s44, %[wo], 1163264\nbuffer_load_dwordx4 v125, %[rw], s44 offen nt lds\ns_waitcnt vmcnt(32)\nds_read_b64_tr_b16 a[116:117], v126 offset:8192\nds_read_b64_tr_b16 a[118:119], v126 offset:8704\nds_read_b64_tr_b16 a[124:125], v126 offset:8224\nds_read_b64_tr_b16 a[126:127], v126 offset:8736\nds_read_b64_tr_b16 a[132:133], v126 offset:8256\n" \
"ds_read_b64_tr_b16 a[134:135], v126 offset:8768\nds_read_b64_tr_b16 a[140:141], v126 offset:8288\nds_read_b64_tr_b16 a[142:143], v126 offset:8800\ns_waitcnt lgkmcnt(0)\ns_add_u32 m0, %[tb], 8192\ns_add_u32 s44, %[wo], 1179648\nbuffer_load_dwordx4 v125, %[rw], s44 offen nt lds\ns_add_u32 m0, %[tb], 9216\n" \
"s_add_u32 s44, %[wo], 1196032\nbuffer_load_dwordx4 v125, %[rw], s44 offen nt lds\ns_add_u32 m0, %[tb], 10240\ns_add_u32 s44, %[wo], 1212416\nbuffer_load_dwordx4 v125, %[rw], s44 offen nt lds\ns_add_u32 m0, %[tb], 11264\ns_add_u32 s44, %[wo], 1228800\nbuffer_load_dwordx4 v125, %[rw], s44 offen nt lds\n" \
"s_waitcnt vmcnt(32)\nds_read_b64_tr_b16 a[144:145], v127 offset:0\nds_read_b64_tr_b16 a[146:147], v127 offset:512\nds_read_b64_tr_b16 v[76:77], v127 offset:32\nds_read_b64_tr_b16 v[78:79], v127 offset:544\nds_read_b64_tr_b16 a[152:153], v127 offset:64\nds_read_b64_tr_b16 a[154:155], v127 offset:576\nds_read_b64_tr_b16 a[160:161], v127 offset:96\n" \
"ds_read_b64_tr_b16 a[162:163], v127 offset:608\ns_waitcnt lgkmcnt(0)\ns_add_u32 m0, %[lw], 0\ns_add_u32 s44, %[wo], 1245184\nbuffer_load_dwordx4 v125, %[rw], s44 offen nt lds\ns_add_u32 m0, %[lw], 1024\ns_add_u32 s44, %[wo], 1261568\nbuffer_load_dwordx4 v125, %[rw], s44 offen nt lds\n" \
"s_add_u32 m0, %[lw], 2048\ns_add_u32 s44, %[wo], 1277952\nbuffer_load_dwordx4 v125, %[rw], s44 offen nt lds\ns_add_u32 m0, %[lw], 3072\ns_add_u32 s44, %[wo], 1294336\nbuffer_load_dwordx4 v125, %[rw], s44 offen nt lds\ns_waitcnt vmcnt(32)\nds_read_b64_tr_b16 a[148:149], v127 offset:4096\n" \
"ds_read_b64_tr_b16 a[150:151], v127 offset:4608\nds_read_b64_tr_b16 v[72:73], v127 offset:4128\nds_read_b64_tr_b16 v[74:75], v127 offset:4640\nds_read_b64_tr_b16 a[156:157], v127 offset:4160\nds_read_b64_tr_b16 a[158:159], v127 offset:4672\nds_read_b64_tr_b16 a[164:165], v127 offset:4192\nds_read_b64_tr_b16 a[166:167], v127 offset:4704\ns_waitcnt lgkmcnt(0)\n" \
"s_add_u32 m0, %[lw], 4096\ns_add_u32 s44, %[wo], 1310720\nbuffer_load_dwordx4 v125, %[rw], s44 offen nt lds\ns_add_u32 m0, %[lw], 5120\ns_add_u32 s44, %[wo], 1327104\nbuffer_load_dwordx4 v125, %[rw], s44 offen nt lds\ns_add_u32 m0, %[lw], 6144\ns_add_u32 s44, %[wo], 1343488\n" \
"buffer_load_dwordx4 v125, %[rw], s44 offen nt lds\ns_add_u32 m0, %[lw], 7168\ns_add_u32 s44, %[wo], 1359872\nbuffer_load_dwordx4 v125, %[rw], s44 offen nt lds\ns_waitcnt vmcnt(32)\nds_read_b64_tr_b16 a[168:169], v127 offset:8192\nds_read_b64_tr_b16 a[170:171], v127 offset:8704\nds_read_b64_tr_b16 a[176:177], v127 offset:8224\n" \
"ds_read_b64_tr_b16 a[178:179], v127 offset:8736\nds_read_b64_tr_b16 v[68:69], v127 offset:8256\nds_read_b64_tr_b16 v[70:71], v127 offset:8768\nds_read_b64_tr_b16 a[184:185], v127 offset:8288\nds_read_b64_tr_b16 a[186:187], v127 offset:8800\ns_waitcnt lgkmcnt(0)\ns_add_u32 m0, %[lw], 8192\ns_add_u32 s44, %[wo], 1376256\n" \
"buffer_load_dwordx4 v125, %[rw], s44 offen nt lds\ns_add_u32 m0, %[lw], 9216\ns_add_u32 s44, %[wo], 1392640\nbuffer_load_dwordx4 v125, %[rw], s44 offen nt lds\ns_add_u32 m0, %[lw], 10240\ns_add_u32 s44, %[wo], 1409024\nbuffer_load_dwordx4 v125, %[rw], s44 offen nt lds\ns_add_u32 m0, %[lw], 11264\n" \
"s_add_u32 s44, %[wo], 1425408\nbuffer_load_dwordx4 v125, %[rw], s44 offen nt lds\ns_waitcnt vmcnt(32)\nds_read_b64_tr_b16 a[172:173], v127 offset:12288\nds_read_b64_tr_b16 a[174:175], v127 offset:12800\nds_read_b64_tr_b16 a[180:181], v127 offset:12320\nds_read_b64_tr_b16 a[182:183], v127 offset:12832\nds_read_b64_tr_b16 v[64:65], v127 offset:12352\n" \
"ds_read_b64_tr_b16 v[66:67], v127 offset:12864\nds_read_b64_tr_b16 a[188:189], v127 offset:12384\nds_read_b64_tr_b16 a[190:191], v127 offset:12896\ns_waitcnt lgkmcnt(0)\ns_add_u32 m0, %[lw], 12288\ns_add_u32 s44, %[wo], 1441792\nbuffer_load_dwordx4 v125, %[rw], s44 offen nt lds\ns_add_u32 m0, %[lw], 13312\n" \
"s_add_u32 s44, %[wo], 1458176\nbuffer_load_dwordx4 v125, %[rw], s44 offen nt lds\ns_add_u32 m0, %[lw], 14336\ns_add_u32 s44, %[wo], 1474560\nbuffer_load_dwordx4 v125, %[rw], s44 offen nt lds\ns_add_u32 m0, %[lw], 15360\ns_add_u32 s44, %[wo], 1490944\nbuffer_load_dwordx4 v125, %[rw], s44 offen nt lds\n" \
"s_waitcnt vmcnt(32)\nds_read_b64_tr_b16 a[192:193], v127 offset:16384\nds_read_b64_tr_b16 a[194:195], v127 offset:16896\nds_read_b64_tr_b16 a[200:201], v127 offset:16416\nds_read_b64_tr_b16 a[202:203], v127 offset:16928\nds_read_b64_tr_b16 a[208:209], v127 offset:16448\nds_read_b64_tr_b16 a[210:211], v127 offset:16960\nds_read_b64_tr_b16 a[216:217], v127 offset:16480\n" \
"ds_read_b64_tr_b16 a[218:219], v127 offset:16992\ns_waitcnt lgkmcnt(0)\ns_add_u32 m0, %[lw], 16384\ns_add_u32 s44, %[wo], 1507328\nbuffer_load_dwordx4 v125, %[rw], s44 offen nt lds\ns_add_u32 m0, %[lw], 17408\ns_add_u32 s44, %[wo], 1523712\nbuffer_load_dwordx4 v125, %[rw], s44 offen nt lds\n" \
"s_add_u32 m0, %[lw], 18432\ns_add_u32 s44, %[wo], 1540096\nbuffer_load_dwordx4 v125, %[rw], s44 offen nt lds\ns_add_u32 m0, %[lw], 19456\ns_add_u32 s44, %[wo], 1556480\nbuffer_load_dwordx4 v125, %[rw], s44 offen nt lds\ns_waitcnt vmcnt(32)\nds_read_b64_tr_b16 a[196:197], v127 offset:20480\n" \
"ds_read_b64_tr_b16 a[198:199], v127 offset:20992\nds_read_b64_tr_b16 a[204:205], v127 offset:20512\nds_read_b64_tr_b16 a[206:207], v127 offset:21024\nds_read_b64_tr_b16 a[212:213], v127 offset:20544\nds_read_b64_tr_b16 a[214:215], v127 offset:21056\nds_read_b64_tr_b16 a[220:221], v127 offset:20576\nds_read_b64_tr_b16 a[222:223], v127 offset:21088\ns_waitcnt lgkmcnt(0)\n" \
"s_add_u32 m0, %[lw], 20480\ns_add_u32 s44, %[wo], 1572864\nbuffer_load_dwordx4 v125, %[rw], s44 offen nt lds\ns_add_u32 m0, %[lw], 21504\ns_add_u32 s44, %[wo], 1589248\nbuffer_load_dwordx4 v125, %[rw], s44 offen nt lds\ns_add_u32 m0, %[lw], 22528\ns_add_u32 s44, %[wo], 1605632\n" \
"buffer_load_dwordx4 v125, %[rw], s44 offen nt lds\ns_add_u32 m0, %[lw], 23552\ns_add_u32 s44, %[wo], 1622016\nbuffer_load_dwordx4 v125, %[rw], s44 offen nt lds\ns_waitcnt vmcnt(32)\nds_read_b64_tr_b16 v[60:61], v126 offset:0\nds_read_b64_tr_b16 v[62:63], v126 offset:512\nds_read_b64_tr_b16 a[224:225], v126 offset:32\n" \
"ds_read_b64_tr_b16 a[226:227], v126 offset:544\nds_read_b64_tr_b16 a[232:233], v126 offset:64\nds_read_b64_tr_b16 a[234:235], v126 offset:576\nds_read_b64_tr_b16 a[240:241], v126 offset:96\nds_read_b64_tr_b16 a[242:243], v126 offset:608\ns_waitcnt lgkmcnt(0)\ns_add_u32 m0, %[tb], 0\ns_add_u32 s44, %[wo], 1638400\n" \
"buffer_load_dwordx4 v125, %[rw], s44 offen nt lds\ns_add_u32 m0, %[tb], 1024\ns_add_u32 s44, %[wo], 1654784\nbuffer_load_dwordx4 v125, %[rw], s44 offen nt lds\ns_add_u32 m0, %[tb], 2048\ns_add_u32 s44, %[wo], 1671168\nbuffer_load_dwordx4 v125, %[rw], s44 offen nt lds\ns_add_u32 m0, %[tb], 3072\n" \
"s_add_u32 s44, %[wo], 1687552\nbuffer_load_dwordx4 v125, %[rw], s44 offen nt lds\ns_waitcnt vmcnt(32)\nds_read_b64_tr_b16 v[56:57], v126 offset:4096\nds_read_b64_tr_b16 v[58:59], v126 offset:4608\nds_read_b64_tr_b16 a[228:229], v126 offset:4128\nds_read_b64_tr_b16 a[230:231], v126 offset:4640\nds_read_b64_tr_b16 a[236:237], v126 offset:4160\n" \
"ds_read_b64_tr_b16 a[238:239], v126 offset:4672\nds_read_b64_tr_b16 a[244:245], v126 offset:4192\nds_read_b64_tr_b16 a[246:247], v126 offset:4704\ns_waitcnt lgkmcnt(0)\ns_add_u32 m0, %[tb], 4096\ns_add_u32 s44, %[wo], 1703936\nbuffer_load_dwordx4 v125, %[rw], s44 offen nt lds\ns_add_u32 m0, %[tb], 5120\n" \
"s_add_u32 s44, %[wo], 1720320\nbuffer_load_dwordx4 v125, %[rw], s44 offen nt lds\ns_add_u32 m0, %[tb], 6144\ns_add_u32 s44, %[wo], 1736704\nbuffer_load_dwordx4 v125, %[rw], s44 offen nt lds\ns_add_u32 m0, %[tb], 7168\ns_add_u32 s44, %[wo], 1753088\nbuffer_load_dwordx4 v125, %[rw], s44 offen nt lds\n" \
"s_waitcnt vmcnt(32)\nds_read_b64_tr_b16 a[248:249], v126 offset:8192\nds_read_b64_tr_b16 a[250:251], v126 offset:8704\nds_read_b64_tr_b16 v[128:129], v126 offset:8224\nds_read_b64_tr_b16 v[130:131], v126 offset:8736\nds_read_b64_tr_b16 v[136:137], v126 offset:8256\nds_read_b64_tr_b16 v[138:139], v126 offset:8768\nds_read_b64_tr_b16 v[144:145], v126 offset:8288\n" \
"ds_read_b64_tr_b16 v[146:147], v126 offset:8800\ns_waitcnt lgkmcnt(0)\ns_add_u32 m0, %[tb], 8192\ns_add_u32 s44, %[wo], 1769472\nbuffer_load_dwordx4 v125, %[rw], s44 offen nt lds\ns_add_u32 m0, %[tb], 9216\ns_add_u32 s44, %[wo], 1785856\nbuffer_load_dwordx4 v125, %[rw], s44 offen nt lds\n" \
"s_add_u32 m0, %[tb], 10240\ns_add_u32 s44, %[wo], 1802240\nbuffer_load_dwordx4 v125, %[rw], s44 offen nt lds\ns_add_u32 m0, %[tb], 11264\ns_add_u32 s44, %[wo], 1818624\nbuffer_load_dwordx4 v125, %[rw], s44 offen nt lds\ns_waitcnt vmcnt(32)\nds_read_b64_tr_b16 a[252:253], v127 offset:0\n" \
"ds_read_b64_tr_b16 a[254:255], v127 offset:512\nds_read_b64_tr_b16 v[132:133], v127 offset:32\nds_read_b64_tr_b16 v[134:135], v127 offset:544\nds_read_b64_tr_b16 v[140:141], v127 offset:64\nds_read_b64_tr_b16 v[142:143], v127 offset:576\nds_read_b64_tr_b16 v[148:149], v127 offset:96\nds_read_b64_tr_b16 v[150:151], v127 offset:608\ns_waitcnt lgkmcnt(0)\n" \
"ds_write_b128 v1, v[92:95] offset:0\nds_write_b128 v1, v[88:91] offset:1024\nds_write_b128 v1, v[84:87] offset:2048\nds_write_b128 v1, v[80:83] offset:3072\ns_waitcnt vmcnt(28)\nds_read_b64_tr_b16 v[152:153], v127 offset:4096\nds_read_b64_tr_b16 v[154:155], v127 offset:4608\nds_read_b64_tr_b16 v[160:161], v127 offset:4128\n" \
"ds_read_b64_tr_b16 v[162:163], v127 offset:4640\nds_read_b64_tr_b16 v[80:81], v127 offset:4160\nds_read_b64_tr_b16 v[82:83], v127 offset:4672\nds_read_b64_tr_b16 v[168:169], v127 offset:4192\nds_read_b64_tr_b16 v[170:171], v127 offset:4704\ns_waitcnt lgkmcnt(0)\nds_write_b128 v1, v[76:79] offset:4096\nds_write_b128 v1, v[72:75] offset:5120\n" \
"ds_write_b128 v1, v[68:71] offset:6144\nds_write_b128 v1, v[64:67] offset:7168\ns_waitcnt vmcnt(24)\nds_read_b64_tr_b16 v[156:157], v127 offset:8192\nds_read_b64_tr_b16 v[158:159], v127 offset:8704\nds_read_b64_tr_b16 v[164:165], v127 offset:8224\nds_read_b64_tr_b16 v[166:167], v127 offset:8736\nds_read_b64_tr_b16 v[64:65], v127 offset:8256\n" \
"ds_read_b64_tr_b16 v[66:67], v127 offset:8768\nds_read_b64_tr_b16 v[172:173], v127 offset:8288\nds_read_b64_tr_b16 v[174:175], v127 offset:8800\ns_waitcnt lgkmcnt(0)\nds_write_b128 v1, v[60:63] offset:8192\nds_write_b128 v1, v[56:59] offset:9216\nds_write_b128 v1, v[80:83] offset:10240\nds_write_b128 v1, v[64:67] offset:11264\n" \
"s_waitcnt vmcnt(20)\nds_read_b64_tr_b16 v[176:177], v127 offset:12288\nds_read_b64_tr_b16 v[178:179], v127 offset:12800\nds_read_b64_tr_b16 v[184:185], v127 offset:12320\nds_read_b64_tr_b16 v[186:187], v127 offset:12832\nds_read_b64_tr_b16 v[192:193], v127 offset:12352\nds_read_b64_tr_b16 v[194:195], v127 offset:12864\nds_read_b64_tr_b16 v[200:201], v127 offset:12384\n" \
"ds_read_b64_tr_b16 v[202:203], v127 offset:12896\ns_waitcnt lgkmcnt(0)\ns_waitcnt vmcnt(16)\nds_read_b64_tr_b16 v[180:181], v127 offset:16384\nds_read_b64_tr_b16 v[182:183], v127 offset:16896\nds_read_b64_tr_b16 v[188:189], v127 offset:16416\nds_read_b64_tr_b16 v[190:191], v127 offset:16928\nds_read_b64_tr_b16 v[196:197], v127 offset:16448\n" \
"ds_read_b64_tr_b16 v[198:199], v127 offset:16960\nds_read_b64_tr_b16 v[204:205], v127 offset:16480\nds_read_b64_tr_b16 v[206:207], v127 offset:16992\ns_waitcnt lgkmcnt(0)\ns_waitcnt vmcnt(12)\nds_read_b64_tr_b16 v[64:65], v127 offset:20480\nds_read_b64_tr_b16 v[66:67], v127 offset:20992\nds_read_b64_tr_b16 v[208:209], v127 offset:20512\n" \
"ds_read_b64_tr_b16 v[210:211], v127 offset:21024\nds_read_b64_tr_b16 v[216:217], v127 offset:20544\nds_read_b64_tr_b16 v[218:219], v127 offset:21056\nds_read_b64_tr_b16 v[224:225], v127 offset:20576\nds_read_b64_tr_b16 v[226:227], v127 offset:21088\ns_waitcnt lgkmcnt(0)\nds_write_b128 v1, v[64:67] offset:12288\ns_waitcnt vmcnt(8)\n" \
"ds_read_b64_tr_b16 v[64:65], v126 offset:0\nds_read_b64_tr_b16 v[66:67], v126 offset:512\nds_read_b64_tr_b16 v[212:213], v126 offset:32\nds_read_b64_tr_b16 v[214:215], v126 offset:544\nds_read_b64_tr_b16 v[220:221], v126 offset:64\nds_read_b64_tr_b16 v[222:223], v126 offset:576\nds_read_b64_tr_b16 v[228:229], v126 offset:96\nds_read_b64_tr_b16 v[230:231], v126 offset:608\n" \
"s_waitcnt lgkmcnt(0)\nds_write_b128 v1, v[64:67] offset:13312\ns_waitcnt vmcnt(4)\nds_read_b64_tr_b16 v[232:233], v126 offset:4096\nds_read_b64_tr_b16 v[234:235], v126 offset:4608\nds_read_b64_tr_b16 v[64:65], v126 offset:4128\nds_read_b64_tr_b16 v[66:67], v126 offset:4640\nds_read_b64_tr_b16 v[240:241], v126 offset:4160\n" \
"ds_read_b64_tr_b16 v[242:243], v126 offset:4672\nds_read_b64_tr_b16 v[248:249], v126 offset:4192\nds_read_b64_tr_b16 v[250:251], v126 offset:4704\ns_waitcnt lgkmcnt(0)\nds_write_b128 v1, v[64:67] offset:14336\ns_waitcnt vmcnt(0)\nds_read_b64_tr_b16 v[236:237], v126 offset:8192\nds_read_b64_tr_b16 v[238:239], v126 offset:8704\n" \
"ds_read_b64_tr_b16 v[64:65], v126 offset:8224\nds_read_b64_tr_b16 v[66:67], v126 offset:8736\nds_read_b64_tr_b16 v[244:245], v126 offset:8256\nds_read_b64_tr_b16 v[246:247], v126 offset:8768\nds_read_b64_tr_b16 v[252:253], v126 offset:8288\nds_read_b64_tr_b16 v[254:255], v126 offset:8800\ns_waitcnt lgkmcnt(0)\nds_write_b128 v1, v[64:67] offset:15360\n" \
"s_waitcnt lgkmcnt(0)\ns_barrier\ns_mov_b32 s40, 0\ns_mov_b32 s43, 0\ns_mov_b32 s56, -2147483648\n.Ldd_next%=:\nbuffer_load_dword v8, v7, %[rs], 0 offen sc1\ns_lshl_b32 s58, s40, 1\n" \
"s_lshl_b32 s44, s40, 19\ns_add_u32 s44, s44, %[ho]\ns_lshr_b32 s45, s44, 8\ns_and_b32 s45, s45, 0x2000\ns_xor_b32 s55, s44, s45\ns_waitcnt vmcnt(0)\nbuffer_store_dword v9, off, %[rs], s56 sc1\n.Ldd_w0%=:\n" \
"v_cmp_gt_i32 vcc, s58, v8\ns_cbranch_vccz .Ldd_w0go%=\ns_sleep 16\nbuffer_load_dword v8, v7, %[rs], 0 offen sc1\ns_waitcnt vmcnt(0)\ns_add_u32 s43, s43, 1\ns_cmp_lt_u32 s43, 262144\ns_cbranch_scc1 .Ldd_w0%=\n" \
"s_branch .Ldd_abort%=\n.Ldd_w0go%=:\ns_add_u32 s60, s55, 0\nbuffer_load_dwordx4 v[48:51], v0, %[rs], s60 offen offset:0 sc1\nbuffer_load_dwordx4 v[52:55], v0, %[rs], s60 offen offset:2048 sc1\ns_add_u32 s60, s55, 4096\nbuffer_load_dwordx4 v[56:59], v0, %[rs], s60 offen offset:0 sc1\nbuffer_load_dwordx4 v[60:63], v0, %[rs], s60 offen offset:2048 sc1\n" \
"s_add_u32 s60, s55, 16384\nbuffer_load_dwordx4 v[64:67], v0, %[rs], s60 offen offset:0 sc1\nbuffer_load_dwordx4 v[68:71], v0, %[rs], s60 offen offset:2048 sc1\ns_add_u32 s60, s55, 20480\nbuffer_load_dwordx4 v[72:75], v0, %[rs], s60 offen offset:0 sc1\nbuffer_load_dwordx4 v[76:79], v0, %[rs], s60 offen offset:2048 sc1\ns_add_u32 s60, s55, 32768\nbuffer_load_dwordx4 v[80:83], v0, %[rs], s60 offen offset:0 sc1\n" \
"buffer_load_dwordx4 v[84:87], v0, %[rs], s60 offen offset:2048 sc1\ns_add_u32 s60, s55, 36864\nbuffer_load_dwordx4 v[88:91], v0, %[rs], s60 offen offset:0 sc1\nbuffer_load_dwordx4 v[92:95], v0, %[rs], s60 offen offset:2048 sc1\ns_add_u32 s60, s55, 49152\nbuffer_load_dwordx4 v[96:99], v0, %[rs], s60 offen offset:0 sc1\nbuffer_load_dwordx4 v[100:103], v0, %[rs], s60 offen offset:2048 sc1\ns_add_u32 s60, s55, 53248\n" \
"buffer_load_dwordx4 v[104:107], v0, %[rs], s60 offen offset:0 sc1\nbuffer_load_dwordx4 v[108:111], v0, %[rs], s60 offen offset:2048 sc1\ns_add_u32 s44, s40, 0\ns_lshl_b32 s44, s44, 17\ns_add_u32 s44, s44, %[xn]\ns_lshr_b32 s45, s44, 8\ns_and_b32 s45, s45, 0x2000\ns_xor_b32 s41, s44, s45\n" \
"s_lshl_b32 s44, s40, 16\ns_cmp_eq_u32 %[l], 0\ns_cselect_b32 s42, s41, s44\ns_add_u32 s58, s58, 1\nds_read_b128 v[120:123], v1 offset:0\ns_waitcnt vmcnt(15)\nv_mfma_f32_16x16x32_bf16 v[16:19], a[0:3], v[48:51], 0\nds_read_b128 v[124:127], v1 offset:1024\n" \
"v_mfma_f32_16x16x32_bf16 v[24:27], a[8:11], v[48:51], 0\nbuffer_load_dword v8, v7, %[rs], 0 offen sc1\nv_mfma_f32_16x16x32_bf16 v[32:35], a[16:19], v[48:51], 0\nv_mfma_f32_16x16x32_bf16 v[40:43], a[24:27], v[48:51], 0\ns_add_u32 s60, s55, 65536\ns_waitcnt vmcnt(15)\nbuffer_load_dwordx4 v[48:51], v0, %[rs], s60 offen offset:0 sc1\nv_mfma_f32_16x16x32_bf16 v[16:19], a[4:7], v[52:55], v[16:19]\n" \
"v_mfma_f32_16x16x32_bf16 v[24:27], a[12:15], v[52:55], v[24:27]\nv_mfma_f32_16x16x32_bf16 v[32:35], a[20:23], v[52:55], v[32:35]\nv_mfma_f32_16x16x32_bf16 v[40:43], a[28:31], v[52:55], v[40:43]\ns_waitcnt vmcnt(15)\nbuffer_load_dwordx4 v[52:55], v0, %[rs], s60 offen offset:2048 sc1\nv_mfma_f32_16x16x32_bf16 v[16:19], a[32:35], v[56:59], v[16:19]\ns_waitcnt lgkmcnt(1)\nv_mfma_f32_16x16x32_bf16 v[24:27], v[120:123], v[56:59], v[24:27]\n" \
"v_mfma_f32_16x16x32_bf16 v[32:35], a[40:43], v[56:59], v[32:35]\nv_mfma_f32_16x16x32_bf16 v[40:43], a[48:51], v[56:59], v[40:43]\ns_add_u32 s60, s55, 69632\ns_waitcnt vmcnt(15)\nbuffer_load_dwordx4 v[56:59], v0, %[rs], s60 offen offset:0 sc1\nv_mfma_f32_16x16x32_bf16 v[16:19], a[36:39], v[60:63], v[16:19]\ns_waitcnt lgkmcnt(0)\nds_read_b128 v[120:123], v1 offset:2048\n" \
"v_mfma_f32_16x16x32_bf16 v[24:27], v[124:127], v[60:63], v[24:27]\nv_mfma_f32_16x16x32_bf16 v[32:35], a[44:47], v[60:63], v[32:35]\nv_mfma_f32_16x16x32_bf16 v[40:43], a[52:55], v[60:63], v[40:43]\ns_waitcnt vmcnt(15)\nbuffer_load_dwordx4 v[60:63], v0, %[rs], s60 offen offset:2048 sc1\nv_mfma_f32_16x16x32_bf16 v[16:19], a[56:59], v[64:67], v[16:19]\nds_read_b128 v[124:127], v1 offset:3072\nv_mfma_f32_16x16x32_bf16 v[24:27], a[64:67], v[64:67], v[24:27]\n" \
"v_mfma_f32_16x16x32_bf16 v[32:35], a[72:75], v[64:67], v[32:35]\nv_mfma_f32_16x16x32_bf16 v[40:43], a[80:83], v[64:67], v[40:43]\ns_add_u32 s60, s55, 81920\ns_waitcnt vmcnt(15)\nbuffer_load_dwordx4 v[64:67], v0, %[rs], s60 offen offset:0 sc1\nv_mfma_f32_16x16x32_bf16 v[16:19], a[60:63], v[68:71], v[16:19]\nv_mfma_f32_16x16x32_bf16 v[24:27], a[68:71], v[68:71], v[24:27]\nv_mfma_f32_16x16x32_bf16 v[32:35], a[76:79], v[68:71], v[32:35]\n" \
"v_mfma_f32_16x16x32_bf16 v[40:43], a[84:87], v[68:71], v[40:43]\ns_waitcnt vmcnt(15)\nbuffer_load_dwordx4 v[68:71], v0, %[rs], s60 offen offset:2048 sc1\nv_mfma_f32_16x16x32_bf16 v[16:19], a[88:91], v[72:75], v[16:19]\nv_mfma_f32_16x16x32_bf16 v[24:27], a[96:99], v[72:75], v[24:27]\nv_mfma_f32_16x16x32_bf16 v[32:35], a[104:107], v[72:75], v[32:35]\ns_waitcnt lgkmcnt(1)\nv_mfma_f32_16x16x32_bf16 v[40:43], v[120:123], v[72:75], v[40:43]\n" \
"s_add_u32 s60, s55, 86016\ns_waitcnt vmcnt(15)\nbuffer_load_dwordx4 v[72:75], v0, %[rs], s60 offen offset:0 sc1\nv_mfma_f32_16x16x32_bf16 v[16:19], a[92:95], v[76:79], v[16:19]\nds_read_b128 v[120:123], v1 offset:4096\nv_mfma_f32_16x16x32_bf16 v[24:27], a[100:103], v[76:79], v[24:27]\nv_mfma_f32_16x16x32_bf16 v[32:35], a[108:111], v[76:79], v[32:35]\ns_waitcnt lgkmcnt(1)\n" \
"v_mfma_f32_16x16x32_bf16 v[40:43], v[124:127], v[76:79], v[40:43]\ns_waitcnt vmcnt(15)\nbuffer_load_dwordx4 v[76:79], v0, %[rs], s60 offen offset:2048 sc1\nv_mfma_f32_16x16x32_bf16 v[16:19], a[112:115], v[80:83], v[16:19]\nds_read_b128 v[124:127], v1 offset:5120\nv_mfma_f32_16x16x32_bf16 v[24:27], a[120:123], v[80:83], v[24:27]\nv_mfma_f32_16x16x32_bf16 v[32:35], a[128:131], v[80:83], v[32:35]\nv_mfma_f32_16x16x32_bf16 v[40:43], a[136:139], v[80:83], v[40:43]\n" \
"s_add_u32 s60, s55, 98304\ns_waitcnt vmcnt(15)\nbuffer_load_dwordx4 v[80:83], v0, %[rs], s60 offen offset:0 sc1\nv_mfma_f32_16x16x32_bf16 v[16:19], a[116:119], v[84:87], v[16:19]\nds_read_b128 v[112:115], v1 offset:6144\nv_mfma_f32_16x16x32_bf16 v[24:27], a[124:127], v[84:87], v[24:27]\nv_mfma_f32_16x16x32_bf16 v[32:35], a[132:135], v[84:87], v[32:35]\nv_mfma_f32_16x16x32_bf16 v[40:43], a[140:143], v[84:87], v[40:43]\n" \
"s_waitcnt vmcnt(15)\nbuffer_load_dwordx4 v[84:87], v0, %[rs], s60 offen offset:2048 sc1\nv_mfma_f32_16x16x32_bf16 v[16:19], a[144:147], v[88:91], v[16:19]\ns_waitcnt lgkmcnt(2)\nds_read_b128 v[116:119], v1 offset:7168\nv_mfma_f32_16x16x32_bf16 v[24:27], v[120:123], v[88:91], v[24:27]\nv_mfma_f32_16x16x32_bf16 v[32:35], a[152:155], v[88:91], v[32:35]\nv_mfma_f32_16x16x32_bf16 v[40:43], a[160:163], v[88:91], v[40:43]\n" \
"s_add_u32 s60, s55, 102400\ns_waitcnt vmcnt(15)\nbuffer_load_dwordx4 v[88:91], v0, %[rs], s60 offen offset:0 sc1\nv_mfma_f32_16x16x32_bf16 v[16:19], a[148:151], v[92:95], v[16:19]\ns_waitcnt lgkmcnt(2)\nv_mfma_f32_16x16x32_bf16 v[24:27], v[124:127], v[92:95], v[24:27]\ns_waitcnt vmcnt(11)\nv_cmp_gt_i32 vcc, s58, v8\n" \
"v_mfma_f32_16x16x32_bf16 v[32:35], a[156:159], v[92:95], v[32:35]\ns_cmp_eq_u64 vcc, 0\ns_cselect_b32 s61, s55, -2147483648\nv_mfma_f32_16x16x32_bf16 v[40:43], a[164:167], v[92:95], v[40:43]\nbuffer_load_dwordx4 v[92:95], v0, %[rs], s60 offen offset:2048 sc1\nv_mfma_f32_16x16x32_bf16 v[16:19], a[168:171], v[96:99], v[16:19]\nv_mfma_f32_16x16x32_bf16 v[24:27], a[176:179], v[96:99], v[24:27]\ns_waitcnt lgkmcnt(1)\n" \
"v_mfma_f32_16x16x32_bf16 v[32:35], v[112:115], v[96:99], v[32:35]\nv_mfma_f32_16x16x32_bf16 v[40:43], a[184:187], v[96:99], v[40:43]\ns_add_u32 s60, s61, 0\nbuffer_load_dwordx4 v[96:99], v0, %[rs], s60 offen offset:1024 sc1\nv_mfma_f32_16x16x32_bf16 v[16:19], a[172:175], v[100:103], v[16:19]\nds_read_b128 v[112:115], v1 offset:8192\nv_mfma_f32_16x16x32_bf16 v[24:27], a[180:183], v[100:103], v[24:27]\ns_waitcnt lgkmcnt(1)\n" \
"v_mfma_f32_16x16x32_bf16 v[32:35], v[116:119], v[100:103], v[32:35]\nv_mfma_f32_16x16x32_bf16 v[40:43], a[188:191], v[100:103], v[40:43]\nbuffer_load_dwordx4 v[100:103], v0, %[rs], s60 offen offset:3072 sc1\nv_mfma_f32_16x16x32_bf16 v[16:19], a[192:195], v[104:107], v[16:19]\nds_read_b128 v[116:119], v1 offset:9216\nv_mfma_f32_16x16x32_bf16 v[24:27], a[200:203], v[104:107], v[24:27]\nv_mfma_f32_16x16x32_bf16 v[32:35], a[208:211], v[104:107], v[32:35]\nv_mfma_f32_16x16x32_bf16 v[40:43], a[216:219], v[104:107], v[40:43]\n" \
"s_add_u32 s60, s61, 4096\nbuffer_load_dwordx4 v[104:107], v0, %[rs], s60 offen offset:1024 sc1\nv_mfma_f32_16x16x32_bf16 v[16:19], a[196:199], v[108:111], v[16:19]\nv_mfma_f32_16x16x32_bf16 v[24:27], a[204:207], v[108:111], v[24:27]\nv_mfma_f32_16x16x32_bf16 v[32:35], a[212:215], v[108:111], v[32:35]\nv_mfma_f32_16x16x32_bf16 v[40:43], a[220:223], v[108:111], v[40:43]\ns_waitcnt vmcnt(14)\ns_waitcnt lgkmcnt(1)\n" \
"buffer_load_dwordx4 v[108:111], v0, %[rs], s60 offen offset:3072 sc1\nv_mfma_f32_16x16x32_bf16 v[16:19], v[112:115], v[48:51], v[16:19]\nv_mfma_f32_16x16x32_bf16 v[24:27], a[224:227], v[48:51], v[24:27]\nv_mfma_f32_16x16x32_bf16 v[32:35], a[232:235], v[48:51], v[32:35]\nv_mfma_f32_16x16x32_bf16 v[40:43], a[240:243], v[48:51], v[40:43]\ns_add_u32 s60, s61, 16384\ns_waitcnt vmcnt(14)\ns_waitcnt lgkmcnt(0)\n" \
"buffer_load_dwordx4 v[48:51], v0, %[rs], s60 offen offset:1024 sc1\nv_mfma_f32_16x16x32_bf16 v[16:19], v[116:119], v[52:55], v[16:19]\nds_read_b128 v[112:115], v1 offset:10240\nv_mfma_f32_16x16x32_bf16 v[24:27], a[228:231], v[52:55], v[24:27]\nv_mfma_f32_16x16x32_bf16 v[32:35], a[236:239], v[52:55], v[32:35]\nv_mfma_f32_16x16x32_bf16 v[40:43], a[244:247], v[52:55], v[40:43]\ns_waitcnt vmcnt(14)\nbuffer_load_dwordx4 v[52:55], v0, %[rs], s60 offen offset:3072 sc1\n" \
"v_mfma_f32_16x16x32_bf16 v[16:19], a[248:251], v[56:59], v[16:19]\nds_read_b128 v[116:119], v1 offset:11264\nv_mfma_f32_16x16x32_bf16 v[24:27], v[128:131], v[56:59], v[24:27]\nv_mfma_f32_16x16x32_bf16 v[32:35], v[136:139], v[56:59], v[32:35]\nv_mfma_f32_16x16x32_bf16 v[40:43], v[144:147], v[56:59], v[40:43]\ns_add_u32 s60, s61, 20480\ns_waitcnt vmcnt(14)\nbuffer_load_dwordx4 v[56:59], v0, %[rs], s60 offen offset:1024 sc1\n" \
"v_mfma_f32_16x16x32_bf16 v[16:19], a[252:255], v[60:63], v[16:19]\nv_mfma_f32_16x16x32_bf16 v[24:27], v[132:135], v[60:63], v[24:27]\nv_mfma_f32_16x16x32_bf16 v[32:35], v[140:143], v[60:63], v[32:35]\nv_mfma_f32_16x16x32_bf16 v[40:43], v[148:151], v[60:63], v[40:43]\ns_waitcnt vmcnt(14)\nbuffer_load_dwordx4 v[60:63], v0, %[rs], s60 offen offset:3072 sc1\nv_mfma_f32_16x16x32_bf16 v[16:19], v[152:155], v[64:67], v[16:19]\nv_mfma_f32_16x16x32_bf16 v[24:27], v[160:163], v[64:67], v[24:27]\n" \
"s_waitcnt lgkmcnt(1)\nv_mfma_f32_16x16x32_bf16 v[32:35], v[112:115], v[64:67], v[32:35]\nv_mfma_f32_16x16x32_bf16 v[40:43], v[168:171], v[64:67], v[40:43]\ns_add_u32 s60, s61, 32768\ns_waitcnt vmcnt(14)\nbuffer_load_dwordx4 v[64:67], v0, %[rs], s60 offen offset:1024 sc1\nv_mfma_f32_16x16x32_bf16 v[16:19], v[156:159], v[68:71], v[16:19]\nds_read_b128 v[112:115], v1 offset:12288\n" \
"v_mfma_f32_16x16x32_bf16 v[24:27], v[164:167], v[68:71], v[24:27]\ns_waitcnt lgkmcnt(1)\nv_mfma_f32_16x16x32_bf16 v[32:35], v[116:119], v[68:71], v[32:35]\nv_mfma_f32_16x16x32_bf16 v[40:43], v[172:175], v[68:71], v[40:43]\ns_waitcnt vmcnt(14)\nbuffer_load_dwordx4 v[68:71], v0, %[rs], s60 offen offset:3072 sc1\nv_mfma_f32_16x16x32_bf16 v[16:19], v[176:179], v[72:75], v[16:19]\nds_read_b128 v[116:119], v1 offset:13312\n" \
"v_mfma_f32_16x16x32_bf16 v[24:27], v[184:187], v[72:75], v[24:27]\nv_mfma_f32_16x16x32_bf16 v[32:35], v[192:195], v[72:75], v[32:35]\nv_mfma_f32_16x16x32_bf16 v[40:43], v[200:203], v[72:75], v[40:43]\ns_add_u32 s60, s61, 36864\ns_waitcnt vmcnt(14)\nbuffer_load_dwordx4 v[72:75], v0, %[rs], s60 offen offset:1024 sc1\nv_mfma_f32_16x16x32_bf16 v[16:19], v[180:183], v[76:79], v[16:19]\nds_read_b128 v[120:123], v1 offset:14336\n" \
"v_mfma_f32_16x16x32_bf16 v[24:27], v[188:191], v[76:79], v[24:27]\nv_mfma_f32_16x16x32_bf16 v[32:35], v[196:199], v[76:79], v[32:35]\nv_mfma_f32_16x16x32_bf16 v[40:43], v[204:207], v[76:79], v[40:43]\ns_waitcnt vmcnt(14)\ns_waitcnt lgkmcnt(2)\nbuffer_load_dwordx4 v[76:79], v0, %[rs], s60 offen offset:3072 sc1\nv_mfma_f32_16x16x32_bf16 v[16:19], v[112:115], v[80:83], v[16:19]\nds_read_b128 v[124:127], v1 offset:15360\n" \
"v_mfma_f32_16x16x32_bf16 v[24:27], v[208:211], v[80:83], v[24:27]\nv_mfma_f32_16x16x32_bf16 v[32:35], v[216:219], v[80:83], v[32:35]\nv_mfma_f32_16x16x32_bf16 v[40:43], v[224:227], v[80:83], v[40:43]\ns_add_u32 s60, s61, 49152\ns_waitcnt vmcnt(14)\ns_waitcnt lgkmcnt(2)\nbuffer_load_dwordx4 v[80:83], v0, %[rs], s60 offen offset:1024 sc1\nv_mfma_f32_16x16x32_bf16 v[16:19], v[116:119], v[84:87], v[16:19]\n" \
"v_mfma_f32_16x16x32_bf16 v[24:27], v[212:215], v[84:87], v[24:27]\nv_mfma_f32_16x16x32_bf16 v[32:35], v[220:223], v[84:87], v[32:35]\nv_mfma_f32_16x16x32_bf16 v[40:43], v[228:231], v[84:87], v[40:43]\ns_waitcnt vmcnt(14)\nbuffer_load_dwordx4 v[84:87], v0, %[rs], s60 offen offset:3072 sc1\nv_mfma_f32_16x16x32_bf16 v[16:19], v[232:235], v[88:91], v[16:19]\ns_waitcnt lgkmcnt(1)\nv_mfma_f32_16x16x32_bf16 v[24:27], v[120:123], v[88:91], v[24:27]\n" \
"v_mfma_f32_16x16x32_bf16 v[32:35], v[240:243], v[88:91], v[32:35]\nv_mfma_f32_16x16x32_bf16 v[40:43], v[248:251], v[88:91], v[40:43]\ns_add_u32 s60, s61, 53248\ns_waitcnt vmcnt(14)\nbuffer_load_dwordx4 v[88:91], v0, %[rs], s60 offen offset:1024 sc1\nv_mfma_f32_16x16x32_bf16 v[16:19], v[236:239], v[92:95], v[16:19]\ns_waitcnt lgkmcnt(0)\nds_read_b128 v[120:123], v1 offset:0\n" \
"v_mfma_f32_16x16x32_bf16 v[24:27], v[124:127], v[92:95], v[24:27]\nv_mfma_f32_16x16x32_bf16 v[32:35], v[244:247], v[92:95], v[32:35]\nv_mfma_f32_16x16x32_bf16 v[40:43], v[252:255], v[92:95], v[40:43]\nbuffer_load_dwordx4 v[92:95], v0, %[rs], s60 offen offset:3072 sc1\ns_cbranch_vccnz .Ldd_slow%=\n.Ldd_slowr%=:\ns_waitcnt vmcnt(15)\nv_mfma_f32_16x16x32_bf16 v[20:23], a[0:3], v[96:99], 0\n" \
"ds_read_b128 v[124:127], v1 offset:1024\nv_mfma_f32_16x16x32_bf16 v[28:31], a[8:11], v[96:99], 0\nv_mfma_f32_16x16x32_bf16 v[36:39], a[16:19], v[96:99], 0\nv_mfma_f32_16x16x32_bf16 v[44:47], a[24:27], v[96:99], 0\ns_add_u32 s60, s55, 65536\ns_waitcnt vmcnt(14)\nbuffer_load_dwordx4 v[96:99], v0, %[rs], s60 offen offset:1024 sc1\nv_mfma_f32_16x16x32_bf16 v[20:23], a[4:7], v[100:103], v[20:23]\n" \
"v_mfma_f32_16x16x32_bf16 v[28:31], a[12:15], v[100:103], v[28:31]\nv_mfma_f32_16x16x32_bf16 v[36:39], a[20:23], v[100:103], v[36:39]\nv_mfma_f32_16x16x32_bf16 v[44:47], a[28:31], v[100:103], v[44:47]\ns_waitcnt vmcnt(14)\nbuffer_load_dwordx4 v[100:103], v0, %[rs], s60 offen offset:3072 sc1\nv_mfma_f32_16x16x32_bf16 v[20:23], a[32:35], v[104:107], v[20:23]\ns_waitcnt lgkmcnt(1)\nv_mfma_f32_16x16x32_bf16 v[28:31], v[120:123], v[104:107], v[28:31]\n" \
"ds_write_b128 v2, v[16:19] offset:0\nv_mfma_f32_16x16x32_bf16 v[36:39], a[40:43], v[104:107], v[36:39]\nv_mfma_f32_16x16x32_bf16 v[44:47], a[48:51], v[104:107], v[44:47]\ns_add_u32 s60, s55, 69632\ns_waitcnt vmcnt(14)\nbuffer_load_dwordx4 v[104:107], v0, %[rs], s60 offen offset:1024 sc1\nv_mfma_f32_16x16x32_bf16 v[20:23], a[36:39], v[108:111], v[20:23]\ns_waitcnt lgkmcnt(1)\n" \
"ds_read_b128 v[120:123], v1 offset:2048\nv_mfma_f32_16x16x32_bf16 v[28:31], v[124:127], v[108:111], v[28:31]\nv_mfma_f32_16x16x32_bf16 v[36:39], a[44:47], v[108:111], v[36:39]\nv_mfma_f32_16x16x32_bf16 v[44:47], a[52:55], v[108:111], v[44:47]\ns_waitcnt vmcnt(14)\nbuffer_load_dwordx4 v[108:111], v0, %[rs], s60 offen offset:3072 sc1\nv_mfma_f32_16x16x32_bf16 v[20:23], a[56:59], v[48:51], v[20:23]\nds_read_b128 v[124:127], v1 offset:3072\n" \
"v_mfma_f32_16x16x32_bf16 v[28:31], a[64:67], v[48:51], v[28:31]\nds_write_b128 v2, v[24:27] offset:2048\nv_mfma_f32_16x16x32_bf16 v[36:39], a[72:75], v[48:51], v[36:39]\nv_mfma_f32_16x16x32_bf16 v[44:47], a[80:83], v[48:51], v[44:47]\ns_add_u32 s60, s55, 81920\ns_waitcnt vmcnt(14)\nbuffer_load_dwordx4 v[48:51], v0, %[rs], s60 offen offset:1024 sc1\nv_mfma_f32_16x16x32_bf16 v[20:23], a[60:63], v[52:55], v[20:23]\n" \
"v_mfma_f32_16x16x32_bf16 v[28:31], a[68:71], v[52:55], v[28:31]\nv_mfma_f32_16x16x32_bf16 v[36:39], a[76:79], v[52:55], v[36:39]\nv_mfma_f32_16x16x32_bf16 v[44:47], a[84:87], v[52:55], v[44:47]\ns_waitcnt vmcnt(14)\nbuffer_load_dwordx4 v[52:55], v0, %[rs], s60 offen offset:3072 sc1\nv_mfma_f32_16x16x32_bf16 v[20:23], a[88:91], v[56:59], v[20:23]\nv_mfma_f32_16x16x32_bf16 v[28:31], a[96:99], v[56:59], v[28:31]\nds_write_b128 v2, v[32:35] offset:4096\n" \
"v_mfma_f32_16x16x32_bf16 v[36:39], a[104:107], v[56:59], v[36:39]\ns_waitcnt lgkmcnt(3)\nv_mfma_f32_16x16x32_bf16 v[44:47], v[120:123], v[56:59], v[44:47]\ns_add_u32 s60, s55, 86016\ns_waitcnt vmcnt(14)\nbuffer_load_dwordx4 v[56:59], v0, %[rs], s60 offen offset:1024 sc1\nv_mfma_f32_16x16x32_bf16 v[20:23], a[92:95], v[60:63], v[20:23]\nds_read_b128 v[120:123], v1 offset:4096\n" \
"v_mfma_f32_16x16x32_bf16 v[28:31], a[100:103], v[60:63], v[28:31]\nv_mfma_f32_16x16x32_bf16 v[36:39], a[108:111], v[60:63], v[36:39]\ns_waitcnt lgkmcnt(3)\nv_mfma_f32_16x16x32_bf16 v[44:47], v[124:127], v[60:63], v[44:47]\ns_waitcnt vmcnt(14)\nbuffer_load_dwordx4 v[60:63], v0, %[rs], s60 offen offset:3072 sc1\nv_mfma_f32_16x16x32_bf16 v[20:23], a[112:115], v[64:67], v[20:23]\nds_read_b128 v[124:127], v1 offset:5120\n" \
"v_mfma_f32_16x16x32_bf16 v[28:31], a[120:123], v[64:67], v[28:31]\nds_write_b128 v2, v[40:43] offset:6144\nv_mfma_f32_16x16x32_bf16 v[36:39], a[128:131], v[64:67], v[36:39]\nv_mfma_f32_16x16x32_bf16 v[44:47], a[136:139], v[64:67], v[44:47]\ns_add_u32 s60, s55, 98304\ns_waitcnt vmcnt(14)\nbuffer_load_dwordx4 v[64:67], v0, %[rs], s60 offen offset:1024 sc1\nv_mfma_f32_16x16x32_bf16 v[20:23], a[116:119], v[68:71], v[20:23]\n" \
"ds_read_b128 v[112:115], v1 offset:6144\nv_mfma_f32_16x16x32_bf16 v[28:31], a[124:127], v[68:71], v[28:31]\nv_mfma_f32_16x16x32_bf16 v[36:39], a[132:135], v[68:71], v[36:39]\nv_mfma_f32_16x16x32_bf16 v[44:47], a[140:143], v[68:71], v[44:47]\ns_waitcnt vmcnt(14)\nbuffer_load_dwordx4 v[68:71], v0, %[rs], s60 offen offset:3072 sc1\nv_mfma_f32_16x16x32_bf16 v[20:23], a[144:147], v[72:75], v[20:23]\ns_waitcnt lgkmcnt(3)\n" \
"ds_read_b128 v[116:119], v1 offset:7168\nv_mfma_f32_16x16x32_bf16 v[28:31], v[120:123], v[72:75], v[28:31]\nv_mfma_f32_16x16x32_bf16 v[36:39], a[152:155], v[72:75], v[36:39]\nv_mfma_f32_16x16x32_bf16 v[44:47], a[160:163], v[72:75], v[44:47]\ns_add_u32 s60, s55, 102400\ns_waitcnt vmcnt(14)\nbuffer_load_dwordx4 v[72:75], v0, %[rs], s60 offen offset:1024 sc1\nv_mfma_f32_16x16x32_bf16 v[20:23], a[148:151], v[76:79], v[20:23]\n" \
"s_waitcnt lgkmcnt(3)\nv_mfma_f32_16x16x32_bf16 v[28:31], v[124:127], v[76:79], v[28:31]\nv_mfma_f32_16x16x32_bf16 v[36:39], a[156:159], v[76:79], v[36:39]\nv_mfma_f32_16x16x32_bf16 v[44:47], a[164:167], v[76:79], v[44:47]\ns_waitcnt vmcnt(14)\nbuffer_load_dwordx4 v[76:79], v0, %[rs], s60 offen offset:3072 sc1\nv_mfma_f32_16x16x32_bf16 v[20:23], a[168:171], v[80:83], v[20:23]\nv_mfma_f32_16x16x32_bf16 v[28:31], a[176:179], v[80:83], v[28:31]\n" \
"s_waitcnt lgkmcnt(1)\nv_mfma_f32_16x16x32_bf16 v[36:39], v[112:115], v[80:83], v[36:39]\nv_mfma_f32_16x16x32_bf16 v[44:47], a[184:187], v[80:83], v[44:47]\ns_waitcnt vmcnt(14)\nbuffer_load_dwordx4 v[12:15], v4, %[rs], s41 offen sc1\nv_mfma_f32_16x16x32_bf16 v[20:23], a[172:175], v[84:87], v[20:23]\nds_read_b128 v[112:115], v1 offset:8192\nv_mfma_f32_16x16x32_bf16 v[28:31], a[180:183], v[84:87], v[28:31]\n" \
"s_waitcnt lgkmcnt(1)\nv_mfma_f32_16x16x32_bf16 v[36:39], v[116:119], v[84:87], v[36:39]\nv_mfma_f32_16x16x32_bf16 v[44:47], a[188:191], v[84:87], v[44:47]\ns_waitcnt vmcnt(14)\nv_mfma_f32_16x16x32_bf16 v[20:23], a[192:195], v[88:91], v[20:23]\nds_read_b128 v[116:119], v1 offset:9216\nv_mfma_f32_16x16x32_bf16 v[28:31], a[200:203], v[88:91], v[28:31]\nv_mfma_f32_16x16x32_bf16 v[36:39], a[208:211], v[88:91], v[36:39]\n" \
"v_mfma_f32_16x16x32_bf16 v[44:47], a[216:219], v[88:91], v[44:47]\ns_waitcnt vmcnt(13)\nv_mfma_f32_16x16x32_bf16 v[20:23], a[196:199], v[92:95], v[20:23]\nv_mfma_f32_16x16x32_bf16 v[28:31], a[204:207], v[92:95], v[28:31]\nv_mfma_f32_16x16x32_bf16 v[36:39], a[212:215], v[92:95], v[36:39]\nv_mfma_f32_16x16x32_bf16 v[44:47], a[220:223], v[92:95], v[44:47]\ns_waitcnt vmcnt(12)\ns_waitcnt lgkmcnt(1)\n" \
"v_mfma_f32_16x16x32_bf16 v[20:23], v[112:115], v[96:99], v[20:23]\nv_mfma_f32_16x16x32_bf16 v[28:31], a[224:227], v[96:99], v[28:31]\nv_mfma_f32_16x16x32_bf16 v[36:39], a[232:235], v[96:99], v[36:39]\nv_mfma_f32_16x16x32_bf16 v[44:47], a[240:243], v[96:99], v[44:47]\ns_waitcnt vmcnt(11)\ns_waitcnt lgkmcnt(0)\nv_mfma_f32_16x16x32_bf16 v[20:23], v[116:119], v[100:103], v[20:23]\nds_read_b128 v[112:115], v1 offset:10240\n" \
"v_mfma_f32_16x16x32_bf16 v[28:31], a[228:231], v[100:103], v[28:31]\nv_mfma_f32_16x16x32_bf16 v[36:39], a[236:239], v[100:103], v[36:39]\nv_mfma_f32_16x16x32_bf16 v[44:47], a[244:247], v[100:103], v[44:47]\ns_waitcnt vmcnt(10)\nv_mfma_f32_16x16x32_bf16 v[20:23], a[248:251], v[104:107], v[20:23]\nds_read_b128 v[116:119], v1 offset:11264\nv_mfma_f32_16x16x32_bf16 v[28:31], v[128:131], v[104:107], v[28:31]\nv_mfma_f32_16x16x32_bf16 v[36:39], v[136:139], v[104:107], v[36:39]\n" \
"v_mfma_f32_16x16x32_bf16 v[44:47], v[144:147], v[104:107], v[44:47]\ns_waitcnt vmcnt(9)\nv_mfma_f32_16x16x32_bf16 v[20:23], a[252:255], v[108:111], v[20:23]\nv_mfma_f32_16x16x32_bf16 v[28:31], v[132:135], v[108:111], v[28:31]\nv_mfma_f32_16x16x32_bf16 v[36:39], v[140:143], v[108:111], v[36:39]\nv_mfma_f32_16x16x32_bf16 v[44:47], v[148:151], v[108:111], v[44:47]\ns_waitcnt vmcnt(8)\nv_mfma_f32_16x16x32_bf16 v[20:23], v[152:155], v[48:51], v[20:23]\n" \
"v_mfma_f32_16x16x32_bf16 v[28:31], v[160:163], v[48:51], v[28:31]\ns_waitcnt lgkmcnt(1)\nv_mfma_f32_16x16x32_bf16 v[36:39], v[112:115], v[48:51], v[36:39]\nv_mfma_f32_16x16x32_bf16 v[44:47], v[168:171], v[48:51], v[44:47]\ns_waitcnt vmcnt(7)\nv_mfma_f32_16x16x32_bf16 v[20:23], v[156:159], v[52:55], v[20:23]\nds_read_b128 v[112:115], v1 offset:12288\nv_mfma_f32_16x16x32_bf16 v[28:31], v[164:167], v[52:55], v[28:31]\n" \
"s_waitcnt lgkmcnt(1)\nv_mfma_f32_16x16x32_bf16 v[36:39], v[116:119], v[52:55], v[36:39]\nv_mfma_f32_16x16x32_bf16 v[44:47], v[172:175], v[52:55], v[44:47]\ns_waitcnt vmcnt(6)\nv_mfma_f32_16x16x32_bf16 v[20:23], v[176:179], v[56:59], v[20:23]\nds_read_b128 v[116:119], v1 offset:13312\nv_mfma_f32_16x16x32_bf16 v[28:31], v[184:187], v[56:59], v[28:31]\nv_mfma_f32_16x16x32_bf16 v[36:39], v[192:195], v[56:59], v[36:39]\n" \
"v_mfma_f32_16x16x32_bf16 v[44:47], v[200:203], v[56:59], v[44:47]\ns_waitcnt vmcnt(5)\nv_mfma_f32_16x16x32_bf16 v[20:23], v[180:183], v[60:63], v[20:23]\nds_read_b128 v[120:123], v1 offset:14336\nv_mfma_f32_16x16x32_bf16 v[28:31], v[188:191], v[60:63], v[28:31]\nv_mfma_f32_16x16x32_bf16 v[36:39], v[196:199], v[60:63], v[36:39]\nv_mfma_f32_16x16x32_bf16 v[44:47], v[204:207], v[60:63], v[44:47]\ns_waitcnt vmcnt(4)\n" \
"s_waitcnt lgkmcnt(2)\nv_mfma_f32_16x16x32_bf16 v[20:23], v[112:115], v[64:67], v[20:23]\nds_read_b128 v[124:127], v1 offset:15360\nv_mfma_f32_16x16x32_bf16 v[28:31], v[208:211], v[64:67], v[28:31]\nv_mfma_f32_16x16x32_bf16 v[36:39], v[216:219], v[64:67], v[36:39]\nv_mfma_f32_16x16x32_bf16 v[44:47], v[224:227], v[64:67], v[44:47]\ns_waitcnt vmcnt(3)\ns_waitcnt lgkmcnt(2)\n" \
"v_mfma_f32_16x16x32_bf16 v[20:23], v[116:119], v[68:71], v[20:23]\nv_mfma_f32_16x16x32_bf16 v[28:31], v[212:215], v[68:71], v[28:31]\nv_mfma_f32_16x16x32_bf16 v[36:39], v[220:223], v[68:71], v[36:39]\nv_mfma_f32_16x16x32_bf16 v[44:47], v[228:231], v[68:71], v[44:47]\ns_waitcnt vmcnt(2)\nv_mfma_f32_16x16x32_bf16 v[20:23], v[232:235], v[72:75], v[20:23]\ns_waitcnt lgkmcnt(1)\nv_mfma_f32_16x16x32_bf16 v[28:31], v[120:123], v[72:75], v[28:31]\n" \
"v_mfma_f32_16x16x32_bf16 v[36:39], v[240:243], v[72:75], v[36:39]\nv_mfma_f32_16x16x32_bf16 v[44:47], v[248:251], v[72:75], v[44:47]\ns_waitcnt vmcnt(1)\nv_mfma_f32_16x16x32_bf16 v[20:23], v[236:239], v[76:79], v[20:23]\ns_waitcnt vmcnt(0)\ns_waitcnt lgkmcnt(0)\nv_lshlrev_b32 v80, 16, v12\nv_and_b32 v81, 0xffff0000, v12\n" \
"v_mfma_f32_16x16x32_bf16 v[28:31], v[124:127], v[76:79], v[28:31]\nv_lshlrev_b32 v82, 16, v13\nv_and_b32 v83, 0xffff0000, v13\nv_mfma_f32_16x16x32_bf16 v[36:39], v[244:247], v[76:79], v[36:39]\nv_lshlrev_b32 v84, 16, v14\nv_and_b32 v85, 0xffff0000, v14\nv_mfma_f32_16x16x32_bf16 v[44:47], v[252:255], v[76:79], v[44:47]\ns_mul_i32 s44, s40, %[l]\n" \
"ds_write_b128 v2, v[20:23] offset:1024\nds_write_b128 v2, v[28:31] offset:3072\ns_nop 0\nds_write_b128 v2, v[36:39] offset:5120\ns_nop 2\nds_write_b128 v2, v[44:47] offset:7168\nv_lshlrev_b32 v86, 16, v15\nv_and_b32 v87, 0xffff0000, v15\n" \
"v_mov_b32 v9, s40\ns_cmp_eq_u32 s44, 0\ns_cselect_b32 s56, %[fd], -2147483648\ns_add_u32 s40, s40, 1\ns_waitcnt lgkmcnt(0)\ns_barrier\nds_read_b128 v[16:19], v3 offset:0\nds_read_b128 v[20:23], v3 offset:256\n" \
"ds_read_b128 v[24:27], v3 offset:8192\nds_read_b128 v[28:31], v3 offset:8448\nds_read_b128 v[32:35], v3 offset:16384\nds_read_b128 v[36:39], v3 offset:16640\nds_read_b128 v[40:43], v3 offset:24576\nds_read_b128 v[44:47], v3 offset:24832\ns_waitcnt lgkmcnt(6)\nv_pk_add_f32 v[16:17], v[16:17], v[80:81]\n" \
"v_pk_add_f32 v[18:19], v[18:19], v[82:83]\nv_pk_add_f32 v[20:21], v[20:21], v[84:85]\nv_pk_add_f32 v[22:23], v[22:23], v[86:87]\ns_waitcnt lgkmcnt(4)\nv_pk_add_f32 v[16:17], v[16:17], v[24:25]\nv_pk_add_f32 v[18:19], v[18:19], v[26:27]\nv_pk_add_f32 v[20:21], v[20:21], v[28:29]\nv_pk_add_f32 v[22:23], v[22:23], v[30:31]\n" \
"s_waitcnt lgkmcnt(2)\nv_pk_add_f32 v[16:17], v[16:17], v[32:33]\nv_pk_add_f32 v[18:19], v[18:19], v[34:35]\nv_pk_add_f32 v[20:21], v[20:21], v[36:37]\nv_pk_add_f32 v[22:23], v[22:23], v[38:39]\ns_waitcnt lgkmcnt(0)\nv_pk_add_f32 v[16:17], v[16:17], v[40:41]\nv_pk_add_f32 v[18:19], v[18:19], v[42:43]\n" \
"v_pk_add_f32 v[20:21], v[20:21], v[44:45]\nv_pk_add_f32 v[22:23], v[22:23], v[46:47]\nv_cvt_pk_bf16_f32 v12, v16, v17\nv_cvt_pk_bf16_f32 v13, v18, v19\nv_cvt_pk_bf16_f32 v14, v20, v21\nv_cvt_pk_bf16_f32 v15, v22, v23\nbuffer_store_dwordx4 v[12:15], v5, %[ro], s42 offen sc1\nv_pk_add_f32 v[24:25], v[16:17], v[18:19]\n" \
"v_pk_add_f32 v[26:27], v[20:21], v[22:23]\nv_pk_add_f32 v[24:25], v[24:25], v[26:27]\nv_add_f32 v24, v24, v25\ns_nop 1\nv_add_f32_dpp v25, v24, v24 row_ror:8 row_mask:0xf bank_mask:0xf\nv_add_f32_dpp v26, v24, v24 row_ror:8 row_mask:0xf bank_mask:0xf\ns_nop 1\nv_permlane16_swap_b32 v25, v26\n" \
"v_add_f32 v24, v25, v26\nv_add_f32 v25, v25, v26\ns_nop 1\nv_permlane32_swap_b32 v24, v25\nv_add_f32 v24, v24, v25\nv_mul_f32 v24, 0x3c800000, v24\nv_pk_add_f32 v[16:17], v[16:17], v[24:25] op_sel_hi:[1,0] neg_lo:[0,1] neg_hi:[0,1]\nv_pk_add_f32 v[18:19], v[18:19], v[24:25] op_sel_hi:[1,0] neg_lo:[0,1] neg_hi:[0,1]\n" \
"v_pk_add_f32 v[20:21], v[20:21], v[24:25] op_sel_hi:[1,0] neg_lo:[0,1] neg_hi:[0,1]\nv_pk_add_f32 v[22:23], v[22:23], v[24:25] op_sel_hi:[1,0] neg_lo:[0,1] neg_hi:[0,1]\nv_pk_mul_f32 v[28:29], v[16:17], v[16:17]\nv_pk_mul_f32 v[30:31], v[18:19], v[18:19]\nv_pk_fma_f32 v[28:29], v[20:21], v[20:21], v[28:29]\nv_pk_fma_f32 v[30:31], v[22:23], v[22:23], v[30:31]\nv_pk_add_f32 v[28:29], v[28:29], v[30:31]\nv_add_f32 v28, v28, v29\n" \
"s_nop 1\nv_add_f32_dpp v29, v28, v28 row_ror:8 row_mask:0xf bank_mask:0xf\nv_add_f32_dpp v30, v28, v28 row_ror:8 row_mask:0xf bank_mask:0xf\ns_nop 1\nv_permlane16_swap_b32 v29, v30\nv_add_f32 v28, v29, v30\nv_add_f32 v29, v29, v30\ns_nop 1\n" \
"v_permlane32_swap_b32 v28, v29\nv_add_f32 v28, v28, v29\nv_mov_b32 v25, v28\nbuffer_store_dwordx2 v[24:25], v6, %[rs], s41 offen sc1\nv_xor_b32 v2, 0x8000, v2\nv_xor_b32 v3, 0x8000, v3\ns_cmp_lt_u32 s40, 64\ns_cbranch_scc1 .Ldd_next%=\n" \
"s_waitcnt vmcnt(0)\nbuffer_store_dword v9, off, %[rs], s56 sc1\ns_branch .Ldd_end%=\n.Ldd_slow%=:\n.Ldd_w1%=:\nv_cmp_gt_i32 vcc, s58, v8\ns_cbranch_vccz .Ldd_w1go%=\ns_sleep 16\n" \
"buffer_load_dword v8, v7, %[rs], 0 offen sc1\ns_waitcnt vmcnt(0)\ns_add_u32 s43, s43, 1\ns_cmp_lt_u32 s43, 262144\ns_cbranch_scc1 .Ldd_w1%=\ns_branch .Ldd_abort%=\n.Ldd_w1go%=:\ns_add_u32 s60, s55, 0\n" \
"buffer_load_dwordx4 v[96:99], v0, %[rs], s60 offen offset:1024 sc1\nbuffer_load_dwordx4 v[100:103], v0, %[rs], s60 offen offset:3072 sc1\ns_add_u32 s60, s55, 4096\nbuffer_load_dwordx4 v[104:107], v0, %[rs], s60 offen offset:1024 sc1\nbuffer_load_dwordx4 v[108:111], v0, %[rs], s60 offen offset:3072 sc1\ns_add_u32 s60, s55, 16384\nbuffer_load_dwordx4 v[48:51], v0, %[rs], s60 offen offset:1024 sc1\nbuffer_load_dwordx4 v[52:55], v0, %[rs], s60 offen offset:3072 sc1\n" \
"s_add_u32 s60, s55, 20480\nbuffer_load_dwordx4 v[56:59], v0, %[rs], s60 offen offset:1024 sc1\nbuffer_load_dwordx4 v[60:63], v0, %[rs], s60 offen offset:3072 sc1\ns_add_u32 s60, s55, 32768\nbuffer_load_dwordx4 v[64:67], v0, %[rs], s60 offen offset:1024 sc1\nbuffer_load_dwordx4 v[68:71], v0, %[rs], s60 offen offset:3072 sc1\ns_add_u32 s60, s55, 36864\nbuffer_load_dwordx4 v[72:75], v0, %[rs], s60 offen offset:1024 sc1\n" \
"buffer_load_dwordx4 v[76:79], v0, %[rs], s60 offen offset:3072 sc1\ns_add_u32 s60, s55, 49152\nbuffer_load_dwordx4 v[80:83], v0, %[rs], s60 offen offset:1024 sc1\nbuffer_load_dwordx4 v[84:87], v0, %[rs], s60 offen offset:3072 sc1\ns_add_u32 s60, s55, 53248\nbuffer_load_dwordx4 v[88:91], v0, %[rs], s60 offen offset:1024 sc1\nbuffer_load_dwordx4 v[92:95], v0, %[rs], s60 offen offset:3072 sc1\ns_branch .Ldd_slowr%=\n" \
".Ldd_abort%=:\ns_mov_b64 exec, -1\nv_mov_b32 v16, 0\ns_movk_i32 s44, 6400\nbuffer_store_dword v16, off, %[rs], s44 sc1\n.Ldd_end%=:\ns_waitcnt vmcnt(0) lgkmcnt(0)\n" \
""

#define V10(p, x) p #x "0", p #x "1", p #x "2", p #x "3", p #x "4", p #x "5", p #x "6", p #x "7", p #x "8", p #x "9"
#define V256(p) V10(p, ), V10(p, 1), V10(p, 2), V10(p, 3), V10(p, 4), V10(p, 5), V10(p, 6), V10(p, 7), V10(p, 8), V10(p, 9), V10(p, 10), V10(p, 11), V10(p, 12), V10(p, 13), V10(p, 14), V10(p, 15), V10(p, 16), \
    V10(p, 17), V10(p, 18), V10(p, 19), V10(p, 20), V10(p, 21), V10(p, 22), V10(p, 23), V10(p, 24), p "250", p "251", p "252", p "253", p "254", p "255"
#define CLOB "memory", "vcc", "scc", V256("v"), V256("a"), V10("s", 4), V10("s", 5), V10("s", 6), V10("s", 7), V10("s", 8)
template <int N> DEVI void table(lds_char* p, const unsigned (&t)[N]) {
#pragma unroll
    for (int i = 0; i < N; ++i) *(l1*)(p + i * 256) = t[i];
    WL0;
}
DEVI unsigned prof_at(int cu, int w) { return O_PROF + (262144 + cu * 2560 + w * 8) * 4; }
DEVI unsigned sp_idx(int lane) { return (lane & 2) ? 0xEEEEu : 0x4444u; }
DEVI int sp_row(int r, int hf) { return 4 * (r >> 1) + 2 * hf + (r & 1); }

DEVI void qk_role(const Args& a, const int w, lds_char* smem, int l, int h) {
    const int lane = lane_id(), g = lane >> 4, n = lane & 15, b = n & 3, p = n >> 2;
    const unsigned l0 = (unsigned)(size_t)smem, tb = l0 + w * 12288, lw = l0 + 55296 + w * 24576, ls = l * SD, IMG = l0 + 32768, MIM = l0 + 49152;
    const unsigned xw = (w >> 1) * 16384 + (w & 1) * 4096, oc = (h >> 3) * 16384 + ls, tr = (8 * g + p) * 128 + b * 16;
    const unsigned cm[4] = {4 * g > n ? 0xF149F2CAu : 0u, 4 * g + 1 > n ? 0xF149F2CAu : 0u, 4 * g + 2 > n ? 0xF149F2CAu : 0u, 4 * g + 3 > n ? 0xF149F2CAu : 0u};
    const unsigned t[19] = {
        l0 + lane * 16, lw + lane * 16, l0 + (g >> 1) * 1024 + (g & 1) * 512 + n * 16, O_E1 + ls + xw + lane * 16, sp_idx(lane),
        IMG + (w >> 1) * 8192 + ((w & 1) * 8 + 2 * g) * 512 + n * 16,
        MIM + (w * 8 + 2 * p) * 144 + g * 32 + b * 4,
        IMG + g * 512 + (w ? n * 16 : 8192 + (16 * (b >> 1) + 8 * (p >> 1) + 2 * (p & 1) + (b & 1)) * 16),
        MIM + g * 144 + (n & 7) * 16, MIM + 2304 + g * 144 + (2 * p + (b & 1)) * 16,
        w ? O_PT + (h >> 3) * 32768 + ls + lane * 16 + (w == 1 ? 16384 + ((h & 7) >> 1) * 1024 + (h & 1) * 8 : (h & 7) * 1024)
          : O_PM + oc + ((h >> 1) & 3) * 1024 + ((2 * (h & 1) + (g >> 1)) * 16 + sp_row(n & 7, g & 1)) * 16 + (n >> 3) * 8,
        w ? O_PT + (h >> 3) * 32768 + ls + 20480 + ((h >> 2) & 1) * 2048 + (w - 1) * 1024 + (h & 3) * 256 + n * 16 + g * 4
          : n >> 3 ? OOB : O_PM + oc + 4096 + ((h & 7) >> 1) * 256 + sp_row(n & 7, g & 1) * 16 + (h & 1) * 8 + (g >> 1) * 4,
        w == 3 ? (l ? F_X + SD + p * 128 + b * 4 : F_X0 + b * 4) : cm[0], cm[1], cm[2], cm[3],
        (lane >> 3) * 8192u + (lane & 7) * 16, tb + tr, lw + tr};
    table(smem + w * 12288 + lane * 4, t);
    asm volatile(QK_ASM : : [ws] "s"(a.ws), [sz] "n"(WS_RS), [rw] "s"(mkrs(w < 2 ? a.Wq : a.Wk, 8u << 20)), [wo] "s"(h * 256 + (w & 1) * 128), [tb] "s"(tb), [lw] "s"(lw), [l0] "s"(l0), [w] "s"(w),
        [xn] "s"(O_XN + ls), [xw] "s"(xw), [fs] "s"(l ? 0 : 128), [sg] "s"(F_Y0 + 4096 + ls + h * 128 + 16), [pr] "s"(prof_at(l * 80 + h, w)) : CLOB, "s36", "s37", "s38", "s39");
    __builtin_amdgcn_endpgm();
}
DEVI void vu_role(const Args& a, const int w, lds_char* smem, int l, int h) {
    const int lane = lane_id(), g = lane >> 4, n = lane & 15, b = n & 3, p = n >> 2;
    const unsigned l0 = (unsigned)(size_t)smem, tb = l0 + w * 12288, lw = l0 + 61440 + w * 24576, ls = l * SD;
    const unsigned xw = (w >> 1) * 16384 + (w & 1) * 4096, tv = (8 * g + p) * 128 + b * 16 + (w & 1) * 64, to = (4 * g + (p & 1)) * 128 + (p >> 1) * 32 + b * 8;
    const unsigned t[18] = {
        l0 + lane * 16, lw + lane * 16, l0 + (g >> 1) * 1024 + (g & 1) * 512 + n * 16, O_E1 + ls + xw + lane * 16, sp_idx(lane),
        l0 + 57344 + (2 * w + (n >> 3)) * 256 + g * 32 + (n & 7) * 2,
        O_UM + ls + (4 * w + (g & 1)) * 65536 + (h >> 3) * 16384 + (g >> 1) * 2048 + (h & 7) * 256 + n * 16,
        sd(O_Y + h * 131072 + ls) + w * 32768 + (g >> 1) * 1024 + (g & 1) * 256 + n * 16,
        l ? F_X + SD + p * 128 + b * 4 : F_X0 + b * 4, l0 + 49152 + (w >> 1) * 2048 + (2 * (w & 1) + (g >> 1)) * 256 + n * 16 + (g & 1) * 8, sp_idx(lane >> 2), l0 + g * 256 + (n & 7) * 16,
        (lane >> 3) * 8192u + (lane & 7) * 16, (8 * (lane >> 5) + ((lane >> 3) & 3)) * 2048u + (lane & 7) * 16, tb + tv - (w & 1) * 12288, lw + tv - (w & 1) * 24576, tb + to, lw + to};
    table(smem + w * 12288 + lane * 4, t);
    asm volatile(VU_ASM : : [ws] "s"(a.ws), [sz] "n"(WS_RS), [rw] "s"(mkrs(a.Wv, 8u << 20)), [ro] "s"(mkrs(a.Wo, 8u << 20)), [wo] "s"(h * 256 + (w >> 1) * 128 + (w & 1) * 262144), [w2] "s"(h * 262144 + w * 512), [tb] "s"(tb), [lw] "s"(lw), [w] "s"(w),
        [xn] "s"(O_XN + ls), [xw] "s"(xw), [fs] "s"(l ? 0 : 128), [xt] "s"(w * 16384 - xw), [fy] "s"(F_Y0 + ls + h * 128 + w * 4), [sg] "s"(w == 1 ? F_Y0 + ls + h * 128 + 16 : OOB), [pr] "s"(prof_at(l * 80 + 32 + h, w) - 128) : CLOB, "s36", "s37", "s38", "s39");
    __builtin_amdgcn_endpgm();
}
DEVI void rd_role(const Args& a, const int w, lds_char* smem, int l, int c) {
    const int lane = lane_id(), g = lane >> 4, n = lane & 15, r = 2 * (n >> 2) + (n & 1), hf = (n >> 1) & 1;
    const rsrc_t rs = mkrs(a.ws, WS_RS);
    const unsigned l0 = (unsigned)(size_t)smem, tb = l0 + 65536 + w * 4096, ls = l * SD, vp = O_E1 + ls + (c >> 3) * 16384 + (c & 7) * 1024 + w * 256 + n * 16;
    {
        const v4u ff = {SENT, SENT, SENT, SENT};
#pragma unroll
        for (int i = 0; i < 8; ++i) st16(ff, rs, O_UM + ls + c * 65536 + w * 16384 + i * 1024 + lane * 16);
#pragma unroll
        for (int i = 0; i < 2; ++i) st16(ff, rs, O_PT + ls + c * 16384 + w * 2048 + i * 1024 + lane * 16);
        st16(ff, rs, c < 8 ? O_PM + ls + w * 16384 + c * 1024 + lane * 16 : OOB);
        st16(ff, rs, g ? vp + g * 32768 : OOB);
        WV0; BAR;
    }
    st4(g & 1 ? *(const unsigned*)(a.mem0 + r * 1024 + c * 64 + w * 16 + 4 * g + 2 * hf) : nosent(*(const unsigned*)(a.mem0 + r * 1024 + c * 64 + w * 16 + 4 * g + 2 * hf)), rs, g & 1 ? vp + g * 4 : OOB, 0);
    WV0;
    st4(nosent(*(const unsigned*)(a.mem0 + r * 1024 + c * 64 + w * 16 + 4 * g + 2 * hf)), rs, g & 1 ? OOB : vp + g * 4, 0);
    const int gi = lane >> 3, r8 = lane & 7;
    const unsigned t[12] = {
        (unsigned)lane * 16, sp_idx(lane), l0 + w * 4096 + g * 576 + ((n >> 3) * 16 + hf * 4 + 2 * ((n >> 2) & 1) + (n & 1)) * 4,
        l0 + (r >> 1) * 576 + w * 128 + (g >> 1) * 64 + (r & 1) * 32 + hf * 16,
        (vp ^ ((g & 1) * 65536)) + (g >> 1) * 8, g & 1 ? SENT : 0u,
        l0 + 16384 + w * 8192 + lane * 16, l0 + 16384 + ((gi >> 1) * 2 + (w >> 1)) * 1024 + ((gi & 1) * 32 + (w & 1) * 8 + r8) * 16,
        (c >> 1) * 16384u + (c & 1) * 4096 + gi * 512 + (w * 8 + r8) * 16,
        gi ? OOB : O_ST - O_XR + c * 256 + ((w & 1) * 8 + r8) * 16 + (w >> 1) * 8,
        F_Y0 + ls + (w * 8 + r8) * 128 + (c >> 2) * 4, F_Y0 + ls + (gi & 1) * 4096 + (w * 8 + r8) * 128 + 16};
    table(smem + 65536 + w * 4096 + lane * 4, t);
    asm volatile(RD_ASM : : [rs] "s"(rs), [tb] "s"(tb), [su] "s"(O_UM + ls + c * 65536 + w * 16384), [sp] "s"(O_PM + ls + w * 16384), [st] "s"(O_PT + ls + w * 32768),
        [sy] "s"(sd(O_Y + w * (1u << 20) + ls) + (c >> 1) * 16384 + (c & 1) * 4096), [at] "s"(O_XR + ls), [du] "s"((c & 2) * 7 / 2), [fa] "s"(F_A + ls + c * 128 + w * 4), [pr] "s"(prof_at(l * 80 + 64 + c, w) + 128) : CLOB);
    __builtin_amdgcn_endpgm();
}
DEVI void gg_role(const Args& a, const int w, lds_char* smem, int l, int gi) {
    const int lane = lane_id(), g = lane >> 4, n = lane & 15, b = n & 3, p = n >> 2;
    const unsigned l0 = (unsigned)(size_t)smem, tb = l0 + w * 12288, lw = l0 + 65536 + w * 24576, ls = l * SD, tv = (8 * g + p) * 128 + b * 16 + (w & 1) * 64, fh = (g >> 1) * 1024 + (g & 1) * 512 + n * 16;
    const unsigned t[9] = {l0 + lane * 16, lw + lane * 16, l0 + fh, F_A + ls + (lane >> 2) * 128 + (lane & 3) * 4, w * 2048u + lane * 16, O_ST - O_XR + g * 1024 + n * 16, (lane >> 3) * 7168u + (lane & 7) * 16, tb + tv - (w & 1) * 12288, lw + tv - (w & 1) * 24576};
    table(smem + w * 12288 + lane * 4, t);
    asm volatile(GG_ASM : : [rs] "s"(mkrs(a.ws, WS_RS)), [rw] "s"(mkrs(w & 1 ? a.Wu : a.Wg, 3584 * 2048)), [wo] "s"(gi * 256 + (w >> 1) * 128), [tb] "s"(tb), [lw] "s"(lw), [w] "s"(w),
        [xn] "s"(O_XR + ls), [xw] "s"((w >> 1) * 16384 + (w & 1) * 4096), [ho] "s"(O_H + ls + gi * 16384), [fh] "s"(F_H + ls + (gi / 7) * 1024 + (gi % 7) * 128 + w * 4), [pr] "s"(prof_at(160 + l * 28 + gi, w) + 128) : CLOB);
    __builtin_amdgcn_endpgm();
}
DEVI void dd_role(const Args& a, const int w, lds_char* smem, int l, int c) {
    const int lane = lane_id(), g = lane >> 4, b = lane & 3, p = (lane >> 2) & 3, gi = lane >> 3, r8 = lane & 7;
    const unsigned l0 = (unsigned)(size_t)smem, tb = l0 + w * 12288, lw = l0 + 65536 + w * 24576, ls = l * SD, tr = (8 * g + p) * 128 + b * 8;
    const unsigned ta = (c >> 1) * 16384u + (c & 1) * 4096 + gi * 512 + (w * 8 + r8) * 16;
    const rsrc_t rs = mkrs(a.ws, WS_RS);
    const unsigned t[11] = {(unsigned)lane * 16, lw + lane * 16, l0 + w * 8192 + lane * 16, l0 + ((gi >> 1) * 2 + (w >> 1)) * 1024 + ((gi & 1) * 32 + (w & 1) * 8 + r8) * 16,
        ta, l ? (w * 8 + r8) * 2048 + c * 128 + gi * 16 : ta, gi || l ? OOB : O_ST - O_XR + 4096 + c * 256 + (w * 8 + r8) * 8,
        F_H + ls + w * 1024 + ((lane >> 2) % 7) * 128 + b * 4, gi * 2048u + r8 * 16, tb + tr, lw + tr};
    table(smem + w * 12288 + lane * 4, t);
    asm volatile(DD_ASM : : [rs] "s"(rs), [rw] "s"(mkrs(a.Wd, 3584 * 2048)), [ro] "s"(mkrs(l ? (char*)a.out : a.ws, l ? 2048 * 2048 : WS_RS)), [wo] "s"(w * (896 * 2048) + c * 128), [tb] "s"(tb), [lw] "s"(lw), [l] "s"(l),
        [xn] "s"(O_XR + ls), [ho] "s"(O_H + ls + w * 7 * 16384), [fd] "s"(F_D + ls + c * 128 + w * 4), [pr] "s"(prof_at(216 + l * 16 + c, w) + 128) : CLOB);
    __builtin_amdgcn_endpgm();
}

#define DLS 500
#define SPL 0
#define PLAN 3
#define PFA 0
#define PFB 2
#define PFL 1
#define VUX 1
#define LNS 50
#define LN1 700
#define DLG 800
#define DLD 1300
#define DL1 1700
#define DLG1 2600
#define DLD1 3000
DEVI Args args(unsigned sh) {
    auto p = (const __attribute__((address_space(4))) Args*)__builtin_amdgcn_kernarg_segment_ptr();
    asm volatile("" : "+s"(p));
    Args a;
    a.x = p->x; a.mem0 = p->mem0; a.Wq = p->Wq; a.Wk = p->Wk; a.Wv = p->Wv; a.Wo = p->Wo; a.Wg = p->Wg; a.Wu = p->Wu; a.Wd = p->Wd; a.ws = p->ws + sh; a.out = p->out;
    return a;
}
DEVI unsigned beat_wait(unsigned x0) {
    const rsrc_t r0 = mkrs(args(0).ws, 2u << 20);
    for (unsigned spins = 0; spins < SPIN_CAP; ++spins) {
        CB;
        const v2u t = __builtin_amdgcn_raw_buffer_load_b64(r0, 0, O_HB, AUX_SC1);
        if (RFL(t[0]) != x0) return RFL(t[1]) & (2u << 20);
        __builtin_amdgcn_s_sleep(8);
    }
    __builtin_amdgcn_endpgm();
}
DEVI void leader(const Args& a, const int w, lds_char* smem, unsigned x0) {
    const int lane = lane_id();
    const rsrc_t r0 = mkrs(a.ws, 4u << 20);
    unsigned t[2] = {~0u, ~0u};
    for (int i = 0; i < 2 * NPRB; ++i) {
        const unsigned t0 = (unsigned)__builtin_amdgcn_s_memtime();
        WL0;
        if (lane == 0) st4(0u, r0, 0, O_PRB + (i & 1) * SD + w * 1024 + (i >> 1) * 128);
        WV0;
        const unsigned d = (unsigned)__builtin_amdgcn_s_memtime() - t0;
        if (i & 1) t[1] = min(t[1], d); else t[0] = min(t[0], d);
    }
    *(l2*)(smem + w * 8) = v2u{t[0], t[1]};
    WL0; BAR;
    const v4u p = *(l4*)smem, q = *(l4*)(smem + 16);
    const unsigned sh = RFL(min(min(p[0], p[2]), min(q[0], q[2])) < min(min(p[1], p[3]), min(q[1], q[3])) ? 2u << 20 : 0u);
    const unsigned o = sh + w * 1024 + lane * 16;
    const v4u ff = {SENT, SENT, SENT, SENT};
#pragma unroll
    for (int i = 0; i < 8; ++i) st16(ff, r0, o + O_E1 + i * 4096);
#pragma unroll
    for (int i = 1; i < 16; ++i) if (i != 2 && i != 11) st16(ff, r0, o + i * 4096);
    WV0; BAR;
    if (w) return;
    for (unsigned spins = 0; spins < SPIN_CAP; ++spins) {
        if (lane == 0) st8(v2u{++x0, sh}, r0, 0, O_HB);
        if (__ballot((int)ld4(r0, sh + F_D + SD + (lane >> 2) * 128 + (lane & 3) * 4, 0) < 0) == 0ull) break;
        __builtin_amdgcn_s_sleep(16);
    }
}
__global__ __launch_bounds__(256, 1) void sys_kernel(Args) {
    extern __shared__ __attribute__((aligned(16))) char smem_[];
    lds_char* smem = (lds_char*)smem_;
    const int x = blockIdx.x & 7, j = blockIdx.x >> 3, wv = RFL(threadIdx.x >> 6), l = x >> 2, xx = x & 3;
    const unsigned t0 = (unsigned)__builtin_amdgcn_s_memrealtime();
    auto until = [&](unsigned dl) AI { while ((unsigned)__builtin_amdgcn_s_memrealtime() - t0 < dl) __builtin_amdgcn_s_sleep(8); };
    unsigned x0 = 0;
    if (l || j >= 16) x0 = RFL(ld4(mkrs(args(0).ws, 2u << 20), 0, O_HB));
    if (j < 16) {
        if (l) { until(j ? LN1 + j * LNS : 0); ln0(args(0), wv, j * 4 + xx, [&]() AI { return beat_wait(x0); }); until(DL1); }
        if (j < 8) qk_role(args(0), wv, smem, l, xx * 8 + j); else vu_role(args(0), wv, smem, l, (xx ^ VUX) * 8 + j - 8);
        return;
    }
    {
        const int lane = lane_id(), q = (j - 16) * 4 + wv, m = SPL && !l ? ((xx & 1) != (SPL & 1)) * 2 + (q >> 5) : q >> 4;
        const Args a = args(0);
        const u16 *W0 = a.Wq, *W1 = a.Wk, *W2 = a.Wv, *W3 = a.Wo;
        asm volatile("" : "+s"(W0), "+s"(W1), "+s"(W2), "+s"(W3));
        const u16* W = m == 0 ? W0 : m == 1 ? W1 : m == 2 ? W2 : W3;
        const rsrc_t rw = mkrs(W, 8u << 20);
        if (l) {
#pragma unroll
            for (int i = 0; i < PFA; ++i) __builtin_amdgcn_raw_ptr_buffer_load_lds(rw, smem + LDSB - 1024 + wv * 256, 4, lane * 256, i * (1u << 20) + (xx * 16 + (q & 15)) * 16384, 0, 0);
        } else {
            const unsigned po = (0x100u - (unsigned)(size_t)W) & 0x300u, hx = SPL ? (xx & 2) + ((q >> 4) & 1) : m < 2 ? xx : xx ^ VUX;
            const unsigned vo = m == 3 ? (lane >> 1) * 1024 + (lane & 1) * 128 : (lane >> 2) * 8192 + ((lane >> 1) & 1) * 1024 + (lane & 1) * 128;
            const unsigned so = po + (m == 3 ? hx * (2u << 20) + (q & 15) * 32768 : hx * 2048 + (q & 15) * 131072);
#pragma unroll
            for (int i = 0; i < PFB; ++i) __builtin_amdgcn_raw_ptr_buffer_load_lds(rw, smem + LDSB - 1024 + wv * 256, 4, vo, so + i * (m == 3 ? 524288 : 2u << 20), 0, 0);
            const unsigned pl = (0x1F00u - (unsigned)(size_t)W) & 0x1F00u;
#pragma unroll
            for (int i = 0; i < PFL; ++i) __builtin_amdgcn_raw_ptr_buffer_load_lds(rw, smem + LDSB - 1024 + wv * 256, 4, (lane >> 1) * 8192 + (lane & 1) * 128, (pl >> 11) == hx && m < 3 ? pl + ((q & 15) + 16 * i) * 262144 : OOB, 0, 0);
        }
    }
    int ro, ix;
    if ((xx & 1) == (PLAN & 1)) { if (j < 24) { ro = 0; ix = (xx >> 1) * 8 + j - 16; } else if (j < 26) { ro = 3; ix = (xx >> 1) * 2 + j - 24; } else { ro = 1; ix = (xx >> 1) * 14 + j - 26; } }
    else if (j < 24) { ro = 1; ix = (xx >> 1) * 14 + j - 10; } else { ro = 2; ix = (xx >> 1) * 8 + j - 24; }
    if (ro == 3 && l) { if (ix == 0) leader(args(0), wv, smem, x0); return; }
    const unsigned sh = beat_wait(x0);
    if (ro == 0) { rd_role(args(sh), wv, smem, l, ix); return; }
    if (ro == 1) { const Args a = args(sh); until(l ? DLG1 : ((((unsigned)(size_t)a.Wg >> 8) + ix) & 3) == 1 || ((((unsigned)(size_t)a.Wu >> 8) + ix) & 3) == 1 ? DLG - DLS : DLG); WV0; BAR; gg_role(a, wv, smem, l, ix); return; }
    if (ro == 2) { const Args a = args(sh); until((l ? DLD1 : DLD) - (((((unsigned)(size_t)a.Wd >> 8) + (ix >> 1)) & 3) == 1 ? DLS + 200 : 0)); WV0; BAR; dd_role(a, wv, smem, l, ix); return; }
    r_role(args(sh), wv, ix);
}

extern "C" void kernel_launch(void* const* d_in, const int* in_sizes, int n_in, void* d_out, int out_size, void* d_ws, size_t ws_size, hipStream_t stream) {
    (void)in_sizes; (void)n_in; (void)out_size; (void)ws_size;
    Args a;
    a.x = (const u16*)d_in[0]; a.mem0 = (const u16*)d_in[1]; a.Wq = (const u16*)d_in[2]; a.Wk = (const u16*)d_in[3]; a.Wv = (const u16*)d_in[4]; a.Wo = (const u16*)d_in[5];
    a.Wg = (const u16*)d_in[8]; a.Wu = (const u16*)d_in[9]; a.Wd = (const u16*)d_in[10];
    a.ws = (char*)(((uintptr_t)d_ws + (2u << 20) - 1) & ~(uintptr_t)((2u << 20) - 1)); a.out = (u16*)d_out;
    (void)hipFuncSetAttribute((const void*)sys_kernel, hipFuncAttributeMaxDynamicSharedMemorySize, LDSB);
    hipLaunchKernelGGL(sys_kernel, dim3(256), dim3(256), LDSB, stream, a);
}
```

```cpp
#include <hip/hip_runtime.h>
#include <stdint.h>

typedef unsigned short u16;
typedef unsigned long long u64;
typedef __attribute__((ext_vector_type(8))) short bf16x8;
typedef __attribute__((ext_vector_type(4))) short s16x4;
typedef __attribute__((ext_vector_type(4))) float f32x4;
typedef __attribute__((ext_vector_type(16))) float f32x16;
typedef __attribute__((ext_vector_type(2))) float f32x2;
typedef __attribute__((ext_vector_type(4))) unsigned v4u;
typedef __attribute__((ext_vector_type(2))) unsigned v2u;
typedef __amdgpu_buffer_rsrc_t rsrc_t;
typedef __attribute__((address_space(3))) char lds_char;
typedef __attribute__((address_space(3))) bf16x8 l8;
typedef __attribute__((address_space(3))) v4u l4;
typedef __attribute__((address_space(3))) v2u l2;
typedef __attribute__((address_space(3))) unsigned l1;
typedef __attribute__((address_space(3))) f32x4 lf4;
typedef __attribute__((address_space(3))) float lf1;
#define DEVI __device__ __forceinline__
#define AI __attribute__((always_inline))

constexpr int NSEG = 64;
constexpr float ATT_SCALE = 0.08838834764831845f;
constexpr float LOG2E = 1.4426950408889634f;
#define NPRB 4
constexpr unsigned SD = 8192;
DEVI unsigned sd(unsigned o) { return o ^ ((o >> 8) & SD); }
constexpr unsigned F_X0 = 32768;
constexpr unsigned F_X = 4096;
constexpr unsigned F_A = 16384;
constexpr unsigned F_H = F_A + 2048;
constexpr unsigned F_D = F_H + 4096;
constexpr unsigned F_Y0 = 49152;
constexpr unsigned F_ABORT = 6400;
constexpr unsigned O_PM = 65536;
constexpr unsigned O_E1 = 131072;
constexpr unsigned O_PT = 262144;
constexpr unsigned O_UM = 524288;
constexpr unsigned CTRL_END = O_UM + (1u << 20);
constexpr unsigned O_PRB = 0x1F0000, O_HB = 0x1FFF00;
constexpr unsigned O_XN = 4u << 20;
constexpr unsigned O_XR = O_XN + (8u << 20);
constexpr unsigned O_ST = O_XR + (8u << 20);
constexpr unsigned O_H = O_ST + (8u << 20);
constexpr unsigned O_Y = O_H + (36u << 20);
constexpr unsigned WS_NEED = O_Y + (8u << 20);
constexpr unsigned O_PROF = WS_NEED;
constexpr unsigned WS_RS = WS_NEED;
static_assert(CTRL_END <= O_PRB && CTRL_END % 16384 == 0 && O_Y % (8u << 20) == 0, "workspace");
constexpr unsigned OOB = 0x80000000u, OOB2 = 0x90000000u, SENT = 0xFFFFFFFFu;

struct Args { const u16 *x, *mem0, *Wq, *Wk, *Wv, *Wo, *Wg, *Wu, *Wd; char* ws; u16* out; };

typedef __attribute__((ext_vector_type(2))) __bf16 bf16x2_t;
DEVI unsigned pack2(float lo, float hi) { f32x2 v = {lo, hi}; bf16x2_t b = __builtin_convertvector(v, bf16x2_t); return __builtin_bit_cast(unsigned, b); }
DEVI float bflo(unsigned u) { return __uint_as_float(u << 16); }
DEVI float bfhi(unsigned u) { return __uint_as_float(u & 0xffff0000u); }
DEVI float rbf(float x) { return bflo(pack2(x, x)); }
DEVI bf16x8 as8(v4u v) { return __builtin_bit_cast(bf16x8, v); }
DEVI f32x4 mfma16(bf16x8 a, bf16x8 b, f32x4 c) { return __builtin_amdgcn_mfma_f32_16x16x32_bf16(a, b, c, 0, 0, 0); }
DEVI f32x16 mfma32(bf16x8 a, bf16x8 b, f32x16 c) { return __builtin_amdgcn_mfma_f32_32x32x16_bf16(a, b, c, 0, 0, 0); }
constexpr int AUX_SC1 = 16;
DEVI v4u ld16(rsrc_t r, unsigned vo, unsigned so) { return __builtin_amdgcn_raw_buffer_load_b128(r, vo, so, AUX_SC1); }
DEVI unsigned ld4(rsrc_t r, unsigned vo, unsigned so) { return __builtin_amdgcn_raw_buffer_load_b32(r, vo, so, AUX_SC1); }
DEVI void st16(v4u v, rsrc_t r, unsigned vo) { __builtin_amdgcn_raw_buffer_store_b128(v, r, vo, 0, AUX_SC1); }
DEVI void st8(v2u v, rsrc_t r, unsigned vo, unsigned so) { __builtin_amdgcn_raw_buffer_store_b64(v, r, vo, so, AUX_SC1); }
DEVI void st4(unsigned v, rsrc_t r, unsigned vo, unsigned so) { __builtin_amdgcn_raw_buffer_store_b32(v, r, vo, so, AUX_SC1); }
DEVI v2u trd(lds_char* p) { return __builtin_bit_cast(v2u, __builtin_amdgcn_ds_read_tr16_b64_v4i16((__attribute__((address_space(3))) s16x4*)p)); }
template <int O1, int O2> DEVI void tra(v2u& lo, v2u& hi, unsigned a) { asm volatile("ds_read_b64_tr_b16 %0, %2 offset:%3\n\tds_read_b64_tr_b16 %1, %2 offset:%4" : "=&v"(lo), "=&v"(hi) : "v"(a), "n"(O1), "n"(O2) : "memory"); }
DEVI void trw(v2u& a, v2u& b, v2u& c, v2u& d) { asm volatile("s_waitcnt lgkmcnt(0)" : "+v"(a), "+v"(b), "+v"(c), "+v"(d)::"memory"); }
DEVI rsrc_t mkrs(const void* p, unsigned n) { return __builtin_amdgcn_make_buffer_rsrc((void*)p, 0, (int)n, 0x00020000); }
#define CB asm volatile("" ::: "memory")
#define SB __builtin_amdgcn_sched_barrier(0)
#define WL0 asm volatile("s_waitcnt lgkmcnt(0)" ::: "memory")
#define WV0 asm volatile("s_waitcnt vmcnt(0)" ::: "memory")
#define WV(n) asm volatile("s_waitcnt vmcnt(%0)" ::"n"(n) : "memory")
#define BAR do { __builtin_amdgcn_s_barrier(); CB; } while (0)
#define RFL(x) __builtin_amdgcn_readfirstlane(x)
DEVI int lane_id() { int x; asm volatile("v_mbcnt_lo_u32_b32 %0, -1, 0\n\tv_mbcnt_hi_u32_b32 %0, -1, %0" : "=v"(x)); return x; }
template <int I> struct IC { static constexpr int v = I; };
template <int B, int E_, class F> DEVI void sfor(F&& f) { if constexpr (B < E_) { f(IC<B>{}); sfor<B + 1, E_>(f); } }
DEVI float red_x32(float x, bool mx) { unsigned u = __float_as_uint(x); auto r = __builtin_amdgcn_permlane32_swap(u, u, false, false); float a = __uint_as_float(r[0]), b = __uint_as_float(r[1]); return mx ? fmaxf(a, b) : a + b; }
DEVI float red_x16(float x, bool mx) { unsigned u = __float_as_uint(x); auto r = __builtin_amdgcn_permlane16_swap(u, u, false, false); float a = __uint_as_float(r[0]), b = __uint_as_float(r[1]); return mx ? fmaxf(a, b) : a + b; }
DEVI unsigned vmax4(v4u d) { return max(max(d[0], d[1]), max(d[2], d[3])); }
DEVI unsigned nosent(unsigned v) { return min(v, SENT - 1u); }
constexpr unsigned SPIN_CAP = 1u << 18;
DEVI bool spin_bail(unsigned& spins, rsrc_t rs) {
    if (++spins > SPIN_CAP) { st4(0u, rs, 0, F_ABORT); return true; }
    return (spins & 255u) == 0u && RFL(ld4(rs, 0, F_ABORT)) != SENT;
}
#define TSTAMP(idx)
constexpr int LDSB = 160 * 1024;
DEVI void idle(unsigned dl) { const unsigned t0 = (unsigned)__builtin_amdgcn_s_memrealtime(); while ((unsigned)__builtin_amdgcn_s_memrealtime() - t0 < dl) __builtin_amdgcn_s_sleep(8); }

DEVI float red_rows(float s) {
    s += __uint_as_float(__builtin_amdgcn_update_dpp(0, __float_as_uint(s), 0x128, 0xf, 0xf, false));
    s = red_x16(s, false); return red_x32(s, false);
}
DEVI f32x2 up2(unsigned u) { return f32x2{bflo(u), bfhi(u)}; }
DEVI float ln_inv(float var) { return __builtin_amdgcn_rcpf(rbf(__builtin_amdgcn_sqrtf(rbf(rbf(fmaxf(var, 0.f)) + 1e-5f)))); }
template <class F> DEVI void ln0(const Args& a, const int w, int b, F&& base) {
    const int lane = lane_id(), row = w * 8 + (lane & 7);
    const rsrc_t rx = mkrs(a.x, 2048 * 2048);
    v4u u[16];
#pragma unroll
    for (int i = 0; i < 16; ++i) u[i] = __builtin_amdgcn_raw_buffer_load_b128(rx, row * 2048 + (lane >> 3) * 16 + i * 128, b * 65536, 0);
    const float k0 = __shfl(bflo(u[0][0]), lane & 7);
    f32x2 s1 = {0.f, 0.f}, s2 = {0.f, 0.f};
#pragma unroll
    for (int i = 0; i < 16; ++i)
#pragma unroll
        for (int e = 0; e < 4; ++e) { const f32x2 d = up2(u[i][e]) - k0; s1 += d; s2 += d * d; }
    const float s = red_rows(s1[0] + s1[1]) * (1.0f / 1024.0f), q = red_rows(s2[0] + s2[1]) * (1.0f / 1024.0f);
    float inv = ln_inv(q - s * s), c = -rbf(k0 + s) * inv;
    asm volatile("" : "+v"(inv), "+v"(c));
    const rsrc_t rs = mkrs(a.ws + base(), WS_RS);
    const unsigned o = sd(O_XN + b * 131072) + (lane >> 3) * 512 + row * 16;
#pragma unroll
    for (int i = 0; i < 16; ++i) {
        v4u r;
#pragma unroll
        for (int e = 0; e < 4; ++e) { const f32x2 z = up2(u[i][e]) * inv + c; r[e] = pack2(z[0], z[1]); }
        st16(r, rs, o + (i >> 1) * 16384 + (i & 1) * 4096);
    }
#pragma unroll
    for (int i = 0; i < 16; ++i) st16(u[i], rs, o + (O_XR - O_XN) + (i >> 1) * 16384 + (i & 1) * 4096);
    st16(u[0], rs, sd(O_ST + b * 131072) + w * 1024 + lane * 16); st16(u[0], rs, sd(O_ST + b * 131072) + 4096 + w * 1024 + lane * 16); st16(u[0], rs, sd(O_ST + SD + b * 131072) + w * 1024 + lane * 16);
    WV0;
    st4(b, rs, 0, F_X0 + b * 128 + w * 4);
}
#define RSLP 8
DEVI void r_role(const Args& a, const int w, int rq) {
    const int lane = lane_id(), row = (w >> 1) * 16 + (rq & 1) * 8 + (lane & 7), cq = (rq >> 1) * 2 + (w & 1);
    const rsrc_t rs = mkrs(a.ws, WS_RS);
    const unsigned fo = cq * 32768 + (lane >> 3) * 512 + row * 16, so = O_ST - O_XR + 4096 + (lane >> 3) * 512 + row * 8;
    auto io = [](int i) -> unsigned { return (i >> 1) * 16384 + (i & 1) * 4096; };
    const unsigned fd = F_D + (lane & 15) * 128 + (row >> 3) * 4;
    for (int b = 0; b < NSEG; ++b) {
        unsigned spins = 0;
        for (;;) {
            CB;
            if (__ballot((int)ld4(rs, fd, 0) < b) == 0ull) break;
            if (spin_bail(spins, rs)) return;
            __builtin_amdgcn_s_sleep(RSLP);
        }
        const unsigned sx = sd(O_XR + b * 131072);
        const v2u s0 = __builtin_amdgcn_raw_buffer_load_b64(rs, so, sx, AUX_SC1), s1 = __builtin_amdgcn_raw_buffer_load_b64(rs, so + 256, sx, AUX_SC1);
        v4u u[4];
#pragma unroll
        for (int i = 0; i < 4; ++i) u[i] = ld16(rs, fo + io(i), sx);
        const float m0 = __uint_as_float(s0[0]), m1 = __uint_as_float(s1[0]), mean = red_rows(m0 + m1) * 0.0625f;
        const float var = red_rows(__uint_as_float(s0[1]) + __uint_as_float(s1[1]) + 64.f * ((m0 - mean) * (m0 - mean) + (m1 - mean) * (m1 - mean))) * (1.0f / 1024.0f);
        const float inv = ln_inv(var), c = -rbf(mean) * inv;
#pragma unroll
        for (int i = 0; i < 4; ++i) {
            v4u o;
#pragma unroll
            for (int e = 0; e < 4; ++e) { const f32x2 z = up2(u[i][e]) * inv + c; o[e] = pack2(z[0], z[1]); }
            st16(o, rs, fo + io(i) + ((sx ^ SD) - (O_XR - O_XN)));
        }
#pragma unroll
        for (int i = 0; i < 4; ++i) st16(u[i], rs, fo + io(i) + (sx ^ SD));
        WV0;
        st4(b, rs, 0, F_X + SD + rq * 128 + w * 4);
        if (rq == 0 && w == 0) TSTAMP(200000 + 64 + b);
    }
}
#define QK_ASM \
"v_mbcnt_lo_u32_b32 v255, -1, 0\nv_mbcnt_hi_u32_b32 v255, -1, v255\nv_lshlrev_b32 v255, 2, v255\nv_add_u32 v255, %[tb], v255\nds_read_b32 v0, v255 offset:0\nds_read_b32 v1, v255 offset:256\nds_read_b32 v2, v255 offset:512\nds_read_b32 v3, v255 offset:768\n" \
"ds_read_b32 v4, v255 offset:1024\nds_read_b32 v5, v255 offset:1280\nds_read_b32 v6, v255 offset:1536\nds_read_b32 v7, v255 offset:1792\nds_read_b32 v8, v255 offset:2048\nds_read_b32 v9, v255 offset:2304\nds_read_b32 v10, v255 offset:2560\nds_read_b32 v11, v255 offset:2816\n" \
"ds_read_b32 v12, v255 offset:3072\nds_read_b32 v13, v255 offset:3328\nds_read_b32 v14, v255 offset:3584\nds_read_b32 v15, v255 offset:3840\nds_read_b32 v92, v255 offset:4096\nds_read_b32 v93, v255 offset:4352\nds_read_b32 v94, v255 offset:4608\ns_waitcnt lgkmcnt(0)\n" \
"s_mov_b64 s[36:37], %[ws]\ns_mov_b32 s38, %[sz]\ns_mov_b32 s39, 0x20000\ns_mov_b32 s45, 2096896\nbuffer_load_dword v95, off, s[36:39], s45 sc1\ns_add_u32 m0, %[lw], 16384\ns_add_u32 s44, %[wo], 0\nbuffer_load_dwordx4 v92, %[rw], s44 offen nt lds\n" \
"s_add_u32 m0, %[lw], 17408\ns_add_u32 s44, %[wo], 65536\nbuffer_load_dwordx4 v92, %[rw], s44 offen nt lds\ns_add_u32 m0, %[lw], 18432\ns_add_u32 s44, %[wo], 131072\nbuffer_load_dwordx4 v92, %[rw], s44 offen nt lds\ns_add_u32 m0, %[lw], 19456\ns_add_u32 s44, %[wo], 196608\n" \
"buffer_load_dwordx4 v92, %[rw], s44 offen nt lds\ns_add_u32 m0, %[lw], 20480\ns_add_u32 s44, %[wo], 262144\nbuffer_load_dwordx4 v92, %[rw], s44 offen nt lds\ns_add_u32 m0, %[lw], 21504\ns_add_u32 s44, %[wo], 327680\nbuffer_load_dwordx4 v92, %[rw], s44 offen nt lds\ns_add_u32 m0, %[lw], 22528\n" \
"s_add_u32 s44, %[wo], 393216\nbuffer_load_dwordx4 v92, %[rw], s44 offen nt lds\ns_add_u32 m0, %[lw], 23552\ns_add_u32 s44, %[wo], 458752\nbuffer_load_dwordx4 v92, %[rw], s44 offen nt lds\ns_add_u32 m0, %[tb], 0\ns_add_u32 s44, %[wo], 524288\nbuffer_load_dwordx4 v92, %[rw], s44 offen nt lds\n" \
"s_add_u32 m0, %[tb], 1024\ns_add_u32 s44, %[wo], 589824\nbuffer_load_dwordx4 v92, %[rw], s44 offen nt lds\ns_add_u32 m0, %[tb], 2048\ns_add_u32 s44, %[wo], 655360\nbuffer_load_dwordx4 v92, %[rw], s44 offen nt lds\ns_add_u32 m0, %[tb], 3072\ns_add_u32 s44, %[wo], 720896\n" \
"buffer_load_dwordx4 v92, %[rw], s44 offen nt lds\ns_add_u32 m0, %[tb], 4096\ns_add_u32 s44, %[wo], 786432\nbuffer_load_dwordx4 v92, %[rw], s44 offen nt lds\ns_add_u32 m0, %[tb], 5120\ns_add_u32 s44, %[wo], 851968\nbuffer_load_dwordx4 v92, %[rw], s44 offen nt lds\ns_add_u32 m0, %[tb], 6144\n" \
"s_add_u32 s44, %[wo], 917504\nbuffer_load_dwordx4 v92, %[rw], s44 offen nt lds\ns_add_u32 m0, %[tb], 7168\ns_add_u32 s44, %[wo], 983040\nbuffer_load_dwordx4 v92, %[rw], s44 offen nt lds\ns_add_u32 m0, %[tb], 8192\ns_add_u32 s44, %[wo], 1048576\nbuffer_load_dwordx4 v92, %[rw], s44 offen nt lds\n" \
"s_add_u32 m0, %[tb], 9216\ns_add_u32 s44, %[wo], 1114112\nbuffer_load_dwordx4 v92, %[rw], s44 offen nt lds\ns_add_u32 m0, %[tb], 10240\ns_add_u32 s44, %[wo], 1179648\nbuffer_load_dwordx4 v92, %[rw], s44 offen nt lds\ns_add_u32 m0, %[tb], 11264\ns_add_u32 s44, %[wo], 1245184\n" \
"buffer_load_dwordx4 v92, %[rw], s44 offen nt lds\ns_add_u32 m0, %[lw], 0\ns_add_u32 s44, %[wo], 1310720\nbuffer_load_dwordx4 v92, %[rw], s44 offen nt lds\ns_add_u32 m0, %[lw], 1024\ns_add_u32 s44, %[wo], 1376256\nbuffer_load_dwordx4 v92, %[rw], s44 offen nt lds\ns_add_u32 m0, %[lw], 2048\n" \
"s_add_u32 s44, %[wo], 1441792\nbuffer_load_dwordx4 v92, %[rw], s44 offen nt lds\ns_add_u32 m0, %[lw], 3072\ns_add_u32 s44, %[wo], 1507328\nbuffer_load_dwordx4 v92, %[rw], s44 offen nt lds\ns_add_u32 m0, %[lw], 4096\ns_add_u32 s44, %[wo], 1572864\nbuffer_load_dwordx4 v92, %[rw], s44 offen nt lds\n" \
"s_add_u32 m0, %[lw], 5120\ns_add_u32 s44, %[wo], 1638400\nbuffer_load_dwordx4 v92, %[rw], s44 offen nt lds\ns_add_u32 m0, %[lw], 6144\ns_add_u32 s44, %[wo], 1703936\nbuffer_load_dwordx4 v92, %[rw], s44 offen nt lds\ns_add_u32 m0, %[lw], 7168\ns_add_u32 s44, %[wo], 1769472\n" \
"buffer_load_dwordx4 v92, %[rw], s44 offen nt lds\ns_add_u32 m0, %[lw], 8192\ns_add_u32 s44, %[wo], 1835008\nbuffer_load_dwordx4 v92, %[rw], s44 offen nt lds\ns_add_u32 m0, %[lw], 9216\ns_add_u32 s44, %[wo], 1900544\nbuffer_load_dwordx4 v92, %[rw], s44 offen nt lds\ns_add_u32 m0, %[lw], 10240\n" \
"s_add_u32 s44, %[wo], 1966080\nbuffer_load_dwordx4 v92, %[rw], s44 offen nt lds\ns_add_u32 m0, %[lw], 11264\ns_add_u32 s44, %[wo], 2031616\nbuffer_load_dwordx4 v92, %[rw], s44 offen nt lds\ns_add_u32 m0, %[lw], 12288\ns_add_u32 s44, %[wo], 2097152\nbuffer_load_dwordx4 v92, %[rw], s44 offen nt lds\n" \
"s_add_u32 m0, %[lw], 13312\ns_add_u32 s44, %[wo], 2162688\nbuffer_load_dwordx4 v92, %[rw], s44 offen nt lds\ns_add_u32 m0, %[lw], 14336\ns_add_u32 s44, %[wo], 2228224\nbuffer_load_dwordx4 v92, %[rw], s44 offen nt lds\ns_add_u32 m0, %[lw], 15360\ns_add_u32 s44, %[wo], 2293760\n" \
"buffer_load_dwordx4 v92, %[rw], s44 offen nt lds\ns_waitcnt vmcnt(32)\nds_read_b64_tr_b16 a[0:1], v94 offset:16384\nds_read_b64_tr_b16 a[2:3], v94 offset:16896\nds_read_b64_tr_b16 a[8:9], v94 offset:16392\nds_read_b64_tr_b16 a[10:11], v94 offset:16904\nds_read_b64_tr_b16 a[16:17], v94 offset:16448\nds_read_b64_tr_b16 a[18:19], v94 offset:16960\n" \
"ds_read_b64_tr_b16 a[24:25], v94 offset:16456\nds_read_b64_tr_b16 a[26:27], v94 offset:16968\ns_waitcnt lgkmcnt(0)\ns_add_u32 m0, %[lw], 16384\ns_add_u32 s44, %[wo], 2359296\nbuffer_load_dwordx4 v92, %[rw], s44 offen nt lds\ns_add_u32 m0, %[lw], 17408\ns_add_u32 s44, %[wo], 2424832\n" \
"buffer_load_dwordx4 v92, %[rw], s44 offen nt lds\ns_add_u32 m0, %[lw], 18432\ns_add_u32 s44, %[wo], 2490368\nbuffer_load_dwordx4 v92, %[rw], s44 offen nt lds\ns_add_u32 m0, %[lw], 19456\ns_add_u32 s44, %[wo], 2555904\nbuffer_load_dwordx4 v92, %[rw], s44 offen nt lds\ns_waitcnt vmcnt(32)\n" \
"ds_read_b64_tr_b16 a[4:5], v94 offset:20480\nds_read_b64_tr_b16 a[6:7], v94 offset:20992\nds_read_b64_tr_b16 a[12:13], v94 offset:20488\nds_read_b64_tr_b16 a[14:15], v94 offset:21000\nds_read_b64_tr_b16 a[20:21], v94 offset:20544\nds_read_b64_tr_b16 a[22:23], v94 offset:21056\nds_read_b64_tr_b16 a[28:29], v94 offset:20552\nds_read_b64_tr_b16 a[30:31], v94 offset:21064\n" \
"s_waitcnt lgkmcnt(0)\ns_add_u32 m0, %[lw], 20480\ns_add_u32 s44, %[wo], 2621440\nbuffer_load_dwordx4 v92, %[rw], s44 offen nt lds\ns_add_u32 m0, %[lw], 21504\ns_add_u32 s44, %[wo], 2686976\nbuffer_load_dwordx4 v92, %[rw], s44 offen nt lds\ns_add_u32 m0, %[lw], 22528\n" \
"s_add_u32 s44, %[wo], 2752512\nbuffer_load_dwordx4 v92, %[rw], s44 offen nt lds\ns_add_u32 m0, %[lw], 23552\ns_add_u32 s44, %[wo], 2818048\nbuffer_load_dwordx4 v92, %[rw], s44 offen nt lds\ns_waitcnt vmcnt(32)\nds_read_b64_tr_b16 a[32:33], v93 offset:0\nds_read_b64_tr_b16 a[34:35], v93 offset:512\n" \
"ds_read_b64_tr_b16 v[88:89], v93 offset:8\nds_read_b64_tr_b16 v[90:91], v93 offset:520\nds_read_b64_tr_b16 a[40:41], v93 offset:64\nds_read_b64_tr_b16 a[42:43], v93 offset:576\nds_read_b64_tr_b16 a[48:49], v93 offset:72\nds_read_b64_tr_b16 a[50:51], v93 offset:584\ns_waitcnt lgkmcnt(0)\ns_add_u32 m0, %[tb], 0\n" \
"s_add_u32 s44, %[wo], 2883584\nbuffer_load_dwordx4 v92, %[rw], s44 offen nt lds\ns_add_u32 m0, %[tb], 1024\ns_add_u32 s44, %[wo], 2949120\nbuffer_load_dwordx4 v92, %[rw], s44 offen nt lds\ns_add_u32 m0, %[tb], 2048\ns_add_u32 s44, %[wo], 3014656\nbuffer_load_dwordx4 v92, %[rw], s44 offen nt lds\n" \
"s_add_u32 m0, %[tb], 3072\ns_add_u32 s44, %[wo], 3080192\nbuffer_load_dwordx4 v92, %[rw], s44 offen nt lds\ns_waitcnt vmcnt(32)\nds_read_b64_tr_b16 a[36:37], v93 offset:4096\nds_read_b64_tr_b16 a[38:39], v93 offset:4608\nds_read_b64_tr_b16 v[84:85], v93 offset:4104\nds_read_b64_tr_b16 v[86:87], v93 offset:4616\n" \
"ds_read_b64_tr_b16 a[44:45], v93 offset:4160\nds_read_b64_tr_b16 a[46:47], v93 offset:4672\nds_read_b64_tr_b16 a[52:53], v93 offset:4168\nds_read_b64_tr_b16 a[54:55], v93 offset:4680\ns_waitcnt lgkmcnt(0)\ns_add_u32 m0, %[tb], 4096\ns_add_u32 s44, %[wo], 3145728\nbuffer_load_dwordx4 v92, %[rw], s44 offen nt lds\n" \
"s_add_u32 m0, %[tb], 5120\ns_add_u32 s44, %[wo], 3211264\nbuffer_load_dwordx4 v92, %[rw], s44 offen nt lds\ns_add_u32 m0, %[tb], 6144\ns_add_u32 s44, %[wo], 3276800\nbuffer_load_dwordx4 v92, %[rw], s44 offen nt lds\ns_add_u32 m0, %[tb], 7168\ns_add_u32 s44, %[wo], 3342336\n" \
"buffer_load_dwordx4 v92, %[rw], s44 offen nt lds\ns_waitcnt vmcnt(32)\nds_read_b64_tr_b16 a[56:57], v93 offset:8192\nds_read_b64_tr_b16 a[58:59], v93 offset:8704\nds_read_b64_tr_b16 a[64:65], v93 offset:8200\nds_read_b64_tr_b16 a[66:67], v93 offset:8712\nds_read_b64_tr_b16 v[80:81], v93 offset:8256\nds_read_b64_tr_b16 v[82:83], v93 offset:8768\n" \
"ds_read_b64_tr_b16 a[72:73], v93 offset:8264\nds_read_b64_tr_b16 a[74:75], v93 offset:8776\ns_waitcnt lgkmcnt(0)\ns_add_u32 m0, %[tb], 8192\ns_add_u32 s44, %[wo], 3407872\nbuffer_load_dwordx4 v92, %[rw], s44 offen nt lds\ns_add_u32 m0, %[tb], 9216\ns_add_u32 s44, %[wo], 3473408\n" \
"buffer_load_dwordx4 v92, %[rw], s44 offen nt lds\ns_add_u32 m0, %[tb], 10240\ns_add_u32 s44, %[wo], 3538944\nbuffer_load_dwordx4 v92, %[rw], s44 offen nt lds\ns_add_u32 m0, %[tb], 11264\ns_add_u32 s44, %[wo], 3604480\nbuffer_load_dwordx4 v92, %[rw], s44 offen nt lds\ns_waitcnt vmcnt(32)\n" \
"ds_read_b64_tr_b16 a[60:61], v94 offset:0\nds_read_b64_tr_b16 a[62:63], v94 offset:512\nds_read_b64_tr_b16 a[68:69], v94 offset:8\nds_read_b64_tr_b16 a[70:71], v94 offset:520\nds_read_b64_tr_b16 v[76:77], v94 offset:64\nds_read_b64_tr_b16 v[78:79], v94 offset:576\nds_read_b64_tr_b16 a[76:77], v94 offset:72\nds_read_b64_tr_b16 a[78:79], v94 offset:584\n" \
"s_waitcnt lgkmcnt(0)\ns_add_u32 m0, %[lw], 0\ns_add_u32 s44, %[wo], 3670016\nbuffer_load_dwordx4 v92, %[rw], s44 offen nt lds\ns_add_u32 m0, %[lw], 1024\ns_add_u32 s44, %[wo], 3735552\nbuffer_load_dwordx4 v92, %[rw], s44 offen nt lds\ns_add_u32 m0, %[lw], 2048\n" \
"s_add_u32 s44, %[wo], 3801088\nbuffer_load_dwordx4 v92, %[rw], s44 offen nt lds\ns_add_u32 m0, %[lw], 3072\ns_add_u32 s44, %[wo], 3866624\nbuffer_load_dwordx4 v92, %[rw], s44 offen nt lds\ns_waitcnt vmcnt(32)\nds_read_b64_tr_b16 a[80:81], v94 offset:4096\nds_read_b64_tr_b16 a[82:83], v94 offset:4608\n" \
"ds_read_b64_tr_b16 a[88:89], v94 offset:4104\nds_read_b64_tr_b16 a[90:91], v94 offset:4616\nds_read_b64_tr_b16 a[96:97], v94 offset:4160\nds_read_b64_tr_b16 a[98:99], v94 offset:4672\nds_read_b64_tr_b16 v[72:73], v94 offset:4168\nds_read_b64_tr_b16 v[74:75], v94 offset:4680\ns_waitcnt lgkmcnt(0)\ns_add_u32 m0, %[lw], 4096\n" \
"s_add_u32 s44, %[wo], 3932160\nbuffer_load_dwordx4 v92, %[rw], s44 offen nt lds\ns_add_u32 m0, %[lw], 5120\ns_add_u32 s44, %[wo], 3997696\nbuffer_load_dwordx4 v92, %[rw], s44 offen nt lds\ns_add_u32 m0, %[lw], 6144\ns_add_u32 s44, %[wo], 4063232\nbuffer_load_dwordx4 v92, %[rw], s44 offen nt lds\n" \
"s_add_u32 m0, %[lw], 7168\ns_add_u32 s44, %[wo], 4128768\nbuffer_load_dwordx4 v92, %[rw], s44 offen nt lds\ns_waitcnt vmcnt(32)\nds_read_b64_tr_b16 a[84:85], v94 offset:8192\nds_read_b64_tr_b16 a[86:87], v94 offset:8704\nds_read_b64_tr_b16 a[92:93], v94 offset:8200\nds_read_b64_tr_b16 a[94:95], v94 offset:8712\n" \
"ds_read_b64_tr_b16 a[100:101], v94 offset:8256\nds_read_b64_tr_b16 a[102:103], v94 offset:8768\nds_read_b64_tr_b16 v[68:69], v94 offset:8264\nds_read_b64_tr_b16 v[70:71], v94 offset:8776\ns_waitcnt lgkmcnt(0)\ns_add_u32 m0, %[lw], 8192\ns_add_u32 s44, %[wo], 4194304\nbuffer_load_dwordx4 v92, %[rw], s44 offen nt lds\n" \
"s_add_u32 m0, %[lw], 9216\ns_add_u32 s44, %[wo], 4259840\nbuffer_load_dwordx4 v92, %[rw], s44 offen nt lds\ns_add_u32 m0, %[lw], 10240\ns_add_u32 s44, %[wo], 4325376\nbuffer_load_dwordx4 v92, %[rw], s44 offen nt lds\ns_add_u32 m0, %[lw], 11264\ns_add_u32 s44, %[wo], 4390912\n" \
"buffer_load_dwordx4 v92, %[rw], s44 offen nt lds\ns_waitcnt vmcnt(32)\nds_read_b64_tr_b16 a[104:105], v94 offset:12288\nds_read_b64_tr_b16 a[106:107], v94 offset:12800\nds_read_b64_tr_b16 a[112:113], v94 offset:12296\nds_read_b64_tr_b16 a[114:115], v94 offset:12808\nds_read_b64_tr_b16 a[120:121], v94 offset:12352\nds_read_b64_tr_b16 a[122:123], v94 offset:12864\n" \
"ds_read_b64_tr_b16 a[128:129], v94 offset:12360\nds_read_b64_tr_b16 a[130:131], v94 offset:12872\ns_waitcnt lgkmcnt(0)\ns_add_u32 m0, %[lw], 12288\ns_add_u32 s44, %[wo], 4456448\nbuffer_load_dwordx4 v92, %[rw], s44 offen nt lds\ns_add_u32 m0, %[lw], 13312\ns_add_u32 s44, %[wo], 4521984\n" \
"buffer_load_dwordx4 v92, %[rw], s44 offen nt lds\ns_add_u32 m0, %[lw], 14336\ns_add_u32 s44, %[wo], 4587520\nbuffer_load_dwordx4 v92, %[rw], s44 offen nt lds\ns_add_u32 m0, %[lw], 15360\ns_add_u32 s44, %[wo], 4653056\nbuffer_load_dwordx4 v92, %[rw], s44 offen nt lds\ns_waitcnt vmcnt(32)\n" \
"ds_read_b64_tr_b16 a[108:109], v94 offset:16384\nds_read_b64_tr_b16 a[110:111], v94 offset:16896\nds_read_b64_tr_b16 a[116:117], v94 offset:16392\nds_read_b64_tr_b16 a[118:119], v94 offset:16904\nds_read_b64_tr_b16 a[124:125], v94 offset:16448\nds_read_b64_tr_b16 a[126:127], v94 offset:16960\nds_read_b64_tr_b16 a[132:133], v94 offset:16456\nds_read_b64_tr_b16 a[134:135], v94 offset:16968\n" \
"s_waitcnt lgkmcnt(0)\ns_add_u32 m0, %[lw], 16384\ns_add_u32 s44, %[wo], 4718592\nbuffer_load_dwordx4 v92, %[rw], s44 offen nt lds\ns_add_u32 m0, %[lw], 17408\ns_add_u32 s44, %[wo], 4784128\nbuffer_load_dwordx4 v92, %[rw], s44 offen nt lds\ns_add_u32 m0, %[lw], 18432\n" \
"s_add_u32 s44, %[wo], 4849664\nbuffer_load_dwordx4 v92, %[rw], s44 offen nt lds\ns_add_u32 m0, %[lw], 19456\ns_add_u32 s44, %[wo], 4915200\nbuffer_load_dwordx4 v92, %[rw], s44 offen nt lds\ns_waitcnt vmcnt(32)\nds_read_b64_tr_b16 a[136:137], v94 offset:20480\nds_read_b64_tr_b16 a[138:139], v94 offset:20992\n" \
"ds_read_b64_tr_b16 v[64:65], v94 offset:20488\nds_read_b64_tr_b16 v[66:67], v94 offset:21000\nds_read_b64_tr_b16 a[144:145], v94 offset:20544\nds_read_b64_tr_b16 a[146:147], v94 offset:21056\nds_read_b64_tr_b16 a[152:153], v94 offset:20552\nds_read_b64_tr_b16 a[154:155], v94 offset:21064\ns_waitcnt lgkmcnt(0)\ns_add_u32 m0, %[lw], 20480\n" \
"s_add_u32 s44, %[wo], 4980736\nbuffer_load_dwordx4 v92, %[rw], s44 offen nt lds\ns_add_u32 m0, %[lw], 21504\ns_add_u32 s44, %[wo], 5046272\nbuffer_load_dwordx4 v92, %[rw], s44 offen nt lds\ns_add_u32 m0, %[lw], 22528\ns_add_u32 s44, %[wo], 5111808\nbuffer_load_dwordx4 v92, %[rw], s44 offen nt lds\n" \
"s_add_u32 m0, %[lw], 23552\ns_add_u32 s44, %[wo], 5177344\nbuffer_load_dwordx4 v92, %[rw], s44 offen nt lds\ns_waitcnt vmcnt(32)\nds_read_b64_tr_b16 a[140:141], v93 offset:0\nds_read_b64_tr_b16 a[142:143], v93 offset:512\nds_read_b64_tr_b16 v[60:61], v93 offset:8\nds_read_b64_tr_b16 v[62:63], v93 offset:520\n" \
"ds_read_b64_tr_b16 a[148:149], v93 offset:64\nds_read_b64_tr_b16 a[150:151], v93 offset:576\nds_read_b64_tr_b16 a[156:157], v93 offset:72\nds_read_b64_tr_b16 a[158:159], v93 offset:584\ns_waitcnt lgkmcnt(0)\ns_add_u32 m0, %[tb], 0\ns_add_u32 s44, %[wo], 5242880\nbuffer_load_dwordx4 v92, %[rw], s44 offen nt lds\n" \
"s_add_u32 m0, %[tb], 1024\ns_add_u32 s44, %[wo], 5308416\nbuffer_load_dwordx4 v92, %[rw], s44 offen nt lds\ns_add_u32 m0, %[tb], 2048\ns_add_u32 s44, %[wo], 5373952\nbuffer_load_dwordx4 v92, %[rw], s44 offen nt lds\ns_add_u32 m0, %[tb], 3072\ns_add_u32 s44, %[wo], 5439488\n" \
"buffer_load_dwordx4 v92, %[rw], s44 offen nt lds\ns_waitcnt vmcnt(32)\nds_read_b64_tr_b16 a[160:161], v93 offset:4096\nds_read_b64_tr_b16 a[162:163], v93 offset:4608\nds_read_b64_tr_b16 a[168:169], v93 offset:4104\nds_read_b64_tr_b16 a[170:171], v93 offset:4616\nds_read_b64_tr_b16 v[56:57], v93 offset:4160\nds_read_b64_tr_b16 v[58:59], v93 offset:4672\n" \
"ds_read_b64_tr_b16 a[176:177], v93 offset:4168\nds_read_b64_tr_b16 a[178:179], v93 offset:4680\ns_waitcnt lgkmcnt(0)\ns_add_u32 m0, %[tb], 4096\ns_add_u32 s44, %[wo], 5505024\nbuffer_load_dwordx4 v92, %[rw], s44 offen nt lds\ns_add_u32 m0, %[tb], 5120\ns_add_u32 s44, %[wo], 5570560\n" \
"buffer_load_dwordx4 v92, %[rw], s44 offen nt lds\ns_add_u32 m0, %[tb], 6144\ns_add_u32 s44, %[wo], 5636096\nbuffer_load_dwordx4 v92, %[rw], s44 offen nt lds\ns_add_u32 m0, %[tb], 7168\ns_add_u32 s44, %[wo], 5701632\nbuffer_load_dwordx4 v92, %[rw], s44 offen nt lds\ns_waitcnt vmcnt(32)\n" \
"ds_read_b64_tr_b16 a[164:165], v93 offset:8192\nds_read_b64_tr_b16 a[166:167], v93 offset:8704\nds_read_b64_tr_b16 a[172:173], v93 offset:8200\nds_read_b64_tr_b16 a[174:175], v93 offset:8712\nds_read_b64_tr_b16 v[52:53], v93 offset:8256\nds_read_b64_tr_b16 v[54:55], v93 offset:8768\nds_read_b64_tr_b16 a[180:181], v93 offset:8264\nds_read_b64_tr_b16 a[182:183], v93 offset:8776\n" \
"s_waitcnt lgkmcnt(0)\ns_add_u32 m0, %[tb], 8192\ns_add_u32 s44, %[wo], 5767168\nbuffer_load_dwordx4 v92, %[rw], s44 offen nt lds\ns_add_u32 m0, %[tb], 9216\ns_add_u32 s44, %[wo], 5832704\nbuffer_load_dwordx4 v92, %[rw], s44 offen nt lds\ns_add_u32 m0, %[tb], 10240\n" \
"s_add_u32 s44, %[wo], 5898240\nbuffer_load_dwordx4 v92, %[rw], s44 offen nt lds\ns_add_u32 m0, %[tb], 11264\ns_add_u32 s44, %[wo], 5963776\nbuffer_load_dwordx4 v92, %[rw], s44 offen nt lds\ns_waitcnt vmcnt(32)\nds_read_b64_tr_b16 a[184:185], v94 offset:0\nds_read_b64_tr_b16 a[186:187], v94 offset:512\n" \
"ds_read_b64_tr_b16 a[192:193], v94 offset:8\nds_read_b64_tr_b16 a[194:195], v94 offset:520\nds_read_b64_tr_b16 a[200:201], v94 offset:64\nds_read_b64_tr_b16 a[202:203], v94 offset:576\nds_read_b64_tr_b16 v[48:49], v94 offset:72\nds_read_b64_tr_b16 v[50:51], v94 offset:584\ns_waitcnt lgkmcnt(0)\ns_add_u32 m0, %[lw], 0\n" \
"s_add_u32 s44, %[wo], 6029312\nbuffer_load_dwordx4 v92, %[rw], s44 offen nt lds\ns_add_u32 m0, %[lw], 1024\ns_add_u32 s44, %[wo], 6094848\nbuffer_load_dwordx4 v92, %[rw], s44 offen nt lds\ns_add_u32 m0, %[lw], 2048\ns_add_u32 s44, %[wo], 6160384\nbuffer_load_dwordx4 v92, %[rw], s44 offen nt lds\n" \
"s_add_u32 m0, %[lw], 3072\ns_add_u32 s44, %[wo], 6225920\nbuffer_load_dwordx4 v92, %[rw], s44 offen nt lds\ns_waitcnt vmcnt(32)\nds_read_b64_tr_b16 a[188:189], v94 offset:4096\nds_read_b64_tr_b16 a[190:191], v94 offset:4608\nds_read_b64_tr_b16 a[196:197], v94 offset:4104\nds_read_b64_tr_b16 a[198:199], v94 offset:4616\n" \
"ds_read_b64_tr_b16 a[204:205], v94 offset:4160\nds_read_b64_tr_b16 a[206:207], v94 offset:4672\nds_read_b64_tr_b16 v[44:45], v94 offset:4168\nds_read_b64_tr_b16 v[46:47], v94 offset:4680\ns_waitcnt lgkmcnt(0)\ns_add_u32 m0, %[lw], 4096\ns_add_u32 s44, %[wo], 6291456\nbuffer_load_dwordx4 v92, %[rw], s44 offen nt lds\n" \
"s_add_u32 m0, %[lw], 5120\ns_add_u32 s44, %[wo], 6356992\nbuffer_load_dwordx4 v92, %[rw], s44 offen nt lds\ns_add_u32 m0, %[lw], 6144\ns_add_u32 s44, %[wo], 6422528\nbuffer_load_dwordx4 v92, %[rw], s44 offen nt lds\ns_add_u32 m0, %[lw], 7168\ns_add_u32 s44, %[wo], 6488064\n" \
"buffer_load_dwordx4 v92, %[rw], s44 offen nt lds\ns_waitcnt vmcnt(32)\nds_read_b64_tr_b16 a[208:209], v94 offset:8192\nds_read_b64_tr_b16 a[210:211], v94 offset:8704\nds_read_b64_tr_b16 a[216:217], v94 offset:8200\nds_read_b64_tr_b16 a[218:219], v94 offset:8712\nds_read_b64_tr_b16 a[224:225], v94 offset:8256\nds_read_b64_tr_b16 a[226:227], v94 offset:8768\n" \
"ds_read_b64_tr_b16 a[232:233], v94 offset:8264\nds_read_b64_tr_b16 a[234:235], v94 offset:8776\ns_waitcnt lgkmcnt(0)\ns_add_u32 m0, %[lw], 8192\ns_add_u32 s44, %[wo], 6553600\nbuffer_load_dwordx4 v92, %[rw], s44 offen nt lds\ns_add_u32 m0, %[lw], 9216\ns_add_u32 s44, %[wo], 6619136\n" \
"buffer_load_dwordx4 v92, %[rw], s44 offen nt lds\ns_add_u32 m0, %[lw], 10240\ns_add_u32 s44, %[wo], 6684672\nbuffer_load_dwordx4 v92, %[rw], s44 offen nt lds\ns_add_u32 m0, %[lw], 11264\ns_add_u32 s44, %[wo], 6750208\nbuffer_load_dwordx4 v92, %[rw], s44 offen nt lds\ns_waitcnt vmcnt(32)\n" \
"ds_read_b64_tr_b16 a[212:213], v94 offset:12288\nds_read_b64_tr_b16 a[214:215], v94 offset:12800\nds_read_b64_tr_b16 a[220:221], v94 offset:12296\nds_read_b64_tr_b16 a[222:223], v94 offset:12808\nds_read_b64_tr_b16 a[228:229], v94 offset:12352\nds_read_b64_tr_b16 a[230:231], v94 offset:12864\nds_read_b64_tr_b16 a[236:237], v94 offset:12360\nds_read_b64_tr_b16 a[238:239], v94 offset:12872\n" \
"s_waitcnt lgkmcnt(0)\ns_add_u32 m0, %[lw], 12288\ns_add_u32 s44, %[wo], 6815744\nbuffer_load_dwordx4 v92, %[rw], s44 offen nt lds\ns_add_u32 m0, %[lw], 13312\ns_add_u32 s44, %[wo], 6881280\nbuffer_load_dwordx4 v92, %[rw], s44 offen nt lds\ns_add_u32 m0, %[lw], 14336\n" \
"s_add_u32 s44, %[wo], 6946816\nbuffer_load_dwordx4 v92, %[rw], s44 offen nt lds\ns_add_u32 m0, %[lw], 15360\ns_add_u32 s44, %[wo], 7012352\nbuffer_load_dwordx4 v92, %[rw], s44 offen nt lds\ns_waitcnt vmcnt(32)\nds_read_b64_tr_b16 a[240:241], v94 offset:16384\nds_read_b64_tr_b16 a[242:243], v94 offset:16896\n" \
"ds_read_b64_tr_b16 v[40:41], v94 offset:16392\nds_read_b64_tr_b16 v[42:43], v94 offset:16904\nds_read_b64_tr_b16 a[248:249], v94 offset:16448\nds_read_b64_tr_b16 a[250:251], v94 offset:16960\nds_read_b64_tr_b16 v[96:97], v94 offset:16456\nds_read_b64_tr_b16 v[98:99], v94 offset:16968\ns_waitcnt lgkmcnt(0)\ns_add_u32 m0, %[lw], 16384\n" \
"s_add_u32 s44, %[wo], 7077888\nbuffer_load_dwordx4 v92, %[rw], s44 offen nt lds\ns_add_u32 m0, %[lw], 17408\ns_add_u32 s44, %[wo], 7143424\nbuffer_load_dwordx4 v92, %[rw], s44 offen nt lds\ns_add_u32 m0, %[lw], 18432\ns_add_u32 s44, %[wo], 7208960\nbuffer_load_dwordx4 v92, %[rw], s44 offen nt lds\n" \
"s_add_u32 m0, %[lw], 19456\ns_add_u32 s44, %[wo], 7274496\nbuffer_load_dwordx4 v92, %[rw], s44 offen nt lds\ns_waitcnt vmcnt(32)\nds_read_b64_tr_b16 a[244:245], v94 offset:20480\nds_read_b64_tr_b16 a[246:247], v94 offset:20992\nds_read_b64_tr_b16 v[36:37], v94 offset:20488\nds_read_b64_tr_b16 v[38:39], v94 offset:21000\n" \
"ds_read_b64_tr_b16 a[252:253], v94 offset:20544\nds_read_b64_tr_b16 a[254:255], v94 offset:21056\nds_read_b64_tr_b16 v[100:101], v94 offset:20552\nds_read_b64_tr_b16 v[102:103], v94 offset:21064\ns_waitcnt lgkmcnt(0)\ns_add_u32 m0, %[lw], 20480\ns_add_u32 s44, %[wo], 7340032\nbuffer_load_dwordx4 v92, %[rw], s44 offen nt lds\n" \
"s_add_u32 m0, %[lw], 21504\ns_add_u32 s44, %[wo], 7405568\nbuffer_load_dwordx4 v92, %[rw], s44 offen nt lds\ns_add_u32 m0, %[lw], 22528\ns_add_u32 s44, %[wo], 7471104\nbuffer_load_dwordx4 v92, %[rw], s44 offen nt lds\ns_add_u32 m0, %[lw], 23552\ns_add_u32 s44, %[wo], 7536640\n" \
"buffer_load_dwordx4 v92, %[rw], s44 offen nt lds\ns_waitcnt vmcnt(32)\nds_read_b64_tr_b16 v[104:105], v93 offset:0\nds_read_b64_tr_b16 v[106:107], v93 offset:512\nds_read_b64_tr_b16 v[112:113], v93 offset:8\nds_read_b64_tr_b16 v[114:115], v93 offset:520\nds_read_b64_tr_b16 v[32:33], v93 offset:64\nds_read_b64_tr_b16 v[34:35], v93 offset:576\n" \
"ds_read_b64_tr_b16 v[120:121], v93 offset:72\nds_read_b64_tr_b16 v[122:123], v93 offset:584\ns_waitcnt lgkmcnt(0)\ns_add_u32 m0, %[tb], 0\ns_add_u32 s44, %[wo], 7602176\nbuffer_load_dwordx4 v92, %[rw], s44 offen nt lds\ns_add_u32 m0, %[tb], 1024\ns_add_u32 s44, %[wo], 7667712\n" \
"buffer_load_dwordx4 v92, %[rw], s44 offen nt lds\ns_add_u32 m0, %[tb], 2048\ns_add_u32 s44, %[wo], 7733248\nbuffer_load_dwordx4 v92, %[rw], s44 offen nt lds\ns_add_u32 m0, %[tb], 3072\ns_add_u32 s44, %[wo], 7798784\nbuffer_load_dwordx4 v92, %[rw], s44 offen nt lds\ns_waitcnt vmcnt(32)\n" \
"ds_read_b64_tr_b16 v[108:109], v93 offset:4096\nds_read_b64_tr_b16 v[110:111], v93 offset:4608\nds_read_b64_tr_b16 v[116:117], v93 offset:4104\nds_read_b64_tr_b16 v[118:119], v93 offset:4616\nds_read_b64_tr_b16 v[28:29], v93 offset:4160\nds_read_b64_tr_b16 v[30:31], v93 offset:4672\nds_read_b64_tr_b16 v[124:125], v93 offset:4168\nds_read_b64_tr_b16 v[126:127], v93 offset:4680\n" \
"s_waitcnt lgkmcnt(0)\ns_add_u32 m0, %[tb], 4096\ns_add_u32 s44, %[wo], 7864320\nbuffer_load_dwordx4 v92, %[rw], s44 offen nt lds\ns_add_u32 m0, %[tb], 5120\ns_add_u32 s44, %[wo], 7929856\nbuffer_load_dwordx4 v92, %[rw], s44 offen nt lds\ns_add_u32 m0, %[tb], 6144\n" \
"s_add_u32 s44, %[wo], 7995392\nbuffer_load_dwordx4 v92, %[rw], s44 offen nt lds\ns_add_u32 m0, %[tb], 7168\ns_add_u32 s44, %[wo], 8060928\nbuffer_load_dwordx4 v92, %[rw], s44 offen nt lds\ns_waitcnt vmcnt(32)\nds_read_b64_tr_b16 v[128:129], v93 offset:8192\nds_read_b64_tr_b16 v[130:131], v93 offset:8704\n" \
"ds_read_b64_tr_b16 v[136:137], v93 offset:8200\nds_read_b64_tr_b16 v[138:139], v93 offset:8712\nds_read_b64_tr_b16 v[144:145], v93 offset:8256\nds_read_b64_tr_b16 v[146:147], v93 offset:8768\nds_read_b64_tr_b16 v[24:25], v93 offset:8264\nds_read_b64_tr_b16 v[26:27], v93 offset:8776\ns_waitcnt lgkmcnt(0)\ns_add_u32 m0, %[tb], 8192\n" \
"s_add_u32 s44, %[wo], 8126464\nbuffer_load_dwordx4 v92, %[rw], s44 offen nt lds\ns_add_u32 m0, %[tb], 9216\ns_add_u32 s44, %[wo], 8192000\nbuffer_load_dwordx4 v92, %[rw], s44 offen nt lds\ns_add_u32 m0, %[tb], 10240\ns_add_u32 s44, %[wo], 8257536\nbuffer_load_dwordx4 v92, %[rw], s44 offen nt lds\n" \
"s_add_u32 m0, %[tb], 11264\ns_add_u32 s44, %[wo], 8323072\nbuffer_load_dwordx4 v92, %[rw], s44 offen nt lds\ns_waitcnt vmcnt(32)\nds_read_b64_tr_b16 v[132:133], v94 offset:0\nds_read_b64_tr_b16 v[134:135], v94 offset:512\nds_read_b64_tr_b16 v[140:141], v94 offset:8\nds_read_b64_tr_b16 v[142:143], v94 offset:520\n" \
"ds_read_b64_tr_b16 v[148:149], v94 offset:64\nds_read_b64_tr_b16 v[150:151], v94 offset:576\nds_read_b64_tr_b16 v[20:21], v94 offset:72\nds_read_b64_tr_b16 v[22:23], v94 offset:584\ns_waitcnt lgkmcnt(0)\nds_write_b128 v1, v[88:91] offset:0\nds_write_b128 v1, v[84:87] offset:1024\nds_write_b128 v1, v[80:83] offset:2048\n" \
"ds_write_b128 v1, v[76:79] offset:3072\ns_waitcnt vmcnt(28)\nds_read_b64_tr_b16 v[152:153], v94 offset:4096\nds_read_b64_tr_b16 v[154:155], v94 offset:4608\nds_read_b64_tr_b16 v[160:161], v94 offset:4104\nds_read_b64_tr_b16 v[162:163], v94 offset:4616\nds_read_b64_tr_b16 v[168:169], v94 offset:4160\nds_read_b64_tr_b16 v[170:171], v94 offset:4672\n" \
"ds_read_b64_tr_b16 v[176:177], v94 offset:4168\nds_read_b64_tr_b16 v[178:179], v94 offset:4680\ns_waitcnt lgkmcnt(0)\nds_write_b128 v1, v[72:75] offset:4096\nds_write_b128 v1, v[68:71] offset:5120\nds_write_b128 v1, v[64:67] offset:6144\nds_write_b128 v1, v[60:63] offset:7168\ns_waitcnt vmcnt(24)\n" \
"ds_read_b64_tr_b16 v[156:157], v94 offset:8192\nds_read_b64_tr_b16 v[158:159], v94 offset:8704\nds_read_b64_tr_b16 v[164:165], v94 offset:8200\nds_read_b64_tr_b16 v[166:167], v94 offset:8712\nds_read_b64_tr_b16 v[172:173], v94 offset:8256\nds_read_b64_tr_b16 v[174:175], v94 offset:8768\nds_read_b64_tr_b16 v[180:181], v94 offset:8264\nds_read_b64_tr_b16 v[182:183], v94 offset:8776\n" \
"s_waitcnt lgkmcnt(0)\nds_write_b128 v1, v[56:59] offset:8192\nds_write_b128 v1, v[52:55] offset:9216\nds_write_b128 v1, v[48:51] offset:10240\nds_write_b128 v1, v[44:47] offset:11264\ns_waitcnt vmcnt(20)\nds_read_b64_tr_b16 v[184:185], v94 offset:12288\nds_read_b64_tr_b16 v[186:187], v94 offset:12800\n" \
"ds_read_b64_tr_b16 v[44:45], v94 offset:12296\nds_read_b64_tr_b16 v[46:47], v94 offset:12808\nds_read_b64_tr_b16 v[192:193], v94 offset:12352\nds_read_b64_tr_b16 v[194:195], v94 offset:12864\nds_read_b64_tr_b16 v[200:201], v94 offset:12360\nds_read_b64_tr_b16 v[202:203], v94 offset:12872\ns_waitcnt lgkmcnt(0)\nds_write_b128 v1, v[40:43] offset:12288\n" \
"ds_write_b128 v1, v[36:39] offset:13312\nds_write_b128 v1, v[32:35] offset:14336\nds_write_b128 v1, v[28:31] offset:15360\ns_waitcnt vmcnt(16)\nds_read_b64_tr_b16 v[188:189], v94 offset:16384\nds_read_b64_tr_b16 v[190:191], v94 offset:16896\nds_read_b64_tr_b16 v[28:29], v94 offset:16392\nds_read_b64_tr_b16 v[30:31], v94 offset:16904\n" \
"ds_read_b64_tr_b16 v[196:197], v94 offset:16448\nds_read_b64_tr_b16 v[198:199], v94 offset:16960\nds_read_b64_tr_b16 v[204:205], v94 offset:16456\nds_read_b64_tr_b16 v[206:207], v94 offset:16968\ns_waitcnt lgkmcnt(0)\nds_write_b128 v1, v[24:27] offset:16384\nds_write_b128 v1, v[20:23] offset:17408\nds_write_b128 v1, v[44:47] offset:18432\n" \
"ds_write_b128 v1, v[28:31] offset:19456\ns_waitcnt vmcnt(12)\nds_read_b64_tr_b16 v[208:209], v94 offset:20480\nds_read_b64_tr_b16 v[210:211], v94 offset:20992\nds_read_b64_tr_b16 v[216:217], v94 offset:20488\nds_read_b64_tr_b16 v[218:219], v94 offset:21000\nds_read_b64_tr_b16 v[28:29], v94 offset:20544\nds_read_b64_tr_b16 v[30:31], v94 offset:21056\n" \
"ds_read_b64_tr_b16 v[224:225], v94 offset:20552\nds_read_b64_tr_b16 v[226:227], v94 offset:21064\ns_waitcnt lgkmcnt(0)\nds_write_b128 v1, v[28:31] offset:20480\ns_waitcnt vmcnt(8)\nds_read_b64_tr_b16 v[212:213], v93 offset:0\nds_read_b64_tr_b16 v[214:215], v93 offset:512\nds_read_b64_tr_b16 v[220:221], v93 offset:8\n" \
"ds_read_b64_tr_b16 v[222:223], v93 offset:520\nds_read_b64_tr_b16 v[28:29], v93 offset:64\nds_read_b64_tr_b16 v[30:31], v93 offset:576\nds_read_b64_tr_b16 v[228:229], v93 offset:72\nds_read_b64_tr_b16 v[230:231], v93 offset:584\ns_waitcnt lgkmcnt(0)\nds_write_b128 v1, v[28:31] offset:21504\ns_waitcnt vmcnt(4)\n" \
"ds_read_b64_tr_b16 v[232:233], v93 offset:4096\nds_read_b64_tr_b16 v[234:235], v93 offset:4608\nds_read_b64_tr_b16 v[240:241], v93 offset:4104\nds_read_b64_tr_b16 v[242:243], v93 offset:4616\nds_read_b64_tr_b16 v[248:249], v93 offset:4160\nds_read_b64_tr_b16 v[250:251], v93 offset:4672\nds_read_b64_tr_b16 v[28:29], v93 offset:4168\nds_read_b64_tr_b16 v[30:31], v93 offset:4680\n" \
"s_waitcnt lgkmcnt(0)\nds_write_b128 v1, v[28:31] offset:22528\ns_waitcnt vmcnt(0)\nds_read_b64_tr_b16 v[236:237], v93 offset:8192\nds_read_b64_tr_b16 v[238:239], v93 offset:8704\nds_read_b64_tr_b16 v[244:245], v93 offset:8200\nds_read_b64_tr_b16 v[246:247], v93 offset:8712\nds_read_b64_tr_b16 v[252:253], v93 offset:8256\n" \
"ds_read_b64_tr_b16 v[254:255], v93 offset:8768\nds_read_b64_tr_b16 v[28:29], v93 offset:8264\nds_read_b64_tr_b16 v[30:31], v93 offset:8776\ns_waitcnt lgkmcnt(0)\nds_write_b128 v1, v[28:31] offset:23552\ns_waitcnt lgkmcnt(0)\ns_barrier\ns_mov_b32 s43, 0\n" \
"s_mov_b32 s86, 110\ns_mov_b32 s89, 0\n.Lqk_hb%=:\nbuffer_load_dwordx2 v[16:17], off, s[36:39], s45 sc1\ns_waitcnt vmcnt(0)\nv_cmp_ne_u32 vcc, v95, v16\ns_cbranch_vccnz .Lqk_hb1%=\ns_sleep 4\n" \
"s_add_u32 s43, s43, 1\ns_cmp_lt_u32 s43, 262144\ns_cbranch_scc1 .Lqk_hb%=\ns_branch .Lqk_abort%=\n.Lqk_hb1%=:\nv_readfirstlane_b32 s44, v17\ns_and_b32 s44, s44, 0x200000\ns_add_u32 s36, s36, s44\n" \
"s_addc_u32 s37, s37, 0\ns_mov_b32 s40, -1\ns_mov_b32 s43, 0\ns_mov_b32 s61, 0x7fff7fff\ns_mov_b32 s58, 0x3e0293ee\ns_mov_b32 s59, 0xbe0293ee\ns_lshl_b32 s57, %[w], 12\ns_mov_b32 s62, 0xff00ff00\n" \
"s_mov_b32 s63, 0xff00ff00\ns_mov_b32 s56, 0\ns_cmp_eq_u32 %[w], 3\ns_cbranch_scc1 .Lqk_fetch0%=\n.Lqk_pwait%=:\ns_barrier\nds_read_b128 v[80:83], v2 offset:0\nds_read_b128 v[84:87], v2 offset:256\n" \
"ds_read_b128 v[88:91], v2 offset:2048\n.Lqk_proj%=:\ns_waitcnt lgkmcnt(2)\nv_mfma_f32_16x16x32_bf16 v[16:19], a[0:3], v[80:83], 0\nds_read_b128 v[92:95], v2 offset:2304\nv_mfma_f32_16x16x32_bf16 v[20:23], a[8:11], v[80:83], 0\nds_read_b128 v[72:75], v1 offset:0\nv_mfma_f32_16x16x32_bf16 v[24:27], a[16:19], v[80:83], 0\n" \
"v_mfma_f32_16x16x32_bf16 v[28:31], a[24:27], v[80:83], 0\ns_waitcnt lgkmcnt(3)\nv_mfma_f32_16x16x32_bf16 v[32:35], a[0:3], v[84:87], 0\nds_read_b128 v[80:83], v2 offset:4096\nv_mfma_f32_16x16x32_bf16 v[36:39], a[8:11], v[84:87], 0\nv_mfma_f32_16x16x32_bf16 v[40:43], a[16:19], v[84:87], 0\nv_mfma_f32_16x16x32_bf16 v[44:47], a[24:27], v[84:87], 0\ns_waitcnt lgkmcnt(3)\n" \
"v_mfma_f32_16x16x32_bf16 v[16:19], a[4:7], v[88:91], v[16:19]\nds_read_b128 v[84:87], v2 offset:4352\nv_mfma_f32_16x16x32_bf16 v[20:23], a[12:15], v[88:91], v[20:23]\nds_read_b128 v[76:79], v1 offset:1024\nv_mfma_f32_16x16x32_bf16 v[24:27], a[20:23], v[88:91], v[24:27]\nv_mfma_f32_16x16x32_bf16 v[28:31], a[28:31], v[88:91], v[28:31]\ns_waitcnt lgkmcnt(4)\nv_mfma_f32_16x16x32_bf16 v[32:35], a[4:7], v[92:95], v[32:35]\n" \
"ds_read_b128 v[88:91], v2 offset:6144\nv_mfma_f32_16x16x32_bf16 v[36:39], a[12:15], v[92:95], v[36:39]\nv_mfma_f32_16x16x32_bf16 v[40:43], a[20:23], v[92:95], v[40:43]\nv_mfma_f32_16x16x32_bf16 v[44:47], a[28:31], v[92:95], v[44:47]\ns_waitcnt lgkmcnt(3)\nv_mfma_f32_16x16x32_bf16 v[16:19], a[32:35], v[80:83], v[16:19]\nds_read_b128 v[92:95], v2 offset:6400\nv_mfma_f32_16x16x32_bf16 v[20:23], v[72:75], v[80:83], v[20:23]\n" \
"ds_read_b128 v[64:67], v1 offset:2048\nv_mfma_f32_16x16x32_bf16 v[24:27], a[40:43], v[80:83], v[24:27]\nv_mfma_f32_16x16x32_bf16 v[28:31], a[48:51], v[80:83], v[28:31]\ns_waitcnt lgkmcnt(4)\nv_mfma_f32_16x16x32_bf16 v[32:35], a[32:35], v[84:87], v[32:35]\nds_read_b128 v[80:83], v2 offset:8192\nv_mfma_f32_16x16x32_bf16 v[36:39], v[72:75], v[84:87], v[36:39]\nv_mfma_f32_16x16x32_bf16 v[40:43], a[40:43], v[84:87], v[40:43]\n" \
"v_mfma_f32_16x16x32_bf16 v[44:47], a[48:51], v[84:87], v[44:47]\ns_waitcnt lgkmcnt(3)\nv_mfma_f32_16x16x32_bf16 v[16:19], a[36:39], v[88:91], v[16:19]\nds_read_b128 v[84:87], v2 offset:8448\nv_mfma_f32_16x16x32_bf16 v[20:23], v[76:79], v[88:91], v[20:23]\nds_read_b128 v[68:71], v1 offset:3072\nv_mfma_f32_16x16x32_bf16 v[24:27], a[44:47], v[88:91], v[24:27]\nv_mfma_f32_16x16x32_bf16 v[28:31], a[52:55], v[88:91], v[28:31]\n" \
"s_waitcnt lgkmcnt(4)\nv_mfma_f32_16x16x32_bf16 v[32:35], a[36:39], v[92:95], v[32:35]\nds_read_b128 v[88:91], v2 offset:10240\nv_mfma_f32_16x16x32_bf16 v[36:39], v[76:79], v[92:95], v[36:39]\nv_mfma_f32_16x16x32_bf16 v[40:43], a[44:47], v[92:95], v[40:43]\nv_mfma_f32_16x16x32_bf16 v[44:47], a[52:55], v[92:95], v[44:47]\ns_waitcnt lgkmcnt(3)\nv_mfma_f32_16x16x32_bf16 v[16:19], a[56:59], v[80:83], v[16:19]\n" \
"ds_read_b128 v[92:95], v2 offset:10496\nv_mfma_f32_16x16x32_bf16 v[20:23], a[64:67], v[80:83], v[20:23]\nds_read_b128 v[72:75], v1 offset:4096\nv_mfma_f32_16x16x32_bf16 v[24:27], v[64:67], v[80:83], v[24:27]\nv_mfma_f32_16x16x32_bf16 v[28:31], a[72:75], v[80:83], v[28:31]\ns_waitcnt lgkmcnt(4)\nv_mfma_f32_16x16x32_bf16 v[32:35], a[56:59], v[84:87], v[32:35]\nds_read_b128 v[80:83], v2 offset:12288\n" \
"v_mfma_f32_16x16x32_bf16 v[36:39], a[64:67], v[84:87], v[36:39]\nv_mfma_f32_16x16x32_bf16 v[40:43], v[64:67], v[84:87], v[40:43]\nv_mfma_f32_16x16x32_bf16 v[44:47], a[72:75], v[84:87], v[44:47]\ns_waitcnt lgkmcnt(3)\nv_mfma_f32_16x16x32_bf16 v[16:19], a[60:63], v[88:91], v[16:19]\nds_read_b128 v[84:87], v2 offset:12544\nv_mfma_f32_16x16x32_bf16 v[20:23], a[68:71], v[88:91], v[20:23]\nds_read_b128 v[76:79], v1 offset:5120\n" \
"v_mfma_f32_16x16x32_bf16 v[24:27], v[68:71], v[88:91], v[24:27]\nv_mfma_f32_16x16x32_bf16 v[28:31], a[76:79], v[88:91], v[28:31]\ns_waitcnt lgkmcnt(4)\nv_mfma_f32_16x16x32_bf16 v[32:35], a[60:63], v[92:95], v[32:35]\nds_read_b128 v[88:91], v2 offset:14336\nv_mfma_f32_16x16x32_bf16 v[36:39], a[68:71], v[92:95], v[36:39]\nv_mfma_f32_16x16x32_bf16 v[40:43], v[68:71], v[92:95], v[40:43]\nv_mfma_f32_16x16x32_bf16 v[44:47], a[76:79], v[92:95], v[44:47]\n" \
".Lqk_pj%=:\ns_waitcnt lgkmcnt(3)\nv_mfma_f32_16x16x32_bf16 v[16:19], a[80:83], v[80:83], v[16:19]\nds_read_b128 v[92:95], v2 offset:14592\nv_mfma_f32_16x16x32_bf16 v[20:23], a[88:91], v[80:83], v[20:23]\ns_add_u32 s40, s40, 1\nv_mfma_f32_16x16x32_bf16 v[24:27], a[96:99], v[80:83], v[24:27]\ns_lshl_b32 s44, s40, 17\n" \
"v_mfma_f32_16x16x32_bf16 v[28:31], v[72:75], v[80:83], v[28:31]\ns_add_u32 s44, s44, %[xn]\ns_waitcnt lgkmcnt(3)\nv_mfma_f32_16x16x32_bf16 v[32:35], a[80:83], v[84:87], v[32:35]\ns_barrier\nds_read_b128 v[80:83], v2 offset:16384\nv_mfma_f32_16x16x32_bf16 v[36:39], a[88:91], v[84:87], v[36:39]\ns_lshr_b32 s45, s44, 8\n" \
"v_mfma_f32_16x16x32_bf16 v[40:43], a[96:99], v[84:87], v[40:43]\ns_and_b32 s45, s45, 0x2000\nv_mfma_f32_16x16x32_bf16 v[44:47], v[72:75], v[84:87], v[44:47]\ns_xor_b32 s41, s44, s45\ns_waitcnt lgkmcnt(2)\nv_mfma_f32_16x16x32_bf16 v[16:19], a[84:87], v[88:91], v[16:19]\nds_read_b128 v[84:87], v2 offset:16640\nv_mfma_f32_16x16x32_bf16 v[20:23], a[92:95], v[88:91], v[20:23]\n" \
"s_add_u32 s41, s41, %[xw]\nv_mfma_f32_16x16x32_bf16 v[24:27], a[100:103], v[88:91], v[24:27]\ns_and_b32 s42, s40, 3\ns_add_u32 m0, s57, 32768\ns_add_u32 s60, s41, 65536\nbuffer_load_dwordx4 v0, s[36:39], s60 offen offset:0 sc1 lds\nv_mfma_f32_16x16x32_bf16 v[28:31], v[76:79], v[88:91], v[28:31]\ns_lshl_b32 s42, s42, 15\n" \
"s_waitcnt lgkmcnt(2)\nv_mfma_f32_16x16x32_bf16 v[32:35], a[84:87], v[92:95], v[32:35]\nds_read_b128 v[88:91], v2 offset:18432\nv_mfma_f32_16x16x32_bf16 v[36:39], a[92:95], v[92:95], v[36:39]\ns_and_b32 s44, s40, 1\nv_mfma_f32_16x16x32_bf16 v[40:43], a[100:103], v[92:95], v[40:43]\ns_lshl_b32 s55, s44, 17\nbuffer_load_dwordx4 v0, s[36:39], s60 offen offset:1024 sc1 lds\n" \
"v_mfma_f32_16x16x32_bf16 v[44:47], v[76:79], v[92:95], v[44:47]\ns_add_u32 s56, s56, %[fs]\ns_waitcnt lgkmcnt(2)\nv_mfma_f32_16x16x32_bf16 v[16:19], a[104:107], v[80:83], v[16:19]\nds_read_b128 v[92:95], v2 offset:18688\nv_mfma_f32_16x16x32_bf16 v[20:23], a[112:115], v[80:83], v[20:23]\nds_read_b128 v[72:75], v1 offset:6144\nv_mfma_f32_16x16x32_bf16 v[24:27], a[120:123], v[80:83], v[24:27]\n" \
"buffer_load_dwordx4 v0, s[36:39], s60 offen offset:2048 sc1 lds\nv_mfma_f32_16x16x32_bf16 v[28:31], a[128:131], v[80:83], v[28:31]\ns_waitcnt lgkmcnt(3)\nv_mfma_f32_16x16x32_bf16 v[32:35], a[104:107], v[84:87], v[32:35]\nds_read_b128 v[80:83], v2 offset:20480\nv_mfma_f32_16x16x32_bf16 v[36:39], a[112:115], v[84:87], v[36:39]\nv_mfma_f32_16x16x32_bf16 v[40:43], a[120:123], v[84:87], v[40:43]\nbuffer_load_dwordx4 v0, s[36:39], s60 offen offset:3072 sc1 lds\n" \
"v_mfma_f32_16x16x32_bf16 v[44:47], a[128:131], v[84:87], v[44:47]\ns_waitcnt lgkmcnt(3)\nv_mfma_f32_16x16x32_bf16 v[16:19], a[108:111], v[88:91], v[16:19]\nds_read_b128 v[84:87], v2 offset:20736\nv_mfma_f32_16x16x32_bf16 v[20:23], a[116:119], v[88:91], v[20:23]\nds_read_b128 v[76:79], v1 offset:7168\nv_mfma_f32_16x16x32_bf16 v[24:27], a[124:127], v[88:91], v[24:27]\nv_mfma_f32_16x16x32_bf16 v[28:31], a[132:135], v[88:91], v[28:31]\n" \
"s_waitcnt lgkmcnt(4)\nv_mfma_f32_16x16x32_bf16 v[32:35], a[108:111], v[92:95], v[32:35]\nds_read_b128 v[88:91], v2 offset:22528\nv_mfma_f32_16x16x32_bf16 v[36:39], a[116:119], v[92:95], v[36:39]\nv_mfma_f32_16x16x32_bf16 v[40:43], a[124:127], v[92:95], v[40:43]\nv_mfma_f32_16x16x32_bf16 v[44:47], a[132:135], v[92:95], v[44:47]\ns_waitcnt lgkmcnt(3)\nv_mfma_f32_16x16x32_bf16 v[16:19], a[136:139], v[80:83], v[16:19]\n" \
"ds_read_b128 v[92:95], v2 offset:22784\nv_mfma_f32_16x16x32_bf16 v[20:23], v[72:75], v[80:83], v[20:23]\nds_read_b128 v[64:67], v1 offset:8192\nv_mfma_f32_16x16x32_bf16 v[24:27], a[144:147], v[80:83], v[24:27]\nv_mfma_f32_16x16x32_bf16 v[28:31], a[152:155], v[80:83], v[28:31]\ns_waitcnt lgkmcnt(4)\nv_mfma_f32_16x16x32_bf16 v[32:35], a[136:139], v[84:87], v[32:35]\nds_read_b128 v[80:83], v2 offset:24576\n" \
"v_mfma_f32_16x16x32_bf16 v[36:39], v[72:75], v[84:87], v[36:39]\nv_mfma_f32_16x16x32_bf16 v[40:43], a[144:147], v[84:87], v[40:43]\nv_mfma_f32_16x16x32_bf16 v[44:47], a[152:155], v[84:87], v[44:47]\ns_waitcnt lgkmcnt(3)\nv_mfma_f32_16x16x32_bf16 v[16:19], a[140:143], v[88:91], v[16:19]\nds_read_b128 v[84:87], v2 offset:24832\nv_mfma_f32_16x16x32_bf16 v[20:23], v[76:79], v[88:91], v[20:23]\nds_read_b128 v[68:71], v1 offset:9216\n" \
"v_mfma_f32_16x16x32_bf16 v[24:27], a[148:151], v[88:91], v[24:27]\nv_mfma_f32_16x16x32_bf16 v[28:31], a[156:159], v[88:91], v[28:31]\ns_waitcnt lgkmcnt(4)\nv_mfma_f32_16x16x32_bf16 v[32:35], a[140:143], v[92:95], v[32:35]\nds_read_b128 v[88:91], v2 offset:26624\nv_mfma_f32_16x16x32_bf16 v[36:39], v[76:79], v[92:95], v[36:39]\nv_mfma_f32_16x16x32_bf16 v[40:43], a[148:151], v[92:95], v[40:43]\nv_mfma_f32_16x16x32_bf16 v[44:47], a[156:159], v[92:95], v[44:47]\n" \
"s_waitcnt lgkmcnt(3)\nv_mfma_f32_16x16x32_bf16 v[16:19], a[160:163], v[80:83], v[16:19]\nds_read_b128 v[92:95], v2 offset:26880\nv_mfma_f32_16x16x32_bf16 v[20:23], a[168:171], v[80:83], v[20:23]\nds_read_b128 v[72:75], v1 offset:10240\nv_mfma_f32_16x16x32_bf16 v[24:27], v[64:67], v[80:83], v[24:27]\nv_mfma_f32_16x16x32_bf16 v[28:31], a[176:179], v[80:83], v[28:31]\ns_waitcnt lgkmcnt(4)\n" \
"v_mfma_f32_16x16x32_bf16 v[32:35], a[160:163], v[84:87], v[32:35]\nds_read_b128 v[80:83], v2 offset:28672\nv_mfma_f32_16x16x32_bf16 v[36:39], a[168:171], v[84:87], v[36:39]\nv_mfma_f32_16x16x32_bf16 v[40:43], v[64:67], v[84:87], v[40:43]\nv_mfma_f32_16x16x32_bf16 v[44:47], a[176:179], v[84:87], v[44:47]\ns_waitcnt lgkmcnt(3)\nv_mfma_f32_16x16x32_bf16 v[16:19], a[164:167], v[88:91], v[16:19]\nds_read_b128 v[84:87], v2 offset:28928\n" \
"v_mfma_f32_16x16x32_bf16 v[20:23], a[172:175], v[88:91], v[20:23]\nds_read_b128 v[76:79], v1 offset:11264\nv_mfma_f32_16x16x32_bf16 v[24:27], v[68:71], v[88:91], v[24:27]\nv_mfma_f32_16x16x32_bf16 v[28:31], a[180:183], v[88:91], v[28:31]\ns_waitcnt lgkmcnt(4)\nv_mfma_f32_16x16x32_bf16 v[32:35], a[164:167], v[92:95], v[32:35]\nds_read_b128 v[88:91], v2 offset:30720\nv_mfma_f32_16x16x32_bf16 v[36:39], a[172:175], v[92:95], v[36:39]\n" \
"v_mfma_f32_16x16x32_bf16 v[40:43], v[68:71], v[92:95], v[40:43]\nv_mfma_f32_16x16x32_bf16 v[44:47], a[180:183], v[92:95], v[44:47]\ns_waitcnt lgkmcnt(3)\nv_mfma_f32_16x16x32_bf16 v[16:19], a[184:187], v[80:83], v[16:19]\nds_read_b128 v[92:95], v2 offset:30976\nv_mfma_f32_16x16x32_bf16 v[20:23], a[192:195], v[80:83], v[20:23]\nv_mfma_f32_16x16x32_bf16 v[24:27], a[200:203], v[80:83], v[24:27]\nv_mfma_f32_16x16x32_bf16 v[28:31], v[72:75], v[80:83], v[28:31]\n" \
"s_waitcnt lgkmcnt(3)\nv_mfma_f32_16x16x32_bf16 v[32:35], a[184:187], v[84:87], v[32:35]\ns_waitcnt vmcnt(0)\ns_barrier\nds_read_b128 v[48:51], v2 offset:32768\nv_mfma_f32_16x16x32_bf16 v[36:39], a[192:195], v[84:87], v[36:39]\nv_mfma_f32_16x16x32_bf16 v[40:43], a[200:203], v[84:87], v[40:43]\ns_add_u32 m0, s57, 0\n" \
"s_add_u32 s60, s41, 98304\nbuffer_load_dwordx4 v0, s[36:39], s60 offen offset:0 sc1 lds\nv_mfma_f32_16x16x32_bf16 v[44:47], v[72:75], v[84:87], v[44:47]\ns_waitcnt lgkmcnt(2)\nv_mfma_f32_16x16x32_bf16 v[16:19], a[188:191], v[88:91], v[16:19]\nds_read_b128 v[52:55], v2 offset:33024\nv_mfma_f32_16x16x32_bf16 v[20:23], a[196:199], v[88:91], v[20:23]\nv_mfma_f32_16x16x32_bf16 v[24:27], a[204:207], v[88:91], v[24:27]\n" \
"buffer_load_dwordx4 v0, s[36:39], s60 offen offset:1024 sc1 lds\nv_mfma_f32_16x16x32_bf16 v[28:31], v[76:79], v[88:91], v[28:31]\ns_waitcnt lgkmcnt(2)\nv_mfma_f32_16x16x32_bf16 v[32:35], a[188:191], v[92:95], v[32:35]\nds_read_b128 v[56:59], v2 offset:34816\nv_mfma_f32_16x16x32_bf16 v[36:39], a[196:199], v[92:95], v[36:39]\nv_mfma_f32_16x16x32_bf16 v[40:43], a[204:207], v[92:95], v[40:43]\nbuffer_load_dwordx4 v0, s[36:39], s60 offen offset:2048 sc1 lds\n" \
"v_mfma_f32_16x16x32_bf16 v[44:47], v[76:79], v[92:95], v[44:47]\ns_waitcnt lgkmcnt(2)\nv_mfma_f32_16x16x32_bf16 v[16:19], a[208:211], v[48:51], v[16:19]\nds_read_b128 v[60:63], v2 offset:35072\nv_mfma_f32_16x16x32_bf16 v[20:23], a[216:219], v[48:51], v[20:23]\nds_read_b128 v[72:75], v1 offset:12288\nv_mfma_f32_16x16x32_bf16 v[24:27], a[224:227], v[48:51], v[24:27]\nbuffer_load_dwordx4 v0, s[36:39], s60 offen offset:3072 sc1 lds\n" \
"v_mfma_f32_16x16x32_bf16 v[28:31], a[232:235], v[48:51], v[28:31]\ns_waitcnt lgkmcnt(3)\nv_mfma_f32_16x16x32_bf16 v[32:35], a[208:211], v[52:55], v[32:35]\nds_read_b128 v[48:51], v2 offset:36864\nv_mfma_f32_16x16x32_bf16 v[36:39], a[216:219], v[52:55], v[36:39]\nv_mfma_f32_16x16x32_bf16 v[40:43], a[224:227], v[52:55], v[40:43]\nv_mfma_f32_16x16x32_bf16 v[44:47], a[232:235], v[52:55], v[44:47]\ns_waitcnt lgkmcnt(3)\n" \
"v_mfma_f32_16x16x32_bf16 v[16:19], a[212:215], v[56:59], v[16:19]\nds_read_b128 v[52:55], v2 offset:37120\nv_mfma_f32_16x16x32_bf16 v[20:23], a[220:223], v[56:59], v[20:23]\nds_read_b128 v[76:79], v1 offset:13312\nv_mfma_f32_16x16x32_bf16 v[24:27], a[228:231], v[56:59], v[24:27]\nv_mfma_f32_16x16x32_bf16 v[28:31], a[236:239], v[56:59], v[28:31]\ns_waitcnt lgkmcnt(4)\nv_mfma_f32_16x16x32_bf16 v[32:35], a[212:215], v[60:63], v[32:35]\n" \
"ds_read_b128 v[56:59], v2 offset:38912\nv_mfma_f32_16x16x32_bf16 v[36:39], a[220:223], v[60:63], v[36:39]\nv_mfma_f32_16x16x32_bf16 v[40:43], a[228:231], v[60:63], v[40:43]\nv_mfma_f32_16x16x32_bf16 v[44:47], a[236:239], v[60:63], v[44:47]\ns_waitcnt lgkmcnt(3)\nv_mfma_f32_16x16x32_bf16 v[16:19], a[240:243], v[48:51], v[16:19]\nds_read_b128 v[60:63], v2 offset:39168\nv_mfma_f32_16x16x32_bf16 v[20:23], v[72:75], v[48:51], v[20:23]\n" \
"ds_read_b128 v[64:67], v1 offset:14336\nv_mfma_f32_16x16x32_bf16 v[24:27], a[248:251], v[48:51], v[24:27]\nv_mfma_f32_16x16x32_bf16 v[28:31], v[96:99], v[48:51], v[28:31]\ns_waitcnt lgkmcnt(4)\nv_mfma_f32_16x16x32_bf16 v[32:35], a[240:243], v[52:55], v[32:35]\nds_read_b128 v[48:51], v2 offset:40960\nv_mfma_f32_16x16x32_bf16 v[36:39], v[72:75], v[52:55], v[36:39]\nv_mfma_f32_16x16x32_bf16 v[40:43], a[248:251], v[52:55], v[40:43]\n" \
"v_mfma_f32_16x16x32_bf16 v[44:47], v[96:99], v[52:55], v[44:47]\ns_waitcnt lgkmcnt(3)\nv_mfma_f32_16x16x32_bf16 v[16:19], a[244:247], v[56:59], v[16:19]\nds_read_b128 v[52:55], v2 offset:41216\nv_mfma_f32_16x16x32_bf16 v[20:23], v[76:79], v[56:59], v[20:23]\nds_read_b128 v[68:71], v1 offset:15360\nv_mfma_f32_16x16x32_bf16 v[24:27], a[252:255], v[56:59], v[24:27]\nv_mfma_f32_16x16x32_bf16 v[28:31], v[100:103], v[56:59], v[28:31]\n" \
"s_waitcnt lgkmcnt(4)\nv_mfma_f32_16x16x32_bf16 v[32:35], a[244:247], v[60:63], v[32:35]\nds_read_b128 v[56:59], v2 offset:43008\nv_mfma_f32_16x16x32_bf16 v[36:39], v[76:79], v[60:63], v[36:39]\nv_mfma_f32_16x16x32_bf16 v[40:43], a[252:255], v[60:63], v[40:43]\nv_mfma_f32_16x16x32_bf16 v[44:47], v[100:103], v[60:63], v[44:47]\ns_waitcnt lgkmcnt(3)\nv_mfma_f32_16x16x32_bf16 v[16:19], v[104:107], v[48:51], v[16:19]\n" \
"ds_read_b128 v[60:63], v2 offset:43264\nv_mfma_f32_16x16x32_bf16 v[20:23], v[112:115], v[48:51], v[20:23]\nds_read_b128 v[72:75], v1 offset:16384\nv_mfma_f32_16x16x32_bf16 v[24:27], v[64:67], v[48:51], v[24:27]\nv_mfma_f32_16x16x32_bf16 v[28:31], v[120:123], v[48:51], v[28:31]\ns_waitcnt lgkmcnt(4)\nv_mfma_f32_16x16x32_bf16 v[32:35], v[104:107], v[52:55], v[32:35]\nds_read_b128 v[48:51], v2 offset:45056\n" \
"v_mfma_f32_16x16x32_bf16 v[36:39], v[112:115], v[52:55], v[36:39]\nv_mfma_f32_16x16x32_bf16 v[40:43], v[64:67], v[52:55], v[40:43]\nv_mfma_f32_16x16x32_bf16 v[44:47], v[120:123], v[52:55], v[44:47]\ns_waitcnt lgkmcnt(3)\nv_mfma_f32_16x16x32_bf16 v[16:19], v[108:111], v[56:59], v[16:19]\nds_read_b128 v[52:55], v2 offset:45312\nv_mfma_f32_16x16x32_bf16 v[20:23], v[116:119], v[56:59], v[20:23]\nds_read_b128 v[76:79], v1 offset:17408\n" \
"v_mfma_f32_16x16x32_bf16 v[24:27], v[68:71], v[56:59], v[24:27]\nv_mfma_f32_16x16x32_bf16 v[28:31], v[124:127], v[56:59], v[28:31]\ns_waitcnt lgkmcnt(4)\nv_mfma_f32_16x16x32_bf16 v[32:35], v[108:111], v[60:63], v[32:35]\nds_read_b128 v[56:59], v2 offset:47104\nv_mfma_f32_16x16x32_bf16 v[36:39], v[116:119], v[60:63], v[36:39]\nv_mfma_f32_16x16x32_bf16 v[40:43], v[68:71], v[60:63], v[40:43]\nv_mfma_f32_16x16x32_bf16 v[44:47], v[124:127], v[60:63], v[44:47]\n" \
"s_waitcnt lgkmcnt(3)\nv_mfma_f32_16x16x32_bf16 v[16:19], v[128:131], v[48:51], v[16:19]\nds_read_b128 v[60:63], v2 offset:47360\nv_mfma_f32_16x16x32_bf16 v[20:23], v[136:139], v[48:51], v[20:23]\nv_mfma_f32_16x16x32_bf16 v[24:27], v[144:147], v[48:51], v[24:27]\nv_mfma_f32_16x16x32_bf16 v[28:31], v[72:75], v[48:51], v[28:31]\ns_waitcnt lgkmcnt(3)\nv_mfma_f32_16x16x32_bf16 v[32:35], v[128:131], v[52:55], v[32:35]\n" \
"s_waitcnt lgkmcnt(0)\ns_waitcnt vmcnt(0)\ns_barrier\nds_read_b128 v[48:51], v2 offset:0\nv_mfma_f32_16x16x32_bf16 v[36:39], v[136:139], v[52:55], v[36:39]\nv_mfma_f32_16x16x32_bf16 v[40:43], v[144:147], v[52:55], v[40:43]\nv_mfma_f32_16x16x32_bf16 v[44:47], v[72:75], v[52:55], v[44:47]\nv_mfma_f32_16x16x32_bf16 v[16:19], v[132:135], v[56:59], v[16:19]\n" \
"ds_read_b128 v[52:55], v2 offset:256\nv_mfma_f32_16x16x32_bf16 v[20:23], v[140:143], v[56:59], v[20:23]\nv_mfma_f32_16x16x32_bf16 v[24:27], v[148:151], v[56:59], v[24:27]\nv_mfma_f32_16x16x32_bf16 v[28:31], v[76:79], v[56:59], v[28:31]\nv_mfma_f32_16x16x32_bf16 v[32:35], v[132:135], v[60:63], v[32:35]\nds_read_b128 v[56:59], v2 offset:2048\nv_mfma_f32_16x16x32_bf16 v[36:39], v[140:143], v[60:63], v[36:39]\nv_mfma_f32_16x16x32_bf16 v[40:43], v[148:151], v[60:63], v[40:43]\n" \
"v_mfma_f32_16x16x32_bf16 v[44:47], v[76:79], v[60:63], v[44:47]\ns_waitcnt lgkmcnt(2)\nv_mfma_f32_16x16x32_bf16 v[16:19], v[152:155], v[48:51], v[16:19]\nds_read_b128 v[60:63], v2 offset:2304\nv_mfma_f32_16x16x32_bf16 v[20:23], v[160:163], v[48:51], v[20:23]\nds_read_b128 v[72:75], v1 offset:18432\nv_mfma_f32_16x16x32_bf16 v[24:27], v[168:171], v[48:51], v[24:27]\nv_mfma_f32_16x16x32_bf16 v[28:31], v[176:179], v[48:51], v[28:31]\n" \
"s_waitcnt lgkmcnt(3)\nv_mfma_f32_16x16x32_bf16 v[32:35], v[152:155], v[52:55], v[32:35]\nds_read_b128 v[48:51], v2 offset:4096\nv_mfma_f32_16x16x32_bf16 v[36:39], v[160:163], v[52:55], v[36:39]\nv_mfma_f32_16x16x32_bf16 v[40:43], v[168:171], v[52:55], v[40:43]\nv_mfma_f32_16x16x32_bf16 v[44:47], v[176:179], v[52:55], v[44:47]\ns_waitcnt lgkmcnt(3)\nv_mfma_f32_16x16x32_bf16 v[16:19], v[156:159], v[56:59], v[16:19]\n" \
"ds_read_b128 v[52:55], v2 offset:4352\nv_mfma_f32_16x16x32_bf16 v[20:23], v[164:167], v[56:59], v[20:23]\nds_read_b128 v[76:79], v1 offset:19456\nv_mfma_f32_16x16x32_bf16 v[24:27], v[172:175], v[56:59], v[24:27]\nv_mfma_f32_16x16x32_bf16 v[28:31], v[180:183], v[56:59], v[28:31]\ns_waitcnt lgkmcnt(4)\nv_mfma_f32_16x16x32_bf16 v[32:35], v[156:159], v[60:63], v[32:35]\nds_read_b128 v[56:59], v2 offset:6144\n" \
"v_mfma_f32_16x16x32_bf16 v[36:39], v[164:167], v[60:63], v[36:39]\nv_mfma_f32_16x16x32_bf16 v[40:43], v[172:175], v[60:63], v[40:43]\nv_mfma_f32_16x16x32_bf16 v[44:47], v[180:183], v[60:63], v[44:47]\ns_waitcnt lgkmcnt(3)\nv_mfma_f32_16x16x32_bf16 v[16:19], v[184:187], v[48:51], v[16:19]\nds_read_b128 v[60:63], v2 offset:6400\nv_mfma_f32_16x16x32_bf16 v[20:23], v[72:75], v[48:51], v[20:23]\nds_read_b128 v[64:67], v1 offset:20480\n" \
"v_mfma_f32_16x16x32_bf16 v[24:27], v[192:195], v[48:51], v[24:27]\nv_mfma_f32_16x16x32_bf16 v[28:31], v[200:203], v[48:51], v[28:31]\ns_waitcnt lgkmcnt(4)\nv_mfma_f32_16x16x32_bf16 v[32:35], v[184:187], v[52:55], v[32:35]\nds_read_b128 v[48:51], v2 offset:8192\nv_mfma_f32_16x16x32_bf16 v[36:39], v[72:75], v[52:55], v[36:39]\nv_mfma_f32_16x16x32_bf16 v[40:43], v[192:195], v[52:55], v[40:43]\nv_mfma_f32_16x16x32_bf16 v[44:47], v[200:203], v[52:55], v[44:47]\n" \
"s_waitcnt lgkmcnt(3)\nv_mfma_f32_16x16x32_bf16 v[16:19], v[188:191], v[56:59], v[16:19]\nds_read_b128 v[52:55], v2 offset:10240\nv_mfma_f32_16x16x32_bf16 v[20:23], v[76:79], v[56:59], v[20:23]\nds_read_b128 v[68:71], v1 offset:21504\nv_mfma_f32_16x16x32_bf16 v[24:27], v[196:199], v[56:59], v[24:27]\nbuffer_load_dwordx4 v[80:83], v3, s[36:39], s42 offen offset:0 sc1\nv_mfma_f32_16x16x32_bf16 v[28:31], v[204:207], v[56:59], v[28:31]\n" \
"s_waitcnt lgkmcnt(4)\nv_mfma_f32_16x16x32_bf16 v[32:35], v[188:191], v[60:63], v[32:35]\nds_read_b128 v[56:59], v2 offset:12288\nv_mfma_f32_16x16x32_bf16 v[36:39], v[76:79], v[60:63], v[36:39]\nds_read_b128 v[72:75], v1 offset:22528\nv_mfma_f32_16x16x32_bf16 v[40:43], v[196:199], v[60:63], v[40:43]\nbuffer_load_dwordx4 v[84:87], v3, s[36:39], s42 offen offset:1024 sc1\nv_mfma_f32_16x16x32_bf16 v[44:47], v[204:207], v[60:63], v[44:47]\n" \
"s_waitcnt lgkmcnt(4)\nv_mfma_f32_16x16x32_bf16 v[16:19], v[208:211], v[48:51], v[16:19]\nds_read_b128 v[60:63], v2 offset:14336\nv_mfma_f32_16x16x32_bf16 v[20:23], v[216:219], v[48:51], v[20:23]\nds_read_b128 v[76:79], v1 offset:23552\nv_mfma_f32_16x16x32_bf16 v[24:27], v[64:67], v[48:51], v[24:27]\nbuffer_load_dwordx4 v[88:91], v3, s[36:39], s42 offen offset:2048 sc1\nv_mfma_f32_16x16x32_bf16 v[28:31], v[224:227], v[48:51], v[28:31]\n" \
"s_waitcnt lgkmcnt(5)\nv_mfma_f32_16x16x32_bf16 v[16:19], v[212:215], v[52:55], v[16:19]\nds_read_b128 v[48:51], v2 offset:8448\nv_mfma_f32_16x16x32_bf16 v[20:23], v[220:223], v[52:55], v[20:23]\ns_waitcnt lgkmcnt(5)\nv_mfma_f32_16x16x32_bf16 v[24:27], v[68:71], v[52:55], v[24:27]\nbuffer_load_dwordx4 v[92:95], v3, s[36:39], s42 offen offset:3072 sc1\nv_mfma_f32_16x16x32_bf16 v[28:31], v[228:231], v[52:55], v[28:31]\n" \
"s_waitcnt lgkmcnt(4)\nv_mfma_f32_16x16x32_bf16 v[16:19], v[232:235], v[56:59], v[16:19]\nds_read_b128 v[52:55], v2 offset:10496\nv_mfma_f32_16x16x32_bf16 v[20:23], v[240:243], v[56:59], v[20:23]\nv_mfma_f32_16x16x32_bf16 v[24:27], v[248:251], v[56:59], v[24:27]\ns_waitcnt lgkmcnt(4)\nv_mfma_f32_16x16x32_bf16 v[28:31], v[72:75], v[56:59], v[28:31]\ns_waitcnt lgkmcnt(3)\n" \
"v_mfma_f32_16x16x32_bf16 v[16:19], v[236:239], v[60:63], v[16:19]\nds_read_b128 v[56:59], v2 offset:12544\nv_mfma_f32_16x16x32_bf16 v[20:23], v[244:247], v[60:63], v[20:23]\nv_mfma_f32_16x16x32_bf16 v[24:27], v[252:255], v[60:63], v[24:27]\ns_waitcnt lgkmcnt(3)\nv_mfma_f32_16x16x32_bf16 v[28:31], v[76:79], v[60:63], v[28:31]\ns_waitcnt lgkmcnt(2)\nv_mfma_f32_16x16x32_bf16 v[32:35], v[208:211], v[48:51], v[32:35]\n" \
"ds_read_b128 v[60:63], v2 offset:14592\nv_mfma_f32_16x16x32_bf16 v[36:39], v[216:219], v[48:51], v[36:39]\nv_cvt_pk_bf16_f32 v20, v16, v20\nv_cvt_pk_bf16_f32 v21, v17, v21\nv_mfma_f32_16x16x32_bf16 v[40:43], v[64:67], v[48:51], v[40:43]\nv_cvt_pk_bf16_f32 v22, v18, v22\nv_cvt_pk_bf16_f32 v23, v19, v23\nv_mfma_f32_16x16x32_bf16 v[44:47], v[224:227], v[48:51], v[44:47]\n" \
"s_waitcnt lgkmcnt(2)\nds_write_b128 v5, v[20:23] offset:0\nv_mfma_f32_16x16x32_bf16 v[32:35], v[212:215], v[52:55], v[32:35]\nv_cvt_pk_bf16_f32 v28, v24, v28\nv_cvt_pk_bf16_f32 v29, v25, v29\nv_mfma_f32_16x16x32_bf16 v[36:39], v[220:223], v[52:55], v[36:39]\nv_cvt_pk_bf16_f32 v30, v26, v30\nv_cvt_pk_bf16_f32 v31, v27, v31\n" \
"v_mfma_f32_16x16x32_bf16 v[40:43], v[68:71], v[52:55], v[40:43]\nv_mov_b64 v[48:49], 0\nv_mov_b64 v[50:51], 0\nv_mfma_f32_16x16x32_bf16 v[44:47], v[228:231], v[52:55], v[44:47]\ns_cmp_eq_u32 %[w], 1\nds_write_b128 v5, v[28:31] offset:512\ns_cbranch_scc1 .Lqk_c1%=\ns_cmp_eq_u32 %[w], 2\n" \
"s_cbranch_scc1 .Lqk_c2%=\ns_cmp_eq_u32 %[w], 3\ns_cbranch_scc1 .Lqk_c3%=\n.Lqk_c0%=:\ns_waitcnt lgkmcnt(3)\nv_mfma_f32_16x16x32_bf16 v[32:35], v[232:235], v[56:59], v[32:35]\nv_mov_b64 v[52:53], 0\nv_mov_b64 v[54:55], 0\n" \
"v_mfma_f32_16x16x32_bf16 v[36:39], v[240:243], v[56:59], v[36:39]\ns_waitcnt lgkmcnt(2)\nds_read_b128 v[64:67], v1 offset:0\nv_mfma_f32_16x16x32_bf16 v[32:35], v[236:239], v[60:63], v[32:35]\nds_read_b128 v[68:71], v1 offset:1024\nv_mfma_f32_16x16x32_bf16 v[36:39], v[244:247], v[60:63], v[36:39]\ns_bfe_i32 s54, s40, 0x10000\nv_mfma_f32_16x16x32_bf16 v[40:43], v[248:251], v[56:59], v[40:43]\n" \
"v_mfma_f32_16x16x32_bf16 v[44:47], v[72:75], v[56:59], v[44:47]\nv_cvt_pk_bf16_f32 v36, v32, v36\nv_cvt_pk_bf16_f32 v37, v33, v37\nv_mfma_f32_16x16x32_bf16 v[40:43], v[252:255], v[60:63], v[40:43]\nv_cvt_pk_bf16_f32 v38, v34, v38\nv_cvt_pk_bf16_f32 v39, v35, v39\nv_mfma_f32_16x16x32_bf16 v[44:47], v[76:79], v[60:63], v[44:47]\nds_write_b128 v5, v[36:39] offset:256\n" \
"v_mov_b64 v[56:57], 0\nv_mov_b64 v[58:59], 0\nv_mov_b64 v[60:61], 0\nv_mov_b64 v[62:63], 0\n.Lqk_poll0%=:\ns_waitcnt vmcnt(0)\nv_smfmac_f32_16x16x64_bf16 v[48:51], v[80:83], a[0:7], v4\nds_write_b128 v0, v[80:83] offset:16384\n" \
"v_max3_u32 v16, v80, v82, v84\nv_smfmac_f32_16x16x64_bf16 v[52:55], v[80:83], a[8:15], v4\nds_read_b128 v[72:75], v1 offset:2048\nds_read_b128 v[76:79], v1 offset:3072\nv_max3_u32 v16, v16, v86, v88\nv_smfmac_f32_16x16x64_bf16 v[56:59], v[80:83], a[16:23], v4\nds_write_b128 v0, v[84:87] offset:17408\nv_max3_u32 v16, v16, v90, v92\n" \
"v_max_u32 v16, v16, v94\nv_smfmac_f32_16x16x64_bf16 v[60:63], v[80:83], a[24:31], v4\nv_cmp_eq_u32 vcc, -1, v16\nv_smfmac_f32_16x16x64_bf16 v[48:51], v[84:87], a[32:39], v4\nds_write_b128 v0, v[88:91] offset:18432\ns_cbranch_vccnz .Lqk_re0%=\ns_waitcnt lgkmcnt(6)\nv_smfmac_f32_16x16x64_bf16 v[52:55], v[84:87], v[64:71], v4\n" \
"ds_read_b128 v[64:67], v1 offset:4096\nds_read_b128 v[68:71], v1 offset:5120\nv_smfmac_f32_16x16x64_bf16 v[56:59], v[84:87], a[40:47], v4\nds_write_b128 v0, v[92:95] offset:19456\nv_smfmac_f32_16x16x64_bf16 v[60:63], v[84:87], a[48:55], v4\nv_smfmac_f32_16x16x64_bf16 v[48:51], v[88:91], a[56:63], v4\nv_smfmac_f32_16x16x64_bf16 v[52:55], v[88:91], a[64:71], v4\ns_waitcnt lgkmcnt(0)\n" \
"s_barrier\ns_memrealtime s[68:69]\nds_read_b128 v[80:83], v0 offset:20480\nds_read_b128 v[84:87], v0 offset:21504\nv_mov_b32 v24, s40\nv_smfmac_f32_16x16x64_bf16 v[56:59], v[88:91], v[72:79], v4\nds_read_b128 v[72:75], v1 offset:6144\nds_read_b128 v[76:79], v1 offset:7168\n" \
"v_cvt_pk_bf16_f32 v44, v40, v44\nv_smfmac_f32_16x16x64_bf16 v[60:63], v[88:91], a[72:79], v4\nds_read_b128 v[88:91], v0 offset:22528\nv_cvt_pk_bf16_f32 v45, v41, v45\nv_smfmac_f32_16x16x64_bf16 v[48:51], v[92:95], a[80:87], v4\nv_cvt_pk_bf16_f32 v46, v42, v46\nv_smfmac_f32_16x16x64_bf16 v[52:55], v[92:95], a[88:95], v4\nv_cvt_pk_bf16_f32 v47, v43, v47\n" \
"v_smfmac_f32_16x16x64_bf16 v[56:59], v[92:95], a[96:103], v4\nv_smfmac_f32_16x16x64_bf16 v[60:63], v[92:95], v[64:71], v4\ns_waitcnt lgkmcnt(4)\nds_write_b128 v5, v[44:47] offset:768\nds_read_b128 v[92:95], v0 offset:23552\nds_read_b128 v[64:67], v1 offset:8192\nds_read_b128 v[68:71], v1 offset:9216\nv_smfmac_f32_16x16x64_bf16 v[48:51], v[80:83], a[104:111], v4\n" \
"s_sub_u32 s88, s86, s89\ns_min_u32 s88, s88, 150\nv_smfmac_f32_16x16x64_bf16 v[52:55], v[80:83], a[112:119], v4\ns_add_u32 s88, s88, s68\ns_mov_b32 s89, s68\nv_mov_b32 v25, s88\nv_smfmac_f32_16x16x64_bf16 v[56:59], v[80:83], a[120:127], v4\ns_mov_b64 exec, 1\n" \
"buffer_store_dwordx2 v[24:25], off, s[36:39], %[sg] sc1\ns_mov_b64 exec, -1\nv_smfmac_f32_16x16x64_bf16 v[60:63], v[80:83], a[128:135], v4\ns_waitcnt lgkmcnt(7)\nds_read_b128 v[80:83], v0 offset:24576\nv_smfmac_f32_16x16x64_bf16 v[48:51], v[84:87], a[136:143], v4\ns_waitcnt lgkmcnt(6)\nv_smfmac_f32_16x16x64_bf16 v[52:55], v[84:87], v[72:79], v4\n" \
"ds_read_b128 v[72:75], v1 offset:10240\nds_read_b128 v[76:79], v1 offset:11264\nv_smfmac_f32_16x16x64_bf16 v[56:59], v[84:87], a[144:151], v4\nv_smfmac_f32_16x16x64_bf16 v[60:63], v[84:87], a[152:159], v4\ns_waitcnt lgkmcnt(7)\nds_read_b128 v[84:87], v0 offset:25600\nv_smfmac_f32_16x16x64_bf16 v[48:51], v[88:91], a[160:167], v4\ns_waitcnt lgkmcnt(7)\n" \
"s_barrier\nv_smfmac_f32_16x16x64_bf16 v[52:55], v[88:91], a[168:175], v4\ns_waitcnt lgkmcnt(4)\nv_smfmac_f32_16x16x64_bf16 v[56:59], v[88:91], v[64:71], v4\nds_read_b128 v[64:67], v1 offset:12288\nds_read_b128 v[68:71], v1 offset:13312\nv_smfmac_f32_16x16x64_bf16 v[60:63], v[88:91], a[176:183], v4\nds_read_b128 v[88:91], v0 offset:26624\n" \
"v_smfmac_f32_16x16x64_bf16 v[48:51], v[92:95], a[184:191], v4\nds_read_b128 v[16:19], v7 offset:0\nv_smfmac_f32_16x16x64_bf16 v[52:55], v[92:95], a[192:199], v4\nv_smfmac_f32_16x16x64_bf16 v[56:59], v[92:95], a[200:207], v4\ns_waitcnt lgkmcnt(5)\nds_read_b128 v[20:23], v7 offset:64\nv_smfmac_f32_16x16x64_bf16 v[60:63], v[92:95], v[72:79], v4\nds_read_b128 v[92:95], v0 offset:27648\n" \
"ds_read_b128 v[72:75], v1 offset:14336\nds_read_b128 v[76:79], v1 offset:15360\nv_smfmac_f32_16x16x64_bf16 v[48:51], v[80:83], a[208:215], v4\nds_read_b128 v[24:27], v7 offset:2048\nv_smfmac_f32_16x16x64_bf16 v[52:55], v[80:83], a[216:223], v4\nv_smfmac_f32_16x16x64_bf16 v[56:59], v[80:83], a[224:231], v4\nds_read_b128 v[28:31], v7 offset:2112\nv_smfmac_f32_16x16x64_bf16 v[60:63], v[80:83], a[232:239], v4\n" \
"s_waitcnt lgkmcnt(10)\nds_read_b128 v[80:83], v0 offset:28672\nv_smfmac_f32_16x16x64_bf16 v[48:51], v[84:87], a[240:247], v4\ns_waitcnt lgkmcnt(9)\nds_read_b128 v[32:35], v7 offset:4096\nv_smfmac_f32_16x16x64_bf16 v[52:55], v[84:87], v[64:71], v4\nds_read_b128 v[64:67], v1 offset:16384\nds_read_b128 v[68:71], v1 offset:17408\n" \
"v_smfmac_f32_16x16x64_bf16 v[56:59], v[84:87], a[248:255], v4\nds_read_b128 v[36:39], v7 offset:4160\nv_smfmac_f32_16x16x64_bf16 v[60:63], v[84:87], v[96:103], v4\ns_waitcnt lgkmcnt(12)\nds_read_b128 v[84:87], v0 offset:29696\nv_smfmac_f32_16x16x64_bf16 v[48:51], v[88:91], v[104:111], v4\nds_read_b128 v[40:43], v7 offset:6144\nv_smfmac_f32_16x16x64_bf16 v[52:55], v[88:91], v[112:119], v4\n" \
"s_waitcnt lgkmcnt(9)\nv_smfmac_f32_16x16x64_bf16 v[56:59], v[88:91], v[72:79], v4\nds_read_b128 v[72:75], v1 offset:18432\nds_read_b128 v[76:79], v1 offset:19456\nv_smfmac_f32_16x16x64_bf16 v[60:63], v[88:91], v[120:127], v4\nds_read_b128 v[88:91], v0 offset:30720\nv_smfmac_f32_16x16x64_bf16 v[48:51], v[92:95], v[128:135], v4\nds_read_b128 v[44:47], v7 offset:6208\n" \
"v_smfmac_f32_16x16x64_bf16 v[52:55], v[92:95], v[136:143], v4\nv_smfmac_f32_16x16x64_bf16 v[56:59], v[92:95], v[144:151], v4\ns_waitcnt lgkmcnt(7)\nv_smfmac_f32_16x16x64_bf16 v[60:63], v[92:95], v[64:71], v4\nds_read_b128 v[92:95], v0 offset:31744\nds_read_b128 v[64:67], v1 offset:20480\nds_read_b128 v[68:71], v1 offset:21504\nv_smfmac_f32_16x16x64_bf16 v[48:51], v[80:83], v[152:159], v4\n" \
"v_smfmac_f32_16x16x64_bf16 v[52:55], v[80:83], v[160:167], v4\nv_smfmac_f32_16x16x64_bf16 v[56:59], v[80:83], v[168:175], v4\nv_smfmac_f32_16x16x64_bf16 v[60:63], v[80:83], v[176:183], v4\ns_waitcnt lgkmcnt(8)\nv_smfmac_f32_16x16x64_bf16 v[48:51], v[84:87], v[184:191], v4\ns_waitcnt lgkmcnt(5)\nv_smfmac_f32_16x16x64_bf16 v[52:55], v[84:87], v[72:79], v4\nds_read_b128 v[72:75], v1 offset:22528\n" \
"ds_read_b128 v[76:79], v1 offset:23552\nv_smfmac_f32_16x16x64_bf16 v[56:59], v[84:87], v[192:199], v4\nv_smfmac_f32_16x16x64_bf16 v[60:63], v[84:87], v[200:207], v4\ns_waitcnt lgkmcnt(6)\nv_smfmac_f32_16x16x64_bf16 v[48:51], v[88:91], v[208:215], v4\nv_smfmac_f32_16x16x64_bf16 v[52:55], v[88:91], v[216:223], v4\ns_waitcnt lgkmcnt(4)\nv_smfmac_f32_16x16x64_bf16 v[48:51], v[92:95], v[232:239], v4\n" \
"v_smfmac_f32_16x16x64_bf16 v[52:55], v[92:95], v[240:247], v4\ns_waitcnt lgkmcnt(2)\nv_smfmac_f32_16x16x64_bf16 v[56:59], v[88:91], v[64:71], v4\nv_add_f32 v48, v48, v50\nv_smfmac_f32_16x16x64_bf16 v[60:63], v[88:91], v[224:231], v4\nv_add_f32 v52, v52, v54\nv_add_f32 v49, v49, v51\nv_smfmac_f32_16x16x64_bf16 v[56:59], v[92:95], v[248:255], v4\n" \
"s_waitcnt lgkmcnt(0)\nv_add_f32 v53, v53, v55\nv_cvt_pk_bf16_f32 v50, v48, v52\nv_smfmac_f32_16x16x64_bf16 v[60:63], v[92:95], v[72:79], v4\nv_cvt_pk_bf16_f32 v51, v49, v53\nds_write2_b32 v6, v50, v51 offset0:0 offset1:4\ns_nop 1\nv_add_f32 v56, v56, v58\n" \
"v_add_f32 v57, v57, v59\ns_nop 1\nv_add_f32 v60, v60, v62\nv_add_f32 v61, v61, v63\nv_cvt_pk_bf16_f32 v62, v56, v60\nv_cvt_pk_bf16_f32 v63, v57, v61\nds_write2_b32 v6, v62, v63 offset0:36 offset1:40\nv_mov_b32 v61, s54\n" \
"s_waitcnt lgkmcnt(0)\ns_barrier\nds_read_b128 v[80:83], v8 offset:0\nds_read_b128 v[84:87], v8 offset:576\nds_read_b128 v[88:91], v8 offset:1152\nds_read_b128 v[92:95], v8 offset:1728\nds_read_b128 v[64:67], v9 offset:0\nds_read_b128 v[68:71], v9 offset:576\n" \
"ds_read_b128 v[72:75], v9 offset:1152\nds_read_b128 v[76:79], v9 offset:1728\nds_read_b32 v60, v0 offset:53760\ns_waitcnt lgkmcnt(8)\nv_mfma_f32_16x16x32_bf16 v[52:55], v[16:19], v[80:83], 0\nv_mfma_f32_16x16x32_bf16 v[56:59], v[20:23], v[80:83], 0\ns_waitcnt lgkmcnt(7)\nv_mfma_f32_16x16x32_bf16 v[52:55], v[24:27], v[84:87], v[52:55]\n" \
"v_mfma_f32_16x16x32_bf16 v[56:59], v[28:31], v[84:87], v[56:59]\ns_waitcnt lgkmcnt(6)\nv_mfma_f32_16x16x32_bf16 v[52:55], v[32:35], v[88:91], v[52:55]\nv_mfma_f32_16x16x32_bf16 v[56:59], v[36:39], v[88:91], v[56:59]\ns_waitcnt lgkmcnt(5)\nv_mfma_f32_16x16x32_bf16 v[52:55], v[40:43], v[92:95], v[52:55]\nv_mfma_f32_16x16x32_bf16 v[56:59], v[44:47], v[92:95], v[56:59]\ns_waitcnt lgkmcnt(4)\n" \
"v_mfma_f32_16x16x32_bf16 v[48:51], v[64:67], v[80:83], 0\ns_waitcnt lgkmcnt(3)\nv_mfma_f32_16x16x32_bf16 v[48:51], v[68:71], v[84:87], v[48:51]\ns_waitcnt lgkmcnt(2)\nds_read_b128 v[80:83], v2 offset:0\nv_mfma_f32_16x16x32_bf16 v[48:51], v[72:75], v[88:91], v[48:51]\ns_waitcnt lgkmcnt(2)\nds_read_b128 v[84:87], v2 offset:256\n" \
"v_mfma_f32_16x16x32_bf16 v[48:51], v[76:79], v[92:95], v[48:51]\nds_read_b128 v[88:91], v2 offset:2048\nv_max3_f32 v62, v52, v53, v54\nv_max3_f32 v63, v55, v56, v57\nv_max3_f32 v62, v62, v58, v59\nv_cndmask_b32 v52, v52, v56, s[62:63]\nv_cndmask_b32 v53, v53, v57, s[62:63]\nv_cndmask_b32 v54, v54, v58, s[62:63]\n" \
"v_cndmask_b32 v55, v55, v59, s[62:63]\nv_max_f32 v62, v62, v63\nv_max3_f32 v63, v62, v48, v49\nv_max3_f32 v62, v62, v48, v49\ns_waitcnt lgkmcnt(2)\nv_mfma_f32_16x16x32_bf16 v[16:19], a[0:3], v[80:83], 0\nds_read_b128 v[92:95], v2 offset:2304\nv_permlane16_swap_b32 v62, v63\n" \
"v_max_f32 v50, v62, v63\nv_mfma_f32_16x16x32_bf16 v[20:23], a[8:11], v[80:83], 0\nds_read_b128 v[72:75], v1 offset:0\nv_max_f32 v62, v62, v63\nv_mfma_f32_16x16x32_bf16 v[24:27], a[16:19], v[80:83], 0\ns_nop 0\nv_permlane32_swap_b32 v62, v50\nv_max_f32 v62, v62, v50\n" \
"v_mul_f32 v62, s59, v62\nv_mfma_f32_16x16x32_bf16 v[28:31], a[24:27], v[80:83], 0\ns_waitcnt lgkmcnt(3)\nv_fma_f32 v52, v52, s58, v62\nv_fma_f32 v53, v53, s58, v62\nv_exp_f32 v52, v52\nv_mfma_f32_16x16x32_bf16 v[32:35], a[0:3], v[84:87], 0\nds_read_b128 v[80:83], v2 offset:4096\n" \
"v_fma_f32 v54, v54, s58, v62\nv_exp_f32 v53, v53\nv_mfma_f32_16x16x32_bf16 v[36:39], a[8:11], v[84:87], 0\nv_fma_f32 v55, v55, s58, v62\nv_exp_f32 v54, v54\nv_add_f32 v63, v52, v53\nv_mfma_f32_16x16x32_bf16 v[40:43], a[16:19], v[84:87], 0\nv_fma_f32 v48, v48, s58, v62\n" \
"v_exp_f32 v55, v55\nv_add_f32 v63, v63, v54\nv_mfma_f32_16x16x32_bf16 v[44:47], a[24:27], v[84:87], 0\ns_waitcnt lgkmcnt(3)\nv_fma_f32 v49, v49, s58, v62\nv_exp_f32 v48, v48\nv_add_f32 v63, v63, v55\nv_mfma_f32_16x16x32_bf16 v[16:19], a[4:7], v[88:91], v[16:19]\n" \
"ds_read_b128 v[84:87], v2 offset:4352\nv_exp_f32 v49, v49\nv_add_f32 v50, v48, v63\nv_mfma_f32_16x16x32_bf16 v[20:23], a[12:15], v[88:91], v[20:23]\nds_read_b128 v[76:79], v1 offset:1024\nv_add_f32 v50, v50, v49\nv_mfma_f32_16x16x32_bf16 v[24:27], a[20:23], v[88:91], v[24:27]\ns_nop 0\n" \
"v_add_f32_dpp v62, v63, v50 row_ror:8 row_mask:0xf bank_mask:0xf\nv_add_f32_dpp v50, v63, v50 row_ror:8 row_mask:0xf bank_mask:0xf\nv_mfma_f32_16x16x32_bf16 v[28:31], a[28:31], v[88:91], v[28:31]\ns_waitcnt lgkmcnt(4)\nv_permlane16_swap_b32 v62, v50\nv_add_f32 v63, v62, v50\nv_add_f32 v50, v62, v50\nv_mfma_f32_16x16x32_bf16 v[32:35], a[4:7], v[92:95], v[32:35]\n" \
"ds_read_b128 v[88:91], v2 offset:6144\nv_permlane32_swap_b32 v63, v50\nv_add_f32 v63, v63, v50\nv_mfma_f32_16x16x32_bf16 v[36:39], a[12:15], v[92:95], v[36:39]\nv_rcp_f32 v63, v63\nv_mfma_f32_16x16x32_bf16 v[40:43], a[20:23], v[92:95], v[40:43]\nv_mul_f32 v52, v52, v63\nv_mul_f32 v53, v53, v63\n" \
"v_mul_f32 v54, v54, v63\nv_mul_f32 v55, v55, v63\nv_mfma_f32_16x16x32_bf16 v[44:47], a[28:31], v[92:95], v[44:47]\ns_waitcnt lgkmcnt(3)\nv_mul_f32 v48, v48, v63\nv_mul_f32 v49, v49, v63\nv_cvt_pk_bf16_f32 v52, v52, v53\nv_cvt_pk_bf16_f32 v53, v54, v55\n" \
"v_mfma_f32_16x16x32_bf16 v[16:19], a[32:35], v[80:83], v[16:19]\nds_read_b128 v[92:95], v2 offset:6400\nv_cvt_pk_bf16_f32 v48, v48, v49\nv_bfi_b32 v52, s61, v52, v61\nv_bfi_b32 v53, s61, v53, v61\nv_mfma_f32_16x16x32_bf16 v[20:23], v[72:75], v[80:83], v[20:23]\nds_read_b128 v[64:67], v1 offset:2048\nv_bfi_b32 v48, s61, v48, v61\n" \
"v_mfma_f32_16x16x32_bf16 v[24:27], a[40:43], v[80:83], v[24:27]\ns_memrealtime s[86:87]\nbuffer_store_dwordx2 v[52:53], v10, s[36:39], 0 offen sc1\nbuffer_store_dword v48, v11, s[36:39], 0 offen sc1\nv_mfma_f32_16x16x32_bf16 v[28:31], a[48:51], v[80:83], v[28:31]\ns_waitcnt lgkmcnt(4)\nv_cmp_ne_u32 vcc, 0, v60\nv_mfma_f32_16x16x32_bf16 v[32:35], a[32:35], v[84:87], v[32:35]\n" \
"ds_read_b128 v[80:83], v2 offset:8192\nv_mfma_f32_16x16x32_bf16 v[36:39], v[72:75], v[84:87], v[36:39]\nv_mfma_f32_16x16x32_bf16 v[40:43], a[40:43], v[84:87], v[40:43]\nv_mfma_f32_16x16x32_bf16 v[44:47], a[48:51], v[84:87], v[44:47]\ns_waitcnt lgkmcnt(3)\nv_mfma_f32_16x16x32_bf16 v[16:19], a[36:39], v[88:91], v[16:19]\nds_read_b128 v[84:87], v2 offset:8448\nv_mfma_f32_16x16x32_bf16 v[20:23], v[76:79], v[88:91], v[20:23]\n" \
"ds_read_b128 v[68:71], v1 offset:3072\nv_mfma_f32_16x16x32_bf16 v[24:27], a[44:47], v[88:91], v[24:27]\nv_mfma_f32_16x16x32_bf16 v[28:31], a[52:55], v[88:91], v[28:31]\ns_waitcnt lgkmcnt(4)\nv_mfma_f32_16x16x32_bf16 v[32:35], a[36:39], v[92:95], v[32:35]\nds_read_b128 v[88:91], v2 offset:10240\nv_mfma_f32_16x16x32_bf16 v[36:39], v[76:79], v[92:95], v[36:39]\nv_mfma_f32_16x16x32_bf16 v[40:43], a[44:47], v[92:95], v[40:43]\n" \
"v_mfma_f32_16x16x32_bf16 v[44:47], a[52:55], v[92:95], v[44:47]\ns_waitcnt lgkmcnt(3)\nv_mfma_f32_16x16x32_bf16 v[16:19], a[56:59], v[80:83], v[16:19]\nds_read_b128 v[92:95], v2 offset:10496\nv_mfma_f32_16x16x32_bf16 v[20:23], a[64:67], v[80:83], v[20:23]\nds_read_b128 v[72:75], v1 offset:4096\nv_mfma_f32_16x16x32_bf16 v[24:27], v[64:67], v[80:83], v[24:27]\nv_mfma_f32_16x16x32_bf16 v[28:31], a[72:75], v[80:83], v[28:31]\n" \
"s_waitcnt lgkmcnt(4)\nv_mfma_f32_16x16x32_bf16 v[32:35], a[56:59], v[84:87], v[32:35]\nds_read_b128 v[80:83], v2 offset:12288\nv_mfma_f32_16x16x32_bf16 v[36:39], a[64:67], v[84:87], v[36:39]\nv_mfma_f32_16x16x32_bf16 v[40:43], v[64:67], v[84:87], v[40:43]\nv_mfma_f32_16x16x32_bf16 v[44:47], a[72:75], v[84:87], v[44:47]\ns_waitcnt lgkmcnt(3)\nv_mfma_f32_16x16x32_bf16 v[16:19], a[60:63], v[88:91], v[16:19]\n" \
"ds_read_b128 v[84:87], v2 offset:12544\nv_mfma_f32_16x16x32_bf16 v[20:23], a[68:71], v[88:91], v[20:23]\nds_read_b128 v[76:79], v1 offset:5120\nv_mfma_f32_16x16x32_bf16 v[24:27], v[68:71], v[88:91], v[24:27]\nv_mfma_f32_16x16x32_bf16 v[28:31], a[76:79], v[88:91], v[28:31]\ns_waitcnt lgkmcnt(4)\nv_mfma_f32_16x16x32_bf16 v[32:35], a[60:63], v[92:95], v[32:35]\nds_read_b128 v[88:91], v2 offset:14336\n" \
"v_mfma_f32_16x16x32_bf16 v[36:39], a[68:71], v[92:95], v[36:39]\nv_mfma_f32_16x16x32_bf16 v[40:43], v[68:71], v[92:95], v[40:43]\nv_mfma_f32_16x16x32_bf16 v[44:47], a[76:79], v[92:95], v[44:47]\ns_cbranch_vccnz .Lqk_pj%=\ns_cmp_lt_i32 s40, 63\ns_waitcnt lgkmcnt(0)\ns_cbranch_scc1 .Lqk_pwait%=\ns_branch .Lqk_end%=\n" \
".Lqk_re0%=:\nv_max_u32 v17, v80, v82\nv_cmp_eq_u32 vcc, -1, v17\ns_cbranch_vccz .Lqk_sk1615%=\ns_mov_b64 exec, vcc\nbuffer_load_dwordx4 v[80:83], v3, s[36:39], s42 offen offset:0 sc1\ns_mov_b64 exec, -1\n.Lqk_sk1615%=:\n" \
"v_max_u32 v17, v84, v86\nv_cmp_eq_u32 vcc, -1, v17\ns_cbranch_vccz .Lqk_sk1622%=\ns_mov_b64 exec, vcc\nbuffer_load_dwordx4 v[84:87], v3, s[36:39], s42 offen offset:1024 sc1\ns_mov_b64 exec, -1\n.Lqk_sk1622%=:\nv_max_u32 v17, v88, v90\n" \
"v_cmp_eq_u32 vcc, -1, v17\ns_cbranch_vccz .Lqk_sk1629%=\ns_mov_b64 exec, vcc\nbuffer_load_dwordx4 v[88:91], v3, s[36:39], s42 offen offset:2048 sc1\ns_mov_b64 exec, -1\n.Lqk_sk1629%=:\nv_max_u32 v17, v92, v94\nv_cmp_eq_u32 vcc, -1, v17\n" \
"s_cbranch_vccz .Lqk_sk1636%=\ns_mov_b64 exec, vcc\nbuffer_load_dwordx4 v[92:95], v3, s[36:39], s42 offen offset:3072 sc1\ns_mov_b64 exec, -1\n.Lqk_sk1636%=:\nv_mov_b64 v[48:49], 0\nv_mov_b64 v[50:51], 0\nv_mov_b64 v[52:53], 0\n" \
"v_mov_b64 v[54:55], 0\nv_mov_b64 v[56:57], 0\nv_mov_b64 v[58:59], 0\nv_mov_b64 v[60:61], 0\nv_mov_b64 v[62:63], 0\ns_add_u32 s43, s43, 1\ns_cmp_lt_u32 s43, 262144\ns_cbranch_scc1 .Lqk_poll0%=\n" \
"s_branch .Lqk_abort%=\n.Lqk_c1%=:\ns_waitcnt lgkmcnt(3)\nv_mfma_f32_16x16x32_bf16 v[32:35], v[232:235], v[56:59], v[32:35]\nv_mov_b64 v[52:53], 0\nv_mov_b64 v[54:55], 0\nv_mfma_f32_16x16x32_bf16 v[36:39], v[240:243], v[56:59], v[36:39]\ns_waitcnt lgkmcnt(2)\n" \
"ds_read_b128 v[64:67], v1 offset:6144\nv_mfma_f32_16x16x32_bf16 v[32:35], v[236:239], v[60:63], v[32:35]\nds_read_b128 v[68:71], v1 offset:7168\nv_mfma_f32_16x16x32_bf16 v[36:39], v[244:247], v[60:63], v[36:39]\ns_bfe_i32 s54, s40, 0x10001\nv_mfma_f32_16x16x32_bf16 v[40:43], v[248:251], v[56:59], v[40:43]\nv_mfma_f32_16x16x32_bf16 v[44:47], v[72:75], v[56:59], v[44:47]\nv_cvt_pk_bf16_f32 v36, v32, v36\n" \
"v_cvt_pk_bf16_f32 v37, v33, v37\nv_mfma_f32_16x16x32_bf16 v[40:43], v[252:255], v[60:63], v[40:43]\nv_cvt_pk_bf16_f32 v38, v34, v38\nv_cvt_pk_bf16_f32 v39, v35, v39\nv_mfma_f32_16x16x32_bf16 v[44:47], v[76:79], v[60:63], v[44:47]\nds_write_b128 v5, v[36:39] offset:256\nv_mov_b64 v[56:57], 0\nv_mov_b64 v[58:59], 0\n" \
"v_mov_b64 v[60:61], 0\nv_mov_b64 v[62:63], 0\n.Lqk_poll1%=:\ns_waitcnt vmcnt(0)\nv_smfmac_f32_16x16x64_bf16 v[48:51], v[80:83], a[104:111], v4\nds_write_b128 v0, v[80:83] offset:20480\nv_max3_u32 v16, v80, v82, v84\nv_smfmac_f32_16x16x64_bf16 v[52:55], v[80:83], a[112:119], v4\n" \
"ds_read_b128 v[72:75], v1 offset:8192\nds_read_b128 v[76:79], v1 offset:9216\nv_max3_u32 v16, v16, v86, v88\nv_smfmac_f32_16x16x64_bf16 v[56:59], v[80:83], a[120:127], v4\nds_write_b128 v0, v[84:87] offset:21504\nv_max3_u32 v16, v16, v90, v92\nv_max_u32 v16, v16, v94\nv_smfmac_f32_16x16x64_bf16 v[60:63], v[80:83], a[128:135], v4\n" \
"v_cmp_eq_u32 vcc, -1, v16\nv_smfmac_f32_16x16x64_bf16 v[48:51], v[84:87], a[136:143], v4\nds_write_b128 v0, v[88:91] offset:22528\ns_cbranch_vccnz .Lqk_re1%=\ns_waitcnt lgkmcnt(6)\nv_smfmac_f32_16x16x64_bf16 v[52:55], v[84:87], v[64:71], v4\nds_read_b128 v[64:67], v1 offset:10240\nds_read_b128 v[68:71], v1 offset:11264\n" \
"v_smfmac_f32_16x16x64_bf16 v[56:59], v[84:87], a[144:151], v4\nds_write_b128 v0, v[92:95] offset:23552\nv_smfmac_f32_16x16x64_bf16 v[60:63], v[84:87], a[152:159], v4\nv_smfmac_f32_16x16x64_bf16 v[48:51], v[88:91], a[160:167], v4\nv_smfmac_f32_16x16x64_bf16 v[52:55], v[88:91], a[168:175], v4\ns_waitcnt lgkmcnt(0)\ns_barrier\nds_read_b128 v[80:83], v0 offset:24576\n" \
"ds_read_b128 v[84:87], v0 offset:25600\nv_smfmac_f32_16x16x64_bf16 v[56:59], v[88:91], v[72:79], v4\nds_read_b128 v[72:75], v1 offset:12288\nds_read_b128 v[76:79], v1 offset:13312\nv_cvt_pk_bf16_f32 v44, v40, v44\nv_smfmac_f32_16x16x64_bf16 v[60:63], v[88:91], a[176:183], v4\nds_read_b128 v[88:91], v0 offset:26624\nv_cvt_pk_bf16_f32 v45, v41, v45\n" \
"v_smfmac_f32_16x16x64_bf16 v[48:51], v[92:95], a[184:191], v4\nv_cvt_pk_bf16_f32 v46, v42, v46\nv_smfmac_f32_16x16x64_bf16 v[52:55], v[92:95], a[192:199], v4\nv_cvt_pk_bf16_f32 v47, v43, v47\nv_smfmac_f32_16x16x64_bf16 v[56:59], v[92:95], a[200:207], v4\nv_smfmac_f32_16x16x64_bf16 v[60:63], v[92:95], v[64:71], v4\ns_waitcnt lgkmcnt(4)\nds_write_b128 v5, v[44:47] offset:768\n" \
"ds_read_b128 v[92:95], v0 offset:27648\nds_read_b128 v[64:67], v1 offset:14336\nds_read_b128 v[68:71], v1 offset:15360\nv_smfmac_f32_16x16x64_bf16 v[48:51], v[80:83], a[208:215], v4\nv_smfmac_f32_16x16x64_bf16 v[52:55], v[80:83], a[216:223], v4\nv_smfmac_f32_16x16x64_bf16 v[56:59], v[80:83], a[224:231], v4\nv_smfmac_f32_16x16x64_bf16 v[60:63], v[80:83], a[232:239], v4\ns_waitcnt lgkmcnt(7)\n" \
"ds_read_b128 v[80:83], v0 offset:28672\nv_smfmac_f32_16x16x64_bf16 v[48:51], v[84:87], a[240:247], v4\ns_waitcnt lgkmcnt(6)\nv_smfmac_f32_16x16x64_bf16 v[52:55], v[84:87], v[72:79], v4\nds_read_b128 v[72:75], v1 offset:16384\nds_read_b128 v[76:79], v1 offset:17408\nv_smfmac_f32_16x16x64_bf16 v[56:59], v[84:87], a[248:255], v4\nv_smfmac_f32_16x16x64_bf16 v[60:63], v[84:87], v[96:103], v4\n" \
"s_waitcnt lgkmcnt(7)\nds_read_b128 v[84:87], v0 offset:29696\nv_smfmac_f32_16x16x64_bf16 v[48:51], v[88:91], v[104:111], v4\ns_waitcnt lgkmcnt(7)\ns_barrier\nv_smfmac_f32_16x16x64_bf16 v[52:55], v[88:91], v[112:119], v4\ns_waitcnt lgkmcnt(4)\nv_smfmac_f32_16x16x64_bf16 v[56:59], v[88:91], v[64:71], v4\n" \
"ds_read_b128 v[64:67], v1 offset:18432\nds_read_b128 v[68:71], v1 offset:19456\nv_smfmac_f32_16x16x64_bf16 v[60:63], v[88:91], v[120:127], v4\nds_read_b128 v[88:91], v0 offset:30720\nv_smfmac_f32_16x16x64_bf16 v[48:51], v[92:95], v[128:135], v4\nds_read_b128 v[16:19], v7 offset:0\nv_smfmac_f32_16x16x64_bf16 v[52:55], v[92:95], v[136:143], v4\nv_smfmac_f32_16x16x64_bf16 v[56:59], v[92:95], v[144:151], v4\n" \
"s_waitcnt lgkmcnt(5)\nds_read_b128 v[20:23], v7 offset:2048\nv_smfmac_f32_16x16x64_bf16 v[60:63], v[92:95], v[72:79], v4\nds_read_b128 v[92:95], v0 offset:31744\nds_read_b128 v[72:75], v1 offset:20480\nds_read_b128 v[76:79], v1 offset:21504\nv_smfmac_f32_16x16x64_bf16 v[48:51], v[80:83], v[152:159], v4\nds_read_b128 v[24:27], v7 offset:4096\n" \
"v_smfmac_f32_16x16x64_bf16 v[52:55], v[80:83], v[160:167], v4\nv_smfmac_f32_16x16x64_bf16 v[56:59], v[80:83], v[168:175], v4\nds_read_b128 v[28:31], v7 offset:6144\nv_smfmac_f32_16x16x64_bf16 v[60:63], v[80:83], v[176:183], v4\ns_waitcnt lgkmcnt(10)\nds_read_b128 v[80:83], v0 offset:16384\nv_smfmac_f32_16x16x64_bf16 v[48:51], v[84:87], v[184:191], v4\ns_waitcnt lgkmcnt(9)\n" \
"ds_read_b128 v[32:35], v7 offset:8192\nv_smfmac_f32_16x16x64_bf16 v[52:55], v[84:87], v[64:71], v4\nds_read_b128 v[64:67], v1 offset:22528\nds_read_b128 v[68:71], v1 offset:23552\nv_smfmac_f32_16x16x64_bf16 v[56:59], v[84:87], v[192:199], v4\nds_read_b128 v[36:39], v7 offset:10240\nv_smfmac_f32_16x16x64_bf16 v[60:63], v[84:87], v[200:207], v4\ns_waitcnt lgkmcnt(12)\n" \
"ds_read_b128 v[84:87], v0 offset:17408\nv_smfmac_f32_16x16x64_bf16 v[48:51], v[88:91], v[208:215], v4\nds_read_b128 v[40:43], v7 offset:12288\nv_smfmac_f32_16x16x64_bf16 v[52:55], v[88:91], v[216:223], v4\ns_waitcnt lgkmcnt(9)\nv_smfmac_f32_16x16x64_bf16 v[56:59], v[88:91], v[72:79], v4\nds_read_b128 v[72:75], v1 offset:0\nds_read_b128 v[76:79], v1 offset:1024\n" \
"v_smfmac_f32_16x16x64_bf16 v[60:63], v[88:91], v[224:231], v4\nds_read_b128 v[88:91], v0 offset:18432\nv_smfmac_f32_16x16x64_bf16 v[48:51], v[92:95], v[232:239], v4\nds_read_b128 v[44:47], v7 offset:14336\nv_smfmac_f32_16x16x64_bf16 v[52:55], v[92:95], v[240:247], v4\nv_smfmac_f32_16x16x64_bf16 v[56:59], v[92:95], v[248:255], v4\ns_waitcnt lgkmcnt(7)\nv_smfmac_f32_16x16x64_bf16 v[60:63], v[92:95], v[64:71], v4\n" \
"ds_read_b128 v[92:95], v0 offset:19456\nds_read_b128 v[64:67], v1 offset:2048\nds_read_b128 v[68:71], v1 offset:3072\nv_smfmac_f32_16x16x64_bf16 v[48:51], v[80:83], a[0:7], v4\nv_smfmac_f32_16x16x64_bf16 v[52:55], v[80:83], a[8:15], v4\nv_smfmac_f32_16x16x64_bf16 v[56:59], v[80:83], a[16:23], v4\nv_smfmac_f32_16x16x64_bf16 v[60:63], v[80:83], a[24:31], v4\ns_waitcnt lgkmcnt(8)\n" \
"v_smfmac_f32_16x16x64_bf16 v[48:51], v[84:87], a[32:39], v4\ns_waitcnt lgkmcnt(5)\nv_smfmac_f32_16x16x64_bf16 v[52:55], v[84:87], v[72:79], v4\nds_read_b128 v[72:75], v1 offset:4096\nds_read_b128 v[76:79], v1 offset:5120\nv_smfmac_f32_16x16x64_bf16 v[56:59], v[84:87], a[40:47], v4\nv_smfmac_f32_16x16x64_bf16 v[60:63], v[84:87], a[48:55], v4\ns_waitcnt lgkmcnt(6)\n" \
"v_smfmac_f32_16x16x64_bf16 v[48:51], v[88:91], a[56:63], v4\nv_smfmac_f32_16x16x64_bf16 v[52:55], v[88:91], a[64:71], v4\ns_waitcnt lgkmcnt(4)\nv_smfmac_f32_16x16x64_bf16 v[48:51], v[92:95], a[80:87], v4\nv_smfmac_f32_16x16x64_bf16 v[52:55], v[92:95], a[88:95], v4\ns_waitcnt lgkmcnt(2)\nv_smfmac_f32_16x16x64_bf16 v[56:59], v[88:91], v[64:71], v4\nv_add_f32 v48, v48, v50\n" \
"v_smfmac_f32_16x16x64_bf16 v[60:63], v[88:91], a[72:79], v4\nv_add_f32 v52, v52, v54\nv_add_f32 v49, v49, v51\nv_smfmac_f32_16x16x64_bf16 v[56:59], v[92:95], a[96:103], v4\ns_waitcnt lgkmcnt(0)\nv_add_f32 v53, v53, v55\nv_cvt_pk_bf16_f32 v50, v48, v52\nv_smfmac_f32_16x16x64_bf16 v[60:63], v[92:95], v[72:79], v4\n" \
"v_cvt_pk_bf16_f32 v51, v49, v53\nds_write2_b32 v6, v50, v51 offset0:0 offset1:4\ns_nop 1\nv_add_f32 v56, v56, v58\nv_add_f32 v57, v57, v59\ns_nop 1\nv_add_f32 v60, v60, v62\nv_add_f32 v61, v61, v63\n" \
"v_cvt_pk_bf16_f32 v62, v56, v60\nv_cvt_pk_bf16_f32 v63, v57, v61\nds_write2_b32 v6, v62, v63 offset0:36 offset1:40\nv_mov_b32 v61, s54\nv_mfma_f32_16x16x32_bf16 v[52:55], v[32:35], v[16:19], v[12:15]\nv_mfma_f32_16x16x32_bf16 v[52:55], v[36:39], v[20:23], v[52:55]\nv_mfma_f32_16x16x32_bf16 v[52:55], v[40:43], v[24:27], v[52:55]\nv_mfma_f32_16x16x32_bf16 v[52:55], v[44:47], v[28:31], v[52:55]\n" \
"s_waitcnt lgkmcnt(0)\ns_barrier\nds_read_b128 v[32:35], v9 offset:0\nds_read_b128 v[36:39], v9 offset:576\nds_read_b128 v[40:43], v9 offset:1152\nds_read_b128 v[44:47], v9 offset:1728\nds_read_b32 v60, v0 offset:53760\nds_read_b128 v[80:83], v2 offset:0\n" \
"ds_read_b128 v[84:87], v2 offset:256\nds_read_b128 v[88:91], v2 offset:2048\ns_waitcnt lgkmcnt(7)\nv_mfma_f32_16x16x32_bf16 v[48:51], v[32:35], v[16:19], 0\ns_waitcnt lgkmcnt(6)\nv_mfma_f32_16x16x32_bf16 v[48:51], v[36:39], v[20:23], v[48:51]\ns_waitcnt lgkmcnt(5)\nv_mfma_f32_16x16x32_bf16 v[48:51], v[40:43], v[24:27], v[48:51]\n" \
"s_waitcnt lgkmcnt(4)\nv_mfma_f32_16x16x32_bf16 v[48:51], v[44:47], v[28:31], v[48:51]\ns_waitcnt lgkmcnt(2)\nv_mfma_f32_16x16x32_bf16 v[16:19], a[0:3], v[80:83], 0\nds_read_b128 v[92:95], v2 offset:2304\nv_max3_f32 v62, v52, v53, v54\nv_mfma_f32_16x16x32_bf16 v[20:23], a[8:11], v[80:83], 0\nds_read_b128 v[72:75], v1 offset:0\n" \
"v_max_f32 v62, v62, v55\nv_mfma_f32_16x16x32_bf16 v[24:27], a[16:19], v[80:83], 0\nv_max3_f32 v63, v62, v48, v49\nv_max3_f32 v62, v62, v48, v49\nv_mfma_f32_16x16x32_bf16 v[28:31], a[24:27], v[80:83], 0\ns_waitcnt lgkmcnt(3)\nv_permlane16_swap_b32 v62, v63\nv_mfma_f32_16x16x32_bf16 v[32:35], a[0:3], v[84:87], 0\n" \
"ds_read_b128 v[80:83], v2 offset:4096\nv_max_f32 v50, v62, v63\nv_mfma_f32_16x16x32_bf16 v[36:39], a[8:11], v[84:87], 0\nv_max_f32 v62, v62, v63\nv_mfma_f32_16x16x32_bf16 v[40:43], a[16:19], v[84:87], 0\ns_nop 0\nv_permlane32_swap_b32 v62, v50\nv_mfma_f32_16x16x32_bf16 v[44:47], a[24:27], v[84:87], 0\n" \
"s_waitcnt lgkmcnt(3)\nv_max_f32 v62, v62, v50\nv_mul_f32 v62, s59, v62\nv_mfma_f32_16x16x32_bf16 v[16:19], a[4:7], v[88:91], v[16:19]\nds_read_b128 v[84:87], v2 offset:4352\nv_fma_f32 v52, v52, s58, v62\nv_mfma_f32_16x16x32_bf16 v[20:23], a[12:15], v[88:91], v[20:23]\nds_read_b128 v[76:79], v1 offset:1024\n" \
"v_fma_f32 v53, v53, s58, v62\nv_mfma_f32_16x16x32_bf16 v[24:27], a[20:23], v[88:91], v[24:27]\nv_exp_f32 v52, v52\nv_mfma_f32_16x16x32_bf16 v[28:31], a[28:31], v[88:91], v[28:31]\ns_waitcnt lgkmcnt(4)\nv_fma_f32 v54, v54, s58, v62\nv_fma_f32 v55, v55, s58, v62\nv_mfma_f32_16x16x32_bf16 v[32:35], a[4:7], v[92:95], v[32:35]\n" \
"ds_read_b128 v[88:91], v2 offset:6144\nv_fma_f32 v48, v48, s58, v62\nv_mfma_f32_16x16x32_bf16 v[36:39], a[12:15], v[92:95], v[36:39]\nv_exp_f32 v53, v53\nv_mfma_f32_16x16x32_bf16 v[40:43], a[20:23], v[92:95], v[40:43]\nv_exp_f32 v54, v54\nv_mfma_f32_16x16x32_bf16 v[44:47], a[28:31], v[92:95], v[44:47]\ns_waitcnt lgkmcnt(3)\n" \
"v_add_f32 v63, v52, v53\nv_add_f32 v63, v63, v54\nv_mfma_f32_16x16x32_bf16 v[16:19], a[32:35], v[80:83], v[16:19]\nds_read_b128 v[92:95], v2 offset:6400\nv_fma_f32 v49, v49, s58, v62\nv_mfma_f32_16x16x32_bf16 v[20:23], v[72:75], v[80:83], v[20:23]\nds_read_b128 v[64:67], v1 offset:2048\nv_mfma_f32_16x16x32_bf16 v[24:27], a[40:43], v[80:83], v[24:27]\n" \
"v_exp_f32 v55, v55\nv_mfma_f32_16x16x32_bf16 v[28:31], a[48:51], v[80:83], v[28:31]\ns_waitcnt lgkmcnt(4)\nv_exp_f32 v48, v48\nv_mfma_f32_16x16x32_bf16 v[32:35], a[32:35], v[84:87], v[32:35]\nds_read_b128 v[80:83], v2 offset:8192\nv_add_f32 v63, v63, v55\nv_mfma_f32_16x16x32_bf16 v[36:39], v[72:75], v[84:87], v[36:39]\n" \
"v_exp_f32 v49, v49\nv_mfma_f32_16x16x32_bf16 v[40:43], a[40:43], v[84:87], v[40:43]\nv_add_f32 v63, v63, v48\nv_add_f32 v62, v63, v49\nv_mfma_f32_16x16x32_bf16 v[44:47], a[48:51], v[84:87], v[44:47]\ns_waitcnt lgkmcnt(3)\nv_add_f32 v50, v63, v49\nv_mfma_f32_16x16x32_bf16 v[16:19], a[36:39], v[88:91], v[16:19]\n" \
"ds_read_b128 v[84:87], v2 offset:8448\nv_mfma_f32_16x16x32_bf16 v[20:23], v[76:79], v[88:91], v[20:23]\nds_read_b128 v[68:71], v1 offset:3072\nv_mfma_f32_16x16x32_bf16 v[24:27], a[44:47], v[88:91], v[24:27]\nv_permlane16_swap_b32 v62, v50\nv_mfma_f32_16x16x32_bf16 v[28:31], a[52:55], v[88:91], v[28:31]\ns_waitcnt lgkmcnt(4)\nv_add_f32 v63, v62, v50\n" \
"v_add_f32 v50, v62, v50\nv_mfma_f32_16x16x32_bf16 v[32:35], a[36:39], v[92:95], v[32:35]\nds_read_b128 v[88:91], v2 offset:10240\nv_mfma_f32_16x16x32_bf16 v[36:39], v[76:79], v[92:95], v[36:39]\nv_permlane32_swap_b32 v63, v50\nv_mfma_f32_16x16x32_bf16 v[40:43], a[44:47], v[92:95], v[40:43]\nv_add_f32 v63, v63, v50\nv_mfma_f32_16x16x32_bf16 v[44:47], a[52:55], v[92:95], v[44:47]\n" \
"s_waitcnt lgkmcnt(3)\nv_rcp_f32 v63, v63\nv_mfma_f32_16x16x32_bf16 v[16:19], a[56:59], v[80:83], v[16:19]\nds_read_b128 v[92:95], v2 offset:10496\nv_mul_f32 v52, v52, v63\nv_mfma_f32_16x16x32_bf16 v[20:23], a[64:67], v[80:83], v[20:23]\nds_read_b128 v[72:75], v1 offset:4096\nv_mul_f32 v53, v53, v63\n" \
"v_mfma_f32_16x16x32_bf16 v[24:27], v[64:67], v[80:83], v[24:27]\nv_mul_f32 v54, v54, v63\nv_mul_f32 v55, v55, v63\nv_mfma_f32_16x16x32_bf16 v[28:31], a[72:75], v[80:83], v[28:31]\ns_waitcnt lgkmcnt(4)\nv_mul_f32 v48, v48, v63\nv_mul_f32 v49, v49, v63\nv_mfma_f32_16x16x32_bf16 v[32:35], a[56:59], v[84:87], v[32:35]\n" \
"ds_read_b128 v[80:83], v2 offset:12288\nv_cvt_pk_bf16_f32 v52, v52, v53\nv_mfma_f32_16x16x32_bf16 v[36:39], a[64:67], v[84:87], v[36:39]\nv_cvt_pk_bf16_f32 v53, v54, v55\nv_cvt_pk_bf16_f32 v48, v48, v49\nv_mfma_f32_16x16x32_bf16 v[40:43], v[64:67], v[84:87], v[40:43]\nv_bfi_b32 v52, s61, v52, v61\nv_bfi_b32 v53, s61, v53, v61\n" \
"v_mfma_f32_16x16x32_bf16 v[44:47], a[72:75], v[84:87], v[44:47]\ns_waitcnt lgkmcnt(3)\nv_bfi_b32 v48, s61, v48, v61\nv_mfma_f32_16x16x32_bf16 v[16:19], a[60:63], v[88:91], v[16:19]\nds_read_b128 v[84:87], v2 offset:12544\nv_mfma_f32_16x16x32_bf16 v[20:23], a[68:71], v[88:91], v[20:23]\nds_read_b128 v[76:79], v1 offset:5120\nv_mfma_f32_16x16x32_bf16 v[24:27], v[68:71], v[88:91], v[24:27]\n" \
"buffer_store_dwordx2 v[52:53], v10, s[36:39], s55 offen sc1\nv_mfma_f32_16x16x32_bf16 v[28:31], a[76:79], v[88:91], v[28:31]\ns_waitcnt lgkmcnt(4)\nbuffer_store_dword v48, v11, s[36:39], s55 offen sc1\nv_mfma_f32_16x16x32_bf16 v[32:35], a[60:63], v[92:95], v[32:35]\nds_read_b128 v[88:91], v2 offset:14336\nv_cmp_ne_u32 vcc, 0, v60\nv_mfma_f32_16x16x32_bf16 v[36:39], a[68:71], v[92:95], v[36:39]\n" \
"v_mfma_f32_16x16x32_bf16 v[40:43], v[68:71], v[92:95], v[40:43]\nv_mfma_f32_16x16x32_bf16 v[44:47], a[76:79], v[92:95], v[44:47]\ns_cbranch_vccnz .Lqk_pj%=\ns_cmp_lt_i32 s40, 63\ns_waitcnt lgkmcnt(0)\ns_cbranch_scc1 .Lqk_pwait%=\ns_branch .Lqk_end%=\n.Lqk_re1%=:\n" \
"v_max_u32 v17, v80, v82\nv_cmp_eq_u32 vcc, -1, v17\ns_cbranch_vccz .Lqk_sk1995%=\ns_mov_b64 exec, vcc\nbuffer_load_dwordx4 v[80:83], v3, s[36:39], s42 offen offset:0 sc1\ns_mov_b64 exec, -1\n.Lqk_sk1995%=:\nv_max_u32 v17, v84, v86\n" \
"v_cmp_eq_u32 vcc, -1, v17\ns_cbranch_vccz .Lqk_sk2002%=\ns_mov_b64 exec, vcc\nbuffer_load_dwordx4 v[84:87], v3, s[36:39], s42 offen offset:1024 sc1\ns_mov_b64 exec, -1\n.Lqk_sk2002%=:\nv_max_u32 v17, v88, v90\nv_cmp_eq_u32 vcc, -1, v17\n" \
"s_cbranch_vccz .Lqk_sk2009%=\ns_mov_b64 exec, vcc\nbuffer_load_dwordx4 v[88:91], v3, s[36:39], s42 offen offset:2048 sc1\ns_mov_b64 exec, -1\n.Lqk_sk2009%=:\nv_max_u32 v17, v92, v94\nv_cmp_eq_u32 vcc, -1, v17\ns_cbranch_vccz .Lqk_sk2016%=\n" \
"s_mov_b64 exec, vcc\nbuffer_load_dwordx4 v[92:95], v3, s[36:39], s42 offen offset:3072 sc1\ns_mov_b64 exec, -1\n.Lqk_sk2016%=:\nv_mov_b64 v[48:49], 0\nv_mov_b64 v[50:51], 0\nv_mov_b64 v[52:53], 0\nv_mov_b64 v[54:55], 0\n" \
"v_mov_b64 v[56:57], 0\nv_mov_b64 v[58:59], 0\nv_mov_b64 v[60:61], 0\nv_mov_b64 v[62:63], 0\ns_add_u32 s43, s43, 1\ns_cmp_lt_u32 s43, 262144\ns_cbranch_scc1 .Lqk_poll1%=\ns_branch .Lqk_abort%=\n" \
".Lqk_c2%=:\ns_waitcnt lgkmcnt(3)\nv_mfma_f32_16x16x32_bf16 v[32:35], v[232:235], v[56:59], v[32:35]\nv_mov_b64 v[52:53], 0\nv_mov_b64 v[54:55], 0\nv_mfma_f32_16x16x32_bf16 v[36:39], v[240:243], v[56:59], v[36:39]\ns_waitcnt lgkmcnt(2)\nds_read_b128 v[64:67], v1 offset:12288\n" \
"v_mfma_f32_16x16x32_bf16 v[32:35], v[236:239], v[60:63], v[32:35]\nds_read_b128 v[68:71], v1 offset:13312\nv_mfma_f32_16x16x32_bf16 v[36:39], v[244:247], v[60:63], v[36:39]\ns_bfe_i32 s54, s40, 0x10001\nv_mfma_f32_16x16x32_bf16 v[40:43], v[248:251], v[56:59], v[40:43]\nv_mfma_f32_16x16x32_bf16 v[44:47], v[72:75], v[56:59], v[44:47]\nv_cvt_pk_bf16_f32 v36, v32, v36\nv_cvt_pk_bf16_f32 v37, v33, v37\n" \
"v_mfma_f32_16x16x32_bf16 v[40:43], v[252:255], v[60:63], v[40:43]\nv_cvt_pk_bf16_f32 v38, v34, v38\nv_cvt_pk_bf16_f32 v39, v35, v39\nv_mfma_f32_16x16x32_bf16 v[44:47], v[76:79], v[60:63], v[44:47]\nds_write_b128 v5, v[36:39] offset:256\nv_mov_b64 v[56:57], 0\nv_mov_b64 v[58:59], 0\nv_mov_b64 v[60:61], 0\n" \
"v_mov_b64 v[62:63], 0\n.Lqk_poll2%=:\ns_waitcnt vmcnt(0)\nv_smfmac_f32_16x16x64_bf16 v[48:51], v[80:83], a[208:215], v4\nds_write_b128 v0, v[80:83] offset:24576\nv_max3_u32 v16, v80, v82, v84\nv_smfmac_f32_16x16x64_bf16 v[52:55], v[80:83], a[216:223], v4\nds_read_b128 v[72:75], v1 offset:14336\n" \
"ds_read_b128 v[76:79], v1 offset:15360\nv_max3_u32 v16, v16, v86, v88\nv_smfmac_f32_16x16x64_bf16 v[56:59], v[80:83], a[224:231], v4\nds_write_b128 v0, v[84:87] offset:25600\nv_max3_u32 v16, v16, v90, v92\nv_max_u32 v16, v16, v94\nv_smfmac_f32_16x16x64_bf16 v[60:63], v[80:83], a[232:239], v4\nv_cmp_eq_u32 vcc, -1, v16\n" \
"v_smfmac_f32_16x16x64_bf16 v[48:51], v[84:87], a[240:247], v4\nds_write_b128 v0, v[88:91] offset:26624\ns_cbranch_vccnz .Lqk_re2%=\ns_waitcnt lgkmcnt(6)\nv_smfmac_f32_16x16x64_bf16 v[52:55], v[84:87], v[64:71], v4\nds_read_b128 v[64:67], v1 offset:16384\nds_read_b128 v[68:71], v1 offset:17408\nv_smfmac_f32_16x16x64_bf16 v[56:59], v[84:87], a[248:255], v4\n" \
"ds_write_b128 v0, v[92:95] offset:27648\nv_smfmac_f32_16x16x64_bf16 v[60:63], v[84:87], v[96:103], v4\nv_smfmac_f32_16x16x64_bf16 v[48:51], v[88:91], v[104:111], v4\nv_smfmac_f32_16x16x64_bf16 v[52:55], v[88:91], v[112:119], v4\ns_waitcnt lgkmcnt(0)\ns_barrier\nds_read_b128 v[80:83], v0 offset:28672\nds_read_b128 v[84:87], v0 offset:29696\n" \
"v_smfmac_f32_16x16x64_bf16 v[56:59], v[88:91], v[72:79], v4\nds_read_b128 v[72:75], v1 offset:18432\nds_read_b128 v[76:79], v1 offset:19456\nv_cvt_pk_bf16_f32 v44, v40, v44\nv_smfmac_f32_16x16x64_bf16 v[60:63], v[88:91], v[120:127], v4\nds_read_b128 v[88:91], v0 offset:30720\nv_cvt_pk_bf16_f32 v45, v41, v45\nv_smfmac_f32_16x16x64_bf16 v[48:51], v[92:95], v[128:135], v4\n" \
"v_cvt_pk_bf16_f32 v46, v42, v46\nv_smfmac_f32_16x16x64_bf16 v[52:55], v[92:95], v[136:143], v4\nv_cvt_pk_bf16_f32 v47, v43, v47\nv_smfmac_f32_16x16x64_bf16 v[56:59], v[92:95], v[144:151], v4\nv_smfmac_f32_16x16x64_bf16 v[60:63], v[92:95], v[64:71], v4\ns_waitcnt lgkmcnt(4)\nds_write_b128 v5, v[44:47] offset:768\nds_read_b128 v[92:95], v0 offset:31744\n" \
"ds_read_b128 v[64:67], v1 offset:20480\nds_read_b128 v[68:71], v1 offset:21504\nv_smfmac_f32_16x16x64_bf16 v[48:51], v[80:83], v[152:159], v4\nv_smfmac_f32_16x16x64_bf16 v[52:55], v[80:83], v[160:167], v4\nv_smfmac_f32_16x16x64_bf16 v[56:59], v[80:83], v[168:175], v4\nv_smfmac_f32_16x16x64_bf16 v[60:63], v[80:83], v[176:183], v4\ns_waitcnt lgkmcnt(7)\nds_read_b128 v[80:83], v0 offset:16384\n" \
"v_smfmac_f32_16x16x64_bf16 v[48:51], v[84:87], v[184:191], v4\ns_waitcnt lgkmcnt(6)\nv_smfmac_f32_16x16x64_bf16 v[52:55], v[84:87], v[72:79], v4\nds_read_b128 v[72:75], v1 offset:22528\nds_read_b128 v[76:79], v1 offset:23552\nv_smfmac_f32_16x16x64_bf16 v[56:59], v[84:87], v[192:199], v4\nv_smfmac_f32_16x16x64_bf16 v[60:63], v[84:87], v[200:207], v4\ns_waitcnt lgkmcnt(7)\n" \
"ds_read_b128 v[84:87], v0 offset:17408\nv_smfmac_f32_16x16x64_bf16 v[48:51], v[88:91], v[208:215], v4\ns_waitcnt lgkmcnt(7)\ns_barrier\nv_smfmac_f32_16x16x64_bf16 v[52:55], v[88:91], v[216:223], v4\ns_waitcnt lgkmcnt(4)\nv_smfmac_f32_16x16x64_bf16 v[56:59], v[88:91], v[64:71], v4\nds_read_b128 v[64:67], v1 offset:0\n" \
"ds_read_b128 v[68:71], v1 offset:1024\nv_smfmac_f32_16x16x64_bf16 v[60:63], v[88:91], v[224:231], v4\nds_read_b128 v[88:91], v0 offset:18432\nv_smfmac_f32_16x16x64_bf16 v[48:51], v[92:95], v[232:239], v4\nds_read_b128 v[16:19], v7 offset:256\nv_smfmac_f32_16x16x64_bf16 v[52:55], v[92:95], v[240:247], v4\nv_smfmac_f32_16x16x64_bf16 v[56:59], v[92:95], v[248:255], v4\ns_waitcnt lgkmcnt(5)\n" \
"ds_read_b128 v[20:23], v7 offset:2304\nv_smfmac_f32_16x16x64_bf16 v[60:63], v[92:95], v[72:79], v4\nds_read_b128 v[92:95], v0 offset:19456\nds_read_b128 v[72:75], v1 offset:2048\nds_read_b128 v[76:79], v1 offset:3072\nv_smfmac_f32_16x16x64_bf16 v[48:51], v[80:83], a[0:7], v4\nds_read_b128 v[24:27], v7 offset:4352\nv_smfmac_f32_16x16x64_bf16 v[52:55], v[80:83], a[8:15], v4\n" \
"v_smfmac_f32_16x16x64_bf16 v[56:59], v[80:83], a[16:23], v4\nds_read_b128 v[28:31], v7 offset:6400\nv_smfmac_f32_16x16x64_bf16 v[60:63], v[80:83], a[24:31], v4\ns_waitcnt lgkmcnt(10)\nds_read_b128 v[80:83], v0 offset:20480\nv_smfmac_f32_16x16x64_bf16 v[48:51], v[84:87], a[32:39], v4\ns_waitcnt lgkmcnt(9)\nds_read_b128 v[32:35], v7 offset:8192\n" \
"v_smfmac_f32_16x16x64_bf16 v[52:55], v[84:87], v[64:71], v4\nds_read_b128 v[64:67], v1 offset:4096\nds_read_b128 v[68:71], v1 offset:5120\nv_smfmac_f32_16x16x64_bf16 v[56:59], v[84:87], a[40:47], v4\nds_read_b128 v[36:39], v7 offset:10240\nv_smfmac_f32_16x16x64_bf16 v[60:63], v[84:87], a[48:55], v4\ns_waitcnt lgkmcnt(12)\nds_read_b128 v[84:87], v0 offset:21504\n" \
"v_smfmac_f32_16x16x64_bf16 v[48:51], v[88:91], a[56:63], v4\nds_read_b128 v[40:43], v7 offset:12288\nv_smfmac_f32_16x16x64_bf16 v[52:55], v[88:91], a[64:71], v4\ns_waitcnt lgkmcnt(9)\nv_smfmac_f32_16x16x64_bf16 v[56:59], v[88:91], v[72:79], v4\nds_read_b128 v[72:75], v1 offset:6144\nds_read_b128 v[76:79], v1 offset:7168\nv_smfmac_f32_16x16x64_bf16 v[60:63], v[88:91], a[72:79], v4\n" \
"ds_read_b128 v[88:91], v0 offset:22528\nv_smfmac_f32_16x16x64_bf16 v[48:51], v[92:95], a[80:87], v4\nds_read_b128 v[44:47], v7 offset:14336\nv_smfmac_f32_16x16x64_bf16 v[52:55], v[92:95], a[88:95], v4\nv_smfmac_f32_16x16x64_bf16 v[56:59], v[92:95], a[96:103], v4\ns_waitcnt lgkmcnt(7)\nv_smfmac_f32_16x16x64_bf16 v[60:63], v[92:95], v[64:71], v4\nds_read_b128 v[92:95], v0 offset:23552\n" \
"ds_read_b128 v[64:67], v1 offset:8192\nds_read_b128 v[68:71], v1 offset:9216\nv_smfmac_f32_16x16x64_bf16 v[48:51], v[80:83], a[104:111], v4\nv_smfmac_f32_16x16x64_bf16 v[52:55], v[80:83], a[112:119], v4\nv_smfmac_f32_16x16x64_bf16 v[56:59], v[80:83], a[120:127], v4\nv_smfmac_f32_16x16x64_bf16 v[60:63], v[80:83], a[128:135], v4\ns_waitcnt lgkmcnt(8)\nv_smfmac_f32_16x16x64_bf16 v[48:51], v[84:87], a[136:143], v4\n" \
"s_waitcnt lgkmcnt(5)\nds_read_b128 v[80:83], v7 offset:8448\nv_smfmac_f32_16x16x64_bf16 v[52:55], v[84:87], v[72:79], v4\nds_read_b128 v[72:75], v1 offset:10240\nds_read_b128 v[76:79], v1 offset:11264\nv_smfmac_f32_16x16x64_bf16 v[56:59], v[84:87], a[144:151], v4\nv_smfmac_f32_16x16x64_bf16 v[60:63], v[84:87], a[152:159], v4\ns_waitcnt lgkmcnt(7)\n" \
"v_smfmac_f32_16x16x64_bf16 v[48:51], v[88:91], a[160:167], v4\nds_read_b128 v[84:87], v7 offset:10496\nv_smfmac_f32_16x16x64_bf16 v[52:55], v[88:91], a[168:175], v4\ns_waitcnt lgkmcnt(6)\nv_smfmac_f32_16x16x64_bf16 v[48:51], v[92:95], a[184:191], v4\nv_smfmac_f32_16x16x64_bf16 v[52:55], v[92:95], a[192:199], v4\ns_waitcnt lgkmcnt(4)\nv_smfmac_f32_16x16x64_bf16 v[56:59], v[88:91], v[64:71], v4\n" \
"ds_read_b128 v[64:67], v7 offset:12544\nv_add_f32 v48, v48, v50\nv_smfmac_f32_16x16x64_bf16 v[60:63], v[88:91], a[176:183], v4\nds_read_b128 v[68:71], v7 offset:14592\nv_add_f32 v52, v52, v54\nv_add_f32 v49, v49, v51\nv_smfmac_f32_16x16x64_bf16 v[56:59], v[92:95], a[200:207], v4\ns_waitcnt lgkmcnt(3)\n" \
"v_add_f32 v53, v53, v55\nv_cvt_pk_bf16_f32 v50, v48, v52\nv_smfmac_f32_16x16x64_bf16 v[60:63], v[92:95], v[72:79], v4\nv_cvt_pk_bf16_f32 v51, v49, v53\nds_write2_b32 v6, v50, v51 offset0:0 offset1:4\ns_nop 1\nv_add_f32 v56, v56, v58\nv_add_f32 v57, v57, v59\n" \
"s_nop 1\nv_add_f32 v60, v60, v62\nv_add_f32 v61, v61, v63\nv_cvt_pk_bf16_f32 v62, v56, v60\nv_cvt_pk_bf16_f32 v63, v57, v61\nds_write2_b32 v6, v62, v63 offset0:36 offset1:40\nv_mov_b32 v61, s54\nv_mfma_f32_16x16x32_bf16 v[52:55], v[32:35], v[16:19], 0\n" \
"v_mfma_f32_16x16x32_bf16 v[52:55], v[36:39], v[20:23], v[52:55]\nv_mfma_f32_16x16x32_bf16 v[52:55], v[40:43], v[24:27], v[52:55]\nv_mfma_f32_16x16x32_bf16 v[52:55], v[44:47], v[28:31], v[52:55]\ns_waitcnt lgkmcnt(0)\ns_barrier\nds_read_b128 v[32:35], v9 offset:0\nds_read_b128 v[36:39], v9 offset:576\nds_read_b128 v[40:43], v9 offset:1152\n" \
"ds_read_b128 v[44:47], v9 offset:1728\nds_read_b32 v60, v0 offset:53760\nv_mfma_f32_16x16x32_bf16 v[56:59], v[80:83], v[16:19], v[12:15]\nv_mfma_f32_16x16x32_bf16 v[56:59], v[84:87], v[20:23], v[56:59]\nv_mfma_f32_16x16x32_bf16 v[56:59], v[64:67], v[24:27], v[56:59]\nv_mfma_f32_16x16x32_bf16 v[56:59], v[68:71], v[28:31], v[56:59]\ns_waitcnt lgkmcnt(4)\nds_read_b128 v[80:83], v2 offset:0\n" \
"ds_read_b128 v[84:87], v2 offset:256\nds_read_b128 v[88:91], v2 offset:2048\nv_mfma_f32_16x16x32_bf16 v[48:51], v[32:35], v[16:19], 0\ns_waitcnt lgkmcnt(6)\nv_mfma_f32_16x16x32_bf16 v[48:51], v[36:39], v[20:23], v[48:51]\ns_waitcnt lgkmcnt(5)\nv_mfma_f32_16x16x32_bf16 v[48:51], v[40:43], v[24:27], v[48:51]\ns_waitcnt lgkmcnt(4)\n" \
"v_mfma_f32_16x16x32_bf16 v[48:51], v[44:47], v[28:31], v[48:51]\ns_waitcnt lgkmcnt(2)\nv_mfma_f32_16x16x32_bf16 v[16:19], a[0:3], v[80:83], 0\nds_read_b128 v[92:95], v2 offset:2304\nv_max3_f32 v62, v52, v53, v54\nv_max3_f32 v62, v62, v56, v57\nv_mfma_f32_16x16x32_bf16 v[20:23], a[8:11], v[80:83], 0\nds_read_b128 v[72:75], v1 offset:0\n" \
"v_max3_f32 v62, v62, v58, v59\nv_max_f32 v62, v62, v55\nv_mfma_f32_16x16x32_bf16 v[24:27], a[16:19], v[80:83], 0\nv_max3_f32 v63, v62, v48, v49\nv_max3_f32 v62, v62, v48, v49\nv_mfma_f32_16x16x32_bf16 v[28:31], a[24:27], v[80:83], 0\ns_waitcnt lgkmcnt(3)\nv_permlane16_swap_b32 v62, v63\n" \
"v_max_f32 v50, v62, v63\nv_mfma_f32_16x16x32_bf16 v[32:35], a[0:3], v[84:87], 0\nds_read_b128 v[80:83], v2 offset:4096\nv_max_f32 v62, v62, v63\nv_mfma_f32_16x16x32_bf16 v[36:39], a[8:11], v[84:87], 0\ns_nop 0\nv_permlane32_swap_b32 v62, v50\nv_max_f32 v62, v62, v50\n" \
"v_mfma_f32_16x16x32_bf16 v[40:43], a[16:19], v[84:87], 0\nv_mul_f32 v62, s59, v62\nv_fma_f32 v52, v52, s58, v62\nv_fma_f32 v53, v53, s58, v62\nv_mfma_f32_16x16x32_bf16 v[44:47], a[24:27], v[84:87], 0\ns_waitcnt lgkmcnt(3)\nv_exp_f32 v52, v52\nv_fma_f32 v54, v54, s58, v62\n" \
"v_mfma_f32_16x16x32_bf16 v[16:19], a[4:7], v[88:91], v[16:19]\nds_read_b128 v[84:87], v2 offset:4352\nv_exp_f32 v53, v53\nv_mfma_f32_16x16x32_bf16 v[20:23], a[12:15], v[88:91], v[20:23]\nds_read_b128 v[76:79], v1 offset:1024\nv_fma_f32 v55, v55, s58, v62\nv_add_f32 v63, v52, v53\nv_mfma_f32_16x16x32_bf16 v[24:27], a[20:23], v[88:91], v[24:27]\n" \
"v_exp_f32 v54, v54\nv_fma_f32 v56, v56, s58, v62\nv_mfma_f32_16x16x32_bf16 v[28:31], a[28:31], v[88:91], v[28:31]\ns_waitcnt lgkmcnt(4)\nv_exp_f32 v55, v55\nv_add_f32 v63, v63, v54\nv_mfma_f32_16x16x32_bf16 v[32:35], a[4:7], v[92:95], v[32:35]\nds_read_b128 v[88:91], v2 offset:6144\n" \
"v_fma_f32 v57, v57, s58, v62\nv_add_f32 v63, v63, v55\nv_mfma_f32_16x16x32_bf16 v[36:39], a[12:15], v[92:95], v[36:39]\nv_exp_f32 v56, v56\nv_fma_f32 v58, v58, s58, v62\nv_mfma_f32_16x16x32_bf16 v[40:43], a[20:23], v[92:95], v[40:43]\nv_exp_f32 v57, v57\nv_add_f32 v63, v63, v56\n" \
"v_mfma_f32_16x16x32_bf16 v[44:47], a[28:31], v[92:95], v[44:47]\ns_waitcnt lgkmcnt(3)\nv_fma_f32 v59, v59, s58, v62\nv_exp_f32 v58, v58\nv_mfma_f32_16x16x32_bf16 v[16:19], a[32:35], v[80:83], v[16:19]\nds_read_b128 v[92:95], v2 offset:6400\nv_add_f32 v63, v63, v57\nv_fma_f32 v48, v48, s58, v62\n" \
"v_mfma_f32_16x16x32_bf16 v[20:23], v[72:75], v[80:83], v[20:23]\nds_read_b128 v[64:67], v1 offset:2048\nv_exp_f32 v59, v59\nv_mfma_f32_16x16x32_bf16 v[24:27], a[40:43], v[80:83], v[24:27]\nv_add_f32 v63, v63, v58\nv_fma_f32 v49, v49, s58, v62\nv_add_f32 v63, v63, v59\nv_mfma_f32_16x16x32_bf16 v[28:31], a[48:51], v[80:83], v[28:31]\n" \
"s_waitcnt lgkmcnt(4)\nv_exp_f32 v48, v48\nv_mfma_f32_16x16x32_bf16 v[32:35], a[32:35], v[84:87], v[32:35]\nds_read_b128 v[80:83], v2 offset:8192\nv_exp_f32 v49, v49\nv_mfma_f32_16x16x32_bf16 v[36:39], v[72:75], v[84:87], v[36:39]\nv_add_f32 v63, v63, v48\nv_add_f32 v62, v63, v49\n" \
"v_add_f32 v50, v63, v49\nv_mfma_f32_16x16x32_bf16 v[40:43], a[40:43], v[84:87], v[40:43]\ns_nop 0\nv_permlane16_swap_b32 v62, v50\nv_add_f32 v63, v62, v50\nv_mfma_f32_16x16x32_bf16 v[44:47], a[48:51], v[84:87], v[44:47]\ns_waitcnt lgkmcnt(3)\nv_add_f32 v50, v62, v50\n" \
"v_mfma_f32_16x16x32_bf16 v[16:19], a[36:39], v[88:91], v[16:19]\nds_read_b128 v[84:87], v2 offset:8448\nv_permlane32_swap_b32 v63, v50\nv_mfma_f32_16x16x32_bf16 v[20:23], v[76:79], v[88:91], v[20:23]\nds_read_b128 v[68:71], v1 offset:3072\nv_add_f32 v63, v63, v50\nv_mfma_f32_16x16x32_bf16 v[24:27], a[44:47], v[88:91], v[24:27]\nv_rcp_f32 v63, v63\n" \
"v_mfma_f32_16x16x32_bf16 v[28:31], a[52:55], v[88:91], v[28:31]\ns_waitcnt lgkmcnt(4)\nv_mul_f32 v52, v52, v63\nv_mul_f32 v53, v53, v63\nv_mul_f32 v54, v54, v63\nv_mfma_f32_16x16x32_bf16 v[32:35], a[36:39], v[92:95], v[32:35]\nds_read_b128 v[88:91], v2 offset:10240\nv_mul_f32 v55, v55, v63\n" \
"v_mul_f32 v56, v56, v63\nv_mfma_f32_16x16x32_bf16 v[36:39], v[76:79], v[92:95], v[36:39]\nv_mul_f32 v57, v57, v63\nv_mul_f32 v58, v58, v63\nv_mul_f32 v59, v59, v63\nv_mfma_f32_16x16x32_bf16 v[40:43], a[44:47], v[92:95], v[40:43]\nv_mul_f32 v48, v48, v63\nv_mul_f32 v49, v49, v63\n" \
"v_cvt_pk_bf16_f32 v52, v52, v53\nv_mfma_f32_16x16x32_bf16 v[44:47], a[52:55], v[92:95], v[44:47]\ns_waitcnt lgkmcnt(3)\nv_cvt_pk_bf16_f32 v53, v54, v55\nv_cvt_pk_bf16_f32 v48, v48, v49\nv_cvt_pk_bf16_f32 v54, v56, v57\nv_mfma_f32_16x16x32_bf16 v[16:19], a[56:59], v[80:83], v[16:19]\nds_read_b128 v[92:95], v2 offset:10496\n" \
"v_cvt_pk_bf16_f32 v55, v58, v59\nv_bfi_b32 v52, s61, v52, v61\nv_mfma_f32_16x16x32_bf16 v[20:23], a[64:67], v[80:83], v[20:23]\nds_read_b128 v[72:75], v1 offset:4096\nv_bfi_b32 v53, s61, v53, v61\nv_bfi_b32 v54, s61, v54, v61\nv_mfma_f32_16x16x32_bf16 v[24:27], v[64:67], v[80:83], v[24:27]\nv_bfi_b32 v55, s61, v55, v61\n" \
"v_bfi_b32 v48, s61, v48, v61\nv_mfma_f32_16x16x32_bf16 v[28:31], a[72:75], v[80:83], v[28:31]\ns_waitcnt lgkmcnt(4)\nbuffer_store_dwordx4 v[52:55], v10, s[36:39], s55 offen sc1\nv_mfma_f32_16x16x32_bf16 v[32:35], a[56:59], v[84:87], v[32:35]\nds_read_b128 v[80:83], v2 offset:12288\nv_mfma_f32_16x16x32_bf16 v[36:39], a[64:67], v[84:87], v[36:39]\nbuffer_store_dword v48, v11, s[36:39], s55 offen sc1\n" \
"v_cmp_ne_u32 vcc, 0, v60\nv_mfma_f32_16x16x32_bf16 v[40:43], v[64:67], v[84:87], v[40:43]\nv_mfma_f32_16x16x32_bf16 v[44:47], a[72:75], v[84:87], v[44:47]\ns_waitcnt lgkmcnt(3)\nv_mfma_f32_16x16x32_bf16 v[16:19], a[60:63], v[88:91], v[16:19]\nds_read_b128 v[84:87], v2 offset:12544\nv_mfma_f32_16x16x32_bf16 v[20:23], a[68:71], v[88:91], v[20:23]\nds_read_b128 v[76:79], v1 offset:5120\n" \
"v_mfma_f32_16x16x32_bf16 v[24:27], v[68:71], v[88:91], v[24:27]\nv_mfma_f32_16x16x32_bf16 v[28:31], a[76:79], v[88:91], v[28:31]\ns_waitcnt lgkmcnt(4)\nv_mfma_f32_16x16x32_bf16 v[32:35], a[60:63], v[92:95], v[32:35]\nds_read_b128 v[88:91], v2 offset:14336\nv_mfma_f32_16x16x32_bf16 v[36:39], a[68:71], v[92:95], v[36:39]\nv_mfma_f32_16x16x32_bf16 v[40:43], v[68:71], v[92:95], v[40:43]\nv_mfma_f32_16x16x32_bf16 v[44:47], a[76:79], v[92:95], v[44:47]\n" \
"s_cbranch_vccnz .Lqk_pj%=\ns_cmp_lt_i32 s40, 63\ns_waitcnt lgkmcnt(0)\ns_cbranch_scc1 .Lqk_pwait%=\ns_branch .Lqk_end%=\n.Lqk_re2%=:\nv_max_u32 v17, v80, v82\nv_cmp_eq_u32 vcc, -1, v17\n" \
"s_cbranch_vccz .Lqk_sk2405%=\ns_mov_b64 exec, vcc\nbuffer_load_dwordx4 v[80:83], v3, s[36:39], s42 offen offset:0 sc1\ns_mov_b64 exec, -1\n.Lqk_sk2405%=:\nv_max_u32 v17, v84, v86\nv_cmp_eq_u32 vcc, -1, v17\ns_cbranch_vccz .Lqk_sk2412%=\n" \
"s_mov_b64 exec, vcc\nbuffer_load_dwordx4 v[84:87], v3, s[36:39], s42 offen offset:1024 sc1\ns_mov_b64 exec, -1\n.Lqk_sk2412%=:\nv_max_u32 v17, v88, v90\nv_cmp_eq_u32 vcc, -1, v17\ns_cbranch_vccz .Lqk_sk2419%=\ns_mov_b64 exec, vcc\n" \
"buffer_load_dwordx4 v[88:91], v3, s[36:39], s42 offen offset:2048 sc1\ns_mov_b64 exec, -1\n.Lqk_sk2419%=:\nv_max_u32 v17, v92, v94\nv_cmp_eq_u32 vcc, -1, v17\ns_cbranch_vccz .Lqk_sk2426%=\ns_mov_b64 exec, vcc\nbuffer_load_dwordx4 v[92:95], v3, s[36:39], s42 offen offset:3072 sc1\n" \
"s_mov_b64 exec, -1\n.Lqk_sk2426%=:\nv_mov_b64 v[48:49], 0\nv_mov_b64 v[50:51], 0\nv_mov_b64 v[52:53], 0\nv_mov_b64 v[54:55], 0\nv_mov_b64 v[56:57], 0\nv_mov_b64 v[58:59], 0\n" \
"v_mov_b64 v[60:61], 0\nv_mov_b64 v[62:63], 0\ns_add_u32 s43, s43, 1\ns_cmp_lt_u32 s43, 262144\ns_cbranch_scc1 .Lqk_poll2%=\ns_branch .Lqk_abort%=\n.Lqk_c3%=:\ns_waitcnt lgkmcnt(3)\n" \
"v_mfma_f32_16x16x32_bf16 v[32:35], v[232:235], v[56:59], v[32:35]\nv_mov_b64 v[52:53], 0\nv_mov_b64 v[54:55], 0\nv_mfma_f32_16x16x32_bf16 v[36:39], v[240:243], v[56:59], v[36:39]\ns_add_u32 s44, s40, 1\ns_lshl_b32 s44, s44, 17\ns_waitcnt lgkmcnt(2)\nds_read_b128 v[64:67], v1 offset:18432\n" \
"v_mfma_f32_16x16x32_bf16 v[32:35], v[236:239], v[60:63], v[32:35]\ns_add_u32 s44, s44, %[xn]\ns_lshr_b32 s45, s44, 8\nds_read_b128 v[68:71], v1 offset:19456\nv_mfma_f32_16x16x32_bf16 v[36:39], v[244:247], v[60:63], v[36:39]\ns_and_b32 s45, s45, 0x2000\ns_xor_b32 s41, s44, s45\nv_mfma_f32_16x16x32_bf16 v[40:43], v[248:251], v[56:59], v[40:43]\n" \
"buffer_load_dword v16, v12, s[36:39], s56 offen sc1\nv_mfma_f32_16x16x32_bf16 v[44:47], v[72:75], v[56:59], v[44:47]\nv_cvt_pk_bf16_f32 v36, v32, v36\nv_cvt_pk_bf16_f32 v37, v33, v37\nv_mfma_f32_16x16x32_bf16 v[40:43], v[252:255], v[60:63], v[40:43]\nv_cvt_pk_bf16_f32 v38, v34, v38\nv_cvt_pk_bf16_f32 v39, v35, v39\nv_mfma_f32_16x16x32_bf16 v[44:47], v[76:79], v[60:63], v[44:47]\n" \
"ds_write_b128 v5, v[36:39] offset:256\nv_mov_b64 v[56:57], 0\nv_mov_b64 v[58:59], 0\nv_mov_b64 v[60:61], 0\nv_mov_b64 v[62:63], 0\n.Lqk_poll3%=:\ns_waitcnt vmcnt(1)\nv_smfmac_f32_16x16x64_bf16 v[48:51], v[80:83], v[152:159], v4\n" \
"ds_write_b128 v0, v[80:83] offset:28672\nv_max3_u32 v17, v80, v82, v84\nv_smfmac_f32_16x16x64_bf16 v[52:55], v[80:83], v[160:167], v4\nds_read_b128 v[72:75], v1 offset:20480\nds_read_b128 v[76:79], v1 offset:21504\nv_max3_u32 v17, v17, v86, v88\nv_smfmac_f32_16x16x64_bf16 v[56:59], v[80:83], v[168:175], v4\nds_write_b128 v0, v[84:87] offset:29696\n" \
"v_max3_u32 v17, v17, v90, v92\nv_max_u32 v17, v17, v94\nv_smfmac_f32_16x16x64_bf16 v[60:63], v[80:83], v[176:183], v4\nv_cmp_eq_u32 vcc, -1, v17\nv_smfmac_f32_16x16x64_bf16 v[48:51], v[84:87], v[184:191], v4\nds_write_b128 v0, v[88:91] offset:30720\ns_cbranch_vccnz .Lqk_re3%=\ns_waitcnt lgkmcnt(6)\n" \
"v_smfmac_f32_16x16x64_bf16 v[52:55], v[84:87], v[64:71], v4\nds_read_b128 v[64:67], v1 offset:22528\nds_read_b128 v[68:71], v1 offset:23552\nv_smfmac_f32_16x16x64_bf16 v[56:59], v[84:87], v[192:199], v4\nds_write_b128 v0, v[92:95] offset:31744\nv_smfmac_f32_16x16x64_bf16 v[60:63], v[84:87], v[200:207], v4\nv_smfmac_f32_16x16x64_bf16 v[48:51], v[88:91], v[208:215], v4\nv_smfmac_f32_16x16x64_bf16 v[52:55], v[88:91], v[216:223], v4\n" \
"s_waitcnt lgkmcnt(0)\ns_barrier\nds_read_b128 v[80:83], v0 offset:16384\nds_read_b128 v[84:87], v0 offset:17408\nv_smfmac_f32_16x16x64_bf16 v[56:59], v[88:91], v[72:79], v4\ns_waitcnt vmcnt(0)\nds_read_b128 v[72:75], v1 offset:0\nds_read_b128 v[76:79], v1 offset:1024\n" \
"v_cvt_pk_bf16_f32 v44, v40, v44\nv_cmp_ge_i32 vcc, s40, v16\nv_smfmac_f32_16x16x64_bf16 v[60:63], v[88:91], v[224:231], v4\ns_cmp_eq_u64 vcc, 0\ns_cselect_b32 s61, s41, -2147483648\ns_cselect_b32 s44, 1, 0\nds_read_b128 v[88:91], v0 offset:18432\nv_cvt_pk_bf16_f32 v45, v41, v45\n" \
"v_smfmac_f32_16x16x64_bf16 v[48:51], v[92:95], v[232:239], v4\ns_add_u32 m0, %[l0], 0\ns_add_u32 s60, s61, 0\nbuffer_load_dwordx4 v0, s[36:39], s60 offen offset:0 sc1 lds\nv_cvt_pk_bf16_f32 v46, v42, v46\nv_smfmac_f32_16x16x64_bf16 v[52:55], v[92:95], v[240:247], v4\nv_cvt_pk_bf16_f32 v47, v43, v47\nv_mov_b32 v16, s44\n" \
"v_smfmac_f32_16x16x64_bf16 v[56:59], v[92:95], v[248:255], v4\nbuffer_load_dwordx4 v0, s[36:39], s60 offen offset:1024 sc1 lds\nv_smfmac_f32_16x16x64_bf16 v[60:63], v[92:95], v[64:71], v4\ns_waitcnt lgkmcnt(4)\nds_write_b128 v5, v[44:47] offset:768\nds_read_b128 v[92:95], v0 offset:19456\nds_read_b128 v[64:67], v1 offset:2048\nds_read_b128 v[68:71], v1 offset:3072\n" \
"ds_write_b32 v0, v16 offset:53760\nv_smfmac_f32_16x16x64_bf16 v[48:51], v[80:83], a[0:7], v4\nbuffer_load_dwordx4 v0, s[36:39], s60 offen offset:2048 sc1 lds\nv_smfmac_f32_16x16x64_bf16 v[52:55], v[80:83], a[8:15], v4\nv_smfmac_f32_16x16x64_bf16 v[56:59], v[80:83], a[16:23], v4\nbuffer_load_dwordx4 v0, s[36:39], s60 offen offset:3072 sc1 lds\nv_smfmac_f32_16x16x64_bf16 v[60:63], v[80:83], a[24:31], v4\ns_waitcnt lgkmcnt(8)\n" \
"ds_read_b128 v[80:83], v0 offset:20480\nv_smfmac_f32_16x16x64_bf16 v[48:51], v[84:87], a[32:39], v4\ns_add_u32 m0, %[l0], 4096\ns_add_u32 s60, s61, 4096\ns_waitcnt lgkmcnt(7)\nbuffer_load_dwordx4 v0, s[36:39], s60 offen offset:0 sc1 lds\nv_smfmac_f32_16x16x64_bf16 v[52:55], v[84:87], v[72:79], v4\nds_read_b128 v[72:75], v1 offset:4096\n" \
"ds_read_b128 v[76:79], v1 offset:5120\nv_smfmac_f32_16x16x64_bf16 v[56:59], v[84:87], a[40:47], v4\nbuffer_load_dwordx4 v0, s[36:39], s60 offen offset:1024 sc1 lds\nv_smfmac_f32_16x16x64_bf16 v[60:63], v[84:87], a[48:55], v4\ns_waitcnt lgkmcnt(8)\nds_read_b128 v[84:87], v0 offset:21504\nv_smfmac_f32_16x16x64_bf16 v[48:51], v[88:91], a[56:63], v4\ns_waitcnt lgkmcnt(8)\n" \
"s_barrier\nbuffer_load_dwordx4 v0, s[36:39], s60 offen offset:2048 sc1 lds\nv_smfmac_f32_16x16x64_bf16 v[52:55], v[88:91], a[64:71], v4\ns_waitcnt lgkmcnt(5)\nv_smfmac_f32_16x16x64_bf16 v[56:59], v[88:91], v[64:71], v4\nds_read_b128 v[64:67], v1 offset:6144\nds_read_b128 v[68:71], v1 offset:7168\nbuffer_load_dwordx4 v0, s[36:39], s60 offen offset:3072 sc1 lds\n" \
"v_smfmac_f32_16x16x64_bf16 v[60:63], v[88:91], a[72:79], v4\nds_read_b128 v[88:91], v0 offset:22528\nv_smfmac_f32_16x16x64_bf16 v[48:51], v[92:95], a[80:87], v4\ns_add_u32 m0, %[l0], 8192\ns_add_u32 s60, s61, 16384\nbuffer_load_dwordx4 v0, s[36:39], s60 offen offset:0 sc1 lds\nv_smfmac_f32_16x16x64_bf16 v[52:55], v[92:95], a[88:95], v4\nv_smfmac_f32_16x16x64_bf16 v[56:59], v[92:95], a[96:103], v4\n" \
"s_waitcnt lgkmcnt(4)\nbuffer_load_dwordx4 v0, s[36:39], s60 offen offset:1024 sc1 lds\nv_smfmac_f32_16x16x64_bf16 v[60:63], v[92:95], v[72:79], v4\nds_read_b128 v[92:95], v0 offset:23552\nds_read_b128 v[72:75], v1 offset:8192\nds_read_b128 v[76:79], v1 offset:9216\nv_smfmac_f32_16x16x64_bf16 v[48:51], v[80:83], a[104:111], v4\nbuffer_load_dwordx4 v0, s[36:39], s60 offen offset:2048 sc1 lds\n" \
"v_smfmac_f32_16x16x64_bf16 v[52:55], v[80:83], a[112:119], v4\nv_smfmac_f32_16x16x64_bf16 v[56:59], v[80:83], a[120:127], v4\nbuffer_load_dwordx4 v0, s[36:39], s60 offen offset:3072 sc1 lds\nv_smfmac_f32_16x16x64_bf16 v[60:63], v[80:83], a[128:135], v4\ns_waitcnt lgkmcnt(6)\nds_read_b128 v[80:83], v0 offset:24576\nv_smfmac_f32_16x16x64_bf16 v[48:51], v[84:87], a[136:143], v4\ns_add_u32 m0, %[l0], 12288\n" \
"s_add_u32 s60, s61, 20480\ns_waitcnt lgkmcnt(5)\nbuffer_load_dwordx4 v0, s[36:39], s60 offen offset:0 sc1 lds\nv_smfmac_f32_16x16x64_bf16 v[52:55], v[84:87], v[64:71], v4\nds_read_b128 v[64:67], v1 offset:10240\nds_read_b128 v[68:71], v1 offset:11264\nv_smfmac_f32_16x16x64_bf16 v[56:59], v[84:87], a[144:151], v4\nbuffer_load_dwordx4 v0, s[36:39], s60 offen offset:1024 sc1 lds\n" \
"v_smfmac_f32_16x16x64_bf16 v[60:63], v[84:87], a[152:159], v4\ns_waitcnt lgkmcnt(6)\nds_read_b128 v[84:87], v0 offset:25600\nv_smfmac_f32_16x16x64_bf16 v[48:51], v[88:91], a[160:167], v4\nbuffer_load_dwordx4 v0, s[36:39], s60 offen offset:2048 sc1 lds\nv_smfmac_f32_16x16x64_bf16 v[52:55], v[88:91], a[168:175], v4\ns_waitcnt lgkmcnt(4)\nv_smfmac_f32_16x16x64_bf16 v[56:59], v[88:91], v[72:79], v4\n" \
"ds_read_b128 v[72:75], v1 offset:12288\nds_read_b128 v[76:79], v1 offset:13312\nbuffer_load_dwordx4 v0, s[36:39], s60 offen offset:3072 sc1 lds\nv_smfmac_f32_16x16x64_bf16 v[60:63], v[88:91], a[176:183], v4\nds_read_b128 v[88:91], v0 offset:26624\nv_smfmac_f32_16x16x64_bf16 v[48:51], v[92:95], a[184:191], v4\nv_smfmac_f32_16x16x64_bf16 v[52:55], v[92:95], a[192:199], v4\nv_smfmac_f32_16x16x64_bf16 v[56:59], v[92:95], a[200:207], v4\n" \
"s_waitcnt lgkmcnt(4)\nv_smfmac_f32_16x16x64_bf16 v[60:63], v[92:95], v[64:71], v4\nds_read_b128 v[92:95], v0 offset:27648\nds_read_b128 v[64:67], v1 offset:14336\nds_read_b128 v[68:71], v1 offset:15360\nv_smfmac_f32_16x16x64_bf16 v[48:51], v[80:83], a[208:215], v4\nv_smfmac_f32_16x16x64_bf16 v[52:55], v[80:83], a[216:223], v4\nv_smfmac_f32_16x16x64_bf16 v[56:59], v[80:83], a[224:231], v4\n" \
"v_smfmac_f32_16x16x64_bf16 v[60:63], v[80:83], a[232:239], v4\ns_waitcnt lgkmcnt(6)\nv_smfmac_f32_16x16x64_bf16 v[48:51], v[84:87], a[240:247], v4\ns_waitcnt lgkmcnt(4)\nv_smfmac_f32_16x16x64_bf16 v[52:55], v[84:87], v[72:79], v4\nds_read_b128 v[72:75], v1 offset:16384\nds_read_b128 v[76:79], v1 offset:17408\nv_smfmac_f32_16x16x64_bf16 v[56:59], v[84:87], a[248:255], v4\n" \
"v_smfmac_f32_16x16x64_bf16 v[60:63], v[84:87], v[96:103], v4\ns_waitcnt lgkmcnt(5)\nv_smfmac_f32_16x16x64_bf16 v[48:51], v[88:91], v[104:111], v4\nv_smfmac_f32_16x16x64_bf16 v[52:55], v[88:91], v[112:119], v4\ns_waitcnt lgkmcnt(4)\nv_smfmac_f32_16x16x64_bf16 v[48:51], v[92:95], v[128:135], v4\nv_smfmac_f32_16x16x64_bf16 v[52:55], v[92:95], v[136:143], v4\ns_waitcnt lgkmcnt(2)\n" \
"v_smfmac_f32_16x16x64_bf16 v[56:59], v[88:91], v[64:71], v4\nv_add_f32 v48, v48, v50\nv_smfmac_f32_16x16x64_bf16 v[60:63], v[88:91], v[120:127], v4\nv_add_f32 v52, v52, v54\nv_add_f32 v49, v49, v51\nv_smfmac_f32_16x16x64_bf16 v[56:59], v[92:95], v[144:151], v4\ns_waitcnt lgkmcnt(0)\nv_add_f32 v53, v53, v55\n" \
"v_cvt_pk_bf16_f32 v50, v48, v52\nv_smfmac_f32_16x16x64_bf16 v[60:63], v[92:95], v[72:79], v4\nv_cvt_pk_bf16_f32 v51, v49, v53\nds_write2_b32 v6, v50, v51 offset0:0 offset1:4\ns_nop 1\nv_add_f32 v56, v56, v58\nv_add_f32 v57, v57, v59\ns_nop 1\n" \
"v_add_f32 v60, v60, v62\nv_add_f32 v61, v61, v63\nv_cvt_pk_bf16_f32 v62, v56, v60\nv_cvt_pk_bf16_f32 v63, v57, v61\nds_write2_b32 v6, v62, v63 offset0:36 offset1:40\ns_waitcnt vmcnt(0)\ns_waitcnt lgkmcnt(0)\ns_barrier\n" \
"s_cbranch_vccnz .Lqk_slow%=\nds_read_b128 v[80:83], v2 offset:0\nds_read_b128 v[84:87], v2 offset:256\nds_read_b128 v[88:91], v2 offset:2048\ns_add_u32 m0, %[l0], 16384\ns_add_u32 s60, s41, 32768\nbuffer_load_dwordx4 v0, s[36:39], s60 offen offset:0 sc1 lds\nbuffer_load_dwordx4 v0, s[36:39], s60 offen offset:1024 sc1 lds\n" \
"buffer_load_dwordx4 v0, s[36:39], s60 offen offset:2048 sc1 lds\nbuffer_load_dwordx4 v0, s[36:39], s60 offen offset:3072 sc1 lds\ns_add_u32 m0, %[l0], 20480\ns_add_u32 s60, s41, 36864\nbuffer_load_dwordx4 v0, s[36:39], s60 offen offset:0 sc1 lds\nbuffer_load_dwordx4 v0, s[36:39], s60 offen offset:1024 sc1 lds\nbuffer_load_dwordx4 v0, s[36:39], s60 offen offset:2048 sc1 lds\nbuffer_load_dwordx4 v0, s[36:39], s60 offen offset:3072 sc1 lds\n" \
"s_add_u32 m0, %[l0], 24576\ns_add_u32 s60, s41, 49152\nbuffer_load_dwordx4 v0, s[36:39], s60 offen offset:0 sc1 lds\nbuffer_load_dwordx4 v0, s[36:39], s60 offen offset:1024 sc1 lds\nbuffer_load_dwordx4 v0, s[36:39], s60 offen offset:2048 sc1 lds\nbuffer_load_dwordx4 v0, s[36:39], s60 offen offset:3072 sc1 lds\ns_add_u32 m0, %[l0], 28672\ns_add_u32 s60, s41, 53248\n" \
"buffer_load_dwordx4 v0, s[36:39], s60 offen offset:0 sc1 lds\nbuffer_load_dwordx4 v0, s[36:39], s60 offen offset:1024 sc1 lds\nbuffer_load_dwordx4 v0, s[36:39], s60 offen offset:2048 sc1 lds\nbuffer_load_dwordx4 v0, s[36:39], s60 offen offset:3072 sc1 lds\ns_waitcnt lgkmcnt(2)\nv_mfma_f32_16x16x32_bf16 v[16:19], a[0:3], v[80:83], 0\nds_read_b128 v[92:95], v2 offset:2304\nv_mfma_f32_16x16x32_bf16 v[20:23], a[8:11], v[80:83], 0\n" \
"ds_read_b128 v[72:75], v1 offset:0\nv_mfma_f32_16x16x32_bf16 v[24:27], a[16:19], v[80:83], 0\nv_mfma_f32_16x16x32_bf16 v[28:31], a[24:27], v[80:83], 0\ns_waitcnt lgkmcnt(3)\nv_mfma_f32_16x16x32_bf16 v[32:35], a[0:3], v[84:87], 0\nds_read_b128 v[80:83], v2 offset:4096\nv_mfma_f32_16x16x32_bf16 v[36:39], a[8:11], v[84:87], 0\nv_mfma_f32_16x16x32_bf16 v[40:43], a[16:19], v[84:87], 0\n" \
"v_mfma_f32_16x16x32_bf16 v[44:47], a[24:27], v[84:87], 0\ns_waitcnt lgkmcnt(3)\nv_mfma_f32_16x16x32_bf16 v[16:19], a[4:7], v[88:91], v[16:19]\nds_read_b128 v[84:87], v2 offset:4352\nv_mfma_f32_16x16x32_bf16 v[20:23], a[12:15], v[88:91], v[20:23]\nds_read_b128 v[76:79], v1 offset:1024\nv_mfma_f32_16x16x32_bf16 v[24:27], a[20:23], v[88:91], v[24:27]\nv_mfma_f32_16x16x32_bf16 v[28:31], a[28:31], v[88:91], v[28:31]\n" \
"s_waitcnt lgkmcnt(4)\nv_mfma_f32_16x16x32_bf16 v[32:35], a[4:7], v[92:95], v[32:35]\nds_read_b128 v[88:91], v2 offset:6144\nv_mfma_f32_16x16x32_bf16 v[36:39], a[12:15], v[92:95], v[36:39]\nv_mfma_f32_16x16x32_bf16 v[40:43], a[20:23], v[92:95], v[40:43]\nv_mfma_f32_16x16x32_bf16 v[44:47], a[28:31], v[92:95], v[44:47]\ns_waitcnt lgkmcnt(3)\nv_mfma_f32_16x16x32_bf16 v[16:19], a[32:35], v[80:83], v[16:19]\n" \
"ds_read_b128 v[92:95], v2 offset:6400\nv_mfma_f32_16x16x32_bf16 v[20:23], v[72:75], v[80:83], v[20:23]\nds_read_b128 v[64:67], v1 offset:2048\nv_mfma_f32_16x16x32_bf16 v[24:27], a[40:43], v[80:83], v[24:27]\nv_mfma_f32_16x16x32_bf16 v[28:31], a[48:51], v[80:83], v[28:31]\ns_waitcnt lgkmcnt(4)\nv_mfma_f32_16x16x32_bf16 v[32:35], a[32:35], v[84:87], v[32:35]\nds_read_b128 v[80:83], v2 offset:8192\n" \
"v_mfma_f32_16x16x32_bf16 v[36:39], v[72:75], v[84:87], v[36:39]\nv_mfma_f32_16x16x32_bf16 v[40:43], a[40:43], v[84:87], v[40:43]\nv_mfma_f32_16x16x32_bf16 v[44:47], a[48:51], v[84:87], v[44:47]\ns_waitcnt lgkmcnt(3)\nv_mfma_f32_16x16x32_bf16 v[16:19], a[36:39], v[88:91], v[16:19]\nds_read_b128 v[84:87], v2 offset:8448\nv_mfma_f32_16x16x32_bf16 v[20:23], v[76:79], v[88:91], v[20:23]\nds_read_b128 v[68:71], v1 offset:3072\n" \
"v_mfma_f32_16x16x32_bf16 v[24:27], a[44:47], v[88:91], v[24:27]\nv_mfma_f32_16x16x32_bf16 v[28:31], a[52:55], v[88:91], v[28:31]\ns_waitcnt lgkmcnt(4)\nv_mfma_f32_16x16x32_bf16 v[32:35], a[36:39], v[92:95], v[32:35]\nds_read_b128 v[88:91], v2 offset:10240\nv_mfma_f32_16x16x32_bf16 v[36:39], v[76:79], v[92:95], v[36:39]\nv_mfma_f32_16x16x32_bf16 v[40:43], a[44:47], v[92:95], v[40:43]\nv_mfma_f32_16x16x32_bf16 v[44:47], a[52:55], v[92:95], v[44:47]\n" \
"s_waitcnt lgkmcnt(3)\nv_mfma_f32_16x16x32_bf16 v[16:19], a[56:59], v[80:83], v[16:19]\nds_read_b128 v[92:95], v2 offset:10496\nv_mfma_f32_16x16x32_bf16 v[20:23], a[64:67], v[80:83], v[20:23]\nds_read_b128 v[72:75], v1 offset:4096\nv_mfma_f32_16x16x32_bf16 v[24:27], v[64:67], v[80:83], v[24:27]\nv_mfma_f32_16x16x32_bf16 v[28:31], a[72:75], v[80:83], v[28:31]\ns_waitcnt lgkmcnt(4)\n" \
"v_mfma_f32_16x16x32_bf16 v[32:35], a[56:59], v[84:87], v[32:35]\nds_read_b128 v[80:83], v2 offset:12288\nv_mfma_f32_16x16x32_bf16 v[36:39], a[64:67], v[84:87], v[36:39]\nv_mfma_f32_16x16x32_bf16 v[40:43], v[64:67], v[84:87], v[40:43]\nv_mfma_f32_16x16x32_bf16 v[44:47], a[72:75], v[84:87], v[44:47]\ns_waitcnt lgkmcnt(3)\nv_mfma_f32_16x16x32_bf16 v[16:19], a[60:63], v[88:91], v[16:19]\nds_read_b128 v[84:87], v2 offset:12544\n" \
"v_mfma_f32_16x16x32_bf16 v[20:23], a[68:71], v[88:91], v[20:23]\nds_read_b128 v[76:79], v1 offset:5120\nv_mfma_f32_16x16x32_bf16 v[24:27], v[68:71], v[88:91], v[24:27]\nv_mfma_f32_16x16x32_bf16 v[28:31], a[76:79], v[88:91], v[28:31]\ns_waitcnt lgkmcnt(4)\nv_mfma_f32_16x16x32_bf16 v[32:35], a[60:63], v[92:95], v[32:35]\nds_read_b128 v[88:91], v2 offset:14336\nv_mfma_f32_16x16x32_bf16 v[36:39], a[68:71], v[92:95], v[36:39]\n" \
"v_mfma_f32_16x16x32_bf16 v[40:43], v[68:71], v[92:95], v[40:43]\nv_mfma_f32_16x16x32_bf16 v[44:47], a[76:79], v[92:95], v[44:47]\ns_waitcnt vmcnt(0)\ns_branch .Lqk_pj%=\n.Lqk_slow%=:\ns_cmp_ge_i32 s40, 63\ns_cbranch_scc1 .Lqk_end%=\n.Lqk_fetch0%=:\n" \
"buffer_load_dword v16, v12, s[36:39], s56 offen sc1\ns_waitcnt vmcnt(0)\nv_cmp_ge_i32 vcc, s40, v16\ns_cbranch_vccz .Lqk_fok%=\ns_add_u32 s43, s43, 1\ns_cmp_lt_u32 s43, 262144\ns_cbranch_scc1 .Lqk_fetch0%=\ns_branch .Lqk_abort%=\n" \
".Lqk_fok%=:\ns_add_u32 s44, s40, 1\ns_lshl_b32 s44, s44, 17\ns_add_u32 s44, s44, %[xn]\ns_lshr_b32 s45, s44, 8\ns_and_b32 s45, s45, 0x2000\ns_xor_b32 s41, s44, s45\ns_add_u32 m0, %[l0], 0\n" \
"s_add_u32 s60, s41, 0\nbuffer_load_dwordx4 v0, s[36:39], s60 offen offset:0 sc1 lds\nbuffer_load_dwordx4 v0, s[36:39], s60 offen offset:1024 sc1 lds\nbuffer_load_dwordx4 v0, s[36:39], s60 offen offset:2048 sc1 lds\nbuffer_load_dwordx4 v0, s[36:39], s60 offen offset:3072 sc1 lds\ns_add_u32 m0, %[l0], 4096\ns_add_u32 s60, s41, 4096\nbuffer_load_dwordx4 v0, s[36:39], s60 offen offset:0 sc1 lds\n" \
"buffer_load_dwordx4 v0, s[36:39], s60 offen offset:1024 sc1 lds\nbuffer_load_dwordx4 v0, s[36:39], s60 offen offset:2048 sc1 lds\nbuffer_load_dwordx4 v0, s[36:39], s60 offen offset:3072 sc1 lds\ns_add_u32 m0, %[l0], 8192\ns_add_u32 s60, s41, 16384\nbuffer_load_dwordx4 v0, s[36:39], s60 offen offset:0 sc1 lds\nbuffer_load_dwordx4 v0, s[36:39], s60 offen offset:1024 sc1 lds\nbuffer_load_dwordx4 v0, s[36:39], s60 offen offset:2048 sc1 lds\n" \
"buffer_load_dwordx4 v0, s[36:39], s60 offen offset:3072 sc1 lds\ns_add_u32 m0, %[l0], 12288\ns_add_u32 s60, s41, 20480\nbuffer_load_dwordx4 v0, s[36:39], s60 offen offset:0 sc1 lds\nbuffer_load_dwordx4 v0, s[36:39], s60 offen offset:1024 sc1 lds\nbuffer_load_dwordx4 v0, s[36:39], s60 offen offset:2048 sc1 lds\nbuffer_load_dwordx4 v0, s[36:39], s60 offen offset:3072 sc1 lds\ns_add_u32 m0, %[l0], 16384\n" \
"s_add_u32 s60, s41, 32768\nbuffer_load_dwordx4 v0, s[36:39], s60 offen offset:0 sc1 lds\nbuffer_load_dwordx4 v0, s[36:39], s60 offen offset:1024 sc1 lds\nbuffer_load_dwordx4 v0, s[36:39], s60 offen offset:2048 sc1 lds\nbuffer_load_dwordx4 v0, s[36:39], s60 offen offset:3072 sc1 lds\ns_add_u32 m0, %[l0], 20480\ns_add_u32 s60, s41, 36864\nbuffer_load_dwordx4 v0, s[36:39], s60 offen offset:0 sc1 lds\n" \
"buffer_load_dwordx4 v0, s[36:39], s60 offen offset:1024 sc1 lds\nbuffer_load_dwordx4 v0, s[36:39], s60 offen offset:2048 sc1 lds\nbuffer_load_dwordx4 v0, s[36:39], s60 offen offset:3072 sc1 lds\ns_add_u32 m0, %[l0], 24576\ns_add_u32 s60, s41, 49152\nbuffer_load_dwordx4 v0, s[36:39], s60 offen offset:0 sc1 lds\nbuffer_load_dwordx4 v0, s[36:39], s60 offen offset:1024 sc1 lds\nbuffer_load_dwordx4 v0, s[36:39], s60 offen offset:2048 sc1 lds\n" \
"buffer_load_dwordx4 v0, s[36:39], s60 offen offset:3072 sc1 lds\ns_add_u32 m0, %[l0], 28672\ns_add_u32 s60, s41, 53248\nbuffer_load_dwordx4 v0, s[36:39], s60 offen offset:0 sc1 lds\nbuffer_load_dwordx4 v0, s[36:39], s60 offen offset:1024 sc1 lds\nbuffer_load_dwordx4 v0, s[36:39], s60 offen offset:2048 sc1 lds\nbuffer_load_dwordx4 v0, s[36:39], s60 offen offset:3072 sc1 lds\ns_waitcnt vmcnt(0)\n" \
"s_branch .Lqk_pwait%=\n.Lqk_re3%=:\nv_max_u32 v17, v80, v82\nv_cmp_eq_u32 vcc, -1, v17\ns_cbranch_vccz .Lqk_sk2839%=\ns_mov_b64 exec, vcc\nbuffer_load_dwordx4 v[80:83], v3, s[36:39], s42 offen offset:0 sc1\ns_mov_b64 exec, -1\n" \
".Lqk_sk2839%=:\nv_max_u32 v17, v84, v86\nv_cmp_eq_u32 vcc, -1, v17\ns_cbranch_vccz .Lqk_sk2846%=\ns_mov_b64 exec, vcc\nbuffer_load_dwordx4 v[84:87], v3, s[36:39], s42 offen offset:1024 sc1\ns_mov_b64 exec, -1\n.Lqk_sk2846%=:\n" \
"v_max_u32 v17, v88, v90\nv_cmp_eq_u32 vcc, -1, v17\ns_cbranch_vccz .Lqk_sk2853%=\ns_mov_b64 exec, vcc\nbuffer_load_dwordx4 v[88:91], v3, s[36:39], s42 offen offset:2048 sc1\ns_mov_b64 exec, -1\n.Lqk_sk2853%=:\nv_max_u32 v17, v92, v94\n" \
"v_cmp_eq_u32 vcc, -1, v17\ns_cbranch_vccz .Lqk_sk2860%=\ns_mov_b64 exec, vcc\nbuffer_load_dwordx4 v[92:95], v3, s[36:39], s42 offen offset:3072 sc1\ns_mov_b64 exec, -1\n.Lqk_sk2860%=:\nbuffer_load_dword v16, v12, s[36:39], s56 offen sc1\nv_mov_b64 v[48:49], 0\n" \
"v_mov_b64 v[50:51], 0\nv_mov_b64 v[52:53], 0\nv_mov_b64 v[54:55], 0\nv_mov_b64 v[56:57], 0\nv_mov_b64 v[58:59], 0\nv_mov_b64 v[60:61], 0\nv_mov_b64 v[62:63], 0\ns_add_u32 s43, s43, 1\n" \
"s_cmp_lt_u32 s43, 262144\ns_cbranch_scc1 .Lqk_poll3%=\ns_branch .Lqk_abort%=\n.Lqk_abort%=:\ns_mov_b64 exec, -1\nv_mov_b32 v16, 0\ns_movk_i32 s44, 6400\nbuffer_store_dword v16, off, s[36:39], s44 sc1\n" \
".Lqk_end%=:\ns_waitcnt vmcnt(0) lgkmcnt(0)\n" \
""
#define VU_ASM \
"v_mbcnt_lo_u32_b32 v255, -1, 0\nv_mbcnt_hi_u32_b32 v255, -1, v255\nv_lshlrev_b32 v255, 2, v255\nv_add_u32 v255, %[tb], v255\nds_read_b32 v0, v255 offset:0\nds_read_b32 v1, v255 offset:256\nds_read_b32 v2, v255 offset:512\nds_read_b32 v3, v255 offset:768\n" \
"ds_read_b32 v4, v255 offset:1024\nds_read_b32 v5, v255 offset:1280\nds_read_b32 v6, v255 offset:1536\nds_read_b32 v7, v255 offset:1792\nds_read_b32 v8, v255 offset:2048\nds_read_b32 v9, v255 offset:2304\nds_read_b32 v10, v255 offset:2560\nds_read_b32 v11, v255 offset:2816\n" \
"ds_read_b32 v90, v255 offset:3072\nds_read_b32 v91, v255 offset:3328\nds_read_b32 v92, v255 offset:3584\nds_read_b32 v93, v255 offset:3840\nds_read_b32 v94, v255 offset:4096\nds_read_b32 v95, v255 offset:4352\ns_waitcnt lgkmcnt(0)\ns_mov_b64 s[36:37], %[ws]\n" \
"s_mov_b32 s38, %[sz]\ns_mov_b32 s39, 0x20000\ns_mov_b32 s45, 2096896\nbuffer_load_dword v15, off, s[36:39], s45 sc1\ns_add_u32 m0, %[lw], 16384\ns_add_u32 s44, %[wo], 0\nbuffer_load_dwordx4 v90, %[rw], s44 offen nt lds\ns_add_u32 m0, %[lw], 17408\n" \
"s_add_u32 s44, %[wo], 65536\nbuffer_load_dwordx4 v90, %[rw], s44 offen nt lds\ns_add_u32 m0, %[lw], 18432\ns_add_u32 s44, %[wo], 131072\nbuffer_load_dwordx4 v90, %[rw], s44 offen nt lds\ns_add_u32 m0, %[lw], 19456\ns_add_u32 s44, %[wo], 196608\nbuffer_load_dwordx4 v90, %[rw], s44 offen nt lds\n" \
"s_add_u32 m0, %[lw], 20480\ns_add_u32 s44, %[wo], 524288\nbuffer_load_dwordx4 v90, %[rw], s44 offen nt lds\ns_add_u32 m0, %[lw], 21504\ns_add_u32 s44, %[wo], 589824\nbuffer_load_dwordx4 v90, %[rw], s44 offen nt lds\ns_add_u32 m0, %[lw], 22528\ns_add_u32 s44, %[wo], 655360\n" \
"buffer_load_dwordx4 v90, %[rw], s44 offen nt lds\ns_add_u32 m0, %[lw], 23552\ns_add_u32 s44, %[wo], 720896\nbuffer_load_dwordx4 v90, %[rw], s44 offen nt lds\ns_add_u32 m0, %[tb], 0\ns_add_u32 s44, %[wo], 1048576\nbuffer_load_dwordx4 v90, %[rw], s44 offen nt lds\ns_add_u32 m0, %[tb], 1024\n" \
"s_add_u32 s44, %[wo], 1114112\nbuffer_load_dwordx4 v90, %[rw], s44 offen nt lds\ns_add_u32 m0, %[tb], 2048\ns_add_u32 s44, %[wo], 1179648\nbuffer_load_dwordx4 v90, %[rw], s44 offen nt lds\ns_add_u32 m0, %[tb], 3072\ns_add_u32 s44, %[wo], 1245184\nbuffer_load_dwordx4 v90, %[rw], s44 offen nt lds\n" \
"s_add_u32 m0, %[tb], 4096\ns_add_u32 s44, %[wo], 1572864\nbuffer_load_dwordx4 v90, %[rw], s44 offen nt lds\ns_add_u32 m0, %[tb], 5120\ns_add_u32 s44, %[wo], 1638400\nbuffer_load_dwordx4 v90, %[rw], s44 offen nt lds\ns_add_u32 m0, %[tb], 6144\ns_add_u32 s44, %[wo], 1703936\n" \
"buffer_load_dwordx4 v90, %[rw], s44 offen nt lds\ns_add_u32 m0, %[tb], 7168\ns_add_u32 s44, %[wo], 1769472\nbuffer_load_dwordx4 v90, %[rw], s44 offen nt lds\ns_add_u32 m0, %[tb], 8192\ns_add_u32 s44, %[wo], 2097152\nbuffer_load_dwordx4 v90, %[rw], s44 offen nt lds\ns_add_u32 m0, %[tb], 9216\n" \
"s_add_u32 s44, %[wo], 2162688\nbuffer_load_dwordx4 v90, %[rw], s44 offen nt lds\ns_add_u32 m0, %[tb], 10240\ns_add_u32 s44, %[wo], 2228224\nbuffer_load_dwordx4 v90, %[rw], s44 offen nt lds\ns_add_u32 m0, %[tb], 11264\ns_add_u32 s44, %[wo], 2293760\nbuffer_load_dwordx4 v90, %[rw], s44 offen nt lds\n" \
"s_add_u32 m0, %[lw], 0\ns_add_u32 s44, %[wo], 2621440\nbuffer_load_dwordx4 v90, %[rw], s44 offen nt lds\ns_add_u32 m0, %[lw], 1024\ns_add_u32 s44, %[wo], 2686976\nbuffer_load_dwordx4 v90, %[rw], s44 offen nt lds\ns_add_u32 m0, %[lw], 2048\ns_add_u32 s44, %[wo], 2752512\n" \
"buffer_load_dwordx4 v90, %[rw], s44 offen nt lds\ns_add_u32 m0, %[lw], 3072\ns_add_u32 s44, %[wo], 2818048\nbuffer_load_dwordx4 v90, %[rw], s44 offen nt lds\ns_add_u32 m0, %[lw], 4096\ns_add_u32 s44, %[wo], 3145728\nbuffer_load_dwordx4 v90, %[rw], s44 offen nt lds\ns_add_u32 m0, %[lw], 5120\n" \
"s_add_u32 s44, %[wo], 3211264\nbuffer_load_dwordx4 v90, %[rw], s44 offen nt lds\ns_add_u32 m0, %[lw], 6144\ns_add_u32 s44, %[wo], 3276800\nbuffer_load_dwordx4 v90, %[rw], s44 offen nt lds\ns_add_u32 m0, %[lw], 7168\ns_add_u32 s44, %[wo], 3342336\nbuffer_load_dwordx4 v90, %[rw], s44 offen nt lds\n" \
"s_add_u32 m0, %[lw], 8192\ns_add_u32 s44, %[wo], 3670016\nbuffer_load_dwordx4 v90, %[rw], s44 offen nt lds\ns_add_u32 m0, %[lw], 9216\ns_add_u32 s44, %[wo], 3735552\nbuffer_load_dwordx4 v90, %[rw], s44 offen nt lds\ns_add_u32 m0, %[lw], 10240\ns_add_u32 s44, %[wo], 3801088\n" \
"buffer_load_dwordx4 v90, %[rw], s44 offen nt lds\ns_add_u32 m0, %[lw], 11264\ns_add_u32 s44, %[wo], 3866624\nbuffer_load_dwordx4 v90, %[rw], s44 offen nt lds\ns_waitcnt vmcnt(28)\ns_barrier\ns_add_u32 m0, %[lw], 12288\ns_add_u32 s44, %[wo], 4194304\n" \
"buffer_load_dwordx4 v90, %[rw], s44 offen nt lds\ns_add_u32 m0, %[lw], 13312\ns_add_u32 s44, %[wo], 4259840\nbuffer_load_dwordx4 v90, %[rw], s44 offen nt lds\ns_add_u32 m0, %[lw], 14336\ns_add_u32 s44, %[wo], 4325376\nbuffer_load_dwordx4 v90, %[rw], s44 offen nt lds\ns_add_u32 m0, %[lw], 15360\n" \
"s_add_u32 s44, %[wo], 4390912\nbuffer_load_dwordx4 v90, %[rw], s44 offen nt lds\nds_read_b64_tr_b16 a[0:1], v93 offset:16384\nds_read_b64_tr_b16 a[2:3], v93 offset:16896\nds_read_b64_tr_b16 a[16:17], v93 offset:16392\nds_read_b64_tr_b16 a[18:19], v93 offset:16904\nds_read_b64_tr_b16 a[4:5], v93 offset:40960\nds_read_b64_tr_b16 a[6:7], v93 offset:41472\n" \
"ds_read_b64_tr_b16 a[20:21], v93 offset:40968\nds_read_b64_tr_b16 a[22:23], v93 offset:41480\ns_waitcnt lgkmcnt(0)\ns_waitcnt vmcnt(28)\ns_barrier\ns_add_u32 m0, %[lw], 16384\ns_add_u32 s44, %[wo], 4718592\nbuffer_load_dwordx4 v90, %[rw], s44 offen nt lds\n" \
"s_add_u32 m0, %[lw], 17408\ns_add_u32 s44, %[wo], 4784128\nbuffer_load_dwordx4 v90, %[rw], s44 offen nt lds\ns_add_u32 m0, %[lw], 18432\ns_add_u32 s44, %[wo], 4849664\nbuffer_load_dwordx4 v90, %[rw], s44 offen nt lds\ns_add_u32 m0, %[lw], 19456\ns_add_u32 s44, %[wo], 4915200\n" \
"buffer_load_dwordx4 v90, %[rw], s44 offen nt lds\nds_read_b64_tr_b16 a[24:25], v93 offset:20480\nds_read_b64_tr_b16 a[26:27], v93 offset:20992\nds_read_b64_tr_b16 a[40:41], v93 offset:20488\nds_read_b64_tr_b16 a[42:43], v93 offset:21000\nds_read_b64_tr_b16 a[28:29], v93 offset:45056\nds_read_b64_tr_b16 a[30:31], v93 offset:45568\nds_read_b64_tr_b16 a[44:45], v93 offset:45064\n" \
"ds_read_b64_tr_b16 a[46:47], v93 offset:45576\ns_waitcnt lgkmcnt(0)\ns_waitcnt vmcnt(28)\ns_barrier\ns_add_u32 m0, %[lw], 20480\ns_add_u32 s44, %[wo], 5242880\nbuffer_load_dwordx4 v90, %[rw], s44 offen nt lds\ns_add_u32 m0, %[lw], 21504\n" \
"s_add_u32 s44, %[wo], 5308416\nbuffer_load_dwordx4 v90, %[rw], s44 offen nt lds\ns_add_u32 m0, %[lw], 22528\ns_add_u32 s44, %[wo], 5373952\nbuffer_load_dwordx4 v90, %[rw], s44 offen nt lds\ns_add_u32 m0, %[lw], 23552\ns_add_u32 s44, %[wo], 5439488\nbuffer_load_dwordx4 v90, %[rw], s44 offen nt lds\n" \
"ds_read_b64_tr_b16 a[56:57], v92 offset:0\nds_read_b64_tr_b16 a[58:59], v92 offset:512\nds_read_b64_tr_b16 a[72:73], v92 offset:8\nds_read_b64_tr_b16 a[74:75], v92 offset:520\nds_read_b64_tr_b16 a[60:61], v92 offset:12288\nds_read_b64_tr_b16 a[62:63], v92 offset:12800\nds_read_b64_tr_b16 a[76:77], v92 offset:12296\nds_read_b64_tr_b16 a[78:79], v92 offset:12808\n" \
"s_waitcnt lgkmcnt(0)\ns_waitcnt vmcnt(28)\ns_barrier\ns_add_u32 m0, %[tb], 0\ns_add_u32 s44, %[wo], 5767168\nbuffer_load_dwordx4 v90, %[rw], s44 offen nt lds\ns_add_u32 m0, %[tb], 1024\ns_add_u32 s44, %[wo], 5832704\n" \
"buffer_load_dwordx4 v90, %[rw], s44 offen nt lds\ns_add_u32 m0, %[tb], 2048\ns_add_u32 s44, %[wo], 5898240\nbuffer_load_dwordx4 v90, %[rw], s44 offen nt lds\ns_add_u32 m0, %[tb], 3072\ns_add_u32 s44, %[wo], 5963776\nbuffer_load_dwordx4 v90, %[rw], s44 offen nt lds\nds_read_b64_tr_b16 v[84:85], v92 offset:4096\n" \
"ds_read_b64_tr_b16 v[86:87], v92 offset:4608\nds_read_b64_tr_b16 a[88:89], v92 offset:4104\nds_read_b64_tr_b16 a[90:91], v92 offset:4616\nds_read_b64_tr_b16 v[80:81], v92 offset:16384\nds_read_b64_tr_b16 v[82:83], v92 offset:16896\nds_read_b64_tr_b16 a[92:93], v92 offset:16392\nds_read_b64_tr_b16 a[94:95], v92 offset:16904\ns_waitcnt lgkmcnt(0)\n" \
"s_waitcnt vmcnt(28)\ns_barrier\ns_add_u32 m0, %[tb], 4096\ns_add_u32 s44, %[wo], 6291456\nbuffer_load_dwordx4 v90, %[rw], s44 offen nt lds\ns_add_u32 m0, %[tb], 5120\ns_add_u32 s44, %[wo], 6356992\nbuffer_load_dwordx4 v90, %[rw], s44 offen nt lds\n" \
"s_add_u32 m0, %[tb], 6144\ns_add_u32 s44, %[wo], 6422528\nbuffer_load_dwordx4 v90, %[rw], s44 offen nt lds\ns_add_u32 m0, %[tb], 7168\ns_add_u32 s44, %[wo], 6488064\nbuffer_load_dwordx4 v90, %[rw], s44 offen nt lds\nds_read_b64_tr_b16 a[104:105], v92 offset:8192\nds_read_b64_tr_b16 a[106:107], v92 offset:8704\n" \
"ds_read_b64_tr_b16 a[120:121], v92 offset:8200\nds_read_b64_tr_b16 a[122:123], v92 offset:8712\nds_read_b64_tr_b16 a[108:109], v92 offset:20480\nds_read_b64_tr_b16 a[110:111], v92 offset:20992\nds_read_b64_tr_b16 a[124:125], v92 offset:20488\nds_read_b64_tr_b16 a[126:127], v92 offset:21000\ns_waitcnt lgkmcnt(0)\ns_waitcnt vmcnt(28)\n" \
"s_barrier\ns_add_u32 m0, %[tb], 8192\ns_add_u32 s44, %[wo], 6815744\nbuffer_load_dwordx4 v90, %[rw], s44 offen nt lds\ns_add_u32 m0, %[tb], 9216\ns_add_u32 s44, %[wo], 6881280\nbuffer_load_dwordx4 v90, %[rw], s44 offen nt lds\ns_add_u32 m0, %[tb], 10240\n" \
"s_add_u32 s44, %[wo], 6946816\nbuffer_load_dwordx4 v90, %[rw], s44 offen nt lds\ns_add_u32 m0, %[tb], 11264\ns_add_u32 s44, %[wo], 7012352\nbuffer_load_dwordx4 v90, %[rw], s44 offen nt lds\nds_read_b64_tr_b16 a[128:129], v93 offset:0\nds_read_b64_tr_b16 a[130:131], v93 offset:512\nds_read_b64_tr_b16 a[144:145], v93 offset:8\n" \
"ds_read_b64_tr_b16 a[146:147], v93 offset:520\nds_read_b64_tr_b16 a[132:133], v93 offset:24576\nds_read_b64_tr_b16 a[134:135], v93 offset:25088\nds_read_b64_tr_b16 a[148:149], v93 offset:24584\nds_read_b64_tr_b16 a[150:151], v93 offset:25096\ns_waitcnt lgkmcnt(0)\ns_waitcnt vmcnt(28)\ns_barrier\n" \
"s_add_u32 m0, %[lw], 0\ns_add_u32 s44, %[wo], 7340032\nbuffer_load_dwordx4 v90, %[rw], s44 offen nt lds\ns_add_u32 m0, %[lw], 1024\ns_add_u32 s44, %[wo], 7405568\nbuffer_load_dwordx4 v90, %[rw], s44 offen nt lds\ns_add_u32 m0, %[lw], 2048\ns_add_u32 s44, %[wo], 7471104\n" \
"buffer_load_dwordx4 v90, %[rw], s44 offen nt lds\ns_add_u32 m0, %[lw], 3072\ns_add_u32 s44, %[wo], 7536640\nbuffer_load_dwordx4 v90, %[rw], s44 offen nt lds\nds_read_b64_tr_b16 a[160:161], v93 offset:4096\nds_read_b64_tr_b16 a[162:163], v93 offset:4608\nds_read_b64_tr_b16 v[76:77], v93 offset:4104\nds_read_b64_tr_b16 v[78:79], v93 offset:4616\n" \
"ds_read_b64_tr_b16 a[164:165], v93 offset:28672\nds_read_b64_tr_b16 a[166:167], v93 offset:29184\nds_read_b64_tr_b16 v[72:73], v93 offset:28680\nds_read_b64_tr_b16 v[74:75], v93 offset:29192\ns_waitcnt lgkmcnt(0)\ns_waitcnt vmcnt(28)\ns_barrier\ns_add_u32 m0, %[lw], 4096\n" \
"s_add_u32 s44, %[wo], 7864320\nbuffer_load_dwordx4 v90, %[rw], s44 offen nt lds\ns_add_u32 m0, %[lw], 5120\ns_add_u32 s44, %[wo], 7929856\nbuffer_load_dwordx4 v90, %[rw], s44 offen nt lds\ns_add_u32 m0, %[lw], 6144\ns_add_u32 s44, %[wo], 7995392\nbuffer_load_dwordx4 v90, %[rw], s44 offen nt lds\n" \
"s_add_u32 m0, %[lw], 7168\ns_add_u32 s44, %[wo], 8060928\nbuffer_load_dwordx4 v90, %[rw], s44 offen nt lds\nds_read_b64_tr_b16 a[184:185], v93 offset:8192\nds_read_b64_tr_b16 a[186:187], v93 offset:8704\nds_read_b64_tr_b16 a[192:193], v93 offset:8200\nds_read_b64_tr_b16 a[194:195], v93 offset:8712\nds_read_b64_tr_b16 a[188:189], v93 offset:32768\n" \
"ds_read_b64_tr_b16 a[190:191], v93 offset:33280\nds_read_b64_tr_b16 a[196:197], v93 offset:32776\nds_read_b64_tr_b16 a[198:199], v93 offset:33288\ns_waitcnt lgkmcnt(0)\ns_waitcnt vmcnt(28)\ns_barrier\ns_add_u32 m0, %[lw], 8192\ns_add_u32 s44, %[w2], 0\n" \
"buffer_load_dwordx4 v91, %[ro], s44 offen nt lds\ns_add_u32 m0, %[lw], 9216\ns_add_u32 s44, %[w2], 32768\nbuffer_load_dwordx4 v91, %[ro], s44 offen nt lds\ns_add_u32 m0, %[lw], 10240\ns_add_u32 s44, %[w2], 65536\nbuffer_load_dwordx4 v91, %[ro], s44 offen nt lds\ns_add_u32 m0, %[lw], 11264\n" \
"s_add_u32 s44, %[w2], 98304\nbuffer_load_dwordx4 v91, %[ro], s44 offen nt lds\nds_read_b64_tr_b16 a[208:209], v93 offset:12288\nds_read_b64_tr_b16 a[210:211], v93 offset:12800\nds_read_b64_tr_b16 a[224:225], v93 offset:12296\nds_read_b64_tr_b16 a[226:227], v93 offset:12808\nds_read_b64_tr_b16 a[212:213], v93 offset:36864\nds_read_b64_tr_b16 a[214:215], v93 offset:37376\n" \
"ds_read_b64_tr_b16 a[228:229], v93 offset:36872\nds_read_b64_tr_b16 a[230:231], v93 offset:37384\ns_waitcnt lgkmcnt(0)\ns_waitcnt vmcnt(28)\ns_barrier\ns_add_u32 m0, %[lw], 12288\ns_add_u32 s44, %[w2], 128\nbuffer_load_dwordx4 v91, %[ro], s44 offen nt lds\n" \
"s_add_u32 m0, %[lw], 13312\ns_add_u32 s44, %[w2], 32896\nbuffer_load_dwordx4 v91, %[ro], s44 offen nt lds\ns_add_u32 m0, %[lw], 14336\ns_add_u32 s44, %[w2], 65664\nbuffer_load_dwordx4 v91, %[ro], s44 offen nt lds\ns_add_u32 m0, %[lw], 15360\ns_add_u32 s44, %[w2], 98432\n" \
"buffer_load_dwordx4 v91, %[ro], s44 offen nt lds\nds_read_b64_tr_b16 v[68:69], v93 offset:16384\nds_read_b64_tr_b16 v[70:71], v93 offset:16896\nds_read_b64_tr_b16 a[240:241], v93 offset:16392\nds_read_b64_tr_b16 a[242:243], v93 offset:16904\nds_read_b64_tr_b16 v[64:65], v93 offset:40960\nds_read_b64_tr_b16 v[66:67], v93 offset:41472\nds_read_b64_tr_b16 a[244:245], v93 offset:40968\n" \
"ds_read_b64_tr_b16 a[246:247], v93 offset:41480\ns_waitcnt lgkmcnt(0)\ns_waitcnt vmcnt(28)\ns_barrier\ns_add_u32 m0, %[lw], 16384\ns_add_u32 s44, %[w2], 256\nbuffer_load_dwordx4 v91, %[ro], s44 offen nt lds\ns_add_u32 m0, %[lw], 17408\n" \
"s_add_u32 s44, %[w2], 33024\nbuffer_load_dwordx4 v91, %[ro], s44 offen nt lds\ns_add_u32 m0, %[lw], 18432\ns_add_u32 s44, %[w2], 65792\nbuffer_load_dwordx4 v91, %[ro], s44 offen nt lds\ns_add_u32 m0, %[lw], 19456\ns_add_u32 s44, %[w2], 98560\nbuffer_load_dwordx4 v91, %[ro], s44 offen nt lds\n" \
"ds_read_b64_tr_b16 v[96:97], v93 offset:20480\nds_read_b64_tr_b16 v[98:99], v93 offset:20992\nds_read_b64_tr_b16 v[112:113], v93 offset:20488\nds_read_b64_tr_b16 v[114:115], v93 offset:21000\nds_read_b64_tr_b16 v[100:101], v93 offset:45056\nds_read_b64_tr_b16 v[102:103], v93 offset:45568\nds_read_b64_tr_b16 v[116:117], v93 offset:45064\nds_read_b64_tr_b16 v[118:119], v93 offset:45576\n" \
"s_waitcnt lgkmcnt(0)\ns_waitcnt vmcnt(28)\ns_barrier\ns_add_u32 m0, %[lw], 20480\ns_add_u32 s44, %[w2], 384\nbuffer_load_dwordx4 v91, %[ro], s44 offen nt lds\ns_add_u32 m0, %[lw], 21504\ns_add_u32 s44, %[w2], 33152\n" \
"buffer_load_dwordx4 v91, %[ro], s44 offen nt lds\ns_add_u32 m0, %[lw], 22528\ns_add_u32 s44, %[w2], 65920\nbuffer_load_dwordx4 v91, %[ro], s44 offen nt lds\ns_add_u32 m0, %[lw], 23552\ns_add_u32 s44, %[w2], 98688\nbuffer_load_dwordx4 v91, %[ro], s44 offen nt lds\nds_read_b64_tr_b16 v[128:129], v92 offset:0\n" \
"ds_read_b64_tr_b16 v[130:131], v92 offset:512\nds_read_b64_tr_b16 v[136:137], v92 offset:8\nds_read_b64_tr_b16 v[138:139], v92 offset:520\nds_read_b64_tr_b16 v[132:133], v92 offset:12288\nds_read_b64_tr_b16 v[134:135], v92 offset:12800\nds_read_b64_tr_b16 v[140:141], v92 offset:12296\nds_read_b64_tr_b16 v[142:143], v92 offset:12808\ns_waitcnt lgkmcnt(0)\n" \
"s_waitcnt vmcnt(28)\ns_barrier\ns_add_u32 m0, %[tb], 0\ns_add_u32 s44, %[w2], 139264\nbuffer_load_dwordx4 v91, %[ro], s44 offen nt lds\ns_add_u32 m0, %[tb], 1024\ns_add_u32 s44, %[w2], 172032\nbuffer_load_dwordx4 v91, %[ro], s44 offen nt lds\n" \
"s_add_u32 m0, %[tb], 2048\ns_add_u32 s44, %[w2], 204800\nbuffer_load_dwordx4 v91, %[ro], s44 offen nt lds\ns_add_u32 m0, %[tb], 3072\ns_add_u32 s44, %[w2], 237568\nbuffer_load_dwordx4 v91, %[ro], s44 offen nt lds\nds_read_b64_tr_b16 v[152:153], v92 offset:4096\nds_read_b64_tr_b16 v[154:155], v92 offset:4608\n" \
"ds_read_b64_tr_b16 v[60:61], v92 offset:4104\nds_read_b64_tr_b16 v[62:63], v92 offset:4616\nds_read_b64_tr_b16 v[156:157], v92 offset:16384\nds_read_b64_tr_b16 v[158:159], v92 offset:16896\nds_read_b64_tr_b16 v[56:57], v92 offset:16392\nds_read_b64_tr_b16 v[58:59], v92 offset:16904\ns_waitcnt lgkmcnt(0)\ns_waitcnt vmcnt(28)\n" \
"s_barrier\ns_add_u32 m0, %[tb], 4096\ns_add_u32 s44, %[w2], 139392\nbuffer_load_dwordx4 v91, %[ro], s44 offen nt lds\ns_add_u32 m0, %[tb], 5120\ns_add_u32 s44, %[w2], 172160\nbuffer_load_dwordx4 v91, %[ro], s44 offen nt lds\ns_add_u32 m0, %[tb], 6144\n" \
"s_add_u32 s44, %[w2], 204928\nbuffer_load_dwordx4 v91, %[ro], s44 offen nt lds\ns_add_u32 m0, %[tb], 7168\ns_add_u32 s44, %[w2], 237696\nbuffer_load_dwordx4 v91, %[ro], s44 offen nt lds\nds_read_b64_tr_b16 v[168:169], v92 offset:8192\nds_read_b64_tr_b16 v[170:171], v92 offset:8704\nds_read_b64_tr_b16 v[184:185], v92 offset:8200\n" \
"ds_read_b64_tr_b16 v[186:187], v92 offset:8712\nds_read_b64_tr_b16 v[172:173], v92 offset:20480\nds_read_b64_tr_b16 v[174:175], v92 offset:20992\nds_read_b64_tr_b16 v[188:189], v92 offset:20488\nds_read_b64_tr_b16 v[190:191], v92 offset:21000\ns_waitcnt lgkmcnt(0)\ns_waitcnt vmcnt(28)\ns_barrier\n" \
"s_add_u32 m0, %[tb], 8192\ns_add_u32 s44, %[w2], 139520\nbuffer_load_dwordx4 v91, %[ro], s44 offen nt lds\ns_add_u32 m0, %[tb], 9216\ns_add_u32 s44, %[w2], 172288\nbuffer_load_dwordx4 v91, %[ro], s44 offen nt lds\ns_add_u32 m0, %[tb], 10240\ns_add_u32 s44, %[w2], 205056\n" \
"buffer_load_dwordx4 v91, %[ro], s44 offen nt lds\ns_add_u32 m0, %[tb], 11264\ns_add_u32 s44, %[w2], 237824\nbuffer_load_dwordx4 v91, %[ro], s44 offen nt lds\nds_read_b64_tr_b16 v[200:201], v93 offset:0\nds_read_b64_tr_b16 v[202:203], v93 offset:512\nds_read_b64_tr_b16 v[216:217], v93 offset:8\nds_read_b64_tr_b16 v[218:219], v93 offset:520\n" \
"ds_read_b64_tr_b16 v[204:205], v93 offset:24576\nds_read_b64_tr_b16 v[206:207], v93 offset:25088\nds_read_b64_tr_b16 v[220:221], v93 offset:24584\nds_read_b64_tr_b16 v[222:223], v93 offset:25096\ns_waitcnt lgkmcnt(0)\ns_waitcnt vmcnt(28)\ns_barrier\ns_add_u32 m0, %[lw], 0\n" \
"s_add_u32 s44, %[w2], 139648\nbuffer_load_dwordx4 v91, %[ro], s44 offen nt lds\ns_add_u32 m0, %[lw], 1024\ns_add_u32 s44, %[w2], 172416\nbuffer_load_dwordx4 v91, %[ro], s44 offen nt lds\ns_add_u32 m0, %[lw], 2048\ns_add_u32 s44, %[w2], 205184\nbuffer_load_dwordx4 v91, %[ro], s44 offen nt lds\n" \
"s_add_u32 m0, %[lw], 3072\ns_add_u32 s44, %[w2], 237952\nbuffer_load_dwordx4 v91, %[ro], s44 offen nt lds\nds_read_b64_tr_b16 v[232:233], v93 offset:4096\nds_read_b64_tr_b16 v[234:235], v93 offset:4608\nds_read_b64_tr_b16 v[240:241], v93 offset:4104\nds_read_b64_tr_b16 v[242:243], v93 offset:4616\nds_read_b64_tr_b16 v[236:237], v93 offset:28672\n" \
"ds_read_b64_tr_b16 v[238:239], v93 offset:29184\nds_read_b64_tr_b16 v[244:245], v93 offset:28680\nds_read_b64_tr_b16 v[246:247], v93 offset:29192\ns_waitcnt lgkmcnt(0)\ns_waitcnt vmcnt(28)\ns_barrier\ns_add_u32 m0, %[lw], 4096\ns_add_u32 s44, %[w2], 131072\n" \
"buffer_load_dwordx4 v91, %[ro], s44 offen nt lds\ns_add_u32 m0, %[lw], 5120\ns_add_u32 s44, %[w2], 163840\nbuffer_load_dwordx4 v91, %[ro], s44 offen nt lds\ns_add_u32 m0, %[lw], 6144\ns_add_u32 s44, %[w2], 196608\nbuffer_load_dwordx4 v91, %[ro], s44 offen nt lds\ns_add_u32 m0, %[lw], 7168\n" \
"s_add_u32 s44, %[w2], 229376\nbuffer_load_dwordx4 v91, %[ro], s44 offen nt lds\nds_read_b64_tr_b16 a[8:9], v95 offset:8192\nds_read_b64_tr_b16 a[10:11], v95 offset:8448\nds_read_b64_tr_b16 a[12:13], v95 offset:10240\nds_read_b64_tr_b16 a[14:15], v95 offset:10496\nds_read_b64_tr_b16 a[216:217], v95 offset:8256\nds_read_b64_tr_b16 a[218:219], v95 offset:8512\n" \
"ds_read_b64_tr_b16 a[220:221], v95 offset:10304\nds_read_b64_tr_b16 a[222:223], v95 offset:10560\ns_waitcnt lgkmcnt(0)\ns_waitcnt vmcnt(28)\ns_barrier\ns_add_u32 m0, %[lw], 8192\ns_add_u32 s44, %[w2], 131200\nbuffer_load_dwordx4 v91, %[ro], s44 offen nt lds\n" \
"s_add_u32 m0, %[lw], 9216\ns_add_u32 s44, %[w2], 163968\nbuffer_load_dwordx4 v91, %[ro], s44 offen nt lds\ns_add_u32 m0, %[lw], 10240\ns_add_u32 s44, %[w2], 196736\nbuffer_load_dwordx4 v91, %[ro], s44 offen nt lds\ns_add_u32 m0, %[lw], 11264\ns_add_u32 s44, %[w2], 229504\n" \
"buffer_load_dwordx4 v91, %[ro], s44 offen nt lds\nds_read_b64_tr_b16 a[64:65], v95 offset:12288\nds_read_b64_tr_b16 a[66:67], v95 offset:12544\nds_read_b64_tr_b16 a[68:69], v95 offset:14336\nds_read_b64_tr_b16 a[70:71], v95 offset:14592\nds_read_b64_tr_b16 v[104:105], v95 offset:12352\nds_read_b64_tr_b16 v[106:107], v95 offset:12608\nds_read_b64_tr_b16 v[108:109], v95 offset:14400\n" \
"ds_read_b64_tr_b16 v[110:111], v95 offset:14656\ns_waitcnt lgkmcnt(0)\ns_waitcnt vmcnt(28)\ns_barrier\ns_add_u32 m0, %[lw], 12288\ns_add_u32 s44, %[w2], 131328\nbuffer_load_dwordx4 v91, %[ro], s44 offen nt lds\ns_add_u32 m0, %[lw], 13312\n" \
"s_add_u32 s44, %[w2], 164096\nbuffer_load_dwordx4 v91, %[ro], s44 offen nt lds\ns_add_u32 m0, %[lw], 14336\ns_add_u32 s44, %[w2], 196864\nbuffer_load_dwordx4 v91, %[ro], s44 offen nt lds\ns_add_u32 m0, %[lw], 15360\ns_add_u32 s44, %[w2], 229632\nbuffer_load_dwordx4 v91, %[ro], s44 offen nt lds\n" \
"ds_read_b64_tr_b16 a[112:113], v95 offset:16384\nds_read_b64_tr_b16 a[114:115], v95 offset:16640\nds_read_b64_tr_b16 a[116:117], v95 offset:18432\nds_read_b64_tr_b16 a[118:119], v95 offset:18688\nds_read_b64_tr_b16 v[160:161], v95 offset:16448\nds_read_b64_tr_b16 v[162:163], v95 offset:16704\nds_read_b64_tr_b16 v[164:165], v95 offset:18496\nds_read_b64_tr_b16 v[166:167], v95 offset:18752\n" \
"s_waitcnt lgkmcnt(0)\ns_waitcnt vmcnt(28)\ns_barrier\ns_add_u32 m0, %[lw], 16384\ns_add_u32 s44, %[w2], 131456\nbuffer_load_dwordx4 v91, %[ro], s44 offen nt lds\ns_add_u32 m0, %[lw], 17408\ns_add_u32 s44, %[w2], 164224\n" \
"buffer_load_dwordx4 v91, %[ro], s44 offen nt lds\ns_add_u32 m0, %[lw], 18432\ns_add_u32 s44, %[w2], 196992\nbuffer_load_dwordx4 v91, %[ro], s44 offen nt lds\ns_add_u32 m0, %[lw], 19456\ns_add_u32 s44, %[w2], 229760\nbuffer_load_dwordx4 v91, %[ro], s44 offen nt lds\nds_read_b64_tr_b16 a[168:169], v95 offset:20480\n" \
"ds_read_b64_tr_b16 a[170:171], v95 offset:20736\nds_read_b64_tr_b16 a[172:173], v95 offset:22528\nds_read_b64_tr_b16 a[174:175], v95 offset:22784\nds_read_b64_tr_b16 v[208:209], v95 offset:20544\nds_read_b64_tr_b16 v[210:211], v95 offset:20800\nds_read_b64_tr_b16 v[212:213], v95 offset:22592\nds_read_b64_tr_b16 v[214:215], v95 offset:22848\ns_waitcnt lgkmcnt(0)\n" \
"s_waitcnt vmcnt(28)\ns_barrier\ns_add_u32 m0, %[lw], 20480\ns_add_u32 s44, %[w2], 8192\nbuffer_load_dwordx4 v91, %[ro], s44 offen nt lds\ns_add_u32 m0, %[lw], 21504\ns_add_u32 s44, %[w2], 40960\nbuffer_load_dwordx4 v91, %[ro], s44 offen nt lds\n" \
"s_add_u32 m0, %[lw], 22528\ns_add_u32 s44, %[w2], 73728\nbuffer_load_dwordx4 v91, %[ro], s44 offen nt lds\ns_add_u32 m0, %[lw], 23552\ns_add_u32 s44, %[w2], 106496\nbuffer_load_dwordx4 v91, %[ro], s44 offen nt lds\nds_read_b64_tr_b16 a[48:49], v94 offset:0\nds_read_b64_tr_b16 a[50:51], v94 offset:256\n" \
"ds_read_b64_tr_b16 a[52:53], v94 offset:2048\nds_read_b64_tr_b16 a[54:55], v94 offset:2304\nds_read_b64_tr_b16 a[248:249], v94 offset:64\nds_read_b64_tr_b16 a[250:251], v94 offset:320\nds_read_b64_tr_b16 a[252:253], v94 offset:2112\nds_read_b64_tr_b16 a[254:255], v94 offset:2368\ns_waitcnt lgkmcnt(0)\ns_waitcnt vmcnt(28)\n" \
"s_barrier\ns_add_u32 m0, %[tb], 0\ns_add_u32 s44, %[w2], 8320\nbuffer_load_dwordx4 v91, %[ro], s44 offen nt lds\ns_add_u32 m0, %[tb], 1024\ns_add_u32 s44, %[w2], 41088\nbuffer_load_dwordx4 v91, %[ro], s44 offen nt lds\ns_add_u32 m0, %[tb], 2048\n" \
"s_add_u32 s44, %[w2], 73856\nbuffer_load_dwordx4 v91, %[ro], s44 offen nt lds\ns_add_u32 m0, %[tb], 3072\ns_add_u32 s44, %[w2], 106624\nbuffer_load_dwordx4 v91, %[ro], s44 offen nt lds\nds_read_b64_tr_b16 a[96:97], v94 offset:4096\nds_read_b64_tr_b16 a[98:99], v94 offset:4352\nds_read_b64_tr_b16 a[100:101], v94 offset:6144\n" \
"ds_read_b64_tr_b16 a[102:103], v94 offset:6400\nds_read_b64_tr_b16 v[144:145], v94 offset:4160\nds_read_b64_tr_b16 v[146:147], v94 offset:4416\nds_read_b64_tr_b16 v[148:149], v94 offset:6208\nds_read_b64_tr_b16 v[150:151], v94 offset:6464\ns_waitcnt lgkmcnt(0)\ns_waitcnt vmcnt(28)\ns_barrier\n" \
"s_add_u32 m0, %[tb], 4096\ns_add_u32 s44, %[w2], 8448\nbuffer_load_dwordx4 v91, %[ro], s44 offen nt lds\ns_add_u32 m0, %[tb], 5120\ns_add_u32 s44, %[w2], 41216\nbuffer_load_dwordx4 v91, %[ro], s44 offen nt lds\ns_add_u32 m0, %[tb], 6144\ns_add_u32 s44, %[w2], 73984\n" \
"buffer_load_dwordx4 v91, %[ro], s44 offen nt lds\ns_add_u32 m0, %[tb], 7168\ns_add_u32 s44, %[w2], 106752\nbuffer_load_dwordx4 v91, %[ro], s44 offen nt lds\nds_read_b64_tr_b16 a[152:153], v94 offset:8192\nds_read_b64_tr_b16 a[154:155], v94 offset:8448\nds_read_b64_tr_b16 a[156:157], v94 offset:10240\nds_read_b64_tr_b16 a[158:159], v94 offset:10496\n" \
"ds_read_b64_tr_b16 v[192:193], v94 offset:8256\nds_read_b64_tr_b16 v[194:195], v94 offset:8512\nds_read_b64_tr_b16 v[196:197], v94 offset:10304\nds_read_b64_tr_b16 v[198:199], v94 offset:10560\ns_waitcnt lgkmcnt(0)\ns_waitcnt vmcnt(28)\ns_barrier\ns_add_u32 m0, %[tb], 8192\n" \
"s_add_u32 s44, %[w2], 8576\nbuffer_load_dwordx4 v91, %[ro], s44 offen nt lds\ns_add_u32 m0, %[tb], 9216\ns_add_u32 s44, %[w2], 41344\nbuffer_load_dwordx4 v91, %[ro], s44 offen nt lds\ns_add_u32 m0, %[tb], 10240\ns_add_u32 s44, %[w2], 74112\nbuffer_load_dwordx4 v91, %[ro], s44 offen nt lds\n" \
"s_add_u32 m0, %[tb], 11264\ns_add_u32 s44, %[w2], 106880\nbuffer_load_dwordx4 v91, %[ro], s44 offen nt lds\nds_read_b64_tr_b16 a[200:201], v95 offset:0\nds_read_b64_tr_b16 a[202:203], v95 offset:256\nds_read_b64_tr_b16 a[204:205], v95 offset:2048\nds_read_b64_tr_b16 a[206:207], v95 offset:2304\nds_read_b64_tr_b16 v[248:249], v95 offset:64\n" \
"ds_read_b64_tr_b16 v[250:251], v95 offset:320\nds_read_b64_tr_b16 v[252:253], v95 offset:2112\nds_read_b64_tr_b16 v[254:255], v95 offset:2368\ns_waitcnt lgkmcnt(0)\ns_waitcnt vmcnt(28)\ns_barrier\nds_read_b64_tr_b16 v[52:53], v95 offset:4096\nds_read_b64_tr_b16 v[54:55], v95 offset:4352\n" \
"ds_read_b64_tr_b16 v[48:49], v95 offset:6144\nds_read_b64_tr_b16 v[50:51], v95 offset:6400\nds_read_b64_tr_b16 v[44:45], v95 offset:4160\nds_read_b64_tr_b16 v[46:47], v95 offset:4416\nds_read_b64_tr_b16 v[40:41], v95 offset:6208\nds_read_b64_tr_b16 v[42:43], v95 offset:6464\ns_waitcnt lgkmcnt(0)\nds_write_b128 v1, v[84:87] offset:0\n" \
"ds_write_b128 v1, v[80:83] offset:1024\nds_write_b128 v1, v[76:79] offset:2048\nds_write_b128 v1, v[72:75] offset:3072\ns_waitcnt vmcnt(24)\ns_barrier\nds_read_b64_tr_b16 a[80:81], v95 offset:8192\nds_read_b64_tr_b16 a[82:83], v95 offset:8448\nds_read_b64_tr_b16 a[84:85], v95 offset:10240\n" \
"ds_read_b64_tr_b16 a[86:87], v95 offset:10496\nds_read_b64_tr_b16 v[120:121], v95 offset:8256\nds_read_b64_tr_b16 v[122:123], v95 offset:8512\nds_read_b64_tr_b16 v[124:125], v95 offset:10304\nds_read_b64_tr_b16 v[126:127], v95 offset:10560\ns_waitcnt lgkmcnt(0)\nds_write_b128 v1, v[68:71] offset:4096\nds_write_b128 v1, v[64:67] offset:5120\n" \
"ds_write_b128 v1, v[60:63] offset:6144\nds_write_b128 v1, v[56:59] offset:7168\ns_waitcnt vmcnt(20)\ns_barrier\nds_read_b64_tr_b16 v[56:57], v95 offset:12288\nds_read_b64_tr_b16 v[58:59], v95 offset:12544\nds_read_b64_tr_b16 v[60:61], v95 offset:14336\nds_read_b64_tr_b16 v[62:63], v95 offset:14592\n" \
"ds_read_b64_tr_b16 v[64:65], v95 offset:12352\nds_read_b64_tr_b16 v[66:67], v95 offset:12608\nds_read_b64_tr_b16 v[68:69], v95 offset:14400\nds_read_b64_tr_b16 v[70:71], v95 offset:14656\ns_waitcnt lgkmcnt(0)\nds_write_b128 v1, v[52:55] offset:8192\nds_write_b128 v1, v[48:51] offset:9216\nds_write_b128 v1, v[44:47] offset:10240\n" \
"ds_write_b128 v1, v[40:43] offset:11264\ns_waitcnt vmcnt(16)\ns_barrier\nds_read_b64_tr_b16 a[176:177], v95 offset:16384\nds_read_b64_tr_b16 a[178:179], v95 offset:16640\nds_read_b64_tr_b16 a[180:181], v95 offset:18432\nds_read_b64_tr_b16 a[182:183], v95 offset:18688\nds_read_b64_tr_b16 v[224:225], v95 offset:16448\n" \
"ds_read_b64_tr_b16 v[226:227], v95 offset:16704\nds_read_b64_tr_b16 v[228:229], v95 offset:18496\nds_read_b64_tr_b16 v[230:231], v95 offset:18752\ns_waitcnt lgkmcnt(0)\nds_write_b128 v1, v[56:59] offset:12288\nds_write_b128 v1, v[60:63] offset:13312\nds_write_b128 v1, v[64:67] offset:14336\nds_write_b128 v1, v[68:71] offset:15360\n" \
"s_waitcnt vmcnt(12)\ns_barrier\nds_read_b64_tr_b16 a[32:33], v95 offset:20480\nds_read_b64_tr_b16 a[34:35], v95 offset:20736\nds_read_b64_tr_b16 a[36:37], v95 offset:22528\nds_read_b64_tr_b16 a[38:39], v95 offset:22784\nds_read_b64_tr_b16 a[232:233], v95 offset:20544\nds_read_b64_tr_b16 a[234:235], v95 offset:20800\n" \
"ds_read_b64_tr_b16 a[236:237], v95 offset:22592\nds_read_b64_tr_b16 a[238:239], v95 offset:22848\ns_waitcnt lgkmcnt(0)\ns_waitcnt vmcnt(8)\ns_barrier\nds_read_b64_tr_b16 v[68:69], v94 offset:0\nds_read_b64_tr_b16 v[70:71], v94 offset:256\nds_read_b64_tr_b16 v[64:65], v94 offset:2048\n" \
"ds_read_b64_tr_b16 v[66:67], v94 offset:2304\nds_read_b64_tr_b16 v[60:61], v94 offset:64\nds_read_b64_tr_b16 v[62:63], v94 offset:320\nds_read_b64_tr_b16 v[56:57], v94 offset:2112\nds_read_b64_tr_b16 v[58:59], v94 offset:2368\ns_waitcnt lgkmcnt(0)\nds_write_b128 v1, v[68:71] offset:16384\nds_write_b128 v1, v[64:67] offset:17408\n" \
"ds_write_b128 v1, v[60:63] offset:18432\nds_write_b128 v1, v[56:59] offset:19456\ns_waitcnt vmcnt(4)\ns_barrier\nds_read_b64_tr_b16 a[136:137], v94 offset:4096\nds_read_b64_tr_b16 a[138:139], v94 offset:4352\nds_read_b64_tr_b16 a[140:141], v94 offset:6144\nds_read_b64_tr_b16 a[142:143], v94 offset:6400\n" \
"ds_read_b64_tr_b16 v[176:177], v94 offset:4160\nds_read_b64_tr_b16 v[178:179], v94 offset:4416\nds_read_b64_tr_b16 v[180:181], v94 offset:6208\nds_read_b64_tr_b16 v[182:183], v94 offset:6464\ns_waitcnt lgkmcnt(0)\ns_waitcnt vmcnt(0)\ns_barrier\nds_read_b64_tr_b16 v[56:57], v94 offset:8192\n" \
"ds_read_b64_tr_b16 v[58:59], v94 offset:8448\nds_read_b64_tr_b16 v[60:61], v94 offset:10240\nds_read_b64_tr_b16 v[62:63], v94 offset:10496\nds_read_b64_tr_b16 v[64:65], v94 offset:8256\nds_read_b64_tr_b16 v[66:67], v94 offset:8512\nds_read_b64_tr_b16 v[68:69], v94 offset:10304\nds_read_b64_tr_b16 v[70:71], v94 offset:10560\ns_waitcnt lgkmcnt(0)\n" \
"ds_write_b128 v1, v[56:59] offset:20480\nds_write_b128 v1, v[60:63] offset:21504\nds_write_b128 v1, v[64:67] offset:22528\nds_write_b128 v1, v[68:71] offset:23552\ns_waitcnt lgkmcnt(0)\ns_barrier\ns_mov_b32 s43, 0\ns_mov_b32 s86, 110\n" \
"s_mov_b32 s89, 0\n.Lvu_hb%=:\nbuffer_load_dwordx2 v[16:17], off, s[36:39], s45 sc1\ns_waitcnt vmcnt(0)\nv_cmp_ne_u32 vcc, v15, v16\ns_cbranch_vccnz .Lvu_hb1%=\ns_sleep 4\ns_add_u32 s43, s43, 1\n" \
"s_cmp_lt_u32 s43, 262144\ns_cbranch_scc1 .Lvu_hb%=\ns_branch .Lvu_abort%=\n.Lvu_hb1%=:\nv_readfirstlane_b32 s44, v17\ns_and_b32 s44, s44, 0x200000\ns_add_u32 s36, s36, s44\ns_addc_u32 s37, s37, 0\n" \
"s_mov_b32 s40, -1\ns_mov_b32 s43, 0\ns_lshl_b32 s57, %[w], 12\ns_mov_b32 s56, 0\ns_mov_b32 s42, 0\ns_mov_b32 s60, -2147483648\ns_mov_b32 s54, -2147483648\ns_add_u32 s44, s40, 1\n" \
"s_lshl_b32 s44, s44, 17\ns_add_u32 s44, s44, %[xn]\ns_lshr_b32 s45, s44, 8\ns_and_b32 s45, s45, 0x2000\ns_xor_b32 s41, s44, s45\ns_add_u32 s41, s41, %[xw]\n.Lvu_f0%=:\nbuffer_load_dword v14, v8, s[36:39], s56 offen sc1\n" \
"s_waitcnt vmcnt(0)\nv_cmp_ge_i32 vcc, s40, v14\ns_cbranch_vccz .Lvu_f0fok%=\ns_add_u32 s43, s43, 1\ns_cmp_lt_u32 s43, 262144\ns_cbranch_scc1 .Lvu_f0%=\ns_branch .Lvu_abort%=\n.Lvu_f0fok%=:\n" \
"s_add_u32 m0, s57, 0\ns_add_u32 s58, s41, 0\nbuffer_load_dwordx4 v0, s[36:39], s58 offen offset:0 sc1 lds\nbuffer_load_dwordx4 v0, s[36:39], s58 offen offset:1024 sc1 lds\nbuffer_load_dwordx4 v0, s[36:39], s58 offen offset:2048 sc1 lds\nbuffer_load_dwordx4 v0, s[36:39], s58 offen offset:3072 sc1 lds\ns_add_u32 m0, s57, 32768\ns_add_u32 s58, s41, 32768\n" \
"buffer_load_dwordx4 v0, s[36:39], s58 offen offset:0 sc1 lds\nbuffer_load_dwordx4 v0, s[36:39], s58 offen offset:1024 sc1 lds\nbuffer_load_dwordx4 v0, s[36:39], s58 offen offset:2048 sc1 lds\nbuffer_load_dwordx4 v0, s[36:39], s58 offen offset:3072 sc1 lds\ns_waitcnt vmcnt(0)\ns_barrier\nds_read_b128 v[32:35], v2 offset:0\nds_read_b128 v[36:39], v2 offset:256\n" \
"ds_read_b128 v[40:43], v2 offset:2048\nds_read_b128 v[44:47], v2 offset:2304\n.Lvu_tok%=:\nds_read_b128 v[64:67], v2 offset:4096\nds_read_b128 v[68:71], v2 offset:4352\ns_waitcnt lgkmcnt(5)\nv_mfma_f32_16x16x32_bf16 v[16:19], a[0:3], v[32:35], 0\nv_cvt_pk_bf16_f32 v92, v80, v81\n" \
"v_cvt_pk_bf16_f32 v93, v82, v83\nv_mfma_f32_16x16x32_bf16 v[20:23], a[16:19], v[32:35], 0\ns_waitcnt lgkmcnt(4)\nds_read_b128 v[32:35], v2 offset:6144\nv_cvt_pk_bf16_f32 v94, v84, v85\nv_cvt_pk_bf16_f32 v95, v86, v87\nv_mfma_f32_16x16x32_bf16 v[24:27], a[0:3], v[36:39], 0\nv_and_or_b32 v92, v92, -2, v13\n" \
"v_and_or_b32 v94, v94, -2, v13\nv_mfma_f32_16x16x32_bf16 v[28:31], a[16:19], v[36:39], 0\ns_waitcnt lgkmcnt(4)\nbuffer_store_dwordx4 v[92:95], v7, s[36:39], s60 offen offset:2560 sc1\nds_read_b128 v[36:39], v2 offset:6400\nv_mfma_f32_16x16x32_bf16 v[16:19], a[4:7], v[40:43], v[16:19]\nv_mfma_f32_16x16x32_bf16 v[20:23], a[20:23], v[40:43], v[20:23]\ns_waitcnt lgkmcnt(4)\n" \
"ds_read_b128 v[40:43], v2 offset:8192\nv_mfma_f32_16x16x32_bf16 v[24:27], a[4:7], v[44:47], v[24:27]\ns_add_u32 m0, s57, 16384\ns_add_u32 s60, s41, 65536\nbuffer_load_dwordx4 v0, s[36:39], s60 offen offset:0 sc1 lds\nv_mfma_f32_16x16x32_bf16 v[28:31], a[20:23], v[44:47], v[28:31]\ns_waitcnt lgkmcnt(4)\nds_read_b128 v[44:47], v2 offset:8448\n" \
"v_mfma_f32_16x16x32_bf16 v[16:19], a[24:27], v[64:67], v[16:19]\nds_read_b128 v[56:59], v1 offset:0\nds_read_b128 v[60:63], v1 offset:1024\nv_mfma_f32_16x16x32_bf16 v[20:23], a[40:43], v[64:67], v[20:23]\ns_waitcnt lgkmcnt(6)\nds_read_b128 v[64:67], v2 offset:10240\nv_mfma_f32_16x16x32_bf16 v[24:27], a[24:27], v[68:71], v[24:27]\nbuffer_load_dwordx4 v0, s[36:39], s60 offen offset:1024 sc1 lds\n" \
"v_mfma_f32_16x16x32_bf16 v[28:31], a[40:43], v[68:71], v[28:31]\ns_waitcnt lgkmcnt(6)\nds_read_b128 v[68:71], v2 offset:10496\nv_mfma_f32_16x16x32_bf16 v[16:19], a[28:31], v[32:35], v[16:19]\nv_mfma_f32_16x16x32_bf16 v[20:23], a[44:47], v[32:35], v[20:23]\ns_waitcnt lgkmcnt(6)\nds_read_b128 v[32:35], v2 offset:12288\nv_mfma_f32_16x16x32_bf16 v[24:27], a[28:31], v[36:39], v[24:27]\n" \
"buffer_load_dwordx4 v0, s[36:39], s60 offen offset:2048 sc1 lds\nv_mfma_f32_16x16x32_bf16 v[28:31], a[44:47], v[36:39], v[28:31]\ns_waitcnt lgkmcnt(6)\nds_read_b128 v[36:39], v2 offset:12544\nv_mfma_f32_16x16x32_bf16 v[16:19], a[56:59], v[40:43], v[16:19]\nv_mfma_f32_16x16x32_bf16 v[20:23], a[72:75], v[40:43], v[20:23]\ns_waitcnt lgkmcnt(6)\nds_read_b128 v[40:43], v2 offset:14336\n" \
"v_mfma_f32_16x16x32_bf16 v[24:27], a[56:59], v[44:47], v[24:27]\nbuffer_load_dwordx4 v0, s[36:39], s60 offen offset:3072 sc1 lds\nv_mfma_f32_16x16x32_bf16 v[28:31], a[72:75], v[44:47], v[28:31]\ns_waitcnt lgkmcnt(4)\nds_read_b128 v[44:47], v2 offset:14592\nv_mfma_f32_16x16x32_bf16 v[16:19], a[60:63], v[64:67], v[16:19]\ns_add_u32 s56, s56, %[fs]\ns_add_u32 s62, s40, 1\n" \
"v_mfma_f32_16x16x32_bf16 v[20:23], a[76:79], v[64:67], v[20:23]\ns_lshr_b32 m0, s57, 4\ns_add_u32 m0, m0, 159744\ns_waitcnt lgkmcnt(4)\nds_read_b128 v[64:67], v2 offset:32768\nv_mfma_f32_16x16x32_bf16 v[24:27], a[60:63], v[68:71], v[24:27]\nbuffer_load_dword v8, s[36:39], s56 offen sc1 lds\nv_lshrrev_b32 v14, 2, v0\n" \
"v_mfma_f32_16x16x32_bf16 v[28:31], a[76:79], v[68:71], v[28:31]\ns_waitcnt lgkmcnt(4)\nds_read_b128 v[68:71], v2 offset:33024\nv_mfma_f32_16x16x32_bf16 v[16:19], v[56:59], v[32:35], v[16:19]\nv_add_u32 v14, 159744, v14\nv_mfma_f32_16x16x32_bf16 v[20:23], a[88:91], v[32:35], v[20:23]\ns_waitcnt lgkmcnt(4)\nds_read_b128 v[32:35], v2 offset:34816\n" \
"v_mfma_f32_16x16x32_bf16 v[24:27], v[56:59], v[36:39], v[24:27]\nv_mfma_f32_16x16x32_bf16 v[28:31], a[88:91], v[36:39], v[28:31]\ns_waitcnt lgkmcnt(4)\nds_read_b128 v[36:39], v2 offset:35072\nv_mfma_f32_16x16x32_bf16 v[16:19], v[60:63], v[40:43], v[16:19]\nv_mfma_f32_16x16x32_bf16 v[20:23], a[92:95], v[40:43], v[20:23]\ns_waitcnt lgkmcnt(4)\nds_read_b128 v[40:43], v2 offset:36864\n" \
"v_mfma_f32_16x16x32_bf16 v[24:27], v[60:63], v[44:47], v[24:27]\nv_mfma_f32_16x16x32_bf16 v[28:31], a[92:95], v[44:47], v[28:31]\ns_waitcnt lgkmcnt(4)\nds_read_b128 v[44:47], v2 offset:37120\nv_mfma_f32_16x16x32_bf16 v[16:19], a[104:107], v[64:67], v[16:19]\nds_read_b128 v[56:59], v1 offset:2048\nds_read_b128 v[60:63], v1 offset:3072\nv_mfma_f32_16x16x32_bf16 v[20:23], a[120:123], v[64:67], v[20:23]\n" \
"s_waitcnt lgkmcnt(6)\nds_read_b128 v[64:67], v2 offset:38912\nv_mfma_f32_16x16x32_bf16 v[24:27], a[104:107], v[68:71], v[24:27]\ns_barrier\nv_mfma_f32_16x16x32_bf16 v[28:31], a[120:123], v[68:71], v[28:31]\ns_waitcnt lgkmcnt(6)\nds_read_b128 v[68:71], v2 offset:39168\nv_mfma_f32_16x16x32_bf16 v[16:19], a[108:111], v[32:35], v[16:19]\n" \
"s_add_u32 m0, s57, 0\ns_add_u32 s60, s41, 98304\nbuffer_load_dwordx4 v0, s[36:39], s60 offen offset:0 sc1 lds\nv_mfma_f32_16x16x32_bf16 v[20:23], a[124:127], v[32:35], v[20:23]\ns_waitcnt lgkmcnt(6)\nds_read_b128 v[32:35], v2 offset:40960\nv_mfma_f32_16x16x32_bf16 v[24:27], a[108:111], v[36:39], v[24:27]\nv_mfma_f32_16x16x32_bf16 v[28:31], a[124:127], v[36:39], v[28:31]\n" \
"s_waitcnt lgkmcnt(6)\nds_read_b128 v[36:39], v2 offset:41216\nv_mfma_f32_16x16x32_bf16 v[16:19], a[128:131], v[40:43], v[16:19]\nbuffer_load_dwordx4 v0, s[36:39], s60 offen offset:1024 sc1 lds\nv_mfma_f32_16x16x32_bf16 v[20:23], a[144:147], v[40:43], v[20:23]\ns_waitcnt lgkmcnt(6)\nds_read_b128 v[40:43], v2 offset:43008\nv_mfma_f32_16x16x32_bf16 v[24:27], a[128:131], v[44:47], v[24:27]\n" \
"v_mfma_f32_16x16x32_bf16 v[28:31], a[144:147], v[44:47], v[28:31]\ns_waitcnt lgkmcnt(4)\nds_read_b128 v[44:47], v2 offset:43264\nv_mfma_f32_16x16x32_bf16 v[16:19], a[132:135], v[64:67], v[16:19]\nbuffer_load_dwordx4 v0, s[36:39], s60 offen offset:2048 sc1 lds\nv_mfma_f32_16x16x32_bf16 v[20:23], a[148:151], v[64:67], v[20:23]\ns_waitcnt lgkmcnt(4)\nds_read_b128 v[64:67], v2 offset:45056\n" \
"v_mfma_f32_16x16x32_bf16 v[24:27], a[132:135], v[68:71], v[24:27]\nv_mfma_f32_16x16x32_bf16 v[28:31], a[148:151], v[68:71], v[28:31]\ns_waitcnt lgkmcnt(4)\nds_read_b128 v[68:71], v2 offset:45312\nv_mfma_f32_16x16x32_bf16 v[16:19], a[160:163], v[32:35], v[16:19]\nbuffer_load_dwordx4 v0, s[36:39], s60 offen offset:3072 sc1 lds\nv_mfma_f32_16x16x32_bf16 v[20:23], v[56:59], v[32:35], v[20:23]\ns_waitcnt lgkmcnt(4)\n" \
"ds_read_b128 v[32:35], v2 offset:47104\nv_mfma_f32_16x16x32_bf16 v[24:27], a[160:163], v[36:39], v[24:27]\ns_add_u32 s44, s40, 2\ns_lshl_b32 s44, s44, 17\nv_mfma_f32_16x16x32_bf16 v[28:31], v[56:59], v[36:39], v[28:31]\ns_waitcnt lgkmcnt(4)\nds_read_b128 v[36:39], v2 offset:47360\nv_mfma_f32_16x16x32_bf16 v[16:19], a[164:167], v[40:43], v[16:19]\n" \
"s_add_u32 s44, s44, %[xn]\ns_lshr_b32 s45, s44, 8\nv_mfma_f32_16x16x32_bf16 v[20:23], v[60:63], v[40:43], v[20:23]\ns_waitcnt vmcnt(4)\ns_barrier\ns_waitcnt lgkmcnt(4)\nds_read_b128 v[40:43], v2 offset:16384\nv_mfma_f32_16x16x32_bf16 v[24:27], a[164:167], v[44:47], v[24:27]\n" \
"s_and_b32 s45, s45, 0x2000\ns_xor_b32 s41, s44, s45\nds_read_b32 v14, v14\nv_mfma_f32_16x16x32_bf16 v[28:31], v[60:63], v[44:47], v[28:31]\ns_waitcnt lgkmcnt(5)\nds_read_b128 v[44:47], v2 offset:16640\nv_mfma_f32_16x16x32_bf16 v[16:19], a[184:187], v[64:67], v[16:19]\nds_read_b128 v[56:59], v1 offset:4096\n" \
"ds_read_b128 v[60:63], v1 offset:5120\nv_mfma_f32_16x16x32_bf16 v[20:23], a[192:195], v[64:67], v[20:23]\ns_waitcnt lgkmcnt(7)\nds_read_b128 v[64:67], v2 offset:18432\nv_mfma_f32_16x16x32_bf16 v[24:27], a[184:187], v[68:71], v[24:27]\ns_add_u32 s41, s41, %[xw]\nv_mfma_f32_16x16x32_bf16 v[28:31], a[192:195], v[68:71], v[28:31]\ns_waitcnt lgkmcnt(7)\n" \
"ds_read_b128 v[68:71], v2 offset:18688\nv_mfma_f32_16x16x32_bf16 v[16:19], a[188:191], v[32:35], v[16:19]\ns_waitcnt lgkmcnt(5)\nv_cmp_ge_i32 s[64:65], s62, v14\nv_mfma_f32_16x16x32_bf16 v[20:23], a[196:199], v[32:35], v[20:23]\nds_read_b128 v[32:35], v2 offset:20480\nv_mfma_f32_16x16x32_bf16 v[24:27], a[188:191], v[36:39], v[24:27]\ns_cmp_eq_u64 s[64:65], 0\n" \
"s_cselect_b32 s61, s41, -2147483648\nv_mfma_f32_16x16x32_bf16 v[28:31], a[196:199], v[36:39], v[28:31]\nds_read_b128 v[36:39], v2 offset:20736\nv_mfma_f32_16x16x32_bf16 v[16:19], a[208:211], v[40:43], v[16:19]\ns_waitcnt vmcnt(4)\nbuffer_store_dword v15, off, s[36:39], s54 sc1\nv_mfma_f32_16x16x32_bf16 v[20:23], a[224:227], v[40:43], v[20:23]\ns_waitcnt lgkmcnt(6)\n" \
"ds_read_b128 v[40:43], v2 offset:22528\nv_mfma_f32_16x16x32_bf16 v[24:27], a[208:211], v[44:47], v[24:27]\ns_add_u32 m0, s57, 32768\ns_add_u32 s58, s61, 32768\nbuffer_load_dwordx4 v0, s[36:39], s58 offen offset:0 sc1 lds\nv_mfma_f32_16x16x32_bf16 v[28:31], a[224:227], v[44:47], v[28:31]\ns_waitcnt lgkmcnt(4)\nds_read_b128 v[44:47], v2 offset:22784\n" \
"v_mfma_f32_16x16x32_bf16 v[16:19], a[212:215], v[64:67], v[16:19]\nv_mfma_f32_16x16x32_bf16 v[20:23], a[228:231], v[64:67], v[20:23]\ns_waitcnt lgkmcnt(4)\nds_read_b128 v[64:67], v2 offset:24576\nv_mfma_f32_16x16x32_bf16 v[24:27], a[212:215], v[68:71], v[24:27]\nbuffer_load_dwordx4 v0, s[36:39], s58 offen offset:1024 sc1 lds\nv_mfma_f32_16x16x32_bf16 v[28:31], a[228:231], v[68:71], v[28:31]\ns_waitcnt lgkmcnt(4)\n" \
"ds_read_b128 v[68:71], v2 offset:24832\nv_mfma_f32_16x16x32_bf16 v[16:19], v[56:59], v[32:35], v[16:19]\nv_mfma_f32_16x16x32_bf16 v[20:23], a[240:243], v[32:35], v[20:23]\ns_waitcnt lgkmcnt(4)\nds_read_b128 v[32:35], v2 offset:26624\nv_mfma_f32_16x16x32_bf16 v[24:27], v[56:59], v[36:39], v[24:27]\nbuffer_load_dwordx4 v0, s[36:39], s58 offen offset:2048 sc1 lds\nv_mfma_f32_16x16x32_bf16 v[28:31], a[240:243], v[36:39], v[28:31]\n" \
"s_waitcnt lgkmcnt(4)\nds_read_b128 v[36:39], v2 offset:26880\nv_mfma_f32_16x16x32_bf16 v[16:19], v[60:63], v[40:43], v[16:19]\nv_mfma_f32_16x16x32_bf16 v[20:23], a[244:247], v[40:43], v[20:23]\ns_waitcnt lgkmcnt(4)\nds_read_b128 v[40:43], v2 offset:28672\nv_mfma_f32_16x16x32_bf16 v[24:27], v[60:63], v[44:47], v[24:27]\nbuffer_load_dwordx4 v0, s[36:39], s58 offen offset:3072 sc1 lds\n" \
"v_mfma_f32_16x16x32_bf16 v[28:31], a[244:247], v[44:47], v[28:31]\ns_waitcnt lgkmcnt(4)\nds_read_b128 v[44:47], v2 offset:28928\nv_mfma_f32_16x16x32_bf16 v[16:19], v[96:99], v[64:67], v[16:19]\nds_read_b128 v[56:59], v1 offset:6144\nds_read_b128 v[60:63], v1 offset:7168\nv_mfma_f32_16x16x32_bf16 v[20:23], v[112:115], v[64:67], v[20:23]\ns_waitcnt lgkmcnt(6)\n" \
"ds_read_b128 v[64:67], v2 offset:30720\nv_mfma_f32_16x16x32_bf16 v[24:27], v[96:99], v[68:71], v[24:27]\nv_mfma_f32_16x16x32_bf16 v[28:31], v[112:115], v[68:71], v[28:31]\ns_waitcnt lgkmcnt(6)\nds_read_b128 v[68:71], v2 offset:30976\nv_mfma_f32_16x16x32_bf16 v[16:19], v[100:103], v[32:35], v[16:19]\nv_mfma_f32_16x16x32_bf16 v[20:23], v[116:119], v[32:35], v[20:23]\ns_waitcnt vmcnt(5)\n" \
"s_barrier\ns_waitcnt lgkmcnt(6)\nds_read_b128 v[32:35], v2 offset:0\nv_mfma_f32_16x16x32_bf16 v[24:27], v[100:103], v[36:39], v[24:27]\nv_mfma_f32_16x16x32_bf16 v[28:31], v[116:119], v[36:39], v[28:31]\ns_waitcnt lgkmcnt(6)\nds_read_b128 v[36:39], v2 offset:256\nv_mfma_f32_16x16x32_bf16 v[16:19], v[128:131], v[40:43], v[16:19]\n" \
"s_add_u32 m0, s57, 16384\ns_nop 0\nbuffer_load_dwordx4 v3, s[36:39], s42 offen offset:0 sc1 lds\nv_mfma_f32_16x16x32_bf16 v[20:23], v[136:139], v[40:43], v[20:23]\ns_waitcnt lgkmcnt(6)\nds_read_b128 v[40:43], v2 offset:2048\nv_mfma_f32_16x16x32_bf16 v[24:27], v[128:131], v[44:47], v[24:27]\nv_mfma_f32_16x16x32_bf16 v[28:31], v[136:139], v[44:47], v[28:31]\n" \
"s_waitcnt lgkmcnt(4)\nds_read_b128 v[44:47], v2 offset:2304\nv_mfma_f32_16x16x32_bf16 v[16:19], v[132:135], v[64:67], v[16:19]\nbuffer_load_dwordx4 v3, s[36:39], s42 offen offset:1024 sc1 lds\nv_mfma_f32_16x16x32_bf16 v[20:23], v[140:143], v[64:67], v[20:23]\ns_waitcnt lgkmcnt(4)\nds_read_b128 v[64:67], v2 offset:4096\nv_mfma_f32_16x16x32_bf16 v[24:27], v[132:135], v[68:71], v[24:27]\n" \
"v_mfma_f32_16x16x32_bf16 v[28:31], v[140:143], v[68:71], v[28:31]\ns_waitcnt lgkmcnt(4)\nds_read_b128 v[68:71], v2 offset:4352\nv_mfma_f32_16x16x32_bf16 v[16:19], v[152:155], v[32:35], v[16:19]\nbuffer_load_dwordx4 v3, s[36:39], s42 offen offset:2048 sc1 lds\nv_mfma_f32_16x16x32_bf16 v[20:23], v[56:59], v[32:35], v[20:23]\ns_waitcnt lgkmcnt(4)\nds_read_b128 v[32:35], v2 offset:6144\n" \
"v_mfma_f32_16x16x32_bf16 v[24:27], v[152:155], v[36:39], v[24:27]\nv_mfma_f32_16x16x32_bf16 v[28:31], v[56:59], v[36:39], v[28:31]\ns_waitcnt lgkmcnt(4)\nds_read_b128 v[36:39], v2 offset:6400\nv_mfma_f32_16x16x32_bf16 v[16:19], v[156:159], v[40:43], v[16:19]\nbuffer_load_dwordx4 v3, s[36:39], s42 offen offset:3072 sc1 lds\nv_add_u32 v14, s57, v0\nv_mfma_f32_16x16x32_bf16 v[20:23], v[60:63], v[40:43], v[20:23]\n" \
"s_waitcnt lgkmcnt(4)\nds_read_b128 v[40:43], v2 offset:8192\nv_mfma_f32_16x16x32_bf16 v[24:27], v[156:159], v[44:47], v[24:27]\nv_mov_b64 v[48:49], 0\nv_mfma_f32_16x16x32_bf16 v[28:31], v[60:63], v[44:47], v[28:31]\ns_waitcnt lgkmcnt(4)\nds_read_b128 v[44:47], v2 offset:10240\nv_mov_b64 v[50:51], 0\n" \
"v_mfma_f32_16x16x32_bf16 v[16:19], v[168:171], v[64:67], v[16:19]\nv_mov_b64 v[52:53], 0\nv_mfma_f32_16x16x32_bf16 v[20:23], v[184:187], v[64:67], v[20:23]\ns_waitcnt lgkmcnt(4)\nds_read_b128 v[64:67], v2 offset:12288\nv_mov_b64 v[54:55], 0\nv_mfma_f32_16x16x32_bf16 v[24:27], v[168:171], v[68:71], v[24:27]\nds_read_b128 v[56:59], v1 offset:0\n" \
"ds_read_b128 v[60:63], v1 offset:1024\nv_mfma_f32_16x16x32_bf16 v[28:31], v[184:187], v[68:71], v[28:31]\ns_waitcnt lgkmcnt(6)\nds_read_b128 v[68:71], v2 offset:14336\nv_mfma_f32_16x16x32_bf16 v[16:19], v[172:175], v[32:35], v[16:19]\nv_mfma_f32_16x16x32_bf16 v[20:23], v[188:191], v[32:35], v[20:23]\ns_waitcnt lgkmcnt(6)\nds_read_b128 v[32:35], v2 offset:8448\n" \
"v_mfma_f32_16x16x32_bf16 v[24:27], v[172:175], v[36:39], v[24:27]\nv_mfma_f32_16x16x32_bf16 v[28:31], v[188:191], v[36:39], v[28:31]\ns_waitcnt lgkmcnt(6)\nds_read_b128 v[36:39], v2 offset:10496\nv_mfma_f32_16x16x32_bf16 v[16:19], v[200:203], v[40:43], v[16:19]\ns_waitcnt vmcnt(0)\nds_read_b128 v[80:83], v14 offset:16384\nds_read_b128 v[84:87], v14 offset:17408\n" \
"v_mfma_f32_16x16x32_bf16 v[20:23], v[216:219], v[40:43], v[20:23]\ns_waitcnt lgkmcnt(8)\nds_read_b128 v[40:43], v2 offset:12544\nv_mfma_f32_16x16x32_bf16 v[16:19], v[204:207], v[44:47], v[16:19]\nds_read_b128 v[88:91], v14 offset:18432\nds_read_b128 v[92:95], v14 offset:19456\nv_mfma_f32_16x16x32_bf16 v[20:23], v[220:223], v[44:47], v[20:23]\ns_waitcnt lgkmcnt(10)\n" \
"ds_read_b128 v[44:47], v2 offset:14592\nv_mfma_f32_16x16x32_bf16 v[16:19], v[232:235], v[64:67], v[16:19]\ns_waitcnt lgkmcnt(4)\nv_max3_u32 v72, v80, v82, v84\nv_mfma_f32_16x16x32_bf16 v[20:23], v[240:243], v[64:67], v[20:23]\ns_waitcnt lgkmcnt(2)\nv_max3_u32 v72, v72, v86, v88\nv_mfma_f32_16x16x32_bf16 v[16:19], v[236:239], v[68:71], v[16:19]\n" \
"s_waitcnt lgkmcnt(1)\nv_max3_u32 v72, v72, v90, v92\nv_max_u32 v72, v72, v94\nv_mfma_f32_16x16x32_bf16 v[20:23], v[244:247], v[68:71], v[20:23]\nv_cmp_eq_u32 vcc, -1, v72\nv_mfma_f32_16x16x32_bf16 v[24:27], v[200:203], v[32:35], v[24:27]\ns_cbranch_vccnz .Lvu_re%=\n.Lvu_rer%=:\n" \
"s_barrier\ns_memrealtime s[68:69]\nds_read_b128 v[80:83], v0 offset:16384\nv_mfma_f32_16x16x32_bf16 v[28:31], v[216:219], v[32:35], v[28:31]\nds_read_b128 v[84:87], v0 offset:17408\nv_mfma_f32_16x16x32_bf16 v[24:27], v[204:207], v[36:39], v[24:27]\nds_read_b128 v[88:91], v0 offset:18432\nv_cvt_pk_bf16_f32 v72, v16, v17\n" \
"v_cvt_pk_bf16_f32 v73, v18, v19\nv_mfma_f32_16x16x32_bf16 v[28:31], v[220:223], v[36:39], v[28:31]\nds_read_b128 v[92:95], v0 offset:19456\nv_cvt_pk_bf16_f32 v74, v20, v21\nv_cvt_pk_bf16_f32 v75, v22, v23\nv_mfma_f32_16x16x32_bf16 v[24:27], v[232:235], v[40:43], v[24:27]\nds_read_b128 v[16:19], v0 offset:20480\nv_mfma_f32_16x16x32_bf16 v[28:31], v[240:243], v[40:43], v[28:31]\n" \
"s_waitcnt lgkmcnt(5)\nds_read_b128 v[20:23], v0 offset:21504\nv_mfma_f32_16x16x32_bf16 v[24:27], v[236:239], v[44:47], v[24:27]\nds_read_b128 v[32:35], v0 offset:22528\nv_mfma_f32_16x16x32_bf16 v[28:31], v[244:247], v[44:47], v[28:31]\ns_cmp_lt_i32 s40, 0\nds_read_b128 v[36:39], v0 offset:23552\nds_write2st64_b64 v9, v[72:73], v[74:75] offset0:0 offset1:8\n" \
"s_cbranch_scc1 .Lvu_first%=\n.Lvu_crit%=:\ns_waitcnt lgkmcnt(8)\nv_smfmac_f32_16x16x64_bf16 v[48:51], v[80:83], a[0:7], v4\ns_waitcnt lgkmcnt(7)\nds_read_b128 v[64:67], v1 offset:4096\nds_read_b128 v[40:43], v0 offset:24576\nv_smfmac_f32_16x16x64_bf16 v[48:51], v[84:87], a[24:31], v4\n" \
"s_waitcnt lgkmcnt(8)\nds_read_b128 v[68:71], v1 offset:5120\nds_read_b128 v[44:47], v0 offset:25600\nv_cvt_pk_bf16_f32 v76, v24, v25\nv_smfmac_f32_16x16x64_bf16 v[48:51], v[88:91], a[56:63], v4\ns_waitcnt lgkmcnt(9)\nds_read_b128 v[80:83], v0 offset:28672\nv_cvt_pk_bf16_f32 v77, v26, v27\n" \
"v_smfmac_f32_16x16x64_bf16 v[48:51], v[92:95], v[56:63], v4\ns_waitcnt lgkmcnt(9)\nds_read_b128 v[84:87], v0 offset:29696\nv_cvt_pk_bf16_f32 v78, v28, v29\nv_smfmac_f32_16x16x64_bf16 v[48:51], v[16:19], a[104:111], v4\ns_waitcnt lgkmcnt(9)\nds_read_b128 v[88:91], v0 offset:30720\nv_cvt_pk_bf16_f32 v79, v30, v31\n" \
"v_smfmac_f32_16x16x64_bf16 v[48:51], v[20:23], a[128:135], v4\ns_waitcnt lgkmcnt(9)\nds_read_b128 v[92:95], v0 offset:31744\nv_mov_b32 v14, s40\nv_smfmac_f32_16x16x64_bf16 v[48:51], v[32:35], a[160:167], v4\ns_waitcnt lgkmcnt(9)\nds_read_b128 v[24:27], v0 offset:26624\nv_smfmac_f32_16x16x64_bf16 v[48:51], v[36:39], a[184:191], v4\n" \
"s_waitcnt lgkmcnt(7)\nds_read_b128 v[28:31], v0 offset:27648\nv_smfmac_f32_16x16x64_bf16 v[48:51], v[40:43], a[208:215], v4\ns_waitcnt lgkmcnt(6)\nds_read_b128 v[56:59], v1 offset:6144\nv_smfmac_f32_16x16x64_bf16 v[48:51], v[44:47], v[64:71], v4\ns_waitcnt lgkmcnt(6)\nds_read_b128 v[60:63], v1 offset:7168\n" \
"v_smfmac_f32_16x16x64_bf16 v[48:51], v[80:83], v[152:159], v4\ns_sub_u32 s88, s86, s89\ns_min_u32 s88, s88, 150\ns_waitcnt lgkmcnt(6)\nds_write2st64_b64 v9, v[76:77], v[78:79] offset0:2 offset1:10\nv_smfmac_f32_16x16x64_bf16 v[48:51], v[84:87], v[168:175], v4\ns_add_u32 s88, s88, s68\ns_mov_b32 s89, s68\n" \
"s_waitcnt lgkmcnt(6)\nv_mov_b32 v15, s88\nv_smfmac_f32_16x16x64_bf16 v[48:51], v[88:91], v[200:207], v4\ns_mov_b64 exec, 1\nbuffer_store_dwordx2 v[14:15], off, s[36:39], %[sg] sc1\ns_mov_b64 exec, -1\ns_waitcnt lgkmcnt(5)\nv_smfmac_f32_16x16x64_bf16 v[48:51], v[92:95], v[232:239], v4\n" \
"s_waitcnt lgkmcnt(4)\nv_smfmac_f32_16x16x64_bf16 v[48:51], v[24:27], v[96:103], v4\ns_waitcnt lgkmcnt(3)\nds_read_b128 v[64:67], v1 offset:2048\nv_smfmac_f32_16x16x64_bf16 v[48:51], v[28:31], v[128:135], v4\ns_waitcnt lgkmcnt(2)\nds_read_b128 v[68:71], v1 offset:3072\nv_smfmac_f32_16x16x64_bf16 v[52:55], v[80:83], v[56:63], v4\n" \
"ds_read_b128 v[56:59], v1 offset:8192\nv_smfmac_f32_16x16x64_bf16 v[52:55], v[84:87], v[184:191], v4\nds_read_b128 v[60:63], v1 offset:9216\nv_add_f32 v48, v48, v50\nv_add_f32 v49, v49, v51\nv_smfmac_f32_16x16x64_bf16 v[52:55], v[88:91], v[216:223], v4\nds_read_b128 v[80:83], v0 offset:16384\nv_cvt_pk_bf16_f32 v14, v48, v49\n" \
"v_smfmac_f32_16x16x64_bf16 v[52:55], v[92:95], v[240:247], v4\nds_write_b16 v5, v14 offset:0\nds_write_b16_d16_hi v5, v14 offset:16\nv_smfmac_f32_16x16x64_bf16 v[52:55], v[16:19], a[120:127], v4\nds_read_b128 v[84:87], v0 offset:17408\nv_mov_b64 v[16:17], 0\nv_smfmac_f32_16x16x64_bf16 v[52:55], v[20:23], a[144:151], v4\nds_read_b128 v[88:91], v0 offset:18432\n" \
"v_mov_b64 v[18:19], 0\nv_smfmac_f32_16x16x64_bf16 v[52:55], v[24:27], v[112:119], v4\nds_read_b128 v[92:95], v0 offset:19456\nv_mov_b64 v[20:21], 0\nv_smfmac_f32_16x16x64_bf16 v[52:55], v[28:31], v[136:143], v4\ns_waitcnt lgkmcnt(8)\nv_mov_b64 v[22:23], 0\nv_smfmac_f32_16x16x64_bf16 v[52:55], v[32:35], v[64:71], v4\n" \
"s_waitcnt lgkmcnt(3)\nds_read_b128 v[64:67], v1 offset:12288\ns_barrier\nds_read_b128 v[72:75], v11 offset:57344\nv_mov_b64 v[24:25], 0\nv_smfmac_f32_16x16x64_bf16 v[52:55], v[36:39], a[192:199], v4\nds_read_b128 v[68:71], v1 offset:13312\nds_read_b128 v[76:79], v11 offset:58368\n" \
"v_mov_b64 v[26:27], 0\nv_smfmac_f32_16x16x64_bf16 v[52:55], v[40:43], a[224:231], v4\nv_mov_b64 v[28:29], 0\nv_smfmac_f32_16x16x64_bf16 v[52:55], v[44:47], a[240:247], v4\nv_mov_b64 v[30:31], 0\nv_smfmac_f32_16x16x64_bf16 v[52:55], v[80:83], a[16:23], v4\ns_waitcnt lgkmcnt(6)\nv_mov_b64 v[32:33], 0\n" \
"v_smfmac_f32_16x16x64_bf16 v[52:55], v[84:87], a[40:47], v4\ns_waitcnt lgkmcnt(5)\nv_mov_b64 v[34:35], 0\nv_smfmac_f32_16x16x64_bf16 v[52:55], v[88:91], a[72:79], v4\ns_waitcnt lgkmcnt(4)\nv_mov_b64 v[36:37], 0\nv_smfmac_f32_16x16x64_bf16 v[52:55], v[92:95], a[88:95], v4\ns_waitcnt lgkmcnt(2)\n" \
"v_mov_b64 v[38:39], 0\nv_smfmac_f32_16x16x64_bf16 v[16:19], v[72:75], a[8:15], v10\ns_waitcnt lgkmcnt(0)\nv_mov_b64 v[40:41], 0\nv_smfmac_f32_16x16x64_bf16 v[16:19], v[76:79], v[56:63], v10\nds_read_b128 v[56:59], v1 offset:10240\nv_add_f32 v52, v52, v54\nv_add_f32 v53, v53, v55\n" \
"v_smfmac_f32_16x16x64_bf16 v[20:23], v[72:75], a[64:71], v10\nds_read_b128 v[60:63], v1 offset:11264\nv_cvt_pk_bf16_f32 v14, v52, v53\nv_smfmac_f32_16x16x64_bf16 v[20:23], v[76:79], a[80:87], v10\nds_write_b16 v5, v14 offset:2048\nds_write_b16_d16_hi v5, v14 offset:2064\nv_mov_b64 v[42:43], 0\nv_smfmac_f32_16x16x64_bf16 v[24:27], v[72:75], a[112:119], v10\n" \
"v_mov_b64 v[44:45], 0\nv_smfmac_f32_16x16x64_bf16 v[24:27], v[76:79], v[64:71], v10\nds_read_b128 v[64:67], v1 offset:14336\nv_mov_b64 v[46:47], 0\nv_smfmac_f32_16x16x64_bf16 v[28:31], v[72:75], a[168:175], v10\nds_read_b128 v[68:71], v1 offset:15360\nv_mov_b64 v[48:49], 0\nv_smfmac_f32_16x16x64_bf16 v[28:31], v[76:79], a[176:183], v10\n" \
"v_mov_b64 v[50:51], 0\nv_smfmac_f32_16x16x64_bf16 v[32:35], v[72:75], a[216:223], v10\ns_waitcnt lgkmcnt(2)\ns_barrier\ns_mov_b32 s59, 0\nds_read_b128 v[80:83], v11 offset:59392\nv_mov_b64 v[52:53], 0\nv_smfmac_f32_16x16x64_bf16 v[32:35], v[76:79], v[56:63], v10\n" \
"ds_read_b128 v[56:59], v1 offset:16384\nds_read_b128 v[84:87], v11 offset:60416\nv_mov_b64 v[54:55], 0\nv_smfmac_f32_16x16x64_bf16 v[36:39], v[72:75], v[104:111], v10\ns_add_u32 m0, s57, 0\ns_add_u32 s58, s61, 0\nds_read_b128 v[60:63], v1 offset:17408\nbuffer_load_dwordx4 v0, s[36:39], s58 offen offset:0 sc1 lds\n" \
"v_smfmac_f32_16x16x64_bf16 v[36:39], v[76:79], v[120:127], v10\nv_smfmac_f32_16x16x64_bf16 v[40:43], v[72:75], v[160:167], v10\ns_waitcnt lgkmcnt(4)\nv_smfmac_f32_16x16x64_bf16 v[40:43], v[76:79], v[64:71], v10\nds_read_b128 v[64:67], v1 offset:20480\nv_smfmac_f32_16x16x64_bf16 v[44:47], v[72:75], v[208:215], v10\nds_read_b128 v[68:71], v1 offset:21504\nbuffer_load_dwordx4 v0, s[36:39], s58 offen offset:1024 sc1 lds\n" \
"v_smfmac_f32_16x16x64_bf16 v[44:47], v[76:79], v[224:231], v10\ns_waitcnt lgkmcnt(5)\nv_smfmac_f32_16x16x64_bf16 v[16:19], v[80:83], a[32:39], v10\ns_waitcnt lgkmcnt(3)\nds_read_b128 v[72:75], v11 offset:49152\nv_smfmac_f32_16x16x64_bf16 v[16:19], v[84:87], a[48:55], v10\ns_waitcnt lgkmcnt(3)\nbuffer_load_dwordx4 v0, s[36:39], s58 offen offset:2048 sc1 lds\n" \
"ds_read_b128 v[76:79], v11 offset:50176\nv_smfmac_f32_16x16x64_bf16 v[20:23], v[80:83], v[56:63], v10\nds_read_b128 v[56:59], v1 offset:18432\nv_smfmac_f32_16x16x64_bf16 v[20:23], v[84:87], a[96:103], v10\nds_read_b128 v[60:63], v1 offset:19456\nv_cvt_pk_bf16_f32 v88, v16, v17\nv_cvt_pk_bf16_f32 v89, v18, v19\nv_and_or_b32 v88, v88, -2, v12\n" \
"v_smfmac_f32_16x16x64_bf16 v[24:27], v[80:83], a[136:143], v10\nbuffer_load_dwordx4 v0, s[36:39], s58 offen offset:3072 sc1 lds\nds_read_b128 v[16:19], v11 offset:51200\nv_smfmac_f32_16x16x64_bf16 v[24:27], v[84:87], a[152:159], v10\ns_waitcnt lgkmcnt(5)\nv_cvt_pk_bf16_f32 v90, v20, v21\nv_cvt_pk_bf16_f32 v91, v22, v23\nv_and_or_b32 v90, v90, -2, v12\n" \
"v_smfmac_f32_16x16x64_bf16 v[28:31], v[80:83], v[64:71], v10\nds_read_b128 v[64:67], v1 offset:22528\nv_permlane16_swap_b32 v89, v91\nv_permlane16_swap_b32 v88, v90\nv_smfmac_f32_16x16x64_bf16 v[28:31], v[84:87], a[200:207], v10\ns_add_u32 s63, s59, 0\nds_read_b128 v[68:71], v1 offset:23552\nbuffer_store_dwordx4 v[88:91], v6, s[36:39], s63 offen sc1\n" \
"v_cvt_pk_bf16_f32 v92, v24, v25\nv_cvt_pk_bf16_f32 v93, v26, v27\nv_and_or_b32 v92, v92, -2, v12\nv_smfmac_f32_16x16x64_bf16 v[32:35], v[80:83], a[232:239], v10\nds_read_b128 v[20:23], v11 offset:52224\nv_smfmac_f32_16x16x64_bf16 v[32:35], v[84:87], a[248:255], v10\ns_waitcnt lgkmcnt(4)\nds_read_b128 v[24:27], v11 offset:53248\n" \
"v_cvt_pk_bf16_f32 v94, v28, v29\nv_cvt_pk_bf16_f32 v95, v30, v31\nv_and_or_b32 v94, v94, -2, v12\nv_smfmac_f32_16x16x64_bf16 v[36:39], v[80:83], v[56:63], v10\nds_read_b128 v[56:59], v1 offset:8192\nv_permlane16_swap_b32 v93, v95\nv_permlane16_swap_b32 v92, v94\nv_smfmac_f32_16x16x64_bf16 v[36:39], v[84:87], v[144:151], v10\n" \
"s_add_u32 s63, s59, 131072\nds_read_b128 v[60:63], v1 offset:9216\nbuffer_store_dwordx4 v[92:95], v6, s[36:39], s63 offen sc1\nv_cvt_pk_bf16_f32 v88, v32, v33\nv_cvt_pk_bf16_f32 v89, v34, v35\nv_and_or_b32 v88, v88, -2, v12\nv_smfmac_f32_16x16x64_bf16 v[40:43], v[80:83], v[176:183], v10\nds_read_b128 v[28:31], v11 offset:54272\n" \
"v_smfmac_f32_16x16x64_bf16 v[40:43], v[84:87], v[192:199], v10\ns_waitcnt lgkmcnt(5)\nds_read_b128 v[32:35], v11 offset:55296\nv_cvt_pk_bf16_f32 v90, v36, v37\nv_cvt_pk_bf16_f32 v91, v38, v39\nv_and_or_b32 v90, v90, -2, v12\nv_smfmac_f32_16x16x64_bf16 v[44:47], v[80:83], v[64:71], v10\nds_read_b128 v[64:67], v1 offset:16384\n" \
"v_permlane16_swap_b32 v89, v91\nv_permlane16_swap_b32 v88, v90\nv_smfmac_f32_16x16x64_bf16 v[44:47], v[84:87], v[248:255], v10\ns_add_u32 s63, s59, 4096\ns_cmp_ge_i32 s40, 63\nds_read_b128 v[68:71], v1 offset:17408\nbuffer_store_dwordx4 v[88:91], v6, s[36:39], s63 offen sc1\nv_cvt_pk_bf16_f32 v92, v40, v41\n" \
"v_cvt_pk_bf16_f32 v93, v42, v43\nv_and_or_b32 v92, v92, -2, v12\ns_cbranch_scc1 .Lvu_last%=\n.Lvu_y%=:\nv_smfmac_f32_16x16x64_bf16 v[48:51], v[72:75], a[8:15], v10\ns_add_u32 s40, s40, 1\nds_read_b128 v[36:39], v11 offset:56320\nv_smfmac_f32_16x16x64_bf16 v[52:55], v[76:79], a[8:15], v10\n" \
"s_and_b32 s44, s40, 1\ns_lshl_b32 s55, s44, 22\ns_waitcnt lgkmcnt(5)\nv_cvt_pk_bf16_f32 v94, v44, v45\nv_cvt_pk_bf16_f32 v95, v46, v47\nv_and_or_b32 v94, v94, -2, v12\nv_smfmac_f32_16x16x64_bf16 v[48:51], v[16:19], v[56:63], v10\ns_bfe_u32 s45, s40, 0x10001\n" \
"v_permlane16_swap_b32 v93, v95\nv_permlane16_swap_b32 v92, v94\nv_smfmac_f32_16x16x64_bf16 v[52:55], v[20:23], v[56:63], v10\ns_add_u32 s63, s59, 135168\nbuffer_store_dwordx4 v[92:95], v6, s[36:39], s63 offen sc1\nv_mov_b64 v[80:81], 0\nv_mov_b64 v[82:83], 0\nv_mov_b64 v[84:85], 0\n" \
"v_mov_b64 v[86:87], 0\nv_smfmac_f32_16x16x64_bf16 v[48:51], v[24:27], a[32:39], v10\ns_memrealtime s[86:87]\ns_waitcnt lgkmcnt(4)\nds_read_b128 v[56:59], v1 offset:12288\nv_smfmac_f32_16x16x64_bf16 v[52:55], v[28:31], a[32:39], v10\ns_waitcnt lgkmcnt(4)\nds_read_b128 v[60:63], v1 offset:13312\n" \
"v_smfmac_f32_16x16x64_bf16 v[48:51], v[32:35], a[48:55], v10\ns_waitcnt lgkmcnt(2)\nv_smfmac_f32_16x16x64_bf16 v[52:55], v[36:39], a[48:55], v10\nv_mov_b32 v13, s45\nv_smfmac_f32_16x16x64_bf16 v[80:83], v[72:75], a[64:71], v10\nv_cvt_pk_bf16_f32 v88, v48, v49\nv_smfmac_f32_16x16x64_bf16 v[84:87], v[76:79], a[64:71], v10\nv_cvt_pk_bf16_f32 v89, v50, v51\n" \
"v_smfmac_f32_16x16x64_bf16 v[80:83], v[16:19], a[80:87], v10\nv_cvt_pk_bf16_f32 v90, v52, v53\nv_smfmac_f32_16x16x64_bf16 v[84:87], v[20:23], a[80:87], v10\ns_add_u32 s60, s55, 0\nv_cvt_pk_bf16_f32 v91, v54, v55\nv_smfmac_f32_16x16x64_bf16 v[80:83], v[24:27], v[64:71], v10\nv_and_or_b32 v88, v88, -2, v13\nv_smfmac_f32_16x16x64_bf16 v[84:87], v[28:31], v[64:71], v10\n" \
"s_and_b32 s42, s40, 3\ns_lshl_b32 s42, s42, 15\nv_and_or_b32 v90, v90, -2, v13\nv_smfmac_f32_16x16x64_bf16 v[80:83], v[32:35], a[96:103], v10\nbuffer_store_dwordx4 v[88:91], v7, s[36:39], s60 offen offset:0 sc1\nv_mov_b64 v[48:49], 0\nv_mov_b64 v[50:51], 0\nv_mov_b64 v[52:53], 0\n" \
"v_mov_b64 v[54:55], 0\nv_smfmac_f32_16x16x64_bf16 v[84:87], v[36:39], a[96:103], v10\nds_read_b128 v[64:67], v1 offset:20480\nv_mov_b32 v12, s44\nv_smfmac_f32_16x16x64_bf16 v[48:51], v[72:75], a[112:119], v10\nds_read_b128 v[68:71], v1 offset:21504\nv_cvt_pk_bf16_f32 v92, v80, v81\nv_smfmac_f32_16x16x64_bf16 v[52:55], v[76:79], a[112:119], v10\n" \
"s_waitcnt lgkmcnt(2)\nv_cvt_pk_bf16_f32 v93, v82, v83\nv_smfmac_f32_16x16x64_bf16 v[48:51], v[16:19], v[56:63], v10\nv_cvt_pk_bf16_f32 v94, v84, v85\nv_smfmac_f32_16x16x64_bf16 v[52:55], v[20:23], v[56:63], v10\ns_add_u32 s60, s55, 4096\nv_cvt_pk_bf16_f32 v95, v86, v87\nv_smfmac_f32_16x16x64_bf16 v[48:51], v[24:27], a[136:143], v10\n" \
"ds_read_b128 v[56:59], v1 offset:10240\nv_and_or_b32 v92, v92, -2, v13\nv_smfmac_f32_16x16x64_bf16 v[52:55], v[28:31], a[136:143], v10\nds_read_b128 v[60:63], v1 offset:11264\nv_and_or_b32 v94, v94, -2, v13\nv_smfmac_f32_16x16x64_bf16 v[48:51], v[32:35], a[152:159], v10\nbuffer_store_dwordx4 v[92:95], v7, s[36:39], s60 offen offset:0 sc1\nv_mov_b64 v[80:81], 0\n" \
"v_mov_b64 v[82:83], 0\nv_mov_b64 v[84:85], 0\nv_mov_b64 v[86:87], 0\nv_smfmac_f32_16x16x64_bf16 v[52:55], v[36:39], a[152:159], v10\ns_waitcnt vmcnt(6)\ns_barrier\nv_smfmac_f32_16x16x64_bf16 v[80:83], v[72:75], a[168:175], v10\nv_cvt_pk_bf16_f32 v88, v48, v49\n" \
"v_smfmac_f32_16x16x64_bf16 v[84:87], v[76:79], a[168:175], v10\nv_cvt_pk_bf16_f32 v89, v50, v51\nv_smfmac_f32_16x16x64_bf16 v[80:83], v[16:19], a[176:183], v10\ns_cmp_lt_u32 s40, 2\ns_cselect_b32 s54, %[fy], -2147483648\nv_cvt_pk_bf16_f32 v90, v52, v53\nv_smfmac_f32_16x16x64_bf16 v[84:87], v[20:23], a[176:183], v10\ns_add_u32 s60, s55, 16384\n" \
"s_waitcnt lgkmcnt(2)\nv_cvt_pk_bf16_f32 v91, v54, v55\nv_smfmac_f32_16x16x64_bf16 v[80:83], v[24:27], v[64:71], v10\nv_and_or_b32 v88, v88, -2, v13\nv_smfmac_f32_16x16x64_bf16 v[84:87], v[28:31], v[64:71], v10\nv_and_or_b32 v90, v90, -2, v13\nv_smfmac_f32_16x16x64_bf16 v[80:83], v[32:35], a[200:207], v10\nbuffer_store_dwordx4 v[88:91], v7, s[36:39], s60 offen offset:0 sc1\n" \
"v_mov_b64 v[48:49], 0\nv_mov_b64 v[50:51], 0\nv_mov_b64 v[52:53], 0\nv_mov_b64 v[54:55], 0\nv_smfmac_f32_16x16x64_bf16 v[84:87], v[36:39], a[200:207], v10\nds_read_b128 v[64:67], v1 offset:18432\nv_smfmac_f32_16x16x64_bf16 v[48:51], v[72:75], a[216:223], v10\nds_read_b128 v[68:71], v1 offset:19456\n" \
"v_cvt_pk_bf16_f32 v92, v80, v81\nv_smfmac_f32_16x16x64_bf16 v[52:55], v[76:79], a[216:223], v10\ns_waitcnt lgkmcnt(2)\nv_cvt_pk_bf16_f32 v93, v82, v83\nv_smfmac_f32_16x16x64_bf16 v[48:51], v[16:19], v[56:63], v10\nv_cvt_pk_bf16_f32 v94, v84, v85\nv_smfmac_f32_16x16x64_bf16 v[52:55], v[20:23], v[56:63], v10\ns_add_u32 s60, s55, 20480\n" \
"v_cvt_pk_bf16_f32 v95, v86, v87\nv_smfmac_f32_16x16x64_bf16 v[48:51], v[24:27], a[232:239], v10\nds_read_b128 v[56:59], v1 offset:14336\nv_and_or_b32 v92, v92, -2, v13\nv_smfmac_f32_16x16x64_bf16 v[52:55], v[28:31], a[232:239], v10\nds_read_b128 v[60:63], v1 offset:15360\nv_and_or_b32 v94, v94, -2, v13\nv_smfmac_f32_16x16x64_bf16 v[48:51], v[32:35], a[248:255], v10\n" \
"buffer_store_dwordx4 v[92:95], v7, s[36:39], s60 offen offset:0 sc1\nv_mov_b64 v[80:81], 0\nv_mov_b64 v[82:83], 0\nv_mov_b64 v[84:85], 0\nv_mov_b64 v[86:87], 0\nv_smfmac_f32_16x16x64_bf16 v[52:55], v[36:39], a[248:255], v10\nv_smfmac_f32_16x16x64_bf16 v[80:83], v[72:75], v[104:111], v10\nv_cvt_pk_bf16_f32 v88, v48, v49\n" \
"v_smfmac_f32_16x16x64_bf16 v[84:87], v[76:79], v[104:111], v10\nv_cvt_pk_bf16_f32 v89, v50, v51\nv_smfmac_f32_16x16x64_bf16 v[80:83], v[16:19], v[120:127], v10\nv_cvt_pk_bf16_f32 v90, v52, v53\nv_smfmac_f32_16x16x64_bf16 v[84:87], v[20:23], v[120:127], v10\ns_add_u32 s60, s55, 0\ns_waitcnt lgkmcnt(2)\nv_cvt_pk_bf16_f32 v91, v54, v55\n" \
"v_smfmac_f32_16x16x64_bf16 v[80:83], v[24:27], v[64:71], v10\nv_and_or_b32 v88, v88, -2, v13\nv_smfmac_f32_16x16x64_bf16 v[84:87], v[28:31], v[64:71], v10\nv_and_or_b32 v90, v90, -2, v13\nv_smfmac_f32_16x16x64_bf16 v[80:83], v[32:35], v[144:151], v10\nbuffer_store_dwordx4 v[88:91], v7, s[36:39], s60 offen offset:2048 sc1\nv_mov_b64 v[48:49], 0\nv_mov_b64 v[50:51], 0\n" \
"v_mov_b64 v[52:53], 0\nv_mov_b64 v[54:55], 0\nv_smfmac_f32_16x16x64_bf16 v[84:87], v[36:39], v[144:151], v10\nds_read_b128 v[64:67], v1 offset:22528\nv_smfmac_f32_16x16x64_bf16 v[48:51], v[72:75], v[160:167], v10\nds_read_b128 v[68:71], v1 offset:23552\nv_cvt_pk_bf16_f32 v92, v80, v81\nv_smfmac_f32_16x16x64_bf16 v[52:55], v[76:79], v[160:167], v10\n" \
"s_waitcnt lgkmcnt(2)\nv_cvt_pk_bf16_f32 v93, v82, v83\nv_smfmac_f32_16x16x64_bf16 v[48:51], v[16:19], v[56:63], v10\nds_read_b128 v[40:43], v11 offset:49280\nv_cvt_pk_bf16_f32 v94, v84, v85\nv_smfmac_f32_16x16x64_bf16 v[52:55], v[20:23], v[56:63], v10\ns_add_u32 s60, s55, 4096\nv_cvt_pk_bf16_f32 v95, v86, v87\n" \
"v_smfmac_f32_16x16x64_bf16 v[48:51], v[24:27], v[176:183], v10\nds_read_b128 v[56:59], v1 offset:8192\nds_read_b128 v[44:47], v11 offset:50304\nv_and_or_b32 v92, v92, -2, v13\nv_smfmac_f32_16x16x64_bf16 v[52:55], v[28:31], v[176:183], v10\nds_read_b128 v[60:63], v1 offset:9216\nv_and_or_b32 v94, v94, -2, v13\nv_smfmac_f32_16x16x64_bf16 v[48:51], v[32:35], v[192:199], v10\n" \
"buffer_store_dwordx4 v[92:95], v7, s[36:39], s60 offen offset:2048 sc1\nv_mov_b64 v[80:81], 0\nv_mov_b64 v[82:83], 0\nv_mov_b64 v[84:85], 0\nv_mov_b64 v[86:87], 0\nv_smfmac_f32_16x16x64_bf16 v[52:55], v[36:39], v[192:199], v10\nv_smfmac_f32_16x16x64_bf16 v[80:83], v[72:75], v[208:215], v10\nds_read_b128 v[72:75], v11 offset:51328\n" \
"v_cvt_pk_bf16_f32 v88, v48, v49\nv_smfmac_f32_16x16x64_bf16 v[84:87], v[76:79], v[208:215], v10\nds_read_b128 v[76:79], v11 offset:52352\nv_cvt_pk_bf16_f32 v89, v50, v51\nv_smfmac_f32_16x16x64_bf16 v[80:83], v[16:19], v[224:231], v10\nds_read_b128 v[16:19], v11 offset:53376\nv_cvt_pk_bf16_f32 v90, v52, v53\nv_smfmac_f32_16x16x64_bf16 v[84:87], v[20:23], v[224:231], v10\n" \
"s_add_u32 s60, s55, 16384\ns_waitcnt lgkmcnt(7)\nds_read_b128 v[20:23], v11 offset:54400\nv_cvt_pk_bf16_f32 v91, v54, v55\nv_smfmac_f32_16x16x64_bf16 v[80:83], v[24:27], v[64:71], v10\nds_read_b128 v[24:27], v11 offset:55424\nv_and_or_b32 v88, v88, -2, v13\nv_smfmac_f32_16x16x64_bf16 v[84:87], v[28:31], v[64:71], v10\n" \
"ds_read_b128 v[28:31], v11 offset:56448\nv_and_or_b32 v90, v90, -2, v13\nv_smfmac_f32_16x16x64_bf16 v[80:83], v[32:35], v[248:255], v10\nbuffer_store_dwordx4 v[88:91], v7, s[36:39], s60 offen offset:2048 sc1\nv_mov_b64 v[48:49], 0\nv_mov_b64 v[50:51], 0\nv_mov_b64 v[52:53], 0\nv_mov_b64 v[54:55], 0\n" \
"v_smfmac_f32_16x16x64_bf16 v[84:87], v[36:39], v[248:255], v10\ns_waitcnt lgkmcnt(9)\nds_read_b128 v[64:67], v1 offset:16384\nv_smfmac_f32_16x16x64_bf16 v[48:51], v[40:43], a[8:15], v10\ns_waitcnt lgkmcnt(8)\nds_read_b128 v[68:71], v1 offset:17408\nv_cvt_pk_bf16_f32 v92, v80, v81\nv_smfmac_f32_16x16x64_bf16 v[52:55], v[44:47], a[8:15], v10\n" \
"s_waitcnt lgkmcnt(7)\nv_cvt_pk_bf16_f32 v93, v82, v83\nv_smfmac_f32_16x16x64_bf16 v[48:51], v[72:75], v[56:63], v10\ns_waitcnt lgkmcnt(6)\nv_cvt_pk_bf16_f32 v94, v84, v85\nv_smfmac_f32_16x16x64_bf16 v[52:55], v[76:79], v[56:63], v10\ns_add_u32 s60, s55, 20480\ns_waitcnt lgkmcnt(5)\n" \
"v_cvt_pk_bf16_f32 v95, v86, v87\nv_smfmac_f32_16x16x64_bf16 v[48:51], v[16:19], a[32:39], v10\ns_waitcnt lgkmcnt(4)\nds_read_b128 v[56:59], v1 offset:12288\nv_and_or_b32 v92, v92, -2, v13\nv_smfmac_f32_16x16x64_bf16 v[52:55], v[20:23], a[32:39], v10\ns_waitcnt lgkmcnt(4)\nds_read_b128 v[60:63], v1 offset:13312\n" \
"v_and_or_b32 v94, v94, -2, v13\nv_smfmac_f32_16x16x64_bf16 v[48:51], v[24:27], a[48:55], v10\ns_waitcnt lgkmcnt(4)\nbuffer_store_dwordx4 v[92:95], v7, s[36:39], s60 offen offset:2048 sc1\nv_mov_b64 v[80:81], 0\nv_mov_b64 v[82:83], 0\nv_mov_b64 v[84:85], 0\nv_mov_b64 v[86:87], 0\n" \
"v_smfmac_f32_16x16x64_bf16 v[52:55], v[28:31], a[48:55], v10\nv_smfmac_f32_16x16x64_bf16 v[80:83], v[40:43], a[64:71], v10\nv_cvt_pk_bf16_f32 v88, v48, v49\nv_smfmac_f32_16x16x64_bf16 v[84:87], v[44:47], a[64:71], v10\nv_cvt_pk_bf16_f32 v89, v50, v51\nv_smfmac_f32_16x16x64_bf16 v[80:83], v[72:75], a[80:87], v10\nv_cvt_pk_bf16_f32 v90, v52, v53\nv_smfmac_f32_16x16x64_bf16 v[84:87], v[76:79], a[80:87], v10\n" \
"s_add_u32 s60, s55, 0\ns_waitcnt lgkmcnt(2)\nv_cvt_pk_bf16_f32 v91, v54, v55\nv_smfmac_f32_16x16x64_bf16 v[80:83], v[16:19], v[64:71], v10\nv_and_or_b32 v88, v88, -2, v13\nv_smfmac_f32_16x16x64_bf16 v[84:87], v[20:23], v[64:71], v10\nv_and_or_b32 v90, v90, -2, v13\nv_smfmac_f32_16x16x64_bf16 v[80:83], v[24:27], a[96:103], v10\n" \
"buffer_store_dwordx4 v[88:91], v7, s[36:39], s60 offen offset:512 sc1\nv_mov_b64 v[48:49], 0\nv_mov_b64 v[50:51], 0\nv_mov_b64 v[52:53], 0\nv_mov_b64 v[54:55], 0\nv_smfmac_f32_16x16x64_bf16 v[84:87], v[28:31], a[96:103], v10\nds_read_b128 v[64:67], v1 offset:20480\nv_smfmac_f32_16x16x64_bf16 v[48:51], v[40:43], a[112:119], v10\n" \
"ds_read_b128 v[68:71], v1 offset:21504\nv_cvt_pk_bf16_f32 v92, v80, v81\nv_smfmac_f32_16x16x64_bf16 v[52:55], v[44:47], a[112:119], v10\ns_waitcnt lgkmcnt(2)\nv_cvt_pk_bf16_f32 v93, v82, v83\nv_smfmac_f32_16x16x64_bf16 v[48:51], v[72:75], v[56:63], v10\nv_cvt_pk_bf16_f32 v94, v84, v85\nv_smfmac_f32_16x16x64_bf16 v[52:55], v[76:79], v[56:63], v10\n" \
"s_add_u32 s60, s55, 4096\nv_cvt_pk_bf16_f32 v95, v86, v87\nv_smfmac_f32_16x16x64_bf16 v[48:51], v[16:19], a[136:143], v10\nds_read_b128 v[56:59], v1 offset:10240\nv_and_or_b32 v92, v92, -2, v13\nv_smfmac_f32_16x16x64_bf16 v[52:55], v[20:23], a[136:143], v10\nds_read_b128 v[60:63], v1 offset:11264\nv_and_or_b32 v94, v94, -2, v13\n" \
"v_smfmac_f32_16x16x64_bf16 v[48:51], v[24:27], a[152:159], v10\nbuffer_store_dwordx4 v[92:95], v7, s[36:39], s60 offen offset:512 sc1\nv_mov_b64 v[80:81], 0\nv_mov_b64 v[82:83], 0\nv_mov_b64 v[84:85], 0\nv_mov_b64 v[86:87], 0\nv_smfmac_f32_16x16x64_bf16 v[52:55], v[28:31], a[152:159], v10\nv_smfmac_f32_16x16x64_bf16 v[80:83], v[40:43], a[168:175], v10\n" \
"v_cvt_pk_bf16_f32 v88, v48, v49\nv_smfmac_f32_16x16x64_bf16 v[84:87], v[44:47], a[168:175], v10\nv_cvt_pk_bf16_f32 v89, v50, v51\nv_smfmac_f32_16x16x64_bf16 v[80:83], v[72:75], a[176:183], v10\nv_cvt_pk_bf16_f32 v90, v52, v53\nv_smfmac_f32_16x16x64_bf16 v[84:87], v[76:79], a[176:183], v10\ns_add_u32 s60, s55, 16384\ns_waitcnt lgkmcnt(2)\n" \
"v_cvt_pk_bf16_f32 v91, v54, v55\nv_smfmac_f32_16x16x64_bf16 v[80:83], v[16:19], v[64:71], v10\nv_and_or_b32 v88, v88, -2, v13\nv_smfmac_f32_16x16x64_bf16 v[84:87], v[20:23], v[64:71], v10\nv_and_or_b32 v90, v90, -2, v13\nv_smfmac_f32_16x16x64_bf16 v[80:83], v[24:27], a[200:207], v10\nbuffer_store_dwordx4 v[88:91], v7, s[36:39], s60 offen offset:512 sc1\nv_mov_b64 v[48:49], 0\n" \
"v_mov_b64 v[50:51], 0\nv_mov_b64 v[52:53], 0\nv_mov_b64 v[54:55], 0\nv_smfmac_f32_16x16x64_bf16 v[84:87], v[28:31], a[200:207], v10\nds_read_b128 v[64:67], v1 offset:18432\nv_smfmac_f32_16x16x64_bf16 v[48:51], v[40:43], a[216:223], v10\nds_read_b128 v[68:71], v1 offset:19456\nv_cvt_pk_bf16_f32 v92, v80, v81\n" \
"v_smfmac_f32_16x16x64_bf16 v[52:55], v[44:47], a[216:223], v10\ns_waitcnt lgkmcnt(2)\nv_cvt_pk_bf16_f32 v93, v82, v83\nv_smfmac_f32_16x16x64_bf16 v[48:51], v[72:75], v[56:63], v10\nv_cvt_pk_bf16_f32 v94, v84, v85\nv_smfmac_f32_16x16x64_bf16 v[52:55], v[76:79], v[56:63], v10\ns_add_u32 s60, s55, 20480\nv_cvt_pk_bf16_f32 v95, v86, v87\n" \
"v_smfmac_f32_16x16x64_bf16 v[48:51], v[16:19], a[232:239], v10\nds_read_b128 v[56:59], v1 offset:14336\nv_and_or_b32 v92, v92, -2, v13\nv_smfmac_f32_16x16x64_bf16 v[52:55], v[20:23], a[232:239], v10\nds_read_b128 v[60:63], v1 offset:15360\nv_and_or_b32 v94, v94, -2, v13\nv_smfmac_f32_16x16x64_bf16 v[48:51], v[24:27], a[248:255], v10\nbuffer_store_dwordx4 v[92:95], v7, s[36:39], s60 offen offset:512 sc1\n" \
"v_mov_b64 v[80:81], 0\nv_mov_b64 v[82:83], 0\nv_mov_b64 v[84:85], 0\nv_mov_b64 v[86:87], 0\nv_smfmac_f32_16x16x64_bf16 v[52:55], v[28:31], a[248:255], v10\nv_smfmac_f32_16x16x64_bf16 v[80:83], v[40:43], v[104:111], v10\nv_cvt_pk_bf16_f32 v88, v48, v49\nv_smfmac_f32_16x16x64_bf16 v[84:87], v[44:47], v[104:111], v10\n" \
"v_cvt_pk_bf16_f32 v89, v50, v51\nv_smfmac_f32_16x16x64_bf16 v[80:83], v[72:75], v[120:127], v10\nv_cvt_pk_bf16_f32 v90, v52, v53\nv_smfmac_f32_16x16x64_bf16 v[84:87], v[76:79], v[120:127], v10\ns_add_u32 s60, s55, 0\ns_waitcnt lgkmcnt(2)\nv_cvt_pk_bf16_f32 v91, v54, v55\nv_smfmac_f32_16x16x64_bf16 v[80:83], v[16:19], v[64:71], v10\n" \
"v_and_or_b32 v88, v88, -2, v13\nv_smfmac_f32_16x16x64_bf16 v[84:87], v[20:23], v[64:71], v10\nv_and_or_b32 v90, v90, -2, v13\nv_smfmac_f32_16x16x64_bf16 v[80:83], v[24:27], v[144:151], v10\nbuffer_store_dwordx4 v[88:91], v7, s[36:39], s60 offen offset:2560 sc1\nv_mov_b64 v[48:49], 0\nv_mov_b64 v[50:51], 0\nv_mov_b64 v[52:53], 0\n" \
"v_mov_b64 v[54:55], 0\nv_smfmac_f32_16x16x64_bf16 v[84:87], v[28:31], v[144:151], v10\nds_read_b128 v[64:67], v1 offset:22528\nv_smfmac_f32_16x16x64_bf16 v[48:51], v[40:43], v[160:167], v10\nds_read_b128 v[68:71], v1 offset:23552\nv_cvt_pk_bf16_f32 v92, v80, v81\nv_smfmac_f32_16x16x64_bf16 v[52:55], v[44:47], v[160:167], v10\ns_waitcnt lgkmcnt(2)\n" \
"v_cvt_pk_bf16_f32 v93, v82, v83\nv_smfmac_f32_16x16x64_bf16 v[48:51], v[72:75], v[56:63], v10\nv_cvt_pk_bf16_f32 v94, v84, v85\nv_smfmac_f32_16x16x64_bf16 v[52:55], v[76:79], v[56:63], v10\ns_add_u32 s60, s55, 4096\nv_cvt_pk_bf16_f32 v95, v86, v87\nv_smfmac_f32_16x16x64_bf16 v[48:51], v[16:19], v[176:183], v10\nv_and_or_b32 v92, v92, -2, v13\n" \
"v_smfmac_f32_16x16x64_bf16 v[52:55], v[20:23], v[176:183], v10\nv_and_or_b32 v94, v94, -2, v13\nv_smfmac_f32_16x16x64_bf16 v[48:51], v[24:27], v[192:199], v10\nbuffer_store_dwordx4 v[92:95], v7, s[36:39], s60 offen offset:2560 sc1\nv_mov_b64 v[80:81], 0\nv_mov_b64 v[82:83], 0\nv_mov_b64 v[84:85], 0\nv_mov_b64 v[86:87], 0\n" \
"v_smfmac_f32_16x16x64_bf16 v[52:55], v[28:31], v[192:199], v10\nds_read_b128 v[32:35], v2 offset:0\nv_smfmac_f32_16x16x64_bf16 v[80:83], v[40:43], v[208:215], v10\nds_read_b128 v[36:39], v2 offset:256\nv_cvt_pk_bf16_f32 v88, v48, v49\nv_smfmac_f32_16x16x64_bf16 v[84:87], v[44:47], v[208:215], v10\nv_cvt_pk_bf16_f32 v89, v50, v51\nv_smfmac_f32_16x16x64_bf16 v[80:83], v[72:75], v[224:231], v10\n" \
"ds_read_b128 v[40:43], v2 offset:2048\nv_cvt_pk_bf16_f32 v90, v52, v53\nv_smfmac_f32_16x16x64_bf16 v[84:87], v[76:79], v[224:231], v10\ns_add_u32 s60, s55, 16384\ns_waitcnt lgkmcnt(3)\nds_read_b128 v[44:47], v2 offset:2304\nv_cvt_pk_bf16_f32 v91, v54, v55\nv_smfmac_f32_16x16x64_bf16 v[80:83], v[16:19], v[64:71], v10\n" \
"v_and_or_b32 v88, v88, -2, v13\nv_smfmac_f32_16x16x64_bf16 v[84:87], v[20:23], v[64:71], v10\nv_and_or_b32 v90, v90, -2, v13\nv_smfmac_f32_16x16x64_bf16 v[80:83], v[24:27], v[248:255], v10\nbuffer_store_dwordx4 v[88:91], v7, s[36:39], s60 offen offset:2560 sc1\nv_mov_b64 v[48:49], 0\nv_mov_b64 v[50:51], 0\nv_mov_b64 v[52:53], 0\n" \
"v_mov_b64 v[54:55], 0\nv_smfmac_f32_16x16x64_bf16 v[84:87], v[28:31], v[248:255], v10\ns_add_u32 s60, s55, 20480\ns_cmp_eq_u64 s[64:65], 0\nv_mov_b32 v15, s40\ns_cbranch_scc1 .Lvu_tok%=\nv_cvt_pk_bf16_f32 v92, v80, v81\nv_cvt_pk_bf16_f32 v93, v82, v83\n" \
"s_nop 1\nv_cvt_pk_bf16_f32 v94, v84, v85\nv_cvt_pk_bf16_f32 v95, v86, v87\nv_and_or_b32 v92, v92, -2, v13\nv_and_or_b32 v94, v94, -2, v13\nbuffer_store_dwordx4 v[92:95], v7, s[36:39], s60 offen offset:2560 sc1\ns_mov_b32 s60, -2147483648\ns_cmp_lt_i32 s40, 63\n" \
"s_cbranch_scc1 .Lvu_f0%=\ns_mov_b32 s61, -2147483648\nv_add_u32 v14, s57, v0\ns_waitcnt lgkmcnt(0)\n.Lvu_nov%=:\ns_add_u32 m0, s57, 16384\ns_nop 0\nbuffer_load_dwordx4 v3, s[36:39], s42 offen offset:0 sc1 lds\n" \
"buffer_load_dwordx4 v3, s[36:39], s42 offen offset:1024 sc1 lds\nbuffer_load_dwordx4 v3, s[36:39], s42 offen offset:2048 sc1 lds\nbuffer_load_dwordx4 v3, s[36:39], s42 offen offset:3072 sc1 lds\ns_waitcnt vmcnt(0)\nds_read_b128 v[80:83], v14 offset:16384\nds_read_b128 v[84:87], v14 offset:17408\nds_read_b128 v[88:91], v14 offset:18432\nds_read_b128 v[92:95], v14 offset:19456\n" \
"s_waitcnt lgkmcnt(2)\nv_max3_u32 v72, v80, v82, v84\ns_waitcnt lgkmcnt(1)\nv_max3_u32 v72, v72, v86, v88\ns_waitcnt lgkmcnt(0)\nv_max3_u32 v72, v72, v90, v92\nv_max_u32 v72, v72, v94\nv_cmp_eq_u32 vcc, -1, v72\n" \
"s_cbranch_vccz .Lvu_novfok%=\ns_add_u32 s43, s43, 1\ns_cmp_lt_u32 s43, 262144\ns_cbranch_scc1 .Lvu_nov%=\ns_branch .Lvu_abort%=\n.Lvu_novfok%=:\ns_waitcnt lgkmcnt(0)\ns_barrier\n" \
"s_memrealtime s[68:69]\nds_read_b128 v[56:59], v1 offset:0\nds_read_b128 v[60:63], v1 offset:1024\nds_read_b128 v[80:83], v0 offset:16384\nds_read_b128 v[84:87], v0 offset:17408\nds_read_b128 v[88:91], v0 offset:18432\nds_read_b128 v[92:95], v0 offset:19456\nds_read_b128 v[16:19], v0 offset:20480\n" \
"ds_read_b128 v[20:23], v0 offset:21504\nds_read_b128 v[32:35], v0 offset:22528\nds_read_b128 v[36:39], v0 offset:23552\ns_waitcnt lgkmcnt(0)\ns_branch .Lvu_crit%=\n.Lvu_re%=:\n.Lvu_re2%=:\ns_add_u32 m0, s57, 16384\n" \
"v_max_u32 v72, v80, v82\nv_cmp_eq_u32 vcc, -1, v72\ns_cbranch_vccz .Lvu_sk1893%=\ns_mov_b64 exec, vcc\nbuffer_load_dwordx4 v3, s[36:39], s42 offen offset:0 sc1 lds\ns_mov_b64 exec, -1\n.Lvu_sk1893%=:\nv_max_u32 v72, v84, v86\n" \
"v_cmp_eq_u32 vcc, -1, v72\ns_cbranch_vccz .Lvu_sk1900%=\ns_mov_b64 exec, vcc\nbuffer_load_dwordx4 v3, s[36:39], s42 offen offset:1024 sc1 lds\ns_mov_b64 exec, -1\n.Lvu_sk1900%=:\nv_max_u32 v72, v88, v90\nv_cmp_eq_u32 vcc, -1, v72\n" \
"s_cbranch_vccz .Lvu_sk1907%=\ns_mov_b64 exec, vcc\nbuffer_load_dwordx4 v3, s[36:39], s42 offen offset:2048 sc1 lds\ns_mov_b64 exec, -1\n.Lvu_sk1907%=:\nv_max_u32 v72, v92, v94\nv_cmp_eq_u32 vcc, -1, v72\ns_cbranch_vccz .Lvu_sk1914%=\n" \
"s_mov_b64 exec, vcc\nbuffer_load_dwordx4 v3, s[36:39], s42 offen offset:3072 sc1 lds\ns_mov_b64 exec, -1\n.Lvu_sk1914%=:\n.Lvu_skf%=:\ns_waitcnt vmcnt(0)\nds_read_b128 v[80:83], v14 offset:16384\nds_read_b128 v[84:87], v14 offset:17408\n" \
"ds_read_b128 v[88:91], v14 offset:18432\nds_read_b128 v[92:95], v14 offset:19456\ns_waitcnt lgkmcnt(2)\nv_max3_u32 v72, v80, v82, v84\ns_waitcnt lgkmcnt(1)\nv_max3_u32 v72, v72, v86, v88\ns_waitcnt lgkmcnt(0)\nv_max3_u32 v72, v72, v90, v92\n" \
"v_max_u32 v72, v72, v94\nv_cmp_eq_u32 vcc, -1, v72\ns_cbranch_vccz .Lvu_rer%=\ns_add_u32 s43, s43, 1\ns_cmp_lt_u32 s43, 262144\ns_cbranch_scc1 .Lvu_re2%=\ns_branch .Lvu_abort%=\n.Lvu_rew%=:\n" \
"ds_read_b128 v[80:83], v14 offset:16384\nds_read_b128 v[84:87], v14 offset:17408\nds_read_b128 v[88:91], v14 offset:18432\nds_read_b128 v[92:95], v14 offset:19456\ns_waitcnt lgkmcnt(0)\ns_branch .Lvu_rer%=\n.Lvu_first%=:\ns_nop 7\n" \
"v_cvt_pk_bf16_f32 v76, v24, v25\nv_cvt_pk_bf16_f32 v77, v26, v27\nv_cvt_pk_bf16_f32 v78, v28, v29\nv_cvt_pk_bf16_f32 v79, v30, v31\nds_write2st64_b64 v9, v[76:77], v[78:79] offset0:2 offset1:10\ns_waitcnt vmcnt(0) lgkmcnt(0)\ns_barrier\nds_read_b128 v[56:59], v1 offset:8192\n" \
"ds_read_b128 v[60:63], v1 offset:9216\nds_read_b128 v[64:67], v1 offset:16384\nds_read_b128 v[68:71], v1 offset:17408\nds_read_b128 v[72:75], v11 offset:49152\nds_read_b128 v[76:79], v11 offset:50176\nds_read_b128 v[16:19], v11 offset:51200\nds_read_b128 v[20:23], v11 offset:52224\nds_read_b128 v[24:27], v11 offset:53248\n" \
"ds_read_b128 v[28:31], v11 offset:54272\nds_read_b128 v[32:35], v11 offset:55296\nds_read_b128 v[36:39], v11 offset:56320\ns_waitcnt lgkmcnt(0)\ns_add_u32 m0, s57, 0\ns_add_u32 s58, s61, 0\nbuffer_load_dwordx4 v0, s[36:39], s58 offen offset:0 sc1 lds\nbuffer_load_dwordx4 v0, s[36:39], s58 offen offset:1024 sc1 lds\n" \
"buffer_load_dwordx4 v0, s[36:39], s58 offen offset:2048 sc1 lds\nbuffer_load_dwordx4 v0, s[36:39], s58 offen offset:3072 sc1 lds\ns_mov_b32 s59, -2147483648\nbuffer_store_dword v14, off, s[36:39], s59\nbuffer_store_dword v14, off, s[36:39], s59\nbuffer_store_dword v14, off, s[36:39], s59\ns_branch .Lvu_y%=\n.Lvu_last%=:\n" \
"s_nop 7\nv_cvt_pk_bf16_f32 v94, v44, v45\nv_cvt_pk_bf16_f32 v95, v46, v47\nv_and_or_b32 v94, v94, -2, v12\ns_nop 0\nv_permlane16_swap_b32 v93, v95\nv_permlane16_swap_b32 v92, v94\ns_add_u32 s63, s59, 135168\n" \
"buffer_store_dwordx4 v[92:95], v6, s[36:39], s63 offen sc1\ns_branch .Lvu_end%=\n.Lvu_abort%=:\ns_mov_b64 exec, -1\nv_mov_b32 v16, 0\ns_movk_i32 s44, 6400\nbuffer_store_dword v16, off, s[36:39], s44 sc1\n.Lvu_end%=:\n" \
"s_waitcnt vmcnt(0) lgkmcnt(0)\n" \
""
#define RD_ASM \
"v_mbcnt_lo_u32_b32 v255, -1, 0\nv_mbcnt_hi_u32_b32 v255, -1, v255\nv_lshlrev_b32 v255, 2, v255\nv_add_u32 v255, %[tb], v255\nds_read_b32 v196, v255 offset:0\nds_read_b32 v197, v255 offset:256\nds_read_b32 v198, v255 offset:512\nds_read_b32 v199, v255 offset:768\n" \
"ds_read_b32 v200, v255 offset:1024\nds_read_b32 v201, v255 offset:1280\nds_read_b32 v202, v255 offset:1536\nds_read_b32 v203, v255 offset:1792\nds_read_b32 v204, v255 offset:2048\nds_read_b32 v205, v255 offset:2304\nds_read_b32 v206, v255 offset:2560\nds_read_b32 v207, v255 offset:2816\n" \
"s_waitcnt lgkmcnt(0)\nv_accvgpr_write_b32 a32, v206\nv_accvgpr_write_b32 a33, v207\ns_mov_b32 s40, 0\ns_mov_b32 s43, 0\ns_add_u32 s57, %[su], 4096\ns_add_u32 s56, %[sp], 4096\ns_mov_b32 s41, %[sy]\n" \
".Lrd_yflag%=:\nv_accvgpr_read_b32 v206, a32\nbuffer_load_dword v208, v206, %[rs], 0 offen sc1\ns_waitcnt vmcnt(0)\nv_cmp_gt_i32 vcc, s40, v208\ns_cbranch_vccz .Lrd_yld%=\ns_add_u32 s43, s43, 1\ns_cmp_lt_u32 s43, 262144\n" \
"s_cbranch_scc1 .Lrd_yflag%=\ns_branch .Lrd_abort%=\n.Lrd_yld%=:\ns_add_u32 s60, s41, 0\nbuffer_load_dwordx4 v[0:3], v196, %[rs], s60 offen offset:0 sc1\ns_add_u32 s60, s41, 0\nbuffer_load_dwordx4 v[32:35], v196, %[rs], s60 offen offset:1024 sc1\ns_add_u32 s60, s41, 0\n" \
"buffer_load_dwordx4 v[64:67], v196, %[rs], s60 offen offset:2048 sc1\ns_add_u32 s60, s41, 131072\nbuffer_load_dwordx4 v[4:7], v196, %[rs], s60 offen offset:0 sc1\ns_add_u32 s60, s41, 131072\nbuffer_load_dwordx4 v[36:39], v196, %[rs], s60 offen offset:1024 sc1\ns_add_u32 s60, s41, 131072\nbuffer_load_dwordx4 v[68:71], v196, %[rs], s60 offen offset:2048 sc1\ns_add_u32 s60, s41, 262144\n" \
"buffer_load_dwordx4 v[8:11], v196, %[rs], s60 offen offset:0 sc1\ns_add_u32 s60, s41, 262144\nbuffer_load_dwordx4 v[40:43], v196, %[rs], s60 offen offset:1024 sc1\ns_add_u32 s60, s41, 262144\nbuffer_load_dwordx4 v[72:75], v196, %[rs], s60 offen offset:2048 sc1\ns_add_u32 s60, s41, 393216\nbuffer_load_dwordx4 v[12:15], v196, %[rs], s60 offen offset:0 sc1\ns_add_u32 s60, s41, 393216\n" \
"buffer_load_dwordx4 v[44:47], v196, %[rs], s60 offen offset:1024 sc1\ns_add_u32 s60, s41, 393216\nbuffer_load_dwordx4 v[76:79], v196, %[rs], s60 offen offset:2048 sc1\ns_add_u32 s60, s41, 524288\nbuffer_load_dwordx4 v[16:19], v196, %[rs], s60 offen offset:0 sc1\ns_add_u32 s60, s41, 524288\nbuffer_load_dwordx4 v[48:51], v196, %[rs], s60 offen offset:1024 sc1\ns_add_u32 s60, s41, 524288\n" \
"buffer_load_dwordx4 v[80:83], v196, %[rs], s60 offen offset:2048 sc1\ns_add_u32 s60, s41, 655360\nbuffer_load_dwordx4 v[20:23], v196, %[rs], s60 offen offset:0 sc1\ns_add_u32 s60, s41, 655360\nbuffer_load_dwordx4 v[52:55], v196, %[rs], s60 offen offset:1024 sc1\ns_add_u32 s60, s41, 655360\nbuffer_load_dwordx4 v[84:87], v196, %[rs], s60 offen offset:2048 sc1\ns_add_u32 s60, s41, 786432\n" \
"buffer_load_dwordx4 v[24:27], v196, %[rs], s60 offen offset:0 sc1\ns_add_u32 s60, s41, 786432\nbuffer_load_dwordx4 v[56:59], v196, %[rs], s60 offen offset:1024 sc1\ns_add_u32 s60, s41, 786432\nbuffer_load_dwordx4 v[88:91], v196, %[rs], s60 offen offset:2048 sc1\ns_add_u32 s60, s41, 917504\nbuffer_load_dwordx4 v[28:31], v196, %[rs], s60 offen offset:0 sc1\ns_add_u32 s60, s41, 917504\n" \
"buffer_load_dwordx4 v[60:63], v196, %[rs], s60 offen offset:1024 sc1\ns_add_u32 s60, s41, 917504\nbuffer_load_dwordx4 v[92:95], v196, %[rs], s60 offen offset:2048 sc1\ns_add_u32 s60, s41, 0\nbuffer_load_dwordx4 v[96:99], v196, %[rs], s60 offen offset:3072 sc1\ns_add_u32 s60, s41, 131072\nbuffer_load_dwordx4 v[100:103], v196, %[rs], s60 offen offset:3072 sc1\ns_add_u32 s60, s41, 262144\n" \
"buffer_load_dwordx4 v[104:107], v196, %[rs], s60 offen offset:3072 sc1\ns_add_u32 s60, s41, 393216\nbuffer_load_dwordx4 v[108:111], v196, %[rs], s60 offen offset:3072 sc1\ns_add_u32 s60, s41, 524288\nbuffer_load_dwordx4 v[112:115], v196, %[rs], s60 offen offset:3072 sc1\ns_add_u32 s60, s41, 655360\nbuffer_load_dwordx4 v[116:119], v196, %[rs], s60 offen offset:3072 sc1\ns_add_u32 s60, s41, 786432\n" \
"buffer_load_dwordx4 v[120:123], v196, %[rs], s60 offen offset:3072 sc1\ns_add_u32 s60, s41, 917504\nbuffer_load_dwordx4 v[124:127], v196, %[rs], s60 offen offset:3072 sc1\ns_mov_b64 exec, -1\nv_accvgpr_read_b32 v212, a33\nbuffer_load_dwordx2 v[214:215], v212, %[rs], 0 offen sc1\ns_waitcnt vmcnt(0)\ns_branch .Lrd_ychk%=\n" \
".Lrd_u2%=:\nbuffer_load_dwordx4 v[136:139], v196, %[rs], %[su] offen offset:2048 sc1\nbuffer_load_dwordx4 v[140:143], v196, %[rs], %[su] offen offset:3072 sc1\ns_add_u32 s43, s43, 1\ns_cmp_ge_u32 s43, 262144\ns_cbranch_scc1 .Lrd_abort%=\ns_waitcnt vmcnt(0)\nv_bitop3_b32 v208, v136, v138, s54 bitop3:0x7e\n" \
"v_bitop3_b32 v209, v140, v142, s54 bitop3:0x7e\nv_bitop3_b32 v208, v208, 1, v209 bitop3:0xc8\nv_cmp_ne_u32 vcc, 0, v208\ns_cbranch_vccnz .Lrd_u2%=\n.Lrd_u3%=:\nbuffer_load_dwordx4 v[128:131], v196, %[rs], %[su] offen offset:0 sc1\nbuffer_load_dwordx4 v[132:135], v196, %[rs], %[su] offen offset:1024 sc1\nbuffer_load_dwordx4 v[144:147], v196, %[rs], s57 offen offset:0 sc1\n" \
"buffer_load_dwordx4 v[148:151], v196, %[rs], s57 offen offset:1024 sc1\nbuffer_load_dwordx4 v[152:155], v196, %[rs], s57 offen offset:2048 sc1\nbuffer_load_dwordx4 v[156:159], v196, %[rs], s57 offen offset:3072 sc1\nbuffer_load_dwordx4 v[160:163], v196, %[rs], %[sp] offen offset:0 sc1\nbuffer_load_dwordx4 v[164:167], v196, %[rs], %[sp] offen offset:1024 sc1\nbuffer_load_dwordx4 v[168:171], v196, %[rs], %[sp] offen offset:2048 sc1\nbuffer_load_dwordx4 v[172:175], v196, %[rs], %[sp] offen offset:3072 sc1\nbuffer_load_dwordx4 v[176:179], v196, %[rs], s56 offen offset:0 sc1\n" \
".Lrd_ra%=:\ns_waitcnt vmcnt(5)\nv_bitop3_b32 v208, v128, v130, s54 bitop3:0x7e\nv_bitop3_b32 v209, v136, v138, s54 bitop3:0x7e\nv_bitop3_b32 v208, v208, 1, v209 bitop3:0xc8\nv_cmp_ne_u32 s[86:87], 0, v208\nv_bitop3_b32 v208, v144, v146, s54 bitop3:0x7e\nv_bitop3_b32 v209, v152, v154, s54 bitop3:0x7e\n" \
"v_bitop3_b32 v208, v208, 1, v209 bitop3:0xc8\nv_cmp_ne_u32 s[88:89], 0, v208\nv_bitop3_b32 v208, v132, v134, s54 bitop3:0x7e\nv_bitop3_b32 v209, v140, v142, s54 bitop3:0x7e\nv_bitop3_b32 v208, v208, 1, v209 bitop3:0xc8\nv_cmp_ne_u32 s[68:69], 0, v208\nv_bitop3_b32 v208, v148, v150, s54 bitop3:0x7e\nv_bitop3_b32 v209, v156, v158, s54 bitop3:0x7e\n" \
"v_bitop3_b32 v208, v208, 1, v209 bitop3:0xc8\nv_cmp_ne_u32 s[62:63], 0, v208\ns_or_b64 vcc, s[86:87], s[88:89]\ns_or_b64 vcc, vcc, s[68:69]\ns_or_b64 vcc, vcc, s[62:63]\ns_waitcnt vmcnt(4)\nv_smfmac_f32_16x16x64_bf16 v[180:183], v[160:163], v[0:7], v197\nv_bitop3_b32 v208, v160, v162, s54 bitop3:0x7e\n" \
"v_smfmac_f32_16x16x64_bf16 v[184:187], v[160:163], v[32:39], v197\nv_cmp_gt_i32 s[46:47], 0, v208\nv_smfmac_f32_16x16x64_bf16 v[188:191], v[160:163], v[64:71], v197\nv_smfmac_f32_16x16x64_bf16 v[192:195], v[160:163], v[96:103], v197\ns_or_b64 vcc, vcc, s[46:47]\ns_waitcnt vmcnt(3)\nv_smfmac_f32_16x16x64_bf16 v[180:183], v[164:167], v[8:15], v197\nv_bitop3_b32 v208, v164, v166, s54 bitop3:0x7e\n" \
"v_smfmac_f32_16x16x64_bf16 v[184:187], v[164:167], v[40:47], v197\nv_cmp_gt_i32 s[48:49], 0, v208\nv_smfmac_f32_16x16x64_bf16 v[188:191], v[164:167], v[72:79], v197\nv_smfmac_f32_16x16x64_bf16 v[192:195], v[164:167], v[104:111], v197\ns_or_b64 vcc, vcc, s[48:49]\ns_waitcnt vmcnt(2)\nv_smfmac_f32_16x16x64_bf16 v[180:183], v[168:171], v[16:23], v197\nv_bitop3_b32 v208, v168, v170, s54 bitop3:0x7e\n" \
"v_smfmac_f32_16x16x64_bf16 v[184:187], v[168:171], v[48:55], v197\nv_cmp_gt_i32 s[50:51], 0, v208\nv_smfmac_f32_16x16x64_bf16 v[188:191], v[168:171], v[80:87], v197\nv_smfmac_f32_16x16x64_bf16 v[192:195], v[168:171], v[112:119], v197\ns_or_b64 vcc, vcc, s[50:51]\ns_waitcnt vmcnt(1)\nv_smfmac_f32_16x16x64_bf16 v[180:183], v[172:175], v[24:31], v197\ns_waitcnt vmcnt(0)\n" \
"v_bitop3_b32 v208, v172, v174, s54 bitop3:0x7e\nv_smfmac_f32_16x16x64_bf16 v[180:183], v[176:179], v[128:135], v197\nv_bitop3_b32 v209, v176, v178, s54 bitop3:0x7e\nv_smfmac_f32_16x16x64_bf16 v[184:187], v[172:175], v[56:63], v197\nv_bitop3_b32 v210, v177, v179, s54 bitop3:0x7e\nv_smfmac_f32_16x16x64_bf16 v[184:187], v[176:179], v[136:143], v197\nv_cmp_gt_i32 s[52:53], 0, v208\nv_smfmac_f32_16x16x64_bf16 v[188:191], v[172:175], v[88:95], v197\n" \
"v_or_b32 v209, v209, v210\nv_smfmac_f32_16x16x64_bf16 v[188:191], v[176:179], v[144:151], v197\ns_or_b64 vcc, vcc, s[52:53]\nv_cmp_gt_i32 s[64:65], 0, v209\nv_smfmac_f32_16x16x64_bf16 v[192:195], v[172:175], v[120:127], v197\nv_smfmac_f32_16x16x64_bf16 v[192:195], v[176:179], v[152:159], v197\ns_or_b64 vcc, vcc, s[64:65]\ns_cbranch_scc1 .Lrd_rb%=\n" \
"s_add_u32 s60, s58, 16384\nv_add_f32 v180, v180, v182\nv_add_f32 v181, v181, v183\nds_write2_b32 v198, v180, v181 offset0:0 offset1:8\nbuffer_load_dwordx4 v[212:215], v196, %[rs], s60 offen offset:0 sc1\nv_add_f32 v184, v184, v186\nv_add_f32 v185, v185, v187\nds_write2_b32 v198, v184, v185 offset0:32 offset1:40\n" \
"s_add_u32 s60, s58, 0\nbuffer_load_dwordx4 v[216:219], v196, %[rs], s60 offen offset:0 sc1\nv_add_f32 v188, v188, v190\nv_add_f32 v189, v189, v191\nds_write2_b32 v198, v188, v189 offset0:64 offset1:72\ns_add_u32 s60, s58, 0\nbuffer_load_dwordx4 v[220:223], v196, %[rs], s60 offen offset:1024 sc1\nv_add_f32 v192, v192, v194\n" \
"v_add_f32 v193, v193, v195\nds_write2_b32 v198, v192, v193 offset0:96 offset1:104\ns_add_u32 s60, s58, 16384\nbuffer_load_dwordx4 v[224:227], v196, %[rs], s60 offen offset:1024 sc1\ns_waitcnt lgkmcnt(0)\ns_barrier\nds_read_b128 v[180:183], v199 offset:0\nds_read_b128 v[184:187], v199 offset:4096\n" \
"ds_read_b128 v[188:191], v199 offset:8192\nds_read_b128 v[192:195], v199 offset:12288\ns_and_b32 s44, s54, 0x80008000\ns_add_u32 s60, s58, 0\nbuffer_load_dwordx4 v[228:231], v196, %[rs], s60 offen offset:2048 sc1\ns_waitcnt lgkmcnt(2)\nv_pk_add_f32 v[182:183], v[182:183], v[186:187]\nv_pk_add_f32 v[180:181], v[180:181], v[184:185]\n" \
"s_waitcnt lgkmcnt(0)\nv_pk_add_f32 v[186:187], v[188:189], v[192:193]\nv_pk_add_f32 v[184:185], v[190:191], v[194:195]\nv_pk_add_f32 v[180:181], v[180:181], v[186:187]\nv_pk_add_f32 v[182:183], v[182:183], v[184:185]\nv_cvt_pk_bf16_f32 v180, v180, v181\nv_xor_b32 v180, s44, v180\nv_cvt_pk_bf16_f32 v181, v182, v183\n" \
"v_min_u32 v180, -2, v180\nv_xor_b32 v209, s44, v181\nv_or_b32 v208, v180, v201\nbuffer_store_dwordx2 v[208:209], v206, %[rs], 0 offen sc1\ns_add_u32 s60, s58, 0\nbuffer_load_dwordx4 v[232:235], v196, %[rs], s60 offen offset:3072 sc1\ns_add_u32 s60, s58, 16384\nbuffer_load_dwordx4 v[236:239], v196, %[rs], s60 offen offset:2048 sc1\n" \
"s_add_u32 s60, s58, 4096\nbuffer_load_dwordx4 v[240:243], v196, %[rs], s60 offen offset:0 sc1\ns_add_u32 s60, s58, 4096\nbuffer_load_dwordx4 v[244:247], v196, %[rs], s60 offen offset:1024 sc1\ns_add_u32 s60, s58, 16384\nbuffer_load_dwordx4 v[248:251], v196, %[rs], s60 offen offset:3072 sc1\ns_add_u32 s60, s58, 20480\nbuffer_load_dwordx4 v[252:255], v196, %[rs], s60 offen offset:0 sc1\n" \
"s_add_u32 s60, s58, 20480\nbuffer_load_dwordx4 v[160:163], v196, %[rs], s60 offen offset:2048 sc1\ns_xor_b32 s41, s41, 4194304\nv_mov_b32 v207, s40\ns_waitcnt vmcnt(12)\nv_bitop3_b32 v209, v212, v214, s61 bitop3:0x7e\nv_cmp_gt_i32 vcc, 0, v209\ns_cbranch_vccnz .Lrd_tre0%=\n" \
".Lrd_tl0%=:\nv_mfma_f32_16x16x16_bf16 a[0:3], v[0:1], v[212:213], 0\ns_add_u32 s60, s58, 4096\nbuffer_load_dwordx4 v[164:167], v196, %[rs], s60 offen offset:2048 sc1\nv_mfma_f32_16x16x16_bf16 a[8:11], v[32:33], v[212:213], 0\nv_mfma_f32_16x16x16_bf16 a[16:19], v[64:65], v[212:213], 0\nv_mfma_f32_16x16x16_bf16 a[24:27], v[96:97], v[212:213], 0\ns_waitcnt vmcnt(12)\n" \
"v_mfma_f32_16x16x16_bf16 a[0:3], v[4:5], v[214:215], a[0:3]\ns_add_u32 s60, s58, 4096\nbuffer_load_dwordx4 v[168:171], v196, %[rs], s60 offen offset:3072 sc1\nv_bitop3_b32 v209, v216, v218, s61 bitop3:0x7e\nv_mfma_f32_16x16x16_bf16 a[8:11], v[36:37], v[214:215], a[8:11]\ns_lshl_b32 s42, s40, 17\ns_add_u32 s42, s42, %[at]\nv_mfma_f32_16x16x16_bf16 a[16:19], v[68:69], v[214:215], a[16:19]\n" \
"v_cmp_gt_i32 vcc, 0, v209\nv_mfma_f32_16x16x16_bf16 a[24:27], v[100:101], v[214:215], a[24:27]\ns_cbranch_vccnz .Lrd_tre1%=\n.Lrd_tl1%=:\ns_waitcnt vmcnt(12)\nv_mfma_f32_16x16x32_bf16 a[4:7], v[0:3], v[216:219], 0\ns_add_u32 s60, s58, 20480\nbuffer_load_dwordx4 v[172:175], v196, %[rs], s60 offen offset:1024 sc1\n" \
"v_bitop3_b32 v209, v220, v222, s61 bitop3:0x7e\nv_mfma_f32_16x16x32_bf16 a[12:15], v[32:35], v[216:219], 0\ns_lshr_b32 s45, s42, 8\ns_and_b32 s45, s45, 0x2000\nv_mfma_f32_16x16x32_bf16 a[20:23], v[64:67], v[216:219], 0\nv_cmp_gt_i32 vcc, 0, v209\nv_mfma_f32_16x16x32_bf16 a[28:31], v[96:99], v[216:219], 0\ns_cbranch_vccnz .Lrd_tre2%=\n" \
".Lrd_tl2%=:\ns_waitcnt vmcnt(12)\nv_mfma_f32_16x16x32_bf16 a[4:7], v[4:7], v[220:223], a[4:7]\ns_add_u32 s60, s58, 20480\nbuffer_load_dwordx4 v[176:179], v196, %[rs], s60 offen offset:3072 sc1\nv_bitop3_b32 v209, v224, v226, s61 bitop3:0x7e\nv_mfma_f32_16x16x32_bf16 a[12:15], v[36:39], v[220:223], a[12:15]\ns_xor_b32 s42, s42, s45\n" \
"s_add_u32 s40, s40, 1\nv_mfma_f32_16x16x32_bf16 a[20:23], v[68:71], v[220:223], a[20:23]\nv_cmp_gt_i32 vcc, 0, v209\nv_mfma_f32_16x16x32_bf16 a[28:31], v[100:103], v[220:223], a[28:31]\ns_cbranch_vccnz .Lrd_tre3%=\n.Lrd_tl3%=:\nv_mfma_f32_16x16x16_bf16 a[0:3], v[8:9], v[224:225], a[0:3]\ns_cmp_eq_u32 s40, 1\n" \
"s_cbranch_scc1 .Lrd_y1%=\n.Lrd_y1r%=:\ns_add_u32 s60, s41, 0\nbuffer_load_dwordx4 v[0:3], v196, %[rs], s60 offen offset:0 sc1\nv_mfma_f32_16x16x16_bf16 a[8:11], v[40:41], v[224:225], a[8:11]\nv_mfma_f32_16x16x16_bf16 a[16:19], v[72:73], v[224:225], a[16:19]\nbuffer_load_dwordx4 v[212:215], v204, %[rs], s42 offen sc1\nv_mfma_f32_16x16x16_bf16 a[24:27], v[104:105], v[224:225], a[24:27]\n" \
"s_waitcnt vmcnt(14)\nv_mfma_f32_16x16x16_bf16 a[0:3], v[12:13], v[226:227], a[0:3]\ns_add_u32 s60, s41, 0\nbuffer_load_dwordx4 v[32:35], v196, %[rs], s60 offen offset:1024 sc1\nv_bitop3_b32 v209, v228, v230, s61 bitop3:0x7e\nv_mfma_f32_16x16x16_bf16 a[8:11], v[44:45], v[226:227], a[8:11]\ns_lshl_b32 s62, s55, 31\ns_or_b32 s62, s62, 1.0\n" \
"s_mov_b32 s63, s62\nv_mfma_f32_16x16x16_bf16 a[16:19], v[76:77], v[226:227], a[16:19]\nv_cmp_gt_i32 vcc, 0, v209\nv_mfma_f32_16x16x16_bf16 a[24:27], v[108:109], v[226:227], a[24:27]\ns_cbranch_vccnz .Lrd_tre4%=\n.Lrd_tl4%=:\ns_waitcnt vmcnt(13)\nv_mfma_f32_16x16x32_bf16 a[4:7], v[8:11], v[228:231], a[4:7]\n" \
"s_add_u32 s60, s41, 0\nbuffer_load_dwordx4 v[64:67], v196, %[rs], s60 offen offset:2048 sc1\nv_bitop3_b32 v209, v232, v234, s61 bitop3:0x7e\nv_mfma_f32_16x16x32_bf16 a[12:15], v[40:43], v[228:231], a[12:15]\nv_mfma_f32_16x16x32_bf16 a[20:23], v[72:75], v[228:231], a[20:23]\nv_cmp_gt_i32 vcc, 0, v209\nv_mfma_f32_16x16x32_bf16 a[28:31], v[104:107], v[228:231], a[28:31]\ns_cbranch_vccnz .Lrd_tre5%=\n" \
".Lrd_tl5%=:\ns_waitcnt vmcnt(13)\nv_mfma_f32_16x16x32_bf16 a[4:7], v[12:15], v[232:235], a[4:7]\ns_add_u32 s60, s41, 131072\nbuffer_load_dwordx4 v[4:7], v196, %[rs], s60 offen offset:0 sc1\nv_bitop3_b32 v209, v236, v238, s61 bitop3:0x7e\nv_mfma_f32_16x16x32_bf16 a[12:15], v[44:47], v[232:235], a[12:15]\nv_mfma_f32_16x16x32_bf16 a[20:23], v[76:79], v[232:235], a[20:23]\n" \
"v_cmp_gt_i32 vcc, 0, v209\nv_mfma_f32_16x16x32_bf16 a[28:31], v[108:111], v[232:235], a[28:31]\ns_cbranch_vccnz .Lrd_tre6%=\n.Lrd_tl6%=:\nv_mfma_f32_16x16x16_bf16 a[0:3], v[16:17], v[236:237], a[0:3]\ns_add_u32 s60, s41, 131072\nbuffer_load_dwordx4 v[36:39], v196, %[rs], s60 offen offset:1024 sc1\nv_mfma_f32_16x16x16_bf16 a[8:11], v[48:49], v[236:237], a[8:11]\n" \
"v_mfma_f32_16x16x16_bf16 a[16:19], v[80:81], v[236:237], a[16:19]\nv_mfma_f32_16x16x16_bf16 a[24:27], v[112:113], v[236:237], a[24:27]\ns_waitcnt vmcnt(14)\nv_mfma_f32_16x16x16_bf16 a[0:3], v[20:21], v[238:239], a[0:3]\ns_add_u32 s60, s41, 131072\nbuffer_load_dwordx4 v[68:71], v196, %[rs], s60 offen offset:2048 sc1\nv_bitop3_b32 v209, v240, v242, s61 bitop3:0x7e\nv_mfma_f32_16x16x16_bf16 a[8:11], v[52:53], v[238:239], a[8:11]\n" \
"v_mfma_f32_16x16x16_bf16 a[16:19], v[84:85], v[238:239], a[16:19]\nv_cmp_gt_i32 vcc, 0, v209\nv_mfma_f32_16x16x16_bf16 a[24:27], v[116:117], v[238:239], a[24:27]\ns_cbranch_vccnz .Lrd_tre7%=\n.Lrd_tl7%=:\ns_waitcnt vmcnt(14)\nv_mfma_f32_16x16x32_bf16 a[4:7], v[16:19], v[240:243], a[4:7]\ns_add_u32 s60, s41, 262144\n" \
"buffer_load_dwordx4 v[8:11], v196, %[rs], s60 offen offset:0 sc1\nv_bitop3_b32 v209, v244, v246, s61 bitop3:0x7e\nv_mfma_f32_16x16x32_bf16 a[12:15], v[48:51], v[240:243], a[12:15]\nv_mfma_f32_16x16x32_bf16 a[20:23], v[80:83], v[240:243], a[20:23]\nv_cmp_gt_i32 vcc, 0, v209\nv_mfma_f32_16x16x32_bf16 a[28:31], v[112:115], v[240:243], a[28:31]\ns_cbranch_vccnz .Lrd_tre8%=\n.Lrd_tl8%=:\n" \
"s_waitcnt vmcnt(14)\nv_mfma_f32_16x16x32_bf16 a[4:7], v[20:23], v[244:247], a[4:7]\ns_add_u32 s60, s41, 262144\nbuffer_load_dwordx4 v[40:43], v196, %[rs], s60 offen offset:1024 sc1\nv_bitop3_b32 v209, v248, v250, s61 bitop3:0x7e\nv_mfma_f32_16x16x32_bf16 a[12:15], v[52:55], v[244:247], a[12:15]\nv_mfma_f32_16x16x32_bf16 a[20:23], v[84:87], v[244:247], a[20:23]\nv_cmp_gt_i32 vcc, 0, v209\n" \
"v_mfma_f32_16x16x32_bf16 a[28:31], v[116:119], v[244:247], a[28:31]\ns_cbranch_vccnz .Lrd_tre9%=\n.Lrd_tl9%=:\nv_mfma_f32_16x16x16_bf16 a[0:3], v[24:25], v[248:249], a[0:3]\ns_add_u32 s60, s41, 262144\nbuffer_load_dwordx4 v[72:75], v196, %[rs], s60 offen offset:2048 sc1\nv_mfma_f32_16x16x16_bf16 a[8:11], v[56:57], v[248:249], a[8:11]\nv_mfma_f32_16x16x16_bf16 a[16:19], v[88:89], v[248:249], a[16:19]\n" \
"v_mfma_f32_16x16x16_bf16 a[24:27], v[120:121], v[248:249], a[24:27]\ns_waitcnt vmcnt(15)\nv_mfma_f32_16x16x16_bf16 a[0:3], v[28:29], v[250:251], a[0:3]\ns_add_u32 s60, s41, 393216\nbuffer_load_dwordx4 v[12:15], v196, %[rs], s60 offen offset:0 sc1\nv_bitop3_b32 v209, v252, v254, s61 bitop3:0x7e\nv_mfma_f32_16x16x16_bf16 a[8:11], v[60:61], v[250:251], a[8:11]\nv_bitop3_b32 v210, v253, v255, s61 bitop3:0x7e\n" \
"v_or_b32 v209, v209, v210\nv_mfma_f32_16x16x16_bf16 a[16:19], v[92:93], v[250:251], a[16:19]\nv_cmp_gt_i32 vcc, 0, v209\nv_mfma_f32_16x16x16_bf16 a[24:27], v[124:125], v[250:251], a[24:27]\ns_cbranch_vccnz .Lrd_tre10%=\n.Lrd_tl10%=:\ns_waitcnt vmcnt(15)\nv_mfma_f32_16x16x32_bf16 a[0:3], v[128:131], v[252:255], a[0:3]\n" \
"s_add_u32 s60, s41, 393216\nbuffer_load_dwordx4 v[44:47], v196, %[rs], s60 offen offset:1024 sc1\nv_bitop3_b32 v209, v160, v162, s61 bitop3:0x7e\nv_mfma_f32_16x16x32_bf16 a[8:11], v[136:139], v[252:255], a[8:11]\nv_bitop3_b32 v210, v161, v163, s61 bitop3:0x7e\nv_or_b32 v209, v209, v210\nv_mfma_f32_16x16x32_bf16 a[16:19], v[144:147], v[252:255], a[16:19]\nv_cmp_gt_i32 vcc, 0, v209\n" \
"v_mfma_f32_16x16x32_bf16 a[24:27], v[152:155], v[252:255], a[24:27]\ns_cbranch_vccnz .Lrd_tre11%=\n.Lrd_tl11%=:\ns_waitcnt vmcnt(15)\nv_mfma_f32_16x16x32_bf16 a[0:3], v[132:135], v[160:163], a[0:3]\ns_add_u32 s60, s41, 393216\nbuffer_load_dwordx4 v[76:79], v196, %[rs], s60 offen offset:2048 sc1\nv_bitop3_b32 v209, v164, v166, s61 bitop3:0x7e\n" \
"v_mfma_f32_16x16x32_bf16 a[8:11], v[140:143], v[160:163], a[8:11]\nv_mfma_f32_16x16x32_bf16 a[16:19], v[148:151], v[160:163], a[16:19]\nv_cmp_gt_i32 vcc, 0, v209\nv_mfma_f32_16x16x32_bf16 a[24:27], v[156:159], v[160:163], a[24:27]\ns_cbranch_vccnz .Lrd_tre12%=\n.Lrd_tl12%=:\ns_waitcnt vmcnt(15)\nv_mfma_f32_16x16x32_bf16 a[4:7], v[24:27], v[164:167], a[4:7]\n" \
"s_add_u32 s60, s41, 524288\nbuffer_load_dwordx4 v[16:19], v196, %[rs], s60 offen offset:0 sc1\nv_bitop3_b32 v209, v168, v170, s61 bitop3:0x7e\nv_mfma_f32_16x16x32_bf16 a[12:15], v[56:59], v[164:167], a[12:15]\nv_mfma_f32_16x16x32_bf16 a[20:23], v[88:91], v[164:167], a[20:23]\nds_write_b128 v202, a[0:3] offset:0\nv_cmp_gt_i32 vcc, 0, v209\nv_mfma_f32_16x16x32_bf16 a[28:31], v[120:123], v[164:167], a[28:31]\n" \
"s_cbranch_vccnz .Lrd_tre13%=\n.Lrd_tl13%=:\ns_waitcnt vmcnt(15)\nv_mfma_f32_16x16x32_bf16 a[4:7], v[28:31], v[168:171], a[4:7]\ns_add_u32 s60, s41, 524288\nbuffer_load_dwordx4 v[48:51], v196, %[rs], s60 offen offset:1024 sc1\nv_bitop3_b32 v209, v172, v174, s61 bitop3:0x7e\nv_mfma_f32_16x16x32_bf16 a[12:15], v[60:63], v[168:171], a[12:15]\n" \
"v_bitop3_b32 v210, v173, v175, s61 bitop3:0x7e\nv_or_b32 v209, v209, v210\nv_mfma_f32_16x16x32_bf16 a[20:23], v[92:95], v[168:171], a[20:23]\nds_write_b128 v202, a[8:11] offset:2048\nv_cmp_gt_i32 vcc, 0, v209\nv_mfma_f32_16x16x32_bf16 a[28:31], v[124:127], v[168:171], a[28:31]\ns_cbranch_vccnz .Lrd_tre14%=\n.Lrd_tl14%=:\n" \
"s_waitcnt vmcnt(15)\nv_mfma_f32_16x16x32_bf16 a[4:7], v[128:131], v[172:175], a[4:7]\ns_add_u32 s60, s41, 524288\nbuffer_load_dwordx4 v[80:83], v196, %[rs], s60 offen offset:2048 sc1\nv_bitop3_b32 v209, v176, v178, s61 bitop3:0x7e\nv_mfma_f32_16x16x32_bf16 a[12:15], v[136:139], v[172:175], a[12:15]\nv_bitop3_b32 v210, v177, v179, s61 bitop3:0x7e\nv_or_b32 v209, v209, v210\n" \
"v_mfma_f32_16x16x32_bf16 a[20:23], v[144:147], v[172:175], a[20:23]\nds_write_b128 v202, a[16:19] offset:4096\nv_cmp_gt_i32 vcc, 0, v209\nv_mfma_f32_16x16x32_bf16 a[28:31], v[152:155], v[172:175], a[28:31]\ns_cbranch_vccnz .Lrd_tre15%=\n.Lrd_tl15%=:\nv_mfma_f32_16x16x32_bf16 a[4:7], v[132:135], v[176:179], a[4:7]\ns_add_u32 s60, s41, 655360\n" \
"s_waitcnt vmcnt(14)\nbuffer_load_dwordx4 v[20:23], v196, %[rs], s60 offen offset:0 sc1\nv_lshlrev_b32 v216, 16, v212\nv_and_b32 v217, 0xffff0000, v212\nv_mfma_f32_16x16x32_bf16 a[12:15], v[140:143], v[176:179], a[12:15]\nv_lshlrev_b32 v218, 16, v213\nv_and_b32 v219, 0xffff0000, v213\nv_mfma_f32_16x16x32_bf16 a[20:23], v[148:151], v[176:179], a[20:23]\n" \
"ds_write_b128 v202, a[24:27] offset:6144\nv_lshlrev_b32 v220, 16, v214\nv_and_b32 v221, 0xffff0000, v214\nv_mfma_f32_16x16x32_bf16 a[28:31], v[156:159], v[176:179], a[28:31]\ns_add_u32 s60, s41, 655360\nbuffer_load_dwordx4 v[52:55], v196, %[rs], s60 offen offset:1024 sc1\ns_add_u32 s60, s41, 655360\nds_write_b128 v202, a[4:7] offset:1024\n" \
"buffer_load_dwordx4 v[84:87], v196, %[rs], s60 offen offset:2048 sc1\nds_write_b128 v202, a[12:15] offset:3072\nv_lshlrev_b32 v222, 16, v215\nv_and_b32 v223, 0xffff0000, v215\ns_add_u32 s60, s41, 786432\nbuffer_load_dwordx4 v[24:27], v196, %[rs], s60 offen offset:0 sc1\nds_write_b128 v202, a[20:23] offset:5120\ns_add_u32 s60, s41, 786432\n" \
"buffer_load_dwordx4 v[56:59], v196, %[rs], s60 offen offset:1024 sc1\nds_write_b128 v202, a[28:31] offset:7168\ns_waitcnt lgkmcnt(0)\ns_barrier\nds_read_b128 v[224:227], v203 offset:0\nds_read_b128 v[228:231], v203 offset:256\nds_read_b128 v[232:235], v203 offset:8192\nds_read_b128 v[236:239], v203 offset:8448\n" \
"ds_read_b128 v[240:243], v203 offset:16384\nds_read_b128 v[244:247], v203 offset:16640\nds_read_b128 v[248:251], v203 offset:24576\nds_read_b128 v[252:255], v203 offset:24832\ns_add_u32 s60, s41, 786432\nbuffer_load_dwordx4 v[88:91], v196, %[rs], s60 offen offset:2048 sc1\ns_waitcnt lgkmcnt(4)\nv_pk_add_f32 v[224:225], v[224:225], v[232:233]\n" \
"v_pk_add_f32 v[226:227], v[226:227], v[234:235]\nv_pk_add_f32 v[228:229], v[228:229], v[236:237]\nv_pk_add_f32 v[230:231], v[230:231], v[238:239]\ns_add_u32 s60, s41, 917504\nbuffer_load_dwordx4 v[28:31], v196, %[rs], s60 offen offset:0 sc1\ns_waitcnt lgkmcnt(2)\nv_pk_add_f32 v[224:225], v[224:225], v[240:241]\nv_pk_add_f32 v[226:227], v[226:227], v[242:243]\n" \
"v_pk_add_f32 v[228:229], v[228:229], v[244:245]\nv_pk_add_f32 v[230:231], v[230:231], v[246:247]\ns_add_u32 s60, s41, 917504\nbuffer_load_dwordx4 v[60:63], v196, %[rs], s60 offen offset:1024 sc1\ns_waitcnt lgkmcnt(0)\nv_pk_add_f32 v[224:225], v[224:225], v[248:249]\nv_pk_add_f32 v[226:227], v[226:227], v[250:251]\nv_pk_add_f32 v[228:229], v[228:229], v[252:253]\n" \
"v_pk_add_f32 v[230:231], v[230:231], v[254:255]\nv_pk_fma_f32 v[224:225], v[224:225], s[62:63], v[216:217]\nv_pk_fma_f32 v[226:227], v[226:227], s[62:63], v[218:219]\nv_pk_fma_f32 v[228:229], v[228:229], s[62:63], v[220:221]\nv_pk_fma_f32 v[230:231], v[230:231], s[62:63], v[222:223]\nv_cvt_pk_bf16_f32 v208, v224, v225\nv_cvt_pk_bf16_f32 v209, v226, v227\nv_cvt_pk_bf16_f32 v210, v228, v229\n" \
"v_cvt_pk_bf16_f32 v211, v230, v231\nbuffer_store_dwordx4 v[208:211], v204, %[rs], s42 offen sc1\nv_pk_add_f32 v[232:233], v[224:225], v[226:227]\nv_pk_add_f32 v[234:235], v[228:229], v[230:231]\nv_pk_add_f32 v[232:233], v[232:233], v[234:235]\ns_add_u32 s60, s41, 917504\nbuffer_load_dwordx4 v[92:95], v196, %[rs], s60 offen offset:2048 sc1\ns_add_u32 s60, s41, 0\n" \
"buffer_load_dwordx4 v[96:99], v196, %[rs], s60 offen offset:3072 sc1\nv_add_f32 v232, v232, v233\ns_nop 1\nv_add_f32_dpp v233, v232, v232 row_ror:8 row_mask:0xf bank_mask:0xf\nv_add_f32_dpp v234, v232, v232 row_ror:8 row_mask:0xf bank_mask:0xf\ns_nop 1\nv_permlane16_swap_b32 v233, v234\nv_add_f32 v232, v233, v234\n" \
"v_add_f32 v233, v233, v234\ns_nop 1\nv_permlane32_swap_b32 v232, v233\nv_add_f32 v232, v232, v233\nv_mul_f32 v232, 0x3c800000, v232\nv_pk_add_f32 v[224:225], v[224:225], v[232:233] op_sel_hi:[1,0] neg_lo:[0,1] neg_hi:[0,1]\nv_pk_add_f32 v[226:227], v[226:227], v[232:233] op_sel_hi:[1,0] neg_lo:[0,1] neg_hi:[0,1]\nv_pk_add_f32 v[228:229], v[228:229], v[232:233] op_sel_hi:[1,0] neg_lo:[0,1] neg_hi:[0,1]\n" \
"v_pk_add_f32 v[230:231], v[230:231], v[232:233] op_sel_hi:[1,0] neg_lo:[0,1] neg_hi:[0,1]\ns_add_u32 s60, s41, 131072\nbuffer_load_dwordx4 v[100:103], v196, %[rs], s60 offen offset:3072 sc1\ns_add_u32 s60, s41, 262144\nbuffer_load_dwordx4 v[104:107], v196, %[rs], s60 offen offset:3072 sc1\nv_pk_mul_f32 v[236:237], v[224:225], v[224:225]\nv_pk_mul_f32 v[238:239], v[226:227], v[226:227]\nv_pk_fma_f32 v[236:237], v[228:229], v[228:229], v[236:237]\n" \
"v_pk_fma_f32 v[238:239], v[230:231], v[230:231], v[238:239]\nv_pk_add_f32 v[236:237], v[236:237], v[238:239]\nv_add_f32 v236, v236, v237\ns_nop 1\nv_add_f32_dpp v237, v236, v236 row_ror:8 row_mask:0xf bank_mask:0xf\nv_add_f32_dpp v238, v236, v236 row_ror:8 row_mask:0xf bank_mask:0xf\ns_nop 1\nv_permlane16_swap_b32 v237, v238\n" \
"v_add_f32 v236, v237, v238\nv_add_f32 v237, v237, v238\ns_nop 1\nv_permlane32_swap_b32 v236, v237\nv_add_f32 v236, v236, v237\nv_mov_b32 v233, v236\nbuffer_store_dwordx2 v[232:233], v205, %[rs], s42 offen sc1\ns_add_u32 s60, s41, 393216\n" \
"buffer_load_dwordx4 v[108:111], v196, %[rs], s60 offen offset:3072 sc1\nv_accvgpr_read_b32 v212, a33\nbuffer_load_dwordx2 v[214:215], v212, %[rs], 0 offen sc1\ns_add_u32 s60, s41, 524288\nbuffer_load_dwordx4 v[112:115], v196, %[rs], s60 offen offset:3072 sc1\ns_add_u32 s60, s41, 655360\nbuffer_load_dwordx4 v[116:119], v196, %[rs], s60 offen offset:3072 sc1\ns_add_u32 s60, s41, 786432\n" \
"buffer_load_dwordx4 v[120:123], v196, %[rs], s60 offen offset:3072 sc1\ns_add_u32 s60, s41, 917504\nbuffer_load_dwordx4 v[124:127], v196, %[rs], s60 offen offset:3072 sc1\ns_waitcnt vmcnt(6)\nbuffer_store_dword v207, off, %[rs], %[fa] sc1\ns_cmp_ge_u32 s40, 64\ns_cbranch_scc1 .Lrd_end%=\n.Lrd_ychk%=:\n" \
"s_and_b32 s44, s40, 1\ns_bfe_u32 s55, s40, 0x10001\ns_mul_i32 s54, s44, 0x80008001\ns_mul_i32 s61, s55, 0x80008000\ns_lshl_b32 s58, s44, 17\ns_add_u32 s58, s58, %[st]\ns_add_u32 s44, s40, 1\ns_and_b32 s44, s44, 3\n" \
"s_lshl_b32 s44, s44, 15\nv_xor_b32 v206, s44, v200\nv_mov_b64 v[180:181], 0\nv_mov_b64 v[182:183], 0\nv_mov_b64 v[184:185], 0\nv_mov_b64 v[186:187], 0\nv_mov_b64 v[188:189], 0\nv_mov_b64 v[190:191], 0\n" \
"v_mov_b64 v[192:193], 0\nv_mov_b64 v[194:195], 0\ns_waitcnt vmcnt(5)\n.Lrd_sg0%=:\nv_cmp_ne_u32 vcc, s40, v214\ns_cbranch_vccnz .Lrd_sg1%=\nv_max_u32_dpp v215, v215, v215 quad_perm:[1,0,3,2] row_mask:0xf bank_mask:0xf\ns_nop 1\n" \
"v_max_u32_dpp v215, v215, v215 quad_perm:[2,3,0,1] row_mask:0xf bank_mask:0xf\ns_nop 1\nv_max_u32_dpp v215, v215, v215 row_half_mirror row_mask:0xf bank_mask:0xf\ns_nop 0\nv_readlane_b32 s86, v215, 0\nv_readlane_b32 s87, v215, 8\ns_add_u32 s86, s86, %[du]\n.Lrd_tw622%=:\n" \
"s_memrealtime s[68:69]\ns_waitcnt lgkmcnt(0)\ns_sub_u32 s44, s86, s68\ns_add_u32 s44, s44, 11\ns_cmp_lt_u32 s44, 250\ns_cbranch_scc1 .Lrd_tw622%=\nbuffer_load_dwordx4 v[128:131], v196, %[rs], %[su] offen offset:0 sc1\nbuffer_load_dwordx4 v[132:135], v196, %[rs], %[su] offen offset:1024 sc1\n" \
"buffer_load_dwordx4 v[136:139], v196, %[rs], %[su] offen offset:2048 sc1\nbuffer_load_dwordx4 v[140:143], v196, %[rs], %[su] offen offset:3072 sc1\n.Lrd_tw633%=:\ns_memrealtime s[68:69]\ns_waitcnt lgkmcnt(0)\ns_sub_u32 s44, s86, s68\ns_add_u32 s44, s44, 21\ns_cmp_lt_u32 s44, 250\n" \
"s_cbranch_scc1 .Lrd_tw633%=\nbuffer_load_dwordx4 v[144:147], v196, %[rs], s57 offen offset:0 sc1\nbuffer_load_dwordx4 v[148:151], v196, %[rs], s57 offen offset:1024 sc1\nbuffer_load_dwordx4 v[152:155], v196, %[rs], s57 offen offset:2048 sc1\nbuffer_load_dwordx4 v[156:159], v196, %[rs], s57 offen offset:3072 sc1\n.Lrd_tw644%=:\ns_memrealtime s[68:69]\ns_waitcnt lgkmcnt(0)\n" \
"s_sub_u32 s44, s87, s68\ns_add_u32 s44, s44, 24\ns_cmp_lt_u32 s44, 250\ns_cbranch_scc1 .Lrd_tw644%=\nbuffer_load_dwordx4 v[160:163], v196, %[rs], %[sp] offen offset:0 sc1\nbuffer_load_dwordx4 v[164:167], v196, %[rs], %[sp] offen offset:1024 sc1\nbuffer_load_dwordx4 v[168:171], v196, %[rs], %[sp] offen offset:2048 sc1\nbuffer_load_dwordx4 v[172:175], v196, %[rs], %[sp] offen offset:3072 sc1\n" \
"buffer_load_dwordx4 v[176:179], v196, %[rs], s56 offen offset:0 sc1\nv_bitop3_b32 v209, v0, v2, s55 bitop3:0x7e\nv_bitop3_b32 v210, v32, v34, s55 bitop3:0x7e\nv_or3_b32 v208, v209, v210, 0\nv_bitop3_b32 v209, v64, v66, s55 bitop3:0x7e\nv_bitop3_b32 v210, v4, v6, s55 bitop3:0x7e\nv_or3_b32 v208, v209, v210, v208\nv_bitop3_b32 v209, v36, v38, s55 bitop3:0x7e\n" \
"v_bitop3_b32 v210, v68, v70, s55 bitop3:0x7e\nv_or3_b32 v208, v209, v210, v208\nv_bitop3_b32 v209, v8, v10, s55 bitop3:0x7e\nv_bitop3_b32 v210, v40, v42, s55 bitop3:0x7e\nv_or3_b32 v208, v209, v210, v208\nv_bitop3_b32 v209, v72, v74, s55 bitop3:0x7e\nv_bitop3_b32 v210, v12, v14, s55 bitop3:0x7e\nv_or3_b32 v208, v209, v210, v208\n" \
"v_bitop3_b32 v209, v44, v46, s55 bitop3:0x7e\nv_bitop3_b32 v210, v76, v78, s55 bitop3:0x7e\nv_or3_b32 v208, v209, v210, v208\nv_bitop3_b32 v209, v16, v18, s55 bitop3:0x7e\nv_bitop3_b32 v210, v48, v50, s55 bitop3:0x7e\nv_or3_b32 v208, v209, v210, v208\nv_bitop3_b32 v209, v80, v82, s55 bitop3:0x7e\nv_bitop3_b32 v210, v20, v22, s55 bitop3:0x7e\n" \
"v_or3_b32 v208, v209, v210, v208\nv_bitop3_b32 v209, v52, v54, s55 bitop3:0x7e\nv_bitop3_b32 v210, v84, v86, s55 bitop3:0x7e\nv_or3_b32 v208, v209, v210, v208\nv_bitop3_b32 v209, v24, v26, s55 bitop3:0x7e\nv_bitop3_b32 v210, v56, v58, s55 bitop3:0x7e\nv_or3_b32 v208, v209, v210, v208\nv_bitop3_b32 v209, v88, v90, s55 bitop3:0x7e\n" \
"v_bitop3_b32 v210, v28, v30, s55 bitop3:0x7e\nv_or3_b32 v208, v209, v210, v208\nv_bitop3_b32 v209, v60, v62, s55 bitop3:0x7e\nv_bitop3_b32 v210, v92, v94, s55 bitop3:0x7e\nv_or3_b32 v208, v209, v210, v208\nv_bitop3_b32 v209, v96, v98, s55 bitop3:0x7e\nv_bitop3_b32 v210, v100, v102, s55 bitop3:0x7e\nv_or3_b32 v208, v209, v210, v208\n" \
"v_bitop3_b32 v209, v104, v106, s55 bitop3:0x7e\nv_bitop3_b32 v210, v108, v110, s55 bitop3:0x7e\nv_or3_b32 v208, v209, v210, v208\ns_waitcnt vmcnt(14)\nv_bitop3_b32 v209, v112, v114, s55 bitop3:0x7e\nv_bitop3_b32 v210, v116, v118, s55 bitop3:0x7e\nv_or3_b32 v208, v209, v210, v208\nv_bitop3_b32 v209, v120, v122, s55 bitop3:0x7e\n" \
"v_bitop3_b32 v210, v124, v126, s55 bitop3:0x7e\nv_or3_b32 v208, v209, v210, v208\nv_and_b32 v208, 1, v208\nv_cmp_ne_u32 vcc, 0, v208\ns_cbranch_vccnz .Lrd_yre%=\ns_branch .Lrd_ra%=\n.Lrd_sg1%=:\nbuffer_load_dwordx2 v[214:215], v212, %[rs], 0 offen sc1\n" \
"s_waitcnt vmcnt(0)\ns_add_u32 s43, s43, 1\ns_cmp_lt_u32 s43, 262144\ns_cbranch_scc1 .Lrd_sg0%=\ns_branch .Lrd_abort%=\n.Lrd_yre%=:\ns_mov_b64 exec, vcc\ns_waitcnt vmcnt(0)\n" \
"s_add_u32 s43, s43, 1\ns_cmp_lt_u32 s43, 262144\ns_cbranch_scc1 .Lrd_yld%=\ns_branch .Lrd_abort%=\n.Lrd_rb%=:\ns_mov_b64 exec, s[46:47]\ns_cbranch_execz .Lrd_rs0%=\nbuffer_load_dwordx4 v[160:163], v196, %[rs], %[sp] offen offset:0 sc1\n" \
".Lrd_rs0%=:\ns_mov_b64 exec, s[48:49]\ns_cbranch_execz .Lrd_rs1%=\nbuffer_load_dwordx4 v[164:167], v196, %[rs], %[sp] offen offset:1024 sc1\n.Lrd_rs1%=:\ns_mov_b64 exec, s[50:51]\ns_cbranch_execz .Lrd_rs2%=\nbuffer_load_dwordx4 v[168:171], v196, %[rs], %[sp] offen offset:2048 sc1\n" \
".Lrd_rs2%=:\ns_mov_b64 exec, s[52:53]\ns_cbranch_execz .Lrd_rs3%=\nbuffer_load_dwordx4 v[172:175], v196, %[rs], %[sp] offen offset:3072 sc1\n.Lrd_rs3%=:\ns_mov_b64 exec, s[64:65]\ns_cbranch_execz .Lrd_rs4%=\nbuffer_load_dwordx4 v[176:179], v196, %[rs], s56 offen offset:0 sc1\n" \
".Lrd_rs4%=:\ns_mov_b64 exec, s[86:87]\ns_cbranch_execz .Lrd_rs5%=\nbuffer_load_dwordx4 v[128:131], v196, %[rs], %[su] offen offset:0 sc1\nbuffer_load_dwordx4 v[136:139], v196, %[rs], %[su] offen offset:2048 sc1\n.Lrd_rs5%=:\ns_mov_b64 exec, s[88:89]\ns_cbranch_execz .Lrd_rs6%=\n" \
"buffer_load_dwordx4 v[144:147], v196, %[rs], s57 offen offset:0 sc1\nbuffer_load_dwordx4 v[152:155], v196, %[rs], s57 offen offset:2048 sc1\n.Lrd_rs6%=:\ns_mov_b64 exec, s[68:69]\ns_cbranch_execz .Lrd_rs7%=\nbuffer_load_dwordx4 v[132:135], v196, %[rs], %[su] offen offset:1024 sc1\nbuffer_load_dwordx4 v[140:143], v196, %[rs], %[su] offen offset:3072 sc1\n.Lrd_rs7%=:\n" \
"s_mov_b64 exec, s[62:63]\ns_cbranch_execz .Lrd_rs8%=\nbuffer_load_dwordx4 v[148:151], v196, %[rs], s57 offen offset:1024 sc1\nbuffer_load_dwordx4 v[156:159], v196, %[rs], s57 offen offset:3072 sc1\n.Lrd_rs8%=:\ns_mov_b64 exec, -1\nv_mov_b64 v[180:181], 0\nv_mov_b64 v[182:183], 0\n" \
"v_mov_b64 v[184:185], 0\nv_mov_b64 v[186:187], 0\nv_mov_b64 v[188:189], 0\nv_mov_b64 v[190:191], 0\nv_mov_b64 v[192:193], 0\nv_mov_b64 v[194:195], 0\ns_waitcnt vmcnt(0)\ns_add_u32 s43, s43, 1\n" \
"s_cmp_lt_u32 s43, 262144\ns_cbranch_scc1 .Lrd_ra%=\ns_branch .Lrd_abort%=\n.Lrd_y1%=:\nv_accvgpr_read_b32 v206, a32\nbuffer_load_dword v208, v206, %[rs], 0 offen sc1\ns_waitcnt vmcnt(0)\nv_cmp_gt_i32 vcc, 1, v208\n" \
"s_cbranch_vccz .Lrd_y1r%=\ns_add_u32 s43, s43, 1\ns_cmp_lt_u32 s43, 262144\ns_cbranch_scc1 .Lrd_y1%=\ns_branch .Lrd_abort%=\n.Lrd_tre0%=:\ns_add_u32 s60, s58, 16384\nbuffer_load_dwordx4 v[212:215], v196, %[rs], s60 offen offset:0 sc1\n" \
"s_waitcnt vmcnt(0)\nv_bitop3_b32 v209, v212, v214, s61 bitop3:0x7e\nv_cmp_gt_i32 vcc, 0, v209\ns_cbranch_vccz .Lrd_tl0%=\ns_add_u32 s43, s43, 1\ns_cmp_lt_u32 s43, 262144\ns_cbranch_scc1 .Lrd_tre0%=\ns_branch .Lrd_abort%=\n" \
".Lrd_tre1%=:\ns_add_u32 s60, s58, 0\nbuffer_load_dwordx4 v[216:219], v196, %[rs], s60 offen offset:0 sc1\ns_waitcnt vmcnt(0)\nv_bitop3_b32 v209, v216, v218, s61 bitop3:0x7e\nv_cmp_gt_i32 vcc, 0, v209\ns_cbranch_vccz .Lrd_tl1%=\ns_add_u32 s43, s43, 1\n" \
"s_cmp_lt_u32 s43, 262144\ns_cbranch_scc1 .Lrd_tre1%=\ns_branch .Lrd_abort%=\n.Lrd_tre2%=:\ns_add_u32 s60, s58, 0\nbuffer_load_dwordx4 v[220:223], v196, %[rs], s60 offen offset:1024 sc1\ns_waitcnt vmcnt(0)\nv_bitop3_b32 v209, v220, v222, s61 bitop3:0x7e\n" \
"v_cmp_gt_i32 vcc, 0, v209\ns_cbranch_vccz .Lrd_tl2%=\ns_add_u32 s43, s43, 1\ns_cmp_lt_u32 s43, 262144\ns_cbranch_scc1 .Lrd_tre2%=\ns_branch .Lrd_abort%=\n.Lrd_tre3%=:\ns_add_u32 s60, s58, 16384\n" \
"buffer_load_dwordx4 v[224:227], v196, %[rs], s60 offen offset:1024 sc1\ns_waitcnt vmcnt(0)\nv_bitop3_b32 v209, v224, v226, s61 bitop3:0x7e\nv_cmp_gt_i32 vcc, 0, v209\ns_cbranch_vccz .Lrd_tl3%=\ns_add_u32 s43, s43, 1\ns_cmp_lt_u32 s43, 262144\ns_cbranch_scc1 .Lrd_tre3%=\n" \
"s_branch .Lrd_abort%=\n.Lrd_tre4%=:\ns_add_u32 s60, s58, 0\nbuffer_load_dwordx4 v[228:231], v196, %[rs], s60 offen offset:2048 sc1\ns_waitcnt vmcnt(0)\nv_bitop3_b32 v209, v228, v230, s61 bitop3:0x7e\nv_cmp_gt_i32 vcc, 0, v209\ns_cbranch_vccz .Lrd_tl4%=\n" \
"s_add_u32 s43, s43, 1\ns_cmp_lt_u32 s43, 262144\ns_cbranch_scc1 .Lrd_tre4%=\ns_branch .Lrd_abort%=\n.Lrd_tre5%=:\ns_add_u32 s60, s58, 0\nbuffer_load_dwordx4 v[232:235], v196, %[rs], s60 offen offset:3072 sc1\ns_waitcnt vmcnt(0)\n" \
"v_bitop3_b32 v209, v232, v234, s61 bitop3:0x7e\nv_cmp_gt_i32 vcc, 0, v209\ns_cbranch_vccz .Lrd_tl5%=\ns_add_u32 s43, s43, 1\ns_cmp_lt_u32 s43, 262144\ns_cbranch_scc1 .Lrd_tre5%=\ns_branch .Lrd_abort%=\n.Lrd_tre6%=:\n" \
"s_add_u32 s60, s58, 16384\nbuffer_load_dwordx4 v[236:239], v196, %[rs], s60 offen offset:2048 sc1\ns_waitcnt vmcnt(0)\nv_bitop3_b32 v209, v236, v238, s61 bitop3:0x7e\nv_cmp_gt_i32 vcc, 0, v209\ns_cbranch_vccz .Lrd_tl6%=\ns_add_u32 s43, s43, 1\ns_cmp_lt_u32 s43, 262144\n" \
"s_cbranch_scc1 .Lrd_tre6%=\ns_branch .Lrd_abort%=\n.Lrd_tre7%=:\ns_add_u32 s60, s58, 4096\nbuffer_load_dwordx4 v[240:243], v196, %[rs], s60 offen offset:0 sc1\ns_waitcnt vmcnt(0)\nv_bitop3_b32 v209, v240, v242, s61 bitop3:0x7e\nv_cmp_gt_i32 vcc, 0, v209\n" \
"s_cbranch_vccz .Lrd_tl7%=\ns_add_u32 s43, s43, 1\ns_cmp_lt_u32 s43, 262144\ns_cbranch_scc1 .Lrd_tre7%=\ns_branch .Lrd_abort%=\n.Lrd_tre8%=:\ns_add_u32 s60, s58, 4096\nbuffer_load_dwordx4 v[244:247], v196, %[rs], s60 offen offset:1024 sc1\n" \
"s_waitcnt vmcnt(0)\nv_bitop3_b32 v209, v244, v246, s61 bitop3:0x7e\nv_cmp_gt_i32 vcc, 0, v209\ns_cbranch_vccz .Lrd_tl8%=\ns_add_u32 s43, s43, 1\ns_cmp_lt_u32 s43, 262144\ns_cbranch_scc1 .Lrd_tre8%=\ns_branch .Lrd_abort%=\n" \
".Lrd_tre9%=:\ns_add_u32 s60, s58, 16384\nbuffer_load_dwordx4 v[248:251], v196, %[rs], s60 offen offset:3072 sc1\ns_waitcnt vmcnt(0)\nv_bitop3_b32 v209, v248, v250, s61 bitop3:0x7e\nv_cmp_gt_i32 vcc, 0, v209\ns_cbranch_vccz .Lrd_tl9%=\ns_add_u32 s43, s43, 1\n" \
"s_cmp_lt_u32 s43, 262144\ns_cbranch_scc1 .Lrd_tre9%=\ns_branch .Lrd_abort%=\n.Lrd_tre10%=:\ns_add_u32 s60, s58, 20480\nbuffer_load_dwordx4 v[252:255], v196, %[rs], s60 offen offset:0 sc1\ns_waitcnt vmcnt(0)\nv_bitop3_b32 v209, v252, v254, s61 bitop3:0x7e\n" \
"v_bitop3_b32 v210, v253, v255, s61 bitop3:0x7e\nv_or_b32 v209, v209, v210\nv_cmp_gt_i32 vcc, 0, v209\ns_cbranch_vccz .Lrd_tl10%=\ns_add_u32 s43, s43, 1\ns_cmp_lt_u32 s43, 262144\ns_cbranch_scc1 .Lrd_tre10%=\ns_branch .Lrd_abort%=\n" \
".Lrd_tre11%=:\ns_add_u32 s60, s58, 20480\nbuffer_load_dwordx4 v[160:163], v196, %[rs], s60 offen offset:2048 sc1\ns_waitcnt vmcnt(0)\nv_bitop3_b32 v209, v160, v162, s61 bitop3:0x7e\nv_bitop3_b32 v210, v161, v163, s61 bitop3:0x7e\nv_or_b32 v209, v209, v210\nv_cmp_gt_i32 vcc, 0, v209\n" \
"s_cbranch_vccz .Lrd_tl11%=\ns_add_u32 s43, s43, 1\ns_cmp_lt_u32 s43, 262144\ns_cbranch_scc1 .Lrd_tre11%=\ns_branch .Lrd_abort%=\n.Lrd_tre12%=:\ns_add_u32 s60, s58, 4096\nbuffer_load_dwordx4 v[164:167], v196, %[rs], s60 offen offset:2048 sc1\n" \
"s_waitcnt vmcnt(0)\nv_bitop3_b32 v209, v164, v166, s61 bitop3:0x7e\nv_cmp_gt_i32 vcc, 0, v209\ns_cbranch_vccz .Lrd_tl12%=\ns_add_u32 s43, s43, 1\ns_cmp_lt_u32 s43, 262144\ns_cbranch_scc1 .Lrd_tre12%=\ns_branch .Lrd_abort%=\n" \
".Lrd_tre13%=:\ns_add_u32 s60, s58, 4096\nbuffer_load_dwordx4 v[168:171], v196, %[rs], s60 offen offset:3072 sc1\ns_waitcnt vmcnt(0)\nv_bitop3_b32 v209, v168, v170, s61 bitop3:0x7e\nv_cmp_gt_i32 vcc, 0, v209\ns_cbranch_vccz .Lrd_tl13%=\ns_add_u32 s43, s43, 1\n" \
"s_cmp_lt_u32 s43, 262144\ns_cbranch_scc1 .Lrd_tre13%=\ns_branch .Lrd_abort%=\n.Lrd_tre14%=:\ns_add_u32 s60, s58, 20480\nbuffer_load_dwordx4 v[172:175], v196, %[rs], s60 offen offset:1024 sc1\ns_waitcnt vmcnt(0)\nv_bitop3_b32 v209, v172, v174, s61 bitop3:0x7e\n" \
"v_bitop3_b32 v210, v173, v175, s61 bitop3:0x7e\nv_or_b32 v209, v209, v210\nv_cmp_gt_i32 vcc, 0, v209\ns_cbranch_vccz .Lrd_tl14%=\ns_add_u32 s43, s43, 1\ns_cmp_lt_u32 s43, 262144\ns_cbranch_scc1 .Lrd_tre14%=\ns_branch .Lrd_abort%=\n" \
".Lrd_tre15%=:\ns_add_u32 s60, s58, 20480\nbuffer_load_dwordx4 v[176:179], v196, %[rs], s60 offen offset:3072 sc1\ns_waitcnt vmcnt(0)\nv_bitop3_b32 v209, v176, v178, s61 bitop3:0x7e\nv_bitop3_b32 v210, v177, v179, s61 bitop3:0x7e\nv_or_b32 v209, v209, v210\nv_cmp_gt_i32 vcc, 0, v209\n" \
"s_cbranch_vccz .Lrd_tl15%=\ns_add_u32 s43, s43, 1\ns_cmp_lt_u32 s43, 262144\ns_cbranch_scc1 .Lrd_tre15%=\ns_branch .Lrd_abort%=\n.Lrd_abort%=:\ns_mov_b64 exec, -1\nv_mov_b32 v16, 0\n" \
"s_movk_i32 s44, 6400\nbuffer_store_dword v16, off, %[rs], s44 sc1\n.Lrd_end%=:\ns_waitcnt vmcnt(0) lgkmcnt(0)\n" \
""
#define GG_ASM \
"v_mbcnt_lo_u32_b32 v255, -1, 0\nv_mbcnt_hi_u32_b32 v255, -1, v255\nv_lshlrev_b32 v255, 2, v255\nv_add_u32 v255, %[tb], v255\nds_read_b32 v0, v255 offset:0\nds_read_b32 v1, v255 offset:256\nds_read_b32 v2, v255 offset:512\nds_read_b32 v3, v255 offset:768\n" \
"ds_read_b32 v4, v255 offset:1024\nds_read_b32 v5, v255 offset:1280\nds_read_b32 v92, v255 offset:1536\nds_read_b32 v93, v255 offset:1792\nds_read_b32 v94, v255 offset:2048\ns_waitcnt lgkmcnt(0)\nv_mov_b32 v6, 0x3f803f80\nv_mov_b32 v7, 0x3f803f80\n" \
"s_add_u32 m0, %[lw], 16384\ns_add_u32 s44, %[wo], 0\nbuffer_load_dwordx4 v92, %[rw], s44 offen nt lds\ns_add_u32 m0, %[lw], 17408\ns_add_u32 s44, %[wo], 57344\nbuffer_load_dwordx4 v92, %[rw], s44 offen nt lds\ns_add_u32 m0, %[lw], 18432\ns_add_u32 s44, %[wo], 114688\n" \
"buffer_load_dwordx4 v92, %[rw], s44 offen nt lds\ns_add_u32 m0, %[lw], 19456\ns_add_u32 s44, %[wo], 172032\nbuffer_load_dwordx4 v92, %[rw], s44 offen nt lds\ns_add_u32 m0, %[lw], 20480\ns_add_u32 s44, %[wo], 229376\nbuffer_load_dwordx4 v92, %[rw], s44 offen nt lds\ns_add_u32 m0, %[lw], 21504\n" \
"s_add_u32 s44, %[wo], 286720\nbuffer_load_dwordx4 v92, %[rw], s44 offen nt lds\ns_add_u32 m0, %[lw], 22528\ns_add_u32 s44, %[wo], 344064\nbuffer_load_dwordx4 v92, %[rw], s44 offen nt lds\ns_add_u32 m0, %[lw], 23552\ns_add_u32 s44, %[wo], 401408\nbuffer_load_dwordx4 v92, %[rw], s44 offen nt lds\n" \
"s_add_u32 m0, %[tb], 0\ns_add_u32 s44, %[wo], 1835008\nbuffer_load_dwordx4 v92, %[rw], s44 offen nt lds\ns_add_u32 m0, %[tb], 1024\ns_add_u32 s44, %[wo], 1892352\nbuffer_load_dwordx4 v92, %[rw], s44 offen nt lds\ns_add_u32 m0, %[tb], 2048\ns_add_u32 s44, %[wo], 1949696\n" \
"buffer_load_dwordx4 v92, %[rw], s44 offen nt lds\ns_add_u32 m0, %[tb], 3072\ns_add_u32 s44, %[wo], 2007040\nbuffer_load_dwordx4 v92, %[rw], s44 offen nt lds\ns_add_u32 m0, %[tb], 4096\ns_add_u32 s44, %[wo], 2064384\nbuffer_load_dwordx4 v92, %[rw], s44 offen nt lds\ns_add_u32 m0, %[tb], 5120\n" \
"s_add_u32 s44, %[wo], 2121728\nbuffer_load_dwordx4 v92, %[rw], s44 offen nt lds\ns_add_u32 m0, %[tb], 6144\ns_add_u32 s44, %[wo], 2179072\nbuffer_load_dwordx4 v92, %[rw], s44 offen nt lds\ns_add_u32 m0, %[tb], 7168\ns_add_u32 s44, %[wo], 2236416\nbuffer_load_dwordx4 v92, %[rw], s44 offen nt lds\n" \
"s_add_u32 m0, %[tb], 8192\ns_add_u32 s44, %[wo], 3670016\nbuffer_load_dwordx4 v92, %[rw], s44 offen nt lds\ns_add_u32 m0, %[tb], 9216\ns_add_u32 s44, %[wo], 3727360\nbuffer_load_dwordx4 v92, %[rw], s44 offen nt lds\ns_add_u32 m0, %[tb], 10240\ns_add_u32 s44, %[wo], 3784704\n" \
"buffer_load_dwordx4 v92, %[rw], s44 offen nt lds\ns_add_u32 m0, %[tb], 11264\ns_add_u32 s44, %[wo], 3842048\nbuffer_load_dwordx4 v92, %[rw], s44 offen nt lds\ns_add_u32 m0, %[lw], 0\ns_add_u32 s44, %[wo], 3899392\nbuffer_load_dwordx4 v92, %[rw], s44 offen nt lds\ns_add_u32 m0, %[lw], 1024\n" \
"s_add_u32 s44, %[wo], 3956736\nbuffer_load_dwordx4 v92, %[rw], s44 offen nt lds\ns_add_u32 m0, %[lw], 2048\ns_add_u32 s44, %[wo], 4014080\nbuffer_load_dwordx4 v92, %[rw], s44 offen nt lds\ns_add_u32 m0, %[lw], 3072\ns_add_u32 s44, %[wo], 4071424\nbuffer_load_dwordx4 v92, %[rw], s44 offen nt lds\n" \
"s_add_u32 m0, %[lw], 4096\ns_add_u32 s44, %[wo], 5505024\nbuffer_load_dwordx4 v92, %[rw], s44 offen nt lds\ns_add_u32 m0, %[lw], 5120\ns_add_u32 s44, %[wo], 5562368\nbuffer_load_dwordx4 v92, %[rw], s44 offen nt lds\ns_add_u32 m0, %[lw], 6144\ns_add_u32 s44, %[wo], 5619712\n" \
"buffer_load_dwordx4 v92, %[rw], s44 offen nt lds\ns_add_u32 m0, %[lw], 7168\ns_add_u32 s44, %[wo], 5677056\nbuffer_load_dwordx4 v92, %[rw], s44 offen nt lds\ns_add_u32 m0, %[lw], 8192\ns_add_u32 s44, %[wo], 5734400\nbuffer_load_dwordx4 v92, %[rw], s44 offen nt lds\ns_add_u32 m0, %[lw], 9216\n" \
"s_add_u32 s44, %[wo], 5791744\nbuffer_load_dwordx4 v92, %[rw], s44 offen nt lds\ns_add_u32 m0, %[lw], 10240\ns_add_u32 s44, %[wo], 5849088\nbuffer_load_dwordx4 v92, %[rw], s44 offen nt lds\ns_add_u32 m0, %[lw], 11264\ns_add_u32 s44, %[wo], 5906432\nbuffer_load_dwordx4 v92, %[rw], s44 offen nt lds\n" \
"s_waitcnt vmcnt(28)\ns_barrier\ns_add_u32 m0, %[lw], 12288\ns_add_u32 s44, %[wo], 458752\nbuffer_load_dwordx4 v92, %[rw], s44 offen nt lds\ns_add_u32 m0, %[lw], 13312\ns_add_u32 s44, %[wo], 516096\nbuffer_load_dwordx4 v92, %[rw], s44 offen nt lds\n" \
"s_add_u32 m0, %[lw], 14336\ns_add_u32 s44, %[wo], 573440\nbuffer_load_dwordx4 v92, %[rw], s44 offen nt lds\ns_add_u32 m0, %[lw], 15360\ns_add_u32 s44, %[wo], 630784\nbuffer_load_dwordx4 v92, %[rw], s44 offen nt lds\nds_read_b64_tr_b16 a[0:1], v94 offset:16384\nds_read_b64_tr_b16 a[2:3], v94 offset:16896\n" \
"ds_read_b64_tr_b16 a[8:9], v94 offset:16392\nds_read_b64_tr_b16 a[10:11], v94 offset:16904\nds_read_b64_tr_b16 a[16:17], v94 offset:40960\nds_read_b64_tr_b16 a[18:19], v94 offset:41472\nds_read_b64_tr_b16 a[24:25], v94 offset:40968\nds_read_b64_tr_b16 a[26:27], v94 offset:41480\ns_waitcnt lgkmcnt(0)\nv_mfma_f32_16x16x16_bf16 v[8:11], a[0:1], v[6:7], 0\n" \
"v_mfma_f32_16x16x16_bf16 v[12:15], a[8:9], v[6:7], 0\nv_mfma_f32_16x16x16_bf16 v[16:19], a[16:17], v[6:7], 0\nv_mfma_f32_16x16x16_bf16 v[20:23], a[24:25], v[6:7], 0\nv_mfma_f32_16x16x16_bf16 v[8:11], a[2:3], v[6:7], v[8:11]\nv_mfma_f32_16x16x16_bf16 v[12:15], a[10:11], v[6:7], v[12:15]\nv_mfma_f32_16x16x16_bf16 v[16:19], a[18:19], v[6:7], v[16:19]\nv_mfma_f32_16x16x16_bf16 v[20:23], a[26:27], v[6:7], v[20:23]\ns_waitcnt vmcnt(28)\n" \
"s_barrier\ns_add_u32 m0, %[lw], 16384\ns_add_u32 s44, %[wo], 688128\nbuffer_load_dwordx4 v92, %[rw], s44 offen nt lds\ns_add_u32 m0, %[lw], 17408\ns_add_u32 s44, %[wo], 745472\nbuffer_load_dwordx4 v92, %[rw], s44 offen nt lds\ns_add_u32 m0, %[lw], 18432\n" \
"s_add_u32 s44, %[wo], 802816\nbuffer_load_dwordx4 v92, %[rw], s44 offen nt lds\ns_add_u32 m0, %[lw], 19456\ns_add_u32 s44, %[wo], 860160\nbuffer_load_dwordx4 v92, %[rw], s44 offen nt lds\nds_read_b64_tr_b16 a[4:5], v94 offset:20480\nds_read_b64_tr_b16 a[6:7], v94 offset:20992\nds_read_b64_tr_b16 a[12:13], v94 offset:20488\n" \
"ds_read_b64_tr_b16 a[14:15], v94 offset:21000\nds_read_b64_tr_b16 a[20:21], v94 offset:45056\nds_read_b64_tr_b16 a[22:23], v94 offset:45568\nds_read_b64_tr_b16 a[28:29], v94 offset:45064\nds_read_b64_tr_b16 a[30:31], v94 offset:45576\ns_waitcnt lgkmcnt(0)\nv_mfma_f32_16x16x16_bf16 v[8:11], a[4:5], v[6:7], v[8:11]\nv_mfma_f32_16x16x16_bf16 v[12:15], a[12:13], v[6:7], v[12:15]\n" \
"v_mfma_f32_16x16x16_bf16 v[16:19], a[20:21], v[6:7], v[16:19]\nv_mfma_f32_16x16x16_bf16 v[20:23], a[28:29], v[6:7], v[20:23]\nv_mfma_f32_16x16x16_bf16 v[8:11], a[6:7], v[6:7], v[8:11]\nv_mfma_f32_16x16x16_bf16 v[12:15], a[14:15], v[6:7], v[12:15]\nv_mfma_f32_16x16x16_bf16 v[16:19], a[22:23], v[6:7], v[16:19]\nv_mfma_f32_16x16x16_bf16 v[20:23], a[30:31], v[6:7], v[20:23]\ns_waitcnt vmcnt(28)\ns_barrier\n" \
"s_add_u32 m0, %[lw], 20480\ns_add_u32 s44, %[wo], 917504\nbuffer_load_dwordx4 v92, %[rw], s44 offen nt lds\ns_add_u32 m0, %[lw], 21504\ns_add_u32 s44, %[wo], 974848\nbuffer_load_dwordx4 v92, %[rw], s44 offen nt lds\ns_add_u32 m0, %[lw], 22528\ns_add_u32 s44, %[wo], 1032192\n" \
"buffer_load_dwordx4 v92, %[rw], s44 offen nt lds\ns_add_u32 m0, %[lw], 23552\ns_add_u32 s44, %[wo], 1089536\nbuffer_load_dwordx4 v92, %[rw], s44 offen nt lds\nds_read_b64_tr_b16 a[104:105], v93 offset:0\nds_read_b64_tr_b16 a[106:107], v93 offset:512\nds_read_b64_tr_b16 a[112:113], v93 offset:8\nds_read_b64_tr_b16 a[114:115], v93 offset:520\n" \
"ds_read_b64_tr_b16 a[120:121], v93 offset:12288\nds_read_b64_tr_b16 a[122:123], v93 offset:12800\nds_read_b64_tr_b16 a[128:129], v93 offset:12296\nds_read_b64_tr_b16 a[130:131], v93 offset:12808\ns_waitcnt lgkmcnt(0)\nv_mfma_f32_16x16x16_bf16 v[8:11], a[104:105], v[6:7], v[8:11]\nv_mfma_f32_16x16x16_bf16 v[12:15], a[112:113], v[6:7], v[12:15]\nv_mfma_f32_16x16x16_bf16 v[16:19], a[120:121], v[6:7], v[16:19]\n" \
"v_mfma_f32_16x16x16_bf16 v[20:23], a[128:129], v[6:7], v[20:23]\nv_mfma_f32_16x16x16_bf16 v[8:11], a[106:107], v[6:7], v[8:11]\nv_mfma_f32_16x16x16_bf16 v[12:15], a[114:115], v[6:7], v[12:15]\nv_mfma_f32_16x16x16_bf16 v[16:19], a[122:123], v[6:7], v[16:19]\nv_mfma_f32_16x16x16_bf16 v[20:23], a[130:131], v[6:7], v[20:23]\ns_waitcnt vmcnt(28)\ns_barrier\ns_add_u32 m0, %[tb], 0\n" \
"s_add_u32 s44, %[wo], 1146880\nbuffer_load_dwordx4 v92, %[rw], s44 offen nt lds\ns_add_u32 m0, %[tb], 1024\ns_add_u32 s44, %[wo], 1204224\nbuffer_load_dwordx4 v92, %[rw], s44 offen nt lds\ns_add_u32 m0, %[tb], 2048\ns_add_u32 s44, %[wo], 1261568\nbuffer_load_dwordx4 v92, %[rw], s44 offen nt lds\n" \
"s_add_u32 m0, %[tb], 3072\ns_add_u32 s44, %[wo], 1318912\nbuffer_load_dwordx4 v92, %[rw], s44 offen nt lds\nds_read_b64_tr_b16 a[108:109], v93 offset:4096\nds_read_b64_tr_b16 a[110:111], v93 offset:4608\nds_read_b64_tr_b16 a[116:117], v93 offset:4104\nds_read_b64_tr_b16 a[118:119], v93 offset:4616\nds_read_b64_tr_b16 a[124:125], v93 offset:16384\n" \
"ds_read_b64_tr_b16 a[126:127], v93 offset:16896\nds_read_b64_tr_b16 a[132:133], v93 offset:16392\nds_read_b64_tr_b16 a[134:135], v93 offset:16904\ns_waitcnt lgkmcnt(0)\nv_mfma_f32_16x16x16_bf16 v[8:11], a[108:109], v[6:7], v[8:11]\nv_mfma_f32_16x16x16_bf16 v[12:15], a[116:117], v[6:7], v[12:15]\nv_mfma_f32_16x16x16_bf16 v[16:19], a[124:125], v[6:7], v[16:19]\nv_mfma_f32_16x16x16_bf16 v[20:23], a[132:133], v[6:7], v[20:23]\n" \
"v_mfma_f32_16x16x16_bf16 v[8:11], a[110:111], v[6:7], v[8:11]\nv_mfma_f32_16x16x16_bf16 v[12:15], a[118:119], v[6:7], v[12:15]\nv_mfma_f32_16x16x16_bf16 v[16:19], a[126:127], v[6:7], v[16:19]\nv_mfma_f32_16x16x16_bf16 v[20:23], a[134:135], v[6:7], v[20:23]\ns_waitcnt vmcnt(28)\ns_barrier\ns_add_u32 m0, %[tb], 4096\ns_add_u32 s44, %[wo], 1376256\n" \
"buffer_load_dwordx4 v92, %[rw], s44 offen nt lds\ns_add_u32 m0, %[tb], 5120\ns_add_u32 s44, %[wo], 1433600\nbuffer_load_dwordx4 v92, %[rw], s44 offen nt lds\ns_add_u32 m0, %[tb], 6144\ns_add_u32 s44, %[wo], 1490944\nbuffer_load_dwordx4 v92, %[rw], s44 offen nt lds\ns_add_u32 m0, %[tb], 7168\n" \
"s_add_u32 s44, %[wo], 1548288\nbuffer_load_dwordx4 v92, %[rw], s44 offen nt lds\nds_read_b64_tr_b16 a[208:209], v93 offset:8192\nds_read_b64_tr_b16 a[210:211], v93 offset:8704\nds_read_b64_tr_b16 a[216:217], v93 offset:8200\nds_read_b64_tr_b16 a[218:219], v93 offset:8712\nds_read_b64_tr_b16 a[224:225], v93 offset:20480\nds_read_b64_tr_b16 a[226:227], v93 offset:20992\n" \
"ds_read_b64_tr_b16 a[232:233], v93 offset:20488\nds_read_b64_tr_b16 a[234:235], v93 offset:21000\ns_waitcnt lgkmcnt(0)\nv_mfma_f32_16x16x16_bf16 v[8:11], a[208:209], v[6:7], v[8:11]\nv_mfma_f32_16x16x16_bf16 v[12:15], a[216:217], v[6:7], v[12:15]\nv_mfma_f32_16x16x16_bf16 v[16:19], a[224:225], v[6:7], v[16:19]\nv_mfma_f32_16x16x16_bf16 v[20:23], a[232:233], v[6:7], v[20:23]\nv_mfma_f32_16x16x16_bf16 v[8:11], a[210:211], v[6:7], v[8:11]\n" \
"v_mfma_f32_16x16x16_bf16 v[12:15], a[218:219], v[6:7], v[12:15]\nv_mfma_f32_16x16x16_bf16 v[16:19], a[226:227], v[6:7], v[16:19]\nv_mfma_f32_16x16x16_bf16 v[20:23], a[234:235], v[6:7], v[20:23]\ns_waitcnt vmcnt(28)\ns_barrier\ns_add_u32 m0, %[tb], 8192\ns_add_u32 s44, %[wo], 1605632\nbuffer_load_dwordx4 v92, %[rw], s44 offen nt lds\n" \
"s_add_u32 m0, %[tb], 9216\ns_add_u32 s44, %[wo], 1662976\nbuffer_load_dwordx4 v92, %[rw], s44 offen nt lds\ns_add_u32 m0, %[tb], 10240\ns_add_u32 s44, %[wo], 1720320\nbuffer_load_dwordx4 v92, %[rw], s44 offen nt lds\ns_add_u32 m0, %[tb], 11264\ns_add_u32 s44, %[wo], 1777664\n" \
"buffer_load_dwordx4 v92, %[rw], s44 offen nt lds\nds_read_b64_tr_b16 a[212:213], v94 offset:0\nds_read_b64_tr_b16 a[214:215], v94 offset:512\nds_read_b64_tr_b16 a[220:221], v94 offset:8\nds_read_b64_tr_b16 a[222:223], v94 offset:520\nds_read_b64_tr_b16 a[228:229], v94 offset:24576\nds_read_b64_tr_b16 a[230:231], v94 offset:25088\nds_read_b64_tr_b16 a[236:237], v94 offset:24584\n" \
"ds_read_b64_tr_b16 a[238:239], v94 offset:25096\ns_waitcnt lgkmcnt(0)\nv_mfma_f32_16x16x16_bf16 v[8:11], a[212:213], v[6:7], v[8:11]\nv_mfma_f32_16x16x16_bf16 v[12:15], a[220:221], v[6:7], v[12:15]\nv_mfma_f32_16x16x16_bf16 v[16:19], a[228:229], v[6:7], v[16:19]\nv_mfma_f32_16x16x16_bf16 v[20:23], a[236:237], v[6:7], v[20:23]\nv_mfma_f32_16x16x16_bf16 v[8:11], a[214:215], v[6:7], v[8:11]\nv_mfma_f32_16x16x16_bf16 v[12:15], a[222:223], v[6:7], v[12:15]\n" \
"v_mfma_f32_16x16x16_bf16 v[16:19], a[230:231], v[6:7], v[16:19]\nv_mfma_f32_16x16x16_bf16 v[20:23], a[238:239], v[6:7], v[20:23]\ns_waitcnt vmcnt(28)\ns_barrier\ns_add_u32 m0, %[lw], 0\ns_add_u32 s44, %[wo], 2293760\nbuffer_load_dwordx4 v92, %[rw], s44 offen nt lds\ns_add_u32 m0, %[lw], 1024\n" \
"s_add_u32 s44, %[wo], 2351104\nbuffer_load_dwordx4 v92, %[rw], s44 offen nt lds\ns_add_u32 m0, %[lw], 2048\ns_add_u32 s44, %[wo], 2408448\nbuffer_load_dwordx4 v92, %[rw], s44 offen nt lds\ns_add_u32 m0, %[lw], 3072\ns_add_u32 s44, %[wo], 2465792\nbuffer_load_dwordx4 v92, %[rw], s44 offen nt lds\n" \
"ds_read_b64_tr_b16 v[152:153], v94 offset:4096\nds_read_b64_tr_b16 v[154:155], v94 offset:4608\nds_read_b64_tr_b16 v[160:161], v94 offset:4104\nds_read_b64_tr_b16 v[162:163], v94 offset:4616\nds_read_b64_tr_b16 v[168:169], v94 offset:28672\nds_read_b64_tr_b16 v[170:171], v94 offset:29184\nds_read_b64_tr_b16 v[176:177], v94 offset:28680\nds_read_b64_tr_b16 v[178:179], v94 offset:29192\n" \
"s_waitcnt lgkmcnt(0)\nv_mfma_f32_16x16x16_bf16 v[8:11], v[152:153], v[6:7], v[8:11]\nv_mfma_f32_16x16x16_bf16 v[12:15], v[160:161], v[6:7], v[12:15]\nv_mfma_f32_16x16x16_bf16 v[16:19], v[168:169], v[6:7], v[16:19]\nv_mfma_f32_16x16x16_bf16 v[20:23], v[176:177], v[6:7], v[20:23]\nv_mfma_f32_16x16x16_bf16 v[8:11], v[154:155], v[6:7], v[8:11]\nv_mfma_f32_16x16x16_bf16 v[12:15], v[162:163], v[6:7], v[12:15]\nv_mfma_f32_16x16x16_bf16 v[16:19], v[170:171], v[6:7], v[16:19]\n" \
"v_mfma_f32_16x16x16_bf16 v[20:23], v[178:179], v[6:7], v[20:23]\ns_waitcnt vmcnt(28)\ns_barrier\ns_add_u32 m0, %[lw], 4096\ns_add_u32 s44, %[wo], 2523136\nbuffer_load_dwordx4 v92, %[rw], s44 offen nt lds\ns_add_u32 m0, %[lw], 5120\ns_add_u32 s44, %[wo], 2580480\n" \
"buffer_load_dwordx4 v92, %[rw], s44 offen nt lds\ns_add_u32 m0, %[lw], 6144\ns_add_u32 s44, %[wo], 2637824\nbuffer_load_dwordx4 v92, %[rw], s44 offen nt lds\ns_add_u32 m0, %[lw], 7168\ns_add_u32 s44, %[wo], 2695168\nbuffer_load_dwordx4 v92, %[rw], s44 offen nt lds\nds_read_b64_tr_b16 v[156:157], v94 offset:8192\n" \
"ds_read_b64_tr_b16 v[158:159], v94 offset:8704\nds_read_b64_tr_b16 v[164:165], v94 offset:8200\nds_read_b64_tr_b16 v[166:167], v94 offset:8712\nds_read_b64_tr_b16 v[172:173], v94 offset:32768\nds_read_b64_tr_b16 v[174:175], v94 offset:33280\nds_read_b64_tr_b16 v[180:181], v94 offset:32776\nds_read_b64_tr_b16 v[182:183], v94 offset:33288\ns_waitcnt lgkmcnt(0)\n" \
"v_mfma_f32_16x16x16_bf16 v[8:11], v[156:157], v[6:7], v[8:11]\nv_mfma_f32_16x16x16_bf16 v[12:15], v[164:165], v[6:7], v[12:15]\nv_mfma_f32_16x16x16_bf16 v[16:19], v[172:173], v[6:7], v[16:19]\nv_mfma_f32_16x16x16_bf16 v[20:23], v[180:181], v[6:7], v[20:23]\nv_mfma_f32_16x16x16_bf16 v[8:11], v[158:159], v[6:7], v[8:11]\nv_mfma_f32_16x16x16_bf16 v[12:15], v[166:167], v[6:7], v[12:15]\nv_mfma_f32_16x16x16_bf16 v[16:19], v[174:175], v[6:7], v[16:19]\nv_mfma_f32_16x16x16_bf16 v[20:23], v[182:183], v[6:7], v[20:23]\n" \
"s_waitcnt vmcnt(28)\ns_barrier\ns_add_u32 m0, %[lw], 8192\ns_add_u32 s44, %[wo], 2752512\nbuffer_load_dwordx4 v92, %[rw], s44 offen nt lds\ns_add_u32 m0, %[lw], 9216\ns_add_u32 s44, %[wo], 2809856\nbuffer_load_dwordx4 v92, %[rw], s44 offen nt lds\n" \
"s_add_u32 m0, %[lw], 10240\ns_add_u32 s44, %[wo], 2867200\nbuffer_load_dwordx4 v92, %[rw], s44 offen nt lds\ns_add_u32 m0, %[lw], 11264\ns_add_u32 s44, %[wo], 2924544\nbuffer_load_dwordx4 v92, %[rw], s44 offen nt lds\nds_read_b64_tr_b16 a[32:33], v94 offset:12288\nds_read_b64_tr_b16 a[34:35], v94 offset:12800\n" \
"ds_read_b64_tr_b16 v[88:89], v94 offset:12296\nds_read_b64_tr_b16 v[90:91], v94 offset:12808\nds_read_b64_tr_b16 a[40:41], v94 offset:36864\nds_read_b64_tr_b16 a[42:43], v94 offset:37376\nds_read_b64_tr_b16 a[48:49], v94 offset:36872\nds_read_b64_tr_b16 a[50:51], v94 offset:37384\ns_waitcnt lgkmcnt(0)\nv_mfma_f32_16x16x16_bf16 v[8:11], a[32:33], v[6:7], v[8:11]\n" \
"v_mfma_f32_16x16x16_bf16 v[12:15], v[88:89], v[6:7], v[12:15]\nv_mfma_f32_16x16x16_bf16 v[16:19], a[40:41], v[6:7], v[16:19]\nv_mfma_f32_16x16x16_bf16 v[20:23], a[48:49], v[6:7], v[20:23]\nv_mfma_f32_16x16x16_bf16 v[8:11], a[34:35], v[6:7], v[8:11]\nv_mfma_f32_16x16x16_bf16 v[12:15], v[90:91], v[6:7], v[12:15]\nv_mfma_f32_16x16x16_bf16 v[16:19], a[42:43], v[6:7], v[16:19]\nv_mfma_f32_16x16x16_bf16 v[20:23], a[50:51], v[6:7], v[20:23]\ns_waitcnt vmcnt(28)\n" \
"s_barrier\ns_add_u32 m0, %[lw], 12288\ns_add_u32 s44, %[wo], 2981888\nbuffer_load_dwordx4 v92, %[rw], s44 offen nt lds\ns_add_u32 m0, %[lw], 13312\ns_add_u32 s44, %[wo], 3039232\nbuffer_load_dwordx4 v92, %[rw], s44 offen nt lds\ns_add_u32 m0, %[lw], 14336\n" \
"s_add_u32 s44, %[wo], 3096576\nbuffer_load_dwordx4 v92, %[rw], s44 offen nt lds\ns_add_u32 m0, %[lw], 15360\ns_add_u32 s44, %[wo], 3153920\nbuffer_load_dwordx4 v92, %[rw], s44 offen nt lds\nds_read_b64_tr_b16 a[36:37], v94 offset:16384\nds_read_b64_tr_b16 a[38:39], v94 offset:16896\nds_read_b64_tr_b16 v[84:85], v94 offset:16392\n" \
"ds_read_b64_tr_b16 v[86:87], v94 offset:16904\nds_read_b64_tr_b16 a[44:45], v94 offset:40960\nds_read_b64_tr_b16 a[46:47], v94 offset:41472\nds_read_b64_tr_b16 a[52:53], v94 offset:40968\nds_read_b64_tr_b16 a[54:55], v94 offset:41480\ns_waitcnt lgkmcnt(0)\nv_mfma_f32_16x16x16_bf16 v[8:11], a[36:37], v[6:7], v[8:11]\nv_mfma_f32_16x16x16_bf16 v[12:15], v[84:85], v[6:7], v[12:15]\n" \
"v_mfma_f32_16x16x16_bf16 v[16:19], a[44:45], v[6:7], v[16:19]\nv_mfma_f32_16x16x16_bf16 v[20:23], a[52:53], v[6:7], v[20:23]\nv_mfma_f32_16x16x16_bf16 v[8:11], a[38:39], v[6:7], v[8:11]\nv_mfma_f32_16x16x16_bf16 v[12:15], v[86:87], v[6:7], v[12:15]\nv_mfma_f32_16x16x16_bf16 v[16:19], a[46:47], v[6:7], v[16:19]\nv_mfma_f32_16x16x16_bf16 v[20:23], a[54:55], v[6:7], v[20:23]\ns_waitcnt vmcnt(28)\ns_barrier\n" \
"s_add_u32 m0, %[lw], 16384\ns_add_u32 s44, %[wo], 3211264\nbuffer_load_dwordx4 v92, %[rw], s44 offen nt lds\ns_add_u32 m0, %[lw], 17408\ns_add_u32 s44, %[wo], 3268608\nbuffer_load_dwordx4 v92, %[rw], s44 offen nt lds\ns_add_u32 m0, %[lw], 18432\ns_add_u32 s44, %[wo], 3325952\n" \
"buffer_load_dwordx4 v92, %[rw], s44 offen nt lds\ns_add_u32 m0, %[lw], 19456\ns_add_u32 s44, %[wo], 3383296\nbuffer_load_dwordx4 v92, %[rw], s44 offen nt lds\nds_read_b64_tr_b16 a[56:57], v94 offset:20480\nds_read_b64_tr_b16 a[58:59], v94 offset:20992\nds_read_b64_tr_b16 a[64:65], v94 offset:20488\nds_read_b64_tr_b16 a[66:67], v94 offset:21000\n" \
"ds_read_b64_tr_b16 v[80:81], v94 offset:45056\nds_read_b64_tr_b16 v[82:83], v94 offset:45568\nds_read_b64_tr_b16 a[72:73], v94 offset:45064\nds_read_b64_tr_b16 a[74:75], v94 offset:45576\ns_waitcnt lgkmcnt(0)\nv_mfma_f32_16x16x16_bf16 v[8:11], a[56:57], v[6:7], v[8:11]\nv_mfma_f32_16x16x16_bf16 v[12:15], a[64:65], v[6:7], v[12:15]\nv_mfma_f32_16x16x16_bf16 v[16:19], v[80:81], v[6:7], v[16:19]\n" \
"v_mfma_f32_16x16x16_bf16 v[20:23], a[72:73], v[6:7], v[20:23]\nv_mfma_f32_16x16x16_bf16 v[8:11], a[58:59], v[6:7], v[8:11]\nv_mfma_f32_16x16x16_bf16 v[12:15], a[66:67], v[6:7], v[12:15]\nv_mfma_f32_16x16x16_bf16 v[16:19], v[82:83], v[6:7], v[16:19]\nv_mfma_f32_16x16x16_bf16 v[20:23], a[74:75], v[6:7], v[20:23]\ns_waitcnt vmcnt(28)\ns_barrier\ns_add_u32 m0, %[lw], 20480\n" \
"s_add_u32 s44, %[wo], 3440640\nbuffer_load_dwordx4 v92, %[rw], s44 offen nt lds\ns_add_u32 m0, %[lw], 21504\ns_add_u32 s44, %[wo], 3497984\nbuffer_load_dwordx4 v92, %[rw], s44 offen nt lds\ns_add_u32 m0, %[lw], 22528\ns_add_u32 s44, %[wo], 3555328\nbuffer_load_dwordx4 v92, %[rw], s44 offen nt lds\n" \
"s_add_u32 m0, %[lw], 23552\ns_add_u32 s44, %[wo], 3612672\nbuffer_load_dwordx4 v92, %[rw], s44 offen nt lds\nds_read_b64_tr_b16 a[60:61], v93 offset:0\nds_read_b64_tr_b16 a[62:63], v93 offset:512\nds_read_b64_tr_b16 a[68:69], v93 offset:8\nds_read_b64_tr_b16 a[70:71], v93 offset:520\nds_read_b64_tr_b16 v[76:77], v93 offset:12288\n" \
"ds_read_b64_tr_b16 v[78:79], v93 offset:12800\nds_read_b64_tr_b16 a[76:77], v93 offset:12296\nds_read_b64_tr_b16 a[78:79], v93 offset:12808\ns_waitcnt lgkmcnt(0)\nv_mfma_f32_16x16x16_bf16 v[8:11], a[60:61], v[6:7], v[8:11]\nv_mfma_f32_16x16x16_bf16 v[12:15], a[68:69], v[6:7], v[12:15]\nv_mfma_f32_16x16x16_bf16 v[16:19], v[76:77], v[6:7], v[16:19]\nv_mfma_f32_16x16x16_bf16 v[20:23], a[76:77], v[6:7], v[20:23]\n" \
"v_mfma_f32_16x16x16_bf16 v[8:11], a[62:63], v[6:7], v[8:11]\nv_mfma_f32_16x16x16_bf16 v[12:15], a[70:71], v[6:7], v[12:15]\nv_mfma_f32_16x16x16_bf16 v[16:19], v[78:79], v[6:7], v[16:19]\nv_mfma_f32_16x16x16_bf16 v[20:23], a[78:79], v[6:7], v[20:23]\ns_waitcnt vmcnt(28)\ns_barrier\ns_add_u32 m0, %[tb], 0\ns_add_u32 s44, %[wo], 4128768\n" \
"buffer_load_dwordx4 v92, %[rw], s44 offen nt lds\ns_add_u32 m0, %[tb], 1024\ns_add_u32 s44, %[wo], 4186112\nbuffer_load_dwordx4 v92, %[rw], s44 offen nt lds\ns_add_u32 m0, %[tb], 2048\ns_add_u32 s44, %[wo], 4243456\nbuffer_load_dwordx4 v92, %[rw], s44 offen nt lds\ns_add_u32 m0, %[tb], 3072\n" \
"s_add_u32 s44, %[wo], 4300800\nbuffer_load_dwordx4 v92, %[rw], s44 offen nt lds\nds_read_b64_tr_b16 a[80:81], v93 offset:4096\nds_read_b64_tr_b16 a[82:83], v93 offset:4608\nds_read_b64_tr_b16 a[88:89], v93 offset:4104\nds_read_b64_tr_b16 a[90:91], v93 offset:4616\nds_read_b64_tr_b16 a[96:97], v93 offset:16384\nds_read_b64_tr_b16 a[98:99], v93 offset:16896\n" \
"ds_read_b64_tr_b16 v[72:73], v93 offset:16392\nds_read_b64_tr_b16 v[74:75], v93 offset:16904\ns_waitcnt lgkmcnt(0)\nv_mfma_f32_16x16x16_bf16 v[8:11], a[80:81], v[6:7], v[8:11]\nv_mfma_f32_16x16x16_bf16 v[12:15], a[88:89], v[6:7], v[12:15]\nv_mfma_f32_16x16x16_bf16 v[16:19], a[96:97], v[6:7], v[16:19]\nv_mfma_f32_16x16x16_bf16 v[20:23], v[72:73], v[6:7], v[20:23]\nv_mfma_f32_16x16x16_bf16 v[8:11], a[82:83], v[6:7], v[8:11]\n" \
"v_mfma_f32_16x16x16_bf16 v[12:15], a[90:91], v[6:7], v[12:15]\nv_mfma_f32_16x16x16_bf16 v[16:19], a[98:99], v[6:7], v[16:19]\nv_mfma_f32_16x16x16_bf16 v[20:23], v[74:75], v[6:7], v[20:23]\ns_waitcnt vmcnt(28)\ns_barrier\ns_add_u32 m0, %[tb], 4096\ns_add_u32 s44, %[wo], 4358144\nbuffer_load_dwordx4 v92, %[rw], s44 offen nt lds\n" \
"s_add_u32 m0, %[tb], 5120\ns_add_u32 s44, %[wo], 4415488\nbuffer_load_dwordx4 v92, %[rw], s44 offen nt lds\ns_add_u32 m0, %[tb], 6144\ns_add_u32 s44, %[wo], 4472832\nbuffer_load_dwordx4 v92, %[rw], s44 offen nt lds\ns_add_u32 m0, %[tb], 7168\ns_add_u32 s44, %[wo], 4530176\n" \
"buffer_load_dwordx4 v92, %[rw], s44 offen nt lds\nds_read_b64_tr_b16 a[84:85], v93 offset:8192\nds_read_b64_tr_b16 a[86:87], v93 offset:8704\nds_read_b64_tr_b16 a[92:93], v93 offset:8200\nds_read_b64_tr_b16 a[94:95], v93 offset:8712\nds_read_b64_tr_b16 a[100:101], v93 offset:20480\nds_read_b64_tr_b16 a[102:103], v93 offset:20992\nds_read_b64_tr_b16 v[68:69], v93 offset:20488\n" \
"ds_read_b64_tr_b16 v[70:71], v93 offset:21000\ns_waitcnt lgkmcnt(0)\nv_mfma_f32_16x16x16_bf16 v[8:11], a[84:85], v[6:7], v[8:11]\nv_mfma_f32_16x16x16_bf16 v[12:15], a[92:93], v[6:7], v[12:15]\nv_mfma_f32_16x16x16_bf16 v[16:19], a[100:101], v[6:7], v[16:19]\nv_mfma_f32_16x16x16_bf16 v[20:23], v[68:69], v[6:7], v[20:23]\nv_mfma_f32_16x16x16_bf16 v[8:11], a[86:87], v[6:7], v[8:11]\nv_mfma_f32_16x16x16_bf16 v[12:15], a[94:95], v[6:7], v[12:15]\n" \
"v_mfma_f32_16x16x16_bf16 v[16:19], a[102:103], v[6:7], v[16:19]\nv_mfma_f32_16x16x16_bf16 v[20:23], v[70:71], v[6:7], v[20:23]\ns_waitcnt vmcnt(28)\ns_barrier\ns_add_u32 m0, %[tb], 8192\ns_add_u32 s44, %[wo], 4587520\nbuffer_load_dwordx4 v92, %[rw], s44 offen nt lds\ns_add_u32 m0, %[tb], 9216\n" \
"s_add_u32 s44, %[wo], 4644864\nbuffer_load_dwordx4 v92, %[rw], s44 offen nt lds\ns_add_u32 m0, %[tb], 10240\ns_add_u32 s44, %[wo], 4702208\nbuffer_load_dwordx4 v92, %[rw], s44 offen nt lds\ns_add_u32 m0, %[tb], 11264\ns_add_u32 s44, %[wo], 4759552\nbuffer_load_dwordx4 v92, %[rw], s44 offen nt lds\n" \
"ds_read_b64_tr_b16 a[136:137], v94 offset:0\nds_read_b64_tr_b16 a[138:139], v94 offset:512\nds_read_b64_tr_b16 v[64:65], v94 offset:8\nds_read_b64_tr_b16 v[66:67], v94 offset:520\nds_read_b64_tr_b16 a[144:145], v94 offset:24576\nds_read_b64_tr_b16 a[146:147], v94 offset:25088\nds_read_b64_tr_b16 a[152:153], v94 offset:24584\nds_read_b64_tr_b16 a[154:155], v94 offset:25096\n" \
"s_waitcnt lgkmcnt(0)\nv_mfma_f32_16x16x16_bf16 v[8:11], a[136:137], v[6:7], v[8:11]\nv_mfma_f32_16x16x16_bf16 v[12:15], v[64:65], v[6:7], v[12:15]\nv_mfma_f32_16x16x16_bf16 v[16:19], a[144:145], v[6:7], v[16:19]\nv_mfma_f32_16x16x16_bf16 v[20:23], a[152:153], v[6:7], v[20:23]\nv_mfma_f32_16x16x16_bf16 v[8:11], a[138:139], v[6:7], v[8:11]\nv_mfma_f32_16x16x16_bf16 v[12:15], v[66:67], v[6:7], v[12:15]\nv_mfma_f32_16x16x16_bf16 v[16:19], a[146:147], v[6:7], v[16:19]\n" \
"v_mfma_f32_16x16x16_bf16 v[20:23], a[154:155], v[6:7], v[20:23]\ns_waitcnt vmcnt(28)\ns_barrier\ns_add_u32 m0, %[lw], 0\ns_add_u32 s44, %[wo], 4816896\nbuffer_load_dwordx4 v92, %[rw], s44 offen nt lds\ns_add_u32 m0, %[lw], 1024\ns_add_u32 s44, %[wo], 4874240\n" \
"buffer_load_dwordx4 v92, %[rw], s44 offen nt lds\ns_add_u32 m0, %[lw], 2048\ns_add_u32 s44, %[wo], 4931584\nbuffer_load_dwordx4 v92, %[rw], s44 offen nt lds\ns_add_u32 m0, %[lw], 3072\ns_add_u32 s44, %[wo], 4988928\nbuffer_load_dwordx4 v92, %[rw], s44 offen nt lds\nds_read_b64_tr_b16 a[140:141], v94 offset:4096\n" \
"ds_read_b64_tr_b16 a[142:143], v94 offset:4608\nds_read_b64_tr_b16 v[60:61], v94 offset:4104\nds_read_b64_tr_b16 v[62:63], v94 offset:4616\nds_read_b64_tr_b16 a[148:149], v94 offset:28672\nds_read_b64_tr_b16 a[150:151], v94 offset:29184\nds_read_b64_tr_b16 a[156:157], v94 offset:28680\nds_read_b64_tr_b16 a[158:159], v94 offset:29192\ns_waitcnt lgkmcnt(0)\n" \
"v_mfma_f32_16x16x16_bf16 v[8:11], a[140:141], v[6:7], v[8:11]\nv_mfma_f32_16x16x16_bf16 v[12:15], v[60:61], v[6:7], v[12:15]\nv_mfma_f32_16x16x16_bf16 v[16:19], a[148:149], v[6:7], v[16:19]\nv_mfma_f32_16x16x16_bf16 v[20:23], a[156:157], v[6:7], v[20:23]\nv_mfma_f32_16x16x16_bf16 v[8:11], a[142:143], v[6:7], v[8:11]\nv_mfma_f32_16x16x16_bf16 v[12:15], v[62:63], v[6:7], v[12:15]\nv_mfma_f32_16x16x16_bf16 v[16:19], a[150:151], v[6:7], v[16:19]\nv_mfma_f32_16x16x16_bf16 v[20:23], a[158:159], v[6:7], v[20:23]\n" \
"s_waitcnt vmcnt(28)\ns_barrier\ns_add_u32 m0, %[lw], 4096\ns_add_u32 s44, %[wo], 5046272\nbuffer_load_dwordx4 v92, %[rw], s44 offen nt lds\ns_add_u32 m0, %[lw], 5120\ns_add_u32 s44, %[wo], 5103616\nbuffer_load_dwordx4 v92, %[rw], s44 offen nt lds\n" \
"s_add_u32 m0, %[lw], 6144\ns_add_u32 s44, %[wo], 5160960\nbuffer_load_dwordx4 v92, %[rw], s44 offen nt lds\ns_add_u32 m0, %[lw], 7168\ns_add_u32 s44, %[wo], 5218304\nbuffer_load_dwordx4 v92, %[rw], s44 offen nt lds\nds_read_b64_tr_b16 a[160:161], v94 offset:8192\nds_read_b64_tr_b16 a[162:163], v94 offset:8704\n" \
"ds_read_b64_tr_b16 a[168:169], v94 offset:8200\nds_read_b64_tr_b16 a[170:171], v94 offset:8712\nds_read_b64_tr_b16 v[56:57], v94 offset:32768\nds_read_b64_tr_b16 v[58:59], v94 offset:33280\nds_read_b64_tr_b16 a[176:177], v94 offset:32776\nds_read_b64_tr_b16 a[178:179], v94 offset:33288\ns_waitcnt lgkmcnt(0)\nv_mfma_f32_16x16x16_bf16 v[8:11], a[160:161], v[6:7], v[8:11]\n" \
"v_mfma_f32_16x16x16_bf16 v[12:15], a[168:169], v[6:7], v[12:15]\nv_mfma_f32_16x16x16_bf16 v[16:19], v[56:57], v[6:7], v[16:19]\nv_mfma_f32_16x16x16_bf16 v[20:23], a[176:177], v[6:7], v[20:23]\nv_mfma_f32_16x16x16_bf16 v[8:11], a[162:163], v[6:7], v[8:11]\nv_mfma_f32_16x16x16_bf16 v[12:15], a[170:171], v[6:7], v[12:15]\nv_mfma_f32_16x16x16_bf16 v[16:19], v[58:59], v[6:7], v[16:19]\nv_mfma_f32_16x16x16_bf16 v[20:23], a[178:179], v[6:7], v[20:23]\ns_waitcnt vmcnt(28)\n" \
"s_barrier\ns_add_u32 m0, %[lw], 8192\ns_add_u32 s44, %[wo], 5275648\nbuffer_load_dwordx4 v92, %[rw], s44 offen nt lds\ns_add_u32 m0, %[lw], 9216\ns_add_u32 s44, %[wo], 5332992\nbuffer_load_dwordx4 v92, %[rw], s44 offen nt lds\ns_add_u32 m0, %[lw], 10240\n" \
"s_add_u32 s44, %[wo], 5390336\nbuffer_load_dwordx4 v92, %[rw], s44 offen nt lds\ns_add_u32 m0, %[lw], 11264\ns_add_u32 s44, %[wo], 5447680\nbuffer_load_dwordx4 v92, %[rw], s44 offen nt lds\nds_read_b64_tr_b16 a[164:165], v94 offset:12288\nds_read_b64_tr_b16 a[166:167], v94 offset:12800\nds_read_b64_tr_b16 a[172:173], v94 offset:12296\n" \
"ds_read_b64_tr_b16 a[174:175], v94 offset:12808\nds_read_b64_tr_b16 v[52:53], v94 offset:36864\nds_read_b64_tr_b16 v[54:55], v94 offset:37376\nds_read_b64_tr_b16 a[180:181], v94 offset:36872\nds_read_b64_tr_b16 a[182:183], v94 offset:37384\ns_waitcnt lgkmcnt(0)\nv_mfma_f32_16x16x16_bf16 v[8:11], a[164:165], v[6:7], v[8:11]\nv_mfma_f32_16x16x16_bf16 v[12:15], a[172:173], v[6:7], v[12:15]\n" \
"v_mfma_f32_16x16x16_bf16 v[16:19], v[52:53], v[6:7], v[16:19]\nv_mfma_f32_16x16x16_bf16 v[20:23], a[180:181], v[6:7], v[20:23]\nv_mfma_f32_16x16x16_bf16 v[8:11], a[166:167], v[6:7], v[8:11]\nv_mfma_f32_16x16x16_bf16 v[12:15], a[174:175], v[6:7], v[12:15]\nv_mfma_f32_16x16x16_bf16 v[16:19], v[54:55], v[6:7], v[16:19]\nv_mfma_f32_16x16x16_bf16 v[20:23], a[182:183], v[6:7], v[20:23]\ns_waitcnt vmcnt(28)\ns_barrier\n" \
"s_add_u32 m0, %[lw], 12288\ns_add_u32 s44, %[wo], 5963776\nbuffer_load_dwordx4 v92, %[rw], s44 offen nt lds\ns_add_u32 m0, %[lw], 13312\ns_add_u32 s44, %[wo], 6021120\nbuffer_load_dwordx4 v92, %[rw], s44 offen nt lds\ns_add_u32 m0, %[lw], 14336\ns_add_u32 s44, %[wo], 6078464\n" \
"buffer_load_dwordx4 v92, %[rw], s44 offen nt lds\ns_add_u32 m0, %[lw], 15360\ns_add_u32 s44, %[wo], 6135808\nbuffer_load_dwordx4 v92, %[rw], s44 offen nt lds\nds_read_b64_tr_b16 a[184:185], v94 offset:16384\nds_read_b64_tr_b16 a[186:187], v94 offset:16896\nds_read_b64_tr_b16 a[192:193], v94 offset:16392\nds_read_b64_tr_b16 a[194:195], v94 offset:16904\n" \
"ds_read_b64_tr_b16 a[200:201], v94 offset:40960\nds_read_b64_tr_b16 a[202:203], v94 offset:41472\nds_read_b64_tr_b16 v[48:49], v94 offset:40968\nds_read_b64_tr_b16 v[50:51], v94 offset:41480\ns_waitcnt lgkmcnt(0)\nv_mfma_f32_16x16x16_bf16 v[8:11], a[184:185], v[6:7], v[8:11]\nv_mfma_f32_16x16x16_bf16 v[12:15], a[192:193], v[6:7], v[12:15]\nv_mfma_f32_16x16x16_bf16 v[16:19], a[200:201], v[6:7], v[16:19]\n" \
"v_mfma_f32_16x16x16_bf16 v[20:23], v[48:49], v[6:7], v[20:23]\nv_mfma_f32_16x16x16_bf16 v[8:11], a[186:187], v[6:7], v[8:11]\nv_mfma_f32_16x16x16_bf16 v[12:15], a[194:195], v[6:7], v[12:15]\nv_mfma_f32_16x16x16_bf16 v[16:19], a[202:203], v[6:7], v[16:19]\nv_mfma_f32_16x16x16_bf16 v[20:23], v[50:51], v[6:7], v[20:23]\ns_waitcnt vmcnt(28)\ns_barrier\ns_add_u32 m0, %[lw], 16384\n" \
"s_add_u32 s44, %[wo], 6193152\nbuffer_load_dwordx4 v92, %[rw], s44 offen nt lds\ns_add_u32 m0, %[lw], 17408\ns_add_u32 s44, %[wo], 6250496\nbuffer_load_dwordx4 v92, %[rw], s44 offen nt lds\ns_add_u32 m0, %[lw], 18432\ns_add_u32 s44, %[wo], 6307840\nbuffer_load_dwordx4 v92, %[rw], s44 offen nt lds\n" \
"s_add_u32 m0, %[lw], 19456\ns_add_u32 s44, %[wo], 6365184\nbuffer_load_dwordx4 v92, %[rw], s44 offen nt lds\nds_read_b64_tr_b16 a[188:189], v94 offset:20480\nds_read_b64_tr_b16 a[190:191], v94 offset:20992\nds_read_b64_tr_b16 a[196:197], v94 offset:20488\nds_read_b64_tr_b16 a[198:199], v94 offset:21000\nds_read_b64_tr_b16 a[204:205], v94 offset:45056\n" \
"ds_read_b64_tr_b16 a[206:207], v94 offset:45568\nds_read_b64_tr_b16 v[44:45], v94 offset:45064\nds_read_b64_tr_b16 v[46:47], v94 offset:45576\ns_waitcnt lgkmcnt(0)\nv_mfma_f32_16x16x16_bf16 v[8:11], a[188:189], v[6:7], v[8:11]\nv_mfma_f32_16x16x16_bf16 v[12:15], a[196:197], v[6:7], v[12:15]\nv_mfma_f32_16x16x16_bf16 v[16:19], a[204:205], v[6:7], v[16:19]\nv_mfma_f32_16x16x16_bf16 v[20:23], v[44:45], v[6:7], v[20:23]\n" \
"v_mfma_f32_16x16x16_bf16 v[8:11], a[190:191], v[6:7], v[8:11]\nv_mfma_f32_16x16x16_bf16 v[12:15], a[198:199], v[6:7], v[12:15]\nv_mfma_f32_16x16x16_bf16 v[16:19], a[206:207], v[6:7], v[16:19]\nv_mfma_f32_16x16x16_bf16 v[20:23], v[46:47], v[6:7], v[20:23]\ns_waitcnt vmcnt(28)\ns_barrier\ns_add_u32 m0, %[lw], 20480\ns_add_u32 s44, %[wo], 6422528\n" \
"buffer_load_dwordx4 v92, %[rw], s44 offen nt lds\ns_add_u32 m0, %[lw], 21504\ns_add_u32 s44, %[wo], 6479872\nbuffer_load_dwordx4 v92, %[rw], s44 offen nt lds\ns_add_u32 m0, %[lw], 22528\ns_add_u32 s44, %[wo], 6537216\nbuffer_load_dwordx4 v92, %[rw], s44 offen nt lds\ns_add_u32 m0, %[lw], 23552\n" \
"s_add_u32 s44, %[wo], 6594560\nbuffer_load_dwordx4 v92, %[rw], s44 offen nt lds\nds_read_b64_tr_b16 a[240:241], v93 offset:0\nds_read_b64_tr_b16 a[242:243], v93 offset:512\nds_read_b64_tr_b16 v[40:41], v93 offset:8\nds_read_b64_tr_b16 v[42:43], v93 offset:520\nds_read_b64_tr_b16 a[248:249], v93 offset:12288\nds_read_b64_tr_b16 a[250:251], v93 offset:12800\n" \
"ds_read_b64_tr_b16 v[96:97], v93 offset:12296\nds_read_b64_tr_b16 v[98:99], v93 offset:12808\ns_waitcnt lgkmcnt(0)\nv_mfma_f32_16x16x16_bf16 v[8:11], a[240:241], v[6:7], v[8:11]\nv_mfma_f32_16x16x16_bf16 v[12:15], v[40:41], v[6:7], v[12:15]\nv_mfma_f32_16x16x16_bf16 v[16:19], a[248:249], v[6:7], v[16:19]\nv_mfma_f32_16x16x16_bf16 v[20:23], v[96:97], v[6:7], v[20:23]\nv_mfma_f32_16x16x16_bf16 v[8:11], a[242:243], v[6:7], v[8:11]\n" \
"v_mfma_f32_16x16x16_bf16 v[12:15], v[42:43], v[6:7], v[12:15]\nv_mfma_f32_16x16x16_bf16 v[16:19], a[250:251], v[6:7], v[16:19]\nv_mfma_f32_16x16x16_bf16 v[20:23], v[98:99], v[6:7], v[20:23]\ns_waitcnt vmcnt(28)\ns_barrier\ns_add_u32 m0, %[tb], 0\ns_add_u32 s44, %[wo], 6651904\nbuffer_load_dwordx4 v92, %[rw], s44 offen nt lds\n" \
"s_add_u32 m0, %[tb], 1024\ns_add_u32 s44, %[wo], 6709248\nbuffer_load_dwordx4 v92, %[rw], s44 offen nt lds\ns_add_u32 m0, %[tb], 2048\ns_add_u32 s44, %[wo], 6766592\nbuffer_load_dwordx4 v92, %[rw], s44 offen nt lds\ns_add_u32 m0, %[tb], 3072\ns_add_u32 s44, %[wo], 6823936\n" \
"buffer_load_dwordx4 v92, %[rw], s44 offen nt lds\nds_read_b64_tr_b16 a[244:245], v93 offset:4096\nds_read_b64_tr_b16 a[246:247], v93 offset:4608\nds_read_b64_tr_b16 v[36:37], v93 offset:4104\nds_read_b64_tr_b16 v[38:39], v93 offset:4616\nds_read_b64_tr_b16 a[252:253], v93 offset:16384\nds_read_b64_tr_b16 a[254:255], v93 offset:16896\nds_read_b64_tr_b16 v[100:101], v93 offset:16392\n" \
"ds_read_b64_tr_b16 v[102:103], v93 offset:16904\ns_waitcnt lgkmcnt(0)\nv_mfma_f32_16x16x16_bf16 v[8:11], a[244:245], v[6:7], v[8:11]\nv_mfma_f32_16x16x16_bf16 v[12:15], v[36:37], v[6:7], v[12:15]\nv_mfma_f32_16x16x16_bf16 v[16:19], a[252:253], v[6:7], v[16:19]\nv_mfma_f32_16x16x16_bf16 v[20:23], v[100:101], v[6:7], v[20:23]\nv_mfma_f32_16x16x16_bf16 v[8:11], a[246:247], v[6:7], v[8:11]\nv_mfma_f32_16x16x16_bf16 v[12:15], v[38:39], v[6:7], v[12:15]\n" \
"v_mfma_f32_16x16x16_bf16 v[16:19], a[254:255], v[6:7], v[16:19]\nv_mfma_f32_16x16x16_bf16 v[20:23], v[102:103], v[6:7], v[20:23]\ns_waitcnt vmcnt(28)\ns_barrier\ns_add_u32 m0, %[tb], 4096\ns_add_u32 s44, %[wo], 6881280\nbuffer_load_dwordx4 v92, %[rw], s44 offen nt lds\ns_add_u32 m0, %[tb], 5120\n" \
"s_add_u32 s44, %[wo], 6938624\nbuffer_load_dwordx4 v92, %[rw], s44 offen nt lds\ns_add_u32 m0, %[tb], 6144\ns_add_u32 s44, %[wo], 6995968\nbuffer_load_dwordx4 v92, %[rw], s44 offen nt lds\ns_add_u32 m0, %[tb], 7168\ns_add_u32 s44, %[wo], 7053312\nbuffer_load_dwordx4 v92, %[rw], s44 offen nt lds\n" \
"ds_read_b64_tr_b16 v[104:105], v93 offset:8192\nds_read_b64_tr_b16 v[106:107], v93 offset:8704\nds_read_b64_tr_b16 v[112:113], v93 offset:8200\nds_read_b64_tr_b16 v[114:115], v93 offset:8712\nds_read_b64_tr_b16 v[32:33], v93 offset:20480\nds_read_b64_tr_b16 v[34:35], v93 offset:20992\nds_read_b64_tr_b16 v[120:121], v93 offset:20488\nds_read_b64_tr_b16 v[122:123], v93 offset:21000\n" \
"s_waitcnt lgkmcnt(0)\nv_mfma_f32_16x16x16_bf16 v[8:11], v[104:105], v[6:7], v[8:11]\nv_mfma_f32_16x16x16_bf16 v[12:15], v[112:113], v[6:7], v[12:15]\nv_mfma_f32_16x16x16_bf16 v[16:19], v[32:33], v[6:7], v[16:19]\nv_mfma_f32_16x16x16_bf16 v[20:23], v[120:121], v[6:7], v[20:23]\nv_mfma_f32_16x16x16_bf16 v[8:11], v[106:107], v[6:7], v[8:11]\nv_mfma_f32_16x16x16_bf16 v[12:15], v[114:115], v[6:7], v[12:15]\nv_mfma_f32_16x16x16_bf16 v[16:19], v[34:35], v[6:7], v[16:19]\n" \
"v_mfma_f32_16x16x16_bf16 v[20:23], v[122:123], v[6:7], v[20:23]\ns_waitcnt vmcnt(28)\ns_barrier\ns_add_u32 m0, %[tb], 8192\ns_add_u32 s44, %[wo], 7110656\nbuffer_load_dwordx4 v92, %[rw], s44 offen nt lds\ns_add_u32 m0, %[tb], 9216\ns_add_u32 s44, %[wo], 7168000\n" \
"buffer_load_dwordx4 v92, %[rw], s44 offen nt lds\ns_add_u32 m0, %[tb], 10240\ns_add_u32 s44, %[wo], 7225344\nbuffer_load_dwordx4 v92, %[rw], s44 offen nt lds\ns_add_u32 m0, %[tb], 11264\ns_add_u32 s44, %[wo], 7282688\nbuffer_load_dwordx4 v92, %[rw], s44 offen nt lds\nds_read_b64_tr_b16 v[108:109], v94 offset:0\n" \
"ds_read_b64_tr_b16 v[110:111], v94 offset:512\nds_read_b64_tr_b16 v[116:117], v94 offset:8\nds_read_b64_tr_b16 v[118:119], v94 offset:520\nds_read_b64_tr_b16 v[28:29], v94 offset:24576\nds_read_b64_tr_b16 v[30:31], v94 offset:25088\nds_read_b64_tr_b16 v[124:125], v94 offset:24584\nds_read_b64_tr_b16 v[126:127], v94 offset:25096\ns_waitcnt lgkmcnt(0)\n" \
"v_mfma_f32_16x16x16_bf16 v[8:11], v[108:109], v[6:7], v[8:11]\nv_mfma_f32_16x16x16_bf16 v[12:15], v[116:117], v[6:7], v[12:15]\nv_mfma_f32_16x16x16_bf16 v[16:19], v[28:29], v[6:7], v[16:19]\nv_mfma_f32_16x16x16_bf16 v[20:23], v[124:125], v[6:7], v[20:23]\nv_mfma_f32_16x16x16_bf16 v[8:11], v[110:111], v[6:7], v[8:11]\nv_mfma_f32_16x16x16_bf16 v[12:15], v[118:119], v[6:7], v[12:15]\nv_mfma_f32_16x16x16_bf16 v[16:19], v[30:31], v[6:7], v[16:19]\nv_mfma_f32_16x16x16_bf16 v[20:23], v[126:127], v[6:7], v[20:23]\n" \
"s_waitcnt vmcnt(28)\ns_barrier\nds_read_b64_tr_b16 v[128:129], v94 offset:4096\nds_read_b64_tr_b16 v[130:131], v94 offset:4608\nds_read_b64_tr_b16 v[136:137], v94 offset:4104\nds_read_b64_tr_b16 v[138:139], v94 offset:4616\nds_read_b64_tr_b16 v[144:145], v94 offset:28672\nds_read_b64_tr_b16 v[146:147], v94 offset:29184\n" \
"ds_read_b64_tr_b16 v[24:25], v94 offset:28680\nds_read_b64_tr_b16 v[26:27], v94 offset:29192\ns_waitcnt lgkmcnt(0)\nv_mfma_f32_16x16x16_bf16 v[8:11], v[128:129], v[6:7], v[8:11]\nv_mfma_f32_16x16x16_bf16 v[12:15], v[136:137], v[6:7], v[12:15]\nv_mfma_f32_16x16x16_bf16 v[16:19], v[144:145], v[6:7], v[16:19]\nv_mfma_f32_16x16x16_bf16 v[20:23], v[24:25], v[6:7], v[20:23]\nv_mfma_f32_16x16x16_bf16 v[8:11], v[130:131], v[6:7], v[8:11]\n" \
"v_mfma_f32_16x16x16_bf16 v[12:15], v[138:139], v[6:7], v[12:15]\nv_mfma_f32_16x16x16_bf16 v[16:19], v[146:147], v[6:7], v[16:19]\nv_mfma_f32_16x16x16_bf16 v[20:23], v[26:27], v[6:7], v[20:23]\ns_waitcnt vmcnt(24)\nds_write_b128 v1, v[88:91] offset:0\nds_write_b128 v1, v[84:87] offset:1024\nds_write_b128 v1, v[80:83] offset:2048\nds_write_b128 v1, v[76:79] offset:3072\n" \
"s_barrier\nds_read_b64_tr_b16 v[132:133], v94 offset:8192\nds_read_b64_tr_b16 v[134:135], v94 offset:8704\nds_read_b64_tr_b16 v[140:141], v94 offset:8200\nds_read_b64_tr_b16 v[142:143], v94 offset:8712\nds_read_b64_tr_b16 v[148:149], v94 offset:32768\nds_read_b64_tr_b16 v[150:151], v94 offset:33280\nds_read_b64_tr_b16 v[76:77], v94 offset:32776\n" \
"ds_read_b64_tr_b16 v[78:79], v94 offset:33288\ns_waitcnt lgkmcnt(0)\nv_mfma_f32_16x16x16_bf16 v[8:11], v[132:133], v[6:7], v[8:11]\nv_mfma_f32_16x16x16_bf16 v[12:15], v[140:141], v[6:7], v[12:15]\nv_mfma_f32_16x16x16_bf16 v[16:19], v[148:149], v[6:7], v[16:19]\nv_mfma_f32_16x16x16_bf16 v[20:23], v[76:77], v[6:7], v[20:23]\nv_mfma_f32_16x16x16_bf16 v[8:11], v[134:135], v[6:7], v[8:11]\nv_mfma_f32_16x16x16_bf16 v[12:15], v[142:143], v[6:7], v[12:15]\n" \
"v_mfma_f32_16x16x16_bf16 v[16:19], v[150:151], v[6:7], v[16:19]\nv_mfma_f32_16x16x16_bf16 v[20:23], v[78:79], v[6:7], v[20:23]\ns_waitcnt vmcnt(20)\nds_write_b128 v1, v[72:75] offset:4096\nds_write_b128 v1, v[68:71] offset:5120\nds_write_b128 v1, v[64:67] offset:6144\nds_write_b128 v1, v[60:63] offset:7168\ns_barrier\n" \
"ds_read_b64_tr_b16 v[184:185], v94 offset:12288\nds_read_b64_tr_b16 v[186:187], v94 offset:12800\nds_read_b64_tr_b16 v[60:61], v94 offset:12296\nds_read_b64_tr_b16 v[62:63], v94 offset:12808\nds_read_b64_tr_b16 v[192:193], v94 offset:36864\nds_read_b64_tr_b16 v[194:195], v94 offset:37376\nds_read_b64_tr_b16 v[200:201], v94 offset:36872\nds_read_b64_tr_b16 v[202:203], v94 offset:37384\n" \
"s_waitcnt lgkmcnt(0)\nv_mfma_f32_16x16x16_bf16 v[8:11], v[184:185], v[6:7], v[8:11]\nv_mfma_f32_16x16x16_bf16 v[12:15], v[60:61], v[6:7], v[12:15]\nv_mfma_f32_16x16x16_bf16 v[16:19], v[192:193], v[6:7], v[16:19]\nv_mfma_f32_16x16x16_bf16 v[20:23], v[200:201], v[6:7], v[20:23]\nv_mfma_f32_16x16x16_bf16 v[8:11], v[186:187], v[6:7], v[8:11]\nv_mfma_f32_16x16x16_bf16 v[12:15], v[62:63], v[6:7], v[12:15]\nv_mfma_f32_16x16x16_bf16 v[16:19], v[194:195], v[6:7], v[16:19]\n" \
"v_mfma_f32_16x16x16_bf16 v[20:23], v[202:203], v[6:7], v[20:23]\ns_waitcnt vmcnt(16)\nds_write_b128 v1, v[56:59] offset:8192\nds_write_b128 v1, v[52:55] offset:9216\nds_write_b128 v1, v[48:51] offset:10240\nds_write_b128 v1, v[44:47] offset:11264\ns_barrier\nds_read_b64_tr_b16 v[188:189], v94 offset:16384\n" \
"ds_read_b64_tr_b16 v[190:191], v94 offset:16896\nds_read_b64_tr_b16 v[44:45], v94 offset:16392\nds_read_b64_tr_b16 v[46:47], v94 offset:16904\nds_read_b64_tr_b16 v[196:197], v94 offset:40960\nds_read_b64_tr_b16 v[198:199], v94 offset:41472\nds_read_b64_tr_b16 v[204:205], v94 offset:40968\nds_read_b64_tr_b16 v[206:207], v94 offset:41480\ns_waitcnt lgkmcnt(0)\n" \
"v_mfma_f32_16x16x16_bf16 v[8:11], v[188:189], v[6:7], v[8:11]\nv_mfma_f32_16x16x16_bf16 v[12:15], v[44:45], v[6:7], v[12:15]\nv_mfma_f32_16x16x16_bf16 v[16:19], v[196:197], v[6:7], v[16:19]\nv_mfma_f32_16x16x16_bf16 v[20:23], v[204:205], v[6:7], v[20:23]\nv_mfma_f32_16x16x16_bf16 v[8:11], v[190:191], v[6:7], v[8:11]\nv_mfma_f32_16x16x16_bf16 v[12:15], v[46:47], v[6:7], v[12:15]\nv_mfma_f32_16x16x16_bf16 v[16:19], v[198:199], v[6:7], v[16:19]\nv_mfma_f32_16x16x16_bf16 v[20:23], v[206:207], v[6:7], v[20:23]\n" \
"s_waitcnt vmcnt(12)\nds_write_b128 v1, v[40:43] offset:12288\nds_write_b128 v1, v[36:39] offset:13312\nds_write_b128 v1, v[32:35] offset:14336\nds_write_b128 v1, v[28:31] offset:15360\ns_barrier\nds_read_b64_tr_b16 v[208:209], v94 offset:20480\nds_read_b64_tr_b16 v[210:211], v94 offset:20992\n" \
"ds_read_b64_tr_b16 v[216:217], v94 offset:20488\nds_read_b64_tr_b16 v[218:219], v94 offset:21000\nds_read_b64_tr_b16 v[28:29], v94 offset:45056\nds_read_b64_tr_b16 v[30:31], v94 offset:45568\nds_read_b64_tr_b16 v[224:225], v94 offset:45064\nds_read_b64_tr_b16 v[226:227], v94 offset:45576\ns_waitcnt lgkmcnt(0)\nv_mfma_f32_16x16x16_bf16 v[8:11], v[208:209], v[6:7], v[8:11]\n" \
"v_mfma_f32_16x16x16_bf16 v[12:15], v[216:217], v[6:7], v[12:15]\nv_mfma_f32_16x16x16_bf16 v[16:19], v[28:29], v[6:7], v[16:19]\nv_mfma_f32_16x16x16_bf16 v[20:23], v[224:225], v[6:7], v[20:23]\nv_mfma_f32_16x16x16_bf16 v[8:11], v[210:211], v[6:7], v[8:11]\nv_mfma_f32_16x16x16_bf16 v[12:15], v[218:219], v[6:7], v[12:15]\nv_mfma_f32_16x16x16_bf16 v[16:19], v[30:31], v[6:7], v[16:19]\nv_mfma_f32_16x16x16_bf16 v[20:23], v[226:227], v[6:7], v[20:23]\ns_waitcnt vmcnt(8)\n" \
"ds_write_b128 v1, v[24:27] offset:16384\nds_write_b128 v1, v[76:79] offset:17408\nds_write_b128 v1, v[60:63] offset:18432\nds_write_b128 v1, v[44:47] offset:19456\ns_barrier\nds_read_b64_tr_b16 v[212:213], v93 offset:0\nds_read_b64_tr_b16 v[214:215], v93 offset:512\nds_read_b64_tr_b16 v[220:221], v93 offset:8\n" \
"ds_read_b64_tr_b16 v[222:223], v93 offset:520\nds_read_b64_tr_b16 v[44:45], v93 offset:12288\nds_read_b64_tr_b16 v[46:47], v93 offset:12800\nds_read_b64_tr_b16 v[228:229], v93 offset:12296\nds_read_b64_tr_b16 v[230:231], v93 offset:12808\ns_waitcnt lgkmcnt(0)\nv_mfma_f32_16x16x16_bf16 v[8:11], v[212:213], v[6:7], v[8:11]\nv_mfma_f32_16x16x16_bf16 v[12:15], v[220:221], v[6:7], v[12:15]\n" \
"v_mfma_f32_16x16x16_bf16 v[16:19], v[44:45], v[6:7], v[16:19]\nv_mfma_f32_16x16x16_bf16 v[20:23], v[228:229], v[6:7], v[20:23]\nv_mfma_f32_16x16x16_bf16 v[8:11], v[214:215], v[6:7], v[8:11]\nv_mfma_f32_16x16x16_bf16 v[12:15], v[222:223], v[6:7], v[12:15]\nv_mfma_f32_16x16x16_bf16 v[16:19], v[46:47], v[6:7], v[16:19]\nv_mfma_f32_16x16x16_bf16 v[20:23], v[230:231], v[6:7], v[20:23]\ns_waitcnt vmcnt(4)\nds_write_b128 v1, v[28:31] offset:20480\n" \
"ds_write_b128 v1, v[44:47] offset:21504\ns_barrier\nds_read_b64_tr_b16 v[232:233], v93 offset:4096\nds_read_b64_tr_b16 v[234:235], v93 offset:4608\nds_read_b64_tr_b16 v[240:241], v93 offset:4104\nds_read_b64_tr_b16 v[242:243], v93 offset:4616\nds_read_b64_tr_b16 v[248:249], v93 offset:16384\nds_read_b64_tr_b16 v[250:251], v93 offset:16896\n" \
"ds_read_b64_tr_b16 v[44:45], v93 offset:16392\nds_read_b64_tr_b16 v[46:47], v93 offset:16904\ns_waitcnt lgkmcnt(0)\nv_mfma_f32_16x16x16_bf16 v[8:11], v[232:233], v[6:7], v[8:11]\nv_mfma_f32_16x16x16_bf16 v[12:15], v[240:241], v[6:7], v[12:15]\nv_mfma_f32_16x16x16_bf16 v[16:19], v[248:249], v[6:7], v[16:19]\nv_mfma_f32_16x16x16_bf16 v[20:23], v[44:45], v[6:7], v[20:23]\nv_mfma_f32_16x16x16_bf16 v[8:11], v[234:235], v[6:7], v[8:11]\n" \
"v_mfma_f32_16x16x16_bf16 v[12:15], v[242:243], v[6:7], v[12:15]\nv_mfma_f32_16x16x16_bf16 v[16:19], v[250:251], v[6:7], v[16:19]\nv_mfma_f32_16x16x16_bf16 v[20:23], v[46:47], v[6:7], v[20:23]\ns_waitcnt vmcnt(0)\nds_write_b128 v1, v[44:47] offset:22528\ns_barrier\nds_read_b64_tr_b16 v[236:237], v93 offset:8192\nds_read_b64_tr_b16 v[238:239], v93 offset:8704\n" \
"ds_read_b64_tr_b16 v[244:245], v93 offset:8200\nds_read_b64_tr_b16 v[246:247], v93 offset:8712\nds_read_b64_tr_b16 v[252:253], v93 offset:20480\nds_read_b64_tr_b16 v[254:255], v93 offset:20992\nds_read_b64_tr_b16 v[44:45], v93 offset:20488\nds_read_b64_tr_b16 v[46:47], v93 offset:21000\ns_waitcnt lgkmcnt(0)\nv_mfma_f32_16x16x16_bf16 v[8:11], v[236:237], v[6:7], v[8:11]\n" \
"v_mfma_f32_16x16x16_bf16 v[12:15], v[244:245], v[6:7], v[12:15]\nv_mfma_f32_16x16x16_bf16 v[16:19], v[252:253], v[6:7], v[16:19]\nv_mfma_f32_16x16x16_bf16 v[20:23], v[44:45], v[6:7], v[20:23]\nv_mfma_f32_16x16x16_bf16 v[8:11], v[238:239], v[6:7], v[8:11]\nv_mfma_f32_16x16x16_bf16 v[12:15], v[246:247], v[6:7], v[12:15]\nv_mfma_f32_16x16x16_bf16 v[16:19], v[254:255], v[6:7], v[16:19]\nv_mfma_f32_16x16x16_bf16 v[20:23], v[46:47], v[6:7], v[20:23]\nds_write_b128 v1, v[44:47] offset:23552\n" \
"s_waitcnt lgkmcnt(0)\ns_barrier\ns_mov_b32 s40, 0\ns_mov_b32 s43, 0\ns_lshl_b32 s57, %[w], 12\ns_mov_b32 s58, 0xbeeff558\ns_mov_b32 s59, 0x409d30fc\ns_mov_b32 s56, -2147483648\n" \
".Lgg_next%=:\ns_cmp_lg_u32 %[w], 0\ns_cbranch_scc1 .Lgg_w1%=\nbuffer_load_dword v56, v3, %[rs], 0 offen sc1\ns_waitcnt vmcnt(0)\nbuffer_store_dword v90, off, %[rs], s56 sc1\n.Lgg_poll%=:\nv_cmp_gt_i32 vcc, s40, v56\n" \
"s_cbranch_vccz .Lgg_go%=\ns_sleep 12\nbuffer_load_dword v56, v3, %[rs], 0 offen sc1\ns_waitcnt vmcnt(0)\ns_add_u32 s43, s43, 1\ns_cmp_lt_u32 s43, 262144\ns_cbranch_scc1 .Lgg_poll%=\ns_branch .Lgg_abort%=\n" \
".Lgg_w1%=:\ns_waitcnt vmcnt(0)\nbuffer_store_dword v90, off, %[rs], s56 sc1\n.Lgg_go%=:\ns_add_u32 s44, s40, 0\ns_lshl_b32 s44, s44, 17\ns_add_u32 s44, s44, %[xn]\ns_lshr_b32 s45, s44, 8\n" \
"s_and_b32 s45, s45, 0x2000\ns_xor_b32 s41, s44, s45\ns_add_u32 s61, s41, %[xw]\ns_barrier\ns_add_u32 m0, s57, 0\ns_add_u32 s60, s61, 0\nbuffer_load_dwordx4 v0, %[rs], s60 offen offset:0 sc1 lds\nbuffer_load_dwordx4 v0, %[rs], s60 offen offset:1024 sc1 lds\n" \
"buffer_load_dwordx4 v0, %[rs], s60 offen offset:2048 sc1 lds\nbuffer_load_dwordx4 v0, %[rs], s60 offen offset:3072 sc1 lds\ns_add_u32 m0, s57, 16384\ns_add_u32 s60, s61, 32768\nbuffer_load_dwordx4 v0, %[rs], s60 offen offset:0 sc1 lds\nbuffer_load_dwordx4 v0, %[rs], s60 offen offset:1024 sc1 lds\nbuffer_load_dwordx4 v0, %[rs], s60 offen offset:2048 sc1 lds\nbuffer_load_dwordx4 v0, %[rs], s60 offen offset:3072 sc1 lds\n" \
"buffer_load_dwordx4 v[40:43], v5, %[rs], s41 offen offset:0 sc1\nbuffer_load_dwordx4 v[44:47], v5, %[rs], s41 offen offset:256 sc1\ns_lshl_b32 s44, s40, 19\ns_add_u32 s44, s44, %[ho]\ns_lshr_b32 s45, s44, 8\ns_and_b32 s45, s45, 0x2000\ns_xor_b32 s55, s44, s45\ns_waitcnt vmcnt(6)\n" \
"s_barrier\nds_read_b128 v[56:59], v2 offset:0\nds_read_b128 v[60:63], v2 offset:2048\nds_read_b128 v[64:67], v2 offset:4096\ns_waitcnt lgkmcnt(2)\nv_mfma_f32_16x16x32_bf16 v[24:27], a[0:3], v[56:59], 0\nds_read_b128 v[68:71], v2 offset:6144\nds_read_b128 v[80:83], v1 offset:0\n" \
"v_mfma_f32_16x16x32_bf16 v[28:31], a[8:11], v[56:59], 0\nbuffer_load_dwordx4 v[48:51], v5, %[rs], s41 offen offset:512 sc1\nv_mfma_f32_16x16x32_bf16 v[32:35], a[16:19], v[56:59], 0\nv_mfma_f32_16x16x32_bf16 v[36:39], a[24:27], v[56:59], 0\ns_waitcnt lgkmcnt(3)\nds_read_b128 v[72:75], v1 offset:1024\nv_mfma_f32_16x16x32_bf16 v[24:27], a[4:7], v[60:63], v[24:27]\nds_read_b128 v[56:59], v2 offset:8192\n" \
"v_mfma_f32_16x16x32_bf16 v[28:31], a[12:15], v[60:63], v[28:31]\nbuffer_load_dwordx4 v[52:55], v5, %[rs], s41 offen offset:768 sc1\nv_mfma_f32_16x16x32_bf16 v[32:35], a[20:23], v[60:63], v[32:35]\nv_mfma_f32_16x16x32_bf16 v[36:39], a[28:31], v[60:63], v[36:39]\ns_waitcnt lgkmcnt(4)\nds_read_b128 v[76:79], v1 offset:2048\nv_mfma_f32_16x16x32_bf16 v[24:27], a[32:35], v[64:67], v[24:27]\ns_waitcnt lgkmcnt(3)\n" \
"ds_read_b128 v[60:63], v2 offset:10240\nv_mfma_f32_16x16x32_bf16 v[28:31], v[80:83], v[64:67], v[28:31]\ns_add_u32 m0, s57, 32768\ns_add_u32 s60, s61, 65536\nbuffer_load_dwordx4 v0, %[rs], s60 offen offset:0 sc1 lds\nv_mfma_f32_16x16x32_bf16 v[32:35], a[40:43], v[64:67], v[32:35]\nv_mfma_f32_16x16x32_bf16 v[36:39], a[48:51], v[64:67], v[36:39]\nds_read_b128 v[80:83], v1 offset:3072\n" \
"v_mfma_f32_16x16x32_bf16 v[24:27], a[36:39], v[68:71], v[24:27]\ns_waitcnt lgkmcnt(4)\nds_read_b128 v[64:67], v2 offset:12288\nv_mfma_f32_16x16x32_bf16 v[28:31], v[72:75], v[68:71], v[28:31]\nbuffer_load_dwordx4 v0, %[rs], s60 offen offset:1024 sc1 lds\nv_mfma_f32_16x16x32_bf16 v[32:35], a[44:47], v[68:71], v[32:35]\nv_mfma_f32_16x16x32_bf16 v[36:39], a[52:55], v[68:71], v[36:39]\ns_waitcnt lgkmcnt(4)\n" \
"ds_read_b128 v[72:75], v1 offset:4096\nv_mfma_f32_16x16x32_bf16 v[24:27], a[56:59], v[56:59], v[24:27]\nds_read_b128 v[68:71], v2 offset:14336\nv_mfma_f32_16x16x32_bf16 v[28:31], a[64:67], v[56:59], v[28:31]\ns_waitcnt lgkmcnt(5)\nbuffer_load_dwordx4 v0, %[rs], s60 offen offset:2048 sc1 lds\nv_mfma_f32_16x16x32_bf16 v[32:35], v[76:79], v[56:59], v[32:35]\nv_mfma_f32_16x16x32_bf16 v[36:39], a[72:75], v[56:59], v[36:39]\n" \
"s_waitcnt lgkmcnt(4)\nds_read_b128 v[76:79], v1 offset:5120\nv_mfma_f32_16x16x32_bf16 v[24:27], a[60:63], v[60:63], v[24:27]\ns_waitcnt vmcnt(7)\ns_barrier\nds_read_b128 v[56:59], v2 offset:16384\nv_mfma_f32_16x16x32_bf16 v[28:31], a[68:71], v[60:63], v[28:31]\ns_waitcnt lgkmcnt(5)\n" \
"buffer_load_dwordx4 v0, %[rs], s60 offen offset:3072 sc1 lds\nv_mfma_f32_16x16x32_bf16 v[32:35], v[80:83], v[60:63], v[32:35]\nv_mfma_f32_16x16x32_bf16 v[36:39], a[76:79], v[60:63], v[36:39]\ns_waitcnt lgkmcnt(4)\nv_mfma_f32_16x16x32_bf16 v[24:27], a[80:83], v[64:67], v[24:27]\nds_read_b128 v[60:63], v2 offset:18432\nv_mfma_f32_16x16x32_bf16 v[28:31], a[88:91], v[64:67], v[28:31]\ns_add_u32 m0, s57, 49152\n" \
"s_add_u32 s60, s61, 98304\nbuffer_load_dwordx4 v0, %[rs], s60 offen offset:0 sc1 lds\nv_mfma_f32_16x16x32_bf16 v[32:35], a[96:99], v[64:67], v[32:35]\ns_waitcnt lgkmcnt(4)\nv_mfma_f32_16x16x32_bf16 v[36:39], v[72:75], v[64:67], v[36:39]\ns_waitcnt lgkmcnt(3)\nv_mfma_f32_16x16x32_bf16 v[24:27], a[84:87], v[68:71], v[24:27]\nds_read_b128 v[64:67], v2 offset:20480\n" \
"v_mfma_f32_16x16x32_bf16 v[28:31], a[92:95], v[68:71], v[28:31]\nbuffer_load_dwordx4 v0, %[rs], s60 offen offset:1024 sc1 lds\nv_mfma_f32_16x16x32_bf16 v[32:35], a[100:103], v[68:71], v[32:35]\ns_waitcnt lgkmcnt(3)\nv_mfma_f32_16x16x32_bf16 v[36:39], v[76:79], v[68:71], v[36:39]\ns_waitcnt lgkmcnt(2)\nds_read_b128 v[76:79], v1 offset:6144\nv_mfma_f32_16x16x32_bf16 v[24:27], a[104:107], v[56:59], v[24:27]\n" \
"ds_read_b128 v[68:71], v2 offset:22528\nv_mfma_f32_16x16x32_bf16 v[28:31], a[112:115], v[56:59], v[28:31]\nbuffer_load_dwordx4 v0, %[rs], s60 offen offset:2048 sc1 lds\nv_mfma_f32_16x16x32_bf16 v[32:35], a[120:123], v[56:59], v[32:35]\nv_mfma_f32_16x16x32_bf16 v[36:39], a[128:131], v[56:59], v[36:39]\ns_waitcnt lgkmcnt(3)\nds_read_b128 v[80:83], v1 offset:7168\nv_mfma_f32_16x16x32_bf16 v[24:27], a[108:111], v[60:63], v[24:27]\n" \
"ds_read_b128 v[56:59], v2 offset:24576\nv_mfma_f32_16x16x32_bf16 v[28:31], a[116:119], v[60:63], v[28:31]\nbuffer_load_dwordx4 v0, %[rs], s60 offen offset:3072 sc1 lds\nv_mfma_f32_16x16x32_bf16 v[32:35], a[124:127], v[60:63], v[32:35]\nv_mfma_f32_16x16x32_bf16 v[36:39], a[132:135], v[60:63], v[36:39]\ns_waitcnt lgkmcnt(4)\nds_read_b128 v[72:75], v1 offset:8192\nv_mfma_f32_16x16x32_bf16 v[24:27], a[136:139], v[64:67], v[24:27]\n" \
"s_waitcnt vmcnt(8)\ns_waitcnt lgkmcnt(4)\nds_read_b128 v[60:63], v2 offset:26624\nv_add_f32 v84, v40, v44\nv_mfma_f32_16x16x32_bf16 v[28:31], v[76:79], v[64:67], v[28:31]\nv_add_f32 v85, v48, v52\nv_add_f32 v84, v84, v85\nv_mfma_f32_16x16x32_bf16 v[32:35], a[144:147], v[64:67], v[32:35]\n" \
"v_mov_b32 v85, v84\nv_mfma_f32_16x16x32_bf16 v[36:39], a[152:155], v[64:67], v[36:39]\ns_waitcnt lgkmcnt(4)\nds_read_b128 v[76:79], v1 offset:9216\nv_mfma_f32_16x16x32_bf16 v[24:27], a[140:143], v[68:71], v[24:27]\ns_waitcnt lgkmcnt(4)\nds_read_b128 v[64:67], v2 offset:28672\nv_mfma_f32_16x16x32_bf16 v[28:31], v[80:83], v[68:71], v[28:31]\n" \
"v_permlane16_swap_b32 v84, v85\nv_mfma_f32_16x16x32_bf16 v[32:35], a[148:151], v[68:71], v[32:35]\nv_add_f32 v84, v84, v85\nv_mov_b32 v85, v84\nv_mfma_f32_16x16x32_bf16 v[36:39], a[156:159], v[68:71], v[36:39]\ns_waitcnt lgkmcnt(4)\nds_read_b128 v[80:83], v1 offset:10240\nv_mfma_f32_16x16x32_bf16 v[24:27], a[160:163], v[56:59], v[24:27]\n" \
"ds_read_b128 v[68:71], v2 offset:30720\nv_mfma_f32_16x16x32_bf16 v[28:31], a[168:171], v[56:59], v[28:31]\ns_waitcnt lgkmcnt(5)\nv_permlane32_swap_b32 v84, v85\nv_mfma_f32_16x16x32_bf16 v[32:35], v[72:75], v[56:59], v[32:35]\nv_add_f32 v84, v84, v85\nv_mul_f32 v84, 0xbd800000, v84\nv_mfma_f32_16x16x32_bf16 v[36:39], a[176:179], v[56:59], v[36:39]\n" \
"s_waitcnt lgkmcnt(4)\nds_read_b128 v[72:75], v1 offset:11264\nv_add_f32 v40, v40, v84\nv_mfma_f32_16x16x32_bf16 v[24:27], a[164:167], v[60:63], v[24:27]\ns_waitcnt vmcnt(4)\ns_barrier\nds_read_b128 v[56:59], v2 offset:32768\nv_mfma_f32_16x16x32_bf16 v[28:31], a[172:175], v[60:63], v[28:31]\n" \
"s_waitcnt lgkmcnt(5)\nv_mul_f32 v40, 0x41000000, v40\nv_fma_f32 v41, v40, v40, v41\nv_mfma_f32_16x16x32_bf16 v[32:35], v[76:79], v[60:63], v[32:35]\nv_add_f32 v44, v44, v84\nv_mul_f32 v44, 0x41000000, v44\nv_mfma_f32_16x16x32_bf16 v[36:39], a[180:183], v[60:63], v[36:39]\ns_waitcnt lgkmcnt(4)\n" \
"v_fma_f32 v45, v44, v44, v45\nv_add_f32 v48, v48, v84\nv_mfma_f32_16x16x32_bf16 v[24:27], a[184:187], v[64:67], v[24:27]\nds_read_b128 v[60:63], v2 offset:34816\nv_mul_f32 v48, 0x41000000, v48\nv_mfma_f32_16x16x32_bf16 v[28:31], a[192:195], v[64:67], v[28:31]\nv_fma_f32 v49, v48, v48, v49\nv_add_f32 v52, v52, v84\n" \
"v_mfma_f32_16x16x32_bf16 v[32:35], a[200:203], v[64:67], v[32:35]\ns_waitcnt lgkmcnt(4)\nv_mul_f32 v52, 0x41000000, v52\nv_fma_f32 v53, v52, v52, v53\nv_mfma_f32_16x16x32_bf16 v[36:39], v[80:83], v[64:67], v[36:39]\ns_waitcnt lgkmcnt(3)\nv_add_f32 v85, v41, v45\nv_add_f32 v86, v49, v53\n" \
"v_mfma_f32_16x16x32_bf16 v[24:27], a[188:191], v[68:71], v[24:27]\nds_read_b128 v[64:67], v2 offset:36864\nv_add_f32 v85, v85, v86\nv_mfma_f32_16x16x32_bf16 v[28:31], a[196:199], v[68:71], v[28:31]\nv_mov_b32 v86, v85\nv_mfma_f32_16x16x32_bf16 v[32:35], a[204:207], v[68:71], v[32:35]\ns_waitcnt lgkmcnt(3)\nv_permlane16_swap_b32 v85, v86\n" \
"v_mfma_f32_16x16x32_bf16 v[36:39], v[72:75], v[68:71], v[36:39]\ns_waitcnt lgkmcnt(2)\nds_read_b128 v[72:75], v1 offset:12288\nv_add_f32 v85, v85, v86\nv_mfma_f32_16x16x32_bf16 v[24:27], a[208:211], v[56:59], v[24:27]\nds_read_b128 v[68:71], v2 offset:38912\nv_mov_b32 v86, v85\nv_mfma_f32_16x16x32_bf16 v[28:31], a[216:219], v[56:59], v[28:31]\n" \
"s_nop 0\nv_permlane32_swap_b32 v85, v86\nv_mfma_f32_16x16x32_bf16 v[32:35], a[224:227], v[56:59], v[32:35]\nv_add_f32 v85, v85, v86\nv_mul_f32 v85, 0x3a800000, v85\nv_mfma_f32_16x16x32_bf16 v[36:39], a[232:235], v[56:59], v[36:39]\ns_waitcnt lgkmcnt(3)\nds_read_b128 v[76:79], v1 offset:13312\n" \
"v_cvt_pk_bf16_f32 v85, v85, v85\nv_mfma_f32_16x16x32_bf16 v[24:27], a[212:215], v[60:63], v[24:27]\nds_read_b128 v[56:59], v2 offset:40960\nv_and_b32 v85, 0xffff0000, v85\nv_mfma_f32_16x16x32_bf16 v[28:31], a[220:223], v[60:63], v[28:31]\nv_add_f32 v85, 0x3727c5ac, v85\nv_cvt_pk_bf16_f32 v85, v85, v85\nv_mfma_f32_16x16x32_bf16 v[32:35], a[228:231], v[60:63], v[32:35]\n" \
"v_and_b32 v85, 0xffff0000, v85\nv_cvt_pk_bf16_f32 v84, v84, v84\nv_mfma_f32_16x16x32_bf16 v[36:39], a[236:239], v[60:63], v[36:39]\ns_waitcnt lgkmcnt(4)\nds_read_b128 v[80:83], v1 offset:14336\nv_and_b32 v84, 0xffff0000, v84\nv_mfma_f32_16x16x32_bf16 v[24:27], a[240:243], v[64:67], v[24:27]\ns_waitcnt lgkmcnt(4)\n" \
"ds_read_b128 v[60:63], v2 offset:43008\nv_mfma_f32_16x16x32_bf16 v[28:31], v[72:75], v[64:67], v[28:31]\nv_sqrt_f32 v85, v85\nv_mfma_f32_16x16x32_bf16 v[32:35], a[248:251], v[64:67], v[32:35]\nv_cvt_pk_bf16_f32 v85, v85, v85\nv_and_b32 v85, 0xffff0000, v85\nv_mfma_f32_16x16x32_bf16 v[36:39], v[96:99], v[64:67], v[36:39]\ns_waitcnt lgkmcnt(4)\n" \
"ds_read_b128 v[72:75], v1 offset:15360\nv_mul_f32 v86, s58, v85\nv_mfma_f32_16x16x32_bf16 v[24:27], a[244:247], v[68:71], v[24:27]\ns_waitcnt lgkmcnt(4)\nds_read_b128 v[64:67], v2 offset:45056\nv_add_f32 v87, v42, v46\nv_mfma_f32_16x16x32_bf16 v[28:31], v[76:79], v[68:71], v[28:31]\nv_rcp_f32 v85, v85\n" \
"v_mfma_f32_16x16x32_bf16 v[32:35], a[252:255], v[68:71], v[32:35]\nv_mul_f32 v85, s58, v85\nv_add_f32 v88, v50, v54\nv_mfma_f32_16x16x32_bf16 v[36:39], v[100:103], v[68:71], v[36:39]\ns_waitcnt lgkmcnt(4)\nds_read_b128 v[76:79], v1 offset:16384\nv_add_f32 v87, v87, v88\nv_mfma_f32_16x16x32_bf16 v[24:27], v[104:107], v[56:59], v[24:27]\n" \
"ds_read_b128 v[68:71], v2 offset:47104\nv_mov_b32 v88, v87\nv_mfma_f32_16x16x32_bf16 v[28:31], v[112:115], v[56:59], v[28:31]\ns_waitcnt lgkmcnt(5)\nv_permlane16_swap_b32 v87, v88\nv_mfma_f32_16x16x32_bf16 v[32:35], v[80:83], v[56:59], v[32:35]\nv_add_f32 v87, v87, v88\nv_mov_b32 v88, v87\n" \
"v_mfma_f32_16x16x32_bf16 v[36:39], v[120:123], v[56:59], v[36:39]\ns_waitcnt lgkmcnt(4)\nds_read_b128 v[80:83], v1 offset:17408\nv_mfma_f32_16x16x32_bf16 v[24:27], v[108:111], v[60:63], v[24:27]\ns_waitcnt vmcnt(0)\ns_barrier\nds_read_b128 v[56:59], v2 offset:49152\nv_mfma_f32_16x16x32_bf16 v[28:31], v[116:119], v[60:63], v[28:31]\n" \
"s_waitcnt lgkmcnt(5)\nv_permlane32_swap_b32 v87, v88\nv_mfma_f32_16x16x32_bf16 v[32:35], v[72:75], v[60:63], v[32:35]\nv_add_f32 v87, v87, v88\nv_mul_f32 v87, 0xbd800000, v87\nv_mfma_f32_16x16x32_bf16 v[36:39], v[124:127], v[60:63], v[36:39]\ns_waitcnt lgkmcnt(4)\nv_add_f32 v42, v42, v87\n" \
"v_mul_f32 v42, 0x41000000, v42\nv_mfma_f32_16x16x32_bf16 v[24:27], v[128:131], v[64:67], v[24:27]\nds_read_b128 v[60:63], v2 offset:51200\nv_fma_f32 v43, v42, v42, v43\nv_mfma_f32_16x16x32_bf16 v[28:31], v[136:139], v[64:67], v[28:31]\nv_add_f32 v46, v46, v87\nv_mul_f32 v46, 0x41000000, v46\nv_mfma_f32_16x16x32_bf16 v[32:35], v[144:147], v[64:67], v[32:35]\n" \
"s_waitcnt lgkmcnt(4)\nv_fma_f32 v47, v46, v46, v47\nv_add_f32 v50, v50, v87\nv_mfma_f32_16x16x32_bf16 v[36:39], v[76:79], v[64:67], v[36:39]\ns_waitcnt lgkmcnt(3)\nv_mul_f32 v50, 0x41000000, v50\nv_fma_f32 v51, v50, v50, v51\nv_mfma_f32_16x16x32_bf16 v[24:27], v[132:135], v[68:71], v[24:27]\n" \
"ds_read_b128 v[64:67], v2 offset:53248\nv_add_f32 v54, v54, v87\nv_mfma_f32_16x16x32_bf16 v[28:31], v[140:143], v[68:71], v[28:31]\nv_mul_f32 v54, 0x41000000, v54\nv_fma_f32 v55, v54, v54, v55\nv_mfma_f32_16x16x32_bf16 v[32:35], v[148:151], v[68:71], v[32:35]\ns_waitcnt lgkmcnt(3)\nv_add_f32 v88, v43, v47\n" \
"v_add_f32 v89, v51, v55\nv_mfma_f32_16x16x32_bf16 v[36:39], v[80:83], v[68:71], v[36:39]\ns_waitcnt lgkmcnt(2)\nds_read_b128 v[80:83], v1 offset:18432\nv_add_f32 v88, v88, v89\nv_mfma_f32_16x16x32_bf16 v[24:27], v[152:155], v[56:59], v[24:27]\nds_read_b128 v[68:71], v2 offset:55296\nv_mov_b32 v89, v88\n" \
"v_mfma_f32_16x16x32_bf16 v[28:31], v[160:163], v[56:59], v[28:31]\ns_nop 0\nv_permlane16_swap_b32 v88, v89\nv_mfma_f32_16x16x32_bf16 v[32:35], v[168:171], v[56:59], v[32:35]\nv_add_f32 v88, v88, v89\nv_mov_b32 v89, v88\nv_mfma_f32_16x16x32_bf16 v[36:39], v[176:179], v[56:59], v[36:39]\ns_waitcnt lgkmcnt(3)\n" \
"ds_read_b128 v[72:75], v1 offset:19456\nv_mfma_f32_16x16x32_bf16 v[24:27], v[156:159], v[60:63], v[24:27]\nds_read_b128 v[56:59], v2 offset:57344\nv_mfma_f32_16x16x32_bf16 v[28:31], v[164:167], v[60:63], v[28:31]\nv_permlane32_swap_b32 v88, v89\nv_mfma_f32_16x16x32_bf16 v[32:35], v[172:175], v[60:63], v[32:35]\nv_add_f32 v88, v88, v89\nv_mul_f32 v88, 0x3a800000, v88\n" \
"v_mfma_f32_16x16x32_bf16 v[36:39], v[180:183], v[60:63], v[36:39]\ns_waitcnt lgkmcnt(4)\nds_read_b128 v[76:79], v1 offset:20480\nv_cvt_pk_bf16_f32 v88, v88, v88\nv_mfma_f32_16x16x32_bf16 v[24:27], v[184:187], v[64:67], v[24:27]\ns_waitcnt lgkmcnt(4)\nds_read_b128 v[60:63], v2 offset:59392\nv_and_b32 v88, 0xffff0000, v88\n" \
"v_mfma_f32_16x16x32_bf16 v[28:31], v[80:83], v[64:67], v[28:31]\nv_add_f32 v88, 0x3727c5ac, v88\nv_cvt_pk_bf16_f32 v88, v88, v88\nv_mfma_f32_16x16x32_bf16 v[32:35], v[192:195], v[64:67], v[32:35]\nv_and_b32 v88, 0xffff0000, v88\nv_cvt_pk_bf16_f32 v87, v87, v87\nv_mfma_f32_16x16x32_bf16 v[36:39], v[200:203], v[64:67], v[36:39]\ns_waitcnt lgkmcnt(4)\n" \
"ds_read_b128 v[80:83], v1 offset:21504\nv_and_b32 v87, 0xffff0000, v87\nv_mfma_f32_16x16x32_bf16 v[24:27], v[188:191], v[68:71], v[24:27]\ns_waitcnt lgkmcnt(4)\nds_read_b128 v[64:67], v2 offset:61440\nv_mfma_f32_16x16x32_bf16 v[28:31], v[72:75], v[68:71], v[28:31]\nv_sqrt_f32 v88, v88\nv_mfma_f32_16x16x32_bf16 v[32:35], v[196:199], v[68:71], v[32:35]\n" \
"v_cvt_pk_bf16_f32 v88, v88, v88\nv_and_b32 v88, 0xffff0000, v88\nv_mfma_f32_16x16x32_bf16 v[36:39], v[204:207], v[68:71], v[36:39]\ns_waitcnt lgkmcnt(4)\nds_read_b128 v[72:75], v1 offset:22528\nv_mul_f32 v89, s58, v88\nv_mfma_f32_16x16x32_bf16 v[24:27], v[208:211], v[56:59], v[24:27]\nds_read_b128 v[68:71], v2 offset:63488\n" \
"v_mfma_f32_16x16x32_bf16 v[28:31], v[216:219], v[56:59], v[28:31]\ns_waitcnt lgkmcnt(5)\nv_rcp_f32 v88, v88\nv_mfma_f32_16x16x32_bf16 v[32:35], v[76:79], v[56:59], v[32:35]\nv_mul_f32 v88, s58, v88\nv_mfma_f32_16x16x32_bf16 v[36:39], v[224:227], v[56:59], v[36:39]\ns_waitcnt lgkmcnt(4)\nds_read_b128 v[76:79], v1 offset:23552\n" \
"v_mfma_f32_16x16x32_bf16 v[24:27], v[212:215], v[60:63], v[24:27]\nds_read_b128 v[56:59], v2 offset:256\nv_mfma_f32_16x16x32_bf16 v[28:31], v[220:223], v[60:63], v[28:31]\ns_waitcnt lgkmcnt(5)\nv_mfma_f32_16x16x32_bf16 v[32:35], v[80:83], v[60:63], v[32:35]\nv_mfma_f32_16x16x32_bf16 v[36:39], v[228:231], v[60:63], v[36:39]\ns_waitcnt lgkmcnt(4)\nv_mfma_f32_16x16x32_bf16 v[24:27], v[232:235], v[64:67], v[24:27]\n" \
"ds_read_b128 v[60:63], v2 offset:2304\nv_mfma_f32_16x16x32_bf16 v[28:31], v[240:243], v[64:67], v[28:31]\nv_mfma_f32_16x16x32_bf16 v[32:35], v[248:251], v[64:67], v[32:35]\ns_waitcnt lgkmcnt(4)\nv_mfma_f32_16x16x32_bf16 v[36:39], v[72:75], v[64:67], v[36:39]\ns_waitcnt lgkmcnt(3)\nv_mfma_f32_16x16x32_bf16 v[24:27], v[236:239], v[68:71], v[24:27]\nds_read_b128 v[64:67], v2 offset:4352\n" \
"v_mfma_f32_16x16x32_bf16 v[28:31], v[244:247], v[68:71], v[28:31]\nv_mfma_f32_16x16x32_bf16 v[32:35], v[252:255], v[68:71], v[32:35]\ns_waitcnt lgkmcnt(3)\nv_mfma_f32_16x16x32_bf16 v[36:39], v[76:79], v[68:71], v[36:39]\ns_waitcnt lgkmcnt(2)\nds_read_b128 v[76:79], v1 offset:0\nv_mfma_f32_16x16x32_bf16 v[40:43], a[0:3], v[56:59], 0\nds_read_b128 v[68:71], v2 offset:6400\n" \
"v_mfma_f32_16x16x32_bf16 v[44:47], a[8:11], v[56:59], 0\ns_lshl_b32 s44, s40, 1\nv_mov_b32 v90, s44\nv_mfma_f32_16x16x32_bf16 v[48:51], a[16:19], v[56:59], 0\nv_mfma_f32_16x16x32_bf16 v[52:55], a[24:27], v[56:59], 0\ns_waitcnt lgkmcnt(3)\nds_read_b128 v[80:83], v1 offset:1024\nv_fma_f32 v24, v8, v84, v24\n" \
"v_mfma_f32_16x16x32_bf16 v[40:43], a[4:7], v[60:63], v[40:43]\nds_read_b128 v[56:59], v2 offset:8448\nv_fma_f32 v25, v9, v84, v25\nv_mfma_f32_16x16x32_bf16 v[44:47], a[12:15], v[60:63], v[44:47]\nv_fma_f32 v32, v16, v84, v32\nv_fma_f32 v33, v17, v84, v33\nv_mfma_f32_16x16x32_bf16 v[48:51], a[20:23], v[60:63], v[48:51]\nv_mul_f32 v24, v24, v85\n" \
"v_mul_f32 v25, v25, v85\nv_mfma_f32_16x16x32_bf16 v[52:55], a[28:31], v[60:63], v[52:55]\ns_waitcnt lgkmcnt(4)\nds_read_b128 v[72:75], v1 offset:2048\nv_fma_f32 v91, v24, v24, s59\nv_mfma_f32_16x16x32_bf16 v[40:43], a[32:35], v[64:67], v[40:43]\ns_waitcnt lgkmcnt(4)\nds_read_b128 v[60:63], v2 offset:10496\n" \
"v_fma_f32 v92, v25, v25, s59\nv_mfma_f32_16x16x32_bf16 v[44:47], v[76:79], v[64:67], v[44:47]\nv_mul_f32 v91, v91, v24\nv_mul_f32 v92, v92, v25\nv_mfma_f32_16x16x32_bf16 v[48:51], a[40:43], v[64:67], v[48:51]\nv_exp_f32 v91, v91\nv_mfma_f32_16x16x32_bf16 v[52:55], a[48:51], v[64:67], v[52:55]\ns_waitcnt lgkmcnt(4)\n" \
"ds_read_b128 v[76:79], v1 offset:3072\nv_mul_f32 v24, v24, v32\nv_mfma_f32_16x16x32_bf16 v[40:43], a[36:39], v[68:71], v[40:43]\ns_waitcnt lgkmcnt(4)\nds_read_b128 v[64:67], v2 offset:12544\nv_mul_f32 v25, v25, v33\nv_mfma_f32_16x16x32_bf16 v[44:47], v[80:83], v[68:71], v[44:47]\nv_exp_f32 v92, v92\n" \
"v_mfma_f32_16x16x32_bf16 v[48:51], a[44:47], v[68:71], v[48:51]\nv_fma_f32 v91, v91, v86, v86\nv_fma_f32 v92, v92, v86, v86\nv_mfma_f32_16x16x32_bf16 v[52:55], a[52:55], v[68:71], v[52:55]\ns_waitcnt lgkmcnt(4)\nds_read_b128 v[80:83], v1 offset:4096\nv_fma_f32 v26, v10, v84, v26\nv_mfma_f32_16x16x32_bf16 v[40:43], a[56:59], v[56:59], v[40:43]\n" \
"ds_read_b128 v[68:71], v2 offset:14592\nv_fma_f32 v27, v11, v84, v27\nv_mfma_f32_16x16x32_bf16 v[44:47], a[64:67], v[56:59], v[44:47]\ns_waitcnt lgkmcnt(5)\nv_rcp_f32 v91, v91\nv_mfma_f32_16x16x32_bf16 v[48:51], v[72:75], v[56:59], v[48:51]\nv_rcp_f32 v92, v92\nv_mfma_f32_16x16x32_bf16 v[52:55], a[72:75], v[56:59], v[52:55]\n" \
"s_waitcnt lgkmcnt(4)\nds_read_b128 v[72:75], v1 offset:5120\nv_mul_f32 v24, v24, v91\nv_mfma_f32_16x16x32_bf16 v[40:43], a[60:63], v[60:63], v[40:43]\nds_read_b128 v[56:59], v2 offset:16640\nv_mul_f32 v25, v25, v92\nv_mfma_f32_16x16x32_bf16 v[44:47], a[68:71], v[60:63], v[44:47]\ns_waitcnt lgkmcnt(5)\n" \
"v_cvt_pk_bf16_f32 v32, v24, v25\nv_fma_f32 v34, v18, v84, v34\nv_mfma_f32_16x16x32_bf16 v[48:51], v[76:79], v[60:63], v[48:51]\nv_fma_f32 v35, v19, v84, v35\nv_mul_f32 v26, v26, v85\nv_mfma_f32_16x16x32_bf16 v[52:55], a[76:79], v[60:63], v[52:55]\ns_waitcnt lgkmcnt(4)\nv_mul_f32 v27, v27, v85\n" \
"v_fma_f32 v91, v26, v26, s59\nv_mfma_f32_16x16x32_bf16 v[40:43], a[80:83], v[64:67], v[40:43]\nds_read_b128 v[60:63], v2 offset:18688\nv_fma_f32 v92, v27, v27, s59\nv_mfma_f32_16x16x32_bf16 v[44:47], a[88:91], v[64:67], v[44:47]\nv_mul_f32 v91, v91, v26\nv_mul_f32 v92, v92, v27\nv_mfma_f32_16x16x32_bf16 v[48:51], a[96:99], v[64:67], v[48:51]\n" \
"s_waitcnt lgkmcnt(4)\nv_exp_f32 v91, v91\nv_mfma_f32_16x16x32_bf16 v[52:55], v[80:83], v[64:67], v[52:55]\ns_waitcnt lgkmcnt(3)\nv_exp_f32 v92, v92\nv_mfma_f32_16x16x32_bf16 v[40:43], a[84:87], v[68:71], v[40:43]\nds_read_b128 v[64:67], v2 offset:20736\nv_mul_f32 v26, v26, v34\n" \
"v_mfma_f32_16x16x32_bf16 v[44:47], a[92:95], v[68:71], v[44:47]\nv_mul_f32 v27, v27, v35\nv_fma_f32 v91, v91, v86, v86\nv_mfma_f32_16x16x32_bf16 v[48:51], a[100:103], v[68:71], v[48:51]\ns_waitcnt lgkmcnt(3)\nv_fma_f32 v92, v92, v86, v86\nv_fma_f32 v28, v12, v84, v28\nv_mfma_f32_16x16x32_bf16 v[52:55], v[72:75], v[68:71], v[52:55]\n" \
"s_waitcnt lgkmcnt(2)\nds_read_b128 v[72:75], v1 offset:6144\nv_fma_f32 v29, v13, v84, v29\nv_mfma_f32_16x16x32_bf16 v[40:43], a[104:107], v[56:59], v[40:43]\nds_read_b128 v[68:71], v2 offset:22784\nv_fma_f32 v36, v20, v84, v36\nv_mfma_f32_16x16x32_bf16 v[44:47], a[112:115], v[56:59], v[44:47]\nv_rcp_f32 v91, v91\n" \
"v_mfma_f32_16x16x32_bf16 v[48:51], a[120:123], v[56:59], v[48:51]\nv_rcp_f32 v92, v92\nv_mfma_f32_16x16x32_bf16 v[52:55], a[128:131], v[56:59], v[52:55]\ns_waitcnt lgkmcnt(3)\nds_read_b128 v[76:79], v1 offset:7168\nv_mul_f32 v26, v26, v91\nv_mfma_f32_16x16x32_bf16 v[40:43], a[108:111], v[60:63], v[40:43]\nds_read_b128 v[56:59], v2 offset:24832\n" \
"v_mul_f32 v27, v27, v92\nv_mfma_f32_16x16x32_bf16 v[44:47], a[116:119], v[60:63], v[44:47]\nv_cvt_pk_bf16_f32 v33, v26, v27\nv_fma_f32 v37, v21, v84, v37\nv_mfma_f32_16x16x32_bf16 v[48:51], a[124:127], v[60:63], v[48:51]\nv_mul_f32 v28, v28, v85\nv_mul_f32 v29, v29, v85\nv_mfma_f32_16x16x32_bf16 v[52:55], a[132:135], v[60:63], v[52:55]\n" \
"s_waitcnt lgkmcnt(4)\nds_read_b128 v[80:83], v1 offset:8192\nv_fma_f32 v91, v28, v28, s59\nv_mfma_f32_16x16x32_bf16 v[40:43], a[136:139], v[64:67], v[40:43]\ns_waitcnt lgkmcnt(4)\nds_read_b128 v[60:63], v2 offset:26880\nv_fma_f32 v92, v29, v29, s59\nv_mfma_f32_16x16x32_bf16 v[44:47], v[72:75], v[64:67], v[44:47]\n" \
"v_mul_f32 v91, v91, v28\nv_mul_f32 v92, v92, v29\nv_mfma_f32_16x16x32_bf16 v[48:51], a[144:147], v[64:67], v[48:51]\nv_exp_f32 v91, v91\nv_mfma_f32_16x16x32_bf16 v[52:55], a[152:155], v[64:67], v[52:55]\ns_waitcnt lgkmcnt(4)\nds_read_b128 v[72:75], v1 offset:9216\nv_mul_f32 v28, v28, v36\n" \
"v_mfma_f32_16x16x32_bf16 v[40:43], a[140:143], v[68:71], v[40:43]\ns_waitcnt lgkmcnt(4)\nds_read_b128 v[64:67], v2 offset:28928\nv_mul_f32 v29, v29, v37\nv_mfma_f32_16x16x32_bf16 v[44:47], v[76:79], v[68:71], v[44:47]\nv_exp_f32 v92, v92\nv_mfma_f32_16x16x32_bf16 v[48:51], a[148:151], v[68:71], v[48:51]\nv_fma_f32 v91, v91, v86, v86\n" \
"v_fma_f32 v92, v92, v86, v86\nv_mfma_f32_16x16x32_bf16 v[52:55], a[156:159], v[68:71], v[52:55]\ns_waitcnt lgkmcnt(4)\nds_read_b128 v[76:79], v1 offset:10240\nv_fma_f32 v30, v14, v84, v30\nv_mfma_f32_16x16x32_bf16 v[40:43], a[160:163], v[56:59], v[40:43]\nds_read_b128 v[68:71], v2 offset:30976\nv_fma_f32 v31, v15, v84, v31\n" \
"v_mfma_f32_16x16x32_bf16 v[44:47], a[168:171], v[56:59], v[44:47]\ns_waitcnt lgkmcnt(5)\nv_rcp_f32 v91, v91\nv_mfma_f32_16x16x32_bf16 v[48:51], v[80:83], v[56:59], v[48:51]\nv_rcp_f32 v92, v92\nv_mfma_f32_16x16x32_bf16 v[52:55], a[176:179], v[56:59], v[52:55]\ns_waitcnt lgkmcnt(4)\nds_read_b128 v[80:83], v1 offset:11264\n" \
"v_mul_f32 v28, v28, v91\nv_mfma_f32_16x16x32_bf16 v[40:43], a[164:167], v[60:63], v[40:43]\nds_read_b128 v[56:59], v2 offset:33024\nv_mul_f32 v29, v29, v92\nv_mfma_f32_16x16x32_bf16 v[44:47], a[172:175], v[60:63], v[44:47]\ns_waitcnt lgkmcnt(5)\nv_cvt_pk_bf16_f32 v34, v28, v29\nv_fma_f32 v38, v22, v84, v38\n" \
"v_mfma_f32_16x16x32_bf16 v[48:51], v[72:75], v[60:63], v[48:51]\nv_fma_f32 v39, v23, v84, v39\nv_mul_f32 v30, v30, v85\nv_mfma_f32_16x16x32_bf16 v[52:55], a[180:183], v[60:63], v[52:55]\ns_waitcnt lgkmcnt(4)\nv_mul_f32 v31, v31, v85\nv_fma_f32 v91, v30, v30, s59\nv_mfma_f32_16x16x32_bf16 v[40:43], a[184:187], v[64:67], v[40:43]\n" \
"ds_read_b128 v[60:63], v2 offset:35072\nv_fma_f32 v92, v31, v31, s59\nv_mfma_f32_16x16x32_bf16 v[44:47], a[192:195], v[64:67], v[44:47]\nv_mul_f32 v91, v91, v30\nv_mul_f32 v92, v92, v31\nv_mfma_f32_16x16x32_bf16 v[48:51], a[200:203], v[64:67], v[48:51]\ns_waitcnt lgkmcnt(4)\nv_exp_f32 v91, v91\n" \
"v_mfma_f32_16x16x32_bf16 v[52:55], v[76:79], v[64:67], v[52:55]\ns_waitcnt lgkmcnt(3)\nv_exp_f32 v92, v92\nv_mfma_f32_16x16x32_bf16 v[40:43], a[188:191], v[68:71], v[40:43]\nds_read_b128 v[64:67], v2 offset:37120\nv_mul_f32 v30, v30, v38\nv_mfma_f32_16x16x32_bf16 v[44:47], a[196:199], v[68:71], v[44:47]\nv_mul_f32 v31, v31, v39\n" \
"v_fma_f32 v91, v91, v86, v86\nv_mfma_f32_16x16x32_bf16 v[48:51], a[204:207], v[68:71], v[48:51]\ns_waitcnt lgkmcnt(3)\nv_fma_f32 v92, v92, v86, v86\nv_mfma_f32_16x16x32_bf16 v[52:55], v[80:83], v[68:71], v[52:55]\ns_waitcnt lgkmcnt(2)\nds_read_b128 v[80:83], v1 offset:12288\nv_mfma_f32_16x16x32_bf16 v[40:43], a[208:211], v[56:59], v[40:43]\n" \
"ds_read_b128 v[68:71], v2 offset:39168\nv_mfma_f32_16x16x32_bf16 v[44:47], a[216:219], v[56:59], v[44:47]\nv_rcp_f32 v91, v91\nv_mfma_f32_16x16x32_bf16 v[48:51], a[224:227], v[56:59], v[48:51]\nv_rcp_f32 v92, v92\nv_mfma_f32_16x16x32_bf16 v[52:55], a[232:235], v[56:59], v[52:55]\ns_waitcnt lgkmcnt(3)\nds_read_b128 v[72:75], v1 offset:13312\n" \
"v_mul_f32 v30, v30, v91\nv_mfma_f32_16x16x32_bf16 v[40:43], a[212:215], v[60:63], v[40:43]\nds_read_b128 v[56:59], v2 offset:41216\nv_mul_f32 v31, v31, v92\nv_mfma_f32_16x16x32_bf16 v[44:47], a[220:223], v[60:63], v[44:47]\nv_cvt_pk_bf16_f32 v35, v30, v31\nv_mfma_f32_16x16x32_bf16 v[48:51], a[228:231], v[60:63], v[48:51]\nbuffer_store_dwordx4 v[32:35], v4, %[rs], s55 offen offset:0 sc1\n" \
"v_mfma_f32_16x16x32_bf16 v[52:55], a[236:239], v[60:63], v[52:55]\ns_waitcnt lgkmcnt(4)\nds_read_b128 v[76:79], v1 offset:14336\nv_mfma_f32_16x16x32_bf16 v[40:43], a[240:243], v[64:67], v[40:43]\ns_waitcnt lgkmcnt(4)\nds_read_b128 v[60:63], v2 offset:43264\nv_mfma_f32_16x16x32_bf16 v[44:47], v[80:83], v[64:67], v[44:47]\nv_mfma_f32_16x16x32_bf16 v[48:51], a[248:251], v[64:67], v[48:51]\n" \
"v_mfma_f32_16x16x32_bf16 v[52:55], v[96:99], v[64:67], v[52:55]\ns_waitcnt lgkmcnt(4)\nds_read_b128 v[80:83], v1 offset:15360\nv_mfma_f32_16x16x32_bf16 v[40:43], a[244:247], v[68:71], v[40:43]\ns_waitcnt lgkmcnt(4)\nds_read_b128 v[64:67], v2 offset:45312\nv_mfma_f32_16x16x32_bf16 v[44:47], v[72:75], v[68:71], v[44:47]\nv_mfma_f32_16x16x32_bf16 v[48:51], a[252:255], v[68:71], v[48:51]\n" \
"v_mfma_f32_16x16x32_bf16 v[52:55], v[100:103], v[68:71], v[52:55]\ns_waitcnt lgkmcnt(4)\nds_read_b128 v[72:75], v1 offset:16384\nv_mfma_f32_16x16x32_bf16 v[40:43], v[104:107], v[56:59], v[40:43]\nds_read_b128 v[68:71], v2 offset:47360\nv_mfma_f32_16x16x32_bf16 v[44:47], v[112:115], v[56:59], v[44:47]\ns_waitcnt lgkmcnt(5)\nv_mfma_f32_16x16x32_bf16 v[48:51], v[76:79], v[56:59], v[48:51]\n" \
"v_mfma_f32_16x16x32_bf16 v[52:55], v[120:123], v[56:59], v[52:55]\ns_waitcnt lgkmcnt(4)\nds_read_b128 v[76:79], v1 offset:17408\nv_mfma_f32_16x16x32_bf16 v[40:43], v[108:111], v[60:63], v[40:43]\nds_read_b128 v[56:59], v2 offset:49408\nv_mfma_f32_16x16x32_bf16 v[44:47], v[116:119], v[60:63], v[44:47]\ns_waitcnt lgkmcnt(5)\nv_mfma_f32_16x16x32_bf16 v[48:51], v[80:83], v[60:63], v[48:51]\n" \
"v_mfma_f32_16x16x32_bf16 v[52:55], v[124:127], v[60:63], v[52:55]\ns_waitcnt lgkmcnt(4)\nv_mfma_f32_16x16x32_bf16 v[40:43], v[128:131], v[64:67], v[40:43]\nds_read_b128 v[60:63], v2 offset:51456\nv_mfma_f32_16x16x32_bf16 v[44:47], v[136:139], v[64:67], v[44:47]\nv_mfma_f32_16x16x32_bf16 v[48:51], v[144:147], v[64:67], v[48:51]\ns_waitcnt lgkmcnt(4)\nv_mfma_f32_16x16x32_bf16 v[52:55], v[72:75], v[64:67], v[52:55]\n" \
"s_waitcnt lgkmcnt(3)\nv_mfma_f32_16x16x32_bf16 v[40:43], v[132:135], v[68:71], v[40:43]\nds_read_b128 v[64:67], v2 offset:53504\nv_mfma_f32_16x16x32_bf16 v[44:47], v[140:143], v[68:71], v[44:47]\nv_mfma_f32_16x16x32_bf16 v[48:51], v[148:151], v[68:71], v[48:51]\ns_waitcnt lgkmcnt(3)\nv_mfma_f32_16x16x32_bf16 v[52:55], v[76:79], v[68:71], v[52:55]\ns_waitcnt lgkmcnt(2)\n" \
"ds_read_b128 v[76:79], v1 offset:18432\nv_mfma_f32_16x16x32_bf16 v[40:43], v[152:155], v[56:59], v[40:43]\nds_read_b128 v[68:71], v2 offset:55552\nv_mfma_f32_16x16x32_bf16 v[44:47], v[160:163], v[56:59], v[44:47]\nv_mfma_f32_16x16x32_bf16 v[48:51], v[168:171], v[56:59], v[48:51]\nv_mfma_f32_16x16x32_bf16 v[52:55], v[176:179], v[56:59], v[52:55]\ns_waitcnt lgkmcnt(3)\nds_read_b128 v[80:83], v1 offset:19456\n" \
"v_mfma_f32_16x16x32_bf16 v[40:43], v[156:159], v[60:63], v[40:43]\nds_read_b128 v[56:59], v2 offset:57600\nv_mfma_f32_16x16x32_bf16 v[44:47], v[164:167], v[60:63], v[44:47]\nv_mfma_f32_16x16x32_bf16 v[48:51], v[172:175], v[60:63], v[48:51]\nv_mfma_f32_16x16x32_bf16 v[52:55], v[180:183], v[60:63], v[52:55]\ns_waitcnt lgkmcnt(4)\nds_read_b128 v[72:75], v1 offset:20480\nv_mfma_f32_16x16x32_bf16 v[40:43], v[184:187], v[64:67], v[40:43]\n" \
"s_waitcnt lgkmcnt(4)\nds_read_b128 v[60:63], v2 offset:59648\nv_mfma_f32_16x16x32_bf16 v[44:47], v[76:79], v[64:67], v[44:47]\ns_waitcnt vmcnt(0)\nbuffer_store_dword v90, off, %[rs], %[fh] sc1\nv_mfma_f32_16x16x32_bf16 v[48:51], v[192:195], v[64:67], v[48:51]\nv_mfma_f32_16x16x32_bf16 v[52:55], v[200:203], v[64:67], v[52:55]\ns_waitcnt lgkmcnt(4)\n" \
"ds_read_b128 v[76:79], v1 offset:21504\nv_mfma_f32_16x16x32_bf16 v[40:43], v[188:191], v[68:71], v[40:43]\ns_waitcnt lgkmcnt(4)\nds_read_b128 v[64:67], v2 offset:61696\nv_mfma_f32_16x16x32_bf16 v[44:47], v[80:83], v[68:71], v[44:47]\nv_mfma_f32_16x16x32_bf16 v[48:51], v[196:199], v[68:71], v[48:51]\nv_mfma_f32_16x16x32_bf16 v[52:55], v[204:207], v[68:71], v[52:55]\ns_waitcnt lgkmcnt(4)\n" \
"ds_read_b128 v[80:83], v1 offset:22528\nv_mfma_f32_16x16x32_bf16 v[40:43], v[208:211], v[56:59], v[40:43]\nds_read_b128 v[68:71], v2 offset:63744\nv_mfma_f32_16x16x32_bf16 v[44:47], v[216:219], v[56:59], v[44:47]\ns_waitcnt lgkmcnt(5)\nv_mfma_f32_16x16x32_bf16 v[48:51], v[72:75], v[56:59], v[48:51]\nv_mfma_f32_16x16x32_bf16 v[52:55], v[224:227], v[56:59], v[52:55]\ns_waitcnt lgkmcnt(4)\n" \
"ds_read_b128 v[72:75], v1 offset:23552\nv_mfma_f32_16x16x32_bf16 v[40:43], v[212:215], v[60:63], v[40:43]\nv_mfma_f32_16x16x32_bf16 v[44:47], v[220:223], v[60:63], v[44:47]\ns_waitcnt lgkmcnt(4)\nv_mfma_f32_16x16x32_bf16 v[48:51], v[76:79], v[60:63], v[48:51]\nv_mfma_f32_16x16x32_bf16 v[52:55], v[228:231], v[60:63], v[52:55]\ns_waitcnt lgkmcnt(3)\nv_mfma_f32_16x16x32_bf16 v[40:43], v[232:235], v[64:67], v[40:43]\n" \
"v_mfma_f32_16x16x32_bf16 v[44:47], v[240:243], v[64:67], v[44:47]\nv_mfma_f32_16x16x32_bf16 v[48:51], v[248:251], v[64:67], v[48:51]\ns_waitcnt lgkmcnt(2)\nv_mfma_f32_16x16x32_bf16 v[52:55], v[80:83], v[64:67], v[52:55]\ns_waitcnt lgkmcnt(1)\nv_mfma_f32_16x16x32_bf16 v[40:43], v[236:239], v[68:71], v[40:43]\nv_mfma_f32_16x16x32_bf16 v[44:47], v[244:247], v[68:71], v[44:47]\nv_mfma_f32_16x16x32_bf16 v[48:51], v[252:255], v[68:71], v[48:51]\n" \
"s_waitcnt lgkmcnt(0)\nv_mfma_f32_16x16x32_bf16 v[52:55], v[72:75], v[68:71], v[52:55]\nv_fma_f32 v40, v8, v87, v40\nv_fma_f32 v41, v9, v87, v41\ns_nop 1\nv_fma_f32 v48, v16, v87, v48\nv_fma_f32 v49, v17, v87, v49\nv_mul_f32 v40, v40, v88\n" \
"v_mul_f32 v41, v41, v88\nv_fma_f32 v91, v40, v40, s59\nv_fma_f32 v92, v41, v41, s59\nv_mul_f32 v91, v91, v40\nv_mul_f32 v92, v92, v41\nv_exp_f32 v91, v91\nv_exp_f32 v92, v92\nv_mul_f32 v40, v40, v48\n" \
"v_mul_f32 v41, v41, v49\nv_fma_f32 v91, v91, v89, v89\nv_fma_f32 v92, v92, v89, v89\nv_rcp_f32 v91, v91\nv_rcp_f32 v92, v92\nv_mul_f32 v40, v40, v91\nv_mul_f32 v41, v41, v92\nv_cvt_pk_bf16_f32 v48, v40, v41\n" \
"v_fma_f32 v42, v10, v87, v42\nv_fma_f32 v43, v11, v87, v43\nv_fma_f32 v50, v18, v87, v50\nv_fma_f32 v51, v19, v87, v51\nv_mul_f32 v42, v42, v88\nv_mul_f32 v43, v43, v88\nv_fma_f32 v91, v42, v42, s59\nv_fma_f32 v92, v43, v43, s59\n" \
"v_mul_f32 v91, v91, v42\nv_mul_f32 v92, v92, v43\nv_exp_f32 v91, v91\nv_exp_f32 v92, v92\nv_mul_f32 v42, v42, v50\nv_mul_f32 v43, v43, v51\nv_fma_f32 v91, v91, v89, v89\nv_fma_f32 v92, v92, v89, v89\n" \
"v_rcp_f32 v91, v91\nv_rcp_f32 v92, v92\nv_mul_f32 v42, v42, v91\nv_mul_f32 v43, v43, v92\nv_cvt_pk_bf16_f32 v49, v42, v43\nv_fma_f32 v44, v12, v87, v44\nv_fma_f32 v45, v13, v87, v45\nv_fma_f32 v52, v20, v87, v52\n" \
"v_fma_f32 v53, v21, v87, v53\nv_mul_f32 v44, v44, v88\nv_mul_f32 v45, v45, v88\nv_fma_f32 v91, v44, v44, s59\nv_fma_f32 v92, v45, v45, s59\nv_mul_f32 v91, v91, v44\nv_mul_f32 v92, v92, v45\nv_exp_f32 v91, v91\n" \
"v_exp_f32 v92, v92\nv_mul_f32 v44, v44, v52\nv_mul_f32 v45, v45, v53\nv_fma_f32 v91, v91, v89, v89\nv_fma_f32 v92, v92, v89, v89\nv_rcp_f32 v91, v91\nv_rcp_f32 v92, v92\nv_mul_f32 v44, v44, v91\n" \
"v_mul_f32 v45, v45, v92\nv_cvt_pk_bf16_f32 v50, v44, v45\nv_fma_f32 v46, v14, v87, v46\nv_fma_f32 v47, v15, v87, v47\nv_fma_f32 v54, v22, v87, v54\nv_fma_f32 v55, v23, v87, v55\nv_mul_f32 v46, v46, v88\nv_mul_f32 v47, v47, v88\n" \
"v_fma_f32 v91, v46, v46, s59\nv_fma_f32 v92, v47, v47, s59\nv_mul_f32 v91, v91, v46\nv_mul_f32 v92, v92, v47\nv_exp_f32 v91, v91\nv_exp_f32 v92, v92\nv_mul_f32 v46, v46, v54\nv_mul_f32 v47, v47, v55\n" \
"v_fma_f32 v91, v91, v89, v89\nv_fma_f32 v92, v92, v89, v89\nv_rcp_f32 v91, v91\nv_rcp_f32 v92, v92\nv_mul_f32 v46, v46, v91\nv_mul_f32 v47, v47, v92\nv_cvt_pk_bf16_f32 v51, v46, v47\nbuffer_store_dwordx4 v[48:51], v4, %[rs], s55 offen offset:1024 sc1\n" \
"v_add_u32 v90, 1, v90\ns_mov_b32 s56, %[fh]\ns_add_u32 s40, s40, 1\ns_cmp_lt_u32 s40, 64\ns_cbranch_scc1 .Lgg_next%=\ns_waitcnt vmcnt(0)\nbuffer_store_dword v90, off, %[rs], s56 sc1\ns_branch .Lgg_end%=\n" \
".Lgg_abort%=:\ns_mov_b64 exec, -1\nv_mov_b32 v16, 0\ns_movk_i32 s44, 6400\nbuffer_store_dword v16, off, %[rs], s44 sc1\n.Lgg_end%=:\ns_waitcnt vmcnt(0) lgkmcnt(0)\n" \
""
#define DD_ASM \
"v_mbcnt_lo_u32_b32 v255, -1, 0\nv_mbcnt_hi_u32_b32 v255, -1, v255\nv_lshlrev_b32 v255, 2, v255\nv_add_u32 v255, %[tb], v255\nds_read_b32 v0, v255 offset:0\nds_read_b32 v1, v255 offset:256\nds_read_b32 v2, v255 offset:512\nds_read_b32 v3, v255 offset:768\n" \
"ds_read_b32 v4, v255 offset:1024\nds_read_b32 v5, v255 offset:1280\nds_read_b32 v6, v255 offset:1536\nds_read_b32 v7, v255 offset:1792\nds_read_b32 v125, v255 offset:2048\nds_read_b32 v126, v255 offset:2304\nds_read_b32 v127, v255 offset:2560\ns_waitcnt lgkmcnt(0)\n" \
"s_add_u32 m0, %[tb], 8192\ns_add_u32 s44, %[wo], 0\nbuffer_load_dwordx4 v125, %[rw], s44 offen nt lds\ns_add_u32 m0, %[tb], 9216\ns_add_u32 s44, %[wo], 16384\nbuffer_load_dwordx4 v125, %[rw], s44 offen nt lds\ns_add_u32 m0, %[tb], 10240\ns_add_u32 s44, %[wo], 32768\n" \
"buffer_load_dwordx4 v125, %[rw], s44 offen nt lds\ns_add_u32 m0, %[tb], 11264\ns_add_u32 s44, %[wo], 49152\nbuffer_load_dwordx4 v125, %[rw], s44 offen nt lds\ns_add_u32 m0, %[lw], 0\ns_add_u32 s44, %[wo], 65536\nbuffer_load_dwordx4 v125, %[rw], s44 offen nt lds\ns_add_u32 m0, %[lw], 1024\n" \
"s_add_u32 s44, %[wo], 81920\nbuffer_load_dwordx4 v125, %[rw], s44 offen nt lds\ns_add_u32 m0, %[lw], 2048\ns_add_u32 s44, %[wo], 98304\nbuffer_load_dwordx4 v125, %[rw], s44 offen nt lds\ns_add_u32 m0, %[lw], 3072\ns_add_u32 s44, %[wo], 114688\nbuffer_load_dwordx4 v125, %[rw], s44 offen nt lds\n" \
"s_add_u32 m0, %[lw], 4096\ns_add_u32 s44, %[wo], 131072\nbuffer_load_dwordx4 v125, %[rw], s44 offen nt lds\ns_add_u32 m0, %[lw], 5120\ns_add_u32 s44, %[wo], 147456\nbuffer_load_dwordx4 v125, %[rw], s44 offen nt lds\ns_add_u32 m0, %[lw], 6144\ns_add_u32 s44, %[wo], 163840\n" \
"buffer_load_dwordx4 v125, %[rw], s44 offen nt lds\ns_add_u32 m0, %[lw], 7168\ns_add_u32 s44, %[wo], 180224\nbuffer_load_dwordx4 v125, %[rw], s44 offen nt lds\ns_add_u32 m0, %[lw], 8192\ns_add_u32 s44, %[wo], 196608\nbuffer_load_dwordx4 v125, %[rw], s44 offen nt lds\ns_add_u32 m0, %[lw], 9216\n" \
"s_add_u32 s44, %[wo], 212992\nbuffer_load_dwordx4 v125, %[rw], s44 offen nt lds\ns_add_u32 m0, %[lw], 10240\ns_add_u32 s44, %[wo], 229376\nbuffer_load_dwordx4 v125, %[rw], s44 offen nt lds\ns_add_u32 m0, %[lw], 11264\ns_add_u32 s44, %[wo], 245760\nbuffer_load_dwordx4 v125, %[rw], s44 offen nt lds\n" \
"s_add_u32 m0, %[lw], 12288\ns_add_u32 s44, %[wo], 262144\nbuffer_load_dwordx4 v125, %[rw], s44 offen nt lds\ns_add_u32 m0, %[lw], 13312\ns_add_u32 s44, %[wo], 278528\nbuffer_load_dwordx4 v125, %[rw], s44 offen nt lds\ns_add_u32 m0, %[lw], 14336\ns_add_u32 s44, %[wo], 294912\n" \
"buffer_load_dwordx4 v125, %[rw], s44 offen nt lds\ns_add_u32 m0, %[lw], 15360\ns_add_u32 s44, %[wo], 311296\nbuffer_load_dwordx4 v125, %[rw], s44 offen nt lds\ns_add_u32 m0, %[lw], 16384\ns_add_u32 s44, %[wo], 327680\nbuffer_load_dwordx4 v125, %[rw], s44 offen nt lds\ns_add_u32 m0, %[lw], 17408\n" \
"s_add_u32 s44, %[wo], 344064\nbuffer_load_dwordx4 v125, %[rw], s44 offen nt lds\ns_add_u32 m0, %[lw], 18432\ns_add_u32 s44, %[wo], 360448\nbuffer_load_dwordx4 v125, %[rw], s44 offen nt lds\ns_add_u32 m0, %[lw], 19456\ns_add_u32 s44, %[wo], 376832\nbuffer_load_dwordx4 v125, %[rw], s44 offen nt lds\n" \
"s_add_u32 m0, %[lw], 20480\ns_add_u32 s44, %[wo], 393216\nbuffer_load_dwordx4 v125, %[rw], s44 offen nt lds\ns_add_u32 m0, %[lw], 21504\ns_add_u32 s44, %[wo], 409600\nbuffer_load_dwordx4 v125, %[rw], s44 offen nt lds\ns_add_u32 m0, %[lw], 22528\ns_add_u32 s44, %[wo], 425984\n" \
"buffer_load_dwordx4 v125, %[rw], s44 offen nt lds\ns_add_u32 m0, %[lw], 23552\ns_add_u32 s44, %[wo], 442368\nbuffer_load_dwordx4 v125, %[rw], s44 offen nt lds\ns_add_u32 m0, %[tb], 0\ns_add_u32 s44, %[wo], 458752\nbuffer_load_dwordx4 v125, %[rw], s44 offen nt lds\ns_add_u32 m0, %[tb], 1024\n" \
"s_add_u32 s44, %[wo], 475136\nbuffer_load_dwordx4 v125, %[rw], s44 offen nt lds\ns_add_u32 m0, %[tb], 2048\ns_add_u32 s44, %[wo], 491520\nbuffer_load_dwordx4 v125, %[rw], s44 offen nt lds\ns_add_u32 m0, %[tb], 3072\ns_add_u32 s44, %[wo], 507904\nbuffer_load_dwordx4 v125, %[rw], s44 offen nt lds\n" \
"s_add_u32 m0, %[tb], 4096\ns_add_u32 s44, %[wo], 524288\nbuffer_load_dwordx4 v125, %[rw], s44 offen nt lds\ns_add_u32 m0, %[tb], 5120\ns_add_u32 s44, %[wo], 540672\nbuffer_load_dwordx4 v125, %[rw], s44 offen nt lds\ns_add_u32 m0, %[tb], 6144\ns_add_u32 s44, %[wo], 557056\n" \
"buffer_load_dwordx4 v125, %[rw], s44 offen nt lds\ns_add_u32 m0, %[tb], 7168\ns_add_u32 s44, %[wo], 573440\nbuffer_load_dwordx4 v125, %[rw], s44 offen nt lds\ns_waitcnt vmcnt(32)\nds_read_b64_tr_b16 a[0:1], v126 offset:8192\nds_read_b64_tr_b16 a[2:3], v126 offset:8704\nds_read_b64_tr_b16 a[8:9], v126 offset:8224\n" \
"ds_read_b64_tr_b16 a[10:11], v126 offset:8736\nds_read_b64_tr_b16 a[16:17], v126 offset:8256\nds_read_b64_tr_b16 a[18:19], v126 offset:8768\nds_read_b64_tr_b16 a[24:25], v126 offset:8288\nds_read_b64_tr_b16 a[26:27], v126 offset:8800\ns_waitcnt lgkmcnt(0)\ns_add_u32 m0, %[tb], 8192\ns_add_u32 s44, %[wo], 589824\n" \
"buffer_load_dwordx4 v125, %[rw], s44 offen nt lds\ns_add_u32 m0, %[tb], 9216\ns_add_u32 s44, %[wo], 606208\nbuffer_load_dwordx4 v125, %[rw], s44 offen nt lds\ns_add_u32 m0, %[tb], 10240\ns_add_u32 s44, %[wo], 622592\nbuffer_load_dwordx4 v125, %[rw], s44 offen nt lds\ns_add_u32 m0, %[tb], 11264\n" \
"s_add_u32 s44, %[wo], 638976\nbuffer_load_dwordx4 v125, %[rw], s44 offen nt lds\ns_waitcnt vmcnt(32)\nds_read_b64_tr_b16 a[4:5], v127 offset:0\nds_read_b64_tr_b16 a[6:7], v127 offset:512\nds_read_b64_tr_b16 a[12:13], v127 offset:32\nds_read_b64_tr_b16 a[14:15], v127 offset:544\nds_read_b64_tr_b16 a[20:21], v127 offset:64\n" \
"ds_read_b64_tr_b16 a[22:23], v127 offset:576\nds_read_b64_tr_b16 a[28:29], v127 offset:96\nds_read_b64_tr_b16 a[30:31], v127 offset:608\ns_waitcnt lgkmcnt(0)\ns_add_u32 m0, %[lw], 0\ns_add_u32 s44, %[wo], 655360\nbuffer_load_dwordx4 v125, %[rw], s44 offen nt lds\ns_add_u32 m0, %[lw], 1024\n" \
"s_add_u32 s44, %[wo], 671744\nbuffer_load_dwordx4 v125, %[rw], s44 offen nt lds\ns_add_u32 m0, %[lw], 2048\ns_add_u32 s44, %[wo], 688128\nbuffer_load_dwordx4 v125, %[rw], s44 offen nt lds\ns_add_u32 m0, %[lw], 3072\ns_add_u32 s44, %[wo], 704512\nbuffer_load_dwordx4 v125, %[rw], s44 offen nt lds\n" \
"s_waitcnt vmcnt(32)\nds_read_b64_tr_b16 a[32:33], v127 offset:4096\nds_read_b64_tr_b16 a[34:35], v127 offset:4608\nds_read_b64_tr_b16 v[92:93], v127 offset:4128\nds_read_b64_tr_b16 v[94:95], v127 offset:4640\nds_read_b64_tr_b16 a[40:41], v127 offset:4160\nds_read_b64_tr_b16 a[42:43], v127 offset:4672\nds_read_b64_tr_b16 a[48:49], v127 offset:4192\n" \
"ds_read_b64_tr_b16 a[50:51], v127 offset:4704\ns_waitcnt lgkmcnt(0)\ns_add_u32 m0, %[lw], 4096\ns_add_u32 s44, %[wo], 720896\nbuffer_load_dwordx4 v125, %[rw], s44 offen nt lds\ns_add_u32 m0, %[lw], 5120\ns_add_u32 s44, %[wo], 737280\nbuffer_load_dwordx4 v125, %[rw], s44 offen nt lds\n" \
"s_add_u32 m0, %[lw], 6144\ns_add_u32 s44, %[wo], 753664\nbuffer_load_dwordx4 v125, %[rw], s44 offen nt lds\ns_add_u32 m0, %[lw], 7168\ns_add_u32 s44, %[wo], 770048\nbuffer_load_dwordx4 v125, %[rw], s44 offen nt lds\ns_waitcnt vmcnt(32)\nds_read_b64_tr_b16 a[36:37], v127 offset:8192\n" \
"ds_read_b64_tr_b16 a[38:39], v127 offset:8704\nds_read_b64_tr_b16 v[88:89], v127 offset:8224\nds_read_b64_tr_b16 v[90:91], v127 offset:8736\nds_read_b64_tr_b16 a[44:45], v127 offset:8256\nds_read_b64_tr_b16 a[46:47], v127 offset:8768\nds_read_b64_tr_b16 a[52:53], v127 offset:8288\nds_read_b64_tr_b16 a[54:55], v127 offset:8800\ns_waitcnt lgkmcnt(0)\n" \
"s_add_u32 m0, %[lw], 8192\ns_add_u32 s44, %[wo], 786432\nbuffer_load_dwordx4 v125, %[rw], s44 offen nt lds\ns_add_u32 m0, %[lw], 9216\ns_add_u32 s44, %[wo], 802816\nbuffer_load_dwordx4 v125, %[rw], s44 offen nt lds\ns_add_u32 m0, %[lw], 10240\ns_add_u32 s44, %[wo], 819200\n" \
"buffer_load_dwordx4 v125, %[rw], s44 offen nt lds\ns_add_u32 m0, %[lw], 11264\ns_add_u32 s44, %[wo], 835584\nbuffer_load_dwordx4 v125, %[rw], s44 offen nt lds\ns_waitcnt vmcnt(32)\nds_read_b64_tr_b16 a[56:57], v127 offset:12288\nds_read_b64_tr_b16 a[58:59], v127 offset:12800\nds_read_b64_tr_b16 a[64:65], v127 offset:12320\n" \
"ds_read_b64_tr_b16 a[66:67], v127 offset:12832\nds_read_b64_tr_b16 a[72:73], v127 offset:12352\nds_read_b64_tr_b16 a[74:75], v127 offset:12864\nds_read_b64_tr_b16 a[80:81], v127 offset:12384\nds_read_b64_tr_b16 a[82:83], v127 offset:12896\ns_waitcnt lgkmcnt(0)\ns_add_u32 m0, %[lw], 12288\ns_add_u32 s44, %[wo], 851968\n" \
"buffer_load_dwordx4 v125, %[rw], s44 offen nt lds\ns_add_u32 m0, %[lw], 13312\ns_add_u32 s44, %[wo], 868352\nbuffer_load_dwordx4 v125, %[rw], s44 offen nt lds\ns_add_u32 m0, %[lw], 14336\ns_add_u32 s44, %[wo], 884736\nbuffer_load_dwordx4 v125, %[rw], s44 offen nt lds\ns_add_u32 m0, %[lw], 15360\n" \
"s_add_u32 s44, %[wo], 901120\nbuffer_load_dwordx4 v125, %[rw], s44 offen nt lds\ns_waitcnt vmcnt(32)\nds_read_b64_tr_b16 a[60:61], v127 offset:16384\nds_read_b64_tr_b16 a[62:63], v127 offset:16896\nds_read_b64_tr_b16 a[68:69], v127 offset:16416\nds_read_b64_tr_b16 a[70:71], v127 offset:16928\nds_read_b64_tr_b16 a[76:77], v127 offset:16448\n" \
"ds_read_b64_tr_b16 a[78:79], v127 offset:16960\nds_read_b64_tr_b16 a[84:85], v127 offset:16480\nds_read_b64_tr_b16 a[86:87], v127 offset:16992\ns_waitcnt lgkmcnt(0)\ns_add_u32 m0, %[lw], 16384\ns_add_u32 s44, %[wo], 917504\nbuffer_load_dwordx4 v125, %[rw], s44 offen nt lds\ns_add_u32 m0, %[lw], 17408\n" \
"s_add_u32 s44, %[wo], 933888\nbuffer_load_dwordx4 v125, %[rw], s44 offen nt lds\ns_add_u32 m0, %[lw], 18432\ns_add_u32 s44, %[wo], 950272\nbuffer_load_dwordx4 v125, %[rw], s44 offen nt lds\ns_add_u32 m0, %[lw], 19456\ns_add_u32 s44, %[wo], 966656\nbuffer_load_dwordx4 v125, %[rw], s44 offen nt lds\n" \
"s_waitcnt vmcnt(32)\nds_read_b64_tr_b16 a[88:89], v127 offset:20480\nds_read_b64_tr_b16 a[90:91], v127 offset:20992\nds_read_b64_tr_b16 a[96:97], v127 offset:20512\nds_read_b64_tr_b16 a[98:99], v127 offset:21024\nds_read_b64_tr_b16 a[104:105], v127 offset:20544\nds_read_b64_tr_b16 a[106:107], v127 offset:21056\nds_read_b64_tr_b16 v[84:85], v127 offset:20576\n" \
"ds_read_b64_tr_b16 v[86:87], v127 offset:21088\ns_waitcnt lgkmcnt(0)\ns_add_u32 m0, %[lw], 20480\ns_add_u32 s44, %[wo], 983040\nbuffer_load_dwordx4 v125, %[rw], s44 offen nt lds\ns_add_u32 m0, %[lw], 21504\ns_add_u32 s44, %[wo], 999424\nbuffer_load_dwordx4 v125, %[rw], s44 offen nt lds\n" \
"s_add_u32 m0, %[lw], 22528\ns_add_u32 s44, %[wo], 1015808\nbuffer_load_dwordx4 v125, %[rw], s44 offen nt lds\ns_add_u32 m0, %[lw], 23552\ns_add_u32 s44, %[wo], 1032192\nbuffer_load_dwordx4 v125, %[rw], s44 offen nt lds\ns_waitcnt vmcnt(32)\nds_read_b64_tr_b16 a[92:93], v126 offset:0\n" \
"ds_read_b64_tr_b16 a[94:95], v126 offset:512\nds_read_b64_tr_b16 a[100:101], v126 offset:32\nds_read_b64_tr_b16 a[102:103], v126 offset:544\nds_read_b64_tr_b16 a[108:109], v126 offset:64\nds_read_b64_tr_b16 a[110:111], v126 offset:576\nds_read_b64_tr_b16 v[80:81], v126 offset:96\nds_read_b64_tr_b16 v[82:83], v126 offset:608\ns_waitcnt lgkmcnt(0)\n" \
"s_add_u32 m0, %[tb], 0\ns_add_u32 s44, %[wo], 1048576\nbuffer_load_dwordx4 v125, %[rw], s44 offen nt lds\ns_add_u32 m0, %[tb], 1024\ns_add_u32 s44, %[wo], 1064960\nbuffer_load_dwordx4 v125, %[rw], s44 offen nt lds\ns_add_u32 m0, %[tb], 2048\ns_add_u32 s44, %[wo], 1081344\n" \
"buffer_load_dwordx4 v125, %[rw], s44 offen nt lds\ns_add_u32 m0, %[tb], 3072\ns_add_u32 s44, %[wo], 1097728\nbuffer_load_dwordx4 v125, %[rw], s44 offen nt lds\ns_waitcnt vmcnt(32)\nds_read_b64_tr_b16 a[112:113], v126 offset:4096\nds_read_b64_tr_b16 a[114:115], v126 offset:4608\nds_read_b64_tr_b16 a[120:121], v126 offset:4128\n" \
"ds_read_b64_tr_b16 a[122:123], v126 offset:4640\nds_read_b64_tr_b16 a[128:129], v126 offset:4160\nds_read_b64_tr_b16 a[130:131], v126 offset:4672\nds_read_b64_tr_b16 a[136:137], v126 offset:4192\nds_read_b64_tr_b16 a[138:139], v126 offset:4704\ns_waitcnt lgkmcnt(0)\ns_add_u32 m0, %[tb], 4096\ns_add_u32 s44, %[wo], 1114112\n" \
"buffer_load_dwordx4 v125, %[rw], s44 offen nt lds\ns_add_u32 m0, %[tb], 5120\ns_add_u32 s44, %[wo], 1130496\nbuffer_load_dwordx4 v125, %[rw], s44 offen nt lds\ns_add_u32 m0, %[tb], 6144\ns_add_u32 s44, %[wo], 1146880\nbuffer_load_dwordx4 v125, %[rw], s44 offen nt lds\ns_add_u32 m0, %[tb], 7168\n" \
"s_add_u32 s44, %[wo], 1163264\nbuffer_load_dwordx4 v125, %[rw], s44 offen nt lds\ns_waitcnt vmcnt(32)\nds_read_b64_tr_b16 a[116:117], v126 offset:8192\nds_read_b64_tr_b16 a[118:119], v126 offset:8704\nds_read_b64_tr_b16 a[124:125], v126 offset:8224\nds_read_b64_tr_b16 a[126:127], v126 offset:8736\nds_read_b64_tr_b16 a[132:133], v126 offset:8256\n" \
"ds_read_b64_tr_b16 a[134:135], v126 offset:8768\nds_read_b64_tr_b16 a[140:141], v126 offset:8288\nds_read_b64_tr_b16 a[142:143], v126 offset:8800\ns_waitcnt lgkmcnt(0)\ns_add_u32 m0, %[tb], 8192\ns_add_u32 s44, %[wo], 1179648\nbuffer_load_dwordx4 v125, %[rw], s44 offen nt lds\ns_add_u32 m0, %[tb], 9216\n" \
"s_add_u32 s44, %[wo], 1196032\nbuffer_load_dwordx4 v125, %[rw], s44 offen nt lds\ns_add_u32 m0, %[tb], 10240\ns_add_u32 s44, %[wo], 1212416\nbuffer_load_dwordx4 v125, %[rw], s44 offen nt lds\ns_add_u32 m0, %[tb], 11264\ns_add_u32 s44, %[wo], 1228800\nbuffer_load_dwordx4 v125, %[rw], s44 offen nt lds\n" \
"s_waitcnt vmcnt(32)\nds_read_b64_tr_b16 a[144:145], v127 offset:0\nds_read_b64_tr_b16 a[146:147], v127 offset:512\nds_read_b64_tr_b16 v[76:77], v127 offset:32\nds_read_b64_tr_b16 v[78:79], v127 offset:544\nds_read_b64_tr_b16 a[152:153], v127 offset:64\nds_read_b64_tr_b16 a[154:155], v127 offset:576\nds_read_b64_tr_b16 a[160:161], v127 offset:96\n" \
"ds_read_b64_tr_b16 a[162:163], v127 offset:608\ns_waitcnt lgkmcnt(0)\ns_add_u32 m0, %[lw], 0\ns_add_u32 s44, %[wo], 1245184\nbuffer_load_dwordx4 v125, %[rw], s44 offen nt lds\ns_add_u32 m0, %[lw], 1024\ns_add_u32 s44, %[wo], 1261568\nbuffer_load_dwordx4 v125, %[rw], s44 offen nt lds\n" \
"s_add_u32 m0, %[lw], 2048\ns_add_u32 s44, %[wo], 1277952\nbuffer_load_dwordx4 v125, %[rw], s44 offen nt lds\ns_add_u32 m0, %[lw], 3072\ns_add_u32 s44, %[wo], 1294336\nbuffer_load_dwordx4 v125, %[rw], s44 offen nt lds\ns_waitcnt vmcnt(32)\nds_read_b64_tr_b16 a[148:149], v127 offset:4096\n" \
"ds_read_b64_tr_b16 a[150:151], v127 offset:4608\nds_read_b64_tr_b16 v[72:73], v127 offset:4128\nds_read_b64_tr_b16 v[74:75], v127 offset:4640\nds_read_b64_tr_b16 a[156:157], v127 offset:4160\nds_read_b64_tr_b16 a[158:159], v127 offset:4672\nds_read_b64_tr_b16 a[164:165], v127 offset:4192\nds_read_b64_tr_b16 a[166:167], v127 offset:4704\ns_waitcnt lgkmcnt(0)\n" \
"s_add_u32 m0, %[lw], 4096\ns_add_u32 s44, %[wo], 1310720\nbuffer_load_dwordx4 v125, %[rw], s44 offen nt lds\ns_add_u32 m0, %[lw], 5120\ns_add_u32 s44, %[wo], 1327104\nbuffer_load_dwordx4 v125, %[rw], s44 offen nt lds\ns_add_u32 m0, %[lw], 6144\ns_add_u32 s44, %[wo], 1343488\n" \
"buffer_load_dwordx4 v125, %[rw], s44 offen nt lds\ns_add_u32 m0, %[lw], 7168\ns_add_u32 s44, %[wo], 1359872\nbuffer_load_dwordx4 v125, %[rw], s44 offen nt lds\ns_waitcnt vmcnt(32)\nds_read_b64_tr_b16 a[168:169], v127 offset:8192\nds_read_b64_tr_b16 a[170:171], v127 offset:8704\nds_read_b64_tr_b16 a[176:177], v127 offset:8224\n" \
"ds_read_b64_tr_b16 a[178:179], v127 offset:8736\nds_read_b64_tr_b16 v[68:69], v127 offset:8256\nds_read_b64_tr_b16 v[70:71], v127 offset:8768\nds_read_b64_tr_b16 a[184:185], v127 offset:8288\nds_read_b64_tr_b16 a[186:187], v127 offset:8800\ns_waitcnt lgkmcnt(0)\ns_add_u32 m0, %[lw], 8192\ns_add_u32 s44, %[wo], 1376256\n" \
"buffer_load_dwordx4 v125, %[rw], s44 offen nt lds\ns_add_u32 m0, %[lw], 9216\ns_add_u32 s44, %[wo], 1392640\nbuffer_load_dwordx4 v125, %[rw], s44 offen nt lds\ns_add_u32 m0, %[lw], 10240\ns_add_u32 s44, %[wo], 1409024\nbuffer_load_dwordx4 v125, %[rw], s44 offen nt lds\ns_add_u32 m0, %[lw], 11264\n" \
"s_add_u32 s44, %[wo], 1425408\nbuffer_load_dwordx4 v125, %[rw], s44 offen nt lds\ns_waitcnt vmcnt(32)\nds_read_b64_tr_b16 a[172:173], v127 offset:12288\nds_read_b64_tr_b16 a[174:175], v127 offset:12800\nds_read_b64_tr_b16 a[180:181], v127 offset:12320\nds_read_b64_tr_b16 a[182:183], v127 offset:12832\nds_read_b64_tr_b16 v[64:65], v127 offset:12352\n" \
"ds_read_b64_tr_b16 v[66:67], v127 offset:12864\nds_read_b64_tr_b16 a[188:189], v127 offset:12384\nds_read_b64_tr_b16 a[190:191], v127 offset:12896\ns_waitcnt lgkmcnt(0)\ns_add_u32 m0, %[lw], 12288\ns_add_u32 s44, %[wo], 1441792\nbuffer_load_dwordx4 v125, %[rw], s44 offen nt lds\ns_add_u32 m0, %[lw], 13312\n" \
"s_add_u32 s44, %[wo], 1458176\nbuffer_load_dwordx4 v125, %[rw], s44 offen nt lds\ns_add_u32 m0, %[lw], 14336\ns_add_u32 s44, %[wo], 1474560\nbuffer_load_dwordx4 v125, %[rw], s44 offen nt lds\ns_add_u32 m0, %[lw], 15360\ns_add_u32 s44, %[wo], 1490944\nbuffer_load_dwordx4 v125, %[rw], s44 offen nt lds\n" \
"s_waitcnt vmcnt(32)\nds_read_b64_tr_b16 a[192:193], v127 offset:16384\nds_read_b64_tr_b16 a[194:195], v127 offset:16896\nds_read_b64_tr_b16 a[200:201], v127 offset:16416\nds_read_b64_tr_b16 a[202:203], v127 offset:16928\nds_read_b64_tr_b16 a[208:209], v127 offset:16448\nds_read_b64_tr_b16 a[210:211], v127 offset:16960\nds_read_b64_tr_b16 a[216:217], v127 offset:16480\n" \
"ds_read_b64_tr_b16 a[218:219], v127 offset:16992\ns_waitcnt lgkmcnt(0)\ns_add_u32 m0, %[lw], 16384\ns_add_u32 s44, %[wo], 1507328\nbuffer_load_dwordx4 v125, %[rw], s44 offen nt lds\ns_add_u32 m0, %[lw], 17408\ns_add_u32 s44, %[wo], 1523712\nbuffer_load_dwordx4 v125, %[rw], s44 offen nt lds\n" \
"s_add_u32 m0, %[lw], 18432\ns_add_u32 s44, %[wo], 1540096\nbuffer_load_dwordx4 v125, %[rw], s44 offen nt lds\ns_add_u32 m0, %[lw], 19456\ns_add_u32 s44, %[wo], 1556480\nbuffer_load_dwordx4 v125, %[rw], s44 offen nt lds\ns_waitcnt vmcnt(32)\nds_read_b64_tr_b16 a[196:197], v127 offset:20480\n" \
"ds_read_b64_tr_b16 a[198:199], v127 offset:20992\nds_read_b64_tr_b16 a[204:205], v127 offset:20512\nds_read_b64_tr_b16 a[206:207], v127 offset:21024\nds_read_b64_tr_b16 a[212:213], v127 offset:20544\nds_read_b64_tr_b16 a[214:215], v127 offset:21056\nds_read_b64_tr_b16 a[220:221], v127 offset:20576\nds_read_b64_tr_b16 a[222:223], v127 offset:21088\ns_waitcnt lgkmcnt(0)\n" \
"s_add_u32 m0, %[lw], 20480\ns_add_u32 s44, %[wo], 1572864\nbuffer_load_dwordx4 v125, %[rw], s44 offen nt lds\ns_add_u32 m0, %[lw], 21504\ns_add_u32 s44, %[wo], 1589248\nbuffer_load_dwordx4 v125, %[rw], s44 offen nt lds\ns_add_u32 m0, %[lw], 22528\ns_add_u32 s44, %[wo], 1605632\n" \
"buffer_load_dwordx4 v125, %[rw], s44 offen nt lds\ns_add_u32 m0, %[lw], 23552\ns_add_u32 s44, %[wo], 1622016\nbuffer_load_dwordx4 v125, %[rw], s44 offen nt lds\ns_waitcnt vmcnt(32)\nds_read_b64_tr_b16 v[60:61], v126 offset:0\nds_read_b64_tr_b16 v[62:63], v126 offset:512\nds_read_b64_tr_b16 a[224:225], v126 offset:32\n" \
"ds_read_b64_tr_b16 a[226:227], v126 offset:544\nds_read_b64_tr_b16 a[232:233], v126 offset:64\nds_read_b64_tr_b16 a[234:235], v126 offset:576\nds_read_b64_tr_b16 a[240:241], v126 offset:96\nds_read_b64_tr_b16 a[242:243], v126 offset:608\ns_waitcnt lgkmcnt(0)\ns_add_u32 m0, %[tb], 0\ns_add_u32 s44, %[wo], 1638400\n" \
"buffer_load_dwordx4 v125, %[rw], s44 offen nt lds\ns_add_u32 m0, %[tb], 1024\ns_add_u32 s44, %[wo], 1654784\nbuffer_load_dwordx4 v125, %[rw], s44 offen nt lds\ns_add_u32 m0, %[tb], 2048\ns_add_u32 s44, %[wo], 1671168\nbuffer_load_dwordx4 v125, %[rw], s44 offen nt lds\ns_add_u32 m0, %[tb], 3072\n" \
"s_add_u32 s44, %[wo], 1687552\nbuffer_load_dwordx4 v125, %[rw], s44 offen nt lds\ns_waitcnt vmcnt(32)\nds_read_b64_tr_b16 v[56:57], v126 offset:4096\nds_read_b64_tr_b16 v[58:59], v126 offset:4608\nds_read_b64_tr_b16 a[228:229], v126 offset:4128\nds_read_b64_tr_b16 a[230:231], v126 offset:4640\nds_read_b64_tr_b16 a[236:237], v126 offset:4160\n" \
"ds_read_b64_tr_b16 a[238:239], v126 offset:4672\nds_read_b64_tr_b16 a[244:245], v126 offset:4192\nds_read_b64_tr_b16 a[246:247], v126 offset:4704\ns_waitcnt lgkmcnt(0)\ns_add_u32 m0, %[tb], 4096\ns_add_u32 s44, %[wo], 1703936\nbuffer_load_dwordx4 v125, %[rw], s44 offen nt lds\ns_add_u32 m0, %[tb], 5120\n" \
"s_add_u32 s44, %[wo], 1720320\nbuffer_load_dwordx4 v125, %[rw], s44 offen nt lds\ns_add_u32 m0, %[tb], 6144\ns_add_u32 s44, %[wo], 1736704\nbuffer_load_dwordx4 v125, %[rw], s44 offen nt lds\ns_add_u32 m0, %[tb], 7168\ns_add_u32 s44, %[wo], 1753088\nbuffer_load_dwordx4 v125, %[rw], s44 offen nt lds\n" \
"s_waitcnt vmcnt(32)\nds_read_b64_tr_b16 a[248:249], v126 offset:8192\nds_read_b64_tr_b16 a[250:251], v126 offset:8704\nds_read_b64_tr_b16 v[128:129], v126 offset:8224\nds_read_b64_tr_b16 v[130:131], v126 offset:8736\nds_read_b64_tr_b16 v[136:137], v126 offset:8256\nds_read_b64_tr_b16 v[138:139], v126 offset:8768\nds_read_b64_tr_b16 v[144:145], v126 offset:8288\n" \
"ds_read_b64_tr_b16 v[146:147], v126 offset:8800\ns_waitcnt lgkmcnt(0)\ns_add_u32 m0, %[tb], 8192\ns_add_u32 s44, %[wo], 1769472\nbuffer_load_dwordx4 v125, %[rw], s44 offen nt lds\ns_add_u32 m0, %[tb], 9216\ns_add_u32 s44, %[wo], 1785856\nbuffer_load_dwordx4 v125, %[rw], s44 offen nt lds\n" \
"s_add_u32 m0, %[tb], 10240\ns_add_u32 s44, %[wo], 1802240\nbuffer_load_dwordx4 v125, %[rw], s44 offen nt lds\ns_add_u32 m0, %[tb], 11264\ns_add_u32 s44, %[wo], 1818624\nbuffer_load_dwordx4 v125, %[rw], s44 offen nt lds\ns_waitcnt vmcnt(32)\nds_read_b64_tr_b16 a[252:253], v127 offset:0\n" \
"ds_read_b64_tr_b16 a[254:255], v127 offset:512\nds_read_b64_tr_b16 v[132:133], v127 offset:32\nds_read_b64_tr_b16 v[134:135], v127 offset:544\nds_read_b64_tr_b16 v[140:141], v127 offset:64\nds_read_b64_tr_b16 v[142:143], v127 offset:576\nds_read_b64_tr_b16 v[148:149], v127 offset:96\nds_read_b64_tr_b16 v[150:151], v127 offset:608\ns_waitcnt lgkmcnt(0)\n" \
"ds_write_b128 v1, v[92:95] offset:0\nds_write_b128 v1, v[88:91] offset:1024\nds_write_b128 v1, v[84:87] offset:2048\nds_write_b128 v1, v[80:83] offset:3072\ns_waitcnt vmcnt(28)\nds_read_b64_tr_b16 v[152:153], v127 offset:4096\nds_read_b64_tr_b16 v[154:155], v127 offset:4608\nds_read_b64_tr_b16 v[160:161], v127 offset:4128\n" \
"ds_read_b64_tr_b16 v[162:163], v127 offset:4640\nds_read_b64_tr_b16 v[80:81], v127 offset:4160\nds_read_b64_tr_b16 v[82:83], v127 offset:4672\nds_read_b64_tr_b16 v[168:169], v127 offset:4192\nds_read_b64_tr_b16 v[170:171], v127 offset:4704\ns_waitcnt lgkmcnt(0)\nds_write_b128 v1, v[76:79] offset:4096\nds_write_b128 v1, v[72:75] offset:5120\n" \
"ds_write_b128 v1, v[68:71] offset:6144\nds_write_b128 v1, v[64:67] offset:7168\ns_waitcnt vmcnt(24)\nds_read_b64_tr_b16 v[156:157], v127 offset:8192\nds_read_b64_tr_b16 v[158:159], v127 offset:8704\nds_read_b64_tr_b16 v[164:165], v127 offset:8224\nds_read_b64_tr_b16 v[166:167], v127 offset:8736\nds_read_b64_tr_b16 v[64:65], v127 offset:8256\n" \
"ds_read_b64_tr_b16 v[66:67], v127 offset:8768\nds_read_b64_tr_b16 v[172:173], v127 offset:8288\nds_read_b64_tr_b16 v[174:175], v127 offset:8800\ns_waitcnt lgkmcnt(0)\nds_write_b128 v1, v[60:63] offset:8192\nds_write_b128 v1, v[56:59] offset:9216\nds_write_b128 v1, v[80:83] offset:10240\nds_write_b128 v1, v[64:67] offset:11264\n" \
"s_waitcnt vmcnt(20)\nds_read_b64_tr_b16 v[176:177], v127 offset:12288\nds_read_b64_tr_b16 v[178:179], v127 offset:12800\nds_read_b64_tr_b16 v[184:185], v127 offset:12320\nds_read_b64_tr_b16 v[186:187], v127 offset:12832\nds_read_b64_tr_b16 v[192:193], v127 offset:12352\nds_read_b64_tr_b16 v[194:195], v127 offset:12864\nds_read_b64_tr_b16 v[200:201], v127 offset:12384\n" \
"ds_read_b64_tr_b16 v[202:203], v127 offset:12896\ns_waitcnt lgkmcnt(0)\ns_waitcnt vmcnt(16)\nds_read_b64_tr_b16 v[180:181], v127 offset:16384\nds_read_b64_tr_b16 v[182:183], v127 offset:16896\nds_read_b64_tr_b16 v[188:189], v127 offset:16416\nds_read_b64_tr_b16 v[190:191], v127 offset:16928\nds_read_b64_tr_b16 v[196:197], v127 offset:16448\n" \
"ds_read_b64_tr_b16 v[198:199], v127 offset:16960\nds_read_b64_tr_b16 v[204:205], v127 offset:16480\nds_read_b64_tr_b16 v[206:207], v127 offset:16992\ns_waitcnt lgkmcnt(0)\ns_waitcnt vmcnt(12)\nds_read_b64_tr_b16 v[64:65], v127 offset:20480\nds_read_b64_tr_b16 v[66:67], v127 offset:20992\nds_read_b64_tr_b16 v[208:209], v127 offset:20512\n" \
"ds_read_b64_tr_b16 v[210:211], v127 offset:21024\nds_read_b64_tr_b16 v[216:217], v127 offset:20544\nds_read_b64_tr_b16 v[218:219], v127 offset:21056\nds_read_b64_tr_b16 v[224:225], v127 offset:20576\nds_read_b64_tr_b16 v[226:227], v127 offset:21088\ns_waitcnt lgkmcnt(0)\nds_write_b128 v1, v[64:67] offset:12288\ns_waitcnt vmcnt(8)\n" \
"ds_read_b64_tr_b16 v[64:65], v126 offset:0\nds_read_b64_tr_b16 v[66:67], v126 offset:512\nds_read_b64_tr_b16 v[212:213], v126 offset:32\nds_read_b64_tr_b16 v[214:215], v126 offset:544\nds_read_b64_tr_b16 v[220:221], v126 offset:64\nds_read_b64_tr_b16 v[222:223], v126 offset:576\nds_read_b64_tr_b16 v[228:229], v126 offset:96\nds_read_b64_tr_b16 v[230:231], v126 offset:608\n" \
"s_waitcnt lgkmcnt(0)\nds_write_b128 v1, v[64:67] offset:13312\ns_waitcnt vmcnt(4)\nds_read_b64_tr_b16 v[232:233], v126 offset:4096\nds_read_b64_tr_b16 v[234:235], v126 offset:4608\nds_read_b64_tr_b16 v[64:65], v126 offset:4128\nds_read_b64_tr_b16 v[66:67], v126 offset:4640\nds_read_b64_tr_b16 v[240:241], v126 offset:4160\n" \
"ds_read_b64_tr_b16 v[242:243], v126 offset:4672\nds_read_b64_tr_b16 v[248:249], v126 offset:4192\nds_read_b64_tr_b16 v[250:251], v126 offset:4704\ns_waitcnt lgkmcnt(0)\nds_write_b128 v1, v[64:67] offset:14336\ns_waitcnt vmcnt(0)\nds_read_b64_tr_b16 v[236:237], v126 offset:8192\nds_read_b64_tr_b16 v[238:239], v126 offset:8704\n" \
"ds_read_b64_tr_b16 v[64:65], v126 offset:8224\nds_read_b64_tr_b16 v[66:67], v126 offset:8736\nds_read_b64_tr_b16 v[244:245], v126 offset:8256\nds_read_b64_tr_b16 v[246:247], v126 offset:8768\nds_read_b64_tr_b16 v[252:253], v126 offset:8288\nds_read_b64_tr_b16 v[254:255], v126 offset:8800\ns_waitcnt lgkmcnt(0)\nds_write_b128 v1, v[64:67] offset:15360\n" \
"s_waitcnt lgkmcnt(0)\ns_barrier\ns_mov_b32 s40, 0\ns_mov_b32 s43, 0\ns_mov_b32 s56, -2147483648\n.Ldd_next%=:\nbuffer_load_dword v8, v7, %[rs], 0 offen sc1\ns_lshl_b32 s58, s40, 1\n" \
"s_lshl_b32 s44, s40, 19\ns_add_u32 s44, s44, %[ho]\ns_lshr_b32 s45, s44, 8\ns_and_b32 s45, s45, 0x2000\ns_xor_b32 s55, s44, s45\ns_waitcnt vmcnt(0)\nbuffer_store_dword v9, off, %[rs], s56 sc1\n.Ldd_w0%=:\n" \
"v_cmp_gt_i32 vcc, s58, v8\ns_cbranch_vccz .Ldd_w0go%=\ns_sleep 16\nbuffer_load_dword v8, v7, %[rs], 0 offen sc1\ns_waitcnt vmcnt(0)\ns_add_u32 s43, s43, 1\ns_cmp_lt_u32 s43, 262144\ns_cbranch_scc1 .Ldd_w0%=\n" \
"s_branch .Ldd_abort%=\n.Ldd_w0go%=:\ns_add_u32 s60, s55, 0\nbuffer_load_dwordx4 v[48:51], v0, %[rs], s60 offen offset:0 sc1\nbuffer_load_dwordx4 v[52:55], v0, %[rs], s60 offen offset:2048 sc1\ns_add_u32 s60, s55, 4096\nbuffer_load_dwordx4 v[56:59], v0, %[rs], s60 offen offset:0 sc1\nbuffer_load_dwordx4 v[60:63], v0, %[rs], s60 offen offset:2048 sc1\n" \
"s_add_u32 s60, s55, 16384\nbuffer_load_dwordx4 v[64:67], v0, %[rs], s60 offen offset:0 sc1\nbuffer_load_dwordx4 v[68:71], v0, %[rs], s60 offen offset:2048 sc1\ns_add_u32 s60, s55, 20480\nbuffer_load_dwordx4 v[72:75], v0, %[rs], s60 offen offset:0 sc1\nbuffer_load_dwordx4 v[76:79], v0, %[rs], s60 offen offset:2048 sc1\ns_add_u32 s60, s55, 32768\nbuffer_load_dwordx4 v[80:83], v0, %[rs], s60 offen offset:0 sc1\n" \
"buffer_load_dwordx4 v[84:87], v0, %[rs], s60 offen offset:2048 sc1\ns_add_u32 s60, s55, 36864\nbuffer_load_dwordx4 v[88:91], v0, %[rs], s60 offen offset:0 sc1\nbuffer_load_dwordx4 v[92:95], v0, %[rs], s60 offen offset:2048 sc1\ns_add_u32 s60, s55, 49152\nbuffer_load_dwordx4 v[96:99], v0, %[rs], s60 offen offset:0 sc1\nbuffer_load_dwordx4 v[100:103], v0, %[rs], s60 offen offset:2048 sc1\ns_add_u32 s60, s55, 53248\n" \
"buffer_load_dwordx4 v[104:107], v0, %[rs], s60 offen offset:0 sc1\nbuffer_load_dwordx4 v[108:111], v0, %[rs], s60 offen offset:2048 sc1\ns_add_u32 s44, s40, 0\ns_lshl_b32 s44, s44, 17\ns_add_u32 s44, s44, %[xn]\ns_lshr_b32 s45, s44, 8\ns_and_b32 s45, s45, 0x2000\ns_xor_b32 s41, s44, s45\n" \
"s_lshl_b32 s44, s40, 16\ns_cmp_eq_u32 %[l], 0\ns_cselect_b32 s42, s41, s44\ns_add_u32 s58, s58, 1\nds_read_b128 v[120:123], v1 offset:0\ns_waitcnt vmcnt(15)\nv_mfma_f32_16x16x32_bf16 v[16:19], a[0:3], v[48:51], 0\nds_read_b128 v[124:127], v1 offset:1024\n" \
"v_mfma_f32_16x16x32_bf16 v[24:27], a[8:11], v[48:51], 0\nbuffer_load_dword v8, v7, %[rs], 0 offen sc1\nv_mfma_f32_16x16x32_bf16 v[32:35], a[16:19], v[48:51], 0\nv_mfma_f32_16x16x32_bf16 v[40:43], a[24:27], v[48:51], 0\ns_add_u32 s60, s55, 65536\ns_waitcnt vmcnt(15)\nbuffer_load_dwordx4 v[48:51], v0, %[rs], s60 offen offset:0 sc1\nv_mfma_f32_16x16x32_bf16 v[16:19], a[4:7], v[52:55], v[16:19]\n" \
"v_mfma_f32_16x16x32_bf16 v[24:27], a[12:15], v[52:55], v[24:27]\nv_mfma_f32_16x16x32_bf16 v[32:35], a[20:23], v[52:55], v[32:35]\nv_mfma_f32_16x16x32_bf16 v[40:43], a[28:31], v[52:55], v[40:43]\ns_waitcnt vmcnt(15)\nbuffer_load_dwordx4 v[52:55], v0, %[rs], s60 offen offset:2048 sc1\nv_mfma_f32_16x16x32_bf16 v[16:19], a[32:35], v[56:59], v[16:19]\ns_waitcnt lgkmcnt(1)\nv_mfma_f32_16x16x32_bf16 v[24:27], v[120:123], v[56:59], v[24:27]\n" \
"v_mfma_f32_16x16x32_bf16 v[32:35], a[40:43], v[56:59], v[32:35]\nv_mfma_f32_16x16x32_bf16 v[40:43], a[48:51], v[56:59], v[40:43]\ns_add_u32 s60, s55, 69632\ns_waitcnt vmcnt(15)\nbuffer_load_dwordx4 v[56:59], v0, %[rs], s60 offen offset:0 sc1\nv_mfma_f32_16x16x32_bf16 v[16:19], a[36:39], v[60:63], v[16:19]\ns_waitcnt lgkmcnt(0)\nds_read_b128 v[120:123], v1 offset:2048\n" \
"v_mfma_f32_16x16x32_bf16 v[24:27], v[124:127], v[60:63], v[24:27]\nv_mfma_f32_16x16x32_bf16 v[32:35], a[44:47], v[60:63], v[32:35]\nv_mfma_f32_16x16x32_bf16 v[40:43], a[52:55], v[60:63], v[40:43]\ns_waitcnt vmcnt(15)\nbuffer_load_dwordx4 v[60:63], v0, %[rs], s60 offen offset:2048 sc1\nv_mfma_f32_16x16x32_bf16 v[16:19], a[56:59], v[64:67], v[16:19]\nds_read_b128 v[124:127], v1 offset:3072\nv_mfma_f32_16x16x32_bf16 v[24:27], a[64:67], v[64:67], v[24:27]\n" \
"v_mfma_f32_16x16x32_bf16 v[32:35], a[72:75], v[64:67], v[32:35]\nv_mfma_f32_16x16x32_bf16 v[40:43], a[80:83], v[64:67], v[40:43]\ns_add_u32 s60, s55, 81920\ns_waitcnt vmcnt(15)\nbuffer_load_dwordx4 v[64:67], v0, %[rs], s60 offen offset:0 sc1\nv_mfma_f32_16x16x32_bf16 v[16:19], a[60:63], v[68:71], v[16:19]\nv_mfma_f32_16x16x32_bf16 v[24:27], a[68:71], v[68:71], v[24:27]\nv_mfma_f32_16x16x32_bf16 v[32:35], a[76:79], v[68:71], v[32:35]\n" \
"v_mfma_f32_16x16x32_bf16 v[40:43], a[84:87], v[68:71], v[40:43]\ns_waitcnt vmcnt(15)\nbuffer_load_dwordx4 v[68:71], v0, %[rs], s60 offen offset:2048 sc1\nv_mfma_f32_16x16x32_bf16 v[16:19], a[88:91], v[72:75], v[16:19]\nv_mfma_f32_16x16x32_bf16 v[24:27], a[96:99], v[72:75], v[24:27]\nv_mfma_f32_16x16x32_bf16 v[32:35], a[104:107], v[72:75], v[32:35]\ns_waitcnt lgkmcnt(1)\nv_mfma_f32_16x16x32_bf16 v[40:43], v[120:123], v[72:75], v[40:43]\n" \
"s_add_u32 s60, s55, 86016\ns_waitcnt vmcnt(15)\nbuffer_load_dwordx4 v[72:75], v0, %[rs], s60 offen offset:0 sc1\nv_mfma_f32_16x16x32_bf16 v[16:19], a[92:95], v[76:79], v[16:19]\nds_read_b128 v[120:123], v1 offset:4096\nv_mfma_f32_16x16x32_bf16 v[24:27], a[100:103], v[76:79], v[24:27]\nv_mfma_f32_16x16x32_bf16 v[32:35], a[108:111], v[76:79], v[32:35]\ns_waitcnt lgkmcnt(1)\n" \
"v_mfma_f32_16x16x32_bf16 v[40:43], v[124:127], v[76:79], v[40:43]\ns_waitcnt vmcnt(15)\nbuffer_load_dwordx4 v[76:79], v0, %[rs], s60 offen offset:2048 sc1\nv_mfma_f32_16x16x32_bf16 v[16:19], a[112:115], v[80:83], v[16:19]\nds_read_b128 v[124:127], v1 offset:5120\nv_mfma_f32_16x16x32_bf16 v[24:27], a[120:123], v[80:83], v[24:27]\nv_mfma_f32_16x16x32_bf16 v[32:35], a[128:131], v[80:83], v[32:35]\nv_mfma_f32_16x16x32_bf16 v[40:43], a[136:139], v[80:83], v[40:43]\n" \
"s_add_u32 s60, s55, 98304\ns_waitcnt vmcnt(15)\nbuffer_load_dwordx4 v[80:83], v0, %[rs], s60 offen offset:0 sc1\nv_mfma_f32_16x16x32_bf16 v[16:19], a[116:119], v[84:87], v[16:19]\nds_read_b128 v[112:115], v1 offset:6144\nv_mfma_f32_16x16x32_bf16 v[24:27], a[124:127], v[84:87], v[24:27]\nv_mfma_f32_16x16x32_bf16 v[32:35], a[132:135], v[84:87], v[32:35]\nv_mfma_f32_16x16x32_bf16 v[40:43], a[140:143], v[84:87], v[40:43]\n" \
"s_waitcnt vmcnt(15)\nbuffer_load_dwordx4 v[84:87], v0, %[rs], s60 offen offset:2048 sc1\nv_mfma_f32_16x16x32_bf16 v[16:19], a[144:147], v[88:91], v[16:19]\ns_waitcnt lgkmcnt(2)\nds_read_b128 v[116:119], v1 offset:7168\nv_mfma_f32_16x16x32_bf16 v[24:27], v[120:123], v[88:91], v[24:27]\nv_mfma_f32_16x16x32_bf16 v[32:35], a[152:155], v[88:91], v[32:35]\nv_mfma_f32_16x16x32_bf16 v[40:43], a[160:163], v[88:91], v[40:43]\n" \
"s_add_u32 s60, s55, 102400\ns_waitcnt vmcnt(15)\nbuffer_load_dwordx4 v[88:91], v0, %[rs], s60 offen offset:0 sc1\nv_mfma_f32_16x16x32_bf16 v[16:19], a[148:151], v[92:95], v[16:19]\ns_waitcnt lgkmcnt(2)\nv_mfma_f32_16x16x32_bf16 v[24:27], v[124:127], v[92:95], v[24:27]\ns_waitcnt vmcnt(11)\nv_cmp_gt_i32 vcc, s58, v8\n" \
"v_mfma_f32_16x16x32_bf16 v[32:35], a[156:159], v[92:95], v[32:35]\ns_cmp_eq_u64 vcc, 0\ns_cselect_b32 s61, s55, -2147483648\nv_mfma_f32_16x16x32_bf16 v[40:43], a[164:167], v[92:95], v[40:43]\nbuffer_load_dwordx4 v[92:95], v0, %[rs], s60 offen offset:2048 sc1\nv_mfma_f32_16x16x32_bf16 v[16:19], a[168:171], v[96:99], v[16:19]\nv_mfma_f32_16x16x32_bf16 v[24:27], a[176:179], v[96:99], v[24:27]\ns_waitcnt lgkmcnt(1)\n" \
"v_mfma_f32_16x16x32_bf16 v[32:35], v[112:115], v[96:99], v[32:35]\nv_mfma_f32_16x16x32_bf16 v[40:43], a[184:187], v[96:99], v[40:43]\ns_add_u32 s60, s61, 0\nbuffer_load_dwordx4 v[96:99], v0, %[rs], s60 offen offset:1024 sc1\nv_mfma_f32_16x16x32_bf16 v[16:19], a[172:175], v[100:103], v[16:19]\nds_read_b128 v[112:115], v1 offset:8192\nv_mfma_f32_16x16x32_bf16 v[24:27], a[180:183], v[100:103], v[24:27]\ns_waitcnt lgkmcnt(1)\n" \
"v_mfma_f32_16x16x32_bf16 v[32:35], v[116:119], v[100:103], v[32:35]\nv_mfma_f32_16x16x32_bf16 v[40:43], a[188:191], v[100:103], v[40:43]\nbuffer_load_dwordx4 v[100:103], v0, %[rs], s60 offen offset:3072 sc1\nv_mfma_f32_16x16x32_bf16 v[16:19], a[192:195], v[104:107], v[16:19]\nds_read_b128 v[116:119], v1 offset:9216\nv_mfma_f32_16x16x32_bf16 v[24:27], a[200:203], v[104:107], v[24:27]\nv_mfma_f32_16x16x32_bf16 v[32:35], a[208:211], v[104:107], v[32:35]\nv_mfma_f32_16x16x32_bf16 v[40:43], a[216:219], v[104:107], v[40:43]\n" \
"s_add_u32 s60, s61, 4096\nbuffer_load_dwordx4 v[104:107], v0, %[rs], s60 offen offset:1024 sc1\nv_mfma_f32_16x16x32_bf16 v[16:19], a[196:199], v[108:111], v[16:19]\nv_mfma_f32_16x16x32_bf16 v[24:27], a[204:207], v[108:111], v[24:27]\nv_mfma_f32_16x16x32_bf16 v[32:35], a[212:215], v[108:111], v[32:35]\nv_mfma_f32_16x16x32_bf16 v[40:43], a[220:223], v[108:111], v[40:43]\ns_waitcnt vmcnt(14)\ns_waitcnt lgkmcnt(1)\n" \
"buffer_load_dwordx4 v[108:111], v0, %[rs], s60 offen offset:3072 sc1\nv_mfma_f32_16x16x32_bf16 v[16:19], v[112:115], v[48:51], v[16:19]\nv_mfma_f32_16x16x32_bf16 v[24:27], a[224:227], v[48:51], v[24:27]\nv_mfma_f32_16x16x32_bf16 v[32:35], a[232:235], v[48:51], v[32:35]\nv_mfma_f32_16x16x32_bf16 v[40:43], a[240:243], v[48:51], v[40:43]\ns_add_u32 s60, s61, 16384\ns_waitcnt vmcnt(14)\ns_waitcnt lgkmcnt(0)\n" \
"buffer_load_dwordx4 v[48:51], v0, %[rs], s60 offen offset:1024 sc1\nv_mfma_f32_16x16x32_bf16 v[16:19], v[116:119], v[52:55], v[16:19]\nds_read_b128 v[112:115], v1 offset:10240\nv_mfma_f32_16x16x32_bf16 v[24:27], a[228:231], v[52:55], v[24:27]\nv_mfma_f32_16x16x32_bf16 v[32:35], a[236:239], v[52:55], v[32:35]\nv_mfma_f32_16x16x32_bf16 v[40:43], a[244:247], v[52:55], v[40:43]\ns_waitcnt vmcnt(14)\nbuffer_load_dwordx4 v[52:55], v0, %[rs], s60 offen offset:3072 sc1\n" \
"v_mfma_f32_16x16x32_bf16 v[16:19], a[248:251], v[56:59], v[16:19]\nds_read_b128 v[116:119], v1 offset:11264\nv_mfma_f32_16x16x32_bf16 v[24:27], v[128:131], v[56:59], v[24:27]\nv_mfma_f32_16x16x32_bf16 v[32:35], v[136:139], v[56:59], v[32:35]\nv_mfma_f32_16x16x32_bf16 v[40:43], v[144:147], v[56:59], v[40:43]\ns_add_u32 s60, s61, 20480\ns_waitcnt vmcnt(14)\nbuffer_load_dwordx4 v[56:59], v0, %[rs], s60 offen offset:1024 sc1\n" \
"v_mfma_f32_16x16x32_bf16 v[16:19], a[252:255], v[60:63], v[16:19]\nv_mfma_f32_16x16x32_bf16 v[24:27], v[132:135], v[60:63], v[24:27]\nv_mfma_f32_16x16x32_bf16 v[32:35], v[140:143], v[60:63], v[32:35]\nv_mfma_f32_16x16x32_bf16 v[40:43], v[148:151], v[60:63], v[40:43]\ns_waitcnt vmcnt(14)\nbuffer_load_dwordx4 v[60:63], v0, %[rs], s60 offen offset:3072 sc1\nv_mfma_f32_16x16x32_bf16 v[16:19], v[152:155], v[64:67], v[16:19]\nv_mfma_f32_16x16x32_bf16 v[24:27], v[160:163], v[64:67], v[24:27]\n" \
"s_waitcnt lgkmcnt(1)\nv_mfma_f32_16x16x32_bf16 v[32:35], v[112:115], v[64:67], v[32:35]\nv_mfma_f32_16x16x32_bf16 v[40:43], v[168:171], v[64:67], v[40:43]\ns_add_u32 s60, s61, 32768\ns_waitcnt vmcnt(14)\nbuffer_load_dwordx4 v[64:67], v0, %[rs], s60 offen offset:1024 sc1\nv_mfma_f32_16x16x32_bf16 v[16:19], v[156:159], v[68:71], v[16:19]\nds_read_b128 v[112:115], v1 offset:12288\n" \
"v_mfma_f32_16x16x32_bf16 v[24:27], v[164:167], v[68:71], v[24:27]\ns_waitcnt lgkmcnt(1)\nv_mfma_f32_16x16x32_bf16 v[32:35], v[116:119], v[68:71], v[32:35]\nv_mfma_f32_16x16x32_bf16 v[40:43], v[172:175], v[68:71], v[40:43]\ns_waitcnt vmcnt(14)\nbuffer_load_dwordx4 v[68:71], v0, %[rs], s60 offen offset:3072 sc1\nv_mfma_f32_16x16x32_bf16 v[16:19], v[176:179], v[72:75], v[16:19]\nds_read_b128 v[116:119], v1 offset:13312\n" \
"v_mfma_f32_16x16x32_bf16 v[24:27], v[184:187], v[72:75], v[24:27]\nv_mfma_f32_16x16x32_bf16 v[32:35], v[192:195], v[72:75], v[32:35]\nv_mfma_f32_16x16x32_bf16 v[40:43], v[200:203], v[72:75], v[40:43]\ns_add_u32 s60, s61, 36864\ns_waitcnt vmcnt(14)\nbuffer_load_dwordx4 v[72:75], v0, %[rs], s60 offen offset:1024 sc1\nv_mfma_f32_16x16x32_bf16 v[16:19], v[180:183], v[76:79], v[16:19]\nds_read_b128 v[120:123], v1 offset:14336\n" \
"v_mfma_f32_16x16x32_bf16 v[24:27], v[188:191], v[76:79], v[24:27]\nv_mfma_f32_16x16x32_bf16 v[32:35], v[196:199], v[76:79], v[32:35]\nv_mfma_f32_16x16x32_bf16 v[40:43], v[204:207], v[76:79], v[40:43]\ns_waitcnt vmcnt(14)\ns_waitcnt lgkmcnt(2)\nbuffer_load_dwordx4 v[76:79], v0, %[rs], s60 offen offset:3072 sc1\nv_mfma_f32_16x16x32_bf16 v[16:19], v[112:115], v[80:83], v[16:19]\nds_read_b128 v[124:127], v1 offset:15360\n" \
"v_mfma_f32_16x16x32_bf16 v[24:27], v[208:211], v[80:83], v[24:27]\nv_mfma_f32_16x16x32_bf16 v[32:35], v[216:219], v[80:83], v[32:35]\nv_mfma_f32_16x16x32_bf16 v[40:43], v[224:227], v[80:83], v[40:43]\ns_add_u32 s60, s61, 49152\ns_waitcnt vmcnt(14)\ns_waitcnt lgkmcnt(2)\nbuffer_load_dwordx4 v[80:83], v0, %[rs], s60 offen offset:1024 sc1\nv_mfma_f32_16x16x32_bf16 v[16:19], v[116:119], v[84:87], v[16:19]\n" \
"v_mfma_f32_16x16x32_bf16 v[24:27], v[212:215], v[84:87], v[24:27]\nv_mfma_f32_16x16x32_bf16 v[32:35], v[220:223], v[84:87], v[32:35]\nv_mfma_f32_16x16x32_bf16 v[40:43], v[228:231], v[84:87], v[40:43]\ns_waitcnt vmcnt(14)\nbuffer_load_dwordx4 v[84:87], v0, %[rs], s60 offen offset:3072 sc1\nv_mfma_f32_16x16x32_bf16 v[16:19], v[232:235], v[88:91], v[16:19]\ns_waitcnt lgkmcnt(1)\nv_mfma_f32_16x16x32_bf16 v[24:27], v[120:123], v[88:91], v[24:27]\n" \
"v_mfma_f32_16x16x32_bf16 v[32:35], v[240:243], v[88:91], v[32:35]\nv_mfma_f32_16x16x32_bf16 v[40:43], v[248:251], v[88:91], v[40:43]\ns_add_u32 s60, s61, 53248\ns_waitcnt vmcnt(14)\nbuffer_load_dwordx4 v[88:91], v0, %[rs], s60 offen offset:1024 sc1\nv_mfma_f32_16x16x32_bf16 v[16:19], v[236:239], v[92:95], v[16:19]\ns_waitcnt lgkmcnt(0)\nds_read_b128 v[120:123], v1 offset:0\n" \
"v_mfma_f32_16x16x32_bf16 v[24:27], v[124:127], v[92:95], v[24:27]\nv_mfma_f32_16x16x32_bf16 v[32:35], v[244:247], v[92:95], v[32:35]\nv_mfma_f32_16x16x32_bf16 v[40:43], v[252:255], v[92:95], v[40:43]\nbuffer_load_dwordx4 v[92:95], v0, %[rs], s60 offen offset:3072 sc1\ns_cbranch_vccnz .Ldd_slow%=\n.Ldd_slowr%=:\ns_waitcnt vmcnt(15)\nv_mfma_f32_16x16x32_bf16 v[20:23], a[0:3], v[96:99], 0\n" \
"ds_read_b128 v[124:127], v1 offset:1024\nv_mfma_f32_16x16x32_bf16 v[28:31], a[8:11], v[96:99], 0\nv_mfma_f32_16x16x32_bf16 v[36:39], a[16:19], v[96:99], 0\nv_mfma_f32_16x16x32_bf16 v[44:47], a[24:27], v[96:99], 0\ns_add_u32 s60, s55, 65536\ns_waitcnt vmcnt(14)\nbuffer_load_dwordx4 v[96:99], v0, %[rs], s60 offen offset:1024 sc1\nv_mfma_f32_16x16x32_bf16 v[20:23], a[4:7], v[100:103], v[20:23]\n" \
"v_mfma_f32_16x16x32_bf16 v[28:31], a[12:15], v[100:103], v[28:31]\nv_mfma_f32_16x16x32_bf16 v[36:39], a[20:23], v[100:103], v[36:39]\nv_mfma_f32_16x16x32_bf16 v[44:47], a[28:31], v[100:103], v[44:47]\ns_waitcnt vmcnt(14)\nbuffer_load_dwordx4 v[100:103], v0, %[rs], s60 offen offset:3072 sc1\nv_mfma_f32_16x16x32_bf16 v[20:23], a[32:35], v[104:107], v[20:23]\ns_waitcnt lgkmcnt(1)\nv_mfma_f32_16x16x32_bf16 v[28:31], v[120:123], v[104:107], v[28:31]\n" \
"ds_write_b128 v2, v[16:19] offset:0\nv_mfma_f32_16x16x32_bf16 v[36:39], a[40:43], v[104:107], v[36:39]\nv_mfma_f32_16x16x32_bf16 v[44:47], a[48:51], v[104:107], v[44:47]\ns_add_u32 s60, s55, 69632\ns_waitcnt vmcnt(14)\nbuffer_load_dwordx4 v[104:107], v0, %[rs], s60 offen offset:1024 sc1\nv_mfma_f32_16x16x32_bf16 v[20:23], a[36:39], v[108:111], v[20:23]\ns_waitcnt lgkmcnt(1)\n" \
"ds_read_b128 v[120:123], v1 offset:2048\nv_mfma_f32_16x16x32_bf16 v[28:31], v[124:127], v[108:111], v[28:31]\nv_mfma_f32_16x16x32_bf16 v[36:39], a[44:47], v[108:111], v[36:39]\nv_mfma_f32_16x16x32_bf16 v[44:47], a[52:55], v[108:111], v[44:47]\ns_waitcnt vmcnt(14)\nbuffer_load_dwordx4 v[108:111], v0, %[rs], s60 offen offset:3072 sc1\nv_mfma_f32_16x16x32_bf16 v[20:23], a[56:59], v[48:51], v[20:23]\nds_read_b128 v[124:127], v1 offset:3072\n" \
"v_mfma_f32_16x16x32_bf16 v[28:31], a[64:67], v[48:51], v[28:31]\nds_write_b128 v2, v[24:27] offset:2048\nv_mfma_f32_16x16x32_bf16 v[36:39], a[72:75], v[48:51], v[36:39]\nv_mfma_f32_16x16x32_bf16 v[44:47], a[80:83], v[48:51], v[44:47]\ns_add_u32 s60, s55, 81920\ns_waitcnt vmcnt(14)\nbuffer_load_dwordx4 v[48:51], v0, %[rs], s60 offen offset:1024 sc1\nv_mfma_f32_16x16x32_bf16 v[20:23], a[60:63], v[52:55], v[20:23]\n" \
"v_mfma_f32_16x16x32_bf16 v[28:31], a[68:71], v[52:55], v[28:31]\nv_mfma_f32_16x16x32_bf16 v[36:39], a[76:79], v[52:55], v[36:39]\nv_mfma_f32_16x16x32_bf16 v[44:47], a[84:87], v[52:55], v[44:47]\ns_waitcnt vmcnt(14)\nbuffer_load_dwordx4 v[52:55], v0, %[rs], s60 offen offset:3072 sc1\nv_mfma_f32_16x16x32_bf16 v[20:23], a[88:91], v[56:59], v[20:23]\nv_mfma_f32_16x16x32_bf16 v[28:31], a[96:99], v[56:59], v[28:31]\nds_write_b128 v2, v[32:35] offset:4096\n" \
"v_mfma_f32_16x16x32_bf16 v[36:39], a[104:107], v[56:59], v[36:39]\ns_waitcnt lgkmcnt(3)\nv_mfma_f32_16x16x32_bf16 v[44:47], v[120:123], v[56:59], v[44:47]\ns_add_u32 s60, s55, 86016\ns_waitcnt vmcnt(14)\nbuffer_load_dwordx4 v[56:59], v0, %[rs], s60 offen offset:1024 sc1\nv_mfma_f32_16x16x32_bf16 v[20:23], a[92:95], v[60:63], v[20:23]\nds_read_b128 v[120:123], v1 offset:4096\n" \
"v_mfma_f32_16x16x32_bf16 v[28:31], a[100:103], v[60:63], v[28:31]\nv_mfma_f32_16x16x32_bf16 v[36:39], a[108:111], v[60:63], v[36:39]\ns_waitcnt lgkmcnt(3)\nv_mfma_f32_16x16x32_bf16 v[44:47], v[124:127], v[60:63], v[44:47]\ns_waitcnt vmcnt(14)\nbuffer_load_dwordx4 v[60:63], v0, %[rs], s60 offen offset:3072 sc1\nv_mfma_f32_16x16x32_bf16 v[20:23], a[112:115], v[64:67], v[20:23]\nds_read_b128 v[124:127], v1 offset:5120\n" \
"v_mfma_f32_16x16x32_bf16 v[28:31], a[120:123], v[64:67], v[28:31]\nds_write_b128 v2, v[40:43] offset:6144\nv_mfma_f32_16x16x32_bf16 v[36:39], a[128:131], v[64:67], v[36:39]\nv_mfma_f32_16x16x32_bf16 v[44:47], a[136:139], v[64:67], v[44:47]\ns_add_u32 s60, s55, 98304\ns_waitcnt vmcnt(14)\nbuffer_load_dwordx4 v[64:67], v0, %[rs], s60 offen offset:1024 sc1\nv_mfma_f32_16x16x32_bf16 v[20:23], a[116:119], v[68:71], v[20:23]\n" \
"ds_read_b128 v[112:115], v1 offset:6144\nv_mfma_f32_16x16x32_bf16 v[28:31], a[124:127], v[68:71], v[28:31]\nv_mfma_f32_16x16x32_bf16 v[36:39], a[132:135], v[68:71], v[36:39]\nv_mfma_f32_16x16x32_bf16 v[44:47], a[140:143], v[68:71], v[44:47]\ns_waitcnt vmcnt(14)\nbuffer_load_dwordx4 v[68:71], v0, %[rs], s60 offen offset:3072 sc1\nv_mfma_f32_16x16x32_bf16 v[20:23], a[144:147], v[72:75], v[20:23]\ns_waitcnt lgkmcnt(3)\n" \
"ds_read_b128 v[116:119], v1 offset:7168\nv_mfma_f32_16x16x32_bf16 v[28:31], v[120:123], v[72:75], v[28:31]\nv_mfma_f32_16x16x32_bf16 v[36:39], a[152:155], v[72:75], v[36:39]\nv_mfma_f32_16x16x32_bf16 v[44:47], a[160:163], v[72:75], v[44:47]\ns_add_u32 s60, s55, 102400\ns_waitcnt vmcnt(14)\nbuffer_load_dwordx4 v[72:75], v0, %[rs], s60 offen offset:1024 sc1\nv_mfma_f32_16x16x32_bf16 v[20:23], a[148:151], v[76:79], v[20:23]\n" \
"s_waitcnt lgkmcnt(3)\nv_mfma_f32_16x16x32_bf16 v[28:31], v[124:127], v[76:79], v[28:31]\nv_mfma_f32_16x16x32_bf16 v[36:39], a[156:159], v[76:79], v[36:39]\nv_mfma_f32_16x16x32_bf16 v[44:47], a[164:167], v[76:79], v[44:47]\ns_waitcnt vmcnt(14)\nbuffer_load_dwordx4 v[76:79], v0, %[rs], s60 offen offset:3072 sc1\nv_mfma_f32_16x16x32_bf16 v[20:23], a[168:171], v[80:83], v[20:23]\nv_mfma_f32_16x16x32_bf16 v[28:31], a[176:179], v[80:83], v[28:31]\n" \
"s_waitcnt lgkmcnt(1)\nv_mfma_f32_16x16x32_bf16 v[36:39], v[112:115], v[80:83], v[36:39]\nv_mfma_f32_16x16x32_bf16 v[44:47], a[184:187], v[80:83], v[44:47]\ns_waitcnt vmcnt(14)\nbuffer_load_dwordx4 v[12:15], v4, %[rs], s41 offen sc1\nv_mfma_f32_16x16x32_bf16 v[20:23], a[172:175], v[84:87], v[20:23]\nds_read_b128 v[112:115], v1 offset:8192\nv_mfma_f32_16x16x32_bf16 v[28:31], a[180:183], v[84:87], v[28:31]\n" \
"s_waitcnt lgkmcnt(1)\nv_mfma_f32_16x16x32_bf16 v[36:39], v[116:119], v[84:87], v[36:39]\nv_mfma_f32_16x16x32_bf16 v[44:47], a[188:191], v[84:87], v[44:47]\ns_waitcnt vmcnt(14)\nv_mfma_f32_16x16x32_bf16 v[20:23], a[192:195], v[88:91], v[20:23]\nds_read_b128 v[116:119], v1 offset:9216\nv_mfma_f32_16x16x32_bf16 v[28:31], a[200:203], v[88:91], v[28:31]\nv_mfma_f32_16x16x32_bf16 v[36:39], a[208:211], v[88:91], v[36:39]\n" \
"v_mfma_f32_16x16x32_bf16 v[44:47], a[216:219], v[88:91], v[44:47]\ns_waitcnt vmcnt(13)\nv_mfma_f32_16x16x32_bf16 v[20:23], a[196:199], v[92:95], v[20:23]\nv_mfma_f32_16x16x32_bf16 v[28:31], a[204:207], v[92:95], v[28:31]\nv_mfma_f32_16x16x32_bf16 v[36:39], a[212:215], v[92:95], v[36:39]\nv_mfma_f32_16x16x32_bf16 v[44:47], a[220:223], v[92:95], v[44:47]\ns_waitcnt vmcnt(12)\ns_waitcnt lgkmcnt(1)\n" \
"v_mfma_f32_16x16x32_bf16 v[20:23], v[112:115], v[96:99], v[20:23]\nv_mfma_f32_16x16x32_bf16 v[28:31], a[224:227], v[96:99], v[28:31]\nv_mfma_f32_16x16x32_bf16 v[36:39], a[232:235], v[96:99], v[36:39]\nv_mfma_f32_16x16x32_bf16 v[44:47], a[240:243], v[96:99], v[44:47]\ns_waitcnt vmcnt(11)\ns_waitcnt lgkmcnt(0)\nv_mfma_f32_16x16x32_bf16 v[20:23], v[116:119], v[100:103], v[20:23]\nds_read_b128 v[112:115], v1 offset:10240\n" \
"v_mfma_f32_16x16x32_bf16 v[28:31], a[228:231], v[100:103], v[28:31]\nv_mfma_f32_16x16x32_bf16 v[36:39], a[236:239], v[100:103], v[36:39]\nv_mfma_f32_16x16x32_bf16 v[44:47], a[244:247], v[100:103], v[44:47]\ns_waitcnt vmcnt(10)\nv_mfma_f32_16x16x32_bf16 v[20:23], a[248:251], v[104:107], v[20:23]\nds_read_b128 v[116:119], v1 offset:11264\nv_mfma_f32_16x16x32_bf16 v[28:31], v[128:131], v[104:107], v[28:31]\nv_mfma_f32_16x16x32_bf16 v[36:39], v[136:139], v[104:107], v[36:39]\n" \
"v_mfma_f32_16x16x32_bf16 v[44:47], v[144:147], v[104:107], v[44:47]\ns_waitcnt vmcnt(9)\nv_mfma_f32_16x16x32_bf16 v[20:23], a[252:255], v[108:111], v[20:23]\nv_mfma_f32_16x16x32_bf16 v[28:31], v[132:135], v[108:111], v[28:31]\nv_mfma_f32_16x16x32_bf16 v[36:39], v[140:143], v[108:111], v[36:39]\nv_mfma_f32_16x16x32_bf16 v[44:47], v[148:151], v[108:111], v[44:47]\ns_waitcnt vmcnt(8)\nv_mfma_f32_16x16x32_bf16 v[20:23], v[152:155], v[48:51], v[20:23]\n" \
"v_mfma_f32_16x16x32_bf16 v[28:31], v[160:163], v[48:51], v[28:31]\ns_waitcnt lgkmcnt(1)\nv_mfma_f32_16x16x32_bf16 v[36:39], v[112:115], v[48:51], v[36:39]\nv_mfma_f32_16x16x32_bf16 v[44:47], v[168:171], v[48:51], v[44:47]\ns_waitcnt vmcnt(7)\nv_mfma_f32_16x16x32_bf16 v[20:23], v[156:159], v[52:55], v[20:23]\nds_read_b128 v[112:115], v1 offset:12288\nv_mfma_f32_16x16x32_bf16 v[28:31], v[164:167], v[52:55], v[28:31]\n" \
"s_waitcnt lgkmcnt(1)\nv_mfma_f32_16x16x32_bf16 v[36:39], v[116:119], v[52:55], v[36:39]\nv_mfma_f32_16x16x32_bf16 v[44:47], v[172:175], v[52:55], v[44:47]\ns_waitcnt vmcnt(6)\nv_mfma_f32_16x16x32_bf16 v[20:23], v[176:179], v[56:59], v[20:23]\nds_read_b128 v[116:119], v1 offset:13312\nv_mfma_f32_16x16x32_bf16 v[28:31], v[184:187], v[56:59], v[28:31]\nv_mfma_f32_16x16x32_bf16 v[36:39], v[192:195], v[56:59], v[36:39]\n" \
"v_mfma_f32_16x16x32_bf16 v[44:47], v[200:203], v[56:59], v[44:47]\ns_waitcnt vmcnt(5)\nv_mfma_f32_16x16x32_bf16 v[20:23], v[180:183], v[60:63], v[20:23]\nds_read_b128 v[120:123], v1 offset:14336\nv_mfma_f32_16x16x32_bf16 v[28:31], v[188:191], v[60:63], v[28:31]\nv_mfma_f32_16x16x32_bf16 v[36:39], v[196:199], v[60:63], v[36:39]\nv_mfma_f32_16x16x32_bf16 v[44:47], v[204:207], v[60:63], v[44:47]\ns_waitcnt vmcnt(4)\n" \
"s_waitcnt lgkmcnt(2)\nv_mfma_f32_16x16x32_bf16 v[20:23], v[112:115], v[64:67], v[20:23]\nds_read_b128 v[124:127], v1 offset:15360\nv_mfma_f32_16x16x32_bf16 v[28:31], v[208:211], v[64:67], v[28:31]\nv_mfma_f32_16x16x32_bf16 v[36:39], v[216:219], v[64:67], v[36:39]\nv_mfma_f32_16x16x32_bf16 v[44:47], v[224:227], v[64:67], v[44:47]\ns_waitcnt vmcnt(3)\ns_waitcnt lgkmcnt(2)\n" \
"v_mfma_f32_16x16x32_bf16 v[20:23], v[116:119], v[68:71], v[20:23]\nv_mfma_f32_16x16x32_bf16 v[28:31], v[212:215], v[68:71], v[28:31]\nv_mfma_f32_16x16x32_bf16 v[36:39], v[220:223], v[68:71], v[36:39]\nv_mfma_f32_16x16x32_bf16 v[44:47], v[228:231], v[68:71], v[44:47]\ns_waitcnt vmcnt(2)\nv_mfma_f32_16x16x32_bf16 v[20:23], v[232:235], v[72:75], v[20:23]\ns_waitcnt lgkmcnt(1)\nv_mfma_f32_16x16x32_bf16 v[28:31], v[120:123], v[72:75], v[28:31]\n" \
"v_mfma_f32_16x16x32_bf16 v[36:39], v[240:243], v[72:75], v[36:39]\nv_mfma_f32_16x16x32_bf16 v[44:47], v[248:251], v[72:75], v[44:47]\ns_waitcnt vmcnt(1)\nv_mfma_f32_16x16x32_bf16 v[20:23], v[236:239], v[76:79], v[20:23]\ns_waitcnt vmcnt(0)\ns_waitcnt lgkmcnt(0)\nv_lshlrev_b32 v80, 16, v12\nv_and_b32 v81, 0xffff0000, v12\n" \
"v_mfma_f32_16x16x32_bf16 v[28:31], v[124:127], v[76:79], v[28:31]\nv_lshlrev_b32 v82, 16, v13\nv_and_b32 v83, 0xffff0000, v13\nv_mfma_f32_16x16x32_bf16 v[36:39], v[244:247], v[76:79], v[36:39]\nv_lshlrev_b32 v84, 16, v14\nv_and_b32 v85, 0xffff0000, v14\nv_mfma_f32_16x16x32_bf16 v[44:47], v[252:255], v[76:79], v[44:47]\ns_mul_i32 s44, s40, %[l]\n" \
"ds_write_b128 v2, v[20:23] offset:1024\nds_write_b128 v2, v[28:31] offset:3072\ns_nop 0\nds_write_b128 v2, v[36:39] offset:5120\ns_nop 2\nds_write_b128 v2, v[44:47] offset:7168\nv_lshlrev_b32 v86, 16, v15\nv_and_b32 v87, 0xffff0000, v15\n" \
"v_mov_b32 v9, s40\ns_cmp_eq_u32 s44, 0\ns_cselect_b32 s56, %[fd], -2147483648\ns_add_u32 s40, s40, 1\ns_waitcnt lgkmcnt(0)\ns_barrier\nds_read_b128 v[16:19], v3 offset:0\nds_read_b128 v[20:23], v3 offset:256\n" \
"ds_read_b128 v[24:27], v3 offset:8192\nds_read_b128 v[28:31], v3 offset:8448\nds_read_b128 v[32:35], v3 offset:16384\nds_read_b128 v[36:39], v3 offset:16640\nds_read_b128 v[40:43], v3 offset:24576\nds_read_b128 v[44:47], v3 offset:24832\ns_waitcnt lgkmcnt(6)\nv_pk_add_f32 v[16:17], v[16:17], v[80:81]\n" \
"v_pk_add_f32 v[18:19], v[18:19], v[82:83]\nv_pk_add_f32 v[20:21], v[20:21], v[84:85]\nv_pk_add_f32 v[22:23], v[22:23], v[86:87]\ns_waitcnt lgkmcnt(4)\nv_pk_add_f32 v[16:17], v[16:17], v[24:25]\nv_pk_add_f32 v[18:19], v[18:19], v[26:27]\nv_pk_add_f32 v[20:21], v[20:21], v[28:29]\nv_pk_add_f32 v[22:23], v[22:23], v[30:31]\n" \
"s_waitcnt lgkmcnt(2)\nv_pk_add_f32 v[16:17], v[16:17], v[32:33]\nv_pk_add_f32 v[18:19], v[18:19], v[34:35]\nv_pk_add_f32 v[20:21], v[20:21], v[36:37]\nv_pk_add_f32 v[22:23], v[22:23], v[38:39]\ns_waitcnt lgkmcnt(0)\nv_pk_add_f32 v[16:17], v[16:17], v[40:41]\nv_pk_add_f32 v[18:19], v[18:19], v[42:43]\n" \
"v_pk_add_f32 v[20:21], v[20:21], v[44:45]\nv_pk_add_f32 v[22:23], v[22:23], v[46:47]\nv_cvt_pk_bf16_f32 v12, v16, v17\nv_cvt_pk_bf16_f32 v13, v18, v19\nv_cvt_pk_bf16_f32 v14, v20, v21\nv_cvt_pk_bf16_f32 v15, v22, v23\nbuffer_store_dwordx4 v[12:15], v5, %[ro], s42 offen sc1\nv_pk_add_f32 v[24:25], v[16:17], v[18:19]\n" \
"v_pk_add_f32 v[26:27], v[20:21], v[22:23]\nv_pk_add_f32 v[24:25], v[24:25], v[26:27]\nv_add_f32 v24, v24, v25\ns_nop 1\nv_add_f32_dpp v25, v24, v24 row_ror:8 row_mask:0xf bank_mask:0xf\nv_add_f32_dpp v26, v24, v24 row_ror:8 row_mask:0xf bank_mask:0xf\ns_nop 1\nv_permlane16_swap_b32 v25, v26\n" \
"v_add_f32 v24, v25, v26\nv_add_f32 v25, v25, v26\ns_nop 1\nv_permlane32_swap_b32 v24, v25\nv_add_f32 v24, v24, v25\nv_mul_f32 v24, 0x3c800000, v24\nv_pk_add_f32 v[16:17], v[16:17], v[24:25] op_sel_hi:[1,0] neg_lo:[0,1] neg_hi:[0,1]\nv_pk_add_f32 v[18:19], v[18:19], v[24:25] op_sel_hi:[1,0] neg_lo:[0,1] neg_hi:[0,1]\n" \
"v_pk_add_f32 v[20:21], v[20:21], v[24:25] op_sel_hi:[1,0] neg_lo:[0,1] neg_hi:[0,1]\nv_pk_add_f32 v[22:23], v[22:23], v[24:25] op_sel_hi:[1,0] neg_lo:[0,1] neg_hi:[0,1]\nv_pk_mul_f32 v[28:29], v[16:17], v[16:17]\nv_pk_mul_f32 v[30:31], v[18:19], v[18:19]\nv_pk_fma_f32 v[28:29], v[20:21], v[20:21], v[28:29]\nv_pk_fma_f32 v[30:31], v[22:23], v[22:23], v[30:31]\nv_pk_add_f32 v[28:29], v[28:29], v[30:31]\nv_add_f32 v28, v28, v29\n" \
"s_nop 1\nv_add_f32_dpp v29, v28, v28 row_ror:8 row_mask:0xf bank_mask:0xf\nv_add_f32_dpp v30, v28, v28 row_ror:8 row_mask:0xf bank_mask:0xf\ns_nop 1\nv_permlane16_swap_b32 v29, v30\nv_add_f32 v28, v29, v30\nv_add_f32 v29, v29, v30\ns_nop 1\n" \
"v_permlane32_swap_b32 v28, v29\nv_add_f32 v28, v28, v29\nv_mov_b32 v25, v28\nbuffer_store_dwordx2 v[24:25], v6, %[rs], s41 offen sc1\nv_xor_b32 v2, 0x8000, v2\nv_xor_b32 v3, 0x8000, v3\ns_cmp_lt_u32 s40, 64\ns_cbranch_scc1 .Ldd_next%=\n" \
"s_waitcnt vmcnt(0)\nbuffer_store_dword v9, off, %[rs], s56 sc1\ns_branch .Ldd_end%=\n.Ldd_slow%=:\n.Ldd_w1%=:\nv_cmp_gt_i32 vcc, s58, v8\ns_cbranch_vccz .Ldd_w1go%=\ns_sleep 16\n" \
"buffer_load_dword v8, v7, %[rs], 0 offen sc1\ns_waitcnt vmcnt(0)\ns_add_u32 s43, s43, 1\ns_cmp_lt_u32 s43, 262144\ns_cbranch_scc1 .Ldd_w1%=\ns_branch .Ldd_abort%=\n.Ldd_w1go%=:\ns_add_u32 s60, s55, 0\n" \
"buffer_load_dwordx4 v[96:99], v0, %[rs], s60 offen offset:1024 sc1\nbuffer_load_dwordx4 v[100:103], v0, %[rs], s60 offen offset:3072 sc1\ns_add_u32 s60, s55, 4096\nbuffer_load_dwordx4 v[104:107], v0, %[rs], s60 offen offset:1024 sc1\nbuffer_load_dwordx4 v[108:111], v0, %[rs], s60 offen offset:3072 sc1\ns_add_u32 s60, s55, 16384\nbuffer_load_dwordx4 v[48:51], v0, %[rs], s60 offen offset:1024 sc1\nbuffer_load_dwordx4 v[52:55], v0, %[rs], s60 offen offset:3072 sc1\n" \
"s_add_u32 s60, s55, 20480\nbuffer_load_dwordx4 v[56:59], v0, %[rs], s60 offen offset:1024 sc1\nbuffer_load_dwordx4 v[60:63], v0, %[rs], s60 offen offset:3072 sc1\ns_add_u32 s60, s55, 32768\nbuffer_load_dwordx4 v[64:67], v0, %[rs], s60 offen offset:1024 sc1\nbuffer_load_dwordx4 v[68:71], v0, %[rs], s60 offen offset:3072 sc1\ns_add_u32 s60, s55, 36864\nbuffer_load_dwordx4 v[72:75], v0, %[rs], s60 offen offset:1024 sc1\n" \
"buffer_load_dwordx4 v[76:79], v0, %[rs], s60 offen offset:3072 sc1\ns_add_u32 s60, s55, 49152\nbuffer_load_dwordx4 v[80:83], v0, %[rs], s60 offen offset:1024 sc1\nbuffer_load_dwordx4 v[84:87], v0, %[rs], s60 offen offset:3072 sc1\ns_add_u32 s60, s55, 53248\nbuffer_load_dwordx4 v[88:91], v0, %[rs], s60 offen offset:1024 sc1\nbuffer_load_dwordx4 v[92:95], v0, %[rs], s60 offen offset:3072 sc1\ns_branch .Ldd_slowr%=\n" \
".Ldd_abort%=:\ns_mov_b64 exec, -1\nv_mov_b32 v16, 0\ns_movk_i32 s44, 6400\nbuffer_store_dword v16, off, %[rs], s44 sc1\n.Ldd_end%=:\ns_waitcnt vmcnt(0) lgkmcnt(0)\n" \
""

#define V10(p, x) p #x "0", p #x "1", p #x "2", p #x "3", p #x "4", p #x "5", p #x "6", p #x "7", p #x "8", p #x "9"
#define V256(p) V10(p, ), V10(p, 1), V10(p, 2), V10(p, 3), V10(p, 4), V10(p, 5), V10(p, 6), V10(p, 7), V10(p, 8), V10(p, 9), V10(p, 10), V10(p, 11), V10(p, 12), V10(p, 13), V10(p, 14), V10(p, 15), V10(p, 16), \
    V10(p, 17), V10(p, 18), V10(p, 19), V10(p, 20), V10(p, 21), V10(p, 22), V10(p, 23), V10(p, 24), p "250", p "251", p "252", p "253", p "254", p "255"
#define CLOB "memory", "vcc", "scc", V256("v"), V256("a"), V10("s", 4), V10("s", 5), V10("s", 6), V10("s", 7), V10("s", 8)
template <int N> DEVI void table(lds_char* p, const unsigned (&t)[N]) {
#pragma unroll
    for (int i = 0; i < N; ++i) *(l1*)(p + i * 256) = t[i];
    WL0;
}
DEVI unsigned prof_at(int cu, int w) { return O_PROF + (262144 + cu * 2560 + w * 8) * 4; }
DEVI unsigned sp_idx(int lane) { return (lane & 2) ? 0xEEEEu : 0x4444u; }
DEVI int sp_row(int r, int hf) { return 4 * (r >> 1) + 2 * hf + (r & 1); }

DEVI void qk_role(const Args& a, const int w, lds_char* smem, int l, int h) {
    const int lane = lane_id(), g = lane >> 4, n = lane & 15, b = n & 3, p = n >> 2;
    const unsigned l0 = (unsigned)(size_t)smem, tb = l0 + w * 12288, lw = l0 + 55296 + w * 24576, ls = l * SD, IMG = l0 + 32768, MIM = l0 + 49152;
    const unsigned xw = (w >> 1) * 16384 + (w & 1) * 4096, oc = (h >> 3) * 16384 + ls, tr = (8 * g + p) * 128 + b * 16;
    const unsigned cm[4] = {4 * g > n ? 0xF149F2CAu : 0u, 4 * g + 1 > n ? 0xF149F2CAu : 0u, 4 * g + 2 > n ? 0xF149F2CAu : 0u, 4 * g + 3 > n ? 0xF149F2CAu : 0u};
    const unsigned t[19] = {
        l0 + lane * 16, lw + lane * 16, l0 + (g >> 1) * 1024 + (g & 1) * 512 + n * 16, O_E1 + ls + xw + lane * 16, sp_idx(lane),
        IMG + (w >> 1) * 8192 + ((w & 1) * 8 + 2 * g) * 512 + n * 16,
        MIM + (w * 8 + 2 * p) * 144 + g * 32 + b * 4,
        IMG + g * 512 + (w ? n * 16 : 8192 + (16 * (b >> 1) + 8 * (p >> 1) + 2 * (p & 1) + (b & 1)) * 16),
        MIM + g * 144 + (n & 7) * 16, MIM + 2304 + g * 144 + (2 * p + (b & 1)) * 16,
        w ? O_PT + (h >> 3) * 32768 + ls + lane * 16 + (w == 1 ? 16384 + ((h & 7) >> 1) * 1024 + (h & 1) * 8 : (h & 7) * 1024)
          : O_PM + oc + ((h >> 1) & 3) * 1024 + ((2 * (h & 1) + (g >> 1)) * 16 + sp_row(n & 7, g & 1)) * 16 + (n >> 3) * 8,
        w ? O_PT + (h >> 3) * 32768 + ls + 20480 + ((h >> 2) & 1) * 2048 + (w - 1) * 1024 + (h & 3) * 256 + n * 16 + g * 4
          : n >> 3 ? OOB : O_PM + oc + 4096 + ((h & 7) >> 1) * 256 + sp_row(n & 7, g & 1) * 16 + (h & 1) * 8 + (g >> 1) * 4,
        w == 3 ? (l ? F_X + SD + p * 128 + b * 4 : F_X0 + b * 4) : cm[0], cm[1], cm[2], cm[3],
        (lane >> 3) * 8192u + (lane & 7) * 16, tb + tr, lw + tr};
    table(smem + w * 12288 + lane * 4, t);
    asm volatile(QK_ASM : : [ws] "s"(a.ws), [sz] "n"(WS_RS), [rw] "s"(mkrs(w < 2 ? a.Wq : a.Wk, 8u << 20)), [wo] "s"(h * 256 + (w & 1) * 128), [tb] "s"(tb), [lw] "s"(lw), [l0] "s"(l0), [w] "s"(w),
        [xn] "s"(O_XN + ls), [xw] "s"(xw), [fs] "s"(l ? 0 : 128), [sg] "s"(F_Y0 + 4096 + ls + h * 128 + 16), [pr] "s"(prof_at(l * 80 + h, w)) : CLOB, "s36", "s37", "s38", "s39");
    __builtin_amdgcn_endpgm();
}
DEVI void vu_role(const Args& a, const int w, lds_char* smem, int l, int h) {
    const int lane = lane_id(), g = lane >> 4, n = lane & 15, b = n & 3, p = n >> 2;
    const unsigned l0 = (unsigned)(size_t)smem, tb = l0 + w * 12288, lw = l0 + 61440 + w * 24576, ls = l * SD;
    const unsigned xw = (w >> 1) * 16384 + (w & 1) * 4096, tv = (8 * g + p) * 128 + b * 16 + (w & 1) * 64, to = (4 * g + (p & 1)) * 128 + (p >> 1) * 32 + b * 8;
    const unsigned t[18] = {
        l0 + lane * 16, lw + lane * 16, l0 + (g >> 1) * 1024 + (g & 1) * 512 + n * 16, O_E1 + ls + xw + lane * 16, sp_idx(lane),
        l0 + 57344 + (2 * w + (n >> 3)) * 256 + g * 32 + (n & 7) * 2,
        O_UM + ls + (4 * w + (g & 1)) * 65536 + (h >> 3) * 16384 + (g >> 1) * 2048 + (h & 7) * 256 + n * 16,
        sd(O_Y + h * 131072 + ls) + w * 32768 + (g >> 1) * 1024 + (g & 1) * 256 + n * 16,
        l ? F_X + SD + p * 128 + b * 4 : F_X0 + b * 4, l0 + 49152 + (w >> 1) * 2048 + (2 * (w & 1) + (g >> 1)) * 256 + n * 16 + (g & 1) * 8, sp_idx(lane >> 2), l0 + g * 256 + (n & 7) * 16,
        (lane >> 3) * 8192u + (lane & 7) * 16, (8 * (lane >> 5) + ((lane >> 3) & 3)) * 2048u + (lane & 7) * 16, tb + tv - (w & 1) * 12288, lw + tv - (w & 1) * 24576, tb + to, lw + to};
    table(smem + w * 12288 + lane * 4, t);
    asm volatile(VU_ASM : : [ws] "s"(a.ws), [sz] "n"(WS_RS), [rw] "s"(mkrs(a.Wv, 8u << 20)), [ro] "s"(mkrs(a.Wo, 8u << 20)), [wo] "s"(h * 256 + (w >> 1) * 128 + (w & 1) * 262144), [w2] "s"(h * 262144 + w * 512), [tb] "s"(tb), [lw] "s"(lw), [w] "s"(w),
        [xn] "s"(O_XN + ls), [xw] "s"(xw), [fs] "s"(l ? 0 : 128), [xt] "s"(w * 16384 - xw), [fy] "s"(F_Y0 + ls + h * 128 + w * 4), [sg] "s"(w == 1 ? F_Y0 + ls + h * 128 + 16 : OOB), [pr] "s"(prof_at(l * 80 + 32 + h, w) - 128) : CLOB, "s36", "s37", "s38", "s39");
    __builtin_amdgcn_endpgm();
}
DEVI void rd_role(const Args& a, const int w, lds_char* smem, int l, int c) {
    const int lane = lane_id(), g = lane >> 4, n = lane & 15, r = 2 * (n >> 2) + (n & 1), hf = (n >> 1) & 1;
    const rsrc_t rs = mkrs(a.ws, WS_RS);
    const unsigned l0 = (unsigned)(size_t)smem, tb = l0 + 65536 + w * 4096, ls = l * SD, vp = O_E1 + ls + (c >> 3) * 16384 + (c & 7) * 1024 + w * 256 + n * 16;
    {
        const v4u ff = {SENT, SENT, SENT, SENT};
#pragma unroll
        for (int i = 0; i < 8; ++i) st16(ff, rs, O_UM + ls + c * 65536 + w * 16384 + i * 1024 + lane * 16);
#pragma unroll
        for (int i = 0; i < 2; ++i) st16(ff, rs, O_PT + ls + c * 16384 + w * 2048 + i * 1024 + lane * 16);
        st16(ff, rs, c < 8 ? O_PM + ls + w * 16384 + c * 1024 + lane * 16 : OOB);
        st16(ff, rs, g ? vp + g * 32768 : OOB);
        WV0; BAR;
    }
    st4(g & 1 ? *(const unsigned*)(a.mem0 + r * 1024 + c * 64 + w * 16 + 4 * g + 2 * hf) : nosent(*(const unsigned*)(a.mem0 + r * 1024 + c * 64 + w * 16 + 4 * g + 2 * hf)), rs, g & 1 ? vp + g * 4 : OOB, 0);
    WV0;
    st4(nosent(*(const unsigned*)(a.mem0 + r * 1024 + c * 64 + w * 16 + 4 * g + 2 * hf)), rs, g & 1 ? OOB : vp + g * 4, 0);
    const int gi = lane >> 3, r8 = lane & 7;
    const unsigned t[12] = {
        (unsigned)lane * 16, sp_idx(lane), l0 + w * 4096 + g * 576 + ((n >> 3) * 16 + hf * 4 + 2 * ((n >> 2) & 1) + (n & 1)) * 4,
        l0 + (r >> 1) * 576 + w * 128 + (g >> 1) * 64 + (r & 1) * 32 + hf * 16,
        (vp ^ ((g & 1) * 65536)) + (g >> 1) * 8, g & 1 ? SENT : 0u,
        l0 + 16384 + w * 8192 + lane * 16, l0 + 16384 + ((gi >> 1) * 2 + (w >> 1)) * 1024 + ((gi & 1) * 32 + (w & 1) * 8 + r8) * 16,
        (c >> 1) * 16384u + (c & 1) * 4096 + gi * 512 + (w * 8 + r8) * 16,
        gi ? OOB : O_ST - O_XR + c * 256 + ((w & 1) * 8 + r8) * 16 + (w >> 1) * 8,
        F_Y0 + ls + (w * 8 + r8) * 128 + (c >> 2) * 4, F_Y0 + ls + (gi & 1) * 4096 + (w * 8 + r8) * 128 + 16};
    table(smem + 65536 + w * 4096 + lane * 4, t);
    asm volatile(RD_ASM : : [rs] "s"(rs), [tb] "s"(tb), [su] "s"(O_UM + ls + c * 65536 + w * 16384), [sp] "s"(O_PM + ls + w * 16384), [st] "s"(O_PT + ls + w * 32768),
        [sy] "s"(sd(O_Y + w * (1u << 20) + ls) + (c >> 1) * 16384 + (c & 1) * 4096), [at] "s"(O_XR + ls), [du] "s"((c & 2) * 5 / 2), [fa] "s"(F_A + ls + c * 128 + w * 4), [pr] "s"(prof_at(l * 80 + 64 + c, w) + 128) : CLOB);
    __builtin_amdgcn_endpgm();
}
DEVI void gg_role(const Args& a, const int w, lds_char* smem, int l, int gi) {
    const int lane = lane_id(), g = lane >> 4, n = lane & 15, b = n & 3, p = n >> 2;
    const unsigned l0 = (unsigned)(size_t)smem, tb = l0 + w * 12288, lw = l0 + 65536 + w * 24576, ls = l * SD, tv = (8 * g + p) * 128 + b * 16 + (w & 1) * 64, fh = (g >> 1) * 1024 + (g & 1) * 512 + n * 16;
    const unsigned t[9] = {l0 + lane * 16, lw + lane * 16, l0 + fh, F_A + ls + (lane >> 2) * 128 + (lane & 3) * 4, w * 2048u + lane * 16, O_ST - O_XR + g * 1024 + n * 16, (lane >> 3) * 7168u + (lane & 7) * 16, tb + tv - (w & 1) * 12288, lw + tv - (w & 1) * 24576};
    table(smem + w * 12288 + lane * 4, t);
    asm volatile(GG_ASM : : [rs] "s"(mkrs(a.ws, WS_RS)), [rw] "s"(mkrs(w & 1 ? a.Wu : a.Wg, 3584 * 2048)), [wo] "s"(gi * 256 + (w >> 1) * 128), [tb] "s"(tb), [lw] "s"(lw), [w] "s"(w),
        [xn] "s"(O_XR + ls), [xw] "s"((w >> 1) * 16384 + (w & 1) * 4096), [ho] "s"(O_H + ls + gi * 16384), [fh] "s"(F_H + ls + (gi / 7) * 1024 + (gi % 7) * 128 + w * 4), [pr] "s"(prof_at(160 + l * 28 + gi, w) + 128) : CLOB);
    __builtin_amdgcn_endpgm();
}
DEVI void dd_role(const Args& a, const int w, lds_char* smem, int l, int c) {
    const int lane = lane_id(), g = lane >> 4, b = lane & 3, p = (lane >> 2) & 3, gi = lane >> 3, r8 = lane & 7;
    const unsigned l0 = (unsigned)(size_t)smem, tb = l0 + w * 12288, lw = l0 + 65536 + w * 24576, ls = l * SD, tr = (8 * g + p) * 128 + b * 8;
    const unsigned ta = (c >> 1) * 16384u + (c & 1) * 4096 + gi * 512 + (w * 8 + r8) * 16;
    const rsrc_t rs = mkrs(a.ws, WS_RS);
    const unsigned t[11] = {(unsigned)lane * 16, lw + lane * 16, l0 + w * 8192 + lane * 16, l0 + ((gi >> 1) * 2 + (w >> 1)) * 1024 + ((gi & 1) * 32 + (w & 1) * 8 + r8) * 16,
        ta, l ? (w * 8 + r8) * 2048 + c * 128 + gi * 16 : ta, gi || l ? OOB : O_ST - O_XR + 4096 + c * 256 + (w * 8 + r8) * 8,
        F_H + ls + w * 1024 + ((lane >> 2) % 7) * 128 + b * 4, gi * 2048u + r8 * 16, tb + tr, lw + tr};
    table(smem + w * 12288 + lane * 4, t);
    asm volatile(DD_ASM : : [rs] "s"(rs), [rw] "s"(mkrs(a.Wd, 3584 * 2048)), [ro] "s"(mkrs(l ? (char*)a.out : a.ws, l ? 2048 * 2048 : WS_RS)), [wo] "s"(w * (896 * 2048) + c * 128), [tb] "s"(tb), [lw] "s"(lw), [l] "s"(l),
        [xn] "s"(O_XR + ls), [ho] "s"(O_H + ls + w * 7 * 16384), [fd] "s"(F_D + ls + c * 128 + w * 4), [pr] "s"(prof_at(216 + l * 16 + c, w) + 128) : CLOB);
    __builtin_amdgcn_endpgm();
}

#define LNW 0
#define DLQ 0
#define DLS 400
#define SPL 0
#define PLAN 3
#define PFA 0
#define PFB 2
#define PFL 1
#define VUX 1
#define LNS 40
#define LN1 700
#define DLG 1100
#define DLD 1500
#define DL1 1500
#define DLG1 2600
#define DLD1 3000
DEVI Args args(unsigned sh) {
    auto p = (const __attribute__((address_space(4))) Args*)__builtin_amdgcn_kernarg_segment_ptr();
    asm volatile("" : "+s"(p));
    Args a;
    a.x = p->x; a.mem0 = p->mem0; a.Wq = p->Wq; a.Wk = p->Wk; a.Wv = p->Wv; a.Wo = p->Wo; a.Wg = p->Wg; a.Wu = p->Wu; a.Wd = p->Wd; a.ws = p->ws + sh; a.out = p->out;
    return a;
}
DEVI unsigned beat_wait(unsigned x0) {
    const rsrc_t r0 = mkrs(args(0).ws, 2u << 20);
    for (unsigned spins = 0; spins < SPIN_CAP; ++spins) {
        CB;
        const v2u t = __builtin_amdgcn_raw_buffer_load_b64(r0, 0, O_HB, AUX_SC1);
        if (RFL(t[0]) != x0) return RFL(t[1]) & (2u << 20);
        __builtin_amdgcn_s_sleep(8);
    }
    __builtin_amdgcn_endpgm();
}
DEVI void leader(const Args& a, const int w, lds_char* smem, unsigned x0) {
    const int lane = lane_id();
    const rsrc_t r0 = mkrs(a.ws, 4u << 20);
    unsigned t[2] = {~0u, ~0u};
    for (int i = 0; i < 2 * NPRB; ++i) {
        const unsigned t0 = (unsigned)__builtin_amdgcn_s_memtime();
        WL0;
        if (lane == 0) st4(0u, r0, 0, O_PRB + (i & 1) * SD + w * 1024 + (i >> 1) * 128);
        WV0;
        const unsigned d = (unsigned)__builtin_amdgcn_s_memtime() - t0;
        if (i & 1) t[1] = min(t[1], d); else t[0] = min(t[0], d);
    }
    *(l2*)(smem + w * 8) = v2u{t[0], t[1]};
    WL0; BAR;
    const v4u p = *(l4*)smem, q = *(l4*)(smem + 16);
    const unsigned sh = RFL(min(min(p[0], p[2]), min(q[0], q[2])) < min(min(p[1], p[3]), min(q[1], q[3])) ? 2u << 20 : 0u);
    const unsigned o = sh + w * 1024 + lane * 16;
    const v4u ff = {SENT, SENT, SENT, SENT};
#pragma unroll
    for (int i = 0; i < 8; ++i) st16(ff, r0, o + O_E1 + i * 4096);
#pragma unroll
    for (int i = 1; i < 16; ++i) if (i != 2 && i != 11) st16(ff, r0, o + i * 4096);
    WV0; BAR;
    if (w) return;
    for (unsigned spins = 0; spins < SPIN_CAP; ++spins) {
        if (lane == 0) st8(v2u{++x0, sh}, r0, 0, O_HB);
        if (__ballot((int)ld4(r0, sh + F_D + SD + (lane >> 2) * 128 + (lane & 3) * 4, 0) < 0) == 0ull) break;
        __builtin_amdgcn_s_sleep(16);
    }
}
__global__ __launch_bounds__(256, 1) void sys_kernel(Args) {
    extern __shared__ __attribute__((aligned(16))) char smem_[];
    lds_char* smem = (lds_char*)smem_;
    const int x = blockIdx.x & 7, j = blockIdx.x >> 3, wv = RFL(threadIdx.x >> 6), l = x >> 2, xx = x & 3;
    const unsigned t0 = (unsigned)__builtin_amdgcn_s_memrealtime();
    auto until = [&](unsigned dl) AI { while ((unsigned)__builtin_amdgcn_s_memrealtime() - t0 < dl) __builtin_amdgcn_s_sleep(8); };
    unsigned x0 = 0;
    if (l || j >= 16) x0 = RFL(ld4(mkrs(args(0).ws, 2u << 20), 0, O_HB));
    if (j < 16) {
        if (l) { until(j ? LN1 + j * LNS : 0); ln0(args(0), wv, j * 4 + xx, [&]() AI { return beat_wait(x0); }); until(DL1); }
        if (DLQ && !l && j < 8) until(DLQ);
        if (j < 8) qk_role(args(0), wv, smem, l, xx * 8 + j); else vu_role(args(0), wv, smem, l, (xx ^ VUX) * 8 + j - 8);
        return;
    }
    {
        const int lane = lane_id(), q = (j - 16) * 4 + wv, m = SPL && !l ? ((xx & 1) != (SPL & 1)) * 2 + (q >> 5) : q >> 4;
        const Args a = args(0);
        const u16 *W0 = a.Wq, *W1 = a.Wk, *W2 = a.Wv, *W3 = a.Wo;
        asm volatile("" : "+s"(W0), "+s"(W1), "+s"(W2), "+s"(W3));
        const u16* W = m == 0 ? W0 : m == 1 ? W1 : m == 2 ? W2 : W3;
        const rsrc_t rw = mkrs(W, 8u << 20);
        if (l) {
#pragma unroll
            for (int i = 0; i < PFA; ++i) __builtin_amdgcn_raw_ptr_buffer_load_lds(rw, smem + LDSB - 1024 + wv * 256, 4, lane * 256, i * (1u << 20) + (xx * 16 + (q & 15)) * 16384, 0, 0);
        } else {
            const unsigned po = (0x100u - (unsigned)(size_t)W) & 0x300u, hx = SPL ? (xx & 2) + ((q >> 4) & 1) : m < 2 ? xx : xx ^ VUX;
            const unsigned vo = m == 3 ? (lane >> 1) * 1024 + (lane & 1) * 128 : (lane >> 2) * 8192 + ((lane >> 1) & 1) * 1024 + (lane & 1) * 128;
            const unsigned so = po + (m == 3 ? hx * (2u << 20) + (q & 15) * 32768 : hx * 2048 + (q & 15) * 131072);
#pragma unroll
            for (int i = 0; i < PFB; ++i) __builtin_amdgcn_raw_ptr_buffer_load_lds(rw, smem + LDSB - 1024 + wv * 256, 4, vo, so + i * (m == 3 ? 524288 : 2u << 20), 0, 0);
            const unsigned pl = (0x1F00u - (unsigned)(size_t)W) & 0x1F00u;
#pragma unroll
            for (int i = 0; i < PFL; ++i) __builtin_amdgcn_raw_ptr_buffer_load_lds(rw, smem + LDSB - 1024 + wv * 256, 4, (lane >> 1) * 8192 + (lane & 1) * 128, (pl >> 11) == hx && m < 3 ? pl + ((q & 15) + 16 * i) * 262144 : OOB, 0, 0);
        }
    }
    int ro, ix;
    if ((xx & 1) == (PLAN & 1)) { if (j < 24) { ro = 0; ix = (xx >> 1) * 8 + j - 16; } else if (j < 26) { ro = 3; ix = (xx >> 1) * 2 + j - 24; } else { ro = 1; ix = (xx >> 1) * 14 + j - 26; } }
    else if (j < 24) { ro = 1; ix = (xx >> 1) * 14 + j - 10; } else { ro = 2; ix = (xx >> 1) * 8 + j - 24; }
    if (ro == 3 && l) { if (ix == 0) leader(args(0), wv, smem, x0); return; }
    const unsigned sh = beat_wait(x0);
    if (ro == 0) { rd_role(args(sh), wv, smem, l, ix); return; }
    if (ro == 1) { const Args a = args(sh); until(l ? DLG1 : ((((unsigned)(size_t)a.Wg >> 8) + ix) & 3) == 1 || ((((unsigned)(size_t)a.Wu >> 8) + ix) & 3) == 1 ? DLG - DLS : DLG); WV0; BAR; gg_role(a, wv, smem, l, ix); return; }
    if (ro == 2) { const Args a = args(sh); until((l ? DLD1 : DLD) - (((((unsigned)(size_t)a.Wd >> 8) + (ix >> 1)) & 3) == 1 ? DLS + 200 : 0)); WV0; BAR; dd_role(a, wv, smem, l, ix); return; }
    r_role(args(sh), wv, ix);
}

extern "C" void kernel_launch(void* const* d_in, const int* in_sizes, int n_in, void* d_out, int out_size, void* d_ws, size_t ws_size, hipStream_t stream) {
    (void)in_sizes; (void)n_in; (void)out_size; (void)ws_size;
    Args a;
    a.x = (const u16*)d_in[0]; a.mem0 = (const u16*)d_in[1]; a.Wq = (const u16*)d_in[2]; a.Wk = (const u16*)d_in[3]; a.Wv = (const u16*)d_in[4]; a.Wo = (const u16*)d_in[5];
    a.Wg = (const u16*)d_in[8]; a.Wu = (const u16*)d_in[9]; a.Wd = (const u16*)d_in[10];
    a.ws = (char*)(((uintptr_t)d_ws + (2u << 20) - 1) & ~(uintptr_t)((2u << 20) - 1)); a.out = (u16*)d_out;
    (void)hipFuncSetAttribute((const void*)sys_kernel, hipFuncAttributeMaxDynamicSharedMemorySize, LDSB);
    hipLaunchKernelGGL(sys_kernel, dim3(256), dim3(256), LDSB, stream, a);
}
```

```cpp
#include <hip/hip_runtime.h>
#include <stdint.h>

typedef unsigned short u16;
typedef unsigned long long u64;
typedef __attribute__((ext_vector_type(8))) short bf16x8;
typedef __attribute__((ext_vector_type(4))) short s16x4;
typedef __attribute__((ext_vector_type(4))) float f32x4;
typedef __attribute__((ext_vector_type(16))) float f32x16;
typedef __attribute__((ext_vector_type(2))) float f32x2;
typedef __attribute__((ext_vector_type(4))) unsigned v4u;
typedef __attribute__((ext_vector_type(2))) unsigned v2u;
typedef __amdgpu_buffer_rsrc_t rsrc_t;
typedef __attribute__((address_space(3))) char lds_char;
typedef __attribute__((address_space(3))) bf16x8 l8;
typedef __attribute__((address_space(3))) v4u l4;
typedef __attribute__((address_space(3))) v2u l2;
typedef __attribute__((address_space(3))) unsigned l1;
typedef __attribute__((address_space(3))) f32x4 lf4;
typedef __attribute__((address_space(3))) float lf1;
#define DEVI __device__ __forceinline__
#define AI __attribute__((always_inline))

constexpr int NSEG = 64;
constexpr float ATT_SCALE = 0.08838834764831845f;
constexpr float LOG2E = 1.4426950408889634f;
#define NPRB 4
constexpr unsigned SD = 8192;
DEVI unsigned sd(unsigned o) { return o ^ ((o >> 8) & SD); }
constexpr unsigned F_X0 = 32768;
constexpr unsigned F_X = 4096;
constexpr unsigned F_A = 16384;
constexpr unsigned F_H = F_A + 2048;
constexpr unsigned F_D = F_H + 4096;
constexpr unsigned F_Y0 = 49152;
constexpr unsigned F_ABORT = 6400;
constexpr unsigned O_PM = 65536;
constexpr unsigned O_E1 = 131072;
constexpr unsigned O_PT = 262144;
constexpr unsigned O_UM = 524288;
constexpr unsigned CTRL_END = O_UM + (1u << 20);
constexpr unsigned O_PRB = 0x1F0000, O_HB = 0x1FFF00;
constexpr unsigned O_XN = 4u << 20;
constexpr unsigned O_XR = O_XN + (8u << 20);
constexpr unsigned O_ST = O_XR + (8u << 20);
constexpr unsigned O_H = O_ST + (8u << 20);
constexpr unsigned O_Y = O_H + (36u << 20);
constexpr unsigned WS_NEED = O_Y + (8u << 20);
constexpr unsigned O_PROF = WS_NEED;
constexpr unsigned WS_RS = WS_NEED;
static_assert(CTRL_END <= O_PRB && CTRL_END % 16384 == 0 && O_Y % (8u << 20) == 0, "workspace");
constexpr unsigned OOB = 0x80000000u, OOB2 = 0x90000000u, SENT = 0xFFFFFFFFu;

struct Args { const u16 *x, *mem0, *Wq, *Wk, *Wv, *Wo, *Wg, *Wu, *Wd; char* ws; u16* out; };

typedef __attribute__((ext_vector_type(2))) __bf16 bf16x2_t;
DEVI unsigned pack2(float lo, float hi) { f32x2 v = {lo, hi}; bf16x2_t b = __builtin_convertvector(v, bf16x2_t); return __builtin_bit_cast(unsigned, b); }
DEVI float bflo(unsigned u) { return __uint_as_float(u << 16); }
DEVI float bfhi(unsigned u) { return __uint_as_float(u & 0xffff0000u); }
DEVI float rbf(float x) { return bflo(pack2(x, x)); }
DEVI bf16x8 as8(v4u v) { return __builtin_bit_cast(bf16x8, v); }
DEVI f32x4 mfma16(bf16x8 a, bf16x8 b, f32x4 c) { return __builtin_amdgcn_mfma_f32_16x16x32_bf16(a, b, c, 0, 0, 0); }
DEVI f32x16 mfma32(bf16x8 a, bf16x8 b, f32x16 c) { return __builtin_amdgcn_mfma_f32_32x32x16_bf16(a, b, c, 0, 0, 0); }
constexpr int AUX_SC1 = 16;
DEVI v4u ld16(rsrc_t r, unsigned vo, unsigned so) { return __builtin_amdgcn_raw_buffer_load_b128(r, vo, so, AUX_SC1); }
DEVI unsigned ld4(rsrc_t r, unsigned vo, unsigned so) { return __builtin_amdgcn_raw_buffer_load_b32(r, vo, so, AUX_SC1); }
DEVI void st16(v4u v, rsrc_t r, unsigned vo) { __builtin_amdgcn_raw_buffer_store_b128(v, r, vo, 0, AUX_SC1); }
DEVI void st8(v2u v, rsrc_t r, unsigned vo, unsigned so) { __builtin_amdgcn_raw_buffer_store_b64(v, r, vo, so, AUX_SC1); }
DEVI void st4(unsigned v, rsrc_t r, unsigned vo, unsigned so) { __builtin_amdgcn_raw_buffer_store_b32(v, r, vo, so, AUX_SC1); }
DEVI v2u trd(lds_char* p) { return __builtin_bit_cast(v2u, __builtin_amdgcn_ds_read_tr16_b64_v4i16((__attribute__((address_space(3))) s16x4*)p)); }
template <int O1, int O2> DEVI void tra(v2u& lo, v2u& hi, unsigned a) { asm volatile("ds_read_b64_tr_b16 %0, %2 offset:%3\n\tds_read_b64_tr_b16 %1, %2 offset:%4" : "=&v"(lo), "=&v"(hi) : "v"(a), "n"(O1), "n"(O2) : "memory"); }
DEVI void trw(v2u& a, v2u& b, v2u& c, v2u& d) { asm volatile("s_waitcnt lgkmcnt(0)" : "+v"(a), "+v"(b), "+v"(c), "+v"(d)::"memory"); }
DEVI rsrc_t mkrs(const void* p, unsigned n) { return __builtin_amdgcn_make_buffer_rsrc((void*)p, 0, (int)n, 0x00020000); }
#define CB asm volatile("" ::: "memory")
#define SB __builtin_amdgcn_sched_barrier(0)
#define WL0 asm volatile("s_waitcnt lgkmcnt(0)" ::: "memory")
#define WV0 asm volatile("s_waitcnt vmcnt(0)" ::: "memory")
#define WV(n) asm volatile("s_waitcnt vmcnt(%0)" ::"n"(n) : "memory")
#define BAR do { __builtin_amdgcn_s_barrier(); CB; } while (0)
#define RFL(x) __builtin_amdgcn_readfirstlane(x)
DEVI int lane_id() { int x; asm volatile("v_mbcnt_lo_u32_b32 %0, -1, 0\n\tv_mbcnt_hi_u32_b32 %0, -1, %0" : "=v"(x)); return x; }
template <int I> struct IC { static constexpr int v = I; };
template <int B, int E_, class F> DEVI void sfor(F&& f) { if constexpr (B < E_) { f(IC<B>{}); sfor<B + 1, E_>(f); } }
DEVI float red_x32(float x, bool mx) { unsigned u = __float_as_uint(x); auto r = __builtin_amdgcn_permlane32_swap(u, u, false, false); float a = __uint_as_float(r[0]), b = __uint_as_float(r[1]); return mx ? fmaxf(a, b) : a + b; }
DEVI float red_x16(float x, bool mx) { unsigned u = __float_as_uint(x); auto r = __builtin_amdgcn_permlane16_swap(u, u, false, false); float a = __uint_as_float(r[0]), b = __uint_as_float(r[1]); return mx ? fmaxf(a, b) : a + b; }
DEVI unsigned vmax4(v4u d) { return max(max(d[0], d[1]), max(d[2], d[3])); }
DEVI unsigned nosent(unsigned v) { return min(v, SENT - 1u); }
constexpr unsigned SPIN_CAP = 1u << 18;
DEVI bool spin_bail(unsigned& spins, rsrc_t rs) {
    if (++spins > SPIN_CAP) { st4(0u, rs, 0, F_ABORT); return true; }
    return (spins & 255u) == 0u && RFL(ld4(rs, 0, F_ABORT)) != SENT;
}
#define TSTAMP(idx)
constexpr int LDSB = 160 * 1024;
DEVI void idle(unsigned dl) { const unsigned t0 = (unsigned)__builtin_amdgcn_s_memrealtime(); while ((unsigned)__builtin_amdgcn_s_memrealtime() - t0 < dl) __builtin_amdgcn_s_sleep(8); }

DEVI float red_rows(float s) {
    s += __uint_as_float(__builtin_amdgcn_update_dpp(0, __float_as_uint(s), 0x128, 0xf, 0xf, false));
    s = red_x16(s, false); return red_x32(s, false);
}
DEVI f32x2 up2(unsigned u) { return f32x2{bflo(u), bfhi(u)}; }
DEVI float ln_inv(float var) { return __builtin_amdgcn_rcpf(rbf(__builtin_amdgcn_sqrtf(rbf(rbf(fmaxf(var, 0.f)) + 1e-5f)))); }
template <class F> DEVI void ln0(const Args& a, const int w, int b, F&& base) {
    const int lane = lane_id(), row = w * 8 + (lane & 7);
    const rsrc_t rx = mkrs(a.x, 2048 * 2048);
    v4u u[16];
#pragma unroll
    for (int i = 0; i < 16; ++i) u[i] = __builtin_amdgcn_raw_buffer_load_b128(rx, row * 2048 + (lane >> 3) * 16 + i * 128, b * 65536, 0);
    const float k0 = __shfl(bflo(u[0][0]), lane & 7);
    f32x2 s1 = {0.f, 0.f}, s2 = {0.f, 0.f};
#pragma unroll
    for (int i = 0; i < 16; ++i)
#pragma unroll
        for (int e = 0; e < 4; ++e) { const f32x2 d = up2(u[i][e]) - k0; s1 += d; s2 += d * d; }
    const float s = red_rows(s1[0] + s1[1]) * (1.0f / 1024.0f), q = red_rows(s2[0] + s2[1]) * (1.0f / 1024.0f);
    float inv = ln_inv(q - s * s), c = -rbf(k0 + s) * inv;
    asm volatile("" : "+v"(inv), "+v"(c));
    const rsrc_t rs = mkrs(a.ws + base(), WS_RS);
    const unsigned o = sd(O_XN + b * 131072) + (lane >> 3) * 512 + row * 16;
#pragma unroll
    for (int i = 0; i < 16; ++i) {
        v4u r;
#pragma unroll
        for (int e = 0; e < 4; ++e) { const f32x2 z = up2(u[i][e]) * inv + c; r[e] = pack2(z[0], z[1]); }
        st16(r, rs, o + (i >> 1) * 16384 + (i & 1) * 4096);
    }
#pragma unroll
    for (int i = 0; i < 16; ++i) st16(u[i], rs, o + (O_XR - O_XN) + (i >> 1) * 16384 + (i & 1) * 4096);
    st16(u[0], rs, sd(O_ST + b * 131072) + w * 1024 + lane * 16); st16(u[0], rs, sd(O_ST + b * 131072) + 4096 + w * 1024 + lane * 16); st16(u[0], rs, sd(O_ST + SD + b * 131072) + w * 1024 + lane * 16);
    WV0;
    st4(b, rs, 0, F_X0 + b * 128 + w * 4);
}
#define RSLP 8
DEVI void r_role(const Args& a, const int w, int rq) {
    const int lane = lane_id(), row = (w >> 1) * 16 + (rq & 1) * 8 + (lane & 7), cq = (rq >> 1) * 2 + (w & 1);
    const rsrc_t rs = mkrs(a.ws, WS_RS);
    const unsigned fo = cq * 32768 + (lane >> 3) * 512 + row * 16, so = O_ST - O_XR + 4096 + (lane >> 3) * 512 + row * 8;
    auto io = [](int i) -> unsigned { return (i >> 1) * 16384 + (i & 1) * 4096; };
    const unsigned fd = F_D + (lane & 15) * 128 + (row >> 3) * 4;
    for (int b = 0; b < NSEG; ++b) {
        unsigned spins = 0;
        for (;;) {
            CB;
            if (__ballot((int)ld4(rs, fd, 0) < b) == 0ull) break;
            if (spin_bail(spins, rs)) return;
            __builtin_amdgcn_s_sleep(RSLP);
        }
        const unsigned sx = sd(O_XR + b * 131072);
        const v2u s0 = __builtin_amdgcn_raw_buffer_load_b64(rs, so, sx, AUX_SC1), s1 = __builtin_amdgcn_raw_buffer_load_b64(rs, so + 256, sx, AUX_SC1);
        v4u u[4];
#pragma unroll
        for (int i = 0; i < 4; ++i) u[i] = ld16(rs, fo + io(i), sx);
        const float m0 = __uint_as_float(s0[0]), m1 = __uint_as_float(s1[0]), mean = red_rows(m0 + m1) * 0.0625f;
        const float var = red_rows(__uint_as_float(s0[1]) + __uint_as_float(s1[1]) + 64.f * ((m0 - mean) * (m0 - mean) + (m1 - mean) * (m1 - mean))) * (1.0f / 1024.0f);
        const float inv = ln_inv(var), c = -rbf(mean) * inv;
#pragma unroll
        for (int i = 0; i < 4; ++i) {
            v4u o;
#pragma unroll
            for (int e = 0; e < 4; ++e) { const f32x2 z = up2(u[i][e]) * inv + c; o[e] = pack2(z[0], z[1]); }
            st16(o, rs, fo + io(i) + ((sx ^ SD) - (O_XR - O_XN)));
        }
#pragma unroll
        for (int i = 0; i < 4; ++i) st16(u[i], rs, fo + io(i) + (sx ^ SD));
        WV0;
        st4(b, rs, 0, F_X + SD + rq * 128 + w * 4);
        if (rq == 0 && w == 0) TSTAMP(200000 + 64 + b);
    }
}
#define QK_ASM \
"v_mbcnt_lo_u32_b32 v255, -1, 0\nv_mbcnt_hi_u32_b32 v255, -1, v255\nv_lshlrev_b32 v255, 2, v255\nv_add_u32 v255, %[tb], v255\nds_read_b32 v0, v255 offset:0\nds_read_b32 v1, v255 offset:256\nds_read_b32 v2, v255 offset:512\nds_read_b32 v3, v255 offset:768\n" \
"ds_read_b32 v4, v255 offset:1024\nds_read_b32 v5, v255 offset:1280\nds_read_b32 v6, v255 offset:1536\nds_read_b32 v7, v255 offset:1792\nds_read_b32 v8, v255 offset:2048\nds_read_b32 v9, v255 offset:2304\nds_read_b32 v10, v255 offset:2560\nds_read_b32 v11, v255 offset:2816\n" \
"ds_read_b32 v12, v255 offset:3072\nds_read_b32 v13, v255 offset:3328\nds_read_b32 v14, v255 offset:3584\nds_read_b32 v15, v255 offset:3840\nds_read_b32 v92, v255 offset:4096\nds_read_b32 v93, v255 offset:4352\nds_read_b32 v94, v255 offset:4608\ns_waitcnt lgkmcnt(0)\n" \
"s_mov_b64 s[36:37], %[ws]\ns_mov_b32 s38, %[sz]\ns_mov_b32 s39, 0x20000\ns_mov_b32 s45, 2096896\nbuffer_load_dword v95, off, s[36:39], s45 sc1\ns_add_u32 m0, %[lw], 16384\ns_add_u32 s44, %[wo], 0\nbuffer_load_dwordx4 v92, %[rw], s44 offen nt lds\n" \
"s_add_u32 m0, %[lw], 17408\ns_add_u32 s44, %[wo], 65536\nbuffer_load_dwordx4 v92, %[rw], s44 offen nt lds\ns_add_u32 m0, %[lw], 18432\ns_add_u32 s44, %[wo], 131072\nbuffer_load_dwordx4 v92, %[rw], s44 offen nt lds\ns_add_u32 m0, %[lw], 19456\ns_add_u32 s44, %[wo], 196608\n" \
"buffer_load_dwordx4 v92, %[rw], s44 offen nt lds\ns_add_u32 m0, %[lw], 20480\ns_add_u32 s44, %[wo], 262144\nbuffer_load_dwordx4 v92, %[rw], s44 offen nt lds\ns_add_u32 m0, %[lw], 21504\ns_add_u32 s44, %[wo], 327680\nbuffer_load_dwordx4 v92, %[rw], s44 offen nt lds\ns_add_u32 m0, %[lw], 22528\n" \
"s_add_u32 s44, %[wo], 393216\nbuffer_load_dwordx4 v92, %[rw], s44 offen nt lds\ns_add_u32 m0, %[lw], 23552\ns_add_u32 s44, %[wo], 458752\nbuffer_load_dwordx4 v92, %[rw], s44 offen nt lds\ns_add_u32 m0, %[tb], 0\ns_add_u32 s44, %[wo], 524288\nbuffer_load_dwordx4 v92, %[rw], s44 offen nt lds\n" \
"s_add_u32 m0, %[tb], 1024\ns_add_u32 s44, %[wo], 589824\nbuffer_load_dwordx4 v92, %[rw], s44 offen nt lds\ns_add_u32 m0, %[tb], 2048\ns_add_u32 s44, %[wo], 655360\nbuffer_load_dwordx4 v92, %[rw], s44 offen nt lds\ns_add_u32 m0, %[tb], 3072\ns_add_u32 s44, %[wo], 720896\n" \
"buffer_load_dwordx4 v92, %[rw], s44 offen nt lds\ns_add_u32 m0, %[tb], 4096\ns_add_u32 s44, %[wo], 786432\nbuffer_load_dwordx4 v92, %[rw], s44 offen nt lds\ns_add_u32 m0, %[tb], 5120\ns_add_u32 s44, %[wo], 851968\nbuffer_load_dwordx4 v92, %[rw], s44 offen nt lds\ns_add_u32 m0, %[tb], 6144\n" \
"s_add_u32 s44, %[wo], 917504\nbuffer_load_dwordx4 v92, %[rw], s44 offen nt lds\ns_add_u32 m0, %[tb], 7168\ns_add_u32 s44, %[wo], 983040\nbuffer_load_dwordx4 v92, %[rw], s44 offen nt lds\ns_add_u32 m0, %[tb], 8192\ns_add_u32 s44, %[wo], 1048576\nbuffer_load_dwordx4 v92, %[rw], s44 offen nt lds\n" \
"s_add_u32 m0, %[tb], 9216\ns_add_u32 s44, %[wo], 1114112\nbuffer_load_dwordx4 v92, %[rw], s44 offen nt lds\ns_add_u32 m0, %[tb], 10240\ns_add_u32 s44, %[wo], 1179648\nbuffer_load_dwordx4 v92, %[rw], s44 offen nt lds\ns_add_u32 m0, %[tb], 11264\ns_add_u32 s44, %[wo], 1245184\n" \
"buffer_load_dwordx4 v92, %[rw], s44 offen nt lds\ns_add_u32 m0, %[lw], 0\ns_add_u32 s44, %[wo], 1310720\nbuffer_load_dwordx4 v92, %[rw], s44 offen nt lds\ns_add_u32 m0, %[lw], 1024\ns_add_u32 s44, %[wo], 1376256\nbuffer_load_dwordx4 v92, %[rw], s44 offen nt lds\ns_add_u32 m0, %[lw], 2048\n" \
"s_add_u32 s44, %[wo], 1441792\nbuffer_load_dwordx4 v92, %[rw], s44 offen nt lds\ns_add_u32 m0, %[lw], 3072\ns_add_u32 s44, %[wo], 1507328\nbuffer_load_dwordx4 v92, %[rw], s44 offen nt lds\ns_add_u32 m0, %[lw], 4096\ns_add_u32 s44, %[wo], 1572864\nbuffer_load_dwordx4 v92, %[rw], s44 offen nt lds\n" \
"s_add_u32 m0, %[lw], 5120\ns_add_u32 s44, %[wo], 1638400\nbuffer_load_dwordx4 v92, %[rw], s44 offen nt lds\ns_add_u32 m0, %[lw], 6144\ns_add_u32 s44, %[wo], 1703936\nbuffer_load_dwordx4 v92, %[rw], s44 offen nt lds\ns_add_u32 m0, %[lw], 7168\ns_add_u32 s44, %[wo], 1769472\n" \
"buffer_load_dwordx4 v92, %[rw], s44 offen nt lds\ns_add_u32 m0, %[lw], 8192\ns_add_u32 s44, %[wo], 1835008\nbuffer_load_dwordx4 v92, %[rw], s44 offen nt lds\ns_add_u32 m0, %[lw], 9216\ns_add_u32 s44, %[wo], 1900544\nbuffer_load_dwordx4 v92, %[rw], s44 offen nt lds\ns_add_u32 m0, %[lw], 10240\n" \
"s_add_u32 s44, %[wo], 1966080\nbuffer_load_dwordx4 v92, %[rw], s44 offen nt lds\ns_add_u32 m0, %[lw], 11264\ns_add_u32 s44, %[wo], 2031616\nbuffer_load_dwordx4 v92, %[rw], s44 offen nt lds\ns_add_u32 m0, %[lw], 12288\ns_add_u32 s44, %[wo], 2097152\nbuffer_load_dwordx4 v92, %[rw], s44 offen nt lds\n" \
"s_add_u32 m0, %[lw], 13312\ns_add_u32 s44, %[wo], 2162688\nbuffer_load_dwordx4 v92, %[rw], s44 offen nt lds\ns_add_u32 m0, %[lw], 14336\ns_add_u32 s44, %[wo], 2228224\nbuffer_load_dwordx4 v92, %[rw], s44 offen nt lds\ns_add_u32 m0, %[lw], 15360\ns_add_u32 s44, %[wo], 2293760\n" \
"buffer_load_dwordx4 v92, %[rw], s44 offen nt lds\ns_waitcnt vmcnt(32)\nds_read_b64_tr_b16 a[0:1], v94 offset:16384\nds_read_b64_tr_b16 a[2:3], v94 offset:16896\nds_read_b64_tr_b16 a[8:9], v94 offset:16392\nds_read_b64_tr_b16 a[10:11], v94 offset:16904\nds_read_b64_tr_b16 a[16:17], v94 offset:16448\nds_read_b64_tr_b16 a[18:19], v94 offset:16960\n" \
"ds_read_b64_tr_b16 a[24:25], v94 offset:16456\nds_read_b64_tr_b16 a[26:27], v94 offset:16968\ns_waitcnt lgkmcnt(0)\ns_add_u32 m0, %[lw], 16384\ns_add_u32 s44, %[wo], 2359296\nbuffer_load_dwordx4 v92, %[rw], s44 offen nt lds\ns_add_u32 m0, %[lw], 17408\ns_add_u32 s44, %[wo], 2424832\n" \
"buffer_load_dwordx4 v92, %[rw], s44 offen nt lds\ns_add_u32 m0, %[lw], 18432\ns_add_u32 s44, %[wo], 2490368\nbuffer_load_dwordx4 v92, %[rw], s44 offen nt lds\ns_add_u32 m0, %[lw], 19456\ns_add_u32 s44, %[wo], 2555904\nbuffer_load_dwordx4 v92, %[rw], s44 offen nt lds\ns_waitcnt vmcnt(32)\n" \
"ds_read_b64_tr_b16 a[4:5], v94 offset:20480\nds_read_b64_tr_b16 a[6:7], v94 offset:20992\nds_read_b64_tr_b16 a[12:13], v94 offset:20488\nds_read_b64_tr_b16 a[14:15], v94 offset:21000\nds_read_b64_tr_b16 a[20:21], v94 offset:20544\nds_read_b64_tr_b16 a[22:23], v94 offset:21056\nds_read_b64_tr_b16 a[28:29], v94 offset:20552\nds_read_b64_tr_b16 a[30:31], v94 offset:21064\n" \
"s_waitcnt lgkmcnt(0)\ns_add_u32 m0, %[lw], 20480\ns_add_u32 s44, %[wo], 2621440\nbuffer_load_dwordx4 v92, %[rw], s44 offen nt lds\ns_add_u32 m0, %[lw], 21504\ns_add_u32 s44, %[wo], 2686976\nbuffer_load_dwordx4 v92, %[rw], s44 offen nt lds\ns_add_u32 m0, %[lw], 22528\n" \
"s_add_u32 s44, %[wo], 2752512\nbuffer_load_dwordx4 v92, %[rw], s44 offen nt lds\ns_add_u32 m0, %[lw], 23552\ns_add_u32 s44, %[wo], 2818048\nbuffer_load_dwordx4 v92, %[rw], s44 offen nt lds\ns_waitcnt vmcnt(32)\nds_read_b64_tr_b16 a[32:33], v93 offset:0\nds_read_b64_tr_b16 a[34:35], v93 offset:512\n" \
"ds_read_b64_tr_b16 v[84:85], v93 offset:8\nds_read_b64_tr_b16 v[86:87], v93 offset:520\nds_read_b64_tr_b16 a[40:41], v93 offset:64\nds_read_b64_tr_b16 a[42:43], v93 offset:576\nds_read_b64_tr_b16 a[48:49], v93 offset:72\nds_read_b64_tr_b16 a[50:51], v93 offset:584\ns_waitcnt lgkmcnt(0)\ns_add_u32 m0, %[tb], 0\n" \
"s_add_u32 s44, %[wo], 2883584\nbuffer_load_dwordx4 v92, %[rw], s44 offen nt lds\ns_add_u32 m0, %[tb], 1024\ns_add_u32 s44, %[wo], 2949120\nbuffer_load_dwordx4 v92, %[rw], s44 offen nt lds\ns_add_u32 m0, %[tb], 2048\ns_add_u32 s44, %[wo], 3014656\nbuffer_load_dwordx4 v92, %[rw], s44 offen nt lds\n" \
"s_add_u32 m0, %[tb], 3072\ns_add_u32 s44, %[wo], 3080192\nbuffer_load_dwordx4 v92, %[rw], s44 offen nt lds\ns_waitcnt vmcnt(32)\nds_read_b64_tr_b16 a[36:37], v93 offset:4096\nds_read_b64_tr_b16 a[38:39], v93 offset:4608\nds_read_b64_tr_b16 v[80:81], v93 offset:4104\nds_read_b64_tr_b16 v[82:83], v93 offset:4616\n" \
"ds_read_b64_tr_b16 a[44:45], v93 offset:4160\nds_read_b64_tr_b16 a[46:47], v93 offset:4672\nds_read_b64_tr_b16 a[52:53], v93 offset:4168\nds_read_b64_tr_b16 a[54:55], v93 offset:4680\ns_waitcnt lgkmcnt(0)\ns_add_u32 m0, %[tb], 4096\ns_add_u32 s44, %[wo], 3145728\nbuffer_load_dwordx4 v92, %[rw], s44 offen nt lds\n" \
"s_add_u32 m0, %[tb], 5120\ns_add_u32 s44, %[wo], 3211264\nbuffer_load_dwordx4 v92, %[rw], s44 offen nt lds\ns_add_u32 m0, %[tb], 6144\ns_add_u32 s44, %[wo], 3276800\nbuffer_load_dwordx4 v92, %[rw], s44 offen nt lds\ns_add_u32 m0, %[tb], 7168\ns_add_u32 s44, %[wo], 3342336\n" \
"buffer_load_dwordx4 v92, %[rw], s44 offen nt lds\ns_waitcnt vmcnt(32)\nds_read_b64_tr_b16 a[56:57], v93 offset:8192\nds_read_b64_tr_b16 a[58:59], v93 offset:8704\nds_read_b64_tr_b16 a[64:65], v93 offset:8200\nds_read_b64_tr_b16 a[66:67], v93 offset:8712\nds_read_b64_tr_b16 v[76:77], v93 offset:8256\nds_read_b64_tr_b16 v[78:79], v93 offset:8768\n" \
"ds_read_b64_tr_b16 a[72:73], v93 offset:8264\nds_read_b64_tr_b16 a[74:75], v93 offset:8776\ns_waitcnt lgkmcnt(0)\ns_add_u32 m0, %[tb], 8192\ns_add_u32 s44, %[wo], 3407872\nbuffer_load_dwordx4 v92, %[rw], s44 offen nt lds\ns_add_u32 m0, %[tb], 9216\ns_add_u32 s44, %[wo], 3473408\n" \
"buffer_load_dwordx4 v92, %[rw], s44 offen nt lds\ns_add_u32 m0, %[tb], 10240\ns_add_u32 s44, %[wo], 3538944\nbuffer_load_dwordx4 v92, %[rw], s44 offen nt lds\ns_add_u32 m0, %[tb], 11264\ns_add_u32 s44, %[wo], 3604480\nbuffer_load_dwordx4 v92, %[rw], s44 offen nt lds\ns_waitcnt vmcnt(32)\n" \
"ds_read_b64_tr_b16 a[60:61], v94 offset:0\nds_read_b64_tr_b16 a[62:63], v94 offset:512\nds_read_b64_tr_b16 a[68:69], v94 offset:8\nds_read_b64_tr_b16 a[70:71], v94 offset:520\nds_read_b64_tr_b16 v[72:73], v94 offset:64\nds_read_b64_tr_b16 v[74:75], v94 offset:576\nds_read_b64_tr_b16 a[76:77], v94 offset:72\nds_read_b64_tr_b16 a[78:79], v94 offset:584\n" \
"s_waitcnt lgkmcnt(0)\ns_add_u32 m0, %[lw], 0\ns_add_u32 s44, %[wo], 3670016\nbuffer_load_dwordx4 v92, %[rw], s44 offen nt lds\ns_add_u32 m0, %[lw], 1024\ns_add_u32 s44, %[wo], 3735552\nbuffer_load_dwordx4 v92, %[rw], s44 offen nt lds\ns_add_u32 m0, %[lw], 2048\n" \
"s_add_u32 s44, %[wo], 3801088\nbuffer_load_dwordx4 v92, %[rw], s44 offen nt lds\ns_add_u32 m0, %[lw], 3072\ns_add_u32 s44, %[wo], 3866624\nbuffer_load_dwordx4 v92, %[rw], s44 offen nt lds\ns_waitcnt vmcnt(32)\nds_read_b64_tr_b16 a[80:81], v94 offset:4096\nds_read_b64_tr_b16 a[82:83], v94 offset:4608\n" \
"ds_read_b64_tr_b16 a[88:89], v94 offset:4104\nds_read_b64_tr_b16 a[90:91], v94 offset:4616\nds_read_b64_tr_b16 a[96:97], v94 offset:4160\nds_read_b64_tr_b16 a[98:99], v94 offset:4672\nds_read_b64_tr_b16 v[68:69], v94 offset:4168\nds_read_b64_tr_b16 v[70:71], v94 offset:4680\ns_waitcnt lgkmcnt(0)\ns_add_u32 m0, %[lw], 4096\n" \
"s_add_u32 s44, %[wo], 3932160\nbuffer_load_dwordx4 v92, %[rw], s44 offen nt lds\ns_add_u32 m0, %[lw], 5120\ns_add_u32 s44, %[wo], 3997696\nbuffer_load_dwordx4 v92, %[rw], s44 offen nt lds\ns_add_u32 m0, %[lw], 6144\ns_add_u32 s44, %[wo], 4063232\nbuffer_load_dwordx4 v92, %[rw], s44 offen nt lds\n" \
"s_add_u32 m0, %[lw], 7168\ns_add_u32 s44, %[wo], 4128768\nbuffer_load_dwordx4 v92, %[rw], s44 offen nt lds\ns_waitcnt vmcnt(32)\nds_read_b64_tr_b16 a[84:85], v94 offset:8192\nds_read_b64_tr_b16 a[86:87], v94 offset:8704\nds_read_b64_tr_b16 a[92:93], v94 offset:8200\nds_read_b64_tr_b16 a[94:95], v94 offset:8712\n" \
"ds_read_b64_tr_b16 a[100:101], v94 offset:8256\nds_read_b64_tr_b16 a[102:103], v94 offset:8768\nds_read_b64_tr_b16 v[64:65], v94 offset:8264\nds_read_b64_tr_b16 v[66:67], v94 offset:8776\ns_waitcnt lgkmcnt(0)\ns_add_u32 m0, %[lw], 8192\ns_add_u32 s44, %[wo], 4194304\nbuffer_load_dwordx4 v92, %[rw], s44 offen nt lds\n" \
"s_add_u32 m0, %[lw], 9216\ns_add_u32 s44, %[wo], 4259840\nbuffer_load_dwordx4 v92, %[rw], s44 offen nt lds\ns_add_u32 m0, %[lw], 10240\ns_add_u32 s44, %[wo], 4325376\nbuffer_load_dwordx4 v92, %[rw], s44 offen nt lds\ns_add_u32 m0, %[lw], 11264\ns_add_u32 s44, %[wo], 4390912\n" \
"buffer_load_dwordx4 v92, %[rw], s44 offen nt lds\ns_waitcnt vmcnt(32)\nds_read_b64_tr_b16 a[104:105], v94 offset:12288\nds_read_b64_tr_b16 a[106:107], v94 offset:12800\nds_read_b64_tr_b16 a[112:113], v94 offset:12296\nds_read_b64_tr_b16 a[114:115], v94 offset:12808\nds_read_b64_tr_b16 a[120:121], v94 offset:12352\nds_read_b64_tr_b16 a[122:123], v94 offset:12864\n" \
"ds_read_b64_tr_b16 a[128:129], v94 offset:12360\nds_read_b64_tr_b16 a[130:131], v94 offset:12872\ns_waitcnt lgkmcnt(0)\ns_add_u32 m0, %[lw], 12288\ns_add_u32 s44, %[wo], 4456448\nbuffer_load_dwordx4 v92, %[rw], s44 offen nt lds\ns_add_u32 m0, %[lw], 13312\ns_add_u32 s44, %[wo], 4521984\n" \
"buffer_load_dwordx4 v92, %[rw], s44 offen nt lds\ns_add_u32 m0, %[lw], 14336\ns_add_u32 s44, %[wo], 4587520\nbuffer_load_dwordx4 v92, %[rw], s44 offen nt lds\ns_add_u32 m0, %[lw], 15360\ns_add_u32 s44, %[wo], 4653056\nbuffer_load_dwordx4 v92, %[rw], s44 offen nt lds\ns_waitcnt vmcnt(32)\n" \
"ds_read_b64_tr_b16 a[108:109], v94 offset:16384\nds_read_b64_tr_b16 a[110:111], v94 offset:16896\nds_read_b64_tr_b16 a[116:117], v94 offset:16392\nds_read_b64_tr_b16 a[118:119], v94 offset:16904\nds_read_b64_tr_b16 a[124:125], v94 offset:16448\nds_read_b64_tr_b16 a[126:127], v94 offset:16960\nds_read_b64_tr_b16 a[132:133], v94 offset:16456\nds_read_b64_tr_b16 a[134:135], v94 offset:16968\n" \
"s_waitcnt lgkmcnt(0)\ns_add_u32 m0, %[lw], 16384\ns_add_u32 s44, %[wo], 4718592\nbuffer_load_dwordx4 v92, %[rw], s44 offen nt lds\ns_add_u32 m0, %[lw], 17408\ns_add_u32 s44, %[wo], 4784128\nbuffer_load_dwordx4 v92, %[rw], s44 offen nt lds\ns_add_u32 m0, %[lw], 18432\n" \
"s_add_u32 s44, %[wo], 4849664\nbuffer_load_dwordx4 v92, %[rw], s44 offen nt lds\ns_add_u32 m0, %[lw], 19456\ns_add_u32 s44, %[wo], 4915200\nbuffer_load_dwordx4 v92, %[rw], s44 offen nt lds\ns_waitcnt vmcnt(32)\nds_read_b64_tr_b16 a[136:137], v94 offset:20480\nds_read_b64_tr_b16 a[138:139], v94 offset:20992\n" \
"ds_read_b64_tr_b16 v[60:61], v94 offset:20488\nds_read_b64_tr_b16 v[62:63], v94 offset:21000\nds_read_b64_tr_b16 a[144:145], v94 offset:20544\nds_read_b64_tr_b16 a[146:147], v94 offset:21056\nds_read_b64_tr_b16 a[152:153], v94 offset:20552\nds_read_b64_tr_b16 a[154:155], v94 offset:21064\ns_waitcnt lgkmcnt(0)\ns_add_u32 m0, %[lw], 20480\n" \
"s_add_u32 s44, %[wo], 4980736\nbuffer_load_dwordx4 v92, %[rw], s44 offen nt lds\ns_add_u32 m0, %[lw], 21504\ns_add_u32 s44, %[wo], 5046272\nbuffer_load_dwordx4 v92, %[rw], s44 offen nt lds\ns_add_u32 m0, %[lw], 22528\ns_add_u32 s44, %[wo], 5111808\nbuffer_load_dwordx4 v92, %[rw], s44 offen nt lds\n" \
"s_add_u32 m0, %[lw], 23552\ns_add_u32 s44, %[wo], 5177344\nbuffer_load_dwordx4 v92, %[rw], s44 offen nt lds\ns_waitcnt vmcnt(32)\nds_read_b64_tr_b16 a[140:141], v93 offset:0\nds_read_b64_tr_b16 a[142:143], v93 offset:512\nds_read_b64_tr_b16 v[56:57], v93 offset:8\nds_read_b64_tr_b16 v[58:59], v93 offset:520\n" \
"ds_read_b64_tr_b16 a[148:149], v93 offset:64\nds_read_b64_tr_b16 a[150:151], v93 offset:576\nds_read_b64_tr_b16 a[156:157], v93 offset:72\nds_read_b64_tr_b16 a[158:159], v93 offset:584\ns_waitcnt lgkmcnt(0)\ns_add_u32 m0, %[tb], 0\ns_add_u32 s44, %[wo], 5242880\nbuffer_load_dwordx4 v92, %[rw], s44 offen nt lds\n" \
"s_add_u32 m0, %[tb], 1024\ns_add_u32 s44, %[wo], 5308416\nbuffer_load_dwordx4 v92, %[rw], s44 offen nt lds\ns_add_u32 m0, %[tb], 2048\ns_add_u32 s44, %[wo], 5373952\nbuffer_load_dwordx4 v92, %[rw], s44 offen nt lds\ns_add_u32 m0, %[tb], 3072\ns_add_u32 s44, %[wo], 5439488\n" \
"buffer_load_dwordx4 v92, %[rw], s44 offen nt lds\ns_waitcnt vmcnt(32)\nds_read_b64_tr_b16 a[160:161], v93 offset:4096\nds_read_b64_tr_b16 a[162:163], v93 offset:4608\nds_read_b64_tr_b16 a[168:169], v93 offset:4104\nds_read_b64_tr_b16 a[170:171], v93 offset:4616\nds_read_b64_tr_b16 v[52:53], v93 offset:4160\nds_read_b64_tr_b16 v[54:55], v93 offset:4672\n" \
"ds_read_b64_tr_b16 a[176:177], v93 offset:4168\nds_read_b64_tr_b16 a[178:179], v93 offset:4680\ns_waitcnt lgkmcnt(0)\ns_add_u32 m0, %[tb], 4096\ns_add_u32 s44, %[wo], 5505024\nbuffer_load_dwordx4 v92, %[rw], s44 offen nt lds\ns_add_u32 m0, %[tb], 5120\ns_add_u32 s44, %[wo], 5570560\n" \
"buffer_load_dwordx4 v92, %[rw], s44 offen nt lds\ns_add_u32 m0, %[tb], 6144\ns_add_u32 s44, %[wo], 5636096\nbuffer_load_dwordx4 v92, %[rw], s44 offen nt lds\ns_add_u32 m0, %[tb], 7168\ns_add_u32 s44, %[wo], 5701632\nbuffer_load_dwordx4 v92, %[rw], s44 offen nt lds\ns_waitcnt vmcnt(32)\n" \
"ds_read_b64_tr_b16 a[164:165], v93 offset:8192\nds_read_b64_tr_b16 a[166:167], v93 offset:8704\nds_read_b64_tr_b16 a[172:173], v93 offset:8200\nds_read_b64_tr_b16 a[174:175], v93 offset:8712\nds_read_b64_tr_b16 v[48:49], v93 offset:8256\nds_read_b64_tr_b16 v[50:51], v93 offset:8768\nds_read_b64_tr_b16 a[180:181], v93 offset:8264\nds_read_b64_tr_b16 a[182:183], v93 offset:8776\n" \
"s_waitcnt lgkmcnt(0)\ns_add_u32 m0, %[tb], 8192\ns_add_u32 s44, %[wo], 5767168\nbuffer_load_dwordx4 v92, %[rw], s44 offen nt lds\ns_add_u32 m0, %[tb], 9216\ns_add_u32 s44, %[wo], 5832704\nbuffer_load_dwordx4 v92, %[rw], s44 offen nt lds\ns_add_u32 m0, %[tb], 10240\n" \
"s_add_u32 s44, %[wo], 5898240\nbuffer_load_dwordx4 v92, %[rw], s44 offen nt lds\ns_add_u32 m0, %[tb], 11264\ns_add_u32 s44, %[wo], 5963776\nbuffer_load_dwordx4 v92, %[rw], s44 offen nt lds\ns_waitcnt vmcnt(32)\nds_read_b64_tr_b16 a[184:185], v94 offset:0\nds_read_b64_tr_b16 a[186:187], v94 offset:512\n" \
"ds_read_b64_tr_b16 a[192:193], v94 offset:8\nds_read_b64_tr_b16 a[194:195], v94 offset:520\nds_read_b64_tr_b16 a[200:201], v94 offset:64\nds_read_b64_tr_b16 a[202:203], v94 offset:576\nds_read_b64_tr_b16 v[44:45], v94 offset:72\nds_read_b64_tr_b16 v[46:47], v94 offset:584\ns_waitcnt lgkmcnt(0)\ns_add_u32 m0, %[lw], 0\n" \
"s_add_u32 s44, %[wo], 6029312\nbuffer_load_dwordx4 v92, %[rw], s44 offen nt lds\ns_add_u32 m0, %[lw], 1024\ns_add_u32 s44, %[wo], 6094848\nbuffer_load_dwordx4 v92, %[rw], s44 offen nt lds\ns_add_u32 m0, %[lw], 2048\ns_add_u32 s44, %[wo], 6160384\nbuffer_load_dwordx4 v92, %[rw], s44 offen nt lds\n" \
"s_add_u32 m0, %[lw], 3072\ns_add_u32 s44, %[wo], 6225920\nbuffer_load_dwordx4 v92, %[rw], s44 offen nt lds\ns_waitcnt vmcnt(32)\nds_read_b64_tr_b16 a[188:189], v94 offset:4096\nds_read_b64_tr_b16 a[190:191], v94 offset:4608\nds_read_b64_tr_b16 a[196:197], v94 offset:4104\nds_read_b64_tr_b16 a[198:199], v94 offset:4616\n" \
"ds_read_b64_tr_b16 a[204:205], v94 offset:4160\nds_read_b64_tr_b16 a[206:207], v94 offset:4672\nds_read_b64_tr_b16 v[40:41], v94 offset:4168\nds_read_b64_tr_b16 v[42:43], v94 offset:4680\ns_waitcnt lgkmcnt(0)\ns_add_u32 m0, %[lw], 4096\ns_add_u32 s44, %[wo], 6291456\nbuffer_load_dwordx4 v92, %[rw], s44 offen nt lds\n" \
"s_add_u32 m0, %[lw], 5120\ns_add_u32 s44, %[wo], 6356992\nbuffer_load_dwordx4 v92, %[rw], s44 offen nt lds\ns_add_u32 m0, %[lw], 6144\ns_add_u32 s44, %[wo], 6422528\nbuffer_load_dwordx4 v92, %[rw], s44 offen nt lds\ns_add_u32 m0, %[lw], 7168\ns_add_u32 s44, %[wo], 6488064\n" \
"buffer_load_dwordx4 v92, %[rw], s44 offen nt lds\ns_waitcnt vmcnt(32)\nds_read_b64_tr_b16 a[208:209], v94 offset:8192\nds_read_b64_tr_b16 a[210:211], v94 offset:8704\nds_read_b64_tr_b16 a[216:217], v94 offset:8200\nds_read_b64_tr_b16 a[218:219], v94 offset:8712\nds_read_b64_tr_b16 a[224:225], v94 offset:8256\nds_read_b64_tr_b16 a[226:227], v94 offset:8768\n" \
"ds_read_b64_tr_b16 a[232:233], v94 offset:8264\nds_read_b64_tr_b16 a[234:235], v94 offset:8776\ns_waitcnt lgkmcnt(0)\ns_add_u32 m0, %[lw], 8192\ns_add_u32 s44, %[wo], 6553600\nbuffer_load_dwordx4 v92, %[rw], s44 offen nt lds\ns_add_u32 m0, %[lw], 9216\ns_add_u32 s44, %[wo], 6619136\n" \
"buffer_load_dwordx4 v92, %[rw], s44 offen nt lds\ns_add_u32 m0, %[lw], 10240\ns_add_u32 s44, %[wo], 6684672\nbuffer_load_dwordx4 v92, %[rw], s44 offen nt lds\ns_add_u32 m0, %[lw], 11264\ns_add_u32 s44, %[wo], 6750208\nbuffer_load_dwordx4 v92, %[rw], s44 offen nt lds\ns_waitcnt vmcnt(32)\n" \
"ds_read_b64_tr_b16 a[212:213], v94 offset:12288\nds_read_b64_tr_b16 a[214:215], v94 offset:12800\nds_read_b64_tr_b16 a[220:221], v94 offset:12296\nds_read_b64_tr_b16 a[222:223], v94 offset:12808\nds_read_b64_tr_b16 a[228:229], v94 offset:12352\nds_read_b64_tr_b16 a[230:231], v94 offset:12864\nds_read_b64_tr_b16 a[236:237], v94 offset:12360\nds_read_b64_tr_b16 a[238:239], v94 offset:12872\n" \
"s_waitcnt lgkmcnt(0)\ns_add_u32 m0, %[lw], 12288\ns_add_u32 s44, %[wo], 6815744\nbuffer_load_dwordx4 v92, %[rw], s44 offen nt lds\ns_add_u32 m0, %[lw], 13312\ns_add_u32 s44, %[wo], 6881280\nbuffer_load_dwordx4 v92, %[rw], s44 offen nt lds\ns_add_u32 m0, %[lw], 14336\n" \
"s_add_u32 s44, %[wo], 6946816\nbuffer_load_dwordx4 v92, %[rw], s44 offen nt lds\ns_add_u32 m0, %[lw], 15360\ns_add_u32 s44, %[wo], 7012352\nbuffer_load_dwordx4 v92, %[rw], s44 offen nt lds\ns_waitcnt vmcnt(32)\nds_read_b64_tr_b16 a[240:241], v94 offset:16384\nds_read_b64_tr_b16 a[242:243], v94 offset:16896\n" \
"ds_read_b64_tr_b16 v[36:37], v94 offset:16392\nds_read_b64_tr_b16 v[38:39], v94 offset:16904\nds_read_b64_tr_b16 a[248:249], v94 offset:16448\nds_read_b64_tr_b16 a[250:251], v94 offset:16960\nds_read_b64_tr_b16 v[96:97], v94 offset:16456\nds_read_b64_tr_b16 v[98:99], v94 offset:16968\ns_waitcnt lgkmcnt(0)\ns_add_u32 m0, %[lw], 16384\n" \
"s_add_u32 s44, %[wo], 7077888\nbuffer_load_dwordx4 v92, %[rw], s44 offen nt lds\ns_add_u32 m0, %[lw], 17408\ns_add_u32 s44, %[wo], 7143424\nbuffer_load_dwordx4 v92, %[rw], s44 offen nt lds\ns_add_u32 m0, %[lw], 18432\ns_add_u32 s44, %[wo], 7208960\nbuffer_load_dwordx4 v92, %[rw], s44 offen nt lds\n" \
"s_add_u32 m0, %[lw], 19456\ns_add_u32 s44, %[wo], 7274496\nbuffer_load_dwordx4 v92, %[rw], s44 offen nt lds\ns_waitcnt vmcnt(32)\nds_read_b64_tr_b16 a[244:245], v94 offset:20480\nds_read_b64_tr_b16 a[246:247], v94 offset:20992\nds_read_b64_tr_b16 v[32:33], v94 offset:20488\nds_read_b64_tr_b16 v[34:35], v94 offset:21000\n" \
"ds_read_b64_tr_b16 a[252:253], v94 offset:20544\nds_read_b64_tr_b16 a[254:255], v94 offset:21056\nds_read_b64_tr_b16 v[100:101], v94 offset:20552\nds_read_b64_tr_b16 v[102:103], v94 offset:21064\ns_waitcnt lgkmcnt(0)\ns_add_u32 m0, %[lw], 20480\ns_add_u32 s44, %[wo], 7340032\nbuffer_load_dwordx4 v92, %[rw], s44 offen nt lds\n" \
"s_add_u32 m0, %[lw], 21504\ns_add_u32 s44, %[wo], 7405568\nbuffer_load_dwordx4 v92, %[rw], s44 offen nt lds\ns_add_u32 m0, %[lw], 22528\ns_add_u32 s44, %[wo], 7471104\nbuffer_load_dwordx4 v92, %[rw], s44 offen nt lds\ns_add_u32 m0, %[lw], 23552\ns_add_u32 s44, %[wo], 7536640\n" \
"buffer_load_dwordx4 v92, %[rw], s44 offen nt lds\ns_waitcnt vmcnt(32)\nds_read_b64_tr_b16 v[104:105], v93 offset:0\nds_read_b64_tr_b16 v[106:107], v93 offset:512\nds_read_b64_tr_b16 v[112:113], v93 offset:8\nds_read_b64_tr_b16 v[114:115], v93 offset:520\nds_read_b64_tr_b16 v[28:29], v93 offset:64\nds_read_b64_tr_b16 v[30:31], v93 offset:576\n" \
"ds_read_b64_tr_b16 v[120:121], v93 offset:72\nds_read_b64_tr_b16 v[122:123], v93 offset:584\ns_waitcnt lgkmcnt(0)\ns_add_u32 m0, %[tb], 0\ns_add_u32 s44, %[wo], 7602176\nbuffer_load_dwordx4 v92, %[rw], s44 offen nt lds\ns_add_u32 m0, %[tb], 1024\ns_add_u32 s44, %[wo], 7667712\n" \
"buffer_load_dwordx4 v92, %[rw], s44 offen nt lds\ns_add_u32 m0, %[tb], 2048\ns_add_u32 s44, %[wo], 7733248\nbuffer_load_dwordx4 v92, %[rw], s44 offen nt lds\ns_add_u32 m0, %[tb], 3072\ns_add_u32 s44, %[wo], 7798784\nbuffer_load_dwordx4 v92, %[rw], s44 offen nt lds\ns_waitcnt vmcnt(32)\n" \
"ds_read_b64_tr_b16 v[108:109], v93 offset:4096\nds_read_b64_tr_b16 v[110:111], v93 offset:4608\nds_read_b64_tr_b16 v[116:117], v93 offset:4104\nds_read_b64_tr_b16 v[118:119], v93 offset:4616\nds_read_b64_tr_b16 v[24:25], v93 offset:4160\nds_read_b64_tr_b16 v[26:27], v93 offset:4672\nds_read_b64_tr_b16 v[124:125], v93 offset:4168\nds_read_b64_tr_b16 v[126:127], v93 offset:4680\n" \
"s_waitcnt lgkmcnt(0)\nbuffer_load_dwordx2 v[88:89], off, s[36:39], s45 sc1\ns_add_u32 m0, %[tb], 4096\ns_add_u32 s44, %[wo], 7864320\nbuffer_load_dwordx4 v92, %[rw], s44 offen nt lds\ns_add_u32 m0, %[tb], 5120\ns_add_u32 s44, %[wo], 7929856\nbuffer_load_dwordx4 v92, %[rw], s44 offen nt lds\n" \
"s_add_u32 m0, %[tb], 6144\ns_add_u32 s44, %[wo], 7995392\nbuffer_load_dwordx4 v92, %[rw], s44 offen nt lds\ns_add_u32 m0, %[tb], 7168\ns_add_u32 s44, %[wo], 8060928\nbuffer_load_dwordx4 v92, %[rw], s44 offen nt lds\ns_waitcnt vmcnt(33)\nds_read_b64_tr_b16 v[128:129], v93 offset:8192\n" \
"ds_read_b64_tr_b16 v[130:131], v93 offset:8704\nds_read_b64_tr_b16 v[136:137], v93 offset:8200\nds_read_b64_tr_b16 v[138:139], v93 offset:8712\nds_read_b64_tr_b16 v[144:145], v93 offset:8256\nds_read_b64_tr_b16 v[146:147], v93 offset:8768\nds_read_b64_tr_b16 v[20:21], v93 offset:8264\nds_read_b64_tr_b16 v[22:23], v93 offset:8776\ns_waitcnt lgkmcnt(0)\n" \
"s_add_u32 m0, %[tb], 8192\ns_add_u32 s44, %[wo], 8126464\nbuffer_load_dwordx4 v92, %[rw], s44 offen nt lds\ns_add_u32 m0, %[tb], 9216\ns_add_u32 s44, %[wo], 8192000\nbuffer_load_dwordx4 v92, %[rw], s44 offen nt lds\ns_add_u32 m0, %[tb], 10240\ns_add_u32 s44, %[wo], 8257536\n" \
"buffer_load_dwordx4 v92, %[rw], s44 offen nt lds\ns_add_u32 m0, %[tb], 11264\ns_add_u32 s44, %[wo], 8323072\nbuffer_load_dwordx4 v92, %[rw], s44 offen nt lds\ns_waitcnt vmcnt(33)\nds_read_b64_tr_b16 v[132:133], v94 offset:0\nds_read_b64_tr_b16 v[134:135], v94 offset:512\nds_read_b64_tr_b16 v[140:141], v94 offset:8\n" \
"ds_read_b64_tr_b16 v[142:143], v94 offset:520\nds_read_b64_tr_b16 v[148:149], v94 offset:64\nds_read_b64_tr_b16 v[150:151], v94 offset:576\nds_read_b64_tr_b16 v[16:17], v94 offset:72\nds_read_b64_tr_b16 v[18:19], v94 offset:584\ns_waitcnt lgkmcnt(0)\nds_write_b128 v1, v[84:87] offset:0\nds_write_b128 v1, v[80:83] offset:1024\n" \
"ds_write_b128 v1, v[76:79] offset:2048\nds_write_b128 v1, v[72:75] offset:3072\ns_waitcnt vmcnt(29)\nds_read_b64_tr_b16 v[152:153], v94 offset:4096\nds_read_b64_tr_b16 v[154:155], v94 offset:4608\nds_read_b64_tr_b16 v[160:161], v94 offset:4104\nds_read_b64_tr_b16 v[162:163], v94 offset:4616\nds_read_b64_tr_b16 v[168:169], v94 offset:4160\n" \
"ds_read_b64_tr_b16 v[170:171], v94 offset:4672\nds_read_b64_tr_b16 v[176:177], v94 offset:4168\nds_read_b64_tr_b16 v[178:179], v94 offset:4680\ns_waitcnt lgkmcnt(0)\nds_write_b128 v1, v[68:71] offset:4096\nds_write_b128 v1, v[64:67] offset:5120\nds_write_b128 v1, v[60:63] offset:6144\nds_write_b128 v1, v[56:59] offset:7168\n" \
"s_waitcnt vmcnt(25)\nds_read_b64_tr_b16 v[156:157], v94 offset:8192\nds_read_b64_tr_b16 v[158:159], v94 offset:8704\nds_read_b64_tr_b16 v[164:165], v94 offset:8200\nds_read_b64_tr_b16 v[166:167], v94 offset:8712\nds_read_b64_tr_b16 v[172:173], v94 offset:8256\nds_read_b64_tr_b16 v[174:175], v94 offset:8768\nds_read_b64_tr_b16 v[180:181], v94 offset:8264\n" \
"ds_read_b64_tr_b16 v[182:183], v94 offset:8776\ns_waitcnt lgkmcnt(0)\nds_write_b128 v1, v[52:55] offset:8192\nds_write_b128 v1, v[48:51] offset:9216\nds_write_b128 v1, v[44:47] offset:10240\nds_write_b128 v1, v[40:43] offset:11264\ns_waitcnt vmcnt(21)\nds_read_b64_tr_b16 v[184:185], v94 offset:12288\n" \
"ds_read_b64_tr_b16 v[186:187], v94 offset:12800\nds_read_b64_tr_b16 v[40:41], v94 offset:12296\nds_read_b64_tr_b16 v[42:43], v94 offset:12808\nds_read_b64_tr_b16 v[192:193], v94 offset:12352\nds_read_b64_tr_b16 v[194:195], v94 offset:12864\nds_read_b64_tr_b16 v[200:201], v94 offset:12360\nds_read_b64_tr_b16 v[202:203], v94 offset:12872\ns_waitcnt lgkmcnt(0)\n" \
"ds_write_b128 v1, v[36:39] offset:12288\nds_write_b128 v1, v[32:35] offset:13312\nds_write_b128 v1, v[28:31] offset:14336\nds_write_b128 v1, v[24:27] offset:15360\ns_waitcnt vmcnt(17)\nds_read_b64_tr_b16 v[188:189], v94 offset:16384\nds_read_b64_tr_b16 v[190:191], v94 offset:16896\nds_read_b64_tr_b16 v[24:25], v94 offset:16392\n" \
"ds_read_b64_tr_b16 v[26:27], v94 offset:16904\nds_read_b64_tr_b16 v[196:197], v94 offset:16448\nds_read_b64_tr_b16 v[198:199], v94 offset:16960\nds_read_b64_tr_b16 v[204:205], v94 offset:16456\nds_read_b64_tr_b16 v[206:207], v94 offset:16968\ns_waitcnt lgkmcnt(0)\nds_write_b128 v1, v[20:23] offset:16384\nds_write_b128 v1, v[16:19] offset:17408\n" \
"ds_write_b128 v1, v[40:43] offset:18432\nds_write_b128 v1, v[24:27] offset:19456\ns_waitcnt vmcnt(13)\nds_read_b64_tr_b16 v[208:209], v94 offset:20480\nds_read_b64_tr_b16 v[210:211], v94 offset:20992\nds_read_b64_tr_b16 v[216:217], v94 offset:20488\nds_read_b64_tr_b16 v[218:219], v94 offset:21000\nds_read_b64_tr_b16 v[24:25], v94 offset:20544\n" \
"ds_read_b64_tr_b16 v[26:27], v94 offset:21056\nds_read_b64_tr_b16 v[224:225], v94 offset:20552\nds_read_b64_tr_b16 v[226:227], v94 offset:21064\ns_waitcnt lgkmcnt(0)\nds_write_b128 v1, v[24:27] offset:20480\ns_waitcnt vmcnt(9)\nds_read_b64_tr_b16 v[212:213], v93 offset:0\nds_read_b64_tr_b16 v[214:215], v93 offset:512\n" \
"ds_read_b64_tr_b16 v[220:221], v93 offset:8\nds_read_b64_tr_b16 v[222:223], v93 offset:520\nds_read_b64_tr_b16 v[24:25], v93 offset:64\nds_read_b64_tr_b16 v[26:27], v93 offset:576\nds_read_b64_tr_b16 v[228:229], v93 offset:72\nds_read_b64_tr_b16 v[230:231], v93 offset:584\ns_waitcnt lgkmcnt(0)\nds_write_b128 v1, v[24:27] offset:21504\n" \
"s_waitcnt vmcnt(4)\nds_read_b64_tr_b16 v[232:233], v93 offset:4096\nds_read_b64_tr_b16 v[234:235], v93 offset:4608\nds_read_b64_tr_b16 v[240:241], v93 offset:4104\nds_read_b64_tr_b16 v[242:243], v93 offset:4616\nds_read_b64_tr_b16 v[248:249], v93 offset:4160\nds_read_b64_tr_b16 v[250:251], v93 offset:4672\nds_read_b64_tr_b16 v[24:25], v93 offset:4168\n" \
"ds_read_b64_tr_b16 v[26:27], v93 offset:4680\ns_waitcnt lgkmcnt(0)\nds_write_b128 v1, v[24:27] offset:22528\ns_waitcnt vmcnt(0)\nds_read_b64_tr_b16 v[236:237], v93 offset:8192\nds_read_b64_tr_b16 v[238:239], v93 offset:8704\nds_read_b64_tr_b16 v[244:245], v93 offset:8200\nds_read_b64_tr_b16 v[246:247], v93 offset:8712\n" \
"ds_read_b64_tr_b16 v[252:253], v93 offset:8256\nds_read_b64_tr_b16 v[254:255], v93 offset:8768\nds_read_b64_tr_b16 v[24:25], v93 offset:8264\nds_read_b64_tr_b16 v[26:27], v93 offset:8776\ns_waitcnt lgkmcnt(0)\ns_mov_b32 s43, 0\ns_mov_b32 s86, 110\ns_mov_b32 s89, 0\n" \
".Lqk_hb%=:\nv_cmp_ne_u32 vcc, v95, v88\ns_cbranch_vccnz .Lqk_hb1%=\ns_sleep 4\nbuffer_load_dwordx2 v[88:89], off, s[36:39], s45 sc1\ns_waitcnt vmcnt(0)\ns_add_u32 s43, s43, 1\ns_cmp_lt_u32 s43, 262144\n" \
"s_cbranch_scc1 .Lqk_hb%=\ns_branch .Lqk_abort%=\n.Lqk_hb1%=:\nv_readfirstlane_b32 s44, v89\ns_and_b32 s44, s44, 0x200000\ns_add_u32 s36, s36, s44\ns_addc_u32 s37, s37, 0\nds_write_b128 v1, v[24:27] offset:23552\n" \
"s_waitcnt lgkmcnt(0)\ns_barrier\ns_mov_b32 s40, -1\ns_mov_b32 s43, 0\ns_mov_b32 s61, 0x7fff7fff\ns_mov_b32 s58, 0x3e0293ee\ns_mov_b32 s59, 0xbe0293ee\ns_lshl_b32 s57, %[w], 12\n" \
"s_mov_b32 s62, 0xff00ff00\ns_mov_b32 s63, 0xff00ff00\ns_mov_b32 s56, 0\ns_cmp_eq_u32 %[w], 3\ns_cbranch_scc1 .Lqk_fetch0%=\n.Lqk_pwait%=:\ns_barrier\nds_read_b128 v[80:83], v2 offset:0\n" \
"ds_read_b128 v[84:87], v2 offset:256\nds_read_b128 v[88:91], v2 offset:2048\n.Lqk_proj%=:\ns_waitcnt lgkmcnt(2)\nv_mfma_f32_16x16x32_bf16 v[16:19], a[0:3], v[80:83], 0\nds_read_b128 v[92:95], v2 offset:2304\nv_mfma_f32_16x16x32_bf16 v[20:23], a[8:11], v[80:83], 0\nds_read_b128 v[72:75], v1 offset:0\n" \
"v_mfma_f32_16x16x32_bf16 v[24:27], a[16:19], v[80:83], 0\nv_mfma_f32_16x16x32_bf16 v[28:31], a[24:27], v[80:83], 0\ns_waitcnt lgkmcnt(3)\nv_mfma_f32_16x16x32_bf16 v[32:35], a[0:3], v[84:87], 0\nds_read_b128 v[80:83], v2 offset:4096\nv_mfma_f32_16x16x32_bf16 v[36:39], a[8:11], v[84:87], 0\nv_mfma_f32_16x16x32_bf16 v[40:43], a[16:19], v[84:87], 0\nv_mfma_f32_16x16x32_bf16 v[44:47], a[24:27], v[84:87], 0\n" \
"s_waitcnt lgkmcnt(3)\nv_mfma_f32_16x16x32_bf16 v[16:19], a[4:7], v[88:91], v[16:19]\nds_read_b128 v[84:87], v2 offset:4352\nv_mfma_f32_16x16x32_bf16 v[20:23], a[12:15], v[88:91], v[20:23]\nds_read_b128 v[76:79], v1 offset:1024\nv_mfma_f32_16x16x32_bf16 v[24:27], a[20:23], v[88:91], v[24:27]\nv_mfma_f32_16x16x32_bf16 v[28:31], a[28:31], v[88:91], v[28:31]\ns_waitcnt lgkmcnt(4)\n" \
"v_mfma_f32_16x16x32_bf16 v[32:35], a[4:7], v[92:95], v[32:35]\nds_read_b128 v[88:91], v2 offset:6144\nv_mfma_f32_16x16x32_bf16 v[36:39], a[12:15], v[92:95], v[36:39]\nv_mfma_f32_16x16x32_bf16 v[40:43], a[20:23], v[92:95], v[40:43]\nv_mfma_f32_16x16x32_bf16 v[44:47], a[28:31], v[92:95], v[44:47]\ns_waitcnt lgkmcnt(3)\nv_mfma_f32_16x16x32_bf16 v[16:19], a[32:35], v[80:83], v[16:19]\nds_read_b128 v[92:95], v2 offset:6400\n" \
"v_mfma_f32_16x16x32_bf16 v[20:23], v[72:75], v[80:83], v[20:23]\nds_read_b128 v[64:67], v1 offset:2048\nv_mfma_f32_16x16x32_bf16 v[24:27], a[40:43], v[80:83], v[24:27]\nv_mfma_f32_16x16x32_bf16 v[28:31], a[48:51], v[80:83], v[28:31]\ns_waitcnt lgkmcnt(4)\nv_mfma_f32_16x16x32_bf16 v[32:35], a[32:35], v[84:87], v[32:35]\nds_read_b128 v[80:83], v2 offset:8192\nv_mfma_f32_16x16x32_bf16 v[36:39], v[72:75], v[84:87], v[36:39]\n" \
"v_mfma_f32_16x16x32_bf16 v[40:43], a[40:43], v[84:87], v[40:43]\nv_mfma_f32_16x16x32_bf16 v[44:47], a[48:51], v[84:87], v[44:47]\ns_waitcnt lgkmcnt(3)\nv_mfma_f32_16x16x32_bf16 v[16:19], a[36:39], v[88:91], v[16:19]\nds_read_b128 v[84:87], v2 offset:8448\nv_mfma_f32_16x16x32_bf16 v[20:23], v[76:79], v[88:91], v[20:23]\nds_read_b128 v[68:71], v1 offset:3072\nv_mfma_f32_16x16x32_bf16 v[24:27], a[44:47], v[88:91], v[24:27]\n" \
"v_mfma_f32_16x16x32_bf16 v[28:31], a[52:55], v[88:91], v[28:31]\ns_waitcnt lgkmcnt(4)\nv_mfma_f32_16x16x32_bf16 v[32:35], a[36:39], v[92:95], v[32:35]\nds_read_b128 v[88:91], v2 offset:10240\nv_mfma_f32_16x16x32_bf16 v[36:39], v[76:79], v[92:95], v[36:39]\nv_mfma_f32_16x16x32_bf16 v[40:43], a[44:47], v[92:95], v[40:43]\nv_mfma_f32_16x16x32_bf16 v[44:47], a[52:55], v[92:95], v[44:47]\ns_waitcnt lgkmcnt(3)\n" \
"v_mfma_f32_16x16x32_bf16 v[16:19], a[56:59], v[80:83], v[16:19]\nds_read_b128 v[92:95], v2 offset:10496\nv_mfma_f32_16x16x32_bf16 v[20:23], a[64:67], v[80:83], v[20:23]\nds_read_b128 v[72:75], v1 offset:4096\nv_mfma_f32_16x16x32_bf16 v[24:27], v[64:67], v[80:83], v[24:27]\nv_mfma_f32_16x16x32_bf16 v[28:31], a[72:75], v[80:83], v[28:31]\ns_waitcnt lgkmcnt(4)\nv_mfma_f32_16x16x32_bf16 v[32:35], a[56:59], v[84:87], v[32:35]\n" \
"ds_read_b128 v[80:83], v2 offset:12288\nv_mfma_f32_16x16x32_bf16 v[36:39], a[64:67], v[84:87], v[36:39]\nv_mfma_f32_16x16x32_bf16 v[40:43], v[64:67], v[84:87], v[40:43]\nv_mfma_f32_16x16x32_bf16 v[44:47], a[72:75], v[84:87], v[44:47]\ns_waitcnt lgkmcnt(3)\nv_mfma_f32_16x16x32_bf16 v[16:19], a[60:63], v[88:91], v[16:19]\nds_read_b128 v[84:87], v2 offset:12544\nv_mfma_f32_16x16x32_bf16 v[20:23], a[68:71], v[88:91], v[20:23]\n" \
"ds_read_b128 v[76:79], v1 offset:5120\nv_mfma_f32_16x16x32_bf16 v[24:27], v[68:71], v[88:91], v[24:27]\nv_mfma_f32_16x16x32_bf16 v[28:31], a[76:79], v[88:91], v[28:31]\ns_waitcnt lgkmcnt(4)\nv_mfma_f32_16x16x32_bf16 v[32:35], a[60:63], v[92:95], v[32:35]\nds_read_b128 v[88:91], v2 offset:14336\nv_mfma_f32_16x16x32_bf16 v[36:39], a[68:71], v[92:95], v[36:39]\nv_mfma_f32_16x16x32_bf16 v[40:43], v[68:71], v[92:95], v[40:43]\n" \
"v_mfma_f32_16x16x32_bf16 v[44:47], a[76:79], v[92:95], v[44:47]\n.Lqk_pj%=:\ns_waitcnt lgkmcnt(3)\nv_mfma_f32_16x16x32_bf16 v[16:19], a[80:83], v[80:83], v[16:19]\nds_read_b128 v[92:95], v2 offset:14592\nv_mfma_f32_16x16x32_bf16 v[20:23], a[88:91], v[80:83], v[20:23]\ns_add_u32 s40, s40, 1\nv_mfma_f32_16x16x32_bf16 v[24:27], a[96:99], v[80:83], v[24:27]\n" \
"s_lshl_b32 s44, s40, 17\nv_mfma_f32_16x16x32_bf16 v[28:31], v[72:75], v[80:83], v[28:31]\ns_add_u32 s44, s44, %[xn]\ns_waitcnt lgkmcnt(3)\nv_mfma_f32_16x16x32_bf16 v[32:35], a[80:83], v[84:87], v[32:35]\ns_barrier\nds_read_b128 v[80:83], v2 offset:16384\nv_mfma_f32_16x16x32_bf16 v[36:39], a[88:91], v[84:87], v[36:39]\n" \
"s_lshr_b32 s45, s44, 8\nv_mfma_f32_16x16x32_bf16 v[40:43], a[96:99], v[84:87], v[40:43]\ns_and_b32 s45, s45, 0x2000\nv_mfma_f32_16x16x32_bf16 v[44:47], v[72:75], v[84:87], v[44:47]\ns_xor_b32 s41, s44, s45\ns_waitcnt lgkmcnt(2)\nv_mfma_f32_16x16x32_bf16 v[16:19], a[84:87], v[88:91], v[16:19]\nds_read_b128 v[84:87], v2 offset:16640\n" \
"v_mfma_f32_16x16x32_bf16 v[20:23], a[92:95], v[88:91], v[20:23]\ns_add_u32 s41, s41, %[xw]\nv_mfma_f32_16x16x32_bf16 v[24:27], a[100:103], v[88:91], v[24:27]\ns_and_b32 s42, s40, 3\ns_add_u32 m0, s57, 32768\ns_add_u32 s60, s41, 65536\nbuffer_load_dwordx4 v0, s[36:39], s60 offen offset:0 sc1 lds\nv_mfma_f32_16x16x32_bf16 v[28:31], v[76:79], v[88:91], v[28:31]\n" \
"s_lshl_b32 s42, s42, 15\ns_waitcnt lgkmcnt(2)\nv_mfma_f32_16x16x32_bf16 v[32:35], a[84:87], v[92:95], v[32:35]\nds_read_b128 v[88:91], v2 offset:18432\nv_mfma_f32_16x16x32_bf16 v[36:39], a[92:95], v[92:95], v[36:39]\ns_and_b32 s44, s40, 1\nv_mfma_f32_16x16x32_bf16 v[40:43], a[100:103], v[92:95], v[40:43]\ns_lshl_b32 s55, s44, 17\n" \
"buffer_load_dwordx4 v0, s[36:39], s60 offen offset:1024 sc1 lds\nv_mfma_f32_16x16x32_bf16 v[44:47], v[76:79], v[92:95], v[44:47]\ns_add_u32 s56, s56, %[fs]\ns_waitcnt lgkmcnt(2)\nv_mfma_f32_16x16x32_bf16 v[16:19], a[104:107], v[80:83], v[16:19]\nds_read_b128 v[92:95], v2 offset:18688\nv_mfma_f32_16x16x32_bf16 v[20:23], a[112:115], v[80:83], v[20:23]\nds_read_b128 v[72:75], v1 offset:6144\n" \
"v_mfma_f32_16x16x32_bf16 v[24:27], a[120:123], v[80:83], v[24:27]\nbuffer_load_dwordx4 v0, s[36:39], s60 offen offset:2048 sc1 lds\nv_mfma_f32_16x16x32_bf16 v[28:31], a[128:131], v[80:83], v[28:31]\ns_waitcnt lgkmcnt(3)\nv_mfma_f32_16x16x32_bf16 v[32:35], a[104:107], v[84:87], v[32:35]\nds_read_b128 v[80:83], v2 offset:20480\nv_mfma_f32_16x16x32_bf16 v[36:39], a[112:115], v[84:87], v[36:39]\nv_mfma_f32_16x16x32_bf16 v[40:43], a[120:123], v[84:87], v[40:43]\n" \
"buffer_load_dwordx4 v0, s[36:39], s60 offen offset:3072 sc1 lds\nv_mfma_f32_16x16x32_bf16 v[44:47], a[128:131], v[84:87], v[44:47]\ns_waitcnt lgkmcnt(3)\nv_mfma_f32_16x16x32_bf16 v[16:19], a[108:111], v[88:91], v[16:19]\nds_read_b128 v[84:87], v2 offset:20736\nv_mfma_f32_16x16x32_bf16 v[20:23], a[116:119], v[88:91], v[20:23]\nds_read_b128 v[76:79], v1 offset:7168\nv_mfma_f32_16x16x32_bf16 v[24:27], a[124:127], v[88:91], v[24:27]\n" \
"v_mfma_f32_16x16x32_bf16 v[28:31], a[132:135], v[88:91], v[28:31]\ns_waitcnt lgkmcnt(4)\nv_mfma_f32_16x16x32_bf16 v[32:35], a[108:111], v[92:95], v[32:35]\nds_read_b128 v[88:91], v2 offset:22528\nv_mfma_f32_16x16x32_bf16 v[36:39], a[116:119], v[92:95], v[36:39]\nv_mfma_f32_16x16x32_bf16 v[40:43], a[124:127], v[92:95], v[40:43]\nv_mfma_f32_16x16x32_bf16 v[44:47], a[132:135], v[92:95], v[44:47]\ns_waitcnt lgkmcnt(3)\n" \
"v_mfma_f32_16x16x32_bf16 v[16:19], a[136:139], v[80:83], v[16:19]\nds_read_b128 v[92:95], v2 offset:22784\nv_mfma_f32_16x16x32_bf16 v[20:23], v[72:75], v[80:83], v[20:23]\nds_read_b128 v[64:67], v1 offset:8192\nv_mfma_f32_16x16x32_bf16 v[24:27], a[144:147], v[80:83], v[24:27]\nv_mfma_f32_16x16x32_bf16 v[28:31], a[152:155], v[80:83], v[28:31]\ns_waitcnt lgkmcnt(4)\nv_mfma_f32_16x16x32_bf16 v[32:35], a[136:139], v[84:87], v[32:35]\n" \
"ds_read_b128 v[80:83], v2 offset:24576\nv_mfma_f32_16x16x32_bf16 v[36:39], v[72:75], v[84:87], v[36:39]\nv_mfma_f32_16x16x32_bf16 v[40:43], a[144:147], v[84:87], v[40:43]\nv_mfma_f32_16x16x32_bf16 v[44:47], a[152:155], v[84:87], v[44:47]\ns_waitcnt lgkmcnt(3)\nv_mfma_f32_16x16x32_bf16 v[16:19], a[140:143], v[88:91], v[16:19]\nds_read_b128 v[84:87], v2 offset:24832\nv_mfma_f32_16x16x32_bf16 v[20:23], v[76:79], v[88:91], v[20:23]\n" \
"ds_read_b128 v[68:71], v1 offset:9216\nv_mfma_f32_16x16x32_bf16 v[24:27], a[148:151], v[88:91], v[24:27]\nv_mfma_f32_16x16x32_bf16 v[28:31], a[156:159], v[88:91], v[28:31]\ns_waitcnt lgkmcnt(4)\nv_mfma_f32_16x16x32_bf16 v[32:35], a[140:143], v[92:95], v[32:35]\nds_read_b128 v[88:91], v2 offset:26624\nv_mfma_f32_16x16x32_bf16 v[36:39], v[76:79], v[92:95], v[36:39]\nv_mfma_f32_16x16x32_bf16 v[40:43], a[148:151], v[92:95], v[40:43]\n" \
"v_mfma_f32_16x16x32_bf16 v[44:47], a[156:159], v[92:95], v[44:47]\ns_waitcnt lgkmcnt(3)\nv_mfma_f32_16x16x32_bf16 v[16:19], a[160:163], v[80:83], v[16:19]\nds_read_b128 v[92:95], v2 offset:26880\nv_mfma_f32_16x16x32_bf16 v[20:23], a[168:171], v[80:83], v[20:23]\nds_read_b128 v[72:75], v1 offset:10240\nv_mfma_f32_16x16x32_bf16 v[24:27], v[64:67], v[80:83], v[24:27]\nv_mfma_f32_16x16x32_bf16 v[28:31], a[176:179], v[80:83], v[28:31]\n" \
"s_waitcnt lgkmcnt(4)\nv_mfma_f32_16x16x32_bf16 v[32:35], a[160:163], v[84:87], v[32:35]\nds_read_b128 v[80:83], v2 offset:28672\nv_mfma_f32_16x16x32_bf16 v[36:39], a[168:171], v[84:87], v[36:39]\nv_mfma_f32_16x16x32_bf16 v[40:43], v[64:67], v[84:87], v[40:43]\nv_mfma_f32_16x16x32_bf16 v[44:47], a[176:179], v[84:87], v[44:47]\ns_waitcnt lgkmcnt(3)\nv_mfma_f32_16x16x32_bf16 v[16:19], a[164:167], v[88:91], v[16:19]\n" \
"ds_read_b128 v[84:87], v2 offset:28928\nv_mfma_f32_16x16x32_bf16 v[20:23], a[172:175], v[88:91], v[20:23]\nds_read_b128 v[76:79], v1 offset:11264\nv_mfma_f32_16x16x32_bf16 v[24:27], v[68:71], v[88:91], v[24:27]\nv_mfma_f32_16x16x32_bf16 v[28:31], a[180:183], v[88:91], v[28:31]\ns_waitcnt lgkmcnt(4)\nv_mfma_f32_16x16x32_bf16 v[32:35], a[164:167], v[92:95], v[32:35]\nds_read_b128 v[88:91], v2 offset:30720\n" \
"v_mfma_f32_16x16x32_bf16 v[36:39], a[172:175], v[92:95], v[36:39]\nv_mfma_f32_16x16x32_bf16 v[40:43], v[68:71], v[92:95], v[40:43]\nv_mfma_f32_16x16x32_bf16 v[44:47], a[180:183], v[92:95], v[44:47]\ns_waitcnt lgkmcnt(3)\nv_mfma_f32_16x16x32_bf16 v[16:19], a[184:187], v[80:83], v[16:19]\nds_read_b128 v[92:95], v2 offset:30976\nv_mfma_f32_16x16x32_bf16 v[20:23], a[192:195], v[80:83], v[20:23]\nv_mfma_f32_16x16x32_bf16 v[24:27], a[200:203], v[80:83], v[24:27]\n" \
"v_mfma_f32_16x16x32_bf16 v[28:31], v[72:75], v[80:83], v[28:31]\ns_waitcnt lgkmcnt(3)\nv_mfma_f32_16x16x32_bf16 v[32:35], a[184:187], v[84:87], v[32:35]\ns_waitcnt vmcnt(0)\ns_barrier\nds_read_b128 v[48:51], v2 offset:32768\nv_mfma_f32_16x16x32_bf16 v[36:39], a[192:195], v[84:87], v[36:39]\nv_mfma_f32_16x16x32_bf16 v[40:43], a[200:203], v[84:87], v[40:43]\n" \
"s_add_u32 m0, s57, 0\ns_add_u32 s60, s41, 98304\nbuffer_load_dwordx4 v0, s[36:39], s60 offen offset:0 sc1 lds\nv_mfma_f32_16x16x32_bf16 v[44:47], v[72:75], v[84:87], v[44:47]\ns_waitcnt lgkmcnt(2)\nv_mfma_f32_16x16x32_bf16 v[16:19], a[188:191], v[88:91], v[16:19]\nds_read_b128 v[52:55], v2 offset:33024\nv_mfma_f32_16x16x32_bf16 v[20:23], a[196:199], v[88:91], v[20:23]\n" \
"v_mfma_f32_16x16x32_bf16 v[24:27], a[204:207], v[88:91], v[24:27]\nbuffer_load_dwordx4 v0, s[36:39], s60 offen offset:1024 sc1 lds\nv_mfma_f32_16x16x32_bf16 v[28:31], v[76:79], v[88:91], v[28:31]\ns_waitcnt lgkmcnt(2)\nv_mfma_f32_16x16x32_bf16 v[32:35], a[188:191], v[92:95], v[32:35]\nds_read_b128 v[56:59], v2 offset:34816\nv_mfma_f32_16x16x32_bf16 v[36:39], a[196:199], v[92:95], v[36:39]\nv_mfma_f32_16x16x32_bf16 v[40:43], a[204:207], v[92:95], v[40:43]\n" \
"buffer_load_dwordx4 v0, s[36:39], s60 offen offset:2048 sc1 lds\nv_mfma_f32_16x16x32_bf16 v[44:47], v[76:79], v[92:95], v[44:47]\ns_waitcnt lgkmcnt(2)\nv_mfma_f32_16x16x32_bf16 v[16:19], a[208:211], v[48:51], v[16:19]\nds_read_b128 v[60:63], v2 offset:35072\nv_mfma_f32_16x16x32_bf16 v[20:23], a[216:219], v[48:51], v[20:23]\nds_read_b128 v[72:75], v1 offset:12288\nv_mfma_f32_16x16x32_bf16 v[24:27], a[224:227], v[48:51], v[24:27]\n" \
"buffer_load_dwordx4 v0, s[36:39], s60 offen offset:3072 sc1 lds\nv_mfma_f32_16x16x32_bf16 v[28:31], a[232:235], v[48:51], v[28:31]\ns_waitcnt lgkmcnt(3)\nv_mfma_f32_16x16x32_bf16 v[32:35], a[208:211], v[52:55], v[32:35]\nds_read_b128 v[48:51], v2 offset:36864\nv_mfma_f32_16x16x32_bf16 v[36:39], a[216:219], v[52:55], v[36:39]\nv_mfma_f32_16x16x32_bf16 v[40:43], a[224:227], v[52:55], v[40:43]\nv_mfma_f32_16x16x32_bf16 v[44:47], a[232:235], v[52:55], v[44:47]\n" \
"s_waitcnt lgkmcnt(3)\nv_mfma_f32_16x16x32_bf16 v[16:19], a[212:215], v[56:59], v[16:19]\nds_read_b128 v[52:55], v2 offset:37120\nv_mfma_f32_16x16x32_bf16 v[20:23], a[220:223], v[56:59], v[20:23]\nds_read_b128 v[76:79], v1 offset:13312\nv_mfma_f32_16x16x32_bf16 v[24:27], a[228:231], v[56:59], v[24:27]\nv_mfma_f32_16x16x32_bf16 v[28:31], a[236:239], v[56:59], v[28:31]\ns_waitcnt lgkmcnt(4)\n" \
"v_mfma_f32_16x16x32_bf16 v[32:35], a[212:215], v[60:63], v[32:35]\nds_read_b128 v[56:59], v2 offset:38912\nv_mfma_f32_16x16x32_bf16 v[36:39], a[220:223], v[60:63], v[36:39]\nv_mfma_f32_16x16x32_bf16 v[40:43], a[228:231], v[60:63], v[40:43]\nv_mfma_f32_16x16x32_bf16 v[44:47], a[236:239], v[60:63], v[44:47]\ns_waitcnt lgkmcnt(3)\nv_mfma_f32_16x16x32_bf16 v[16:19], a[240:243], v[48:51], v[16:19]\nds_read_b128 v[60:63], v2 offset:39168\n" \
"v_mfma_f32_16x16x32_bf16 v[20:23], v[72:75], v[48:51], v[20:23]\nds_read_b128 v[64:67], v1 offset:14336\nv_mfma_f32_16x16x32_bf16 v[24:27], a[248:251], v[48:51], v[24:27]\nv_mfma_f32_16x16x32_bf16 v[28:31], v[96:99], v[48:51], v[28:31]\ns_waitcnt lgkmcnt(4)\nv_mfma_f32_16x16x32_bf16 v[32:35], a[240:243], v[52:55], v[32:35]\nds_read_b128 v[48:51], v2 offset:40960\nv_mfma_f32_16x16x32_bf16 v[36:39], v[72:75], v[52:55], v[36:39]\n" \
"v_mfma_f32_16x16x32_bf16 v[40:43], a[248:251], v[52:55], v[40:43]\nv_mfma_f32_16x16x32_bf16 v[44:47], v[96:99], v[52:55], v[44:47]\ns_waitcnt lgkmcnt(3)\nv_mfma_f32_16x16x32_bf16 v[16:19], a[244:247], v[56:59], v[16:19]\nds_read_b128 v[52:55], v2 offset:41216\nv_mfma_f32_16x16x32_bf16 v[20:23], v[76:79], v[56:59], v[20:23]\nds_read_b128 v[68:71], v1 offset:15360\nv_mfma_f32_16x16x32_bf16 v[24:27], a[252:255], v[56:59], v[24:27]\n" \
"v_mfma_f32_16x16x32_bf16 v[28:31], v[100:103], v[56:59], v[28:31]\ns_waitcnt lgkmcnt(4)\nv_mfma_f32_16x16x32_bf16 v[32:35], a[244:247], v[60:63], v[32:35]\nds_read_b128 v[56:59], v2 offset:43008\nv_mfma_f32_16x16x32_bf16 v[36:39], v[76:79], v[60:63], v[36:39]\nv_mfma_f32_16x16x32_bf16 v[40:43], a[252:255], v[60:63], v[40:43]\nv_mfma_f32_16x16x32_bf16 v[44:47], v[100:103], v[60:63], v[44:47]\ns_waitcnt lgkmcnt(3)\n" \
"v_mfma_f32_16x16x32_bf16 v[16:19], v[104:107], v[48:51], v[16:19]\nds_read_b128 v[60:63], v2 offset:43264\nv_mfma_f32_16x16x32_bf16 v[20:23], v[112:115], v[48:51], v[20:23]\nds_read_b128 v[72:75], v1 offset:16384\nv_mfma_f32_16x16x32_bf16 v[24:27], v[64:67], v[48:51], v[24:27]\nv_mfma_f32_16x16x32_bf16 v[28:31], v[120:123], v[48:51], v[28:31]\ns_waitcnt lgkmcnt(4)\nv_mfma_f32_16x16x32_bf16 v[32:35], v[104:107], v[52:55], v[32:35]\n" \
"ds_read_b128 v[48:51], v2 offset:45056\nv_mfma_f32_16x16x32_bf16 v[36:39], v[112:115], v[52:55], v[36:39]\nv_mfma_f32_16x16x32_bf16 v[40:43], v[64:67], v[52:55], v[40:43]\nv_mfma_f32_16x16x32_bf16 v[44:47], v[120:123], v[52:55], v[44:47]\ns_waitcnt lgkmcnt(3)\nv_mfma_f32_16x16x32_bf16 v[16:19], v[108:111], v[56:59], v[16:19]\nds_read_b128 v[52:55], v2 offset:45312\nv_mfma_f32_16x16x32_bf16 v[20:23], v[116:119], v[56:59], v[20:23]\n" \
"ds_read_b128 v[76:79], v1 offset:17408\nv_mfma_f32_16x16x32_bf16 v[24:27], v[68:71], v[56:59], v[24:27]\nv_mfma_f32_16x16x32_bf16 v[28:31], v[124:127], v[56:59], v[28:31]\ns_waitcnt lgkmcnt(4)\nv_mfma_f32_16x16x32_bf16 v[32:35], v[108:111], v[60:63], v[32:35]\nds_read_b128 v[56:59], v2 offset:47104\nv_mfma_f32_16x16x32_bf16 v[36:39], v[116:119], v[60:63], v[36:39]\nv_mfma_f32_16x16x32_bf16 v[40:43], v[68:71], v[60:63], v[40:43]\n" \
"v_mfma_f32_16x16x32_bf16 v[44:47], v[124:127], v[60:63], v[44:47]\ns_waitcnt lgkmcnt(3)\nv_mfma_f32_16x16x32_bf16 v[16:19], v[128:131], v[48:51], v[16:19]\nds_read_b128 v[60:63], v2 offset:47360\nv_mfma_f32_16x16x32_bf16 v[20:23], v[136:139], v[48:51], v[20:23]\nv_mfma_f32_16x16x32_bf16 v[24:27], v[144:147], v[48:51], v[24:27]\nv_mfma_f32_16x16x32_bf16 v[28:31], v[72:75], v[48:51], v[28:31]\ns_waitcnt lgkmcnt(3)\n" \
"v_mfma_f32_16x16x32_bf16 v[32:35], v[128:131], v[52:55], v[32:35]\ns_waitcnt lgkmcnt(0)\ns_waitcnt vmcnt(0)\ns_barrier\nds_read_b128 v[48:51], v2 offset:0\nv_mfma_f32_16x16x32_bf16 v[36:39], v[136:139], v[52:55], v[36:39]\nv_mfma_f32_16x16x32_bf16 v[40:43], v[144:147], v[52:55], v[40:43]\nv_mfma_f32_16x16x32_bf16 v[44:47], v[72:75], v[52:55], v[44:47]\n" \
"v_mfma_f32_16x16x32_bf16 v[16:19], v[132:135], v[56:59], v[16:19]\nds_read_b128 v[52:55], v2 offset:256\nv_mfma_f32_16x16x32_bf16 v[20:23], v[140:143], v[56:59], v[20:23]\nv_mfma_f32_16x16x32_bf16 v[24:27], v[148:151], v[56:59], v[24:27]\nv_mfma_f32_16x16x32_bf16 v[28:31], v[76:79], v[56:59], v[28:31]\nv_mfma_f32_16x16x32_bf16 v[32:35], v[132:135], v[60:63], v[32:35]\nds_read_b128 v[56:59], v2 offset:2048\nv_mfma_f32_16x16x32_bf16 v[36:39], v[140:143], v[60:63], v[36:39]\n" \
"v_mfma_f32_16x16x32_bf16 v[40:43], v[148:151], v[60:63], v[40:43]\nv_mfma_f32_16x16x32_bf16 v[44:47], v[76:79], v[60:63], v[44:47]\ns_waitcnt lgkmcnt(2)\nv_mfma_f32_16x16x32_bf16 v[16:19], v[152:155], v[48:51], v[16:19]\nds_read_b128 v[60:63], v2 offset:2304\nv_mfma_f32_16x16x32_bf16 v[20:23], v[160:163], v[48:51], v[20:23]\nds_read_b128 v[72:75], v1 offset:18432\nv_mfma_f32_16x16x32_bf16 v[24:27], v[168:171], v[48:51], v[24:27]\n" \
"v_mfma_f32_16x16x32_bf16 v[28:31], v[176:179], v[48:51], v[28:31]\ns_waitcnt lgkmcnt(3)\nv_mfma_f32_16x16x32_bf16 v[32:35], v[152:155], v[52:55], v[32:35]\nds_read_b128 v[48:51], v2 offset:4096\nv_mfma_f32_16x16x32_bf16 v[36:39], v[160:163], v[52:55], v[36:39]\nv_mfma_f32_16x16x32_bf16 v[40:43], v[168:171], v[52:55], v[40:43]\nv_mfma_f32_16x16x32_bf16 v[44:47], v[176:179], v[52:55], v[44:47]\ns_waitcnt lgkmcnt(3)\n" \
"v_mfma_f32_16x16x32_bf16 v[16:19], v[156:159], v[56:59], v[16:19]\nds_read_b128 v[52:55], v2 offset:4352\nv_mfma_f32_16x16x32_bf16 v[20:23], v[164:167], v[56:59], v[20:23]\nds_read_b128 v[76:79], v1 offset:19456\nv_mfma_f32_16x16x32_bf16 v[24:27], v[172:175], v[56:59], v[24:27]\nv_mfma_f32_16x16x32_bf16 v[28:31], v[180:183], v[56:59], v[28:31]\ns_waitcnt lgkmcnt(4)\nv_mfma_f32_16x16x32_bf16 v[32:35], v[156:159], v[60:63], v[32:35]\n" \
"ds_read_b128 v[56:59], v2 offset:6144\nv_mfma_f32_16x16x32_bf16 v[36:39], v[164:167], v[60:63], v[36:39]\nv_mfma_f32_16x16x32_bf16 v[40:43], v[172:175], v[60:63], v[40:43]\nv_mfma_f32_16x16x32_bf16 v[44:47], v[180:183], v[60:63], v[44:47]\ns_waitcnt lgkmcnt(3)\nv_mfma_f32_16x16x32_bf16 v[16:19], v[184:187], v[48:51], v[16:19]\nds_read_b128 v[60:63], v2 offset:6400\nv_mfma_f32_16x16x32_bf16 v[20:23], v[72:75], v[48:51], v[20:23]\n" \
"ds_read_b128 v[64:67], v1 offset:20480\nv_mfma_f32_16x16x32_bf16 v[24:27], v[192:195], v[48:51], v[24:27]\nv_mfma_f32_16x16x32_bf16 v[28:31], v[200:203], v[48:51], v[28:31]\ns_waitcnt lgkmcnt(4)\nv_mfma_f32_16x16x32_bf16 v[32:35], v[184:187], v[52:55], v[32:35]\nds_read_b128 v[48:51], v2 offset:8192\nv_mfma_f32_16x16x32_bf16 v[36:39], v[72:75], v[52:55], v[36:39]\nv_mfma_f32_16x16x32_bf16 v[40:43], v[192:195], v[52:55], v[40:43]\n" \
"v_mfma_f32_16x16x32_bf16 v[44:47], v[200:203], v[52:55], v[44:47]\ns_waitcnt lgkmcnt(3)\nv_mfma_f32_16x16x32_bf16 v[16:19], v[188:191], v[56:59], v[16:19]\nds_read_b128 v[52:55], v2 offset:10240\nv_mfma_f32_16x16x32_bf16 v[20:23], v[76:79], v[56:59], v[20:23]\nds_read_b128 v[68:71], v1 offset:21504\nv_mfma_f32_16x16x32_bf16 v[24:27], v[196:199], v[56:59], v[24:27]\nbuffer_load_dwordx4 v[80:83], v3, s[36:39], s42 offen offset:0 sc1\n" \
"v_mfma_f32_16x16x32_bf16 v[28:31], v[204:207], v[56:59], v[28:31]\ns_waitcnt lgkmcnt(4)\nv_mfma_f32_16x16x32_bf16 v[32:35], v[188:191], v[60:63], v[32:35]\nds_read_b128 v[56:59], v2 offset:12288\nv_mfma_f32_16x16x32_bf16 v[36:39], v[76:79], v[60:63], v[36:39]\nds_read_b128 v[72:75], v1 offset:22528\nv_mfma_f32_16x16x32_bf16 v[40:43], v[196:199], v[60:63], v[40:43]\nbuffer_load_dwordx4 v[84:87], v3, s[36:39], s42 offen offset:1024 sc1\n" \
"v_mfma_f32_16x16x32_bf16 v[44:47], v[204:207], v[60:63], v[44:47]\ns_waitcnt lgkmcnt(4)\nv_mfma_f32_16x16x32_bf16 v[16:19], v[208:211], v[48:51], v[16:19]\nds_read_b128 v[60:63], v2 offset:14336\nv_mfma_f32_16x16x32_bf16 v[20:23], v[216:219], v[48:51], v[20:23]\nds_read_b128 v[76:79], v1 offset:23552\nv_mfma_f32_16x16x32_bf16 v[24:27], v[64:67], v[48:51], v[24:27]\nbuffer_load_dwordx4 v[88:91], v3, s[36:39], s42 offen offset:2048 sc1\n" \
"v_mfma_f32_16x16x32_bf16 v[28:31], v[224:227], v[48:51], v[28:31]\ns_waitcnt lgkmcnt(5)\nv_mfma_f32_16x16x32_bf16 v[16:19], v[212:215], v[52:55], v[16:19]\nds_read_b128 v[48:51], v2 offset:8448\nv_mfma_f32_16x16x32_bf16 v[20:23], v[220:223], v[52:55], v[20:23]\ns_waitcnt lgkmcnt(5)\nv_mfma_f32_16x16x32_bf16 v[24:27], v[68:71], v[52:55], v[24:27]\nbuffer_load_dwordx4 v[92:95], v3, s[36:39], s42 offen offset:3072 sc1\n" \
"v_mfma_f32_16x16x32_bf16 v[28:31], v[228:231], v[52:55], v[28:31]\ns_waitcnt lgkmcnt(4)\nv_mfma_f32_16x16x32_bf16 v[16:19], v[232:235], v[56:59], v[16:19]\nds_read_b128 v[52:55], v2 offset:10496\nv_mfma_f32_16x16x32_bf16 v[20:23], v[240:243], v[56:59], v[20:23]\nv_mfma_f32_16x16x32_bf16 v[24:27], v[248:251], v[56:59], v[24:27]\ns_waitcnt lgkmcnt(4)\nv_mfma_f32_16x16x32_bf16 v[28:31], v[72:75], v[56:59], v[28:31]\n" \
"s_waitcnt lgkmcnt(3)\nv_mfma_f32_16x16x32_bf16 v[16:19], v[236:239], v[60:63], v[16:19]\nds_read_b128 v[56:59], v2 offset:12544\nv_mfma_f32_16x16x32_bf16 v[20:23], v[244:247], v[60:63], v[20:23]\nv_mfma_f32_16x16x32_bf16 v[24:27], v[252:255], v[60:63], v[24:27]\ns_waitcnt lgkmcnt(3)\nv_mfma_f32_16x16x32_bf16 v[28:31], v[76:79], v[60:63], v[28:31]\ns_waitcnt lgkmcnt(2)\n" \
"v_mfma_f32_16x16x32_bf16 v[32:35], v[208:211], v[48:51], v[32:35]\nds_read_b128 v[60:63], v2 offset:14592\nv_mfma_f32_16x16x32_bf16 v[36:39], v[216:219], v[48:51], v[36:39]\nv_cvt_pk_bf16_f32 v20, v16, v20\nv_cvt_pk_bf16_f32 v21, v17, v21\nv_mfma_f32_16x16x32_bf16 v[40:43], v[64:67], v[48:51], v[40:43]\nv_cvt_pk_bf16_f32 v22, v18, v22\nv_cvt_pk_bf16_f32 v23, v19, v23\n" \
"v_mfma_f32_16x16x32_bf16 v[44:47], v[224:227], v[48:51], v[44:47]\ns_waitcnt lgkmcnt(2)\nds_write_b128 v5, v[20:23] offset:0\nv_mfma_f32_16x16x32_bf16 v[32:35], v[212:215], v[52:55], v[32:35]\nv_cvt_pk_bf16_f32 v28, v24, v28\nv_cvt_pk_bf16_f32 v29, v25, v29\nv_mfma_f32_16x16x32_bf16 v[36:39], v[220:223], v[52:55], v[36:39]\nv_cvt_pk_bf16_f32 v30, v26, v30\n" \
"v_cvt_pk_bf16_f32 v31, v27, v31\nv_mfma_f32_16x16x32_bf16 v[40:43], v[68:71], v[52:55], v[40:43]\nv_mov_b64 v[48:49], 0\nv_mov_b64 v[50:51], 0\nv_mfma_f32_16x16x32_bf16 v[44:47], v[228:231], v[52:55], v[44:47]\ns_cmp_eq_u32 %[w], 1\nds_write_b128 v5, v[28:31] offset:512\ns_cbranch_scc1 .Lqk_c1%=\n" \
"s_cmp_eq_u32 %[w], 2\ns_cbranch_scc1 .Lqk_c2%=\ns_cmp_eq_u32 %[w], 3\ns_cbranch_scc1 .Lqk_c3%=\n.Lqk_c0%=:\ns_waitcnt lgkmcnt(3)\nv_mfma_f32_16x16x32_bf16 v[32:35], v[232:235], v[56:59], v[32:35]\nv_mov_b64 v[52:53], 0\n" \
"v_mov_b64 v[54:55], 0\nv_mfma_f32_16x16x32_bf16 v[36:39], v[240:243], v[56:59], v[36:39]\ns_waitcnt lgkmcnt(2)\nds_read_b128 v[64:67], v1 offset:0\nv_mfma_f32_16x16x32_bf16 v[32:35], v[236:239], v[60:63], v[32:35]\nds_read_b128 v[68:71], v1 offset:1024\nv_mfma_f32_16x16x32_bf16 v[36:39], v[244:247], v[60:63], v[36:39]\ns_bfe_i32 s54, s40, 0x10000\n" \
"v_mfma_f32_16x16x32_bf16 v[40:43], v[248:251], v[56:59], v[40:43]\nv_mfma_f32_16x16x32_bf16 v[44:47], v[72:75], v[56:59], v[44:47]\nv_cvt_pk_bf16_f32 v36, v32, v36\nv_cvt_pk_bf16_f32 v37, v33, v37\nv_mfma_f32_16x16x32_bf16 v[40:43], v[252:255], v[60:63], v[40:43]\nv_cvt_pk_bf16_f32 v38, v34, v38\nv_cvt_pk_bf16_f32 v39, v35, v39\nv_mfma_f32_16x16x32_bf16 v[44:47], v[76:79], v[60:63], v[44:47]\n" \
"ds_write_b128 v5, v[36:39] offset:256\nv_mov_b64 v[56:57], 0\nv_mov_b64 v[58:59], 0\nv_mov_b64 v[60:61], 0\nv_mov_b64 v[62:63], 0\n.Lqk_poll0%=:\ns_waitcnt vmcnt(0)\nv_smfmac_f32_16x16x64_bf16 v[48:51], v[80:83], a[0:7], v4\n" \
"ds_write_b128 v0, v[80:83] offset:16384\nv_max3_u32 v16, v80, v82, v84\nv_smfmac_f32_16x16x64_bf16 v[52:55], v[80:83], a[8:15], v4\nds_read_b128 v[72:75], v1 offset:2048\nds_read_b128 v[76:79], v1 offset:3072\nv_max3_u32 v16, v16, v86, v88\nv_smfmac_f32_16x16x64_bf16 v[56:59], v[80:83], a[16:23], v4\nds_write_b128 v0, v[84:87] offset:17408\n" \
"v_max3_u32 v16, v16, v90, v92\nv_max_u32 v16, v16, v94\nv_smfmac_f32_16x16x64_bf16 v[60:63], v[80:83], a[24:31], v4\nv_cmp_eq_u32 vcc, -1, v16\nv_smfmac_f32_16x16x64_bf16 v[48:51], v[84:87], a[32:39], v4\nds_write_b128 v0, v[88:91] offset:18432\ns_cbranch_vccnz .Lqk_re0%=\ns_waitcnt lgkmcnt(6)\n" \
"v_smfmac_f32_16x16x64_bf16 v[52:55], v[84:87], v[64:71], v4\nds_read_b128 v[64:67], v1 offset:4096\nds_read_b128 v[68:71], v1 offset:5120\nv_smfmac_f32_16x16x64_bf16 v[56:59], v[84:87], a[40:47], v4\nds_write_b128 v0, v[92:95] offset:19456\nv_smfmac_f32_16x16x64_bf16 v[60:63], v[84:87], a[48:55], v4\nv_smfmac_f32_16x16x64_bf16 v[48:51], v[88:91], a[56:63], v4\nv_smfmac_f32_16x16x64_bf16 v[52:55], v[88:91], a[64:71], v4\n" \
"s_waitcnt lgkmcnt(0)\ns_barrier\ns_memrealtime s[68:69]\nds_read_b128 v[80:83], v0 offset:20480\nds_read_b128 v[84:87], v0 offset:21504\nv_mov_b32 v24, s40\nv_smfmac_f32_16x16x64_bf16 v[56:59], v[88:91], v[72:79], v4\nds_read_b128 v[72:75], v1 offset:6144\n" \
"ds_read_b128 v[76:79], v1 offset:7168\nv_cvt_pk_bf16_f32 v44, v40, v44\nv_smfmac_f32_16x16x64_bf16 v[60:63], v[88:91], a[72:79], v4\nds_read_b128 v[88:91], v0 offset:22528\nv_cvt_pk_bf16_f32 v45, v41, v45\nv_smfmac_f32_16x16x64_bf16 v[48:51], v[92:95], a[80:87], v4\nv_cvt_pk_bf16_f32 v46, v42, v46\nv_smfmac_f32_16x16x64_bf16 v[52:55], v[92:95], a[88:95], v4\n" \
"v_cvt_pk_bf16_f32 v47, v43, v47\nv_smfmac_f32_16x16x64_bf16 v[56:59], v[92:95], a[96:103], v4\nv_smfmac_f32_16x16x64_bf16 v[60:63], v[92:95], v[64:71], v4\ns_waitcnt lgkmcnt(4)\nds_write_b128 v5, v[44:47] offset:768\nds_read_b128 v[92:95], v0 offset:23552\nds_read_b128 v[64:67], v1 offset:8192\nds_read_b128 v[68:71], v1 offset:9216\n" \
"v_smfmac_f32_16x16x64_bf16 v[48:51], v[80:83], a[104:111], v4\ns_sub_u32 s88, s86, s89\ns_min_u32 s88, s88, 150\nv_smfmac_f32_16x16x64_bf16 v[52:55], v[80:83], a[112:119], v4\ns_add_u32 s88, s88, s68\ns_mov_b32 s89, s68\nv_mov_b32 v25, s88\nv_smfmac_f32_16x16x64_bf16 v[56:59], v[80:83], a[120:127], v4\n" \
"s_mov_b64 exec, 1\nbuffer_store_dwordx2 v[24:25], off, s[36:39], %[sg] sc1\ns_mov_b64 exec, -1\nv_smfmac_f32_16x16x64_bf16 v[60:63], v[80:83], a[128:135], v4\ns_waitcnt lgkmcnt(7)\nds_read_b128 v[80:83], v0 offset:24576\nv_smfmac_f32_16x16x64_bf16 v[48:51], v[84:87], a[136:143], v4\ns_waitcnt lgkmcnt(6)\n" \
"v_smfmac_f32_16x16x64_bf16 v[52:55], v[84:87], v[72:79], v4\nds_read_b128 v[72:75], v1 offset:10240\nds_read_b128 v[76:79], v1 offset:11264\nv_smfmac_f32_16x16x64_bf16 v[56:59], v[84:87], a[144:151], v4\nv_smfmac_f32_16x16x64_bf16 v[60:63], v[84:87], a[152:159], v4\ns_waitcnt lgkmcnt(7)\nds_read_b128 v[84:87], v0 offset:25600\nv_smfmac_f32_16x16x64_bf16 v[48:51], v[88:91], a[160:167], v4\n" \
"s_waitcnt lgkmcnt(7)\ns_barrier\nv_smfmac_f32_16x16x64_bf16 v[52:55], v[88:91], a[168:175], v4\ns_waitcnt lgkmcnt(4)\nv_smfmac_f32_16x16x64_bf16 v[56:59], v[88:91], v[64:71], v4\nds_read_b128 v[64:67], v1 offset:12288\nds_read_b128 v[68:71], v1 offset:13312\nv_smfmac_f32_16x16x64_bf16 v[60:63], v[88:91], a[176:183], v4\n" \
"ds_read_b128 v[88:91], v0 offset:26624\nv_smfmac_f32_16x16x64_bf16 v[48:51], v[92:95], a[184:191], v4\nds_read_b128 v[16:19], v7 offset:0\nv_smfmac_f32_16x16x64_bf16 v[52:55], v[92:95], a[192:199], v4\nv_smfmac_f32_16x16x64_bf16 v[56:59], v[92:95], a[200:207], v4\ns_waitcnt lgkmcnt(5)\nds_read_b128 v[20:23], v7 offset:64\nv_smfmac_f32_16x16x64_bf16 v[60:63], v[92:95], v[72:79], v4\n" \
"ds_read_b128 v[92:95], v0 offset:27648\nds_read_b128 v[72:75], v1 offset:14336\nds_read_b128 v[76:79], v1 offset:15360\nv_smfmac_f32_16x16x64_bf16 v[48:51], v[80:83], a[208:215], v4\nds_read_b128 v[24:27], v7 offset:2048\nv_smfmac_f32_16x16x64_bf16 v[52:55], v[80:83], a[216:223], v4\nv_smfmac_f32_16x16x64_bf16 v[56:59], v[80:83], a[224:231], v4\nds_read_b128 v[28:31], v7 offset:2112\n" \
"v_smfmac_f32_16x16x64_bf16 v[60:63], v[80:83], a[232:239], v4\ns_waitcnt lgkmcnt(10)\nds_read_b128 v[80:83], v0 offset:28672\nv_smfmac_f32_16x16x64_bf16 v[48:51], v[84:87], a[240:247], v4\ns_waitcnt lgkmcnt(9)\nds_read_b128 v[32:35], v7 offset:4096\nv_smfmac_f32_16x16x64_bf16 v[52:55], v[84:87], v[64:71], v4\nds_read_b128 v[64:67], v1 offset:16384\n" \
"ds_read_b128 v[68:71], v1 offset:17408\nv_smfmac_f32_16x16x64_bf16 v[56:59], v[84:87], a[248:255], v4\nds_read_b128 v[36:39], v7 offset:4160\nv_smfmac_f32_16x16x64_bf16 v[60:63], v[84:87], v[96:103], v4\ns_waitcnt lgkmcnt(12)\nds_read_b128 v[84:87], v0 offset:29696\nv_smfmac_f32_16x16x64_bf16 v[48:51], v[88:91], v[104:111], v4\nds_read_b128 v[40:43], v7 offset:6144\n" \
"v_smfmac_f32_16x16x64_bf16 v[52:55], v[88:91], v[112:119], v4\ns_waitcnt lgkmcnt(9)\nv_smfmac_f32_16x16x64_bf16 v[56:59], v[88:91], v[72:79], v4\nds_read_b128 v[72:75], v1 offset:18432\nds_read_b128 v[76:79], v1 offset:19456\nv_smfmac_f32_16x16x64_bf16 v[60:63], v[88:91], v[120:127], v4\nds_read_b128 v[88:91], v0 offset:30720\nv_smfmac_f32_16x16x64_bf16 v[48:51], v[92:95], v[128:135], v4\n" \
"ds_read_b128 v[44:47], v7 offset:6208\nv_smfmac_f32_16x16x64_bf16 v[52:55], v[92:95], v[136:143], v4\nv_smfmac_f32_16x16x64_bf16 v[56:59], v[92:95], v[144:151], v4\ns_waitcnt lgkmcnt(7)\nv_smfmac_f32_16x16x64_bf16 v[60:63], v[92:95], v[64:71], v4\nds_read_b128 v[92:95], v0 offset:31744\nds_read_b128 v[64:67], v1 offset:20480\nds_read_b128 v[68:71], v1 offset:21504\n" \
"v_smfmac_f32_16x16x64_bf16 v[48:51], v[80:83], v[152:159], v4\nv_smfmac_f32_16x16x64_bf16 v[52:55], v[80:83], v[160:167], v4\nv_smfmac_f32_16x16x64_bf16 v[56:59], v[80:83], v[168:175], v4\nv_smfmac_f32_16x16x64_bf16 v[60:63], v[80:83], v[176:183], v4\ns_waitcnt lgkmcnt(8)\nv_smfmac_f32_16x16x64_bf16 v[48:51], v[84:87], v[184:191], v4\ns_waitcnt lgkmcnt(5)\nv_smfmac_f32_16x16x64_bf16 v[52:55], v[84:87], v[72:79], v4\n" \
"ds_read_b128 v[72:75], v1 offset:22528\nds_read_b128 v[76:79], v1 offset:23552\nv_smfmac_f32_16x16x64_bf16 v[56:59], v[84:87], v[192:199], v4\nv_smfmac_f32_16x16x64_bf16 v[60:63], v[84:87], v[200:207], v4\ns_waitcnt lgkmcnt(6)\nv_smfmac_f32_16x16x64_bf16 v[48:51], v[88:91], v[208:215], v4\nv_smfmac_f32_16x16x64_bf16 v[52:55], v[88:91], v[216:223], v4\ns_waitcnt lgkmcnt(4)\n" \
"v_smfmac_f32_16x16x64_bf16 v[48:51], v[92:95], v[232:239], v4\nv_smfmac_f32_16x16x64_bf16 v[52:55], v[92:95], v[240:247], v4\ns_waitcnt lgkmcnt(2)\nv_smfmac_f32_16x16x64_bf16 v[56:59], v[88:91], v[64:71], v4\nv_add_f32 v48, v48, v50\nv_smfmac_f32_16x16x64_bf16 v[60:63], v[88:91], v[224:231], v4\nv_add_f32 v52, v52, v54\nv_add_f32 v49, v49, v51\n" \
"v_smfmac_f32_16x16x64_bf16 v[56:59], v[92:95], v[248:255], v4\ns_waitcnt lgkmcnt(0)\nv_add_f32 v53, v53, v55\nv_cvt_pk_bf16_f32 v50, v48, v52\nv_smfmac_f32_16x16x64_bf16 v[60:63], v[92:95], v[72:79], v4\nv_cvt_pk_bf16_f32 v51, v49, v53\nds_write2_b32 v6, v50, v51 offset0:0 offset1:4\ns_nop 1\n" \
"v_add_f32 v56, v56, v58\nv_add_f32 v57, v57, v59\ns_nop 1\nv_add_f32 v60, v60, v62\nv_add_f32 v61, v61, v63\nv_cvt_pk_bf16_f32 v62, v56, v60\nv_cvt_pk_bf16_f32 v63, v57, v61\nds_write2_b32 v6, v62, v63 offset0:36 offset1:40\n" \
"v_mov_b32 v61, s54\ns_waitcnt lgkmcnt(0)\ns_barrier\nds_read_b128 v[80:83], v8 offset:0\nds_read_b128 v[84:87], v8 offset:576\nds_read_b128 v[88:91], v8 offset:1152\nds_read_b128 v[92:95], v8 offset:1728\nds_read_b128 v[64:67], v9 offset:0\n" \
"ds_read_b128 v[68:71], v9 offset:576\nds_read_b128 v[72:75], v9 offset:1152\nds_read_b128 v[76:79], v9 offset:1728\nds_read_b32 v60, v0 offset:53760\ns_waitcnt lgkmcnt(8)\nv_mfma_f32_16x16x32_bf16 v[52:55], v[16:19], v[80:83], 0\nv_mfma_f32_16x16x32_bf16 v[56:59], v[20:23], v[80:83], 0\ns_waitcnt lgkmcnt(7)\n" \
"v_mfma_f32_16x16x32_bf16 v[52:55], v[24:27], v[84:87], v[52:55]\nv_mfma_f32_16x16x32_bf16 v[56:59], v[28:31], v[84:87], v[56:59]\ns_waitcnt lgkmcnt(6)\nv_mfma_f32_16x16x32_bf16 v[52:55], v[32:35], v[88:91], v[52:55]\nv_mfma_f32_16x16x32_bf16 v[56:59], v[36:39], v[88:91], v[56:59]\ns_waitcnt lgkmcnt(5)\nv_mfma_f32_16x16x32_bf16 v[52:55], v[40:43], v[92:95], v[52:55]\nv_mfma_f32_16x16x32_bf16 v[56:59], v[44:47], v[92:95], v[56:59]\n" \
"s_waitcnt lgkmcnt(4)\nv_mfma_f32_16x16x32_bf16 v[48:51], v[64:67], v[80:83], 0\ns_waitcnt lgkmcnt(3)\nv_mfma_f32_16x16x32_bf16 v[48:51], v[68:71], v[84:87], v[48:51]\ns_waitcnt lgkmcnt(2)\nds_read_b128 v[80:83], v2 offset:0\nv_mfma_f32_16x16x32_bf16 v[48:51], v[72:75], v[88:91], v[48:51]\ns_waitcnt lgkmcnt(2)\n" \
"ds_read_b128 v[84:87], v2 offset:256\nv_mfma_f32_16x16x32_bf16 v[48:51], v[76:79], v[92:95], v[48:51]\nds_read_b128 v[88:91], v2 offset:2048\nv_max3_f32 v62, v52, v53, v54\nv_max3_f32 v63, v55, v56, v57\nv_max3_f32 v62, v62, v58, v59\nv_cndmask_b32 v52, v52, v56, s[62:63]\nv_cndmask_b32 v53, v53, v57, s[62:63]\n" \
"v_cndmask_b32 v54, v54, v58, s[62:63]\nv_cndmask_b32 v55, v55, v59, s[62:63]\nv_max_f32 v62, v62, v63\nv_max3_f32 v63, v62, v48, v49\nv_max3_f32 v62, v62, v48, v49\ns_waitcnt lgkmcnt(2)\nv_mfma_f32_16x16x32_bf16 v[16:19], a[0:3], v[80:83], 0\nds_read_b128 v[92:95], v2 offset:2304\n" \
"v_permlane16_swap_b32 v62, v63\nv_mfma_f32_16x16x32_bf16 v[20:23], a[8:11], v[80:83], 0\nds_read_b128 v[72:75], v1 offset:0\nv_max_f32 v50, v62, v63\nv_max_f32 v62, v62, v63\nv_mfma_f32_16x16x32_bf16 v[24:27], a[16:19], v[80:83], 0\ns_nop 0\nv_permlane32_swap_b32 v62, v50\n" \
"v_max_f32 v62, v62, v50\nv_mfma_f32_16x16x32_bf16 v[28:31], a[24:27], v[80:83], 0\ns_waitcnt lgkmcnt(3)\nv_mul_f32 v62, s59, v62\nv_fma_f32 v52, v52, s58, v62\nv_fma_f32 v53, v53, s58, v62\nv_mfma_f32_16x16x32_bf16 v[32:35], a[0:3], v[84:87], 0\nds_read_b128 v[80:83], v2 offset:4096\n" \
"v_exp_f32 v52, v52\nv_mfma_f32_16x16x32_bf16 v[36:39], a[8:11], v[84:87], 0\nv_fma_f32 v54, v54, s58, v62\nv_exp_f32 v53, v53\nv_mfma_f32_16x16x32_bf16 v[40:43], a[16:19], v[84:87], 0\nv_fma_f32 v55, v55, s58, v62\nv_exp_f32 v54, v54\nv_mfma_f32_16x16x32_bf16 v[44:47], a[24:27], v[84:87], 0\n" \
"s_waitcnt lgkmcnt(3)\nv_add_f32 v63, v52, v53\nv_fma_f32 v48, v48, s58, v62\nv_add_f32 v63, v63, v54\nv_mfma_f32_16x16x32_bf16 v[16:19], a[4:7], v[88:91], v[16:19]\nds_read_b128 v[84:87], v2 offset:4352\nv_exp_f32 v55, v55\nv_mfma_f32_16x16x32_bf16 v[20:23], a[12:15], v[88:91], v[20:23]\n" \
"ds_read_b128 v[76:79], v1 offset:1024\nv_fma_f32 v49, v49, s58, v62\nv_add_f32 v63, v63, v55\nv_mfma_f32_16x16x32_bf16 v[24:27], a[20:23], v[88:91], v[24:27]\nv_exp_f32 v48, v48\nv_mfma_f32_16x16x32_bf16 v[28:31], a[28:31], v[88:91], v[28:31]\ns_waitcnt lgkmcnt(4)\nv_exp_f32 v49, v49\n" \
"v_add_f32 v50, v48, v63\nv_mfma_f32_16x16x32_bf16 v[32:35], a[4:7], v[92:95], v[32:35]\nds_read_b128 v[88:91], v2 offset:6144\nv_add_f32 v50, v50, v49\nv_mfma_f32_16x16x32_bf16 v[36:39], a[12:15], v[92:95], v[36:39]\ns_nop 0\nv_add_f32_dpp v62, v63, v50 row_ror:8 row_mask:0xf bank_mask:0xf\nv_add_f32_dpp v50, v63, v50 row_ror:8 row_mask:0xf bank_mask:0xf\n" \
"v_mfma_f32_16x16x32_bf16 v[40:43], a[20:23], v[92:95], v[40:43]\ns_nop 0\nv_permlane16_swap_b32 v62, v50\nv_add_f32 v63, v62, v50\nv_mfma_f32_16x16x32_bf16 v[44:47], a[28:31], v[92:95], v[44:47]\ns_waitcnt lgkmcnt(3)\nv_add_f32 v50, v62, v50\nv_mfma_f32_16x16x32_bf16 v[16:19], a[32:35], v[80:83], v[16:19]\n" \
"ds_read_b128 v[92:95], v2 offset:6400\nv_permlane32_swap_b32 v63, v50\nv_mfma_f32_16x16x32_bf16 v[20:23], v[72:75], v[80:83], v[20:23]\nds_read_b128 v[64:67], v1 offset:2048\nv_add_f32 v63, v63, v50\nv_mfma_f32_16x16x32_bf16 v[24:27], a[40:43], v[80:83], v[24:27]\nv_rcp_f32 v63, v63\nv_mfma_f32_16x16x32_bf16 v[28:31], a[48:51], v[80:83], v[28:31]\n" \
"s_waitcnt lgkmcnt(4)\nv_mul_f32 v52, v52, v63\nv_mul_f32 v53, v53, v63\nv_mul_f32 v54, v54, v63\nv_mfma_f32_16x16x32_bf16 v[32:35], a[32:35], v[84:87], v[32:35]\nds_read_b128 v[80:83], v2 offset:8192\nv_mul_f32 v55, v55, v63\nv_mul_f32 v48, v48, v63\n" \
"v_mfma_f32_16x16x32_bf16 v[36:39], v[72:75], v[84:87], v[36:39]\nv_mul_f32 v49, v49, v63\nv_cvt_pk_bf16_f32 v52, v52, v53\nv_cvt_pk_bf16_f32 v53, v54, v55\nv_mfma_f32_16x16x32_bf16 v[40:43], a[40:43], v[84:87], v[40:43]\nv_cvt_pk_bf16_f32 v48, v48, v49\nv_bfi_b32 v52, s61, v52, v61\nv_bfi_b32 v53, s61, v53, v61\n" \
"v_mfma_f32_16x16x32_bf16 v[44:47], a[48:51], v[84:87], v[44:47]\ns_waitcnt lgkmcnt(3)\nv_bfi_b32 v48, s61, v48, v61\nv_mfma_f32_16x16x32_bf16 v[16:19], a[36:39], v[88:91], v[16:19]\nds_read_b128 v[84:87], v2 offset:8448\nv_mfma_f32_16x16x32_bf16 v[20:23], v[76:79], v[88:91], v[20:23]\nds_read_b128 v[68:71], v1 offset:3072\nv_mfma_f32_16x16x32_bf16 v[24:27], a[44:47], v[88:91], v[24:27]\n" \
"buffer_store_dwordx2 v[52:53], v10, s[36:39], 0 offen sc1\nv_mfma_f32_16x16x32_bf16 v[28:31], a[52:55], v[88:91], v[28:31]\ns_memrealtime s[86:87]\ns_waitcnt lgkmcnt(4)\nbuffer_store_dword v48, v11, s[36:39], 0 offen sc1\nv_cmp_ne_u32 vcc, 0, v60\nv_mfma_f32_16x16x32_bf16 v[32:35], a[36:39], v[92:95], v[32:35]\nds_read_b128 v[88:91], v2 offset:10240\n" \
"v_mfma_f32_16x16x32_bf16 v[36:39], v[76:79], v[92:95], v[36:39]\nv_mfma_f32_16x16x32_bf16 v[40:43], a[44:47], v[92:95], v[40:43]\nv_mfma_f32_16x16x32_bf16 v[44:47], a[52:55], v[92:95], v[44:47]\ns_waitcnt lgkmcnt(3)\nv_mfma_f32_16x16x32_bf16 v[16:19], a[56:59], v[80:83], v[16:19]\nds_read_b128 v[92:95], v2 offset:10496\nv_mfma_f32_16x16x32_bf16 v[20:23], a[64:67], v[80:83], v[20:23]\nds_read_b128 v[72:75], v1 offset:4096\n" \
"v_mfma_f32_16x16x32_bf16 v[24:27], v[64:67], v[80:83], v[24:27]\nv_mfma_f32_16x16x32_bf16 v[28:31], a[72:75], v[80:83], v[28:31]\ns_waitcnt lgkmcnt(4)\nv_mfma_f32_16x16x32_bf16 v[32:35], a[56:59], v[84:87], v[32:35]\nds_read_b128 v[80:83], v2 offset:12288\nv_mfma_f32_16x16x32_bf16 v[36:39], a[64:67], v[84:87], v[36:39]\nv_mfma_f32_16x16x32_bf16 v[40:43], v[64:67], v[84:87], v[40:43]\nv_mfma_f32_16x16x32_bf16 v[44:47], a[72:75], v[84:87], v[44:47]\n" \
"s_waitcnt lgkmcnt(3)\nv_mfma_f32_16x16x32_bf16 v[16:19], a[60:63], v[88:91], v[16:19]\nds_read_b128 v[84:87], v2 offset:12544\nv_mfma_f32_16x16x32_bf16 v[20:23], a[68:71], v[88:91], v[20:23]\nds_read_b128 v[76:79], v1 offset:5120\nv_mfma_f32_16x16x32_bf16 v[24:27], v[68:71], v[88:91], v[24:27]\nv_mfma_f32_16x16x32_bf16 v[28:31], a[76:79], v[88:91], v[28:31]\ns_waitcnt lgkmcnt(4)\n" \
"v_mfma_f32_16x16x32_bf16 v[32:35], a[60:63], v[92:95], v[32:35]\nds_read_b128 v[88:91], v2 offset:14336\nv_mfma_f32_16x16x32_bf16 v[36:39], a[68:71], v[92:95], v[36:39]\nv_mfma_f32_16x16x32_bf16 v[40:43], v[68:71], v[92:95], v[40:43]\nv_mfma_f32_16x16x32_bf16 v[44:47], a[76:79], v[92:95], v[44:47]\ns_cbranch_vccnz .Lqk_pj%=\ns_cmp_lt_i32 s40, 63\ns_waitcnt lgkmcnt(0)\n" \
"s_cbranch_scc1 .Lqk_pwait%=\ns_branch .Lqk_end%=\n.Lqk_re0%=:\nv_max_u32 v17, v80, v82\nv_cmp_eq_u32 vcc, -1, v17\ns_cbranch_vccz .Lqk_sk1616%=\ns_mov_b64 exec, vcc\nbuffer_load_dwordx4 v[80:83], v3, s[36:39], s42 offen offset:0 sc1\n" \
"s_mov_b64 exec, -1\n.Lqk_sk1616%=:\nv_max_u32 v17, v84, v86\nv_cmp_eq_u32 vcc, -1, v17\ns_cbranch_vccz .Lqk_sk1623%=\ns_mov_b64 exec, vcc\nbuffer_load_dwordx4 v[84:87], v3, s[36:39], s42 offen offset:1024 sc1\ns_mov_b64 exec, -1\n" \
".Lqk_sk1623%=:\nv_max_u32 v17, v88, v90\nv_cmp_eq_u32 vcc, -1, v17\ns_cbranch_vccz .Lqk_sk1630%=\ns_mov_b64 exec, vcc\nbuffer_load_dwordx4 v[88:91], v3, s[36:39], s42 offen offset:2048 sc1\ns_mov_b64 exec, -1\n.Lqk_sk1630%=:\n" \
"v_max_u32 v17, v92, v94\nv_cmp_eq_u32 vcc, -1, v17\ns_cbranch_vccz .Lqk_sk1637%=\ns_mov_b64 exec, vcc\nbuffer_load_dwordx4 v[92:95], v3, s[36:39], s42 offen offset:3072 sc1\ns_mov_b64 exec, -1\n.Lqk_sk1637%=:\nv_mov_b64 v[48:49], 0\n" \
"v_mov_b64 v[50:51], 0\nv_mov_b64 v[52:53], 0\nv_mov_b64 v[54:55], 0\nv_mov_b64 v[56:57], 0\nv_mov_b64 v[58:59], 0\nv_mov_b64 v[60:61], 0\nv_mov_b64 v[62:63], 0\ns_add_u32 s43, s43, 1\n" \
"s_cmp_lt_u32 s43, 262144\ns_cbranch_scc1 .Lqk_poll0%=\ns_branch .Lqk_abort%=\n.Lqk_c1%=:\ns_waitcnt lgkmcnt(3)\nv_mfma_f32_16x16x32_bf16 v[32:35], v[232:235], v[56:59], v[32:35]\nv_mov_b64 v[52:53], 0\nv_mov_b64 v[54:55], 0\n" \
"v_mfma_f32_16x16x32_bf16 v[36:39], v[240:243], v[56:59], v[36:39]\ns_waitcnt lgkmcnt(2)\nds_read_b128 v[64:67], v1 offset:6144\nv_mfma_f32_16x16x32_bf16 v[32:35], v[236:239], v[60:63], v[32:35]\nds_read_b128 v[68:71], v1 offset:7168\nv_mfma_f32_16x16x32_bf16 v[36:39], v[244:247], v[60:63], v[36:39]\ns_bfe_i32 s54, s40, 0x10001\nv_mfma_f32_16x16x32_bf16 v[40:43], v[248:251], v[56:59], v[40:43]\n" \
"v_mfma_f32_16x16x32_bf16 v[44:47], v[72:75], v[56:59], v[44:47]\nv_cvt_pk_bf16_f32 v36, v32, v36\nv_cvt_pk_bf16_f32 v37, v33, v37\nv_mfma_f32_16x16x32_bf16 v[40:43], v[252:255], v[60:63], v[40:43]\nv_cvt_pk_bf16_f32 v38, v34, v38\nv_cvt_pk_bf16_f32 v39, v35, v39\nv_mfma_f32_16x16x32_bf16 v[44:47], v[76:79], v[60:63], v[44:47]\nds_write_b128 v5, v[36:39] offset:256\n" \
"v_mov_b64 v[56:57], 0\nv_mov_b64 v[58:59], 0\nv_mov_b64 v[60:61], 0\nv_mov_b64 v[62:63], 0\n.Lqk_poll1%=:\ns_waitcnt vmcnt(0)\nv_smfmac_f32_16x16x64_bf16 v[48:51], v[80:83], a[104:111], v4\nds_write_b128 v0, v[80:83] offset:20480\n" \
"v_max3_u32 v16, v80, v82, v84\nv_smfmac_f32_16x16x64_bf16 v[52:55], v[80:83], a[112:119], v4\nds_read_b128 v[72:75], v1 offset:8192\nds_read_b128 v[76:79], v1 offset:9216\nv_max3_u32 v16, v16, v86, v88\nv_smfmac_f32_16x16x64_bf16 v[56:59], v[80:83], a[120:127], v4\nds_write_b128 v0, v[84:87] offset:21504\nv_max3_u32 v16, v16, v90, v92\n" \
"v_max_u32 v16, v16, v94\nv_smfmac_f32_16x16x64_bf16 v[60:63], v[80:83], a[128:135], v4\nv_cmp_eq_u32 vcc, -1, v16\nv_smfmac_f32_16x16x64_bf16 v[48:51], v[84:87], a[136:143], v4\nds_write_b128 v0, v[88:91] offset:22528\ns_cbranch_vccnz .Lqk_re1%=\ns_waitcnt lgkmcnt(6)\nv_smfmac_f32_16x16x64_bf16 v[52:55], v[84:87], v[64:71], v4\n" \
"ds_read_b128 v[64:67], v1 offset:10240\nds_read_b128 v[68:71], v1 offset:11264\nv_smfmac_f32_16x16x64_bf16 v[56:59], v[84:87], a[144:151], v4\nds_write_b128 v0, v[92:95] offset:23552\nv_smfmac_f32_16x16x64_bf16 v[60:63], v[84:87], a[152:159], v4\nv_smfmac_f32_16x16x64_bf16 v[48:51], v[88:91], a[160:167], v4\nv_smfmac_f32_16x16x64_bf16 v[52:55], v[88:91], a[168:175], v4\ns_waitcnt lgkmcnt(0)\n" \
"s_barrier\nds_read_b128 v[80:83], v0 offset:24576\nds_read_b128 v[84:87], v0 offset:25600\nv_smfmac_f32_16x16x64_bf16 v[56:59], v[88:91], v[72:79], v4\nds_read_b128 v[72:75], v1 offset:12288\nds_read_b128 v[76:79], v1 offset:13312\nv_cvt_pk_bf16_f32 v44, v40, v44\nv_smfmac_f32_16x16x64_bf16 v[60:63], v[88:91], a[176:183], v4\n" \
"ds_read_b128 v[88:91], v0 offset:26624\nv_cvt_pk_bf16_f32 v45, v41, v45\nv_smfmac_f32_16x16x64_bf16 v[48:51], v[92:95], a[184:191], v4\nv_cvt_pk_bf16_f32 v46, v42, v46\nv_smfmac_f32_16x16x64_bf16 v[52:55], v[92:95], a[192:199], v4\nv_cvt_pk_bf16_f32 v47, v43, v47\nv_smfmac_f32_16x16x64_bf16 v[56:59], v[92:95], a[200:207], v4\nv_smfmac_f32_16x16x64_bf16 v[60:63], v[92:95], v[64:71], v4\n" \
"s_waitcnt lgkmcnt(4)\nds_write_b128 v5, v[44:47] offset:768\nds_read_b128 v[92:95], v0 offset:27648\nds_read_b128 v[64:67], v1 offset:14336\nds_read_b128 v[68:71], v1 offset:15360\nv_smfmac_f32_16x16x64_bf16 v[48:51], v[80:83], a[208:215], v4\nv_smfmac_f32_16x16x64_bf16 v[52:55], v[80:83], a[216:223], v4\nv_smfmac_f32_16x16x64_bf16 v[56:59], v[80:83], a[224:231], v4\n" \
"v_smfmac_f32_16x16x64_bf16 v[60:63], v[80:83], a[232:239], v4\ns_waitcnt lgkmcnt(7)\nds_read_b128 v[80:83], v0 offset:28672\nv_smfmac_f32_16x16x64_bf16 v[48:51], v[84:87], a[240:247], v4\ns_waitcnt lgkmcnt(6)\nv_smfmac_f32_16x16x64_bf16 v[52:55], v[84:87], v[72:79], v4\nds_read_b128 v[72:75], v1 offset:16384\nds_read_b128 v[76:79], v1 offset:17408\n" \
"v_smfmac_f32_16x16x64_bf16 v[56:59], v[84:87], a[248:255], v4\nv_smfmac_f32_16x16x64_bf16 v[60:63], v[84:87], v[96:103], v4\ns_waitcnt lgkmcnt(7)\nds_read_b128 v[84:87], v0 offset:29696\nv_smfmac_f32_16x16x64_bf16 v[48:51], v[88:91], v[104:111], v4\ns_waitcnt lgkmcnt(7)\ns_barrier\nv_smfmac_f32_16x16x64_bf16 v[52:55], v[88:91], v[112:119], v4\n" \
"s_waitcnt lgkmcnt(4)\nv_smfmac_f32_16x16x64_bf16 v[56:59], v[88:91], v[64:71], v4\nds_read_b128 v[64:67], v1 offset:18432\nds_read_b128 v[68:71], v1 offset:19456\nv_smfmac_f32_16x16x64_bf16 v[60:63], v[88:91], v[120:127], v4\nds_read_b128 v[88:91], v0 offset:30720\nv_smfmac_f32_16x16x64_bf16 v[48:51], v[92:95], v[128:135], v4\nds_read_b128 v[16:19], v7 offset:0\n" \
"v_smfmac_f32_16x16x64_bf16 v[52:55], v[92:95], v[136:143], v4\nv_smfmac_f32_16x16x64_bf16 v[56:59], v[92:95], v[144:151], v4\ns_waitcnt lgkmcnt(5)\nds_read_b128 v[20:23], v7 offset:2048\nv_smfmac_f32_16x16x64_bf16 v[60:63], v[92:95], v[72:79], v4\nds_read_b128 v[92:95], v0 offset:31744\nds_read_b128 v[72:75], v1 offset:20480\nds_read_b128 v[76:79], v1 offset:21504\n" \
"v_smfmac_f32_16x16x64_bf16 v[48:51], v[80:83], v[152:159], v4\nds_read_b128 v[24:27], v7 offset:4096\nv_smfmac_f32_16x16x64_bf16 v[52:55], v[80:83], v[160:167], v4\nv_smfmac_f32_16x16x64_bf16 v[56:59], v[80:83], v[168:175], v4\nds_read_b128 v[28:31], v7 offset:6144\nv_smfmac_f32_16x16x64_bf16 v[60:63], v[80:83], v[176:183], v4\ns_waitcnt lgkmcnt(10)\nds_read_b128 v[80:83], v0 offset:16384\n" \
"v_smfmac_f32_16x16x64_bf16 v[48:51], v[84:87], v[184:191], v4\ns_waitcnt lgkmcnt(9)\nds_read_b128 v[32:35], v7 offset:8192\nv_smfmac_f32_16x16x64_bf16 v[52:55], v[84:87], v[64:71], v4\nds_read_b128 v[64:67], v1 offset:22528\nds_read_b128 v[68:71], v1 offset:23552\nv_smfmac_f32_16x16x64_bf16 v[56:59], v[84:87], v[192:199], v4\nds_read_b128 v[36:39], v7 offset:10240\n" \
"v_smfmac_f32_16x16x64_bf16 v[60:63], v[84:87], v[200:207], v4\ns_waitcnt lgkmcnt(12)\nds_read_b128 v[84:87], v0 offset:17408\nv_smfmac_f32_16x16x64_bf16 v[48:51], v[88:91], v[208:215], v4\nds_read_b128 v[40:43], v7 offset:12288\nv_smfmac_f32_16x16x64_bf16 v[52:55], v[88:91], v[216:223], v4\ns_waitcnt lgkmcnt(9)\nv_smfmac_f32_16x16x64_bf16 v[56:59], v[88:91], v[72:79], v4\n" \
"ds_read_b128 v[72:75], v1 offset:0\nds_read_b128 v[76:79], v1 offset:1024\nv_smfmac_f32_16x16x64_bf16 v[60:63], v[88:91], v[224:231], v4\nds_read_b128 v[88:91], v0 offset:18432\nv_smfmac_f32_16x16x64_bf16 v[48:51], v[92:95], v[232:239], v4\nds_read_b128 v[44:47], v7 offset:14336\nv_smfmac_f32_16x16x64_bf16 v[52:55], v[92:95], v[240:247], v4\nv_smfmac_f32_16x16x64_bf16 v[56:59], v[92:95], v[248:255], v4\n" \
"s_waitcnt lgkmcnt(7)\nv_smfmac_f32_16x16x64_bf16 v[60:63], v[92:95], v[64:71], v4\nds_read_b128 v[92:95], v0 offset:19456\nds_read_b128 v[64:67], v1 offset:2048\nds_read_b128 v[68:71], v1 offset:3072\nv_smfmac_f32_16x16x64_bf16 v[48:51], v[80:83], a[0:7], v4\nv_smfmac_f32_16x16x64_bf16 v[52:55], v[80:83], a[8:15], v4\nv_smfmac_f32_16x16x64_bf16 v[56:59], v[80:83], a[16:23], v4\n" \
"v_smfmac_f32_16x16x64_bf16 v[60:63], v[80:83], a[24:31], v4\ns_waitcnt lgkmcnt(8)\nv_smfmac_f32_16x16x64_bf16 v[48:51], v[84:87], a[32:39], v4\ns_waitcnt lgkmcnt(5)\nv_smfmac_f32_16x16x64_bf16 v[52:55], v[84:87], v[72:79], v4\nds_read_b128 v[72:75], v1 offset:4096\nds_read_b128 v[76:79], v1 offset:5120\nv_smfmac_f32_16x16x64_bf16 v[56:59], v[84:87], a[40:47], v4\n" \
"v_smfmac_f32_16x16x64_bf16 v[60:63], v[84:87], a[48:55], v4\ns_waitcnt lgkmcnt(6)\nv_smfmac_f32_16x16x64_bf16 v[48:51], v[88:91], a[56:63], v4\nv_smfmac_f32_16x16x64_bf16 v[52:55], v[88:91], a[64:71], v4\ns_waitcnt lgkmcnt(4)\nv_smfmac_f32_16x16x64_bf16 v[48:51], v[92:95], a[80:87], v4\nv_smfmac_f32_16x16x64_bf16 v[52:55], v[92:95], a[88:95], v4\ns_waitcnt lgkmcnt(2)\n" \
"v_smfmac_f32_16x16x64_bf16 v[56:59], v[88:91], v[64:71], v4\nv_add_f32 v48, v48, v50\nv_smfmac_f32_16x16x64_bf16 v[60:63], v[88:91], a[72:79], v4\nv_add_f32 v52, v52, v54\nv_add_f32 v49, v49, v51\nv_smfmac_f32_16x16x64_bf16 v[56:59], v[92:95], a[96:103], v4\ns_waitcnt lgkmcnt(0)\nv_add_f32 v53, v53, v55\n" \
"v_cvt_pk_bf16_f32 v50, v48, v52\nv_smfmac_f32_16x16x64_bf16 v[60:63], v[92:95], v[72:79], v4\nv_cvt_pk_bf16_f32 v51, v49, v53\nds_write2_b32 v6, v50, v51 offset0:0 offset1:4\ns_nop 1\nv_add_f32 v56, v56, v58\nv_add_f32 v57, v57, v59\ns_nop 1\n" \
"v_add_f32 v60, v60, v62\nv_add_f32 v61, v61, v63\nv_cvt_pk_bf16_f32 v62, v56, v60\nv_cvt_pk_bf16_f32 v63, v57, v61\nds_write2_b32 v6, v62, v63 offset0:36 offset1:40\nv_mov_b32 v61, s54\nv_mfma_f32_16x16x32_bf16 v[52:55], v[32:35], v[16:19], v[12:15]\nv_mfma_f32_16x16x32_bf16 v[52:55], v[36:39], v[20:23], v[52:55]\n" \
"v_mfma_f32_16x16x32_bf16 v[52:55], v[40:43], v[24:27], v[52:55]\nv_mfma_f32_16x16x32_bf16 v[52:55], v[44:47], v[28:31], v[52:55]\ns_waitcnt lgkmcnt(0)\ns_barrier\nds_read_b128 v[32:35], v9 offset:0\nds_read_b128 v[36:39], v9 offset:576\nds_read_b128 v[40:43], v9 offset:1152\nds_read_b128 v[44:47], v9 offset:1728\n" \
"ds_read_b32 v60, v0 offset:53760\nds_read_b128 v[80:83], v2 offset:0\nds_read_b128 v[84:87], v2 offset:256\nds_read_b128 v[88:91], v2 offset:2048\ns_waitcnt lgkmcnt(7)\nv_mfma_f32_16x16x32_bf16 v[48:51], v[32:35], v[16:19], 0\ns_waitcnt lgkmcnt(6)\nv_mfma_f32_16x16x32_bf16 v[48:51], v[36:39], v[20:23], v[48:51]\n" \
"s_waitcnt lgkmcnt(5)\nv_mfma_f32_16x16x32_bf16 v[48:51], v[40:43], v[24:27], v[48:51]\ns_waitcnt lgkmcnt(4)\nv_mfma_f32_16x16x32_bf16 v[48:51], v[44:47], v[28:31], v[48:51]\ns_waitcnt lgkmcnt(2)\nv_mfma_f32_16x16x32_bf16 v[16:19], a[0:3], v[80:83], 0\nds_read_b128 v[92:95], v2 offset:2304\nv_max3_f32 v62, v52, v53, v54\n" \
"v_mfma_f32_16x16x32_bf16 v[20:23], a[8:11], v[80:83], 0\nds_read_b128 v[72:75], v1 offset:0\nv_max_f32 v62, v62, v55\nv_mfma_f32_16x16x32_bf16 v[24:27], a[16:19], v[80:83], 0\nv_max3_f32 v63, v62, v48, v49\nv_max3_f32 v62, v62, v48, v49\nv_mfma_f32_16x16x32_bf16 v[28:31], a[24:27], v[80:83], 0\ns_waitcnt lgkmcnt(3)\n" \
"v_permlane16_swap_b32 v62, v63\nv_mfma_f32_16x16x32_bf16 v[32:35], a[0:3], v[84:87], 0\nds_read_b128 v[80:83], v2 offset:4096\nv_max_f32 v50, v62, v63\nv_mfma_f32_16x16x32_bf16 v[36:39], a[8:11], v[84:87], 0\nv_max_f32 v62, v62, v63\nv_mfma_f32_16x16x32_bf16 v[40:43], a[16:19], v[84:87], 0\ns_nop 0\n" \
"v_permlane32_swap_b32 v62, v50\nv_mfma_f32_16x16x32_bf16 v[44:47], a[24:27], v[84:87], 0\ns_waitcnt lgkmcnt(3)\nv_max_f32 v62, v62, v50\nv_mul_f32 v62, s59, v62\nv_mfma_f32_16x16x32_bf16 v[16:19], a[4:7], v[88:91], v[16:19]\nds_read_b128 v[84:87], v2 offset:4352\nv_fma_f32 v52, v52, s58, v62\n" \
"v_mfma_f32_16x16x32_bf16 v[20:23], a[12:15], v[88:91], v[20:23]\nds_read_b128 v[76:79], v1 offset:1024\nv_fma_f32 v53, v53, s58, v62\nv_mfma_f32_16x16x32_bf16 v[24:27], a[20:23], v[88:91], v[24:27]\nv_exp_f32 v52, v52\nv_mfma_f32_16x16x32_bf16 v[28:31], a[28:31], v[88:91], v[28:31]\ns_waitcnt lgkmcnt(4)\nv_fma_f32 v54, v54, s58, v62\n" \
"v_fma_f32 v55, v55, s58, v62\nv_mfma_f32_16x16x32_bf16 v[32:35], a[4:7], v[92:95], v[32:35]\nds_read_b128 v[88:91], v2 offset:6144\nv_fma_f32 v48, v48, s58, v62\nv_mfma_f32_16x16x32_bf16 v[36:39], a[12:15], v[92:95], v[36:39]\nv_exp_f32 v53, v53\nv_mfma_f32_16x16x32_bf16 v[40:43], a[20:23], v[92:95], v[40:43]\nv_exp_f32 v54, v54\n" \
"v_mfma_f32_16x16x32_bf16 v[44:47], a[28:31], v[92:95], v[44:47]\ns_waitcnt lgkmcnt(3)\nv_add_f32 v63, v52, v53\nv_add_f32 v63, v63, v54\nv_mfma_f32_16x16x32_bf16 v[16:19], a[32:35], v[80:83], v[16:19]\nds_read_b128 v[92:95], v2 offset:6400\nv_fma_f32 v49, v49, s58, v62\nv_mfma_f32_16x16x32_bf16 v[20:23], v[72:75], v[80:83], v[20:23]\n" \
"ds_read_b128 v[64:67], v1 offset:2048\nv_mfma_f32_16x16x32_bf16 v[24:27], a[40:43], v[80:83], v[24:27]\nv_exp_f32 v55, v55\nv_mfma_f32_16x16x32_bf16 v[28:31], a[48:51], v[80:83], v[28:31]\ns_waitcnt lgkmcnt(4)\nv_exp_f32 v48, v48\nv_mfma_f32_16x16x32_bf16 v[32:35], a[32:35], v[84:87], v[32:35]\nds_read_b128 v[80:83], v2 offset:8192\n" \
"v_add_f32 v63, v63, v55\nv_mfma_f32_16x16x32_bf16 v[36:39], v[72:75], v[84:87], v[36:39]\nv_exp_f32 v49, v49\nv_mfma_f32_16x16x32_bf16 v[40:43], a[40:43], v[84:87], v[40:43]\nv_add_f32 v63, v63, v48\nv_add_f32 v62, v63, v49\nv_mfma_f32_16x16x32_bf16 v[44:47], a[48:51], v[84:87], v[44:47]\ns_waitcnt lgkmcnt(3)\n" \
"v_add_f32 v50, v63, v49\nv_mfma_f32_16x16x32_bf16 v[16:19], a[36:39], v[88:91], v[16:19]\nds_read_b128 v[84:87], v2 offset:8448\nv_mfma_f32_16x16x32_bf16 v[20:23], v[76:79], v[88:91], v[20:23]\nds_read_b128 v[68:71], v1 offset:3072\nv_mfma_f32_16x16x32_bf16 v[24:27], a[44:47], v[88:91], v[24:27]\nv_permlane16_swap_b32 v62, v50\nv_mfma_f32_16x16x32_bf16 v[28:31], a[52:55], v[88:91], v[28:31]\n" \
"s_waitcnt lgkmcnt(4)\nv_add_f32 v63, v62, v50\nv_add_f32 v50, v62, v50\nv_mfma_f32_16x16x32_bf16 v[32:35], a[36:39], v[92:95], v[32:35]\nds_read_b128 v[88:91], v2 offset:10240\nv_mfma_f32_16x16x32_bf16 v[36:39], v[76:79], v[92:95], v[36:39]\nv_permlane32_swap_b32 v63, v50\nv_mfma_f32_16x16x32_bf16 v[40:43], a[44:47], v[92:95], v[40:43]\n" \
"v_add_f32 v63, v63, v50\nv_mfma_f32_16x16x32_bf16 v[44:47], a[52:55], v[92:95], v[44:47]\ns_waitcnt lgkmcnt(3)\nv_rcp_f32 v63, v63\nv_mfma_f32_16x16x32_bf16 v[16:19], a[56:59], v[80:83], v[16:19]\nds_read_b128 v[92:95], v2 offset:10496\nv_mul_f32 v52, v52, v63\nv_mfma_f32_16x16x32_bf16 v[20:23], a[64:67], v[80:83], v[20:23]\n" \
"ds_read_b128 v[72:75], v1 offset:4096\nv_mul_f32 v53, v53, v63\nv_mfma_f32_16x16x32_bf16 v[24:27], v[64:67], v[80:83], v[24:27]\nv_mul_f32 v54, v54, v63\nv_mul_f32 v55, v55, v63\nv_mfma_f32_16x16x32_bf16 v[28:31], a[72:75], v[80:83], v[28:31]\ns_waitcnt lgkmcnt(4)\nv_mul_f32 v48, v48, v63\n" \
"v_mul_f32 v49, v49, v63\nv_mfma_f32_16x16x32_bf16 v[32:35], a[56:59], v[84:87], v[32:35]\nds_read_b128 v[80:83], v2 offset:12288\nv_cvt_pk_bf16_f32 v52, v52, v53\nv_mfma_f32_16x16x32_bf16 v[36:39], a[64:67], v[84:87], v[36:39]\nv_cvt_pk_bf16_f32 v53, v54, v55\nv_cvt_pk_bf16_f32 v48, v48, v49\nv_mfma_f32_16x16x32_bf16 v[40:43], v[64:67], v[84:87], v[40:43]\n" \
"v_bfi_b32 v52, s61, v52, v61\nv_bfi_b32 v53, s61, v53, v61\nv_mfma_f32_16x16x32_bf16 v[44:47], a[72:75], v[84:87], v[44:47]\ns_waitcnt lgkmcnt(3)\nv_bfi_b32 v48, s61, v48, v61\nv_mfma_f32_16x16x32_bf16 v[16:19], a[60:63], v[88:91], v[16:19]\nds_read_b128 v[84:87], v2 offset:12544\nv_mfma_f32_16x16x32_bf16 v[20:23], a[68:71], v[88:91], v[20:23]\n" \
"ds_read_b128 v[76:79], v1 offset:5120\nv_mfma_f32_16x16x32_bf16 v[24:27], v[68:71], v[88:91], v[24:27]\nbuffer_store_dwordx2 v[52:53], v10, s[36:39], s55 offen sc1\nv_mfma_f32_16x16x32_bf16 v[28:31], a[76:79], v[88:91], v[28:31]\ns_waitcnt lgkmcnt(4)\nbuffer_store_dword v48, v11, s[36:39], s55 offen sc1\nv_mfma_f32_16x16x32_bf16 v[32:35], a[60:63], v[92:95], v[32:35]\nds_read_b128 v[88:91], v2 offset:14336\n" \
"v_cmp_ne_u32 vcc, 0, v60\nv_mfma_f32_16x16x32_bf16 v[36:39], a[68:71], v[92:95], v[36:39]\nv_mfma_f32_16x16x32_bf16 v[40:43], v[68:71], v[92:95], v[40:43]\nv_mfma_f32_16x16x32_bf16 v[44:47], a[76:79], v[92:95], v[44:47]\ns_cbranch_vccnz .Lqk_pj%=\ns_cmp_lt_i32 s40, 63\ns_waitcnt lgkmcnt(0)\ns_cbranch_scc1 .Lqk_pwait%=\n" \
"s_branch .Lqk_end%=\n.Lqk_re1%=:\nv_max_u32 v17, v80, v82\nv_cmp_eq_u32 vcc, -1, v17\ns_cbranch_vccz .Lqk_sk1996%=\ns_mov_b64 exec, vcc\nbuffer_load_dwordx4 v[80:83], v3, s[36:39], s42 offen offset:0 sc1\ns_mov_b64 exec, -1\n" \
".Lqk_sk1996%=:\nv_max_u32 v17, v84, v86\nv_cmp_eq_u32 vcc, -1, v17\ns_cbranch_vccz .Lqk_sk2003%=\ns_mov_b64 exec, vcc\nbuffer_load_dwordx4 v[84:87], v3, s[36:39], s42 offen offset:1024 sc1\ns_mov_b64 exec, -1\n.Lqk_sk2003%=:\n" \
"v_max_u32 v17, v88, v90\nv_cmp_eq_u32 vcc, -1, v17\ns_cbranch_vccz .Lqk_sk2010%=\ns_mov_b64 exec, vcc\nbuffer_load_dwordx4 v[88:91], v3, s[36:39], s42 offen offset:2048 sc1\ns_mov_b64 exec, -1\n.Lqk_sk2010%=:\nv_max_u32 v17, v92, v94\n" \
"v_cmp_eq_u32 vcc, -1, v17\ns_cbranch_vccz .Lqk_sk2017%=\ns_mov_b64 exec, vcc\nbuffer_load_dwordx4 v[92:95], v3, s[36:39], s42 offen offset:3072 sc1\ns_mov_b64 exec, -1\n.Lqk_sk2017%=:\nv_mov_b64 v[48:49], 0\nv_mov_b64 v[50:51], 0\n" \
"v_mov_b64 v[52:53], 0\nv_mov_b64 v[54:55], 0\nv_mov_b64 v[56:57], 0\nv_mov_b64 v[58:59], 0\nv_mov_b64 v[60:61], 0\nv_mov_b64 v[62:63], 0\ns_add_u32 s43, s43, 1\ns_cmp_lt_u32 s43, 262144\n" \
"s_cbranch_scc1 .Lqk_poll1%=\ns_branch .Lqk_abort%=\n.Lqk_c2%=:\ns_waitcnt lgkmcnt(3)\nv_mfma_f32_16x16x32_bf16 v[32:35], v[232:235], v[56:59], v[32:35]\nv_mov_b64 v[52:53], 0\nv_mov_b64 v[54:55], 0\nv_mfma_f32_16x16x32_bf16 v[36:39], v[240:243], v[56:59], v[36:39]\n" \
"s_waitcnt lgkmcnt(2)\nds_read_b128 v[64:67], v1 offset:12288\nv_mfma_f32_16x16x32_bf16 v[32:35], v[236:239], v[60:63], v[32:35]\nds_read_b128 v[68:71], v1 offset:13312\nv_mfma_f32_16x16x32_bf16 v[36:39], v[244:247], v[60:63], v[36:39]\ns_bfe_i32 s54, s40, 0x10001\nv_mfma_f32_16x16x32_bf16 v[40:43], v[248:251], v[56:59], v[40:43]\nv_mfma_f32_16x16x32_bf16 v[44:47], v[72:75], v[56:59], v[44:47]\n" \
"v_cvt_pk_bf16_f32 v36, v32, v36\nv_cvt_pk_bf16_f32 v37, v33, v37\nv_mfma_f32_16x16x32_bf16 v[40:43], v[252:255], v[60:63], v[40:43]\nv_cvt_pk_bf16_f32 v38, v34, v38\nv_cvt_pk_bf16_f32 v39, v35, v39\nv_mfma_f32_16x16x32_bf16 v[44:47], v[76:79], v[60:63], v[44:47]\nds_write_b128 v5, v[36:39] offset:256\nv_mov_b64 v[56:57], 0\n" \
"v_mov_b64 v[58:59], 0\nv_mov_b64 v[60:61], 0\nv_mov_b64 v[62:63], 0\n.Lqk_poll2%=:\ns_waitcnt vmcnt(0)\nv_smfmac_f32_16x16x64_bf16 v[48:51], v[80:83], a[208:215], v4\nds_write_b128 v0, v[80:83] offset:24576\nv_max3_u32 v16, v80, v82, v84\n" \
"v_smfmac_f32_16x16x64_bf16 v[52:55], v[80:83], a[216:223], v4\nds_read_b128 v[72:75], v1 offset:14336\nds_read_b128 v[76:79], v1 offset:15360\nv_max3_u32 v16, v16, v86, v88\nv_smfmac_f32_16x16x64_bf16 v[56:59], v[80:83], a[224:231], v4\nds_write_b128 v0, v[84:87] offset:25600\nv_max3_u32 v16, v16, v90, v92\nv_max_u32 v16, v16, v94\n" \
"v_smfmac_f32_16x16x64_bf16 v[60:63], v[80:83], a[232:239], v4\nv_cmp_eq_u32 vcc, -1, v16\nv_smfmac_f32_16x16x64_bf16 v[48:51], v[84:87], a[240:247], v4\nds_write_b128 v0, v[88:91] offset:26624\ns_cbranch_vccnz .Lqk_re2%=\ns_waitcnt lgkmcnt(6)\nv_smfmac_f32_16x16x64_bf16 v[52:55], v[84:87], v[64:71], v4\nds_read_b128 v[64:67], v1 offset:16384\n" \
"ds_read_b128 v[68:71], v1 offset:17408\nv_smfmac_f32_16x16x64_bf16 v[56:59], v[84:87], a[248:255], v4\nds_write_b128 v0, v[92:95] offset:27648\nv_smfmac_f32_16x16x64_bf16 v[60:63], v[84:87], v[96:103], v4\nv_smfmac_f32_16x16x64_bf16 v[48:51], v[88:91], v[104:111], v4\nv_smfmac_f32_16x16x64_bf16 v[52:55], v[88:91], v[112:119], v4\ns_waitcnt lgkmcnt(0)\ns_barrier\n" \
"ds_read_b128 v[80:83], v0 offset:28672\nds_read_b128 v[84:87], v0 offset:29696\nv_smfmac_f32_16x16x64_bf16 v[56:59], v[88:91], v[72:79], v4\nds_read_b128 v[72:75], v1 offset:18432\nds_read_b128 v[76:79], v1 offset:19456\nv_cvt_pk_bf16_f32 v44, v40, v44\nv_smfmac_f32_16x16x64_bf16 v[60:63], v[88:91], v[120:127], v4\nds_read_b128 v[88:91], v0 offset:30720\n" \
"v_cvt_pk_bf16_f32 v45, v41, v45\nv_smfmac_f32_16x16x64_bf16 v[48:51], v[92:95], v[128:135], v4\nv_cvt_pk_bf16_f32 v46, v42, v46\nv_smfmac_f32_16x16x64_bf16 v[52:55], v[92:95], v[136:143], v4\nv_cvt_pk_bf16_f32 v47, v43, v47\nv_smfmac_f32_16x16x64_bf16 v[56:59], v[92:95], v[144:151], v4\nv_smfmac_f32_16x16x64_bf16 v[60:63], v[92:95], v[64:71], v4\ns_waitcnt lgkmcnt(4)\n" \
"ds_write_b128 v5, v[44:47] offset:768\nds_read_b128 v[92:95], v0 offset:31744\nds_read_b128 v[64:67], v1 offset:20480\nds_read_b128 v[68:71], v1 offset:21504\nv_smfmac_f32_16x16x64_bf16 v[48:51], v[80:83], v[152:159], v4\nv_smfmac_f32_16x16x64_bf16 v[52:55], v[80:83], v[160:167], v4\nv_smfmac_f32_16x16x64_bf16 v[56:59], v[80:83], v[168:175], v4\nv_smfmac_f32_16x16x64_bf16 v[60:63], v[80:83], v[176:183], v4\n" \
"s_waitcnt lgkmcnt(7)\nds_read_b128 v[80:83], v0 offset:16384\nv_smfmac_f32_16x16x64_bf16 v[48:51], v[84:87], v[184:191], v4\ns_waitcnt lgkmcnt(6)\nv_smfmac_f32_16x16x64_bf16 v[52:55], v[84:87], v[72:79], v4\nds_read_b128 v[72:75], v1 offset:22528\nds_read_b128 v[76:79], v1 offset:23552\nv_smfmac_f32_16x16x64_bf16 v[56:59], v[84:87], v[192:199], v4\n" \
"v_smfmac_f32_16x16x64_bf16 v[60:63], v[84:87], v[200:207], v4\ns_waitcnt lgkmcnt(7)\nds_read_b128 v[84:87], v0 offset:17408\nv_smfmac_f32_16x16x64_bf16 v[48:51], v[88:91], v[208:215], v4\ns_waitcnt lgkmcnt(7)\ns_barrier\nv_smfmac_f32_16x16x64_bf16 v[52:55], v[88:91], v[216:223], v4\ns_waitcnt lgkmcnt(4)\n" \
"v_smfmac_f32_16x16x64_bf16 v[56:59], v[88:91], v[64:71], v4\nds_read_b128 v[64:67], v1 offset:0\nds_read_b128 v[68:71], v1 offset:1024\nv_smfmac_f32_16x16x64_bf16 v[60:63], v[88:91], v[224:231], v4\nds_read_b128 v[88:91], v0 offset:18432\nv_smfmac_f32_16x16x64_bf16 v[48:51], v[92:95], v[232:239], v4\nds_read_b128 v[16:19], v7 offset:256\nv_smfmac_f32_16x16x64_bf16 v[52:55], v[92:95], v[240:247], v4\n" \
"v_smfmac_f32_16x16x64_bf16 v[56:59], v[92:95], v[248:255], v4\ns_waitcnt lgkmcnt(5)\nds_read_b128 v[20:23], v7 offset:2304\nv_smfmac_f32_16x16x64_bf16 v[60:63], v[92:95], v[72:79], v4\nds_read_b128 v[92:95], v0 offset:19456\nds_read_b128 v[72:75], v1 offset:2048\nds_read_b128 v[76:79], v1 offset:3072\nv_smfmac_f32_16x16x64_bf16 v[48:51], v[80:83], a[0:7], v4\n" \
"ds_read_b128 v[24:27], v7 offset:4352\nv_smfmac_f32_16x16x64_bf16 v[52:55], v[80:83], a[8:15], v4\nv_smfmac_f32_16x16x64_bf16 v[56:59], v[80:83], a[16:23], v4\nds_read_b128 v[28:31], v7 offset:6400\nv_smfmac_f32_16x16x64_bf16 v[60:63], v[80:83], a[24:31], v4\ns_waitcnt lgkmcnt(10)\nds_read_b128 v[80:83], v0 offset:20480\nv_smfmac_f32_16x16x64_bf16 v[48:51], v[84:87], a[32:39], v4\n" \
"s_waitcnt lgkmcnt(9)\nds_read_b128 v[32:35], v7 offset:8192\nv_smfmac_f32_16x16x64_bf16 v[52:55], v[84:87], v[64:71], v4\nds_read_b128 v[64:67], v1 offset:4096\nds_read_b128 v[68:71], v1 offset:5120\nv_smfmac_f32_16x16x64_bf16 v[56:59], v[84:87], a[40:47], v4\nds_read_b128 v[36:39], v7 offset:10240\nv_smfmac_f32_16x16x64_bf16 v[60:63], v[84:87], a[48:55], v4\n" \
"s_waitcnt lgkmcnt(12)\nds_read_b128 v[84:87], v0 offset:21504\nv_smfmac_f32_16x16x64_bf16 v[48:51], v[88:91], a[56:63], v4\nds_read_b128 v[40:43], v7 offset:12288\nv_smfmac_f32_16x16x64_bf16 v[52:55], v[88:91], a[64:71], v4\ns_waitcnt lgkmcnt(9)\nv_smfmac_f32_16x16x64_bf16 v[56:59], v[88:91], v[72:79], v4\nds_read_b128 v[72:75], v1 offset:6144\n" \
"ds_read_b128 v[76:79], v1 offset:7168\nv_smfmac_f32_16x16x64_bf16 v[60:63], v[88:91], a[72:79], v4\nds_read_b128 v[88:91], v0 offset:22528\nv_smfmac_f32_16x16x64_bf16 v[48:51], v[92:95], a[80:87], v4\nds_read_b128 v[44:47], v7 offset:14336\nv_smfmac_f32_16x16x64_bf16 v[52:55], v[92:95], a[88:95], v4\nv_smfmac_f32_16x16x64_bf16 v[56:59], v[92:95], a[96:103], v4\ns_waitcnt lgkmcnt(7)\n" \
"v_smfmac_f32_16x16x64_bf16 v[60:63], v[92:95], v[64:71], v4\nds_read_b128 v[92:95], v0 offset:23552\nds_read_b128 v[64:67], v1 offset:8192\nds_read_b128 v[68:71], v1 offset:9216\nv_smfmac_f32_16x16x64_bf16 v[48:51], v[80:83], a[104:111], v4\nv_smfmac_f32_16x16x64_bf16 v[52:55], v[80:83], a[112:119], v4\nv_smfmac_f32_16x16x64_bf16 v[56:59], v[80:83], a[120:127], v4\nv_smfmac_f32_16x16x64_bf16 v[60:63], v[80:83], a[128:135], v4\n" \
"s_waitcnt lgkmcnt(8)\nv_smfmac_f32_16x16x64_bf16 v[48:51], v[84:87], a[136:143], v4\ns_waitcnt lgkmcnt(5)\nds_read_b128 v[80:83], v7 offset:8448\nv_smfmac_f32_16x16x64_bf16 v[52:55], v[84:87], v[72:79], v4\nds_read_b128 v[72:75], v1 offset:10240\nds_read_b128 v[76:79], v1 offset:11264\nv_smfmac_f32_16x16x64_bf16 v[56:59], v[84:87], a[144:151], v4\n" \
"v_smfmac_f32_16x16x64_bf16 v[60:63], v[84:87], a[152:159], v4\ns_waitcnt lgkmcnt(7)\nv_smfmac_f32_16x16x64_bf16 v[48:51], v[88:91], a[160:167], v4\nds_read_b128 v[84:87], v7 offset:10496\nv_smfmac_f32_16x16x64_bf16 v[52:55], v[88:91], a[168:175], v4\ns_waitcnt lgkmcnt(6)\nv_smfmac_f32_16x16x64_bf16 v[48:51], v[92:95], a[184:191], v4\nv_smfmac_f32_16x16x64_bf16 v[52:55], v[92:95], a[192:199], v4\n" \
"s_waitcnt lgkmcnt(4)\nv_smfmac_f32_16x16x64_bf16 v[56:59], v[88:91], v[64:71], v4\nds_read_b128 v[64:67], v7 offset:12544\nv_add_f32 v48, v48, v50\nv_smfmac_f32_16x16x64_bf16 v[60:63], v[88:91], a[176:183], v4\nds_read_b128 v[68:71], v7 offset:14592\nv_add_f32 v52, v52, v54\nv_add_f32 v49, v49, v51\n" \
"v_smfmac_f32_16x16x64_bf16 v[56:59], v[92:95], a[200:207], v4\ns_waitcnt lgkmcnt(3)\nv_add_f32 v53, v53, v55\nv_cvt_pk_bf16_f32 v50, v48, v52\nv_smfmac_f32_16x16x64_bf16 v[60:63], v[92:95], v[72:79], v4\nv_cvt_pk_bf16_f32 v51, v49, v53\nds_write2_b32 v6, v50, v51 offset0:0 offset1:4\ns_nop 1\n" \
"v_add_f32 v56, v56, v58\nv_add_f32 v57, v57, v59\ns_nop 1\nv_add_f32 v60, v60, v62\nv_add_f32 v61, v61, v63\nv_cvt_pk_bf16_f32 v62, v56, v60\nv_cvt_pk_bf16_f32 v63, v57, v61\nds_write2_b32 v6, v62, v63 offset0:36 offset1:40\n" \
"v_mov_b32 v61, s54\nv_mfma_f32_16x16x32_bf16 v[52:55], v[32:35], v[16:19], 0\nv_mfma_f32_16x16x32_bf16 v[52:55], v[36:39], v[20:23], v[52:55]\nv_mfma_f32_16x16x32_bf16 v[52:55], v[40:43], v[24:27], v[52:55]\nv_mfma_f32_16x16x32_bf16 v[52:55], v[44:47], v[28:31], v[52:55]\ns_waitcnt lgkmcnt(0)\ns_barrier\nds_read_b128 v[32:35], v9 offset:0\n" \
"ds_read_b128 v[36:39], v9 offset:576\nds_read_b128 v[40:43], v9 offset:1152\nds_read_b128 v[44:47], v9 offset:1728\nds_read_b32 v60, v0 offset:53760\nv_mfma_f32_16x16x32_bf16 v[56:59], v[80:83], v[16:19], v[12:15]\nv_mfma_f32_16x16x32_bf16 v[56:59], v[84:87], v[20:23], v[56:59]\nv_mfma_f32_16x16x32_bf16 v[56:59], v[64:67], v[24:27], v[56:59]\nv_mfma_f32_16x16x32_bf16 v[56:59], v[68:71], v[28:31], v[56:59]\n" \
"s_waitcnt lgkmcnt(4)\nds_read_b128 v[80:83], v2 offset:0\nds_read_b128 v[84:87], v2 offset:256\nds_read_b128 v[88:91], v2 offset:2048\nv_mfma_f32_16x16x32_bf16 v[48:51], v[32:35], v[16:19], 0\ns_waitcnt lgkmcnt(6)\nv_mfma_f32_16x16x32_bf16 v[48:51], v[36:39], v[20:23], v[48:51]\ns_waitcnt lgkmcnt(5)\n" \
"v_mfma_f32_16x16x32_bf16 v[48:51], v[40:43], v[24:27], v[48:51]\ns_waitcnt lgkmcnt(4)\nv_mfma_f32_16x16x32_bf16 v[48:51], v[44:47], v[28:31], v[48:51]\ns_waitcnt lgkmcnt(2)\nv_mfma_f32_16x16x32_bf16 v[16:19], a[0:3], v[80:83], 0\nds_read_b128 v[92:95], v2 offset:2304\nv_max3_f32 v62, v52, v53, v54\nv_max3_f32 v62, v62, v56, v57\n" \
"v_mfma_f32_16x16x32_bf16 v[20:23], a[8:11], v[80:83], 0\nds_read_b128 v[72:75], v1 offset:0\nv_max3_f32 v62, v62, v58, v59\nv_max_f32 v62, v62, v55\nv_mfma_f32_16x16x32_bf16 v[24:27], a[16:19], v[80:83], 0\nv_max3_f32 v63, v62, v48, v49\nv_max3_f32 v62, v62, v48, v49\nv_mfma_f32_16x16x32_bf16 v[28:31], a[24:27], v[80:83], 0\n" \
"s_waitcnt lgkmcnt(3)\nv_permlane16_swap_b32 v62, v63\nv_max_f32 v50, v62, v63\nv_mfma_f32_16x16x32_bf16 v[32:35], a[0:3], v[84:87], 0\nds_read_b128 v[80:83], v2 offset:4096\nv_max_f32 v62, v62, v63\nv_mfma_f32_16x16x32_bf16 v[36:39], a[8:11], v[84:87], 0\ns_nop 0\n" \
"v_permlane32_swap_b32 v62, v50\nv_max_f32 v62, v62, v50\nv_mfma_f32_16x16x32_bf16 v[40:43], a[16:19], v[84:87], 0\nv_mul_f32 v62, s59, v62\nv_fma_f32 v52, v52, s58, v62\nv_fma_f32 v53, v53, s58, v62\nv_mfma_f32_16x16x32_bf16 v[44:47], a[24:27], v[84:87], 0\ns_waitcnt lgkmcnt(3)\n" \
"v_exp_f32 v52, v52\nv_fma_f32 v54, v54, s58, v62\nv_mfma_f32_16x16x32_bf16 v[16:19], a[4:7], v[88:91], v[16:19]\nds_read_b128 v[84:87], v2 offset:4352\nv_exp_f32 v53, v53\nv_mfma_f32_16x16x32_bf16 v[20:23], a[12:15], v[88:91], v[20:23]\nds_read_b128 v[76:79], v1 offset:1024\nv_fma_f32 v55, v55, s58, v62\n" \
"v_add_f32 v63, v52, v53\nv_mfma_f32_16x16x32_bf16 v[24:27], a[20:23], v[88:91], v[24:27]\nv_exp_f32 v54, v54\nv_fma_f32 v56, v56, s58, v62\nv_mfma_f32_16x16x32_bf16 v[28:31], a[28:31], v[88:91], v[28:31]\ns_waitcnt lgkmcnt(4)\nv_exp_f32 v55, v55\nv_add_f32 v63, v63, v54\n" \
"v_mfma_f32_16x16x32_bf16 v[32:35], a[4:7], v[92:95], v[32:35]\nds_read_b128 v[88:91], v2 offset:6144\nv_fma_f32 v57, v57, s58, v62\nv_add_f32 v63, v63, v55\nv_mfma_f32_16x16x32_bf16 v[36:39], a[12:15], v[92:95], v[36:39]\nv_exp_f32 v56, v56\nv_fma_f32 v58, v58, s58, v62\nv_mfma_f32_16x16x32_bf16 v[40:43], a[20:23], v[92:95], v[40:43]\n" \
"v_exp_f32 v57, v57\nv_add_f32 v63, v63, v56\nv_mfma_f32_16x16x32_bf16 v[44:47], a[28:31], v[92:95], v[44:47]\ns_waitcnt lgkmcnt(3)\nv_fma_f32 v59, v59, s58, v62\nv_exp_f32 v58, v58\nv_mfma_f32_16x16x32_bf16 v[16:19], a[32:35], v[80:83], v[16:19]\nds_read_b128 v[92:95], v2 offset:6400\n" \
"v_add_f32 v63, v63, v57\nv_fma_f32 v48, v48, s58, v62\nv_mfma_f32_16x16x32_bf16 v[20:23], v[72:75], v[80:83], v[20:23]\nds_read_b128 v[64:67], v1 offset:2048\nv_exp_f32 v59, v59\nv_mfma_f32_16x16x32_bf16 v[24:27], a[40:43], v[80:83], v[24:27]\nv_add_f32 v63, v63, v58\nv_fma_f32 v49, v49, s58, v62\n" \
"v_add_f32 v63, v63, v59\nv_mfma_f32_16x16x32_bf16 v[28:31], a[48:51], v[80:83], v[28:31]\ns_waitcnt lgkmcnt(4)\nv_exp_f32 v48, v48\nv_mfma_f32_16x16x32_bf16 v[32:35], a[32:35], v[84:87], v[32:35]\nds_read_b128 v[80:83], v2 offset:8192\nv_exp_f32 v49, v49\nv_mfma_f32_16x16x32_bf16 v[36:39], v[72:75], v[84:87], v[36:39]\n" \
"v_add_f32 v63, v63, v48\nv_add_f32 v62, v63, v49\nv_add_f32 v50, v63, v49\nv_mfma_f32_16x16x32_bf16 v[40:43], a[40:43], v[84:87], v[40:43]\ns_nop 0\nv_permlane16_swap_b32 v62, v50\nv_add_f32 v63, v62, v50\nv_mfma_f32_16x16x32_bf16 v[44:47], a[48:51], v[84:87], v[44:47]\n" \
"s_waitcnt lgkmcnt(3)\nv_add_f32 v50, v62, v50\nv_mfma_f32_16x16x32_bf16 v[16:19], a[36:39], v[88:91], v[16:19]\nds_read_b128 v[84:87], v2 offset:8448\nv_permlane32_swap_b32 v63, v50\nv_mfma_f32_16x16x32_bf16 v[20:23], v[76:79], v[88:91], v[20:23]\nds_read_b128 v[68:71], v1 offset:3072\nv_add_f32 v63, v63, v50\n" \
"v_mfma_f32_16x16x32_bf16 v[24:27], a[44:47], v[88:91], v[24:27]\nv_rcp_f32 v63, v63\nv_mfma_f32_16x16x32_bf16 v[28:31], a[52:55], v[88:91], v[28:31]\ns_waitcnt lgkmcnt(4)\nv_mul_f32 v52, v52, v63\nv_mul_f32 v53, v53, v63\nv_mul_f32 v54, v54, v63\nv_mfma_f32_16x16x32_bf16 v[32:35], a[36:39], v[92:95], v[32:35]\n" \
"ds_read_b128 v[88:91], v2 offset:10240\nv_mul_f32 v55, v55, v63\nv_mul_f32 v56, v56, v63\nv_mfma_f32_16x16x32_bf16 v[36:39], v[76:79], v[92:95], v[36:39]\nv_mul_f32 v57, v57, v63\nv_mul_f32 v58, v58, v63\nv_mul_f32 v59, v59, v63\nv_mfma_f32_16x16x32_bf16 v[40:43], a[44:47], v[92:95], v[40:43]\n" \
"v_mul_f32 v48, v48, v63\nv_mul_f32 v49, v49, v63\nv_cvt_pk_bf16_f32 v52, v52, v53\nv_mfma_f32_16x16x32_bf16 v[44:47], a[52:55], v[92:95], v[44:47]\ns_waitcnt lgkmcnt(3)\nv_cvt_pk_bf16_f32 v53, v54, v55\nv_cvt_pk_bf16_f32 v48, v48, v49\nv_cvt_pk_bf16_f32 v54, v56, v57\n" \
"v_mfma_f32_16x16x32_bf16 v[16:19], a[56:59], v[80:83], v[16:19]\nds_read_b128 v[92:95], v2 offset:10496\nv_cvt_pk_bf16_f32 v55, v58, v59\nv_bfi_b32 v52, s61, v52, v61\nv_mfma_f32_16x16x32_bf16 v[20:23], a[64:67], v[80:83], v[20:23]\nds_read_b128 v[72:75], v1 offset:4096\nv_bfi_b32 v53, s61, v53, v61\nv_bfi_b32 v54, s61, v54, v61\n" \
"v_mfma_f32_16x16x32_bf16 v[24:27], v[64:67], v[80:83], v[24:27]\nv_bfi_b32 v55, s61, v55, v61\nv_bfi_b32 v48, s61, v48, v61\nv_mfma_f32_16x16x32_bf16 v[28:31], a[72:75], v[80:83], v[28:31]\ns_waitcnt lgkmcnt(4)\nbuffer_store_dwordx4 v[52:55], v10, s[36:39], s55 offen sc1\nv_mfma_f32_16x16x32_bf16 v[32:35], a[56:59], v[84:87], v[32:35]\nds_read_b128 v[80:83], v2 offset:12288\n" \
"v_mfma_f32_16x16x32_bf16 v[36:39], a[64:67], v[84:87], v[36:39]\nbuffer_store_dword v48, v11, s[36:39], s55 offen sc1\nv_cmp_ne_u32 vcc, 0, v60\nv_mfma_f32_16x16x32_bf16 v[40:43], v[64:67], v[84:87], v[40:43]\nv_mfma_f32_16x16x32_bf16 v[44:47], a[72:75], v[84:87], v[44:47]\ns_waitcnt lgkmcnt(3)\nv_mfma_f32_16x16x32_bf16 v[16:19], a[60:63], v[88:91], v[16:19]\nds_read_b128 v[84:87], v2 offset:12544\n" \
"v_mfma_f32_16x16x32_bf16 v[20:23], a[68:71], v[88:91], v[20:23]\nds_read_b128 v[76:79], v1 offset:5120\nv_mfma_f32_16x16x32_bf16 v[24:27], v[68:71], v[88:91], v[24:27]\nv_mfma_f32_16x16x32_bf16 v[28:31], a[76:79], v[88:91], v[28:31]\ns_waitcnt lgkmcnt(4)\nv_mfma_f32_16x16x32_bf16 v[32:35], a[60:63], v[92:95], v[32:35]\nds_read_b128 v[88:91], v2 offset:14336\nv_mfma_f32_16x16x32_bf16 v[36:39], a[68:71], v[92:95], v[36:39]\n" \
"v_mfma_f32_16x16x32_bf16 v[40:43], v[68:71], v[92:95], v[40:43]\nv_mfma_f32_16x16x32_bf16 v[44:47], a[76:79], v[92:95], v[44:47]\ns_cbranch_vccnz .Lqk_pj%=\ns_cmp_lt_i32 s40, 63\ns_waitcnt lgkmcnt(0)\ns_cbranch_scc1 .Lqk_pwait%=\ns_branch .Lqk_end%=\n.Lqk_re2%=:\n" \
"v_max_u32 v17, v80, v82\nv_cmp_eq_u32 vcc, -1, v17\ns_cbranch_vccz .Lqk_sk2406%=\ns_mov_b64 exec, vcc\nbuffer_load_dwordx4 v[80:83], v3, s[36:39], s42 offen offset:0 sc1\ns_mov_b64 exec, -1\n.Lqk_sk2406%=:\nv_max_u32 v17, v84, v86\n" \
"v_cmp_eq_u32 vcc, -1, v17\ns_cbranch_vccz .Lqk_sk2413%=\ns_mov_b64 exec, vcc\nbuffer_load_dwordx4 v[84:87], v3, s[36:39], s42 offen offset:1024 sc1\ns_mov_b64 exec, -1\n.Lqk_sk2413%=:\nv_max_u32 v17, v88, v90\nv_cmp_eq_u32 vcc, -1, v17\n" \
"s_cbranch_vccz .Lqk_sk2420%=\ns_mov_b64 exec, vcc\nbuffer_load_dwordx4 v[88:91], v3, s[36:39], s42 offen offset:2048 sc1\ns_mov_b64 exec, -1\n.Lqk_sk2420%=:\nv_max_u32 v17, v92, v94\nv_cmp_eq_u32 vcc, -1, v17\ns_cbranch_vccz .Lqk_sk2427%=\n" \
"s_mov_b64 exec, vcc\nbuffer_load_dwordx4 v[92:95], v3, s[36:39], s42 offen offset:3072 sc1\ns_mov_b64 exec, -1\n.Lqk_sk2427%=:\nv_mov_b64 v[48:49], 0\nv_mov_b64 v[50:51], 0\nv_mov_b64 v[52:53], 0\nv_mov_b64 v[54:55], 0\n" \
"v_mov_b64 v[56:57], 0\nv_mov_b64 v[58:59], 0\nv_mov_b64 v[60:61], 0\nv_mov_b64 v[62:63], 0\ns_add_u32 s43, s43, 1\ns_cmp_lt_u32 s43, 262144\ns_cbranch_scc1 .Lqk_poll2%=\ns_branch .Lqk_abort%=\n" \
".Lqk_c3%=:\ns_waitcnt lgkmcnt(3)\nv_mfma_f32_16x16x32_bf16 v[32:35], v[232:235], v[56:59], v[32:35]\nv_mov_b64 v[52:53], 0\nv_mov_b64 v[54:55], 0\nv_mfma_f32_16x16x32_bf16 v[36:39], v[240:243], v[56:59], v[36:39]\ns_add_u32 s44, s40, 1\ns_lshl_b32 s44, s44, 17\n" \
"s_waitcnt lgkmcnt(2)\nds_read_b128 v[64:67], v1 offset:18432\nv_mfma_f32_16x16x32_bf16 v[32:35], v[236:239], v[60:63], v[32:35]\ns_add_u32 s44, s44, %[xn]\ns_lshr_b32 s45, s44, 8\nds_read_b128 v[68:71], v1 offset:19456\nv_mfma_f32_16x16x32_bf16 v[36:39], v[244:247], v[60:63], v[36:39]\ns_and_b32 s45, s45, 0x2000\n" \
"s_xor_b32 s41, s44, s45\nv_mfma_f32_16x16x32_bf16 v[40:43], v[248:251], v[56:59], v[40:43]\nbuffer_load_dword v16, v12, s[36:39], s56 offen sc1\nv_mfma_f32_16x16x32_bf16 v[44:47], v[72:75], v[56:59], v[44:47]\nv_cvt_pk_bf16_f32 v36, v32, v36\nv_cvt_pk_bf16_f32 v37, v33, v37\nv_mfma_f32_16x16x32_bf16 v[40:43], v[252:255], v[60:63], v[40:43]\nv_cvt_pk_bf16_f32 v38, v34, v38\n" \
"v_cvt_pk_bf16_f32 v39, v35, v39\nv_mfma_f32_16x16x32_bf16 v[44:47], v[76:79], v[60:63], v[44:47]\nds_write_b128 v5, v[36:39] offset:256\nv_mov_b64 v[56:57], 0\nv_mov_b64 v[58:59], 0\nv_mov_b64 v[60:61], 0\nv_mov_b64 v[62:63], 0\n.Lqk_poll3%=:\n" \
"s_waitcnt vmcnt(1)\nv_smfmac_f32_16x16x64_bf16 v[48:51], v[80:83], v[152:159], v4\nds_write_b128 v0, v[80:83] offset:28672\nv_max3_u32 v17, v80, v82, v84\nv_smfmac_f32_16x16x64_bf16 v[52:55], v[80:83], v[160:167], v4\nds_read_b128 v[72:75], v1 offset:20480\nds_read_b128 v[76:79], v1 offset:21504\nv_max3_u32 v17, v17, v86, v88\n" \
"v_smfmac_f32_16x16x64_bf16 v[56:59], v[80:83], v[168:175], v4\nds_write_b128 v0, v[84:87] offset:29696\nv_max3_u32 v17, v17, v90, v92\nv_max_u32 v17, v17, v94\nv_smfmac_f32_16x16x64_bf16 v[60:63], v[80:83], v[176:183], v4\nv_cmp_eq_u32 vcc, -1, v17\nv_smfmac_f32_16x16x64_bf16 v[48:51], v[84:87], v[184:191], v4\nds_write_b128 v0, v[88:91] offset:30720\n" \
"s_cbranch_vccnz .Lqk_re3%=\ns_waitcnt lgkmcnt(6)\nv_smfmac_f32_16x16x64_bf16 v[52:55], v[84:87], v[64:71], v4\nds_read_b128 v[64:67], v1 offset:22528\nds_read_b128 v[68:71], v1 offset:23552\nv_smfmac_f32_16x16x64_bf16 v[56:59], v[84:87], v[192:199], v4\nds_write_b128 v0, v[92:95] offset:31744\nv_smfmac_f32_16x16x64_bf16 v[60:63], v[84:87], v[200:207], v4\n" \
"v_smfmac_f32_16x16x64_bf16 v[48:51], v[88:91], v[208:215], v4\nv_smfmac_f32_16x16x64_bf16 v[52:55], v[88:91], v[216:223], v4\ns_waitcnt lgkmcnt(0)\ns_barrier\nds_read_b128 v[80:83], v0 offset:16384\nds_read_b128 v[84:87], v0 offset:17408\nv_smfmac_f32_16x16x64_bf16 v[56:59], v[88:91], v[72:79], v4\ns_waitcnt vmcnt(0)\n" \
"ds_read_b128 v[72:75], v1 offset:0\nds_read_b128 v[76:79], v1 offset:1024\nv_cvt_pk_bf16_f32 v44, v40, v44\nv_cmp_ge_i32 vcc, s40, v16\nv_smfmac_f32_16x16x64_bf16 v[60:63], v[88:91], v[224:231], v4\ns_cmp_eq_u64 vcc, 0\ns_cselect_b32 s61, s41, -2147483648\ns_cselect_b32 s44, 1, 0\n" \
"ds_read_b128 v[88:91], v0 offset:18432\nv_cvt_pk_bf16_f32 v45, v41, v45\nv_smfmac_f32_16x16x64_bf16 v[48:51], v[92:95], v[232:239], v4\ns_add_u32 m0, %[l0], 0\ns_add_u32 s60, s61, 0\nbuffer_load_dwordx4 v0, s[36:39], s60 offen offset:0 sc1 lds\nv_cvt_pk_bf16_f32 v46, v42, v46\nv_smfmac_f32_16x16x64_bf16 v[52:55], v[92:95], v[240:247], v4\n" \
"v_cvt_pk_bf16_f32 v47, v43, v47\nv_mov_b32 v16, s44\nv_smfmac_f32_16x16x64_bf16 v[56:59], v[92:95], v[248:255], v4\nbuffer_load_dwordx4 v0, s[36:39], s60 offen offset:1024 sc1 lds\nv_smfmac_f32_16x16x64_bf16 v[60:63], v[92:95], v[64:71], v4\ns_waitcnt lgkmcnt(4)\nds_write_b128 v5, v[44:47] offset:768\nds_read_b128 v[92:95], v0 offset:19456\n" \
"ds_read_b128 v[64:67], v1 offset:2048\nds_read_b128 v[68:71], v1 offset:3072\nds_write_b32 v0, v16 offset:53760\nv_smfmac_f32_16x16x64_bf16 v[48:51], v[80:83], a[0:7], v4\nbuffer_load_dwordx4 v0, s[36:39], s60 offen offset:2048 sc1 lds\nv_smfmac_f32_16x16x64_bf16 v[52:55], v[80:83], a[8:15], v4\nv_smfmac_f32_16x16x64_bf16 v[56:59], v[80:83], a[16:23], v4\nbuffer_load_dwordx4 v0, s[36:39], s60 offen offset:3072 sc1 lds\n" \
"v_smfmac_f32_16x16x64_bf16 v[60:63], v[80:83], a[24:31], v4\ns_waitcnt lgkmcnt(8)\nds_read_b128 v[80:83], v0 offset:20480\nv_smfmac_f32_16x16x64_bf16 v[48:51], v[84:87], a[32:39], v4\ns_add_u32 m0, %[l0], 4096\ns_add_u32 s60, s61, 4096\ns_waitcnt lgkmcnt(7)\nbuffer_load_dwordx4 v0, s[36:39], s60 offen offset:0 sc1 lds\n" \
"v_smfmac_f32_16x16x64_bf16 v[52:55], v[84:87], v[72:79], v4\nds_read_b128 v[72:75], v1 offset:4096\nds_read_b128 v[76:79], v1 offset:5120\nv_smfmac_f32_16x16x64_bf16 v[56:59], v[84:87], a[40:47], v4\nbuffer_load_dwordx4 v0, s[36:39], s60 offen offset:1024 sc1 lds\nv_smfmac_f32_16x16x64_bf16 v[60:63], v[84:87], a[48:55], v4\ns_waitcnt lgkmcnt(8)\nds_read_b128 v[84:87], v0 offset:21504\n" \
"v_smfmac_f32_16x16x64_bf16 v[48:51], v[88:91], a[56:63], v4\ns_waitcnt lgkmcnt(8)\ns_barrier\nbuffer_load_dwordx4 v0, s[36:39], s60 offen offset:2048 sc1 lds\nv_smfmac_f32_16x16x64_bf16 v[52:55], v[88:91], a[64:71], v4\ns_waitcnt lgkmcnt(5)\nv_smfmac_f32_16x16x64_bf16 v[56:59], v[88:91], v[64:71], v4\nds_read_b128 v[64:67], v1 offset:6144\n" \
"ds_read_b128 v[68:71], v1 offset:7168\nbuffer_load_dwordx4 v0, s[36:39], s60 offen offset:3072 sc1 lds\nv_smfmac_f32_16x16x64_bf16 v[60:63], v[88:91], a[72:79], v4\nds_read_b128 v[88:91], v0 offset:22528\nv_smfmac_f32_16x16x64_bf16 v[48:51], v[92:95], a[80:87], v4\ns_add_u32 m0, %[l0], 8192\ns_add_u32 s60, s61, 16384\nbuffer_load_dwordx4 v0, s[36:39], s60 offen offset:0 sc1 lds\n" \
"v_smfmac_f32_16x16x64_bf16 v[52:55], v[92:95], a[88:95], v4\nv_smfmac_f32_16x16x64_bf16 v[56:59], v[92:95], a[96:103], v4\ns_waitcnt lgkmcnt(4)\nbuffer_load_dwordx4 v0, s[36:39], s60 offen offset:1024 sc1 lds\nv_smfmac_f32_16x16x64_bf16 v[60:63], v[92:95], v[72:79], v4\nds_read_b128 v[92:95], v0 offset:23552\nds_read_b128 v[72:75], v1 offset:8192\nds_read_b128 v[76:79], v1 offset:9216\n" \
"v_smfmac_f32_16x16x64_bf16 v[48:51], v[80:83], a[104:111], v4\nbuffer_load_dwordx4 v0, s[36:39], s60 offen offset:2048 sc1 lds\nv_smfmac_f32_16x16x64_bf16 v[52:55], v[80:83], a[112:119], v4\nv_smfmac_f32_16x16x64_bf16 v[56:59], v[80:83], a[120:127], v4\nbuffer_load_dwordx4 v0, s[36:39], s60 offen offset:3072 sc1 lds\nv_smfmac_f32_16x16x64_bf16 v[60:63], v[80:83], a[128:135], v4\ns_waitcnt lgkmcnt(6)\nds_read_b128 v[80:83], v0 offset:24576\n" \
"v_smfmac_f32_16x16x64_bf16 v[48:51], v[84:87], a[136:143], v4\ns_add_u32 m0, %[l0], 12288\ns_add_u32 s60, s61, 20480\ns_waitcnt lgkmcnt(5)\nbuffer_load_dwordx4 v0, s[36:39], s60 offen offset:0 sc1 lds\nv_smfmac_f32_16x16x64_bf16 v[52:55], v[84:87], v[64:71], v4\nds_read_b128 v[64:67], v1 offset:10240\nds_read_b128 v[68:71], v1 offset:11264\n" \
"v_smfmac_f32_16x16x64_bf16 v[56:59], v[84:87], a[144:151], v4\nbuffer_load_dwordx4 v0, s[36:39], s60 offen offset:1024 sc1 lds\nv_smfmac_f32_16x16x64_bf16 v[60:63], v[84:87], a[152:159], v4\ns_waitcnt lgkmcnt(6)\nds_read_b128 v[84:87], v0 offset:25600\nv_smfmac_f32_16x16x64_bf16 v[48:51], v[88:91], a[160:167], v4\nbuffer_load_dwordx4 v0, s[36:39], s60 offen offset:2048 sc1 lds\nv_smfmac_f32_16x16x64_bf16 v[52:55], v[88:91], a[168:175], v4\n" \
"s_waitcnt lgkmcnt(4)\nv_smfmac_f32_16x16x64_bf16 v[56:59], v[88:91], v[72:79], v4\nds_read_b128 v[72:75], v1 offset:12288\nds_read_b128 v[76:79], v1 offset:13312\nbuffer_load_dwordx4 v0, s[36:39], s60 offen offset:3072 sc1 lds\nv_smfmac_f32_16x16x64_bf16 v[60:63], v[88:91], a[176:183], v4\nds_read_b128 v[88:91], v0 offset:26624\nv_smfmac_f32_16x16x64_bf16 v[48:51], v[92:95], a[184:191], v4\n" \
"v_smfmac_f32_16x16x64_bf16 v[52:55], v[92:95], a[192:199], v4\nv_smfmac_f32_16x16x64_bf16 v[56:59], v[92:95], a[200:207], v4\ns_waitcnt lgkmcnt(4)\nv_smfmac_f32_16x16x64_bf16 v[60:63], v[92:95], v[64:71], v4\nds_read_b128 v[92:95], v0 offset:27648\nds_read_b128 v[64:67], v1 offset:14336\nds_read_b128 v[68:71], v1 offset:15360\nv_smfmac_f32_16x16x64_bf16 v[48:51], v[80:83], a[208:215], v4\n" \
"v_smfmac_f32_16x16x64_bf16 v[52:55], v[80:83], a[216:223], v4\nv_smfmac_f32_16x16x64_bf16 v[56:59], v[80:83], a[224:231], v4\nv_smfmac_f32_16x16x64_bf16 v[60:63], v[80:83], a[232:239], v4\ns_waitcnt lgkmcnt(6)\nv_smfmac_f32_16x16x64_bf16 v[48:51], v[84:87], a[240:247], v4\ns_waitcnt lgkmcnt(4)\nv_smfmac_f32_16x16x64_bf16 v[52:55], v[84:87], v[72:79], v4\nds_read_b128 v[72:75], v1 offset:16384\n" \
"ds_read_b128 v[76:79], v1 offset:17408\nv_smfmac_f32_16x16x64_bf16 v[56:59], v[84:87], a[248:255], v4\nv_smfmac_f32_16x16x64_bf16 v[60:63], v[84:87], v[96:103], v4\ns_waitcnt lgkmcnt(5)\nv_smfmac_f32_16x16x64_bf16 v[48:51], v[88:91], v[104:111], v4\nv_smfmac_f32_16x16x64_bf16 v[52:55], v[88:91], v[112:119], v4\ns_waitcnt lgkmcnt(4)\nv_smfmac_f32_16x16x64_bf16 v[48:51], v[92:95], v[128:135], v4\n" \
"v_smfmac_f32_16x16x64_bf16 v[52:55], v[92:95], v[136:143], v4\ns_waitcnt lgkmcnt(2)\nv_smfmac_f32_16x16x64_bf16 v[56:59], v[88:91], v[64:71], v4\nv_add_f32 v48, v48, v50\nv_smfmac_f32_16x16x64_bf16 v[60:63], v[88:91], v[120:127], v4\nv_add_f32 v52, v52, v54\nv_add_f32 v49, v49, v51\nv_smfmac_f32_16x16x64_bf16 v[56:59], v[92:95], v[144:151], v4\n" \
"s_waitcnt lgkmcnt(0)\nv_add_f32 v53, v53, v55\nv_cvt_pk_bf16_f32 v50, v48, v52\nv_smfmac_f32_16x16x64_bf16 v[60:63], v[92:95], v[72:79], v4\nv_cvt_pk_bf16_f32 v51, v49, v53\nds_write2_b32 v6, v50, v51 offset0:0 offset1:4\ns_nop 1\nv_add_f32 v56, v56, v58\n" \
"v_add_f32 v57, v57, v59\ns_nop 1\nv_add_f32 v60, v60, v62\nv_add_f32 v61, v61, v63\nv_cvt_pk_bf16_f32 v62, v56, v60\nv_cvt_pk_bf16_f32 v63, v57, v61\nds_write2_b32 v6, v62, v63 offset0:36 offset1:40\ns_waitcnt vmcnt(0)\n" \
"s_waitcnt lgkmcnt(0)\ns_barrier\ns_cbranch_vccnz .Lqk_slow%=\nds_read_b128 v[80:83], v2 offset:0\nds_read_b128 v[84:87], v2 offset:256\nds_read_b128 v[88:91], v2 offset:2048\ns_add_u32 m0, %[l0], 16384\ns_add_u32 s60, s41, 32768\n" \
"buffer_load_dwordx4 v0, s[36:39], s60 offen offset:0 sc1 lds\nbuffer_load_dwordx4 v0, s[36:39], s60 offen offset:1024 sc1 lds\nbuffer_load_dwordx4 v0, s[36:39], s60 offen offset:2048 sc1 lds\nbuffer_load_dwordx4 v0, s[36:39], s60 offen offset:3072 sc1 lds\ns_add_u32 m0, %[l0], 20480\ns_add_u32 s60, s41, 36864\nbuffer_load_dwordx4 v0, s[36:39], s60 offen offset:0 sc1 lds\nbuffer_load_dwordx4 v0, s[36:39], s60 offen offset:1024 sc1 lds\n" \
"buffer_load_dwordx4 v0, s[36:39], s60 offen offset:2048 sc1 lds\nbuffer_load_dwordx4 v0, s[36:39], s60 offen offset:3072 sc1 lds\ns_add_u32 m0, %[l0], 24576\ns_add_u32 s60, s41, 49152\nbuffer_load_dwordx4 v0, s[36:39], s60 offen offset:0 sc1 lds\nbuffer_load_dwordx4 v0, s[36:39], s60 offen offset:1024 sc1 lds\nbuffer_load_dwordx4 v0, s[36:39], s60 offen offset:2048 sc1 lds\nbuffer_load_dwordx4 v0, s[36:39], s60 offen offset:3072 sc1 lds\n" \
"s_add_u32 m0, %[l0], 28672\ns_add_u32 s60, s41, 53248\nbuffer_load_dwordx4 v0, s[36:39], s60 offen offset:0 sc1 lds\nbuffer_load_dwordx4 v0, s[36:39], s60 offen offset:1024 sc1 lds\nbuffer_load_dwordx4 v0, s[36:39], s60 offen offset:2048 sc1 lds\nbuffer_load_dwordx4 v0, s[36:39], s60 offen offset:3072 sc1 lds\ns_waitcnt lgkmcnt(2)\nv_mfma_f32_16x16x32_bf16 v[16:19], a[0:3], v[80:83], 0\n" \
"ds_read_b128 v[92:95], v2 offset:2304\nv_mfma_f32_16x16x32_bf16 v[20:23], a[8:11], v[80:83], 0\nds_read_b128 v[72:75], v1 offset:0\nv_mfma_f32_16x16x32_bf16 v[24:27], a[16:19], v[80:83], 0\nv_mfma_f32_16x16x32_bf16 v[28:31], a[24:27], v[80:83], 0\ns_waitcnt lgkmcnt(3)\nv_mfma_f32_16x16x32_bf16 v[32:35], a[0:3], v[84:87], 0\nds_read_b128 v[80:83], v2 offset:4096\n" \
"v_mfma_f32_16x16x32_bf16 v[36:39], a[8:11], v[84:87], 0\nv_mfma_f32_16x16x32_bf16 v[40:43], a[16:19], v[84:87], 0\nv_mfma_f32_16x16x32_bf16 v[44:47], a[24:27], v[84:87], 0\ns_waitcnt lgkmcnt(3)\nv_mfma_f32_16x16x32_bf16 v[16:19], a[4:7], v[88:91], v[16:19]\nds_read_b128 v[84:87], v2 offset:4352\nv_mfma_f32_16x16x32_bf16 v[20:23], a[12:15], v[88:91], v[20:23]\nds_read_b128 v[76:79], v1 offset:1024\n" \
"v_mfma_f32_16x16x32_bf16 v[24:27], a[20:23], v[88:91], v[24:27]\nv_mfma_f32_16x16x32_bf16 v[28:31], a[28:31], v[88:91], v[28:31]\ns_waitcnt lgkmcnt(4)\nv_mfma_f32_16x16x32_bf16 v[32:35], a[4:7], v[92:95], v[32:35]\nds_read_b128 v[88:91], v2 offset:6144\nv_mfma_f32_16x16x32_bf16 v[36:39], a[12:15], v[92:95], v[36:39]\nv_mfma_f32_16x16x32_bf16 v[40:43], a[20:23], v[92:95], v[40:43]\nv_mfma_f32_16x16x32_bf16 v[44:47], a[28:31], v[92:95], v[44:47]\n" \
"s_waitcnt lgkmcnt(3)\nv_mfma_f32_16x16x32_bf16 v[16:19], a[32:35], v[80:83], v[16:19]\nds_read_b128 v[92:95], v2 offset:6400\nv_mfma_f32_16x16x32_bf16 v[20:23], v[72:75], v[80:83], v[20:23]\nds_read_b128 v[64:67], v1 offset:2048\nv_mfma_f32_16x16x32_bf16 v[24:27], a[40:43], v[80:83], v[24:27]\nv_mfma_f32_16x16x32_bf16 v[28:31], a[48:51], v[80:83], v[28:31]\ns_waitcnt lgkmcnt(4)\n" \
"v_mfma_f32_16x16x32_bf16 v[32:35], a[32:35], v[84:87], v[32:35]\nds_read_b128 v[80:83], v2 offset:8192\nv_mfma_f32_16x16x32_bf16 v[36:39], v[72:75], v[84:87], v[36:39]\nv_mfma_f32_16x16x32_bf16 v[40:43], a[40:43], v[84:87], v[40:43]\nv_mfma_f32_16x16x32_bf16 v[44:47], a[48:51], v[84:87], v[44:47]\ns_waitcnt lgkmcnt(3)\nv_mfma_f32_16x16x32_bf16 v[16:19], a[36:39], v[88:91], v[16:19]\nds_read_b128 v[84:87], v2 offset:8448\n" \
"v_mfma_f32_16x16x32_bf16 v[20:23], v[76:79], v[88:91], v[20:23]\nds_read_b128 v[68:71], v1 offset:3072\nv_mfma_f32_16x16x32_bf16 v[24:27], a[44:47], v[88:91], v[24:27]\nv_mfma_f32_16x16x32_bf16 v[28:31], a[52:55], v[88:91], v[28:31]\ns_waitcnt lgkmcnt(4)\nv_mfma_f32_16x16x32_bf16 v[32:35], a[36:39], v[92:95], v[32:35]\nds_read_b128 v[88:91], v2 offset:10240\nv_mfma_f32_16x16x32_bf16 v[36:39], v[76:79], v[92:95], v[36:39]\n" \
"v_mfma_f32_16x16x32_bf16 v[40:43], a[44:47], v[92:95], v[40:43]\nv_mfma_f32_16x16x32_bf16 v[44:47], a[52:55], v[92:95], v[44:47]\ns_waitcnt lgkmcnt(3)\nv_mfma_f32_16x16x32_bf16 v[16:19], a[56:59], v[80:83], v[16:19]\nds_read_b128 v[92:95], v2 offset:10496\nv_mfma_f32_16x16x32_bf16 v[20:23], a[64:67], v[80:83], v[20:23]\nds_read_b128 v[72:75], v1 offset:4096\nv_mfma_f32_16x16x32_bf16 v[24:27], v[64:67], v[80:83], v[24:27]\n" \
"v_mfma_f32_16x16x32_bf16 v[28:31], a[72:75], v[80:83], v[28:31]\ns_waitcnt lgkmcnt(4)\nv_mfma_f32_16x16x32_bf16 v[32:35], a[56:59], v[84:87], v[32:35]\nds_read_b128 v[80:83], v2 offset:12288\nv_mfma_f32_16x16x32_bf16 v[36:39], a[64:67], v[84:87], v[36:39]\nv_mfma_f32_16x16x32_bf16 v[40:43], v[64:67], v[84:87], v[40:43]\nv_mfma_f32_16x16x32_bf16 v[44:47], a[72:75], v[84:87], v[44:47]\ns_waitcnt lgkmcnt(3)\n" \
"v_mfma_f32_16x16x32_bf16 v[16:19], a[60:63], v[88:91], v[16:19]\nds_read_b128 v[84:87], v2 offset:12544\nv_mfma_f32_16x16x32_bf16 v[20:23], a[68:71], v[88:91], v[20:23]\nds_read_b128 v[76:79], v1 offset:5120\nv_mfma_f32_16x16x32_bf16 v[24:27], v[68:71], v[88:91], v[24:27]\nv_mfma_f32_16x16x32_bf16 v[28:31], a[76:79], v[88:91], v[28:31]\ns_waitcnt lgkmcnt(4)\nv_mfma_f32_16x16x32_bf16 v[32:35], a[60:63], v[92:95], v[32:35]\n" \
"ds_read_b128 v[88:91], v2 offset:14336\nv_mfma_f32_16x16x32_bf16 v[36:39], a[68:71], v[92:95], v[36:39]\nv_mfma_f32_16x16x32_bf16 v[40:43], v[68:71], v[92:95], v[40:43]\nv_mfma_f32_16x16x32_bf16 v[44:47], a[76:79], v[92:95], v[44:47]\ns_waitcnt vmcnt(0)\ns_branch .Lqk_pj%=\n.Lqk_slow%=:\ns_cmp_ge_i32 s40, 63\n" \
"s_cbranch_scc1 .Lqk_end%=\n.Lqk_fetch0%=:\nbuffer_load_dword v16, v12, s[36:39], s56 offen sc1\ns_waitcnt vmcnt(0)\nv_cmp_ge_i32 vcc, s40, v16\ns_cbranch_vccz .Lqk_fok%=\ns_add_u32 s43, s43, 1\ns_cmp_lt_u32 s43, 262144\n" \
"s_cbranch_scc1 .Lqk_fetch0%=\ns_branch .Lqk_abort%=\n.Lqk_fok%=:\ns_add_u32 s44, s40, 1\ns_lshl_b32 s44, s44, 17\ns_add_u32 s44, s44, %[xn]\ns_lshr_b32 s45, s44, 8\ns_and_b32 s45, s45, 0x2000\n" \
"s_xor_b32 s41, s44, s45\ns_add_u32 m0, %[l0], 0\ns_add_u32 s60, s41, 0\nbuffer_load_dwordx4 v0, s[36:39], s60 offen offset:0 sc1 lds\nbuffer_load_dwordx4 v0, s[36:39], s60 offen offset:1024 sc1 lds\nbuffer_load_dwordx4 v0, s[36:39], s60 offen offset:2048 sc1 lds\nbuffer_load_dwordx4 v0, s[36:39], s60 offen offset:3072 sc1 lds\ns_add_u32 m0, %[l0], 4096\n" \
"s_add_u32 s60, s41, 4096\nbuffer_load_dwordx4 v0, s[36:39], s60 offen offset:0 sc1 lds\nbuffer_load_dwordx4 v0, s[36:39], s60 offen offset:1024 sc1 lds\nbuffer_load_dwordx4 v0, s[36:39], s60 offen offset:2048 sc1 lds\nbuffer_load_dwordx4 v0, s[36:39], s60 offen offset:3072 sc1 lds\ns_add_u32 m0, %[l0], 8192\ns_add_u32 s60, s41, 16384\nbuffer_load_dwordx4 v0, s[36:39], s60 offen offset:0 sc1 lds\n" \
"buffer_load_dwordx4 v0, s[36:39], s60 offen offset:1024 sc1 lds\nbuffer_load_dwordx4 v0, s[36:39], s60 offen offset:2048 sc1 lds\nbuffer_load_dwordx4 v0, s[36:39], s60 offen offset:3072 sc1 lds\ns_add_u32 m0, %[l0], 12288\ns_add_u32 s60, s41, 20480\nbuffer_load_dwordx4 v0, s[36:39], s60 offen offset:0 sc1 lds\nbuffer_load_dwordx4 v0, s[36:39], s60 offen offset:1024 sc1 lds\nbuffer_load_dwordx4 v0, s[36:39], s60 offen offset:2048 sc1 lds\n" \
"buffer_load_dwordx4 v0, s[36:39], s60 offen offset:3072 sc1 lds\ns_add_u32 m0, %[l0], 16384\ns_add_u32 s60, s41, 32768\nbuffer_load_dwordx4 v0, s[36:39], s60 offen offset:0 sc1 lds\nbuffer_load_dwordx4 v0, s[36:39], s60 offen offset:1024 sc1 lds\nbuffer_load_dwordx4 v0, s[36:39], s60 offen offset:2048 sc1 lds\nbuffer_load_dwordx4 v0, s[36:39], s60 offen offset:3072 sc1 lds\ns_add_u32 m0, %[l0], 20480\n" \
"s_add_u32 s60, s41, 36864\nbuffer_load_dwordx4 v0, s[36:39], s60 offen offset:0 sc1 lds\nbuffer_load_dwordx4 v0, s[36:39], s60 offen offset:1024 sc1 lds\nbuffer_load_dwordx4 v0, s[36:39], s60 offen offset:2048 sc1 lds\nbuffer_load_dwordx4 v0, s[36:39], s60 offen offset:3072 sc1 lds\ns_add_u32 m0, %[l0], 24576\ns_add_u32 s60, s41, 49152\nbuffer_load_dwordx4 v0, s[36:39], s60 offen offset:0 sc1 lds\n" \
"buffer_load_dwordx4 v0, s[36:39], s60 offen offset:1024 sc1 lds\nbuffer_load_dwordx4 v0, s[36:39], s60 offen offset:2048 sc1 lds\nbuffer_load_dwordx4 v0, s[36:39], s60 offen offset:3072 sc1 lds\ns_add_u32 m0, %[l0], 28672\ns_add_u32 s60, s41, 53248\nbuffer_load_dwordx4 v0, s[36:39], s60 offen offset:0 sc1 lds\nbuffer_load_dwordx4 v0, s[36:39], s60 offen offset:1024 sc1 lds\nbuffer_load_dwordx4 v0, s[36:39], s60 offen offset:2048 sc1 lds\n" \
"buffer_load_dwordx4 v0, s[36:39], s60 offen offset:3072 sc1 lds\ns_waitcnt vmcnt(0)\ns_branch .Lqk_pwait%=\n.Lqk_re3%=:\nv_max_u32 v17, v80, v82\nv_cmp_eq_u32 vcc, -1, v17\ns_cbranch_vccz .Lqk_sk2840%=\ns_mov_b64 exec, vcc\n" \
"buffer_load_dwordx4 v[80:83], v3, s[36:39], s42 offen offset:0 sc1\ns_mov_b64 exec, -1\n.Lqk_sk2840%=:\nv_max_u32 v17, v84, v86\nv_cmp_eq_u32 vcc, -1, v17\ns_cbranch_vccz .Lqk_sk2847%=\ns_mov_b64 exec, vcc\nbuffer_load_dwordx4 v[84:87], v3, s[36:39], s42 offen offset:1024 sc1\n" \
"s_mov_b64 exec, -1\n.Lqk_sk2847%=:\nv_max_u32 v17, v88, v90\nv_cmp_eq_u32 vcc, -1, v17\ns_cbranch_vccz .Lqk_sk2854%=\ns_mov_b64 exec, vcc\nbuffer_load_dwordx4 v[88:91], v3, s[36:39], s42 offen offset:2048 sc1\ns_mov_b64 exec, -1\n" \
".Lqk_sk2854%=:\nv_max_u32 v17, v92, v94\nv_cmp_eq_u32 vcc, -1, v17\ns_cbranch_vccz .Lqk_sk2861%=\ns_mov_b64 exec, vcc\nbuffer_load_dwordx4 v[92:95], v3, s[36:39], s42 offen offset:3072 sc1\ns_mov_b64 exec, -1\n.Lqk_sk2861%=:\n" \
"buffer_load_dword v16, v12, s[36:39], s56 offen sc1\nv_mov_b64 v[48:49], 0\nv_mov_b64 v[50:51], 0\nv_mov_b64 v[52:53], 0\nv_mov_b64 v[54:55], 0\nv_mov_b64 v[56:57], 0\nv_mov_b64 v[58:59], 0\nv_mov_b64 v[60:61], 0\n" \
"v_mov_b64 v[62:63], 0\ns_add_u32 s43, s43, 1\ns_cmp_lt_u32 s43, 262144\ns_cbranch_scc1 .Lqk_poll3%=\ns_branch .Lqk_abort%=\n.Lqk_abort%=:\ns_mov_b64 exec, -1\nv_mov_b32 v16, 0\n" \
"s_movk_i32 s44, 6400\nbuffer_store_dword v16, off, s[36:39], s44 sc1\n.Lqk_end%=:\ns_waitcnt vmcnt(0) lgkmcnt(0)\n" \
""
#define VU_ASM \
"v_mbcnt_lo_u32_b32 v255, -1, 0\nv_mbcnt_hi_u32_b32 v255, -1, v255\nv_lshlrev_b32 v255, 2, v255\nv_add_u32 v255, %[tb], v255\nds_read_b32 v0, v255 offset:0\nds_read_b32 v1, v255 offset:256\nds_read_b32 v2, v255 offset:512\nds_read_b32 v3, v255 offset:768\n" \
"ds_read_b32 v4, v255 offset:1024\nds_read_b32 v5, v255 offset:1280\nds_read_b32 v6, v255 offset:1536\nds_read_b32 v7, v255 offset:1792\nds_read_b32 v8, v255 offset:2048\nds_read_b32 v9, v255 offset:2304\nds_read_b32 v10, v255 offset:2560\nds_read_b32 v11, v255 offset:2816\n" \
"ds_read_b32 v90, v255 offset:3072\nds_read_b32 v91, v255 offset:3328\nds_read_b32 v92, v255 offset:3584\nds_read_b32 v93, v255 offset:3840\nds_read_b32 v94, v255 offset:4096\nds_read_b32 v95, v255 offset:4352\ns_waitcnt lgkmcnt(0)\ns_mov_b64 s[36:37], %[ws]\n" \
"s_mov_b32 s38, %[sz]\ns_mov_b32 s39, 0x20000\ns_mov_b32 s45, 2096896\nbuffer_load_dword v15, off, s[36:39], s45 sc1\ns_add_u32 m0, %[lw], 16384\ns_add_u32 s44, %[wo], 0\nbuffer_load_dwordx4 v90, %[rw], s44 offen nt lds\ns_add_u32 m0, %[lw], 17408\n" \
"s_add_u32 s44, %[wo], 65536\nbuffer_load_dwordx4 v90, %[rw], s44 offen nt lds\ns_add_u32 m0, %[lw], 18432\ns_add_u32 s44, %[wo], 131072\nbuffer_load_dwordx4 v90, %[rw], s44 offen nt lds\ns_add_u32 m0, %[lw], 19456\ns_add_u32 s44, %[wo], 196608\nbuffer_load_dwordx4 v90, %[rw], s44 offen nt lds\n" \
"s_add_u32 m0, %[lw], 20480\ns_add_u32 s44, %[wo], 524288\nbuffer_load_dwordx4 v90, %[rw], s44 offen nt lds\ns_add_u32 m0, %[lw], 21504\ns_add_u32 s44, %[wo], 589824\nbuffer_load_dwordx4 v90, %[rw], s44 offen nt lds\ns_add_u32 m0, %[lw], 22528\ns_add_u32 s44, %[wo], 655360\n" \
"buffer_load_dwordx4 v90, %[rw], s44 offen nt lds\ns_add_u32 m0, %[lw], 23552\ns_add_u32 s44, %[wo], 720896\nbuffer_load_dwordx4 v90, %[rw], s44 offen nt lds\ns_add_u32 m0, %[tb], 0\ns_add_u32 s44, %[wo], 1048576\nbuffer_load_dwordx4 v90, %[rw], s44 offen nt lds\ns_add_u32 m0, %[tb], 1024\n" \
"s_add_u32 s44, %[wo], 1114112\nbuffer_load_dwordx4 v90, %[rw], s44 offen nt lds\ns_add_u32 m0, %[tb], 2048\ns_add_u32 s44, %[wo], 1179648\nbuffer_load_dwordx4 v90, %[rw], s44 offen nt lds\ns_add_u32 m0, %[tb], 3072\ns_add_u32 s44, %[wo], 1245184\nbuffer_load_dwordx4 v90, %[rw], s44 offen nt lds\n" \
"s_add_u32 m0, %[tb], 4096\ns_add_u32 s44, %[wo], 1572864\nbuffer_load_dwordx4 v90, %[rw], s44 offen nt lds\ns_add_u32 m0, %[tb], 5120\ns_add_u32 s44, %[wo], 1638400\nbuffer_load_dwordx4 v90, %[rw], s44 offen nt lds\ns_add_u32 m0, %[tb], 6144\ns_add_u32 s44, %[wo], 1703936\n" \
"buffer_load_dwordx4 v90, %[rw], s44 offen nt lds\ns_add_u32 m0, %[tb], 7168\ns_add_u32 s44, %[wo], 1769472\nbuffer_load_dwordx4 v90, %[rw], s44 offen nt lds\ns_add_u32 m0, %[tb], 8192\ns_add_u32 s44, %[wo], 2097152\nbuffer_load_dwordx4 v90, %[rw], s44 offen nt lds\ns_add_u32 m0, %[tb], 9216\n" \
"s_add_u32 s44, %[wo], 2162688\nbuffer_load_dwordx4 v90, %[rw], s44 offen nt lds\ns_add_u32 m0, %[tb], 10240\ns_add_u32 s44, %[wo], 2228224\nbuffer_load_dwordx4 v90, %[rw], s44 offen nt lds\ns_add_u32 m0, %[tb], 11264\ns_add_u32 s44, %[wo], 2293760\nbuffer_load_dwordx4 v90, %[rw], s44 offen nt lds\n" \
"s_add_u32 m0, %[lw], 0\ns_add_u32 s44, %[wo], 2621440\nbuffer_load_dwordx4 v90, %[rw], s44 offen nt lds\ns_add_u32 m0, %[lw], 1024\ns_add_u32 s44, %[wo], 2686976\nbuffer_load_dwordx4 v90, %[rw], s44 offen nt lds\ns_add_u32 m0, %[lw], 2048\ns_add_u32 s44, %[wo], 2752512\n" \
"buffer_load_dwordx4 v90, %[rw], s44 offen nt lds\ns_add_u32 m0, %[lw], 3072\ns_add_u32 s44, %[wo], 2818048\nbuffer_load_dwordx4 v90, %[rw], s44 offen nt lds\ns_add_u32 m0, %[lw], 4096\ns_add_u32 s44, %[wo], 3145728\nbuffer_load_dwordx4 v90, %[rw], s44 offen nt lds\ns_add_u32 m0, %[lw], 5120\n" \
"s_add_u32 s44, %[wo], 3211264\nbuffer_load_dwordx4 v90, %[rw], s44 offen nt lds\ns_add_u32 m0, %[lw], 6144\ns_add_u32 s44, %[wo], 3276800\nbuffer_load_dwordx4 v90, %[rw], s44 offen nt lds\ns_add_u32 m0, %[lw], 7168\ns_add_u32 s44, %[wo], 3342336\nbuffer_load_dwordx4 v90, %[rw], s44 offen nt lds\n" \
"s_add_u32 m0, %[lw], 8192\ns_add_u32 s44, %[wo], 3670016\nbuffer_load_dwordx4 v90, %[rw], s44 offen nt lds\ns_add_u32 m0, %[lw], 9216\ns_add_u32 s44, %[wo], 3735552\nbuffer_load_dwordx4 v90, %[rw], s44 offen nt lds\ns_add_u32 m0, %[lw], 10240\ns_add_u32 s44, %[wo], 3801088\n" \
"buffer_load_dwordx4 v90, %[rw], s44 offen nt lds\ns_add_u32 m0, %[lw], 11264\ns_add_u32 s44, %[wo], 3866624\nbuffer_load_dwordx4 v90, %[rw], s44 offen nt lds\ns_waitcnt vmcnt(28)\ns_barrier\ns_add_u32 m0, %[lw], 12288\ns_add_u32 s44, %[wo], 4194304\n" \
"buffer_load_dwordx4 v90, %[rw], s44 offen nt lds\ns_add_u32 m0, %[lw], 13312\ns_add_u32 s44, %[wo], 4259840\nbuffer_load_dwordx4 v90, %[rw], s44 offen nt lds\ns_add_u32 m0, %[lw], 14336\ns_add_u32 s44, %[wo], 4325376\nbuffer_load_dwordx4 v90, %[rw], s44 offen nt lds\ns_add_u32 m0, %[lw], 15360\n" \
"s_add_u32 s44, %[wo], 4390912\nbuffer_load_dwordx4 v90, %[rw], s44 offen nt lds\nds_read_b64_tr_b16 a[0:1], v93 offset:16384\nds_read_b64_tr_b16 a[2:3], v93 offset:16896\nds_read_b64_tr_b16 a[16:17], v93 offset:16392\nds_read_b64_tr_b16 a[18:19], v93 offset:16904\nds_read_b64_tr_b16 a[4:5], v93 offset:40960\nds_read_b64_tr_b16 a[6:7], v93 offset:41472\n" \
"ds_read_b64_tr_b16 a[20:21], v93 offset:40968\nds_read_b64_tr_b16 a[22:23], v93 offset:41480\ns_waitcnt lgkmcnt(0)\ns_waitcnt vmcnt(28)\ns_barrier\ns_add_u32 m0, %[lw], 16384\ns_add_u32 s44, %[wo], 4718592\nbuffer_load_dwordx4 v90, %[rw], s44 offen nt lds\n" \
"s_add_u32 m0, %[lw], 17408\ns_add_u32 s44, %[wo], 4784128\nbuffer_load_dwordx4 v90, %[rw], s44 offen nt lds\ns_add_u32 m0, %[lw], 18432\ns_add_u32 s44, %[wo], 4849664\nbuffer_load_dwordx4 v90, %[rw], s44 offen nt lds\ns_add_u32 m0, %[lw], 19456\ns_add_u32 s44, %[wo], 4915200\n" \
"buffer_load_dwordx4 v90, %[rw], s44 offen nt lds\nds_read_b64_tr_b16 a[24:25], v93 offset:20480\nds_read_b64_tr_b16 a[26:27], v93 offset:20992\nds_read_b64_tr_b16 a[40:41], v93 offset:20488\nds_read_b64_tr_b16 a[42:43], v93 offset:21000\nds_read_b64_tr_b16 a[28:29], v93 offset:45056\nds_read_b64_tr_b16 a[30:31], v93 offset:45568\nds_read_b64_tr_b16 a[44:45], v93 offset:45064\n" \
"ds_read_b64_tr_b16 a[46:47], v93 offset:45576\ns_waitcnt lgkmcnt(0)\ns_waitcnt vmcnt(28)\ns_barrier\ns_add_u32 m0, %[lw], 20480\ns_add_u32 s44, %[wo], 5242880\nbuffer_load_dwordx4 v90, %[rw], s44 offen nt lds\ns_add_u32 m0, %[lw], 21504\n" \
"s_add_u32 s44, %[wo], 5308416\nbuffer_load_dwordx4 v90, %[rw], s44 offen nt lds\ns_add_u32 m0, %[lw], 22528\ns_add_u32 s44, %[wo], 5373952\nbuffer_load_dwordx4 v90, %[rw], s44 offen nt lds\ns_add_u32 m0, %[lw], 23552\ns_add_u32 s44, %[wo], 5439488\nbuffer_load_dwordx4 v90, %[rw], s44 offen nt lds\n" \
"ds_read_b64_tr_b16 a[56:57], v92 offset:0\nds_read_b64_tr_b16 a[58:59], v92 offset:512\nds_read_b64_tr_b16 a[72:73], v92 offset:8\nds_read_b64_tr_b16 a[74:75], v92 offset:520\nds_read_b64_tr_b16 a[60:61], v92 offset:12288\nds_read_b64_tr_b16 a[62:63], v92 offset:12800\nds_read_b64_tr_b16 a[76:77], v92 offset:12296\nds_read_b64_tr_b16 a[78:79], v92 offset:12808\n" \
"s_waitcnt lgkmcnt(0)\ns_waitcnt vmcnt(28)\ns_barrier\ns_add_u32 m0, %[tb], 0\ns_add_u32 s44, %[wo], 5767168\nbuffer_load_dwordx4 v90, %[rw], s44 offen nt lds\ns_add_u32 m0, %[tb], 1024\ns_add_u32 s44, %[wo], 5832704\n" \
"buffer_load_dwordx4 v90, %[rw], s44 offen nt lds\ns_add_u32 m0, %[tb], 2048\ns_add_u32 s44, %[wo], 5898240\nbuffer_load_dwordx4 v90, %[rw], s44 offen nt lds\ns_add_u32 m0, %[tb], 3072\ns_add_u32 s44, %[wo], 5963776\nbuffer_load_dwordx4 v90, %[rw], s44 offen nt lds\nds_read_b64_tr_b16 v[84:85], v92 offset:4096\n" \
"ds_read_b64_tr_b16 v[86:87], v92 offset:4608\nds_read_b64_tr_b16 a[88:89], v92 offset:4104\nds_read_b64_tr_b16 a[90:91], v92 offset:4616\nds_read_b64_tr_b16 v[80:81], v92 offset:16384\nds_read_b64_tr_b16 v[82:83], v92 offset:16896\nds_read_b64_tr_b16 a[92:93], v92 offset:16392\nds_read_b64_tr_b16 a[94:95], v92 offset:16904\ns_waitcnt lgkmcnt(0)\n" \
"s_waitcnt vmcnt(28)\ns_barrier\ns_add_u32 m0, %[tb], 4096\ns_add_u32 s44, %[wo], 6291456\nbuffer_load_dwordx4 v90, %[rw], s44 offen nt lds\ns_add_u32 m0, %[tb], 5120\ns_add_u32 s44, %[wo], 6356992\nbuffer_load_dwordx4 v90, %[rw], s44 offen nt lds\n" \
"s_add_u32 m0, %[tb], 6144\ns_add_u32 s44, %[wo], 6422528\nbuffer_load_dwordx4 v90, %[rw], s44 offen nt lds\ns_add_u32 m0, %[tb], 7168\ns_add_u32 s44, %[wo], 6488064\nbuffer_load_dwordx4 v90, %[rw], s44 offen nt lds\nds_read_b64_tr_b16 a[104:105], v92 offset:8192\nds_read_b64_tr_b16 a[106:107], v92 offset:8704\n" \
"ds_read_b64_tr_b16 a[120:121], v92 offset:8200\nds_read_b64_tr_b16 a[122:123], v92 offset:8712\nds_read_b64_tr_b16 a[108:109], v92 offset:20480\nds_read_b64_tr_b16 a[110:111], v92 offset:20992\nds_read_b64_tr_b16 a[124:125], v92 offset:20488\nds_read_b64_tr_b16 a[126:127], v92 offset:21000\ns_waitcnt lgkmcnt(0)\ns_waitcnt vmcnt(28)\n" \
"s_barrier\ns_add_u32 m0, %[tb], 8192\ns_add_u32 s44, %[wo], 6815744\nbuffer_load_dwordx4 v90, %[rw], s44 offen nt lds\ns_add_u32 m0, %[tb], 9216\ns_add_u32 s44, %[wo], 6881280\nbuffer_load_dwordx4 v90, %[rw], s44 offen nt lds\ns_add_u32 m0, %[tb], 10240\n" \
"s_add_u32 s44, %[wo], 6946816\nbuffer_load_dwordx4 v90, %[rw], s44 offen nt lds\ns_add_u32 m0, %[tb], 11264\ns_add_u32 s44, %[wo], 7012352\nbuffer_load_dwordx4 v90, %[rw], s44 offen nt lds\nds_read_b64_tr_b16 a[128:129], v93 offset:0\nds_read_b64_tr_b16 a[130:131], v93 offset:512\nds_read_b64_tr_b16 a[144:145], v93 offset:8\n" \
"ds_read_b64_tr_b16 a[146:147], v93 offset:520\nds_read_b64_tr_b16 a[132:133], v93 offset:24576\nds_read_b64_tr_b16 a[134:135], v93 offset:25088\nds_read_b64_tr_b16 a[148:149], v93 offset:24584\nds_read_b64_tr_b16 a[150:151], v93 offset:25096\ns_waitcnt lgkmcnt(0)\ns_waitcnt vmcnt(28)\ns_barrier\n" \
"s_add_u32 m0, %[lw], 0\ns_add_u32 s44, %[wo], 7340032\nbuffer_load_dwordx4 v90, %[rw], s44 offen nt lds\ns_add_u32 m0, %[lw], 1024\ns_add_u32 s44, %[wo], 7405568\nbuffer_load_dwordx4 v90, %[rw], s44 offen nt lds\ns_add_u32 m0, %[lw], 2048\ns_add_u32 s44, %[wo], 7471104\n" \
"buffer_load_dwordx4 v90, %[rw], s44 offen nt lds\ns_add_u32 m0, %[lw], 3072\ns_add_u32 s44, %[wo], 7536640\nbuffer_load_dwordx4 v90, %[rw], s44 offen nt lds\nds_read_b64_tr_b16 a[160:161], v93 offset:4096\nds_read_b64_tr_b16 a[162:163], v93 offset:4608\nds_read_b64_tr_b16 v[76:77], v93 offset:4104\nds_read_b64_tr_b16 v[78:79], v93 offset:4616\n" \
"ds_read_b64_tr_b16 a[164:165], v93 offset:28672\nds_read_b64_tr_b16 a[166:167], v93 offset:29184\nds_read_b64_tr_b16 v[72:73], v93 offset:28680\nds_read_b64_tr_b16 v[74:75], v93 offset:29192\ns_waitcnt lgkmcnt(0)\ns_waitcnt vmcnt(28)\ns_barrier\ns_add_u32 m0, %[lw], 4096\n" \
"s_add_u32 s44, %[wo], 7864320\nbuffer_load_dwordx4 v90, %[rw], s44 offen nt lds\ns_add_u32 m0, %[lw], 5120\ns_add_u32 s44, %[wo], 7929856\nbuffer_load_dwordx4 v90, %[rw], s44 offen nt lds\ns_add_u32 m0, %[lw], 6144\ns_add_u32 s44, %[wo], 7995392\nbuffer_load_dwordx4 v90, %[rw], s44 offen nt lds\n" \
"s_add_u32 m0, %[lw], 7168\ns_add_u32 s44, %[wo], 8060928\nbuffer_load_dwordx4 v90, %[rw], s44 offen nt lds\nds_read_b64_tr_b16 a[184:185], v93 offset:8192\nds_read_b64_tr_b16 a[186:187], v93 offset:8704\nds_read_b64_tr_b16 a[192:193], v93 offset:8200\nds_read_b64_tr_b16 a[194:195], v93 offset:8712\nds_read_b64_tr_b16 a[188:189], v93 offset:32768\n" \
"ds_read_b64_tr_b16 a[190:191], v93 offset:33280\nds_read_b64_tr_b16 a[196:197], v93 offset:32776\nds_read_b64_tr_b16 a[198:199], v93 offset:33288\ns_waitcnt lgkmcnt(0)\ns_waitcnt vmcnt(28)\ns_barrier\ns_add_u32 m0, %[lw], 8192\ns_add_u32 s44, %[w2], 0\n" \
"buffer_load_dwordx4 v91, %[ro], s44 offen nt lds\ns_add_u32 m0, %[lw], 9216\ns_add_u32 s44, %[w2], 32768\nbuffer_load_dwordx4 v91, %[ro], s44 offen nt lds\ns_add_u32 m0, %[lw], 10240\ns_add_u32 s44, %[w2], 65536\nbuffer_load_dwordx4 v91, %[ro], s44 offen nt lds\ns_add_u32 m0, %[lw], 11264\n" \
"s_add_u32 s44, %[w2], 98304\nbuffer_load_dwordx4 v91, %[ro], s44 offen nt lds\nds_read_b64_tr_b16 a[208:209], v93 offset:12288\nds_read_b64_tr_b16 a[210:211], v93 offset:12800\nds_read_b64_tr_b16 a[224:225], v93 offset:12296\nds_read_b64_tr_b16 a[226:227], v93 offset:12808\nds_read_b64_tr_b16 a[212:213], v93 offset:36864\nds_read_b64_tr_b16 a[214:215], v93 offset:37376\n" \
"ds_read_b64_tr_b16 a[228:229], v93 offset:36872\nds_read_b64_tr_b16 a[230:231], v93 offset:37384\ns_waitcnt lgkmcnt(0)\ns_waitcnt vmcnt(28)\ns_barrier\ns_add_u32 m0, %[lw], 12288\ns_add_u32 s44, %[w2], 128\nbuffer_load_dwordx4 v91, %[ro], s44 offen nt lds\n" \
"s_add_u32 m0, %[lw], 13312\ns_add_u32 s44, %[w2], 32896\nbuffer_load_dwordx4 v91, %[ro], s44 offen nt lds\ns_add_u32 m0, %[lw], 14336\ns_add_u32 s44, %[w2], 65664\nbuffer_load_dwordx4 v91, %[ro], s44 offen nt lds\ns_add_u32 m0, %[lw], 15360\ns_add_u32 s44, %[w2], 98432\n" \
"buffer_load_dwordx4 v91, %[ro], s44 offen nt lds\nds_read_b64_tr_b16 v[68:69], v93 offset:16384\nds_read_b64_tr_b16 v[70:71], v93 offset:16896\nds_read_b64_tr_b16 a[240:241], v93 offset:16392\nds_read_b64_tr_b16 a[242:243], v93 offset:16904\nds_read_b64_tr_b16 v[64:65], v93 offset:40960\nds_read_b64_tr_b16 v[66:67], v93 offset:41472\nds_read_b64_tr_b16 a[244:245], v93 offset:40968\n" \
"ds_read_b64_tr_b16 a[246:247], v93 offset:41480\ns_waitcnt lgkmcnt(0)\ns_waitcnt vmcnt(28)\ns_barrier\ns_add_u32 m0, %[lw], 16384\ns_add_u32 s44, %[w2], 256\nbuffer_load_dwordx4 v91, %[ro], s44 offen nt lds\ns_add_u32 m0, %[lw], 17408\n" \
"s_add_u32 s44, %[w2], 33024\nbuffer_load_dwordx4 v91, %[ro], s44 offen nt lds\ns_add_u32 m0, %[lw], 18432\ns_add_u32 s44, %[w2], 65792\nbuffer_load_dwordx4 v91, %[ro], s44 offen nt lds\ns_add_u32 m0, %[lw], 19456\ns_add_u32 s44, %[w2], 98560\nbuffer_load_dwordx4 v91, %[ro], s44 offen nt lds\n" \
"ds_read_b64_tr_b16 v[96:97], v93 offset:20480\nds_read_b64_tr_b16 v[98:99], v93 offset:20992\nds_read_b64_tr_b16 v[112:113], v93 offset:20488\nds_read_b64_tr_b16 v[114:115], v93 offset:21000\nds_read_b64_tr_b16 v[100:101], v93 offset:45056\nds_read_b64_tr_b16 v[102:103], v93 offset:45568\nds_read_b64_tr_b16 v[116:117], v93 offset:45064\nds_read_b64_tr_b16 v[118:119], v93 offset:45576\n" \
"s_waitcnt lgkmcnt(0)\ns_waitcnt vmcnt(28)\ns_barrier\ns_add_u32 m0, %[lw], 20480\ns_add_u32 s44, %[w2], 384\nbuffer_load_dwordx4 v91, %[ro], s44 offen nt lds\ns_add_u32 m0, %[lw], 21504\ns_add_u32 s44, %[w2], 33152\n" \
"buffer_load_dwordx4 v91, %[ro], s44 offen nt lds\ns_add_u32 m0, %[lw], 22528\ns_add_u32 s44, %[w2], 65920\nbuffer_load_dwordx4 v91, %[ro], s44 offen nt lds\ns_add_u32 m0, %[lw], 23552\ns_add_u32 s44, %[w2], 98688\nbuffer_load_dwordx4 v91, %[ro], s44 offen nt lds\nds_read_b64_tr_b16 v[128:129], v92 offset:0\n" \
"ds_read_b64_tr_b16 v[130:131], v92 offset:512\nds_read_b64_tr_b16 v[136:137], v92 offset:8\nds_read_b64_tr_b16 v[138:139], v92 offset:520\nds_read_b64_tr_b16 v[132:133], v92 offset:12288\nds_read_b64_tr_b16 v[134:135], v92 offset:12800\nds_read_b64_tr_b16 v[140:141], v92 offset:12296\nds_read_b64_tr_b16 v[142:143], v92 offset:12808\ns_waitcnt lgkmcnt(0)\n" \
"s_waitcnt vmcnt(28)\ns_barrier\ns_add_u32 m0, %[tb], 0\ns_add_u32 s44, %[w2], 139264\nbuffer_load_dwordx4 v91, %[ro], s44 offen nt lds\ns_add_u32 m0, %[tb], 1024\ns_add_u32 s44, %[w2], 172032\nbuffer_load_dwordx4 v91, %[ro], s44 offen nt lds\n" \
"s_add_u32 m0, %[tb], 2048\ns_add_u32 s44, %[w2], 204800\nbuffer_load_dwordx4 v91, %[ro], s44 offen nt lds\ns_add_u32 m0, %[tb], 3072\ns_add_u32 s44, %[w2], 237568\nbuffer_load_dwordx4 v91, %[ro], s44 offen nt lds\nds_read_b64_tr_b16 v[152:153], v92 offset:4096\nds_read_b64_tr_b16 v[154:155], v92 offset:4608\n" \
"ds_read_b64_tr_b16 v[60:61], v92 offset:4104\nds_read_b64_tr_b16 v[62:63], v92 offset:4616\nds_read_b64_tr_b16 v[156:157], v92 offset:16384\nds_read_b64_tr_b16 v[158:159], v92 offset:16896\nds_read_b64_tr_b16 v[56:57], v92 offset:16392\nds_read_b64_tr_b16 v[58:59], v92 offset:16904\ns_waitcnt lgkmcnt(0)\ns_waitcnt vmcnt(28)\n" \
"s_barrier\ns_add_u32 m0, %[tb], 4096\ns_add_u32 s44, %[w2], 139392\nbuffer_load_dwordx4 v91, %[ro], s44 offen nt lds\ns_add_u32 m0, %[tb], 5120\ns_add_u32 s44, %[w2], 172160\nbuffer_load_dwordx4 v91, %[ro], s44 offen nt lds\ns_add_u32 m0, %[tb], 6144\n" \
"s_add_u32 s44, %[w2], 204928\nbuffer_load_dwordx4 v91, %[ro], s44 offen nt lds\ns_add_u32 m0, %[tb], 7168\ns_add_u32 s44, %[w2], 237696\nbuffer_load_dwordx4 v91, %[ro], s44 offen nt lds\nds_read_b64_tr_b16 v[168:169], v92 offset:8192\nds_read_b64_tr_b16 v[170:171], v92 offset:8704\nds_read_b64_tr_b16 v[184:185], v92 offset:8200\n" \
"ds_read_b64_tr_b16 v[186:187], v92 offset:8712\nds_read_b64_tr_b16 v[172:173], v92 offset:20480\nds_read_b64_tr_b16 v[174:175], v92 offset:20992\nds_read_b64_tr_b16 v[188:189], v92 offset:20488\nds_read_b64_tr_b16 v[190:191], v92 offset:21000\ns_waitcnt lgkmcnt(0)\ns_waitcnt vmcnt(28)\ns_barrier\n" \
"s_add_u32 m0, %[tb], 8192\ns_add_u32 s44, %[w2], 139520\nbuffer_load_dwordx4 v91, %[ro], s44 offen nt lds\ns_add_u32 m0, %[tb], 9216\ns_add_u32 s44, %[w2], 172288\nbuffer_load_dwordx4 v91, %[ro], s44 offen nt lds\ns_add_u32 m0, %[tb], 10240\ns_add_u32 s44, %[w2], 205056\n" \
"buffer_load_dwordx4 v91, %[ro], s44 offen nt lds\ns_add_u32 m0, %[tb], 11264\ns_add_u32 s44, %[w2], 237824\nbuffer_load_dwordx4 v91, %[ro], s44 offen nt lds\nds_read_b64_tr_b16 v[200:201], v93 offset:0\nds_read_b64_tr_b16 v[202:203], v93 offset:512\nds_read_b64_tr_b16 v[216:217], v93 offset:8\nds_read_b64_tr_b16 v[218:219], v93 offset:520\n" \
"ds_read_b64_tr_b16 v[204:205], v93 offset:24576\nds_read_b64_tr_b16 v[206:207], v93 offset:25088\nds_read_b64_tr_b16 v[220:221], v93 offset:24584\nds_read_b64_tr_b16 v[222:223], v93 offset:25096\ns_waitcnt lgkmcnt(0)\ns_waitcnt vmcnt(28)\ns_barrier\ns_add_u32 m0, %[lw], 0\n" \
"s_add_u32 s44, %[w2], 139648\nbuffer_load_dwordx4 v91, %[ro], s44 offen nt lds\ns_add_u32 m0, %[lw], 1024\ns_add_u32 s44, %[w2], 172416\nbuffer_load_dwordx4 v91, %[ro], s44 offen nt lds\ns_add_u32 m0, %[lw], 2048\ns_add_u32 s44, %[w2], 205184\nbuffer_load_dwordx4 v91, %[ro], s44 offen nt lds\n" \
"s_add_u32 m0, %[lw], 3072\ns_add_u32 s44, %[w2], 237952\nbuffer_load_dwordx4 v91, %[ro], s44 offen nt lds\nds_read_b64_tr_b16 v[232:233], v93 offset:4096\nds_read_b64_tr_b16 v[234:235], v93 offset:4608\nds_read_b64_tr_b16 v[240:241], v93 offset:4104\nds_read_b64_tr_b16 v[242:243], v93 offset:4616\nds_read_b64_tr_b16 v[236:237], v93 offset:28672\n" \
"ds_read_b64_tr_b16 v[238:239], v93 offset:29184\nds_read_b64_tr_b16 v[244:245], v93 offset:28680\nds_read_b64_tr_b16 v[246:247], v93 offset:29192\ns_waitcnt lgkmcnt(0)\ns_waitcnt vmcnt(28)\ns_barrier\ns_add_u32 m0, %[lw], 4096\ns_add_u32 s44, %[w2], 131072\n" \
"buffer_load_dwordx4 v91, %[ro], s44 offen nt lds\ns_add_u32 m0, %[lw], 5120\ns_add_u32 s44, %[w2], 163840\nbuffer_load_dwordx4 v91, %[ro], s44 offen nt lds\ns_add_u32 m0, %[lw], 6144\ns_add_u32 s44, %[w2], 196608\nbuffer_load_dwordx4 v91, %[ro], s44 offen nt lds\ns_add_u32 m0, %[lw], 7168\n" \
"s_add_u32 s44, %[w2], 229376\nbuffer_load_dwordx4 v91, %[ro], s44 offen nt lds\nds_read_b64_tr_b16 a[8:9], v95 offset:8192\nds_read_b64_tr_b16 a[10:11], v95 offset:8448\nds_read_b64_tr_b16 a[12:13], v95 offset:10240\nds_read_b64_tr_b16 a[14:15], v95 offset:10496\nds_read_b64_tr_b16 a[216:217], v95 offset:8256\nds_read_b64_tr_b16 a[218:219], v95 offset:8512\n" \
"ds_read_b64_tr_b16 a[220:221], v95 offset:10304\nds_read_b64_tr_b16 a[222:223], v95 offset:10560\ns_waitcnt lgkmcnt(0)\ns_waitcnt vmcnt(28)\ns_barrier\ns_add_u32 m0, %[lw], 8192\ns_add_u32 s44, %[w2], 131200\nbuffer_load_dwordx4 v91, %[ro], s44 offen nt lds\n" \
"s_add_u32 m0, %[lw], 9216\ns_add_u32 s44, %[w2], 163968\nbuffer_load_dwordx4 v91, %[ro], s44 offen nt lds\ns_add_u32 m0, %[lw], 10240\ns_add_u32 s44, %[w2], 196736\nbuffer_load_dwordx4 v91, %[ro], s44 offen nt lds\ns_add_u32 m0, %[lw], 11264\ns_add_u32 s44, %[w2], 229504\n" \
"buffer_load_dwordx4 v91, %[ro], s44 offen nt lds\nds_read_b64_tr_b16 a[64:65], v95 offset:12288\nds_read_b64_tr_b16 a[66:67], v95 offset:12544\nds_read_b64_tr_b16 a[68:69], v95 offset:14336\nds_read_b64_tr_b16 a[70:71], v95 offset:14592\nds_read_b64_tr_b16 v[104:105], v95 offset:12352\nds_read_b64_tr_b16 v[106:107], v95 offset:12608\nds_read_b64_tr_b16 v[108:109], v95 offset:14400\n" \
"ds_read_b64_tr_b16 v[110:111], v95 offset:14656\ns_waitcnt lgkmcnt(0)\ns_waitcnt vmcnt(28)\ns_barrier\ns_add_u32 m0, %[lw], 12288\ns_add_u32 s44, %[w2], 131328\nbuffer_load_dwordx4 v91, %[ro], s44 offen nt lds\ns_add_u32 m0, %[lw], 13312\n" \
"s_add_u32 s44, %[w2], 164096\nbuffer_load_dwordx4 v91, %[ro], s44 offen nt lds\ns_add_u32 m0, %[lw], 14336\ns_add_u32 s44, %[w2], 196864\nbuffer_load_dwordx4 v91, %[ro], s44 offen nt lds\ns_add_u32 m0, %[lw], 15360\ns_add_u32 s44, %[w2], 229632\nbuffer_load_dwordx4 v91, %[ro], s44 offen nt lds\n" \
"ds_read_b64_tr_b16 a[112:113], v95 offset:16384\nds_read_b64_tr_b16 a[114:115], v95 offset:16640\nds_read_b64_tr_b16 a[116:117], v95 offset:18432\nds_read_b64_tr_b16 a[118:119], v95 offset:18688\nds_read_b64_tr_b16 v[160:161], v95 offset:16448\nds_read_b64_tr_b16 v[162:163], v95 offset:16704\nds_read_b64_tr_b16 v[164:165], v95 offset:18496\nds_read_b64_tr_b16 v[166:167], v95 offset:18752\n" \
"s_waitcnt lgkmcnt(0)\ns_waitcnt vmcnt(28)\ns_barrier\ns_add_u32 m0, %[lw], 16384\ns_add_u32 s44, %[w2], 131456\nbuffer_load_dwordx4 v91, %[ro], s44 offen nt lds\ns_add_u32 m0, %[lw], 17408\ns_add_u32 s44, %[w2], 164224\n" \
"buffer_load_dwordx4 v91, %[ro], s44 offen nt lds\ns_add_u32 m0, %[lw], 18432\ns_add_u32 s44, %[w2], 196992\nbuffer_load_dwordx4 v91, %[ro], s44 offen nt lds\ns_add_u32 m0, %[lw], 19456\ns_add_u32 s44, %[w2], 229760\nbuffer_load_dwordx4 v91, %[ro], s44 offen nt lds\nds_read_b64_tr_b16 a[168:169], v95 offset:20480\n" \
"ds_read_b64_tr_b16 a[170:171], v95 offset:20736\nds_read_b64_tr_b16 a[172:173], v95 offset:22528\nds_read_b64_tr_b16 a[174:175], v95 offset:22784\nds_read_b64_tr_b16 v[208:209], v95 offset:20544\nds_read_b64_tr_b16 v[210:211], v95 offset:20800\nds_read_b64_tr_b16 v[212:213], v95 offset:22592\nds_read_b64_tr_b16 v[214:215], v95 offset:22848\ns_waitcnt lgkmcnt(0)\n" \
"s_waitcnt vmcnt(28)\ns_barrier\ns_add_u32 m0, %[lw], 20480\ns_add_u32 s44, %[w2], 8192\nbuffer_load_dwordx4 v91, %[ro], s44 offen nt lds\ns_add_u32 m0, %[lw], 21504\ns_add_u32 s44, %[w2], 40960\nbuffer_load_dwordx4 v91, %[ro], s44 offen nt lds\n" \
"s_add_u32 m0, %[lw], 22528\ns_add_u32 s44, %[w2], 73728\nbuffer_load_dwordx4 v91, %[ro], s44 offen nt lds\ns_add_u32 m0, %[lw], 23552\ns_add_u32 s44, %[w2], 106496\nbuffer_load_dwordx4 v91, %[ro], s44 offen nt lds\nds_read_b64_tr_b16 a[48:49], v94 offset:0\nds_read_b64_tr_b16 a[50:51], v94 offset:256\n" \
"ds_read_b64_tr_b16 a[52:53], v94 offset:2048\nds_read_b64_tr_b16 a[54:55], v94 offset:2304\nds_read_b64_tr_b16 a[248:249], v94 offset:64\nds_read_b64_tr_b16 a[250:251], v94 offset:320\nds_read_b64_tr_b16 a[252:253], v94 offset:2112\nds_read_b64_tr_b16 a[254:255], v94 offset:2368\ns_waitcnt lgkmcnt(0)\ns_waitcnt vmcnt(28)\n" \
"s_barrier\ns_add_u32 m0, %[tb], 0\ns_add_u32 s44, %[w2], 8320\nbuffer_load_dwordx4 v91, %[ro], s44 offen nt lds\ns_add_u32 m0, %[tb], 1024\ns_add_u32 s44, %[w2], 41088\nbuffer_load_dwordx4 v91, %[ro], s44 offen nt lds\ns_add_u32 m0, %[tb], 2048\n" \
"s_add_u32 s44, %[w2], 73856\nbuffer_load_dwordx4 v91, %[ro], s44 offen nt lds\ns_add_u32 m0, %[tb], 3072\ns_add_u32 s44, %[w2], 106624\nbuffer_load_dwordx4 v91, %[ro], s44 offen nt lds\nds_read_b64_tr_b16 a[96:97], v94 offset:4096\nds_read_b64_tr_b16 a[98:99], v94 offset:4352\nds_read_b64_tr_b16 a[100:101], v94 offset:6144\n" \
"ds_read_b64_tr_b16 a[102:103], v94 offset:6400\nds_read_b64_tr_b16 v[144:145], v94 offset:4160\nds_read_b64_tr_b16 v[146:147], v94 offset:4416\nds_read_b64_tr_b16 v[148:149], v94 offset:6208\nds_read_b64_tr_b16 v[150:151], v94 offset:6464\ns_waitcnt lgkmcnt(0)\nbuffer_load_dwordx2 v[12:13], off, s[36:39], s45 sc1\ns_waitcnt vmcnt(29)\n" \
"s_barrier\ns_add_u32 m0, %[tb], 4096\ns_add_u32 s44, %[w2], 8448\nbuffer_load_dwordx4 v91, %[ro], s44 offen nt lds\ns_add_u32 m0, %[tb], 5120\ns_add_u32 s44, %[w2], 41216\nbuffer_load_dwordx4 v91, %[ro], s44 offen nt lds\ns_add_u32 m0, %[tb], 6144\n" \
"s_add_u32 s44, %[w2], 73984\nbuffer_load_dwordx4 v91, %[ro], s44 offen nt lds\ns_add_u32 m0, %[tb], 7168\ns_add_u32 s44, %[w2], 106752\nbuffer_load_dwordx4 v91, %[ro], s44 offen nt lds\nds_read_b64_tr_b16 a[152:153], v94 offset:8192\nds_read_b64_tr_b16 a[154:155], v94 offset:8448\nds_read_b64_tr_b16 a[156:157], v94 offset:10240\n" \
"ds_read_b64_tr_b16 a[158:159], v94 offset:10496\nds_read_b64_tr_b16 v[192:193], v94 offset:8256\nds_read_b64_tr_b16 v[194:195], v94 offset:8512\nds_read_b64_tr_b16 v[196:197], v94 offset:10304\nds_read_b64_tr_b16 v[198:199], v94 offset:10560\ns_waitcnt lgkmcnt(0)\ns_waitcnt vmcnt(29)\ns_barrier\n" \
"s_add_u32 m0, %[tb], 8192\ns_add_u32 s44, %[w2], 8576\nbuffer_load_dwordx4 v91, %[ro], s44 offen nt lds\ns_add_u32 m0, %[tb], 9216\ns_add_u32 s44, %[w2], 41344\nbuffer_load_dwordx4 v91, %[ro], s44 offen nt lds\ns_add_u32 m0, %[tb], 10240\ns_add_u32 s44, %[w2], 74112\n" \
"buffer_load_dwordx4 v91, %[ro], s44 offen nt lds\ns_add_u32 m0, %[tb], 11264\ns_add_u32 s44, %[w2], 106880\nbuffer_load_dwordx4 v91, %[ro], s44 offen nt lds\nds_read_b64_tr_b16 a[200:201], v95 offset:0\nds_read_b64_tr_b16 a[202:203], v95 offset:256\nds_read_b64_tr_b16 a[204:205], v95 offset:2048\nds_read_b64_tr_b16 a[206:207], v95 offset:2304\n" \
"ds_read_b64_tr_b16 v[248:249], v95 offset:64\nds_read_b64_tr_b16 v[250:251], v95 offset:320\nds_read_b64_tr_b16 v[252:253], v95 offset:2112\nds_read_b64_tr_b16 v[254:255], v95 offset:2368\ns_waitcnt lgkmcnt(0)\ns_waitcnt vmcnt(29)\ns_barrier\nds_read_b64_tr_b16 v[52:53], v95 offset:4096\n" \
"ds_read_b64_tr_b16 v[54:55], v95 offset:4352\nds_read_b64_tr_b16 v[48:49], v95 offset:6144\nds_read_b64_tr_b16 v[50:51], v95 offset:6400\nds_read_b64_tr_b16 v[44:45], v95 offset:4160\nds_read_b64_tr_b16 v[46:47], v95 offset:4416\nds_read_b64_tr_b16 v[40:41], v95 offset:6208\nds_read_b64_tr_b16 v[42:43], v95 offset:6464\ns_waitcnt lgkmcnt(0)\n" \
"ds_write_b128 v1, v[84:87] offset:0\nds_write_b128 v1, v[80:83] offset:1024\nds_write_b128 v1, v[76:79] offset:2048\nds_write_b128 v1, v[72:75] offset:3072\ns_waitcnt vmcnt(25)\ns_barrier\nds_read_b64_tr_b16 a[80:81], v95 offset:8192\nds_read_b64_tr_b16 a[82:83], v95 offset:8448\n" \
"ds_read_b64_tr_b16 a[84:85], v95 offset:10240\nds_read_b64_tr_b16 a[86:87], v95 offset:10496\nds_read_b64_tr_b16 v[120:121], v95 offset:8256\nds_read_b64_tr_b16 v[122:123], v95 offset:8512\nds_read_b64_tr_b16 v[124:125], v95 offset:10304\nds_read_b64_tr_b16 v[126:127], v95 offset:10560\ns_waitcnt lgkmcnt(0)\nds_write_b128 v1, v[68:71] offset:4096\n" \
"ds_write_b128 v1, v[64:67] offset:5120\nds_write_b128 v1, v[60:63] offset:6144\nds_write_b128 v1, v[56:59] offset:7168\ns_waitcnt vmcnt(21)\ns_barrier\nds_read_b64_tr_b16 v[56:57], v95 offset:12288\nds_read_b64_tr_b16 v[58:59], v95 offset:12544\nds_read_b64_tr_b16 v[60:61], v95 offset:14336\n" \
"ds_read_b64_tr_b16 v[62:63], v95 offset:14592\nds_read_b64_tr_b16 v[64:65], v95 offset:12352\nds_read_b64_tr_b16 v[66:67], v95 offset:12608\nds_read_b64_tr_b16 v[68:69], v95 offset:14400\nds_read_b64_tr_b16 v[70:71], v95 offset:14656\ns_waitcnt lgkmcnt(0)\nds_write_b128 v1, v[52:55] offset:8192\nds_write_b128 v1, v[48:51] offset:9216\n" \
"ds_write_b128 v1, v[44:47] offset:10240\nds_write_b128 v1, v[40:43] offset:11264\ns_waitcnt vmcnt(17)\ns_barrier\nds_read_b64_tr_b16 a[176:177], v95 offset:16384\nds_read_b64_tr_b16 a[178:179], v95 offset:16640\nds_read_b64_tr_b16 a[180:181], v95 offset:18432\nds_read_b64_tr_b16 a[182:183], v95 offset:18688\n" \
"ds_read_b64_tr_b16 v[224:225], v95 offset:16448\nds_read_b64_tr_b16 v[226:227], v95 offset:16704\nds_read_b64_tr_b16 v[228:229], v95 offset:18496\nds_read_b64_tr_b16 v[230:231], v95 offset:18752\ns_waitcnt lgkmcnt(0)\nds_write_b128 v1, v[56:59] offset:12288\nds_write_b128 v1, v[60:63] offset:13312\nds_write_b128 v1, v[64:67] offset:14336\n" \
"ds_write_b128 v1, v[68:71] offset:15360\ns_waitcnt vmcnt(13)\ns_barrier\nds_read_b64_tr_b16 a[32:33], v95 offset:20480\nds_read_b64_tr_b16 a[34:35], v95 offset:20736\nds_read_b64_tr_b16 a[36:37], v95 offset:22528\nds_read_b64_tr_b16 a[38:39], v95 offset:22784\nds_read_b64_tr_b16 a[232:233], v95 offset:20544\n" \
"ds_read_b64_tr_b16 a[234:235], v95 offset:20800\nds_read_b64_tr_b16 a[236:237], v95 offset:22592\nds_read_b64_tr_b16 a[238:239], v95 offset:22848\ns_waitcnt lgkmcnt(0)\ns_waitcnt vmcnt(9)\ns_barrier\nds_read_b64_tr_b16 v[68:69], v94 offset:0\nds_read_b64_tr_b16 v[70:71], v94 offset:256\n" \
"ds_read_b64_tr_b16 v[64:65], v94 offset:2048\nds_read_b64_tr_b16 v[66:67], v94 offset:2304\nds_read_b64_tr_b16 v[60:61], v94 offset:64\nds_read_b64_tr_b16 v[62:63], v94 offset:320\nds_read_b64_tr_b16 v[56:57], v94 offset:2112\nds_read_b64_tr_b16 v[58:59], v94 offset:2368\ns_waitcnt lgkmcnt(0)\nds_write_b128 v1, v[68:71] offset:16384\n" \
"ds_write_b128 v1, v[64:67] offset:17408\nds_write_b128 v1, v[60:63] offset:18432\nds_write_b128 v1, v[56:59] offset:19456\ns_waitcnt vmcnt(4)\ns_barrier\nds_read_b64_tr_b16 a[136:137], v94 offset:4096\nds_read_b64_tr_b16 a[138:139], v94 offset:4352\nds_read_b64_tr_b16 a[140:141], v94 offset:6144\n" \
"ds_read_b64_tr_b16 a[142:143], v94 offset:6400\nds_read_b64_tr_b16 v[176:177], v94 offset:4160\nds_read_b64_tr_b16 v[178:179], v94 offset:4416\nds_read_b64_tr_b16 v[180:181], v94 offset:6208\nds_read_b64_tr_b16 v[182:183], v94 offset:6464\ns_waitcnt lgkmcnt(0)\ns_waitcnt vmcnt(0)\ns_barrier\n" \
"ds_read_b64_tr_b16 v[56:57], v94 offset:8192\nds_read_b64_tr_b16 v[58:59], v94 offset:8448\nds_read_b64_tr_b16 v[60:61], v94 offset:10240\nds_read_b64_tr_b16 v[62:63], v94 offset:10496\nds_read_b64_tr_b16 v[64:65], v94 offset:8256\nds_read_b64_tr_b16 v[66:67], v94 offset:8512\nds_read_b64_tr_b16 v[68:69], v94 offset:10304\nds_read_b64_tr_b16 v[70:71], v94 offset:10560\n" \
"s_waitcnt lgkmcnt(0)\ns_mov_b32 s43, 0\ns_mov_b32 s86, 110\ns_mov_b32 s89, 0\n.Lvu_hb%=:\nv_cmp_ne_u32 vcc, v15, v12\ns_cbranch_vccnz .Lvu_hb1%=\ns_sleep 4\n" \
"buffer_load_dwordx2 v[12:13], off, s[36:39], s45 sc1\ns_waitcnt vmcnt(0)\ns_add_u32 s43, s43, 1\ns_cmp_lt_u32 s43, 262144\ns_cbranch_scc1 .Lvu_hb%=\ns_branch .Lvu_abort%=\n.Lvu_hb1%=:\nv_readfirstlane_b32 s44, v13\n" \
"s_and_b32 s44, s44, 0x200000\ns_add_u32 s36, s36, s44\ns_addc_u32 s37, s37, 0\nds_write_b128 v1, v[56:59] offset:20480\nds_write_b128 v1, v[60:63] offset:21504\nds_write_b128 v1, v[64:67] offset:22528\nds_write_b128 v1, v[68:71] offset:23552\ns_waitcnt lgkmcnt(0)\n" \
"s_barrier\ns_mov_b32 s40, -1\ns_mov_b32 s43, 0\ns_lshl_b32 s57, %[w], 12\ns_mov_b32 s56, 0\ns_mov_b32 s42, 0\ns_mov_b32 s60, -2147483648\ns_mov_b32 s54, -2147483648\n" \
"s_add_u32 s44, s40, 1\ns_lshl_b32 s44, s44, 17\ns_add_u32 s44, s44, %[xn]\ns_lshr_b32 s45, s44, 8\ns_and_b32 s45, s45, 0x2000\ns_xor_b32 s41, s44, s45\ns_add_u32 s41, s41, %[xw]\n.Lvu_f0%=:\n" \
"buffer_load_dword v14, v8, s[36:39], s56 offen sc1\ns_waitcnt vmcnt(0)\nv_cmp_ge_i32 vcc, s40, v14\ns_cbranch_vccz .Lvu_f0fok%=\ns_add_u32 s43, s43, 1\ns_cmp_lt_u32 s43, 262144\ns_cbranch_scc1 .Lvu_f0%=\ns_branch .Lvu_abort%=\n" \
".Lvu_f0fok%=:\ns_add_u32 m0, s57, 0\ns_add_u32 s58, s41, 0\nbuffer_load_dwordx4 v0, s[36:39], s58 offen offset:0 sc1 lds\nbuffer_load_dwordx4 v0, s[36:39], s58 offen offset:1024 sc1 lds\nbuffer_load_dwordx4 v0, s[36:39], s58 offen offset:2048 sc1 lds\nbuffer_load_dwordx4 v0, s[36:39], s58 offen offset:3072 sc1 lds\ns_add_u32 m0, s57, 32768\n" \
"s_add_u32 s58, s41, 32768\nbuffer_load_dwordx4 v0, s[36:39], s58 offen offset:0 sc1 lds\nbuffer_load_dwordx4 v0, s[36:39], s58 offen offset:1024 sc1 lds\nbuffer_load_dwordx4 v0, s[36:39], s58 offen offset:2048 sc1 lds\nbuffer_load_dwordx4 v0, s[36:39], s58 offen offset:3072 sc1 lds\ns_waitcnt vmcnt(0)\ns_barrier\nds_read_b128 v[32:35], v2 offset:0\n" \
"ds_read_b128 v[36:39], v2 offset:256\nds_read_b128 v[40:43], v2 offset:2048\nds_read_b128 v[44:47], v2 offset:2304\n.Lvu_tok%=:\nds_read_b128 v[64:67], v2 offset:4096\nds_read_b128 v[68:71], v2 offset:4352\ns_waitcnt lgkmcnt(5)\nv_mfma_f32_16x16x32_bf16 v[16:19], a[0:3], v[32:35], 0\n" \
"v_cvt_pk_bf16_f32 v92, v80, v81\nv_cvt_pk_bf16_f32 v93, v82, v83\nv_mfma_f32_16x16x32_bf16 v[20:23], a[16:19], v[32:35], 0\ns_waitcnt lgkmcnt(4)\nds_read_b128 v[32:35], v2 offset:6144\nv_cvt_pk_bf16_f32 v94, v84, v85\nv_cvt_pk_bf16_f32 v95, v86, v87\nv_mfma_f32_16x16x32_bf16 v[24:27], a[0:3], v[36:39], 0\n" \
"v_and_or_b32 v92, v92, -2, v13\nv_and_or_b32 v94, v94, -2, v13\nv_mfma_f32_16x16x32_bf16 v[28:31], a[16:19], v[36:39], 0\ns_waitcnt lgkmcnt(4)\nbuffer_store_dwordx4 v[92:95], v7, s[36:39], s60 offen offset:2560 sc1\nds_read_b128 v[36:39], v2 offset:6400\nv_mfma_f32_16x16x32_bf16 v[16:19], a[4:7], v[40:43], v[16:19]\nv_mfma_f32_16x16x32_bf16 v[20:23], a[20:23], v[40:43], v[20:23]\n" \
"s_waitcnt lgkmcnt(4)\nds_read_b128 v[40:43], v2 offset:8192\nv_mfma_f32_16x16x32_bf16 v[24:27], a[4:7], v[44:47], v[24:27]\ns_add_u32 m0, s57, 16384\ns_add_u32 s60, s41, 65536\nbuffer_load_dwordx4 v0, s[36:39], s60 offen offset:0 sc1 lds\nv_mfma_f32_16x16x32_bf16 v[28:31], a[20:23], v[44:47], v[28:31]\ns_waitcnt lgkmcnt(4)\n" \
"ds_read_b128 v[44:47], v2 offset:8448\nv_mfma_f32_16x16x32_bf16 v[16:19], a[24:27], v[64:67], v[16:19]\nds_read_b128 v[56:59], v1 offset:0\nds_read_b128 v[60:63], v1 offset:1024\nv_mfma_f32_16x16x32_bf16 v[20:23], a[40:43], v[64:67], v[20:23]\ns_waitcnt lgkmcnt(6)\nds_read_b128 v[64:67], v2 offset:10240\nv_mfma_f32_16x16x32_bf16 v[24:27], a[24:27], v[68:71], v[24:27]\n" \
"buffer_load_dwordx4 v0, s[36:39], s60 offen offset:1024 sc1 lds\nv_mfma_f32_16x16x32_bf16 v[28:31], a[40:43], v[68:71], v[28:31]\ns_waitcnt lgkmcnt(6)\nds_read_b128 v[68:71], v2 offset:10496\nv_mfma_f32_16x16x32_bf16 v[16:19], a[28:31], v[32:35], v[16:19]\nv_mfma_f32_16x16x32_bf16 v[20:23], a[44:47], v[32:35], v[20:23]\ns_waitcnt lgkmcnt(6)\nds_read_b128 v[32:35], v2 offset:12288\n" \
"v_mfma_f32_16x16x32_bf16 v[24:27], a[28:31], v[36:39], v[24:27]\nbuffer_load_dwordx4 v0, s[36:39], s60 offen offset:2048 sc1 lds\nv_mfma_f32_16x16x32_bf16 v[28:31], a[44:47], v[36:39], v[28:31]\ns_waitcnt lgkmcnt(6)\nds_read_b128 v[36:39], v2 offset:12544\nv_mfma_f32_16x16x32_bf16 v[16:19], a[56:59], v[40:43], v[16:19]\nv_mfma_f32_16x16x32_bf16 v[20:23], a[72:75], v[40:43], v[20:23]\ns_waitcnt lgkmcnt(6)\n" \
"ds_read_b128 v[40:43], v2 offset:14336\nv_mfma_f32_16x16x32_bf16 v[24:27], a[56:59], v[44:47], v[24:27]\nbuffer_load_dwordx4 v0, s[36:39], s60 offen offset:3072 sc1 lds\nv_mfma_f32_16x16x32_bf16 v[28:31], a[72:75], v[44:47], v[28:31]\ns_waitcnt lgkmcnt(4)\nds_read_b128 v[44:47], v2 offset:14592\nv_mfma_f32_16x16x32_bf16 v[16:19], a[60:63], v[64:67], v[16:19]\ns_add_u32 s56, s56, %[fs]\n" \
"s_add_u32 s62, s40, 1\nv_mfma_f32_16x16x32_bf16 v[20:23], a[76:79], v[64:67], v[20:23]\ns_lshr_b32 m0, s57, 4\ns_add_u32 m0, m0, 159744\ns_waitcnt lgkmcnt(4)\nds_read_b128 v[64:67], v2 offset:32768\nv_mfma_f32_16x16x32_bf16 v[24:27], a[60:63], v[68:71], v[24:27]\nbuffer_load_dword v8, s[36:39], s56 offen sc1 lds\n" \
"v_lshrrev_b32 v14, 2, v0\nv_mfma_f32_16x16x32_bf16 v[28:31], a[76:79], v[68:71], v[28:31]\ns_waitcnt lgkmcnt(4)\nds_read_b128 v[68:71], v2 offset:33024\nv_mfma_f32_16x16x32_bf16 v[16:19], v[56:59], v[32:35], v[16:19]\nv_add_u32 v14, 159744, v14\nv_mfma_f32_16x16x32_bf16 v[20:23], a[88:91], v[32:35], v[20:23]\ns_waitcnt lgkmcnt(4)\n" \
"ds_read_b128 v[32:35], v2 offset:34816\nv_mfma_f32_16x16x32_bf16 v[24:27], v[56:59], v[36:39], v[24:27]\nv_mfma_f32_16x16x32_bf16 v[28:31], a[88:91], v[36:39], v[28:31]\ns_waitcnt lgkmcnt(4)\nds_read_b128 v[36:39], v2 offset:35072\nv_mfma_f32_16x16x32_bf16 v[16:19], v[60:63], v[40:43], v[16:19]\nv_mfma_f32_16x16x32_bf16 v[20:23], a[92:95], v[40:43], v[20:23]\ns_waitcnt lgkmcnt(4)\n" \
"ds_read_b128 v[40:43], v2 offset:36864\nv_mfma_f32_16x16x32_bf16 v[24:27], v[60:63], v[44:47], v[24:27]\nv_mfma_f32_16x16x32_bf16 v[28:31], a[92:95], v[44:47], v[28:31]\ns_waitcnt lgkmcnt(4)\nds_read_b128 v[44:47], v2 offset:37120\nv_mfma_f32_16x16x32_bf16 v[16:19], a[104:107], v[64:67], v[16:19]\nds_read_b128 v[56:59], v1 offset:2048\nds_read_b128 v[60:63], v1 offset:3072\n" \
"v_mfma_f32_16x16x32_bf16 v[20:23], a[120:123], v[64:67], v[20:23]\ns_waitcnt lgkmcnt(6)\nds_read_b128 v[64:67], v2 offset:38912\nv_mfma_f32_16x16x32_bf16 v[24:27], a[104:107], v[68:71], v[24:27]\ns_barrier\nv_mfma_f32_16x16x32_bf16 v[28:31], a[120:123], v[68:71], v[28:31]\ns_waitcnt lgkmcnt(6)\nds_read_b128 v[68:71], v2 offset:39168\n" \
"v_mfma_f32_16x16x32_bf16 v[16:19], a[108:111], v[32:35], v[16:19]\ns_add_u32 m0, s57, 0\ns_add_u32 s60, s41, 98304\nbuffer_load_dwordx4 v0, s[36:39], s60 offen offset:0 sc1 lds\nv_mfma_f32_16x16x32_bf16 v[20:23], a[124:127], v[32:35], v[20:23]\ns_waitcnt lgkmcnt(6)\nds_read_b128 v[32:35], v2 offset:40960\nv_mfma_f32_16x16x32_bf16 v[24:27], a[108:111], v[36:39], v[24:27]\n" \
"v_mfma_f32_16x16x32_bf16 v[28:31], a[124:127], v[36:39], v[28:31]\ns_waitcnt lgkmcnt(6)\nds_read_b128 v[36:39], v2 offset:41216\nv_mfma_f32_16x16x32_bf16 v[16:19], a[128:131], v[40:43], v[16:19]\nbuffer_load_dwordx4 v0, s[36:39], s60 offen offset:1024 sc1 lds\nv_mfma_f32_16x16x32_bf16 v[20:23], a[144:147], v[40:43], v[20:23]\ns_waitcnt lgkmcnt(6)\nds_read_b128 v[40:43], v2 offset:43008\n" \
"v_mfma_f32_16x16x32_bf16 v[24:27], a[128:131], v[44:47], v[24:27]\nv_mfma_f32_16x16x32_bf16 v[28:31], a[144:147], v[44:47], v[28:31]\ns_waitcnt lgkmcnt(4)\nds_read_b128 v[44:47], v2 offset:43264\nv_mfma_f32_16x16x32_bf16 v[16:19], a[132:135], v[64:67], v[16:19]\nbuffer_load_dwordx4 v0, s[36:39], s60 offen offset:2048 sc1 lds\nv_mfma_f32_16x16x32_bf16 v[20:23], a[148:151], v[64:67], v[20:23]\ns_waitcnt lgkmcnt(4)\n" \
"ds_read_b128 v[64:67], v2 offset:45056\nv_mfma_f32_16x16x32_bf16 v[24:27], a[132:135], v[68:71], v[24:27]\nv_mfma_f32_16x16x32_bf16 v[28:31], a[148:151], v[68:71], v[28:31]\ns_waitcnt lgkmcnt(4)\nds_read_b128 v[68:71], v2 offset:45312\nv_mfma_f32_16x16x32_bf16 v[16:19], a[160:163], v[32:35], v[16:19]\nbuffer_load_dwordx4 v0, s[36:39], s60 offen offset:3072 sc1 lds\nv_mfma_f32_16x16x32_bf16 v[20:23], v[56:59], v[32:35], v[20:23]\n" \
"s_waitcnt lgkmcnt(4)\nds_read_b128 v[32:35], v2 offset:47104\nv_mfma_f32_16x16x32_bf16 v[24:27], a[160:163], v[36:39], v[24:27]\ns_add_u32 s44, s40, 2\ns_lshl_b32 s44, s44, 17\nv_mfma_f32_16x16x32_bf16 v[28:31], v[56:59], v[36:39], v[28:31]\ns_waitcnt lgkmcnt(4)\nds_read_b128 v[36:39], v2 offset:47360\n" \
"v_mfma_f32_16x16x32_bf16 v[16:19], a[164:167], v[40:43], v[16:19]\ns_add_u32 s44, s44, %[xn]\ns_lshr_b32 s45, s44, 8\nv_mfma_f32_16x16x32_bf16 v[20:23], v[60:63], v[40:43], v[20:23]\ns_waitcnt lgkmcnt(4)\nv_mfma_f32_16x16x32_bf16 v[24:27], a[164:167], v[44:47], v[24:27]\ns_and_b32 s45, s45, 0x2000\ns_xor_b32 s41, s44, s45\n" \
"v_mfma_f32_16x16x32_bf16 v[28:31], v[60:63], v[44:47], v[28:31]\ns_waitcnt lgkmcnt(0)\ns_waitcnt vmcnt(4)\ns_barrier\nds_read_b128 v[40:43], v2 offset:16384\nds_read_b128 v[44:47], v2 offset:16640\nv_mfma_f32_16x16x32_bf16 v[16:19], a[184:187], v[64:67], v[16:19]\nds_read_b32 v14, v14\n" \
"v_mfma_f32_16x16x32_bf16 v[20:23], a[192:195], v[64:67], v[20:23]\nds_read_b128 v[64:67], v2 offset:18432\nv_mfma_f32_16x16x32_bf16 v[24:27], a[184:187], v[68:71], v[24:27]\ns_add_u32 s41, s41, %[xw]\nv_mfma_f32_16x16x32_bf16 v[28:31], a[192:195], v[68:71], v[28:31]\nds_read_b128 v[68:71], v2 offset:18688\nv_mfma_f32_16x16x32_bf16 v[16:19], a[188:191], v[32:35], v[16:19]\nds_read_b128 v[56:59], v1 offset:4096\n" \
"ds_read_b128 v[60:63], v1 offset:5120\nv_mfma_f32_16x16x32_bf16 v[20:23], a[196:199], v[32:35], v[20:23]\nds_read_b128 v[32:35], v2 offset:20480\nv_mfma_f32_16x16x32_bf16 v[24:27], a[188:191], v[36:39], v[24:27]\ns_waitcnt lgkmcnt(5)\nv_cmp_ge_i32 s[64:65], s62, v14\nv_mfma_f32_16x16x32_bf16 v[28:31], a[196:199], v[36:39], v[28:31]\ns_cmp_eq_u64 s[64:65], 0\n" \
"s_cselect_b32 s61, s41, -2147483648\nds_read_b128 v[36:39], v2 offset:20736\nv_mfma_f32_16x16x32_bf16 v[16:19], a[208:211], v[40:43], v[16:19]\ns_waitcnt vmcnt(4)\nbuffer_store_dword v15, off, s[36:39], s54 sc1\nv_mfma_f32_16x16x32_bf16 v[20:23], a[224:227], v[40:43], v[20:23]\nds_read_b128 v[40:43], v2 offset:22528\nv_mfma_f32_16x16x32_bf16 v[24:27], a[208:211], v[44:47], v[24:27]\n" \
"s_add_u32 m0, s57, 32768\ns_add_u32 s58, s61, 32768\nbuffer_load_dwordx4 v0, s[36:39], s58 offen offset:0 sc1 lds\nv_mfma_f32_16x16x32_bf16 v[28:31], a[224:227], v[44:47], v[28:31]\ns_waitcnt lgkmcnt(6)\nds_read_b128 v[44:47], v2 offset:22784\nv_mfma_f32_16x16x32_bf16 v[16:19], a[212:215], v[64:67], v[16:19]\nv_mfma_f32_16x16x32_bf16 v[20:23], a[228:231], v[64:67], v[20:23]\n" \
"s_waitcnt lgkmcnt(6)\nds_read_b128 v[64:67], v2 offset:24576\nv_mfma_f32_16x16x32_bf16 v[24:27], a[212:215], v[68:71], v[24:27]\nbuffer_load_dwordx4 v0, s[36:39], s58 offen offset:1024 sc1 lds\nv_mfma_f32_16x16x32_bf16 v[28:31], a[228:231], v[68:71], v[28:31]\ns_waitcnt lgkmcnt(4)\nds_read_b128 v[68:71], v2 offset:24832\nv_mfma_f32_16x16x32_bf16 v[16:19], v[56:59], v[32:35], v[16:19]\n" \
"v_mfma_f32_16x16x32_bf16 v[20:23], a[240:243], v[32:35], v[20:23]\ns_waitcnt lgkmcnt(4)\nds_read_b128 v[32:35], v2 offset:26624\nv_mfma_f32_16x16x32_bf16 v[24:27], v[56:59], v[36:39], v[24:27]\nbuffer_load_dwordx4 v0, s[36:39], s58 offen offset:2048 sc1 lds\nv_mfma_f32_16x16x32_bf16 v[28:31], a[240:243], v[36:39], v[28:31]\ns_waitcnt lgkmcnt(4)\nds_read_b128 v[36:39], v2 offset:26880\n" \
"v_mfma_f32_16x16x32_bf16 v[16:19], v[60:63], v[40:43], v[16:19]\nv_mfma_f32_16x16x32_bf16 v[20:23], a[244:247], v[40:43], v[20:23]\ns_waitcnt lgkmcnt(4)\nds_read_b128 v[40:43], v2 offset:28672\nv_mfma_f32_16x16x32_bf16 v[24:27], v[60:63], v[44:47], v[24:27]\nbuffer_load_dwordx4 v0, s[36:39], s58 offen offset:3072 sc1 lds\nv_mfma_f32_16x16x32_bf16 v[28:31], a[244:247], v[44:47], v[28:31]\ns_waitcnt lgkmcnt(4)\n" \
"ds_read_b128 v[44:47], v2 offset:28928\nv_mfma_f32_16x16x32_bf16 v[16:19], v[96:99], v[64:67], v[16:19]\nds_read_b128 v[56:59], v1 offset:6144\nds_read_b128 v[60:63], v1 offset:7168\nv_mfma_f32_16x16x32_bf16 v[20:23], v[112:115], v[64:67], v[20:23]\ns_waitcnt lgkmcnt(6)\nds_read_b128 v[64:67], v2 offset:30720\nv_mfma_f32_16x16x32_bf16 v[24:27], v[96:99], v[68:71], v[24:27]\n" \
"v_mfma_f32_16x16x32_bf16 v[28:31], v[112:115], v[68:71], v[28:31]\ns_waitcnt lgkmcnt(6)\nds_read_b128 v[68:71], v2 offset:30976\nv_mfma_f32_16x16x32_bf16 v[16:19], v[100:103], v[32:35], v[16:19]\nv_mfma_f32_16x16x32_bf16 v[20:23], v[116:119], v[32:35], v[20:23]\ns_waitcnt lgkmcnt(6)\nv_mfma_f32_16x16x32_bf16 v[24:27], v[100:103], v[36:39], v[24:27]\nv_mfma_f32_16x16x32_bf16 v[28:31], v[116:119], v[36:39], v[28:31]\n" \
"s_waitcnt lgkmcnt(0)\ns_waitcnt vmcnt(5)\ns_barrier\nds_read_b128 v[32:35], v2 offset:0\nds_read_b128 v[36:39], v2 offset:256\nv_mfma_f32_16x16x32_bf16 v[16:19], v[128:131], v[40:43], v[16:19]\ns_add_u32 m0, s57, 16384\ns_nop 0\n" \
"buffer_load_dwordx4 v3, s[36:39], s42 offen offset:0 sc1 lds\nv_mfma_f32_16x16x32_bf16 v[20:23], v[136:139], v[40:43], v[20:23]\nds_read_b128 v[40:43], v2 offset:2048\nv_mfma_f32_16x16x32_bf16 v[24:27], v[128:131], v[44:47], v[24:27]\nv_mfma_f32_16x16x32_bf16 v[28:31], v[136:139], v[44:47], v[28:31]\nds_read_b128 v[44:47], v2 offset:2304\nv_mfma_f32_16x16x32_bf16 v[16:19], v[132:135], v[64:67], v[16:19]\nbuffer_load_dwordx4 v3, s[36:39], s42 offen offset:1024 sc1 lds\n" \
"v_mfma_f32_16x16x32_bf16 v[20:23], v[140:143], v[64:67], v[20:23]\nds_read_b128 v[64:67], v2 offset:4096\nv_mfma_f32_16x16x32_bf16 v[24:27], v[132:135], v[68:71], v[24:27]\nv_mfma_f32_16x16x32_bf16 v[28:31], v[140:143], v[68:71], v[28:31]\ns_waitcnt lgkmcnt(4)\nds_read_b128 v[68:71], v2 offset:4352\nv_mfma_f32_16x16x32_bf16 v[16:19], v[152:155], v[32:35], v[16:19]\nbuffer_load_dwordx4 v3, s[36:39], s42 offen offset:2048 sc1 lds\n" \
"v_mfma_f32_16x16x32_bf16 v[20:23], v[56:59], v[32:35], v[20:23]\ns_waitcnt lgkmcnt(4)\nds_read_b128 v[32:35], v2 offset:6144\nv_mfma_f32_16x16x32_bf16 v[24:27], v[152:155], v[36:39], v[24:27]\nv_mfma_f32_16x16x32_bf16 v[28:31], v[56:59], v[36:39], v[28:31]\ns_waitcnt lgkmcnt(4)\nds_read_b128 v[36:39], v2 offset:6400\nv_mfma_f32_16x16x32_bf16 v[16:19], v[156:159], v[40:43], v[16:19]\n" \
"buffer_load_dwordx4 v3, s[36:39], s42 offen offset:3072 sc1 lds\nv_add_u32 v14, s57, v0\nv_mfma_f32_16x16x32_bf16 v[20:23], v[60:63], v[40:43], v[20:23]\ns_waitcnt lgkmcnt(4)\nds_read_b128 v[40:43], v2 offset:8192\nv_mfma_f32_16x16x32_bf16 v[24:27], v[156:159], v[44:47], v[24:27]\nv_mov_b64 v[48:49], 0\nv_mfma_f32_16x16x32_bf16 v[28:31], v[60:63], v[44:47], v[28:31]\n" \
"s_waitcnt lgkmcnt(4)\nds_read_b128 v[44:47], v2 offset:10240\nv_mov_b64 v[50:51], 0\nv_mfma_f32_16x16x32_bf16 v[16:19], v[168:171], v[64:67], v[16:19]\nv_mov_b64 v[52:53], 0\nv_mfma_f32_16x16x32_bf16 v[20:23], v[184:187], v[64:67], v[20:23]\ns_waitcnt lgkmcnt(4)\nds_read_b128 v[64:67], v2 offset:12288\n" \
"v_mov_b64 v[54:55], 0\nv_mfma_f32_16x16x32_bf16 v[24:27], v[168:171], v[68:71], v[24:27]\nds_read_b128 v[56:59], v1 offset:0\nds_read_b128 v[60:63], v1 offset:1024\nv_mfma_f32_16x16x32_bf16 v[28:31], v[184:187], v[68:71], v[28:31]\ns_waitcnt lgkmcnt(6)\nds_read_b128 v[68:71], v2 offset:14336\nv_mfma_f32_16x16x32_bf16 v[16:19], v[172:175], v[32:35], v[16:19]\n" \
"v_mfma_f32_16x16x32_bf16 v[20:23], v[188:191], v[32:35], v[20:23]\ns_waitcnt lgkmcnt(6)\nds_read_b128 v[32:35], v2 offset:8448\nv_mfma_f32_16x16x32_bf16 v[24:27], v[172:175], v[36:39], v[24:27]\nv_mfma_f32_16x16x32_bf16 v[28:31], v[188:191], v[36:39], v[28:31]\ns_waitcnt lgkmcnt(6)\nds_read_b128 v[36:39], v2 offset:10496\nv_mfma_f32_16x16x32_bf16 v[16:19], v[200:203], v[40:43], v[16:19]\n" \
"s_waitcnt vmcnt(0)\nds_read_b128 v[80:83], v14 offset:16384\nds_read_b128 v[84:87], v14 offset:17408\nv_mfma_f32_16x16x32_bf16 v[20:23], v[216:219], v[40:43], v[20:23]\ns_waitcnt lgkmcnt(8)\nds_read_b128 v[40:43], v2 offset:12544\nv_mfma_f32_16x16x32_bf16 v[16:19], v[204:207], v[44:47], v[16:19]\nds_read_b128 v[88:91], v14 offset:18432\n" \
"ds_read_b128 v[92:95], v14 offset:19456\nv_mfma_f32_16x16x32_bf16 v[20:23], v[220:223], v[44:47], v[20:23]\ns_waitcnt lgkmcnt(10)\nds_read_b128 v[44:47], v2 offset:14592\nv_mfma_f32_16x16x32_bf16 v[16:19], v[232:235], v[64:67], v[16:19]\ns_waitcnt lgkmcnt(4)\nv_max3_u32 v72, v80, v82, v84\nv_mfma_f32_16x16x32_bf16 v[20:23], v[240:243], v[64:67], v[20:23]\n" \
"s_waitcnt lgkmcnt(2)\nv_max3_u32 v72, v72, v86, v88\nv_mfma_f32_16x16x32_bf16 v[16:19], v[236:239], v[68:71], v[16:19]\ns_waitcnt lgkmcnt(1)\nv_max3_u32 v72, v72, v90, v92\nv_max_u32 v72, v72, v94\nv_mfma_f32_16x16x32_bf16 v[20:23], v[244:247], v[68:71], v[20:23]\nv_cmp_eq_u32 vcc, -1, v72\n" \
"v_mfma_f32_16x16x32_bf16 v[24:27], v[200:203], v[32:35], v[24:27]\ns_cbranch_vccnz .Lvu_re%=\n.Lvu_rer%=:\ns_barrier\ns_memrealtime s[68:69]\nds_read_b128 v[80:83], v0 offset:16384\nv_mfma_f32_16x16x32_bf16 v[28:31], v[216:219], v[32:35], v[28:31]\nds_read_b128 v[84:87], v0 offset:17408\n" \
"v_mfma_f32_16x16x32_bf16 v[24:27], v[204:207], v[36:39], v[24:27]\nds_read_b128 v[88:91], v0 offset:18432\nv_cvt_pk_bf16_f32 v72, v16, v17\nv_cvt_pk_bf16_f32 v73, v18, v19\nv_mfma_f32_16x16x32_bf16 v[28:31], v[220:223], v[36:39], v[28:31]\nds_read_b128 v[92:95], v0 offset:19456\nv_cvt_pk_bf16_f32 v74, v20, v21\nv_cvt_pk_bf16_f32 v75, v22, v23\n" \
"v_mfma_f32_16x16x32_bf16 v[24:27], v[232:235], v[40:43], v[24:27]\nds_read_b128 v[16:19], v0 offset:20480\nv_mfma_f32_16x16x32_bf16 v[28:31], v[240:243], v[40:43], v[28:31]\ns_waitcnt lgkmcnt(5)\nds_read_b128 v[20:23], v0 offset:21504\nv_mfma_f32_16x16x32_bf16 v[24:27], v[236:239], v[44:47], v[24:27]\nds_read_b128 v[32:35], v0 offset:22528\nv_mfma_f32_16x16x32_bf16 v[28:31], v[244:247], v[44:47], v[28:31]\n" \
"s_cmp_lt_i32 s40, 0\nds_read_b128 v[36:39], v0 offset:23552\nds_write2st64_b64 v9, v[72:73], v[74:75] offset0:0 offset1:8\ns_cbranch_scc1 .Lvu_first%=\n.Lvu_crit%=:\ns_waitcnt lgkmcnt(8)\nv_smfmac_f32_16x16x64_bf16 v[48:51], v[80:83], a[0:7], v4\ns_waitcnt lgkmcnt(7)\n" \
"ds_read_b128 v[64:67], v1 offset:4096\nds_read_b128 v[40:43], v0 offset:24576\nv_smfmac_f32_16x16x64_bf16 v[48:51], v[84:87], a[24:31], v4\ns_waitcnt lgkmcnt(8)\nds_read_b128 v[68:71], v1 offset:5120\nds_read_b128 v[44:47], v0 offset:25600\nv_cvt_pk_bf16_f32 v76, v24, v25\nv_smfmac_f32_16x16x64_bf16 v[48:51], v[88:91], a[56:63], v4\n" \
"s_waitcnt lgkmcnt(9)\nds_read_b128 v[80:83], v0 offset:28672\nv_cvt_pk_bf16_f32 v77, v26, v27\nv_smfmac_f32_16x16x64_bf16 v[48:51], v[92:95], v[56:63], v4\ns_waitcnt lgkmcnt(9)\nds_read_b128 v[84:87], v0 offset:29696\nv_cvt_pk_bf16_f32 v78, v28, v29\nv_smfmac_f32_16x16x64_bf16 v[48:51], v[16:19], a[104:111], v4\n" \
"s_waitcnt lgkmcnt(9)\nds_read_b128 v[88:91], v0 offset:30720\nv_cvt_pk_bf16_f32 v79, v30, v31\nv_smfmac_f32_16x16x64_bf16 v[48:51], v[20:23], a[128:135], v4\ns_waitcnt lgkmcnt(9)\nds_read_b128 v[92:95], v0 offset:31744\nv_mov_b32 v14, s40\nv_smfmac_f32_16x16x64_bf16 v[48:51], v[32:35], a[160:167], v4\n" \
"s_waitcnt lgkmcnt(9)\nds_read_b128 v[24:27], v0 offset:26624\nv_smfmac_f32_16x16x64_bf16 v[48:51], v[36:39], a[184:191], v4\ns_waitcnt lgkmcnt(7)\nds_read_b128 v[28:31], v0 offset:27648\nv_smfmac_f32_16x16x64_bf16 v[48:51], v[40:43], a[208:215], v4\ns_waitcnt lgkmcnt(6)\nds_read_b128 v[56:59], v1 offset:6144\n" \
"v_smfmac_f32_16x16x64_bf16 v[48:51], v[44:47], v[64:71], v4\ns_waitcnt lgkmcnt(6)\nds_read_b128 v[60:63], v1 offset:7168\nv_smfmac_f32_16x16x64_bf16 v[48:51], v[80:83], v[152:159], v4\ns_sub_u32 s88, s86, s89\ns_min_u32 s88, s88, 150\ns_waitcnt lgkmcnt(6)\nds_write2st64_b64 v9, v[76:77], v[78:79] offset0:2 offset1:10\n" \
"v_smfmac_f32_16x16x64_bf16 v[48:51], v[84:87], v[168:175], v4\ns_add_u32 s88, s88, s68\ns_mov_b32 s89, s68\ns_waitcnt lgkmcnt(6)\nv_mov_b32 v15, s88\nv_smfmac_f32_16x16x64_bf16 v[48:51], v[88:91], v[200:207], v4\ns_mov_b64 exec, 1\nbuffer_store_dwordx2 v[14:15], off, s[36:39], %[sg] sc1\n" \
"s_mov_b64 exec, -1\ns_waitcnt lgkmcnt(5)\nv_smfmac_f32_16x16x64_bf16 v[48:51], v[92:95], v[232:239], v4\ns_waitcnt lgkmcnt(4)\nv_smfmac_f32_16x16x64_bf16 v[48:51], v[24:27], v[96:103], v4\ns_waitcnt lgkmcnt(3)\nds_read_b128 v[64:67], v1 offset:2048\nv_smfmac_f32_16x16x64_bf16 v[48:51], v[28:31], v[128:135], v4\n" \
"s_waitcnt lgkmcnt(2)\nds_read_b128 v[68:71], v1 offset:3072\nv_smfmac_f32_16x16x64_bf16 v[52:55], v[80:83], v[56:63], v4\nds_read_b128 v[56:59], v1 offset:8192\nv_smfmac_f32_16x16x64_bf16 v[52:55], v[84:87], v[184:191], v4\nds_read_b128 v[60:63], v1 offset:9216\nv_add_f32 v48, v48, v50\nv_add_f32 v49, v49, v51\n" \
"v_smfmac_f32_16x16x64_bf16 v[52:55], v[88:91], v[216:223], v4\nds_read_b128 v[80:83], v0 offset:16384\nv_cvt_pk_bf16_f32 v14, v48, v49\nv_smfmac_f32_16x16x64_bf16 v[52:55], v[92:95], v[240:247], v4\nds_write_b16 v5, v14 offset:0\nds_write_b16_d16_hi v5, v14 offset:16\nv_smfmac_f32_16x16x64_bf16 v[52:55], v[16:19], a[120:127], v4\nds_read_b128 v[84:87], v0 offset:17408\n" \
"v_mov_b64 v[16:17], 0\nv_smfmac_f32_16x16x64_bf16 v[52:55], v[20:23], a[144:151], v4\nds_read_b128 v[88:91], v0 offset:18432\nv_mov_b64 v[18:19], 0\nv_smfmac_f32_16x16x64_bf16 v[52:55], v[24:27], v[112:119], v4\nds_read_b128 v[92:95], v0 offset:19456\nv_mov_b64 v[20:21], 0\nv_smfmac_f32_16x16x64_bf16 v[52:55], v[28:31], v[136:143], v4\n" \
"s_waitcnt lgkmcnt(8)\nv_mov_b64 v[22:23], 0\nv_smfmac_f32_16x16x64_bf16 v[52:55], v[32:35], v[64:71], v4\ns_waitcnt lgkmcnt(3)\nds_read_b128 v[64:67], v1 offset:12288\ns_barrier\nds_read_b128 v[72:75], v11 offset:57344\nv_mov_b64 v[24:25], 0\n" \
"v_smfmac_f32_16x16x64_bf16 v[52:55], v[36:39], a[192:199], v4\nds_read_b128 v[68:71], v1 offset:13312\nds_read_b128 v[76:79], v11 offset:58368\nv_mov_b64 v[26:27], 0\nv_smfmac_f32_16x16x64_bf16 v[52:55], v[40:43], a[224:231], v4\nv_mov_b64 v[28:29], 0\nv_smfmac_f32_16x16x64_bf16 v[52:55], v[44:47], a[240:247], v4\nv_mov_b64 v[30:31], 0\n" \
"v_smfmac_f32_16x16x64_bf16 v[52:55], v[80:83], a[16:23], v4\ns_waitcnt lgkmcnt(6)\nv_mov_b64 v[32:33], 0\nv_smfmac_f32_16x16x64_bf16 v[52:55], v[84:87], a[40:47], v4\ns_waitcnt lgkmcnt(5)\nv_mov_b64 v[34:35], 0\nv_smfmac_f32_16x16x64_bf16 v[52:55], v[88:91], a[72:79], v4\ns_waitcnt lgkmcnt(4)\n" \
"v_mov_b64 v[36:37], 0\nv_smfmac_f32_16x16x64_bf16 v[52:55], v[92:95], a[88:95], v4\ns_waitcnt lgkmcnt(2)\nv_mov_b64 v[38:39], 0\nv_smfmac_f32_16x16x64_bf16 v[16:19], v[72:75], a[8:15], v10\ns_waitcnt lgkmcnt(0)\nv_mov_b64 v[40:41], 0\nv_smfmac_f32_16x16x64_bf16 v[16:19], v[76:79], v[56:63], v10\n" \
"ds_read_b128 v[56:59], v1 offset:10240\nv_add_f32 v52, v52, v54\nv_add_f32 v53, v53, v55\nv_smfmac_f32_16x16x64_bf16 v[20:23], v[72:75], a[64:71], v10\nds_read_b128 v[60:63], v1 offset:11264\nv_cvt_pk_bf16_f32 v14, v52, v53\nv_smfmac_f32_16x16x64_bf16 v[20:23], v[76:79], a[80:87], v10\nds_write_b16 v5, v14 offset:2048\n" \
"ds_write_b16_d16_hi v5, v14 offset:2064\nv_mov_b64 v[42:43], 0\nv_smfmac_f32_16x16x64_bf16 v[24:27], v[72:75], a[112:119], v10\nv_mov_b64 v[44:45], 0\nv_smfmac_f32_16x16x64_bf16 v[24:27], v[76:79], v[64:71], v10\nds_read_b128 v[64:67], v1 offset:14336\nv_mov_b64 v[46:47], 0\nv_smfmac_f32_16x16x64_bf16 v[28:31], v[72:75], a[168:175], v10\n" \
"ds_read_b128 v[68:71], v1 offset:15360\nv_mov_b64 v[48:49], 0\nv_smfmac_f32_16x16x64_bf16 v[28:31], v[76:79], a[176:183], v10\nv_mov_b64 v[50:51], 0\nv_smfmac_f32_16x16x64_bf16 v[32:35], v[72:75], a[216:223], v10\ns_waitcnt lgkmcnt(2)\ns_barrier\ns_mov_b32 s59, 0\n" \
"ds_read_b128 v[80:83], v11 offset:59392\nv_mov_b64 v[52:53], 0\nv_smfmac_f32_16x16x64_bf16 v[32:35], v[76:79], v[56:63], v10\nds_read_b128 v[56:59], v1 offset:16384\nds_read_b128 v[84:87], v11 offset:60416\nv_mov_b64 v[54:55], 0\nv_smfmac_f32_16x16x64_bf16 v[36:39], v[72:75], v[104:111], v10\ns_add_u32 m0, s57, 0\n" \
"s_add_u32 s58, s61, 0\nds_read_b128 v[60:63], v1 offset:17408\nbuffer_load_dwordx4 v0, s[36:39], s58 offen offset:0 sc1 lds\nv_smfmac_f32_16x16x64_bf16 v[36:39], v[76:79], v[120:127], v10\nv_smfmac_f32_16x16x64_bf16 v[40:43], v[72:75], v[160:167], v10\ns_waitcnt lgkmcnt(4)\nv_smfmac_f32_16x16x64_bf16 v[40:43], v[76:79], v[64:71], v10\nds_read_b128 v[64:67], v1 offset:20480\n" \
"v_smfmac_f32_16x16x64_bf16 v[44:47], v[72:75], v[208:215], v10\nds_read_b128 v[68:71], v1 offset:21504\nbuffer_load_dwordx4 v0, s[36:39], s58 offen offset:1024 sc1 lds\nv_smfmac_f32_16x16x64_bf16 v[44:47], v[76:79], v[224:231], v10\ns_waitcnt lgkmcnt(5)\nv_smfmac_f32_16x16x64_bf16 v[16:19], v[80:83], a[32:39], v10\ns_waitcnt lgkmcnt(3)\nds_read_b128 v[72:75], v11 offset:49152\n" \
"v_smfmac_f32_16x16x64_bf16 v[16:19], v[84:87], a[48:55], v10\ns_waitcnt lgkmcnt(3)\nbuffer_load_dwordx4 v0, s[36:39], s58 offen offset:2048 sc1 lds\nds_read_b128 v[76:79], v11 offset:50176\nv_smfmac_f32_16x16x64_bf16 v[20:23], v[80:83], v[56:63], v10\nds_read_b128 v[56:59], v1 offset:18432\nv_smfmac_f32_16x16x64_bf16 v[20:23], v[84:87], a[96:103], v10\nds_read_b128 v[60:63], v1 offset:19456\n" \
"v_cvt_pk_bf16_f32 v88, v16, v17\nv_cvt_pk_bf16_f32 v89, v18, v19\nv_and_or_b32 v88, v88, -2, v12\nv_smfmac_f32_16x16x64_bf16 v[24:27], v[80:83], a[136:143], v10\nbuffer_load_dwordx4 v0, s[36:39], s58 offen offset:3072 sc1 lds\nds_read_b128 v[16:19], v11 offset:51200\nv_smfmac_f32_16x16x64_bf16 v[24:27], v[84:87], a[152:159], v10\ns_waitcnt lgkmcnt(5)\n" \
"v_cvt_pk_bf16_f32 v90, v20, v21\nv_cvt_pk_bf16_f32 v91, v22, v23\nv_and_or_b32 v90, v90, -2, v12\nv_smfmac_f32_16x16x64_bf16 v[28:31], v[80:83], v[64:71], v10\nds_read_b128 v[64:67], v1 offset:22528\nv_permlane16_swap_b32 v89, v91\nv_permlane16_swap_b32 v88, v90\nv_smfmac_f32_16x16x64_bf16 v[28:31], v[84:87], a[200:207], v10\n" \
"s_add_u32 s63, s59, 0\nds_read_b128 v[68:71], v1 offset:23552\nbuffer_store_dwordx4 v[88:91], v6, s[36:39], s63 offen sc1\nv_cvt_pk_bf16_f32 v92, v24, v25\nv_cvt_pk_bf16_f32 v93, v26, v27\nv_and_or_b32 v92, v92, -2, v12\nv_smfmac_f32_16x16x64_bf16 v[32:35], v[80:83], a[232:239], v10\nds_read_b128 v[20:23], v11 offset:52224\n" \
"v_smfmac_f32_16x16x64_bf16 v[32:35], v[84:87], a[248:255], v10\ns_waitcnt lgkmcnt(4)\nds_read_b128 v[24:27], v11 offset:53248\nv_cvt_pk_bf16_f32 v94, v28, v29\nv_cvt_pk_bf16_f32 v95, v30, v31\nv_and_or_b32 v94, v94, -2, v12\nv_smfmac_f32_16x16x64_bf16 v[36:39], v[80:83], v[56:63], v10\nds_read_b128 v[56:59], v1 offset:8192\n" \
"v_permlane16_swap_b32 v93, v95\nv_permlane16_swap_b32 v92, v94\nv_smfmac_f32_16x16x64_bf16 v[36:39], v[84:87], v[144:151], v10\ns_add_u32 s63, s59, 131072\nds_read_b128 v[60:63], v1 offset:9216\nbuffer_store_dwordx4 v[92:95], v6, s[36:39], s63 offen sc1\nv_cvt_pk_bf16_f32 v88, v32, v33\nv_cvt_pk_bf16_f32 v89, v34, v35\n" \
"v_and_or_b32 v88, v88, -2, v12\nv_smfmac_f32_16x16x64_bf16 v[40:43], v[80:83], v[176:183], v10\nds_read_b128 v[28:31], v11 offset:54272\nv_smfmac_f32_16x16x64_bf16 v[40:43], v[84:87], v[192:199], v10\ns_waitcnt lgkmcnt(5)\nds_read_b128 v[32:35], v11 offset:55296\nv_cvt_pk_bf16_f32 v90, v36, v37\nv_cvt_pk_bf16_f32 v91, v38, v39\n" \
"v_and_or_b32 v90, v90, -2, v12\nv_smfmac_f32_16x16x64_bf16 v[44:47], v[80:83], v[64:71], v10\nds_read_b128 v[64:67], v1 offset:16384\nv_permlane16_swap_b32 v89, v91\nv_permlane16_swap_b32 v88, v90\nv_smfmac_f32_16x16x64_bf16 v[44:47], v[84:87], v[248:255], v10\ns_add_u32 s63, s59, 4096\ns_cmp_ge_i32 s40, 63\n" \
"ds_read_b128 v[68:71], v1 offset:17408\nbuffer_store_dwordx4 v[88:91], v6, s[36:39], s63 offen sc1\nv_cvt_pk_bf16_f32 v92, v40, v41\nv_cvt_pk_bf16_f32 v93, v42, v43\nv_and_or_b32 v92, v92, -2, v12\ns_cbranch_scc1 .Lvu_last%=\n.Lvu_y%=:\nv_smfmac_f32_16x16x64_bf16 v[48:51], v[72:75], a[8:15], v10\n" \
"s_add_u32 s40, s40, 1\nds_read_b128 v[36:39], v11 offset:56320\nv_smfmac_f32_16x16x64_bf16 v[52:55], v[76:79], a[8:15], v10\ns_and_b32 s44, s40, 1\ns_lshl_b32 s55, s44, 22\ns_waitcnt lgkmcnt(5)\nv_cvt_pk_bf16_f32 v94, v44, v45\nv_cvt_pk_bf16_f32 v95, v46, v47\n" \
"v_and_or_b32 v94, v94, -2, v12\nv_smfmac_f32_16x16x64_bf16 v[48:51], v[16:19], v[56:63], v10\ns_bfe_u32 s45, s40, 0x10001\nv_permlane16_swap_b32 v93, v95\nv_permlane16_swap_b32 v92, v94\nv_smfmac_f32_16x16x64_bf16 v[52:55], v[20:23], v[56:63], v10\ns_add_u32 s63, s59, 135168\nbuffer_store_dwordx4 v[92:95], v6, s[36:39], s63 offen sc1\n" \
"v_mov_b64 v[80:81], 0\nv_mov_b64 v[82:83], 0\nv_mov_b64 v[84:85], 0\nv_mov_b64 v[86:87], 0\nv_smfmac_f32_16x16x64_bf16 v[48:51], v[24:27], a[32:39], v10\ns_memrealtime s[86:87]\ns_waitcnt lgkmcnt(4)\nds_read_b128 v[56:59], v1 offset:12288\n" \
"v_smfmac_f32_16x16x64_bf16 v[52:55], v[28:31], a[32:39], v10\ns_waitcnt lgkmcnt(4)\nds_read_b128 v[60:63], v1 offset:13312\nv_smfmac_f32_16x16x64_bf16 v[48:51], v[32:35], a[48:55], v10\ns_waitcnt lgkmcnt(2)\nv_smfmac_f32_16x16x64_bf16 v[52:55], v[36:39], a[48:55], v10\nv_mov_b32 v13, s45\nv_smfmac_f32_16x16x64_bf16 v[80:83], v[72:75], a[64:71], v10\n" \
"v_cvt_pk_bf16_f32 v88, v48, v49\nv_smfmac_f32_16x16x64_bf16 v[84:87], v[76:79], a[64:71], v10\nv_cvt_pk_bf16_f32 v89, v50, v51\nv_smfmac_f32_16x16x64_bf16 v[80:83], v[16:19], a[80:87], v10\nv_cvt_pk_bf16_f32 v90, v52, v53\nv_smfmac_f32_16x16x64_bf16 v[84:87], v[20:23], a[80:87], v10\ns_add_u32 s60, s55, 0\nv_cvt_pk_bf16_f32 v91, v54, v55\n" \
"v_smfmac_f32_16x16x64_bf16 v[80:83], v[24:27], v[64:71], v10\nv_and_or_b32 v88, v88, -2, v13\nv_smfmac_f32_16x16x64_bf16 v[84:87], v[28:31], v[64:71], v10\ns_and_b32 s42, s40, 3\ns_lshl_b32 s42, s42, 15\nv_and_or_b32 v90, v90, -2, v13\nv_smfmac_f32_16x16x64_bf16 v[80:83], v[32:35], a[96:103], v10\nbuffer_store_dwordx4 v[88:91], v7, s[36:39], s60 offen offset:0 sc1\n" \
"v_mov_b64 v[48:49], 0\nv_mov_b64 v[50:51], 0\nv_mov_b64 v[52:53], 0\nv_mov_b64 v[54:55], 0\nv_smfmac_f32_16x16x64_bf16 v[84:87], v[36:39], a[96:103], v10\nds_read_b128 v[64:67], v1 offset:20480\nv_mov_b32 v12, s44\nv_smfmac_f32_16x16x64_bf16 v[48:51], v[72:75], a[112:119], v10\n" \
"ds_read_b128 v[68:71], v1 offset:21504\nv_cvt_pk_bf16_f32 v92, v80, v81\nv_smfmac_f32_16x16x64_bf16 v[52:55], v[76:79], a[112:119], v10\ns_waitcnt lgkmcnt(2)\nv_cvt_pk_bf16_f32 v93, v82, v83\nv_smfmac_f32_16x16x64_bf16 v[48:51], v[16:19], v[56:63], v10\nv_cvt_pk_bf16_f32 v94, v84, v85\nv_smfmac_f32_16x16x64_bf16 v[52:55], v[20:23], v[56:63], v10\n" \
"s_add_u32 s60, s55, 4096\nv_cvt_pk_bf16_f32 v95, v86, v87\nv_smfmac_f32_16x16x64_bf16 v[48:51], v[24:27], a[136:143], v10\nds_read_b128 v[56:59], v1 offset:10240\nv_and_or_b32 v92, v92, -2, v13\nv_smfmac_f32_16x16x64_bf16 v[52:55], v[28:31], a[136:143], v10\nds_read_b128 v[60:63], v1 offset:11264\nv_and_or_b32 v94, v94, -2, v13\n" \
"v_smfmac_f32_16x16x64_bf16 v[48:51], v[32:35], a[152:159], v10\nbuffer_store_dwordx4 v[92:95], v7, s[36:39], s60 offen offset:0 sc1\nv_mov_b64 v[80:81], 0\nv_mov_b64 v[82:83], 0\nv_mov_b64 v[84:85], 0\nv_mov_b64 v[86:87], 0\nv_smfmac_f32_16x16x64_bf16 v[52:55], v[36:39], a[152:159], v10\ns_waitcnt vmcnt(6)\n" \
"s_barrier\nv_smfmac_f32_16x16x64_bf16 v[80:83], v[72:75], a[168:175], v10\nv_cvt_pk_bf16_f32 v88, v48, v49\nv_smfmac_f32_16x16x64_bf16 v[84:87], v[76:79], a[168:175], v10\nv_cvt_pk_bf16_f32 v89, v50, v51\nv_smfmac_f32_16x16x64_bf16 v[80:83], v[16:19], a[176:183], v10\ns_cmp_lt_u32 s40, 2\ns_cselect_b32 s54, %[fy], -2147483648\n" \
"v_cvt_pk_bf16_f32 v90, v52, v53\nv_smfmac_f32_16x16x64_bf16 v[84:87], v[20:23], a[176:183], v10\ns_add_u32 s60, s55, 16384\ns_waitcnt lgkmcnt(2)\nv_cvt_pk_bf16_f32 v91, v54, v55\nv_smfmac_f32_16x16x64_bf16 v[80:83], v[24:27], v[64:71], v10\nv_and_or_b32 v88, v88, -2, v13\nv_smfmac_f32_16x16x64_bf16 v[84:87], v[28:31], v[64:71], v10\n" \
"v_and_or_b32 v90, v90, -2, v13\nv_smfmac_f32_16x16x64_bf16 v[80:83], v[32:35], a[200:207], v10\nbuffer_store_dwordx4 v[88:91], v7, s[36:39], s60 offen offset:0 sc1\nv_mov_b64 v[48:49], 0\nv_mov_b64 v[50:51], 0\nv_mov_b64 v[52:53], 0\nv_mov_b64 v[54:55], 0\nv_smfmac_f32_16x16x64_bf16 v[84:87], v[36:39], a[200:207], v10\n" \
"ds_read_b128 v[64:67], v1 offset:18432\nv_smfmac_f32_16x16x64_bf16 v[48:51], v[72:75], a[216:223], v10\nds_read_b128 v[68:71], v1 offset:19456\nv_cvt_pk_bf16_f32 v92, v80, v81\nv_smfmac_f32_16x16x64_bf16 v[52:55], v[76:79], a[216:223], v10\ns_waitcnt lgkmcnt(2)\nv_cvt_pk_bf16_f32 v93, v82, v83\nv_smfmac_f32_16x16x64_bf16 v[48:51], v[16:19], v[56:63], v10\n" \
"v_cvt_pk_bf16_f32 v94, v84, v85\nv_smfmac_f32_16x16x64_bf16 v[52:55], v[20:23], v[56:63], v10\ns_add_u32 s60, s55, 20480\nv_cvt_pk_bf16_f32 v95, v86, v87\nv_smfmac_f32_16x16x64_bf16 v[48:51], v[24:27], a[232:239], v10\nds_read_b128 v[56:59], v1 offset:14336\nv_and_or_b32 v92, v92, -2, v13\nv_smfmac_f32_16x16x64_bf16 v[52:55], v[28:31], a[232:239], v10\n" \
"ds_read_b128 v[60:63], v1 offset:15360\nv_and_or_b32 v94, v94, -2, v13\nv_smfmac_f32_16x16x64_bf16 v[48:51], v[32:35], a[248:255], v10\nbuffer_store_dwordx4 v[92:95], v7, s[36:39], s60 offen offset:0 sc1\nv_mov_b64 v[80:81], 0\nv_mov_b64 v[82:83], 0\nv_mov_b64 v[84:85], 0\nv_mov_b64 v[86:87], 0\n" \
"v_smfmac_f32_16x16x64_bf16 v[52:55], v[36:39], a[248:255], v10\nv_smfmac_f32_16x16x64_bf16 v[80:83], v[72:75], v[104:111], v10\nv_cvt_pk_bf16_f32 v88, v48, v49\nv_smfmac_f32_16x16x64_bf16 v[84:87], v[76:79], v[104:111], v10\nv_cvt_pk_bf16_f32 v89, v50, v51\nv_smfmac_f32_16x16x64_bf16 v[80:83], v[16:19], v[120:127], v10\nv_cvt_pk_bf16_f32 v90, v52, v53\nv_smfmac_f32_16x16x64_bf16 v[84:87], v[20:23], v[120:127], v10\n" \
"s_add_u32 s60, s55, 0\ns_waitcnt lgkmcnt(2)\nv_cvt_pk_bf16_f32 v91, v54, v55\nv_smfmac_f32_16x16x64_bf16 v[80:83], v[24:27], v[64:71], v10\nv_and_or_b32 v88, v88, -2, v13\nv_smfmac_f32_16x16x64_bf16 v[84:87], v[28:31], v[64:71], v10\nv_and_or_b32 v90, v90, -2, v13\nv_smfmac_f32_16x16x64_bf16 v[80:83], v[32:35], v[144:151], v10\n" \
"buffer_store_dwordx4 v[88:91], v7, s[36:39], s60 offen offset:2048 sc1\nv_mov_b64 v[48:49], 0\nv_mov_b64 v[50:51], 0\nv_mov_b64 v[52:53], 0\nv_mov_b64 v[54:55], 0\nv_smfmac_f32_16x16x64_bf16 v[84:87], v[36:39], v[144:151], v10\nds_read_b128 v[64:67], v1 offset:22528\nv_smfmac_f32_16x16x64_bf16 v[48:51], v[72:75], v[160:167], v10\n" \
"ds_read_b128 v[68:71], v1 offset:23552\nv_cvt_pk_bf16_f32 v92, v80, v81\nv_smfmac_f32_16x16x64_bf16 v[52:55], v[76:79], v[160:167], v10\ns_waitcnt lgkmcnt(2)\nv_cvt_pk_bf16_f32 v93, v82, v83\nv_smfmac_f32_16x16x64_bf16 v[48:51], v[16:19], v[56:63], v10\nds_read_b128 v[40:43], v11 offset:49280\nv_cvt_pk_bf16_f32 v94, v84, v85\n" \
"v_smfmac_f32_16x16x64_bf16 v[52:55], v[20:23], v[56:63], v10\ns_add_u32 s60, s55, 4096\nv_cvt_pk_bf16_f32 v95, v86, v87\nv_smfmac_f32_16x16x64_bf16 v[48:51], v[24:27], v[176:183], v10\nds_read_b128 v[56:59], v1 offset:8192\nds_read_b128 v[44:47], v11 offset:50304\nv_and_or_b32 v92, v92, -2, v13\nv_smfmac_f32_16x16x64_bf16 v[52:55], v[28:31], v[176:183], v10\n" \
"ds_read_b128 v[60:63], v1 offset:9216\nv_and_or_b32 v94, v94, -2, v13\nv_smfmac_f32_16x16x64_bf16 v[48:51], v[32:35], v[192:199], v10\nbuffer_store_dwordx4 v[92:95], v7, s[36:39], s60 offen offset:2048 sc1\nv_mov_b64 v[80:81], 0\nv_mov_b64 v[82:83], 0\nv_mov_b64 v[84:85], 0\nv_mov_b64 v[86:87], 0\n" \
"v_smfmac_f32_16x16x64_bf16 v[52:55], v[36:39], v[192:199], v10\nv_smfmac_f32_16x16x64_bf16 v[80:83], v[72:75], v[208:215], v10\nds_read_b128 v[72:75], v11 offset:51328\nv_cvt_pk_bf16_f32 v88, v48, v49\nv_smfmac_f32_16x16x64_bf16 v[84:87], v[76:79], v[208:215], v10\nds_read_b128 v[76:79], v11 offset:52352\nv_cvt_pk_bf16_f32 v89, v50, v51\nv_smfmac_f32_16x16x64_bf16 v[80:83], v[16:19], v[224:231], v10\n" \
"ds_read_b128 v[16:19], v11 offset:53376\nv_cvt_pk_bf16_f32 v90, v52, v53\nv_smfmac_f32_16x16x64_bf16 v[84:87], v[20:23], v[224:231], v10\ns_add_u32 s60, s55, 16384\ns_waitcnt lgkmcnt(7)\nds_read_b128 v[20:23], v11 offset:54400\nv_cvt_pk_bf16_f32 v91, v54, v55\nv_smfmac_f32_16x16x64_bf16 v[80:83], v[24:27], v[64:71], v10\n" \
"ds_read_b128 v[24:27], v11 offset:55424\nv_and_or_b32 v88, v88, -2, v13\nv_smfmac_f32_16x16x64_bf16 v[84:87], v[28:31], v[64:71], v10\nds_read_b128 v[28:31], v11 offset:56448\nv_and_or_b32 v90, v90, -2, v13\nv_smfmac_f32_16x16x64_bf16 v[80:83], v[32:35], v[248:255], v10\nbuffer_store_dwordx4 v[88:91], v7, s[36:39], s60 offen offset:2048 sc1\nv_mov_b64 v[48:49], 0\n" \
"v_mov_b64 v[50:51], 0\nv_mov_b64 v[52:53], 0\nv_mov_b64 v[54:55], 0\nv_smfmac_f32_16x16x64_bf16 v[84:87], v[36:39], v[248:255], v10\ns_waitcnt lgkmcnt(9)\nds_read_b128 v[64:67], v1 offset:16384\nv_smfmac_f32_16x16x64_bf16 v[48:51], v[40:43], a[8:15], v10\ns_waitcnt lgkmcnt(8)\n" \
"ds_read_b128 v[68:71], v1 offset:17408\nv_cvt_pk_bf16_f32 v92, v80, v81\nv_smfmac_f32_16x16x64_bf16 v[52:55], v[44:47], a[8:15], v10\ns_waitcnt lgkmcnt(7)\nv_cvt_pk_bf16_f32 v93, v82, v83\nv_smfmac_f32_16x16x64_bf16 v[48:51], v[72:75], v[56:63], v10\ns_waitcnt lgkmcnt(6)\nv_cvt_pk_bf16_f32 v94, v84, v85\n" \
"v_smfmac_f32_16x16x64_bf16 v[52:55], v[76:79], v[56:63], v10\ns_add_u32 s60, s55, 20480\ns_waitcnt lgkmcnt(5)\nv_cvt_pk_bf16_f32 v95, v86, v87\nv_smfmac_f32_16x16x64_bf16 v[48:51], v[16:19], a[32:39], v10\ns_waitcnt lgkmcnt(4)\nds_read_b128 v[56:59], v1 offset:12288\nv_and_or_b32 v92, v92, -2, v13\n" \
"v_smfmac_f32_16x16x64_bf16 v[52:55], v[20:23], a[32:39], v10\ns_waitcnt lgkmcnt(4)\nds_read_b128 v[60:63], v1 offset:13312\nv_and_or_b32 v94, v94, -2, v13\nv_smfmac_f32_16x16x64_bf16 v[48:51], v[24:27], a[48:55], v10\ns_waitcnt lgkmcnt(4)\nbuffer_store_dwordx4 v[92:95], v7, s[36:39], s60 offen offset:2048 sc1\nv_mov_b64 v[80:81], 0\n" \
"v_mov_b64 v[82:83], 0\nv_mov_b64 v[84:85], 0\nv_mov_b64 v[86:87], 0\nv_smfmac_f32_16x16x64_bf16 v[52:55], v[28:31], a[48:55], v10\nv_smfmac_f32_16x16x64_bf16 v[80:83], v[40:43], a[64:71], v10\nv_cvt_pk_bf16_f32 v88, v48, v49\nv_smfmac_f32_16x16x64_bf16 v[84:87], v[44:47], a[64:71], v10\nv_cvt_pk_bf16_f32 v89, v50, v51\n" \
"v_smfmac_f32_16x16x64_bf16 v[80:83], v[72:75], a[80:87], v10\nv_cvt_pk_bf16_f32 v90, v52, v53\nv_smfmac_f32_16x16x64_bf16 v[84:87], v[76:79], a[80:87], v10\ns_add_u32 s60, s55, 0\ns_waitcnt lgkmcnt(2)\nv_cvt_pk_bf16_f32 v91, v54, v55\nv_smfmac_f32_16x16x64_bf16 v[80:83], v[16:19], v[64:71], v10\nv_and_or_b32 v88, v88, -2, v13\n" \
"v_smfmac_f32_16x16x64_bf16 v[84:87], v[20:23], v[64:71], v10\nv_and_or_b32 v90, v90, -2, v13\nv_smfmac_f32_16x16x64_bf16 v[80:83], v[24:27], a[96:103], v10\nbuffer_store_dwordx4 v[88:91], v7, s[36:39], s60 offen offset:512 sc1\nv_mov_b64 v[48:49], 0\nv_mov_b64 v[50:51], 0\nv_mov_b64 v[52:53], 0\nv_mov_b64 v[54:55], 0\n" \
"v_smfmac_f32_16x16x64_bf16 v[84:87], v[28:31], a[96:103], v10\nds_read_b128 v[64:67], v1 offset:20480\nv_smfmac_f32_16x16x64_bf16 v[48:51], v[40:43], a[112:119], v10\nds_read_b128 v[68:71], v1 offset:21504\nv_cvt_pk_bf16_f32 v92, v80, v81\nv_smfmac_f32_16x16x64_bf16 v[52:55], v[44:47], a[112:119], v10\ns_waitcnt lgkmcnt(2)\nv_cvt_pk_bf16_f32 v93, v82, v83\n" \
"v_smfmac_f32_16x16x64_bf16 v[48:51], v[72:75], v[56:63], v10\nv_cvt_pk_bf16_f32 v94, v84, v85\nv_smfmac_f32_16x16x64_bf16 v[52:55], v[76:79], v[56:63], v10\ns_add_u32 s60, s55, 4096\nv_cvt_pk_bf16_f32 v95, v86, v87\nv_smfmac_f32_16x16x64_bf16 v[48:51], v[16:19], a[136:143], v10\nds_read_b128 v[56:59], v1 offset:10240\nv_and_or_b32 v92, v92, -2, v13\n" \
"v_smfmac_f32_16x16x64_bf16 v[52:55], v[20:23], a[136:143], v10\nds_read_b128 v[60:63], v1 offset:11264\nv_and_or_b32 v94, v94, -2, v13\nv_smfmac_f32_16x16x64_bf16 v[48:51], v[24:27], a[152:159], v10\nbuffer_store_dwordx4 v[92:95], v7, s[36:39], s60 offen offset:512 sc1\nv_mov_b64 v[80:81], 0\nv_mov_b64 v[82:83], 0\nv_mov_b64 v[84:85], 0\n" \
"v_mov_b64 v[86:87], 0\nv_smfmac_f32_16x16x64_bf16 v[52:55], v[28:31], a[152:159], v10\nv_smfmac_f32_16x16x64_bf16 v[80:83], v[40:43], a[168:175], v10\nv_cvt_pk_bf16_f32 v88, v48, v49\nv_smfmac_f32_16x16x64_bf16 v[84:87], v[44:47], a[168:175], v10\nv_cvt_pk_bf16_f32 v89, v50, v51\nv_smfmac_f32_16x16x64_bf16 v[80:83], v[72:75], a[176:183], v10\nv_cvt_pk_bf16_f32 v90, v52, v53\n" \
"v_smfmac_f32_16x16x64_bf16 v[84:87], v[76:79], a[176:183], v10\ns_add_u32 s60, s55, 16384\ns_waitcnt lgkmcnt(2)\nv_cvt_pk_bf16_f32 v91, v54, v55\nv_smfmac_f32_16x16x64_bf16 v[80:83], v[16:19], v[64:71], v10\nv_and_or_b32 v88, v88, -2, v13\nv_smfmac_f32_16x16x64_bf16 v[84:87], v[20:23], v[64:71], v10\nv_and_or_b32 v90, v90, -2, v13\n" \
"v_smfmac_f32_16x16x64_bf16 v[80:83], v[24:27], a[200:207], v10\nbuffer_store_dwordx4 v[88:91], v7, s[36:39], s60 offen offset:512 sc1\nv_mov_b64 v[48:49], 0\nv_mov_b64 v[50:51], 0\nv_mov_b64 v[52:53], 0\nv_mov_b64 v[54:55], 0\nv_smfmac_f32_16x16x64_bf16 v[84:87], v[28:31], a[200:207], v10\nds_read_b128 v[64:67], v1 offset:18432\n" \
"v_smfmac_f32_16x16x64_bf16 v[48:51], v[40:43], a[216:223], v10\nds_read_b128 v[68:71], v1 offset:19456\nv_cvt_pk_bf16_f32 v92, v80, v81\nv_smfmac_f32_16x16x64_bf16 v[52:55], v[44:47], a[216:223], v10\ns_waitcnt lgkmcnt(2)\nv_cvt_pk_bf16_f32 v93, v82, v83\nv_smfmac_f32_16x16x64_bf16 v[48:51], v[72:75], v[56:63], v10\nv_cvt_pk_bf16_f32 v94, v84, v85\n" \
"v_smfmac_f32_16x16x64_bf16 v[52:55], v[76:79], v[56:63], v10\ns_add_u32 s60, s55, 20480\nv_cvt_pk_bf16_f32 v95, v86, v87\nv_smfmac_f32_16x16x64_bf16 v[48:51], v[16:19], a[232:239], v10\nds_read_b128 v[56:59], v1 offset:14336\nv_and_or_b32 v92, v92, -2, v13\nv_smfmac_f32_16x16x64_bf16 v[52:55], v[20:23], a[232:239], v10\nds_read_b128 v[60:63], v1 offset:15360\n" \
"v_and_or_b32 v94, v94, -2, v13\nv_smfmac_f32_16x16x64_bf16 v[48:51], v[24:27], a[248:255], v10\nbuffer_store_dwordx4 v[92:95], v7, s[36:39], s60 offen offset:512 sc1\nv_mov_b64 v[80:81], 0\nv_mov_b64 v[82:83], 0\nv_mov_b64 v[84:85], 0\nv_mov_b64 v[86:87], 0\nv_smfmac_f32_16x16x64_bf16 v[52:55], v[28:31], a[248:255], v10\n" \
"v_smfmac_f32_16x16x64_bf16 v[80:83], v[40:43], v[104:111], v10\nv_cvt_pk_bf16_f32 v88, v48, v49\nv_smfmac_f32_16x16x64_bf16 v[84:87], v[44:47], v[104:111], v10\nv_cvt_pk_bf16_f32 v89, v50, v51\nv_smfmac_f32_16x16x64_bf16 v[80:83], v[72:75], v[120:127], v10\nv_cvt_pk_bf16_f32 v90, v52, v53\nv_smfmac_f32_16x16x64_bf16 v[84:87], v[76:79], v[120:127], v10\ns_add_u32 s60, s55, 0\n" \
"s_waitcnt lgkmcnt(2)\nv_cvt_pk_bf16_f32 v91, v54, v55\nv_smfmac_f32_16x16x64_bf16 v[80:83], v[16:19], v[64:71], v10\nv_and_or_b32 v88, v88, -2, v13\nv_smfmac_f32_16x16x64_bf16 v[84:87], v[20:23], v[64:71], v10\nv_and_or_b32 v90, v90, -2, v13\nv_smfmac_f32_16x16x64_bf16 v[80:83], v[24:27], v[144:151], v10\nbuffer_store_dwordx4 v[88:91], v7, s[36:39], s60 offen offset:2560 sc1\n" \
"v_mov_b64 v[48:49], 0\nv_mov_b64 v[50:51], 0\nv_mov_b64 v[52:53], 0\nv_mov_b64 v[54:55], 0\nv_smfmac_f32_16x16x64_bf16 v[84:87], v[28:31], v[144:151], v10\nds_read_b128 v[64:67], v1 offset:22528\nv_smfmac_f32_16x16x64_bf16 v[48:51], v[40:43], v[160:167], v10\nds_read_b128 v[68:71], v1 offset:23552\n" \
"v_cvt_pk_bf16_f32 v92, v80, v81\nv_smfmac_f32_16x16x64_bf16 v[52:55], v[44:47], v[160:167], v10\ns_waitcnt lgkmcnt(2)\nv_cvt_pk_bf16_f32 v93, v82, v83\nv_smfmac_f32_16x16x64_bf16 v[48:51], v[72:75], v[56:63], v10\nv_cvt_pk_bf16_f32 v94, v84, v85\nv_smfmac_f32_16x16x64_bf16 v[52:55], v[76:79], v[56:63], v10\ns_add_u32 s60, s55, 4096\n" \
"v_cvt_pk_bf16_f32 v95, v86, v87\nv_smfmac_f32_16x16x64_bf16 v[48:51], v[16:19], v[176:183], v10\nv_and_or_b32 v92, v92, -2, v13\nv_smfmac_f32_16x16x64_bf16 v[52:55], v[20:23], v[176:183], v10\nv_and_or_b32 v94, v94, -2, v13\nv_smfmac_f32_16x16x64_bf16 v[48:51], v[24:27], v[192:199], v10\nbuffer_store_dwordx4 v[92:95], v7, s[36:39], s60 offen offset:2560 sc1\nv_mov_b64 v[80:81], 0\n" \
"v_mov_b64 v[82:83], 0\nv_mov_b64 v[84:85], 0\nv_mov_b64 v[86:87], 0\nv_smfmac_f32_16x16x64_bf16 v[52:55], v[28:31], v[192:199], v10\nds_read_b128 v[32:35], v2 offset:0\nv_smfmac_f32_16x16x64_bf16 v[80:83], v[40:43], v[208:215], v10\nds_read_b128 v[36:39], v2 offset:256\nv_cvt_pk_bf16_f32 v88, v48, v49\n" \
"v_smfmac_f32_16x16x64_bf16 v[84:87], v[44:47], v[208:215], v10\nv_cvt_pk_bf16_f32 v89, v50, v51\nv_smfmac_f32_16x16x64_bf16 v[80:83], v[72:75], v[224:231], v10\nds_read_b128 v[40:43], v2 offset:2048\nv_cvt_pk_bf16_f32 v90, v52, v53\nv_smfmac_f32_16x16x64_bf16 v[84:87], v[76:79], v[224:231], v10\ns_add_u32 s60, s55, 16384\ns_waitcnt lgkmcnt(3)\n" \
"ds_read_b128 v[44:47], v2 offset:2304\nv_cvt_pk_bf16_f32 v91, v54, v55\nv_smfmac_f32_16x16x64_bf16 v[80:83], v[16:19], v[64:71], v10\nv_and_or_b32 v88, v88, -2, v13\nv_smfmac_f32_16x16x64_bf16 v[84:87], v[20:23], v[64:71], v10\nv_and_or_b32 v90, v90, -2, v13\nv_smfmac_f32_16x16x64_bf16 v[80:83], v[24:27], v[248:255], v10\nbuffer_store_dwordx4 v[88:91], v7, s[36:39], s60 offen offset:2560 sc1\n" \
"v_mov_b64 v[48:49], 0\nv_mov_b64 v[50:51], 0\nv_mov_b64 v[52:53], 0\nv_mov_b64 v[54:55], 0\nv_smfmac_f32_16x16x64_bf16 v[84:87], v[28:31], v[248:255], v10\ns_add_u32 s60, s55, 20480\ns_cmp_eq_u64 s[64:65], 0\nv_mov_b32 v15, s40\n" \
"s_cbranch_scc1 .Lvu_tok%=\nv_cvt_pk_bf16_f32 v92, v80, v81\nv_cvt_pk_bf16_f32 v93, v82, v83\ns_nop 1\nv_cvt_pk_bf16_f32 v94, v84, v85\nv_cvt_pk_bf16_f32 v95, v86, v87\nv_and_or_b32 v92, v92, -2, v13\nv_and_or_b32 v94, v94, -2, v13\n" \
"buffer_store_dwordx4 v[92:95], v7, s[36:39], s60 offen offset:2560 sc1\ns_mov_b32 s60, -2147483648\ns_cmp_lt_i32 s40, 63\ns_cbranch_scc1 .Lvu_f0%=\ns_mov_b32 s61, -2147483648\nv_add_u32 v14, s57, v0\ns_waitcnt lgkmcnt(0)\n.Lvu_nov%=:\n" \
"s_add_u32 m0, s57, 16384\ns_nop 0\nbuffer_load_dwordx4 v3, s[36:39], s42 offen offset:0 sc1 lds\nbuffer_load_dwordx4 v3, s[36:39], s42 offen offset:1024 sc1 lds\nbuffer_load_dwordx4 v3, s[36:39], s42 offen offset:2048 sc1 lds\nbuffer_load_dwordx4 v3, s[36:39], s42 offen offset:3072 sc1 lds\ns_waitcnt vmcnt(0)\nds_read_b128 v[80:83], v14 offset:16384\n" \
"ds_read_b128 v[84:87], v14 offset:17408\nds_read_b128 v[88:91], v14 offset:18432\nds_read_b128 v[92:95], v14 offset:19456\ns_waitcnt lgkmcnt(2)\nv_max3_u32 v72, v80, v82, v84\ns_waitcnt lgkmcnt(1)\nv_max3_u32 v72, v72, v86, v88\ns_waitcnt lgkmcnt(0)\n" \
"v_max3_u32 v72, v72, v90, v92\nv_max_u32 v72, v72, v94\nv_cmp_eq_u32 vcc, -1, v72\ns_cbranch_vccz .Lvu_novfok%=\ns_add_u32 s43, s43, 1\ns_cmp_lt_u32 s43, 262144\ns_cbranch_scc1 .Lvu_nov%=\ns_branch .Lvu_abort%=\n" \
".Lvu_novfok%=:\ns_waitcnt lgkmcnt(0)\ns_barrier\ns_memrealtime s[68:69]\nds_read_b128 v[56:59], v1 offset:0\nds_read_b128 v[60:63], v1 offset:1024\nds_read_b128 v[80:83], v0 offset:16384\nds_read_b128 v[84:87], v0 offset:17408\n" \
"ds_read_b128 v[88:91], v0 offset:18432\nds_read_b128 v[92:95], v0 offset:19456\nds_read_b128 v[16:19], v0 offset:20480\nds_read_b128 v[20:23], v0 offset:21504\nds_read_b128 v[32:35], v0 offset:22528\nds_read_b128 v[36:39], v0 offset:23552\ns_waitcnt lgkmcnt(0)\ns_branch .Lvu_crit%=\n" \
".Lvu_re%=:\n.Lvu_re2%=:\ns_add_u32 m0, s57, 16384\nv_max_u32 v72, v80, v82\nv_cmp_eq_u32 vcc, -1, v72\ns_cbranch_vccz .Lvu_sk1888%=\ns_mov_b64 exec, vcc\nbuffer_load_dwordx4 v3, s[36:39], s42 offen offset:0 sc1 lds\n" \
"s_mov_b64 exec, -1\n.Lvu_sk1888%=:\nv_max_u32 v72, v84, v86\nv_cmp_eq_u32 vcc, -1, v72\ns_cbranch_vccz .Lvu_sk1895%=\ns_mov_b64 exec, vcc\nbuffer_load_dwordx4 v3, s[36:39], s42 offen offset:1024 sc1 lds\ns_mov_b64 exec, -1\n" \
".Lvu_sk1895%=:\nv_max_u32 v72, v88, v90\nv_cmp_eq_u32 vcc, -1, v72\ns_cbranch_vccz .Lvu_sk1902%=\ns_mov_b64 exec, vcc\nbuffer_load_dwordx4 v3, s[36:39], s42 offen offset:2048 sc1 lds\ns_mov_b64 exec, -1\n.Lvu_sk1902%=:\n" \
"v_max_u32 v72, v92, v94\nv_cmp_eq_u32 vcc, -1, v72\ns_cbranch_vccz .Lvu_sk1909%=\ns_mov_b64 exec, vcc\nbuffer_load_dwordx4 v3, s[36:39], s42 offen offset:3072 sc1 lds\ns_mov_b64 exec, -1\n.Lvu_sk1909%=:\n.Lvu_skf%=:\n" \
"s_waitcnt vmcnt(0)\nds_read_b128 v[80:83], v14 offset:16384\nds_read_b128 v[84:87], v14 offset:17408\nds_read_b128 v[88:91], v14 offset:18432\nds_read_b128 v[92:95], v14 offset:19456\ns_waitcnt lgkmcnt(2)\nv_max3_u32 v72, v80, v82, v84\ns_waitcnt lgkmcnt(1)\n" \
"v_max3_u32 v72, v72, v86, v88\ns_waitcnt lgkmcnt(0)\nv_max3_u32 v72, v72, v90, v92\nv_max_u32 v72, v72, v94\nv_cmp_eq_u32 vcc, -1, v72\ns_cbranch_vccz .Lvu_rer%=\ns_add_u32 s43, s43, 1\ns_cmp_lt_u32 s43, 262144\n" \
"s_cbranch_scc1 .Lvu_re2%=\ns_branch .Lvu_abort%=\n.Lvu_rew%=:\nds_read_b128 v[80:83], v14 offset:16384\nds_read_b128 v[84:87], v14 offset:17408\nds_read_b128 v[88:91], v14 offset:18432\nds_read_b128 v[92:95], v14 offset:19456\ns_waitcnt lgkmcnt(0)\n" \
"s_branch .Lvu_rer%=\n.Lvu_first%=:\ns_nop 7\nv_cvt_pk_bf16_f32 v76, v24, v25\nv_cvt_pk_bf16_f32 v77, v26, v27\nv_cvt_pk_bf16_f32 v78, v28, v29\nv_cvt_pk_bf16_f32 v79, v30, v31\nds_write2st64_b64 v9, v[76:77], v[78:79] offset0:2 offset1:10\n" \
"s_waitcnt vmcnt(0) lgkmcnt(0)\ns_barrier\nds_read_b128 v[56:59], v1 offset:8192\nds_read_b128 v[60:63], v1 offset:9216\nds_read_b128 v[64:67], v1 offset:16384\nds_read_b128 v[68:71], v1 offset:17408\nds_read_b128 v[72:75], v11 offset:49152\nds_read_b128 v[76:79], v11 offset:50176\n" \
"ds_read_b128 v[16:19], v11 offset:51200\nds_read_b128 v[20:23], v11 offset:52224\nds_read_b128 v[24:27], v11 offset:53248\nds_read_b128 v[28:31], v11 offset:54272\nds_read_b128 v[32:35], v11 offset:55296\nds_read_b128 v[36:39], v11 offset:56320\ns_waitcnt lgkmcnt(0)\ns_add_u32 m0, s57, 0\n" \
"s_add_u32 s58, s61, 0\nbuffer_load_dwordx4 v0, s[36:39], s58 offen offset:0 sc1 lds\nbuffer_load_dwordx4 v0, s[36:39], s58 offen offset:1024 sc1 lds\nbuffer_load_dwordx4 v0, s[36:39], s58 offen offset:2048 sc1 lds\nbuffer_load_dwordx4 v0, s[36:39], s58 offen offset:3072 sc1 lds\ns_mov_b32 s59, -2147483648\nbuffer_store_dword v14, off, s[36:39], s59\nbuffer_store_dword v14, off, s[36:39], s59\n" \
"buffer_store_dword v14, off, s[36:39], s59\ns_branch .Lvu_y%=\n.Lvu_last%=:\ns_nop 7\nv_cvt_pk_bf16_f32 v94, v44, v45\nv_cvt_pk_bf16_f32 v95, v46, v47\nv_and_or_b32 v94, v94, -2, v12\ns_nop 0\n" \
"v_permlane16_swap_b32 v93, v95\nv_permlane16_swap_b32 v92, v94\ns_add_u32 s63, s59, 135168\nbuffer_store_dwordx4 v[92:95], v6, s[36:39], s63 offen sc1\ns_branch .Lvu_end%=\n.Lvu_abort%=:\ns_mov_b64 exec, -1\nv_mov_b32 v16, 0\n" \
"s_movk_i32 s44, 6400\nbuffer_store_dword v16, off, s[36:39], s44 sc1\n.Lvu_end%=:\ns_waitcnt vmcnt(0) lgkmcnt(0)\n" \
""
#define RD_ASM \
"v_mbcnt_lo_u32_b32 v255, -1, 0\nv_mbcnt_hi_u32_b32 v255, -1, v255\nv_lshlrev_b32 v255, 2, v255\nv_add_u32 v255, %[tb], v255\nds_read_b32 v196, v255 offset:0\nds_read_b32 v197, v255 offset:256\nds_read_b32 v198, v255 offset:512\nds_read_b32 v199, v255 offset:768\n" \
"ds_read_b32 v200, v255 offset:1024\nds_read_b32 v201, v255 offset:1280\nds_read_b32 v202, v255 offset:1536\nds_read_b32 v203, v255 offset:1792\nds_read_b32 v204, v255 offset:2048\nds_read_b32 v205, v255 offset:2304\nds_read_b32 v206, v255 offset:2560\nds_read_b32 v207, v255 offset:2816\n" \
"s_waitcnt lgkmcnt(0)\nv_accvgpr_write_b32 a32, v206\nv_accvgpr_write_b32 a33, v207\ns_mov_b32 s40, 0\ns_mov_b32 s43, 0\ns_add_u32 s57, %[su], 4096\ns_add_u32 s56, %[sp], 4096\ns_mov_b32 s41, %[sy]\n" \
".Lrd_yflag%=:\nv_accvgpr_read_b32 v206, a32\nbuffer_load_dword v208, v206, %[rs], 0 offen sc1\ns_waitcnt vmcnt(0)\nv_cmp_gt_i32 vcc, s40, v208\ns_cbranch_vccz .Lrd_yld%=\ns_add_u32 s43, s43, 1\ns_cmp_lt_u32 s43, 262144\n" \
"s_cbranch_scc1 .Lrd_yflag%=\ns_branch .Lrd_abort%=\n.Lrd_yld%=:\ns_add_u32 s60, s41, 0\nbuffer_load_dwordx4 v[0:3], v196, %[rs], s60 offen offset:0 sc1\ns_add_u32 s60, s41, 0\nbuffer_load_dwordx4 v[32:35], v196, %[rs], s60 offen offset:1024 sc1\ns_add_u32 s60, s41, 0\n" \
"buffer_load_dwordx4 v[64:67], v196, %[rs], s60 offen offset:2048 sc1\ns_add_u32 s60, s41, 131072\nbuffer_load_dwordx4 v[4:7], v196, %[rs], s60 offen offset:0 sc1\ns_add_u32 s60, s41, 131072\nbuffer_load_dwordx4 v[36:39], v196, %[rs], s60 offen offset:1024 sc1\ns_add_u32 s60, s41, 131072\nbuffer_load_dwordx4 v[68:71], v196, %[rs], s60 offen offset:2048 sc1\ns_add_u32 s60, s41, 262144\n" \
"buffer_load_dwordx4 v[8:11], v196, %[rs], s60 offen offset:0 sc1\ns_add_u32 s60, s41, 262144\nbuffer_load_dwordx4 v[40:43], v196, %[rs], s60 offen offset:1024 sc1\ns_add_u32 s60, s41, 262144\nbuffer_load_dwordx4 v[72:75], v196, %[rs], s60 offen offset:2048 sc1\ns_add_u32 s60, s41, 393216\nbuffer_load_dwordx4 v[12:15], v196, %[rs], s60 offen offset:0 sc1\ns_add_u32 s60, s41, 393216\n" \
"buffer_load_dwordx4 v[44:47], v196, %[rs], s60 offen offset:1024 sc1\ns_add_u32 s60, s41, 393216\nbuffer_load_dwordx4 v[76:79], v196, %[rs], s60 offen offset:2048 sc1\ns_add_u32 s60, s41, 524288\nbuffer_load_dwordx4 v[16:19], v196, %[rs], s60 offen offset:0 sc1\ns_add_u32 s60, s41, 524288\nbuffer_load_dwordx4 v[48:51], v196, %[rs], s60 offen offset:1024 sc1\ns_add_u32 s60, s41, 524288\n" \
"buffer_load_dwordx4 v[80:83], v196, %[rs], s60 offen offset:2048 sc1\ns_add_u32 s60, s41, 655360\nbuffer_load_dwordx4 v[20:23], v196, %[rs], s60 offen offset:0 sc1\ns_add_u32 s60, s41, 655360\nbuffer_load_dwordx4 v[52:55], v196, %[rs], s60 offen offset:1024 sc1\ns_add_u32 s60, s41, 655360\nbuffer_load_dwordx4 v[84:87], v196, %[rs], s60 offen offset:2048 sc1\ns_add_u32 s60, s41, 786432\n" \
"buffer_load_dwordx4 v[24:27], v196, %[rs], s60 offen offset:0 sc1\ns_add_u32 s60, s41, 786432\nbuffer_load_dwordx4 v[56:59], v196, %[rs], s60 offen offset:1024 sc1\ns_add_u32 s60, s41, 786432\nbuffer_load_dwordx4 v[88:91], v196, %[rs], s60 offen offset:2048 sc1\ns_add_u32 s60, s41, 917504\nbuffer_load_dwordx4 v[28:31], v196, %[rs], s60 offen offset:0 sc1\ns_add_u32 s60, s41, 917504\n" \
"buffer_load_dwordx4 v[60:63], v196, %[rs], s60 offen offset:1024 sc1\ns_add_u32 s60, s41, 917504\nbuffer_load_dwordx4 v[92:95], v196, %[rs], s60 offen offset:2048 sc1\ns_add_u32 s60, s41, 0\nbuffer_load_dwordx4 v[96:99], v196, %[rs], s60 offen offset:3072 sc1\ns_add_u32 s60, s41, 131072\nbuffer_load_dwordx4 v[100:103], v196, %[rs], s60 offen offset:3072 sc1\ns_add_u32 s60, s41, 262144\n" \
"buffer_load_dwordx4 v[104:107], v196, %[rs], s60 offen offset:3072 sc1\ns_add_u32 s60, s41, 393216\nbuffer_load_dwordx4 v[108:111], v196, %[rs], s60 offen offset:3072 sc1\ns_add_u32 s60, s41, 524288\nbuffer_load_dwordx4 v[112:115], v196, %[rs], s60 offen offset:3072 sc1\ns_add_u32 s60, s41, 655360\nbuffer_load_dwordx4 v[116:119], v196, %[rs], s60 offen offset:3072 sc1\ns_add_u32 s60, s41, 786432\n" \
"buffer_load_dwordx4 v[120:123], v196, %[rs], s60 offen offset:3072 sc1\ns_add_u32 s60, s41, 917504\nbuffer_load_dwordx4 v[124:127], v196, %[rs], s60 offen offset:3072 sc1\ns_mov_b64 exec, -1\nv_accvgpr_read_b32 v212, a33\nbuffer_load_dwordx2 v[214:215], v212, %[rs], 0 offen sc1\ns_waitcnt vmcnt(0)\ns_branch .Lrd_ychk%=\n" \
".Lrd_u2%=:\nbuffer_load_dwordx4 v[136:139], v196, %[rs], %[su] offen offset:2048 sc1\nbuffer_load_dwordx4 v[140:143], v196, %[rs], %[su] offen offset:3072 sc1\ns_add_u32 s43, s43, 1\ns_cmp_ge_u32 s43, 262144\ns_cbranch_scc1 .Lrd_abort%=\ns_waitcnt vmcnt(0)\nv_bitop3_b32 v208, v136, v138, s54 bitop3:0x7e\n" \
"v_bitop3_b32 v209, v140, v142, s54 bitop3:0x7e\nv_bitop3_b32 v208, v208, 1, v209 bitop3:0xc8\nv_cmp_ne_u32 vcc, 0, v208\ns_cbranch_vccnz .Lrd_u2%=\n.Lrd_u3%=:\nbuffer_load_dwordx4 v[128:131], v196, %[rs], %[su] offen offset:0 sc1\nbuffer_load_dwordx4 v[132:135], v196, %[rs], %[su] offen offset:1024 sc1\nbuffer_load_dwordx4 v[144:147], v196, %[rs], s57 offen offset:0 sc1\n" \
"buffer_load_dwordx4 v[148:151], v196, %[rs], s57 offen offset:1024 sc1\nbuffer_load_dwordx4 v[152:155], v196, %[rs], s57 offen offset:2048 sc1\nbuffer_load_dwordx4 v[156:159], v196, %[rs], s57 offen offset:3072 sc1\nbuffer_load_dwordx4 v[160:163], v196, %[rs], %[sp] offen offset:0 sc1\nbuffer_load_dwordx4 v[164:167], v196, %[rs], %[sp] offen offset:1024 sc1\nbuffer_load_dwordx4 v[168:171], v196, %[rs], %[sp] offen offset:2048 sc1\nbuffer_load_dwordx4 v[172:175], v196, %[rs], %[sp] offen offset:3072 sc1\nbuffer_load_dwordx4 v[176:179], v196, %[rs], s56 offen offset:0 sc1\n" \
".Lrd_ra%=:\ns_waitcnt vmcnt(5)\nv_bitop3_b32 v208, v128, v130, s54 bitop3:0x7e\nv_bitop3_b32 v209, v136, v138, s54 bitop3:0x7e\nv_bitop3_b32 v208, v208, 1, v209 bitop3:0xc8\nv_cmp_ne_u32 s[86:87], 0, v208\nv_bitop3_b32 v208, v144, v146, s54 bitop3:0x7e\nv_bitop3_b32 v209, v152, v154, s54 bitop3:0x7e\n" \
"v_bitop3_b32 v208, v208, 1, v209 bitop3:0xc8\nv_cmp_ne_u32 s[88:89], 0, v208\nv_bitop3_b32 v208, v132, v134, s54 bitop3:0x7e\nv_bitop3_b32 v209, v140, v142, s54 bitop3:0x7e\nv_bitop3_b32 v208, v208, 1, v209 bitop3:0xc8\nv_cmp_ne_u32 s[68:69], 0, v208\nv_bitop3_b32 v208, v148, v150, s54 bitop3:0x7e\nv_bitop3_b32 v209, v156, v158, s54 bitop3:0x7e\n" \
"v_bitop3_b32 v208, v208, 1, v209 bitop3:0xc8\nv_cmp_ne_u32 s[62:63], 0, v208\ns_or_b64 vcc, s[86:87], s[88:89]\ns_or_b64 vcc, vcc, s[68:69]\ns_or_b64 vcc, vcc, s[62:63]\ns_waitcnt vmcnt(4)\nv_smfmac_f32_16x16x64_bf16 v[180:183], v[160:163], v[0:7], v197\nv_bitop3_b32 v208, v160, v162, s54 bitop3:0x7e\n" \
"v_smfmac_f32_16x16x64_bf16 v[184:187], v[160:163], v[32:39], v197\nv_cmp_gt_i32 s[46:47], 0, v208\nv_smfmac_f32_16x16x64_bf16 v[188:191], v[160:163], v[64:71], v197\nv_smfmac_f32_16x16x64_bf16 v[192:195], v[160:163], v[96:103], v197\ns_or_b64 vcc, vcc, s[46:47]\ns_waitcnt vmcnt(3)\nv_smfmac_f32_16x16x64_bf16 v[180:183], v[164:167], v[8:15], v197\nv_bitop3_b32 v208, v164, v166, s54 bitop3:0x7e\n" \
"v_smfmac_f32_16x16x64_bf16 v[184:187], v[164:167], v[40:47], v197\nv_cmp_gt_i32 s[48:49], 0, v208\nv_smfmac_f32_16x16x64_bf16 v[188:191], v[164:167], v[72:79], v197\nv_smfmac_f32_16x16x64_bf16 v[192:195], v[164:167], v[104:111], v197\ns_or_b64 vcc, vcc, s[48:49]\ns_waitcnt vmcnt(2)\nv_smfmac_f32_16x16x64_bf16 v[180:183], v[168:171], v[16:23], v197\nv_bitop3_b32 v208, v168, v170, s54 bitop3:0x7e\n" \
"v_smfmac_f32_16x16x64_bf16 v[184:187], v[168:171], v[48:55], v197\nv_cmp_gt_i32 s[50:51], 0, v208\nv_smfmac_f32_16x16x64_bf16 v[188:191], v[168:171], v[80:87], v197\nv_smfmac_f32_16x16x64_bf16 v[192:195], v[168:171], v[112:119], v197\ns_or_b64 vcc, vcc, s[50:51]\ns_waitcnt vmcnt(1)\nv_smfmac_f32_16x16x64_bf16 v[180:183], v[172:175], v[24:31], v197\ns_waitcnt vmcnt(0)\n" \
"v_bitop3_b32 v208, v172, v174, s54 bitop3:0x7e\nv_smfmac_f32_16x16x64_bf16 v[180:183], v[176:179], v[128:135], v197\nv_bitop3_b32 v209, v176, v178, s54 bitop3:0x7e\nv_smfmac_f32_16x16x64_bf16 v[184:187], v[172:175], v[56:63], v197\nv_bitop3_b32 v210, v177, v179, s54 bitop3:0x7e\nv_smfmac_f32_16x16x64_bf16 v[184:187], v[176:179], v[136:143], v197\nv_cmp_gt_i32 s[52:53], 0, v208\nv_smfmac_f32_16x16x64_bf16 v[188:191], v[172:175], v[88:95], v197\n" \
"v_or_b32 v209, v209, v210\nv_smfmac_f32_16x16x64_bf16 v[188:191], v[176:179], v[144:151], v197\ns_or_b64 vcc, vcc, s[52:53]\nv_cmp_gt_i32 s[64:65], 0, v209\nv_smfmac_f32_16x16x64_bf16 v[192:195], v[172:175], v[120:127], v197\nv_smfmac_f32_16x16x64_bf16 v[192:195], v[176:179], v[152:159], v197\ns_or_b64 vcc, vcc, s[64:65]\ns_cbranch_scc1 .Lrd_rb%=\n" \
"s_add_u32 s60, s58, 16384\nv_add_f32 v180, v180, v182\nv_add_f32 v181, v181, v183\nds_write2_b32 v198, v180, v181 offset0:0 offset1:8\nbuffer_load_dwordx4 v[212:215], v196, %[rs], s60 offen offset:0 sc1\nv_add_f32 v184, v184, v186\nv_add_f32 v185, v185, v187\nds_write2_b32 v198, v184, v185 offset0:32 offset1:40\n" \
"s_add_u32 s60, s58, 0\nbuffer_load_dwordx4 v[216:219], v196, %[rs], s60 offen offset:0 sc1\nv_add_f32 v188, v188, v190\nv_add_f32 v189, v189, v191\nds_write2_b32 v198, v188, v189 offset0:64 offset1:72\ns_add_u32 s60, s58, 0\nbuffer_load_dwordx4 v[220:223], v196, %[rs], s60 offen offset:1024 sc1\nv_add_f32 v192, v192, v194\n" \
"v_add_f32 v193, v193, v195\nds_write2_b32 v198, v192, v193 offset0:96 offset1:104\ns_add_u32 s60, s58, 16384\nbuffer_load_dwordx4 v[224:227], v196, %[rs], s60 offen offset:1024 sc1\ns_waitcnt lgkmcnt(0)\ns_barrier\nds_read_b128 v[180:183], v199 offset:0\nds_read_b128 v[184:187], v199 offset:4096\n" \
"ds_read_b128 v[188:191], v199 offset:8192\nds_read_b128 v[192:195], v199 offset:12288\ns_and_b32 s44, s54, 0x80008000\ns_add_u32 s60, s58, 0\nbuffer_load_dwordx4 v[228:231], v196, %[rs], s60 offen offset:2048 sc1\ns_waitcnt lgkmcnt(2)\nv_pk_add_f32 v[182:183], v[182:183], v[186:187]\nv_pk_add_f32 v[180:181], v[180:181], v[184:185]\n" \
"s_waitcnt lgkmcnt(0)\nv_pk_add_f32 v[186:187], v[188:189], v[192:193]\nv_pk_add_f32 v[184:185], v[190:191], v[194:195]\nv_pk_add_f32 v[180:181], v[180:181], v[186:187]\nv_pk_add_f32 v[182:183], v[182:183], v[184:185]\nv_cvt_pk_bf16_f32 v180, v180, v181\nv_xor_b32 v180, s44, v180\nv_cvt_pk_bf16_f32 v181, v182, v183\n" \
"v_min_u32 v180, -2, v180\nv_xor_b32 v209, s44, v181\nv_or_b32 v208, v180, v201\nbuffer_store_dwordx2 v[208:209], v206, %[rs], 0 offen sc1\ns_add_u32 s60, s58, 0\nbuffer_load_dwordx4 v[232:235], v196, %[rs], s60 offen offset:3072 sc1\ns_add_u32 s60, s58, 16384\nbuffer_load_dwordx4 v[236:239], v196, %[rs], s60 offen offset:2048 sc1\n" \
"s_add_u32 s60, s58, 4096\nbuffer_load_dwordx4 v[240:243], v196, %[rs], s60 offen offset:0 sc1\ns_add_u32 s60, s58, 4096\nbuffer_load_dwordx4 v[244:247], v196, %[rs], s60 offen offset:1024 sc1\ns_add_u32 s60, s58, 16384\nbuffer_load_dwordx4 v[248:251], v196, %[rs], s60 offen offset:3072 sc1\ns_add_u32 s60, s58, 20480\nbuffer_load_dwordx4 v[252:255], v196, %[rs], s60 offen offset:0 sc1\n" \
"s_add_u32 s60, s58, 20480\nbuffer_load_dwordx4 v[160:163], v196, %[rs], s60 offen offset:2048 sc1\ns_xor_b32 s41, s41, 4194304\nv_mov_b32 v207, s40\ns_waitcnt vmcnt(12)\nv_bitop3_b32 v209, v212, v214, s61 bitop3:0x7e\nv_cmp_gt_i32 vcc, 0, v209\ns_cbranch_vccnz .Lrd_tre0%=\n" \
".Lrd_tl0%=:\nv_mfma_f32_16x16x16_bf16 a[0:3], v[0:1], v[212:213], 0\ns_add_u32 s60, s58, 4096\nbuffer_load_dwordx4 v[164:167], v196, %[rs], s60 offen offset:2048 sc1\nv_mfma_f32_16x16x16_bf16 a[8:11], v[32:33], v[212:213], 0\nv_mfma_f32_16x16x16_bf16 a[16:19], v[64:65], v[212:213], 0\nv_mfma_f32_16x16x16_bf16 a[24:27], v[96:97], v[212:213], 0\ns_waitcnt vmcnt(12)\n" \
"v_mfma_f32_16x16x16_bf16 a[0:3], v[4:5], v[214:215], a[0:3]\ns_add_u32 s60, s58, 4096\nbuffer_load_dwordx4 v[168:171], v196, %[rs], s60 offen offset:3072 sc1\nv_bitop3_b32 v209, v216, v218, s61 bitop3:0x7e\nv_mfma_f32_16x16x16_bf16 a[8:11], v[36:37], v[214:215], a[8:11]\ns_lshl_b32 s42, s40, 17\ns_add_u32 s42, s42, %[at]\nv_mfma_f32_16x16x16_bf16 a[16:19], v[68:69], v[214:215], a[16:19]\n" \
"v_cmp_gt_i32 vcc, 0, v209\nv_mfma_f32_16x16x16_bf16 a[24:27], v[100:101], v[214:215], a[24:27]\ns_cbranch_vccnz .Lrd_tre1%=\n.Lrd_tl1%=:\ns_waitcnt vmcnt(12)\nv_mfma_f32_16x16x32_bf16 a[4:7], v[0:3], v[216:219], 0\ns_add_u32 s60, s58, 20480\nbuffer_load_dwordx4 v[172:175], v196, %[rs], s60 offen offset:1024 sc1\n" \
"v_bitop3_b32 v209, v220, v222, s61 bitop3:0x7e\nv_mfma_f32_16x16x32_bf16 a[12:15], v[32:35], v[216:219], 0\ns_lshr_b32 s45, s42, 8\ns_and_b32 s45, s45, 0x2000\nv_mfma_f32_16x16x32_bf16 a[20:23], v[64:67], v[216:219], 0\nv_cmp_gt_i32 vcc, 0, v209\nv_mfma_f32_16x16x32_bf16 a[28:31], v[96:99], v[216:219], 0\ns_cbranch_vccnz .Lrd_tre2%=\n" \
".Lrd_tl2%=:\ns_waitcnt vmcnt(12)\nv_mfma_f32_16x16x32_bf16 a[4:7], v[4:7], v[220:223], a[4:7]\ns_add_u32 s60, s58, 20480\nbuffer_load_dwordx4 v[176:179], v196, %[rs], s60 offen offset:3072 sc1\nv_bitop3_b32 v209, v224, v226, s61 bitop3:0x7e\nv_mfma_f32_16x16x32_bf16 a[12:15], v[36:39], v[220:223], a[12:15]\ns_xor_b32 s42, s42, s45\n" \
"s_add_u32 s40, s40, 1\nv_mfma_f32_16x16x32_bf16 a[20:23], v[68:71], v[220:223], a[20:23]\nv_cmp_gt_i32 vcc, 0, v209\nv_mfma_f32_16x16x32_bf16 a[28:31], v[100:103], v[220:223], a[28:31]\ns_cbranch_vccnz .Lrd_tre3%=\n.Lrd_tl3%=:\nv_mfma_f32_16x16x16_bf16 a[0:3], v[8:9], v[224:225], a[0:3]\ns_cmp_eq_u32 s40, 1\n" \
"s_cbranch_scc1 .Lrd_y1%=\n.Lrd_y1r%=:\ns_add_u32 s60, s41, 0\nbuffer_load_dwordx4 v[0:3], v196, %[rs], s60 offen offset:0 sc1\nv_mfma_f32_16x16x16_bf16 a[8:11], v[40:41], v[224:225], a[8:11]\nv_mfma_f32_16x16x16_bf16 a[16:19], v[72:73], v[224:225], a[16:19]\nbuffer_load_dwordx4 v[212:215], v204, %[rs], s42 offen sc1\nv_mfma_f32_16x16x16_bf16 a[24:27], v[104:105], v[224:225], a[24:27]\n" \
"s_waitcnt vmcnt(14)\nv_mfma_f32_16x16x16_bf16 a[0:3], v[12:13], v[226:227], a[0:3]\ns_add_u32 s60, s41, 0\nbuffer_load_dwordx4 v[32:35], v196, %[rs], s60 offen offset:1024 sc1\nv_bitop3_b32 v209, v228, v230, s61 bitop3:0x7e\nv_mfma_f32_16x16x16_bf16 a[8:11], v[44:45], v[226:227], a[8:11]\ns_lshl_b32 s62, s55, 31\ns_or_b32 s62, s62, 1.0\n" \
"s_mov_b32 s63, s62\nv_mfma_f32_16x16x16_bf16 a[16:19], v[76:77], v[226:227], a[16:19]\nv_cmp_gt_i32 vcc, 0, v209\nv_mfma_f32_16x16x16_bf16 a[24:27], v[108:109], v[226:227], a[24:27]\ns_cbranch_vccnz .Lrd_tre4%=\n.Lrd_tl4%=:\ns_waitcnt vmcnt(13)\nv_mfma_f32_16x16x32_bf16 a[4:7], v[8:11], v[228:231], a[4:7]\n" \
"s_add_u32 s60, s41, 0\nbuffer_load_dwordx4 v[64:67], v196, %[rs], s60 offen offset:2048 sc1\nv_bitop3_b32 v209, v232, v234, s61 bitop3:0x7e\nv_mfma_f32_16x16x32_bf16 a[12:15], v[40:43], v[228:231], a[12:15]\nv_mfma_f32_16x16x32_bf16 a[20:23], v[72:75], v[228:231], a[20:23]\nv_cmp_gt_i32 vcc, 0, v209\nv_mfma_f32_16x16x32_bf16 a[28:31], v[104:107], v[228:231], a[28:31]\ns_cbranch_vccnz .Lrd_tre5%=\n" \
".Lrd_tl5%=:\ns_waitcnt vmcnt(13)\nv_mfma_f32_16x16x32_bf16 a[4:7], v[12:15], v[232:235], a[4:7]\ns_add_u32 s60, s41, 131072\nbuffer_load_dwordx4 v[4:7], v196, %[rs], s60 offen offset:0 sc1\nv_bitop3_b32 v209, v236, v238, s61 bitop3:0x7e\nv_mfma_f32_16x16x32_bf16 a[12:15], v[44:47], v[232:235], a[12:15]\nv_mfma_f32_16x16x32_bf16 a[20:23], v[76:79], v[232:235], a[20:23]\n" \
"v_cmp_gt_i32 vcc, 0, v209\nv_mfma_f32_16x16x32_bf16 a[28:31], v[108:111], v[232:235], a[28:31]\ns_cbranch_vccnz .Lrd_tre6%=\n.Lrd_tl6%=:\nv_mfma_f32_16x16x16_bf16 a[0:3], v[16:17], v[236:237], a[0:3]\ns_add_u32 s60, s41, 131072\nbuffer_load_dwordx4 v[36:39], v196, %[rs], s60 offen offset:1024 sc1\nv_mfma_f32_16x16x16_bf16 a[8:11], v[48:49], v[236:237], a[8:11]\n" \
"v_mfma_f32_16x16x16_bf16 a[16:19], v[80:81], v[236:237], a[16:19]\nv_mfma_f32_16x16x16_bf16 a[24:27], v[112:113], v[236:237], a[24:27]\ns_waitcnt vmcnt(14)\nv_mfma_f32_16x16x16_bf16 a[0:3], v[20:21], v[238:239], a[0:3]\ns_add_u32 s60, s41, 131072\nbuffer_load_dwordx4 v[68:71], v196, %[rs], s60 offen offset:2048 sc1\nv_bitop3_b32 v209, v240, v242, s61 bitop3:0x7e\nv_mfma_f32_16x16x16_bf16 a[8:11], v[52:53], v[238:239], a[8:11]\n" \
"v_mfma_f32_16x16x16_bf16 a[16:19], v[84:85], v[238:239], a[16:19]\nv_cmp_gt_i32 vcc, 0, v209\nv_mfma_f32_16x16x16_bf16 a[24:27], v[116:117], v[238:239], a[24:27]\ns_cbranch_vccnz .Lrd_tre7%=\n.Lrd_tl7%=:\ns_waitcnt vmcnt(14)\nv_mfma_f32_16x16x32_bf16 a[4:7], v[16:19], v[240:243], a[4:7]\ns_add_u32 s60, s41, 262144\n" \
"buffer_load_dwordx4 v[8:11], v196, %[rs], s60 offen offset:0 sc1\nv_bitop3_b32 v209, v244, v246, s61 bitop3:0x7e\nv_mfma_f32_16x16x32_bf16 a[12:15], v[48:51], v[240:243], a[12:15]\nv_mfma_f32_16x16x32_bf16 a[20:23], v[80:83], v[240:243], a[20:23]\nv_cmp_gt_i32 vcc, 0, v209\nv_mfma_f32_16x16x32_bf16 a[28:31], v[112:115], v[240:243], a[28:31]\ns_cbranch_vccnz .Lrd_tre8%=\n.Lrd_tl8%=:\n" \
"s_waitcnt vmcnt(14)\nv_mfma_f32_16x16x32_bf16 a[4:7], v[20:23], v[244:247], a[4:7]\ns_add_u32 s60, s41, 262144\nbuffer_load_dwordx4 v[40:43], v196, %[rs], s60 offen offset:1024 sc1\nv_bitop3_b32 v209, v248, v250, s61 bitop3:0x7e\nv_mfma_f32_16x16x32_bf16 a[12:15], v[52:55], v[244:247], a[12:15]\nv_mfma_f32_16x16x32_bf16 a[20:23], v[84:87], v[244:247], a[20:23]\nv_cmp_gt_i32 vcc, 0, v209\n" \
"v_mfma_f32_16x16x32_bf16 a[28:31], v[116:119], v[244:247], a[28:31]\ns_cbranch_vccnz .Lrd_tre9%=\n.Lrd_tl9%=:\nv_mfma_f32_16x16x16_bf16 a[0:3], v[24:25], v[248:249], a[0:3]\ns_add_u32 s60, s41, 262144\nbuffer_load_dwordx4 v[72:75], v196, %[rs], s60 offen offset:2048 sc1\nv_mfma_f32_16x16x16_bf16 a[8:11], v[56:57], v[248:249], a[8:11]\nv_mfma_f32_16x16x16_bf16 a[16:19], v[88:89], v[248:249], a[16:19]\n" \
"v_mfma_f32_16x16x16_bf16 a[24:27], v[120:121], v[248:249], a[24:27]\ns_waitcnt vmcnt(15)\nv_mfma_f32_16x16x16_bf16 a[0:3], v[28:29], v[250:251], a[0:3]\ns_add_u32 s60, s41, 393216\nbuffer_load_dwordx4 v[12:15], v196, %[rs], s60 offen offset:0 sc1\nv_bitop3_b32 v209, v252, v254, s61 bitop3:0x7e\nv_mfma_f32_16x16x16_bf16 a[8:11], v[60:61], v[250:251], a[8:11]\nv_bitop3_b32 v210, v253, v255, s61 bitop3:0x7e\n" \
"v_or_b32 v209, v209, v210\nv_mfma_f32_16x16x16_bf16 a[16:19], v[92:93], v[250:251], a[16:19]\nv_cmp_gt_i32 vcc, 0, v209\nv_mfma_f32_16x16x16_bf16 a[24:27], v[124:125], v[250:251], a[24:27]\ns_cbranch_vccnz .Lrd_tre10%=\n.Lrd_tl10%=:\ns_waitcnt vmcnt(15)\nv_mfma_f32_16x16x32_bf16 a[0:3], v[128:131], v[252:255], a[0:3]\n" \
"s_add_u32 s60, s41, 393216\nbuffer_load_dwordx4 v[44:47], v196, %[rs], s60 offen offset:1024 sc1\nv_bitop3_b32 v209, v160, v162, s61 bitop3:0x7e\nv_mfma_f32_16x16x32_bf16 a[8:11], v[136:139], v[252:255], a[8:11]\nv_bitop3_b32 v210, v161, v163, s61 bitop3:0x7e\nv_or_b32 v209, v209, v210\nv_mfma_f32_16x16x32_bf16 a[16:19], v[144:147], v[252:255], a[16:19]\nv_cmp_gt_i32 vcc, 0, v209\n" \
"v_mfma_f32_16x16x32_bf16 a[24:27], v[152:155], v[252:255], a[24:27]\ns_cbranch_vccnz .Lrd_tre11%=\n.Lrd_tl11%=:\ns_waitcnt vmcnt(15)\nv_mfma_f32_16x16x32_bf16 a[0:3], v[132:135], v[160:163], a[0:3]\ns_add_u32 s60, s41, 393216\nbuffer_load_dwordx4 v[76:79], v196, %[rs], s60 offen offset:2048 sc1\nv_bitop3_b32 v209, v164, v166, s61 bitop3:0x7e\n" \
"v_mfma_f32_16x16x32_bf16 a[8:11], v[140:143], v[160:163], a[8:11]\nv_mfma_f32_16x16x32_bf16 a[16:19], v[148:151], v[160:163], a[16:19]\nv_cmp_gt_i32 vcc, 0, v209\nv_mfma_f32_16x16x32_bf16 a[24:27], v[156:159], v[160:163], a[24:27]\ns_cbranch_vccnz .Lrd_tre12%=\n.Lrd_tl12%=:\ns_waitcnt vmcnt(15)\nv_mfma_f32_16x16x32_bf16 a[4:7], v[24:27], v[164:167], a[4:7]\n" \
"s_add_u32 s60, s41, 524288\nbuffer_load_dwordx4 v[16:19], v196, %[rs], s60 offen offset:0 sc1\nv_bitop3_b32 v209, v168, v170, s61 bitop3:0x7e\nv_mfma_f32_16x16x32_bf16 a[12:15], v[56:59], v[164:167], a[12:15]\nv_mfma_f32_16x16x32_bf16 a[20:23], v[88:91], v[164:167], a[20:23]\nds_write_b128 v202, a[0:3] offset:0\nv_cmp_gt_i32 vcc, 0, v209\nv_mfma_f32_16x16x32_bf16 a[28:31], v[120:123], v[164:167], a[28:31]\n" \
"s_cbranch_vccnz .Lrd_tre13%=\n.Lrd_tl13%=:\ns_waitcnt vmcnt(15)\nv_mfma_f32_16x16x32_bf16 a[4:7], v[28:31], v[168:171], a[4:7]\ns_add_u32 s60, s41, 524288\nbuffer_load_dwordx4 v[48:51], v196, %[rs], s60 offen offset:1024 sc1\nv_bitop3_b32 v209, v172, v174, s61 bitop3:0x7e\nv_mfma_f32_16x16x32_bf16 a[12:15], v[60:63], v[168:171], a[12:15]\n" \
"v_bitop3_b32 v210, v173, v175, s61 bitop3:0x7e\nv_or_b32 v209, v209, v210\nv_mfma_f32_16x16x32_bf16 a[20:23], v[92:95], v[168:171], a[20:23]\nds_write_b128 v202, a[8:11] offset:2048\nv_cmp_gt_i32 vcc, 0, v209\nv_mfma_f32_16x16x32_bf16 a[28:31], v[124:127], v[168:171], a[28:31]\ns_cbranch_vccnz .Lrd_tre14%=\n.Lrd_tl14%=:\n" \
"s_waitcnt vmcnt(15)\nv_mfma_f32_16x16x32_bf16 a[4:7], v[128:131], v[172:175], a[4:7]\ns_add_u32 s60, s41, 524288\nbuffer_load_dwordx4 v[80:83], v196, %[rs], s60 offen offset:2048 sc1\nv_bitop3_b32 v209, v176, v178, s61 bitop3:0x7e\nv_mfma_f32_16x16x32_bf16 a[12:15], v[136:139], v[172:175], a[12:15]\nv_bitop3_b32 v210, v177, v179, s61 bitop3:0x7e\nv_or_b32 v209, v209, v210\n" \
"v_mfma_f32_16x16x32_bf16 a[20:23], v[144:147], v[172:175], a[20:23]\nds_write_b128 v202, a[16:19] offset:4096\nv_cmp_gt_i32 vcc, 0, v209\nv_mfma_f32_16x16x32_bf16 a[28:31], v[152:155], v[172:175], a[28:31]\ns_cbranch_vccnz .Lrd_tre15%=\n.Lrd_tl15%=:\nv_mfma_f32_16x16x32_bf16 a[4:7], v[132:135], v[176:179], a[4:7]\ns_add_u32 s60, s41, 655360\n" \
"s_waitcnt vmcnt(14)\nbuffer_load_dwordx4 v[20:23], v196, %[rs], s60 offen offset:0 sc1\nv_lshlrev_b32 v216, 16, v212\nv_and_b32 v217, 0xffff0000, v212\nv_mfma_f32_16x16x32_bf16 a[12:15], v[140:143], v[176:179], a[12:15]\nv_lshlrev_b32 v218, 16, v213\nv_and_b32 v219, 0xffff0000, v213\nv_mfma_f32_16x16x32_bf16 a[20:23], v[148:151], v[176:179], a[20:23]\n" \
"ds_write_b128 v202, a[24:27] offset:6144\nv_lshlrev_b32 v220, 16, v214\nv_and_b32 v221, 0xffff0000, v214\nv_mfma_f32_16x16x32_bf16 a[28:31], v[156:159], v[176:179], a[28:31]\ns_add_u32 s60, s41, 655360\nbuffer_load_dwordx4 v[52:55], v196, %[rs], s60 offen offset:1024 sc1\ns_add_u32 s60, s41, 655360\nds_write_b128 v202, a[4:7] offset:1024\n" \
"buffer_load_dwordx4 v[84:87], v196, %[rs], s60 offen offset:2048 sc1\nds_write_b128 v202, a[12:15] offset:3072\nv_lshlrev_b32 v222, 16, v215\nv_and_b32 v223, 0xffff0000, v215\ns_add_u32 s60, s41, 786432\nbuffer_load_dwordx4 v[24:27], v196, %[rs], s60 offen offset:0 sc1\nds_write_b128 v202, a[20:23] offset:5120\ns_add_u32 s60, s41, 786432\n" \
"buffer_load_dwordx4 v[56:59], v196, %[rs], s60 offen offset:1024 sc1\nds_write_b128 v202, a[28:31] offset:7168\ns_waitcnt lgkmcnt(0)\ns_barrier\nds_read_b128 v[224:227], v203 offset:0\nds_read_b128 v[228:231], v203 offset:256\nds_read_b128 v[232:235], v203 offset:8192\nds_read_b128 v[236:239], v203 offset:8448\n" \
"ds_read_b128 v[240:243], v203 offset:16384\nds_read_b128 v[244:247], v203 offset:16640\nds_read_b128 v[248:251], v203 offset:24576\nds_read_b128 v[252:255], v203 offset:24832\ns_add_u32 s60, s41, 786432\nbuffer_load_dwordx4 v[88:91], v196, %[rs], s60 offen offset:2048 sc1\ns_waitcnt lgkmcnt(4)\nv_pk_add_f32 v[224:225], v[224:225], v[232:233]\n" \
"v_pk_add_f32 v[226:227], v[226:227], v[234:235]\nv_pk_add_f32 v[228:229], v[228:229], v[236:237]\nv_pk_add_f32 v[230:231], v[230:231], v[238:239]\ns_add_u32 s60, s41, 917504\nbuffer_load_dwordx4 v[28:31], v196, %[rs], s60 offen offset:0 sc1\ns_waitcnt lgkmcnt(2)\nv_pk_add_f32 v[224:225], v[224:225], v[240:241]\nv_pk_add_f32 v[226:227], v[226:227], v[242:243]\n" \
"v_pk_add_f32 v[228:229], v[228:229], v[244:245]\nv_pk_add_f32 v[230:231], v[230:231], v[246:247]\ns_add_u32 s60, s41, 917504\nbuffer_load_dwordx4 v[60:63], v196, %[rs], s60 offen offset:1024 sc1\ns_waitcnt lgkmcnt(0)\nv_pk_add_f32 v[224:225], v[224:225], v[248:249]\nv_pk_add_f32 v[226:227], v[226:227], v[250:251]\nv_pk_add_f32 v[228:229], v[228:229], v[252:253]\n" \
"v_pk_add_f32 v[230:231], v[230:231], v[254:255]\nv_pk_fma_f32 v[224:225], v[224:225], s[62:63], v[216:217]\nv_pk_fma_f32 v[226:227], v[226:227], s[62:63], v[218:219]\nv_pk_fma_f32 v[228:229], v[228:229], s[62:63], v[220:221]\nv_pk_fma_f32 v[230:231], v[230:231], s[62:63], v[222:223]\nv_cvt_pk_bf16_f32 v208, v224, v225\nv_cvt_pk_bf16_f32 v209, v226, v227\nv_cvt_pk_bf16_f32 v210, v228, v229\n" \
"v_cvt_pk_bf16_f32 v211, v230, v231\nbuffer_store_dwordx4 v[208:211], v204, %[rs], s42 offen sc1\nv_pk_add_f32 v[232:233], v[224:225], v[226:227]\nv_pk_add_f32 v[234:235], v[228:229], v[230:231]\nv_pk_add_f32 v[232:233], v[232:233], v[234:235]\ns_add_u32 s60, s41, 917504\nbuffer_load_dwordx4 v[92:95], v196, %[rs], s60 offen offset:2048 sc1\ns_add_u32 s60, s41, 0\n" \
"buffer_load_dwordx4 v[96:99], v196, %[rs], s60 offen offset:3072 sc1\nv_add_f32 v232, v232, v233\ns_nop 1\nv_add_f32_dpp v233, v232, v232 row_ror:8 row_mask:0xf bank_mask:0xf\nv_add_f32_dpp v234, v232, v232 row_ror:8 row_mask:0xf bank_mask:0xf\ns_nop 1\nv_permlane16_swap_b32 v233, v234\nv_add_f32 v232, v233, v234\n" \
"v_add_f32 v233, v233, v234\ns_nop 1\nv_permlane32_swap_b32 v232, v233\nv_add_f32 v232, v232, v233\nv_mul_f32 v232, 0x3c800000, v232\nv_pk_add_f32 v[224:225], v[224:225], v[232:233] op_sel_hi:[1,0] neg_lo:[0,1] neg_hi:[0,1]\nv_pk_add_f32 v[226:227], v[226:227], v[232:233] op_sel_hi:[1,0] neg_lo:[0,1] neg_hi:[0,1]\nv_pk_add_f32 v[228:229], v[228:229], v[232:233] op_sel_hi:[1,0] neg_lo:[0,1] neg_hi:[0,1]\n" \
"v_pk_add_f32 v[230:231], v[230:231], v[232:233] op_sel_hi:[1,0] neg_lo:[0,1] neg_hi:[0,1]\ns_add_u32 s60, s41, 131072\nbuffer_load_dwordx4 v[100:103], v196, %[rs], s60 offen offset:3072 sc1\ns_add_u32 s60, s41, 262144\nbuffer_load_dwordx4 v[104:107], v196, %[rs], s60 offen offset:3072 sc1\nv_pk_mul_f32 v[236:237], v[224:225], v[224:225]\nv_pk_mul_f32 v[238:239], v[226:227], v[226:227]\nv_pk_fma_f32 v[236:237], v[228:229], v[228:229], v[236:237]\n" \
"v_pk_fma_f32 v[238:239], v[230:231], v[230:231], v[238:239]\nv_pk_add_f32 v[236:237], v[236:237], v[238:239]\nv_add_f32 v236, v236, v237\ns_nop 1\nv_add_f32_dpp v237, v236, v236 row_ror:8 row_mask:0xf bank_mask:0xf\nv_add_f32_dpp v238, v236, v236 row_ror:8 row_mask:0xf bank_mask:0xf\ns_nop 1\nv_permlane16_swap_b32 v237, v238\n" \
"v_add_f32 v236, v237, v238\nv_add_f32 v237, v237, v238\ns_nop 1\nv_permlane32_swap_b32 v236, v237\nv_add_f32 v236, v236, v237\nv_mov_b32 v233, v236\nbuffer_store_dwordx2 v[232:233], v205, %[rs], s42 offen sc1\ns_add_u32 s60, s41, 393216\n" \
"buffer_load_dwordx4 v[108:111], v196, %[rs], s60 offen offset:3072 sc1\nv_accvgpr_read_b32 v212, a33\nbuffer_load_dwordx2 v[214:215], v212, %[rs], 0 offen sc1\ns_add_u32 s60, s41, 524288\nbuffer_load_dwordx4 v[112:115], v196, %[rs], s60 offen offset:3072 sc1\ns_add_u32 s60, s41, 655360\nbuffer_load_dwordx4 v[116:119], v196, %[rs], s60 offen offset:3072 sc1\ns_add_u32 s60, s41, 786432\n" \
"buffer_load_dwordx4 v[120:123], v196, %[rs], s60 offen offset:3072 sc1\ns_add_u32 s60, s41, 917504\nbuffer_load_dwordx4 v[124:127], v196, %[rs], s60 offen offset:3072 sc1\ns_waitcnt vmcnt(6)\nbuffer_store_dword v207, off, %[rs], %[fa] sc1\ns_cmp_ge_u32 s40, 64\ns_cbranch_scc1 .Lrd_end%=\n.Lrd_ychk%=:\n" \
"s_and_b32 s44, s40, 1\ns_bfe_u32 s55, s40, 0x10001\ns_mul_i32 s54, s44, 0x80008001\ns_mul_i32 s61, s55, 0x80008000\ns_lshl_b32 s58, s44, 17\ns_add_u32 s58, s58, %[st]\ns_add_u32 s44, s40, 1\ns_and_b32 s44, s44, 3\n" \
"s_lshl_b32 s44, s44, 15\nv_xor_b32 v206, s44, v200\nv_mov_b64 v[180:181], 0\nv_mov_b64 v[182:183], 0\nv_mov_b64 v[184:185], 0\nv_mov_b64 v[186:187], 0\nv_mov_b64 v[188:189], 0\nv_mov_b64 v[190:191], 0\n" \
"v_mov_b64 v[192:193], 0\nv_mov_b64 v[194:195], 0\ns_waitcnt vmcnt(5)\n.Lrd_sg0%=:\nv_cmp_ne_u32 vcc, s40, v214\ns_cbranch_vccnz .Lrd_sg1%=\nv_max_u32_dpp v215, v215, v215 quad_perm:[1,0,3,2] row_mask:0xf bank_mask:0xf\ns_nop 1\n" \
"v_max_u32_dpp v215, v215, v215 quad_perm:[2,3,0,1] row_mask:0xf bank_mask:0xf\ns_nop 1\nv_max_u32_dpp v215, v215, v215 row_half_mirror row_mask:0xf bank_mask:0xf\ns_nop 0\nv_readlane_b32 s86, v215, 0\nv_readlane_b32 s87, v215, 8\ns_add_u32 s86, s86, %[du]\n.Lrd_tw622%=:\n" \
"s_memrealtime s[68:69]\ns_waitcnt lgkmcnt(0)\ns_sub_u32 s44, s86, s68\ns_add_u32 s44, s44, 11\ns_cmp_lt_u32 s44, 250\ns_cbranch_scc1 .Lrd_tw622%=\nbuffer_load_dwordx4 v[128:131], v196, %[rs], %[su] offen offset:0 sc1\nbuffer_load_dwordx4 v[132:135], v196, %[rs], %[su] offen offset:1024 sc1\n" \
"buffer_load_dwordx4 v[136:139], v196, %[rs], %[su] offen offset:2048 sc1\nbuffer_load_dwordx4 v[140:143], v196, %[rs], %[su] offen offset:3072 sc1\n.Lrd_tw633%=:\ns_memrealtime s[68:69]\ns_waitcnt lgkmcnt(0)\ns_sub_u32 s44, s86, s68\ns_add_u32 s44, s44, 21\ns_cmp_lt_u32 s44, 250\n" \
"s_cbranch_scc1 .Lrd_tw633%=\nbuffer_load_dwordx4 v[144:147], v196, %[rs], s57 offen offset:0 sc1\nbuffer_load_dwordx4 v[148:151], v196, %[rs], s57 offen offset:1024 sc1\nbuffer_load_dwordx4 v[152:155], v196, %[rs], s57 offen offset:2048 sc1\nbuffer_load_dwordx4 v[156:159], v196, %[rs], s57 offen offset:3072 sc1\n.Lrd_tw644%=:\ns_memrealtime s[68:69]\ns_waitcnt lgkmcnt(0)\n" \
"s_sub_u32 s44, s87, s68\ns_add_u32 s44, s44, 24\ns_cmp_lt_u32 s44, 250\ns_cbranch_scc1 .Lrd_tw644%=\nbuffer_load_dwordx4 v[160:163], v196, %[rs], %[sp] offen offset:0 sc1\nbuffer_load_dwordx4 v[164:167], v196, %[rs], %[sp] offen offset:1024 sc1\nbuffer_load_dwordx4 v[168:171], v196, %[rs], %[sp] offen offset:2048 sc1\nbuffer_load_dwordx4 v[172:175], v196, %[rs], %[sp] offen offset:3072 sc1\n" \
"buffer_load_dwordx4 v[176:179], v196, %[rs], s56 offen offset:0 sc1\nv_bitop3_b32 v209, v0, v2, s55 bitop3:0x7e\nv_bitop3_b32 v210, v32, v34, s55 bitop3:0x7e\nv_or3_b32 v208, v209, v210, 0\nv_bitop3_b32 v209, v64, v66, s55 bitop3:0x7e\nv_bitop3_b32 v210, v4, v6, s55 bitop3:0x7e\nv_or3_b32 v208, v209, v210, v208\nv_bitop3_b32 v209, v36, v38, s55 bitop3:0x7e\n" \
"v_bitop3_b32 v210, v68, v70, s55 bitop3:0x7e\nv_or3_b32 v208, v209, v210, v208\nv_bitop3_b32 v209, v8, v10, s55 bitop3:0x7e\nv_bitop3_b32 v210, v40, v42, s55 bitop3:0x7e\nv_or3_b32 v208, v209, v210, v208\nv_bitop3_b32 v209, v72, v74, s55 bitop3:0x7e\nv_bitop3_b32 v210, v12, v14, s55 bitop3:0x7e\nv_or3_b32 v208, v209, v210, v208\n" \
"v_bitop3_b32 v209, v44, v46, s55 bitop3:0x7e\nv_bitop3_b32 v210, v76, v78, s55 bitop3:0x7e\nv_or3_b32 v208, v209, v210, v208\nv_bitop3_b32 v209, v16, v18, s55 bitop3:0x7e\nv_bitop3_b32 v210, v48, v50, s55 bitop3:0x7e\nv_or3_b32 v208, v209, v210, v208\nv_bitop3_b32 v209, v80, v82, s55 bitop3:0x7e\nv_bitop3_b32 v210, v20, v22, s55 bitop3:0x7e\n" \
"v_or3_b32 v208, v209, v210, v208\nv_bitop3_b32 v209, v52, v54, s55 bitop3:0x7e\nv_bitop3_b32 v210, v84, v86, s55 bitop3:0x7e\nv_or3_b32 v208, v209, v210, v208\nv_bitop3_b32 v209, v24, v26, s55 bitop3:0x7e\nv_bitop3_b32 v210, v56, v58, s55 bitop3:0x7e\nv_or3_b32 v208, v209, v210, v208\nv_bitop3_b32 v209, v88, v90, s55 bitop3:0x7e\n" \
"v_bitop3_b32 v210, v28, v30, s55 bitop3:0x7e\nv_or3_b32 v208, v209, v210, v208\nv_bitop3_b32 v209, v60, v62, s55 bitop3:0x7e\nv_bitop3_b32 v210, v92, v94, s55 bitop3:0x7e\nv_or3_b32 v208, v209, v210, v208\nv_bitop3_b32 v209, v96, v98, s55 bitop3:0x7e\nv_bitop3_b32 v210, v100, v102, s55 bitop3:0x7e\nv_or3_b32 v208, v209, v210, v208\n" \
"v_bitop3_b32 v209, v104, v106, s55 bitop3:0x7e\nv_bitop3_b32 v210, v108, v110, s55 bitop3:0x7e\nv_or3_b32 v208, v209, v210, v208\ns_waitcnt vmcnt(14)\nv_bitop3_b32 v209, v112, v114, s55 bitop3:0x7e\nv_bitop3_b32 v210, v116, v118, s55 bitop3:0x7e\nv_or3_b32 v208, v209, v210, v208\nv_bitop3_b32 v209, v120, v122, s55 bitop3:0x7e\n" \
"v_bitop3_b32 v210, v124, v126, s55 bitop3:0x7e\nv_or3_b32 v208, v209, v210, v208\nv_and_b32 v208, 1, v208\nv_cmp_ne_u32 vcc, 0, v208\ns_cbranch_vccnz .Lrd_yre%=\ns_branch .Lrd_ra%=\n.Lrd_sg1%=:\nbuffer_load_dwordx2 v[214:215], v212, %[rs], 0 offen sc1\n" \
"s_waitcnt vmcnt(0)\ns_add_u32 s43, s43, 1\ns_cmp_lt_u32 s43, 262144\ns_cbranch_scc1 .Lrd_sg0%=\ns_branch .Lrd_abort%=\n.Lrd_yre%=:\ns_mov_b64 exec, vcc\ns_waitcnt vmcnt(0)\n" \
"s_add_u32 s43, s43, 1\ns_cmp_lt_u32 s43, 262144\ns_cbranch_scc1 .Lrd_yld%=\ns_branch .Lrd_abort%=\n.Lrd_rb%=:\ns_mov_b64 exec, s[46:47]\ns_cbranch_execz .Lrd_rs0%=\nbuffer_load_dwordx4 v[160:163], v196, %[rs], %[sp] offen offset:0 sc1\n" \
".Lrd_rs0%=:\ns_mov_b64 exec, s[48:49]\ns_cbranch_execz .Lrd_rs1%=\nbuffer_load_dwordx4 v[164:167], v196, %[rs], %[sp] offen offset:1024 sc1\n.Lrd_rs1%=:\ns_mov_b64 exec, s[50:51]\ns_cbranch_execz .Lrd_rs2%=\nbuffer_load_dwordx4 v[168:171], v196, %[rs], %[sp] offen offset:2048 sc1\n" \
".Lrd_rs2%=:\ns_mov_b64 exec, s[52:53]\ns_cbranch_execz .Lrd_rs3%=\nbuffer_load_dwordx4 v[172:175], v196, %[rs], %[sp] offen offset:3072 sc1\n.Lrd_rs3%=:\ns_mov_b64 exec, s[64:65]\ns_cbranch_execz .Lrd_rs4%=\nbuffer_load_dwordx4 v[176:179], v196, %[rs], s56 offen offset:0 sc1\n" \
".Lrd_rs4%=:\ns_mov_b64 exec, s[86:87]\ns_cbranch_execz .Lrd_rs5%=\nbuffer_load_dwordx4 v[128:131], v196, %[rs], %[su] offen offset:0 sc1\nbuffer_load_dwordx4 v[136:139], v196, %[rs], %[su] offen offset:2048 sc1\n.Lrd_rs5%=:\ns_mov_b64 exec, s[88:89]\ns_cbranch_execz .Lrd_rs6%=\n" \
"buffer_load_dwordx4 v[144:147], v196, %[rs], s57 offen offset:0 sc1\nbuffer_load_dwordx4 v[152:155], v196, %[rs], s57 offen offset:2048 sc1\n.Lrd_rs6%=:\ns_mov_b64 exec, s[68:69]\ns_cbranch_execz .Lrd_rs7%=\nbuffer_load_dwordx4 v[132:135], v196, %[rs], %[su] offen offset:1024 sc1\nbuffer_load_dwordx4 v[140:143], v196, %[rs], %[su] offen offset:3072 sc1\n.Lrd_rs7%=:\n" \
"s_mov_b64 exec, s[62:63]\ns_cbranch_execz .Lrd_rs8%=\nbuffer_load_dwordx4 v[148:151], v196, %[rs], s57 offen offset:1024 sc1\nbuffer_load_dwordx4 v[156:159], v196, %[rs], s57 offen offset:3072 sc1\n.Lrd_rs8%=:\ns_mov_b64 exec, -1\nv_mov_b64 v[180:181], 0\nv_mov_b64 v[182:183], 0\n" \
"v_mov_b64 v[184:185], 0\nv_mov_b64 v[186:187], 0\nv_mov_b64 v[188:189], 0\nv_mov_b64 v[190:191], 0\nv_mov_b64 v[192:193], 0\nv_mov_b64 v[194:195], 0\ns_waitcnt vmcnt(0)\ns_add_u32 s43, s43, 1\n" \
"s_cmp_lt_u32 s43, 262144\ns_cbranch_scc1 .Lrd_ra%=\ns_branch .Lrd_abort%=\n.Lrd_y1%=:\nv_accvgpr_read_b32 v206, a32\nbuffer_load_dword v208, v206, %[rs], 0 offen sc1\ns_waitcnt vmcnt(0)\nv_cmp_gt_i32 vcc, 1, v208\n" \
"s_cbranch_vccz .Lrd_y1r%=\ns_add_u32 s43, s43, 1\ns_cmp_lt_u32 s43, 262144\ns_cbranch_scc1 .Lrd_y1%=\ns_branch .Lrd_abort%=\n.Lrd_tre0%=:\ns_add_u32 s60, s58, 16384\nbuffer_load_dwordx4 v[212:215], v196, %[rs], s60 offen offset:0 sc1\n" \
"s_waitcnt vmcnt(0)\nv_bitop3_b32 v209, v212, v214, s61 bitop3:0x7e\nv_cmp_gt_i32 vcc, 0, v209\ns_cbranch_vccz .Lrd_tl0%=\ns_add_u32 s43, s43, 1\ns_cmp_lt_u32 s43, 262144\ns_cbranch_scc1 .Lrd_tre0%=\ns_branch .Lrd_abort%=\n" \
".Lrd_tre1%=:\ns_add_u32 s60, s58, 0\nbuffer_load_dwordx4 v[216:219], v196, %[rs], s60 offen offset:0 sc1\ns_waitcnt vmcnt(0)\nv_bitop3_b32 v209, v216, v218, s61 bitop3:0x7e\nv_cmp_gt_i32 vcc, 0, v209\ns_cbranch_vccz .Lrd_tl1%=\ns_add_u32 s43, s43, 1\n" \
"s_cmp_lt_u32 s43, 262144\ns_cbranch_scc1 .Lrd_tre1%=\ns_branch .Lrd_abort%=\n.Lrd_tre2%=:\ns_add_u32 s60, s58, 0\nbuffer_load_dwordx4 v[220:223], v196, %[rs], s60 offen offset:1024 sc1\ns_waitcnt vmcnt(0)\nv_bitop3_b32 v209, v220, v222, s61 bitop3:0x7e\n" \
"v_cmp_gt_i32 vcc, 0, v209\ns_cbranch_vccz .Lrd_tl2%=\ns_add_u32 s43, s43, 1\ns_cmp_lt_u32 s43, 262144\ns_cbranch_scc1 .Lrd_tre2%=\ns_branch .Lrd_abort%=\n.Lrd_tre3%=:\ns_add_u32 s60, s58, 16384\n" \
"buffer_load_dwordx4 v[224:227], v196, %[rs], s60 offen offset:1024 sc1\ns_waitcnt vmcnt(0)\nv_bitop3_b32 v209, v224, v226, s61 bitop3:0x7e\nv_cmp_gt_i32 vcc, 0, v209\ns_cbranch_vccz .Lrd_tl3%=\ns_add_u32 s43, s43, 1\ns_cmp_lt_u32 s43, 262144\ns_cbranch_scc1 .Lrd_tre3%=\n" \
"s_branch .Lrd_abort%=\n.Lrd_tre4%=:\ns_add_u32 s60, s58, 0\nbuffer_load_dwordx4 v[228:231], v196, %[rs], s60 offen offset:2048 sc1\ns_waitcnt vmcnt(0)\nv_bitop3_b32 v209, v228, v230, s61 bitop3:0x7e\nv_cmp_gt_i32 vcc, 0, v209\ns_cbranch_vccz .Lrd_tl4%=\n" \
"s_add_u32 s43, s43, 1\ns_cmp_lt_u32 s43, 262144\ns_cbranch_scc1 .Lrd_tre4%=\ns_branch .Lrd_abort%=\n.Lrd_tre5%=:\ns_add_u32 s60, s58, 0\nbuffer_load_dwordx4 v[232:235], v196, %[rs], s60 offen offset:3072 sc1\ns_waitcnt vmcnt(0)\n" \
"v_bitop3_b32 v209, v232, v234, s61 bitop3:0x7e\nv_cmp_gt_i32 vcc, 0, v209\ns_cbranch_vccz .Lrd_tl5%=\ns_add_u32 s43, s43, 1\ns_cmp_lt_u32 s43, 262144\ns_cbranch_scc1 .Lrd_tre5%=\ns_branch .Lrd_abort%=\n.Lrd_tre6%=:\n" \
"s_add_u32 s60, s58, 16384\nbuffer_load_dwordx4 v[236:239], v196, %[rs], s60 offen offset:2048 sc1\ns_waitcnt vmcnt(0)\nv_bitop3_b32 v209, v236, v238, s61 bitop3:0x7e\nv_cmp_gt_i32 vcc, 0, v209\ns_cbranch_vccz .Lrd_tl6%=\ns_add_u32 s43, s43, 1\ns_cmp_lt_u32 s43, 262144\n" \
"s_cbranch_scc1 .Lrd_tre6%=\ns_branch .Lrd_abort%=\n.Lrd_tre7%=:\ns_add_u32 s60, s58, 4096\nbuffer_load_dwordx4 v[240:243], v196, %[rs], s60 offen offset:0 sc1\ns_waitcnt vmcnt(0)\nv_bitop3_b32 v209, v240, v242, s61 bitop3:0x7e\nv_cmp_gt_i32 vcc, 0, v209\n" \
"s_cbranch_vccz .Lrd_tl7%=\ns_add_u32 s43, s43, 1\ns_cmp_lt_u32 s43, 262144\ns_cbranch_scc1 .Lrd_tre7%=\ns_branch .Lrd_abort%=\n.Lrd_tre8%=:\ns_add_u32 s60, s58, 4096\nbuffer_load_dwordx4 v[244:247], v196, %[rs], s60 offen offset:1024 sc1\n" \
"s_waitcnt vmcnt(0)\nv_bitop3_b32 v209, v244, v246, s61 bitop3:0x7e\nv_cmp_gt_i32 vcc, 0, v209\ns_cbranch_vccz .Lrd_tl8%=\ns_add_u32 s43, s43, 1\ns_cmp_lt_u32 s43, 262144\ns_cbranch_scc1 .Lrd_tre8%=\ns_branch .Lrd_abort%=\n" \
".Lrd_tre9%=:\ns_add_u32 s60, s58, 16384\nbuffer_load_dwordx4 v[248:251], v196, %[rs], s60 offen offset:3072 sc1\ns_waitcnt vmcnt(0)\nv_bitop3_b32 v209, v248, v250, s61 bitop3:0x7e\nv_cmp_gt_i32 vcc, 0, v209\ns_cbranch_vccz .Lrd_tl9%=\ns_add_u32 s43, s43, 1\n" \
"s_cmp_lt_u32 s43, 262144\ns_cbranch_scc1 .Lrd_tre9%=\ns_branch .Lrd_abort%=\n.Lrd_tre10%=:\ns_add_u32 s60, s58, 20480\nbuffer_load_dwordx4 v[252:255], v196, %[rs], s60 offen offset:0 sc1\ns_waitcnt vmcnt(0)\nv_bitop3_b32 v209, v252, v254, s61 bitop3:0x7e\n" \
"v_bitop3_b32 v210, v253, v255, s61 bitop3:0x7e\nv_or_b32 v209, v209, v210\nv_cmp_gt_i32 vcc, 0, v209\ns_cbranch_vccz .Lrd_tl10%=\ns_add_u32 s43, s43, 1\ns_cmp_lt_u32 s43, 262144\ns_cbranch_scc1 .Lrd_tre10%=\ns_branch .Lrd_abort%=\n" \
".Lrd_tre11%=:\ns_add_u32 s60, s58, 20480\nbuffer_load_dwordx4 v[160:163], v196, %[rs], s60 offen offset:2048 sc1\ns_waitcnt vmcnt(0)\nv_bitop3_b32 v209, v160, v162, s61 bitop3:0x7e\nv_bitop3_b32 v210, v161, v163, s61 bitop3:0x7e\nv_or_b32 v209, v209, v210\nv_cmp_gt_i32 vcc, 0, v209\n" \
"s_cbranch_vccz .Lrd_tl11%=\ns_add_u32 s43, s43, 1\ns_cmp_lt_u32 s43, 262144\ns_cbranch_scc1 .Lrd_tre11%=\ns_branch .Lrd_abort%=\n.Lrd_tre12%=:\ns_add_u32 s60, s58, 4096\nbuffer_load_dwordx4 v[164:167], v196, %[rs], s60 offen offset:2048 sc1\n" \
"s_waitcnt vmcnt(0)\nv_bitop3_b32 v209, v164, v166, s61 bitop3:0x7e\nv_cmp_gt_i32 vcc, 0, v209\ns_cbranch_vccz .Lrd_tl12%=\ns_add_u32 s43, s43, 1\ns_cmp_lt_u32 s43, 262144\ns_cbranch_scc1 .Lrd_tre12%=\ns_branch .Lrd_abort%=\n" \
".Lrd_tre13%=:\ns_add_u32 s60, s58, 4096\nbuffer_load_dwordx4 v[168:171], v196, %[rs], s60 offen offset:3072 sc1\ns_waitcnt vmcnt(0)\nv_bitop3_b32 v209, v168, v170, s61 bitop3:0x7e\nv_cmp_gt_i32 vcc, 0, v209\ns_cbranch_vccz .Lrd_tl13%=\ns_add_u32 s43, s43, 1\n" \
"s_cmp_lt_u32 s43, 262144\ns_cbranch_scc1 .Lrd_tre13%=\ns_branch .Lrd_abort%=\n.Lrd_tre14%=:\ns_add_u32 s60, s58, 20480\nbuffer_load_dwordx4 v[172:175], v196, %[rs], s60 offen offset:1024 sc1\ns_waitcnt vmcnt(0)\nv_bitop3_b32 v209, v172, v174, s61 bitop3:0x7e\n" \
"v_bitop3_b32 v210, v173, v175, s61 bitop3:0x7e\nv_or_b32 v209, v209, v210\nv_cmp_gt_i32 vcc, 0, v209\ns_cbranch_vccz .Lrd_tl14%=\ns_add_u32 s43, s43, 1\ns_cmp_lt_u32 s43, 262144\ns_cbranch_scc1 .Lrd_tre14%=\ns_branch .Lrd_abort%=\n" \
".Lrd_tre15%=:\ns_add_u32 s60, s58, 20480\nbuffer_load_dwordx4 v[176:179], v196, %[rs], s60 offen offset:3072 sc1\ns_waitcnt vmcnt(0)\nv_bitop3_b32 v209, v176, v178, s61 bitop3:0x7e\nv_bitop3_b32 v210, v177, v179, s61 bitop3:0x7e\nv_or_b32 v209, v209, v210\nv_cmp_gt_i32 vcc, 0, v209\n" \
"s_cbranch_vccz .Lrd_tl15%=\ns_add_u32 s43, s43, 1\ns_cmp_lt_u32 s43, 262144\ns_cbranch_scc1 .Lrd_tre15%=\ns_branch .Lrd_abort%=\n.Lrd_abort%=:\ns_mov_b64 exec, -1\nv_mov_b32 v16, 0\n" \
"s_movk_i32 s44, 6400\nbuffer_store_dword v16, off, %[rs], s44 sc1\n.Lrd_end%=:\ns_waitcnt vmcnt(0) lgkmcnt(0)\n" \
""
#define GG_ASM \
"v_mbcnt_lo_u32_b32 v255, -1, 0\nv_mbcnt_hi_u32_b32 v255, -1, v255\nv_lshlrev_b32 v255, 2, v255\nv_add_u32 v255, %[tb], v255\nds_read_b32 v0, v255 offset:0\nds_read_b32 v1, v255 offset:256\nds_read_b32 v2, v255 offset:512\nds_read_b32 v3, v255 offset:768\n" \
"ds_read_b32 v4, v255 offset:1024\nds_read_b32 v5, v255 offset:1280\nds_read_b32 v92, v255 offset:1536\nds_read_b32 v93, v255 offset:1792\nds_read_b32 v94, v255 offset:2048\ns_waitcnt lgkmcnt(0)\nv_mov_b32 v6, 0x3f803f80\nv_mov_b32 v7, 0x3f803f80\n" \
"s_add_u32 m0, %[lw], 16384\ns_add_u32 s44, %[wo], 0\nbuffer_load_dwordx4 v92, %[rw], s44 offen nt lds\ns_add_u32 m0, %[lw], 17408\ns_add_u32 s44, %[wo], 57344\nbuffer_load_dwordx4 v92, %[rw], s44 offen nt lds\ns_add_u32 m0, %[lw], 18432\ns_add_u32 s44, %[wo], 114688\n" \
"buffer_load_dwordx4 v92, %[rw], s44 offen nt lds\ns_add_u32 m0, %[lw], 19456\ns_add_u32 s44, %[wo], 172032\nbuffer_load_dwordx4 v92, %[rw], s44 offen nt lds\ns_add_u32 m0, %[lw], 20480\ns_add_u32 s44, %[wo], 229376\nbuffer_load_dwordx4 v92, %[rw], s44 offen nt lds\ns_add_u32 m0, %[lw], 21504\n" \
"s_add_u32 s44, %[wo], 286720\nbuffer_load_dwordx4 v92, %[rw], s44 offen nt lds\ns_add_u32 m0, %[lw], 22528\ns_add_u32 s44, %[wo], 344064\nbuffer_load_dwordx4 v92, %[rw], s44 offen nt lds\ns_add_u32 m0, %[lw], 23552\ns_add_u32 s44, %[wo], 401408\nbuffer_load_dwordx4 v92, %[rw], s44 offen nt lds\n" \
"s_add_u32 m0, %[tb], 0\ns_add_u32 s44, %[wo], 1835008\nbuffer_load_dwordx4 v92, %[rw], s44 offen nt lds\ns_add_u32 m0, %[tb], 1024\ns_add_u32 s44, %[wo], 1892352\nbuffer_load_dwordx4 v92, %[rw], s44 offen nt lds\ns_add_u32 m0, %[tb], 2048\ns_add_u32 s44, %[wo], 1949696\n" \
"buffer_load_dwordx4 v92, %[rw], s44 offen nt lds\ns_add_u32 m0, %[tb], 3072\ns_add_u32 s44, %[wo], 2007040\nbuffer_load_dwordx4 v92, %[rw], s44 offen nt lds\ns_add_u32 m0, %[tb], 4096\ns_add_u32 s44, %[wo], 2064384\nbuffer_load_dwordx4 v92, %[rw], s44 offen nt lds\ns_add_u32 m0, %[tb], 5120\n" \
"s_add_u32 s44, %[wo], 2121728\nbuffer_load_dwordx4 v92, %[rw], s44 offen nt lds\ns_add_u32 m0, %[tb], 6144\ns_add_u32 s44, %[wo], 2179072\nbuffer_load_dwordx4 v92, %[rw], s44 offen nt lds\ns_add_u32 m0, %[tb], 7168\ns_add_u32 s44, %[wo], 2236416\nbuffer_load_dwordx4 v92, %[rw], s44 offen nt lds\n" \
"s_add_u32 m0, %[tb], 8192\ns_add_u32 s44, %[wo], 3670016\nbuffer_load_dwordx4 v92, %[rw], s44 offen nt lds\ns_add_u32 m0, %[tb], 9216\ns_add_u32 s44, %[wo], 3727360\nbuffer_load_dwordx4 v92, %[rw], s44 offen nt lds\ns_add_u32 m0, %[tb], 10240\ns_add_u32 s44, %[wo], 3784704\n" \
"buffer_load_dwordx4 v92, %[rw], s44 offen nt lds\ns_add_u32 m0, %[tb], 11264\ns_add_u32 s44, %[wo], 3842048\nbuffer_load_dwordx4 v92, %[rw], s44 offen nt lds\ns_add_u32 m0, %[lw], 0\ns_add_u32 s44, %[wo], 3899392\nbuffer_load_dwordx4 v92, %[rw], s44 offen nt lds\ns_add_u32 m0, %[lw], 1024\n" \
"s_add_u32 s44, %[wo], 3956736\nbuffer_load_dwordx4 v92, %[rw], s44 offen nt lds\ns_add_u32 m0, %[lw], 2048\ns_add_u32 s44, %[wo], 4014080\nbuffer_load_dwordx4 v92, %[rw], s44 offen nt lds\ns_add_u32 m0, %[lw], 3072\ns_add_u32 s44, %[wo], 4071424\nbuffer_load_dwordx4 v92, %[rw], s44 offen nt lds\n" \
"s_add_u32 m0, %[lw], 4096\ns_add_u32 s44, %[wo], 5505024\nbuffer_load_dwordx4 v92, %[rw], s44 offen nt lds\ns_add_u32 m0, %[lw], 5120\ns_add_u32 s44, %[wo], 5562368\nbuffer_load_dwordx4 v92, %[rw], s44 offen nt lds\ns_add_u32 m0, %[lw], 6144\ns_add_u32 s44, %[wo], 5619712\n" \
"buffer_load_dwordx4 v92, %[rw], s44 offen nt lds\ns_add_u32 m0, %[lw], 7168\ns_add_u32 s44, %[wo], 5677056\nbuffer_load_dwordx4 v92, %[rw], s44 offen nt lds\ns_add_u32 m0, %[lw], 8192\ns_add_u32 s44, %[wo], 5734400\nbuffer_load_dwordx4 v92, %[rw], s44 offen nt lds\ns_add_u32 m0, %[lw], 9216\n" \
"s_add_u32 s44, %[wo], 5791744\nbuffer_load_dwordx4 v92, %[rw], s44 offen nt lds\ns_add_u32 m0, %[lw], 10240\ns_add_u32 s44, %[wo], 5849088\nbuffer_load_dwordx4 v92, %[rw], s44 offen nt lds\ns_add_u32 m0, %[lw], 11264\ns_add_u32 s44, %[wo], 5906432\nbuffer_load_dwordx4 v92, %[rw], s44 offen nt lds\n" \
"s_waitcnt vmcnt(28)\ns_barrier\ns_add_u32 m0, %[lw], 12288\ns_add_u32 s44, %[wo], 458752\nbuffer_load_dwordx4 v92, %[rw], s44 offen nt lds\ns_add_u32 m0, %[lw], 13312\ns_add_u32 s44, %[wo], 516096\nbuffer_load_dwordx4 v92, %[rw], s44 offen nt lds\n" \
"s_add_u32 m0, %[lw], 14336\ns_add_u32 s44, %[wo], 573440\nbuffer_load_dwordx4 v92, %[rw], s44 offen nt lds\ns_add_u32 m0, %[lw], 15360\ns_add_u32 s44, %[wo], 630784\nbuffer_load_dwordx4 v92, %[rw], s44 offen nt lds\nds_read_b64_tr_b16 a[0:1], v94 offset:16384\nds_read_b64_tr_b16 a[2:3], v94 offset:16896\n" \
"ds_read_b64_tr_b16 a[8:9], v94 offset:16392\nds_read_b64_tr_b16 a[10:11], v94 offset:16904\nds_read_b64_tr_b16 a[16:17], v94 offset:40960\nds_read_b64_tr_b16 a[18:19], v94 offset:41472\nds_read_b64_tr_b16 a[24:25], v94 offset:40968\nds_read_b64_tr_b16 a[26:27], v94 offset:41480\ns_waitcnt lgkmcnt(0)\nv_mfma_f32_16x16x16_bf16 v[8:11], a[0:1], v[6:7], 0\n" \
"v_mfma_f32_16x16x16_bf16 v[12:15], a[8:9], v[6:7], 0\nv_mfma_f32_16x16x16_bf16 v[16:19], a[16:17], v[6:7], 0\nv_mfma_f32_16x16x16_bf16 v[20:23], a[24:25], v[6:7], 0\nv_mfma_f32_16x16x16_bf16 v[8:11], a[2:3], v[6:7], v[8:11]\nv_mfma_f32_16x16x16_bf16 v[12:15], a[10:11], v[6:7], v[12:15]\nv_mfma_f32_16x16x16_bf16 v[16:19], a[18:19], v[6:7], v[16:19]\nv_mfma_f32_16x16x16_bf16 v[20:23], a[26:27], v[6:7], v[20:23]\ns_waitcnt vmcnt(28)\n" \
"s_barrier\ns_add_u32 m0, %[lw], 16384\ns_add_u32 s44, %[wo], 688128\nbuffer_load_dwordx4 v92, %[rw], s44 offen nt lds\ns_add_u32 m0, %[lw], 17408\ns_add_u32 s44, %[wo], 745472\nbuffer_load_dwordx4 v92, %[rw], s44 offen nt lds\ns_add_u32 m0, %[lw], 18432\n" \
"s_add_u32 s44, %[wo], 802816\nbuffer_load_dwordx4 v92, %[rw], s44 offen nt lds\ns_add_u32 m0, %[lw], 19456\ns_add_u32 s44, %[wo], 860160\nbuffer_load_dwordx4 v92, %[rw], s44 offen nt lds\nds_read_b64_tr_b16 a[4:5], v94 offset:20480\nds_read_b64_tr_b16 a[6:7], v94 offset:20992\nds_read_b64_tr_b16 a[12:13], v94 offset:20488\n" \
"ds_read_b64_tr_b16 a[14:15], v94 offset:21000\nds_read_b64_tr_b16 a[20:21], v94 offset:45056\nds_read_b64_tr_b16 a[22:23], v94 offset:45568\nds_read_b64_tr_b16 a[28:29], v94 offset:45064\nds_read_b64_tr_b16 a[30:31], v94 offset:45576\ns_waitcnt lgkmcnt(0)\nv_mfma_f32_16x16x16_bf16 v[8:11], a[4:5], v[6:7], v[8:11]\nv_mfma_f32_16x16x16_bf16 v[12:15], a[12:13], v[6:7], v[12:15]\n" \
"v_mfma_f32_16x16x16_bf16 v[16:19], a[20:21], v[6:7], v[16:19]\nv_mfma_f32_16x16x16_bf16 v[20:23], a[28:29], v[6:7], v[20:23]\nv_mfma_f32_16x16x16_bf16 v[8:11], a[6:7], v[6:7], v[8:11]\nv_mfma_f32_16x16x16_bf16 v[12:15], a[14:15], v[6:7], v[12:15]\nv_mfma_f32_16x16x16_bf16 v[16:19], a[22:23], v[6:7], v[16:19]\nv_mfma_f32_16x16x16_bf16 v[20:23], a[30:31], v[6:7], v[20:23]\ns_waitcnt vmcnt(28)\ns_barrier\n" \
"s_add_u32 m0, %[lw], 20480\ns_add_u32 s44, %[wo], 917504\nbuffer_load_dwordx4 v92, %[rw], s44 offen nt lds\ns_add_u32 m0, %[lw], 21504\ns_add_u32 s44, %[wo], 974848\nbuffer_load_dwordx4 v92, %[rw], s44 offen nt lds\ns_add_u32 m0, %[lw], 22528\ns_add_u32 s44, %[wo], 1032192\n" \
"buffer_load_dwordx4 v92, %[rw], s44 offen nt lds\ns_add_u32 m0, %[lw], 23552\ns_add_u32 s44, %[wo], 1089536\nbuffer_load_dwordx4 v92, %[rw], s44 offen nt lds\nds_read_b64_tr_b16 a[104:105], v93 offset:0\nds_read_b64_tr_b16 a[106:107], v93 offset:512\nds_read_b64_tr_b16 a[112:113], v93 offset:8\nds_read_b64_tr_b16 a[114:115], v93 offset:520\n" \
"ds_read_b64_tr_b16 a[120:121], v93 offset:12288\nds_read_b64_tr_b16 a[122:123], v93 offset:12800\nds_read_b64_tr_b16 a[128:129], v93 offset:12296\nds_read_b64_tr_b16 a[130:131], v93 offset:12808\ns_waitcnt lgkmcnt(0)\nv_mfma_f32_16x16x16_bf16 v[8:11], a[104:105], v[6:7], v[8:11]\nv_mfma_f32_16x16x16_bf16 v[12:15], a[112:113], v[6:7], v[12:15]\nv_mfma_f32_16x16x16_bf16 v[16:19], a[120:121], v[6:7], v[16:19]\n" \
"v_mfma_f32_16x16x16_bf16 v[20:23], a[128:129], v[6:7], v[20:23]\nv_mfma_f32_16x16x16_bf16 v[8:11], a[106:107], v[6:7], v[8:11]\nv_mfma_f32_16x16x16_bf16 v[12:15], a[114:115], v[6:7], v[12:15]\nv_mfma_f32_16x16x16_bf16 v[16:19], a[122:123], v[6:7], v[16:19]\nv_mfma_f32_16x16x16_bf16 v[20:23], a[130:131], v[6:7], v[20:23]\ns_waitcnt vmcnt(28)\ns_barrier\ns_add_u32 m0, %[tb], 0\n" \
"s_add_u32 s44, %[wo], 1146880\nbuffer_load_dwordx4 v92, %[rw], s44 offen nt lds\ns_add_u32 m0, %[tb], 1024\ns_add_u32 s44, %[wo], 1204224\nbuffer_load_dwordx4 v92, %[rw], s44 offen nt lds\ns_add_u32 m0, %[tb], 2048\ns_add_u32 s44, %[wo], 1261568\nbuffer_load_dwordx4 v92, %[rw], s44 offen nt lds\n" \
"s_add_u32 m0, %[tb], 3072\ns_add_u32 s44, %[wo], 1318912\nbuffer_load_dwordx4 v92, %[rw], s44 offen nt lds\nds_read_b64_tr_b16 a[108:109], v93 offset:4096\nds_read_b64_tr_b16 a[110:111], v93 offset:4608\nds_read_b64_tr_b16 a[116:117], v93 offset:4104\nds_read_b64_tr_b16 a[118:119], v93 offset:4616\nds_read_b64_tr_b16 a[124:125], v93 offset:16384\n" \
"ds_read_b64_tr_b16 a[126:127], v93 offset:16896\nds_read_b64_tr_b16 a[132:133], v93 offset:16392\nds_read_b64_tr_b16 a[134:135], v93 offset:16904\ns_waitcnt lgkmcnt(0)\nv_mfma_f32_16x16x16_bf16 v[8:11], a[108:109], v[6:7], v[8:11]\nv_mfma_f32_16x16x16_bf16 v[12:15], a[116:117], v[6:7], v[12:15]\nv_mfma_f32_16x16x16_bf16 v[16:19], a[124:125], v[6:7], v[16:19]\nv_mfma_f32_16x16x16_bf16 v[20:23], a[132:133], v[6:7], v[20:23]\n" \
"v_mfma_f32_16x16x16_bf16 v[8:11], a[110:111], v[6:7], v[8:11]\nv_mfma_f32_16x16x16_bf16 v[12:15], a[118:119], v[6:7], v[12:15]\nv_mfma_f32_16x16x16_bf16 v[16:19], a[126:127], v[6:7], v[16:19]\nv_mfma_f32_16x16x16_bf16 v[20:23], a[134:135], v[6:7], v[20:23]\ns_waitcnt vmcnt(28)\ns_barrier\ns_add_u32 m0, %[tb], 4096\ns_add_u32 s44, %[wo], 1376256\n" \
"buffer_load_dwordx4 v92, %[rw], s44 offen nt lds\ns_add_u32 m0, %[tb], 5120\ns_add_u32 s44, %[wo], 1433600\nbuffer_load_dwordx4 v92, %[rw], s44 offen nt lds\ns_add_u32 m0, %[tb], 6144\ns_add_u32 s44, %[wo], 1490944\nbuffer_load_dwordx4 v92, %[rw], s44 offen nt lds\ns_add_u32 m0, %[tb], 7168\n" \
"s_add_u32 s44, %[wo], 1548288\nbuffer_load_dwordx4 v92, %[rw], s44 offen nt lds\nds_read_b64_tr_b16 a[208:209], v93 offset:8192\nds_read_b64_tr_b16 a[210:211], v93 offset:8704\nds_read_b64_tr_b16 a[216:217], v93 offset:8200\nds_read_b64_tr_b16 a[218:219], v93 offset:8712\nds_read_b64_tr_b16 a[224:225], v93 offset:20480\nds_read_b64_tr_b16 a[226:227], v93 offset:20992\n" \
"ds_read_b64_tr_b16 a[232:233], v93 offset:20488\nds_read_b64_tr_b16 a[234:235], v93 offset:21000\ns_waitcnt lgkmcnt(0)\nv_mfma_f32_16x16x16_bf16 v[8:11], a[208:209], v[6:7], v[8:11]\nv_mfma_f32_16x16x16_bf16 v[12:15], a[216:217], v[6:7], v[12:15]\nv_mfma_f32_16x16x16_bf16 v[16:19], a[224:225], v[6:7], v[16:19]\nv_mfma_f32_16x16x16_bf16 v[20:23], a[232:233], v[6:7], v[20:23]\nv_mfma_f32_16x16x16_bf16 v[8:11], a[210:211], v[6:7], v[8:11]\n" \
"v_mfma_f32_16x16x16_bf16 v[12:15], a[218:219], v[6:7], v[12:15]\nv_mfma_f32_16x16x16_bf16 v[16:19], a[226:227], v[6:7], v[16:19]\nv_mfma_f32_16x16x16_bf16 v[20:23], a[234:235], v[6:7], v[20:23]\ns_waitcnt vmcnt(28)\ns_barrier\ns_add_u32 m0, %[tb], 8192\ns_add_u32 s44, %[wo], 1605632\nbuffer_load_dwordx4 v92, %[rw], s44 offen nt lds\n" \
"s_add_u32 m0, %[tb], 9216\ns_add_u32 s44, %[wo], 1662976\nbuffer_load_dwordx4 v92, %[rw], s44 offen nt lds\ns_add_u32 m0, %[tb], 10240\ns_add_u32 s44, %[wo], 1720320\nbuffer_load_dwordx4 v92, %[rw], s44 offen nt lds\ns_add_u32 m0, %[tb], 11264\ns_add_u32 s44, %[wo], 1777664\n" \
"buffer_load_dwordx4 v92, %[rw], s44 offen nt lds\nds_read_b64_tr_b16 a[212:213], v94 offset:0\nds_read_b64_tr_b16 a[214:215], v94 offset:512\nds_read_b64_tr_b16 a[220:221], v94 offset:8\nds_read_b64_tr_b16 a[222:223], v94 offset:520\nds_read_b64_tr_b16 a[228:229], v94 offset:24576\nds_read_b64_tr_b16 a[230:231], v94 offset:25088\nds_read_b64_tr_b16 a[236:237], v94 offset:24584\n" \
"ds_read_b64_tr_b16 a[238:239], v94 offset:25096\ns_waitcnt lgkmcnt(0)\nv_mfma_f32_16x16x16_bf16 v[8:11], a[212:213], v[6:7], v[8:11]\nv_mfma_f32_16x16x16_bf16 v[12:15], a[220:221], v[6:7], v[12:15]\nv_mfma_f32_16x16x16_bf16 v[16:19], a[228:229], v[6:7], v[16:19]\nv_mfma_f32_16x16x16_bf16 v[20:23], a[236:237], v[6:7], v[20:23]\nv_mfma_f32_16x16x16_bf16 v[8:11], a[214:215], v[6:7], v[8:11]\nv_mfma_f32_16x16x16_bf16 v[12:15], a[222:223], v[6:7], v[12:15]\n" \
"v_mfma_f32_16x16x16_bf16 v[16:19], a[230:231], v[6:7], v[16:19]\nv_mfma_f32_16x16x16_bf16 v[20:23], a[238:239], v[6:7], v[20:23]\ns_waitcnt vmcnt(28)\ns_barrier\ns_add_u32 m0, %[lw], 0\ns_add_u32 s44, %[wo], 2293760\nbuffer_load_dwordx4 v92, %[rw], s44 offen nt lds\ns_add_u32 m0, %[lw], 1024\n" \
"s_add_u32 s44, %[wo], 2351104\nbuffer_load_dwordx4 v92, %[rw], s44 offen nt lds\ns_add_u32 m0, %[lw], 2048\ns_add_u32 s44, %[wo], 2408448\nbuffer_load_dwordx4 v92, %[rw], s44 offen nt lds\ns_add_u32 m0, %[lw], 3072\ns_add_u32 s44, %[wo], 2465792\nbuffer_load_dwordx4 v92, %[rw], s44 offen nt lds\n" \
"ds_read_b64_tr_b16 v[152:153], v94 offset:4096\nds_read_b64_tr_b16 v[154:155], v94 offset:4608\nds_read_b64_tr_b16 v[160:161], v94 offset:4104\nds_read_b64_tr_b16 v[162:163], v94 offset:4616\nds_read_b64_tr_b16 v[168:169], v94 offset:28672\nds_read_b64_tr_b16 v[170:171], v94 offset:29184\nds_read_b64_tr_b16 v[176:177], v94 offset:28680\nds_read_b64_tr_b16 v[178:179], v94 offset:29192\n" \
"s_waitcnt lgkmcnt(0)\nv_mfma_f32_16x16x16_bf16 v[8:11], v[152:153], v[6:7], v[8:11]\nv_mfma_f32_16x16x16_bf16 v[12:15], v[160:161], v[6:7], v[12:15]\nv_mfma_f32_16x16x16_bf16 v[16:19], v[168:169], v[6:7], v[16:19]\nv_mfma_f32_16x16x16_bf16 v[20:23], v[176:177], v[6:7], v[20:23]\nv_mfma_f32_16x16x16_bf16 v[8:11], v[154:155], v[6:7], v[8:11]\nv_mfma_f32_16x16x16_bf16 v[12:15], v[162:163], v[6:7], v[12:15]\nv_mfma_f32_16x16x16_bf16 v[16:19], v[170:171], v[6:7], v[16:19]\n" \
"v_mfma_f32_16x16x16_bf16 v[20:23], v[178:179], v[6:7], v[20:23]\ns_waitcnt vmcnt(28)\ns_barrier\ns_add_u32 m0, %[lw], 4096\ns_add_u32 s44, %[wo], 2523136\nbuffer_load_dwordx4 v92, %[rw], s44 offen nt lds\ns_add_u32 m0, %[lw], 5120\ns_add_u32 s44, %[wo], 2580480\n" \
"buffer_load_dwordx4 v92, %[rw], s44 offen nt lds\ns_add_u32 m0, %[lw], 6144\ns_add_u32 s44, %[wo], 2637824\nbuffer_load_dwordx4 v92, %[rw], s44 offen nt lds\ns_add_u32 m0, %[lw], 7168\ns_add_u32 s44, %[wo], 2695168\nbuffer_load_dwordx4 v92, %[rw], s44 offen nt lds\nds_read_b64_tr_b16 v[156:157], v94 offset:8192\n" \
"ds_read_b64_tr_b16 v[158:159], v94 offset:8704\nds_read_b64_tr_b16 v[164:165], v94 offset:8200\nds_read_b64_tr_b16 v[166:167], v94 offset:8712\nds_read_b64_tr_b16 v[172:173], v94 offset:32768\nds_read_b64_tr_b16 v[174:175], v94 offset:33280\nds_read_b64_tr_b16 v[180:181], v94 offset:32776\nds_read_b64_tr_b16 v[182:183], v94 offset:33288\ns_waitcnt lgkmcnt(0)\n" \
"v_mfma_f32_16x16x16_bf16 v[8:11], v[156:157], v[6:7], v[8:11]\nv_mfma_f32_16x16x16_bf16 v[12:15], v[164:165], v[6:7], v[12:15]\nv_mfma_f32_16x16x16_bf16 v[16:19], v[172:173], v[6:7], v[16:19]\nv_mfma_f32_16x16x16_bf16 v[20:23], v[180:181], v[6:7], v[20:23]\nv_mfma_f32_16x16x16_bf16 v[8:11], v[158:159], v[6:7], v[8:11]\nv_mfma_f32_16x16x16_bf16 v[12:15], v[166:167], v[6:7], v[12:15]\nv_mfma_f32_16x16x16_bf16 v[16:19], v[174:175], v[6:7], v[16:19]\nv_mfma_f32_16x16x16_bf16 v[20:23], v[182:183], v[6:7], v[20:23]\n" \
"s_waitcnt vmcnt(28)\ns_barrier\ns_add_u32 m0, %[lw], 8192\ns_add_u32 s44, %[wo], 2752512\nbuffer_load_dwordx4 v92, %[rw], s44 offen nt lds\ns_add_u32 m0, %[lw], 9216\ns_add_u32 s44, %[wo], 2809856\nbuffer_load_dwordx4 v92, %[rw], s44 offen nt lds\n" \
"s_add_u32 m0, %[lw], 10240\ns_add_u32 s44, %[wo], 2867200\nbuffer_load_dwordx4 v92, %[rw], s44 offen nt lds\ns_add_u32 m0, %[lw], 11264\ns_add_u32 s44, %[wo], 2924544\nbuffer_load_dwordx4 v92, %[rw], s44 offen nt lds\nds_read_b64_tr_b16 a[32:33], v94 offset:12288\nds_read_b64_tr_b16 a[34:35], v94 offset:12800\n" \
"ds_read_b64_tr_b16 v[88:89], v94 offset:12296\nds_read_b64_tr_b16 v[90:91], v94 offset:12808\nds_read_b64_tr_b16 a[40:41], v94 offset:36864\nds_read_b64_tr_b16 a[42:43], v94 offset:37376\nds_read_b64_tr_b16 a[48:49], v94 offset:36872\nds_read_b64_tr_b16 a[50:51], v94 offset:37384\ns_waitcnt lgkmcnt(0)\nv_mfma_f32_16x16x16_bf16 v[8:11], a[32:33], v[6:7], v[8:11]\n" \
"v_mfma_f32_16x16x16_bf16 v[12:15], v[88:89], v[6:7], v[12:15]\nv_mfma_f32_16x16x16_bf16 v[16:19], a[40:41], v[6:7], v[16:19]\nv_mfma_f32_16x16x16_bf16 v[20:23], a[48:49], v[6:7], v[20:23]\nv_mfma_f32_16x16x16_bf16 v[8:11], a[34:35], v[6:7], v[8:11]\nv_mfma_f32_16x16x16_bf16 v[12:15], v[90:91], v[6:7], v[12:15]\nv_mfma_f32_16x16x16_bf16 v[16:19], a[42:43], v[6:7], v[16:19]\nv_mfma_f32_16x16x16_bf16 v[20:23], a[50:51], v[6:7], v[20:23]\ns_waitcnt vmcnt(28)\n" \
"s_barrier\ns_add_u32 m0, %[lw], 12288\ns_add_u32 s44, %[wo], 2981888\nbuffer_load_dwordx4 v92, %[rw], s44 offen nt lds\ns_add_u32 m0, %[lw], 13312\ns_add_u32 s44, %[wo], 3039232\nbuffer_load_dwordx4 v92, %[rw], s44 offen nt lds\ns_add_u32 m0, %[lw], 14336\n" \
"s_add_u32 s44, %[wo], 3096576\nbuffer_load_dwordx4 v92, %[rw], s44 offen nt lds\ns_add_u32 m0, %[lw], 15360\ns_add_u32 s44, %[wo], 3153920\nbuffer_load_dwordx4 v92, %[rw], s44 offen nt lds\nds_read_b64_tr_b16 a[36:37], v94 offset:16384\nds_read_b64_tr_b16 a[38:39], v94 offset:16896\nds_read_b64_tr_b16 v[84:85], v94 offset:16392\n" \
"ds_read_b64_tr_b16 v[86:87], v94 offset:16904\nds_read_b64_tr_b16 a[44:45], v94 offset:40960\nds_read_b64_tr_b16 a[46:47], v94 offset:41472\nds_read_b64_tr_b16 a[52:53], v94 offset:40968\nds_read_b64_tr_b16 a[54:55], v94 offset:41480\ns_waitcnt lgkmcnt(0)\nv_mfma_f32_16x16x16_bf16 v[8:11], a[36:37], v[6:7], v[8:11]\nv_mfma_f32_16x16x16_bf16 v[12:15], v[84:85], v[6:7], v[12:15]\n" \
"v_mfma_f32_16x16x16_bf16 v[16:19], a[44:45], v[6:7], v[16:19]\nv_mfma_f32_16x16x16_bf16 v[20:23], a[52:53], v[6:7], v[20:23]\nv_mfma_f32_16x16x16_bf16 v[8:11], a[38:39], v[6:7], v[8:11]\nv_mfma_f32_16x16x16_bf16 v[12:15], v[86:87], v[6:7], v[12:15]\nv_mfma_f32_16x16x16_bf16 v[16:19], a[46:47], v[6:7], v[16:19]\nv_mfma_f32_16x16x16_bf16 v[20:23], a[54:55], v[6:7], v[20:23]\ns_waitcnt vmcnt(28)\ns_barrier\n" \
"s_add_u32 m0, %[lw], 16384\ns_add_u32 s44, %[wo], 3211264\nbuffer_load_dwordx4 v92, %[rw], s44 offen nt lds\ns_add_u32 m0, %[lw], 17408\ns_add_u32 s44, %[wo], 3268608\nbuffer_load_dwordx4 v92, %[rw], s44 offen nt lds\ns_add_u32 m0, %[lw], 18432\ns_add_u32 s44, %[wo], 3325952\n" \
"buffer_load_dwordx4 v92, %[rw], s44 offen nt lds\ns_add_u32 m0, %[lw], 19456\ns_add_u32 s44, %[wo], 3383296\nbuffer_load_dwordx4 v92, %[rw], s44 offen nt lds\nds_read_b64_tr_b16 a[56:57], v94 offset:20480\nds_read_b64_tr_b16 a[58:59], v94 offset:20992\nds_read_b64_tr_b16 a[64:65], v94 offset:20488\nds_read_b64_tr_b16 a[66:67], v94 offset:21000\n" \
"ds_read_b64_tr_b16 v[80:81], v94 offset:45056\nds_read_b64_tr_b16 v[82:83], v94 offset:45568\nds_read_b64_tr_b16 a[72:73], v94 offset:45064\nds_read_b64_tr_b16 a[74:75], v94 offset:45576\ns_waitcnt lgkmcnt(0)\nv_mfma_f32_16x16x16_bf16 v[8:11], a[56:57], v[6:7], v[8:11]\nv_mfma_f32_16x16x16_bf16 v[12:15], a[64:65], v[6:7], v[12:15]\nv_mfma_f32_16x16x16_bf16 v[16:19], v[80:81], v[6:7], v[16:19]\n" \
"v_mfma_f32_16x16x16_bf16 v[20:23], a[72:73], v[6:7], v[20:23]\nv_mfma_f32_16x16x16_bf16 v[8:11], a[58:59], v[6:7], v[8:11]\nv_mfma_f32_16x16x16_bf16 v[12:15], a[66:67], v[6:7], v[12:15]\nv_mfma_f32_16x16x16_bf16 v[16:19], v[82:83], v[6:7], v[16:19]\nv_mfma_f32_16x16x16_bf16 v[20:23], a[74:75], v[6:7], v[20:23]\ns_waitcnt vmcnt(28)\ns_barrier\ns_add_u32 m0, %[lw], 20480\n" \
"s_add_u32 s44, %[wo], 3440640\nbuffer_load_dwordx4 v92, %[rw], s44 offen nt lds\ns_add_u32 m0, %[lw], 21504\ns_add_u32 s44, %[wo], 3497984\nbuffer_load_dwordx4 v92, %[rw], s44 offen nt lds\ns_add_u32 m0, %[lw], 22528\ns_add_u32 s44, %[wo], 3555328\nbuffer_load_dwordx4 v92, %[rw], s44 offen nt lds\n" \
"s_add_u32 m0, %[lw], 23552\ns_add_u32 s44, %[wo], 3612672\nbuffer_load_dwordx4 v92, %[rw], s44 offen nt lds\nds_read_b64_tr_b16 a[60:61], v93 offset:0\nds_read_b64_tr_b16 a[62:63], v93 offset:512\nds_read_b64_tr_b16 a[68:69], v93 offset:8\nds_read_b64_tr_b16 a[70:71], v93 offset:520\nds_read_b64_tr_b16 v[76:77], v93 offset:12288\n" \
"ds_read_b64_tr_b16 v[78:79], v93 offset:12800\nds_read_b64_tr_b16 a[76:77], v93 offset:12296\nds_read_b64_tr_b16 a[78:79], v93 offset:12808\ns_waitcnt lgkmcnt(0)\nv_mfma_f32_16x16x16_bf16 v[8:11], a[60:61], v[6:7], v[8:11]\nv_mfma_f32_16x16x16_bf16 v[12:15], a[68:69], v[6:7], v[12:15]\nv_mfma_f32_16x16x16_bf16 v[16:19], v[76:77], v[6:7], v[16:19]\nv_mfma_f32_16x16x16_bf16 v[20:23], a[76:77], v[6:7], v[20:23]\n" \
"v_mfma_f32_16x16x16_bf16 v[8:11], a[62:63], v[6:7], v[8:11]\nv_mfma_f32_16x16x16_bf16 v[12:15], a[70:71], v[6:7], v[12:15]\nv_mfma_f32_16x16x16_bf16 v[16:19], v[78:79], v[6:7], v[16:19]\nv_mfma_f32_16x16x16_bf16 v[20:23], a[78:79], v[6:7], v[20:23]\ns_waitcnt vmcnt(28)\ns_barrier\ns_add_u32 m0, %[tb], 0\ns_add_u32 s44, %[wo], 4128768\n" \
"buffer_load_dwordx4 v92, %[rw], s44 offen nt lds\ns_add_u32 m0, %[tb], 1024\ns_add_u32 s44, %[wo], 4186112\nbuffer_load_dwordx4 v92, %[rw], s44 offen nt lds\ns_add_u32 m0, %[tb], 2048\ns_add_u32 s44, %[wo], 4243456\nbuffer_load_dwordx4 v92, %[rw], s44 offen nt lds\ns_add_u32 m0, %[tb], 3072\n" \
"s_add_u32 s44, %[wo], 4300800\nbuffer_load_dwordx4 v92, %[rw], s44 offen nt lds\nds_read_b64_tr_b16 a[80:81], v93 offset:4096\nds_read_b64_tr_b16 a[82:83], v93 offset:4608\nds_read_b64_tr_b16 a[88:89], v93 offset:4104\nds_read_b64_tr_b16 a[90:91], v93 offset:4616\nds_read_b64_tr_b16 a[96:97], v93 offset:16384\nds_read_b64_tr_b16 a[98:99], v93 offset:16896\n" \
"ds_read_b64_tr_b16 v[72:73], v93 offset:16392\nds_read_b64_tr_b16 v[74:75], v93 offset:16904\ns_waitcnt lgkmcnt(0)\nv_mfma_f32_16x16x16_bf16 v[8:11], a[80:81], v[6:7], v[8:11]\nv_mfma_f32_16x16x16_bf16 v[12:15], a[88:89], v[6:7], v[12:15]\nv_mfma_f32_16x16x16_bf16 v[16:19], a[96:97], v[6:7], v[16:19]\nv_mfma_f32_16x16x16_bf16 v[20:23], v[72:73], v[6:7], v[20:23]\nv_mfma_f32_16x16x16_bf16 v[8:11], a[82:83], v[6:7], v[8:11]\n" \
"v_mfma_f32_16x16x16_bf16 v[12:15], a[90:91], v[6:7], v[12:15]\nv_mfma_f32_16x16x16_bf16 v[16:19], a[98:99], v[6:7], v[16:19]\nv_mfma_f32_16x16x16_bf16 v[20:23], v[74:75], v[6:7], v[20:23]\ns_waitcnt vmcnt(28)\ns_barrier\ns_add_u32 m0, %[tb], 4096\ns_add_u32 s44, %[wo], 4358144\nbuffer_load_dwordx4 v92, %[rw], s44 offen nt lds\n" \
"s_add_u32 m0, %[tb], 5120\ns_add_u32 s44, %[wo], 4415488\nbuffer_load_dwordx4 v92, %[rw], s44 offen nt lds\ns_add_u32 m0, %[tb], 6144\ns_add_u32 s44, %[wo], 4472832\nbuffer_load_dwordx4 v92, %[rw], s44 offen nt lds\ns_add_u32 m0, %[tb], 7168\ns_add_u32 s44, %[wo], 4530176\n" \
"buffer_load_dwordx4 v92, %[rw], s44 offen nt lds\nds_read_b64_tr_b16 a[84:85], v93 offset:8192\nds_read_b64_tr_b16 a[86:87], v93 offset:8704\nds_read_b64_tr_b16 a[92:93], v93 offset:8200\nds_read_b64_tr_b16 a[94:95], v93 offset:8712\nds_read_b64_tr_b16 a[100:101], v93 offset:20480\nds_read_b64_tr_b16 a[102:103], v93 offset:20992\nds_read_b64_tr_b16 v[68:69], v93 offset:20488\n" \
"ds_read_b64_tr_b16 v[70:71], v93 offset:21000\ns_waitcnt lgkmcnt(0)\nv_mfma_f32_16x16x16_bf16 v[8:11], a[84:85], v[6:7], v[8:11]\nv_mfma_f32_16x16x16_bf16 v[12:15], a[92:93], v[6:7], v[12:15]\nv_mfma_f32_16x16x16_bf16 v[16:19], a[100:101], v[6:7], v[16:19]\nv_mfma_f32_16x16x16_bf16 v[20:23], v[68:69], v[6:7], v[20:23]\nv_mfma_f32_16x16x16_bf16 v[8:11], a[86:87], v[6:7], v[8:11]\nv_mfma_f32_16x16x16_bf16 v[12:15], a[94:95], v[6:7], v[12:15]\n" \
"v_mfma_f32_16x16x16_bf16 v[16:19], a[102:103], v[6:7], v[16:19]\nv_mfma_f32_16x16x16_bf16 v[20:23], v[70:71], v[6:7], v[20:23]\ns_waitcnt vmcnt(28)\ns_barrier\ns_add_u32 m0, %[tb], 8192\ns_add_u32 s44, %[wo], 4587520\nbuffer_load_dwordx4 v92, %[rw], s44 offen nt lds\ns_add_u32 m0, %[tb], 9216\n" \
"s_add_u32 s44, %[wo], 4644864\nbuffer_load_dwordx4 v92, %[rw], s44 offen nt lds\ns_add_u32 m0, %[tb], 10240\ns_add_u32 s44, %[wo], 4702208\nbuffer_load_dwordx4 v92, %[rw], s44 offen nt lds\ns_add_u32 m0, %[tb], 11264\ns_add_u32 s44, %[wo], 4759552\nbuffer_load_dwordx4 v92, %[rw], s44 offen nt lds\n" \
"ds_read_b64_tr_b16 a[136:137], v94 offset:0\nds_read_b64_tr_b16 a[138:139], v94 offset:512\nds_read_b64_tr_b16 v[64:65], v94 offset:8\nds_read_b64_tr_b16 v[66:67], v94 offset:520\nds_read_b64_tr_b16 a[144:145], v94 offset:24576\nds_read_b64_tr_b16 a[146:147], v94 offset:25088\nds_read_b64_tr_b16 a[152:153], v94 offset:24584\nds_read_b64_tr_b16 a[154:155], v94 offset:25096\n" \
"s_waitcnt lgkmcnt(0)\nv_mfma_f32_16x16x16_bf16 v[8:11], a[136:137], v[6:7], v[8:11]\nv_mfma_f32_16x16x16_bf16 v[12:15], v[64:65], v[6:7], v[12:15]\nv_mfma_f32_16x16x16_bf16 v[16:19], a[144:145], v[6:7], v[16:19]\nv_mfma_f32_16x16x16_bf16 v[20:23], a[152:153], v[6:7], v[20:23]\nv_mfma_f32_16x16x16_bf16 v[8:11], a[138:139], v[6:7], v[8:11]\nv_mfma_f32_16x16x16_bf16 v[12:15], v[66:67], v[6:7], v[12:15]\nv_mfma_f32_16x16x16_bf16 v[16:19], a[146:147], v[6:7], v[16:19]\n" \
"v_mfma_f32_16x16x16_bf16 v[20:23], a[154:155], v[6:7], v[20:23]\ns_waitcnt vmcnt(28)\ns_barrier\ns_add_u32 m0, %[lw], 0\ns_add_u32 s44, %[wo], 4816896\nbuffer_load_dwordx4 v92, %[rw], s44 offen nt lds\ns_add_u32 m0, %[lw], 1024\ns_add_u32 s44, %[wo], 4874240\n" \
"buffer_load_dwordx4 v92, %[rw], s44 offen nt lds\ns_add_u32 m0, %[lw], 2048\ns_add_u32 s44, %[wo], 4931584\nbuffer_load_dwordx4 v92, %[rw], s44 offen nt lds\ns_add_u32 m0, %[lw], 3072\ns_add_u32 s44, %[wo], 4988928\nbuffer_load_dwordx4 v92, %[rw], s44 offen nt lds\nds_read_b64_tr_b16 a[140:141], v94 offset:4096\n" \
"ds_read_b64_tr_b16 a[142:143], v94 offset:4608\nds_read_b64_tr_b16 v[60:61], v94 offset:4104\nds_read_b64_tr_b16 v[62:63], v94 offset:4616\nds_read_b64_tr_b16 a[148:149], v94 offset:28672\nds_read_b64_tr_b16 a[150:151], v94 offset:29184\nds_read_b64_tr_b16 a[156:157], v94 offset:28680\nds_read_b64_tr_b16 a[158:159], v94 offset:29192\ns_waitcnt lgkmcnt(0)\n" \
"v_mfma_f32_16x16x16_bf16 v[8:11], a[140:141], v[6:7], v[8:11]\nv_mfma_f32_16x16x16_bf16 v[12:15], v[60:61], v[6:7], v[12:15]\nv_mfma_f32_16x16x16_bf16 v[16:19], a[148:149], v[6:7], v[16:19]\nv_mfma_f32_16x16x16_bf16 v[20:23], a[156:157], v[6:7], v[20:23]\nv_mfma_f32_16x16x16_bf16 v[8:11], a[142:143], v[6:7], v[8:11]\nv_mfma_f32_16x16x16_bf16 v[12:15], v[62:63], v[6:7], v[12:15]\nv_mfma_f32_16x16x16_bf16 v[16:19], a[150:151], v[6:7], v[16:19]\nv_mfma_f32_16x16x16_bf16 v[20:23], a[158:159], v[6:7], v[20:23]\n" \
"s_waitcnt vmcnt(28)\ns_barrier\ns_add_u32 m0, %[lw], 4096\ns_add_u32 s44, %[wo], 5046272\nbuffer_load_dwordx4 v92, %[rw], s44 offen nt lds\ns_add_u32 m0, %[lw], 5120\ns_add_u32 s44, %[wo], 5103616\nbuffer_load_dwordx4 v92, %[rw], s44 offen nt lds\n" \
"s_add_u32 m0, %[lw], 6144\ns_add_u32 s44, %[wo], 5160960\nbuffer_load_dwordx4 v92, %[rw], s44 offen nt lds\ns_add_u32 m0, %[lw], 7168\ns_add_u32 s44, %[wo], 5218304\nbuffer_load_dwordx4 v92, %[rw], s44 offen nt lds\nds_read_b64_tr_b16 a[160:161], v94 offset:8192\nds_read_b64_tr_b16 a[162:163], v94 offset:8704\n" \
"ds_read_b64_tr_b16 a[168:169], v94 offset:8200\nds_read_b64_tr_b16 a[170:171], v94 offset:8712\nds_read_b64_tr_b16 v[56:57], v94 offset:32768\nds_read_b64_tr_b16 v[58:59], v94 offset:33280\nds_read_b64_tr_b16 a[176:177], v94 offset:32776\nds_read_b64_tr_b16 a[178:179], v94 offset:33288\ns_waitcnt lgkmcnt(0)\nv_mfma_f32_16x16x16_bf16 v[8:11], a[160:161], v[6:7], v[8:11]\n" \
"v_mfma_f32_16x16x16_bf16 v[12:15], a[168:169], v[6:7], v[12:15]\nv_mfma_f32_16x16x16_bf16 v[16:19], v[56:57], v[6:7], v[16:19]\nv_mfma_f32_16x16x16_bf16 v[20:23], a[176:177], v[6:7], v[20:23]\nv_mfma_f32_16x16x16_bf16 v[8:11], a[162:163], v[6:7], v[8:11]\nv_mfma_f32_16x16x16_bf16 v[12:15], a[170:171], v[6:7], v[12:15]\nv_mfma_f32_16x16x16_bf16 v[16:19], v[58:59], v[6:7], v[16:19]\nv_mfma_f32_16x16x16_bf16 v[20:23], a[178:179], v[6:7], v[20:23]\ns_waitcnt vmcnt(28)\n" \
"s_barrier\ns_add_u32 m0, %[lw], 8192\ns_add_u32 s44, %[wo], 5275648\nbuffer_load_dwordx4 v92, %[rw], s44 offen nt lds\ns_add_u32 m0, %[lw], 9216\ns_add_u32 s44, %[wo], 5332992\nbuffer_load_dwordx4 v92, %[rw], s44 offen nt lds\ns_add_u32 m0, %[lw], 10240\n" \
"s_add_u32 s44, %[wo], 5390336\nbuffer_load_dwordx4 v92, %[rw], s44 offen nt lds\ns_add_u32 m0, %[lw], 11264\ns_add_u32 s44, %[wo], 5447680\nbuffer_load_dwordx4 v92, %[rw], s44 offen nt lds\nds_read_b64_tr_b16 a[164:165], v94 offset:12288\nds_read_b64_tr_b16 a[166:167], v94 offset:12800\nds_read_b64_tr_b16 a[172:173], v94 offset:12296\n" \
"ds_read_b64_tr_b16 a[174:175], v94 offset:12808\nds_read_b64_tr_b16 v[52:53], v94 offset:36864\nds_read_b64_tr_b16 v[54:55], v94 offset:37376\nds_read_b64_tr_b16 a[180:181], v94 offset:36872\nds_read_b64_tr_b16 a[182:183], v94 offset:37384\ns_waitcnt lgkmcnt(0)\nv_mfma_f32_16x16x16_bf16 v[8:11], a[164:165], v[6:7], v[8:11]\nv_mfma_f32_16x16x16_bf16 v[12:15], a[172:173], v[6:7], v[12:15]\n" \
"v_mfma_f32_16x16x16_bf16 v[16:19], v[52:53], v[6:7], v[16:19]\nv_mfma_f32_16x16x16_bf16 v[20:23], a[180:181], v[6:7], v[20:23]\nv_mfma_f32_16x16x16_bf16 v[8:11], a[166:167], v[6:7], v[8:11]\nv_mfma_f32_16x16x16_bf16 v[12:15], a[174:175], v[6:7], v[12:15]\nv_mfma_f32_16x16x16_bf16 v[16:19], v[54:55], v[6:7], v[16:19]\nv_mfma_f32_16x16x16_bf16 v[20:23], a[182:183], v[6:7], v[20:23]\ns_waitcnt vmcnt(28)\ns_barrier\n" \
"s_add_u32 m0, %[lw], 12288\ns_add_u32 s44, %[wo], 5963776\nbuffer_load_dwordx4 v92, %[rw], s44 offen nt lds\ns_add_u32 m0, %[lw], 13312\ns_add_u32 s44, %[wo], 6021120\nbuffer_load_dwordx4 v92, %[rw], s44 offen nt lds\ns_add_u32 m0, %[lw], 14336\ns_add_u32 s44, %[wo], 6078464\n" \
"buffer_load_dwordx4 v92, %[rw], s44 offen nt lds\ns_add_u32 m0, %[lw], 15360\ns_add_u32 s44, %[wo], 6135808\nbuffer_load_dwordx4 v92, %[rw], s44 offen nt lds\nds_read_b64_tr_b16 a[184:185], v94 offset:16384\nds_read_b64_tr_b16 a[186:187], v94 offset:16896\nds_read_b64_tr_b16 a[192:193], v94 offset:16392\nds_read_b64_tr_b16 a[194:195], v94 offset:16904\n" \
"ds_read_b64_tr_b16 a[200:201], v94 offset:40960\nds_read_b64_tr_b16 a[202:203], v94 offset:41472\nds_read_b64_tr_b16 v[48:49], v94 offset:40968\nds_read_b64_tr_b16 v[50:51], v94 offset:41480\ns_waitcnt lgkmcnt(0)\nv_mfma_f32_16x16x16_bf16 v[8:11], a[184:185], v[6:7], v[8:11]\nv_mfma_f32_16x16x16_bf16 v[12:15], a[192:193], v[6:7], v[12:15]\nv_mfma_f32_16x16x16_bf16 v[16:19], a[200:201], v[6:7], v[16:19]\n" \
"v_mfma_f32_16x16x16_bf16 v[20:23], v[48:49], v[6:7], v[20:23]\nv_mfma_f32_16x16x16_bf16 v[8:11], a[186:187], v[6:7], v[8:11]\nv_mfma_f32_16x16x16_bf16 v[12:15], a[194:195], v[6:7], v[12:15]\nv_mfma_f32_16x16x16_bf16 v[16:19], a[202:203], v[6:7], v[16:19]\nv_mfma_f32_16x16x16_bf16 v[20:23], v[50:51], v[6:7], v[20:23]\ns_waitcnt vmcnt(28)\ns_barrier\ns_add_u32 m0, %[lw], 16384\n" \
"s_add_u32 s44, %[wo], 6193152\nbuffer_load_dwordx4 v92, %[rw], s44 offen nt lds\ns_add_u32 m0, %[lw], 17408\ns_add_u32 s44, %[wo], 6250496\nbuffer_load_dwordx4 v92, %[rw], s44 offen nt lds\ns_add_u32 m0, %[lw], 18432\ns_add_u32 s44, %[wo], 6307840\nbuffer_load_dwordx4 v92, %[rw], s44 offen nt lds\n" \
"s_add_u32 m0, %[lw], 19456\ns_add_u32 s44, %[wo], 6365184\nbuffer_load_dwordx4 v92, %[rw], s44 offen nt lds\nds_read_b64_tr_b16 a[188:189], v94 offset:20480\nds_read_b64_tr_b16 a[190:191], v94 offset:20992\nds_read_b64_tr_b16 a[196:197], v94 offset:20488\nds_read_b64_tr_b16 a[198:199], v94 offset:21000\nds_read_b64_tr_b16 a[204:205], v94 offset:45056\n" \
"ds_read_b64_tr_b16 a[206:207], v94 offset:45568\nds_read_b64_tr_b16 v[44:45], v94 offset:45064\nds_read_b64_tr_b16 v[46:47], v94 offset:45576\ns_waitcnt lgkmcnt(0)\nv_mfma_f32_16x16x16_bf16 v[8:11], a[188:189], v[6:7], v[8:11]\nv_mfma_f32_16x16x16_bf16 v[12:15], a[196:197], v[6:7], v[12:15]\nv_mfma_f32_16x16x16_bf16 v[16:19], a[204:205], v[6:7], v[16:19]\nv_mfma_f32_16x16x16_bf16 v[20:23], v[44:45], v[6:7], v[20:23]\n" \
"v_mfma_f32_16x16x16_bf16 v[8:11], a[190:191], v[6:7], v[8:11]\nv_mfma_f32_16x16x16_bf16 v[12:15], a[198:199], v[6:7], v[12:15]\nv_mfma_f32_16x16x16_bf16 v[16:19], a[206:207], v[6:7], v[16:19]\nv_mfma_f32_16x16x16_bf16 v[20:23], v[46:47], v[6:7], v[20:23]\ns_waitcnt vmcnt(28)\ns_barrier\ns_add_u32 m0, %[lw], 20480\ns_add_u32 s44, %[wo], 6422528\n" \
"buffer_load_dwordx4 v92, %[rw], s44 offen nt lds\ns_add_u32 m0, %[lw], 21504\ns_add_u32 s44, %[wo], 6479872\nbuffer_load_dwordx4 v92, %[rw], s44 offen nt lds\ns_add_u32 m0, %[lw], 22528\ns_add_u32 s44, %[wo], 6537216\nbuffer_load_dwordx4 v92, %[rw], s44 offen nt lds\ns_add_u32 m0, %[lw], 23552\n" \
"s_add_u32 s44, %[wo], 6594560\nbuffer_load_dwordx4 v92, %[rw], s44 offen nt lds\nds_read_b64_tr_b16 a[240:241], v93 offset:0\nds_read_b64_tr_b16 a[242:243], v93 offset:512\nds_read_b64_tr_b16 v[40:41], v93 offset:8\nds_read_b64_tr_b16 v[42:43], v93 offset:520\nds_read_b64_tr_b16 a[248:249], v93 offset:12288\nds_read_b64_tr_b16 a[250:251], v93 offset:12800\n" \
"ds_read_b64_tr_b16 v[96:97], v93 offset:12296\nds_read_b64_tr_b16 v[98:99], v93 offset:12808\ns_waitcnt lgkmcnt(0)\nv_mfma_f32_16x16x16_bf16 v[8:11], a[240:241], v[6:7], v[8:11]\nv_mfma_f32_16x16x16_bf16 v[12:15], v[40:41], v[6:7], v[12:15]\nv_mfma_f32_16x16x16_bf16 v[16:19], a[248:249], v[6:7], v[16:19]\nv_mfma_f32_16x16x16_bf16 v[20:23], v[96:97], v[6:7], v[20:23]\nv_mfma_f32_16x16x16_bf16 v[8:11], a[242:243], v[6:7], v[8:11]\n" \
"v_mfma_f32_16x16x16_bf16 v[12:15], v[42:43], v[6:7], v[12:15]\nv_mfma_f32_16x16x16_bf16 v[16:19], a[250:251], v[6:7], v[16:19]\nv_mfma_f32_16x16x16_bf16 v[20:23], v[98:99], v[6:7], v[20:23]\ns_waitcnt vmcnt(28)\ns_barrier\ns_add_u32 m0, %[tb], 0\ns_add_u32 s44, %[wo], 6651904\nbuffer_load_dwordx4 v92, %[rw], s44 offen nt lds\n" \
"s_add_u32 m0, %[tb], 1024\ns_add_u32 s44, %[wo], 6709248\nbuffer_load_dwordx4 v92, %[rw], s44 offen nt lds\ns_add_u32 m0, %[tb], 2048\ns_add_u32 s44, %[wo], 6766592\nbuffer_load_dwordx4 v92, %[rw], s44 offen nt lds\ns_add_u32 m0, %[tb], 3072\ns_add_u32 s44, %[wo], 6823936\n" \
"buffer_load_dwordx4 v92, %[rw], s44 offen nt lds\nds_read_b64_tr_b16 a[244:245], v93 offset:4096\nds_read_b64_tr_b16 a[246:247], v93 offset:4608\nds_read_b64_tr_b16 v[36:37], v93 offset:4104\nds_read_b64_tr_b16 v[38:39], v93 offset:4616\nds_read_b64_tr_b16 a[252:253], v93 offset:16384\nds_read_b64_tr_b16 a[254:255], v93 offset:16896\nds_read_b64_tr_b16 v[100:101], v93 offset:16392\n" \
"ds_read_b64_tr_b16 v[102:103], v93 offset:16904\ns_waitcnt lgkmcnt(0)\nv_mfma_f32_16x16x16_bf16 v[8:11], a[244:245], v[6:7], v[8:11]\nv_mfma_f32_16x16x16_bf16 v[12:15], v[36:37], v[6:7], v[12:15]\nv_mfma_f32_16x16x16_bf16 v[16:19], a[252:253], v[6:7], v[16:19]\nv_mfma_f32_16x16x16_bf16 v[20:23], v[100:101], v[6:7], v[20:23]\nv_mfma_f32_16x16x16_bf16 v[8:11], a[246:247], v[6:7], v[8:11]\nv_mfma_f32_16x16x16_bf16 v[12:15], v[38:39], v[6:7], v[12:15]\n" \
"v_mfma_f32_16x16x16_bf16 v[16:19], a[254:255], v[6:7], v[16:19]\nv_mfma_f32_16x16x16_bf16 v[20:23], v[102:103], v[6:7], v[20:23]\ns_waitcnt vmcnt(28)\ns_barrier\ns_add_u32 m0, %[tb], 4096\ns_add_u32 s44, %[wo], 6881280\nbuffer_load_dwordx4 v92, %[rw], s44 offen nt lds\ns_add_u32 m0, %[tb], 5120\n" \
"s_add_u32 s44, %[wo], 6938624\nbuffer_load_dwordx4 v92, %[rw], s44 offen nt lds\ns_add_u32 m0, %[tb], 6144\ns_add_u32 s44, %[wo], 6995968\nbuffer_load_dwordx4 v92, %[rw], s44 offen nt lds\ns_add_u32 m0, %[tb], 7168\ns_add_u32 s44, %[wo], 7053312\nbuffer_load_dwordx4 v92, %[rw], s44 offen nt lds\n" \
"ds_read_b64_tr_b16 v[104:105], v93 offset:8192\nds_read_b64_tr_b16 v[106:107], v93 offset:8704\nds_read_b64_tr_b16 v[112:113], v93 offset:8200\nds_read_b64_tr_b16 v[114:115], v93 offset:8712\nds_read_b64_tr_b16 v[32:33], v93 offset:20480\nds_read_b64_tr_b16 v[34:35], v93 offset:20992\nds_read_b64_tr_b16 v[120:121], v93 offset:20488\nds_read_b64_tr_b16 v[122:123], v93 offset:21000\n" \
"s_waitcnt lgkmcnt(0)\nv_mfma_f32_16x16x16_bf16 v[8:11], v[104:105], v[6:7], v[8:11]\nv_mfma_f32_16x16x16_bf16 v[12:15], v[112:113], v[6:7], v[12:15]\nv_mfma_f32_16x16x16_bf16 v[16:19], v[32:33], v[6:7], v[16:19]\nv_mfma_f32_16x16x16_bf16 v[20:23], v[120:121], v[6:7], v[20:23]\nv_mfma_f32_16x16x16_bf16 v[8:11], v[106:107], v[6:7], v[8:11]\nv_mfma_f32_16x16x16_bf16 v[12:15], v[114:115], v[6:7], v[12:15]\nv_mfma_f32_16x16x16_bf16 v[16:19], v[34:35], v[6:7], v[16:19]\n" \
"v_mfma_f32_16x16x16_bf16 v[20:23], v[122:123], v[6:7], v[20:23]\ns_waitcnt vmcnt(28)\ns_barrier\ns_add_u32 m0, %[tb], 8192\ns_add_u32 s44, %[wo], 7110656\nbuffer_load_dwordx4 v92, %[rw], s44 offen nt lds\ns_add_u32 m0, %[tb], 9216\ns_add_u32 s44, %[wo], 7168000\n" \
"buffer_load_dwordx4 v92, %[rw], s44 offen nt lds\ns_add_u32 m0, %[tb], 10240\ns_add_u32 s44, %[wo], 7225344\nbuffer_load_dwordx4 v92, %[rw], s44 offen nt lds\ns_add_u32 m0, %[tb], 11264\ns_add_u32 s44, %[wo], 7282688\nbuffer_load_dwordx4 v92, %[rw], s44 offen nt lds\nds_read_b64_tr_b16 v[108:109], v94 offset:0\n" \
"ds_read_b64_tr_b16 v[110:111], v94 offset:512\nds_read_b64_tr_b16 v[116:117], v94 offset:8\nds_read_b64_tr_b16 v[118:119], v94 offset:520\nds_read_b64_tr_b16 v[28:29], v94 offset:24576\nds_read_b64_tr_b16 v[30:31], v94 offset:25088\nds_read_b64_tr_b16 v[124:125], v94 offset:24584\nds_read_b64_tr_b16 v[126:127], v94 offset:25096\ns_waitcnt lgkmcnt(0)\n" \
"v_mfma_f32_16x16x16_bf16 v[8:11], v[108:109], v[6:7], v[8:11]\nv_mfma_f32_16x16x16_bf16 v[12:15], v[116:117], v[6:7], v[12:15]\nv_mfma_f32_16x16x16_bf16 v[16:19], v[28:29], v[6:7], v[16:19]\nv_mfma_f32_16x16x16_bf16 v[20:23], v[124:125], v[6:7], v[20:23]\nv_mfma_f32_16x16x16_bf16 v[8:11], v[110:111], v[6:7], v[8:11]\nv_mfma_f32_16x16x16_bf16 v[12:15], v[118:119], v[6:7], v[12:15]\nv_mfma_f32_16x16x16_bf16 v[16:19], v[30:31], v[6:7], v[16:19]\nv_mfma_f32_16x16x16_bf16 v[20:23], v[126:127], v[6:7], v[20:23]\n" \
"s_waitcnt vmcnt(28)\ns_barrier\nds_read_b64_tr_b16 v[128:129], v94 offset:4096\nds_read_b64_tr_b16 v[130:131], v94 offset:4608\nds_read_b64_tr_b16 v[136:137], v94 offset:4104\nds_read_b64_tr_b16 v[138:139], v94 offset:4616\nds_read_b64_tr_b16 v[144:145], v94 offset:28672\nds_read_b64_tr_b16 v[146:147], v94 offset:29184\n" \
"ds_read_b64_tr_b16 v[24:25], v94 offset:28680\nds_read_b64_tr_b16 v[26:27], v94 offset:29192\ns_waitcnt lgkmcnt(0)\nv_mfma_f32_16x16x16_bf16 v[8:11], v[128:129], v[6:7], v[8:11]\nv_mfma_f32_16x16x16_bf16 v[12:15], v[136:137], v[6:7], v[12:15]\nv_mfma_f32_16x16x16_bf16 v[16:19], v[144:145], v[6:7], v[16:19]\nv_mfma_f32_16x16x16_bf16 v[20:23], v[24:25], v[6:7], v[20:23]\nv_mfma_f32_16x16x16_bf16 v[8:11], v[130:131], v[6:7], v[8:11]\n" \
"v_mfma_f32_16x16x16_bf16 v[12:15], v[138:139], v[6:7], v[12:15]\nv_mfma_f32_16x16x16_bf16 v[16:19], v[146:147], v[6:7], v[16:19]\nv_mfma_f32_16x16x16_bf16 v[20:23], v[26:27], v[6:7], v[20:23]\ns_waitcnt vmcnt(24)\nds_write_b128 v1, v[88:91] offset:0\nds_write_b128 v1, v[84:87] offset:1024\nds_write_b128 v1, v[80:83] offset:2048\nds_write_b128 v1, v[76:79] offset:3072\n" \
"s_barrier\nds_read_b64_tr_b16 v[132:133], v94 offset:8192\nds_read_b64_tr_b16 v[134:135], v94 offset:8704\nds_read_b64_tr_b16 v[140:141], v94 offset:8200\nds_read_b64_tr_b16 v[142:143], v94 offset:8712\nds_read_b64_tr_b16 v[148:149], v94 offset:32768\nds_read_b64_tr_b16 v[150:151], v94 offset:33280\nds_read_b64_tr_b16 v[76:77], v94 offset:32776\n" \
"ds_read_b64_tr_b16 v[78:79], v94 offset:33288\ns_waitcnt lgkmcnt(0)\nv_mfma_f32_16x16x16_bf16 v[8:11], v[132:133], v[6:7], v[8:11]\nv_mfma_f32_16x16x16_bf16 v[12:15], v[140:141], v[6:7], v[12:15]\nv_mfma_f32_16x16x16_bf16 v[16:19], v[148:149], v[6:7], v[16:19]\nv_mfma_f32_16x16x16_bf16 v[20:23], v[76:77], v[6:7], v[20:23]\nv_mfma_f32_16x16x16_bf16 v[8:11], v[134:135], v[6:7], v[8:11]\nv_mfma_f32_16x16x16_bf16 v[12:15], v[142:143], v[6:7], v[12:15]\n" \
"v_mfma_f32_16x16x16_bf16 v[16:19], v[150:151], v[6:7], v[16:19]\nv_mfma_f32_16x16x16_bf16 v[20:23], v[78:79], v[6:7], v[20:23]\ns_waitcnt vmcnt(20)\nds_write_b128 v1, v[72:75] offset:4096\nds_write_b128 v1, v[68:71] offset:5120\nds_write_b128 v1, v[64:67] offset:6144\nds_write_b128 v1, v[60:63] offset:7168\ns_barrier\n" \
"ds_read_b64_tr_b16 v[184:185], v94 offset:12288\nds_read_b64_tr_b16 v[186:187], v94 offset:12800\nds_read_b64_tr_b16 v[60:61], v94 offset:12296\nds_read_b64_tr_b16 v[62:63], v94 offset:12808\nds_read_b64_tr_b16 v[192:193], v94 offset:36864\nds_read_b64_tr_b16 v[194:195], v94 offset:37376\nds_read_b64_tr_b16 v[200:201], v94 offset:36872\nds_read_b64_tr_b16 v[202:203], v94 offset:37384\n" \
"s_waitcnt lgkmcnt(0)\nv_mfma_f32_16x16x16_bf16 v[8:11], v[184:185], v[6:7], v[8:11]\nv_mfma_f32_16x16x16_bf16 v[12:15], v[60:61], v[6:7], v[12:15]\nv_mfma_f32_16x16x16_bf16 v[16:19], v[192:193], v[6:7], v[16:19]\nv_mfma_f32_16x16x16_bf16 v[20:23], v[200:201], v[6:7], v[20:23]\nv_mfma_f32_16x16x16_bf16 v[8:11], v[186:187], v[6:7], v[8:11]\nv_mfma_f32_16x16x16_bf16 v[12:15], v[62:63], v[6:7], v[12:15]\nv_mfma_f32_16x16x16_bf16 v[16:19], v[194:195], v[6:7], v[16:19]\n" \
"v_mfma_f32_16x16x16_bf16 v[20:23], v[202:203], v[6:7], v[20:23]\ns_waitcnt vmcnt(16)\nds_write_b128 v1, v[56:59] offset:8192\nds_write_b128 v1, v[52:55] offset:9216\nds_write_b128 v1, v[48:51] offset:10240\nds_write_b128 v1, v[44:47] offset:11264\ns_barrier\nds_read_b64_tr_b16 v[188:189], v94 offset:16384\n" \
"ds_read_b64_tr_b16 v[190:191], v94 offset:16896\nds_read_b64_tr_b16 v[44:45], v94 offset:16392\nds_read_b64_tr_b16 v[46:47], v94 offset:16904\nds_read_b64_tr_b16 v[196:197], v94 offset:40960\nds_read_b64_tr_b16 v[198:199], v94 offset:41472\nds_read_b64_tr_b16 v[204:205], v94 offset:40968\nds_read_b64_tr_b16 v[206:207], v94 offset:41480\ns_waitcnt lgkmcnt(0)\n" \
"v_mfma_f32_16x16x16_bf16 v[8:11], v[188:189], v[6:7], v[8:11]\nv_mfma_f32_16x16x16_bf16 v[12:15], v[44:45], v[6:7], v[12:15]\nv_mfma_f32_16x16x16_bf16 v[16:19], v[196:197], v[6:7], v[16:19]\nv_mfma_f32_16x16x16_bf16 v[20:23], v[204:205], v[6:7], v[20:23]\nv_mfma_f32_16x16x16_bf16 v[8:11], v[190:191], v[6:7], v[8:11]\nv_mfma_f32_16x16x16_bf16 v[12:15], v[46:47], v[6:7], v[12:15]\nv_mfma_f32_16x16x16_bf16 v[16:19], v[198:199], v[6:7], v[16:19]\nv_mfma_f32_16x16x16_bf16 v[20:23], v[206:207], v[6:7], v[20:23]\n" \
"s_waitcnt vmcnt(12)\nds_write_b128 v1, v[40:43] offset:12288\nds_write_b128 v1, v[36:39] offset:13312\nds_write_b128 v1, v[32:35] offset:14336\nds_write_b128 v1, v[28:31] offset:15360\ns_barrier\nds_read_b64_tr_b16 v[208:209], v94 offset:20480\nds_read_b64_tr_b16 v[210:211], v94 offset:20992\n" \
"ds_read_b64_tr_b16 v[216:217], v94 offset:20488\nds_read_b64_tr_b16 v[218:219], v94 offset:21000\nds_read_b64_tr_b16 v[28:29], v94 offset:45056\nds_read_b64_tr_b16 v[30:31], v94 offset:45568\nds_read_b64_tr_b16 v[224:225], v94 offset:45064\nds_read_b64_tr_b16 v[226:227], v94 offset:45576\ns_waitcnt lgkmcnt(0)\nv_mfma_f32_16x16x16_bf16 v[8:11], v[208:209], v[6:7], v[8:11]\n" \
"v_mfma_f32_16x16x16_bf16 v[12:15], v[216:217], v[6:7], v[12:15]\nv_mfma_f32_16x16x16_bf16 v[16:19], v[28:29], v[6:7], v[16:19]\nv_mfma_f32_16x16x16_bf16 v[20:23], v[224:225], v[6:7], v[20:23]\nv_mfma_f32_16x16x16_bf16 v[8:11], v[210:211], v[6:7], v[8:11]\nv_mfma_f32_16x16x16_bf16 v[12:15], v[218:219], v[6:7], v[12:15]\nv_mfma_f32_16x16x16_bf16 v[16:19], v[30:31], v[6:7], v[16:19]\nv_mfma_f32_16x16x16_bf16 v[20:23], v[226:227], v[6:7], v[20:23]\ns_waitcnt vmcnt(8)\n" \
"ds_write_b128 v1, v[24:27] offset:16384\nds_write_b128 v1, v[76:79] offset:17408\nds_write_b128 v1, v[60:63] offset:18432\nds_write_b128 v1, v[44:47] offset:19456\ns_barrier\nds_read_b64_tr_b16 v[212:213], v93 offset:0\nds_read_b64_tr_b16 v[214:215], v93 offset:512\nds_read_b64_tr_b16 v[220:221], v93 offset:8\n" \
"ds_read_b64_tr_b16 v[222:223], v93 offset:520\nds_read_b64_tr_b16 v[44:45], v93 offset:12288\nds_read_b64_tr_b16 v[46:47], v93 offset:12800\nds_read_b64_tr_b16 v[228:229], v93 offset:12296\nds_read_b64_tr_b16 v[230:231], v93 offset:12808\ns_waitcnt lgkmcnt(0)\nv_mfma_f32_16x16x16_bf16 v[8:11], v[212:213], v[6:7], v[8:11]\nv_mfma_f32_16x16x16_bf16 v[12:15], v[220:221], v[6:7], v[12:15]\n" \
"v_mfma_f32_16x16x16_bf16 v[16:19], v[44:45], v[6:7], v[16:19]\nv_mfma_f32_16x16x16_bf16 v[20:23], v[228:229], v[6:7], v[20:23]\nv_mfma_f32_16x16x16_bf16 v[8:11], v[214:215], v[6:7], v[8:11]\nv_mfma_f32_16x16x16_bf16 v[12:15], v[222:223], v[6:7], v[12:15]\nv_mfma_f32_16x16x16_bf16 v[16:19], v[46:47], v[6:7], v[16:19]\nv_mfma_f32_16x16x16_bf16 v[20:23], v[230:231], v[6:7], v[20:23]\ns_waitcnt vmcnt(4)\nds_write_b128 v1, v[28:31] offset:20480\n" \
"ds_write_b128 v1, v[44:47] offset:21504\ns_barrier\nds_read_b64_tr_b16 v[232:233], v93 offset:4096\nds_read_b64_tr_b16 v[234:235], v93 offset:4608\nds_read_b64_tr_b16 v[240:241], v93 offset:4104\nds_read_b64_tr_b16 v[242:243], v93 offset:4616\nds_read_b64_tr_b16 v[248:249], v93 offset:16384\nds_read_b64_tr_b16 v[250:251], v93 offset:16896\n" \
"ds_read_b64_tr_b16 v[44:45], v93 offset:16392\nds_read_b64_tr_b16 v[46:47], v93 offset:16904\ns_waitcnt lgkmcnt(0)\nv_mfma_f32_16x16x16_bf16 v[8:11], v[232:233], v[6:7], v[8:11]\nv_mfma_f32_16x16x16_bf16 v[12:15], v[240:241], v[6:7], v[12:15]\nv_mfma_f32_16x16x16_bf16 v[16:19], v[248:249], v[6:7], v[16:19]\nv_mfma_f32_16x16x16_bf16 v[20:23], v[44:45], v[6:7], v[20:23]\nv_mfma_f32_16x16x16_bf16 v[8:11], v[234:235], v[6:7], v[8:11]\n" \
"v_mfma_f32_16x16x16_bf16 v[12:15], v[242:243], v[6:7], v[12:15]\nv_mfma_f32_16x16x16_bf16 v[16:19], v[250:251], v[6:7], v[16:19]\nv_mfma_f32_16x16x16_bf16 v[20:23], v[46:47], v[6:7], v[20:23]\ns_waitcnt vmcnt(0)\nds_write_b128 v1, v[44:47] offset:22528\ns_barrier\nds_read_b64_tr_b16 v[236:237], v93 offset:8192\nds_read_b64_tr_b16 v[238:239], v93 offset:8704\n" \
"ds_read_b64_tr_b16 v[244:245], v93 offset:8200\nds_read_b64_tr_b16 v[246:247], v93 offset:8712\nds_read_b64_tr_b16 v[252:253], v93 offset:20480\nds_read_b64_tr_b16 v[254:255], v93 offset:20992\nds_read_b64_tr_b16 v[44:45], v93 offset:20488\nds_read_b64_tr_b16 v[46:47], v93 offset:21000\ns_waitcnt lgkmcnt(0)\nv_mfma_f32_16x16x16_bf16 v[8:11], v[236:237], v[6:7], v[8:11]\n" \
"v_mfma_f32_16x16x16_bf16 v[12:15], v[244:245], v[6:7], v[12:15]\nv_mfma_f32_16x16x16_bf16 v[16:19], v[252:253], v[6:7], v[16:19]\nv_mfma_f32_16x16x16_bf16 v[20:23], v[44:45], v[6:7], v[20:23]\nv_mfma_f32_16x16x16_bf16 v[8:11], v[238:239], v[6:7], v[8:11]\nv_mfma_f32_16x16x16_bf16 v[12:15], v[246:247], v[6:7], v[12:15]\nv_mfma_f32_16x16x16_bf16 v[16:19], v[254:255], v[6:7], v[16:19]\nv_mfma_f32_16x16x16_bf16 v[20:23], v[46:47], v[6:7], v[20:23]\nds_write_b128 v1, v[44:47] offset:23552\n" \
"s_waitcnt lgkmcnt(0)\ns_barrier\ns_mov_b32 s40, 0\ns_mov_b32 s43, 0\ns_lshl_b32 s57, %[w], 12\ns_mov_b32 s58, 0xbeeff558\ns_mov_b32 s59, 0x409d30fc\ns_mov_b32 s56, -2147483648\n" \
".Lgg_next%=:\ns_cmp_lg_u32 %[w], 0\ns_cbranch_scc1 .Lgg_w1%=\nbuffer_load_dword v56, v3, %[rs], 0 offen sc1\ns_waitcnt vmcnt(0)\nbuffer_store_dword v90, off, %[rs], s56 sc1\n.Lgg_poll%=:\nv_cmp_gt_i32 vcc, s40, v56\n" \
"s_cbranch_vccz .Lgg_go%=\ns_cmp_ge_u32 s40, 62\ns_cbranch_scc1 .Lgg_ns1049%=\ns_sleep 12\n.Lgg_ns1049%=:\ns_sleep 1\nbuffer_load_dword v56, v3, %[rs], 0 offen sc1\ns_waitcnt vmcnt(0)\n" \
"s_add_u32 s43, s43, 1\ns_cmp_lt_u32 s43, 262144\ns_cbranch_scc1 .Lgg_poll%=\ns_branch .Lgg_abort%=\n.Lgg_w1%=:\ns_waitcnt vmcnt(0)\nbuffer_store_dword v90, off, %[rs], s56 sc1\n.Lgg_go%=:\n" \
"s_add_u32 s44, s40, 0\ns_lshl_b32 s44, s44, 17\ns_add_u32 s44, s44, %[xn]\ns_lshr_b32 s45, s44, 8\ns_and_b32 s45, s45, 0x2000\ns_xor_b32 s41, s44, s45\ns_add_u32 s61, s41, %[xw]\ns_barrier\n" \
"s_add_u32 m0, s57, 0\ns_add_u32 s60, s61, 0\nbuffer_load_dwordx4 v0, %[rs], s60 offen offset:0 sc1 lds\nbuffer_load_dwordx4 v0, %[rs], s60 offen offset:1024 sc1 lds\nbuffer_load_dwordx4 v0, %[rs], s60 offen offset:2048 sc1 lds\nbuffer_load_dwordx4 v0, %[rs], s60 offen offset:3072 sc1 lds\ns_add_u32 m0, s57, 16384\ns_add_u32 s60, s61, 32768\n" \
"buffer_load_dwordx4 v0, %[rs], s60 offen offset:0 sc1 lds\nbuffer_load_dwordx4 v0, %[rs], s60 offen offset:1024 sc1 lds\nbuffer_load_dwordx4 v0, %[rs], s60 offen offset:2048 sc1 lds\nbuffer_load_dwordx4 v0, %[rs], s60 offen offset:3072 sc1 lds\nbuffer_load_dwordx4 v[40:43], v5, %[rs], s41 offen offset:0 sc1\nbuffer_load_dwordx4 v[44:47], v5, %[rs], s41 offen offset:256 sc1\ns_lshl_b32 s44, s40, 19\ns_add_u32 s44, s44, %[ho]\n" \
"s_lshr_b32 s45, s44, 8\ns_and_b32 s45, s45, 0x2000\ns_xor_b32 s55, s44, s45\ns_waitcnt vmcnt(6)\ns_barrier\nds_read_b128 v[56:59], v2 offset:0\nds_read_b128 v[60:63], v2 offset:2048\nds_read_b128 v[64:67], v2 offset:4096\n" \
"s_waitcnt lgkmcnt(2)\nv_mfma_f32_16x16x32_bf16 v[24:27], a[0:3], v[56:59], 0\nds_read_b128 v[68:71], v2 offset:6144\nds_read_b128 v[80:83], v1 offset:0\nv_mfma_f32_16x16x32_bf16 v[28:31], a[8:11], v[56:59], 0\nbuffer_load_dwordx4 v[48:51], v5, %[rs], s41 offen offset:512 sc1\nv_mfma_f32_16x16x32_bf16 v[32:35], a[16:19], v[56:59], 0\nv_mfma_f32_16x16x32_bf16 v[36:39], a[24:27], v[56:59], 0\n" \
"s_waitcnt lgkmcnt(3)\nds_read_b128 v[72:75], v1 offset:1024\nv_mfma_f32_16x16x32_bf16 v[24:27], a[4:7], v[60:63], v[24:27]\nds_read_b128 v[56:59], v2 offset:8192\nv_mfma_f32_16x16x32_bf16 v[28:31], a[12:15], v[60:63], v[28:31]\nbuffer_load_dwordx4 v[52:55], v5, %[rs], s41 offen offset:768 sc1\nv_mfma_f32_16x16x32_bf16 v[32:35], a[20:23], v[60:63], v[32:35]\nv_mfma_f32_16x16x32_bf16 v[36:39], a[28:31], v[60:63], v[36:39]\n" \
"s_waitcnt lgkmcnt(4)\nds_read_b128 v[76:79], v1 offset:2048\nv_mfma_f32_16x16x32_bf16 v[24:27], a[32:35], v[64:67], v[24:27]\ns_waitcnt lgkmcnt(3)\nds_read_b128 v[60:63], v2 offset:10240\nv_mfma_f32_16x16x32_bf16 v[28:31], v[80:83], v[64:67], v[28:31]\ns_add_u32 m0, s57, 32768\ns_add_u32 s60, s61, 65536\n" \
"buffer_load_dwordx4 v0, %[rs], s60 offen offset:0 sc1 lds\nv_mfma_f32_16x16x32_bf16 v[32:35], a[40:43], v[64:67], v[32:35]\nv_mfma_f32_16x16x32_bf16 v[36:39], a[48:51], v[64:67], v[36:39]\nds_read_b128 v[80:83], v1 offset:3072\nv_mfma_f32_16x16x32_bf16 v[24:27], a[36:39], v[68:71], v[24:27]\ns_waitcnt lgkmcnt(4)\nds_read_b128 v[64:67], v2 offset:12288\nv_mfma_f32_16x16x32_bf16 v[28:31], v[72:75], v[68:71], v[28:31]\n" \
"buffer_load_dwordx4 v0, %[rs], s60 offen offset:1024 sc1 lds\nv_mfma_f32_16x16x32_bf16 v[32:35], a[44:47], v[68:71], v[32:35]\nv_mfma_f32_16x16x32_bf16 v[36:39], a[52:55], v[68:71], v[36:39]\ns_waitcnt lgkmcnt(4)\nds_read_b128 v[72:75], v1 offset:4096\nv_mfma_f32_16x16x32_bf16 v[24:27], a[56:59], v[56:59], v[24:27]\nds_read_b128 v[68:71], v2 offset:14336\nv_mfma_f32_16x16x32_bf16 v[28:31], a[64:67], v[56:59], v[28:31]\n" \
"s_waitcnt lgkmcnt(5)\nbuffer_load_dwordx4 v0, %[rs], s60 offen offset:2048 sc1 lds\nv_mfma_f32_16x16x32_bf16 v[32:35], v[76:79], v[56:59], v[32:35]\nv_mfma_f32_16x16x32_bf16 v[36:39], a[72:75], v[56:59], v[36:39]\ns_waitcnt lgkmcnt(4)\nds_read_b128 v[76:79], v1 offset:5120\nv_mfma_f32_16x16x32_bf16 v[24:27], a[60:63], v[60:63], v[24:27]\ns_waitcnt vmcnt(7)\n" \
"s_barrier\nds_read_b128 v[56:59], v2 offset:16384\nv_mfma_f32_16x16x32_bf16 v[28:31], a[68:71], v[60:63], v[28:31]\ns_waitcnt lgkmcnt(5)\nbuffer_load_dwordx4 v0, %[rs], s60 offen offset:3072 sc1 lds\nv_mfma_f32_16x16x32_bf16 v[32:35], v[80:83], v[60:63], v[32:35]\nv_mfma_f32_16x16x32_bf16 v[36:39], a[76:79], v[60:63], v[36:39]\ns_waitcnt lgkmcnt(4)\n" \
"v_mfma_f32_16x16x32_bf16 v[24:27], a[80:83], v[64:67], v[24:27]\nds_read_b128 v[60:63], v2 offset:18432\nv_mfma_f32_16x16x32_bf16 v[28:31], a[88:91], v[64:67], v[28:31]\ns_add_u32 m0, s57, 49152\ns_add_u32 s60, s61, 98304\nbuffer_load_dwordx4 v0, %[rs], s60 offen offset:0 sc1 lds\nv_mfma_f32_16x16x32_bf16 v[32:35], a[96:99], v[64:67], v[32:35]\ns_waitcnt lgkmcnt(4)\n" \
"v_mfma_f32_16x16x32_bf16 v[36:39], v[72:75], v[64:67], v[36:39]\ns_waitcnt lgkmcnt(3)\nv_mfma_f32_16x16x32_bf16 v[24:27], a[84:87], v[68:71], v[24:27]\nds_read_b128 v[64:67], v2 offset:20480\nv_mfma_f32_16x16x32_bf16 v[28:31], a[92:95], v[68:71], v[28:31]\nbuffer_load_dwordx4 v0, %[rs], s60 offen offset:1024 sc1 lds\nv_mfma_f32_16x16x32_bf16 v[32:35], a[100:103], v[68:71], v[32:35]\ns_waitcnt lgkmcnt(3)\n" \
"v_mfma_f32_16x16x32_bf16 v[36:39], v[76:79], v[68:71], v[36:39]\ns_waitcnt lgkmcnt(2)\nds_read_b128 v[76:79], v1 offset:6144\nv_mfma_f32_16x16x32_bf16 v[24:27], a[104:107], v[56:59], v[24:27]\nds_read_b128 v[68:71], v2 offset:22528\nv_mfma_f32_16x16x32_bf16 v[28:31], a[112:115], v[56:59], v[28:31]\nbuffer_load_dwordx4 v0, %[rs], s60 offen offset:2048 sc1 lds\nv_mfma_f32_16x16x32_bf16 v[32:35], a[120:123], v[56:59], v[32:35]\n" \
"v_mfma_f32_16x16x32_bf16 v[36:39], a[128:131], v[56:59], v[36:39]\ns_waitcnt lgkmcnt(3)\nds_read_b128 v[80:83], v1 offset:7168\nv_mfma_f32_16x16x32_bf16 v[24:27], a[108:111], v[60:63], v[24:27]\nds_read_b128 v[56:59], v2 offset:24576\nv_mfma_f32_16x16x32_bf16 v[28:31], a[116:119], v[60:63], v[28:31]\nbuffer_load_dwordx4 v0, %[rs], s60 offen offset:3072 sc1 lds\nv_mfma_f32_16x16x32_bf16 v[32:35], a[124:127], v[60:63], v[32:35]\n" \
"v_mfma_f32_16x16x32_bf16 v[36:39], a[132:135], v[60:63], v[36:39]\ns_waitcnt lgkmcnt(4)\nds_read_b128 v[72:75], v1 offset:8192\nv_mfma_f32_16x16x32_bf16 v[24:27], a[136:139], v[64:67], v[24:27]\ns_waitcnt vmcnt(8)\ns_waitcnt lgkmcnt(4)\nds_read_b128 v[60:63], v2 offset:26624\nv_add_f32 v84, v40, v44\n" \
"v_mfma_f32_16x16x32_bf16 v[28:31], v[76:79], v[64:67], v[28:31]\nv_add_f32 v85, v48, v52\nv_add_f32 v84, v84, v85\nv_mfma_f32_16x16x32_bf16 v[32:35], a[144:147], v[64:67], v[32:35]\nv_mov_b32 v85, v84\nv_mfma_f32_16x16x32_bf16 v[36:39], a[152:155], v[64:67], v[36:39]\ns_waitcnt lgkmcnt(4)\nds_read_b128 v[76:79], v1 offset:9216\n" \
"v_mfma_f32_16x16x32_bf16 v[24:27], a[140:143], v[68:71], v[24:27]\ns_waitcnt lgkmcnt(4)\nds_read_b128 v[64:67], v2 offset:28672\nv_mfma_f32_16x16x32_bf16 v[28:31], v[80:83], v[68:71], v[28:31]\nv_permlane16_swap_b32 v84, v85\nv_mfma_f32_16x16x32_bf16 v[32:35], a[148:151], v[68:71], v[32:35]\nv_add_f32 v84, v84, v85\nv_mov_b32 v85, v84\n" \
"v_mfma_f32_16x16x32_bf16 v[36:39], a[156:159], v[68:71], v[36:39]\ns_waitcnt lgkmcnt(4)\nds_read_b128 v[80:83], v1 offset:10240\nv_mfma_f32_16x16x32_bf16 v[24:27], a[160:163], v[56:59], v[24:27]\nds_read_b128 v[68:71], v2 offset:30720\nv_mfma_f32_16x16x32_bf16 v[28:31], a[168:171], v[56:59], v[28:31]\ns_waitcnt lgkmcnt(5)\nv_permlane32_swap_b32 v84, v85\n" \
"v_mfma_f32_16x16x32_bf16 v[32:35], v[72:75], v[56:59], v[32:35]\nv_add_f32 v84, v84, v85\nv_mul_f32 v84, 0xbd800000, v84\nv_mfma_f32_16x16x32_bf16 v[36:39], a[176:179], v[56:59], v[36:39]\ns_waitcnt lgkmcnt(4)\nds_read_b128 v[72:75], v1 offset:11264\nv_add_f32 v40, v40, v84\nv_mfma_f32_16x16x32_bf16 v[24:27], a[164:167], v[60:63], v[24:27]\n" \
"s_waitcnt vmcnt(4)\ns_barrier\nds_read_b128 v[56:59], v2 offset:32768\nv_mfma_f32_16x16x32_bf16 v[28:31], a[172:175], v[60:63], v[28:31]\ns_waitcnt lgkmcnt(5)\nv_mul_f32 v40, 0x41000000, v40\nv_fma_f32 v41, v40, v40, v41\nv_mfma_f32_16x16x32_bf16 v[32:35], v[76:79], v[60:63], v[32:35]\n" \
"v_add_f32 v44, v44, v84\nv_mul_f32 v44, 0x41000000, v44\nv_mfma_f32_16x16x32_bf16 v[36:39], a[180:183], v[60:63], v[36:39]\ns_waitcnt lgkmcnt(4)\nv_fma_f32 v45, v44, v44, v45\nv_add_f32 v48, v48, v84\nv_mfma_f32_16x16x32_bf16 v[24:27], a[184:187], v[64:67], v[24:27]\nds_read_b128 v[60:63], v2 offset:34816\n" \
"v_mul_f32 v48, 0x41000000, v48\nv_mfma_f32_16x16x32_bf16 v[28:31], a[192:195], v[64:67], v[28:31]\nv_fma_f32 v49, v48, v48, v49\nv_add_f32 v52, v52, v84\nv_mfma_f32_16x16x32_bf16 v[32:35], a[200:203], v[64:67], v[32:35]\ns_waitcnt lgkmcnt(4)\nv_mul_f32 v52, 0x41000000, v52\nv_fma_f32 v53, v52, v52, v53\n" \
"v_mfma_f32_16x16x32_bf16 v[36:39], v[80:83], v[64:67], v[36:39]\ns_waitcnt lgkmcnt(3)\nv_add_f32 v85, v41, v45\nv_add_f32 v86, v49, v53\nv_mfma_f32_16x16x32_bf16 v[24:27], a[188:191], v[68:71], v[24:27]\nds_read_b128 v[64:67], v2 offset:36864\nv_add_f32 v85, v85, v86\nv_mfma_f32_16x16x32_bf16 v[28:31], a[196:199], v[68:71], v[28:31]\n" \
"v_mov_b32 v86, v85\nv_mfma_f32_16x16x32_bf16 v[32:35], a[204:207], v[68:71], v[32:35]\ns_waitcnt lgkmcnt(3)\nv_permlane16_swap_b32 v85, v86\nv_mfma_f32_16x16x32_bf16 v[36:39], v[72:75], v[68:71], v[36:39]\ns_waitcnt lgkmcnt(2)\nds_read_b128 v[72:75], v1 offset:12288\nv_add_f32 v85, v85, v86\n" \
"v_mfma_f32_16x16x32_bf16 v[24:27], a[208:211], v[56:59], v[24:27]\nds_read_b128 v[68:71], v2 offset:38912\nv_mov_b32 v86, v85\nv_mfma_f32_16x16x32_bf16 v[28:31], a[216:219], v[56:59], v[28:31]\ns_nop 0\nv_permlane32_swap_b32 v85, v86\nv_mfma_f32_16x16x32_bf16 v[32:35], a[224:227], v[56:59], v[32:35]\nv_add_f32 v85, v85, v86\n" \
"v_mul_f32 v85, 0x3a800000, v85\nv_mfma_f32_16x16x32_bf16 v[36:39], a[232:235], v[56:59], v[36:39]\ns_waitcnt lgkmcnt(3)\nds_read_b128 v[76:79], v1 offset:13312\nv_cvt_pk_bf16_f32 v85, v85, v85\nv_mfma_f32_16x16x32_bf16 v[24:27], a[212:215], v[60:63], v[24:27]\nds_read_b128 v[56:59], v2 offset:40960\nv_and_b32 v85, 0xffff0000, v85\n" \
"v_mfma_f32_16x16x32_bf16 v[28:31], a[220:223], v[60:63], v[28:31]\nv_add_f32 v85, 0x3727c5ac, v85\nv_cvt_pk_bf16_f32 v85, v85, v85\nv_mfma_f32_16x16x32_bf16 v[32:35], a[228:231], v[60:63], v[32:35]\nv_and_b32 v85, 0xffff0000, v85\nv_cvt_pk_bf16_f32 v84, v84, v84\nv_mfma_f32_16x16x32_bf16 v[36:39], a[236:239], v[60:63], v[36:39]\ns_waitcnt lgkmcnt(4)\n" \
"ds_read_b128 v[80:83], v1 offset:14336\nv_and_b32 v84, 0xffff0000, v84\nv_mfma_f32_16x16x32_bf16 v[24:27], a[240:243], v[64:67], v[24:27]\ns_waitcnt lgkmcnt(4)\nds_read_b128 v[60:63], v2 offset:43008\nv_mfma_f32_16x16x32_bf16 v[28:31], v[72:75], v[64:67], v[28:31]\nv_sqrt_f32 v85, v85\nv_mfma_f32_16x16x32_bf16 v[32:35], a[248:251], v[64:67], v[32:35]\n" \
"v_cvt_pk_bf16_f32 v85, v85, v85\nv_and_b32 v85, 0xffff0000, v85\nv_mfma_f32_16x16x32_bf16 v[36:39], v[96:99], v[64:67], v[36:39]\ns_waitcnt lgkmcnt(4)\nds_read_b128 v[72:75], v1 offset:15360\nv_mul_f32 v86, s58, v85\nv_mfma_f32_16x16x32_bf16 v[24:27], a[244:247], v[68:71], v[24:27]\ns_waitcnt lgkmcnt(4)\n" \
"ds_read_b128 v[64:67], v2 offset:45056\nv_add_f32 v87, v42, v46\nv_mfma_f32_16x16x32_bf16 v[28:31], v[76:79], v[68:71], v[28:31]\nv_rcp_f32 v85, v85\nv_mfma_f32_16x16x32_bf16 v[32:35], a[252:255], v[68:71], v[32:35]\nv_mul_f32 v85, s58, v85\nv_add_f32 v88, v50, v54\nv_mfma_f32_16x16x32_bf16 v[36:39], v[100:103], v[68:71], v[36:39]\n" \
"s_waitcnt lgkmcnt(4)\nds_read_b128 v[76:79], v1 offset:16384\nv_add_f32 v87, v87, v88\nv_mfma_f32_16x16x32_bf16 v[24:27], v[104:107], v[56:59], v[24:27]\nds_read_b128 v[68:71], v2 offset:47104\nv_mov_b32 v88, v87\nv_mfma_f32_16x16x32_bf16 v[28:31], v[112:115], v[56:59], v[28:31]\ns_waitcnt lgkmcnt(5)\n" \
"v_permlane16_swap_b32 v87, v88\nv_mfma_f32_16x16x32_bf16 v[32:35], v[80:83], v[56:59], v[32:35]\nv_add_f32 v87, v87, v88\nv_mov_b32 v88, v87\nv_mfma_f32_16x16x32_bf16 v[36:39], v[120:123], v[56:59], v[36:39]\ns_waitcnt lgkmcnt(4)\nds_read_b128 v[80:83], v1 offset:17408\nv_mfma_f32_16x16x32_bf16 v[24:27], v[108:111], v[60:63], v[24:27]\n" \
"s_waitcnt vmcnt(0)\ns_barrier\nds_read_b128 v[56:59], v2 offset:49152\nv_mfma_f32_16x16x32_bf16 v[28:31], v[116:119], v[60:63], v[28:31]\ns_waitcnt lgkmcnt(5)\nv_permlane32_swap_b32 v87, v88\nv_mfma_f32_16x16x32_bf16 v[32:35], v[72:75], v[60:63], v[32:35]\nv_add_f32 v87, v87, v88\n" \
"v_mul_f32 v87, 0xbd800000, v87\nv_mfma_f32_16x16x32_bf16 v[36:39], v[124:127], v[60:63], v[36:39]\ns_waitcnt lgkmcnt(4)\nv_add_f32 v42, v42, v87\nv_mul_f32 v42, 0x41000000, v42\nv_mfma_f32_16x16x32_bf16 v[24:27], v[128:131], v[64:67], v[24:27]\nds_read_b128 v[60:63], v2 offset:51200\nv_fma_f32 v43, v42, v42, v43\n" \
"v_mfma_f32_16x16x32_bf16 v[28:31], v[136:139], v[64:67], v[28:31]\nv_add_f32 v46, v46, v87\nv_mul_f32 v46, 0x41000000, v46\nv_mfma_f32_16x16x32_bf16 v[32:35], v[144:147], v[64:67], v[32:35]\ns_waitcnt lgkmcnt(4)\nv_fma_f32 v47, v46, v46, v47\nv_add_f32 v50, v50, v87\nv_mfma_f32_16x16x32_bf16 v[36:39], v[76:79], v[64:67], v[36:39]\n" \
"s_waitcnt lgkmcnt(3)\nv_mul_f32 v50, 0x41000000, v50\nv_fma_f32 v51, v50, v50, v51\nv_mfma_f32_16x16x32_bf16 v[24:27], v[132:135], v[68:71], v[24:27]\nds_read_b128 v[64:67], v2 offset:53248\nv_add_f32 v54, v54, v87\nv_mfma_f32_16x16x32_bf16 v[28:31], v[140:143], v[68:71], v[28:31]\nv_mul_f32 v54, 0x41000000, v54\n" \
"v_fma_f32 v55, v54, v54, v55\nv_mfma_f32_16x16x32_bf16 v[32:35], v[148:151], v[68:71], v[32:35]\ns_waitcnt lgkmcnt(3)\nv_add_f32 v88, v43, v47\nv_add_f32 v89, v51, v55\nv_mfma_f32_16x16x32_bf16 v[36:39], v[80:83], v[68:71], v[36:39]\ns_waitcnt lgkmcnt(2)\nds_read_b128 v[80:83], v1 offset:18432\n" \
"v_add_f32 v88, v88, v89\nv_mfma_f32_16x16x32_bf16 v[24:27], v[152:155], v[56:59], v[24:27]\nds_read_b128 v[68:71], v2 offset:55296\nv_mov_b32 v89, v88\nv_mfma_f32_16x16x32_bf16 v[28:31], v[160:163], v[56:59], v[28:31]\ns_nop 0\nv_permlane16_swap_b32 v88, v89\nv_mfma_f32_16x16x32_bf16 v[32:35], v[168:171], v[56:59], v[32:35]\n" \
"v_add_f32 v88, v88, v89\nv_mov_b32 v89, v88\nv_mfma_f32_16x16x32_bf16 v[36:39], v[176:179], v[56:59], v[36:39]\ns_waitcnt lgkmcnt(3)\nds_read_b128 v[72:75], v1 offset:19456\nv_mfma_f32_16x16x32_bf16 v[24:27], v[156:159], v[60:63], v[24:27]\nds_read_b128 v[56:59], v2 offset:57344\nv_mfma_f32_16x16x32_bf16 v[28:31], v[164:167], v[60:63], v[28:31]\n" \
"v_permlane32_swap_b32 v88, v89\nv_mfma_f32_16x16x32_bf16 v[32:35], v[172:175], v[60:63], v[32:35]\nv_add_f32 v88, v88, v89\nv_mul_f32 v88, 0x3a800000, v88\nv_mfma_f32_16x16x32_bf16 v[36:39], v[180:183], v[60:63], v[36:39]\ns_waitcnt lgkmcnt(4)\nds_read_b128 v[76:79], v1 offset:20480\nv_cvt_pk_bf16_f32 v88, v88, v88\n" \
"v_mfma_f32_16x16x32_bf16 v[24:27], v[184:187], v[64:67], v[24:27]\ns_waitcnt lgkmcnt(4)\nds_read_b128 v[60:63], v2 offset:59392\nv_and_b32 v88, 0xffff0000, v88\nv_mfma_f32_16x16x32_bf16 v[28:31], v[80:83], v[64:67], v[28:31]\nv_add_f32 v88, 0x3727c5ac, v88\nv_cvt_pk_bf16_f32 v88, v88, v88\nv_mfma_f32_16x16x32_bf16 v[32:35], v[192:195], v[64:67], v[32:35]\n" \
"v_and_b32 v88, 0xffff0000, v88\nv_cvt_pk_bf16_f32 v87, v87, v87\nv_mfma_f32_16x16x32_bf16 v[36:39], v[200:203], v[64:67], v[36:39]\ns_waitcnt lgkmcnt(4)\nds_read_b128 v[80:83], v1 offset:21504\nv_and_b32 v87, 0xffff0000, v87\nv_mfma_f32_16x16x32_bf16 v[24:27], v[188:191], v[68:71], v[24:27]\ns_waitcnt lgkmcnt(4)\n" \
"ds_read_b128 v[64:67], v2 offset:61440\nv_mfma_f32_16x16x32_bf16 v[28:31], v[72:75], v[68:71], v[28:31]\nv_sqrt_f32 v88, v88\nv_mfma_f32_16x16x32_bf16 v[32:35], v[196:199], v[68:71], v[32:35]\nv_cvt_pk_bf16_f32 v88, v88, v88\nv_and_b32 v88, 0xffff0000, v88\nv_mfma_f32_16x16x32_bf16 v[36:39], v[204:207], v[68:71], v[36:39]\ns_waitcnt lgkmcnt(4)\n" \
"ds_read_b128 v[72:75], v1 offset:22528\nv_mul_f32 v89, s58, v88\nv_mfma_f32_16x16x32_bf16 v[24:27], v[208:211], v[56:59], v[24:27]\nds_read_b128 v[68:71], v2 offset:63488\nv_mfma_f32_16x16x32_bf16 v[28:31], v[216:219], v[56:59], v[28:31]\ns_waitcnt lgkmcnt(5)\nv_rcp_f32 v88, v88\nv_mfma_f32_16x16x32_bf16 v[32:35], v[76:79], v[56:59], v[32:35]\n" \
"v_mul_f32 v88, s58, v88\nv_mfma_f32_16x16x32_bf16 v[36:39], v[224:227], v[56:59], v[36:39]\ns_waitcnt lgkmcnt(4)\nds_read_b128 v[76:79], v1 offset:23552\nv_mfma_f32_16x16x32_bf16 v[24:27], v[212:215], v[60:63], v[24:27]\nds_read_b128 v[56:59], v2 offset:256\nv_mfma_f32_16x16x32_bf16 v[28:31], v[220:223], v[60:63], v[28:31]\ns_waitcnt lgkmcnt(5)\n" \
"v_mfma_f32_16x16x32_bf16 v[32:35], v[80:83], v[60:63], v[32:35]\nv_mfma_f32_16x16x32_bf16 v[36:39], v[228:231], v[60:63], v[36:39]\ns_waitcnt lgkmcnt(4)\nv_mfma_f32_16x16x32_bf16 v[24:27], v[232:235], v[64:67], v[24:27]\nds_read_b128 v[60:63], v2 offset:2304\nv_mfma_f32_16x16x32_bf16 v[28:31], v[240:243], v[64:67], v[28:31]\nv_mfma_f32_16x16x32_bf16 v[32:35], v[248:251], v[64:67], v[32:35]\ns_waitcnt lgkmcnt(4)\n" \
"v_mfma_f32_16x16x32_bf16 v[36:39], v[72:75], v[64:67], v[36:39]\ns_waitcnt lgkmcnt(3)\nv_mfma_f32_16x16x32_bf16 v[24:27], v[236:239], v[68:71], v[24:27]\nds_read_b128 v[64:67], v2 offset:4352\nv_mfma_f32_16x16x32_bf16 v[28:31], v[244:247], v[68:71], v[28:31]\nv_mfma_f32_16x16x32_bf16 v[32:35], v[252:255], v[68:71], v[32:35]\ns_waitcnt lgkmcnt(3)\nv_mfma_f32_16x16x32_bf16 v[36:39], v[76:79], v[68:71], v[36:39]\n" \
"s_waitcnt lgkmcnt(2)\nds_read_b128 v[76:79], v1 offset:0\nv_mfma_f32_16x16x32_bf16 v[40:43], a[0:3], v[56:59], 0\nds_read_b128 v[68:71], v2 offset:6400\nv_mfma_f32_16x16x32_bf16 v[44:47], a[8:11], v[56:59], 0\ns_lshl_b32 s44, s40, 1\nv_mov_b32 v90, s44\nv_mfma_f32_16x16x32_bf16 v[48:51], a[16:19], v[56:59], 0\n" \
"v_mfma_f32_16x16x32_bf16 v[52:55], a[24:27], v[56:59], 0\ns_waitcnt lgkmcnt(3)\nds_read_b128 v[80:83], v1 offset:1024\nv_fma_f32 v24, v8, v84, v24\nv_mfma_f32_16x16x32_bf16 v[40:43], a[4:7], v[60:63], v[40:43]\nds_read_b128 v[56:59], v2 offset:8448\nv_fma_f32 v25, v9, v84, v25\nv_mfma_f32_16x16x32_bf16 v[44:47], a[12:15], v[60:63], v[44:47]\n" \
"v_fma_f32 v32, v16, v84, v32\nv_fma_f32 v33, v17, v84, v33\nv_mfma_f32_16x16x32_bf16 v[48:51], a[20:23], v[60:63], v[48:51]\nv_mul_f32 v24, v24, v85\nv_mul_f32 v25, v25, v85\nv_mfma_f32_16x16x32_bf16 v[52:55], a[28:31], v[60:63], v[52:55]\ns_waitcnt lgkmcnt(4)\nds_read_b128 v[72:75], v1 offset:2048\n" \
"v_fma_f32 v91, v24, v24, s59\nv_mfma_f32_16x16x32_bf16 v[40:43], a[32:35], v[64:67], v[40:43]\ns_waitcnt lgkmcnt(4)\nds_read_b128 v[60:63], v2 offset:10496\nv_fma_f32 v92, v25, v25, s59\nv_mfma_f32_16x16x32_bf16 v[44:47], v[76:79], v[64:67], v[44:47]\nv_mul_f32 v91, v91, v24\nv_mul_f32 v92, v92, v25\n" \
"v_mfma_f32_16x16x32_bf16 v[48:51], a[40:43], v[64:67], v[48:51]\nv_exp_f32 v91, v91\nv_mfma_f32_16x16x32_bf16 v[52:55], a[48:51], v[64:67], v[52:55]\ns_waitcnt lgkmcnt(4)\nds_read_b128 v[76:79], v1 offset:3072\nv_mul_f32 v24, v24, v32\nv_mfma_f32_16x16x32_bf16 v[40:43], a[36:39], v[68:71], v[40:43]\ns_waitcnt lgkmcnt(4)\n" \
"ds_read_b128 v[64:67], v2 offset:12544\nv_mul_f32 v25, v25, v33\nv_mfma_f32_16x16x32_bf16 v[44:47], v[80:83], v[68:71], v[44:47]\nv_exp_f32 v92, v92\nv_mfma_f32_16x16x32_bf16 v[48:51], a[44:47], v[68:71], v[48:51]\nv_fma_f32 v91, v91, v86, v86\nv_fma_f32 v92, v92, v86, v86\nv_mfma_f32_16x16x32_bf16 v[52:55], a[52:55], v[68:71], v[52:55]\n" \
"s_waitcnt lgkmcnt(4)\nds_read_b128 v[80:83], v1 offset:4096\nv_fma_f32 v26, v10, v84, v26\nv_mfma_f32_16x16x32_bf16 v[40:43], a[56:59], v[56:59], v[40:43]\nds_read_b128 v[68:71], v2 offset:14592\nv_fma_f32 v27, v11, v84, v27\nv_mfma_f32_16x16x32_bf16 v[44:47], a[64:67], v[56:59], v[44:47]\ns_waitcnt lgkmcnt(5)\n" \
"v_rcp_f32 v91, v91\nv_mfma_f32_16x16x32_bf16 v[48:51], v[72:75], v[56:59], v[48:51]\nv_rcp_f32 v92, v92\nv_mfma_f32_16x16x32_bf16 v[52:55], a[72:75], v[56:59], v[52:55]\ns_waitcnt lgkmcnt(4)\nds_read_b128 v[72:75], v1 offset:5120\nv_mul_f32 v24, v24, v91\nv_mfma_f32_16x16x32_bf16 v[40:43], a[60:63], v[60:63], v[40:43]\n" \
"ds_read_b128 v[56:59], v2 offset:16640\nv_mul_f32 v25, v25, v92\nv_mfma_f32_16x16x32_bf16 v[44:47], a[68:71], v[60:63], v[44:47]\ns_waitcnt lgkmcnt(5)\nv_cvt_pk_bf16_f32 v32, v24, v25\nv_fma_f32 v34, v18, v84, v34\nv_mfma_f32_16x16x32_bf16 v[48:51], v[76:79], v[60:63], v[48:51]\nv_fma_f32 v35, v19, v84, v35\n" \
"v_mul_f32 v26, v26, v85\nv_mfma_f32_16x16x32_bf16 v[52:55], a[76:79], v[60:63], v[52:55]\ns_waitcnt lgkmcnt(4)\nv_mul_f32 v27, v27, v85\nv_fma_f32 v91, v26, v26, s59\nv_mfma_f32_16x16x32_bf16 v[40:43], a[80:83], v[64:67], v[40:43]\nds_read_b128 v[60:63], v2 offset:18688\nv_fma_f32 v92, v27, v27, s59\n" \
"v_mfma_f32_16x16x32_bf16 v[44:47], a[88:91], v[64:67], v[44:47]\nv_mul_f32 v91, v91, v26\nv_mul_f32 v92, v92, v27\nv_mfma_f32_16x16x32_bf16 v[48:51], a[96:99], v[64:67], v[48:51]\ns_waitcnt lgkmcnt(4)\nv_exp_f32 v91, v91\nv_mfma_f32_16x16x32_bf16 v[52:55], v[80:83], v[64:67], v[52:55]\ns_waitcnt lgkmcnt(3)\n" \
"v_exp_f32 v92, v92\nv_mfma_f32_16x16x32_bf16 v[40:43], a[84:87], v[68:71], v[40:43]\nds_read_b128 v[64:67], v2 offset:20736\nv_mul_f32 v26, v26, v34\nv_mfma_f32_16x16x32_bf16 v[44:47], a[92:95], v[68:71], v[44:47]\nv_mul_f32 v27, v27, v35\nv_fma_f32 v91, v91, v86, v86\nv_mfma_f32_16x16x32_bf16 v[48:51], a[100:103], v[68:71], v[48:51]\n" \
"s_waitcnt lgkmcnt(3)\nv_fma_f32 v92, v92, v86, v86\nv_fma_f32 v28, v12, v84, v28\nv_mfma_f32_16x16x32_bf16 v[52:55], v[72:75], v[68:71], v[52:55]\ns_waitcnt lgkmcnt(2)\nds_read_b128 v[72:75], v1 offset:6144\nv_fma_f32 v29, v13, v84, v29\nv_mfma_f32_16x16x32_bf16 v[40:43], a[104:107], v[56:59], v[40:43]\n" \
"ds_read_b128 v[68:71], v2 offset:22784\nv_fma_f32 v36, v20, v84, v36\nv_mfma_f32_16x16x32_bf16 v[44:47], a[112:115], v[56:59], v[44:47]\nv_rcp_f32 v91, v91\nv_mfma_f32_16x16x32_bf16 v[48:51], a[120:123], v[56:59], v[48:51]\nv_rcp_f32 v92, v92\nv_mfma_f32_16x16x32_bf16 v[52:55], a[128:131], v[56:59], v[52:55]\ns_waitcnt lgkmcnt(3)\n" \
"ds_read_b128 v[76:79], v1 offset:7168\nv_mul_f32 v26, v26, v91\nv_mfma_f32_16x16x32_bf16 v[40:43], a[108:111], v[60:63], v[40:43]\nds_read_b128 v[56:59], v2 offset:24832\nv_mul_f32 v27, v27, v92\nv_mfma_f32_16x16x32_bf16 v[44:47], a[116:119], v[60:63], v[44:47]\nv_cvt_pk_bf16_f32 v33, v26, v27\nv_fma_f32 v37, v21, v84, v37\n" \
"v_mfma_f32_16x16x32_bf16 v[48:51], a[124:127], v[60:63], v[48:51]\nv_mul_f32 v28, v28, v85\nv_mul_f32 v29, v29, v85\nv_mfma_f32_16x16x32_bf16 v[52:55], a[132:135], v[60:63], v[52:55]\ns_waitcnt lgkmcnt(4)\nds_read_b128 v[80:83], v1 offset:8192\nv_fma_f32 v91, v28, v28, s59\nv_mfma_f32_16x16x32_bf16 v[40:43], a[136:139], v[64:67], v[40:43]\n" \
"s_waitcnt lgkmcnt(4)\nds_read_b128 v[60:63], v2 offset:26880\nv_fma_f32 v92, v29, v29, s59\nv_mfma_f32_16x16x32_bf16 v[44:47], v[72:75], v[64:67], v[44:47]\nv_mul_f32 v91, v91, v28\nv_mul_f32 v92, v92, v29\nv_mfma_f32_16x16x32_bf16 v[48:51], a[144:147], v[64:67], v[48:51]\nv_exp_f32 v91, v91\n" \
"v_mfma_f32_16x16x32_bf16 v[52:55], a[152:155], v[64:67], v[52:55]\ns_waitcnt lgkmcnt(4)\nds_read_b128 v[72:75], v1 offset:9216\nv_mul_f32 v28, v28, v36\nv_mfma_f32_16x16x32_bf16 v[40:43], a[140:143], v[68:71], v[40:43]\ns_waitcnt lgkmcnt(4)\nds_read_b128 v[64:67], v2 offset:28928\nv_mul_f32 v29, v29, v37\n" \
"v_mfma_f32_16x16x32_bf16 v[44:47], v[76:79], v[68:71], v[44:47]\nv_exp_f32 v92, v92\nv_mfma_f32_16x16x32_bf16 v[48:51], a[148:151], v[68:71], v[48:51]\nv_fma_f32 v91, v91, v86, v86\nv_fma_f32 v92, v92, v86, v86\nv_mfma_f32_16x16x32_bf16 v[52:55], a[156:159], v[68:71], v[52:55]\ns_waitcnt lgkmcnt(4)\nds_read_b128 v[76:79], v1 offset:10240\n" \
"v_fma_f32 v30, v14, v84, v30\nv_mfma_f32_16x16x32_bf16 v[40:43], a[160:163], v[56:59], v[40:43]\nds_read_b128 v[68:71], v2 offset:30976\nv_fma_f32 v31, v15, v84, v31\nv_mfma_f32_16x16x32_bf16 v[44:47], a[168:171], v[56:59], v[44:47]\ns_waitcnt lgkmcnt(5)\nv_rcp_f32 v91, v91\nv_mfma_f32_16x16x32_bf16 v[48:51], v[80:83], v[56:59], v[48:51]\n" \
"v_rcp_f32 v92, v92\nv_mfma_f32_16x16x32_bf16 v[52:55], a[176:179], v[56:59], v[52:55]\ns_waitcnt lgkmcnt(4)\nds_read_b128 v[80:83], v1 offset:11264\nv_mul_f32 v28, v28, v91\nv_mfma_f32_16x16x32_bf16 v[40:43], a[164:167], v[60:63], v[40:43]\nds_read_b128 v[56:59], v2 offset:33024\nv_mul_f32 v29, v29, v92\n" \
"v_mfma_f32_16x16x32_bf16 v[44:47], a[172:175], v[60:63], v[44:47]\ns_waitcnt lgkmcnt(5)\nv_cvt_pk_bf16_f32 v34, v28, v29\nv_fma_f32 v38, v22, v84, v38\nv_mfma_f32_16x16x32_bf16 v[48:51], v[72:75], v[60:63], v[48:51]\nv_fma_f32 v39, v23, v84, v39\nv_mul_f32 v30, v30, v85\nv_mfma_f32_16x16x32_bf16 v[52:55], a[180:183], v[60:63], v[52:55]\n" \
"s_waitcnt lgkmcnt(4)\nv_mul_f32 v31, v31, v85\nv_fma_f32 v91, v30, v30, s59\nv_mfma_f32_16x16x32_bf16 v[40:43], a[184:187], v[64:67], v[40:43]\nds_read_b128 v[60:63], v2 offset:35072\nv_fma_f32 v92, v31, v31, s59\nv_mfma_f32_16x16x32_bf16 v[44:47], a[192:195], v[64:67], v[44:47]\nv_mul_f32 v91, v91, v30\n" \
"v_mul_f32 v92, v92, v31\nv_mfma_f32_16x16x32_bf16 v[48:51], a[200:203], v[64:67], v[48:51]\ns_waitcnt lgkmcnt(4)\nv_exp_f32 v91, v91\nv_mfma_f32_16x16x32_bf16 v[52:55], v[76:79], v[64:67], v[52:55]\ns_waitcnt lgkmcnt(3)\nv_exp_f32 v92, v92\nv_mfma_f32_16x16x32_bf16 v[40:43], a[188:191], v[68:71], v[40:43]\n" \
"ds_read_b128 v[64:67], v2 offset:37120\nv_mul_f32 v30, v30, v38\nv_mfma_f32_16x16x32_bf16 v[44:47], a[196:199], v[68:71], v[44:47]\nv_mul_f32 v31, v31, v39\nv_fma_f32 v91, v91, v86, v86\nv_mfma_f32_16x16x32_bf16 v[48:51], a[204:207], v[68:71], v[48:51]\ns_waitcnt lgkmcnt(3)\nv_fma_f32 v92, v92, v86, v86\n" \
"v_mfma_f32_16x16x32_bf16 v[52:55], v[80:83], v[68:71], v[52:55]\ns_waitcnt lgkmcnt(2)\nds_read_b128 v[80:83], v1 offset:12288\nv_mfma_f32_16x16x32_bf16 v[40:43], a[208:211], v[56:59], v[40:43]\nds_read_b128 v[68:71], v2 offset:39168\nv_mfma_f32_16x16x32_bf16 v[44:47], a[216:219], v[56:59], v[44:47]\nv_rcp_f32 v91, v91\nv_mfma_f32_16x16x32_bf16 v[48:51], a[224:227], v[56:59], v[48:51]\n" \
"v_rcp_f32 v92, v92\nv_mfma_f32_16x16x32_bf16 v[52:55], a[232:235], v[56:59], v[52:55]\ns_waitcnt lgkmcnt(3)\nds_read_b128 v[72:75], v1 offset:13312\nv_mul_f32 v30, v30, v91\nv_mfma_f32_16x16x32_bf16 v[40:43], a[212:215], v[60:63], v[40:43]\nds_read_b128 v[56:59], v2 offset:41216\nv_mul_f32 v31, v31, v92\n" \
"v_mfma_f32_16x16x32_bf16 v[44:47], a[220:223], v[60:63], v[44:47]\nv_cvt_pk_bf16_f32 v35, v30, v31\nv_mfma_f32_16x16x32_bf16 v[48:51], a[228:231], v[60:63], v[48:51]\nbuffer_store_dwordx4 v[32:35], v4, %[rs], s55 offen offset:0 sc1\nv_mfma_f32_16x16x32_bf16 v[52:55], a[236:239], v[60:63], v[52:55]\ns_waitcnt lgkmcnt(4)\nds_read_b128 v[76:79], v1 offset:14336\nv_mfma_f32_16x16x32_bf16 v[40:43], a[240:243], v[64:67], v[40:43]\n" \
"s_waitcnt lgkmcnt(4)\nds_read_b128 v[60:63], v2 offset:43264\nv_mfma_f32_16x16x32_bf16 v[44:47], v[80:83], v[64:67], v[44:47]\nv_mfma_f32_16x16x32_bf16 v[48:51], a[248:251], v[64:67], v[48:51]\nv_mfma_f32_16x16x32_bf16 v[52:55], v[96:99], v[64:67], v[52:55]\ns_waitcnt lgkmcnt(4)\nds_read_b128 v[80:83], v1 offset:15360\nv_mfma_f32_16x16x32_bf16 v[40:43], a[244:247], v[68:71], v[40:43]\n" \
"s_waitcnt lgkmcnt(4)\nds_read_b128 v[64:67], v2 offset:45312\nv_mfma_f32_16x16x32_bf16 v[44:47], v[72:75], v[68:71], v[44:47]\nv_mfma_f32_16x16x32_bf16 v[48:51], a[252:255], v[68:71], v[48:51]\nv_mfma_f32_16x16x32_bf16 v[52:55], v[100:103], v[68:71], v[52:55]\ns_waitcnt lgkmcnt(4)\nds_read_b128 v[72:75], v1 offset:16384\nv_mfma_f32_16x16x32_bf16 v[40:43], v[104:107], v[56:59], v[40:43]\n" \
"ds_read_b128 v[68:71], v2 offset:47360\nv_mfma_f32_16x16x32_bf16 v[44:47], v[112:115], v[56:59], v[44:47]\ns_waitcnt lgkmcnt(5)\nv_mfma_f32_16x16x32_bf16 v[48:51], v[76:79], v[56:59], v[48:51]\nv_mfma_f32_16x16x32_bf16 v[52:55], v[120:123], v[56:59], v[52:55]\ns_waitcnt lgkmcnt(4)\nds_read_b128 v[76:79], v1 offset:17408\nv_mfma_f32_16x16x32_bf16 v[40:43], v[108:111], v[60:63], v[40:43]\n" \
"ds_read_b128 v[56:59], v2 offset:49408\nv_mfma_f32_16x16x32_bf16 v[44:47], v[116:119], v[60:63], v[44:47]\ns_waitcnt lgkmcnt(5)\nv_mfma_f32_16x16x32_bf16 v[48:51], v[80:83], v[60:63], v[48:51]\nv_mfma_f32_16x16x32_bf16 v[52:55], v[124:127], v[60:63], v[52:55]\ns_waitcnt lgkmcnt(4)\nv_mfma_f32_16x16x32_bf16 v[40:43], v[128:131], v[64:67], v[40:43]\nds_read_b128 v[60:63], v2 offset:51456\n" \
"v_mfma_f32_16x16x32_bf16 v[44:47], v[136:139], v[64:67], v[44:47]\nv_mfma_f32_16x16x32_bf16 v[48:51], v[144:147], v[64:67], v[48:51]\ns_waitcnt lgkmcnt(4)\nv_mfma_f32_16x16x32_bf16 v[52:55], v[72:75], v[64:67], v[52:55]\ns_waitcnt lgkmcnt(3)\nv_mfma_f32_16x16x32_bf16 v[40:43], v[132:135], v[68:71], v[40:43]\nds_read_b128 v[64:67], v2 offset:53504\nv_mfma_f32_16x16x32_bf16 v[44:47], v[140:143], v[68:71], v[44:47]\n" \
"v_mfma_f32_16x16x32_bf16 v[48:51], v[148:151], v[68:71], v[48:51]\ns_waitcnt lgkmcnt(3)\nv_mfma_f32_16x16x32_bf16 v[52:55], v[76:79], v[68:71], v[52:55]\ns_waitcnt lgkmcnt(2)\nds_read_b128 v[76:79], v1 offset:18432\nv_mfma_f32_16x16x32_bf16 v[40:43], v[152:155], v[56:59], v[40:43]\nds_read_b128 v[68:71], v2 offset:55552\nv_mfma_f32_16x16x32_bf16 v[44:47], v[160:163], v[56:59], v[44:47]\n" \
"v_mfma_f32_16x16x32_bf16 v[48:51], v[168:171], v[56:59], v[48:51]\nv_mfma_f32_16x16x32_bf16 v[52:55], v[176:179], v[56:59], v[52:55]\ns_waitcnt lgkmcnt(3)\nds_read_b128 v[80:83], v1 offset:19456\nv_mfma_f32_16x16x32_bf16 v[40:43], v[156:159], v[60:63], v[40:43]\nds_read_b128 v[56:59], v2 offset:57600\nv_mfma_f32_16x16x32_bf16 v[44:47], v[164:167], v[60:63], v[44:47]\nv_mfma_f32_16x16x32_bf16 v[48:51], v[172:175], v[60:63], v[48:51]\n" \
"v_mfma_f32_16x16x32_bf16 v[52:55], v[180:183], v[60:63], v[52:55]\ns_waitcnt lgkmcnt(4)\nds_read_b128 v[72:75], v1 offset:20480\nv_mfma_f32_16x16x32_bf16 v[40:43], v[184:187], v[64:67], v[40:43]\ns_waitcnt lgkmcnt(4)\nds_read_b128 v[60:63], v2 offset:59648\nv_mfma_f32_16x16x32_bf16 v[44:47], v[76:79], v[64:67], v[44:47]\ns_waitcnt vmcnt(0)\n" \
"buffer_store_dword v90, off, %[rs], %[fh] sc1\nv_mfma_f32_16x16x32_bf16 v[48:51], v[192:195], v[64:67], v[48:51]\nv_mfma_f32_16x16x32_bf16 v[52:55], v[200:203], v[64:67], v[52:55]\ns_waitcnt lgkmcnt(4)\nds_read_b128 v[76:79], v1 offset:21504\nv_mfma_f32_16x16x32_bf16 v[40:43], v[188:191], v[68:71], v[40:43]\ns_waitcnt lgkmcnt(4)\nds_read_b128 v[64:67], v2 offset:61696\n" \
"v_mfma_f32_16x16x32_bf16 v[44:47], v[80:83], v[68:71], v[44:47]\nv_mfma_f32_16x16x32_bf16 v[48:51], v[196:199], v[68:71], v[48:51]\nv_mfma_f32_16x16x32_bf16 v[52:55], v[204:207], v[68:71], v[52:55]\ns_waitcnt lgkmcnt(4)\nds_read_b128 v[80:83], v1 offset:22528\nv_mfma_f32_16x16x32_bf16 v[40:43], v[208:211], v[56:59], v[40:43]\nds_read_b128 v[68:71], v2 offset:63744\nv_mfma_f32_16x16x32_bf16 v[44:47], v[216:219], v[56:59], v[44:47]\n" \
"s_waitcnt lgkmcnt(5)\nv_mfma_f32_16x16x32_bf16 v[48:51], v[72:75], v[56:59], v[48:51]\nv_mfma_f32_16x16x32_bf16 v[52:55], v[224:227], v[56:59], v[52:55]\ns_waitcnt lgkmcnt(4)\nds_read_b128 v[72:75], v1 offset:23552\nv_mfma_f32_16x16x32_bf16 v[40:43], v[212:215], v[60:63], v[40:43]\nv_mfma_f32_16x16x32_bf16 v[44:47], v[220:223], v[60:63], v[44:47]\ns_waitcnt lgkmcnt(4)\n" \
"v_mfma_f32_16x16x32_bf16 v[48:51], v[76:79], v[60:63], v[48:51]\nv_mfma_f32_16x16x32_bf16 v[52:55], v[228:231], v[60:63], v[52:55]\ns_waitcnt lgkmcnt(3)\nv_mfma_f32_16x16x32_bf16 v[40:43], v[232:235], v[64:67], v[40:43]\nv_mfma_f32_16x16x32_bf16 v[44:47], v[240:243], v[64:67], v[44:47]\nv_mfma_f32_16x16x32_bf16 v[48:51], v[248:251], v[64:67], v[48:51]\ns_waitcnt lgkmcnt(2)\nv_mfma_f32_16x16x32_bf16 v[52:55], v[80:83], v[64:67], v[52:55]\n" \
"s_waitcnt lgkmcnt(1)\nv_mfma_f32_16x16x32_bf16 v[40:43], v[236:239], v[68:71], v[40:43]\nv_mfma_f32_16x16x32_bf16 v[44:47], v[244:247], v[68:71], v[44:47]\nv_mfma_f32_16x16x32_bf16 v[48:51], v[252:255], v[68:71], v[48:51]\ns_waitcnt lgkmcnt(0)\nv_mfma_f32_16x16x32_bf16 v[52:55], v[72:75], v[68:71], v[52:55]\nv_fma_f32 v40, v8, v87, v40\nv_fma_f32 v41, v9, v87, v41\n" \
"s_nop 1\nv_fma_f32 v48, v16, v87, v48\nv_fma_f32 v49, v17, v87, v49\nv_mul_f32 v40, v40, v88\nv_mul_f32 v41, v41, v88\nv_fma_f32 v91, v40, v40, s59\nv_fma_f32 v92, v41, v41, s59\nv_mul_f32 v91, v91, v40\n" \
"v_mul_f32 v92, v92, v41\nv_exp_f32 v91, v91\nv_exp_f32 v92, v92\nv_mul_f32 v40, v40, v48\nv_mul_f32 v41, v41, v49\nv_fma_f32 v91, v91, v89, v89\nv_fma_f32 v92, v92, v89, v89\nv_rcp_f32 v91, v91\n" \
"v_rcp_f32 v92, v92\nv_mul_f32 v40, v40, v91\nv_mul_f32 v41, v41, v92\nv_cvt_pk_bf16_f32 v48, v40, v41\nv_fma_f32 v42, v10, v87, v42\nv_fma_f32 v43, v11, v87, v43\nv_fma_f32 v50, v18, v87, v50\nv_fma_f32 v51, v19, v87, v51\n" \
"v_mul_f32 v42, v42, v88\nv_mul_f32 v43, v43, v88\nv_fma_f32 v91, v42, v42, s59\nv_fma_f32 v92, v43, v43, s59\nv_mul_f32 v91, v91, v42\nv_mul_f32 v92, v92, v43\nv_exp_f32 v91, v91\nv_exp_f32 v92, v92\n" \
"v_mul_f32 v42, v42, v50\nv_mul_f32 v43, v43, v51\nv_fma_f32 v91, v91, v89, v89\nv_fma_f32 v92, v92, v89, v89\nv_rcp_f32 v91, v91\nv_rcp_f32 v92, v92\nv_mul_f32 v42, v42, v91\nv_mul_f32 v43, v43, v92\n" \
"v_cvt_pk_bf16_f32 v49, v42, v43\nv_fma_f32 v44, v12, v87, v44\nv_fma_f32 v45, v13, v87, v45\nv_fma_f32 v52, v20, v87, v52\nv_fma_f32 v53, v21, v87, v53\nv_mul_f32 v44, v44, v88\nv_mul_f32 v45, v45, v88\nv_fma_f32 v91, v44, v44, s59\n" \
"v_fma_f32 v92, v45, v45, s59\nv_mul_f32 v91, v91, v44\nv_mul_f32 v92, v92, v45\nv_exp_f32 v91, v91\nv_exp_f32 v92, v92\nv_mul_f32 v44, v44, v52\nv_mul_f32 v45, v45, v53\nv_fma_f32 v91, v91, v89, v89\n" \
"v_fma_f32 v92, v92, v89, v89\nv_rcp_f32 v91, v91\nv_rcp_f32 v92, v92\nv_mul_f32 v44, v44, v91\nv_mul_f32 v45, v45, v92\nv_cvt_pk_bf16_f32 v50, v44, v45\nv_fma_f32 v46, v14, v87, v46\nv_fma_f32 v47, v15, v87, v47\n" \
"v_fma_f32 v54, v22, v87, v54\nv_fma_f32 v55, v23, v87, v55\nv_mul_f32 v46, v46, v88\nv_mul_f32 v47, v47, v88\nv_fma_f32 v91, v46, v46, s59\nv_fma_f32 v92, v47, v47, s59\nv_mul_f32 v91, v91, v46\nv_mul_f32 v92, v92, v47\n" \
"v_exp_f32 v91, v91\nv_exp_f32 v92, v92\nv_mul_f32 v46, v46, v54\nv_mul_f32 v47, v47, v55\nv_fma_f32 v91, v91, v89, v89\nv_fma_f32 v92, v92, v89, v89\nv_rcp_f32 v91, v91\nv_rcp_f32 v92, v92\n" \
"v_mul_f32 v46, v46, v91\nv_mul_f32 v47, v47, v92\nv_cvt_pk_bf16_f32 v51, v46, v47\nbuffer_store_dwordx4 v[48:51], v4, %[rs], s55 offen offset:1024 sc1\nv_add_u32 v90, 1, v90\ns_mov_b32 s56, %[fh]\ns_add_u32 s40, s40, 1\ns_cmp_lt_u32 s40, 64\n" \
"s_cbranch_scc1 .Lgg_next%=\ns_waitcnt vmcnt(0)\nbuffer_store_dword v90, off, %[rs], s56 sc1\ns_branch .Lgg_end%=\n.Lgg_abort%=:\ns_mov_b64 exec, -1\nv_mov_b32 v16, 0\ns_movk_i32 s44, 6400\n" \
"buffer_store_dword v16, off, %[rs], s44 sc1\n.Lgg_end%=:\ns_waitcnt vmcnt(0) lgkmcnt(0)\n" \
""
#define DD_ASM \
"v_mbcnt_lo_u32_b32 v255, -1, 0\nv_mbcnt_hi_u32_b32 v255, -1, v255\nv_lshlrev_b32 v255, 2, v255\nv_add_u32 v255, %[tb], v255\nds_read_b32 v0, v255 offset:0\nds_read_b32 v1, v255 offset:256\nds_read_b32 v2, v255 offset:512\nds_read_b32 v3, v255 offset:768\n" \
"ds_read_b32 v4, v255 offset:1024\nds_read_b32 v5, v255 offset:1280\nds_read_b32 v6, v255 offset:1536\nds_read_b32 v7, v255 offset:1792\nds_read_b32 v125, v255 offset:2048\nds_read_b32 v126, v255 offset:2304\nds_read_b32 v127, v255 offset:2560\ns_waitcnt lgkmcnt(0)\n" \
"s_add_u32 m0, %[tb], 8192\ns_add_u32 s44, %[wo], 0\nbuffer_load_dwordx4 v125, %[rw], s44 offen nt lds\ns_add_u32 m0, %[tb], 9216\ns_add_u32 s44, %[wo], 16384\nbuffer_load_dwordx4 v125, %[rw], s44 offen nt lds\ns_add_u32 m0, %[tb], 10240\ns_add_u32 s44, %[wo], 32768\n" \
"buffer_load_dwordx4 v125, %[rw], s44 offen nt lds\ns_add_u32 m0, %[tb], 11264\ns_add_u32 s44, %[wo], 49152\nbuffer_load_dwordx4 v125, %[rw], s44 offen nt lds\ns_add_u32 m0, %[lw], 0\ns_add_u32 s44, %[wo], 65536\nbuffer_load_dwordx4 v125, %[rw], s44 offen nt lds\ns_add_u32 m0, %[lw], 1024\n" \
"s_add_u32 s44, %[wo], 81920\nbuffer_load_dwordx4 v125, %[rw], s44 offen nt lds\ns_add_u32 m0, %[lw], 2048\ns_add_u32 s44, %[wo], 98304\nbuffer_load_dwordx4 v125, %[rw], s44 offen nt lds\ns_add_u32 m0, %[lw], 3072\ns_add_u32 s44, %[wo], 114688\nbuffer_load_dwordx4 v125, %[rw], s44 offen nt lds\n" \
"s_add_u32 m0, %[lw], 4096\ns_add_u32 s44, %[wo], 131072\nbuffer_load_dwordx4 v125, %[rw], s44 offen nt lds\ns_add_u32 m0, %[lw], 5120\ns_add_u32 s44, %[wo], 147456\nbuffer_load_dwordx4 v125, %[rw], s44 offen nt lds\ns_add_u32 m0, %[lw], 6144\ns_add_u32 s44, %[wo], 163840\n" \
"buffer_load_dwordx4 v125, %[rw], s44 offen nt lds\ns_add_u32 m0, %[lw], 7168\ns_add_u32 s44, %[wo], 180224\nbuffer_load_dwordx4 v125, %[rw], s44 offen nt lds\ns_add_u32 m0, %[lw], 8192\ns_add_u32 s44, %[wo], 196608\nbuffer_load_dwordx4 v125, %[rw], s44 offen nt lds\ns_add_u32 m0, %[lw], 9216\n" \
"s_add_u32 s44, %[wo], 212992\nbuffer_load_dwordx4 v125, %[rw], s44 offen nt lds\ns_add_u32 m0, %[lw], 10240\ns_add_u32 s44, %[wo], 229376\nbuffer_load_dwordx4 v125, %[rw], s44 offen nt lds\ns_add_u32 m0, %[lw], 11264\ns_add_u32 s44, %[wo], 245760\nbuffer_load_dwordx4 v125, %[rw], s44 offen nt lds\n" \
"s_add_u32 m0, %[lw], 12288\ns_add_u32 s44, %[wo], 262144\nbuffer_load_dwordx4 v125, %[rw], s44 offen nt lds\ns_add_u32 m0, %[lw], 13312\ns_add_u32 s44, %[wo], 278528\nbuffer_load_dwordx4 v125, %[rw], s44 offen nt lds\ns_add_u32 m0, %[lw], 14336\ns_add_u32 s44, %[wo], 294912\n" \
"buffer_load_dwordx4 v125, %[rw], s44 offen nt lds\ns_add_u32 m0, %[lw], 15360\ns_add_u32 s44, %[wo], 311296\nbuffer_load_dwordx4 v125, %[rw], s44 offen nt lds\ns_add_u32 m0, %[lw], 16384\ns_add_u32 s44, %[wo], 327680\nbuffer_load_dwordx4 v125, %[rw], s44 offen nt lds\ns_add_u32 m0, %[lw], 17408\n" \
"s_add_u32 s44, %[wo], 344064\nbuffer_load_dwordx4 v125, %[rw], s44 offen nt lds\ns_add_u32 m0, %[lw], 18432\ns_add_u32 s44, %[wo], 360448\nbuffer_load_dwordx4 v125, %[rw], s44 offen nt lds\ns_add_u32 m0, %[lw], 19456\ns_add_u32 s44, %[wo], 376832\nbuffer_load_dwordx4 v125, %[rw], s44 offen nt lds\n" \
"s_add_u32 m0, %[lw], 20480\ns_add_u32 s44, %[wo], 393216\nbuffer_load_dwordx4 v125, %[rw], s44 offen nt lds\ns_add_u32 m0, %[lw], 21504\ns_add_u32 s44, %[wo], 409600\nbuffer_load_dwordx4 v125, %[rw], s44 offen nt lds\ns_add_u32 m0, %[lw], 22528\ns_add_u32 s44, %[wo], 425984\n" \
"buffer_load_dwordx4 v125, %[rw], s44 offen nt lds\ns_add_u32 m0, %[lw], 23552\ns_add_u32 s44, %[wo], 442368\nbuffer_load_dwordx4 v125, %[rw], s44 offen nt lds\ns_add_u32 m0, %[tb], 0\ns_add_u32 s44, %[wo], 458752\nbuffer_load_dwordx4 v125, %[rw], s44 offen nt lds\ns_add_u32 m0, %[tb], 1024\n" \
"s_add_u32 s44, %[wo], 475136\nbuffer_load_dwordx4 v125, %[rw], s44 offen nt lds\ns_add_u32 m0, %[tb], 2048\ns_add_u32 s44, %[wo], 491520\nbuffer_load_dwordx4 v125, %[rw], s44 offen nt lds\ns_add_u32 m0, %[tb], 3072\ns_add_u32 s44, %[wo], 507904\nbuffer_load_dwordx4 v125, %[rw], s44 offen nt lds\n" \
"s_add_u32 m0, %[tb], 4096\ns_add_u32 s44, %[wo], 524288\nbuffer_load_dwordx4 v125, %[rw], s44 offen nt lds\ns_add_u32 m0, %[tb], 5120\ns_add_u32 s44, %[wo], 540672\nbuffer_load_dwordx4 v125, %[rw], s44 offen nt lds\ns_add_u32 m0, %[tb], 6144\ns_add_u32 s44, %[wo], 557056\n" \
"buffer_load_dwordx4 v125, %[rw], s44 offen nt lds\ns_add_u32 m0, %[tb], 7168\ns_add_u32 s44, %[wo], 573440\nbuffer_load_dwordx4 v125, %[rw], s44 offen nt lds\ns_waitcnt vmcnt(32)\nds_read_b64_tr_b16 a[0:1], v126 offset:8192\nds_read_b64_tr_b16 a[2:3], v126 offset:8704\nds_read_b64_tr_b16 a[8:9], v126 offset:8224\n" \
"ds_read_b64_tr_b16 a[10:11], v126 offset:8736\nds_read_b64_tr_b16 a[16:17], v126 offset:8256\nds_read_b64_tr_b16 a[18:19], v126 offset:8768\nds_read_b64_tr_b16 a[24:25], v126 offset:8288\nds_read_b64_tr_b16 a[26:27], v126 offset:8800\ns_waitcnt lgkmcnt(0)\ns_add_u32 m0, %[tb], 8192\ns_add_u32 s44, %[wo], 589824\n" \
"buffer_load_dwordx4 v125, %[rw], s44 offen nt lds\ns_add_u32 m0, %[tb], 9216\ns_add_u32 s44, %[wo], 606208\nbuffer_load_dwordx4 v125, %[rw], s44 offen nt lds\ns_add_u32 m0, %[tb], 10240\ns_add_u32 s44, %[wo], 622592\nbuffer_load_dwordx4 v125, %[rw], s44 offen nt lds\ns_add_u32 m0, %[tb], 11264\n" \
"s_add_u32 s44, %[wo], 638976\nbuffer_load_dwordx4 v125, %[rw], s44 offen nt lds\ns_waitcnt vmcnt(32)\nds_read_b64_tr_b16 a[4:5], v127 offset:0\nds_read_b64_tr_b16 a[6:7], v127 offset:512\nds_read_b64_tr_b16 a[12:13], v127 offset:32\nds_read_b64_tr_b16 a[14:15], v127 offset:544\nds_read_b64_tr_b16 a[20:21], v127 offset:64\n" \
"ds_read_b64_tr_b16 a[22:23], v127 offset:576\nds_read_b64_tr_b16 a[28:29], v127 offset:96\nds_read_b64_tr_b16 a[30:31], v127 offset:608\ns_waitcnt lgkmcnt(0)\ns_add_u32 m0, %[lw], 0\ns_add_u32 s44, %[wo], 655360\nbuffer_load_dwordx4 v125, %[rw], s44 offen nt lds\ns_add_u32 m0, %[lw], 1024\n" \
"s_add_u32 s44, %[wo], 671744\nbuffer_load_dwordx4 v125, %[rw], s44 offen nt lds\ns_add_u32 m0, %[lw], 2048\ns_add_u32 s44, %[wo], 688128\nbuffer_load_dwordx4 v125, %[rw], s44 offen nt lds\ns_add_u32 m0, %[lw], 3072\ns_add_u32 s44, %[wo], 704512\nbuffer_load_dwordx4 v125, %[rw], s44 offen nt lds\n" \
"s_waitcnt vmcnt(32)\nds_read_b64_tr_b16 a[32:33], v127 offset:4096\nds_read_b64_tr_b16 a[34:35], v127 offset:4608\nds_read_b64_tr_b16 v[92:93], v127 offset:4128\nds_read_b64_tr_b16 v[94:95], v127 offset:4640\nds_read_b64_tr_b16 a[40:41], v127 offset:4160\nds_read_b64_tr_b16 a[42:43], v127 offset:4672\nds_read_b64_tr_b16 a[48:49], v127 offset:4192\n" \
"ds_read_b64_tr_b16 a[50:51], v127 offset:4704\ns_waitcnt lgkmcnt(0)\ns_add_u32 m0, %[lw], 4096\ns_add_u32 s44, %[wo], 720896\nbuffer_load_dwordx4 v125, %[rw], s44 offen nt lds\ns_add_u32 m0, %[lw], 5120\ns_add_u32 s44, %[wo], 737280\nbuffer_load_dwordx4 v125, %[rw], s44 offen nt lds\n" \
"s_add_u32 m0, %[lw], 6144\ns_add_u32 s44, %[wo], 753664\nbuffer_load_dwordx4 v125, %[rw], s44 offen nt lds\ns_add_u32 m0, %[lw], 7168\ns_add_u32 s44, %[wo], 770048\nbuffer_load_dwordx4 v125, %[rw], s44 offen nt lds\ns_waitcnt vmcnt(32)\nds_read_b64_tr_b16 a[36:37], v127 offset:8192\n" \
"ds_read_b64_tr_b16 a[38:39], v127 offset:8704\nds_read_b64_tr_b16 v[88:89], v127 offset:8224\nds_read_b64_tr_b16 v[90:91], v127 offset:8736\nds_read_b64_tr_b16 a[44:45], v127 offset:8256\nds_read_b64_tr_b16 a[46:47], v127 offset:8768\nds_read_b64_tr_b16 a[52:53], v127 offset:8288\nds_read_b64_tr_b16 a[54:55], v127 offset:8800\ns_waitcnt lgkmcnt(0)\n" \
"s_add_u32 m0, %[lw], 8192\ns_add_u32 s44, %[wo], 786432\nbuffer_load_dwordx4 v125, %[rw], s44 offen nt lds\ns_add_u32 m0, %[lw], 9216\ns_add_u32 s44, %[wo], 802816\nbuffer_load_dwordx4 v125, %[rw], s44 offen nt lds\ns_add_u32 m0, %[lw], 10240\ns_add_u32 s44, %[wo], 819200\n" \
"buffer_load_dwordx4 v125, %[rw], s44 offen nt lds\ns_add_u32 m0, %[lw], 11264\ns_add_u32 s44, %[wo], 835584\nbuffer_load_dwordx4 v125, %[rw], s44 offen nt lds\ns_waitcnt vmcnt(32)\nds_read_b64_tr_b16 a[56:57], v127 offset:12288\nds_read_b64_tr_b16 a[58:59], v127 offset:12800\nds_read_b64_tr_b16 a[64:65], v127 offset:12320\n" \
"ds_read_b64_tr_b16 a[66:67], v127 offset:12832\nds_read_b64_tr_b16 a[72:73], v127 offset:12352\nds_read_b64_tr_b16 a[74:75], v127 offset:12864\nds_read_b64_tr_b16 a[80:81], v127 offset:12384\nds_read_b64_tr_b16 a[82:83], v127 offset:12896\ns_waitcnt lgkmcnt(0)\ns_add_u32 m0, %[lw], 12288\ns_add_u32 s44, %[wo], 851968\n" \
"buffer_load_dwordx4 v125, %[rw], s44 offen nt lds\ns_add_u32 m0, %[lw], 13312\ns_add_u32 s44, %[wo], 868352\nbuffer_load_dwordx4 v125, %[rw], s44 offen nt lds\ns_add_u32 m0, %[lw], 14336\ns_add_u32 s44, %[wo], 884736\nbuffer_load_dwordx4 v125, %[rw], s44 offen nt lds\ns_add_u32 m0, %[lw], 15360\n" \
"s_add_u32 s44, %[wo], 901120\nbuffer_load_dwordx4 v125, %[rw], s44 offen nt lds\ns_waitcnt vmcnt(32)\nds_read_b64_tr_b16 a[60:61], v127 offset:16384\nds_read_b64_tr_b16 a[62:63], v127 offset:16896\nds_read_b64_tr_b16 a[68:69], v127 offset:16416\nds_read_b64_tr_b16 a[70:71], v127 offset:16928\nds_read_b64_tr_b16 a[76:77], v127 offset:16448\n" \
"ds_read_b64_tr_b16 a[78:79], v127 offset:16960\nds_read_b64_tr_b16 a[84:85], v127 offset:16480\nds_read_b64_tr_b16 a[86:87], v127 offset:16992\ns_waitcnt lgkmcnt(0)\ns_add_u32 m0, %[lw], 16384\ns_add_u32 s44, %[wo], 917504\nbuffer_load_dwordx4 v125, %[rw], s44 offen nt lds\ns_add_u32 m0, %[lw], 17408\n" \
"s_add_u32 s44, %[wo], 933888\nbuffer_load_dwordx4 v125, %[rw], s44 offen nt lds\ns_add_u32 m0, %[lw], 18432\ns_add_u32 s44, %[wo], 950272\nbuffer_load_dwordx4 v125, %[rw], s44 offen nt lds\ns_add_u32 m0, %[lw], 19456\ns_add_u32 s44, %[wo], 966656\nbuffer_load_dwordx4 v125, %[rw], s44 offen nt lds\n" \
"s_waitcnt vmcnt(32)\nds_read_b64_tr_b16 a[88:89], v127 offset:20480\nds_read_b64_tr_b16 a[90:91], v127 offset:20992\nds_read_b64_tr_b16 a[96:97], v127 offset:20512\nds_read_b64_tr_b16 a[98:99], v127 offset:21024\nds_read_b64_tr_b16 a[104:105], v127 offset:20544\nds_read_b64_tr_b16 a[106:107], v127 offset:21056\nds_read_b64_tr_b16 v[84:85], v127 offset:20576\n" \
"ds_read_b64_tr_b16 v[86:87], v127 offset:21088\ns_waitcnt lgkmcnt(0)\ns_add_u32 m0, %[lw], 20480\ns_add_u32 s44, %[wo], 983040\nbuffer_load_dwordx4 v125, %[rw], s44 offen nt lds\ns_add_u32 m0, %[lw], 21504\ns_add_u32 s44, %[wo], 999424\nbuffer_load_dwordx4 v125, %[rw], s44 offen nt lds\n" \
"s_add_u32 m0, %[lw], 22528\ns_add_u32 s44, %[wo], 1015808\nbuffer_load_dwordx4 v125, %[rw], s44 offen nt lds\ns_add_u32 m0, %[lw], 23552\ns_add_u32 s44, %[wo], 1032192\nbuffer_load_dwordx4 v125, %[rw], s44 offen nt lds\ns_waitcnt vmcnt(32)\nds_read_b64_tr_b16 a[92:93], v126 offset:0\n" \
"ds_read_b64_tr_b16 a[94:95], v126 offset:512\nds_read_b64_tr_b16 a[100:101], v126 offset:32\nds_read_b64_tr_b16 a[102:103], v126 offset:544\nds_read_b64_tr_b16 a[108:109], v126 offset:64\nds_read_b64_tr_b16 a[110:111], v126 offset:576\nds_read_b64_tr_b16 v[80:81], v126 offset:96\nds_read_b64_tr_b16 v[82:83], v126 offset:608\ns_waitcnt lgkmcnt(0)\n" \
"s_add_u32 m0, %[tb], 0\ns_add_u32 s44, %[wo], 1048576\nbuffer_load_dwordx4 v125, %[rw], s44 offen nt lds\ns_add_u32 m0, %[tb], 1024\ns_add_u32 s44, %[wo], 1064960\nbuffer_load_dwordx4 v125, %[rw], s44 offen nt lds\ns_add_u32 m0, %[tb], 2048\ns_add_u32 s44, %[wo], 1081344\n" \
"buffer_load_dwordx4 v125, %[rw], s44 offen nt lds\ns_add_u32 m0, %[tb], 3072\ns_add_u32 s44, %[wo], 1097728\nbuffer_load_dwordx4 v125, %[rw], s44 offen nt lds\ns_waitcnt vmcnt(32)\nds_read_b64_tr_b16 a[112:113], v126 offset:4096\nds_read_b64_tr_b16 a[114:115], v126 offset:4608\nds_read_b64_tr_b16 a[120:121], v126 offset:4128\n" \
"ds_read_b64_tr_b16 a[122:123], v126 offset:4640\nds_read_b64_tr_b16 a[128:129], v126 offset:4160\nds_read_b64_tr_b16 a[130:131], v126 offset:4672\nds_read_b64_tr_b16 a[136:137], v126 offset:4192\nds_read_b64_tr_b16 a[138:139], v126 offset:4704\ns_waitcnt lgkmcnt(0)\ns_add_u32 m0, %[tb], 4096\ns_add_u32 s44, %[wo], 1114112\n" \
"buffer_load_dwordx4 v125, %[rw], s44 offen nt lds\ns_add_u32 m0, %[tb], 5120\ns_add_u32 s44, %[wo], 1130496\nbuffer_load_dwordx4 v125, %[rw], s44 offen nt lds\ns_add_u32 m0, %[tb], 6144\ns_add_u32 s44, %[wo], 1146880\nbuffer_load_dwordx4 v125, %[rw], s44 offen nt lds\ns_add_u32 m0, %[tb], 7168\n" \
"s_add_u32 s44, %[wo], 1163264\nbuffer_load_dwordx4 v125, %[rw], s44 offen nt lds\ns_waitcnt vmcnt(32)\nds_read_b64_tr_b16 a[116:117], v126 offset:8192\nds_read_b64_tr_b16 a[118:119], v126 offset:8704\nds_read_b64_tr_b16 a[124:125], v126 offset:8224\nds_read_b64_tr_b16 a[126:127], v126 offset:8736\nds_read_b64_tr_b16 a[132:133], v126 offset:8256\n" \
"ds_read_b64_tr_b16 a[134:135], v126 offset:8768\nds_read_b64_tr_b16 a[140:141], v126 offset:8288\nds_read_b64_tr_b16 a[142:143], v126 offset:8800\ns_waitcnt lgkmcnt(0)\ns_add_u32 m0, %[tb], 8192\ns_add_u32 s44, %[wo], 1179648\nbuffer_load_dwordx4 v125, %[rw], s44 offen nt lds\ns_add_u32 m0, %[tb], 9216\n" \
"s_add_u32 s44, %[wo], 1196032\nbuffer_load_dwordx4 v125, %[rw], s44 offen nt lds\ns_add_u32 m0, %[tb], 10240\ns_add_u32 s44, %[wo], 1212416\nbuffer_load_dwordx4 v125, %[rw], s44 offen nt lds\ns_add_u32 m0, %[tb], 11264\ns_add_u32 s44, %[wo], 1228800\nbuffer_load_dwordx4 v125, %[rw], s44 offen nt lds\n" \
"s_waitcnt vmcnt(32)\nds_read_b64_tr_b16 a[144:145], v127 offset:0\nds_read_b64_tr_b16 a[146:147], v127 offset:512\nds_read_b64_tr_b16 v[76:77], v127 offset:32\nds_read_b64_tr_b16 v[78:79], v127 offset:544\nds_read_b64_tr_b16 a[152:153], v127 offset:64\nds_read_b64_tr_b16 a[154:155], v127 offset:576\nds_read_b64_tr_b16 a[160:161], v127 offset:96\n" \
"ds_read_b64_tr_b16 a[162:163], v127 offset:608\ns_waitcnt lgkmcnt(0)\ns_add_u32 m0, %[lw], 0\ns_add_u32 s44, %[wo], 1245184\nbuffer_load_dwordx4 v125, %[rw], s44 offen nt lds\ns_add_u32 m0, %[lw], 1024\ns_add_u32 s44, %[wo], 1261568\nbuffer_load_dwordx4 v125, %[rw], s44 offen nt lds\n" \
"s_add_u32 m0, %[lw], 2048\ns_add_u32 s44, %[wo], 1277952\nbuffer_load_dwordx4 v125, %[rw], s44 offen nt lds\ns_add_u32 m0, %[lw], 3072\ns_add_u32 s44, %[wo], 1294336\nbuffer_load_dwordx4 v125, %[rw], s44 offen nt lds\ns_waitcnt vmcnt(32)\nds_read_b64_tr_b16 a[148:149], v127 offset:4096\n" \
"ds_read_b64_tr_b16 a[150:151], v127 offset:4608\nds_read_b64_tr_b16 v[72:73], v127 offset:4128\nds_read_b64_tr_b16 v[74:75], v127 offset:4640\nds_read_b64_tr_b16 a[156:157], v127 offset:4160\nds_read_b64_tr_b16 a[158:159], v127 offset:4672\nds_read_b64_tr_b16 a[164:165], v127 offset:4192\nds_read_b64_tr_b16 a[166:167], v127 offset:4704\ns_waitcnt lgkmcnt(0)\n" \
"s_add_u32 m0, %[lw], 4096\ns_add_u32 s44, %[wo], 1310720\nbuffer_load_dwordx4 v125, %[rw], s44 offen nt lds\ns_add_u32 m0, %[lw], 5120\ns_add_u32 s44, %[wo], 1327104\nbuffer_load_dwordx4 v125, %[rw], s44 offen nt lds\ns_add_u32 m0, %[lw], 6144\ns_add_u32 s44, %[wo], 1343488\n" \
"buffer_load_dwordx4 v125, %[rw], s44 offen nt lds\ns_add_u32 m0, %[lw], 7168\ns_add_u32 s44, %[wo], 1359872\nbuffer_load_dwordx4 v125, %[rw], s44 offen nt lds\ns_waitcnt vmcnt(32)\nds_read_b64_tr_b16 a[168:169], v127 offset:8192\nds_read_b64_tr_b16 a[170:171], v127 offset:8704\nds_read_b64_tr_b16 a[176:177], v127 offset:8224\n" \
"ds_read_b64_tr_b16 a[178:179], v127 offset:8736\nds_read_b64_tr_b16 v[68:69], v127 offset:8256\nds_read_b64_tr_b16 v[70:71], v127 offset:8768\nds_read_b64_tr_b16 a[184:185], v127 offset:8288\nds_read_b64_tr_b16 a[186:187], v127 offset:8800\ns_waitcnt lgkmcnt(0)\ns_add_u32 m0, %[lw], 8192\ns_add_u32 s44, %[wo], 1376256\n" \
"buffer_load_dwordx4 v125, %[rw], s44 offen nt lds\ns_add_u32 m0, %[lw], 9216\ns_add_u32 s44, %[wo], 1392640\nbuffer_load_dwordx4 v125, %[rw], s44 offen nt lds\ns_add_u32 m0, %[lw], 10240\ns_add_u32 s44, %[wo], 1409024\nbuffer_load_dwordx4 v125, %[rw], s44 offen nt lds\ns_add_u32 m0, %[lw], 11264\n" \
"s_add_u32 s44, %[wo], 1425408\nbuffer_load_dwordx4 v125, %[rw], s44 offen nt lds\ns_waitcnt vmcnt(32)\nds_read_b64_tr_b16 a[172:173], v127 offset:12288\nds_read_b64_tr_b16 a[174:175], v127 offset:12800\nds_read_b64_tr_b16 a[180:181], v127 offset:12320\nds_read_b64_tr_b16 a[182:183], v127 offset:12832\nds_read_b64_tr_b16 v[64:65], v127 offset:12352\n" \
"ds_read_b64_tr_b16 v[66:67], v127 offset:12864\nds_read_b64_tr_b16 a[188:189], v127 offset:12384\nds_read_b64_tr_b16 a[190:191], v127 offset:12896\ns_waitcnt lgkmcnt(0)\ns_add_u32 m0, %[lw], 12288\ns_add_u32 s44, %[wo], 1441792\nbuffer_load_dwordx4 v125, %[rw], s44 offen nt lds\ns_add_u32 m0, %[lw], 13312\n" \
"s_add_u32 s44, %[wo], 1458176\nbuffer_load_dwordx4 v125, %[rw], s44 offen nt lds\ns_add_u32 m0, %[lw], 14336\ns_add_u32 s44, %[wo], 1474560\nbuffer_load_dwordx4 v125, %[rw], s44 offen nt lds\ns_add_u32 m0, %[lw], 15360\ns_add_u32 s44, %[wo], 1490944\nbuffer_load_dwordx4 v125, %[rw], s44 offen nt lds\n" \
"s_waitcnt vmcnt(32)\nds_read_b64_tr_b16 a[192:193], v127 offset:16384\nds_read_b64_tr_b16 a[194:195], v127 offset:16896\nds_read_b64_tr_b16 a[200:201], v127 offset:16416\nds_read_b64_tr_b16 a[202:203], v127 offset:16928\nds_read_b64_tr_b16 a[208:209], v127 offset:16448\nds_read_b64_tr_b16 a[210:211], v127 offset:16960\nds_read_b64_tr_b16 a[216:217], v127 offset:16480\n" \
"ds_read_b64_tr_b16 a[218:219], v127 offset:16992\ns_waitcnt lgkmcnt(0)\ns_add_u32 m0, %[lw], 16384\ns_add_u32 s44, %[wo], 1507328\nbuffer_load_dwordx4 v125, %[rw], s44 offen nt lds\ns_add_u32 m0, %[lw], 17408\ns_add_u32 s44, %[wo], 1523712\nbuffer_load_dwordx4 v125, %[rw], s44 offen nt lds\n" \
"s_add_u32 m0, %[lw], 18432\ns_add_u32 s44, %[wo], 1540096\nbuffer_load_dwordx4 v125, %[rw], s44 offen nt lds\ns_add_u32 m0, %[lw], 19456\ns_add_u32 s44, %[wo], 1556480\nbuffer_load_dwordx4 v125, %[rw], s44 offen nt lds\ns_waitcnt vmcnt(32)\nds_read_b64_tr_b16 a[196:197], v127 offset:20480\n" \
"ds_read_b64_tr_b16 a[198:199], v127 offset:20992\nds_read_b64_tr_b16 a[204:205], v127 offset:20512\nds_read_b64_tr_b16 a[206:207], v127 offset:21024\nds_read_b64_tr_b16 a[212:213], v127 offset:20544\nds_read_b64_tr_b16 a[214:215], v127 offset:21056\nds_read_b64_tr_b16 a[220:221], v127 offset:20576\nds_read_b64_tr_b16 a[222:223], v127 offset:21088\ns_waitcnt lgkmcnt(0)\n" \
"s_add_u32 m0, %[lw], 20480\ns_add_u32 s44, %[wo], 1572864\nbuffer_load_dwordx4 v125, %[rw], s44 offen nt lds\ns_add_u32 m0, %[lw], 21504\ns_add_u32 s44, %[wo], 1589248\nbuffer_load_dwordx4 v125, %[rw], s44 offen nt lds\ns_add_u32 m0, %[lw], 22528\ns_add_u32 s44, %[wo], 1605632\n" \
"buffer_load_dwordx4 v125, %[rw], s44 offen nt lds\ns_add_u32 m0, %[lw], 23552\ns_add_u32 s44, %[wo], 1622016\nbuffer_load_dwordx4 v125, %[rw], s44 offen nt lds\ns_waitcnt vmcnt(32)\nds_read_b64_tr_b16 v[60:61], v126 offset:0\nds_read_b64_tr_b16 v[62:63], v126 offset:512\nds_read_b64_tr_b16 a[224:225], v126 offset:32\n" \
"ds_read_b64_tr_b16 a[226:227], v126 offset:544\nds_read_b64_tr_b16 a[232:233], v126 offset:64\nds_read_b64_tr_b16 a[234:235], v126 offset:576\nds_read_b64_tr_b16 a[240:241], v126 offset:96\nds_read_b64_tr_b16 a[242:243], v126 offset:608\ns_waitcnt lgkmcnt(0)\ns_add_u32 m0, %[tb], 0\ns_add_u32 s44, %[wo], 1638400\n" \
"buffer_load_dwordx4 v125, %[rw], s44 offen nt lds\ns_add_u32 m0, %[tb], 1024\ns_add_u32 s44, %[wo], 1654784\nbuffer_load_dwordx4 v125, %[rw], s44 offen nt lds\ns_add_u32 m0, %[tb], 2048\ns_add_u32 s44, %[wo], 1671168\nbuffer_load_dwordx4 v125, %[rw], s44 offen nt lds\ns_add_u32 m0, %[tb], 3072\n" \
"s_add_u32 s44, %[wo], 1687552\nbuffer_load_dwordx4 v125, %[rw], s44 offen nt lds\ns_waitcnt vmcnt(32)\nds_read_b64_tr_b16 v[56:57], v126 offset:4096\nds_read_b64_tr_b16 v[58:59], v126 offset:4608\nds_read_b64_tr_b16 a[228:229], v126 offset:4128\nds_read_b64_tr_b16 a[230:231], v126 offset:4640\nds_read_b64_tr_b16 a[236:237], v126 offset:4160\n" \
"ds_read_b64_tr_b16 a[238:239], v126 offset:4672\nds_read_b64_tr_b16 a[244:245], v126 offset:4192\nds_read_b64_tr_b16 a[246:247], v126 offset:4704\ns_waitcnt lgkmcnt(0)\ns_add_u32 m0, %[tb], 4096\ns_add_u32 s44, %[wo], 1703936\nbuffer_load_dwordx4 v125, %[rw], s44 offen nt lds\ns_add_u32 m0, %[tb], 5120\n" \
"s_add_u32 s44, %[wo], 1720320\nbuffer_load_dwordx4 v125, %[rw], s44 offen nt lds\ns_add_u32 m0, %[tb], 6144\ns_add_u32 s44, %[wo], 1736704\nbuffer_load_dwordx4 v125, %[rw], s44 offen nt lds\ns_add_u32 m0, %[tb], 7168\ns_add_u32 s44, %[wo], 1753088\nbuffer_load_dwordx4 v125, %[rw], s44 offen nt lds\n" \
"s_waitcnt vmcnt(32)\nds_read_b64_tr_b16 a[248:249], v126 offset:8192\nds_read_b64_tr_b16 a[250:251], v126 offset:8704\nds_read_b64_tr_b16 v[128:129], v126 offset:8224\nds_read_b64_tr_b16 v[130:131], v126 offset:8736\nds_read_b64_tr_b16 v[136:137], v126 offset:8256\nds_read_b64_tr_b16 v[138:139], v126 offset:8768\nds_read_b64_tr_b16 v[144:145], v126 offset:8288\n" \
"ds_read_b64_tr_b16 v[146:147], v126 offset:8800\ns_waitcnt lgkmcnt(0)\ns_add_u32 m0, %[tb], 8192\ns_add_u32 s44, %[wo], 1769472\nbuffer_load_dwordx4 v125, %[rw], s44 offen nt lds\ns_add_u32 m0, %[tb], 9216\ns_add_u32 s44, %[wo], 1785856\nbuffer_load_dwordx4 v125, %[rw], s44 offen nt lds\n" \
"s_add_u32 m0, %[tb], 10240\ns_add_u32 s44, %[wo], 1802240\nbuffer_load_dwordx4 v125, %[rw], s44 offen nt lds\ns_add_u32 m0, %[tb], 11264\ns_add_u32 s44, %[wo], 1818624\nbuffer_load_dwordx4 v125, %[rw], s44 offen nt lds\ns_waitcnt vmcnt(32)\nds_read_b64_tr_b16 a[252:253], v127 offset:0\n" \
"ds_read_b64_tr_b16 a[254:255], v127 offset:512\nds_read_b64_tr_b16 v[132:133], v127 offset:32\nds_read_b64_tr_b16 v[134:135], v127 offset:544\nds_read_b64_tr_b16 v[140:141], v127 offset:64\nds_read_b64_tr_b16 v[142:143], v127 offset:576\nds_read_b64_tr_b16 v[148:149], v127 offset:96\nds_read_b64_tr_b16 v[150:151], v127 offset:608\ns_waitcnt lgkmcnt(0)\n" \
"ds_write_b128 v1, v[92:95] offset:0\nds_write_b128 v1, v[88:91] offset:1024\nds_write_b128 v1, v[84:87] offset:2048\nds_write_b128 v1, v[80:83] offset:3072\ns_waitcnt vmcnt(28)\nds_read_b64_tr_b16 v[152:153], v127 offset:4096\nds_read_b64_tr_b16 v[154:155], v127 offset:4608\nds_read_b64_tr_b16 v[160:161], v127 offset:4128\n" \
"ds_read_b64_tr_b16 v[162:163], v127 offset:4640\nds_read_b64_tr_b16 v[80:81], v127 offset:4160\nds_read_b64_tr_b16 v[82:83], v127 offset:4672\nds_read_b64_tr_b16 v[168:169], v127 offset:4192\nds_read_b64_tr_b16 v[170:171], v127 offset:4704\ns_waitcnt lgkmcnt(0)\nds_write_b128 v1, v[76:79] offset:4096\nds_write_b128 v1, v[72:75] offset:5120\n" \
"ds_write_b128 v1, v[68:71] offset:6144\nds_write_b128 v1, v[64:67] offset:7168\ns_waitcnt vmcnt(24)\nds_read_b64_tr_b16 v[156:157], v127 offset:8192\nds_read_b64_tr_b16 v[158:159], v127 offset:8704\nds_read_b64_tr_b16 v[164:165], v127 offset:8224\nds_read_b64_tr_b16 v[166:167], v127 offset:8736\nds_read_b64_tr_b16 v[64:65], v127 offset:8256\n" \
"ds_read_b64_tr_b16 v[66:67], v127 offset:8768\nds_read_b64_tr_b16 v[172:173], v127 offset:8288\nds_read_b64_tr_b16 v[174:175], v127 offset:8800\ns_waitcnt lgkmcnt(0)\nds_write_b128 v1, v[60:63] offset:8192\nds_write_b128 v1, v[56:59] offset:9216\nds_write_b128 v1, v[80:83] offset:10240\nds_write_b128 v1, v[64:67] offset:11264\n" \
"s_waitcnt vmcnt(20)\nds_read_b64_tr_b16 v[176:177], v127 offset:12288\nds_read_b64_tr_b16 v[178:179], v127 offset:12800\nds_read_b64_tr_b16 v[184:185], v127 offset:12320\nds_read_b64_tr_b16 v[186:187], v127 offset:12832\nds_read_b64_tr_b16 v[192:193], v127 offset:12352\nds_read_b64_tr_b16 v[194:195], v127 offset:12864\nds_read_b64_tr_b16 v[200:201], v127 offset:12384\n" \
"ds_read_b64_tr_b16 v[202:203], v127 offset:12896\ns_waitcnt lgkmcnt(0)\ns_waitcnt vmcnt(16)\nds_read_b64_tr_b16 v[180:181], v127 offset:16384\nds_read_b64_tr_b16 v[182:183], v127 offset:16896\nds_read_b64_tr_b16 v[188:189], v127 offset:16416\nds_read_b64_tr_b16 v[190:191], v127 offset:16928\nds_read_b64_tr_b16 v[196:197], v127 offset:16448\n" \
"ds_read_b64_tr_b16 v[198:199], v127 offset:16960\nds_read_b64_tr_b16 v[204:205], v127 offset:16480\nds_read_b64_tr_b16 v[206:207], v127 offset:16992\ns_waitcnt lgkmcnt(0)\ns_waitcnt vmcnt(12)\nds_read_b64_tr_b16 v[64:65], v127 offset:20480\nds_read_b64_tr_b16 v[66:67], v127 offset:20992\nds_read_b64_tr_b16 v[208:209], v127 offset:20512\n" \
"ds_read_b64_tr_b16 v[210:211], v127 offset:21024\nds_read_b64_tr_b16 v[216:217], v127 offset:20544\nds_read_b64_tr_b16 v[218:219], v127 offset:21056\nds_read_b64_tr_b16 v[224:225], v127 offset:20576\nds_read_b64_tr_b16 v[226:227], v127 offset:21088\ns_waitcnt lgkmcnt(0)\nds_write_b128 v1, v[64:67] offset:12288\ns_waitcnt vmcnt(8)\n" \
"ds_read_b64_tr_b16 v[64:65], v126 offset:0\nds_read_b64_tr_b16 v[66:67], v126 offset:512\nds_read_b64_tr_b16 v[212:213], v126 offset:32\nds_read_b64_tr_b16 v[214:215], v126 offset:544\nds_read_b64_tr_b16 v[220:221], v126 offset:64\nds_read_b64_tr_b16 v[222:223], v126 offset:576\nds_read_b64_tr_b16 v[228:229], v126 offset:96\nds_read_b64_tr_b16 v[230:231], v126 offset:608\n" \
"s_waitcnt lgkmcnt(0)\nds_write_b128 v1, v[64:67] offset:13312\ns_waitcnt vmcnt(4)\nds_read_b64_tr_b16 v[232:233], v126 offset:4096\nds_read_b64_tr_b16 v[234:235], v126 offset:4608\nds_read_b64_tr_b16 v[64:65], v126 offset:4128\nds_read_b64_tr_b16 v[66:67], v126 offset:4640\nds_read_b64_tr_b16 v[240:241], v126 offset:4160\n" \
"ds_read_b64_tr_b16 v[242:243], v126 offset:4672\nds_read_b64_tr_b16 v[248:249], v126 offset:4192\nds_read_b64_tr_b16 v[250:251], v126 offset:4704\ns_waitcnt lgkmcnt(0)\nds_write_b128 v1, v[64:67] offset:14336\ns_waitcnt vmcnt(0)\nds_read_b64_tr_b16 v[236:237], v126 offset:8192\nds_read_b64_tr_b16 v[238:239], v126 offset:8704\n" \
"ds_read_b64_tr_b16 v[64:65], v126 offset:8224\nds_read_b64_tr_b16 v[66:67], v126 offset:8736\nds_read_b64_tr_b16 v[244:245], v126 offset:8256\nds_read_b64_tr_b16 v[246:247], v126 offset:8768\nds_read_b64_tr_b16 v[252:253], v126 offset:8288\nds_read_b64_tr_b16 v[254:255], v126 offset:8800\ns_waitcnt lgkmcnt(0)\nds_write_b128 v1, v[64:67] offset:15360\n" \
"s_waitcnt lgkmcnt(0)\ns_barrier\ns_mov_b32 s40, 0\ns_mov_b32 s43, 0\ns_mov_b32 s56, -2147483648\n.Ldd_next%=:\nbuffer_load_dword v8, v7, %[rs], 0 offen sc1\ns_lshl_b32 s58, s40, 1\n" \
"s_lshl_b32 s44, s40, 19\ns_add_u32 s44, s44, %[ho]\ns_lshr_b32 s45, s44, 8\ns_and_b32 s45, s45, 0x2000\ns_xor_b32 s55, s44, s45\ns_waitcnt vmcnt(0)\nbuffer_store_dword v9, off, %[rs], s56 sc1\n.Ldd_w0%=:\n" \
"v_cmp_gt_i32 vcc, s58, v8\ns_cbranch_vccz .Ldd_w0go%=\ns_cmp_ge_u32 s40, 62\ns_cbranch_scc1 .Ldd_ns666%=\ns_sleep 16\n.Ldd_ns666%=:\ns_sleep 1\nbuffer_load_dword v8, v7, %[rs], 0 offen sc1\n" \
"s_waitcnt vmcnt(0)\ns_add_u32 s43, s43, 1\ns_cmp_lt_u32 s43, 262144\ns_cbranch_scc1 .Ldd_w0%=\ns_branch .Ldd_abort%=\n.Ldd_w0go%=:\ns_add_u32 s60, s55, 0\nbuffer_load_dwordx4 v[48:51], v0, %[rs], s60 offen offset:0 sc1\n" \
"buffer_load_dwordx4 v[52:55], v0, %[rs], s60 offen offset:2048 sc1\ns_add_u32 s60, s55, 4096\nbuffer_load_dwordx4 v[56:59], v0, %[rs], s60 offen offset:0 sc1\nbuffer_load_dwordx4 v[60:63], v0, %[rs], s60 offen offset:2048 sc1\ns_add_u32 s60, s55, 16384\nbuffer_load_dwordx4 v[64:67], v0, %[rs], s60 offen offset:0 sc1\nbuffer_load_dwordx4 v[68:71], v0, %[rs], s60 offen offset:2048 sc1\ns_add_u32 s60, s55, 20480\n" \
"buffer_load_dwordx4 v[72:75], v0, %[rs], s60 offen offset:0 sc1\nbuffer_load_dwordx4 v[76:79], v0, %[rs], s60 offen offset:2048 sc1\ns_add_u32 s60, s55, 32768\nbuffer_load_dwordx4 v[80:83], v0, %[rs], s60 offen offset:0 sc1\nbuffer_load_dwordx4 v[84:87], v0, %[rs], s60 offen offset:2048 sc1\ns_add_u32 s60, s55, 36864\nbuffer_load_dwordx4 v[88:91], v0, %[rs], s60 offen offset:0 sc1\nbuffer_load_dwordx4 v[92:95], v0, %[rs], s60 offen offset:2048 sc1\n" \
"s_add_u32 s60, s55, 49152\nbuffer_load_dwordx4 v[96:99], v0, %[rs], s60 offen offset:0 sc1\nbuffer_load_dwordx4 v[100:103], v0, %[rs], s60 offen offset:2048 sc1\ns_add_u32 s60, s55, 53248\nbuffer_load_dwordx4 v[104:107], v0, %[rs], s60 offen offset:0 sc1\nbuffer_load_dwordx4 v[108:111], v0, %[rs], s60 offen offset:2048 sc1\ns_add_u32 s44, s40, 0\ns_lshl_b32 s44, s44, 17\n" \
"s_add_u32 s44, s44, %[xn]\ns_lshr_b32 s45, s44, 8\ns_and_b32 s45, s45, 0x2000\ns_xor_b32 s41, s44, s45\ns_lshl_b32 s44, s40, 16\ns_cmp_eq_u32 %[l], 0\ns_cselect_b32 s42, s41, s44\ns_add_u32 s58, s58, 1\n" \
"ds_read_b128 v[120:123], v1 offset:0\ns_waitcnt vmcnt(15)\nv_mfma_f32_16x16x32_bf16 v[16:19], a[0:3], v[48:51], 0\nds_read_b128 v[124:127], v1 offset:1024\nv_mfma_f32_16x16x32_bf16 v[24:27], a[8:11], v[48:51], 0\nbuffer_load_dword v8, v7, %[rs], 0 offen sc1\nv_mfma_f32_16x16x32_bf16 v[32:35], a[16:19], v[48:51], 0\nv_mfma_f32_16x16x32_bf16 v[40:43], a[24:27], v[48:51], 0\n" \
"s_add_u32 s60, s55, 65536\ns_waitcnt vmcnt(15)\nbuffer_load_dwordx4 v[48:51], v0, %[rs], s60 offen offset:0 sc1\nv_mfma_f32_16x16x32_bf16 v[16:19], a[4:7], v[52:55], v[16:19]\nv_mfma_f32_16x16x32_bf16 v[24:27], a[12:15], v[52:55], v[24:27]\nv_mfma_f32_16x16x32_bf16 v[32:35], a[20:23], v[52:55], v[32:35]\nv_mfma_f32_16x16x32_bf16 v[40:43], a[28:31], v[52:55], v[40:43]\ns_waitcnt vmcnt(15)\n" \
"buffer_load_dwordx4 v[52:55], v0, %[rs], s60 offen offset:2048 sc1\nv_mfma_f32_16x16x32_bf16 v[16:19], a[32:35], v[56:59], v[16:19]\ns_waitcnt lgkmcnt(1)\nv_mfma_f32_16x16x32_bf16 v[24:27], v[120:123], v[56:59], v[24:27]\nv_mfma_f32_16x16x32_bf16 v[32:35], a[40:43], v[56:59], v[32:35]\nv_mfma_f32_16x16x32_bf16 v[40:43], a[48:51], v[56:59], v[40:43]\ns_add_u32 s60, s55, 69632\ns_waitcnt vmcnt(15)\n" \
"buffer_load_dwordx4 v[56:59], v0, %[rs], s60 offen offset:0 sc1\nv_mfma_f32_16x16x32_bf16 v[16:19], a[36:39], v[60:63], v[16:19]\ns_waitcnt lgkmcnt(0)\nds_read_b128 v[120:123], v1 offset:2048\nv_mfma_f32_16x16x32_bf16 v[24:27], v[124:127], v[60:63], v[24:27]\nv_mfma_f32_16x16x32_bf16 v[32:35], a[44:47], v[60:63], v[32:35]\nv_mfma_f32_16x16x32_bf16 v[40:43], a[52:55], v[60:63], v[40:43]\ns_waitcnt vmcnt(15)\n" \
"buffer_load_dwordx4 v[60:63], v0, %[rs], s60 offen offset:2048 sc1\nv_mfma_f32_16x16x32_bf16 v[16:19], a[56:59], v[64:67], v[16:19]\nds_read_b128 v[124:127], v1 offset:3072\nv_mfma_f32_16x16x32_bf16 v[24:27], a[64:67], v[64:67], v[24:27]\nv_mfma_f32_16x16x32_bf16 v[32:35], a[72:75], v[64:67], v[32:35]\nv_mfma_f32_16x16x32_bf16 v[40:43], a[80:83], v[64:67], v[40:43]\ns_add_u32 s60, s55, 81920\ns_waitcnt vmcnt(15)\n" \
"buffer_load_dwordx4 v[64:67], v0, %[rs], s60 offen offset:0 sc1\nv_mfma_f32_16x16x32_bf16 v[16:19], a[60:63], v[68:71], v[16:19]\nv_mfma_f32_16x16x32_bf16 v[24:27], a[68:71], v[68:71], v[24:27]\nv_mfma_f32_16x16x32_bf16 v[32:35], a[76:79], v[68:71], v[32:35]\nv_mfma_f32_16x16x32_bf16 v[40:43], a[84:87], v[68:71], v[40:43]\ns_waitcnt vmcnt(15)\nbuffer_load_dwordx4 v[68:71], v0, %[rs], s60 offen offset:2048 sc1\nv_mfma_f32_16x16x32_bf16 v[16:19], a[88:91], v[72:75], v[16:19]\n" \
"v_mfma_f32_16x16x32_bf16 v[24:27], a[96:99], v[72:75], v[24:27]\nv_mfma_f32_16x16x32_bf16 v[32:35], a[104:107], v[72:75], v[32:35]\ns_waitcnt lgkmcnt(1)\nv_mfma_f32_16x16x32_bf16 v[40:43], v[120:123], v[72:75], v[40:43]\ns_add_u32 s60, s55, 86016\ns_waitcnt vmcnt(15)\nbuffer_load_dwordx4 v[72:75], v0, %[rs], s60 offen offset:0 sc1\nv_mfma_f32_16x16x32_bf16 v[16:19], a[92:95], v[76:79], v[16:19]\n" \
"ds_read_b128 v[120:123], v1 offset:4096\nv_mfma_f32_16x16x32_bf16 v[24:27], a[100:103], v[76:79], v[24:27]\nv_mfma_f32_16x16x32_bf16 v[32:35], a[108:111], v[76:79], v[32:35]\ns_waitcnt lgkmcnt(1)\nv_mfma_f32_16x16x32_bf16 v[40:43], v[124:127], v[76:79], v[40:43]\ns_waitcnt vmcnt(15)\nbuffer_load_dwordx4 v[76:79], v0, %[rs], s60 offen offset:2048 sc1\nv_mfma_f32_16x16x32_bf16 v[16:19], a[112:115], v[80:83], v[16:19]\n" \
"ds_read_b128 v[124:127], v1 offset:5120\nv_mfma_f32_16x16x32_bf16 v[24:27], a[120:123], v[80:83], v[24:27]\nv_mfma_f32_16x16x32_bf16 v[32:35], a[128:131], v[80:83], v[32:35]\nv_mfma_f32_16x16x32_bf16 v[40:43], a[136:139], v[80:83], v[40:43]\ns_add_u32 s60, s55, 98304\ns_waitcnt vmcnt(15)\nbuffer_load_dwordx4 v[80:83], v0, %[rs], s60 offen offset:0 sc1\nv_mfma_f32_16x16x32_bf16 v[16:19], a[116:119], v[84:87], v[16:19]\n" \
"ds_read_b128 v[112:115], v1 offset:6144\nv_mfma_f32_16x16x32_bf16 v[24:27], a[124:127], v[84:87], v[24:27]\nv_mfma_f32_16x16x32_bf16 v[32:35], a[132:135], v[84:87], v[32:35]\nv_mfma_f32_16x16x32_bf16 v[40:43], a[140:143], v[84:87], v[40:43]\ns_waitcnt vmcnt(15)\nbuffer_load_dwordx4 v[84:87], v0, %[rs], s60 offen offset:2048 sc1\nv_mfma_f32_16x16x32_bf16 v[16:19], a[144:147], v[88:91], v[16:19]\ns_waitcnt lgkmcnt(2)\n" \
"ds_read_b128 v[116:119], v1 offset:7168\nv_mfma_f32_16x16x32_bf16 v[24:27], v[120:123], v[88:91], v[24:27]\nv_mfma_f32_16x16x32_bf16 v[32:35], a[152:155], v[88:91], v[32:35]\nv_mfma_f32_16x16x32_bf16 v[40:43], a[160:163], v[88:91], v[40:43]\ns_add_u32 s60, s55, 102400\ns_waitcnt vmcnt(15)\nbuffer_load_dwordx4 v[88:91], v0, %[rs], s60 offen offset:0 sc1\nv_mfma_f32_16x16x32_bf16 v[16:19], a[148:151], v[92:95], v[16:19]\n" \
"s_waitcnt lgkmcnt(2)\nv_mfma_f32_16x16x32_bf16 v[24:27], v[124:127], v[92:95], v[24:27]\ns_waitcnt vmcnt(11)\nv_cmp_gt_i32 vcc, s58, v8\nv_mfma_f32_16x16x32_bf16 v[32:35], a[156:159], v[92:95], v[32:35]\ns_cmp_eq_u64 vcc, 0\ns_cselect_b32 s61, s55, -2147483648\nv_mfma_f32_16x16x32_bf16 v[40:43], a[164:167], v[92:95], v[40:43]\n" \
"buffer_load_dwordx4 v[92:95], v0, %[rs], s60 offen offset:2048 sc1\nv_mfma_f32_16x16x32_bf16 v[16:19], a[168:171], v[96:99], v[16:19]\nv_mfma_f32_16x16x32_bf16 v[24:27], a[176:179], v[96:99], v[24:27]\ns_waitcnt lgkmcnt(1)\nv_mfma_f32_16x16x32_bf16 v[32:35], v[112:115], v[96:99], v[32:35]\nv_mfma_f32_16x16x32_bf16 v[40:43], a[184:187], v[96:99], v[40:43]\ns_add_u32 s60, s61, 0\nbuffer_load_dwordx4 v[96:99], v0, %[rs], s60 offen offset:1024 sc1\n" \
"v_mfma_f32_16x16x32_bf16 v[16:19], a[172:175], v[100:103], v[16:19]\nds_read_b128 v[112:115], v1 offset:8192\nv_mfma_f32_16x16x32_bf16 v[24:27], a[180:183], v[100:103], v[24:27]\ns_waitcnt lgkmcnt(1)\nv_mfma_f32_16x16x32_bf16 v[32:35], v[116:119], v[100:103], v[32:35]\nv_mfma_f32_16x16x32_bf16 v[40:43], a[188:191], v[100:103], v[40:43]\nbuffer_load_dwordx4 v[100:103], v0, %[rs], s60 offen offset:3072 sc1\nv_mfma_f32_16x16x32_bf16 v[16:19], a[192:195], v[104:107], v[16:19]\n" \
"ds_read_b128 v[116:119], v1 offset:9216\nv_mfma_f32_16x16x32_bf16 v[24:27], a[200:203], v[104:107], v[24:27]\nv_mfma_f32_16x16x32_bf16 v[32:35], a[208:211], v[104:107], v[32:35]\nv_mfma_f32_16x16x32_bf16 v[40:43], a[216:219], v[104:107], v[40:43]\ns_add_u32 s60, s61, 4096\nbuffer_load_dwordx4 v[104:107], v0, %[rs], s60 offen offset:1024 sc1\nv_mfma_f32_16x16x32_bf16 v[16:19], a[196:199], v[108:111], v[16:19]\nv_mfma_f32_16x16x32_bf16 v[24:27], a[204:207], v[108:111], v[24:27]\n" \
"v_mfma_f32_16x16x32_bf16 v[32:35], a[212:215], v[108:111], v[32:35]\nv_mfma_f32_16x16x32_bf16 v[40:43], a[220:223], v[108:111], v[40:43]\ns_waitcnt vmcnt(14)\ns_waitcnt lgkmcnt(1)\nbuffer_load_dwordx4 v[108:111], v0, %[rs], s60 offen offset:3072 sc1\nv_mfma_f32_16x16x32_bf16 v[16:19], v[112:115], v[48:51], v[16:19]\nv_mfma_f32_16x16x32_bf16 v[24:27], a[224:227], v[48:51], v[24:27]\nv_mfma_f32_16x16x32_bf16 v[32:35], a[232:235], v[48:51], v[32:35]\n" \
"v_mfma_f32_16x16x32_bf16 v[40:43], a[240:243], v[48:51], v[40:43]\ns_add_u32 s60, s61, 16384\ns_waitcnt vmcnt(14)\ns_waitcnt lgkmcnt(0)\nbuffer_load_dwordx4 v[48:51], v0, %[rs], s60 offen offset:1024 sc1\nv_mfma_f32_16x16x32_bf16 v[16:19], v[116:119], v[52:55], v[16:19]\nds_read_b128 v[112:115], v1 offset:10240\nv_mfma_f32_16x16x32_bf16 v[24:27], a[228:231], v[52:55], v[24:27]\n" \
"v_mfma_f32_16x16x32_bf16 v[32:35], a[236:239], v[52:55], v[32:35]\nv_mfma_f32_16x16x32_bf16 v[40:43], a[244:247], v[52:55], v[40:43]\ns_waitcnt vmcnt(14)\nbuffer_load_dwordx4 v[52:55], v0, %[rs], s60 offen offset:3072 sc1\nv_mfma_f32_16x16x32_bf16 v[16:19], a[248:251], v[56:59], v[16:19]\nds_read_b128 v[116:119], v1 offset:11264\nv_mfma_f32_16x16x32_bf16 v[24:27], v[128:131], v[56:59], v[24:27]\nv_mfma_f32_16x16x32_bf16 v[32:35], v[136:139], v[56:59], v[32:35]\n" \
"v_mfma_f32_16x16x32_bf16 v[40:43], v[144:147], v[56:59], v[40:43]\ns_add_u32 s60, s61, 20480\ns_waitcnt vmcnt(14)\nbuffer_load_dwordx4 v[56:59], v0, %[rs], s60 offen offset:1024 sc1\nv_mfma_f32_16x16x32_bf16 v[16:19], a[252:255], v[60:63], v[16:19]\nv_mfma_f32_16x16x32_bf16 v[24:27], v[132:135], v[60:63], v[24:27]\nv_mfma_f32_16x16x32_bf16 v[32:35], v[140:143], v[60:63], v[32:35]\nv_mfma_f32_16x16x32_bf16 v[40:43], v[148:151], v[60:63], v[40:43]\n" \
"s_waitcnt vmcnt(14)\nbuffer_load_dwordx4 v[60:63], v0, %[rs], s60 offen offset:3072 sc1\nv_mfma_f32_16x16x32_bf16 v[16:19], v[152:155], v[64:67], v[16:19]\nv_mfma_f32_16x16x32_bf16 v[24:27], v[160:163], v[64:67], v[24:27]\ns_waitcnt lgkmcnt(1)\nv_mfma_f32_16x16x32_bf16 v[32:35], v[112:115], v[64:67], v[32:35]\nv_mfma_f32_16x16x32_bf16 v[40:43], v[168:171], v[64:67], v[40:43]\ns_add_u32 s60, s61, 32768\n" \
"s_waitcnt vmcnt(14)\nbuffer_load_dwordx4 v[64:67], v0, %[rs], s60 offen offset:1024 sc1\nv_mfma_f32_16x16x32_bf16 v[16:19], v[156:159], v[68:71], v[16:19]\nds_read_b128 v[112:115], v1 offset:12288\nv_mfma_f32_16x16x32_bf16 v[24:27], v[164:167], v[68:71], v[24:27]\ns_waitcnt lgkmcnt(1)\nv_mfma_f32_16x16x32_bf16 v[32:35], v[116:119], v[68:71], v[32:35]\nv_mfma_f32_16x16x32_bf16 v[40:43], v[172:175], v[68:71], v[40:43]\n" \
"s_waitcnt vmcnt(14)\nbuffer_load_dwordx4 v[68:71], v0, %[rs], s60 offen offset:3072 sc1\nv_mfma_f32_16x16x32_bf16 v[16:19], v[176:179], v[72:75], v[16:19]\nds_read_b128 v[116:119], v1 offset:13312\nv_mfma_f32_16x16x32_bf16 v[24:27], v[184:187], v[72:75], v[24:27]\nv_mfma_f32_16x16x32_bf16 v[32:35], v[192:195], v[72:75], v[32:35]\nv_mfma_f32_16x16x32_bf16 v[40:43], v[200:203], v[72:75], v[40:43]\ns_add_u32 s60, s61, 36864\n" \
"s_waitcnt vmcnt(14)\nbuffer_load_dwordx4 v[72:75], v0, %[rs], s60 offen offset:1024 sc1\nv_mfma_f32_16x16x32_bf16 v[16:19], v[180:183], v[76:79], v[16:19]\nds_read_b128 v[120:123], v1 offset:14336\nv_mfma_f32_16x16x32_bf16 v[24:27], v[188:191], v[76:79], v[24:27]\nv_mfma_f32_16x16x32_bf16 v[32:35], v[196:199], v[76:79], v[32:35]\nv_mfma_f32_16x16x32_bf16 v[40:43], v[204:207], v[76:79], v[40:43]\ns_waitcnt vmcnt(14)\n" \
"s_waitcnt lgkmcnt(2)\nbuffer_load_dwordx4 v[76:79], v0, %[rs], s60 offen offset:3072 sc1\nv_mfma_f32_16x16x32_bf16 v[16:19], v[112:115], v[80:83], v[16:19]\nds_read_b128 v[124:127], v1 offset:15360\nv_mfma_f32_16x16x32_bf16 v[24:27], v[208:211], v[80:83], v[24:27]\nv_mfma_f32_16x16x32_bf16 v[32:35], v[216:219], v[80:83], v[32:35]\nv_mfma_f32_16x16x32_bf16 v[40:43], v[224:227], v[80:83], v[40:43]\ns_add_u32 s60, s61, 49152\n" \
"s_waitcnt vmcnt(14)\ns_waitcnt lgkmcnt(2)\nbuffer_load_dwordx4 v[80:83], v0, %[rs], s60 offen offset:1024 sc1\nv_mfma_f32_16x16x32_bf16 v[16:19], v[116:119], v[84:87], v[16:19]\nv_mfma_f32_16x16x32_bf16 v[24:27], v[212:215], v[84:87], v[24:27]\nv_mfma_f32_16x16x32_bf16 v[32:35], v[220:223], v[84:87], v[32:35]\nv_mfma_f32_16x16x32_bf16 v[40:43], v[228:231], v[84:87], v[40:43]\ns_waitcnt vmcnt(14)\n" \
"buffer_load_dwordx4 v[84:87], v0, %[rs], s60 offen offset:3072 sc1\nv_mfma_f32_16x16x32_bf16 v[16:19], v[232:235], v[88:91], v[16:19]\ns_waitcnt lgkmcnt(1)\nv_mfma_f32_16x16x32_bf16 v[24:27], v[120:123], v[88:91], v[24:27]\nv_mfma_f32_16x16x32_bf16 v[32:35], v[240:243], v[88:91], v[32:35]\nv_mfma_f32_16x16x32_bf16 v[40:43], v[248:251], v[88:91], v[40:43]\ns_add_u32 s60, s61, 53248\ns_waitcnt vmcnt(14)\n" \
"buffer_load_dwordx4 v[88:91], v0, %[rs], s60 offen offset:1024 sc1\nv_mfma_f32_16x16x32_bf16 v[16:19], v[236:239], v[92:95], v[16:19]\ns_waitcnt lgkmcnt(0)\nds_read_b128 v[120:123], v1 offset:0\nv_mfma_f32_16x16x32_bf16 v[24:27], v[124:127], v[92:95], v[24:27]\nv_mfma_f32_16x16x32_bf16 v[32:35], v[244:247], v[92:95], v[32:35]\nv_mfma_f32_16x16x32_bf16 v[40:43], v[252:255], v[92:95], v[40:43]\nbuffer_load_dwordx4 v[92:95], v0, %[rs], s60 offen offset:3072 sc1\n" \
"s_cbranch_vccnz .Ldd_slow%=\n.Ldd_slowr%=:\ns_waitcnt vmcnt(15)\nv_mfma_f32_16x16x32_bf16 v[20:23], a[0:3], v[96:99], 0\nds_read_b128 v[124:127], v1 offset:1024\nv_mfma_f32_16x16x32_bf16 v[28:31], a[8:11], v[96:99], 0\nv_mfma_f32_16x16x32_bf16 v[36:39], a[16:19], v[96:99], 0\nv_mfma_f32_16x16x32_bf16 v[44:47], a[24:27], v[96:99], 0\n" \
"s_add_u32 s60, s55, 65536\ns_waitcnt vmcnt(14)\nbuffer_load_dwordx4 v[96:99], v0, %[rs], s60 offen offset:1024 sc1\nv_mfma_f32_16x16x32_bf16 v[20:23], a[4:7], v[100:103], v[20:23]\nv_mfma_f32_16x16x32_bf16 v[28:31], a[12:15], v[100:103], v[28:31]\nv_mfma_f32_16x16x32_bf16 v[36:39], a[20:23], v[100:103], v[36:39]\nv_mfma_f32_16x16x32_bf16 v[44:47], a[28:31], v[100:103], v[44:47]\ns_waitcnt vmcnt(14)\n" \
"buffer_load_dwordx4 v[100:103], v0, %[rs], s60 offen offset:3072 sc1\nv_mfma_f32_16x16x32_bf16 v[20:23], a[32:35], v[104:107], v[20:23]\ns_waitcnt lgkmcnt(1)\nv_mfma_f32_16x16x32_bf16 v[28:31], v[120:123], v[104:107], v[28:31]\nds_write_b128 v2, v[16:19] offset:0\nv_mfma_f32_16x16x32_bf16 v[36:39], a[40:43], v[104:107], v[36:39]\nv_mfma_f32_16x16x32_bf16 v[44:47], a[48:51], v[104:107], v[44:47]\ns_add_u32 s60, s55, 69632\n" \
"s_waitcnt vmcnt(14)\nbuffer_load_dwordx4 v[104:107], v0, %[rs], s60 offen offset:1024 sc1\nv_mfma_f32_16x16x32_bf16 v[20:23], a[36:39], v[108:111], v[20:23]\ns_waitcnt lgkmcnt(1)\nds_read_b128 v[120:123], v1 offset:2048\nv_mfma_f32_16x16x32_bf16 v[28:31], v[124:127], v[108:111], v[28:31]\nv_mfma_f32_16x16x32_bf16 v[36:39], a[44:47], v[108:111], v[36:39]\nv_mfma_f32_16x16x32_bf16 v[44:47], a[52:55], v[108:111], v[44:47]\n" \
"s_waitcnt vmcnt(14)\nbuffer_load_dwordx4 v[108:111], v0, %[rs], s60 offen offset:3072 sc1\nv_mfma_f32_16x16x32_bf16 v[20:23], a[56:59], v[48:51], v[20:23]\nds_read_b128 v[124:127], v1 offset:3072\nv_mfma_f32_16x16x32_bf16 v[28:31], a[64:67], v[48:51], v[28:31]\nds_write_b128 v2, v[24:27] offset:2048\nv_mfma_f32_16x16x32_bf16 v[36:39], a[72:75], v[48:51], v[36:39]\nv_mfma_f32_16x16x32_bf16 v[44:47], a[80:83], v[48:51], v[44:47]\n" \
"s_add_u32 s60, s55, 81920\ns_waitcnt vmcnt(14)\nbuffer_load_dwordx4 v[48:51], v0, %[rs], s60 offen offset:1024 sc1\nv_mfma_f32_16x16x32_bf16 v[20:23], a[60:63], v[52:55], v[20:23]\nv_mfma_f32_16x16x32_bf16 v[28:31], a[68:71], v[52:55], v[28:31]\nv_mfma_f32_16x16x32_bf16 v[36:39], a[76:79], v[52:55], v[36:39]\nv_mfma_f32_16x16x32_bf16 v[44:47], a[84:87], v[52:55], v[44:47]\ns_waitcnt vmcnt(14)\n" \
"buffer_load_dwordx4 v[52:55], v0, %[rs], s60 offen offset:3072 sc1\nv_mfma_f32_16x16x32_bf16 v[20:23], a[88:91], v[56:59], v[20:23]\nv_mfma_f32_16x16x32_bf16 v[28:31], a[96:99], v[56:59], v[28:31]\nds_write_b128 v2, v[32:35] offset:4096\nv_mfma_f32_16x16x32_bf16 v[36:39], a[104:107], v[56:59], v[36:39]\ns_waitcnt lgkmcnt(3)\nv_mfma_f32_16x16x32_bf16 v[44:47], v[120:123], v[56:59], v[44:47]\ns_add_u32 s60, s55, 86016\n" \
"s_waitcnt vmcnt(14)\nbuffer_load_dwordx4 v[56:59], v0, %[rs], s60 offen offset:1024 sc1\nv_mfma_f32_16x16x32_bf16 v[20:23], a[92:95], v[60:63], v[20:23]\nds_read_b128 v[120:123], v1 offset:4096\nv_mfma_f32_16x16x32_bf16 v[28:31], a[100:103], v[60:63], v[28:31]\nv_mfma_f32_16x16x32_bf16 v[36:39], a[108:111], v[60:63], v[36:39]\ns_waitcnt lgkmcnt(3)\nv_mfma_f32_16x16x32_bf16 v[44:47], v[124:127], v[60:63], v[44:47]\n" \
"s_waitcnt vmcnt(14)\nbuffer_load_dwordx4 v[60:63], v0, %[rs], s60 offen offset:3072 sc1\nv_mfma_f32_16x16x32_bf16 v[20:23], a[112:115], v[64:67], v[20:23]\nds_read_b128 v[124:127], v1 offset:5120\nv_mfma_f32_16x16x32_bf16 v[28:31], a[120:123], v[64:67], v[28:31]\nds_write_b128 v2, v[40:43] offset:6144\nv_mfma_f32_16x16x32_bf16 v[36:39], a[128:131], v[64:67], v[36:39]\nv_mfma_f32_16x16x32_bf16 v[44:47], a[136:139], v[64:67], v[44:47]\n" \
"s_add_u32 s60, s55, 98304\ns_waitcnt vmcnt(14)\nbuffer_load_dwordx4 v[64:67], v0, %[rs], s60 offen offset:1024 sc1\nv_mfma_f32_16x16x32_bf16 v[20:23], a[116:119], v[68:71], v[20:23]\nds_read_b128 v[112:115], v1 offset:6144\nv_mfma_f32_16x16x32_bf16 v[28:31], a[124:127], v[68:71], v[28:31]\nv_mfma_f32_16x16x32_bf16 v[36:39], a[132:135], v[68:71], v[36:39]\nv_mfma_f32_16x16x32_bf16 v[44:47], a[140:143], v[68:71], v[44:47]\n" \
"s_waitcnt vmcnt(14)\nbuffer_load_dwordx4 v[68:71], v0, %[rs], s60 offen offset:3072 sc1\nv_mfma_f32_16x16x32_bf16 v[20:23], a[144:147], v[72:75], v[20:23]\ns_waitcnt lgkmcnt(3)\nds_read_b128 v[116:119], v1 offset:7168\nv_mfma_f32_16x16x32_bf16 v[28:31], v[120:123], v[72:75], v[28:31]\nv_mfma_f32_16x16x32_bf16 v[36:39], a[152:155], v[72:75], v[36:39]\nv_mfma_f32_16x16x32_bf16 v[44:47], a[160:163], v[72:75], v[44:47]\n" \
"s_add_u32 s60, s55, 102400\ns_waitcnt vmcnt(14)\nbuffer_load_dwordx4 v[72:75], v0, %[rs], s60 offen offset:1024 sc1\nv_mfma_f32_16x16x32_bf16 v[20:23], a[148:151], v[76:79], v[20:23]\ns_waitcnt lgkmcnt(3)\nv_mfma_f32_16x16x32_bf16 v[28:31], v[124:127], v[76:79], v[28:31]\nv_mfma_f32_16x16x32_bf16 v[36:39], a[156:159], v[76:79], v[36:39]\nv_mfma_f32_16x16x32_bf16 v[44:47], a[164:167], v[76:79], v[44:47]\n" \
"s_waitcnt vmcnt(14)\nbuffer_load_dwordx4 v[76:79], v0, %[rs], s60 offen offset:3072 sc1\nv_mfma_f32_16x16x32_bf16 v[20:23], a[168:171], v[80:83], v[20:23]\nv_mfma_f32_16x16x32_bf16 v[28:31], a[176:179], v[80:83], v[28:31]\ns_waitcnt lgkmcnt(1)\nv_mfma_f32_16x16x32_bf16 v[36:39], v[112:115], v[80:83], v[36:39]\nv_mfma_f32_16x16x32_bf16 v[44:47], a[184:187], v[80:83], v[44:47]\ns_waitcnt vmcnt(14)\n" \
"buffer_load_dwordx4 v[12:15], v4, %[rs], s41 offen sc1\nv_mfma_f32_16x16x32_bf16 v[20:23], a[172:175], v[84:87], v[20:23]\nds_read_b128 v[112:115], v1 offset:8192\nv_mfma_f32_16x16x32_bf16 v[28:31], a[180:183], v[84:87], v[28:31]\ns_waitcnt lgkmcnt(1)\nv_mfma_f32_16x16x32_bf16 v[36:39], v[116:119], v[84:87], v[36:39]\nv_mfma_f32_16x16x32_bf16 v[44:47], a[188:191], v[84:87], v[44:47]\ns_waitcnt vmcnt(14)\n" \
"v_mfma_f32_16x16x32_bf16 v[20:23], a[192:195], v[88:91], v[20:23]\nds_read_b128 v[116:119], v1 offset:9216\nv_mfma_f32_16x16x32_bf16 v[28:31], a[200:203], v[88:91], v[28:31]\nv_mfma_f32_16x16x32_bf16 v[36:39], a[208:211], v[88:91], v[36:39]\nv_mfma_f32_16x16x32_bf16 v[44:47], a[216:219], v[88:91], v[44:47]\ns_waitcnt vmcnt(13)\nv_mfma_f32_16x16x32_bf16 v[20:23], a[196:199], v[92:95], v[20:23]\nv_mfma_f32_16x16x32_bf16 v[28:31], a[204:207], v[92:95], v[28:31]\n" \
"v_mfma_f32_16x16x32_bf16 v[36:39], a[212:215], v[92:95], v[36:39]\nv_mfma_f32_16x16x32_bf16 v[44:47], a[220:223], v[92:95], v[44:47]\ns_waitcnt vmcnt(12)\ns_waitcnt lgkmcnt(1)\nv_mfma_f32_16x16x32_bf16 v[20:23], v[112:115], v[96:99], v[20:23]\nv_mfma_f32_16x16x32_bf16 v[28:31], a[224:227], v[96:99], v[28:31]\nv_mfma_f32_16x16x32_bf16 v[36:39], a[232:235], v[96:99], v[36:39]\nv_mfma_f32_16x16x32_bf16 v[44:47], a[240:243], v[96:99], v[44:47]\n" \
"s_waitcnt vmcnt(11)\ns_waitcnt lgkmcnt(0)\nv_mfma_f32_16x16x32_bf16 v[20:23], v[116:119], v[100:103], v[20:23]\nds_read_b128 v[112:115], v1 offset:10240\nv_mfma_f32_16x16x32_bf16 v[28:31], a[228:231], v[100:103], v[28:31]\nv_mfma_f32_16x16x32_bf16 v[36:39], a[236:239], v[100:103], v[36:39]\nv_mfma_f32_16x16x32_bf16 v[44:47], a[244:247], v[100:103], v[44:47]\ns_waitcnt vmcnt(10)\n" \
"v_mfma_f32_16x16x32_bf16 v[20:23], a[248:251], v[104:107], v[20:23]\nds_read_b128 v[116:119], v1 offset:11264\nv_mfma_f32_16x16x32_bf16 v[28:31], v[128:131], v[104:107], v[28:31]\nv_mfma_f32_16x16x32_bf16 v[36:39], v[136:139], v[104:107], v[36:39]\nv_mfma_f32_16x16x32_bf16 v[44:47], v[144:147], v[104:107], v[44:47]\ns_waitcnt vmcnt(9)\nv_mfma_f32_16x16x32_bf16 v[20:23], a[252:255], v[108:111], v[20:23]\nv_mfma_f32_16x16x32_bf16 v[28:31], v[132:135], v[108:111], v[28:31]\n" \
"v_mfma_f32_16x16x32_bf16 v[36:39], v[140:143], v[108:111], v[36:39]\nv_mfma_f32_16x16x32_bf16 v[44:47], v[148:151], v[108:111], v[44:47]\ns_waitcnt vmcnt(8)\nv_mfma_f32_16x16x32_bf16 v[20:23], v[152:155], v[48:51], v[20:23]\nv_mfma_f32_16x16x32_bf16 v[28:31], v[160:163], v[48:51], v[28:31]\ns_waitcnt lgkmcnt(1)\nv_mfma_f32_16x16x32_bf16 v[36:39], v[112:115], v[48:51], v[36:39]\nv_mfma_f32_16x16x32_bf16 v[44:47], v[168:171], v[48:51], v[44:47]\n" \
"s_waitcnt vmcnt(7)\nv_mfma_f32_16x16x32_bf16 v[20:23], v[156:159], v[52:55], v[20:23]\nds_read_b128 v[112:115], v1 offset:12288\nv_mfma_f32_16x16x32_bf16 v[28:31], v[164:167], v[52:55], v[28:31]\ns_waitcnt lgkmcnt(1)\nv_mfma_f32_16x16x32_bf16 v[36:39], v[116:119], v[52:55], v[36:39]\nv_mfma_f32_16x16x32_bf16 v[44:47], v[172:175], v[52:55], v[44:47]\ns_waitcnt vmcnt(6)\n" \
"v_mfma_f32_16x16x32_bf16 v[20:23], v[176:179], v[56:59], v[20:23]\nds_read_b128 v[116:119], v1 offset:13312\nv_mfma_f32_16x16x32_bf16 v[28:31], v[184:187], v[56:59], v[28:31]\nv_mfma_f32_16x16x32_bf16 v[36:39], v[192:195], v[56:59], v[36:39]\nv_mfma_f32_16x16x32_bf16 v[44:47], v[200:203], v[56:59], v[44:47]\ns_waitcnt vmcnt(5)\nv_mfma_f32_16x16x32_bf16 v[20:23], v[180:183], v[60:63], v[20:23]\nds_read_b128 v[120:123], v1 offset:14336\n" \
"v_mfma_f32_16x16x32_bf16 v[28:31], v[188:191], v[60:63], v[28:31]\nv_mfma_f32_16x16x32_bf16 v[36:39], v[196:199], v[60:63], v[36:39]\nv_mfma_f32_16x16x32_bf16 v[44:47], v[204:207], v[60:63], v[44:47]\ns_waitcnt vmcnt(4)\ns_waitcnt lgkmcnt(2)\nv_mfma_f32_16x16x32_bf16 v[20:23], v[112:115], v[64:67], v[20:23]\nds_read_b128 v[124:127], v1 offset:15360\nv_mfma_f32_16x16x32_bf16 v[28:31], v[208:211], v[64:67], v[28:31]\n" \
"v_mfma_f32_16x16x32_bf16 v[36:39], v[216:219], v[64:67], v[36:39]\nv_mfma_f32_16x16x32_bf16 v[44:47], v[224:227], v[64:67], v[44:47]\ns_waitcnt vmcnt(3)\ns_waitcnt lgkmcnt(2)\nv_mfma_f32_16x16x32_bf16 v[20:23], v[116:119], v[68:71], v[20:23]\nv_mfma_f32_16x16x32_bf16 v[28:31], v[212:215], v[68:71], v[28:31]\nv_mfma_f32_16x16x32_bf16 v[36:39], v[220:223], v[68:71], v[36:39]\nv_mfma_f32_16x16x32_bf16 v[44:47], v[228:231], v[68:71], v[44:47]\n" \
"s_waitcnt vmcnt(2)\nv_mfma_f32_16x16x32_bf16 v[20:23], v[232:235], v[72:75], v[20:23]\ns_waitcnt lgkmcnt(1)\nv_mfma_f32_16x16x32_bf16 v[28:31], v[120:123], v[72:75], v[28:31]\nv_mfma_f32_16x16x32_bf16 v[36:39], v[240:243], v[72:75], v[36:39]\nv_mfma_f32_16x16x32_bf16 v[44:47], v[248:251], v[72:75], v[44:47]\ns_waitcnt vmcnt(1)\nv_mfma_f32_16x16x32_bf16 v[20:23], v[236:239], v[76:79], v[20:23]\n" \
"s_waitcnt vmcnt(0)\ns_waitcnt lgkmcnt(0)\nv_lshlrev_b32 v80, 16, v12\nv_and_b32 v81, 0xffff0000, v12\nv_mfma_f32_16x16x32_bf16 v[28:31], v[124:127], v[76:79], v[28:31]\nv_lshlrev_b32 v82, 16, v13\nv_and_b32 v83, 0xffff0000, v13\nv_mfma_f32_16x16x32_bf16 v[36:39], v[244:247], v[76:79], v[36:39]\n" \
"v_lshlrev_b32 v84, 16, v14\nv_and_b32 v85, 0xffff0000, v14\nv_mfma_f32_16x16x32_bf16 v[44:47], v[252:255], v[76:79], v[44:47]\ns_mul_i32 s44, s40, %[l]\nds_write_b128 v2, v[20:23] offset:1024\nds_write_b128 v2, v[28:31] offset:3072\ns_nop 0\nds_write_b128 v2, v[36:39] offset:5120\n" \
"s_nop 2\nds_write_b128 v2, v[44:47] offset:7168\nv_lshlrev_b32 v86, 16, v15\nv_and_b32 v87, 0xffff0000, v15\nv_mov_b32 v9, s40\ns_cmp_eq_u32 s44, 0\ns_cselect_b32 s56, %[fd], -2147483648\ns_add_u32 s40, s40, 1\n" \
"s_waitcnt lgkmcnt(0)\ns_barrier\nds_read_b128 v[16:19], v3 offset:0\nds_read_b128 v[20:23], v3 offset:256\nds_read_b128 v[24:27], v3 offset:8192\nds_read_b128 v[28:31], v3 offset:8448\nds_read_b128 v[32:35], v3 offset:16384\nds_read_b128 v[36:39], v3 offset:16640\n" \
"ds_read_b128 v[40:43], v3 offset:24576\nds_read_b128 v[44:47], v3 offset:24832\ns_waitcnt lgkmcnt(6)\nv_pk_add_f32 v[16:17], v[16:17], v[80:81]\nv_pk_add_f32 v[18:19], v[18:19], v[82:83]\nv_pk_add_f32 v[20:21], v[20:21], v[84:85]\nv_pk_add_f32 v[22:23], v[22:23], v[86:87]\ns_waitcnt lgkmcnt(4)\n" \
"v_pk_add_f32 v[16:17], v[16:17], v[24:25]\nv_pk_add_f32 v[18:19], v[18:19], v[26:27]\nv_pk_add_f32 v[20:21], v[20:21], v[28:29]\nv_pk_add_f32 v[22:23], v[22:23], v[30:31]\ns_waitcnt lgkmcnt(2)\nv_pk_add_f32 v[16:17], v[16:17], v[32:33]\nv_pk_add_f32 v[18:19], v[18:19], v[34:35]\nv_pk_add_f32 v[20:21], v[20:21], v[36:37]\n" \
"v_pk_add_f32 v[22:23], v[22:23], v[38:39]\ns_waitcnt lgkmcnt(0)\nv_pk_add_f32 v[16:17], v[16:17], v[40:41]\nv_pk_add_f32 v[18:19], v[18:19], v[42:43]\nv_pk_add_f32 v[20:21], v[20:21], v[44:45]\nv_pk_add_f32 v[22:23], v[22:23], v[46:47]\nv_cvt_pk_bf16_f32 v12, v16, v17\nv_cvt_pk_bf16_f32 v13, v18, v19\n" \
"v_cvt_pk_bf16_f32 v14, v20, v21\nv_cvt_pk_bf16_f32 v15, v22, v23\nbuffer_store_dwordx4 v[12:15], v5, %[ro], s42 offen sc1\nv_pk_add_f32 v[24:25], v[16:17], v[18:19]\nv_pk_add_f32 v[26:27], v[20:21], v[22:23]\nv_pk_add_f32 v[24:25], v[24:25], v[26:27]\nv_add_f32 v24, v24, v25\ns_nop 1\n" \
"v_add_f32_dpp v25, v24, v24 row_ror:8 row_mask:0xf bank_mask:0xf\nv_add_f32_dpp v26, v24, v24 row_ror:8 row_mask:0xf bank_mask:0xf\ns_nop 1\nv_permlane16_swap_b32 v25, v26\nv_add_f32 v24, v25, v26\nv_add_f32 v25, v25, v26\ns_nop 1\nv_permlane32_swap_b32 v24, v25\n" \
"v_add_f32 v24, v24, v25\nv_mul_f32 v24, 0x3c800000, v24\nv_pk_add_f32 v[16:17], v[16:17], v[24:25] op_sel_hi:[1,0] neg_lo:[0,1] neg_hi:[0,1]\nv_pk_add_f32 v[18:19], v[18:19], v[24:25] op_sel_hi:[1,0] neg_lo:[0,1] neg_hi:[0,1]\nv_pk_add_f32 v[20:21], v[20:21], v[24:25] op_sel_hi:[1,0] neg_lo:[0,1] neg_hi:[0,1]\nv_pk_add_f32 v[22:23], v[22:23], v[24:25] op_sel_hi:[1,0] neg_lo:[0,1] neg_hi:[0,1]\nv_pk_mul_f32 v[28:29], v[16:17], v[16:17]\nv_pk_mul_f32 v[30:31], v[18:19], v[18:19]\n" \
"v_pk_fma_f32 v[28:29], v[20:21], v[20:21], v[28:29]\nv_pk_fma_f32 v[30:31], v[22:23], v[22:23], v[30:31]\nv_pk_add_f32 v[28:29], v[28:29], v[30:31]\nv_add_f32 v28, v28, v29\ns_nop 1\nv_add_f32_dpp v29, v28, v28 row_ror:8 row_mask:0xf bank_mask:0xf\nv_add_f32_dpp v30, v28, v28 row_ror:8 row_mask:0xf bank_mask:0xf\ns_nop 1\n" \
"v_permlane16_swap_b32 v29, v30\nv_add_f32 v28, v29, v30\nv_add_f32 v29, v29, v30\ns_nop 1\nv_permlane32_swap_b32 v28, v29\nv_add_f32 v28, v28, v29\nv_mov_b32 v25, v28\nbuffer_store_dwordx2 v[24:25], v6, %[rs], s41 offen sc1\n" \
"v_xor_b32 v2, 0x8000, v2\nv_xor_b32 v3, 0x8000, v3\ns_cmp_lt_u32 s40, 64\ns_cbranch_scc1 .Ldd_next%=\ns_waitcnt vmcnt(0)\nbuffer_store_dword v9, off, %[rs], s56 sc1\ns_branch .Ldd_end%=\n.Ldd_slow%=:\n" \
".Ldd_w1%=:\nv_cmp_gt_i32 vcc, s58, v8\ns_cbranch_vccz .Ldd_w1go%=\ns_cmp_ge_u32 s40, 62\ns_cbranch_scc1 .Ldd_ns1219%=\ns_sleep 16\n.Ldd_ns1219%=:\ns_sleep 1\n" \
"buffer_load_dword v8, v7, %[rs], 0 offen sc1\ns_waitcnt vmcnt(0)\ns_add_u32 s43, s43, 1\ns_cmp_lt_u32 s43, 262144\ns_cbranch_scc1 .Ldd_w1%=\ns_branch .Ldd_abort%=\n.Ldd_w1go%=:\ns_add_u32 s60, s55, 0\n" \
"buffer_load_dwordx4 v[96:99], v0, %[rs], s60 offen offset:1024 sc1\nbuffer_load_dwordx4 v[100:103], v0, %[rs], s60 offen offset:3072 sc1\ns_add_u32 s60, s55, 4096\nbuffer_load_dwordx4 v[104:107], v0, %[rs], s60 offen offset:1024 sc1\nbuffer_load_dwordx4 v[108:111], v0, %[rs], s60 offen offset:3072 sc1\ns_add_u32 s60, s55, 16384\nbuffer_load_dwordx4 v[48:51], v0, %[rs], s60 offen offset:1024 sc1\nbuffer_load_dwordx4 v[52:55], v0, %[rs], s60 offen offset:3072 sc1\n" \
"s_add_u32 s60, s55, 20480\nbuffer_load_dwordx4 v[56:59], v0, %[rs], s60 offen offset:1024 sc1\nbuffer_load_dwordx4 v[60:63], v0, %[rs], s60 offen offset:3072 sc1\ns_add_u32 s60, s55, 32768\nbuffer_load_dwordx4 v[64:67], v0, %[rs], s60 offen offset:1024 sc1\nbuffer_load_dwordx4 v[68:71], v0, %[rs], s60 offen offset:3072 sc1\ns_add_u32 s60, s55, 36864\nbuffer_load_dwordx4 v[72:75], v0, %[rs], s60 offen offset:1024 sc1\n" \
"buffer_load_dwordx4 v[76:79], v0, %[rs], s60 offen offset:3072 sc1\ns_add_u32 s60, s55, 49152\nbuffer_load_dwordx4 v[80:83], v0, %[rs], s60 offen offset:1024 sc1\nbuffer_load_dwordx4 v[84:87], v0, %[rs], s60 offen offset:3072 sc1\ns_add_u32 s60, s55, 53248\nbuffer_load_dwordx4 v[88:91], v0, %[rs], s60 offen offset:1024 sc1\nbuffer_load_dwordx4 v[92:95], v0, %[rs], s60 offen offset:3072 sc1\ns_branch .Ldd_slowr%=\n" \
".Ldd_abort%=:\ns_mov_b64 exec, -1\nv_mov_b32 v16, 0\ns_movk_i32 s44, 6400\nbuffer_store_dword v16, off, %[rs], s44 sc1\n.Ldd_end%=:\ns_waitcnt vmcnt(0) lgkmcnt(0)\n" \
""

#define V10(p, x) p #x "0", p #x "1", p #x "2", p #x "3", p #x "4", p #x "5", p #x "6", p #x "7", p #x "8", p #x "9"
#define V256(p) V10(p, ), V10(p, 1), V10(p, 2), V10(p, 3), V10(p, 4), V10(p, 5), V10(p, 6), V10(p, 7), V10(p, 8), V10(p, 9), V10(p, 10), V10(p, 11), V10(p, 12), V10(p, 13), V10(p, 14), V10(p, 15), V10(p, 16), \
    V10(p, 17), V10(p, 18), V10(p, 19), V10(p, 20), V10(p, 21), V10(p, 22), V10(p, 23), V10(p, 24), p "250", p "251", p "252", p "253", p "254", p "255"
#define CLOB "memory", "vcc", "scc", V256("v"), V256("a"), V10("s", 4), V10("s", 5), V10("s", 6), V10("s", 7), V10("s", 8)
template <int N> DEVI void table(lds_char* p, const unsigned (&t)[N]) {
#pragma unroll
    for (int i = 0; i < N; ++i) *(l1*)(p + i * 256) = t[i];
    WL0;
}
DEVI unsigned prof_at(int cu, int w) { return O_PROF + (262144 + cu * 2560 + w * 8) * 4; }
DEVI unsigned sp_idx(int lane) { return (lane & 2) ? 0xEEEEu : 0x4444u; }
DEVI int sp_row(int r, int hf) { return 4 * (r >> 1) + 2 * hf + (r & 1); }

DEVI void qk_role(const Args& a, const int w, lds_char* smem, int l, int h) {
    const int lane = lane_id(), g = lane >> 4, n = lane & 15, b = n & 3, p = n >> 2;
    const unsigned l0 = (unsigned)(size_t)smem, tb = l0 + w * 12288, lw = l0 + 55296 + w * 24576, ls = l * SD, IMG = l0 + 32768, MIM = l0 + 49152;
    const unsigned xw = (w >> 1) * 16384 + (w & 1) * 4096, oc = (h >> 3) * 16384 + ls, tr = (8 * g + p) * 128 + b * 16;
    const unsigned cm[4] = {4 * g > n ? 0xF149F2CAu : 0u, 4 * g + 1 > n ? 0xF149F2CAu : 0u, 4 * g + 2 > n ? 0xF149F2CAu : 0u, 4 * g + 3 > n ? 0xF149F2CAu : 0u};
    const unsigned t[19] = {
        l0 + lane * 16, lw + lane * 16, l0 + (g >> 1) * 1024 + (g & 1) * 512 + n * 16, O_E1 + ls + xw + lane * 16, sp_idx(lane),
        IMG + (w >> 1) * 8192 + ((w & 1) * 8 + 2 * g) * 512 + n * 16,
        MIM + (w * 8 + 2 * p) * 144 + g * 32 + b * 4,
        IMG + g * 512 + (w ? n * 16 : 8192 + (16 * (b >> 1) + 8 * (p >> 1) + 2 * (p & 1) + (b & 1)) * 16),
        MIM + g * 144 + (n & 7) * 16, MIM + 2304 + g * 144 + (2 * p + (b & 1)) * 16,
        w ? O_PT + (h >> 3) * 32768 + ls + lane * 16 + (w == 1 ? 16384 + ((h & 7) >> 1) * 1024 + (h & 1) * 8 : (h & 7) * 1024)
          : O_PM + oc + ((h >> 1) & 3) * 1024 + ((2 * (h & 1) + (g >> 1)) * 16 + sp_row(n & 7, g & 1)) * 16 + (n >> 3) * 8,
        w ? O_PT + (h >> 3) * 32768 + ls + 20480 + ((h >> 2) & 1) * 2048 + (w - 1) * 1024 + (h & 3) * 256 + n * 16 + g * 4
          : n >> 3 ? OOB : O_PM + oc + 4096 + ((h & 7) >> 1) * 256 + sp_row(n & 7, g & 1) * 16 + (h & 1) * 8 + (g >> 1) * 4,
        w == 3 ? (l ? F_X + SD + p * 128 + b * 4 : F_X0 + b * 4) : cm[0], cm[1], cm[2], cm[3],
        (lane >> 3) * 8192u + (lane & 7) * 16, tb + tr, lw + tr};
    table(smem + w * 12288 + lane * 4, t);
    asm volatile(QK_ASM : : [ws] "s"(a.ws), [sz] "n"(WS_RS), [rw] "s"(mkrs(w < 2 ? a.Wq : a.Wk, 8u << 20)), [wo] "s"(h * 256 + (w & 1) * 128), [tb] "s"(tb), [lw] "s"(lw), [l0] "s"(l0), [w] "s"(w),
        [xn] "s"(O_XN + ls), [xw] "s"(xw), [fs] "s"(l ? 0 : 128), [sg] "s"(F_Y0 + 4096 + ls + h * 128 + 16), [pr] "s"(prof_at(l * 80 + h, w)) : CLOB, "s36", "s37", "s38", "s39");
    __builtin_amdgcn_endpgm();
}
DEVI void vu_role(const Args& a, const int w, lds_char* smem, int l, int h) {
    const int lane = lane_id(), g = lane >> 4, n = lane & 15, b = n & 3, p = n >> 2;
    const unsigned l0 = (unsigned)(size_t)smem, tb = l0 + w * 12288, lw = l0 + 61440 + w * 24576, ls = l * SD;
    const unsigned xw = (w >> 1) * 16384 + (w & 1) * 4096, tv = (8 * g + p) * 128 + b * 16 + (w & 1) * 64, to = (4 * g + (p & 1)) * 128 + (p >> 1) * 32 + b * 8;
    const unsigned t[18] = {
        l0 + lane * 16, lw + lane * 16, l0 + (g >> 1) * 1024 + (g & 1) * 512 + n * 16, O_E1 + ls + xw + lane * 16, sp_idx(lane),
        l0 + 57344 + (2 * w + (n >> 3)) * 256 + g * 32 + (n & 7) * 2,
        O_UM + ls + (4 * w + (g & 1)) * 65536 + (h >> 3) * 16384 + (g >> 1) * 2048 + (h & 7) * 256 + n * 16,
        sd(O_Y + h * 131072 + ls) + w * 32768 + (g >> 1) * 1024 + (g & 1) * 256 + n * 16,
        l ? F_X + SD + p * 128 + b * 4 : F_X0 + b * 4, l0 + 49152 + (w >> 1) * 2048 + (2 * (w & 1) + (g >> 1)) * 256 + n * 16 + (g & 1) * 8, sp_idx(lane >> 2), l0 + g * 256 + (n & 7) * 16,
        (lane >> 3) * 8192u + (lane & 7) * 16, (8 * (lane >> 5) + ((lane >> 3) & 3)) * 2048u + (lane & 7) * 16, tb + tv - (w & 1) * 12288, lw + tv - (w & 1) * 24576, tb + to, lw + to};
    table(smem + w * 12288 + lane * 4, t);
    asm volatile(VU_ASM : : [ws] "s"(a.ws), [sz] "n"(WS_RS), [rw] "s"(mkrs(a.Wv, 8u << 20)), [ro] "s"(mkrs(a.Wo, 8u << 20)), [wo] "s"(h * 256 + (w >> 1) * 128 + (w & 1) * 262144), [w2] "s"(h * 262144 + w * 512), [tb] "s"(tb), [lw] "s"(lw), [w] "s"(w),
        [xn] "s"(O_XN + ls), [xw] "s"(xw), [fs] "s"(l ? 0 : 128), [xt] "s"(w * 16384 - xw), [fy] "s"(F_Y0 + ls + h * 128 + w * 4), [sg] "s"(w == 1 ? F_Y0 + ls + h * 128 + 16 : OOB), [pr] "s"(prof_at(l * 80 + 32 + h, w) - 128) : CLOB, "s36", "s37", "s38", "s39");
    __builtin_amdgcn_endpgm();
}
DEVI void rd_role(const Args& a, const int w, lds_char* smem, int l, int c) {
    const int lane = lane_id(), g = lane >> 4, n = lane & 15, r = 2 * (n >> 2) + (n & 1), hf = (n >> 1) & 1;
    const rsrc_t rs = mkrs(a.ws, WS_RS);
    const unsigned l0 = (unsigned)(size_t)smem, tb = l0 + 65536 + w * 4096, ls = l * SD, vp = O_E1 + ls + (c >> 3) * 16384 + (c & 7) * 1024 + w * 256 + n * 16;
    {
        const v4u ff = {SENT, SENT, SENT, SENT};
#pragma unroll
        for (int i = 0; i < 8; ++i) st16(ff, rs, O_UM + ls + c * 65536 + w * 16384 + i * 1024 + lane * 16);
#pragma unroll
        for (int i = 0; i < 2; ++i) st16(ff, rs, O_PT + ls + c * 16384 + w * 2048 + i * 1024 + lane * 16);
        st16(ff, rs, c < 8 ? O_PM + ls + w * 16384 + c * 1024 + lane * 16 : OOB);
        st16(ff, rs, g ? vp + g * 32768 : OOB);
        WV0; BAR;
    }
    st4(g & 1 ? *(const unsigned*)(a.mem0 + r * 1024 + c * 64 + w * 16 + 4 * g + 2 * hf) : nosent(*(const unsigned*)(a.mem0 + r * 1024 + c * 64 + w * 16 + 4 * g + 2 * hf)), rs, g & 1 ? vp + g * 4 : OOB, 0);
    WV0;
    st4(nosent(*(const unsigned*)(a.mem0 + r * 1024 + c * 64 + w * 16 + 4 * g + 2 * hf)), rs, g & 1 ? OOB : vp + g * 4, 0);
    const int gi = lane >> 3, r8 = lane & 7;
    const unsigned t[12] = {
        (unsigned)lane * 16, sp_idx(lane), l0 + w * 4096 + g * 576 + ((n >> 3) * 16 + hf * 4 + 2 * ((n >> 2) & 1) + (n & 1)) * 4,
        l0 + (r >> 1) * 576 + w * 128 + (g >> 1) * 64 + (r & 1) * 32 + hf * 16,
        (vp ^ ((g & 1) * 65536)) + (g >> 1) * 8, g & 1 ? SENT : 0u,
        l0 + 16384 + w * 8192 + lane * 16, l0 + 16384 + ((gi >> 1) * 2 + (w >> 1)) * 1024 + ((gi & 1) * 32 + (w & 1) * 8 + r8) * 16,
        (c >> 1) * 16384u + (c & 1) * 4096 + gi * 512 + (w * 8 + r8) * 16,
        gi ? OOB : O_ST - O_XR + c * 256 + ((w & 1) * 8 + r8) * 16 + (w >> 1) * 8,
        F_Y0 + ls + (w * 8 + r8) * 128 + (c >> 2) * 4, F_Y0 + ls + (gi & 1) * 4096 + (w * 8 + r8) * 128 + 16};
    table(smem + 65536 + w * 4096 + lane * 4, t);
    asm volatile(RD_ASM : : [rs] "s"(rs), [tb] "s"(tb), [su] "s"(O_UM + ls + c * 65536 + w * 16384), [sp] "s"(O_PM + ls + w * 16384), [st] "s"(O_PT + ls + w * 32768),
        [sy] "s"(sd(O_Y + w * (1u << 20) + ls) + (c >> 1) * 16384 + (c & 1) * 4096), [at] "s"(O_XR + ls), [du] "s"((c & 2) * 5 / 2), [fa] "s"(F_A + ls + c * 128 + w * 4), [pr] "s"(prof_at(l * 80 + 64 + c, w) + 128) : CLOB);
    __builtin_amdgcn_endpgm();
}
DEVI void gg_role(const Args& a, const int w, lds_char* smem, int l, int gi) {
    const int lane = lane_id(), g = lane >> 4, n = lane & 15, b = n & 3, p = n >> 2;
    const unsigned l0 = (unsigned)(size_t)smem, tb = l0 + w * 12288, lw = l0 + 65536 + w * 24576, ls = l * SD, tv = (8 * g + p) * 128 + b * 16 + (w & 1) * 64, fh = (g >> 1) * 1024 + (g & 1) * 512 + n * 16;
    const unsigned t[9] = {l0 + lane * 16, lw + lane * 16, l0 + fh, F_A + ls + (lane >> 2) * 128 + (lane & 3) * 4, w * 2048u + lane * 16, O_ST - O_XR + g * 1024 + n * 16, (lane >> 3) * 7168u + (lane & 7) * 16, tb + tv - (w & 1) * 12288, lw + tv - (w & 1) * 24576};
    table(smem + w * 12288 + lane * 4, t);
    asm volatile(GG_ASM : : [rs] "s"(mkrs(a.ws, WS_RS)), [rw] "s"(mkrs(w & 1 ? a.Wu : a.Wg, 3584 * 2048)), [wo] "s"(gi * 256 + (w >> 1) * 128), [tb] "s"(tb), [lw] "s"(lw), [w] "s"(w),
        [xn] "s"(O_XR + ls), [xw] "s"((w >> 1) * 16384 + (w & 1) * 4096), [ho] "s"(O_H + ls + gi * 16384), [fh] "s"(F_H + ls + (gi / 7) * 1024 + (gi % 7) * 128 + w * 4), [pr] "s"(prof_at(160 + l * 28 + gi, w) + 128) : CLOB);
    __builtin_amdgcn_endpgm();
}
DEVI void dd_role(const Args& a, const int w, lds_char* smem, int l, int c) {
    const int lane = lane_id(), g = lane >> 4, b = lane & 3, p = (lane >> 2) & 3, gi = lane >> 3, r8 = lane & 7;
    const unsigned l0 = (unsigned)(size_t)smem, tb = l0 + w * 12288, lw = l0 + 65536 + w * 24576, ls = l * SD, tr = (8 * g + p) * 128 + b * 8;
    const unsigned ta = (c >> 1) * 16384u + (c & 1) * 4096 + gi * 512 + (w * 8 + r8) * 16;
    const rsrc_t rs = mkrs(a.ws, WS_RS);
    const unsigned t[11] = {(unsigned)lane * 16, lw + lane * 16, l0 + w * 8192 + lane * 16, l0 + ((gi >> 1) * 2 + (w >> 1)) * 1024 + ((gi & 1) * 32 + (w & 1) * 8 + r8) * 16,
        ta, l ? (w * 8 + r8) * 2048 + c * 128 + gi * 16 : ta, gi || l ? OOB : O_ST - O_XR + 4096 + c * 256 + (w * 8 + r8) * 8,
        F_H + ls + w * 1024 + ((lane >> 2) % 7) * 128 + b * 4, gi * 2048u + r8 * 16, tb + tr, lw + tr};
    table(smem + w * 12288 + lane * 4, t);
    asm volatile(DD_ASM : : [rs] "s"(rs), [rw] "s"(mkrs(a.Wd, 3584 * 2048)), [ro] "s"(mkrs(l ? (char*)a.out : a.ws, l ? 2048 * 2048 : WS_RS)), [wo] "s"(w * (896 * 2048) + c * 128), [tb] "s"(tb), [lw] "s"(lw), [l] "s"(l),
        [xn] "s"(O_XR + ls), [ho] "s"(O_H + ls + w * 7 * 16384), [fd] "s"(F_D + ls + c * 128 + w * 4), [pr] "s"(prof_at(216 + l * 16 + c, w) + 128) : CLOB);
    __builtin_amdgcn_endpgm();
}

#define CPF 74
#define CPJ 0
#define LNW 0
#define DLQ 0
#define DLS 400
#define SPL 0
#define PLAN 3
#define PFA 0
#define PFB 2
#define PFL 1
#define VUX 1
#define LNS 40
#define LN1 700
#define DLG 1100
#define DLD 1500
#define DL1 1500
#define DLG1 2600
#define DLD1 3000
DEVI Args args(unsigned sh) {
    auto p = (const __attribute__((address_space(4))) Args*)__builtin_amdgcn_kernarg_segment_ptr();
    asm volatile("" : "+s"(p));
    Args a;
    a.x = p->x; a.mem0 = p->mem0; a.Wq = p->Wq; a.Wk = p->Wk; a.Wv = p->Wv; a.Wo = p->Wo; a.Wg = p->Wg; a.Wu = p->Wu; a.Wd = p->Wd; a.ws = p->ws + sh; a.out = p->out;
    return a;
}
DEVI unsigned beat_wait(unsigned x0) {
    const rsrc_t r0 = mkrs(args(0).ws, 2u << 20);
    for (unsigned spins = 0; spins < SPIN_CAP; ++spins) {
        CB;
        const v2u t = __builtin_amdgcn_raw_buffer_load_b64(r0, 0, O_HB, AUX_SC1);
        if (RFL(t[0]) != x0) return RFL(t[1]) & (2u << 20);
        __builtin_amdgcn_s_sleep(8);
    }
    __builtin_amdgcn_endpgm();
}
DEVI void leader(const Args& a, const int w, lds_char* smem, unsigned x0) {
    const int lane = lane_id();
    const rsrc_t r0 = mkrs(a.ws, 4u << 20);
    unsigned t[2] = {~0u, ~0u};
    for (int i = 0; i < 2 * NPRB; ++i) {
        const unsigned t0 = (unsigned)__builtin_amdgcn_s_memtime();
        WL0;
        if (lane == 0) st4(0u, r0, 0, O_PRB + (i & 1) * SD + w * 1024 + (i >> 1) * 128);
        WV0;
        const unsigned d = (unsigned)__builtin_amdgcn_s_memtime() - t0;
        if (i & 1) t[1] = min(t[1], d); else t[0] = min(t[0], d);
    }
    *(l2*)(smem + w * 8) = v2u{t[0], t[1]};
    WL0; BAR;
    const v4u p = *(l4*)smem, q = *(l4*)(smem + 16);
    const unsigned sh = RFL(min(min(p[0], p[2]), min(q[0], q[2])) < min(min(p[1], p[3]), min(q[1], q[3])) ? 2u << 20 : 0u);
    const unsigned o = sh + w * 1024 + lane * 16;
    const v4u ff = {SENT, SENT, SENT, SENT};
#pragma unroll
    for (int i = 0; i < 8; ++i) st16(ff, r0, o + O_E1 + i * 4096);
#pragma unroll
    for (int i = 1; i < 16; ++i) if (i != 2 && i != 11) st16(ff, r0, o + i * 4096);
    WV0; BAR;
    if (w) return;
    for (unsigned spins = 0; spins < SPIN_CAP; ++spins) {
        if (lane == 0) st8(v2u{++x0, sh}, r0, 0, O_HB);
        if (__ballot((int)ld4(r0, sh + F_D + SD + (lane >> 2) * 128 + (lane & 3) * 4, 0) < 0) == 0ull) break;
        __builtin_amdgcn_s_sleep(16);
    }
}
__global__ __launch_bounds__(256, 1) void sys_kernel(Args) {
    extern __shared__ __attribute__((aligned(16))) char smem_[];
    lds_char* smem = (lds_char*)smem_;
    const int x = blockIdx.x & 7, j = blockIdx.x >> 3, wv = RFL(threadIdx.x >> 6), l = x >> 2, xx = x & 3;
    const unsigned t0 = (unsigned)__builtin_amdgcn_s_memrealtime();
    auto until = [&](unsigned dl) AI { while ((unsigned)__builtin_amdgcn_s_memrealtime() - t0 < dl) __builtin_amdgcn_s_sleep(8); };
    unsigned x0 = 0;
    if (l || j >= 16) x0 = RFL(ld4(mkrs(args(0).ws, 2u << 20), 0, O_HB));
    if (j < 16) {
        if (l) { until(j ? LN1 + j * LNS : 0); ln0(args(0), wv, j * 4 + xx, [&]() AI { return beat_wait(x0); }); until(DL1); }
        if (DLQ && !l && j < 8) until(DLQ);
        if (j < 8) qk_role(args(0), wv, smem, l, xx * 8 + j); else vu_role(args(0), wv, smem, l, (xx ^ VUX) * 8 + j - 8);
        return;
    }
    if (j >= 16 + CPJ && j < 20 + CPJ) {
        unsigned long long pc; unsigned n;
        asm volatile("s_getpc_b64 %0\n\ts_mov_b32 %1, .-_Z10sys_kernel4Args" : "=s"(pc), "=s"(n));
        const rsrc_t rc = mkrs((const void*)(pc - n), CPF * 1024);
        __builtin_amdgcn_raw_ptr_buffer_load_lds(rc, smem + LDSB - 1024 + wv * 256, 4, lane_id() * 128, ((j - 16 - CPJ) * 4 + wv) * 8192, 0, 0);
    }
    {
        const int lane = lane_id(), q = (j - 16) * 4 + wv, m = SPL && !l ? ((xx & 1) != (SPL & 1)) * 2 + (q >> 5) : q >> 4;
        const Args a = args(0);
        const u16 *W0 = a.Wq, *W1 = a.Wk, *W2 = a.Wv, *W3 = a.Wo;
        asm volatile("" : "+s"(W0), "+s"(W1), "+s"(W2), "+s"(W3));
        const u16* W = m == 0 ? W0 : m == 1 ? W1 : m == 2 ? W2 : W3;
        const rsrc_t rw = mkrs(W, 8u << 20);
        if (l) {
#pragma unroll
            for (int i = 0; i < PFA; ++i) __builtin_amdgcn_raw_ptr_buffer_load_lds(rw, smem + LDSB - 1024 + wv * 256, 4, lane * 256, i * (1u << 20) + (xx * 16 + (q & 15)) * 16384, 0, 0);
        } else {
            const unsigned po = (0x100u - (unsigned)(size_t)W) & 0x300u, hx = SPL ? (xx & 2) + ((q >> 4) & 1) : m < 2 ? xx : xx ^ VUX;
            const unsigned vo = m == 3 ? (lane >> 1) * 1024 + (lane & 1) * 128 : (lane >> 2) * 8192 + ((lane >> 1) & 1) * 1024 + (lane & 1) * 128;
            const unsigned so = po + (m == 3 ? hx * (2u << 20) + (q & 15) * 32768 : hx * 2048 + (q & 15) * 131072);
#pragma unroll
            for (int i = 0; i < PFB; ++i) __builtin_amdgcn_raw_ptr_buffer_load_lds(rw, smem + LDSB - 1024 + wv * 256, 4, vo, so + i * (m == 3 ? 524288 : 2u << 20), 0, 0);
            const unsigned pl = (0x1F00u - (unsigned)(size_t)W) & 0x1F00u;
#pragma unroll
            for (int i = 0; i < PFL; ++i) __builtin_amdgcn_raw_ptr_buffer_load_lds(rw, smem + LDSB - 1024 + wv * 256, 4, (lane >> 1) * 8192 + (lane & 1) * 128, (pl >> 11) == hx && m < 3 ? pl + ((q & 15) + 16 * i) * 262144 : OOB, 0, 0);
        }
    }
    int ro, ix;
    if ((xx & 1) == (PLAN & 1)) { if (j < 24) { ro = 0; ix = (xx >> 1) * 8 + j - 16; } else if (j < 26) { ro = 3; ix = (xx >> 1) * 2 + j - 24; } else { ro = 1; ix = (xx >> 1) * 14 + j - 26; } }
    else if (j < 24) { ro = 1; ix = (xx >> 1) * 14 + j - 10; } else { ro = 2; ix = (xx >> 1) * 8 + j - 24; }
    if (ro == 3 && l) { if (ix == 0) leader(args(0), wv, smem, x0); return; }
    const unsigned sh = beat_wait(x0);
    if (ro == 0) { rd_role(args(sh), wv, smem, l, ix); return; }
    if (ro == 1) { const Args a = args(sh); until(l ? DLG1 : ((((unsigned)(size_t)a.Wg >> 8) + ix) & 3) == 1 || ((((unsigned)(size_t)a.Wu >> 8) + ix) & 3) == 1 ? DLG - DLS : DLG); WV0; BAR; gg_role(a, wv, smem, l, ix); return; }
    if (ro == 2) { const Args a = args(sh); until((l ? DLD1 : DLD) - (((((unsigned)(size_t)a.Wd >> 8) + (ix >> 1)) & 3) == 1 ? DLS + 200 : 0)); WV0; BAR; dd_role(a, wv, smem, l, ix); return; }
    r_role(args(sh), wv, ix);
}

extern "C" void kernel_launch(void* const* d_in, const int* in_sizes, int n_in, void* d_out, int out_size, void* d_ws, size_t ws_size, hipStream_t stream) {
    (void)in_sizes; (void)n_in; (void)out_size; (void)ws_size;
    Args a;
    a.x = (const u16*)d_in[0]; a.mem0 = (const u16*)d_in[1]; a.Wq = (const u16*)d_in[2]; a.Wk = (const u16*)d_in[3]; a.Wv = (const u16*)d_in[4]; a.Wo = (const u16*)d_in[5];
    a.Wg = (const u16*)d_in[8]; a.Wu = (const u16*)d_in[9]; a.Wd = (const u16*)d_in[10];
    a.ws = (char*)(((uintptr_t)d_ws + (2u << 20) - 1) & ~(uintptr_t)((2u << 20) - 1)); a.out = (u16*)d_out;
    (void)hipFuncSetAttribute((const void*)sys_kernel, hipFuncAttributeMaxDynamicSharedMemorySize, LDSB);
    hipLaunchKernelGGL(sys_kernel, dim3(256), dim3(256), LDSB, stream, a);
}
```

```cpp
#include <hip/hip_runtime.h>
#include <stdint.h>

typedef unsigned short u16;
typedef unsigned long long u64;
typedef __attribute__((ext_vector_type(8))) short bf16x8;
typedef __attribute__((ext_vector_type(4))) short s16x4;
typedef __attribute__((ext_vector_type(4))) float f32x4;
typedef __attribute__((ext_vector_type(16))) float f32x16;
typedef __attribute__((ext_vector_type(2))) float f32x2;
typedef __attribute__((ext_vector_type(4))) unsigned v4u;
typedef __attribute__((ext_vector_type(2))) unsigned v2u;
typedef __amdgpu_buffer_rsrc_t rsrc_t;
typedef __attribute__((address_space(3))) char lds_char;
typedef __attribute__((address_space(3))) bf16x8 l8;
typedef __attribute__((address_space(3))) v4u l4;
typedef __attribute__((address_space(3))) v2u l2;
typedef __attribute__((address_space(3))) unsigned l1;
typedef __attribute__((address_space(3))) f32x4 lf4;
typedef __attribute__((address_space(3))) float lf1;
#define DEVI __device__ __forceinline__
#define AI __attribute__((always_inline))

constexpr int NSEG = 64;
constexpr float ATT_SCALE = 0.08838834764831845f;
constexpr float LOG2E = 1.4426950408889634f;
#define NPRB 4
constexpr unsigned SD = 8192;
DEVI unsigned sd(unsigned o) { return o ^ ((o >> 8) & SD); }
constexpr unsigned F_X0 = 32768;
constexpr unsigned F_X = 4096;
constexpr unsigned F_A = 16384;
constexpr unsigned F_H = F_A + 2048;
constexpr unsigned F_D = F_H + 4096;
constexpr unsigned F_Y0 = 49152;
constexpr unsigned F_ABORT = 6400;
constexpr unsigned O_PM = 65536;
constexpr unsigned O_E1 = 131072;
constexpr unsigned O_PT = 262144;
constexpr unsigned O_UM = 524288;
constexpr unsigned CTRL_END = O_UM + (1u << 20);
constexpr unsigned O_PRB = 0x1F0000, O_HB = 0x1FFF00;
constexpr unsigned O_XN = 4u << 20;
constexpr unsigned O_XR = O_XN + (8u << 20);
constexpr unsigned O_ST = O_XR + (8u << 20);
constexpr unsigned O_H = O_ST + (8u << 20);
constexpr unsigned O_Y = O_H + (36u << 20);
constexpr unsigned WS_NEED = O_Y + (8u << 20);
constexpr unsigned O_PROF = WS_NEED;
constexpr unsigned WS_RS = WS_NEED;
static_assert(CTRL_END <= O_PRB && CTRL_END % 16384 == 0 && O_Y % (8u << 20) == 0, "workspace");
constexpr unsigned OOB = 0x80000000u, OOB2 = 0x90000000u, SENT = 0xFFFFFFFFu;

struct Args { const u16 *x, *mem0, *Wq, *Wk, *Wv, *Wo, *Wg, *Wu, *Wd; char* ws; u16* out; };

typedef __attribute__((ext_vector_type(2))) __bf16 bf16x2_t;
DEVI unsigned pack2(float lo, float hi) { f32x2 v = {lo, hi}; bf16x2_t b = __builtin_convertvector(v, bf16x2_t); return __builtin_bit_cast(unsigned, b); }
DEVI float bflo(unsigned u) { return __uint_as_float(u << 16); }
DEVI float bfhi(unsigned u) { return __uint_as_float(u & 0xffff0000u); }
DEVI float rbf(float x) { return bflo(pack2(x, x)); }
DEVI bf16x8 as8(v4u v) { return __builtin_bit_cast(bf16x8, v); }
DEVI f32x4 mfma16(bf16x8 a, bf16x8 b, f32x4 c) { return __builtin_amdgcn_mfma_f32_16x16x32_bf16(a, b, c, 0, 0, 0); }
DEVI f32x16 mfma32(bf16x8 a, bf16x8 b, f32x16 c) { return __builtin_amdgcn_mfma_f32_32x32x16_bf16(a, b, c, 0, 0, 0); }
constexpr int AUX_SC1 = 16;
DEVI v4u ld16(rsrc_t r, unsigned vo, unsigned so) { return __builtin_amdgcn_raw_buffer_load_b128(r, vo, so, AUX_SC1); }
DEVI unsigned ld4(rsrc_t r, unsigned vo, unsigned so) { return __builtin_amdgcn_raw_buffer_load_b32(r, vo, so, AUX_SC1); }
DEVI void st16(v4u v, rsrc_t r, unsigned vo) { __builtin_amdgcn_raw_buffer_store_b128(v, r, vo, 0, AUX_SC1); }
DEVI void st8(v2u v, rsrc_t r, unsigned vo, unsigned so) { __builtin_amdgcn_raw_buffer_store_b64(v, r, vo, so, AUX_SC1); }
DEVI void st4(unsigned v, rsrc_t r, unsigned vo, unsigned so) { __builtin_amdgcn_raw_buffer_store_b32(v, r, vo, so, AUX_SC1); }
DEVI v2u trd(lds_char* p) { return __builtin_bit_cast(v2u, __builtin_amdgcn_ds_read_tr16_b64_v4i16((__attribute__((address_space(3))) s16x4*)p)); }
template <int O1, int O2> DEVI void tra(v2u& lo, v2u& hi, unsigned a) { asm volatile("ds_read_b64_tr_b16 %0, %2 offset:%3\n\tds_read_b64_tr_b16 %1, %2 offset:%4" : "=&v"(lo), "=&v"(hi) : "v"(a), "n"(O1), "n"(O2) : "memory"); }
DEVI void trw(v2u& a, v2u& b, v2u& c, v2u& d) { asm volatile("s_waitcnt lgkmcnt(0)" : "+v"(a), "+v"(b), "+v"(c), "+v"(d)::"memory"); }
DEVI rsrc_t mkrs(const void* p, unsigned n) { return __builtin_amdgcn_make_buffer_rsrc((void*)p, 0, (int)n, 0x00020000); }
#define CB asm volatile("" ::: "memory")
#define SB __builtin_amdgcn_sched_barrier(0)
#define WL0 asm volatile("s_waitcnt lgkmcnt(0)" ::: "memory")
#define WV0 asm volatile("s_waitcnt vmcnt(0)" ::: "memory")
#define WV(n) asm volatile("s_waitcnt vmcnt(%0)" ::"n"(n) : "memory")
#define BAR do { __builtin_amdgcn_s_barrier(); CB; } while (0)
#define RFL(x) __builtin_amdgcn_readfirstlane(x)
DEVI int lane_id() { int x; asm volatile("v_mbcnt_lo_u32_b32 %0, -1, 0\n\tv_mbcnt_hi_u32_b32 %0, -1, %0" : "=v"(x)); return x; }
template <int I> struct IC { static constexpr int v = I; };
template <int B, int E_, class F> DEVI void sfor(F&& f) { if constexpr (B < E_) { f(IC<B>{}); sfor<B + 1, E_>(f); } }
DEVI float red_x32(float x, bool mx) { unsigned u = __float_as_uint(x); auto r = __builtin_amdgcn_permlane32_swap(u, u, false, false); float a = __uint_as_float(r[0]), b = __uint_as_float(r[1]); return mx ? fmaxf(a, b) : a + b; }
DEVI float red_x16(float x, bool mx) { unsigned u = __float_as_uint(x); auto r = __builtin_amdgcn_permlane16_swap(u, u, false, false); float a = __uint_as_float(r[0]), b = __uint_as_float(r[1]); return mx ? fmaxf(a, b) : a + b; }
DEVI unsigned vmax4(v4u d) { return max(max(d[0], d[1]), max(d[2], d[3])); }
DEVI unsigned nosent(unsigned v) { return min(v, SENT - 1u); }
constexpr unsigned SPIN_CAP = 1u << 18;
DEVI bool spin_bail(unsigned& spins, rsrc_t rs) {
    if (++spins > SPIN_CAP) { st4(0u, rs, 0, F_ABORT); return true; }
    return (spins & 255u) == 0u && RFL(ld4(rs, 0, F_ABORT)) != SENT;
}
#define TSTAMP(idx)
constexpr int LDSB = 160 * 1024;
DEVI void idle(unsigned dl) { const unsigned t0 = (unsigned)__builtin_amdgcn_s_memrealtime(); while ((unsigned)__builtin_amdgcn_s_memrealtime() - t0 < dl) __builtin_amdgcn_s_sleep(8); }
#define D1F 0
#define X1F 0

DEVI float red_rows(float s) {
    s += __uint_as_float(__builtin_amdgcn_update_dpp(0, __float_as_uint(s), 0x128, 0xf, 0xf, false));
    s = red_x16(s, false); return red_x32(s, false);
}
DEVI f32x2 up2(unsigned u) { return f32x2{bflo(u), bfhi(u)}; }
DEVI float ln_inv(float var) { return __builtin_amdgcn_rcpf(rbf(__builtin_amdgcn_sqrtf(rbf(rbf(fmaxf(var, 0.f)) + 1e-5f)))); }
template <class F> DEVI void ln0(const Args& a, const int w, int b, F&& base) {
    const int lane = lane_id(), row = w * 8 + (lane & 7);
    const rsrc_t rx = mkrs(a.x, 2048 * 2048);
    v4u u[16];
#pragma unroll
    for (int i = 0; i < 16; ++i) u[i] = __builtin_amdgcn_raw_buffer_load_b128(rx, row * 2048 + (lane >> 3) * 16 + i * 128, b * 65536, 0);
    const float k0 = __shfl(bflo(u[0][0]), lane & 7);
    f32x2 s1 = {0.f, 0.f}, s2 = {0.f, 0.f};
#pragma unroll
    for (int i = 0; i < 16; ++i)
#pragma unroll
        for (int e = 0; e < 4; ++e) { const f32x2 d = up2(u[i][e]) - k0; s1 += d; s2 += d * d; }
    const float s = red_rows(s1[0] + s1[1]) * (1.0f / 1024.0f), q = red_rows(s2[0] + s2[1]) * (1.0f / 1024.0f);
    float inv = ln_inv(q - s * s), c = -rbf(k0 + s) * inv;
    asm volatile("" : "+v"(inv), "+v"(c));
    const rsrc_t rs = mkrs(a.ws + base(), WS_RS);
    const unsigned o = sd(O_XN + b * 131072) + (lane >> 3) * 512 + row * 16;
#pragma unroll
    for (int i = 0; i < 16; ++i) {
        v4u r;
#pragma unroll
        for (int e = 0; e < 4; ++e) { const f32x2 z = up2(u[i][e]) * inv + c; r[e] = pack2(z[0], z[1]); }
        st16(r, rs, o + (i >> 1) * 16384 + (i & 1) * 4096);
    }
#pragma unroll
    for (int i = 0; i < 16; ++i) st16(u[i], rs, o + (O_XR - O_XN) + (i >> 1) * 16384 + (i & 1) * 4096);
    st16(u[0], rs, sd(O_ST + b * 131072) + w * 1024 + lane * 16); st16(u[0], rs, sd(O_ST + b * 131072) + 4096 + w * 1024 + lane * 16); st16(u[0], rs, sd(O_ST + SD + b * 131072) + w * 1024 + lane * 16);
    WV0;
    st4(b, rs, 0, F_X0 + b * 128 + w * 4);
}
#define RSLP 8
DEVI void r_role(const Args& a, const int w, int rq) {
    const int lane = lane_id(), row = (w >> 1) * 16 + (rq & 1) * 8 + (lane & 7), cq = (rq >> 1) * 2 + (w & 1);
    const rsrc_t rs = mkrs(a.ws, WS_RS);
    const unsigned fo = cq * 32768 + (lane >> 3) * 512 + row * 16, so = O_ST - O_XR + 4096 + (lane >> 3) * 512 + row * 8;
    auto io = [](int i) -> unsigned { return (i >> 1) * 16384 + (i & 1) * 4096; };
    const unsigned fd = F_D + (lane & 15) * 128 + (D1F ? 0 : (row >> 3) * 4);
    for (int b = 0; b < NSEG; ++b) {
        unsigned spins = 0;
        for (;;) {
            CB;
            if (__ballot((int)ld4(rs, fd, 0) < b) == 0ull) break;
            if (spin_bail(spins, rs)) return;
            __builtin_amdgcn_s_sleep(RSLP);
        }
        const unsigned sx = sd(O_XR + b * 131072);
        const v2u s0 = __builtin_amdgcn_raw_buffer_load_b64(rs, so, sx, AUX_SC1), s1 = __builtin_amdgcn_raw_buffer_load_b64(rs, so + 256, sx, AUX_SC1);
        v4u u[4];
#pragma unroll
        for (int i = 0; i < 4; ++i) u[i] = ld16(rs, fo + io(i), sx);
        const float m0 = __uint_as_float(s0[0]), m1 = __uint_as_float(s1[0]), mean = red_rows(m0 + m1) * 0.0625f;
        const float var = red_rows(__uint_as_float(s0[1]) + __uint_as_float(s1[1]) + 64.f * ((m0 - mean) * (m0 - mean) + (m1 - mean) * (m1 - mean))) * (1.0f / 1024.0f);
        const float inv = ln_inv(var), c = -rbf(mean) * inv;
#pragma unroll
        for (int i = 0; i < 4; ++i) {
            v4u o;
#pragma unroll
            for (int e = 0; e < 4; ++e) { const f32x2 z = up2(u[i][e]) * inv + c; o[e] = pack2(z[0], z[1]); }
            st16(o, rs, fo + io(i) + ((sx ^ SD) - (O_XR - O_XN)));
        }
#pragma unroll
        for (int i = 0; i < 4; ++i) st16(u[i], rs, fo + io(i) + (sx ^ SD));
        WV0;
        if (X1F) BAR;
        st4(b, rs, 0, X1F && w ? OOB : F_X + SD + rq * 128 + w * 4);
        if (rq == 0 && w == 0) TSTAMP(200000 + 64 + b);
    }
}
#define QK_ASM \
"v_mbcnt_lo_u32_b32 v255, -1, 0\nv_mbcnt_hi_u32_b32 v255, -1, v255\nv_lshlrev_b32 v255, 2, v255\nv_add_u32 v255, %[tb], v255\nds_read_b32 v0, v255 offset:0\nds_read_b32 v1, v255 offset:256\nds_read_b32 v2, v255 offset:512\nds_read_b32 v3, v255 offset:768\n" \
"ds_read_b32 v4, v255 offset:1024\nds_read_b32 v5, v255 offset:1280\nds_read_b32 v6, v255 offset:1536\nds_read_b32 v7, v255 offset:1792\nds_read_b32 v8, v255 offset:2048\nds_read_b32 v9, v255 offset:2304\nds_read_b32 v10, v255 offset:2560\nds_read_b32 v11, v255 offset:2816\n" \
"ds_read_b32 v12, v255 offset:3072\nds_read_b32 v13, v255 offset:3328\nds_read_b32 v14, v255 offset:3584\nds_read_b32 v15, v255 offset:3840\nds_read_b32 v92, v255 offset:4096\nds_read_b32 v93, v255 offset:4352\nds_read_b32 v94, v255 offset:4608\ns_waitcnt lgkmcnt(0)\n" \
"s_mov_b64 s[36:37], %[ws]\ns_mov_b32 s38, %[sz]\ns_mov_b32 s39, 0x20000\ns_mov_b32 s45, 2096896\nbuffer_load_dword v95, off, s[36:39], s45 sc1\ns_add_u32 m0, %[lw], 16384\ns_add_u32 s44, %[wo], 0\nbuffer_load_dwordx4 v92, %[rw], s44 offen nt lds\n" \
"s_add_u32 m0, %[lw], 17408\ns_add_u32 s44, %[wo], 65536\nbuffer_load_dwordx4 v92, %[rw], s44 offen nt lds\ns_add_u32 m0, %[lw], 18432\ns_add_u32 s44, %[wo], 131072\nbuffer_load_dwordx4 v92, %[rw], s44 offen nt lds\ns_add_u32 m0, %[lw], 19456\ns_add_u32 s44, %[wo], 196608\n" \
"buffer_load_dwordx4 v92, %[rw], s44 offen nt lds\ns_add_u32 m0, %[lw], 20480\ns_add_u32 s44, %[wo], 262144\nbuffer_load_dwordx4 v92, %[rw], s44 offen nt lds\ns_add_u32 m0, %[lw], 21504\ns_add_u32 s44, %[wo], 327680\nbuffer_load_dwordx4 v92, %[rw], s44 offen nt lds\ns_add_u32 m0, %[lw], 22528\n" \
"s_add_u32 s44, %[wo], 393216\nbuffer_load_dwordx4 v92, %[rw], s44 offen nt lds\ns_add_u32 m0, %[lw], 23552\ns_add_u32 s44, %[wo], 458752\nbuffer_load_dwordx4 v92, %[rw], s44 offen nt lds\ns_add_u32 m0, %[tb], 0\ns_add_u32 s44, %[wo], 524288\nbuffer_load_dwordx4 v92, %[rw], s44 offen nt lds\n" \
"s_add_u32 m0, %[tb], 1024\ns_add_u32 s44, %[wo], 589824\nbuffer_load_dwordx4 v92, %[rw], s44 offen nt lds\ns_add_u32 m0, %[tb], 2048\ns_add_u32 s44, %[wo], 655360\nbuffer_load_dwordx4 v92, %[rw], s44 offen nt lds\ns_add_u32 m0, %[tb], 3072\ns_add_u32 s44, %[wo], 720896\n" \
"buffer_load_dwordx4 v92, %[rw], s44 offen nt lds\ns_add_u32 m0, %[tb], 4096\ns_add_u32 s44, %[wo], 786432\nbuffer_load_dwordx4 v92, %[rw], s44 offen nt lds\ns_add_u32 m0, %[tb], 5120\ns_add_u32 s44, %[wo], 851968\nbuffer_load_dwordx4 v92, %[rw], s44 offen nt lds\ns_add_u32 m0, %[tb], 6144\n" \
"s_add_u32 s44, %[wo], 917504\nbuffer_load_dwordx4 v92, %[rw], s44 offen nt lds\ns_add_u32 m0, %[tb], 7168\ns_add_u32 s44, %[wo], 983040\nbuffer_load_dwordx4 v92, %[rw], s44 offen nt lds\ns_add_u32 m0, %[tb], 8192\ns_add_u32 s44, %[wo], 1048576\nbuffer_load_dwordx4 v92, %[rw], s44 offen nt lds\n" \
"s_add_u32 m0, %[tb], 9216\ns_add_u32 s44, %[wo], 1114112\nbuffer_load_dwordx4 v92, %[rw], s44 offen nt lds\ns_add_u32 m0, %[tb], 10240\ns_add_u32 s44, %[wo], 1179648\nbuffer_load_dwordx4 v92, %[rw], s44 offen nt lds\ns_add_u32 m0, %[tb], 11264\ns_add_u32 s44, %[wo], 1245184\n" \
"buffer_load_dwordx4 v92, %[rw], s44 offen nt lds\ns_add_u32 m0, %[lw], 0\ns_add_u32 s44, %[wo], 1310720\nbuffer_load_dwordx4 v92, %[rw], s44 offen nt lds\ns_add_u32 m0, %[lw], 1024\ns_add_u32 s44, %[wo], 1376256\nbuffer_load_dwordx4 v92, %[rw], s44 offen nt lds\ns_add_u32 m0, %[lw], 2048\n" \
"s_add_u32 s44, %[wo], 1441792\nbuffer_load_dwordx4 v92, %[rw], s44 offen nt lds\ns_add_u32 m0, %[lw], 3072\ns_add_u32 s44, %[wo], 1507328\nbuffer_load_dwordx4 v92, %[rw], s44 offen nt lds\ns_add_u32 m0, %[lw], 4096\ns_add_u32 s44, %[wo], 1572864\nbuffer_load_dwordx4 v92, %[rw], s44 offen nt lds\n" \
"s_add_u32 m0, %[lw], 5120\ns_add_u32 s44, %[wo], 1638400\nbuffer_load_dwordx4 v92, %[rw], s44 offen nt lds\ns_add_u32 m0, %[lw], 6144\ns_add_u32 s44, %[wo], 1703936\nbuffer_load_dwordx4 v92, %[rw], s44 offen nt lds\ns_add_u32 m0, %[lw], 7168\ns_add_u32 s44, %[wo], 1769472\n" \
"buffer_load_dwordx4 v92, %[rw], s44 offen nt lds\ns_add_u32 m0, %[lw], 8192\ns_add_u32 s44, %[wo], 1835008\nbuffer_load_dwordx4 v92, %[rw], s44 offen nt lds\ns_add_u32 m0, %[lw], 9216\ns_add_u32 s44, %[wo], 1900544\nbuffer_load_dwordx4 v92, %[rw], s44 offen nt lds\ns_add_u32 m0, %[lw], 10240\n" \
"s_add_u32 s44, %[wo], 1966080\nbuffer_load_dwordx4 v92, %[rw], s44 offen nt lds\ns_add_u32 m0, %[lw], 11264\ns_add_u32 s44, %[wo], 2031616\nbuffer_load_dwordx4 v92, %[rw], s44 offen nt lds\ns_add_u32 m0, %[lw], 12288\ns_add_u32 s44, %[wo], 2097152\nbuffer_load_dwordx4 v92, %[rw], s44 offen nt lds\n" \
"s_add_u32 m0, %[lw], 13312\ns_add_u32 s44, %[wo], 2162688\nbuffer_load_dwordx4 v92, %[rw], s44 offen nt lds\ns_add_u32 m0, %[lw], 14336\ns_add_u32 s44, %[wo], 2228224\nbuffer_load_dwordx4 v92, %[rw], s44 offen nt lds\ns_add_u32 m0, %[lw], 15360\ns_add_u32 s44, %[wo], 2293760\n" \
"buffer_load_dwordx4 v92, %[rw], s44 offen nt lds\ns_waitcnt vmcnt(32)\nds_read_b64_tr_b16 a[0:1], v94 offset:16384\nds_read_b64_tr_b16 a[2:3], v94 offset:16896\nds_read_b64_tr_b16 a[8:9], v94 offset:16392\nds_read_b64_tr_b16 a[10:11], v94 offset:16904\nds_read_b64_tr_b16 a[16:17], v94 offset:16448\nds_read_b64_tr_b16 a[18:19], v94 offset:16960\n" \
"ds_read_b64_tr_b16 a[24:25], v94 offset:16456\nds_read_b64_tr_b16 a[26:27], v94 offset:16968\ns_waitcnt lgkmcnt(0)\ns_add_u32 m0, %[lw], 16384\ns_add_u32 s44, %[wo], 2359296\nbuffer_load_dwordx4 v92, %[rw], s44 offen nt lds\ns_add_u32 m0, %[lw], 17408\ns_add_u32 s44, %[wo], 2424832\n" \
"buffer_load_dwordx4 v92, %[rw], s44 offen nt lds\ns_add_u32 m0, %[lw], 18432\ns_add_u32 s44, %[wo], 2490368\nbuffer_load_dwordx4 v92, %[rw], s44 offen nt lds\ns_add_u32 m0, %[lw], 19456\ns_add_u32 s44, %[wo], 2555904\nbuffer_load_dwordx4 v92, %[rw], s44 offen nt lds\ns_waitcnt vmcnt(32)\n" \
"ds_read_b64_tr_b16 a[4:5], v94 offset:20480\nds_read_b64_tr_b16 a[6:7], v94 offset:20992\nds_read_b64_tr_b16 a[12:13], v94 offset:20488\nds_read_b64_tr_b16 a[14:15], v94 offset:21000\nds_read_b64_tr_b16 a[20:21], v94 offset:20544\nds_read_b64_tr_b16 a[22:23], v94 offset:21056\nds_read_b64_tr_b16 a[28:29], v94 offset:20552\nds_read_b64_tr_b16 a[30:31], v94 offset:21064\n" \
"s_waitcnt lgkmcnt(0)\ns_add_u32 m0, %[lw], 20480\ns_add_u32 s44, %[wo], 2621440\nbuffer_load_dwordx4 v92, %[rw], s44 offen nt lds\ns_add_u32 m0, %[lw], 21504\ns_add_u32 s44, %[wo], 2686976\nbuffer_load_dwordx4 v92, %[rw], s44 offen nt lds\ns_add_u32 m0, %[lw], 22528\n" \
"s_add_u32 s44, %[wo], 2752512\nbuffer_load_dwordx4 v92, %[rw], s44 offen nt lds\ns_add_u32 m0, %[lw], 23552\ns_add_u32 s44, %[wo], 2818048\nbuffer_load_dwordx4 v92, %[rw], s44 offen nt lds\ns_waitcnt vmcnt(32)\nds_read_b64_tr_b16 a[32:33], v93 offset:0\nds_read_b64_tr_b16 a[34:35], v93 offset:512\n" \
"ds_read_b64_tr_b16 v[84:85], v93 offset:8\nds_read_b64_tr_b16 v[86:87], v93 offset:520\nds_read_b64_tr_b16 a[40:41], v93 offset:64\nds_read_b64_tr_b16 a[42:43], v93 offset:576\nds_read_b64_tr_b16 a[48:49], v93 offset:72\nds_read_b64_tr_b16 a[50:51], v93 offset:584\ns_waitcnt lgkmcnt(0)\ns_add_u32 m0, %[tb], 0\n" \
"s_add_u32 s44, %[wo], 2883584\nbuffer_load_dwordx4 v92, %[rw], s44 offen nt lds\ns_add_u32 m0, %[tb], 1024\ns_add_u32 s44, %[wo], 2949120\nbuffer_load_dwordx4 v92, %[rw], s44 offen nt lds\ns_add_u32 m0, %[tb], 2048\ns_add_u32 s44, %[wo], 3014656\nbuffer_load_dwordx4 v92, %[rw], s44 offen nt lds\n" \
"s_add_u32 m0, %[tb], 3072\ns_add_u32 s44, %[wo], 3080192\nbuffer_load_dwordx4 v92, %[rw], s44 offen nt lds\ns_waitcnt vmcnt(32)\nds_read_b64_tr_b16 a[36:37], v93 offset:4096\nds_read_b64_tr_b16 a[38:39], v93 offset:4608\nds_read_b64_tr_b16 v[80:81], v93 offset:4104\nds_read_b64_tr_b16 v[82:83], v93 offset:4616\n" \
"ds_read_b64_tr_b16 a[44:45], v93 offset:4160\nds_read_b64_tr_b16 a[46:47], v93 offset:4672\nds_read_b64_tr_b16 a[52:53], v93 offset:4168\nds_read_b64_tr_b16 a[54:55], v93 offset:4680\ns_waitcnt lgkmcnt(0)\ns_add_u32 m0, %[tb], 4096\ns_add_u32 s44, %[wo], 3145728\nbuffer_load_dwordx4 v92, %[rw], s44 offen nt lds\n" \
"s_add_u32 m0, %[tb], 5120\ns_add_u32 s44, %[wo], 3211264\nbuffer_load_dwordx4 v92, %[rw], s44 offen nt lds\ns_add_u32 m0, %[tb], 6144\ns_add_u32 s44, %[wo], 3276800\nbuffer_load_dwordx4 v92, %[rw], s44 offen nt lds\ns_add_u32 m0, %[tb], 7168\ns_add_u32 s44, %[wo], 3342336\n" \
"buffer_load_dwordx4 v92, %[rw], s44 offen nt lds\ns_waitcnt vmcnt(32)\nds_read_b64_tr_b16 a[56:57], v93 offset:8192\nds_read_b64_tr_b16 a[58:59], v93 offset:8704\nds_read_b64_tr_b16 a[64:65], v93 offset:8200\nds_read_b64_tr_b16 a[66:67], v93 offset:8712\nds_read_b64_tr_b16 v[76:77], v93 offset:8256\nds_read_b64_tr_b16 v[78:79], v93 offset:8768\n" \
"ds_read_b64_tr_b16 a[72:73], v93 offset:8264\nds_read_b64_tr_b16 a[74:75], v93 offset:8776\ns_waitcnt lgkmcnt(0)\ns_add_u32 m0, %[tb], 8192\ns_add_u32 s44, %[wo], 3407872\nbuffer_load_dwordx4 v92, %[rw], s44 offen nt lds\ns_add_u32 m0, %[tb], 9216\ns_add_u32 s44, %[wo], 3473408\n" \
"buffer_load_dwordx4 v92, %[rw], s44 offen nt lds\ns_add_u32 m0, %[tb], 10240\ns_add_u32 s44, %[wo], 3538944\nbuffer_load_dwordx4 v92, %[rw], s44 offen nt lds\ns_add_u32 m0, %[tb], 11264\ns_add_u32 s44, %[wo], 3604480\nbuffer_load_dwordx4 v92, %[rw], s44 offen nt lds\ns_waitcnt vmcnt(32)\n" \
"ds_read_b64_tr_b16 a[60:61], v94 offset:0\nds_read_b64_tr_b16 a[62:63], v94 offset:512\nds_read_b64_tr_b16 a[68:69], v94 offset:8\nds_read_b64_tr_b16 a[70:71], v94 offset:520\nds_read_b64_tr_b16 v[72:73], v94 offset:64\nds_read_b64_tr_b16 v[74:75], v94 offset:576\nds_read_b64_tr_b16 a[76:77], v94 offset:72\nds_read_b64_tr_b16 a[78:79], v94 offset:584\n" \
"s_waitcnt lgkmcnt(0)\ns_add_u32 m0, %[lw], 0\ns_add_u32 s44, %[wo], 3670016\nbuffer_load_dwordx4 v92, %[rw], s44 offen nt lds\ns_add_u32 m0, %[lw], 1024\ns_add_u32 s44, %[wo], 3735552\nbuffer_load_dwordx4 v92, %[rw], s44 offen nt lds\ns_add_u32 m0, %[lw], 2048\n" \
"s_add_u32 s44, %[wo], 3801088\nbuffer_load_dwordx4 v92, %[rw], s44 offen nt lds\ns_add_u32 m0, %[lw], 3072\ns_add_u32 s44, %[wo], 3866624\nbuffer_load_dwordx4 v92, %[rw], s44 offen nt lds\ns_waitcnt vmcnt(32)\nds_read_b64_tr_b16 a[80:81], v94 offset:4096\nds_read_b64_tr_b16 a[82:83], v94 offset:4608\n" \
"ds_read_b64_tr_b16 a[88:89], v94 offset:4104\nds_read_b64_tr_b16 a[90:91], v94 offset:4616\nds_read_b64_tr_b16 a[96:97], v94 offset:4160\nds_read_b64_tr_b16 a[98:99], v94 offset:4672\nds_read_b64_tr_b16 v[68:69], v94 offset:4168\nds_read_b64_tr_b16 v[70:71], v94 offset:4680\ns_waitcnt lgkmcnt(0)\ns_add_u32 m0, %[lw], 4096\n" \
"s_add_u32 s44, %[wo], 3932160\nbuffer_load_dwordx4 v92, %[rw], s44 offen nt lds\ns_add_u32 m0, %[lw], 5120\ns_add_u32 s44, %[wo], 3997696\nbuffer_load_dwordx4 v92, %[rw], s44 offen nt lds\ns_add_u32 m0, %[lw], 6144\ns_add_u32 s44, %[wo], 4063232\nbuffer_load_dwordx4 v92, %[rw], s44 offen nt lds\n" \
"s_add_u32 m0, %[lw], 7168\ns_add_u32 s44, %[wo], 4128768\nbuffer_load_dwordx4 v92, %[rw], s44 offen nt lds\ns_waitcnt vmcnt(32)\nds_read_b64_tr_b16 a[84:85], v94 offset:8192\nds_read_b64_tr_b16 a[86:87], v94 offset:8704\nds_read_b64_tr_b16 a[92:93], v94 offset:8200\nds_read_b64_tr_b16 a[94:95], v94 offset:8712\n" \
"ds_read_b64_tr_b16 a[100:101], v94 offset:8256\nds_read_b64_tr_b16 a[102:103], v94 offset:8768\nds_read_b64_tr_b16 v[64:65], v94 offset:8264\nds_read_b64_tr_b16 v[66:67], v94 offset:8776\ns_waitcnt lgkmcnt(0)\ns_add_u32 m0, %[lw], 8192\ns_add_u32 s44, %[wo], 4194304\nbuffer_load_dwordx4 v92, %[rw], s44 offen nt lds\n" \
"s_add_u32 m0, %[lw], 9216\ns_add_u32 s44, %[wo], 4259840\nbuffer_load_dwordx4 v92, %[rw], s44 offen nt lds\ns_add_u32 m0, %[lw], 10240\ns_add_u32 s44, %[wo], 4325376\nbuffer_load_dwordx4 v92, %[rw], s44 offen nt lds\ns_add_u32 m0, %[lw], 11264\ns_add_u32 s44, %[wo], 4390912\n" \
"buffer_load_dwordx4 v92, %[rw], s44 offen nt lds\ns_waitcnt vmcnt(32)\nds_read_b64_tr_b16 a[104:105], v94 offset:12288\nds_read_b64_tr_b16 a[106:107], v94 offset:12800\nds_read_b64_tr_b16 a[112:113], v94 offset:12296\nds_read_b64_tr_b16 a[114:115], v94 offset:12808\nds_read_b64_tr_b16 a[120:121], v94 offset:12352\nds_read_b64_tr_b16 a[122:123], v94 offset:12864\n" \
"ds_read_b64_tr_b16 a[128:129], v94 offset:12360\nds_read_b64_tr_b16 a[130:131], v94 offset:12872\ns_waitcnt lgkmcnt(0)\ns_add_u32 m0, %[lw], 12288\ns_add_u32 s44, %[wo], 4456448\nbuffer_load_dwordx4 v92, %[rw], s44 offen nt lds\ns_add_u32 m0, %[lw], 13312\ns_add_u32 s44, %[wo], 4521984\n" \
"buffer_load_dwordx4 v92, %[rw], s44 offen nt lds\ns_add_u32 m0, %[lw], 14336\ns_add_u32 s44, %[wo], 4587520\nbuffer_load_dwordx4 v92, %[rw], s44 offen nt lds\ns_add_u32 m0, %[lw], 15360\ns_add_u32 s44, %[wo], 4653056\nbuffer_load_dwordx4 v92, %[rw], s44 offen nt lds\ns_waitcnt vmcnt(32)\n" \
"ds_read_b64_tr_b16 a[108:109], v94 offset:16384\nds_read_b64_tr_b16 a[110:111], v94 offset:16896\nds_read_b64_tr_b16 a[116:117], v94 offset:16392\nds_read_b64_tr_b16 a[118:119], v94 offset:16904\nds_read_b64_tr_b16 a[124:125], v94 offset:16448\nds_read_b64_tr_b16 a[126:127], v94 offset:16960\nds_read_b64_tr_b16 a[132:133], v94 offset:16456\nds_read_b64_tr_b16 a[134:135], v94 offset:16968\n" \
"s_waitcnt lgkmcnt(0)\ns_add_u32 m0, %[lw], 16384\ns_add_u32 s44, %[wo], 4718592\nbuffer_load_dwordx4 v92, %[rw], s44 offen nt lds\ns_add_u32 m0, %[lw], 17408\ns_add_u32 s44, %[wo], 4784128\nbuffer_load_dwordx4 v92, %[rw], s44 offen nt lds\ns_add_u32 m0, %[lw], 18432\n" \
"s_add_u32 s44, %[wo], 4849664\nbuffer_load_dwordx4 v92, %[rw], s44 offen nt lds\ns_add_u32 m0, %[lw], 19456\ns_add_u32 s44, %[wo], 4915200\nbuffer_load_dwordx4 v92, %[rw], s44 offen nt lds\ns_waitcnt vmcnt(32)\nds_read_b64_tr_b16 a[136:137], v94 offset:20480\nds_read_b64_tr_b16 a[138:139], v94 offset:20992\n" \
"ds_read_b64_tr_b16 v[60:61], v94 offset:20488\nds_read_b64_tr_b16 v[62:63], v94 offset:21000\nds_read_b64_tr_b16 a[144:145], v94 offset:20544\nds_read_b64_tr_b16 a[146:147], v94 offset:21056\nds_read_b64_tr_b16 a[152:153], v94 offset:20552\nds_read_b64_tr_b16 a[154:155], v94 offset:21064\ns_waitcnt lgkmcnt(0)\ns_add_u32 m0, %[lw], 20480\n" \
"s_add_u32 s44, %[wo], 4980736\nbuffer_load_dwordx4 v92, %[rw], s44 offen nt lds\ns_add_u32 m0, %[lw], 21504\ns_add_u32 s44, %[wo], 5046272\nbuffer_load_dwordx4 v92, %[rw], s44 offen nt lds\ns_add_u32 m0, %[lw], 22528\ns_add_u32 s44, %[wo], 5111808\nbuffer_load_dwordx4 v92, %[rw], s44 offen nt lds\n" \
"s_add_u32 m0, %[lw], 23552\ns_add_u32 s44, %[wo], 5177344\nbuffer_load_dwordx4 v92, %[rw], s44 offen nt lds\ns_waitcnt vmcnt(32)\nds_read_b64_tr_b16 a[140:141], v93 offset:0\nds_read_b64_tr_b16 a[142:143], v93 offset:512\nds_read_b64_tr_b16 v[56:57], v93 offset:8\nds_read_b64_tr_b16 v[58:59], v93 offset:520\n" \
"ds_read_b64_tr_b16 a[148:149], v93 offset:64\nds_read_b64_tr_b16 a[150:151], v93 offset:576\nds_read_b64_tr_b16 a[156:157], v93 offset:72\nds_read_b64_tr_b16 a[158:159], v93 offset:584\ns_waitcnt lgkmcnt(0)\ns_add_u32 m0, %[tb], 0\ns_add_u32 s44, %[wo], 5242880\nbuffer_load_dwordx4 v92, %[rw], s44 offen nt lds\n" \
"s_add_u32 m0, %[tb], 1024\ns_add_u32 s44, %[wo], 5308416\nbuffer_load_dwordx4 v92, %[rw], s44 offen nt lds\ns_add_u32 m0, %[tb], 2048\ns_add_u32 s44, %[wo], 5373952\nbuffer_load_dwordx4 v92, %[rw], s44 offen nt lds\ns_add_u32 m0, %[tb], 3072\ns_add_u32 s44, %[wo], 5439488\n" \
"buffer_load_dwordx4 v92, %[rw], s44 offen nt lds\ns_waitcnt vmcnt(32)\nds_read_b64_tr_b16 a[160:161], v93 offset:4096\nds_read_b64_tr_b16 a[162:163], v93 offset:4608\nds_read_b64_tr_b16 a[168:169], v93 offset:4104\nds_read_b64_tr_b16 a[170:171], v93 offset:4616\nds_read_b64_tr_b16 v[52:53], v93 offset:4160\nds_read_b64_tr_b16 v[54:55], v93 offset:4672\n" \
"ds_read_b64_tr_b16 a[176:177], v93 offset:4168\nds_read_b64_tr_b16 a[178:179], v93 offset:4680\ns_waitcnt lgkmcnt(0)\ns_add_u32 m0, %[tb], 4096\ns_add_u32 s44, %[wo], 5505024\nbuffer_load_dwordx4 v92, %[rw], s44 offen nt lds\ns_add_u32 m0, %[tb], 5120\ns_add_u32 s44, %[wo], 5570560\n" \
"buffer_load_dwordx4 v92, %[rw], s44 offen nt lds\ns_add_u32 m0, %[tb], 6144\ns_add_u32 s44, %[wo], 5636096\nbuffer_load_dwordx4 v92, %[rw], s44 offen nt lds\ns_add_u32 m0, %[tb], 7168\ns_add_u32 s44, %[wo], 5701632\nbuffer_load_dwordx4 v92, %[rw], s44 offen nt lds\ns_waitcnt vmcnt(32)\n" \
"ds_read_b64_tr_b16 a[164:165], v93 offset:8192\nds_read_b64_tr_b16 a[166:167], v93 offset:8704\nds_read_b64_tr_b16 a[172:173], v93 offset:8200\nds_read_b64_tr_b16 a[174:175], v93 offset:8712\nds_read_b64_tr_b16 v[48:49], v93 offset:8256\nds_read_b64_tr_b16 v[50:51], v93 offset:8768\nds_read_b64_tr_b16 a[180:181], v93 offset:8264\nds_read_b64_tr_b16 a[182:183], v93 offset:8776\n" \
"s_waitcnt lgkmcnt(0)\ns_add_u32 m0, %[tb], 8192\ns_add_u32 s44, %[wo], 5767168\nbuffer_load_dwordx4 v92, %[rw], s44 offen nt lds\ns_add_u32 m0, %[tb], 9216\ns_add_u32 s44, %[wo], 5832704\nbuffer_load_dwordx4 v92, %[rw], s44 offen nt lds\ns_add_u32 m0, %[tb], 10240\n" \
"s_add_u32 s44, %[wo], 5898240\nbuffer_load_dwordx4 v92, %[rw], s44 offen nt lds\ns_add_u32 m0, %[tb], 11264\ns_add_u32 s44, %[wo], 5963776\nbuffer_load_dwordx4 v92, %[rw], s44 offen nt lds\ns_waitcnt vmcnt(32)\nds_read_b64_tr_b16 a[184:185], v94 offset:0\nds_read_b64_tr_b16 a[186:187], v94 offset:512\n" \
"ds_read_b64_tr_b16 a[192:193], v94 offset:8\nds_read_b64_tr_b16 a[194:195], v94 offset:520\nds_read_b64_tr_b16 a[200:201], v94 offset:64\nds_read_b64_tr_b16 a[202:203], v94 offset:576\nds_read_b64_tr_b16 v[44:45], v94 offset:72\nds_read_b64_tr_b16 v[46:47], v94 offset:584\ns_waitcnt lgkmcnt(0)\ns_add_u32 m0, %[lw], 0\n" \
"s_add_u32 s44, %[wo], 6029312\nbuffer_load_dwordx4 v92, %[rw], s44 offen nt lds\ns_add_u32 m0, %[lw], 1024\ns_add_u32 s44, %[wo], 6094848\nbuffer_load_dwordx4 v92, %[rw], s44 offen nt lds\ns_add_u32 m0, %[lw], 2048\ns_add_u32 s44, %[wo], 6160384\nbuffer_load_dwordx4 v92, %[rw], s44 offen nt lds\n" \
"s_add_u32 m0, %[lw], 3072\ns_add_u32 s44, %[wo], 6225920\nbuffer_load_dwordx4 v92, %[rw], s44 offen nt lds\ns_waitcnt vmcnt(32)\nds_read_b64_tr_b16 a[188:189], v94 offset:4096\nds_read_b64_tr_b16 a[190:191], v94 offset:4608\nds_read_b64_tr_b16 a[196:197], v94 offset:4104\nds_read_b64_tr_b16 a[198:199], v94 offset:4616\n" \
"ds_read_b64_tr_b16 a[204:205], v94 offset:4160\nds_read_b64_tr_b16 a[206:207], v94 offset:4672\nds_read_b64_tr_b16 v[40:41], v94 offset:4168\nds_read_b64_tr_b16 v[42:43], v94 offset:4680\ns_waitcnt lgkmcnt(0)\ns_add_u32 m0, %[lw], 4096\ns_add_u32 s44, %[wo], 6291456\nbuffer_load_dwordx4 v92, %[rw], s44 offen nt lds\n" \
"s_add_u32 m0, %[lw], 5120\ns_add_u32 s44, %[wo], 6356992\nbuffer_load_dwordx4 v92, %[rw], s44 offen nt lds\ns_add_u32 m0, %[lw], 6144\ns_add_u32 s44, %[wo], 6422528\nbuffer_load_dwordx4 v92, %[rw], s44 offen nt lds\ns_add_u32 m0, %[lw], 7168\ns_add_u32 s44, %[wo], 6488064\n" \
"buffer_load_dwordx4 v92, %[rw], s44 offen nt lds\ns_waitcnt vmcnt(32)\nds_read_b64_tr_b16 a[208:209], v94 offset:8192\nds_read_b64_tr_b16 a[210:211], v94 offset:8704\nds_read_b64_tr_b16 a[216:217], v94 offset:8200\nds_read_b64_tr_b16 a[218:219], v94 offset:8712\nds_read_b64_tr_b16 a[224:225], v94 offset:8256\nds_read_b64_tr_b16 a[226:227], v94 offset:8768\n" \
"ds_read_b64_tr_b16 a[232:233], v94 offset:8264\nds_read_b64_tr_b16 a[234:235], v94 offset:8776\ns_waitcnt lgkmcnt(0)\ns_add_u32 m0, %[lw], 8192\ns_add_u32 s44, %[wo], 6553600\nbuffer_load_dwordx4 v92, %[rw], s44 offen nt lds\ns_add_u32 m0, %[lw], 9216\ns_add_u32 s44, %[wo], 6619136\n" \
"buffer_load_dwordx4 v92, %[rw], s44 offen nt lds\ns_add_u32 m0, %[lw], 10240\ns_add_u32 s44, %[wo], 6684672\nbuffer_load_dwordx4 v92, %[rw], s44 offen nt lds\ns_add_u32 m0, %[lw], 11264\ns_add_u32 s44, %[wo], 6750208\nbuffer_load_dwordx4 v92, %[rw], s44 offen nt lds\ns_waitcnt vmcnt(32)\n" \
"ds_read_b64_tr_b16 a[212:213], v94 offset:12288\nds_read_b64_tr_b16 a[214:215], v94 offset:12800\nds_read_b64_tr_b16 a[220:221], v94 offset:12296\nds_read_b64_tr_b16 a[222:223], v94 offset:12808\nds_read_b64_tr_b16 a[228:229], v94 offset:12352\nds_read_b64_tr_b16 a[230:231], v94 offset:12864\nds_read_b64_tr_b16 a[236:237], v94 offset:12360\nds_read_b64_tr_b16 a[238:239], v94 offset:12872\n" \
"s_waitcnt lgkmcnt(0)\ns_add_u32 m0, %[lw], 12288\ns_add_u32 s44, %[wo], 6815744\nbuffer_load_dwordx4 v92, %[rw], s44 offen nt lds\ns_add_u32 m0, %[lw], 13312\ns_add_u32 s44, %[wo], 6881280\nbuffer_load_dwordx4 v92, %[rw], s44 offen nt lds\ns_add_u32 m0, %[lw], 14336\n" \
"s_add_u32 s44, %[wo], 6946816\nbuffer_load_dwordx4 v92, %[rw], s44 offen nt lds\ns_add_u32 m0, %[lw], 15360\ns_add_u32 s44, %[wo], 7012352\nbuffer_load_dwordx4 v92, %[rw], s44 offen nt lds\ns_waitcnt vmcnt(32)\nds_read_b64_tr_b16 a[240:241], v94 offset:16384\nds_read_b64_tr_b16 a[242:243], v94 offset:16896\n" \
"ds_read_b64_tr_b16 v[36:37], v94 offset:16392\nds_read_b64_tr_b16 v[38:39], v94 offset:16904\nds_read_b64_tr_b16 a[248:249], v94 offset:16448\nds_read_b64_tr_b16 a[250:251], v94 offset:16960\nds_read_b64_tr_b16 v[96:97], v94 offset:16456\nds_read_b64_tr_b16 v[98:99], v94 offset:16968\ns_waitcnt lgkmcnt(0)\ns_add_u32 m0, %[lw], 16384\n" \
"s_add_u32 s44, %[wo], 7077888\nbuffer_load_dwordx4 v92, %[rw], s44 offen nt lds\ns_add_u32 m0, %[lw], 17408\ns_add_u32 s44, %[wo], 7143424\nbuffer_load_dwordx4 v92, %[rw], s44 offen nt lds\ns_add_u32 m0, %[lw], 18432\ns_add_u32 s44, %[wo], 7208960\nbuffer_load_dwordx4 v92, %[rw], s44 offen nt lds\n" \
"s_add_u32 m0, %[lw], 19456\ns_add_u32 s44, %[wo], 7274496\nbuffer_load_dwordx4 v92, %[rw], s44 offen nt lds\ns_waitcnt vmcnt(32)\nds_read_b64_tr_b16 a[244:245], v94 offset:20480\nds_read_b64_tr_b16 a[246:247], v94 offset:20992\nds_read_b64_tr_b16 v[32:33], v94 offset:20488\nds_read_b64_tr_b16 v[34:35], v94 offset:21000\n" \
"ds_read_b64_tr_b16 a[252:253], v94 offset:20544\nds_read_b64_tr_b16 a[254:255], v94 offset:21056\nds_read_b64_tr_b16 v[100:101], v94 offset:20552\nds_read_b64_tr_b16 v[102:103], v94 offset:21064\ns_waitcnt lgkmcnt(0)\ns_add_u32 m0, %[lw], 20480\ns_add_u32 s44, %[wo], 7340032\nbuffer_load_dwordx4 v92, %[rw], s44 offen nt lds\n" \
"s_add_u32 m0, %[lw], 21504\ns_add_u32 s44, %[wo], 7405568\nbuffer_load_dwordx4 v92, %[rw], s44 offen nt lds\ns_add_u32 m0, %[lw], 22528\ns_add_u32 s44, %[wo], 7471104\nbuffer_load_dwordx4 v92, %[rw], s44 offen nt lds\ns_add_u32 m0, %[lw], 23552\ns_add_u32 s44, %[wo], 7536640\n" \
"buffer_load_dwordx4 v92, %[rw], s44 offen nt lds\ns_waitcnt vmcnt(32)\nds_read_b64_tr_b16 v[104:105], v93 offset:0\nds_read_b64_tr_b16 v[106:107], v93 offset:512\nds_read_b64_tr_b16 v[112:113], v93 offset:8\nds_read_b64_tr_b16 v[114:115], v93 offset:520\nds_read_b64_tr_b16 v[28:29], v93 offset:64\nds_read_b64_tr_b16 v[30:31], v93 offset:576\n" \
"ds_read_b64_tr_b16 v[120:121], v93 offset:72\nds_read_b64_tr_b16 v[122:123], v93 offset:584\ns_waitcnt lgkmcnt(0)\ns_add_u32 m0, %[tb], 0\ns_add_u32 s44, %[wo], 7602176\nbuffer_load_dwordx4 v92, %[rw], s44 offen nt lds\ns_add_u32 m0, %[tb], 1024\ns_add_u32 s44, %[wo], 7667712\n" \
"buffer_load_dwordx4 v92, %[rw], s44 offen nt lds\ns_add_u32 m0, %[tb], 2048\ns_add_u32 s44, %[wo], 7733248\nbuffer_load_dwordx4 v92, %[rw], s44 offen nt lds\ns_add_u32 m0, %[tb], 3072\ns_add_u32 s44, %[wo], 7798784\nbuffer_load_dwordx4 v92, %[rw], s44 offen nt lds\ns_waitcnt vmcnt(32)\n" \
"ds_read_b64_tr_b16 v[108:109], v93 offset:4096\nds_read_b64_tr_b16 v[110:111], v93 offset:4608\nds_read_b64_tr_b16 v[116:117], v93 offset:4104\nds_read_b64_tr_b16 v[118:119], v93 offset:4616\nds_read_b64_tr_b16 v[24:25], v93 offset:4160\nds_read_b64_tr_b16 v[26:27], v93 offset:4672\nds_read_b64_tr_b16 v[124:125], v93 offset:4168\nds_read_b64_tr_b16 v[126:127], v93 offset:4680\n" \
"s_waitcnt lgkmcnt(0)\nbuffer_load_dwordx2 v[88:89], off, s[36:39], s45 sc1\ns_add_u32 m0, %[tb], 4096\ns_add_u32 s44, %[wo], 7864320\nbuffer_load_dwordx4 v92, %[rw], s44 offen nt lds\ns_add_u32 m0, %[tb], 5120\ns_add_u32 s44, %[wo], 7929856\nbuffer_load_dwordx4 v92, %[rw], s44 offen nt lds\n" \
"s_add_u32 m0, %[tb], 6144\ns_add_u32 s44, %[wo], 7995392\nbuffer_load_dwordx4 v92, %[rw], s44 offen nt lds\ns_add_u32 m0, %[tb], 7168\ns_add_u32 s44, %[wo], 8060928\nbuffer_load_dwordx4 v92, %[rw], s44 offen nt lds\ns_waitcnt vmcnt(33)\nds_read_b64_tr_b16 v[128:129], v93 offset:8192\n" \
"ds_read_b64_tr_b16 v[130:131], v93 offset:8704\nds_read_b64_tr_b16 v[136:137], v93 offset:8200\nds_read_b64_tr_b16 v[138:139], v93 offset:8712\nds_read_b64_tr_b16 v[144:145], v93 offset:8256\nds_read_b64_tr_b16 v[146:147], v93 offset:8768\nds_read_b64_tr_b16 v[20:21], v93 offset:8264\nds_read_b64_tr_b16 v[22:23], v93 offset:8776\ns_waitcnt lgkmcnt(0)\n" \
"s_add_u32 m0, %[tb], 8192\ns_add_u32 s44, %[wo], 8126464\nbuffer_load_dwordx4 v92, %[rw], s44 offen nt lds\ns_add_u32 m0, %[tb], 9216\ns_add_u32 s44, %[wo], 8192000\nbuffer_load_dwordx4 v92, %[rw], s44 offen nt lds\ns_add_u32 m0, %[tb], 10240\ns_add_u32 s44, %[wo], 8257536\n" \
"buffer_load_dwordx4 v92, %[rw], s44 offen nt lds\ns_add_u32 m0, %[tb], 11264\ns_add_u32 s44, %[wo], 8323072\nbuffer_load_dwordx4 v92, %[rw], s44 offen nt lds\ns_waitcnt vmcnt(33)\nds_read_b64_tr_b16 v[132:133], v94 offset:0\nds_read_b64_tr_b16 v[134:135], v94 offset:512\nds_read_b64_tr_b16 v[140:141], v94 offset:8\n" \
"ds_read_b64_tr_b16 v[142:143], v94 offset:520\nds_read_b64_tr_b16 v[148:149], v94 offset:64\nds_read_b64_tr_b16 v[150:151], v94 offset:576\nds_read_b64_tr_b16 v[16:17], v94 offset:72\nds_read_b64_tr_b16 v[18:19], v94 offset:584\ns_waitcnt lgkmcnt(0)\nds_write_b128 v1, v[84:87] offset:0\nds_write_b128 v1, v[80:83] offset:1024\n" \
"ds_write_b128 v1, v[76:79] offset:2048\nds_write_b128 v1, v[72:75] offset:3072\ns_waitcnt vmcnt(29)\nds_read_b64_tr_b16 v[152:153], v94 offset:4096\nds_read_b64_tr_b16 v[154:155], v94 offset:4608\nds_read_b64_tr_b16 v[160:161], v94 offset:4104\nds_read_b64_tr_b16 v[162:163], v94 offset:4616\nds_read_b64_tr_b16 v[168:169], v94 offset:4160\n" \
"ds_read_b64_tr_b16 v[170:171], v94 offset:4672\nds_read_b64_tr_b16 v[176:177], v94 offset:4168\nds_read_b64_tr_b16 v[178:179], v94 offset:4680\ns_waitcnt lgkmcnt(0)\nds_write_b128 v1, v[68:71] offset:4096\nds_write_b128 v1, v[64:67] offset:5120\nds_write_b128 v1, v[60:63] offset:6144\nds_write_b128 v1, v[56:59] offset:7168\n" \
"s_waitcnt vmcnt(25)\nds_read_b64_tr_b16 v[156:157], v94 offset:8192\nds_read_b64_tr_b16 v[158:159], v94 offset:8704\nds_read_b64_tr_b16 v[164:165], v94 offset:8200\nds_read_b64_tr_b16 v[166:167], v94 offset:8712\nds_read_b64_tr_b16 v[172:173], v94 offset:8256\nds_read_b64_tr_b16 v[174:175], v94 offset:8768\nds_read_b64_tr_b16 v[180:181], v94 offset:8264\n" \
"ds_read_b64_tr_b16 v[182:183], v94 offset:8776\ns_waitcnt lgkmcnt(0)\nds_write_b128 v1, v[52:55] offset:8192\nds_write_b128 v1, v[48:51] offset:9216\nds_write_b128 v1, v[44:47] offset:10240\nds_write_b128 v1, v[40:43] offset:11264\ns_waitcnt vmcnt(21)\nds_read_b64_tr_b16 v[184:185], v94 offset:12288\n" \
"ds_read_b64_tr_b16 v[186:187], v94 offset:12800\nds_read_b64_tr_b16 v[40:41], v94 offset:12296\nds_read_b64_tr_b16 v[42:43], v94 offset:12808\nds_read_b64_tr_b16 v[192:193], v94 offset:12352\nds_read_b64_tr_b16 v[194:195], v94 offset:12864\nds_read_b64_tr_b16 v[200:201], v94 offset:12360\nds_read_b64_tr_b16 v[202:203], v94 offset:12872\ns_waitcnt lgkmcnt(0)\n" \
"ds_write_b128 v1, v[36:39] offset:12288\nds_write_b128 v1, v[32:35] offset:13312\nds_write_b128 v1, v[28:31] offset:14336\nds_write_b128 v1, v[24:27] offset:15360\ns_waitcnt vmcnt(17)\nds_read_b64_tr_b16 v[188:189], v94 offset:16384\nds_read_b64_tr_b16 v[190:191], v94 offset:16896\nds_read_b64_tr_b16 v[24:25], v94 offset:16392\n" \
"ds_read_b64_tr_b16 v[26:27], v94 offset:16904\nds_read_b64_tr_b16 v[196:197], v94 offset:16448\nds_read_b64_tr_b16 v[198:199], v94 offset:16960\nds_read_b64_tr_b16 v[204:205], v94 offset:16456\nds_read_b64_tr_b16 v[206:207], v94 offset:16968\ns_waitcnt lgkmcnt(0)\nds_write_b128 v1, v[20:23] offset:16384\nds_write_b128 v1, v[16:19] offset:17408\n" \
"ds_write_b128 v1, v[40:43] offset:18432\nds_write_b128 v1, v[24:27] offset:19456\ns_waitcnt vmcnt(13)\nds_read_b64_tr_b16 v[208:209], v94 offset:20480\nds_read_b64_tr_b16 v[210:211], v94 offset:20992\nds_read_b64_tr_b16 v[216:217], v94 offset:20488\nds_read_b64_tr_b16 v[218:219], v94 offset:21000\nds_read_b64_tr_b16 v[24:25], v94 offset:20544\n" \
"ds_read_b64_tr_b16 v[26:27], v94 offset:21056\nds_read_b64_tr_b16 v[224:225], v94 offset:20552\nds_read_b64_tr_b16 v[226:227], v94 offset:21064\ns_waitcnt lgkmcnt(0)\nds_write_b128 v1, v[24:27] offset:20480\ns_waitcnt vmcnt(9)\nds_read_b64_tr_b16 v[212:213], v93 offset:0\nds_read_b64_tr_b16 v[214:215], v93 offset:512\n" \
"ds_read_b64_tr_b16 v[220:221], v93 offset:8\nds_read_b64_tr_b16 v[222:223], v93 offset:520\nds_read_b64_tr_b16 v[24:25], v93 offset:64\nds_read_b64_tr_b16 v[26:27], v93 offset:576\nds_read_b64_tr_b16 v[228:229], v93 offset:72\nds_read_b64_tr_b16 v[230:231], v93 offset:584\ns_waitcnt lgkmcnt(0)\nds_write_b128 v1, v[24:27] offset:21504\n" \
"s_waitcnt vmcnt(4)\nds_read_b64_tr_b16 v[232:233], v93 offset:4096\nds_read_b64_tr_b16 v[234:235], v93 offset:4608\nds_read_b64_tr_b16 v[240:241], v93 offset:4104\nds_read_b64_tr_b16 v[242:243], v93 offset:4616\nds_read_b64_tr_b16 v[248:249], v93 offset:4160\nds_read_b64_tr_b16 v[250:251], v93 offset:4672\nds_read_b64_tr_b16 v[24:25], v93 offset:4168\n" \
"ds_read_b64_tr_b16 v[26:27], v93 offset:4680\ns_waitcnt lgkmcnt(0)\nds_write_b128 v1, v[24:27] offset:22528\ns_waitcnt vmcnt(0)\nds_read_b64_tr_b16 v[236:237], v93 offset:8192\nds_read_b64_tr_b16 v[238:239], v93 offset:8704\nds_read_b64_tr_b16 v[244:245], v93 offset:8200\nds_read_b64_tr_b16 v[246:247], v93 offset:8712\n" \
"ds_read_b64_tr_b16 v[252:253], v93 offset:8256\nds_read_b64_tr_b16 v[254:255], v93 offset:8768\nds_read_b64_tr_b16 v[24:25], v93 offset:8264\nds_read_b64_tr_b16 v[26:27], v93 offset:8776\ns_waitcnt lgkmcnt(0)\ns_mov_b32 s43, 0\ns_mov_b32 s86, 110\ns_mov_b32 s89, 0\n" \
".Lqk_hb%=:\nv_cmp_ne_u32 vcc, v95, v88\ns_cbranch_vccnz .Lqk_hb1%=\ns_sleep 4\nbuffer_load_dwordx2 v[88:89], off, s[36:39], s45 sc1\ns_waitcnt vmcnt(0)\ns_add_u32 s43, s43, 1\ns_cmp_lt_u32 s43, 262144\n" \
"s_cbranch_scc1 .Lqk_hb%=\ns_branch .Lqk_abort%=\n.Lqk_hb1%=:\nv_readfirstlane_b32 s44, v89\ns_and_b32 s44, s44, 0x200000\ns_add_u32 s36, s36, s44\ns_addc_u32 s37, s37, 0\nds_write_b128 v1, v[24:27] offset:23552\n" \
"s_waitcnt lgkmcnt(0)\ns_barrier\ns_mov_b32 s40, -1\ns_mov_b32 s43, 0\ns_mov_b32 s61, 0x7fff7fff\ns_mov_b32 s58, 0x3e0293ee\ns_mov_b32 s59, 0xbe0293ee\ns_lshl_b32 s57, %[w], 12\n" \
"s_mov_b32 s62, 0xff00ff00\ns_mov_b32 s63, 0xff00ff00\ns_mov_b32 s56, 0\ns_cmp_eq_u32 %[w], 3\ns_cbranch_scc1 .Lqk_fetch0%=\n.Lqk_pwait%=:\ns_barrier\nds_read_b128 v[80:83], v2 offset:0\n" \
"ds_read_b128 v[84:87], v2 offset:256\nds_read_b128 v[88:91], v2 offset:2048\n.Lqk_proj%=:\ns_waitcnt lgkmcnt(2)\nv_mfma_f32_16x16x32_bf16 v[16:19], a[0:3], v[80:83], 0\nds_read_b128 v[92:95], v2 offset:2304\nv_mfma_f32_16x16x32_bf16 v[20:23], a[8:11], v[80:83], 0\nds_read_b128 v[72:75], v1 offset:0\n" \
"v_mfma_f32_16x16x32_bf16 v[24:27], a[16:19], v[80:83], 0\nv_mfma_f32_16x16x32_bf16 v[28:31], a[24:27], v[80:83], 0\ns_waitcnt lgkmcnt(3)\nv_mfma_f32_16x16x32_bf16 v[32:35], a[0:3], v[84:87], 0\nds_read_b128 v[80:83], v2 offset:4096\nv_mfma_f32_16x16x32_bf16 v[36:39], a[8:11], v[84:87], 0\nv_mfma_f32_16x16x32_bf16 v[40:43], a[16:19], v[84:87], 0\nv_mfma_f32_16x16x32_bf16 v[44:47], a[24:27], v[84:87], 0\n" \
"s_waitcnt lgkmcnt(3)\nv_mfma_f32_16x16x32_bf16 v[16:19], a[4:7], v[88:91], v[16:19]\nds_read_b128 v[84:87], v2 offset:4352\nv_mfma_f32_16x16x32_bf16 v[20:23], a[12:15], v[88:91], v[20:23]\nds_read_b128 v[76:79], v1 offset:1024\nv_mfma_f32_16x16x32_bf16 v[24:27], a[20:23], v[88:91], v[24:27]\nv_mfma_f32_16x16x32_bf16 v[28:31], a[28:31], v[88:91], v[28:31]\ns_waitcnt lgkmcnt(4)\n" \
"v_mfma_f32_16x16x32_bf16 v[32:35], a[4:7], v[92:95], v[32:35]\nds_read_b128 v[88:91], v2 offset:6144\nv_mfma_f32_16x16x32_bf16 v[36:39], a[12:15], v[92:95], v[36:39]\nv_mfma_f32_16x16x32_bf16 v[40:43], a[20:23], v[92:95], v[40:43]\nv_mfma_f32_16x16x32_bf16 v[44:47], a[28:31], v[92:95], v[44:47]\ns_waitcnt lgkmcnt(3)\nv_mfma_f32_16x16x32_bf16 v[16:19], a[32:35], v[80:83], v[16:19]\nds_read_b128 v[92:95], v2 offset:6400\n" \
"v_mfma_f32_16x16x32_bf16 v[20:23], v[72:75], v[80:83], v[20:23]\nds_read_b128 v[64:67], v1 offset:2048\nv_mfma_f32_16x16x32_bf16 v[24:27], a[40:43], v[80:83], v[24:27]\nv_mfma_f32_16x16x32_bf16 v[28:31], a[48:51], v[80:83], v[28:31]\ns_waitcnt lgkmcnt(4)\nv_mfma_f32_16x16x32_bf16 v[32:35], a[32:35], v[84:87], v[32:35]\nds_read_b128 v[80:83], v2 offset:8192\nv_mfma_f32_16x16x32_bf16 v[36:39], v[72:75], v[84:87], v[36:39]\n" \
"v_mfma_f32_16x16x32_bf16 v[40:43], a[40:43], v[84:87], v[40:43]\nv_mfma_f32_16x16x32_bf16 v[44:47], a[48:51], v[84:87], v[44:47]\ns_waitcnt lgkmcnt(3)\nv_mfma_f32_16x16x32_bf16 v[16:19], a[36:39], v[88:91], v[16:19]\nds_read_b128 v[84:87], v2 offset:8448\nv_mfma_f32_16x16x32_bf16 v[20:23], v[76:79], v[88:91], v[20:23]\nds_read_b128 v[68:71], v1 offset:3072\nv_mfma_f32_16x16x32_bf16 v[24:27], a[44:47], v[88:91], v[24:27]\n" \
"v_mfma_f32_16x16x32_bf16 v[28:31], a[52:55], v[88:91], v[28:31]\ns_waitcnt lgkmcnt(4)\nv_mfma_f32_16x16x32_bf16 v[32:35], a[36:39], v[92:95], v[32:35]\nds_read_b128 v[88:91], v2 offset:10240\nv_mfma_f32_16x16x32_bf16 v[36:39], v[76:79], v[92:95], v[36:39]\nv_mfma_f32_16x16x32_bf16 v[40:43], a[44:47], v[92:95], v[40:43]\nv_mfma_f32_16x16x32_bf16 v[44:47], a[52:55], v[92:95], v[44:47]\ns_waitcnt lgkmcnt(3)\n" \
"v_mfma_f32_16x16x32_bf16 v[16:19], a[56:59], v[80:83], v[16:19]\nds_read_b128 v[92:95], v2 offset:10496\nv_mfma_f32_16x16x32_bf16 v[20:23], a[64:67], v[80:83], v[20:23]\nds_read_b128 v[72:75], v1 offset:4096\nv_mfma_f32_16x16x32_bf16 v[24:27], v[64:67], v[80:83], v[24:27]\nv_mfma_f32_16x16x32_bf16 v[28:31], a[72:75], v[80:83], v[28:31]\ns_waitcnt lgkmcnt(4)\nv_mfma_f32_16x16x32_bf16 v[32:35], a[56:59], v[84:87], v[32:35]\n" \
"ds_read_b128 v[80:83], v2 offset:12288\nv_mfma_f32_16x16x32_bf16 v[36:39], a[64:67], v[84:87], v[36:39]\nv_mfma_f32_16x16x32_bf16 v[40:43], v[64:67], v[84:87], v[40:43]\nv_mfma_f32_16x16x32_bf16 v[44:47], a[72:75], v[84:87], v[44:47]\ns_waitcnt lgkmcnt(3)\nv_mfma_f32_16x16x32_bf16 v[16:19], a[60:63], v[88:91], v[16:19]\nds_read_b128 v[84:87], v2 offset:12544\nv_mfma_f32_16x16x32_bf16 v[20:23], a[68:71], v[88:91], v[20:23]\n" \
"ds_read_b128 v[76:79], v1 offset:5120\nv_mfma_f32_16x16x32_bf16 v[24:27], v[68:71], v[88:91], v[24:27]\nv_mfma_f32_16x16x32_bf16 v[28:31], a[76:79], v[88:91], v[28:31]\ns_waitcnt lgkmcnt(4)\nv_mfma_f32_16x16x32_bf16 v[32:35], a[60:63], v[92:95], v[32:35]\nds_read_b128 v[88:91], v2 offset:14336\nv_mfma_f32_16x16x32_bf16 v[36:39], a[68:71], v[92:95], v[36:39]\nv_mfma_f32_16x16x32_bf16 v[40:43], v[68:71], v[92:95], v[40:43]\n" \
"v_mfma_f32_16x16x32_bf16 v[44:47], a[76:79], v[92:95], v[44:47]\n.Lqk_pj%=:\ns_waitcnt lgkmcnt(3)\nv_mfma_f32_16x16x32_bf16 v[16:19], a[80:83], v[80:83], v[16:19]\nds_read_b128 v[92:95], v2 offset:14592\nv_mfma_f32_16x16x32_bf16 v[20:23], a[88:91], v[80:83], v[20:23]\ns_add_u32 s40, s40, 1\nv_mfma_f32_16x16x32_bf16 v[24:27], a[96:99], v[80:83], v[24:27]\n" \
"s_lshl_b32 s44, s40, 17\nv_mfma_f32_16x16x32_bf16 v[28:31], v[72:75], v[80:83], v[28:31]\ns_add_u32 s44, s44, %[xn]\ns_waitcnt lgkmcnt(3)\nv_mfma_f32_16x16x32_bf16 v[32:35], a[80:83], v[84:87], v[32:35]\ns_barrier\nds_read_b128 v[80:83], v2 offset:16384\nv_mfma_f32_16x16x32_bf16 v[36:39], a[88:91], v[84:87], v[36:39]\n" \
"s_lshr_b32 s45, s44, 8\nv_mfma_f32_16x16x32_bf16 v[40:43], a[96:99], v[84:87], v[40:43]\ns_and_b32 s45, s45, 0x2000\nv_mfma_f32_16x16x32_bf16 v[44:47], v[72:75], v[84:87], v[44:47]\ns_xor_b32 s41, s44, s45\ns_waitcnt lgkmcnt(2)\nv_mfma_f32_16x16x32_bf16 v[16:19], a[84:87], v[88:91], v[16:19]\nds_read_b128 v[84:87], v2 offset:16640\n" \
"v_mfma_f32_16x16x32_bf16 v[20:23], a[92:95], v[88:91], v[20:23]\ns_add_u32 s41, s41, %[xw]\ns_and_b32 s42, s40, 3\ns_add_u32 m0, s57, 32768\nv_mfma_f32_16x16x32_bf16 v[24:27], a[100:103], v[88:91], v[24:27]\ns_add_u32 s60, s41, 65536\nbuffer_load_dwordx4 v0, s[36:39], s60 offen offset:0 sc1 lds\nv_mfma_f32_16x16x32_bf16 v[28:31], v[76:79], v[88:91], v[28:31]\n" \
"s_lshl_b32 s42, s42, 15\ns_waitcnt lgkmcnt(2)\nv_mfma_f32_16x16x32_bf16 v[32:35], a[84:87], v[92:95], v[32:35]\nds_read_b128 v[88:91], v2 offset:18432\nv_mfma_f32_16x16x32_bf16 v[36:39], a[92:95], v[92:95], v[36:39]\ns_and_b32 s44, s40, 1\nv_mfma_f32_16x16x32_bf16 v[40:43], a[100:103], v[92:95], v[40:43]\ns_lshl_b32 s55, s44, 17\n" \
"buffer_load_dwordx4 v0, s[36:39], s60 offen offset:1024 sc1 lds\nv_mfma_f32_16x16x32_bf16 v[44:47], v[76:79], v[92:95], v[44:47]\ns_add_u32 s56, s56, %[fs]\ns_waitcnt lgkmcnt(2)\nv_mfma_f32_16x16x32_bf16 v[16:19], a[104:107], v[80:83], v[16:19]\nds_read_b128 v[92:95], v2 offset:18688\nv_mfma_f32_16x16x32_bf16 v[20:23], a[112:115], v[80:83], v[20:23]\nds_read_b128 v[72:75], v1 offset:6144\n" \
"v_mfma_f32_16x16x32_bf16 v[24:27], a[120:123], v[80:83], v[24:27]\nbuffer_load_dwordx4 v0, s[36:39], s60 offen offset:2048 sc1 lds\nv_mfma_f32_16x16x32_bf16 v[28:31], a[128:131], v[80:83], v[28:31]\ns_waitcnt lgkmcnt(3)\nv_mfma_f32_16x16x32_bf16 v[32:35], a[104:107], v[84:87], v[32:35]\nds_read_b128 v[80:83], v2 offset:20480\nv_mfma_f32_16x16x32_bf16 v[36:39], a[112:115], v[84:87], v[36:39]\nv_mfma_f32_16x16x32_bf16 v[40:43], a[120:123], v[84:87], v[40:43]\n" \
"buffer_load_dwordx4 v0, s[36:39], s60 offen offset:3072 sc1 lds\nv_mfma_f32_16x16x32_bf16 v[44:47], a[128:131], v[84:87], v[44:47]\ns_waitcnt lgkmcnt(3)\nv_mfma_f32_16x16x32_bf16 v[16:19], a[108:111], v[88:91], v[16:19]\nds_read_b128 v[84:87], v2 offset:20736\nv_mfma_f32_16x16x32_bf16 v[20:23], a[116:119], v[88:91], v[20:23]\nds_read_b128 v[76:79], v1 offset:7168\nv_mfma_f32_16x16x32_bf16 v[24:27], a[124:127], v[88:91], v[24:27]\n" \
"v_mfma_f32_16x16x32_bf16 v[28:31], a[132:135], v[88:91], v[28:31]\ns_waitcnt lgkmcnt(4)\nv_mfma_f32_16x16x32_bf16 v[32:35], a[108:111], v[92:95], v[32:35]\nds_read_b128 v[88:91], v2 offset:22528\nv_mfma_f32_16x16x32_bf16 v[36:39], a[116:119], v[92:95], v[36:39]\nv_mfma_f32_16x16x32_bf16 v[40:43], a[124:127], v[92:95], v[40:43]\nv_mfma_f32_16x16x32_bf16 v[44:47], a[132:135], v[92:95], v[44:47]\ns_waitcnt lgkmcnt(3)\n" \
"v_mfma_f32_16x16x32_bf16 v[16:19], a[136:139], v[80:83], v[16:19]\nds_read_b128 v[92:95], v2 offset:22784\nv_mfma_f32_16x16x32_bf16 v[20:23], v[72:75], v[80:83], v[20:23]\nds_read_b128 v[64:67], v1 offset:8192\nv_mfma_f32_16x16x32_bf16 v[24:27], a[144:147], v[80:83], v[24:27]\nv_mfma_f32_16x16x32_bf16 v[28:31], a[152:155], v[80:83], v[28:31]\ns_waitcnt lgkmcnt(4)\nv_mfma_f32_16x16x32_bf16 v[32:35], a[136:139], v[84:87], v[32:35]\n" \
"ds_read_b128 v[80:83], v2 offset:24576\nv_mfma_f32_16x16x32_bf16 v[36:39], v[72:75], v[84:87], v[36:39]\nv_mfma_f32_16x16x32_bf16 v[40:43], a[144:147], v[84:87], v[40:43]\nv_mfma_f32_16x16x32_bf16 v[44:47], a[152:155], v[84:87], v[44:47]\ns_waitcnt lgkmcnt(3)\nv_mfma_f32_16x16x32_bf16 v[16:19], a[140:143], v[88:91], v[16:19]\nds_read_b128 v[84:87], v2 offset:24832\nv_mfma_f32_16x16x32_bf16 v[20:23], v[76:79], v[88:91], v[20:23]\n" \
"ds_read_b128 v[68:71], v1 offset:9216\nv_mfma_f32_16x16x32_bf16 v[24:27], a[148:151], v[88:91], v[24:27]\nv_mfma_f32_16x16x32_bf16 v[28:31], a[156:159], v[88:91], v[28:31]\ns_waitcnt lgkmcnt(4)\nv_mfma_f32_16x16x32_bf16 v[32:35], a[140:143], v[92:95], v[32:35]\nds_read_b128 v[88:91], v2 offset:26624\nv_mfma_f32_16x16x32_bf16 v[36:39], v[76:79], v[92:95], v[36:39]\nv_mfma_f32_16x16x32_bf16 v[40:43], a[148:151], v[92:95], v[40:43]\n" \
"v_mfma_f32_16x16x32_bf16 v[44:47], a[156:159], v[92:95], v[44:47]\ns_waitcnt lgkmcnt(3)\nv_mfma_f32_16x16x32_bf16 v[16:19], a[160:163], v[80:83], v[16:19]\nds_read_b128 v[92:95], v2 offset:26880\nv_mfma_f32_16x16x32_bf16 v[20:23], a[168:171], v[80:83], v[20:23]\nds_read_b128 v[72:75], v1 offset:10240\nv_mfma_f32_16x16x32_bf16 v[24:27], v[64:67], v[80:83], v[24:27]\nv_mfma_f32_16x16x32_bf16 v[28:31], a[176:179], v[80:83], v[28:31]\n" \
"s_waitcnt lgkmcnt(4)\nv_mfma_f32_16x16x32_bf16 v[32:35], a[160:163], v[84:87], v[32:35]\nds_read_b128 v[80:83], v2 offset:28672\nv_mfma_f32_16x16x32_bf16 v[36:39], a[168:171], v[84:87], v[36:39]\nv_mfma_f32_16x16x32_bf16 v[40:43], v[64:67], v[84:87], v[40:43]\nv_mfma_f32_16x16x32_bf16 v[44:47], a[176:179], v[84:87], v[44:47]\ns_waitcnt lgkmcnt(3)\nv_mfma_f32_16x16x32_bf16 v[16:19], a[164:167], v[88:91], v[16:19]\n" \
"ds_read_b128 v[84:87], v2 offset:28928\nv_mfma_f32_16x16x32_bf16 v[20:23], a[172:175], v[88:91], v[20:23]\nds_read_b128 v[76:79], v1 offset:11264\nv_mfma_f32_16x16x32_bf16 v[24:27], v[68:71], v[88:91], v[24:27]\nv_mfma_f32_16x16x32_bf16 v[28:31], a[180:183], v[88:91], v[28:31]\ns_waitcnt lgkmcnt(4)\nv_mfma_f32_16x16x32_bf16 v[32:35], a[164:167], v[92:95], v[32:35]\nds_read_b128 v[88:91], v2 offset:30720\n" \
"v_mfma_f32_16x16x32_bf16 v[36:39], a[172:175], v[92:95], v[36:39]\nv_mfma_f32_16x16x32_bf16 v[40:43], v[68:71], v[92:95], v[40:43]\nv_mfma_f32_16x16x32_bf16 v[44:47], a[180:183], v[92:95], v[44:47]\ns_waitcnt lgkmcnt(3)\nv_mfma_f32_16x16x32_bf16 v[16:19], a[184:187], v[80:83], v[16:19]\nds_read_b128 v[92:95], v2 offset:30976\nv_mfma_f32_16x16x32_bf16 v[20:23], a[192:195], v[80:83], v[20:23]\nv_mfma_f32_16x16x32_bf16 v[24:27], a[200:203], v[80:83], v[24:27]\n" \
"v_mfma_f32_16x16x32_bf16 v[28:31], v[72:75], v[80:83], v[28:31]\ns_waitcnt lgkmcnt(3)\nv_mfma_f32_16x16x32_bf16 v[32:35], a[184:187], v[84:87], v[32:35]\ns_waitcnt vmcnt(0)\ns_barrier\nds_read_b128 v[48:51], v2 offset:32768\nv_mfma_f32_16x16x32_bf16 v[36:39], a[192:195], v[84:87], v[36:39]\ns_add_u32 m0, s57, 0\n" \
"v_mfma_f32_16x16x32_bf16 v[40:43], a[200:203], v[84:87], v[40:43]\ns_add_u32 s60, s41, 98304\nbuffer_load_dwordx4 v0, s[36:39], s60 offen offset:0 sc1 lds\nv_mfma_f32_16x16x32_bf16 v[44:47], v[72:75], v[84:87], v[44:47]\ns_waitcnt lgkmcnt(2)\nv_mfma_f32_16x16x32_bf16 v[16:19], a[188:191], v[88:91], v[16:19]\nds_read_b128 v[52:55], v2 offset:33024\nv_mfma_f32_16x16x32_bf16 v[20:23], a[196:199], v[88:91], v[20:23]\n" \
"v_mfma_f32_16x16x32_bf16 v[24:27], a[204:207], v[88:91], v[24:27]\nbuffer_load_dwordx4 v0, s[36:39], s60 offen offset:1024 sc1 lds\nv_mfma_f32_16x16x32_bf16 v[28:31], v[76:79], v[88:91], v[28:31]\ns_waitcnt lgkmcnt(2)\nv_mfma_f32_16x16x32_bf16 v[32:35], a[188:191], v[92:95], v[32:35]\nds_read_b128 v[56:59], v2 offset:34816\nv_mfma_f32_16x16x32_bf16 v[36:39], a[196:199], v[92:95], v[36:39]\nv_mfma_f32_16x16x32_bf16 v[40:43], a[204:207], v[92:95], v[40:43]\n" \
"buffer_load_dwordx4 v0, s[36:39], s60 offen offset:2048 sc1 lds\nv_mfma_f32_16x16x32_bf16 v[44:47], v[76:79], v[92:95], v[44:47]\ns_waitcnt lgkmcnt(2)\nv_mfma_f32_16x16x32_bf16 v[16:19], a[208:211], v[48:51], v[16:19]\nds_read_b128 v[60:63], v2 offset:35072\nv_mfma_f32_16x16x32_bf16 v[20:23], a[216:219], v[48:51], v[20:23]\nds_read_b128 v[72:75], v1 offset:12288\nv_mfma_f32_16x16x32_bf16 v[24:27], a[224:227], v[48:51], v[24:27]\n" \
"buffer_load_dwordx4 v0, s[36:39], s60 offen offset:3072 sc1 lds\nv_mfma_f32_16x16x32_bf16 v[28:31], a[232:235], v[48:51], v[28:31]\ns_waitcnt lgkmcnt(3)\nv_mfma_f32_16x16x32_bf16 v[32:35], a[208:211], v[52:55], v[32:35]\nds_read_b128 v[48:51], v2 offset:36864\nv_mfma_f32_16x16x32_bf16 v[36:39], a[216:219], v[52:55], v[36:39]\nv_mfma_f32_16x16x32_bf16 v[40:43], a[224:227], v[52:55], v[40:43]\nv_mfma_f32_16x16x32_bf16 v[44:47], a[232:235], v[52:55], v[44:47]\n" \
"s_waitcnt lgkmcnt(3)\nv_mfma_f32_16x16x32_bf16 v[16:19], a[212:215], v[56:59], v[16:19]\nds_read_b128 v[52:55], v2 offset:37120\nv_mfma_f32_16x16x32_bf16 v[20:23], a[220:223], v[56:59], v[20:23]\nds_read_b128 v[76:79], v1 offset:13312\nv_mfma_f32_16x16x32_bf16 v[24:27], a[228:231], v[56:59], v[24:27]\nv_mfma_f32_16x16x32_bf16 v[28:31], a[236:239], v[56:59], v[28:31]\ns_waitcnt lgkmcnt(4)\n" \
"v_mfma_f32_16x16x32_bf16 v[32:35], a[212:215], v[60:63], v[32:35]\nds_read_b128 v[56:59], v2 offset:38912\nv_mfma_f32_16x16x32_bf16 v[36:39], a[220:223], v[60:63], v[36:39]\nv_mfma_f32_16x16x32_bf16 v[40:43], a[228:231], v[60:63], v[40:43]\nv_mfma_f32_16x16x32_bf16 v[44:47], a[236:239], v[60:63], v[44:47]\ns_waitcnt lgkmcnt(3)\nv_mfma_f32_16x16x32_bf16 v[16:19], a[240:243], v[48:51], v[16:19]\nds_read_b128 v[60:63], v2 offset:39168\n" \
"v_mfma_f32_16x16x32_bf16 v[20:23], v[72:75], v[48:51], v[20:23]\nds_read_b128 v[64:67], v1 offset:14336\nv_mfma_f32_16x16x32_bf16 v[24:27], a[248:251], v[48:51], v[24:27]\nv_mfma_f32_16x16x32_bf16 v[28:31], v[96:99], v[48:51], v[28:31]\ns_waitcnt lgkmcnt(4)\nv_mfma_f32_16x16x32_bf16 v[32:35], a[240:243], v[52:55], v[32:35]\nds_read_b128 v[48:51], v2 offset:40960\nv_mfma_f32_16x16x32_bf16 v[36:39], v[72:75], v[52:55], v[36:39]\n" \
"v_mfma_f32_16x16x32_bf16 v[40:43], a[248:251], v[52:55], v[40:43]\nv_mfma_f32_16x16x32_bf16 v[44:47], v[96:99], v[52:55], v[44:47]\ns_waitcnt lgkmcnt(3)\nv_mfma_f32_16x16x32_bf16 v[16:19], a[244:247], v[56:59], v[16:19]\nds_read_b128 v[52:55], v2 offset:41216\nv_mfma_f32_16x16x32_bf16 v[20:23], v[76:79], v[56:59], v[20:23]\nds_read_b128 v[68:71], v1 offset:15360\nv_mfma_f32_16x16x32_bf16 v[24:27], a[252:255], v[56:59], v[24:27]\n" \
"v_mfma_f32_16x16x32_bf16 v[28:31], v[100:103], v[56:59], v[28:31]\ns_waitcnt lgkmcnt(4)\nv_mfma_f32_16x16x32_bf16 v[32:35], a[244:247], v[60:63], v[32:35]\nds_read_b128 v[56:59], v2 offset:43008\nv_mfma_f32_16x16x32_bf16 v[36:39], v[76:79], v[60:63], v[36:39]\nv_mfma_f32_16x16x32_bf16 v[40:43], a[252:255], v[60:63], v[40:43]\nv_mfma_f32_16x16x32_bf16 v[44:47], v[100:103], v[60:63], v[44:47]\ns_waitcnt lgkmcnt(3)\n" \
"v_mfma_f32_16x16x32_bf16 v[16:19], v[104:107], v[48:51], v[16:19]\nds_read_b128 v[60:63], v2 offset:43264\nv_mfma_f32_16x16x32_bf16 v[20:23], v[112:115], v[48:51], v[20:23]\nds_read_b128 v[72:75], v1 offset:16384\nv_mfma_f32_16x16x32_bf16 v[24:27], v[64:67], v[48:51], v[24:27]\nv_mfma_f32_16x16x32_bf16 v[28:31], v[120:123], v[48:51], v[28:31]\ns_waitcnt lgkmcnt(4)\nv_mfma_f32_16x16x32_bf16 v[32:35], v[104:107], v[52:55], v[32:35]\n" \
"ds_read_b128 v[48:51], v2 offset:45056\nv_mfma_f32_16x16x32_bf16 v[36:39], v[112:115], v[52:55], v[36:39]\nv_mfma_f32_16x16x32_bf16 v[40:43], v[64:67], v[52:55], v[40:43]\nv_mfma_f32_16x16x32_bf16 v[44:47], v[120:123], v[52:55], v[44:47]\ns_waitcnt lgkmcnt(3)\nv_mfma_f32_16x16x32_bf16 v[16:19], v[108:111], v[56:59], v[16:19]\nds_read_b128 v[52:55], v2 offset:45312\nv_mfma_f32_16x16x32_bf16 v[20:23], v[116:119], v[56:59], v[20:23]\n" \
"ds_read_b128 v[76:79], v1 offset:17408\nv_mfma_f32_16x16x32_bf16 v[24:27], v[68:71], v[56:59], v[24:27]\nv_mfma_f32_16x16x32_bf16 v[28:31], v[124:127], v[56:59], v[28:31]\ns_waitcnt lgkmcnt(4)\nv_mfma_f32_16x16x32_bf16 v[32:35], v[108:111], v[60:63], v[32:35]\nds_read_b128 v[56:59], v2 offset:47104\nv_mfma_f32_16x16x32_bf16 v[36:39], v[116:119], v[60:63], v[36:39]\nv_mfma_f32_16x16x32_bf16 v[40:43], v[68:71], v[60:63], v[40:43]\n" \
"v_mfma_f32_16x16x32_bf16 v[44:47], v[124:127], v[60:63], v[44:47]\ns_waitcnt lgkmcnt(3)\nv_mfma_f32_16x16x32_bf16 v[16:19], v[128:131], v[48:51], v[16:19]\nds_read_b128 v[60:63], v2 offset:47360\nv_mfma_f32_16x16x32_bf16 v[20:23], v[136:139], v[48:51], v[20:23]\nv_mfma_f32_16x16x32_bf16 v[24:27], v[144:147], v[48:51], v[24:27]\nv_mfma_f32_16x16x32_bf16 v[28:31], v[72:75], v[48:51], v[28:31]\ns_waitcnt lgkmcnt(3)\n" \
"v_mfma_f32_16x16x32_bf16 v[32:35], v[128:131], v[52:55], v[32:35]\ns_waitcnt lgkmcnt(0)\ns_waitcnt vmcnt(0)\ns_barrier\nds_read_b128 v[48:51], v2 offset:0\nv_mfma_f32_16x16x32_bf16 v[36:39], v[136:139], v[52:55], v[36:39]\nv_mfma_f32_16x16x32_bf16 v[40:43], v[144:147], v[52:55], v[40:43]\nv_mfma_f32_16x16x32_bf16 v[44:47], v[72:75], v[52:55], v[44:47]\n" \
"v_mfma_f32_16x16x32_bf16 v[16:19], v[132:135], v[56:59], v[16:19]\nds_read_b128 v[52:55], v2 offset:256\nv_mfma_f32_16x16x32_bf16 v[20:23], v[140:143], v[56:59], v[20:23]\nv_mfma_f32_16x16x32_bf16 v[24:27], v[148:151], v[56:59], v[24:27]\nv_mfma_f32_16x16x32_bf16 v[28:31], v[76:79], v[56:59], v[28:31]\nv_mfma_f32_16x16x32_bf16 v[32:35], v[132:135], v[60:63], v[32:35]\nds_read_b128 v[56:59], v2 offset:2048\nv_mfma_f32_16x16x32_bf16 v[36:39], v[140:143], v[60:63], v[36:39]\n" \
"v_mfma_f32_16x16x32_bf16 v[40:43], v[148:151], v[60:63], v[40:43]\nv_mfma_f32_16x16x32_bf16 v[44:47], v[76:79], v[60:63], v[44:47]\ns_waitcnt lgkmcnt(2)\nv_mfma_f32_16x16x32_bf16 v[16:19], v[152:155], v[48:51], v[16:19]\nds_read_b128 v[60:63], v2 offset:2304\nv_mfma_f32_16x16x32_bf16 v[20:23], v[160:163], v[48:51], v[20:23]\nds_read_b128 v[72:75], v1 offset:18432\nv_mfma_f32_16x16x32_bf16 v[24:27], v[168:171], v[48:51], v[24:27]\n" \
"v_mfma_f32_16x16x32_bf16 v[28:31], v[176:179], v[48:51], v[28:31]\ns_waitcnt lgkmcnt(3)\nv_mfma_f32_16x16x32_bf16 v[32:35], v[152:155], v[52:55], v[32:35]\nds_read_b128 v[48:51], v2 offset:4096\nv_mfma_f32_16x16x32_bf16 v[36:39], v[160:163], v[52:55], v[36:39]\nv_mfma_f32_16x16x32_bf16 v[40:43], v[168:171], v[52:55], v[40:43]\nv_mfma_f32_16x16x32_bf16 v[44:47], v[176:179], v[52:55], v[44:47]\ns_waitcnt lgkmcnt(3)\n" \
"v_mfma_f32_16x16x32_bf16 v[16:19], v[156:159], v[56:59], v[16:19]\nds_read_b128 v[52:55], v2 offset:4352\nv_mfma_f32_16x16x32_bf16 v[20:23], v[164:167], v[56:59], v[20:23]\nds_read_b128 v[76:79], v1 offset:19456\nv_mfma_f32_16x16x32_bf16 v[24:27], v[172:175], v[56:59], v[24:27]\nv_mfma_f32_16x16x32_bf16 v[28:31], v[180:183], v[56:59], v[28:31]\ns_waitcnt lgkmcnt(4)\nv_mfma_f32_16x16x32_bf16 v[32:35], v[156:159], v[60:63], v[32:35]\n" \
"ds_read_b128 v[56:59], v2 offset:6144\nv_mfma_f32_16x16x32_bf16 v[36:39], v[164:167], v[60:63], v[36:39]\nv_mfma_f32_16x16x32_bf16 v[40:43], v[172:175], v[60:63], v[40:43]\nv_mfma_f32_16x16x32_bf16 v[44:47], v[180:183], v[60:63], v[44:47]\ns_waitcnt lgkmcnt(3)\nv_mfma_f32_16x16x32_bf16 v[16:19], v[184:187], v[48:51], v[16:19]\nds_read_b128 v[60:63], v2 offset:6400\nv_mfma_f32_16x16x32_bf16 v[20:23], v[72:75], v[48:51], v[20:23]\n" \
"ds_read_b128 v[64:67], v1 offset:20480\nv_mfma_f32_16x16x32_bf16 v[24:27], v[192:195], v[48:51], v[24:27]\nv_mfma_f32_16x16x32_bf16 v[28:31], v[200:203], v[48:51], v[28:31]\ns_waitcnt lgkmcnt(4)\nv_mfma_f32_16x16x32_bf16 v[32:35], v[184:187], v[52:55], v[32:35]\nds_read_b128 v[48:51], v2 offset:8192\nv_mfma_f32_16x16x32_bf16 v[36:39], v[72:75], v[52:55], v[36:39]\nv_mfma_f32_16x16x32_bf16 v[40:43], v[192:195], v[52:55], v[40:43]\n" \
"v_mfma_f32_16x16x32_bf16 v[44:47], v[200:203], v[52:55], v[44:47]\ns_waitcnt lgkmcnt(3)\nv_mfma_f32_16x16x32_bf16 v[16:19], v[188:191], v[56:59], v[16:19]\nds_read_b128 v[52:55], v2 offset:10240\nv_mfma_f32_16x16x32_bf16 v[20:23], v[76:79], v[56:59], v[20:23]\nds_read_b128 v[68:71], v1 offset:21504\nv_mfma_f32_16x16x32_bf16 v[24:27], v[196:199], v[56:59], v[24:27]\nbuffer_load_dwordx4 v[80:83], v3, s[36:39], s42 offen offset:0 sc1\n" \
"v_mfma_f32_16x16x32_bf16 v[28:31], v[204:207], v[56:59], v[28:31]\ns_waitcnt lgkmcnt(4)\nv_mfma_f32_16x16x32_bf16 v[32:35], v[188:191], v[60:63], v[32:35]\nds_read_b128 v[56:59], v2 offset:12288\nv_mfma_f32_16x16x32_bf16 v[36:39], v[76:79], v[60:63], v[36:39]\nds_read_b128 v[72:75], v1 offset:22528\nv_mfma_f32_16x16x32_bf16 v[40:43], v[196:199], v[60:63], v[40:43]\nbuffer_load_dwordx4 v[84:87], v3, s[36:39], s42 offen offset:1024 sc1\n" \
"v_mfma_f32_16x16x32_bf16 v[44:47], v[204:207], v[60:63], v[44:47]\ns_waitcnt lgkmcnt(4)\nv_mfma_f32_16x16x32_bf16 v[16:19], v[208:211], v[48:51], v[16:19]\nds_read_b128 v[60:63], v2 offset:14336\nv_mfma_f32_16x16x32_bf16 v[20:23], v[216:219], v[48:51], v[20:23]\nds_read_b128 v[76:79], v1 offset:23552\nv_mfma_f32_16x16x32_bf16 v[24:27], v[64:67], v[48:51], v[24:27]\nbuffer_load_dwordx4 v[88:91], v3, s[36:39], s42 offen offset:2048 sc1\n" \
"v_mfma_f32_16x16x32_bf16 v[28:31], v[224:227], v[48:51], v[28:31]\ns_waitcnt lgkmcnt(5)\nv_mfma_f32_16x16x32_bf16 v[16:19], v[212:215], v[52:55], v[16:19]\nds_read_b128 v[48:51], v2 offset:8448\nv_mfma_f32_16x16x32_bf16 v[20:23], v[220:223], v[52:55], v[20:23]\ns_waitcnt lgkmcnt(5)\nv_mfma_f32_16x16x32_bf16 v[24:27], v[68:71], v[52:55], v[24:27]\nbuffer_load_dwordx4 v[92:95], v3, s[36:39], s42 offen offset:3072 sc1\n" \
"v_mfma_f32_16x16x32_bf16 v[28:31], v[228:231], v[52:55], v[28:31]\ns_waitcnt lgkmcnt(4)\nv_mfma_f32_16x16x32_bf16 v[16:19], v[232:235], v[56:59], v[16:19]\nds_read_b128 v[52:55], v2 offset:10496\nv_mfma_f32_16x16x32_bf16 v[20:23], v[240:243], v[56:59], v[20:23]\nv_mfma_f32_16x16x32_bf16 v[24:27], v[248:251], v[56:59], v[24:27]\ns_waitcnt lgkmcnt(4)\nv_mfma_f32_16x16x32_bf16 v[28:31], v[72:75], v[56:59], v[28:31]\n" \
"s_waitcnt lgkmcnt(3)\nv_mfma_f32_16x16x32_bf16 v[16:19], v[236:239], v[60:63], v[16:19]\nds_read_b128 v[56:59], v2 offset:12544\nv_mfma_f32_16x16x32_bf16 v[20:23], v[244:247], v[60:63], v[20:23]\nv_mfma_f32_16x16x32_bf16 v[24:27], v[252:255], v[60:63], v[24:27]\ns_waitcnt lgkmcnt(3)\nv_mfma_f32_16x16x32_bf16 v[28:31], v[76:79], v[60:63], v[28:31]\ns_waitcnt lgkmcnt(2)\n" \
"v_mfma_f32_16x16x32_bf16 v[32:35], v[208:211], v[48:51], v[32:35]\nds_read_b128 v[60:63], v2 offset:14592\nv_mfma_f32_16x16x32_bf16 v[36:39], v[216:219], v[48:51], v[36:39]\nv_cvt_pk_bf16_f32 v20, v16, v20\nv_cvt_pk_bf16_f32 v21, v17, v21\nv_mfma_f32_16x16x32_bf16 v[40:43], v[64:67], v[48:51], v[40:43]\nv_cvt_pk_bf16_f32 v22, v18, v22\nv_cvt_pk_bf16_f32 v23, v19, v23\n" \
"v_mfma_f32_16x16x32_bf16 v[44:47], v[224:227], v[48:51], v[44:47]\ns_waitcnt lgkmcnt(2)\nds_write_b128 v5, v[20:23] offset:0\nv_mfma_f32_16x16x32_bf16 v[32:35], v[212:215], v[52:55], v[32:35]\nv_cvt_pk_bf16_f32 v28, v24, v28\nv_cvt_pk_bf16_f32 v29, v25, v29\nv_mfma_f32_16x16x32_bf16 v[36:39], v[220:223], v[52:55], v[36:39]\nv_cvt_pk_bf16_f32 v30, v26, v30\n" \
"v_cvt_pk_bf16_f32 v31, v27, v31\nv_mfma_f32_16x16x32_bf16 v[40:43], v[68:71], v[52:55], v[40:43]\nv_mov_b64 v[48:49], 0\nv_mov_b64 v[50:51], 0\nv_mfma_f32_16x16x32_bf16 v[44:47], v[228:231], v[52:55], v[44:47]\ns_cmp_eq_u32 %[w], 1\nds_write_b128 v5, v[28:31] offset:512\ns_cbranch_scc1 .Lqk_c1%=\n" \
"s_cmp_eq_u32 %[w], 2\ns_cbranch_scc1 .Lqk_c2%=\ns_cmp_eq_u32 %[w], 3\ns_cbranch_scc1 .Lqk_c3%=\n.Lqk_c0%=:\ns_waitcnt lgkmcnt(3)\nv_mfma_f32_16x16x32_bf16 v[32:35], v[232:235], v[56:59], v[32:35]\nv_mov_b64 v[52:53], 0\n" \
"v_mov_b64 v[54:55], 0\nv_mfma_f32_16x16x32_bf16 v[36:39], v[240:243], v[56:59], v[36:39]\ns_waitcnt lgkmcnt(2)\nds_read_b128 v[64:67], v1 offset:0\nv_mfma_f32_16x16x32_bf16 v[32:35], v[236:239], v[60:63], v[32:35]\nds_read_b128 v[68:71], v1 offset:1024\nv_mfma_f32_16x16x32_bf16 v[36:39], v[244:247], v[60:63], v[36:39]\ns_bfe_i32 s54, s40, 0x10000\n" \
"v_mfma_f32_16x16x32_bf16 v[40:43], v[248:251], v[56:59], v[40:43]\nv_mfma_f32_16x16x32_bf16 v[44:47], v[72:75], v[56:59], v[44:47]\nv_cvt_pk_bf16_f32 v36, v32, v36\nv_cvt_pk_bf16_f32 v37, v33, v37\nv_mfma_f32_16x16x32_bf16 v[40:43], v[252:255], v[60:63], v[40:43]\nv_cvt_pk_bf16_f32 v38, v34, v38\nv_cvt_pk_bf16_f32 v39, v35, v39\nv_mfma_f32_16x16x32_bf16 v[44:47], v[76:79], v[60:63], v[44:47]\n" \
"ds_write_b128 v5, v[36:39] offset:256\nv_mov_b64 v[56:57], 0\nv_mov_b64 v[58:59], 0\nv_mov_b64 v[60:61], 0\nv_mov_b64 v[62:63], 0\n.Lqk_poll0%=:\ns_waitcnt vmcnt(0)\nv_smfmac_f32_16x16x64_bf16 v[48:51], v[80:83], a[0:7], v4\n" \
"ds_write_b128 v0, v[80:83] offset:16384\nv_max3_u32 v16, v80, v82, v84\nv_smfmac_f32_16x16x64_bf16 v[52:55], v[80:83], a[8:15], v4\nds_read_b128 v[72:75], v1 offset:2048\nds_read_b128 v[76:79], v1 offset:3072\nv_max3_u32 v16, v16, v86, v88\nv_smfmac_f32_16x16x64_bf16 v[56:59], v[80:83], a[16:23], v4\nds_write_b128 v0, v[84:87] offset:17408\n" \
"v_max3_u32 v16, v16, v90, v92\nv_max_u32 v16, v16, v94\nv_smfmac_f32_16x16x64_bf16 v[60:63], v[80:83], a[24:31], v4\nv_cmp_eq_u32 vcc, -1, v16\nv_smfmac_f32_16x16x64_bf16 v[48:51], v[84:87], a[32:39], v4\nds_write_b128 v0, v[88:91] offset:18432\ns_cbranch_vccnz .Lqk_re0%=\ns_waitcnt lgkmcnt(6)\n" \
"v_smfmac_f32_16x16x64_bf16 v[52:55], v[84:87], v[64:71], v4\nds_read_b128 v[64:67], v1 offset:4096\nds_read_b128 v[68:71], v1 offset:5120\nv_smfmac_f32_16x16x64_bf16 v[56:59], v[84:87], a[40:47], v4\nds_write_b128 v0, v[92:95] offset:19456\nv_smfmac_f32_16x16x64_bf16 v[60:63], v[84:87], a[48:55], v4\nv_smfmac_f32_16x16x64_bf16 v[48:51], v[88:91], a[56:63], v4\nv_smfmac_f32_16x16x64_bf16 v[52:55], v[88:91], a[64:71], v4\n" \
"s_waitcnt lgkmcnt(0)\ns_barrier\ns_memrealtime s[68:69]\nds_read_b128 v[80:83], v0 offset:20480\nds_read_b128 v[84:87], v0 offset:21504\nv_mov_b32 v24, s40\nv_smfmac_f32_16x16x64_bf16 v[56:59], v[88:91], v[72:79], v4\nds_read_b128 v[72:75], v1 offset:6144\n" \
"ds_read_b128 v[76:79], v1 offset:7168\nv_cvt_pk_bf16_f32 v44, v40, v44\nv_smfmac_f32_16x16x64_bf16 v[60:63], v[88:91], a[72:79], v4\nds_read_b128 v[88:91], v0 offset:22528\nv_cvt_pk_bf16_f32 v45, v41, v45\nv_smfmac_f32_16x16x64_bf16 v[48:51], v[92:95], a[80:87], v4\nv_cvt_pk_bf16_f32 v46, v42, v46\nv_smfmac_f32_16x16x64_bf16 v[52:55], v[92:95], a[88:95], v4\n" \
"v_cvt_pk_bf16_f32 v47, v43, v47\nv_smfmac_f32_16x16x64_bf16 v[56:59], v[92:95], a[96:103], v4\nv_smfmac_f32_16x16x64_bf16 v[60:63], v[92:95], v[64:71], v4\ns_waitcnt lgkmcnt(4)\nds_write_b128 v5, v[44:47] offset:768\nds_read_b128 v[92:95], v0 offset:23552\nds_read_b128 v[64:67], v1 offset:8192\nds_read_b128 v[68:71], v1 offset:9216\n" \
"v_smfmac_f32_16x16x64_bf16 v[48:51], v[80:83], a[104:111], v4\ns_sub_u32 s88, s86, s89\ns_min_u32 s88, s88, 150\nv_smfmac_f32_16x16x64_bf16 v[52:55], v[80:83], a[112:119], v4\ns_add_u32 s88, s88, s68\ns_mov_b32 s89, s68\nv_mov_b32 v25, s88\nv_smfmac_f32_16x16x64_bf16 v[56:59], v[80:83], a[120:127], v4\n" \
"buffer_store_dwordx2 v[24:25], off, s[36:39], %[sg] sc1\nv_smfmac_f32_16x16x64_bf16 v[60:63], v[80:83], a[128:135], v4\ns_waitcnt lgkmcnt(7)\nds_read_b128 v[80:83], v0 offset:24576\nv_smfmac_f32_16x16x64_bf16 v[48:51], v[84:87], a[136:143], v4\ns_waitcnt lgkmcnt(6)\nv_smfmac_f32_16x16x64_bf16 v[52:55], v[84:87], v[72:79], v4\nds_read_b128 v[72:75], v1 offset:10240\n" \
"ds_read_b128 v[76:79], v1 offset:11264\nv_smfmac_f32_16x16x64_bf16 v[56:59], v[84:87], a[144:151], v4\nv_smfmac_f32_16x16x64_bf16 v[60:63], v[84:87], a[152:159], v4\ns_waitcnt lgkmcnt(7)\nds_read_b128 v[84:87], v0 offset:25600\nv_smfmac_f32_16x16x64_bf16 v[48:51], v[88:91], a[160:167], v4\ns_waitcnt lgkmcnt(7)\ns_barrier\n" \
"v_smfmac_f32_16x16x64_bf16 v[52:55], v[88:91], a[168:175], v4\ns_waitcnt lgkmcnt(4)\nv_smfmac_f32_16x16x64_bf16 v[56:59], v[88:91], v[64:71], v4\nds_read_b128 v[64:67], v1 offset:12288\nds_read_b128 v[68:71], v1 offset:13312\nv_smfmac_f32_16x16x64_bf16 v[60:63], v[88:91], a[176:183], v4\nds_read_b128 v[88:91], v0 offset:26624\nv_smfmac_f32_16x16x64_bf16 v[48:51], v[92:95], a[184:191], v4\n" \
"ds_read_b128 v[16:19], v7 offset:0\nv_smfmac_f32_16x16x64_bf16 v[52:55], v[92:95], a[192:199], v4\nv_smfmac_f32_16x16x64_bf16 v[56:59], v[92:95], a[200:207], v4\ns_waitcnt lgkmcnt(5)\nds_read_b128 v[20:23], v7 offset:64\nv_smfmac_f32_16x16x64_bf16 v[60:63], v[92:95], v[72:79], v4\nds_read_b128 v[92:95], v0 offset:27648\nds_read_b128 v[72:75], v1 offset:14336\n" \
"ds_read_b128 v[76:79], v1 offset:15360\nv_smfmac_f32_16x16x64_bf16 v[48:51], v[80:83], a[208:215], v4\nds_read_b128 v[24:27], v7 offset:2048\nv_smfmac_f32_16x16x64_bf16 v[52:55], v[80:83], a[216:223], v4\nv_smfmac_f32_16x16x64_bf16 v[56:59], v[80:83], a[224:231], v4\nds_read_b128 v[28:31], v7 offset:2112\nv_smfmac_f32_16x16x64_bf16 v[60:63], v[80:83], a[232:239], v4\ns_waitcnt lgkmcnt(10)\n" \
"ds_read_b128 v[80:83], v0 offset:28672\nv_smfmac_f32_16x16x64_bf16 v[48:51], v[84:87], a[240:247], v4\ns_waitcnt lgkmcnt(9)\nds_read_b128 v[32:35], v7 offset:4096\nv_smfmac_f32_16x16x64_bf16 v[52:55], v[84:87], v[64:71], v4\nds_read_b128 v[64:67], v1 offset:16384\nds_read_b128 v[68:71], v1 offset:17408\nv_smfmac_f32_16x16x64_bf16 v[56:59], v[84:87], a[248:255], v4\n" \
"ds_read_b128 v[36:39], v7 offset:4160\nv_smfmac_f32_16x16x64_bf16 v[60:63], v[84:87], v[96:103], v4\ns_waitcnt lgkmcnt(12)\nds_read_b128 v[84:87], v0 offset:29696\nv_smfmac_f32_16x16x64_bf16 v[48:51], v[88:91], v[104:111], v4\nds_read_b128 v[40:43], v7 offset:6144\nv_smfmac_f32_16x16x64_bf16 v[52:55], v[88:91], v[112:119], v4\ns_waitcnt lgkmcnt(9)\n" \
"v_smfmac_f32_16x16x64_bf16 v[56:59], v[88:91], v[72:79], v4\nds_read_b128 v[72:75], v1 offset:18432\nds_read_b128 v[76:79], v1 offset:19456\nv_smfmac_f32_16x16x64_bf16 v[60:63], v[88:91], v[120:127], v4\nds_read_b128 v[88:91], v0 offset:30720\nv_smfmac_f32_16x16x64_bf16 v[48:51], v[92:95], v[128:135], v4\nds_read_b128 v[44:47], v7 offset:6208\nv_smfmac_f32_16x16x64_bf16 v[52:55], v[92:95], v[136:143], v4\n" \
"v_smfmac_f32_16x16x64_bf16 v[56:59], v[92:95], v[144:151], v4\ns_waitcnt lgkmcnt(7)\nv_smfmac_f32_16x16x64_bf16 v[60:63], v[92:95], v[64:71], v4\nds_read_b128 v[92:95], v0 offset:31744\nds_read_b128 v[64:67], v1 offset:20480\nds_read_b128 v[68:71], v1 offset:21504\nv_smfmac_f32_16x16x64_bf16 v[48:51], v[80:83], v[152:159], v4\nv_smfmac_f32_16x16x64_bf16 v[52:55], v[80:83], v[160:167], v4\n" \
"v_smfmac_f32_16x16x64_bf16 v[56:59], v[80:83], v[168:175], v4\nv_smfmac_f32_16x16x64_bf16 v[60:63], v[80:83], v[176:183], v4\ns_waitcnt lgkmcnt(8)\nv_smfmac_f32_16x16x64_bf16 v[48:51], v[84:87], v[184:191], v4\ns_waitcnt lgkmcnt(5)\nv_smfmac_f32_16x16x64_bf16 v[52:55], v[84:87], v[72:79], v4\nds_read_b128 v[72:75], v1 offset:22528\nds_read_b128 v[76:79], v1 offset:23552\n" \
"v_smfmac_f32_16x16x64_bf16 v[56:59], v[84:87], v[192:199], v4\nv_smfmac_f32_16x16x64_bf16 v[60:63], v[84:87], v[200:207], v4\ns_waitcnt lgkmcnt(6)\nv_smfmac_f32_16x16x64_bf16 v[48:51], v[88:91], v[208:215], v4\nv_smfmac_f32_16x16x64_bf16 v[52:55], v[88:91], v[216:223], v4\ns_waitcnt lgkmcnt(4)\nv_smfmac_f32_16x16x64_bf16 v[48:51], v[92:95], v[232:239], v4\nv_smfmac_f32_16x16x64_bf16 v[52:55], v[92:95], v[240:247], v4\n" \
"s_waitcnt lgkmcnt(2)\nv_smfmac_f32_16x16x64_bf16 v[56:59], v[88:91], v[64:71], v4\nv_add_f32 v48, v48, v50\nv_smfmac_f32_16x16x64_bf16 v[60:63], v[88:91], v[224:231], v4\nv_add_f32 v52, v52, v54\nv_add_f32 v49, v49, v51\nv_smfmac_f32_16x16x64_bf16 v[56:59], v[92:95], v[248:255], v4\ns_waitcnt lgkmcnt(0)\n" \
"v_add_f32 v53, v53, v55\nv_cvt_pk_bf16_f32 v50, v48, v52\nv_smfmac_f32_16x16x64_bf16 v[60:63], v[92:95], v[72:79], v4\nv_cvt_pk_bf16_f32 v51, v49, v53\nds_write2_b32 v6, v50, v51 offset0:0 offset1:4\ns_nop 1\nv_add_f32 v56, v56, v58\nv_add_f32 v57, v57, v59\n" \
"s_nop 1\nv_add_f32 v60, v60, v62\nv_add_f32 v61, v61, v63\nv_cvt_pk_bf16_f32 v62, v56, v60\nv_cvt_pk_bf16_f32 v63, v57, v61\nds_write2_b32 v6, v62, v63 offset0:36 offset1:40\nv_mov_b32 v61, s54\ns_waitcnt lgkmcnt(0)\n" \
"s_barrier\nds_read_b128 v[80:83], v8 offset:0\nds_read_b128 v[84:87], v8 offset:576\nds_read_b128 v[88:91], v8 offset:1152\nds_read_b128 v[92:95], v8 offset:1728\nds_read_b128 v[64:67], v9 offset:0\nds_read_b128 v[68:71], v9 offset:576\nds_read_b128 v[72:75], v9 offset:1152\n" \
"ds_read_b128 v[76:79], v9 offset:1728\nds_read_b32 v60, v0 offset:53760\ns_waitcnt lgkmcnt(8)\nv_mfma_f32_16x16x32_bf16 v[52:55], v[16:19], v[80:83], 0\nv_mfma_f32_16x16x32_bf16 v[56:59], v[20:23], v[80:83], 0\ns_waitcnt lgkmcnt(7)\nv_mfma_f32_16x16x32_bf16 v[52:55], v[24:27], v[84:87], v[52:55]\nv_mfma_f32_16x16x32_bf16 v[56:59], v[28:31], v[84:87], v[56:59]\n" \
"s_waitcnt lgkmcnt(6)\nv_mfma_f32_16x16x32_bf16 v[52:55], v[32:35], v[88:91], v[52:55]\nv_mfma_f32_16x16x32_bf16 v[56:59], v[36:39], v[88:91], v[56:59]\ns_waitcnt lgkmcnt(5)\nv_mfma_f32_16x16x32_bf16 v[52:55], v[40:43], v[92:95], v[52:55]\nv_mfma_f32_16x16x32_bf16 v[56:59], v[44:47], v[92:95], v[56:59]\ns_waitcnt lgkmcnt(4)\nv_mfma_f32_16x16x32_bf16 v[48:51], v[64:67], v[80:83], 0\n" \
"s_waitcnt lgkmcnt(3)\nv_mfma_f32_16x16x32_bf16 v[48:51], v[68:71], v[84:87], v[48:51]\ns_waitcnt lgkmcnt(2)\nds_read_b128 v[80:83], v2 offset:0\nv_mfma_f32_16x16x32_bf16 v[48:51], v[72:75], v[88:91], v[48:51]\ns_waitcnt lgkmcnt(2)\nds_read_b128 v[84:87], v2 offset:256\nv_mfma_f32_16x16x32_bf16 v[48:51], v[76:79], v[92:95], v[48:51]\n" \
"ds_read_b128 v[88:91], v2 offset:2048\nv_max3_f32 v62, v52, v53, v54\nv_max3_f32 v63, v55, v56, v57\nv_max3_f32 v62, v62, v58, v59\nv_cndmask_b32 v52, v52, v56, s[62:63]\nv_cndmask_b32 v53, v53, v57, s[62:63]\nv_cndmask_b32 v54, v54, v58, s[62:63]\nv_cndmask_b32 v55, v55, v59, s[62:63]\n" \
"v_max_f32 v62, v62, v63\nv_max3_f32 v63, v62, v48, v49\nv_max3_f32 v62, v62, v48, v49\ns_waitcnt lgkmcnt(2)\nv_mfma_f32_16x16x32_bf16 v[16:19], a[0:3], v[80:83], 0\nds_read_b128 v[92:95], v2 offset:2304\nv_permlane16_swap_b32 v62, v63\nv_mfma_f32_16x16x32_bf16 v[20:23], a[8:11], v[80:83], 0\n" \
"ds_read_b128 v[72:75], v1 offset:0\nv_max_f32 v50, v62, v63\nv_max_f32 v62, v62, v63\nv_mfma_f32_16x16x32_bf16 v[24:27], a[16:19], v[80:83], 0\ns_nop 0\nv_permlane32_swap_b32 v62, v50\nv_max_f32 v62, v62, v50\nv_mfma_f32_16x16x32_bf16 v[28:31], a[24:27], v[80:83], 0\n" \
"s_waitcnt lgkmcnt(3)\nv_mul_f32 v62, s59, v62\nv_fma_f32 v52, v52, s58, v62\nv_fma_f32 v53, v53, s58, v62\nv_mfma_f32_16x16x32_bf16 v[32:35], a[0:3], v[84:87], 0\nds_read_b128 v[80:83], v2 offset:4096\nv_exp_f32 v52, v52\nv_mfma_f32_16x16x32_bf16 v[36:39], a[8:11], v[84:87], 0\n" \
"v_fma_f32 v54, v54, s58, v62\nv_exp_f32 v53, v53\nv_mfma_f32_16x16x32_bf16 v[40:43], a[16:19], v[84:87], 0\nv_fma_f32 v55, v55, s58, v62\nv_exp_f32 v54, v54\nv_mfma_f32_16x16x32_bf16 v[44:47], a[24:27], v[84:87], 0\ns_waitcnt lgkmcnt(3)\nv_add_f32 v63, v52, v53\n" \
"v_fma_f32 v48, v48, s58, v62\nv_add_f32 v63, v63, v54\nv_mfma_f32_16x16x32_bf16 v[16:19], a[4:7], v[88:91], v[16:19]\nds_read_b128 v[84:87], v2 offset:4352\nv_exp_f32 v55, v55\nv_mfma_f32_16x16x32_bf16 v[20:23], a[12:15], v[88:91], v[20:23]\nds_read_b128 v[76:79], v1 offset:1024\nv_fma_f32 v49, v49, s58, v62\n" \
"v_add_f32 v63, v63, v55\nv_mfma_f32_16x16x32_bf16 v[24:27], a[20:23], v[88:91], v[24:27]\nv_exp_f32 v48, v48\nv_mfma_f32_16x16x32_bf16 v[28:31], a[28:31], v[88:91], v[28:31]\ns_waitcnt lgkmcnt(4)\nv_exp_f32 v49, v49\nv_add_f32 v50, v48, v63\nv_mfma_f32_16x16x32_bf16 v[32:35], a[4:7], v[92:95], v[32:35]\n" \
"ds_read_b128 v[88:91], v2 offset:6144\nv_add_f32 v50, v50, v49\nv_mfma_f32_16x16x32_bf16 v[36:39], a[12:15], v[92:95], v[36:39]\ns_nop 0\nv_add_f32_dpp v62, v63, v50 row_ror:8 row_mask:0xf bank_mask:0xf\nv_add_f32_dpp v50, v63, v50 row_ror:8 row_mask:0xf bank_mask:0xf\nv_mfma_f32_16x16x32_bf16 v[40:43], a[20:23], v[92:95], v[40:43]\ns_nop 0\n" \
"v_permlane16_swap_b32 v62, v50\nv_add_f32 v63, v62, v50\nv_mfma_f32_16x16x32_bf16 v[44:47], a[28:31], v[92:95], v[44:47]\ns_waitcnt lgkmcnt(3)\nv_add_f32 v50, v62, v50\nv_mfma_f32_16x16x32_bf16 v[16:19], a[32:35], v[80:83], v[16:19]\nds_read_b128 v[92:95], v2 offset:6400\nv_permlane32_swap_b32 v63, v50\n" \
"v_mfma_f32_16x16x32_bf16 v[20:23], v[72:75], v[80:83], v[20:23]\nds_read_b128 v[64:67], v1 offset:2048\nv_add_f32 v63, v63, v50\nv_mfma_f32_16x16x32_bf16 v[24:27], a[40:43], v[80:83], v[24:27]\nv_rcp_f32 v63, v63\nv_mfma_f32_16x16x32_bf16 v[28:31], a[48:51], v[80:83], v[28:31]\ns_waitcnt lgkmcnt(4)\nv_mul_f32 v52, v52, v63\n" \
"v_mul_f32 v53, v53, v63\nv_mul_f32 v54, v54, v63\nv_mfma_f32_16x16x32_bf16 v[32:35], a[32:35], v[84:87], v[32:35]\nds_read_b128 v[80:83], v2 offset:8192\nv_mul_f32 v55, v55, v63\nv_mul_f32 v48, v48, v63\nv_mfma_f32_16x16x32_bf16 v[36:39], v[72:75], v[84:87], v[36:39]\nv_mul_f32 v49, v49, v63\n" \
"v_cvt_pk_bf16_f32 v52, v52, v53\nv_cvt_pk_bf16_f32 v53, v54, v55\nv_mfma_f32_16x16x32_bf16 v[40:43], a[40:43], v[84:87], v[40:43]\nv_cvt_pk_bf16_f32 v48, v48, v49\nv_bfi_b32 v52, s61, v52, v61\nv_bfi_b32 v53, s61, v53, v61\nv_mfma_f32_16x16x32_bf16 v[44:47], a[48:51], v[84:87], v[44:47]\ns_waitcnt lgkmcnt(3)\n" \
"v_bfi_b32 v48, s61, v48, v61\nv_mfma_f32_16x16x32_bf16 v[16:19], a[36:39], v[88:91], v[16:19]\nds_read_b128 v[84:87], v2 offset:8448\nv_mfma_f32_16x16x32_bf16 v[20:23], v[76:79], v[88:91], v[20:23]\nds_read_b128 v[68:71], v1 offset:3072\nv_mfma_f32_16x16x32_bf16 v[24:27], a[44:47], v[88:91], v[24:27]\nbuffer_store_dwordx2 v[52:53], v10, s[36:39], 0 offen sc1\nv_mfma_f32_16x16x32_bf16 v[28:31], a[52:55], v[88:91], v[28:31]\n" \
"s_memrealtime s[86:87]\ns_waitcnt lgkmcnt(4)\nbuffer_store_dword v48, v11, s[36:39], 0 offen sc1\nv_cmp_ne_u32 vcc, 0, v60\nv_mfma_f32_16x16x32_bf16 v[32:35], a[36:39], v[92:95], v[32:35]\nds_read_b128 v[88:91], v2 offset:10240\nv_mfma_f32_16x16x32_bf16 v[36:39], v[76:79], v[92:95], v[36:39]\nv_mfma_f32_16x16x32_bf16 v[40:43], a[44:47], v[92:95], v[40:43]\n" \
"v_mfma_f32_16x16x32_bf16 v[44:47], a[52:55], v[92:95], v[44:47]\ns_waitcnt lgkmcnt(3)\nv_mfma_f32_16x16x32_bf16 v[16:19], a[56:59], v[80:83], v[16:19]\nds_read_b128 v[92:95], v2 offset:10496\nv_mfma_f32_16x16x32_bf16 v[20:23], a[64:67], v[80:83], v[20:23]\nds_read_b128 v[72:75], v1 offset:4096\nv_mfma_f32_16x16x32_bf16 v[24:27], v[64:67], v[80:83], v[24:27]\nv_mfma_f32_16x16x32_bf16 v[28:31], a[72:75], v[80:83], v[28:31]\n" \
"s_waitcnt lgkmcnt(4)\nv_mfma_f32_16x16x32_bf16 v[32:35], a[56:59], v[84:87], v[32:35]\nds_read_b128 v[80:83], v2 offset:12288\nv_mfma_f32_16x16x32_bf16 v[36:39], a[64:67], v[84:87], v[36:39]\nv_mfma_f32_16x16x32_bf16 v[40:43], v[64:67], v[84:87], v[40:43]\nv_mfma_f32_16x16x32_bf16 v[44:47], a[72:75], v[84:87], v[44:47]\ns_waitcnt lgkmcnt(3)\nv_mfma_f32_16x16x32_bf16 v[16:19], a[60:63], v[88:91], v[16:19]\n" \
"ds_read_b128 v[84:87], v2 offset:12544\nv_mfma_f32_16x16x32_bf16 v[20:23], a[68:71], v[88:91], v[20:23]\nds_read_b128 v[76:79], v1 offset:5120\nv_mfma_f32_16x16x32_bf16 v[24:27], v[68:71], v[88:91], v[24:27]\nv_mfma_f32_16x16x32_bf16 v[28:31], a[76:79], v[88:91], v[28:31]\ns_waitcnt lgkmcnt(4)\nv_mfma_f32_16x16x32_bf16 v[32:35], a[60:63], v[92:95], v[32:35]\nds_read_b128 v[88:91], v2 offset:14336\n" \
"v_mfma_f32_16x16x32_bf16 v[36:39], a[68:71], v[92:95], v[36:39]\nv_mfma_f32_16x16x32_bf16 v[40:43], v[68:71], v[92:95], v[40:43]\nv_mfma_f32_16x16x32_bf16 v[44:47], a[76:79], v[92:95], v[44:47]\ns_cbranch_vccnz .Lqk_pj%=\ns_cmp_lt_i32 s40, 63\ns_waitcnt lgkmcnt(0)\ns_cbranch_scc1 .Lqk_pwait%=\ns_branch .Lqk_end%=\n" \
".Lqk_re0%=:\nv_max_u32 v17, v80, v82\nv_cmp_eq_u32 vcc, -1, v17\ns_cbranch_vccz .Lqk_sk1614%=\ns_mov_b64 exec, vcc\nbuffer_load_dwordx4 v[80:83], v3, s[36:39], s42 offen offset:0 sc1\ns_mov_b64 exec, -1\n.Lqk_sk1614%=:\n" \
"v_max_u32 v17, v84, v86\nv_cmp_eq_u32 vcc, -1, v17\ns_cbranch_vccz .Lqk_sk1621%=\ns_mov_b64 exec, vcc\nbuffer_load_dwordx4 v[84:87], v3, s[36:39], s42 offen offset:1024 sc1\ns_mov_b64 exec, -1\n.Lqk_sk1621%=:\nv_max_u32 v17, v88, v90\n" \
"v_cmp_eq_u32 vcc, -1, v17\ns_cbranch_vccz .Lqk_sk1628%=\ns_mov_b64 exec, vcc\nbuffer_load_dwordx4 v[88:91], v3, s[36:39], s42 offen offset:2048 sc1\ns_mov_b64 exec, -1\n.Lqk_sk1628%=:\nv_max_u32 v17, v92, v94\nv_cmp_eq_u32 vcc, -1, v17\n" \
"s_cbranch_vccz .Lqk_sk1635%=\ns_mov_b64 exec, vcc\nbuffer_load_dwordx4 v[92:95], v3, s[36:39], s42 offen offset:3072 sc1\ns_mov_b64 exec, -1\n.Lqk_sk1635%=:\nv_mov_b64 v[48:49], 0\nv_mov_b64 v[50:51], 0\nv_mov_b64 v[52:53], 0\n" \
"v_mov_b64 v[54:55], 0\nv_mov_b64 v[56:57], 0\nv_mov_b64 v[58:59], 0\nv_mov_b64 v[60:61], 0\nv_mov_b64 v[62:63], 0\ns_add_u32 s43, s43, 1\ns_cmp_lt_u32 s43, 262144\ns_cbranch_scc1 .Lqk_poll0%=\n" \
"s_branch .Lqk_abort%=\n.Lqk_c1%=:\ns_waitcnt lgkmcnt(3)\nv_mfma_f32_16x16x32_bf16 v[32:35], v[232:235], v[56:59], v[32:35]\nv_mov_b64 v[52:53], 0\nv_mov_b64 v[54:55], 0\nv_mfma_f32_16x16x32_bf16 v[36:39], v[240:243], v[56:59], v[36:39]\ns_waitcnt lgkmcnt(2)\n" \
"ds_read_b128 v[64:67], v1 offset:6144\nv_mfma_f32_16x16x32_bf16 v[32:35], v[236:239], v[60:63], v[32:35]\nds_read_b128 v[68:71], v1 offset:7168\nv_mfma_f32_16x16x32_bf16 v[36:39], v[244:247], v[60:63], v[36:39]\ns_bfe_i32 s54, s40, 0x10001\nv_mfma_f32_16x16x32_bf16 v[40:43], v[248:251], v[56:59], v[40:43]\nv_mfma_f32_16x16x32_bf16 v[44:47], v[72:75], v[56:59], v[44:47]\nv_cvt_pk_bf16_f32 v36, v32, v36\n" \
"v_cvt_pk_bf16_f32 v37, v33, v37\nv_mfma_f32_16x16x32_bf16 v[40:43], v[252:255], v[60:63], v[40:43]\nv_cvt_pk_bf16_f32 v38, v34, v38\nv_cvt_pk_bf16_f32 v39, v35, v39\nv_mfma_f32_16x16x32_bf16 v[44:47], v[76:79], v[60:63], v[44:47]\nds_write_b128 v5, v[36:39] offset:256\nv_mov_b64 v[56:57], 0\nv_mov_b64 v[58:59], 0\n" \
"v_mov_b64 v[60:61], 0\nv_mov_b64 v[62:63], 0\n.Lqk_poll1%=:\ns_waitcnt vmcnt(0)\nv_smfmac_f32_16x16x64_bf16 v[48:51], v[80:83], a[104:111], v4\nds_write_b128 v0, v[80:83] offset:20480\nv_max3_u32 v16, v80, v82, v84\nv_smfmac_f32_16x16x64_bf16 v[52:55], v[80:83], a[112:119], v4\n" \
"ds_read_b128 v[72:75], v1 offset:8192\nds_read_b128 v[76:79], v1 offset:9216\nv_max3_u32 v16, v16, v86, v88\nv_smfmac_f32_16x16x64_bf16 v[56:59], v[80:83], a[120:127], v4\nds_write_b128 v0, v[84:87] offset:21504\nv_max3_u32 v16, v16, v90, v92\nv_max_u32 v16, v16, v94\nv_smfmac_f32_16x16x64_bf16 v[60:63], v[80:83], a[128:135], v4\n" \
"v_cmp_eq_u32 vcc, -1, v16\nv_smfmac_f32_16x16x64_bf16 v[48:51], v[84:87], a[136:143], v4\nds_write_b128 v0, v[88:91] offset:22528\ns_cbranch_vccnz .Lqk_re1%=\ns_waitcnt lgkmcnt(6)\nv_smfmac_f32_16x16x64_bf16 v[52:55], v[84:87], v[64:71], v4\nds_read_b128 v[64:67], v1 offset:10240\nds_read_b128 v[68:71], v1 offset:11264\n" \
"v_smfmac_f32_16x16x64_bf16 v[56:59], v[84:87], a[144:151], v4\nds_write_b128 v0, v[92:95] offset:23552\nv_smfmac_f32_16x16x64_bf16 v[60:63], v[84:87], a[152:159], v4\nv_smfmac_f32_16x16x64_bf16 v[48:51], v[88:91], a[160:167], v4\nv_smfmac_f32_16x16x64_bf16 v[52:55], v[88:91], a[168:175], v4\ns_waitcnt lgkmcnt(0)\ns_barrier\nds_read_b128 v[80:83], v0 offset:24576\n" \
"ds_read_b128 v[84:87], v0 offset:25600\nv_smfmac_f32_16x16x64_bf16 v[56:59], v[88:91], v[72:79], v4\nds_read_b128 v[72:75], v1 offset:12288\nds_read_b128 v[76:79], v1 offset:13312\nv_cvt_pk_bf16_f32 v44, v40, v44\nv_smfmac_f32_16x16x64_bf16 v[60:63], v[88:91], a[176:183], v4\nds_read_b128 v[88:91], v0 offset:26624\nv_cvt_pk_bf16_f32 v45, v41, v45\n" \
"v_smfmac_f32_16x16x64_bf16 v[48:51], v[92:95], a[184:191], v4\nv_cvt_pk_bf16_f32 v46, v42, v46\nv_smfmac_f32_16x16x64_bf16 v[52:55], v[92:95], a[192:199], v4\nv_cvt_pk_bf16_f32 v47, v43, v47\nv_smfmac_f32_16x16x64_bf16 v[56:59], v[92:95], a[200:207], v4\nv_smfmac_f32_16x16x64_bf16 v[60:63], v[92:95], v[64:71], v4\ns_waitcnt lgkmcnt(4)\nds_write_b128 v5, v[44:47] offset:768\n" \
"ds_read_b128 v[92:95], v0 offset:27648\nds_read_b128 v[64:67], v1 offset:14336\nds_read_b128 v[68:71], v1 offset:15360\nv_smfmac_f32_16x16x64_bf16 v[48:51], v[80:83], a[208:215], v4\nv_smfmac_f32_16x16x64_bf16 v[52:55], v[80:83], a[216:223], v4\nv_smfmac_f32_16x16x64_bf16 v[56:59], v[80:83], a[224:231], v4\nv_smfmac_f32_16x16x64_bf16 v[60:63], v[80:83], a[232:239], v4\ns_waitcnt lgkmcnt(7)\n" \
"ds_read_b128 v[80:83], v0 offset:28672\nv_smfmac_f32_16x16x64_bf16 v[48:51], v[84:87], a[240:247], v4\ns_waitcnt lgkmcnt(6)\nv_smfmac_f32_16x16x64_bf16 v[52:55], v[84:87], v[72:79], v4\nds_read_b128 v[72:75], v1 offset:16384\nds_read_b128 v[76:79], v1 offset:17408\nv_smfmac_f32_16x16x64_bf16 v[56:59], v[84:87], a[248:255], v4\nv_smfmac_f32_16x16x64_bf16 v[60:63], v[84:87], v[96:103], v4\n" \
"s_waitcnt lgkmcnt(7)\nds_read_b128 v[84:87], v0 offset:29696\nv_smfmac_f32_16x16x64_bf16 v[48:51], v[88:91], v[104:111], v4\ns_waitcnt lgkmcnt(7)\ns_barrier\nv_smfmac_f32_16x16x64_bf16 v[52:55], v[88:91], v[112:119], v4\ns_waitcnt lgkmcnt(4)\nv_smfmac_f32_16x16x64_bf16 v[56:59], v[88:91], v[64:71], v4\n" \
"ds_read_b128 v[64:67], v1 offset:18432\nds_read_b128 v[68:71], v1 offset:19456\nv_smfmac_f32_16x16x64_bf16 v[60:63], v[88:91], v[120:127], v4\nds_read_b128 v[88:91], v0 offset:30720\nv_smfmac_f32_16x16x64_bf16 v[48:51], v[92:95], v[128:135], v4\nds_read_b128 v[16:19], v7 offset:0\nv_smfmac_f32_16x16x64_bf16 v[52:55], v[92:95], v[136:143], v4\nv_smfmac_f32_16x16x64_bf16 v[56:59], v[92:95], v[144:151], v4\n" \
"s_waitcnt lgkmcnt(5)\nds_read_b128 v[20:23], v7 offset:2048\nv_smfmac_f32_16x16x64_bf16 v[60:63], v[92:95], v[72:79], v4\nds_read_b128 v[92:95], v0 offset:31744\nds_read_b128 v[72:75], v1 offset:20480\nds_read_b128 v[76:79], v1 offset:21504\nv_smfmac_f32_16x16x64_bf16 v[48:51], v[80:83], v[152:159], v4\nds_read_b128 v[24:27], v7 offset:4096\n" \
"v_smfmac_f32_16x16x64_bf16 v[52:55], v[80:83], v[160:167], v4\nv_smfmac_f32_16x16x64_bf16 v[56:59], v[80:83], v[168:175], v4\nds_read_b128 v[28:31], v7 offset:6144\nv_smfmac_f32_16x16x64_bf16 v[60:63], v[80:83], v[176:183], v4\ns_waitcnt lgkmcnt(10)\nds_read_b128 v[80:83], v0 offset:16384\nv_smfmac_f32_16x16x64_bf16 v[48:51], v[84:87], v[184:191], v4\ns_waitcnt lgkmcnt(9)\n" \
"ds_read_b128 v[32:35], v7 offset:8192\nv_smfmac_f32_16x16x64_bf16 v[52:55], v[84:87], v[64:71], v4\nds_read_b128 v[64:67], v1 offset:22528\nds_read_b128 v[68:71], v1 offset:23552\nv_smfmac_f32_16x16x64_bf16 v[56:59], v[84:87], v[192:199], v4\nds_read_b128 v[36:39], v7 offset:10240\nv_smfmac_f32_16x16x64_bf16 v[60:63], v[84:87], v[200:207], v4\ns_waitcnt lgkmcnt(12)\n" \
"ds_read_b128 v[84:87], v0 offset:17408\nv_smfmac_f32_16x16x64_bf16 v[48:51], v[88:91], v[208:215], v4\nds_read_b128 v[40:43], v7 offset:12288\nv_smfmac_f32_16x16x64_bf16 v[52:55], v[88:91], v[216:223], v4\ns_waitcnt lgkmcnt(9)\nv_smfmac_f32_16x16x64_bf16 v[56:59], v[88:91], v[72:79], v4\nds_read_b128 v[72:75], v1 offset:0\nds_read_b128 v[76:79], v1 offset:1024\n" \
"v_smfmac_f32_16x16x64_bf16 v[60:63], v[88:91], v[224:231], v4\nds_read_b128 v[88:91], v0 offset:18432\nv_smfmac_f32_16x16x64_bf16 v[48:51], v[92:95], v[232:239], v4\nds_read_b128 v[44:47], v7 offset:14336\nv_smfmac_f32_16x16x64_bf16 v[52:55], v[92:95], v[240:247], v4\nv_smfmac_f32_16x16x64_bf16 v[56:59], v[92:95], v[248:255], v4\ns_waitcnt lgkmcnt(7)\nv_smfmac_f32_16x16x64_bf16 v[60:63], v[92:95], v[64:71], v4\n" \
"ds_read_b128 v[92:95], v0 offset:19456\nds_read_b128 v[64:67], v1 offset:2048\nds_read_b128 v[68:71], v1 offset:3072\nv_smfmac_f32_16x16x64_bf16 v[48:51], v[80:83], a[0:7], v4\nv_smfmac_f32_16x16x64_bf16 v[52:55], v[80:83], a[8:15], v4\nv_smfmac_f32_16x16x64_bf16 v[56:59], v[80:83], a[16:23], v4\nv_smfmac_f32_16x16x64_bf16 v[60:63], v[80:83], a[24:31], v4\ns_waitcnt lgkmcnt(8)\n" \
"v_smfmac_f32_16x16x64_bf16 v[48:51], v[84:87], a[32:39], v4\ns_waitcnt lgkmcnt(5)\nv_smfmac_f32_16x16x64_bf16 v[52:55], v[84:87], v[72:79], v4\nds_read_b128 v[72:75], v1 offset:4096\nds_read_b128 v[76:79], v1 offset:5120\nv_smfmac_f32_16x16x64_bf16 v[56:59], v[84:87], a[40:47], v4\nv_smfmac_f32_16x16x64_bf16 v[60:63], v[84:87], a[48:55], v4\ns_waitcnt lgkmcnt(6)\n" \
"v_smfmac_f32_16x16x64_bf16 v[48:51], v[88:91], a[56:63], v4\nv_smfmac_f32_16x16x64_bf16 v[52:55], v[88:91], a[64:71], v4\ns_waitcnt lgkmcnt(4)\nv_smfmac_f32_16x16x64_bf16 v[48:51], v[92:95], a[80:87], v4\nv_smfmac_f32_16x16x64_bf16 v[52:55], v[92:95], a[88:95], v4\ns_waitcnt lgkmcnt(2)\nv_smfmac_f32_16x16x64_bf16 v[56:59], v[88:91], v[64:71], v4\nv_add_f32 v48, v48, v50\n" \
"v_smfmac_f32_16x16x64_bf16 v[60:63], v[88:91], a[72:79], v4\nv_add_f32 v52, v52, v54\nv_add_f32 v49, v49, v51\nv_smfmac_f32_16x16x64_bf16 v[56:59], v[92:95], a[96:103], v4\ns_waitcnt lgkmcnt(0)\nv_add_f32 v53, v53, v55\nv_cvt_pk_bf16_f32 v50, v48, v52\nv_smfmac_f32_16x16x64_bf16 v[60:63], v[92:95], v[72:79], v4\n" \
"v_cvt_pk_bf16_f32 v51, v49, v53\nds_write2_b32 v6, v50, v51 offset0:0 offset1:4\ns_nop 1\nv_add_f32 v56, v56, v58\nv_add_f32 v57, v57, v59\ns_nop 1\nv_add_f32 v60, v60, v62\nv_add_f32 v61, v61, v63\n" \
"v_cvt_pk_bf16_f32 v62, v56, v60\nv_cvt_pk_bf16_f32 v63, v57, v61\nds_write2_b32 v6, v62, v63 offset0:36 offset1:40\nv_mov_b32 v61, s54\nv_mfma_f32_16x16x32_bf16 v[52:55], v[32:35], v[16:19], v[12:15]\nv_mfma_f32_16x16x32_bf16 v[52:55], v[36:39], v[20:23], v[52:55]\nv_mfma_f32_16x16x32_bf16 v[52:55], v[40:43], v[24:27], v[52:55]\nv_mfma_f32_16x16x32_bf16 v[52:55], v[44:47], v[28:31], v[52:55]\n" \
"s_waitcnt lgkmcnt(0)\ns_barrier\nds_read_b128 v[32:35], v9 offset:0\nds_read_b128 v[36:39], v9 offset:576\nds_read_b128 v[40:43], v9 offset:1152\nds_read_b128 v[44:47], v9 offset:1728\nds_read_b32 v60, v0 offset:53760\nds_read_b128 v[80:83], v2 offset:0\n" \
"ds_read_b128 v[84:87], v2 offset:256\nds_read_b128 v[88:91], v2 offset:2048\ns_waitcnt lgkmcnt(7)\nv_mfma_f32_16x16x32_bf16 v[48:51], v[32:35], v[16:19], 0\ns_waitcnt lgkmcnt(6)\nv_mfma_f32_16x16x32_bf16 v[48:51], v[36:39], v[20:23], v[48:51]\ns_waitcnt lgkmcnt(5)\nv_mfma_f32_16x16x32_bf16 v[48:51], v[40:43], v[24:27], v[48:51]\n" \
"s_waitcnt lgkmcnt(4)\nv_mfma_f32_16x16x32_bf16 v[48:51], v[44:47], v[28:31], v[48:51]\ns_waitcnt lgkmcnt(2)\nv_mfma_f32_16x16x32_bf16 v[16:19], a[0:3], v[80:83], 0\nds_read_b128 v[92:95], v2 offset:2304\nv_max3_f32 v62, v52, v53, v54\nv_mfma_f32_16x16x32_bf16 v[20:23], a[8:11], v[80:83], 0\nds_read_b128 v[72:75], v1 offset:0\n" \
"v_max_f32 v62, v62, v55\nv_mfma_f32_16x16x32_bf16 v[24:27], a[16:19], v[80:83], 0\nv_max3_f32 v63, v62, v48, v49\nv_max3_f32 v62, v62, v48, v49\nv_mfma_f32_16x16x32_bf16 v[28:31], a[24:27], v[80:83], 0\ns_waitcnt lgkmcnt(3)\nv_permlane16_swap_b32 v62, v63\nv_mfma_f32_16x16x32_bf16 v[32:35], a[0:3], v[84:87], 0\n" \
"ds_read_b128 v[80:83], v2 offset:4096\nv_max_f32 v50, v62, v63\nv_mfma_f32_16x16x32_bf16 v[36:39], a[8:11], v[84:87], 0\nv_max_f32 v62, v62, v63\nv_mfma_f32_16x16x32_bf16 v[40:43], a[16:19], v[84:87], 0\ns_nop 0\nv_permlane32_swap_b32 v62, v50\nv_mfma_f32_16x16x32_bf16 v[44:47], a[24:27], v[84:87], 0\n" \
"s_waitcnt lgkmcnt(3)\nv_max_f32 v62, v62, v50\nv_mul_f32 v62, s59, v62\nv_mfma_f32_16x16x32_bf16 v[16:19], a[4:7], v[88:91], v[16:19]\nds_read_b128 v[84:87], v2 offset:4352\nv_fma_f32 v52, v52, s58, v62\nv_mfma_f32_16x16x32_bf16 v[20:23], a[12:15], v[88:91], v[20:23]\nds_read_b128 v[76:79], v1 offset:1024\n" \
"v_fma_f32 v53, v53, s58, v62\nv_mfma_f32_16x16x32_bf16 v[24:27], a[20:23], v[88:91], v[24:27]\nv_exp_f32 v52, v52\nv_mfma_f32_16x16x32_bf16 v[28:31], a[28:31], v[88:91], v[28:31]\ns_waitcnt lgkmcnt(4)\nv_fma_f32 v54, v54, s58, v62\nv_fma_f32 v55, v55, s58, v62\nv_mfma_f32_16x16x32_bf16 v[32:35], a[4:7], v[92:95], v[32:35]\n" \
"ds_read_b128 v[88:91], v2 offset:6144\nv_fma_f32 v48, v48, s58, v62\nv_mfma_f32_16x16x32_bf16 v[36:39], a[12:15], v[92:95], v[36:39]\nv_exp_f32 v53, v53\nv_mfma_f32_16x16x32_bf16 v[40:43], a[20:23], v[92:95], v[40:43]\nv_exp_f32 v54, v54\nv_mfma_f32_16x16x32_bf16 v[44:47], a[28:31], v[92:95], v[44:47]\ns_waitcnt lgkmcnt(3)\n" \
"v_add_f32 v63, v52, v53\nv_add_f32 v63, v63, v54\nv_mfma_f32_16x16x32_bf16 v[16:19], a[32:35], v[80:83], v[16:19]\nds_read_b128 v[92:95], v2 offset:6400\nv_fma_f32 v49, v49, s58, v62\nv_mfma_f32_16x16x32_bf16 v[20:23], v[72:75], v[80:83], v[20:23]\nds_read_b128 v[64:67], v1 offset:2048\nv_mfma_f32_16x16x32_bf16 v[24:27], a[40:43], v[80:83], v[24:27]\n" \
"v_exp_f32 v55, v55\nv_mfma_f32_16x16x32_bf16 v[28:31], a[48:51], v[80:83], v[28:31]\ns_waitcnt lgkmcnt(4)\nv_exp_f32 v48, v48\nv_mfma_f32_16x16x32_bf16 v[32:35], a[32:35], v[84:87], v[32:35]\nds_read_b128 v[80:83], v2 offset:8192\nv_add_f32 v63, v63, v55\nv_mfma_f32_16x16x32_bf16 v[36:39], v[72:75], v[84:87], v[36:39]\n" \
"v_exp_f32 v49, v49\nv_mfma_f32_16x16x32_bf16 v[40:43], a[40:43], v[84:87], v[40:43]\nv_add_f32 v63, v63, v48\nv_add_f32 v62, v63, v49\nv_mfma_f32_16x16x32_bf16 v[44:47], a[48:51], v[84:87], v[44:47]\ns_waitcnt lgkmcnt(3)\nv_add_f32 v50, v63, v49\nv_mfma_f32_16x16x32_bf16 v[16:19], a[36:39], v[88:91], v[16:19]\n" \
"ds_read_b128 v[84:87], v2 offset:8448\nv_mfma_f32_16x16x32_bf16 v[20:23], v[76:79], v[88:91], v[20:23]\nds_read_b128 v[68:71], v1 offset:3072\nv_mfma_f32_16x16x32_bf16 v[24:27], a[44:47], v[88:91], v[24:27]\nv_permlane16_swap_b32 v62, v50\nv_mfma_f32_16x16x32_bf16 v[28:31], a[52:55], v[88:91], v[28:31]\ns_waitcnt lgkmcnt(4)\nv_add_f32 v63, v62, v50\n" \
"v_add_f32 v50, v62, v50\nv_mfma_f32_16x16x32_bf16 v[32:35], a[36:39], v[92:95], v[32:35]\nds_read_b128 v[88:91], v2 offset:10240\nv_mfma_f32_16x16x32_bf16 v[36:39], v[76:79], v[92:95], v[36:39]\nv_permlane32_swap_b32 v63, v50\nv_mfma_f32_16x16x32_bf16 v[40:43], a[44:47], v[92:95], v[40:43]\nv_add_f32 v63, v63, v50\nv_mfma_f32_16x16x32_bf16 v[44:47], a[52:55], v[92:95], v[44:47]\n" \
"s_waitcnt lgkmcnt(3)\nv_rcp_f32 v63, v63\nv_mfma_f32_16x16x32_bf16 v[16:19], a[56:59], v[80:83], v[16:19]\nds_read_b128 v[92:95], v2 offset:10496\nv_mul_f32 v52, v52, v63\nv_mfma_f32_16x16x32_bf16 v[20:23], a[64:67], v[80:83], v[20:23]\nds_read_b128 v[72:75], v1 offset:4096\nv_mul_f32 v53, v53, v63\n" \
"v_mfma_f32_16x16x32_bf16 v[24:27], v[64:67], v[80:83], v[24:27]\nv_mul_f32 v54, v54, v63\nv_mul_f32 v55, v55, v63\nv_mfma_f32_16x16x32_bf16 v[28:31], a[72:75], v[80:83], v[28:31]\ns_waitcnt lgkmcnt(4)\nv_mul_f32 v48, v48, v63\nv_mul_f32 v49, v49, v63\nv_mfma_f32_16x16x32_bf16 v[32:35], a[56:59], v[84:87], v[32:35]\n" \
"ds_read_b128 v[80:83], v2 offset:12288\nv_cvt_pk_bf16_f32 v52, v52, v53\nv_mfma_f32_16x16x32_bf16 v[36:39], a[64:67], v[84:87], v[36:39]\nv_cvt_pk_bf16_f32 v53, v54, v55\nv_cvt_pk_bf16_f32 v48, v48, v49\nv_mfma_f32_16x16x32_bf16 v[40:43], v[64:67], v[84:87], v[40:43]\nv_bfi_b32 v52, s61, v52, v61\nv_bfi_b32 v53, s61, v53, v61\n" \
"v_mfma_f32_16x16x32_bf16 v[44:47], a[72:75], v[84:87], v[44:47]\ns_waitcnt lgkmcnt(3)\nv_bfi_b32 v48, s61, v48, v61\nv_mfma_f32_16x16x32_bf16 v[16:19], a[60:63], v[88:91], v[16:19]\nds_read_b128 v[84:87], v2 offset:12544\nv_mfma_f32_16x16x32_bf16 v[20:23], a[68:71], v[88:91], v[20:23]\nds_read_b128 v[76:79], v1 offset:5120\nv_mfma_f32_16x16x32_bf16 v[24:27], v[68:71], v[88:91], v[24:27]\n" \
"buffer_store_dwordx2 v[52:53], v10, s[36:39], s55 offen sc1\nv_mfma_f32_16x16x32_bf16 v[28:31], a[76:79], v[88:91], v[28:31]\ns_waitcnt lgkmcnt(4)\nbuffer_store_dword v48, v11, s[36:39], s55 offen sc1\nv_mfma_f32_16x16x32_bf16 v[32:35], a[60:63], v[92:95], v[32:35]\nds_read_b128 v[88:91], v2 offset:14336\nv_cmp_ne_u32 vcc, 0, v60\nv_mfma_f32_16x16x32_bf16 v[36:39], a[68:71], v[92:95], v[36:39]\n" \
"v_mfma_f32_16x16x32_bf16 v[40:43], v[68:71], v[92:95], v[40:43]\nv_mfma_f32_16x16x32_bf16 v[44:47], a[76:79], v[92:95], v[44:47]\ns_cbranch_vccnz .Lqk_pj%=\ns_cmp_lt_i32 s40, 63\ns_waitcnt lgkmcnt(0)\ns_cbranch_scc1 .Lqk_pwait%=\ns_branch .Lqk_end%=\n.Lqk_re1%=:\n" \
"v_max_u32 v17, v80, v82\nv_cmp_eq_u32 vcc, -1, v17\ns_cbranch_vccz .Lqk_sk1994%=\ns_mov_b64 exec, vcc\nbuffer_load_dwordx4 v[80:83], v3, s[36:39], s42 offen offset:0 sc1\ns_mov_b64 exec, -1\n.Lqk_sk1994%=:\nv_max_u32 v17, v84, v86\n" \
"v_cmp_eq_u32 vcc, -1, v17\ns_cbranch_vccz .Lqk_sk2001%=\ns_mov_b64 exec, vcc\nbuffer_load_dwordx4 v[84:87], v3, s[36:39], s42 offen offset:1024 sc1\ns_mov_b64 exec, -1\n.Lqk_sk2001%=:\nv_max_u32 v17, v88, v90\nv_cmp_eq_u32 vcc, -1, v17\n" \
"s_cbranch_vccz .Lqk_sk2008%=\ns_mov_b64 exec, vcc\nbuffer_load_dwordx4 v[88:91], v3, s[36:39], s42 offen offset:2048 sc1\ns_mov_b64 exec, -1\n.Lqk_sk2008%=:\nv_max_u32 v17, v92, v94\nv_cmp_eq_u32 vcc, -1, v17\ns_cbranch_vccz .Lqk_sk2015%=\n" \
"s_mov_b64 exec, vcc\nbuffer_load_dwordx4 v[92:95], v3, s[36:39], s42 offen offset:3072 sc1\ns_mov_b64 exec, -1\n.Lqk_sk2015%=:\nv_mov_b64 v[48:49], 0\nv_mov_b64 v[50:51], 0\nv_mov_b64 v[52:53], 0\nv_mov_b64 v[54:55], 0\n" \
"v_mov_b64 v[56:57], 0\nv_mov_b64 v[58:59], 0\nv_mov_b64 v[60:61], 0\nv_mov_b64 v[62:63], 0\ns_add_u32 s43, s43, 1\ns_cmp_lt_u32 s43, 262144\ns_cbranch_scc1 .Lqk_poll1%=\ns_branch .Lqk_abort%=\n" \
".Lqk_c2%=:\ns_waitcnt lgkmcnt(3)\nv_mfma_f32_16x16x32_bf16 v[32:35], v[232:235], v[56:59], v[32:35]\nv_mov_b64 v[52:53], 0\nv_mov_b64 v[54:55], 0\nv_mfma_f32_16x16x32_bf16 v[36:39], v[240:243], v[56:59], v[36:39]\ns_waitcnt lgkmcnt(2)\nds_read_b128 v[64:67], v1 offset:12288\n" \
"v_mfma_f32_16x16x32_bf16 v[32:35], v[236:239], v[60:63], v[32:35]\nds_read_b128 v[68:71], v1 offset:13312\nv_mfma_f32_16x16x32_bf16 v[36:39], v[244:247], v[60:63], v[36:39]\ns_bfe_i32 s54, s40, 0x10001\nv_mfma_f32_16x16x32_bf16 v[40:43], v[248:251], v[56:59], v[40:43]\nv_mfma_f32_16x16x32_bf16 v[44:47], v[72:75], v[56:59], v[44:47]\nv_cvt_pk_bf16_f32 v36, v32, v36\nv_cvt_pk_bf16_f32 v37, v33, v37\n" \
"v_mfma_f32_16x16x32_bf16 v[40:43], v[252:255], v[60:63], v[40:43]\nv_cvt_pk_bf16_f32 v38, v34, v38\nv_cvt_pk_bf16_f32 v39, v35, v39\nv_mfma_f32_16x16x32_bf16 v[44:47], v[76:79], v[60:63], v[44:47]\nds_write_b128 v5, v[36:39] offset:256\nv_mov_b64 v[56:57], 0\nv_mov_b64 v[58:59], 0\nv_mov_b64 v[60:61], 0\n" \
"v_mov_b64 v[62:63], 0\n.Lqk_poll2%=:\ns_waitcnt vmcnt(0)\nv_smfmac_f32_16x16x64_bf16 v[48:51], v[80:83], a[208:215], v4\nds_write_b128 v0, v[80:83] offset:24576\nv_max3_u32 v16, v80, v82, v84\nv_smfmac_f32_16x16x64_bf16 v[52:55], v[80:83], a[216:223], v4\nds_read_b128 v[72:75], v1 offset:14336\n" \
"ds_read_b128 v[76:79], v1 offset:15360\nv_max3_u32 v16, v16, v86, v88\nv_smfmac_f32_16x16x64_bf16 v[56:59], v[80:83], a[224:231], v4\nds_write_b128 v0, v[84:87] offset:25600\nv_max3_u32 v16, v16, v90, v92\nv_max_u32 v16, v16, v94\nv_smfmac_f32_16x16x64_bf16 v[60:63], v[80:83], a[232:239], v4\nv_cmp_eq_u32 vcc, -1, v16\n" \
"v_smfmac_f32_16x16x64_bf16 v[48:51], v[84:87], a[240:247], v4\nds_write_b128 v0, v[88:91] offset:26624\ns_cbranch_vccnz .Lqk_re2%=\ns_waitcnt lgkmcnt(6)\nv_smfmac_f32_16x16x64_bf16 v[52:55], v[84:87], v[64:71], v4\nds_read_b128 v[64:67], v1 offset:16384\nds_read_b128 v[68:71], v1 offset:17408\nv_smfmac_f32_16x16x64_bf16 v[56:59], v[84:87], a[248:255], v4\n" \
"ds_write_b128 v0, v[92:95] offset:27648\nv_smfmac_f32_16x16x64_bf16 v[60:63], v[84:87], v[96:103], v4\nv_smfmac_f32_16x16x64_bf16 v[48:51], v[88:91], v[104:111], v4\nv_smfmac_f32_16x16x64_bf16 v[52:55], v[88:91], v[112:119], v4\ns_waitcnt lgkmcnt(0)\ns_barrier\nds_read_b128 v[80:83], v0 offset:28672\nds_read_b128 v[84:87], v0 offset:29696\n" \
"v_smfmac_f32_16x16x64_bf16 v[56:59], v[88:91], v[72:79], v4\nds_read_b128 v[72:75], v1 offset:18432\nds_read_b128 v[76:79], v1 offset:19456\nv_cvt_pk_bf16_f32 v44, v40, v44\nv_smfmac_f32_16x16x64_bf16 v[60:63], v[88:91], v[120:127], v4\nds_read_b128 v[88:91], v0 offset:30720\nv_cvt_pk_bf16_f32 v45, v41, v45\nv_smfmac_f32_16x16x64_bf16 v[48:51], v[92:95], v[128:135], v4\n" \
"v_cvt_pk_bf16_f32 v46, v42, v46\nv_smfmac_f32_16x16x64_bf16 v[52:55], v[92:95], v[136:143], v4\nv_cvt_pk_bf16_f32 v47, v43, v47\nv_smfmac_f32_16x16x64_bf16 v[56:59], v[92:95], v[144:151], v4\nv_smfmac_f32_16x16x64_bf16 v[60:63], v[92:95], v[64:71], v4\ns_waitcnt lgkmcnt(4)\nds_write_b128 v5, v[44:47] offset:768\nds_read_b128 v[92:95], v0 offset:31744\n" \
"ds_read_b128 v[64:67], v1 offset:20480\nds_read_b128 v[68:71], v1 offset:21504\nv_smfmac_f32_16x16x64_bf16 v[48:51], v[80:83], v[152:159], v4\nv_smfmac_f32_16x16x64_bf16 v[52:55], v[80:83], v[160:167], v4\nv_smfmac_f32_16x16x64_bf16 v[56:59], v[80:83], v[168:175], v4\nv_smfmac_f32_16x16x64_bf16 v[60:63], v[80:83], v[176:183], v4\ns_waitcnt lgkmcnt(7)\nds_read_b128 v[80:83], v0 offset:16384\n" \
"v_smfmac_f32_16x16x64_bf16 v[48:51], v[84:87], v[184:191], v4\ns_waitcnt lgkmcnt(6)\nv_smfmac_f32_16x16x64_bf16 v[52:55], v[84:87], v[72:79], v4\nds_read_b128 v[72:75], v1 offset:22528\nds_read_b128 v[76:79], v1 offset:23552\nv_smfmac_f32_16x16x64_bf16 v[56:59], v[84:87], v[192:199], v4\nv_smfmac_f32_16x16x64_bf16 v[60:63], v[84:87], v[200:207], v4\ns_waitcnt lgkmcnt(7)\n" \
"ds_read_b128 v[84:87], v0 offset:17408\nv_smfmac_f32_16x16x64_bf16 v[48:51], v[88:91], v[208:215], v4\ns_waitcnt lgkmcnt(7)\ns_barrier\nv_smfmac_f32_16x16x64_bf16 v[52:55], v[88:91], v[216:223], v4\ns_waitcnt lgkmcnt(4)\nv_smfmac_f32_16x16x64_bf16 v[56:59], v[88:91], v[64:71], v4\nds_read_b128 v[64:67], v1 offset:0\n" \
"ds_read_b128 v[68:71], v1 offset:1024\nv_smfmac_f32_16x16x64_bf16 v[60:63], v[88:91], v[224:231], v4\nds_read_b128 v[88:91], v0 offset:18432\nv_smfmac_f32_16x16x64_bf16 v[48:51], v[92:95], v[232:239], v4\nds_read_b128 v[16:19], v7 offset:256\nv_smfmac_f32_16x16x64_bf16 v[52:55], v[92:95], v[240:247], v4\nv_smfmac_f32_16x16x64_bf16 v[56:59], v[92:95], v[248:255], v4\ns_waitcnt lgkmcnt(5)\n" \
"ds_read_b128 v[20:23], v7 offset:2304\nv_smfmac_f32_16x16x64_bf16 v[60:63], v[92:95], v[72:79], v4\nds_read_b128 v[92:95], v0 offset:19456\nds_read_b128 v[72:75], v1 offset:2048\nds_read_b128 v[76:79], v1 offset:3072\nv_smfmac_f32_16x16x64_bf16 v[48:51], v[80:83], a[0:7], v4\nds_read_b128 v[24:27], v7 offset:4352\nv_smfmac_f32_16x16x64_bf16 v[52:55], v[80:83], a[8:15], v4\n" \
"v_smfmac_f32_16x16x64_bf16 v[56:59], v[80:83], a[16:23], v4\nds_read_b128 v[28:31], v7 offset:6400\nv_smfmac_f32_16x16x64_bf16 v[60:63], v[80:83], a[24:31], v4\ns_waitcnt lgkmcnt(10)\nds_read_b128 v[80:83], v0 offset:20480\nv_smfmac_f32_16x16x64_bf16 v[48:51], v[84:87], a[32:39], v4\ns_waitcnt lgkmcnt(9)\nds_read_b128 v[32:35], v7 offset:8192\n" \
"v_smfmac_f32_16x16x64_bf16 v[52:55], v[84:87], v[64:71], v4\nds_read_b128 v[64:67], v1 offset:4096\nds_read_b128 v[68:71], v1 offset:5120\nv_smfmac_f32_16x16x64_bf16 v[56:59], v[84:87], a[40:47], v4\nds_read_b128 v[36:39], v7 offset:10240\nv_smfmac_f32_16x16x64_bf16 v[60:63], v[84:87], a[48:55], v4\ns_waitcnt lgkmcnt(12)\nds_read_b128 v[84:87], v0 offset:21504\n" \
"v_smfmac_f32_16x16x64_bf16 v[48:51], v[88:91], a[56:63], v4\nds_read_b128 v[40:43], v7 offset:12288\nv_smfmac_f32_16x16x64_bf16 v[52:55], v[88:91], a[64:71], v4\ns_waitcnt lgkmcnt(9)\nv_smfmac_f32_16x16x64_bf16 v[56:59], v[88:91], v[72:79], v4\nds_read_b128 v[72:75], v1 offset:6144\nds_read_b128 v[76:79], v1 offset:7168\nv_smfmac_f32_16x16x64_bf16 v[60:63], v[88:91], a[72:79], v4\n" \
"ds_read_b128 v[88:91], v0 offset:22528\nv_smfmac_f32_16x16x64_bf16 v[48:51], v[92:95], a[80:87], v4\nds_read_b128 v[44:47], v7 offset:14336\nv_smfmac_f32_16x16x64_bf16 v[52:55], v[92:95], a[88:95], v4\nv_smfmac_f32_16x16x64_bf16 v[56:59], v[92:95], a[96:103], v4\ns_waitcnt lgkmcnt(7)\nv_smfmac_f32_16x16x64_bf16 v[60:63], v[92:95], v[64:71], v4\nds_read_b128 v[92:95], v0 offset:23552\n" \
"ds_read_b128 v[64:67], v1 offset:8192\nds_read_b128 v[68:71], v1 offset:9216\nv_smfmac_f32_16x16x64_bf16 v[48:51], v[80:83], a[104:111], v4\nv_smfmac_f32_16x16x64_bf16 v[52:55], v[80:83], a[112:119], v4\nv_smfmac_f32_16x16x64_bf16 v[56:59], v[80:83], a[120:127], v4\nv_smfmac_f32_16x16x64_bf16 v[60:63], v[80:83], a[128:135], v4\ns_waitcnt lgkmcnt(8)\nv_smfmac_f32_16x16x64_bf16 v[48:51], v[84:87], a[136:143], v4\n" \
"s_waitcnt lgkmcnt(5)\nds_read_b128 v[80:83], v7 offset:8448\nv_smfmac_f32_16x16x64_bf16 v[52:55], v[84:87], v[72:79], v4\nds_read_b128 v[72:75], v1 offset:10240\nds_read_b128 v[76:79], v1 offset:11264\nv_smfmac_f32_16x16x64_bf16 v[56:59], v[84:87], a[144:151], v4\nv_smfmac_f32_16x16x64_bf16 v[60:63], v[84:87], a[152:159], v4\ns_waitcnt lgkmcnt(7)\n" \
"v_smfmac_f32_16x16x64_bf16 v[48:51], v[88:91], a[160:167], v4\nds_read_b128 v[84:87], v7 offset:10496\nv_smfmac_f32_16x16x64_bf16 v[52:55], v[88:91], a[168:175], v4\ns_waitcnt lgkmcnt(6)\nv_smfmac_f32_16x16x64_bf16 v[48:51], v[92:95], a[184:191], v4\nv_smfmac_f32_16x16x64_bf16 v[52:55], v[92:95], a[192:199], v4\ns_waitcnt lgkmcnt(4)\nv_smfmac_f32_16x16x64_bf16 v[56:59], v[88:91], v[64:71], v4\n" \
"ds_read_b128 v[64:67], v7 offset:12544\nv_add_f32 v48, v48, v50\nv_smfmac_f32_16x16x64_bf16 v[60:63], v[88:91], a[176:183], v4\nds_read_b128 v[68:71], v7 offset:14592\nv_add_f32 v52, v52, v54\nv_add_f32 v49, v49, v51\nv_smfmac_f32_16x16x64_bf16 v[56:59], v[92:95], a[200:207], v4\ns_waitcnt lgkmcnt(3)\n" \
"v_add_f32 v53, v53, v55\nv_cvt_pk_bf16_f32 v50, v48, v52\nv_smfmac_f32_16x16x64_bf16 v[60:63], v[92:95], v[72:79], v4\nv_cvt_pk_bf16_f32 v51, v49, v53\nds_write2_b32 v6, v50, v51 offset0:0 offset1:4\ns_nop 1\nv_add_f32 v56, v56, v58\nv_add_f32 v57, v57, v59\n" \
"s_nop 1\nv_add_f32 v60, v60, v62\nv_add_f32 v61, v61, v63\nv_cvt_pk_bf16_f32 v62, v56, v60\nv_cvt_pk_bf16_f32 v63, v57, v61\nds_write2_b32 v6, v62, v63 offset0:36 offset1:40\nv_mov_b32 v61, s54\nv_mfma_f32_16x16x32_bf16 v[52:55], v[32:35], v[16:19], 0\n" \
"v_mfma_f32_16x16x32_bf16 v[52:55], v[36:39], v[20:23], v[52:55]\nv_mfma_f32_16x16x32_bf16 v[52:55], v[40:43], v[24:27], v[52:55]\nv_mfma_f32_16x16x32_bf16 v[52:55], v[44:47], v[28:31], v[52:55]\ns_waitcnt lgkmcnt(0)\ns_barrier\nds_read_b128 v[32:35], v9 offset:0\nds_read_b128 v[36:39], v9 offset:576\nds_read_b128 v[40:43], v9 offset:1152\n" \
"ds_read_b128 v[44:47], v9 offset:1728\nds_read_b32 v60, v0 offset:53760\nv_mfma_f32_16x16x32_bf16 v[56:59], v[80:83], v[16:19], v[12:15]\nv_mfma_f32_16x16x32_bf16 v[56:59], v[84:87], v[20:23], v[56:59]\nv_mfma_f32_16x16x32_bf16 v[56:59], v[64:67], v[24:27], v[56:59]\nv_mfma_f32_16x16x32_bf16 v[56:59], v[68:71], v[28:31], v[56:59]\ns_waitcnt lgkmcnt(4)\nds_read_b128 v[80:83], v2 offset:0\n" \
"ds_read_b128 v[84:87], v2 offset:256\nds_read_b128 v[88:91], v2 offset:2048\nv_mfma_f32_16x16x32_bf16 v[48:51], v[32:35], v[16:19], 0\ns_waitcnt lgkmcnt(6)\nv_mfma_f32_16x16x32_bf16 v[48:51], v[36:39], v[20:23], v[48:51]\ns_waitcnt lgkmcnt(5)\nv_mfma_f32_16x16x32_bf16 v[48:51], v[40:43], v[24:27], v[48:51]\ns_waitcnt lgkmcnt(4)\n" \
"v_mfma_f32_16x16x32_bf16 v[48:51], v[44:47], v[28:31], v[48:51]\ns_waitcnt lgkmcnt(2)\nv_mfma_f32_16x16x32_bf16 v[16:19], a[0:3], v[80:83], 0\nds_read_b128 v[92:95], v2 offset:2304\nv_max3_f32 v62, v52, v53, v54\nv_max3_f32 v62, v62, v56, v57\nv_mfma_f32_16x16x32_bf16 v[20:23], a[8:11], v[80:83], 0\nds_read_b128 v[72:75], v1 offset:0\n" \
"v_max3_f32 v62, v62, v58, v59\nv_max_f32 v62, v62, v55\nv_mfma_f32_16x16x32_bf16 v[24:27], a[16:19], v[80:83], 0\nv_max3_f32 v63, v62, v48, v49\nv_max3_f32 v62, v62, v48, v49\nv_mfma_f32_16x16x32_bf16 v[28:31], a[24:27], v[80:83], 0\ns_waitcnt lgkmcnt(3)\nv_permlane16_swap_b32 v62, v63\n" \
"v_max_f32 v50, v62, v63\nv_mfma_f32_16x16x32_bf16 v[32:35], a[0:3], v[84:87], 0\nds_read_b128 v[80:83], v2 offset:4096\nv_max_f32 v62, v62, v63\nv_mfma_f32_16x16x32_bf16 v[36:39], a[8:11], v[84:87], 0\ns_nop 0\nv_permlane32_swap_b32 v62, v50\nv_max_f32 v62, v62, v50\n" \
"v_mfma_f32_16x16x32_bf16 v[40:43], a[16:19], v[84:87], 0\nv_mul_f32 v62, s59, v62\nv_fma_f32 v52, v52, s58, v62\nv_fma_f32 v53, v53, s58, v62\nv_mfma_f32_16x16x32_bf16 v[44:47], a[24:27], v[84:87], 0\ns_waitcnt lgkmcnt(3)\nv_exp_f32 v52, v52\nv_fma_f32 v54, v54, s58, v62\n" \
"v_mfma_f32_16x16x32_bf16 v[16:19], a[4:7], v[88:91], v[16:19]\nds_read_b128 v[84:87], v2 offset:4352\nv_exp_f32 v53, v53\nv_mfma_f32_16x16x32_bf16 v[20:23], a[12:15], v[88:91], v[20:23]\nds_read_b128 v[76:79], v1 offset:1024\nv_fma_f32 v55, v55, s58, v62\nv_add_f32 v63, v52, v53\nv_mfma_f32_16x16x32_bf16 v[24:27], a[20:23], v[88:91], v[24:27]\n" \
"v_exp_f32 v54, v54\nv_fma_f32 v56, v56, s58, v62\nv_mfma_f32_16x16x32_bf16 v[28:31], a[28:31], v[88:91], v[28:31]\ns_waitcnt lgkmcnt(4)\nv_exp_f32 v55, v55\nv_add_f32 v63, v63, v54\nv_mfma_f32_16x16x32_bf16 v[32:35], a[4:7], v[92:95], v[32:35]\nds_read_b128 v[88:91], v2 offset:6144\n" \
"v_fma_f32 v57, v57, s58, v62\nv_add_f32 v63, v63, v55\nv_mfma_f32_16x16x32_bf16 v[36:39], a[12:15], v[92:95], v[36:39]\nv_exp_f32 v56, v56\nv_fma_f32 v58, v58, s58, v62\nv_mfma_f32_16x16x32_bf16 v[40:43], a[20:23], v[92:95], v[40:43]\nv_exp_f32 v57, v57\nv_add_f32 v63, v63, v56\n" \
"v_mfma_f32_16x16x32_bf16 v[44:47], a[28:31], v[92:95], v[44:47]\ns_waitcnt lgkmcnt(3)\nv_fma_f32 v59, v59, s58, v62\nv_exp_f32 v58, v58\nv_mfma_f32_16x16x32_bf16 v[16:19], a[32:35], v[80:83], v[16:19]\nds_read_b128 v[92:95], v2 offset:6400\nv_add_f32 v63, v63, v57\nv_fma_f32 v48, v48, s58, v62\n" \
"v_mfma_f32_16x16x32_bf16 v[20:23], v[72:75], v[80:83], v[20:23]\nds_read_b128 v[64:67], v1 offset:2048\nv_exp_f32 v59, v59\nv_mfma_f32_16x16x32_bf16 v[24:27], a[40:43], v[80:83], v[24:27]\nv_add_f32 v63, v63, v58\nv_fma_f32 v49, v49, s58, v62\nv_add_f32 v63, v63, v59\nv_mfma_f32_16x16x32_bf16 v[28:31], a[48:51], v[80:83], v[28:31]\n" \
"s_waitcnt lgkmcnt(4)\nv_exp_f32 v48, v48\nv_mfma_f32_16x16x32_bf16 v[32:35], a[32:35], v[84:87], v[32:35]\nds_read_b128 v[80:83], v2 offset:8192\nv_exp_f32 v49, v49\nv_mfma_f32_16x16x32_bf16 v[36:39], v[72:75], v[84:87], v[36:39]\nv_add_f32 v63, v63, v48\nv_add_f32 v62, v63, v49\n" \
"v_add_f32 v50, v63, v49\nv_mfma_f32_16x16x32_bf16 v[40:43], a[40:43], v[84:87], v[40:43]\ns_nop 0\nv_permlane16_swap_b32 v62, v50\nv_add_f32 v63, v62, v50\nv_mfma_f32_16x16x32_bf16 v[44:47], a[48:51], v[84:87], v[44:47]\ns_waitcnt lgkmcnt(3)\nv_add_f32 v50, v62, v50\n" \
"v_mfma_f32_16x16x32_bf16 v[16:19], a[36:39], v[88:91], v[16:19]\nds_read_b128 v[84:87], v2 offset:8448\nv_permlane32_swap_b32 v63, v50\nv_mfma_f32_16x16x32_bf16 v[20:23], v[76:79], v[88:91], v[20:23]\nds_read_b128 v[68:71], v1 offset:3072\nv_add_f32 v63, v63, v50\nv_mfma_f32_16x16x32_bf16 v[24:27], a[44:47], v[88:91], v[24:27]\nv_rcp_f32 v63, v63\n" \
"v_mfma_f32_16x16x32_bf16 v[28:31], a[52:55], v[88:91], v[28:31]\ns_waitcnt lgkmcnt(4)\nv_mul_f32 v52, v52, v63\nv_mul_f32 v53, v53, v63\nv_mul_f32 v54, v54, v63\nv_mfma_f32_16x16x32_bf16 v[32:35], a[36:39], v[92:95], v[32:35]\nds_read_b128 v[88:91], v2 offset:10240\nv_mul_f32 v55, v55, v63\n" \
"v_mul_f32 v56, v56, v63\nv_mfma_f32_16x16x32_bf16 v[36:39], v[76:79], v[92:95], v[36:39]\nv_mul_f32 v57, v57, v63\nv_mul_f32 v58, v58, v63\nv_mul_f32 v59, v59, v63\nv_mfma_f32_16x16x32_bf16 v[40:43], a[44:47], v[92:95], v[40:43]\nv_mul_f32 v48, v48, v63\nv_mul_f32 v49, v49, v63\n" \
"v_cvt_pk_bf16_f32 v52, v52, v53\nv_mfma_f32_16x16x32_bf16 v[44:47], a[52:55], v[92:95], v[44:47]\ns_waitcnt lgkmcnt(3)\nv_cvt_pk_bf16_f32 v53, v54, v55\nv_cvt_pk_bf16_f32 v48, v48, v49\nv_cvt_pk_bf16_f32 v54, v56, v57\nv_mfma_f32_16x16x32_bf16 v[16:19], a[56:59], v[80:83], v[16:19]\nds_read_b128 v[92:95], v2 offset:10496\n" \
"v_cvt_pk_bf16_f32 v55, v58, v59\nv_bfi_b32 v52, s61, v52, v61\nv_mfma_f32_16x16x32_bf16 v[20:23], a[64:67], v[80:83], v[20:23]\nds_read_b128 v[72:75], v1 offset:4096\nv_bfi_b32 v53, s61, v53, v61\nv_bfi_b32 v54, s61, v54, v61\nv_mfma_f32_16x16x32_bf16 v[24:27], v[64:67], v[80:83], v[24:27]\nv_bfi_b32 v55, s61, v55, v61\n" \
"v_bfi_b32 v48, s61, v48, v61\nv_mfma_f32_16x16x32_bf16 v[28:31], a[72:75], v[80:83], v[28:31]\ns_waitcnt lgkmcnt(4)\nbuffer_store_dwordx4 v[52:55], v10, s[36:39], s55 offen sc1\nv_mfma_f32_16x16x32_bf16 v[32:35], a[56:59], v[84:87], v[32:35]\nds_read_b128 v[80:83], v2 offset:12288\nv_mfma_f32_16x16x32_bf16 v[36:39], a[64:67], v[84:87], v[36:39]\nbuffer_store_dword v48, v11, s[36:39], s55 offen sc1\n" \
"v_cmp_ne_u32 vcc, 0, v60\nv_mfma_f32_16x16x32_bf16 v[40:43], v[64:67], v[84:87], v[40:43]\nv_mfma_f32_16x16x32_bf16 v[44:47], a[72:75], v[84:87], v[44:47]\ns_waitcnt lgkmcnt(3)\nv_mfma_f32_16x16x32_bf16 v[16:19], a[60:63], v[88:91], v[16:19]\nds_read_b128 v[84:87], v2 offset:12544\nv_mfma_f32_16x16x32_bf16 v[20:23], a[68:71], v[88:91], v[20:23]\nds_read_b128 v[76:79], v1 offset:5120\n" \
"v_mfma_f32_16x16x32_bf16 v[24:27], v[68:71], v[88:91], v[24:27]\nv_mfma_f32_16x16x32_bf16 v[28:31], a[76:79], v[88:91], v[28:31]\ns_waitcnt lgkmcnt(4)\nv_mfma_f32_16x16x32_bf16 v[32:35], a[60:63], v[92:95], v[32:35]\nds_read_b128 v[88:91], v2 offset:14336\nv_mfma_f32_16x16x32_bf16 v[36:39], a[68:71], v[92:95], v[36:39]\nv_mfma_f32_16x16x32_bf16 v[40:43], v[68:71], v[92:95], v[40:43]\nv_mfma_f32_16x16x32_bf16 v[44:47], a[76:79], v[92:95], v[44:47]\n" \
"s_cbranch_vccnz .Lqk_pj%=\ns_cmp_lt_i32 s40, 63\ns_waitcnt lgkmcnt(0)\ns_cbranch_scc1 .Lqk_pwait%=\ns_branch .Lqk_end%=\n.Lqk_re2%=:\nv_max_u32 v17, v80, v82\nv_cmp_eq_u32 vcc, -1, v17\n" \
"s_cbranch_vccz .Lqk_sk2404%=\ns_mov_b64 exec, vcc\nbuffer_load_dwordx4 v[80:83], v3, s[36:39], s42 offen offset:0 sc1\ns_mov_b64 exec, -1\n.Lqk_sk2404%=:\nv_max_u32 v17, v84, v86\nv_cmp_eq_u32 vcc, -1, v17\ns_cbranch_vccz .Lqk_sk2411%=\n" \
"s_mov_b64 exec, vcc\nbuffer_load_dwordx4 v[84:87], v3, s[36:39], s42 offen offset:1024 sc1\ns_mov_b64 exec, -1\n.Lqk_sk2411%=:\nv_max_u32 v17, v88, v90\nv_cmp_eq_u32 vcc, -1, v17\ns_cbranch_vccz .Lqk_sk2418%=\ns_mov_b64 exec, vcc\n" \
"buffer_load_dwordx4 v[88:91], v3, s[36:39], s42 offen offset:2048 sc1\ns_mov_b64 exec, -1\n.Lqk_sk2418%=:\nv_max_u32 v17, v92, v94\nv_cmp_eq_u32 vcc, -1, v17\ns_cbranch_vccz .Lqk_sk2425%=\ns_mov_b64 exec, vcc\nbuffer_load_dwordx4 v[92:95], v3, s[36:39], s42 offen offset:3072 sc1\n" \
"s_mov_b64 exec, -1\n.Lqk_sk2425%=:\nv_mov_b64 v[48:49], 0\nv_mov_b64 v[50:51], 0\nv_mov_b64 v[52:53], 0\nv_mov_b64 v[54:55], 0\nv_mov_b64 v[56:57], 0\nv_mov_b64 v[58:59], 0\n" \
"v_mov_b64 v[60:61], 0\nv_mov_b64 v[62:63], 0\ns_add_u32 s43, s43, 1\ns_cmp_lt_u32 s43, 262144\ns_cbranch_scc1 .Lqk_poll2%=\ns_branch .Lqk_abort%=\n.Lqk_c3%=:\ns_waitcnt lgkmcnt(3)\n" \
"v_mfma_f32_16x16x32_bf16 v[32:35], v[232:235], v[56:59], v[32:35]\ns_add_u32 s44, s40, 1\nv_mov_b64 v[52:53], 0\nv_mov_b64 v[54:55], 0\nv_mfma_f32_16x16x32_bf16 v[36:39], v[240:243], v[56:59], v[36:39]\ns_lshl_b32 s44, s44, 17\ns_waitcnt lgkmcnt(2)\nds_read_b128 v[64:67], v1 offset:18432\n" \
"v_mfma_f32_16x16x32_bf16 v[32:35], v[236:239], v[60:63], v[32:35]\ns_add_u32 s44, s44, %[xn]\ns_lshr_b32 s45, s44, 8\nds_read_b128 v[68:71], v1 offset:19456\nv_mfma_f32_16x16x32_bf16 v[36:39], v[244:247], v[60:63], v[36:39]\ns_and_b32 s45, s45, 0x2000\ns_xor_b32 s41, s44, s45\nv_mfma_f32_16x16x32_bf16 v[40:43], v[248:251], v[56:59], v[40:43]\n" \
"buffer_load_dword v16, v12, s[36:39], s56 offen sc1\nv_mfma_f32_16x16x32_bf16 v[44:47], v[72:75], v[56:59], v[44:47]\nv_cvt_pk_bf16_f32 v36, v32, v36\nv_cvt_pk_bf16_f32 v37, v33, v37\nv_mfma_f32_16x16x32_bf16 v[40:43], v[252:255], v[60:63], v[40:43]\nv_cvt_pk_bf16_f32 v38, v34, v38\nv_cvt_pk_bf16_f32 v39, v35, v39\nv_mfma_f32_16x16x32_bf16 v[44:47], v[76:79], v[60:63], v[44:47]\n" \
"ds_write_b128 v5, v[36:39] offset:256\nv_mov_b64 v[56:57], 0\nv_mov_b64 v[58:59], 0\nv_mov_b64 v[60:61], 0\nv_mov_b64 v[62:63], 0\n.Lqk_poll3%=:\ns_waitcnt vmcnt(1)\nv_smfmac_f32_16x16x64_bf16 v[48:51], v[80:83], v[152:159], v4\n" \
"ds_write_b128 v0, v[80:83] offset:28672\nv_max3_u32 v17, v80, v82, v84\nv_smfmac_f32_16x16x64_bf16 v[52:55], v[80:83], v[160:167], v4\nds_read_b128 v[72:75], v1 offset:20480\nds_read_b128 v[76:79], v1 offset:21504\nv_max3_u32 v17, v17, v86, v88\nv_smfmac_f32_16x16x64_bf16 v[56:59], v[80:83], v[168:175], v4\nds_write_b128 v0, v[84:87] offset:29696\n" \
"v_max3_u32 v17, v17, v90, v92\nv_max_u32 v17, v17, v94\nv_smfmac_f32_16x16x64_bf16 v[60:63], v[80:83], v[176:183], v4\nv_cmp_eq_u32 vcc, -1, v17\nv_smfmac_f32_16x16x64_bf16 v[48:51], v[84:87], v[184:191], v4\nds_write_b128 v0, v[88:91] offset:30720\ns_cbranch_vccnz .Lqk_re3%=\ns_waitcnt lgkmcnt(6)\n" \
"v_smfmac_f32_16x16x64_bf16 v[52:55], v[84:87], v[64:71], v4\nds_read_b128 v[64:67], v1 offset:22528\nds_read_b128 v[68:71], v1 offset:23552\nv_smfmac_f32_16x16x64_bf16 v[56:59], v[84:87], v[192:199], v4\nds_write_b128 v0, v[92:95] offset:31744\nv_smfmac_f32_16x16x64_bf16 v[60:63], v[84:87], v[200:207], v4\nv_smfmac_f32_16x16x64_bf16 v[48:51], v[88:91], v[208:215], v4\nv_smfmac_f32_16x16x64_bf16 v[52:55], v[88:91], v[216:223], v4\n" \
"s_waitcnt lgkmcnt(0)\ns_barrier\nds_read_b128 v[80:83], v0 offset:16384\nds_read_b128 v[84:87], v0 offset:17408\nv_smfmac_f32_16x16x64_bf16 v[56:59], v[88:91], v[72:79], v4\ns_waitcnt vmcnt(0)\nds_read_b128 v[72:75], v1 offset:0\nds_read_b128 v[76:79], v1 offset:1024\n" \
"v_cvt_pk_bf16_f32 v44, v40, v44\nv_cmp_ge_i32 vcc, s40, v16\nv_smfmac_f32_16x16x64_bf16 v[60:63], v[88:91], v[224:231], v4\ns_cmp_eq_u64 vcc, 0\ns_cselect_b32 s61, s41, -2147483648\ns_cselect_b32 s44, 1, 0\nds_read_b128 v[88:91], v0 offset:18432\nv_cvt_pk_bf16_f32 v45, v41, v45\n" \
"v_smfmac_f32_16x16x64_bf16 v[48:51], v[92:95], v[232:239], v4\ns_add_u32 m0, %[l0], 0\ns_add_u32 s60, s61, 0\nbuffer_load_dwordx4 v0, s[36:39], s60 offen offset:0 sc1 lds\nv_cvt_pk_bf16_f32 v46, v42, v46\nv_smfmac_f32_16x16x64_bf16 v[52:55], v[92:95], v[240:247], v4\nv_cvt_pk_bf16_f32 v47, v43, v47\nv_mov_b32 v16, s44\n" \
"v_smfmac_f32_16x16x64_bf16 v[56:59], v[92:95], v[248:255], v4\nbuffer_load_dwordx4 v0, s[36:39], s60 offen offset:1024 sc1 lds\nv_smfmac_f32_16x16x64_bf16 v[60:63], v[92:95], v[64:71], v4\ns_waitcnt lgkmcnt(4)\nds_write_b128 v5, v[44:47] offset:768\nds_read_b128 v[92:95], v0 offset:19456\nds_read_b128 v[64:67], v1 offset:2048\nds_read_b128 v[68:71], v1 offset:3072\n" \
"ds_write_b32 v0, v16 offset:53760\nv_smfmac_f32_16x16x64_bf16 v[48:51], v[80:83], a[0:7], v4\nbuffer_load_dwordx4 v0, s[36:39], s60 offen offset:2048 sc1 lds\nv_smfmac_f32_16x16x64_bf16 v[52:55], v[80:83], a[8:15], v4\nv_smfmac_f32_16x16x64_bf16 v[56:59], v[80:83], a[16:23], v4\nbuffer_load_dwordx4 v0, s[36:39], s60 offen offset:3072 sc1 lds\nv_smfmac_f32_16x16x64_bf16 v[60:63], v[80:83], a[24:31], v4\ns_waitcnt lgkmcnt(8)\n" \
"s_add_u32 m0, %[l0], 4096\nds_read_b128 v[80:83], v0 offset:20480\nv_smfmac_f32_16x16x64_bf16 v[48:51], v[84:87], a[32:39], v4\ns_add_u32 s60, s61, 4096\ns_waitcnt lgkmcnt(7)\nbuffer_load_dwordx4 v0, s[36:39], s60 offen offset:0 sc1 lds\nv_smfmac_f32_16x16x64_bf16 v[52:55], v[84:87], v[72:79], v4\nds_read_b128 v[72:75], v1 offset:4096\n" \
"ds_read_b128 v[76:79], v1 offset:5120\nv_smfmac_f32_16x16x64_bf16 v[56:59], v[84:87], a[40:47], v4\nbuffer_load_dwordx4 v0, s[36:39], s60 offen offset:1024 sc1 lds\nv_smfmac_f32_16x16x64_bf16 v[60:63], v[84:87], a[48:55], v4\ns_waitcnt lgkmcnt(8)\nds_read_b128 v[84:87], v0 offset:21504\nv_smfmac_f32_16x16x64_bf16 v[48:51], v[88:91], a[56:63], v4\ns_waitcnt lgkmcnt(8)\n" \
"s_barrier\nbuffer_load_dwordx4 v0, s[36:39], s60 offen offset:2048 sc1 lds\nv_smfmac_f32_16x16x64_bf16 v[52:55], v[88:91], a[64:71], v4\ns_waitcnt lgkmcnt(5)\nv_smfmac_f32_16x16x64_bf16 v[56:59], v[88:91], v[64:71], v4\nds_read_b128 v[64:67], v1 offset:6144\nds_read_b128 v[68:71], v1 offset:7168\nbuffer_load_dwordx4 v0, s[36:39], s60 offen offset:3072 sc1 lds\n" \
"v_smfmac_f32_16x16x64_bf16 v[60:63], v[88:91], a[72:79], v4\ns_add_u32 m0, %[l0], 8192\nds_read_b128 v[88:91], v0 offset:22528\nv_smfmac_f32_16x16x64_bf16 v[48:51], v[92:95], a[80:87], v4\ns_add_u32 s60, s61, 16384\nbuffer_load_dwordx4 v0, s[36:39], s60 offen offset:0 sc1 lds\nv_smfmac_f32_16x16x64_bf16 v[52:55], v[92:95], a[88:95], v4\nv_smfmac_f32_16x16x64_bf16 v[56:59], v[92:95], a[96:103], v4\n" \
"s_waitcnt lgkmcnt(4)\nbuffer_load_dwordx4 v0, s[36:39], s60 offen offset:1024 sc1 lds\nv_smfmac_f32_16x16x64_bf16 v[60:63], v[92:95], v[72:79], v4\nds_read_b128 v[92:95], v0 offset:23552\nds_read_b128 v[72:75], v1 offset:8192\nds_read_b128 v[76:79], v1 offset:9216\nv_smfmac_f32_16x16x64_bf16 v[48:51], v[80:83], a[104:111], v4\nbuffer_load_dwordx4 v0, s[36:39], s60 offen offset:2048 sc1 lds\n" \
"v_smfmac_f32_16x16x64_bf16 v[52:55], v[80:83], a[112:119], v4\nv_smfmac_f32_16x16x64_bf16 v[56:59], v[80:83], a[120:127], v4\nbuffer_load_dwordx4 v0, s[36:39], s60 offen offset:3072 sc1 lds\nv_smfmac_f32_16x16x64_bf16 v[60:63], v[80:83], a[128:135], v4\ns_waitcnt lgkmcnt(6)\ns_add_u32 m0, %[l0], 12288\nds_read_b128 v[80:83], v0 offset:24576\nv_smfmac_f32_16x16x64_bf16 v[48:51], v[84:87], a[136:143], v4\n" \
"s_add_u32 s60, s61, 20480\ns_waitcnt lgkmcnt(5)\nbuffer_load_dwordx4 v0, s[36:39], s60 offen offset:0 sc1 lds\nv_smfmac_f32_16x16x64_bf16 v[52:55], v[84:87], v[64:71], v4\nds_read_b128 v[64:67], v1 offset:10240\nds_read_b128 v[68:71], v1 offset:11264\nv_smfmac_f32_16x16x64_bf16 v[56:59], v[84:87], a[144:151], v4\nbuffer_load_dwordx4 v0, s[36:39], s60 offen offset:1024 sc1 lds\n" \
"v_smfmac_f32_16x16x64_bf16 v[60:63], v[84:87], a[152:159], v4\ns_waitcnt lgkmcnt(6)\nds_read_b128 v[84:87], v0 offset:25600\nv_smfmac_f32_16x16x64_bf16 v[48:51], v[88:91], a[160:167], v4\nbuffer_load_dwordx4 v0, s[36:39], s60 offen offset:2048 sc1 lds\nv_smfmac_f32_16x16x64_bf16 v[52:55], v[88:91], a[168:175], v4\ns_waitcnt lgkmcnt(4)\nv_smfmac_f32_16x16x64_bf16 v[56:59], v[88:91], v[72:79], v4\n" \
"ds_read_b128 v[72:75], v1 offset:12288\nds_read_b128 v[76:79], v1 offset:13312\nbuffer_load_dwordx4 v0, s[36:39], s60 offen offset:3072 sc1 lds\nv_smfmac_f32_16x16x64_bf16 v[60:63], v[88:91], a[176:183], v4\nds_read_b128 v[88:91], v0 offset:26624\nv_smfmac_f32_16x16x64_bf16 v[48:51], v[92:95], a[184:191], v4\nv_smfmac_f32_16x16x64_bf16 v[52:55], v[92:95], a[192:199], v4\nv_smfmac_f32_16x16x64_bf16 v[56:59], v[92:95], a[200:207], v4\n" \
"s_waitcnt lgkmcnt(4)\nv_smfmac_f32_16x16x64_bf16 v[60:63], v[92:95], v[64:71], v4\nds_read_b128 v[92:95], v0 offset:27648\nds_read_b128 v[64:67], v1 offset:14336\nds_read_b128 v[68:71], v1 offset:15360\nv_smfmac_f32_16x16x64_bf16 v[48:51], v[80:83], a[208:215], v4\nv_smfmac_f32_16x16x64_bf16 v[52:55], v[80:83], a[216:223], v4\nv_smfmac_f32_16x16x64_bf16 v[56:59], v[80:83], a[224:231], v4\n" \
"v_smfmac_f32_16x16x64_bf16 v[60:63], v[80:83], a[232:239], v4\ns_waitcnt lgkmcnt(6)\nv_smfmac_f32_16x16x64_bf16 v[48:51], v[84:87], a[240:247], v4\ns_waitcnt lgkmcnt(4)\nv_smfmac_f32_16x16x64_bf16 v[52:55], v[84:87], v[72:79], v4\nds_read_b128 v[72:75], v1 offset:16384\nds_read_b128 v[76:79], v1 offset:17408\nv_smfmac_f32_16x16x64_bf16 v[56:59], v[84:87], a[248:255], v4\n" \
"v_smfmac_f32_16x16x64_bf16 v[60:63], v[84:87], v[96:103], v4\ns_waitcnt lgkmcnt(5)\nv_smfmac_f32_16x16x64_bf16 v[48:51], v[88:91], v[104:111], v4\nv_smfmac_f32_16x16x64_bf16 v[52:55], v[88:91], v[112:119], v4\ns_waitcnt lgkmcnt(4)\nv_smfmac_f32_16x16x64_bf16 v[48:51], v[92:95], v[128:135], v4\nv_smfmac_f32_16x16x64_bf16 v[52:55], v[92:95], v[136:143], v4\ns_waitcnt lgkmcnt(2)\n" \
"v_smfmac_f32_16x16x64_bf16 v[56:59], v[88:91], v[64:71], v4\nv_add_f32 v48, v48, v50\nv_smfmac_f32_16x16x64_bf16 v[60:63], v[88:91], v[120:127], v4\nv_add_f32 v52, v52, v54\nv_add_f32 v49, v49, v51\nv_smfmac_f32_16x16x64_bf16 v[56:59], v[92:95], v[144:151], v4\ns_waitcnt lgkmcnt(0)\nv_add_f32 v53, v53, v55\n" \
"v_cvt_pk_bf16_f32 v50, v48, v52\nv_smfmac_f32_16x16x64_bf16 v[60:63], v[92:95], v[72:79], v4\nv_cvt_pk_bf16_f32 v51, v49, v53\nds_write2_b32 v6, v50, v51 offset0:0 offset1:4\ns_nop 1\nv_add_f32 v56, v56, v58\nv_add_f32 v57, v57, v59\ns_nop 1\n" \
"v_add_f32 v60, v60, v62\nv_add_f32 v61, v61, v63\nv_cvt_pk_bf16_f32 v62, v56, v60\nv_cvt_pk_bf16_f32 v63, v57, v61\nds_write2_b32 v6, v62, v63 offset0:36 offset1:40\ns_waitcnt vmcnt(0)\ns_waitcnt lgkmcnt(0)\ns_barrier\n" \
"s_cbranch_vccnz .Lqk_slow%=\nds_read_b128 v[80:83], v2 offset:0\nds_read_b128 v[84:87], v2 offset:256\nds_read_b128 v[88:91], v2 offset:2048\ns_add_u32 m0, %[l0], 16384\ns_add_u32 s60, s41, 32768\nbuffer_load_dwordx4 v0, s[36:39], s60 offen offset:0 sc1 lds\nbuffer_load_dwordx4 v0, s[36:39], s60 offen offset:1024 sc1 lds\n" \
"buffer_load_dwordx4 v0, s[36:39], s60 offen offset:2048 sc1 lds\nbuffer_load_dwordx4 v0, s[36:39], s60 offen offset:3072 sc1 lds\ns_add_u32 m0, %[l0], 20480\ns_add_u32 s60, s41, 36864\nbuffer_load_dwordx4 v0, s[36:39], s60 offen offset:0 sc1 lds\nbuffer_load_dwordx4 v0, s[36:39], s60 offen offset:1024 sc1 lds\nbuffer_load_dwordx4 v0, s[36:39], s60 offen offset:2048 sc1 lds\nbuffer_load_dwordx4 v0, s[36:39], s60 offen offset:3072 sc1 lds\n" \
"s_add_u32 m0, %[l0], 24576\ns_add_u32 s60, s41, 49152\nbuffer_load_dwordx4 v0, s[36:39], s60 offen offset:0 sc1 lds\nbuffer_load_dwordx4 v0, s[36:39], s60 offen offset:1024 sc1 lds\nbuffer_load_dwordx4 v0, s[36:39], s60 offen offset:2048 sc1 lds\nbuffer_load_dwordx4 v0, s[36:39], s60 offen offset:3072 sc1 lds\ns_add_u32 m0, %[l0], 28672\ns_add_u32 s60, s41, 53248\n" \
"buffer_load_dwordx4 v0, s[36:39], s60 offen offset:0 sc1 lds\nbuffer_load_dwordx4 v0, s[36:39], s60 offen offset:1024 sc1 lds\nbuffer_load_dwordx4 v0, s[36:39], s60 offen offset:2048 sc1 lds\nbuffer_load_dwordx4 v0, s[36:39], s60 offen offset:3072 sc1 lds\ns_waitcnt lgkmcnt(2)\nv_mfma_f32_16x16x32_bf16 v[16:19], a[0:3], v[80:83], 0\nds_read_b128 v[92:95], v2 offset:2304\nv_mfma_f32_16x16x32_bf16 v[20:23], a[8:11], v[80:83], 0\n" \
"ds_read_b128 v[72:75], v1 offset:0\nv_mfma_f32_16x16x32_bf16 v[24:27], a[16:19], v[80:83], 0\nv_mfma_f32_16x16x32_bf16 v[28:31], a[24:27], v[80:83], 0\ns_waitcnt lgkmcnt(3)\nv_mfma_f32_16x16x32_bf16 v[32:35], a[0:3], v[84:87], 0\nds_read_b128 v[80:83], v2 offset:4096\nv_mfma_f32_16x16x32_bf16 v[36:39], a[8:11], v[84:87], 0\nv_mfma_f32_16x16x32_bf16 v[40:43], a[16:19], v[84:87], 0\n" \
"v_mfma_f32_16x16x32_bf16 v[44:47], a[24:27], v[84:87], 0\ns_waitcnt lgkmcnt(3)\nv_mfma_f32_16x16x32_bf16 v[16:19], a[4:7], v[88:91], v[16:19]\nds_read_b128 v[84:87], v2 offset:4352\nv_mfma_f32_16x16x32_bf16 v[20:23], a[12:15], v[88:91], v[20:23]\nds_read_b128 v[76:79], v1 offset:1024\nv_mfma_f32_16x16x32_bf16 v[24:27], a[20:23], v[88:91], v[24:27]\nv_mfma_f32_16x16x32_bf16 v[28:31], a[28:31], v[88:91], v[28:31]\n" \
"s_waitcnt lgkmcnt(4)\nv_mfma_f32_16x16x32_bf16 v[32:35], a[4:7], v[92:95], v[32:35]\nds_read_b128 v[88:91], v2 offset:6144\nv_mfma_f32_16x16x32_bf16 v[36:39], a[12:15], v[92:95], v[36:39]\nv_mfma_f32_16x16x32_bf16 v[40:43], a[20:23], v[92:95], v[40:43]\nv_mfma_f32_16x16x32_bf16 v[44:47], a[28:31], v[92:95], v[44:47]\ns_waitcnt lgkmcnt(3)\nv_mfma_f32_16x16x32_bf16 v[16:19], a[32:35], v[80:83], v[16:19]\n" \
"ds_read_b128 v[92:95], v2 offset:6400\nv_mfma_f32_16x16x32_bf16 v[20:23], v[72:75], v[80:83], v[20:23]\nds_read_b128 v[64:67], v1 offset:2048\nv_mfma_f32_16x16x32_bf16 v[24:27], a[40:43], v[80:83], v[24:27]\nv_mfma_f32_16x16x32_bf16 v[28:31], a[48:51], v[80:83], v[28:31]\ns_waitcnt lgkmcnt(4)\nv_mfma_f32_16x16x32_bf16 v[32:35], a[32:35], v[84:87], v[32:35]\nds_read_b128 v[80:83], v2 offset:8192\n" \
"v_mfma_f32_16x16x32_bf16 v[36:39], v[72:75], v[84:87], v[36:39]\nv_mfma_f32_16x16x32_bf16 v[40:43], a[40:43], v[84:87], v[40:43]\nv_mfma_f32_16x16x32_bf16 v[44:47], a[48:51], v[84:87], v[44:47]\ns_waitcnt lgkmcnt(3)\nv_mfma_f32_16x16x32_bf16 v[16:19], a[36:39], v[88:91], v[16:19]\nds_read_b128 v[84:87], v2 offset:8448\nv_mfma_f32_16x16x32_bf16 v[20:23], v[76:79], v[88:91], v[20:23]\nds_read_b128 v[68:71], v1 offset:3072\n" \
"v_mfma_f32_16x16x32_bf16 v[24:27], a[44:47], v[88:91], v[24:27]\nv_mfma_f32_16x16x32_bf16 v[28:31], a[52:55], v[88:91], v[28:31]\ns_waitcnt lgkmcnt(4)\nv_mfma_f32_16x16x32_bf16 v[32:35], a[36:39], v[92:95], v[32:35]\nds_read_b128 v[88:91], v2 offset:10240\nv_mfma_f32_16x16x32_bf16 v[36:39], v[76:79], v[92:95], v[36:39]\nv_mfma_f32_16x16x32_bf16 v[40:43], a[44:47], v[92:95], v[40:43]\nv_mfma_f32_16x16x32_bf16 v[44:47], a[52:55], v[92:95], v[44:47]\n" \
"s_waitcnt lgkmcnt(3)\nv_mfma_f32_16x16x32_bf16 v[16:19], a[56:59], v[80:83], v[16:19]\nds_read_b128 v[92:95], v2 offset:10496\nv_mfma_f32_16x16x32_bf16 v[20:23], a[64:67], v[80:83], v[20:23]\nds_read_b128 v[72:75], v1 offset:4096\nv_mfma_f32_16x16x32_bf16 v[24:27], v[64:67], v[80:83], v[24:27]\nv_mfma_f32_16x16x32_bf16 v[28:31], a[72:75], v[80:83], v[28:31]\ns_waitcnt lgkmcnt(4)\n" \
"v_mfma_f32_16x16x32_bf16 v[32:35], a[56:59], v[84:87], v[32:35]\nds_read_b128 v[80:83], v2 offset:12288\nv_mfma_f32_16x16x32_bf16 v[36:39], a[64:67], v[84:87], v[36:39]\nv_mfma_f32_16x16x32_bf16 v[40:43], v[64:67], v[84:87], v[40:43]\nv_mfma_f32_16x16x32_bf16 v[44:47], a[72:75], v[84:87], v[44:47]\ns_waitcnt lgkmcnt(3)\nv_mfma_f32_16x16x32_bf16 v[16:19], a[60:63], v[88:91], v[16:19]\nds_read_b128 v[84:87], v2 offset:12544\n" \
"v_mfma_f32_16x16x32_bf16 v[20:23], a[68:71], v[88:91], v[20:23]\nds_read_b128 v[76:79], v1 offset:5120\nv_mfma_f32_16x16x32_bf16 v[24:27], v[68:71], v[88:91], v[24:27]\nv_mfma_f32_16x16x32_bf16 v[28:31], a[76:79], v[88:91], v[28:31]\ns_waitcnt lgkmcnt(4)\nv_mfma_f32_16x16x32_bf16 v[32:35], a[60:63], v[92:95], v[32:35]\nds_read_b128 v[88:91], v2 offset:14336\nv_mfma_f32_16x16x32_bf16 v[36:39], a[68:71], v[92:95], v[36:39]\n" \
"v_mfma_f32_16x16x32_bf16 v[40:43], v[68:71], v[92:95], v[40:43]\nv_mfma_f32_16x16x32_bf16 v[44:47], a[76:79], v[92:95], v[44:47]\ns_waitcnt vmcnt(0)\ns_branch .Lqk_pj%=\n.Lqk_slow%=:\ns_cmp_ge_i32 s40, 63\ns_cbranch_scc1 .Lqk_end%=\n.Lqk_fetch0%=:\n" \
"buffer_load_dword v16, v12, s[36:39], s56 offen sc1\ns_waitcnt vmcnt(0)\nv_cmp_ge_i32 vcc, s40, v16\ns_cbranch_vccz .Lqk_fok%=\ns_add_u32 s43, s43, 1\ns_cmp_lt_u32 s43, 262144\ns_cbranch_scc1 .Lqk_fetch0%=\ns_branch .Lqk_abort%=\n" \
".Lqk_fok%=:\ns_add_u32 s44, s40, 1\ns_lshl_b32 s44, s44, 17\ns_add_u32 s44, s44, %[xn]\ns_lshr_b32 s45, s44, 8\ns_and_b32 s45, s45, 0x2000\ns_xor_b32 s41, s44, s45\ns_add_u32 m0, %[l0], 0\n" \
"s_add_u32 s60, s41, 0\nbuffer_load_dwordx4 v0, s[36:39], s60 offen offset:0 sc1 lds\nbuffer_load_dwordx4 v0, s[36:39], s60 offen offset:1024 sc1 lds\nbuffer_load_dwordx4 v0, s[36:39], s60 offen offset:2048 sc1 lds\nbuffer_load_dwordx4 v0, s[36:39], s60 offen offset:3072 sc1 lds\ns_add_u32 m0, %[l0], 4096\ns_add_u32 s60, s41, 4096\nbuffer_load_dwordx4 v0, s[36:39], s60 offen offset:0 sc1 lds\n" \
"buffer_load_dwordx4 v0, s[36:39], s60 offen offset:1024 sc1 lds\nbuffer_load_dwordx4 v0, s[36:39], s60 offen offset:2048 sc1 lds\nbuffer_load_dwordx4 v0, s[36:39], s60 offen offset:3072 sc1 lds\ns_add_u32 m0, %[l0], 8192\ns_add_u32 s60, s41, 16384\nbuffer_load_dwordx4 v0, s[36:39], s60 offen offset:0 sc1 lds\nbuffer_load_dwordx4 v0, s[36:39], s60 offen offset:1024 sc1 lds\nbuffer_load_dwordx4 v0, s[36:39], s60 offen offset:2048 sc1 lds\n" \
"buffer_load_dwordx4 v0, s[36:39], s60 offen offset:3072 sc1 lds\ns_add_u32 m0, %[l0], 12288\ns_add_u32 s60, s41, 20480\nbuffer_load_dwordx4 v0, s[36:39], s60 offen offset:0 sc1 lds\nbuffer_load_dwordx4 v0, s[36:39], s60 offen offset:1024 sc1 lds\nbuffer_load_dwordx4 v0, s[36:39], s60 offen offset:2048 sc1 lds\nbuffer_load_dwordx4 v0, s[36:39], s60 offen offset:3072 sc1 lds\ns_add_u32 m0, %[l0], 16384\n" \
"s_add_u32 s60, s41, 32768\nbuffer_load_dwordx4 v0, s[36:39], s60 offen offset:0 sc1 lds\nbuffer_load_dwordx4 v0, s[36:39], s60 offen offset:1024 sc1 lds\nbuffer_load_dwordx4 v0, s[36:39], s60 offen offset:2048 sc1 lds\nbuffer_load_dwordx4 v0, s[36:39], s60 offen offset:3072 sc1 lds\ns_add_u32 m0, %[l0], 20480\ns_add_u32 s60, s41, 36864\nbuffer_load_dwordx4 v0, s[36:39], s60 offen offset:0 sc1 lds\n" \
"buffer_load_dwordx4 v0, s[36:39], s60 offen offset:1024 sc1 lds\nbuffer_load_dwordx4 v0, s[36:39], s60 offen offset:2048 sc1 lds\nbuffer_load_dwordx4 v0, s[36:39], s60 offen offset:3072 sc1 lds\ns_add_u32 m0, %[l0], 24576\ns_add_u32 s60, s41, 49152\nbuffer_load_dwordx4 v0, s[36:39], s60 offen offset:0 sc1 lds\nbuffer_load_dwordx4 v0, s[36:39], s60 offen offset:1024 sc1 lds\nbuffer_load_dwordx4 v0, s[36:39], s60 offen offset:2048 sc1 lds\n" \
"buffer_load_dwordx4 v0, s[36:39], s60 offen offset:3072 sc1 lds\ns_add_u32 m0, %[l0], 28672\ns_add_u32 s60, s41, 53248\nbuffer_load_dwordx4 v0, s[36:39], s60 offen offset:0 sc1 lds\nbuffer_load_dwordx4 v0, s[36:39], s60 offen offset:1024 sc1 lds\nbuffer_load_dwordx4 v0, s[36:39], s60 offen offset:2048 sc1 lds\nbuffer_load_dwordx4 v0, s[36:39], s60 offen offset:3072 sc1 lds\ns_waitcnt vmcnt(0)\n" \
"s_branch .Lqk_pwait%=\n.Lqk_re3%=:\nv_max_u32 v17, v80, v82\nv_cmp_eq_u32 vcc, -1, v17\ns_cbranch_vccz .Lqk_sk2838%=\ns_mov_b64 exec, vcc\nbuffer_load_dwordx4 v[80:83], v3, s[36:39], s42 offen offset:0 sc1\ns_mov_b64 exec, -1\n" \
".Lqk_sk2838%=:\nv_max_u32 v17, v84, v86\nv_cmp_eq_u32 vcc, -1, v17\ns_cbranch_vccz .Lqk_sk2845%=\ns_mov_b64 exec, vcc\nbuffer_load_dwordx4 v[84:87], v3, s[36:39], s42 offen offset:1024 sc1\ns_mov_b64 exec, -1\n.Lqk_sk2845%=:\n" \
"v_max_u32 v17, v88, v90\nv_cmp_eq_u32 vcc, -1, v17\ns_cbranch_vccz .Lqk_sk2852%=\ns_mov_b64 exec, vcc\nbuffer_load_dwordx4 v[88:91], v3, s[36:39], s42 offen offset:2048 sc1\ns_mov_b64 exec, -1\n.Lqk_sk2852%=:\nv_max_u32 v17, v92, v94\n" \
"v_cmp_eq_u32 vcc, -1, v17\ns_cbranch_vccz .Lqk_sk2859%=\ns_mov_b64 exec, vcc\nbuffer_load_dwordx4 v[92:95], v3, s[36:39], s42 offen offset:3072 sc1\ns_mov_b64 exec, -1\n.Lqk_sk2859%=:\nbuffer_load_dword v16, v12, s[36:39], s56 offen sc1\nv_mov_b64 v[48:49], 0\n" \
"v_mov_b64 v[50:51], 0\nv_mov_b64 v[52:53], 0\nv_mov_b64 v[54:55], 0\nv_mov_b64 v[56:57], 0\nv_mov_b64 v[58:59], 0\nv_mov_b64 v[60:61], 0\nv_mov_b64 v[62:63], 0\ns_add_u32 s43, s43, 1\n" \
"s_cmp_lt_u32 s43, 262144\ns_cbranch_scc1 .Lqk_poll3%=\ns_branch .Lqk_abort%=\n.Lqk_abort%=:\ns_mov_b64 exec, -1\nv_mov_b32 v16, 0\ns_movk_i32 s44, 6400\nbuffer_store_dword v16, off, s[36:39], s44 sc1\n" \
".Lqk_end%=:\ns_waitcnt vmcnt(0) lgkmcnt(0)\n" \
""
#define VU_ASM \
"v_mbcnt_lo_u32_b32 v255, -1, 0\nv_mbcnt_hi_u32_b32 v255, -1, v255\nv_lshlrev_b32 v255, 2, v255\nv_add_u32 v255, %[tb], v255\nds_read_b32 v0, v255 offset:0\nds_read_b32 v1, v255 offset:256\nds_read_b32 v2, v255 offset:512\nds_read_b32 v3, v255 offset:768\n" \
"ds_read_b32 v4, v255 offset:1024\nds_read_b32 v5, v255 offset:1280\nds_read_b32 v6, v255 offset:1536\nds_read_b32 v7, v255 offset:1792\nds_read_b32 v8, v255 offset:2048\nds_read_b32 v9, v255 offset:2304\nds_read_b32 v10, v255 offset:2560\nds_read_b32 v11, v255 offset:2816\n" \
"ds_read_b32 v90, v255 offset:3072\nds_read_b32 v91, v255 offset:3328\nds_read_b32 v92, v255 offset:3584\nds_read_b32 v93, v255 offset:3840\nds_read_b32 v94, v255 offset:4096\nds_read_b32 v95, v255 offset:4352\ns_waitcnt lgkmcnt(0)\ns_mov_b64 s[36:37], %[ws]\n" \
"s_mov_b32 s38, %[sz]\ns_mov_b32 s39, 0x20000\ns_mov_b32 s45, 2096896\nbuffer_load_dword v15, off, s[36:39], s45 sc1\ns_add_u32 m0, %[lw], 16384\ns_add_u32 s44, %[wo], 0\nbuffer_load_dwordx4 v90, %[rw], s44 offen nt lds\ns_add_u32 m0, %[lw], 17408\n" \
"s_add_u32 s44, %[wo], 65536\nbuffer_load_dwordx4 v90, %[rw], s44 offen nt lds\ns_add_u32 m0, %[lw], 18432\ns_add_u32 s44, %[wo], 131072\nbuffer_load_dwordx4 v90, %[rw], s44 offen nt lds\ns_add_u32 m0, %[lw], 19456\ns_add_u32 s44, %[wo], 196608\nbuffer_load_dwordx4 v90, %[rw], s44 offen nt lds\n" \
"s_add_u32 m0, %[lw], 20480\ns_add_u32 s44, %[wo], 524288\nbuffer_load_dwordx4 v90, %[rw], s44 offen nt lds\ns_add_u32 m0, %[lw], 21504\ns_add_u32 s44, %[wo], 589824\nbuffer_load_dwordx4 v90, %[rw], s44 offen nt lds\ns_add_u32 m0, %[lw], 22528\ns_add_u32 s44, %[wo], 655360\n" \
"buffer_load_dwordx4 v90, %[rw], s44 offen nt lds\ns_add_u32 m0, %[lw], 23552\ns_add_u32 s44, %[wo], 720896\nbuffer_load_dwordx4 v90, %[rw], s44 offen nt lds\ns_add_u32 m0, %[tb], 0\ns_add_u32 s44, %[wo], 1048576\nbuffer_load_dwordx4 v90, %[rw], s44 offen nt lds\ns_add_u32 m0, %[tb], 1024\n" \
"s_add_u32 s44, %[wo], 1114112\nbuffer_load_dwordx4 v90, %[rw], s44 offen nt lds\ns_add_u32 m0, %[tb], 2048\ns_add_u32 s44, %[wo], 1179648\nbuffer_load_dwordx4 v90, %[rw], s44 offen nt lds\ns_add_u32 m0, %[tb], 3072\ns_add_u32 s44, %[wo], 1245184\nbuffer_load_dwordx4 v90, %[rw], s44 offen nt lds\n" \
"s_add_u32 m0, %[tb], 4096\ns_add_u32 s44, %[wo], 1572864\nbuffer_load_dwordx4 v90, %[rw], s44 offen nt lds\ns_add_u32 m0, %[tb], 5120\ns_add_u32 s44, %[wo], 1638400\nbuffer_load_dwordx4 v90, %[rw], s44 offen nt lds\ns_add_u32 m0, %[tb], 6144\ns_add_u32 s44, %[wo], 1703936\n" \
"buffer_load_dwordx4 v90, %[rw], s44 offen nt lds\ns_add_u32 m0, %[tb], 7168\ns_add_u32 s44, %[wo], 1769472\nbuffer_load_dwordx4 v90, %[rw], s44 offen nt lds\ns_add_u32 m0, %[tb], 8192\ns_add_u32 s44, %[wo], 2097152\nbuffer_load_dwordx4 v90, %[rw], s44 offen nt lds\ns_add_u32 m0, %[tb], 9216\n" \
"s_add_u32 s44, %[wo], 2162688\nbuffer_load_dwordx4 v90, %[rw], s44 offen nt lds\ns_add_u32 m0, %[tb], 10240\ns_add_u32 s44, %[wo], 2228224\nbuffer_load_dwordx4 v90, %[rw], s44 offen nt lds\ns_add_u32 m0, %[tb], 11264\ns_add_u32 s44, %[wo], 2293760\nbuffer_load_dwordx4 v90, %[rw], s44 offen nt lds\n" \
"s_add_u32 m0, %[lw], 0\ns_add_u32 s44, %[wo], 2621440\nbuffer_load_dwordx4 v90, %[rw], s44 offen nt lds\ns_add_u32 m0, %[lw], 1024\ns_add_u32 s44, %[wo], 2686976\nbuffer_load_dwordx4 v90, %[rw], s44 offen nt lds\ns_add_u32 m0, %[lw], 2048\ns_add_u32 s44, %[wo], 2752512\n" \
"buffer_load_dwordx4 v90, %[rw], s44 offen nt lds\ns_add_u32 m0, %[lw], 3072\ns_add_u32 s44, %[wo], 2818048\nbuffer_load_dwordx4 v90, %[rw], s44 offen nt lds\ns_add_u32 m0, %[lw], 4096\ns_add_u32 s44, %[wo], 3145728\nbuffer_load_dwordx4 v90, %[rw], s44 offen nt lds\ns_add_u32 m0, %[lw], 5120\n" \
"s_add_u32 s44, %[wo], 3211264\nbuffer_load_dwordx4 v90, %[rw], s44 offen nt lds\ns_add_u32 m0, %[lw], 6144\ns_add_u32 s44, %[wo], 3276800\nbuffer_load_dwordx4 v90, %[rw], s44 offen nt lds\ns_add_u32 m0, %[lw], 7168\ns_add_u32 s44, %[wo], 3342336\nbuffer_load_dwordx4 v90, %[rw], s44 offen nt lds\n" \
"s_add_u32 m0, %[lw], 8192\ns_add_u32 s44, %[wo], 3670016\nbuffer_load_dwordx4 v90, %[rw], s44 offen nt lds\ns_add_u32 m0, %[lw], 9216\ns_add_u32 s44, %[wo], 3735552\nbuffer_load_dwordx4 v90, %[rw], s44 offen nt lds\ns_add_u32 m0, %[lw], 10240\ns_add_u32 s44, %[wo], 3801088\n" \
"buffer_load_dwordx4 v90, %[rw], s44 offen nt lds\ns_add_u32 m0, %[lw], 11264\ns_add_u32 s44, %[wo], 3866624\nbuffer_load_dwordx4 v90, %[rw], s44 offen nt lds\ns_waitcnt vmcnt(28)\ns_barrier\ns_add_u32 m0, %[lw], 12288\ns_add_u32 s44, %[wo], 4194304\n" \
"buffer_load_dwordx4 v90, %[rw], s44 offen nt lds\ns_add_u32 m0, %[lw], 13312\ns_add_u32 s44, %[wo], 4259840\nbuffer_load_dwordx4 v90, %[rw], s44 offen nt lds\ns_add_u32 m0, %[lw], 14336\ns_add_u32 s44, %[wo], 4325376\nbuffer_load_dwordx4 v90, %[rw], s44 offen nt lds\ns_add_u32 m0, %[lw], 15360\n" \
"s_add_u32 s44, %[wo], 4390912\nbuffer_load_dwordx4 v90, %[rw], s44 offen nt lds\nds_read_b64_tr_b16 a[0:1], v93 offset:16384\nds_read_b64_tr_b16 a[2:3], v93 offset:16896\nds_read_b64_tr_b16 a[16:17], v93 offset:16392\nds_read_b64_tr_b16 a[18:19], v93 offset:16904\nds_read_b64_tr_b16 a[4:5], v93 offset:40960\nds_read_b64_tr_b16 a[6:7], v93 offset:41472\n" \
"ds_read_b64_tr_b16 a[20:21], v93 offset:40968\nds_read_b64_tr_b16 a[22:23], v93 offset:41480\ns_waitcnt lgkmcnt(0)\ns_waitcnt vmcnt(28)\ns_barrier\ns_add_u32 m0, %[lw], 16384\ns_add_u32 s44, %[wo], 4718592\nbuffer_load_dwordx4 v90, %[rw], s44 offen nt lds\n" \
"s_add_u32 m0, %[lw], 17408\ns_add_u32 s44, %[wo], 4784128\nbuffer_load_dwordx4 v90, %[rw], s44 offen nt lds\ns_add_u32 m0, %[lw], 18432\ns_add_u32 s44, %[wo], 4849664\nbuffer_load_dwordx4 v90, %[rw], s44 offen nt lds\ns_add_u32 m0, %[lw], 19456\ns_add_u32 s44, %[wo], 4915200\n" \
"buffer_load_dwordx4 v90, %[rw], s44 offen nt lds\nds_read_b64_tr_b16 a[24:25], v93 offset:20480\nds_read_b64_tr_b16 a[26:27], v93 offset:20992\nds_read_b64_tr_b16 a[40:41], v93 offset:20488\nds_read_b64_tr_b16 a[42:43], v93 offset:21000\nds_read_b64_tr_b16 a[28:29], v93 offset:45056\nds_read_b64_tr_b16 a[30:31], v93 offset:45568\nds_read_b64_tr_b16 a[44:45], v93 offset:45064\n" \
"ds_read_b64_tr_b16 a[46:47], v93 offset:45576\ns_waitcnt lgkmcnt(0)\ns_waitcnt vmcnt(28)\ns_barrier\ns_add_u32 m0, %[lw], 20480\ns_add_u32 s44, %[wo], 5242880\nbuffer_load_dwordx4 v90, %[rw], s44 offen nt lds\ns_add_u32 m0, %[lw], 21504\n" \
"s_add_u32 s44, %[wo], 5308416\nbuffer_load_dwordx4 v90, %[rw], s44 offen nt lds\ns_add_u32 m0, %[lw], 22528\ns_add_u32 s44, %[wo], 5373952\nbuffer_load_dwordx4 v90, %[rw], s44 offen nt lds\ns_add_u32 m0, %[lw], 23552\ns_add_u32 s44, %[wo], 5439488\nbuffer_load_dwordx4 v90, %[rw], s44 offen nt lds\n" \
"ds_read_b64_tr_b16 a[56:57], v92 offset:0\nds_read_b64_tr_b16 a[58:59], v92 offset:512\nds_read_b64_tr_b16 a[72:73], v92 offset:8\nds_read_b64_tr_b16 a[74:75], v92 offset:520\nds_read_b64_tr_b16 a[60:61], v92 offset:12288\nds_read_b64_tr_b16 a[62:63], v92 offset:12800\nds_read_b64_tr_b16 a[76:77], v92 offset:12296\nds_read_b64_tr_b16 a[78:79], v92 offset:12808\n" \
"s_waitcnt lgkmcnt(0)\ns_waitcnt vmcnt(28)\ns_barrier\ns_add_u32 m0, %[tb], 0\ns_add_u32 s44, %[wo], 5767168\nbuffer_load_dwordx4 v90, %[rw], s44 offen nt lds\ns_add_u32 m0, %[tb], 1024\ns_add_u32 s44, %[wo], 5832704\n" \
"buffer_load_dwordx4 v90, %[rw], s44 offen nt lds\ns_add_u32 m0, %[tb], 2048\ns_add_u32 s44, %[wo], 5898240\nbuffer_load_dwordx4 v90, %[rw], s44 offen nt lds\ns_add_u32 m0, %[tb], 3072\ns_add_u32 s44, %[wo], 5963776\nbuffer_load_dwordx4 v90, %[rw], s44 offen nt lds\nds_read_b64_tr_b16 v[84:85], v92 offset:4096\n" \
"ds_read_b64_tr_b16 v[86:87], v92 offset:4608\nds_read_b64_tr_b16 a[88:89], v92 offset:4104\nds_read_b64_tr_b16 a[90:91], v92 offset:4616\nds_read_b64_tr_b16 v[80:81], v92 offset:16384\nds_read_b64_tr_b16 v[82:83], v92 offset:16896\nds_read_b64_tr_b16 a[92:93], v92 offset:16392\nds_read_b64_tr_b16 a[94:95], v92 offset:16904\ns_waitcnt lgkmcnt(0)\n" \
"s_waitcnt vmcnt(28)\ns_barrier\ns_add_u32 m0, %[tb], 4096\ns_add_u32 s44, %[wo], 6291456\nbuffer_load_dwordx4 v90, %[rw], s44 offen nt lds\ns_add_u32 m0, %[tb], 5120\ns_add_u32 s44, %[wo], 6356992\nbuffer_load_dwordx4 v90, %[rw], s44 offen nt lds\n" \
"s_add_u32 m0, %[tb], 6144\ns_add_u32 s44, %[wo], 6422528\nbuffer_load_dwordx4 v90, %[rw], s44 offen nt lds\ns_add_u32 m0, %[tb], 7168\ns_add_u32 s44, %[wo], 6488064\nbuffer_load_dwordx4 v90, %[rw], s44 offen nt lds\nds_read_b64_tr_b16 a[104:105], v92 offset:8192\nds_read_b64_tr_b16 a[106:107], v92 offset:8704\n" \
"ds_read_b64_tr_b16 a[120:121], v92 offset:8200\nds_read_b64_tr_b16 a[122:123], v92 offset:8712\nds_read_b64_tr_b16 a[108:109], v92 offset:20480\nds_read_b64_tr_b16 a[110:111], v92 offset:20992\nds_read_b64_tr_b16 a[124:125], v92 offset:20488\nds_read_b64_tr_b16 a[126:127], v92 offset:21000\ns_waitcnt lgkmcnt(0)\ns_waitcnt vmcnt(28)\n" \
"s_barrier\ns_add_u32 m0, %[tb], 8192\ns_add_u32 s44, %[wo], 6815744\nbuffer_load_dwordx4 v90, %[rw], s44 offen nt lds\ns_add_u32 m0, %[tb], 9216\ns_add_u32 s44, %[wo], 6881280\nbuffer_load_dwordx4 v90, %[rw], s44 offen nt lds\ns_add_u32 m0, %[tb], 10240\n" \
"s_add_u32 s44, %[wo], 6946816\nbuffer_load_dwordx4 v90, %[rw], s44 offen nt lds\ns_add_u32 m0, %[tb], 11264\ns_add_u32 s44, %[wo], 7012352\nbuffer_load_dwordx4 v90, %[rw], s44 offen nt lds\nds_read_b64_tr_b16 a[128:129], v93 offset:0\nds_read_b64_tr_b16 a[130:131], v93 offset:512\nds_read_b64_tr_b16 a[144:145], v93 offset:8\n" \
"ds_read_b64_tr_b16 a[146:147], v93 offset:520\nds_read_b64_tr_b16 a[132:133], v93 offset:24576\nds_read_b64_tr_b16 a[134:135], v93 offset:25088\nds_read_b64_tr_b16 a[148:149], v93 offset:24584\nds_read_b64_tr_b16 a[150:151], v93 offset:25096\ns_waitcnt lgkmcnt(0)\ns_waitcnt vmcnt(28)\ns_barrier\n" \
"s_add_u32 m0, %[lw], 0\ns_add_u32 s44, %[wo], 7340032\nbuffer_load_dwordx4 v90, %[rw], s44 offen nt lds\ns_add_u32 m0, %[lw], 1024\ns_add_u32 s44, %[wo], 7405568\nbuffer_load_dwordx4 v90, %[rw], s44 offen nt lds\ns_add_u32 m0, %[lw], 2048\ns_add_u32 s44, %[wo], 7471104\n" \
"buffer_load_dwordx4 v90, %[rw], s44 offen nt lds\ns_add_u32 m0, %[lw], 3072\ns_add_u32 s44, %[wo], 7536640\nbuffer_load_dwordx4 v90, %[rw], s44 offen nt lds\nds_read_b64_tr_b16 a[160:161], v93 offset:4096\nds_read_b64_tr_b16 a[162:163], v93 offset:4608\nds_read_b64_tr_b16 v[76:77], v93 offset:4104\nds_read_b64_tr_b16 v[78:79], v93 offset:4616\n" \
"ds_read_b64_tr_b16 a[164:165], v93 offset:28672\nds_read_b64_tr_b16 a[166:167], v93 offset:29184\nds_read_b64_tr_b16 v[72:73], v93 offset:28680\nds_read_b64_tr_b16 v[74:75], v93 offset:29192\ns_waitcnt lgkmcnt(0)\ns_waitcnt vmcnt(28)\ns_barrier\ns_add_u32 m0, %[lw], 4096\n" \
"s_add_u32 s44, %[wo], 7864320\nbuffer_load_dwordx4 v90, %[rw], s44 offen nt lds\ns_add_u32 m0, %[lw], 5120\ns_add_u32 s44, %[wo], 7929856\nbuffer_load_dwordx4 v90, %[rw], s44 offen nt lds\ns_add_u32 m0, %[lw], 6144\ns_add_u32 s44, %[wo], 7995392\nbuffer_load_dwordx4 v90, %[rw], s44 offen nt lds\n" \
"s_add_u32 m0, %[lw], 7168\ns_add_u32 s44, %[wo], 8060928\nbuffer_load_dwordx4 v90, %[rw], s44 offen nt lds\nds_read_b64_tr_b16 a[184:185], v93 offset:8192\nds_read_b64_tr_b16 a[186:187], v93 offset:8704\nds_read_b64_tr_b16 a[192:193], v93 offset:8200\nds_read_b64_tr_b16 a[194:195], v93 offset:8712\nds_read_b64_tr_b16 a[188:189], v93 offset:32768\n" \
"ds_read_b64_tr_b16 a[190:191], v93 offset:33280\nds_read_b64_tr_b16 a[196:197], v93 offset:32776\nds_read_b64_tr_b16 a[198:199], v93 offset:33288\ns_waitcnt lgkmcnt(0)\ns_waitcnt vmcnt(28)\ns_barrier\ns_add_u32 m0, %[lw], 8192\ns_add_u32 s44, %[w2], 0\n" \
"buffer_load_dwordx4 v91, %[ro], s44 offen nt lds\ns_add_u32 m0, %[lw], 9216\ns_add_u32 s44, %[w2], 32768\nbuffer_load_dwordx4 v91, %[ro], s44 offen nt lds\ns_add_u32 m0, %[lw], 10240\ns_add_u32 s44, %[w2], 65536\nbuffer_load_dwordx4 v91, %[ro], s44 offen nt lds\ns_add_u32 m0, %[lw], 11264\n" \
"s_add_u32 s44, %[w2], 98304\nbuffer_load_dwordx4 v91, %[ro], s44 offen nt lds\nds_read_b64_tr_b16 a[208:209], v93 offset:12288\nds_read_b64_tr_b16 a[210:211], v93 offset:12800\nds_read_b64_tr_b16 a[224:225], v93 offset:12296\nds_read_b64_tr_b16 a[226:227], v93 offset:12808\nds_read_b64_tr_b16 a[212:213], v93 offset:36864\nds_read_b64_tr_b16 a[214:215], v93 offset:37376\n" \
"ds_read_b64_tr_b16 a[228:229], v93 offset:36872\nds_read_b64_tr_b16 a[230:231], v93 offset:37384\ns_waitcnt lgkmcnt(0)\ns_waitcnt vmcnt(28)\ns_barrier\ns_add_u32 m0, %[lw], 12288\ns_add_u32 s44, %[w2], 128\nbuffer_load_dwordx4 v91, %[ro], s44 offen nt lds\n" \
"s_add_u32 m0, %[lw], 13312\ns_add_u32 s44, %[w2], 32896\nbuffer_load_dwordx4 v91, %[ro], s44 offen nt lds\ns_add_u32 m0, %[lw], 14336\ns_add_u32 s44, %[w2], 65664\nbuffer_load_dwordx4 v91, %[ro], s44 offen nt lds\ns_add_u32 m0, %[lw], 15360\ns_add_u32 s44, %[w2], 98432\n" \
"buffer_load_dwordx4 v91, %[ro], s44 offen nt lds\nds_read_b64_tr_b16 v[68:69], v93 offset:16384\nds_read_b64_tr_b16 v[70:71], v93 offset:16896\nds_read_b64_tr_b16 a[240:241], v93 offset:16392\nds_read_b64_tr_b16 a[242:243], v93 offset:16904\nds_read_b64_tr_b16 v[64:65], v93 offset:40960\nds_read_b64_tr_b16 v[66:67], v93 offset:41472\nds_read_b64_tr_b16 a[244:245], v93 offset:40968\n" \
"ds_read_b64_tr_b16 a[246:247], v93 offset:41480\ns_waitcnt lgkmcnt(0)\ns_waitcnt vmcnt(28)\ns_barrier\ns_add_u32 m0, %[lw], 16384\ns_add_u32 s44, %[w2], 256\nbuffer_load_dwordx4 v91, %[ro], s44 offen nt lds\ns_add_u32 m0, %[lw], 17408\n" \
"s_add_u32 s44, %[w2], 33024\nbuffer_load_dwordx4 v91, %[ro], s44 offen nt lds\ns_add_u32 m0, %[lw], 18432\ns_add_u32 s44, %[w2], 65792\nbuffer_load_dwordx4 v91, %[ro], s44 offen nt lds\ns_add_u32 m0, %[lw], 19456\ns_add_u32 s44, %[w2], 98560\nbuffer_load_dwordx4 v91, %[ro], s44 offen nt lds\n" \
"ds_read_b64_tr_b16 v[96:97], v93 offset:20480\nds_read_b64_tr_b16 v[98:99], v93 offset:20992\nds_read_b64_tr_b16 v[112:113], v93 offset:20488\nds_read_b64_tr_b16 v[114:115], v93 offset:21000\nds_read_b64_tr_b16 v[100:101], v93 offset:45056\nds_read_b64_tr_b16 v[102:103], v93 offset:45568\nds_read_b64_tr_b16 v[116:117], v93 offset:45064\nds_read_b64_tr_b16 v[118:119], v93 offset:45576\n" \
"s_waitcnt lgkmcnt(0)\ns_waitcnt vmcnt(28)\ns_barrier\ns_add_u32 m0, %[lw], 20480\ns_add_u32 s44, %[w2], 384\nbuffer_load_dwordx4 v91, %[ro], s44 offen nt lds\ns_add_u32 m0, %[lw], 21504\ns_add_u32 s44, %[w2], 33152\n" \
"buffer_load_dwordx4 v91, %[ro], s44 offen nt lds\ns_add_u32 m0, %[lw], 22528\ns_add_u32 s44, %[w2], 65920\nbuffer_load_dwordx4 v91, %[ro], s44 offen nt lds\ns_add_u32 m0, %[lw], 23552\ns_add_u32 s44, %[w2], 98688\nbuffer_load_dwordx4 v91, %[ro], s44 offen nt lds\nds_read_b64_tr_b16 v[128:129], v92 offset:0\n" \
"ds_read_b64_tr_b16 v[130:131], v92 offset:512\nds_read_b64_tr_b16 v[136:137], v92 offset:8\nds_read_b64_tr_b16 v[138:139], v92 offset:520\nds_read_b64_tr_b16 v[132:133], v92 offset:12288\nds_read_b64_tr_b16 v[134:135], v92 offset:12800\nds_read_b64_tr_b16 v[140:141], v92 offset:12296\nds_read_b64_tr_b16 v[142:143], v92 offset:12808\ns_waitcnt lgkmcnt(0)\n" \
"s_waitcnt vmcnt(28)\ns_barrier\ns_add_u32 m0, %[tb], 0\ns_add_u32 s44, %[w2], 139264\nbuffer_load_dwordx4 v91, %[ro], s44 offen nt lds\ns_add_u32 m0, %[tb], 1024\ns_add_u32 s44, %[w2], 172032\nbuffer_load_dwordx4 v91, %[ro], s44 offen nt lds\n" \
"s_add_u32 m0, %[tb], 2048\ns_add_u32 s44, %[w2], 204800\nbuffer_load_dwordx4 v91, %[ro], s44 offen nt lds\ns_add_u32 m0, %[tb], 3072\ns_add_u32 s44, %[w2], 237568\nbuffer_load_dwordx4 v91, %[ro], s44 offen nt lds\nds_read_b64_tr_b16 v[152:153], v92 offset:4096\nds_read_b64_tr_b16 v[154:155], v92 offset:4608\n" \
"ds_read_b64_tr_b16 v[60:61], v92 offset:4104\nds_read_b64_tr_b16 v[62:63], v92 offset:4616\nds_read_b64_tr_b16 v[156:157], v92 offset:16384\nds_read_b64_tr_b16 v[158:159], v92 offset:16896\nds_read_b64_tr_b16 v[56:57], v92 offset:16392\nds_read_b64_tr_b16 v[58:59], v92 offset:16904\ns_waitcnt lgkmcnt(0)\ns_waitcnt vmcnt(28)\n" \
"s_barrier\ns_add_u32 m0, %[tb], 4096\ns_add_u32 s44, %[w2], 139392\nbuffer_load_dwordx4 v91, %[ro], s44 offen nt lds\ns_add_u32 m0, %[tb], 5120\ns_add_u32 s44, %[w2], 172160\nbuffer_load_dwordx4 v91, %[ro], s44 offen nt lds\ns_add_u32 m0, %[tb], 6144\n" \
"s_add_u32 s44, %[w2], 204928\nbuffer_load_dwordx4 v91, %[ro], s44 offen nt lds\ns_add_u32 m0, %[tb], 7168\ns_add_u32 s44, %[w2], 237696\nbuffer_load_dwordx4 v91, %[ro], s44 offen nt lds\nds_read_b64_tr_b16 v[168:169], v92 offset:8192\nds_read_b64_tr_b16 v[170:171], v92 offset:8704\nds_read_b64_tr_b16 v[184:185], v92 offset:8200\n" \
"ds_read_b64_tr_b16 v[186:187], v92 offset:8712\nds_read_b64_tr_b16 v[172:173], v92 offset:20480\nds_read_b64_tr_b16 v[174:175], v92 offset:20992\nds_read_b64_tr_b16 v[188:189], v92 offset:20488\nds_read_b64_tr_b16 v[190:191], v92 offset:21000\ns_waitcnt lgkmcnt(0)\ns_waitcnt vmcnt(28)\ns_barrier\n" \
"s_add_u32 m0, %[tb], 8192\ns_add_u32 s44, %[w2], 139520\nbuffer_load_dwordx4 v91, %[ro], s44 offen nt lds\ns_add_u32 m0, %[tb], 9216\ns_add_u32 s44, %[w2], 172288\nbuffer_load_dwordx4 v91, %[ro], s44 offen nt lds\ns_add_u32 m0, %[tb], 10240\ns_add_u32 s44, %[w2], 205056\n" \
"buffer_load_dwordx4 v91, %[ro], s44 offen nt lds\ns_add_u32 m0, %[tb], 11264\ns_add_u32 s44, %[w2], 237824\nbuffer_load_dwordx4 v91, %[ro], s44 offen nt lds\nds_read_b64_tr_b16 v[200:201], v93 offset:0\nds_read_b64_tr_b16 v[202:203], v93 offset:512\nds_read_b64_tr_b16 v[216:217], v93 offset:8\nds_read_b64_tr_b16 v[218:219], v93 offset:520\n" \
"ds_read_b64_tr_b16 v[204:205], v93 offset:24576\nds_read_b64_tr_b16 v[206:207], v93 offset:25088\nds_read_b64_tr_b16 v[220:221], v93 offset:24584\nds_read_b64_tr_b16 v[222:223], v93 offset:25096\ns_waitcnt lgkmcnt(0)\ns_waitcnt vmcnt(28)\ns_barrier\ns_add_u32 m0, %[lw], 0\n" \
"s_add_u32 s44, %[w2], 139648\nbuffer_load_dwordx4 v91, %[ro], s44 offen nt lds\ns_add_u32 m0, %[lw], 1024\ns_add_u32 s44, %[w2], 172416\nbuffer_load_dwordx4 v91, %[ro], s44 offen nt lds\ns_add_u32 m0, %[lw], 2048\ns_add_u32 s44, %[w2], 205184\nbuffer_load_dwordx4 v91, %[ro], s44 offen nt lds\n" \
"s_add_u32 m0, %[lw], 3072\ns_add_u32 s44, %[w2], 237952\nbuffer_load_dwordx4 v91, %[ro], s44 offen nt lds\nds_read_b64_tr_b16 v[232:233], v93 offset:4096\nds_read_b64_tr_b16 v[234:235], v93 offset:4608\nds_read_b64_tr_b16 v[240:241], v93 offset:4104\nds_read_b64_tr_b16 v[242:243], v93 offset:4616\nds_read_b64_tr_b16 v[236:237], v93 offset:28672\n" \
"ds_read_b64_tr_b16 v[238:239], v93 offset:29184\nds_read_b64_tr_b16 v[244:245], v93 offset:28680\nds_read_b64_tr_b16 v[246:247], v93 offset:29192\ns_waitcnt lgkmcnt(0)\ns_waitcnt vmcnt(28)\ns_barrier\ns_add_u32 m0, %[lw], 4096\ns_add_u32 s44, %[w2], 131072\n" \
"buffer_load_dwordx4 v91, %[ro], s44 offen nt lds\ns_add_u32 m0, %[lw], 5120\ns_add_u32 s44, %[w2], 163840\nbuffer_load_dwordx4 v91, %[ro], s44 offen nt lds\ns_add_u32 m0, %[lw], 6144\ns_add_u32 s44, %[w2], 196608\nbuffer_load_dwordx4 v91, %[ro], s44 offen nt lds\ns_add_u32 m0, %[lw], 7168\n" \
"s_add_u32 s44, %[w2], 229376\nbuffer_load_dwordx4 v91, %[ro], s44 offen nt lds\nds_read_b64_tr_b16 a[8:9], v95 offset:8192\nds_read_b64_tr_b16 a[10:11], v95 offset:8448\nds_read_b64_tr_b16 a[12:13], v95 offset:10240\nds_read_b64_tr_b16 a[14:15], v95 offset:10496\nds_read_b64_tr_b16 a[216:217], v95 offset:8256\nds_read_b64_tr_b16 a[218:219], v95 offset:8512\n" \
"ds_read_b64_tr_b16 a[220:221], v95 offset:10304\nds_read_b64_tr_b16 a[222:223], v95 offset:10560\ns_waitcnt lgkmcnt(0)\ns_waitcnt vmcnt(28)\ns_barrier\ns_add_u32 m0, %[lw], 8192\ns_add_u32 s44, %[w2], 131200\nbuffer_load_dwordx4 v91, %[ro], s44 offen nt lds\n" \
"s_add_u32 m0, %[lw], 9216\ns_add_u32 s44, %[w2], 163968\nbuffer_load_dwordx4 v91, %[ro], s44 offen nt lds\ns_add_u32 m0, %[lw], 10240\ns_add_u32 s44, %[w2], 196736\nbuffer_load_dwordx4 v91, %[ro], s44 offen nt lds\ns_add_u32 m0, %[lw], 11264\ns_add_u32 s44, %[w2], 229504\n" \
"buffer_load_dwordx4 v91, %[ro], s44 offen nt lds\nds_read_b64_tr_b16 a[64:65], v95 offset:12288\nds_read_b64_tr_b16 a[66:67], v95 offset:12544\nds_read_b64_tr_b16 a[68:69], v95 offset:14336\nds_read_b64_tr_b16 a[70:71], v95 offset:14592\nds_read_b64_tr_b16 v[104:105], v95 offset:12352\nds_read_b64_tr_b16 v[106:107], v95 offset:12608\nds_read_b64_tr_b16 v[108:109], v95 offset:14400\n" \
"ds_read_b64_tr_b16 v[110:111], v95 offset:14656\ns_waitcnt lgkmcnt(0)\ns_waitcnt vmcnt(28)\ns_barrier\ns_add_u32 m0, %[lw], 12288\ns_add_u32 s44, %[w2], 131328\nbuffer_load_dwordx4 v91, %[ro], s44 offen nt lds\ns_add_u32 m0, %[lw], 13312\n" \
"s_add_u32 s44, %[w2], 164096\nbuffer_load_dwordx4 v91, %[ro], s44 offen nt lds\ns_add_u32 m0, %[lw], 14336\ns_add_u32 s44, %[w2], 196864\nbuffer_load_dwordx4 v91, %[ro], s44 offen nt lds\ns_add_u32 m0, %[lw], 15360\ns_add_u32 s44, %[w2], 229632\nbuffer_load_dwordx4 v91, %[ro], s44 offen nt lds\n" \
"ds_read_b64_tr_b16 a[112:113], v95 offset:16384\nds_read_b64_tr_b16 a[114:115], v95 offset:16640\nds_read_b64_tr_b16 a[116:117], v95 offset:18432\nds_read_b64_tr_b16 a[118:119], v95 offset:18688\nds_read_b64_tr_b16 v[160:161], v95 offset:16448\nds_read_b64_tr_b16 v[162:163], v95 offset:16704\nds_read_b64_tr_b16 v[164:165], v95 offset:18496\nds_read_b64_tr_b16 v[166:167], v95 offset:18752\n" \
"s_waitcnt lgkmcnt(0)\ns_waitcnt vmcnt(28)\ns_barrier\ns_add_u32 m0, %[lw], 16384\ns_add_u32 s44, %[w2], 131456\nbuffer_load_dwordx4 v91, %[ro], s44 offen nt lds\ns_add_u32 m0, %[lw], 17408\ns_add_u32 s44, %[w2], 164224\n" \
"buffer_load_dwordx4 v91, %[ro], s44 offen nt lds\ns_add_u32 m0, %[lw], 18432\ns_add_u32 s44, %[w2], 196992\nbuffer_load_dwordx4 v91, %[ro], s44 offen nt lds\ns_add_u32 m0, %[lw], 19456\ns_add_u32 s44, %[w2], 229760\nbuffer_load_dwordx4 v91, %[ro], s44 offen nt lds\nds_read_b64_tr_b16 a[168:169], v95 offset:20480\n" \
"ds_read_b64_tr_b16 a[170:171], v95 offset:20736\nds_read_b64_tr_b16 a[172:173], v95 offset:22528\nds_read_b64_tr_b16 a[174:175], v95 offset:22784\nds_read_b64_tr_b16 v[208:209], v95 offset:20544\nds_read_b64_tr_b16 v[210:211], v95 offset:20800\nds_read_b64_tr_b16 v[212:213], v95 offset:22592\nds_read_b64_tr_b16 v[214:215], v95 offset:22848\ns_waitcnt lgkmcnt(0)\n" \
"s_waitcnt vmcnt(28)\ns_barrier\ns_add_u32 m0, %[lw], 20480\ns_add_u32 s44, %[w2], 8192\nbuffer_load_dwordx4 v91, %[ro], s44 offen nt lds\ns_add_u32 m0, %[lw], 21504\ns_add_u32 s44, %[w2], 40960\nbuffer_load_dwordx4 v91, %[ro], s44 offen nt lds\n" \
"s_add_u32 m0, %[lw], 22528\ns_add_u32 s44, %[w2], 73728\nbuffer_load_dwordx4 v91, %[ro], s44 offen nt lds\ns_add_u32 m0, %[lw], 23552\ns_add_u32 s44, %[w2], 106496\nbuffer_load_dwordx4 v91, %[ro], s44 offen nt lds\nds_read_b64_tr_b16 a[48:49], v94 offset:0\nds_read_b64_tr_b16 a[50:51], v94 offset:256\n" \
"ds_read_b64_tr_b16 a[52:53], v94 offset:2048\nds_read_b64_tr_b16 a[54:55], v94 offset:2304\nds_read_b64_tr_b16 a[248:249], v94 offset:64\nds_read_b64_tr_b16 a[250:251], v94 offset:320\nds_read_b64_tr_b16 a[252:253], v94 offset:2112\nds_read_b64_tr_b16 a[254:255], v94 offset:2368\ns_waitcnt lgkmcnt(0)\ns_waitcnt vmcnt(28)\n" \
"s_barrier\ns_add_u32 m0, %[tb], 0\ns_add_u32 s44, %[w2], 8320\nbuffer_load_dwordx4 v91, %[ro], s44 offen nt lds\ns_add_u32 m0, %[tb], 1024\ns_add_u32 s44, %[w2], 41088\nbuffer_load_dwordx4 v91, %[ro], s44 offen nt lds\ns_add_u32 m0, %[tb], 2048\n" \
"s_add_u32 s44, %[w2], 73856\nbuffer_load_dwordx4 v91, %[ro], s44 offen nt lds\ns_add_u32 m0, %[tb], 3072\ns_add_u32 s44, %[w2], 106624\nbuffer_load_dwordx4 v91, %[ro], s44 offen nt lds\nds_read_b64_tr_b16 a[96:97], v94 offset:4096\nds_read_b64_tr_b16 a[98:99], v94 offset:4352\nds_read_b64_tr_b16 a[100:101], v94 offset:6144\n" \
"ds_read_b64_tr_b16 a[102:103], v94 offset:6400\nds_read_b64_tr_b16 v[144:145], v94 offset:4160\nds_read_b64_tr_b16 v[146:147], v94 offset:4416\nds_read_b64_tr_b16 v[148:149], v94 offset:6208\nds_read_b64_tr_b16 v[150:151], v94 offset:6464\ns_waitcnt lgkmcnt(0)\nbuffer_load_dwordx2 v[12:13], off, s[36:39], s45 sc1\ns_waitcnt vmcnt(29)\n" \
"s_barrier\ns_add_u32 m0, %[tb], 4096\ns_add_u32 s44, %[w2], 8448\nbuffer_load_dwordx4 v91, %[ro], s44 offen nt lds\ns_add_u32 m0, %[tb], 5120\ns_add_u32 s44, %[w2], 41216\nbuffer_load_dwordx4 v91, %[ro], s44 offen nt lds\ns_add_u32 m0, %[tb], 6144\n" \
"s_add_u32 s44, %[w2], 73984\nbuffer_load_dwordx4 v91, %[ro], s44 offen nt lds\ns_add_u32 m0, %[tb], 7168\ns_add_u32 s44, %[w2], 106752\nbuffer_load_dwordx4 v91, %[ro], s44 offen nt lds\nds_read_b64_tr_b16 a[152:153], v94 offset:8192\nds_read_b64_tr_b16 a[154:155], v94 offset:8448\nds_read_b64_tr_b16 a[156:157], v94 offset:10240\n" \
"ds_read_b64_tr_b16 a[158:159], v94 offset:10496\nds_read_b64_tr_b16 v[192:193], v94 offset:8256\nds_read_b64_tr_b16 v[194:195], v94 offset:8512\nds_read_b64_tr_b16 v[196:197], v94 offset:10304\nds_read_b64_tr_b16 v[198:199], v94 offset:10560\ns_waitcnt lgkmcnt(0)\ns_waitcnt vmcnt(29)\ns_barrier\n" \
"s_add_u32 m0, %[tb], 8192\ns_add_u32 s44, %[w2], 8576\nbuffer_load_dwordx4 v91, %[ro], s44 offen nt lds\ns_add_u32 m0, %[tb], 9216\ns_add_u32 s44, %[w2], 41344\nbuffer_load_dwordx4 v91, %[ro], s44 offen nt lds\ns_add_u32 m0, %[tb], 10240\ns_add_u32 s44, %[w2], 74112\n" \
"buffer_load_dwordx4 v91, %[ro], s44 offen nt lds\ns_add_u32 m0, %[tb], 11264\ns_add_u32 s44, %[w2], 106880\nbuffer_load_dwordx4 v91, %[ro], s44 offen nt lds\nds_read_b64_tr_b16 a[200:201], v95 offset:0\nds_read_b64_tr_b16 a[202:203], v95 offset:256\nds_read_b64_tr_b16 a[204:205], v95 offset:2048\nds_read_b64_tr_b16 a[206:207], v95 offset:2304\n" \
"ds_read_b64_tr_b16 v[248:249], v95 offset:64\nds_read_b64_tr_b16 v[250:251], v95 offset:320\nds_read_b64_tr_b16 v[252:253], v95 offset:2112\nds_read_b64_tr_b16 v[254:255], v95 offset:2368\ns_waitcnt lgkmcnt(0)\ns_waitcnt vmcnt(29)\ns_barrier\nds_read_b64_tr_b16 v[52:53], v95 offset:4096\n" \
"ds_read_b64_tr_b16 v[54:55], v95 offset:4352\nds_read_b64_tr_b16 v[48:49], v95 offset:6144\nds_read_b64_tr_b16 v[50:51], v95 offset:6400\nds_read_b64_tr_b16 v[44:45], v95 offset:4160\nds_read_b64_tr_b16 v[46:47], v95 offset:4416\nds_read_b64_tr_b16 v[40:41], v95 offset:6208\nds_read_b64_tr_b16 v[42:43], v95 offset:6464\ns_waitcnt lgkmcnt(0)\n" \
"ds_write_b128 v1, v[84:87] offset:0\nds_write_b128 v1, v[80:83] offset:1024\nds_write_b128 v1, v[76:79] offset:2048\nds_write_b128 v1, v[72:75] offset:3072\ns_waitcnt vmcnt(25)\ns_barrier\nds_read_b64_tr_b16 a[80:81], v95 offset:8192\nds_read_b64_tr_b16 a[82:83], v95 offset:8448\n" \
"ds_read_b64_tr_b16 a[84:85], v95 offset:10240\nds_read_b64_tr_b16 a[86:87], v95 offset:10496\nds_read_b64_tr_b16 v[120:121], v95 offset:8256\nds_read_b64_tr_b16 v[122:123], v95 offset:8512\nds_read_b64_tr_b16 v[124:125], v95 offset:10304\nds_read_b64_tr_b16 v[126:127], v95 offset:10560\ns_waitcnt lgkmcnt(0)\nds_write_b128 v1, v[68:71] offset:4096\n" \
"ds_write_b128 v1, v[64:67] offset:5120\nds_write_b128 v1, v[60:63] offset:6144\nds_write_b128 v1, v[56:59] offset:7168\ns_waitcnt vmcnt(21)\ns_barrier\nds_read_b64_tr_b16 v[56:57], v95 offset:12288\nds_read_b64_tr_b16 v[58:59], v95 offset:12544\nds_read_b64_tr_b16 v[60:61], v95 offset:14336\n" \
"ds_read_b64_tr_b16 v[62:63], v95 offset:14592\nds_read_b64_tr_b16 v[64:65], v95 offset:12352\nds_read_b64_tr_b16 v[66:67], v95 offset:12608\nds_read_b64_tr_b16 v[68:69], v95 offset:14400\nds_read_b64_tr_b16 v[70:71], v95 offset:14656\ns_waitcnt lgkmcnt(0)\nds_write_b128 v1, v[52:55] offset:8192\nds_write_b128 v1, v[48:51] offset:9216\n" \
"ds_write_b128 v1, v[44:47] offset:10240\nds_write_b128 v1, v[40:43] offset:11264\ns_waitcnt vmcnt(17)\ns_barrier\nds_read_b64_tr_b16 a[176:177], v95 offset:16384\nds_read_b64_tr_b16 a[178:179], v95 offset:16640\nds_read_b64_tr_b16 a[180:181], v95 offset:18432\nds_read_b64_tr_b16 a[182:183], v95 offset:18688\n" \
"ds_read_b64_tr_b16 v[224:225], v95 offset:16448\nds_read_b64_tr_b16 v[226:227], v95 offset:16704\nds_read_b64_tr_b16 v[228:229], v95 offset:18496\nds_read_b64_tr_b16 v[230:231], v95 offset:18752\ns_waitcnt lgkmcnt(0)\nds_write_b128 v1, v[56:59] offset:12288\nds_write_b128 v1, v[60:63] offset:13312\nds_write_b128 v1, v[64:67] offset:14336\n" \
"ds_write_b128 v1, v[68:71] offset:15360\ns_waitcnt vmcnt(13)\ns_barrier\nds_read_b64_tr_b16 a[32:33], v95 offset:20480\nds_read_b64_tr_b16 a[34:35], v95 offset:20736\nds_read_b64_tr_b16 a[36:37], v95 offset:22528\nds_read_b64_tr_b16 a[38:39], v95 offset:22784\nds_read_b64_tr_b16 a[232:233], v95 offset:20544\n" \
"ds_read_b64_tr_b16 a[234:235], v95 offset:20800\nds_read_b64_tr_b16 a[236:237], v95 offset:22592\nds_read_b64_tr_b16 a[238:239], v95 offset:22848\ns_waitcnt lgkmcnt(0)\ns_waitcnt vmcnt(9)\ns_barrier\nds_read_b64_tr_b16 v[68:69], v94 offset:0\nds_read_b64_tr_b16 v[70:71], v94 offset:256\n" \
"ds_read_b64_tr_b16 v[64:65], v94 offset:2048\nds_read_b64_tr_b16 v[66:67], v94 offset:2304\nds_read_b64_tr_b16 v[60:61], v94 offset:64\nds_read_b64_tr_b16 v[62:63], v94 offset:320\nds_read_b64_tr_b16 v[56:57], v94 offset:2112\nds_read_b64_tr_b16 v[58:59], v94 offset:2368\ns_waitcnt lgkmcnt(0)\nds_write_b128 v1, v[68:71] offset:16384\n" \
"ds_write_b128 v1, v[64:67] offset:17408\nds_write_b128 v1, v[60:63] offset:18432\nds_write_b128 v1, v[56:59] offset:19456\ns_waitcnt vmcnt(4)\ns_barrier\nds_read_b64_tr_b16 a[136:137], v94 offset:4096\nds_read_b64_tr_b16 a[138:139], v94 offset:4352\nds_read_b64_tr_b16 a[140:141], v94 offset:6144\n" \
"ds_read_b64_tr_b16 a[142:143], v94 offset:6400\nds_read_b64_tr_b16 v[176:177], v94 offset:4160\nds_read_b64_tr_b16 v[178:179], v94 offset:4416\nds_read_b64_tr_b16 v[180:181], v94 offset:6208\nds_read_b64_tr_b16 v[182:183], v94 offset:6464\ns_waitcnt lgkmcnt(0)\ns_waitcnt vmcnt(0)\ns_barrier\n" \
"ds_read_b64_tr_b16 v[56:57], v94 offset:8192\nds_read_b64_tr_b16 v[58:59], v94 offset:8448\nds_read_b64_tr_b16 v[60:61], v94 offset:10240\nds_read_b64_tr_b16 v[62:63], v94 offset:10496\nds_read_b64_tr_b16 v[64:65], v94 offset:8256\nds_read_b64_tr_b16 v[66:67], v94 offset:8512\nds_read_b64_tr_b16 v[68:69], v94 offset:10304\nds_read_b64_tr_b16 v[70:71], v94 offset:10560\n" \
"s_waitcnt lgkmcnt(0)\ns_mov_b32 s43, 0\ns_mov_b32 s86, 110\ns_mov_b32 s89, 0\n.Lvu_hb%=:\nv_cmp_ne_u32 vcc, v15, v12\ns_cbranch_vccnz .Lvu_hb1%=\ns_sleep 4\n" \
"buffer_load_dwordx2 v[12:13], off, s[36:39], s45 sc1\ns_waitcnt vmcnt(0)\ns_add_u32 s43, s43, 1\ns_cmp_lt_u32 s43, 262144\ns_cbranch_scc1 .Lvu_hb%=\ns_branch .Lvu_abort%=\n.Lvu_hb1%=:\nv_readfirstlane_b32 s44, v13\n" \
"s_and_b32 s44, s44, 0x200000\ns_add_u32 s36, s36, s44\ns_addc_u32 s37, s37, 0\nds_write_b128 v1, v[56:59] offset:20480\nds_write_b128 v1, v[60:63] offset:21504\nds_write_b128 v1, v[64:67] offset:22528\nds_write_b128 v1, v[68:71] offset:23552\ns_waitcnt lgkmcnt(0)\n" \
"s_barrier\ns_mov_b32 s40, -1\ns_mov_b32 s43, 0\ns_lshl_b32 s57, %[w], 12\ns_mov_b32 s56, 0\ns_mov_b32 s42, 0\ns_mov_b32 s60, -2147483648\ns_mov_b32 s54, -2147483648\n" \
"s_add_u32 s44, s40, 1\ns_lshl_b32 s44, s44, 17\ns_add_u32 s44, s44, %[xn]\ns_lshr_b32 s45, s44, 8\ns_and_b32 s45, s45, 0x2000\ns_xor_b32 s41, s44, s45\ns_add_u32 s41, s41, %[xw]\n.Lvu_f0%=:\n" \
"buffer_load_dword v14, v8, s[36:39], s56 offen sc1\ns_waitcnt vmcnt(0)\nv_cmp_ge_i32 vcc, s40, v14\ns_cbranch_vccz .Lvu_f0fok%=\ns_add_u32 s43, s43, 1\ns_cmp_lt_u32 s43, 262144\ns_cbranch_scc1 .Lvu_f0%=\ns_branch .Lvu_abort%=\n" \
".Lvu_f0fok%=:\ns_add_u32 m0, s57, 0\ns_add_u32 s58, s41, 0\nbuffer_load_dwordx4 v0, s[36:39], s58 offen offset:0 sc1 lds\nbuffer_load_dwordx4 v0, s[36:39], s58 offen offset:1024 sc1 lds\nbuffer_load_dwordx4 v0, s[36:39], s58 offen offset:2048 sc1 lds\nbuffer_load_dwordx4 v0, s[36:39], s58 offen offset:3072 sc1 lds\ns_add_u32 m0, s57, 32768\n" \
"s_add_u32 s58, s41, 32768\nbuffer_load_dwordx4 v0, s[36:39], s58 offen offset:0 sc1 lds\nbuffer_load_dwordx4 v0, s[36:39], s58 offen offset:1024 sc1 lds\nbuffer_load_dwordx4 v0, s[36:39], s58 offen offset:2048 sc1 lds\nbuffer_load_dwordx4 v0, s[36:39], s58 offen offset:3072 sc1 lds\ns_waitcnt vmcnt(0)\ns_barrier\nds_read_b128 v[32:35], v2 offset:0\n" \
"ds_read_b128 v[36:39], v2 offset:256\nds_read_b128 v[40:43], v2 offset:2048\nds_read_b128 v[44:47], v2 offset:2304\n.Lvu_tok%=:\nds_read_b128 v[64:67], v2 offset:4096\nds_read_b128 v[68:71], v2 offset:4352\ns_waitcnt lgkmcnt(5)\nv_mfma_f32_16x16x32_bf16 v[16:19], a[0:3], v[32:35], 0\n" \
"v_cvt_pk_bf16_f32 v92, v80, v81\nv_cvt_pk_bf16_f32 v93, v82, v83\nv_mfma_f32_16x16x32_bf16 v[20:23], a[16:19], v[32:35], 0\ns_waitcnt lgkmcnt(4)\nds_read_b128 v[32:35], v2 offset:6144\nv_cvt_pk_bf16_f32 v94, v84, v85\nv_cvt_pk_bf16_f32 v95, v86, v87\nv_mfma_f32_16x16x32_bf16 v[24:27], a[0:3], v[36:39], 0\n" \
"v_and_or_b32 v92, v92, -2, v13\nv_and_or_b32 v94, v94, -2, v13\nv_mfma_f32_16x16x32_bf16 v[28:31], a[16:19], v[36:39], 0\ns_waitcnt lgkmcnt(4)\nbuffer_store_dwordx4 v[92:95], v7, s[36:39], s60 offen offset:2560 sc1\nds_read_b128 v[36:39], v2 offset:6400\nv_mfma_f32_16x16x32_bf16 v[16:19], a[4:7], v[40:43], v[16:19]\nv_mfma_f32_16x16x32_bf16 v[20:23], a[20:23], v[40:43], v[20:23]\n" \
"s_waitcnt lgkmcnt(4)\ns_add_u32 m0, s57, 16384\nds_read_b128 v[40:43], v2 offset:8192\nv_mfma_f32_16x16x32_bf16 v[24:27], a[4:7], v[44:47], v[24:27]\ns_add_u32 s60, s41, 65536\nbuffer_load_dwordx4 v0, s[36:39], s60 offen offset:0 sc1 lds\nv_mfma_f32_16x16x32_bf16 v[28:31], a[20:23], v[44:47], v[28:31]\ns_waitcnt lgkmcnt(4)\n" \
"ds_read_b128 v[44:47], v2 offset:8448\nv_mfma_f32_16x16x32_bf16 v[16:19], a[24:27], v[64:67], v[16:19]\nds_read_b128 v[56:59], v1 offset:0\nds_read_b128 v[60:63], v1 offset:1024\nv_mfma_f32_16x16x32_bf16 v[20:23], a[40:43], v[64:67], v[20:23]\ns_waitcnt lgkmcnt(6)\nds_read_b128 v[64:67], v2 offset:10240\nv_mfma_f32_16x16x32_bf16 v[24:27], a[24:27], v[68:71], v[24:27]\n" \
"buffer_load_dwordx4 v0, s[36:39], s60 offen offset:1024 sc1 lds\nv_mfma_f32_16x16x32_bf16 v[28:31], a[40:43], v[68:71], v[28:31]\ns_waitcnt lgkmcnt(6)\nds_read_b128 v[68:71], v2 offset:10496\nv_mfma_f32_16x16x32_bf16 v[16:19], a[28:31], v[32:35], v[16:19]\nv_mfma_f32_16x16x32_bf16 v[20:23], a[44:47], v[32:35], v[20:23]\ns_waitcnt lgkmcnt(6)\nds_read_b128 v[32:35], v2 offset:12288\n" \
"v_mfma_f32_16x16x32_bf16 v[24:27], a[28:31], v[36:39], v[24:27]\nbuffer_load_dwordx4 v0, s[36:39], s60 offen offset:2048 sc1 lds\nv_mfma_f32_16x16x32_bf16 v[28:31], a[44:47], v[36:39], v[28:31]\ns_waitcnt lgkmcnt(6)\nds_read_b128 v[36:39], v2 offset:12544\nv_mfma_f32_16x16x32_bf16 v[16:19], a[56:59], v[40:43], v[16:19]\nv_mfma_f32_16x16x32_bf16 v[20:23], a[72:75], v[40:43], v[20:23]\ns_waitcnt lgkmcnt(6)\n" \
"ds_read_b128 v[40:43], v2 offset:14336\nv_mfma_f32_16x16x32_bf16 v[24:27], a[56:59], v[44:47], v[24:27]\nbuffer_load_dwordx4 v0, s[36:39], s60 offen offset:3072 sc1 lds\nv_mfma_f32_16x16x32_bf16 v[28:31], a[72:75], v[44:47], v[28:31]\ns_waitcnt lgkmcnt(4)\nds_read_b128 v[44:47], v2 offset:14592\nv_mfma_f32_16x16x32_bf16 v[16:19], a[60:63], v[64:67], v[16:19]\ns_add_u32 s56, s56, %[fs]\n" \
"s_add_u32 s62, s40, 1\ns_lshr_b32 m0, s57, 4\nv_mfma_f32_16x16x32_bf16 v[20:23], a[76:79], v[64:67], v[20:23]\ns_add_u32 m0, m0, 159744\ns_waitcnt lgkmcnt(4)\nds_read_b128 v[64:67], v2 offset:32768\nv_mfma_f32_16x16x32_bf16 v[24:27], a[60:63], v[68:71], v[24:27]\nbuffer_load_dword v8, s[36:39], s56 offen sc1 lds\n" \
"v_lshrrev_b32 v14, 2, v0\nv_mfma_f32_16x16x32_bf16 v[28:31], a[76:79], v[68:71], v[28:31]\ns_waitcnt lgkmcnt(4)\nds_read_b128 v[68:71], v2 offset:33024\nv_mfma_f32_16x16x32_bf16 v[16:19], v[56:59], v[32:35], v[16:19]\nv_add_u32 v14, 159744, v14\nv_mfma_f32_16x16x32_bf16 v[20:23], a[88:91], v[32:35], v[20:23]\ns_waitcnt lgkmcnt(4)\n" \
"ds_read_b128 v[32:35], v2 offset:34816\nv_mfma_f32_16x16x32_bf16 v[24:27], v[56:59], v[36:39], v[24:27]\nv_mfma_f32_16x16x32_bf16 v[28:31], a[88:91], v[36:39], v[28:31]\ns_waitcnt lgkmcnt(4)\nds_read_b128 v[36:39], v2 offset:35072\nv_mfma_f32_16x16x32_bf16 v[16:19], v[60:63], v[40:43], v[16:19]\nv_mfma_f32_16x16x32_bf16 v[20:23], a[92:95], v[40:43], v[20:23]\ns_waitcnt lgkmcnt(4)\n" \
"ds_read_b128 v[40:43], v2 offset:36864\nv_mfma_f32_16x16x32_bf16 v[24:27], v[60:63], v[44:47], v[24:27]\nv_mfma_f32_16x16x32_bf16 v[28:31], a[92:95], v[44:47], v[28:31]\ns_waitcnt lgkmcnt(4)\nds_read_b128 v[44:47], v2 offset:37120\nv_mfma_f32_16x16x32_bf16 v[16:19], a[104:107], v[64:67], v[16:19]\nds_read_b128 v[56:59], v1 offset:2048\nds_read_b128 v[60:63], v1 offset:3072\n" \
"v_mfma_f32_16x16x32_bf16 v[20:23], a[120:123], v[64:67], v[20:23]\ns_waitcnt lgkmcnt(6)\nds_read_b128 v[64:67], v2 offset:38912\nv_mfma_f32_16x16x32_bf16 v[24:27], a[104:107], v[68:71], v[24:27]\ns_barrier\nv_mfma_f32_16x16x32_bf16 v[28:31], a[120:123], v[68:71], v[28:31]\ns_waitcnt lgkmcnt(6)\ns_add_u32 m0, s57, 0\n" \
"ds_read_b128 v[68:71], v2 offset:39168\nv_mfma_f32_16x16x32_bf16 v[16:19], a[108:111], v[32:35], v[16:19]\ns_add_u32 s60, s41, 98304\nbuffer_load_dwordx4 v0, s[36:39], s60 offen offset:0 sc1 lds\nv_mfma_f32_16x16x32_bf16 v[20:23], a[124:127], v[32:35], v[20:23]\ns_waitcnt lgkmcnt(6)\nds_read_b128 v[32:35], v2 offset:40960\nv_mfma_f32_16x16x32_bf16 v[24:27], a[108:111], v[36:39], v[24:27]\n" \
"v_mfma_f32_16x16x32_bf16 v[28:31], a[124:127], v[36:39], v[28:31]\ns_waitcnt lgkmcnt(6)\nds_read_b128 v[36:39], v2 offset:41216\nv_mfma_f32_16x16x32_bf16 v[16:19], a[128:131], v[40:43], v[16:19]\nbuffer_load_dwordx4 v0, s[36:39], s60 offen offset:1024 sc1 lds\nv_mfma_f32_16x16x32_bf16 v[20:23], a[144:147], v[40:43], v[20:23]\ns_waitcnt lgkmcnt(6)\nds_read_b128 v[40:43], v2 offset:43008\n" \
"v_mfma_f32_16x16x32_bf16 v[24:27], a[128:131], v[44:47], v[24:27]\nv_mfma_f32_16x16x32_bf16 v[28:31], a[144:147], v[44:47], v[28:31]\ns_waitcnt lgkmcnt(4)\nds_read_b128 v[44:47], v2 offset:43264\nv_mfma_f32_16x16x32_bf16 v[16:19], a[132:135], v[64:67], v[16:19]\nbuffer_load_dwordx4 v0, s[36:39], s60 offen offset:2048 sc1 lds\nv_mfma_f32_16x16x32_bf16 v[20:23], a[148:151], v[64:67], v[20:23]\ns_waitcnt lgkmcnt(4)\n" \
"ds_read_b128 v[64:67], v2 offset:45056\nv_mfma_f32_16x16x32_bf16 v[24:27], a[132:135], v[68:71], v[24:27]\nv_mfma_f32_16x16x32_bf16 v[28:31], a[148:151], v[68:71], v[28:31]\ns_waitcnt lgkmcnt(4)\nds_read_b128 v[68:71], v2 offset:45312\nv_mfma_f32_16x16x32_bf16 v[16:19], a[160:163], v[32:35], v[16:19]\nbuffer_load_dwordx4 v0, s[36:39], s60 offen offset:3072 sc1 lds\nv_mfma_f32_16x16x32_bf16 v[20:23], v[56:59], v[32:35], v[20:23]\n" \
"s_waitcnt lgkmcnt(4)\nds_read_b128 v[32:35], v2 offset:47104\nv_mfma_f32_16x16x32_bf16 v[24:27], a[160:163], v[36:39], v[24:27]\ns_add_u32 s44, s40, 2\ns_lshl_b32 s44, s44, 17\nv_mfma_f32_16x16x32_bf16 v[28:31], v[56:59], v[36:39], v[28:31]\ns_waitcnt lgkmcnt(4)\nds_read_b128 v[36:39], v2 offset:47360\n" \
"v_mfma_f32_16x16x32_bf16 v[16:19], a[164:167], v[40:43], v[16:19]\ns_add_u32 s44, s44, %[xn]\ns_lshr_b32 s45, s44, 8\nv_mfma_f32_16x16x32_bf16 v[20:23], v[60:63], v[40:43], v[20:23]\ns_waitcnt lgkmcnt(4)\nv_mfma_f32_16x16x32_bf16 v[24:27], a[164:167], v[44:47], v[24:27]\ns_and_b32 s45, s45, 0x2000\ns_xor_b32 s41, s44, s45\n" \
"v_mfma_f32_16x16x32_bf16 v[28:31], v[60:63], v[44:47], v[28:31]\ns_waitcnt lgkmcnt(0)\ns_waitcnt vmcnt(4)\ns_barrier\nds_read_b128 v[40:43], v2 offset:16384\nds_read_b128 v[44:47], v2 offset:16640\nv_mfma_f32_16x16x32_bf16 v[16:19], a[184:187], v[64:67], v[16:19]\nds_read_b32 v14, v14\n" \
"v_mfma_f32_16x16x32_bf16 v[20:23], a[192:195], v[64:67], v[20:23]\nds_read_b128 v[64:67], v2 offset:18432\nv_mfma_f32_16x16x32_bf16 v[24:27], a[184:187], v[68:71], v[24:27]\ns_add_u32 s41, s41, %[xw]\nv_mfma_f32_16x16x32_bf16 v[28:31], a[192:195], v[68:71], v[28:31]\nds_read_b128 v[68:71], v2 offset:18688\nv_mfma_f32_16x16x32_bf16 v[16:19], a[188:191], v[32:35], v[16:19]\nds_read_b128 v[56:59], v1 offset:4096\n" \
"ds_read_b128 v[60:63], v1 offset:5120\nv_mfma_f32_16x16x32_bf16 v[20:23], a[196:199], v[32:35], v[20:23]\nds_read_b128 v[32:35], v2 offset:20480\nv_mfma_f32_16x16x32_bf16 v[24:27], a[188:191], v[36:39], v[24:27]\ns_waitcnt lgkmcnt(5)\nv_cmp_ge_i32 s[64:65], s62, v14\nv_mfma_f32_16x16x32_bf16 v[28:31], a[196:199], v[36:39], v[28:31]\ns_cmp_eq_u64 s[64:65], 0\n" \
"s_cselect_b32 s61, s41, -2147483648\nds_read_b128 v[36:39], v2 offset:20736\nv_mfma_f32_16x16x32_bf16 v[16:19], a[208:211], v[40:43], v[16:19]\ns_waitcnt vmcnt(4)\nbuffer_store_dword v15, off, s[36:39], s54 sc1\nv_mfma_f32_16x16x32_bf16 v[20:23], a[224:227], v[40:43], v[20:23]\ns_add_u32 m0, s57, 32768\nds_read_b128 v[40:43], v2 offset:22528\n" \
"v_mfma_f32_16x16x32_bf16 v[24:27], a[208:211], v[44:47], v[24:27]\ns_add_u32 s58, s61, 32768\nbuffer_load_dwordx4 v0, s[36:39], s58 offen offset:0 sc1 lds\nv_mfma_f32_16x16x32_bf16 v[28:31], a[224:227], v[44:47], v[28:31]\ns_waitcnt lgkmcnt(6)\nds_read_b128 v[44:47], v2 offset:22784\nv_mfma_f32_16x16x32_bf16 v[16:19], a[212:215], v[64:67], v[16:19]\nv_mfma_f32_16x16x32_bf16 v[20:23], a[228:231], v[64:67], v[20:23]\n" \
"s_waitcnt lgkmcnt(6)\nds_read_b128 v[64:67], v2 offset:24576\nv_mfma_f32_16x16x32_bf16 v[24:27], a[212:215], v[68:71], v[24:27]\nbuffer_load_dwordx4 v0, s[36:39], s58 offen offset:1024 sc1 lds\nv_mfma_f32_16x16x32_bf16 v[28:31], a[228:231], v[68:71], v[28:31]\ns_waitcnt lgkmcnt(4)\nds_read_b128 v[68:71], v2 offset:24832\nv_mfma_f32_16x16x32_bf16 v[16:19], v[56:59], v[32:35], v[16:19]\n" \
"v_mfma_f32_16x16x32_bf16 v[20:23], a[240:243], v[32:35], v[20:23]\ns_waitcnt lgkmcnt(4)\nds_read_b128 v[32:35], v2 offset:26624\nv_mfma_f32_16x16x32_bf16 v[24:27], v[56:59], v[36:39], v[24:27]\nbuffer_load_dwordx4 v0, s[36:39], s58 offen offset:2048 sc1 lds\nv_mfma_f32_16x16x32_bf16 v[28:31], a[240:243], v[36:39], v[28:31]\ns_waitcnt lgkmcnt(4)\nds_read_b128 v[36:39], v2 offset:26880\n" \
"v_mfma_f32_16x16x32_bf16 v[16:19], v[60:63], v[40:43], v[16:19]\nv_mfma_f32_16x16x32_bf16 v[20:23], a[244:247], v[40:43], v[20:23]\ns_waitcnt lgkmcnt(4)\nds_read_b128 v[40:43], v2 offset:28672\nv_mfma_f32_16x16x32_bf16 v[24:27], v[60:63], v[44:47], v[24:27]\nbuffer_load_dwordx4 v0, s[36:39], s58 offen offset:3072 sc1 lds\nv_mfma_f32_16x16x32_bf16 v[28:31], a[244:247], v[44:47], v[28:31]\ns_waitcnt lgkmcnt(4)\n" \
"ds_read_b128 v[44:47], v2 offset:28928\nv_mfma_f32_16x16x32_bf16 v[16:19], v[96:99], v[64:67], v[16:19]\nds_read_b128 v[56:59], v1 offset:6144\nds_read_b128 v[60:63], v1 offset:7168\nv_mfma_f32_16x16x32_bf16 v[20:23], v[112:115], v[64:67], v[20:23]\ns_waitcnt lgkmcnt(6)\nds_read_b128 v[64:67], v2 offset:30720\nv_mfma_f32_16x16x32_bf16 v[24:27], v[96:99], v[68:71], v[24:27]\n" \
"v_mfma_f32_16x16x32_bf16 v[28:31], v[112:115], v[68:71], v[28:31]\ns_waitcnt lgkmcnt(6)\nds_read_b128 v[68:71], v2 offset:30976\nv_mfma_f32_16x16x32_bf16 v[16:19], v[100:103], v[32:35], v[16:19]\nv_mfma_f32_16x16x32_bf16 v[20:23], v[116:119], v[32:35], v[20:23]\ns_waitcnt lgkmcnt(6)\nv_mfma_f32_16x16x32_bf16 v[24:27], v[100:103], v[36:39], v[24:27]\nv_mfma_f32_16x16x32_bf16 v[28:31], v[116:119], v[36:39], v[28:31]\n" \
"s_waitcnt lgkmcnt(0)\ns_waitcnt vmcnt(5)\ns_barrier\nds_read_b128 v[32:35], v2 offset:0\nds_read_b128 v[36:39], v2 offset:256\nv_mfma_f32_16x16x32_bf16 v[16:19], v[128:131], v[40:43], v[16:19]\ns_add_u32 m0, s57, 16384\ns_nop 0\n" \
"buffer_load_dwordx4 v3, s[36:39], s42 offen offset:0 sc1 lds\nv_mfma_f32_16x16x32_bf16 v[20:23], v[136:139], v[40:43], v[20:23]\nds_read_b128 v[40:43], v2 offset:2048\nv_mfma_f32_16x16x32_bf16 v[24:27], v[128:131], v[44:47], v[24:27]\nv_mfma_f32_16x16x32_bf16 v[28:31], v[136:139], v[44:47], v[28:31]\nds_read_b128 v[44:47], v2 offset:2304\nv_mfma_f32_16x16x32_bf16 v[16:19], v[132:135], v[64:67], v[16:19]\nbuffer_load_dwordx4 v3, s[36:39], s42 offen offset:1024 sc1 lds\n" \
"v_mfma_f32_16x16x32_bf16 v[20:23], v[140:143], v[64:67], v[20:23]\nds_read_b128 v[64:67], v2 offset:4096\nv_mfma_f32_16x16x32_bf16 v[24:27], v[132:135], v[68:71], v[24:27]\nv_mfma_f32_16x16x32_bf16 v[28:31], v[140:143], v[68:71], v[28:31]\ns_waitcnt lgkmcnt(4)\nds_read_b128 v[68:71], v2 offset:4352\nv_mfma_f32_16x16x32_bf16 v[16:19], v[152:155], v[32:35], v[16:19]\nbuffer_load_dwordx4 v3, s[36:39], s42 offen offset:2048 sc1 lds\n" \
"v_mfma_f32_16x16x32_bf16 v[20:23], v[56:59], v[32:35], v[20:23]\ns_waitcnt lgkmcnt(4)\nds_read_b128 v[32:35], v2 offset:6144\nv_mfma_f32_16x16x32_bf16 v[24:27], v[152:155], v[36:39], v[24:27]\nv_mfma_f32_16x16x32_bf16 v[28:31], v[56:59], v[36:39], v[28:31]\ns_waitcnt lgkmcnt(4)\nds_read_b128 v[36:39], v2 offset:6400\nv_mfma_f32_16x16x32_bf16 v[16:19], v[156:159], v[40:43], v[16:19]\n" \
"buffer_load_dwordx4 v3, s[36:39], s42 offen offset:3072 sc1 lds\nv_add_u32 v14, s57, v0\nv_mfma_f32_16x16x32_bf16 v[20:23], v[60:63], v[40:43], v[20:23]\ns_waitcnt lgkmcnt(4)\nds_read_b128 v[40:43], v2 offset:8192\nv_mfma_f32_16x16x32_bf16 v[24:27], v[156:159], v[44:47], v[24:27]\nv_mov_b64 v[48:49], 0\nv_mfma_f32_16x16x32_bf16 v[28:31], v[60:63], v[44:47], v[28:31]\n" \
"s_waitcnt lgkmcnt(4)\nds_read_b128 v[44:47], v2 offset:10240\nv_mov_b64 v[50:51], 0\nv_mfma_f32_16x16x32_bf16 v[16:19], v[168:171], v[64:67], v[16:19]\nv_mov_b64 v[52:53], 0\nv_mfma_f32_16x16x32_bf16 v[20:23], v[184:187], v[64:67], v[20:23]\ns_waitcnt lgkmcnt(4)\nds_read_b128 v[64:67], v2 offset:12288\n" \
"v_mov_b64 v[54:55], 0\nv_mfma_f32_16x16x32_bf16 v[24:27], v[168:171], v[68:71], v[24:27]\nds_read_b128 v[56:59], v1 offset:0\nds_read_b128 v[60:63], v1 offset:1024\nv_mfma_f32_16x16x32_bf16 v[28:31], v[184:187], v[68:71], v[28:31]\ns_waitcnt lgkmcnt(6)\nds_read_b128 v[68:71], v2 offset:14336\nv_mfma_f32_16x16x32_bf16 v[16:19], v[172:175], v[32:35], v[16:19]\n" \
"v_mfma_f32_16x16x32_bf16 v[20:23], v[188:191], v[32:35], v[20:23]\ns_waitcnt lgkmcnt(6)\nds_read_b128 v[32:35], v2 offset:8448\nv_mfma_f32_16x16x32_bf16 v[24:27], v[172:175], v[36:39], v[24:27]\nv_mfma_f32_16x16x32_bf16 v[28:31], v[188:191], v[36:39], v[28:31]\ns_waitcnt lgkmcnt(6)\nds_read_b128 v[36:39], v2 offset:10496\nv_mfma_f32_16x16x32_bf16 v[16:19], v[200:203], v[40:43], v[16:19]\n" \
"s_waitcnt vmcnt(0)\nds_read_b128 v[80:83], v14 offset:16384\nds_read_b128 v[84:87], v14 offset:17408\nv_mfma_f32_16x16x32_bf16 v[20:23], v[216:219], v[40:43], v[20:23]\ns_waitcnt lgkmcnt(8)\nds_read_b128 v[40:43], v2 offset:12544\nv_mfma_f32_16x16x32_bf16 v[16:19], v[204:207], v[44:47], v[16:19]\nds_read_b128 v[88:91], v14 offset:18432\n" \
"ds_read_b128 v[92:95], v14 offset:19456\nv_mfma_f32_16x16x32_bf16 v[20:23], v[220:223], v[44:47], v[20:23]\ns_waitcnt lgkmcnt(10)\nds_read_b128 v[44:47], v2 offset:14592\nv_mfma_f32_16x16x32_bf16 v[16:19], v[232:235], v[64:67], v[16:19]\ns_waitcnt lgkmcnt(4)\nv_max3_u32 v72, v80, v82, v84\nv_mfma_f32_16x16x32_bf16 v[20:23], v[240:243], v[64:67], v[20:23]\n" \
"s_waitcnt lgkmcnt(2)\nv_max3_u32 v72, v72, v86, v88\nv_mfma_f32_16x16x32_bf16 v[16:19], v[236:239], v[68:71], v[16:19]\ns_waitcnt lgkmcnt(1)\nv_max3_u32 v72, v72, v90, v92\nv_max_u32 v72, v72, v94\nv_mfma_f32_16x16x32_bf16 v[20:23], v[244:247], v[68:71], v[20:23]\nv_cmp_eq_u32 vcc, -1, v72\n" \
"v_mfma_f32_16x16x32_bf16 v[24:27], v[200:203], v[32:35], v[24:27]\ns_cbranch_vccnz .Lvu_re%=\n.Lvu_rer%=:\ns_barrier\ns_memrealtime s[68:69]\nds_read_b128 v[80:83], v0 offset:16384\nv_mfma_f32_16x16x32_bf16 v[28:31], v[216:219], v[32:35], v[28:31]\nds_read_b128 v[84:87], v0 offset:17408\n" \
"v_mfma_f32_16x16x32_bf16 v[24:27], v[204:207], v[36:39], v[24:27]\nds_read_b128 v[88:91], v0 offset:18432\nv_cvt_pk_bf16_f32 v72, v16, v17\nv_cvt_pk_bf16_f32 v73, v18, v19\nv_mfma_f32_16x16x32_bf16 v[28:31], v[220:223], v[36:39], v[28:31]\nds_read_b128 v[92:95], v0 offset:19456\nv_cvt_pk_bf16_f32 v74, v20, v21\nv_cvt_pk_bf16_f32 v75, v22, v23\n" \
"v_mfma_f32_16x16x32_bf16 v[24:27], v[232:235], v[40:43], v[24:27]\nds_read_b128 v[16:19], v0 offset:20480\nv_mfma_f32_16x16x32_bf16 v[28:31], v[240:243], v[40:43], v[28:31]\ns_waitcnt lgkmcnt(5)\nds_read_b128 v[20:23], v0 offset:21504\nv_mfma_f32_16x16x32_bf16 v[24:27], v[236:239], v[44:47], v[24:27]\nds_read_b128 v[32:35], v0 offset:22528\nv_mfma_f32_16x16x32_bf16 v[28:31], v[244:247], v[44:47], v[28:31]\n" \
"s_cmp_lt_i32 s40, 0\nds_read_b128 v[36:39], v0 offset:23552\nds_write2st64_b64 v9, v[72:73], v[74:75] offset0:0 offset1:8\ns_cbranch_scc1 .Lvu_first%=\n.Lvu_crit%=:\ns_waitcnt lgkmcnt(8)\nv_smfmac_f32_16x16x64_bf16 v[48:51], v[80:83], a[0:7], v4\ns_waitcnt lgkmcnt(7)\n" \
"ds_read_b128 v[64:67], v1 offset:4096\nds_read_b128 v[40:43], v0 offset:24576\nv_mov_b32 v14, s40\nv_smfmac_f32_16x16x64_bf16 v[48:51], v[84:87], a[24:31], v4\ns_waitcnt lgkmcnt(8)\nds_read_b128 v[68:71], v1 offset:5120\nds_read_b128 v[44:47], v0 offset:25600\nv_cvt_pk_bf16_f32 v76, v24, v25\n" \
"v_smfmac_f32_16x16x64_bf16 v[48:51], v[88:91], a[56:63], v4\ns_sub_u32 s88, s86, s89\ns_min_u32 s88, s88, 150\ns_waitcnt lgkmcnt(9)\nds_read_b128 v[80:83], v0 offset:28672\nv_cvt_pk_bf16_f32 v77, v26, v27\nv_smfmac_f32_16x16x64_bf16 v[48:51], v[92:95], v[56:63], v4\ns_waitcnt lgkmcnt(9)\n" \
"ds_read_b128 v[84:87], v0 offset:29696\nv_cvt_pk_bf16_f32 v78, v28, v29\nv_smfmac_f32_16x16x64_bf16 v[48:51], v[16:19], a[104:111], v4\ns_add_u32 s88, s88, s68\ns_mov_b32 s89, s68\ns_waitcnt lgkmcnt(9)\nds_read_b128 v[88:91], v0 offset:30720\nv_cvt_pk_bf16_f32 v79, v30, v31\n" \
"v_smfmac_f32_16x16x64_bf16 v[48:51], v[20:23], a[128:135], v4\ns_waitcnt lgkmcnt(9)\nds_read_b128 v[92:95], v0 offset:31744\nv_mov_b32 v15, s88\nv_smfmac_f32_16x16x64_bf16 v[48:51], v[32:35], a[160:167], v4\ns_waitcnt lgkmcnt(9)\nds_read_b128 v[24:27], v0 offset:26624\nbuffer_store_dwordx2 v[14:15], off, s[36:39], %[sg] sc1\n" \
"v_smfmac_f32_16x16x64_bf16 v[48:51], v[36:39], a[184:191], v4\ns_waitcnt lgkmcnt(7)\nds_read_b128 v[28:31], v0 offset:27648\nv_smfmac_f32_16x16x64_bf16 v[48:51], v[40:43], a[208:215], v4\ns_waitcnt lgkmcnt(6)\nds_read_b128 v[56:59], v1 offset:6144\nv_smfmac_f32_16x16x64_bf16 v[48:51], v[44:47], v[64:71], v4\ns_waitcnt lgkmcnt(6)\n" \
"ds_read_b128 v[60:63], v1 offset:7168\nv_smfmac_f32_16x16x64_bf16 v[48:51], v[80:83], v[152:159], v4\ns_waitcnt lgkmcnt(6)\nds_write2st64_b64 v9, v[76:77], v[78:79] offset0:2 offset1:10\nv_smfmac_f32_16x16x64_bf16 v[48:51], v[84:87], v[168:175], v4\ns_waitcnt lgkmcnt(6)\nv_smfmac_f32_16x16x64_bf16 v[48:51], v[88:91], v[200:207], v4\ns_waitcnt lgkmcnt(5)\n" \
"v_smfmac_f32_16x16x64_bf16 v[48:51], v[92:95], v[232:239], v4\ns_waitcnt lgkmcnt(4)\nv_smfmac_f32_16x16x64_bf16 v[48:51], v[24:27], v[96:103], v4\ns_waitcnt lgkmcnt(3)\nds_read_b128 v[64:67], v1 offset:2048\nv_smfmac_f32_16x16x64_bf16 v[48:51], v[28:31], v[128:135], v4\ns_waitcnt lgkmcnt(2)\nds_read_b128 v[68:71], v1 offset:3072\n" \
"v_smfmac_f32_16x16x64_bf16 v[52:55], v[80:83], v[56:63], v4\nds_read_b128 v[56:59], v1 offset:8192\nv_smfmac_f32_16x16x64_bf16 v[52:55], v[84:87], v[184:191], v4\nds_read_b128 v[60:63], v1 offset:9216\nv_add_f32 v48, v48, v50\nv_smfmac_f32_16x16x64_bf16 v[52:55], v[88:91], v[216:223], v4\nds_read_b128 v[80:83], v0 offset:16384\nv_add_f32 v49, v49, v51\n" \
"v_smfmac_f32_16x16x64_bf16 v[52:55], v[92:95], v[240:247], v4\nds_read_b128 v[84:87], v0 offset:17408\nv_cvt_pk_bf16_f32 v14, v48, v49\nv_smfmac_f32_16x16x64_bf16 v[52:55], v[16:19], a[120:127], v4\nds_write_b16 v5, v14 offset:0\nds_write_b16_d16_hi v5, v14 offset:16\nv_smfmac_f32_16x16x64_bf16 v[52:55], v[20:23], a[144:151], v4\nds_read_b128 v[88:91], v0 offset:18432\n" \
"v_mov_b64 v[16:17], 0\nv_smfmac_f32_16x16x64_bf16 v[52:55], v[24:27], v[112:119], v4\nds_read_b128 v[92:95], v0 offset:19456\nv_mov_b64 v[18:19], 0\nv_smfmac_f32_16x16x64_bf16 v[52:55], v[28:31], v[136:143], v4\ns_waitcnt lgkmcnt(8)\nv_mov_b64 v[20:21], 0\nv_smfmac_f32_16x16x64_bf16 v[52:55], v[32:35], v[64:71], v4\n" \
"s_waitcnt lgkmcnt(2)\nds_read_b128 v[64:67], v1 offset:12288\ns_barrier\nds_read_b128 v[72:75], v11 offset:57344\nv_smfmac_f32_16x16x64_bf16 v[52:55], v[36:39], a[192:199], v4\nds_read_b128 v[68:71], v1 offset:13312\nds_read_b128 v[76:79], v11 offset:58368\nv_mov_b64 v[22:23], 0\n" \
"v_smfmac_f32_16x16x64_bf16 v[52:55], v[40:43], a[224:231], v4\nv_mov_b64 v[24:25], 0\nv_smfmac_f32_16x16x64_bf16 v[52:55], v[44:47], a[240:247], v4\nv_mov_b64 v[26:27], 0\nv_smfmac_f32_16x16x64_bf16 v[52:55], v[80:83], a[16:23], v4\nv_mov_b64 v[28:29], 0\nv_smfmac_f32_16x16x64_bf16 v[52:55], v[84:87], a[40:47], v4\ns_waitcnt lgkmcnt(5)\n" \
"v_mov_b64 v[30:31], 0\nv_smfmac_f32_16x16x64_bf16 v[52:55], v[88:91], a[72:79], v4\ns_waitcnt lgkmcnt(4)\nv_mov_b64 v[32:33], 0\nv_smfmac_f32_16x16x64_bf16 v[52:55], v[92:95], a[88:95], v4\ns_waitcnt lgkmcnt(2)\nv_mov_b64 v[34:35], 0\nv_smfmac_f32_16x16x64_bf16 v[16:19], v[72:75], a[8:15], v10\n" \
"s_waitcnt lgkmcnt(0)\nv_mov_b64 v[36:37], 0\nv_smfmac_f32_16x16x64_bf16 v[16:19], v[76:79], v[56:63], v10\nds_read_b128 v[56:59], v1 offset:10240\nv_add_f32 v52, v52, v54\nv_smfmac_f32_16x16x64_bf16 v[20:23], v[72:75], a[64:71], v10\nds_read_b128 v[60:63], v1 offset:11264\nv_add_f32 v53, v53, v55\n" \
"v_smfmac_f32_16x16x64_bf16 v[20:23], v[76:79], a[80:87], v10\nv_cvt_pk_bf16_f32 v14, v52, v53\nv_smfmac_f32_16x16x64_bf16 v[24:27], v[72:75], a[112:119], v10\nds_write_b16 v5, v14 offset:2048\nds_write_b16_d16_hi v5, v14 offset:2064\nv_smfmac_f32_16x16x64_bf16 v[24:27], v[76:79], v[64:71], v10\nds_read_b128 v[64:67], v1 offset:14336\nv_mov_b64 v[38:39], 0\n" \
"v_smfmac_f32_16x16x64_bf16 v[28:31], v[72:75], a[168:175], v10\nds_read_b128 v[68:71], v1 offset:15360\nv_mov_b64 v[40:41], 0\nv_smfmac_f32_16x16x64_bf16 v[28:31], v[76:79], a[176:183], v10\nv_mov_b64 v[42:43], 0\nv_smfmac_f32_16x16x64_bf16 v[32:35], v[72:75], a[216:223], v10\ns_waitcnt lgkmcnt(2)\ns_barrier\n" \
"s_mov_b32 s59, 0\nds_read_b128 v[80:83], v11 offset:59392\nv_smfmac_f32_16x16x64_bf16 v[32:35], v[76:79], v[56:63], v10\ns_add_u32 m0, s57, 0\ns_add_u32 s58, s61, 0\nds_read_b128 v[56:59], v1 offset:16384\nbuffer_load_dwordx4 v0, s[36:39], s58 offen offset:0 sc1 lds\nds_read_b128 v[84:87], v11 offset:60416\n" \
"v_smfmac_f32_16x16x64_bf16 v[36:39], v[72:75], v[104:111], v10\nds_read_b128 v[60:63], v1 offset:17408\nv_mov_b64 v[44:45], 0\nv_smfmac_f32_16x16x64_bf16 v[36:39], v[76:79], v[120:127], v10\nv_mov_b64 v[46:47], 0\nv_smfmac_f32_16x16x64_bf16 v[40:43], v[72:75], v[160:167], v10\ns_waitcnt lgkmcnt(4)\nbuffer_load_dwordx4 v0, s[36:39], s58 offen offset:1024 sc1 lds\n" \
"v_smfmac_f32_16x16x64_bf16 v[40:43], v[76:79], v[64:71], v10\nds_read_b128 v[64:67], v1 offset:20480\nv_mov_b64 v[48:49], 0\nv_smfmac_f32_16x16x64_bf16 v[44:47], v[72:75], v[208:215], v10\nds_read_b128 v[68:71], v1 offset:21504\nv_mov_b64 v[50:51], 0\nv_smfmac_f32_16x16x64_bf16 v[44:47], v[76:79], v[224:231], v10\ns_waitcnt lgkmcnt(5)\n" \
"buffer_load_dwordx4 v0, s[36:39], s58 offen offset:2048 sc1 lds\nv_smfmac_f32_16x16x64_bf16 v[16:19], v[80:83], a[32:39], v10\ns_waitcnt lgkmcnt(3)\nds_read_b128 v[72:75], v11 offset:49152\nv_mov_b64 v[52:53], 0\nv_smfmac_f32_16x16x64_bf16 v[16:19], v[84:87], a[48:55], v10\ns_waitcnt lgkmcnt(3)\nds_read_b128 v[76:79], v11 offset:50176\n" \
"v_mov_b64 v[54:55], 0\nv_smfmac_f32_16x16x64_bf16 v[20:23], v[80:83], v[56:63], v10\nds_read_b128 v[56:59], v1 offset:18432\nbuffer_load_dwordx4 v0, s[36:39], s58 offen offset:3072 sc1 lds\nv_smfmac_f32_16x16x64_bf16 v[20:23], v[84:87], a[96:103], v10\nds_read_b128 v[60:63], v1 offset:19456\nv_cvt_pk_bf16_f32 v88, v16, v17\nv_smfmac_f32_16x16x64_bf16 v[24:27], v[80:83], a[136:143], v10\n" \
"v_and_or_b32 v88, v88, -2, v12\nv_cvt_pk_bf16_f32 v89, v18, v19\nv_smfmac_f32_16x16x64_bf16 v[24:27], v[84:87], a[152:159], v10\ns_waitcnt lgkmcnt(4)\nv_cvt_pk_bf16_f32 v90, v20, v21\nv_cvt_pk_bf16_f32 v91, v22, v23\nv_and_or_b32 v90, v90, -2, v12\nv_smfmac_f32_16x16x64_bf16 v[28:31], v[80:83], v[64:71], v10\n" \
"ds_read_b128 v[64:67], v1 offset:22528\nv_permlane16_swap_b32 v89, v91\nv_smfmac_f32_16x16x64_bf16 v[28:31], v[84:87], a[200:207], v10\nds_read_b128 v[68:71], v1 offset:23552\nv_permlane16_swap_b32 v88, v90\nv_smfmac_f32_16x16x64_bf16 v[32:35], v[80:83], a[232:239], v10\ns_add_u32 s63, s59, 0\nds_read_b128 v[16:19], v11 offset:51200\n" \
"v_cvt_pk_bf16_f32 v92, v24, v25\nv_and_or_b32 v92, v92, -2, v12\nv_smfmac_f32_16x16x64_bf16 v[32:35], v[84:87], a[248:255], v10\ns_waitcnt lgkmcnt(3)\nds_read_b128 v[20:23], v11 offset:52224\nbuffer_store_dwordx4 v[88:91], v6, s[36:39], s63 offen sc1\nv_cvt_pk_bf16_f32 v93, v26, v27\nv_cvt_pk_bf16_f32 v94, v28, v29\n" \
"v_cvt_pk_bf16_f32 v95, v30, v31\nv_and_or_b32 v94, v94, -2, v12\nv_smfmac_f32_16x16x64_bf16 v[36:39], v[80:83], v[56:63], v10\nds_read_b128 v[56:59], v1 offset:8192\nv_permlane16_swap_b32 v93, v95\nv_smfmac_f32_16x16x64_bf16 v[36:39], v[84:87], v[144:151], v10\nds_read_b128 v[60:63], v1 offset:9216\nv_permlane16_swap_b32 v92, v94\n" \
"v_smfmac_f32_16x16x64_bf16 v[40:43], v[80:83], v[176:183], v10\ns_add_u32 s63, s59, 131072\nds_read_b128 v[24:27], v11 offset:53248\nv_cvt_pk_bf16_f32 v88, v32, v33\nv_and_or_b32 v88, v88, -2, v12\nv_smfmac_f32_16x16x64_bf16 v[40:43], v[84:87], v[192:199], v10\ns_waitcnt lgkmcnt(5)\nds_read_b128 v[28:31], v11 offset:54272\n" \
"buffer_store_dwordx4 v[92:95], v6, s[36:39], s63 offen sc1\nv_cvt_pk_bf16_f32 v89, v34, v35\nv_cvt_pk_bf16_f32 v90, v36, v37\nv_cvt_pk_bf16_f32 v91, v38, v39\nv_and_or_b32 v90, v90, -2, v12\nv_smfmac_f32_16x16x64_bf16 v[44:47], v[80:83], v[64:71], v10\nds_read_b128 v[64:67], v1 offset:16384\nv_permlane16_swap_b32 v89, v91\n" \
"v_smfmac_f32_16x16x64_bf16 v[44:47], v[84:87], v[248:255], v10\ns_cmp_ge_i32 s40, 63\nds_read_b128 v[68:71], v1 offset:17408\nv_permlane16_swap_b32 v88, v90\ns_cbranch_scc1 .Lvu_last%=\n.Lvu_y%=:\nv_smfmac_f32_16x16x64_bf16 v[48:51], v[72:75], a[8:15], v10\ns_add_u32 s63, s59, 4096\n" \
"ds_read_b128 v[32:35], v11 offset:55296\nds_read_b128 v[36:39], v11 offset:56320\nv_cvt_pk_bf16_f32 v92, v40, v41\nv_and_or_b32 v92, v92, -2, v12\nv_smfmac_f32_16x16x64_bf16 v[52:55], v[76:79], a[8:15], v10\ns_waitcnt lgkmcnt(9)\ns_waitcnt lgkmcnt(6)\nbuffer_store_dwordx4 v[88:91], v6, s[36:39], s63 offen sc1\n" \
"v_cvt_pk_bf16_f32 v93, v42, v43\nv_cvt_pk_bf16_f32 v94, v44, v45\nv_cvt_pk_bf16_f32 v95, v46, v47\nv_and_or_b32 v94, v94, -2, v12\nv_smfmac_f32_16x16x64_bf16 v[48:51], v[16:19], v[56:63], v10\ns_add_u32 s40, s40, 1\ns_and_b32 s44, s40, 1\nv_permlane16_swap_b32 v93, v95\n" \
"v_smfmac_f32_16x16x64_bf16 v[52:55], v[20:23], v[56:63], v10\ns_lshl_b32 s55, s44, 22\ns_waitcnt lgkmcnt(5)\nv_permlane16_swap_b32 v92, v94\nv_smfmac_f32_16x16x64_bf16 v[48:51], v[24:27], a[32:39], v10\ns_waitcnt lgkmcnt(4)\ns_add_u32 s63, s59, 135168\nds_read_b128 v[56:59], v1 offset:12288\n" \
"v_smfmac_f32_16x16x64_bf16 v[52:55], v[28:31], a[32:39], v10\ns_waitcnt lgkmcnt(2)\nds_read_b128 v[60:63], v1 offset:13312\nbuffer_store_dwordx4 v[92:95], v6, s[36:39], s63 offen sc1\nv_mov_b64 v[80:81], 0\nv_mov_b64 v[82:83], 0\nv_mov_b64 v[84:85], 0\nv_mov_b64 v[86:87], 0\n" \
"v_smfmac_f32_16x16x64_bf16 v[48:51], v[32:35], a[48:55], v10\ns_memrealtime s[86:87]\ns_bfe_u32 s45, s40, 0x10001\ns_waitcnt lgkmcnt(2)\nv_smfmac_f32_16x16x64_bf16 v[52:55], v[36:39], a[48:55], v10\nv_mov_b32 v13, s45\nv_smfmac_f32_16x16x64_bf16 v[80:83], v[72:75], a[64:71], v10\nv_cvt_pk_bf16_f32 v88, v48, v49\n" \
"v_smfmac_f32_16x16x64_bf16 v[84:87], v[76:79], a[64:71], v10\nv_cvt_pk_bf16_f32 v89, v50, v51\nv_smfmac_f32_16x16x64_bf16 v[80:83], v[16:19], a[80:87], v10\nv_cvt_pk_bf16_f32 v90, v52, v53\nv_smfmac_f32_16x16x64_bf16 v[84:87], v[20:23], a[80:87], v10\ns_add_u32 s60, s55, 0\nv_cvt_pk_bf16_f32 v91, v54, v55\nv_smfmac_f32_16x16x64_bf16 v[80:83], v[24:27], v[64:71], v10\n" \
"v_and_or_b32 v88, v88, -2, v13\nv_smfmac_f32_16x16x64_bf16 v[84:87], v[28:31], v[64:71], v10\ns_and_b32 s42, s40, 3\ns_lshl_b32 s42, s42, 15\nv_and_or_b32 v90, v90, -2, v13\nv_smfmac_f32_16x16x64_bf16 v[80:83], v[32:35], a[96:103], v10\nbuffer_store_dwordx4 v[88:91], v7, s[36:39], s60 offen offset:0 sc1\nv_mov_b64 v[48:49], 0\n" \
"v_mov_b64 v[50:51], 0\nv_mov_b64 v[52:53], 0\nv_mov_b64 v[54:55], 0\nv_smfmac_f32_16x16x64_bf16 v[84:87], v[36:39], a[96:103], v10\nds_read_b128 v[64:67], v1 offset:20480\nv_mov_b32 v12, s44\nv_smfmac_f32_16x16x64_bf16 v[48:51], v[72:75], a[112:119], v10\nds_read_b128 v[68:71], v1 offset:21504\n" \
"v_cvt_pk_bf16_f32 v92, v80, v81\nv_smfmac_f32_16x16x64_bf16 v[52:55], v[76:79], a[112:119], v10\ns_waitcnt lgkmcnt(2)\nv_cvt_pk_bf16_f32 v93, v82, v83\nv_smfmac_f32_16x16x64_bf16 v[48:51], v[16:19], v[56:63], v10\nv_cvt_pk_bf16_f32 v94, v84, v85\nv_smfmac_f32_16x16x64_bf16 v[52:55], v[20:23], v[56:63], v10\ns_add_u32 s60, s55, 4096\n" \
"v_cvt_pk_bf16_f32 v95, v86, v87\nv_smfmac_f32_16x16x64_bf16 v[48:51], v[24:27], a[136:143], v10\nds_read_b128 v[56:59], v1 offset:10240\nv_and_or_b32 v92, v92, -2, v13\nv_smfmac_f32_16x16x64_bf16 v[52:55], v[28:31], a[136:143], v10\nds_read_b128 v[60:63], v1 offset:11264\nv_and_or_b32 v94, v94, -2, v13\nv_smfmac_f32_16x16x64_bf16 v[48:51], v[32:35], a[152:159], v10\n" \
"buffer_store_dwordx4 v[92:95], v7, s[36:39], s60 offen offset:0 sc1\nv_mov_b64 v[80:81], 0\nv_mov_b64 v[82:83], 0\nv_mov_b64 v[84:85], 0\nv_mov_b64 v[86:87], 0\nv_smfmac_f32_16x16x64_bf16 v[52:55], v[36:39], a[152:159], v10\ns_waitcnt vmcnt(6)\ns_barrier\n" \
"v_smfmac_f32_16x16x64_bf16 v[80:83], v[72:75], a[168:175], v10\nv_cvt_pk_bf16_f32 v88, v48, v49\nv_smfmac_f32_16x16x64_bf16 v[84:87], v[76:79], a[168:175], v10\nv_cvt_pk_bf16_f32 v89, v50, v51\nv_smfmac_f32_16x16x64_bf16 v[80:83], v[16:19], a[176:183], v10\ns_cmp_lt_u32 s40, 2\ns_cselect_b32 s54, %[fy], -2147483648\nv_cvt_pk_bf16_f32 v90, v52, v53\n" \
"v_smfmac_f32_16x16x64_bf16 v[84:87], v[20:23], a[176:183], v10\ns_add_u32 s60, s55, 16384\ns_waitcnt lgkmcnt(2)\nv_cvt_pk_bf16_f32 v91, v54, v55\nv_smfmac_f32_16x16x64_bf16 v[80:83], v[24:27], v[64:71], v10\nv_and_or_b32 v88, v88, -2, v13\nv_smfmac_f32_16x16x64_bf16 v[84:87], v[28:31], v[64:71], v10\nv_and_or_b32 v90, v90, -2, v13\n" \
"v_smfmac_f32_16x16x64_bf16 v[80:83], v[32:35], a[200:207], v10\nbuffer_store_dwordx4 v[88:91], v7, s[36:39], s60 offen offset:0 sc1\nv_mov_b64 v[48:49], 0\nv_mov_b64 v[50:51], 0\nv_mov_b64 v[52:53], 0\nv_mov_b64 v[54:55], 0\nv_smfmac_f32_16x16x64_bf16 v[84:87], v[36:39], a[200:207], v10\nds_read_b128 v[64:67], v1 offset:18432\n" \
"v_smfmac_f32_16x16x64_bf16 v[48:51], v[72:75], a[216:223], v10\nds_read_b128 v[68:71], v1 offset:19456\nv_cvt_pk_bf16_f32 v92, v80, v81\nv_smfmac_f32_16x16x64_bf16 v[52:55], v[76:79], a[216:223], v10\ns_waitcnt lgkmcnt(2)\nv_cvt_pk_bf16_f32 v93, v82, v83\nv_smfmac_f32_16x16x64_bf16 v[48:51], v[16:19], v[56:63], v10\nv_cvt_pk_bf16_f32 v94, v84, v85\n" \
"v_smfmac_f32_16x16x64_bf16 v[52:55], v[20:23], v[56:63], v10\ns_add_u32 s60, s55, 20480\nv_cvt_pk_bf16_f32 v95, v86, v87\nv_smfmac_f32_16x16x64_bf16 v[48:51], v[24:27], a[232:239], v10\nds_read_b128 v[56:59], v1 offset:14336\nv_and_or_b32 v92, v92, -2, v13\nv_smfmac_f32_16x16x64_bf16 v[52:55], v[28:31], a[232:239], v10\nds_read_b128 v[60:63], v1 offset:15360\n" \
"v_and_or_b32 v94, v94, -2, v13\nv_smfmac_f32_16x16x64_bf16 v[48:51], v[32:35], a[248:255], v10\nbuffer_store_dwordx4 v[92:95], v7, s[36:39], s60 offen offset:0 sc1\nv_mov_b64 v[80:81], 0\nv_mov_b64 v[82:83], 0\nv_mov_b64 v[84:85], 0\nv_mov_b64 v[86:87], 0\nv_smfmac_f32_16x16x64_bf16 v[52:55], v[36:39], a[248:255], v10\n" \
"v_smfmac_f32_16x16x64_bf16 v[80:83], v[72:75], v[104:111], v10\nv_cvt_pk_bf16_f32 v88, v48, v49\nv_smfmac_f32_16x16x64_bf16 v[84:87], v[76:79], v[104:111], v10\nv_cvt_pk_bf16_f32 v89, v50, v51\nv_smfmac_f32_16x16x64_bf16 v[80:83], v[16:19], v[120:127], v10\nv_cvt_pk_bf16_f32 v90, v52, v53\nv_smfmac_f32_16x16x64_bf16 v[84:87], v[20:23], v[120:127], v10\ns_add_u32 s60, s55, 0\n" \
"s_waitcnt lgkmcnt(2)\nv_cvt_pk_bf16_f32 v91, v54, v55\nv_smfmac_f32_16x16x64_bf16 v[80:83], v[24:27], v[64:71], v10\nv_and_or_b32 v88, v88, -2, v13\nv_smfmac_f32_16x16x64_bf16 v[84:87], v[28:31], v[64:71], v10\nv_and_or_b32 v90, v90, -2, v13\nv_smfmac_f32_16x16x64_bf16 v[80:83], v[32:35], v[144:151], v10\nbuffer_store_dwordx4 v[88:91], v7, s[36:39], s60 offen offset:2048 sc1\n" \
"v_mov_b64 v[48:49], 0\nv_mov_b64 v[50:51], 0\nv_mov_b64 v[52:53], 0\nv_mov_b64 v[54:55], 0\nv_smfmac_f32_16x16x64_bf16 v[84:87], v[36:39], v[144:151], v10\nds_read_b128 v[64:67], v1 offset:22528\nv_smfmac_f32_16x16x64_bf16 v[48:51], v[72:75], v[160:167], v10\nds_read_b128 v[68:71], v1 offset:23552\n" \
"v_cvt_pk_bf16_f32 v92, v80, v81\nv_smfmac_f32_16x16x64_bf16 v[52:55], v[76:79], v[160:167], v10\ns_waitcnt lgkmcnt(2)\nv_cvt_pk_bf16_f32 v93, v82, v83\nv_smfmac_f32_16x16x64_bf16 v[48:51], v[16:19], v[56:63], v10\nds_read_b128 v[40:43], v11 offset:49280\nv_cvt_pk_bf16_f32 v94, v84, v85\nv_smfmac_f32_16x16x64_bf16 v[52:55], v[20:23], v[56:63], v10\n" \
"s_add_u32 s60, s55, 4096\nv_cvt_pk_bf16_f32 v95, v86, v87\nv_smfmac_f32_16x16x64_bf16 v[48:51], v[24:27], v[176:183], v10\nds_read_b128 v[56:59], v1 offset:8192\nds_read_b128 v[44:47], v11 offset:50304\nv_and_or_b32 v92, v92, -2, v13\nv_smfmac_f32_16x16x64_bf16 v[52:55], v[28:31], v[176:183], v10\nds_read_b128 v[60:63], v1 offset:9216\n" \
"v_and_or_b32 v94, v94, -2, v13\nv_smfmac_f32_16x16x64_bf16 v[48:51], v[32:35], v[192:199], v10\nbuffer_store_dwordx4 v[92:95], v7, s[36:39], s60 offen offset:2048 sc1\nv_mov_b64 v[80:81], 0\nv_mov_b64 v[82:83], 0\nv_mov_b64 v[84:85], 0\nv_mov_b64 v[86:87], 0\nv_smfmac_f32_16x16x64_bf16 v[52:55], v[36:39], v[192:199], v10\n" \
"v_smfmac_f32_16x16x64_bf16 v[80:83], v[72:75], v[208:215], v10\nds_read_b128 v[72:75], v11 offset:51328\nv_cvt_pk_bf16_f32 v88, v48, v49\nv_smfmac_f32_16x16x64_bf16 v[84:87], v[76:79], v[208:215], v10\nds_read_b128 v[76:79], v11 offset:52352\nv_cvt_pk_bf16_f32 v89, v50, v51\nv_smfmac_f32_16x16x64_bf16 v[80:83], v[16:19], v[224:231], v10\ns_add_u32 s60, s55, 16384\n" \
"ds_read_b128 v[16:19], v11 offset:53376\nv_cvt_pk_bf16_f32 v90, v52, v53\nv_smfmac_f32_16x16x64_bf16 v[84:87], v[20:23], v[224:231], v10\ns_waitcnt lgkmcnt(7)\nds_read_b128 v[20:23], v11 offset:54400\nv_cvt_pk_bf16_f32 v91, v54, v55\nv_smfmac_f32_16x16x64_bf16 v[80:83], v[24:27], v[64:71], v10\nds_read_b128 v[24:27], v11 offset:55424\n" \
"v_and_or_b32 v88, v88, -2, v13\nv_smfmac_f32_16x16x64_bf16 v[84:87], v[28:31], v[64:71], v10\nds_read_b128 v[28:31], v11 offset:56448\nv_and_or_b32 v90, v90, -2, v13\nv_smfmac_f32_16x16x64_bf16 v[80:83], v[32:35], v[248:255], v10\nbuffer_store_dwordx4 v[88:91], v7, s[36:39], s60 offen offset:2048 sc1\nv_mov_b64 v[48:49], 0\nv_mov_b64 v[50:51], 0\n" \
"v_mov_b64 v[52:53], 0\nv_mov_b64 v[54:55], 0\nv_smfmac_f32_16x16x64_bf16 v[84:87], v[36:39], v[248:255], v10\ns_waitcnt lgkmcnt(9)\nds_read_b128 v[64:67], v1 offset:16384\nv_smfmac_f32_16x16x64_bf16 v[48:51], v[40:43], a[8:15], v10\ns_waitcnt lgkmcnt(8)\nds_read_b128 v[68:71], v1 offset:17408\n" \
"v_cvt_pk_bf16_f32 v92, v80, v81\nv_smfmac_f32_16x16x64_bf16 v[52:55], v[44:47], a[8:15], v10\ns_waitcnt lgkmcnt(7)\nv_cvt_pk_bf16_f32 v93, v82, v83\nv_smfmac_f32_16x16x64_bf16 v[48:51], v[72:75], v[56:63], v10\ns_waitcnt lgkmcnt(6)\nv_cvt_pk_bf16_f32 v94, v84, v85\nv_smfmac_f32_16x16x64_bf16 v[52:55], v[76:79], v[56:63], v10\n" \
"s_add_u32 s60, s55, 20480\ns_waitcnt lgkmcnt(5)\nv_cvt_pk_bf16_f32 v95, v86, v87\nv_smfmac_f32_16x16x64_bf16 v[48:51], v[16:19], a[32:39], v10\ns_waitcnt lgkmcnt(4)\nds_read_b128 v[56:59], v1 offset:12288\nv_and_or_b32 v92, v92, -2, v13\nv_smfmac_f32_16x16x64_bf16 v[52:55], v[20:23], a[32:39], v10\n" \
"s_waitcnt lgkmcnt(4)\nds_read_b128 v[60:63], v1 offset:13312\nv_and_or_b32 v94, v94, -2, v13\nv_smfmac_f32_16x16x64_bf16 v[48:51], v[24:27], a[48:55], v10\ns_waitcnt lgkmcnt(4)\nbuffer_store_dwordx4 v[92:95], v7, s[36:39], s60 offen offset:2048 sc1\nv_mov_b64 v[80:81], 0\nv_mov_b64 v[82:83], 0\n" \
"v_mov_b64 v[84:85], 0\nv_mov_b64 v[86:87], 0\nv_smfmac_f32_16x16x64_bf16 v[52:55], v[28:31], a[48:55], v10\nv_smfmac_f32_16x16x64_bf16 v[80:83], v[40:43], a[64:71], v10\nv_cvt_pk_bf16_f32 v88, v48, v49\nv_smfmac_f32_16x16x64_bf16 v[84:87], v[44:47], a[64:71], v10\nv_cvt_pk_bf16_f32 v89, v50, v51\nv_smfmac_f32_16x16x64_bf16 v[80:83], v[72:75], a[80:87], v10\n" \
"v_cvt_pk_bf16_f32 v90, v52, v53\nv_smfmac_f32_16x16x64_bf16 v[84:87], v[76:79], a[80:87], v10\ns_add_u32 s60, s55, 0\ns_waitcnt lgkmcnt(2)\nv_cvt_pk_bf16_f32 v91, v54, v55\nv_smfmac_f32_16x16x64_bf16 v[80:83], v[16:19], v[64:71], v10\nv_and_or_b32 v88, v88, -2, v13\nv_smfmac_f32_16x16x64_bf16 v[84:87], v[20:23], v[64:71], v10\n" \
"v_and_or_b32 v90, v90, -2, v13\nv_smfmac_f32_16x16x64_bf16 v[80:83], v[24:27], a[96:103], v10\nbuffer_store_dwordx4 v[88:91], v7, s[36:39], s60 offen offset:512 sc1\nv_mov_b64 v[48:49], 0\nv_mov_b64 v[50:51], 0\nv_mov_b64 v[52:53], 0\nv_mov_b64 v[54:55], 0\nv_smfmac_f32_16x16x64_bf16 v[84:87], v[28:31], a[96:103], v10\n" \
"ds_read_b128 v[64:67], v1 offset:20480\nv_smfmac_f32_16x16x64_bf16 v[48:51], v[40:43], a[112:119], v10\nds_read_b128 v[68:71], v1 offset:21504\nv_cvt_pk_bf16_f32 v92, v80, v81\nv_smfmac_f32_16x16x64_bf16 v[52:55], v[44:47], a[112:119], v10\ns_waitcnt lgkmcnt(2)\nv_cvt_pk_bf16_f32 v93, v82, v83\nv_smfmac_f32_16x16x64_bf16 v[48:51], v[72:75], v[56:63], v10\n" \
"v_cvt_pk_bf16_f32 v94, v84, v85\nv_smfmac_f32_16x16x64_bf16 v[52:55], v[76:79], v[56:63], v10\ns_add_u32 s60, s55, 4096\nv_cvt_pk_bf16_f32 v95, v86, v87\nv_smfmac_f32_16x16x64_bf16 v[48:51], v[16:19], a[136:143], v10\nds_read_b128 v[56:59], v1 offset:10240\nv_and_or_b32 v92, v92, -2, v13\nv_smfmac_f32_16x16x64_bf16 v[52:55], v[20:23], a[136:143], v10\n" \
"ds_read_b128 v[60:63], v1 offset:11264\nv_and_or_b32 v94, v94, -2, v13\nv_smfmac_f32_16x16x64_bf16 v[48:51], v[24:27], a[152:159], v10\nbuffer_store_dwordx4 v[92:95], v7, s[36:39], s60 offen offset:512 sc1\nv_mov_b64 v[80:81], 0\nv_mov_b64 v[82:83], 0\nv_mov_b64 v[84:85], 0\nv_mov_b64 v[86:87], 0\n" \
"v_smfmac_f32_16x16x64_bf16 v[52:55], v[28:31], a[152:159], v10\nv_smfmac_f32_16x16x64_bf16 v[80:83], v[40:43], a[168:175], v10\nv_cvt_pk_bf16_f32 v88, v48, v49\nv_smfmac_f32_16x16x64_bf16 v[84:87], v[44:47], a[168:175], v10\nv_cvt_pk_bf16_f32 v89, v50, v51\nv_smfmac_f32_16x16x64_bf16 v[80:83], v[72:75], a[176:183], v10\nv_cvt_pk_bf16_f32 v90, v52, v53\nv_smfmac_f32_16x16x64_bf16 v[84:87], v[76:79], a[176:183], v10\n" \
"s_add_u32 s60, s55, 16384\ns_waitcnt lgkmcnt(2)\nv_cvt_pk_bf16_f32 v91, v54, v55\nv_smfmac_f32_16x16x64_bf16 v[80:83], v[16:19], v[64:71], v10\nv_and_or_b32 v88, v88, -2, v13\nv_smfmac_f32_16x16x64_bf16 v[84:87], v[20:23], v[64:71], v10\nv_and_or_b32 v90, v90, -2, v13\nv_smfmac_f32_16x16x64_bf16 v[80:83], v[24:27], a[200:207], v10\n" \
"buffer_store_dwordx4 v[88:91], v7, s[36:39], s60 offen offset:512 sc1\nv_mov_b64 v[48:49], 0\nv_mov_b64 v[50:51], 0\nv_mov_b64 v[52:53], 0\nv_mov_b64 v[54:55], 0\nv_smfmac_f32_16x16x64_bf16 v[84:87], v[28:31], a[200:207], v10\nds_read_b128 v[64:67], v1 offset:18432\nv_smfmac_f32_16x16x64_bf16 v[48:51], v[40:43], a[216:223], v10\n" \
"ds_read_b128 v[68:71], v1 offset:19456\nv_cvt_pk_bf16_f32 v92, v80, v81\nv_smfmac_f32_16x16x64_bf16 v[52:55], v[44:47], a[216:223], v10\ns_waitcnt lgkmcnt(2)\nv_cvt_pk_bf16_f32 v93, v82, v83\nv_smfmac_f32_16x16x64_bf16 v[48:51], v[72:75], v[56:63], v10\nv_cvt_pk_bf16_f32 v94, v84, v85\nv_smfmac_f32_16x16x64_bf16 v[52:55], v[76:79], v[56:63], v10\n" \
"s_add_u32 s60, s55, 20480\nv_cvt_pk_bf16_f32 v95, v86, v87\nv_smfmac_f32_16x16x64_bf16 v[48:51], v[16:19], a[232:239], v10\nds_read_b128 v[56:59], v1 offset:14336\nv_and_or_b32 v92, v92, -2, v13\nv_smfmac_f32_16x16x64_bf16 v[52:55], v[20:23], a[232:239], v10\nds_read_b128 v[60:63], v1 offset:15360\nv_and_or_b32 v94, v94, -2, v13\n" \
"v_smfmac_f32_16x16x64_bf16 v[48:51], v[24:27], a[248:255], v10\nbuffer_store_dwordx4 v[92:95], v7, s[36:39], s60 offen offset:512 sc1\nv_mov_b64 v[80:81], 0\nv_mov_b64 v[82:83], 0\nv_mov_b64 v[84:85], 0\nv_mov_b64 v[86:87], 0\nv_smfmac_f32_16x16x64_bf16 v[52:55], v[28:31], a[248:255], v10\nv_smfmac_f32_16x16x64_bf16 v[80:83], v[40:43], v[104:111], v10\n" \
"v_cvt_pk_bf16_f32 v88, v48, v49\nv_smfmac_f32_16x16x64_bf16 v[84:87], v[44:47], v[104:111], v10\nv_cvt_pk_bf16_f32 v89, v50, v51\nv_smfmac_f32_16x16x64_bf16 v[80:83], v[72:75], v[120:127], v10\nv_cvt_pk_bf16_f32 v90, v52, v53\nv_smfmac_f32_16x16x64_bf16 v[84:87], v[76:79], v[120:127], v10\ns_add_u32 s60, s55, 0\ns_waitcnt lgkmcnt(2)\n" \
"v_cvt_pk_bf16_f32 v91, v54, v55\nv_smfmac_f32_16x16x64_bf16 v[80:83], v[16:19], v[64:71], v10\nv_and_or_b32 v88, v88, -2, v13\nv_smfmac_f32_16x16x64_bf16 v[84:87], v[20:23], v[64:71], v10\nv_and_or_b32 v90, v90, -2, v13\nv_smfmac_f32_16x16x64_bf16 v[80:83], v[24:27], v[144:151], v10\nbuffer_store_dwordx4 v[88:91], v7, s[36:39], s60 offen offset:2560 sc1\nv_mov_b64 v[48:49], 0\n" \
"v_mov_b64 v[50:51], 0\nv_mov_b64 v[52:53], 0\nv_mov_b64 v[54:55], 0\nv_smfmac_f32_16x16x64_bf16 v[84:87], v[28:31], v[144:151], v10\nds_read_b128 v[64:67], v1 offset:22528\nv_smfmac_f32_16x16x64_bf16 v[48:51], v[40:43], v[160:167], v10\nds_read_b128 v[68:71], v1 offset:23552\nv_cvt_pk_bf16_f32 v92, v80, v81\n" \
"v_smfmac_f32_16x16x64_bf16 v[52:55], v[44:47], v[160:167], v10\ns_waitcnt lgkmcnt(2)\nv_cvt_pk_bf16_f32 v93, v82, v83\nv_smfmac_f32_16x16x64_bf16 v[48:51], v[72:75], v[56:63], v10\nv_cvt_pk_bf16_f32 v94, v84, v85\nv_smfmac_f32_16x16x64_bf16 v[52:55], v[76:79], v[56:63], v10\ns_add_u32 s60, s55, 4096\nv_cvt_pk_bf16_f32 v95, v86, v87\n" \
"v_smfmac_f32_16x16x64_bf16 v[48:51], v[16:19], v[176:183], v10\nv_and_or_b32 v92, v92, -2, v13\nv_smfmac_f32_16x16x64_bf16 v[52:55], v[20:23], v[176:183], v10\nv_and_or_b32 v94, v94, -2, v13\nv_smfmac_f32_16x16x64_bf16 v[48:51], v[24:27], v[192:199], v10\nbuffer_store_dwordx4 v[92:95], v7, s[36:39], s60 offen offset:2560 sc1\nv_mov_b64 v[80:81], 0\nv_mov_b64 v[82:83], 0\n" \
"v_mov_b64 v[84:85], 0\nv_mov_b64 v[86:87], 0\nv_smfmac_f32_16x16x64_bf16 v[52:55], v[28:31], v[192:199], v10\nds_read_b128 v[32:35], v2 offset:0\nv_smfmac_f32_16x16x64_bf16 v[80:83], v[40:43], v[208:215], v10\nds_read_b128 v[36:39], v2 offset:256\nv_cvt_pk_bf16_f32 v88, v48, v49\nv_smfmac_f32_16x16x64_bf16 v[84:87], v[44:47], v[208:215], v10\n" \
"v_cvt_pk_bf16_f32 v89, v50, v51\nv_smfmac_f32_16x16x64_bf16 v[80:83], v[72:75], v[224:231], v10\ns_add_u32 s60, s55, 16384\nds_read_b128 v[40:43], v2 offset:2048\nv_cvt_pk_bf16_f32 v90, v52, v53\nv_smfmac_f32_16x16x64_bf16 v[84:87], v[76:79], v[224:231], v10\ns_waitcnt lgkmcnt(3)\nds_read_b128 v[44:47], v2 offset:2304\n" \
"v_cvt_pk_bf16_f32 v91, v54, v55\nv_smfmac_f32_16x16x64_bf16 v[80:83], v[16:19], v[64:71], v10\nv_and_or_b32 v88, v88, -2, v13\nv_smfmac_f32_16x16x64_bf16 v[84:87], v[20:23], v[64:71], v10\nv_and_or_b32 v90, v90, -2, v13\nv_smfmac_f32_16x16x64_bf16 v[80:83], v[24:27], v[248:255], v10\nbuffer_store_dwordx4 v[88:91], v7, s[36:39], s60 offen offset:2560 sc1\nv_mov_b64 v[48:49], 0\n" \
"v_mov_b64 v[50:51], 0\nv_mov_b64 v[52:53], 0\nv_mov_b64 v[54:55], 0\nv_smfmac_f32_16x16x64_bf16 v[84:87], v[28:31], v[248:255], v10\ns_add_u32 s60, s55, 20480\ns_cmp_eq_u64 s[64:65], 0\nv_mov_b32 v15, s40\ns_cbranch_scc1 .Lvu_tok%=\n" \
"v_cvt_pk_bf16_f32 v92, v80, v81\nv_cvt_pk_bf16_f32 v93, v82, v83\ns_nop 1\nv_cvt_pk_bf16_f32 v94, v84, v85\nv_cvt_pk_bf16_f32 v95, v86, v87\nv_and_or_b32 v92, v92, -2, v13\nv_and_or_b32 v94, v94, -2, v13\nbuffer_store_dwordx4 v[92:95], v7, s[36:39], s60 offen offset:2560 sc1\n" \
"s_mov_b32 s60, -2147483648\ns_cmp_lt_i32 s40, 63\ns_cbranch_scc1 .Lvu_f0%=\ns_mov_b32 s61, -2147483648\nv_add_u32 v14, s57, v0\ns_waitcnt lgkmcnt(0)\n.Lvu_nov%=:\ns_add_u32 m0, s57, 16384\n" \
"s_nop 0\nbuffer_load_dwordx4 v3, s[36:39], s42 offen offset:0 sc1 lds\nbuffer_load_dwordx4 v3, s[36:39], s42 offen offset:1024 sc1 lds\nbuffer_load_dwordx4 v3, s[36:39], s42 offen offset:2048 sc1 lds\nbuffer_load_dwordx4 v3, s[36:39], s42 offen offset:3072 sc1 lds\ns_waitcnt vmcnt(0)\nds_read_b128 v[80:83], v14 offset:16384\nds_read_b128 v[84:87], v14 offset:17408\n" \
"ds_read_b128 v[88:91], v14 offset:18432\nds_read_b128 v[92:95], v14 offset:19456\ns_waitcnt lgkmcnt(2)\nv_max3_u32 v72, v80, v82, v84\ns_waitcnt lgkmcnt(1)\nv_max3_u32 v72, v72, v86, v88\ns_waitcnt lgkmcnt(0)\nv_max3_u32 v72, v72, v90, v92\n" \
"v_max_u32 v72, v72, v94\nv_cmp_eq_u32 vcc, -1, v72\ns_cbranch_vccz .Lvu_novfok%=\ns_add_u32 s43, s43, 1\ns_cmp_lt_u32 s43, 262144\ns_cbranch_scc1 .Lvu_nov%=\ns_branch .Lvu_abort%=\n.Lvu_novfok%=:\n" \
"s_waitcnt lgkmcnt(0)\ns_barrier\ns_memrealtime s[68:69]\nds_read_b128 v[56:59], v1 offset:0\nds_read_b128 v[60:63], v1 offset:1024\nds_read_b128 v[80:83], v0 offset:16384\nds_read_b128 v[84:87], v0 offset:17408\nds_read_b128 v[88:91], v0 offset:18432\n" \
"ds_read_b128 v[92:95], v0 offset:19456\nds_read_b128 v[16:19], v0 offset:20480\nds_read_b128 v[20:23], v0 offset:21504\nds_read_b128 v[32:35], v0 offset:22528\nds_read_b128 v[36:39], v0 offset:23552\ns_waitcnt lgkmcnt(0)\ns_branch .Lvu_crit%=\n.Lvu_re%=:\n" \
".Lvu_re2%=:\ns_add_u32 m0, s57, 16384\nv_max_u32 v72, v80, v82\nv_cmp_eq_u32 vcc, -1, v72\ns_cbranch_vccz .Lvu_sk1887%=\ns_mov_b64 exec, vcc\nbuffer_load_dwordx4 v3, s[36:39], s42 offen offset:0 sc1 lds\ns_mov_b64 exec, -1\n" \
".Lvu_sk1887%=:\nv_max_u32 v72, v84, v86\nv_cmp_eq_u32 vcc, -1, v72\ns_cbranch_vccz .Lvu_sk1894%=\ns_mov_b64 exec, vcc\nbuffer_load_dwordx4 v3, s[36:39], s42 offen offset:1024 sc1 lds\ns_mov_b64 exec, -1\n.Lvu_sk1894%=:\n" \
"v_max_u32 v72, v88, v90\nv_cmp_eq_u32 vcc, -1, v72\ns_cbranch_vccz .Lvu_sk1901%=\ns_mov_b64 exec, vcc\nbuffer_load_dwordx4 v3, s[36:39], s42 offen offset:2048 sc1 lds\ns_mov_b64 exec, -1\n.Lvu_sk1901%=:\nv_max_u32 v72, v92, v94\n" \
"v_cmp_eq_u32 vcc, -1, v72\ns_cbranch_vccz .Lvu_sk1908%=\ns_mov_b64 exec, vcc\nbuffer_load_dwordx4 v3, s[36:39], s42 offen offset:3072 sc1 lds\ns_mov_b64 exec, -1\n.Lvu_sk1908%=:\n.Lvu_skf%=:\ns_waitcnt vmcnt(0)\n" \
"ds_read_b128 v[80:83], v14 offset:16384\nds_read_b128 v[84:87], v14 offset:17408\nds_read_b128 v[88:91], v14 offset:18432\nds_read_b128 v[92:95], v14 offset:19456\ns_waitcnt lgkmcnt(2)\nv_max3_u32 v72, v80, v82, v84\ns_waitcnt lgkmcnt(1)\nv_max3_u32 v72, v72, v86, v88\n" \
"s_waitcnt lgkmcnt(0)\nv_max3_u32 v72, v72, v90, v92\nv_max_u32 v72, v72, v94\nv_cmp_eq_u32 vcc, -1, v72\ns_cbranch_vccz .Lvu_rer%=\ns_add_u32 s43, s43, 1\ns_cmp_lt_u32 s43, 262144\ns_cbranch_scc1 .Lvu_re2%=\n" \
"s_branch .Lvu_abort%=\n.Lvu_rew%=:\nds_read_b128 v[80:83], v14 offset:16384\nds_read_b128 v[84:87], v14 offset:17408\nds_read_b128 v[88:91], v14 offset:18432\nds_read_b128 v[92:95], v14 offset:19456\ns_waitcnt lgkmcnt(0)\ns_branch .Lvu_rer%=\n" \
".Lvu_first%=:\ns_nop 7\nv_cvt_pk_bf16_f32 v76, v24, v25\nv_cvt_pk_bf16_f32 v77, v26, v27\nv_cvt_pk_bf16_f32 v78, v28, v29\nv_cvt_pk_bf16_f32 v79, v30, v31\nds_write2st64_b64 v9, v[76:77], v[78:79] offset0:2 offset1:10\ns_waitcnt vmcnt(0) lgkmcnt(0)\n" \
"s_barrier\nds_read_b128 v[56:59], v1 offset:8192\nds_read_b128 v[60:63], v1 offset:9216\nds_read_b128 v[64:67], v1 offset:16384\nds_read_b128 v[68:71], v1 offset:17408\nds_read_b128 v[72:75], v11 offset:49152\nds_read_b128 v[76:79], v11 offset:50176\nds_read_b128 v[16:19], v11 offset:51200\n" \
"ds_read_b128 v[20:23], v11 offset:52224\nds_read_b128 v[24:27], v11 offset:53248\nds_read_b128 v[28:31], v11 offset:54272\nds_read_b128 v[32:35], v11 offset:55296\nds_read_b128 v[36:39], v11 offset:56320\ns_waitcnt lgkmcnt(0)\ns_add_u32 m0, s57, 0\ns_add_u32 s58, s61, 0\n" \
"buffer_load_dwordx4 v0, s[36:39], s58 offen offset:0 sc1 lds\nbuffer_load_dwordx4 v0, s[36:39], s58 offen offset:1024 sc1 lds\nbuffer_load_dwordx4 v0, s[36:39], s58 offen offset:2048 sc1 lds\nbuffer_load_dwordx4 v0, s[36:39], s58 offen offset:3072 sc1 lds\ns_mov_b32 s59, -2147483648\nbuffer_store_dword v14, off, s[36:39], s59\nbuffer_store_dword v14, off, s[36:39], s59\nbuffer_store_dword v14, off, s[36:39], s59\n" \
"s_branch .Lvu_y%=\n.Lvu_last%=:\ns_nop 7\nv_cvt_pk_bf16_f32 v92, v40, v41\nv_and_or_b32 v92, v92, -2, v12\ns_add_u32 s63, s59, 4096\nbuffer_store_dwordx4 v[88:91], v6, s[36:39], s63 offen sc1\nv_cvt_pk_bf16_f32 v93, v42, v43\n" \
"v_cvt_pk_bf16_f32 v94, v44, v45\nv_cvt_pk_bf16_f32 v95, v46, v47\nv_and_or_b32 v94, v94, -2, v12\ns_nop 0\nv_permlane16_swap_b32 v93, v95\nv_permlane16_swap_b32 v92, v94\ns_add_u32 s63, s59, 135168\nbuffer_store_dwordx4 v[92:95], v6, s[36:39], s63 offen sc1\n" \
"s_branch .Lvu_end%=\n.Lvu_abort%=:\ns_mov_b64 exec, -1\nv_mov_b32 v16, 0\ns_movk_i32 s44, 6400\nbuffer_store_dword v16, off, s[36:39], s44 sc1\n.Lvu_end%=:\ns_waitcnt vmcnt(0) lgkmcnt(0)\n" \
""
#define RD_ASM \
"v_mbcnt_lo_u32_b32 v255, -1, 0\nv_mbcnt_hi_u32_b32 v255, -1, v255\nv_lshlrev_b32 v255, 2, v255\nv_add_u32 v255, %[tb], v255\nds_read_b32 v196, v255 offset:0\nds_read_b32 v197, v255 offset:256\nds_read_b32 v198, v255 offset:512\nds_read_b32 v199, v255 offset:768\n" \
"ds_read_b32 v200, v255 offset:1024\nds_read_b32 v201, v255 offset:1280\nds_read_b32 v202, v255 offset:1536\nds_read_b32 v203, v255 offset:1792\nds_read_b32 v204, v255 offset:2048\nds_read_b32 v205, v255 offset:2304\nds_read_b32 v206, v255 offset:2560\nds_read_b32 v207, v255 offset:2816\n" \
"s_waitcnt lgkmcnt(0)\nv_accvgpr_write_b32 a32, v206\nv_accvgpr_write_b32 a33, v207\ns_mov_b32 s40, 0\ns_mov_b32 s43, 0\ns_add_u32 s57, %[su], 4096\ns_add_u32 s56, %[sp], 4096\ns_mov_b32 s41, %[sy]\n" \
"s_mov_b32 s59, -2147483648\n.Lrd_yflag%=:\nv_accvgpr_read_b32 v206, a32\nbuffer_load_dword v208, v206, %[rs], 0 offen sc1\ns_waitcnt vmcnt(0)\nv_cmp_gt_i32 vcc, s40, v208\ns_cbranch_vccz .Lrd_yld%=\ns_add_u32 s43, s43, 1\n" \
"s_cmp_lt_u32 s43, 262144\ns_cbranch_scc1 .Lrd_yflag%=\ns_branch .Lrd_abort%=\n.Lrd_yld%=:\ns_add_u32 s60, s41, 0\nbuffer_load_dwordx4 v[0:3], v196, %[rs], s60 offen offset:0 sc1\ns_add_u32 s60, s41, 0\nbuffer_load_dwordx4 v[32:35], v196, %[rs], s60 offen offset:1024 sc1\n" \
"s_add_u32 s60, s41, 0\nbuffer_load_dwordx4 v[64:67], v196, %[rs], s60 offen offset:2048 sc1\ns_add_u32 s60, s41, 131072\nbuffer_load_dwordx4 v[4:7], v196, %[rs], s60 offen offset:0 sc1\ns_add_u32 s60, s41, 131072\nbuffer_load_dwordx4 v[36:39], v196, %[rs], s60 offen offset:1024 sc1\ns_add_u32 s60, s41, 131072\nbuffer_load_dwordx4 v[68:71], v196, %[rs], s60 offen offset:2048 sc1\n" \
"s_add_u32 s60, s41, 262144\nbuffer_load_dwordx4 v[8:11], v196, %[rs], s60 offen offset:0 sc1\ns_add_u32 s60, s41, 262144\nbuffer_load_dwordx4 v[40:43], v196, %[rs], s60 offen offset:1024 sc1\ns_add_u32 s60, s41, 262144\nbuffer_load_dwordx4 v[72:75], v196, %[rs], s60 offen offset:2048 sc1\ns_add_u32 s60, s41, 393216\nbuffer_load_dwordx4 v[12:15], v196, %[rs], s60 offen offset:0 sc1\n" \
"s_add_u32 s60, s41, 393216\nbuffer_load_dwordx4 v[44:47], v196, %[rs], s60 offen offset:1024 sc1\ns_add_u32 s60, s41, 393216\nbuffer_load_dwordx4 v[76:79], v196, %[rs], s60 offen offset:2048 sc1\ns_add_u32 s60, s41, 524288\nbuffer_load_dwordx4 v[16:19], v196, %[rs], s60 offen offset:0 sc1\ns_add_u32 s60, s41, 524288\nbuffer_load_dwordx4 v[48:51], v196, %[rs], s60 offen offset:1024 sc1\n" \
"s_add_u32 s60, s41, 524288\nbuffer_load_dwordx4 v[80:83], v196, %[rs], s60 offen offset:2048 sc1\ns_add_u32 s60, s41, 655360\nbuffer_load_dwordx4 v[20:23], v196, %[rs], s60 offen offset:0 sc1\ns_add_u32 s60, s41, 655360\nbuffer_load_dwordx4 v[52:55], v196, %[rs], s60 offen offset:1024 sc1\ns_add_u32 s60, s41, 655360\nbuffer_load_dwordx4 v[84:87], v196, %[rs], s60 offen offset:2048 sc1\n" \
"s_add_u32 s60, s41, 786432\nbuffer_load_dwordx4 v[24:27], v196, %[rs], s60 offen offset:0 sc1\ns_add_u32 s60, s41, 786432\nbuffer_load_dwordx4 v[56:59], v196, %[rs], s60 offen offset:1024 sc1\ns_add_u32 s60, s41, 786432\nbuffer_load_dwordx4 v[88:91], v196, %[rs], s60 offen offset:2048 sc1\ns_add_u32 s60, s41, 917504\nbuffer_load_dwordx4 v[28:31], v196, %[rs], s60 offen offset:0 sc1\n" \
"s_add_u32 s60, s41, 917504\nbuffer_load_dwordx4 v[60:63], v196, %[rs], s60 offen offset:1024 sc1\ns_add_u32 s60, s41, 917504\nbuffer_load_dwordx4 v[92:95], v196, %[rs], s60 offen offset:2048 sc1\ns_add_u32 s60, s41, 0\nbuffer_load_dwordx4 v[96:99], v196, %[rs], s60 offen offset:3072 sc1\ns_add_u32 s60, s41, 131072\nbuffer_load_dwordx4 v[100:103], v196, %[rs], s60 offen offset:3072 sc1\n" \
"s_add_u32 s60, s41, 262144\nbuffer_load_dwordx4 v[104:107], v196, %[rs], s60 offen offset:3072 sc1\ns_add_u32 s60, s41, 393216\nbuffer_load_dwordx4 v[108:111], v196, %[rs], s60 offen offset:3072 sc1\ns_add_u32 s60, s41, 524288\nbuffer_load_dwordx4 v[112:115], v196, %[rs], s60 offen offset:3072 sc1\ns_add_u32 s60, s41, 655360\nbuffer_load_dwordx4 v[116:119], v196, %[rs], s60 offen offset:3072 sc1\n" \
"s_add_u32 s60, s41, 786432\nbuffer_load_dwordx4 v[120:123], v196, %[rs], s60 offen offset:3072 sc1\ns_add_u32 s60, s41, 917504\nbuffer_load_dwordx4 v[124:127], v196, %[rs], s60 offen offset:3072 sc1\ns_mov_b64 exec, -1\nv_accvgpr_read_b32 v212, a33\nbuffer_load_dwordx2 v[214:215], v212, %[rs], 0 offen sc1\ns_waitcnt vmcnt(0)\n" \
"s_branch .Lrd_ychk%=\n.Lrd_u2%=:\nbuffer_load_dwordx4 v[136:139], v196, %[rs], %[su] offen offset:2048 sc1\nbuffer_load_dwordx4 v[140:143], v196, %[rs], %[su] offen offset:3072 sc1\ns_add_u32 s43, s43, 1\ns_cmp_ge_u32 s43, 262144\ns_cbranch_scc1 .Lrd_abort%=\ns_waitcnt vmcnt(0)\n" \
"v_bitop3_b32 v208, v136, v138, s54 bitop3:0x7e\nv_bitop3_b32 v209, v140, v142, s54 bitop3:0x7e\nv_bitop3_b32 v208, v208, 1, v209 bitop3:0xc8\nv_cmp_ne_u32 vcc, 0, v208\ns_cbranch_vccnz .Lrd_u2%=\n.Lrd_u3%=:\nbuffer_load_dwordx4 v[128:131], v196, %[rs], %[su] offen offset:0 sc1\nbuffer_load_dwordx4 v[132:135], v196, %[rs], %[su] offen offset:1024 sc1\n" \
"buffer_load_dwordx4 v[144:147], v196, %[rs], s57 offen offset:0 sc1\nbuffer_load_dwordx4 v[148:151], v196, %[rs], s57 offen offset:1024 sc1\nbuffer_load_dwordx4 v[152:155], v196, %[rs], s57 offen offset:2048 sc1\nbuffer_load_dwordx4 v[156:159], v196, %[rs], s57 offen offset:3072 sc1\nbuffer_load_dwordx4 v[160:163], v196, %[rs], %[sp] offen offset:0 sc1\nbuffer_load_dwordx4 v[164:167], v196, %[rs], %[sp] offen offset:1024 sc1\nbuffer_load_dwordx4 v[168:171], v196, %[rs], %[sp] offen offset:2048 sc1\nbuffer_load_dwordx4 v[172:175], v196, %[rs], %[sp] offen offset:3072 sc1\n" \
"buffer_load_dwordx4 v[176:179], v196, %[rs], s56 offen offset:0 sc1\n.Lrd_ra%=:\ns_waitcnt vmcnt(5)\nv_bitop3_b32 v208, v128, v130, s54 bitop3:0x7e\nv_bitop3_b32 v209, v136, v138, s54 bitop3:0x7e\nv_bitop3_b32 v208, v208, 1, v209 bitop3:0xc8\nv_cmp_ne_u32 s[86:87], 0, v208\nv_bitop3_b32 v208, v144, v146, s54 bitop3:0x7e\n" \
"v_bitop3_b32 v209, v152, v154, s54 bitop3:0x7e\nv_bitop3_b32 v208, v208, 1, v209 bitop3:0xc8\nv_cmp_ne_u32 s[88:89], 0, v208\nv_bitop3_b32 v208, v132, v134, s54 bitop3:0x7e\nv_bitop3_b32 v209, v140, v142, s54 bitop3:0x7e\nv_bitop3_b32 v208, v208, 1, v209 bitop3:0xc8\nv_cmp_ne_u32 s[68:69], 0, v208\nv_bitop3_b32 v208, v148, v150, s54 bitop3:0x7e\n" \
"v_bitop3_b32 v209, v156, v158, s54 bitop3:0x7e\nv_bitop3_b32 v208, v208, 1, v209 bitop3:0xc8\nv_cmp_ne_u32 s[62:63], 0, v208\ns_or_b64 vcc, s[86:87], s[88:89]\ns_or_b64 vcc, vcc, s[68:69]\ns_or_b64 vcc, vcc, s[62:63]\ns_waitcnt vmcnt(4)\nv_smfmac_f32_16x16x64_bf16 v[180:183], v[160:163], v[0:7], v197\n" \
"v_bitop3_b32 v208, v160, v162, s54 bitop3:0x7e\nv_smfmac_f32_16x16x64_bf16 v[184:187], v[160:163], v[32:39], v197\nv_cmp_gt_i32 s[46:47], 0, v208\nv_smfmac_f32_16x16x64_bf16 v[188:191], v[160:163], v[64:71], v197\nv_smfmac_f32_16x16x64_bf16 v[192:195], v[160:163], v[96:103], v197\ns_or_b64 vcc, vcc, s[46:47]\ns_waitcnt vmcnt(3)\nv_smfmac_f32_16x16x64_bf16 v[180:183], v[164:167], v[8:15], v197\n" \
"v_bitop3_b32 v208, v164, v166, s54 bitop3:0x7e\nv_smfmac_f32_16x16x64_bf16 v[184:187], v[164:167], v[40:47], v197\nv_cmp_gt_i32 s[48:49], 0, v208\nv_smfmac_f32_16x16x64_bf16 v[188:191], v[164:167], v[72:79], v197\nv_smfmac_f32_16x16x64_bf16 v[192:195], v[164:167], v[104:111], v197\ns_or_b64 vcc, vcc, s[48:49]\ns_waitcnt vmcnt(2)\nv_smfmac_f32_16x16x64_bf16 v[180:183], v[168:171], v[16:23], v197\n" \
"v_bitop3_b32 v208, v168, v170, s54 bitop3:0x7e\nv_smfmac_f32_16x16x64_bf16 v[184:187], v[168:171], v[48:55], v197\nv_cmp_gt_i32 s[50:51], 0, v208\nv_smfmac_f32_16x16x64_bf16 v[188:191], v[168:171], v[80:87], v197\nv_smfmac_f32_16x16x64_bf16 v[192:195], v[168:171], v[112:119], v197\ns_or_b64 vcc, vcc, s[50:51]\ns_waitcnt vmcnt(1)\nv_smfmac_f32_16x16x64_bf16 v[180:183], v[172:175], v[24:31], v197\n" \
"s_waitcnt vmcnt(0)\nv_bitop3_b32 v208, v172, v174, s54 bitop3:0x7e\nv_smfmac_f32_16x16x64_bf16 v[180:183], v[176:179], v[128:135], v197\nv_bitop3_b32 v209, v176, v178, s54 bitop3:0x7e\nv_smfmac_f32_16x16x64_bf16 v[184:187], v[172:175], v[56:63], v197\nv_bitop3_b32 v210, v177, v179, s54 bitop3:0x7e\nv_smfmac_f32_16x16x64_bf16 v[184:187], v[176:179], v[136:143], v197\nv_cmp_gt_i32 s[52:53], 0, v208\n" \
"v_smfmac_f32_16x16x64_bf16 v[188:191], v[172:175], v[88:95], v197\nv_or_b32 v209, v209, v210\nv_smfmac_f32_16x16x64_bf16 v[188:191], v[176:179], v[144:151], v197\ns_or_b64 vcc, vcc, s[52:53]\nv_cmp_gt_i32 s[64:65], 0, v209\nv_smfmac_f32_16x16x64_bf16 v[192:195], v[172:175], v[120:127], v197\nv_smfmac_f32_16x16x64_bf16 v[192:195], v[176:179], v[152:159], v197\ns_or_b64 vcc, vcc, s[64:65]\n" \
"s_cbranch_scc1 .Lrd_rb%=\ns_add_u32 s60, s58, 16384\nv_add_f32 v180, v180, v182\nv_add_f32 v181, v181, v183\nds_write2_b32 v198, v180, v181 offset0:0 offset1:8\nbuffer_load_dwordx4 v[212:215], v196, %[rs], s60 offen offset:0 sc1\nv_add_f32 v184, v184, v186\nv_add_f32 v185, v185, v187\n" \
"ds_write2_b32 v198, v184, v185 offset0:32 offset1:40\ns_add_u32 s60, s58, 0\nbuffer_load_dwordx4 v[216:219], v196, %[rs], s60 offen offset:0 sc1\nv_add_f32 v188, v188, v190\nv_add_f32 v189, v189, v191\nds_write2_b32 v198, v188, v189 offset0:64 offset1:72\ns_add_u32 s60, s58, 0\nbuffer_load_dwordx4 v[220:223], v196, %[rs], s60 offen offset:1024 sc1\n" \
"v_add_f32 v192, v192, v194\nv_add_f32 v193, v193, v195\nds_write2_b32 v198, v192, v193 offset0:96 offset1:104\ns_add_u32 s60, s58, 16384\nbuffer_load_dwordx4 v[224:227], v196, %[rs], s60 offen offset:1024 sc1\ns_waitcnt lgkmcnt(0)\ns_barrier\nds_read_b128 v[180:183], v199 offset:0\n" \
"ds_read_b128 v[184:187], v199 offset:4096\nds_read_b128 v[188:191], v199 offset:8192\nds_read_b128 v[192:195], v199 offset:12288\ns_and_b32 s44, s54, 0x80008000\ns_add_u32 s60, s58, 0\nbuffer_load_dwordx4 v[228:231], v196, %[rs], s60 offen offset:2048 sc1\ns_waitcnt lgkmcnt(2)\nv_pk_add_f32 v[182:183], v[182:183], v[186:187]\n" \
"v_pk_add_f32 v[180:181], v[180:181], v[184:185]\ns_waitcnt lgkmcnt(0)\nv_pk_add_f32 v[186:187], v[188:189], v[192:193]\nv_pk_add_f32 v[184:185], v[190:191], v[194:195]\nv_pk_add_f32 v[180:181], v[180:181], v[186:187]\nv_pk_add_f32 v[182:183], v[182:183], v[184:185]\nv_cvt_pk_bf16_f32 v180, v180, v181\nv_xor_b32 v180, s44, v180\n" \
"v_cvt_pk_bf16_f32 v181, v182, v183\nv_min_u32 v180, -2, v180\nv_xor_b32 v209, s44, v181\nv_or_b32 v208, v180, v201\nbuffer_store_dwordx2 v[208:209], v206, %[rs], 0 offen sc1\ns_add_u32 s60, s58, 0\nbuffer_load_dwordx4 v[232:235], v196, %[rs], s60 offen offset:3072 sc1\ns_add_u32 s60, s58, 16384\n" \
"buffer_load_dwordx4 v[236:239], v196, %[rs], s60 offen offset:2048 sc1\ns_add_u32 s60, s58, 4096\nbuffer_load_dwordx4 v[240:243], v196, %[rs], s60 offen offset:0 sc1\ns_add_u32 s60, s58, 4096\nbuffer_load_dwordx4 v[244:247], v196, %[rs], s60 offen offset:1024 sc1\ns_add_u32 s60, s58, 16384\nbuffer_load_dwordx4 v[248:251], v196, %[rs], s60 offen offset:3072 sc1\ns_xor_b32 s41, s41, 4194304\n" \
"v_mov_b32 v207, s40\ns_waitcnt vmcnt(10)\nv_bitop3_b32 v209, v212, v214, s61 bitop3:0x7e\nv_cmp_gt_i32 vcc, 0, v209\ns_cbranch_vccnz .Lrd_tre0%=\n.Lrd_tl0%=:\nv_mfma_f32_16x16x16_bf16 a[0:3], v[0:1], v[212:213], 0\ns_add_u32 s60, s58, 20480\n" \
"buffer_load_dwordx4 v[252:255], v196, %[rs], s60 offen offset:0 sc1\nv_mfma_f32_16x16x16_bf16 a[8:11], v[32:33], v[212:213], 0\nv_mfma_f32_16x16x16_bf16 a[16:19], v[64:65], v[212:213], 0\nv_mfma_f32_16x16x16_bf16 a[24:27], v[96:97], v[212:213], 0\ns_waitcnt vmcnt(10)\nv_mfma_f32_16x16x16_bf16 a[0:3], v[4:5], v[214:215], a[0:3]\ns_add_u32 s60, s58, 20480\nbuffer_load_dwordx4 v[160:163], v196, %[rs], s60 offen offset:2048 sc1\n" \
"v_bitop3_b32 v209, v216, v218, s61 bitop3:0x7e\nv_mfma_f32_16x16x16_bf16 a[8:11], v[36:37], v[214:215], a[8:11]\ns_lshl_b32 s42, s40, 17\ns_add_u32 s42, s42, %[at]\nv_mfma_f32_16x16x16_bf16 a[16:19], v[68:69], v[214:215], a[16:19]\nv_cmp_gt_i32 vcc, 0, v209\nv_mfma_f32_16x16x16_bf16 a[24:27], v[100:101], v[214:215], a[24:27]\ns_cbranch_vccnz .Lrd_tre1%=\n" \
".Lrd_tl1%=:\ns_waitcnt vmcnt(10)\nv_mfma_f32_16x16x32_bf16 a[4:7], v[0:3], v[216:219], 0\ns_add_u32 s60, s58, 4096\nbuffer_load_dwordx4 v[164:167], v196, %[rs], s60 offen offset:2048 sc1\nv_bitop3_b32 v209, v220, v222, s61 bitop3:0x7e\nv_mfma_f32_16x16x32_bf16 a[12:15], v[32:35], v[216:219], 0\ns_lshr_b32 s45, s42, 8\n" \
"s_and_b32 s45, s45, 0x2000\nv_mfma_f32_16x16x32_bf16 a[20:23], v[64:67], v[216:219], 0\nv_cmp_gt_i32 vcc, 0, v209\nv_mfma_f32_16x16x32_bf16 a[28:31], v[96:99], v[216:219], 0\ns_cbranch_vccnz .Lrd_tre2%=\n.Lrd_tl2%=:\ns_waitcnt vmcnt(10)\nv_mfma_f32_16x16x32_bf16 a[4:7], v[4:7], v[220:223], a[4:7]\n" \
"s_add_u32 s60, s58, 4096\nbuffer_load_dwordx4 v[168:171], v196, %[rs], s60 offen offset:3072 sc1\nv_bitop3_b32 v209, v224, v226, s61 bitop3:0x7e\nv_mfma_f32_16x16x32_bf16 a[12:15], v[36:39], v[220:223], a[12:15]\ns_xor_b32 s42, s42, s45\ns_add_u32 s40, s40, 1\nv_mfma_f32_16x16x32_bf16 a[20:23], v[68:71], v[220:223], a[20:23]\nv_cmp_gt_i32 vcc, 0, v209\n" \
"v_mfma_f32_16x16x32_bf16 a[28:31], v[100:103], v[220:223], a[28:31]\ns_cbranch_vccnz .Lrd_tre3%=\n.Lrd_tl3%=:\nv_mfma_f32_16x16x16_bf16 a[0:3], v[8:9], v[224:225], a[0:3]\ns_add_u32 s60, s58, 20480\nbuffer_load_dwordx4 v[172:175], v196, %[rs], s60 offen offset:1024 sc1\nv_mfma_f32_16x16x16_bf16 a[8:11], v[40:41], v[224:225], a[8:11]\nv_mfma_f32_16x16x16_bf16 a[16:19], v[72:73], v[224:225], a[16:19]\n" \
"buffer_load_dwordx4 v[212:215], v204, %[rs], s42 offen sc1\nv_mfma_f32_16x16x16_bf16 a[24:27], v[104:105], v[224:225], a[24:27]\ns_waitcnt vmcnt(12)\nv_mfma_f32_16x16x16_bf16 a[0:3], v[12:13], v[226:227], a[0:3]\ns_add_u32 s60, s58, 20480\nbuffer_load_dwordx4 v[176:179], v196, %[rs], s60 offen offset:3072 sc1\nv_bitop3_b32 v209, v228, v230, s61 bitop3:0x7e\nv_mfma_f32_16x16x16_bf16 a[8:11], v[44:45], v[226:227], a[8:11]\n" \
"s_lshl_b32 s62, s55, 31\ns_or_b32 s62, s62, 1.0\ns_mov_b32 s63, s62\nv_mfma_f32_16x16x16_bf16 a[16:19], v[76:77], v[226:227], a[16:19]\nv_cmp_gt_i32 vcc, 0, v209\nv_mfma_f32_16x16x16_bf16 a[24:27], v[108:109], v[226:227], a[24:27]\ns_cbranch_vccnz .Lrd_tre4%=\n.Lrd_tl4%=:\n" \
"s_waitcnt vmcnt(11)\nv_mfma_f32_16x16x32_bf16 a[4:7], v[8:11], v[228:231], a[4:7]\ns_cmp_eq_u32 s40, 1\ns_cbranch_scc1 .Lrd_y1%=\n.Lrd_y1r%=:\ns_add_u32 s60, s41, 0\nbuffer_load_dwordx4 v[0:3], v196, %[rs], s60 offen offset:0 sc1\nv_bitop3_b32 v209, v232, v234, s61 bitop3:0x7e\n" \
"v_mfma_f32_16x16x32_bf16 a[12:15], v[40:43], v[228:231], a[12:15]\nv_mfma_f32_16x16x32_bf16 a[20:23], v[72:75], v[228:231], a[20:23]\nv_cmp_gt_i32 vcc, 0, v209\nv_mfma_f32_16x16x32_bf16 a[28:31], v[104:107], v[228:231], a[28:31]\ns_cbranch_vccnz .Lrd_tre5%=\n.Lrd_tl5%=:\ns_waitcnt vmcnt(11)\nv_mfma_f32_16x16x32_bf16 a[4:7], v[12:15], v[232:235], a[4:7]\n" \
"s_add_u32 s60, s41, 0\nbuffer_load_dwordx4 v[32:35], v196, %[rs], s60 offen offset:1024 sc1\nv_bitop3_b32 v209, v236, v238, s61 bitop3:0x7e\nv_mfma_f32_16x16x32_bf16 a[12:15], v[44:47], v[232:235], a[12:15]\nv_mfma_f32_16x16x32_bf16 a[20:23], v[76:79], v[232:235], a[20:23]\nv_cmp_gt_i32 vcc, 0, v209\nv_mfma_f32_16x16x32_bf16 a[28:31], v[108:111], v[232:235], a[28:31]\ns_cbranch_vccnz .Lrd_tre6%=\n" \
".Lrd_tl6%=:\nv_mfma_f32_16x16x16_bf16 a[0:3], v[16:17], v[236:237], a[0:3]\ns_add_u32 s60, s41, 0\nbuffer_load_dwordx4 v[64:67], v196, %[rs], s60 offen offset:2048 sc1\nv_mfma_f32_16x16x16_bf16 a[8:11], v[48:49], v[236:237], a[8:11]\nv_mfma_f32_16x16x16_bf16 a[16:19], v[80:81], v[236:237], a[16:19]\nv_mfma_f32_16x16x16_bf16 a[24:27], v[112:113], v[236:237], a[24:27]\ns_waitcnt vmcnt(12)\n" \
"v_mfma_f32_16x16x16_bf16 a[0:3], v[20:21], v[238:239], a[0:3]\ns_add_u32 s60, s41, 131072\nbuffer_load_dwordx4 v[4:7], v196, %[rs], s60 offen offset:0 sc1\nv_bitop3_b32 v209, v240, v242, s61 bitop3:0x7e\nv_mfma_f32_16x16x16_bf16 a[8:11], v[52:53], v[238:239], a[8:11]\nv_mfma_f32_16x16x16_bf16 a[16:19], v[84:85], v[238:239], a[16:19]\nv_cmp_gt_i32 vcc, 0, v209\nv_mfma_f32_16x16x16_bf16 a[24:27], v[116:117], v[238:239], a[24:27]\n" \
"s_cbranch_vccnz .Lrd_tre7%=\n.Lrd_tl7%=:\ns_waitcnt vmcnt(12)\nv_mfma_f32_16x16x32_bf16 a[4:7], v[16:19], v[240:243], a[4:7]\ns_add_u32 s60, s41, 131072\nbuffer_load_dwordx4 v[36:39], v196, %[rs], s60 offen offset:1024 sc1\nv_bitop3_b32 v209, v244, v246, s61 bitop3:0x7e\nv_mfma_f32_16x16x32_bf16 a[12:15], v[48:51], v[240:243], a[12:15]\n" \
"v_mfma_f32_16x16x32_bf16 a[20:23], v[80:83], v[240:243], a[20:23]\nv_cmp_gt_i32 vcc, 0, v209\nv_mfma_f32_16x16x32_bf16 a[28:31], v[112:115], v[240:243], a[28:31]\ns_cbranch_vccnz .Lrd_tre8%=\n.Lrd_tl8%=:\ns_waitcnt vmcnt(12)\nv_mfma_f32_16x16x32_bf16 a[4:7], v[20:23], v[244:247], a[4:7]\ns_add_u32 s60, s41, 131072\n" \
"buffer_load_dwordx4 v[68:71], v196, %[rs], s60 offen offset:2048 sc1\nv_bitop3_b32 v209, v248, v250, s61 bitop3:0x7e\nv_mfma_f32_16x16x32_bf16 a[12:15], v[52:55], v[244:247], a[12:15]\nv_mfma_f32_16x16x32_bf16 a[20:23], v[84:87], v[244:247], a[20:23]\nv_cmp_gt_i32 vcc, 0, v209\nv_mfma_f32_16x16x32_bf16 a[28:31], v[116:119], v[244:247], a[28:31]\ns_cbranch_vccnz .Lrd_tre9%=\n.Lrd_tl9%=:\n" \
"v_mfma_f32_16x16x16_bf16 a[0:3], v[24:25], v[248:249], a[0:3]\ns_add_u32 s60, s41, 262144\nbuffer_load_dwordx4 v[8:11], v196, %[rs], s60 offen offset:0 sc1\nv_mfma_f32_16x16x16_bf16 a[8:11], v[56:57], v[248:249], a[8:11]\nv_mfma_f32_16x16x16_bf16 a[16:19], v[88:89], v[248:249], a[16:19]\nv_mfma_f32_16x16x16_bf16 a[24:27], v[120:121], v[248:249], a[24:27]\ns_waitcnt vmcnt(13)\nv_mfma_f32_16x16x16_bf16 a[0:3], v[28:29], v[250:251], a[0:3]\n" \
"s_add_u32 s60, s41, 262144\nbuffer_load_dwordx4 v[40:43], v196, %[rs], s60 offen offset:1024 sc1\nv_bitop3_b32 v209, v252, v254, s61 bitop3:0x7e\nv_mfma_f32_16x16x16_bf16 a[8:11], v[60:61], v[250:251], a[8:11]\nv_bitop3_b32 v210, v253, v255, s61 bitop3:0x7e\nv_or_b32 v209, v209, v210\nv_mfma_f32_16x16x16_bf16 a[16:19], v[92:93], v[250:251], a[16:19]\nv_cmp_gt_i32 vcc, 0, v209\n" \
"v_mfma_f32_16x16x16_bf16 a[24:27], v[124:125], v[250:251], a[24:27]\ns_cbranch_vccnz .Lrd_tre10%=\n.Lrd_tl10%=:\ns_waitcnt vmcnt(13)\nv_mfma_f32_16x16x32_bf16 a[0:3], v[128:131], v[252:255], a[0:3]\ns_add_u32 s60, s41, 262144\nbuffer_load_dwordx4 v[72:75], v196, %[rs], s60 offen offset:2048 sc1\nv_bitop3_b32 v209, v160, v162, s61 bitop3:0x7e\n" \
"v_mfma_f32_16x16x32_bf16 a[8:11], v[136:139], v[252:255], a[8:11]\nv_bitop3_b32 v210, v161, v163, s61 bitop3:0x7e\nv_or_b32 v209, v209, v210\nv_mfma_f32_16x16x32_bf16 a[16:19], v[144:147], v[252:255], a[16:19]\nv_cmp_gt_i32 vcc, 0, v209\nv_mfma_f32_16x16x32_bf16 a[24:27], v[152:155], v[252:255], a[24:27]\ns_cbranch_vccnz .Lrd_tre11%=\n.Lrd_tl11%=:\n" \
"s_waitcnt vmcnt(13)\nv_mfma_f32_16x16x32_bf16 a[0:3], v[132:135], v[160:163], a[0:3]\ns_add_u32 s60, s41, 393216\nbuffer_load_dwordx4 v[12:15], v196, %[rs], s60 offen offset:0 sc1\nv_bitop3_b32 v209, v164, v166, s61 bitop3:0x7e\nv_mfma_f32_16x16x32_bf16 a[8:11], v[140:143], v[160:163], a[8:11]\nv_mfma_f32_16x16x32_bf16 a[16:19], v[148:151], v[160:163], a[16:19]\nv_cmp_gt_i32 vcc, 0, v209\n" \
"v_mfma_f32_16x16x32_bf16 a[24:27], v[156:159], v[160:163], a[24:27]\ns_cbranch_vccnz .Lrd_tre12%=\n.Lrd_tl12%=:\ns_waitcnt vmcnt(13)\nv_mfma_f32_16x16x32_bf16 a[4:7], v[24:27], v[164:167], a[4:7]\ns_add_u32 s60, s41, 393216\nbuffer_load_dwordx4 v[44:47], v196, %[rs], s60 offen offset:1024 sc1\nv_bitop3_b32 v209, v168, v170, s61 bitop3:0x7e\n" \
"v_mfma_f32_16x16x32_bf16 a[12:15], v[56:59], v[164:167], a[12:15]\nv_mfma_f32_16x16x32_bf16 a[20:23], v[88:91], v[164:167], a[20:23]\nds_write_b128 v202, a[0:3] offset:0\nv_cmp_gt_i32 vcc, 0, v209\nv_mfma_f32_16x16x32_bf16 a[28:31], v[120:123], v[164:167], a[28:31]\ns_cbranch_vccnz .Lrd_tre13%=\n.Lrd_tl13%=:\ns_waitcnt vmcnt(13)\n" \
"v_mfma_f32_16x16x32_bf16 a[4:7], v[28:31], v[168:171], a[4:7]\ns_add_u32 s60, s41, 393216\nbuffer_load_dwordx4 v[76:79], v196, %[rs], s60 offen offset:2048 sc1\nv_bitop3_b32 v209, v172, v174, s61 bitop3:0x7e\nv_mfma_f32_16x16x32_bf16 a[12:15], v[60:63], v[168:171], a[12:15]\nv_bitop3_b32 v210, v173, v175, s61 bitop3:0x7e\nv_or_b32 v209, v209, v210\nv_mfma_f32_16x16x32_bf16 a[20:23], v[92:95], v[168:171], a[20:23]\n" \
"ds_write_b128 v202, a[8:11] offset:2048\nv_cmp_gt_i32 vcc, 0, v209\nv_mfma_f32_16x16x32_bf16 a[28:31], v[124:127], v[168:171], a[28:31]\ns_cbranch_vccnz .Lrd_tre14%=\n.Lrd_tl14%=:\ns_waitcnt vmcnt(12)\nv_mfma_f32_16x16x32_bf16 a[4:7], v[128:131], v[172:175], a[4:7]\ns_add_u32 s60, s41, 524288\n" \
"buffer_load_dwordx4 v[16:19], v196, %[rs], s60 offen offset:0 sc1\nv_bitop3_b32 v209, v176, v178, s61 bitop3:0x7e\nv_mfma_f32_16x16x32_bf16 a[12:15], v[136:139], v[172:175], a[12:15]\nv_bitop3_b32 v210, v177, v179, s61 bitop3:0x7e\nv_or_b32 v209, v209, v210\nv_mfma_f32_16x16x32_bf16 a[20:23], v[144:147], v[172:175], a[20:23]\nds_write_b128 v202, a[16:19] offset:4096\nv_cmp_gt_i32 vcc, 0, v209\n" \
"v_mfma_f32_16x16x32_bf16 a[28:31], v[152:155], v[172:175], a[28:31]\ns_cbranch_vccnz .Lrd_tre15%=\n.Lrd_tl15%=:\nv_mfma_f32_16x16x32_bf16 a[4:7], v[132:135], v[176:179], a[4:7]\ns_add_u32 s60, s41, 524288\nbuffer_load_dwordx4 v[48:51], v196, %[rs], s60 offen offset:1024 sc1\nv_lshlrev_b32 v216, 16, v212\nv_and_b32 v217, 0xffff0000, v212\n" \
"v_mfma_f32_16x16x32_bf16 a[12:15], v[140:143], v[176:179], a[12:15]\nv_lshlrev_b32 v218, 16, v213\nv_and_b32 v219, 0xffff0000, v213\nv_mfma_f32_16x16x32_bf16 a[20:23], v[148:151], v[176:179], a[20:23]\nds_write_b128 v202, a[24:27] offset:6144\nv_lshlrev_b32 v220, 16, v214\nv_and_b32 v221, 0xffff0000, v214\nv_mfma_f32_16x16x32_bf16 a[28:31], v[156:159], v[176:179], a[28:31]\n" \
"s_add_u32 s60, s41, 524288\nbuffer_load_dwordx4 v[80:83], v196, %[rs], s60 offen offset:2048 sc1\ns_add_u32 s60, s41, 655360\nds_write_b128 v202, a[4:7] offset:1024\nbuffer_load_dwordx4 v[20:23], v196, %[rs], s60 offen offset:0 sc1\nds_write_b128 v202, a[12:15] offset:3072\nv_lshlrev_b32 v222, 16, v215\nv_and_b32 v223, 0xffff0000, v215\n" \
"s_add_u32 s60, s41, 655360\nbuffer_load_dwordx4 v[52:55], v196, %[rs], s60 offen offset:1024 sc1\nds_write_b128 v202, a[20:23] offset:5120\ns_add_u32 s60, s41, 655360\nbuffer_load_dwordx4 v[84:87], v196, %[rs], s60 offen offset:2048 sc1\nds_write_b128 v202, a[28:31] offset:7168\ns_waitcnt lgkmcnt(0)\ns_barrier\n" \
"ds_read_b128 v[224:227], v203 offset:0\nds_read_b128 v[228:231], v203 offset:256\nds_read_b128 v[232:235], v203 offset:8192\nds_read_b128 v[236:239], v203 offset:8448\nds_read_b128 v[240:243], v203 offset:16384\nds_read_b128 v[244:247], v203 offset:16640\nds_read_b128 v[248:251], v203 offset:24576\nds_read_b128 v[252:255], v203 offset:24832\n" \
"s_add_u32 s60, s41, 786432\nbuffer_load_dwordx4 v[24:27], v196, %[rs], s60 offen offset:0 sc1\ns_waitcnt lgkmcnt(4)\nv_pk_add_f32 v[224:225], v[224:225], v[232:233]\nv_pk_add_f32 v[226:227], v[226:227], v[234:235]\nv_pk_add_f32 v[228:229], v[228:229], v[236:237]\nv_pk_add_f32 v[230:231], v[230:231], v[238:239]\ns_add_u32 s60, s41, 786432\n" \
"buffer_load_dwordx4 v[56:59], v196, %[rs], s60 offen offset:1024 sc1\ns_waitcnt lgkmcnt(2)\nv_pk_add_f32 v[224:225], v[224:225], v[240:241]\nv_pk_add_f32 v[226:227], v[226:227], v[242:243]\nv_pk_add_f32 v[228:229], v[228:229], v[244:245]\nv_pk_add_f32 v[230:231], v[230:231], v[246:247]\ns_add_u32 s60, s41, 786432\nbuffer_load_dwordx4 v[88:91], v196, %[rs], s60 offen offset:2048 sc1\n" \
"s_waitcnt lgkmcnt(0)\nv_pk_add_f32 v[224:225], v[224:225], v[248:249]\nv_pk_add_f32 v[226:227], v[226:227], v[250:251]\nv_pk_add_f32 v[228:229], v[228:229], v[252:253]\nv_pk_add_f32 v[230:231], v[230:231], v[254:255]\nv_pk_fma_f32 v[224:225], v[224:225], s[62:63], v[216:217]\nv_pk_fma_f32 v[226:227], v[226:227], s[62:63], v[218:219]\nv_pk_fma_f32 v[228:229], v[228:229], s[62:63], v[220:221]\n" \
"v_pk_fma_f32 v[230:231], v[230:231], s[62:63], v[222:223]\nv_cvt_pk_bf16_f32 v208, v224, v225\nv_cvt_pk_bf16_f32 v209, v226, v227\nv_cvt_pk_bf16_f32 v210, v228, v229\nv_cvt_pk_bf16_f32 v211, v230, v231\nbuffer_store_dwordx4 v[208:211], v204, %[rs], s42 offen sc1\nv_pk_add_f32 v[232:233], v[224:225], v[226:227]\nv_pk_add_f32 v[234:235], v[228:229], v[230:231]\n" \
"v_pk_add_f32 v[232:233], v[232:233], v[234:235]\ns_add_u32 s60, s41, 917504\nbuffer_load_dwordx4 v[28:31], v196, %[rs], s60 offen offset:0 sc1\ns_add_u32 s60, s41, 917504\nbuffer_load_dwordx4 v[60:63], v196, %[rs], s60 offen offset:1024 sc1\nv_add_f32 v232, v232, v233\ns_nop 1\nv_add_f32_dpp v233, v232, v232 row_ror:8 row_mask:0xf bank_mask:0xf\n" \
"v_add_f32_dpp v234, v232, v232 row_ror:8 row_mask:0xf bank_mask:0xf\ns_nop 1\nv_permlane16_swap_b32 v233, v234\nv_add_f32 v232, v233, v234\nv_add_f32 v233, v233, v234\ns_nop 1\nv_permlane32_swap_b32 v232, v233\nv_add_f32 v232, v232, v233\n" \
"v_mul_f32 v232, 0x3c800000, v232\nv_pk_add_f32 v[224:225], v[224:225], v[232:233] op_sel_hi:[1,0] neg_lo:[0,1] neg_hi:[0,1]\nv_pk_add_f32 v[226:227], v[226:227], v[232:233] op_sel_hi:[1,0] neg_lo:[0,1] neg_hi:[0,1]\nv_pk_add_f32 v[228:229], v[228:229], v[232:233] op_sel_hi:[1,0] neg_lo:[0,1] neg_hi:[0,1]\nv_pk_add_f32 v[230:231], v[230:231], v[232:233] op_sel_hi:[1,0] neg_lo:[0,1] neg_hi:[0,1]\ns_add_u32 s60, s41, 917504\nbuffer_load_dwordx4 v[92:95], v196, %[rs], s60 offen offset:2048 sc1\ns_add_u32 s60, s41, 0\n" \
"buffer_load_dwordx4 v[96:99], v196, %[rs], s60 offen offset:3072 sc1\nv_pk_mul_f32 v[236:237], v[224:225], v[224:225]\nv_pk_mul_f32 v[238:239], v[226:227], v[226:227]\nv_pk_fma_f32 v[236:237], v[228:229], v[228:229], v[236:237]\nv_pk_fma_f32 v[238:239], v[230:231], v[230:231], v[238:239]\nv_pk_add_f32 v[236:237], v[236:237], v[238:239]\nv_add_f32 v236, v236, v237\ns_nop 1\n" \
"v_add_f32_dpp v237, v236, v236 row_ror:8 row_mask:0xf bank_mask:0xf\nv_add_f32_dpp v238, v236, v236 row_ror:8 row_mask:0xf bank_mask:0xf\ns_nop 1\nv_permlane16_swap_b32 v237, v238\nv_add_f32 v236, v237, v238\nv_add_f32 v237, v237, v238\ns_nop 1\nv_permlane32_swap_b32 v236, v237\n" \
"v_add_f32 v236, v236, v237\nv_mov_b32 v233, v236\nbuffer_store_dwordx2 v[232:233], v205, %[rs], s42 offen sc1\ns_mov_b32 s59, %[fa]\ns_add_u32 s60, s41, 131072\nbuffer_load_dwordx4 v[100:103], v196, %[rs], s60 offen offset:3072 sc1\nv_accvgpr_read_b32 v212, a33\nbuffer_load_dwordx2 v[214:215], v212, %[rs], 0 offen sc1\n" \
"s_add_u32 s60, s41, 262144\nbuffer_load_dwordx4 v[104:107], v196, %[rs], s60 offen offset:3072 sc1\ns_add_u32 s60, s41, 393216\nbuffer_load_dwordx4 v[108:111], v196, %[rs], s60 offen offset:3072 sc1\ns_add_u32 s60, s41, 524288\nbuffer_load_dwordx4 v[112:115], v196, %[rs], s60 offen offset:3072 sc1\ns_add_u32 s60, s41, 655360\nbuffer_load_dwordx4 v[116:119], v196, %[rs], s60 offen offset:3072 sc1\n" \
"s_add_u32 s60, s41, 786432\nbuffer_load_dwordx4 v[120:123], v196, %[rs], s60 offen offset:3072 sc1\ns_add_u32 s60, s41, 917504\nbuffer_load_dwordx4 v[124:127], v196, %[rs], s60 offen offset:3072 sc1\ns_cmp_ge_u32 s40, 64\ns_cbranch_scc1 .Lrd_fin%=\n.Lrd_ychk%=:\ns_and_b32 s44, s40, 1\n" \
"s_bfe_u32 s55, s40, 0x10001\ns_mul_i32 s54, s44, 0x80008001\ns_mul_i32 s61, s55, 0x80008000\ns_lshl_b32 s58, s44, 17\ns_add_u32 s58, s58, %[st]\ns_add_u32 s44, s40, 1\ns_and_b32 s44, s44, 3\ns_lshl_b32 s44, s44, 15\n" \
"v_xor_b32 v206, s44, v200\nv_mov_b64 v[180:181], 0\nv_mov_b64 v[182:183], 0\nv_mov_b64 v[184:185], 0\nv_mov_b64 v[186:187], 0\nv_mov_b64 v[188:189], 0\nv_mov_b64 v[190:191], 0\nv_mov_b64 v[192:193], 0\n" \
"v_mov_b64 v[194:195], 0\ns_waitcnt vmcnt(6)\n.Lrd_sg0%=:\ns_memrealtime s[68:69]\nv_cmp_ne_u32 vcc, s40, v214\ns_cbranch_vccnz .Lrd_sg1%=\nv_max_u32_dpp v215, v215, v215 quad_perm:[1,0,3,2] row_mask:0xf bank_mask:0xf\ns_nop 1\n" \
"v_max_u32_dpp v215, v215, v215 quad_perm:[2,3,0,1] row_mask:0xf bank_mask:0xf\ns_nop 1\nv_max_u32_dpp v215, v215, v215 row_half_mirror row_mask:0xf bank_mask:0xf\ns_nop 0\nv_readlane_b32 s86, v215, 0\nv_readlane_b32 s87, v215, 8\ns_add_u32 s86, s86, %[du]\n.Lrd_tw622%=:\n" \
"s_waitcnt lgkmcnt(0)\ns_sub_u32 s44, s86, s68\ns_add_u32 s44, s44, 11\ns_cmp_lt_u32 s44, 250\ns_cbranch_scc1 .Lrd_tw622x%=\nbuffer_load_dwordx4 v[128:131], v196, %[rs], %[su] offen offset:0 sc1\nbuffer_load_dwordx4 v[132:135], v196, %[rs], %[su] offen offset:1024 sc1\ns_memrealtime s[68:69]\n" \
"buffer_load_dwordx4 v[136:139], v196, %[rs], %[su] offen offset:2048 sc1\nbuffer_load_dwordx4 v[140:143], v196, %[rs], %[su] offen offset:3072 sc1\n.Lrd_tw633%=:\ns_waitcnt lgkmcnt(0)\ns_sub_u32 s44, s86, s68\ns_add_u32 s44, s44, 21\ns_cmp_lt_u32 s44, 250\ns_cbranch_scc1 .Lrd_tw633x%=\n" \
"buffer_load_dwordx4 v[144:147], v196, %[rs], s57 offen offset:0 sc1\nbuffer_load_dwordx4 v[148:151], v196, %[rs], s57 offen offset:1024 sc1\ns_memrealtime s[68:69]\nbuffer_load_dwordx4 v[152:155], v196, %[rs], s57 offen offset:2048 sc1\nbuffer_load_dwordx4 v[156:159], v196, %[rs], s57 offen offset:3072 sc1\nbuffer_store_dword v207, off, %[rs], s59 sc1\ns_mov_b32 s59, -2147483648\n.Lrd_tw646%=:\n" \
"s_waitcnt lgkmcnt(0)\ns_sub_u32 s44, s87, s68\ns_add_u32 s44, s44, 24\ns_cmp_lt_u32 s44, 250\ns_cbranch_scc1 .Lrd_tw646x%=\nbuffer_load_dwordx4 v[160:163], v196, %[rs], %[sp] offen offset:0 sc1\nbuffer_load_dwordx4 v[164:167], v196, %[rs], %[sp] offen offset:1024 sc1\nbuffer_load_dwordx4 v[168:171], v196, %[rs], %[sp] offen offset:2048 sc1\n" \
"buffer_load_dwordx4 v[172:175], v196, %[rs], %[sp] offen offset:3072 sc1\nbuffer_load_dwordx4 v[176:179], v196, %[rs], s56 offen offset:0 sc1\nv_bitop3_b32 v209, v0, v2, s55 bitop3:0x7e\nv_bitop3_b32 v210, v32, v34, s55 bitop3:0x7e\nv_or3_b32 v208, v209, v210, 0\nv_bitop3_b32 v209, v64, v66, s55 bitop3:0x7e\nv_bitop3_b32 v210, v4, v6, s55 bitop3:0x7e\nv_or3_b32 v208, v209, v210, v208\n" \
"v_bitop3_b32 v209, v36, v38, s55 bitop3:0x7e\nv_bitop3_b32 v210, v68, v70, s55 bitop3:0x7e\nv_or3_b32 v208, v209, v210, v208\nv_bitop3_b32 v209, v8, v10, s55 bitop3:0x7e\nv_bitop3_b32 v210, v40, v42, s55 bitop3:0x7e\nv_or3_b32 v208, v209, v210, v208\nv_bitop3_b32 v209, v72, v74, s55 bitop3:0x7e\nv_bitop3_b32 v210, v12, v14, s55 bitop3:0x7e\n" \
"v_or3_b32 v208, v209, v210, v208\nv_bitop3_b32 v209, v44, v46, s55 bitop3:0x7e\nv_bitop3_b32 v210, v76, v78, s55 bitop3:0x7e\nv_or3_b32 v208, v209, v210, v208\nv_bitop3_b32 v209, v16, v18, s55 bitop3:0x7e\nv_bitop3_b32 v210, v48, v50, s55 bitop3:0x7e\nv_or3_b32 v208, v209, v210, v208\nv_bitop3_b32 v209, v80, v82, s55 bitop3:0x7e\n" \
"v_bitop3_b32 v210, v20, v22, s55 bitop3:0x7e\nv_or3_b32 v208, v209, v210, v208\nv_bitop3_b32 v209, v52, v54, s55 bitop3:0x7e\nv_bitop3_b32 v210, v84, v86, s55 bitop3:0x7e\nv_or3_b32 v208, v209, v210, v208\nv_bitop3_b32 v209, v24, v26, s55 bitop3:0x7e\nv_bitop3_b32 v210, v56, v58, s55 bitop3:0x7e\nv_or3_b32 v208, v209, v210, v208\n" \
"v_bitop3_b32 v209, v88, v90, s55 bitop3:0x7e\nv_bitop3_b32 v210, v28, v30, s55 bitop3:0x7e\nv_or3_b32 v208, v209, v210, v208\nv_bitop3_b32 v209, v60, v62, s55 bitop3:0x7e\nv_bitop3_b32 v210, v92, v94, s55 bitop3:0x7e\nv_or3_b32 v208, v209, v210, v208\ns_waitcnt vmcnt(18)\nv_bitop3_b32 v209, v96, v98, s55 bitop3:0x7e\n" \
"v_bitop3_b32 v210, v100, v102, s55 bitop3:0x7e\nv_or3_b32 v208, v209, v210, v208\nv_bitop3_b32 v209, v104, v106, s55 bitop3:0x7e\nv_bitop3_b32 v210, v108, v110, s55 bitop3:0x7e\nv_or3_b32 v208, v209, v210, v208\ns_waitcnt vmcnt(14)\nv_bitop3_b32 v209, v112, v114, s55 bitop3:0x7e\nv_bitop3_b32 v210, v116, v118, s55 bitop3:0x7e\n" \
"v_or3_b32 v208, v209, v210, v208\nv_bitop3_b32 v209, v120, v122, s55 bitop3:0x7e\nv_bitop3_b32 v210, v124, v126, s55 bitop3:0x7e\nv_or3_b32 v208, v209, v210, v208\nv_and_b32 v208, 1, v208\nv_cmp_ne_u32 vcc, 0, v208\ns_cbranch_vccnz .Lrd_yre%=\ns_branch .Lrd_ra%=\n" \
".Lrd_sg1%=:\nbuffer_load_dwordx2 v[214:215], v212, %[rs], 0 offen sc1\ns_waitcnt vmcnt(0)\nbuffer_store_dword v207, off, %[rs], s59 sc1\ns_mov_b32 s59, -2147483648\ns_add_u32 s43, s43, 1\ns_cmp_lt_u32 s43, 262144\ns_cbranch_scc1 .Lrd_sg0%=\n" \
"s_branch .Lrd_abort%=\n.Lrd_tw622x%=:\ns_memrealtime s[68:69]\ns_branch .Lrd_tw622%=\n.Lrd_tw633x%=:\ns_memrealtime s[68:69]\ns_branch .Lrd_tw633%=\n.Lrd_tw646x%=:\n" \
"s_memrealtime s[68:69]\ns_branch .Lrd_tw646%=\n.Lrd_fin%=:\ns_waitcnt vmcnt(0)\nbuffer_store_dword v207, off, %[rs], s59 sc1\ns_mov_b32 s59, -2147483648\ns_branch .Lrd_end%=\n.Lrd_yre%=:\n" \
"s_mov_b64 exec, vcc\ns_waitcnt vmcnt(0)\ns_add_u32 s43, s43, 1\ns_cmp_lt_u32 s43, 262144\ns_cbranch_scc1 .Lrd_yld%=\ns_branch .Lrd_abort%=\n.Lrd_rb%=:\ns_mov_b64 exec, s[46:47]\n" \
"s_cbranch_execz .Lrd_rs0%=\nbuffer_load_dwordx4 v[160:163], v196, %[rs], %[sp] offen offset:0 sc1\n.Lrd_rs0%=:\ns_mov_b64 exec, s[48:49]\ns_cbranch_execz .Lrd_rs1%=\nbuffer_load_dwordx4 v[164:167], v196, %[rs], %[sp] offen offset:1024 sc1\n.Lrd_rs1%=:\ns_mov_b64 exec, s[50:51]\n" \
"s_cbranch_execz .Lrd_rs2%=\nbuffer_load_dwordx4 v[168:171], v196, %[rs], %[sp] offen offset:2048 sc1\n.Lrd_rs2%=:\ns_mov_b64 exec, s[52:53]\ns_cbranch_execz .Lrd_rs3%=\nbuffer_load_dwordx4 v[172:175], v196, %[rs], %[sp] offen offset:3072 sc1\n.Lrd_rs3%=:\ns_mov_b64 exec, s[64:65]\n" \
"s_cbranch_execz .Lrd_rs4%=\nbuffer_load_dwordx4 v[176:179], v196, %[rs], s56 offen offset:0 sc1\n.Lrd_rs4%=:\ns_mov_b64 exec, s[86:87]\ns_cbranch_execz .Lrd_rs5%=\nbuffer_load_dwordx4 v[128:131], v196, %[rs], %[su] offen offset:0 sc1\nbuffer_load_dwordx4 v[136:139], v196, %[rs], %[su] offen offset:2048 sc1\n.Lrd_rs5%=:\n" \
"s_mov_b64 exec, s[88:89]\ns_cbranch_execz .Lrd_rs6%=\nbuffer_load_dwordx4 v[144:147], v196, %[rs], s57 offen offset:0 sc1\nbuffer_load_dwordx4 v[152:155], v196, %[rs], s57 offen offset:2048 sc1\n.Lrd_rs6%=:\ns_mov_b64 exec, s[68:69]\ns_cbranch_execz .Lrd_rs7%=\nbuffer_load_dwordx4 v[132:135], v196, %[rs], %[su] offen offset:1024 sc1\n" \
"buffer_load_dwordx4 v[140:143], v196, %[rs], %[su] offen offset:3072 sc1\n.Lrd_rs7%=:\ns_mov_b64 exec, s[62:63]\ns_cbranch_execz .Lrd_rs8%=\nbuffer_load_dwordx4 v[148:151], v196, %[rs], s57 offen offset:1024 sc1\nbuffer_load_dwordx4 v[156:159], v196, %[rs], s57 offen offset:3072 sc1\n.Lrd_rs8%=:\ns_mov_b64 exec, -1\n" \
"v_mov_b64 v[180:181], 0\nv_mov_b64 v[182:183], 0\nv_mov_b64 v[184:185], 0\nv_mov_b64 v[186:187], 0\nv_mov_b64 v[188:189], 0\nv_mov_b64 v[190:191], 0\nv_mov_b64 v[192:193], 0\nv_mov_b64 v[194:195], 0\n" \
"s_waitcnt vmcnt(0)\ns_add_u32 s43, s43, 1\ns_cmp_lt_u32 s43, 262144\ns_cbranch_scc1 .Lrd_ra%=\ns_branch .Lrd_abort%=\n.Lrd_y1%=:\nv_accvgpr_read_b32 v206, a32\nbuffer_load_dword v208, v206, %[rs], 0 offen sc1\n" \
"s_waitcnt vmcnt(0)\nv_cmp_gt_i32 vcc, 1, v208\ns_cbranch_vccz .Lrd_y1r%=\ns_add_u32 s43, s43, 1\ns_cmp_lt_u32 s43, 262144\ns_cbranch_scc1 .Lrd_y1%=\ns_branch .Lrd_abort%=\n.Lrd_tre0%=:\n" \
"s_add_u32 s60, s58, 16384\nbuffer_load_dwordx4 v[212:215], v196, %[rs], s60 offen offset:0 sc1\ns_waitcnt vmcnt(0)\nv_bitop3_b32 v209, v212, v214, s61 bitop3:0x7e\nv_cmp_gt_i32 vcc, 0, v209\ns_cbranch_vccz .Lrd_tl0%=\ns_add_u32 s43, s43, 1\ns_cmp_lt_u32 s43, 262144\n" \
"s_cbranch_scc1 .Lrd_tre0%=\ns_branch .Lrd_abort%=\n.Lrd_tre1%=:\ns_add_u32 s60, s58, 0\nbuffer_load_dwordx4 v[216:219], v196, %[rs], s60 offen offset:0 sc1\ns_waitcnt vmcnt(0)\nv_bitop3_b32 v209, v216, v218, s61 bitop3:0x7e\nv_cmp_gt_i32 vcc, 0, v209\n" \
"s_cbranch_vccz .Lrd_tl1%=\ns_add_u32 s43, s43, 1\ns_cmp_lt_u32 s43, 262144\ns_cbranch_scc1 .Lrd_tre1%=\ns_branch .Lrd_abort%=\n.Lrd_tre2%=:\ns_add_u32 s60, s58, 0\nbuffer_load_dwordx4 v[220:223], v196, %[rs], s60 offen offset:1024 sc1\n" \
"s_waitcnt vmcnt(0)\nv_bitop3_b32 v209, v220, v222, s61 bitop3:0x7e\nv_cmp_gt_i32 vcc, 0, v209\ns_cbranch_vccz .Lrd_tl2%=\ns_add_u32 s43, s43, 1\ns_cmp_lt_u32 s43, 262144\ns_cbranch_scc1 .Lrd_tre2%=\ns_branch .Lrd_abort%=\n" \
".Lrd_tre3%=:\ns_add_u32 s60, s58, 16384\nbuffer_load_dwordx4 v[224:227], v196, %[rs], s60 offen offset:1024 sc1\ns_waitcnt vmcnt(0)\nv_bitop3_b32 v209, v224, v226, s61 bitop3:0x7e\nv_cmp_gt_i32 vcc, 0, v209\ns_cbranch_vccz .Lrd_tl3%=\ns_add_u32 s43, s43, 1\n" \
"s_cmp_lt_u32 s43, 262144\ns_cbranch_scc1 .Lrd_tre3%=\ns_branch .Lrd_abort%=\n.Lrd_tre4%=:\ns_add_u32 s60, s58, 0\nbuffer_load_dwordx4 v[228:231], v196, %[rs], s60 offen offset:2048 sc1\ns_waitcnt vmcnt(0)\nv_bitop3_b32 v209, v228, v230, s61 bitop3:0x7e\n" \
"v_cmp_gt_i32 vcc, 0, v209\ns_cbranch_vccz .Lrd_tl4%=\ns_add_u32 s43, s43, 1\ns_cmp_lt_u32 s43, 262144\ns_cbranch_scc1 .Lrd_tre4%=\ns_branch .Lrd_abort%=\n.Lrd_tre5%=:\ns_add_u32 s60, s58, 0\n" \
"buffer_load_dwordx4 v[232:235], v196, %[rs], s60 offen offset:3072 sc1\ns_waitcnt vmcnt(0)\nv_bitop3_b32 v209, v232, v234, s61 bitop3:0x7e\nv_cmp_gt_i32 vcc, 0, v209\ns_cbranch_vccz .Lrd_tl5%=\ns_add_u32 s43, s43, 1\ns_cmp_lt_u32 s43, 262144\ns_cbranch_scc1 .Lrd_tre5%=\n" \
"s_branch .Lrd_abort%=\n.Lrd_tre6%=:\ns_add_u32 s60, s58, 16384\nbuffer_load_dwordx4 v[236:239], v196, %[rs], s60 offen offset:2048 sc1\ns_waitcnt vmcnt(0)\nv_bitop3_b32 v209, v236, v238, s61 bitop3:0x7e\nv_cmp_gt_i32 vcc, 0, v209\ns_cbranch_vccz .Lrd_tl6%=\n" \
"s_add_u32 s43, s43, 1\ns_cmp_lt_u32 s43, 262144\ns_cbranch_scc1 .Lrd_tre6%=\ns_branch .Lrd_abort%=\n.Lrd_tre7%=:\ns_add_u32 s60, s58, 4096\nbuffer_load_dwordx4 v[240:243], v196, %[rs], s60 offen offset:0 sc1\ns_waitcnt vmcnt(0)\n" \
"v_bitop3_b32 v209, v240, v242, s61 bitop3:0x7e\nv_cmp_gt_i32 vcc, 0, v209\ns_cbranch_vccz .Lrd_tl7%=\ns_add_u32 s43, s43, 1\ns_cmp_lt_u32 s43, 262144\ns_cbranch_scc1 .Lrd_tre7%=\ns_branch .Lrd_abort%=\n.Lrd_tre8%=:\n" \
"s_add_u32 s60, s58, 4096\nbuffer_load_dwordx4 v[244:247], v196, %[rs], s60 offen offset:1024 sc1\ns_waitcnt vmcnt(0)\nv_bitop3_b32 v209, v244, v246, s61 bitop3:0x7e\nv_cmp_gt_i32 vcc, 0, v209\ns_cbranch_vccz .Lrd_tl8%=\ns_add_u32 s43, s43, 1\ns_cmp_lt_u32 s43, 262144\n" \
"s_cbranch_scc1 .Lrd_tre8%=\ns_branch .Lrd_abort%=\n.Lrd_tre9%=:\ns_add_u32 s60, s58, 16384\nbuffer_load_dwordx4 v[248:251], v196, %[rs], s60 offen offset:3072 sc1\ns_waitcnt vmcnt(0)\nv_bitop3_b32 v209, v248, v250, s61 bitop3:0x7e\nv_cmp_gt_i32 vcc, 0, v209\n" \
"s_cbranch_vccz .Lrd_tl9%=\ns_add_u32 s43, s43, 1\ns_cmp_lt_u32 s43, 262144\ns_cbranch_scc1 .Lrd_tre9%=\ns_branch .Lrd_abort%=\n.Lrd_tre10%=:\ns_add_u32 s60, s58, 20480\nbuffer_load_dwordx4 v[252:255], v196, %[rs], s60 offen offset:0 sc1\n" \
"s_waitcnt vmcnt(0)\nv_bitop3_b32 v209, v252, v254, s61 bitop3:0x7e\nv_bitop3_b32 v210, v253, v255, s61 bitop3:0x7e\nv_or_b32 v209, v209, v210\nv_cmp_gt_i32 vcc, 0, v209\ns_cbranch_vccz .Lrd_tl10%=\ns_add_u32 s43, s43, 1\ns_cmp_lt_u32 s43, 262144\n" \
"s_cbranch_scc1 .Lrd_tre10%=\ns_branch .Lrd_abort%=\n.Lrd_tre11%=:\ns_add_u32 s60, s58, 20480\nbuffer_load_dwordx4 v[160:163], v196, %[rs], s60 offen offset:2048 sc1\ns_waitcnt vmcnt(0)\nv_bitop3_b32 v209, v160, v162, s61 bitop3:0x7e\nv_bitop3_b32 v210, v161, v163, s61 bitop3:0x7e\n" \
"v_or_b32 v209, v209, v210\nv_cmp_gt_i32 vcc, 0, v209\ns_cbranch_vccz .Lrd_tl11%=\ns_add_u32 s43, s43, 1\ns_cmp_lt_u32 s43, 262144\ns_cbranch_scc1 .Lrd_tre11%=\ns_branch .Lrd_abort%=\n.Lrd_tre12%=:\n" \
"s_add_u32 s60, s58, 4096\nbuffer_load_dwordx4 v[164:167], v196, %[rs], s60 offen offset:2048 sc1\ns_waitcnt vmcnt(0)\nv_bitop3_b32 v209, v164, v166, s61 bitop3:0x7e\nv_cmp_gt_i32 vcc, 0, v209\ns_cbranch_vccz .Lrd_tl12%=\ns_add_u32 s43, s43, 1\ns_cmp_lt_u32 s43, 262144\n" \
"s_cbranch_scc1 .Lrd_tre12%=\ns_branch .Lrd_abort%=\n.Lrd_tre13%=:\ns_add_u32 s60, s58, 4096\nbuffer_load_dwordx4 v[168:171], v196, %[rs], s60 offen offset:3072 sc1\ns_waitcnt vmcnt(0)\nv_bitop3_b32 v209, v168, v170, s61 bitop3:0x7e\nv_cmp_gt_i32 vcc, 0, v209\n" \
"s_cbranch_vccz .Lrd_tl13%=\ns_add_u32 s43, s43, 1\ns_cmp_lt_u32 s43, 262144\ns_cbranch_scc1 .Lrd_tre13%=\ns_branch .Lrd_abort%=\n.Lrd_tre14%=:\ns_add_u32 s60, s58, 20480\nbuffer_load_dwordx4 v[172:175], v196, %[rs], s60 offen offset:1024 sc1\n" \
"s_waitcnt vmcnt(0)\nv_bitop3_b32 v209, v172, v174, s61 bitop3:0x7e\nv_bitop3_b32 v210, v173, v175, s61 bitop3:0x7e\nv_or_b32 v209, v209, v210\nv_cmp_gt_i32 vcc, 0, v209\ns_cbranch_vccz .Lrd_tl14%=\ns_add_u32 s43, s43, 1\ns_cmp_lt_u32 s43, 262144\n" \
"s_cbranch_scc1 .Lrd_tre14%=\ns_branch .Lrd_abort%=\n.Lrd_tre15%=:\ns_add_u32 s60, s58, 20480\nbuffer_load_dwordx4 v[176:179], v196, %[rs], s60 offen offset:3072 sc1\ns_waitcnt vmcnt(0)\nv_bitop3_b32 v209, v176, v178, s61 bitop3:0x7e\nv_bitop3_b32 v210, v177, v179, s61 bitop3:0x7e\n" \
"v_or_b32 v209, v209, v210\nv_cmp_gt_i32 vcc, 0, v209\ns_cbranch_vccz .Lrd_tl15%=\ns_add_u32 s43, s43, 1\ns_cmp_lt_u32 s43, 262144\ns_cbranch_scc1 .Lrd_tre15%=\ns_branch .Lrd_abort%=\n.Lrd_abort%=:\n" \
"s_mov_b64 exec, -1\nv_mov_b32 v16, 0\ns_movk_i32 s44, 6400\nbuffer_store_dword v16, off, %[rs], s44 sc1\n.Lrd_end%=:\ns_waitcnt vmcnt(0) lgkmcnt(0)\n" \
""
#define GG_ASM \
"v_mbcnt_lo_u32_b32 v255, -1, 0\nv_mbcnt_hi_u32_b32 v255, -1, v255\nv_lshlrev_b32 v255, 2, v255\nv_add_u32 v255, %[tb], v255\nds_read_b32 v0, v255 offset:0\nds_read_b32 v1, v255 offset:256\nds_read_b32 v2, v255 offset:512\nds_read_b32 v3, v255 offset:768\n" \
"ds_read_b32 v4, v255 offset:1024\nds_read_b32 v5, v255 offset:1280\nds_read_b32 v92, v255 offset:1536\nds_read_b32 v93, v255 offset:1792\nds_read_b32 v94, v255 offset:2048\ns_waitcnt lgkmcnt(0)\nv_mov_b32 v6, 0x3f803f80\nv_mov_b32 v7, 0x3f803f80\n" \
"s_add_u32 m0, %[lw], 16384\ns_add_u32 s44, %[wo], 0\nbuffer_load_dwordx4 v92, %[rw], s44 offen nt lds\ns_add_u32 m0, %[lw], 17408\ns_add_u32 s44, %[wo], 57344\nbuffer_load_dwordx4 v92, %[rw], s44 offen nt lds\ns_add_u32 m0, %[lw], 18432\ns_add_u32 s44, %[wo], 114688\n" \
"buffer_load_dwordx4 v92, %[rw], s44 offen nt lds\ns_add_u32 m0, %[lw], 19456\ns_add_u32 s44, %[wo], 172032\nbuffer_load_dwordx4 v92, %[rw], s44 offen nt lds\ns_add_u32 m0, %[lw], 20480\ns_add_u32 s44, %[wo], 229376\nbuffer_load_dwordx4 v92, %[rw], s44 offen nt lds\ns_add_u32 m0, %[lw], 21504\n" \
"s_add_u32 s44, %[wo], 286720\nbuffer_load_dwordx4 v92, %[rw], s44 offen nt lds\ns_add_u32 m0, %[lw], 22528\ns_add_u32 s44, %[wo], 344064\nbuffer_load_dwordx4 v92, %[rw], s44 offen nt lds\ns_add_u32 m0, %[lw], 23552\ns_add_u32 s44, %[wo], 401408\nbuffer_load_dwordx4 v92, %[rw], s44 offen nt lds\n" \
"s_add_u32 m0, %[tb], 0\ns_add_u32 s44, %[wo], 1835008\nbuffer_load_dwordx4 v92, %[rw], s44 offen nt lds\ns_add_u32 m0, %[tb], 1024\ns_add_u32 s44, %[wo], 1892352\nbuffer_load_dwordx4 v92, %[rw], s44 offen nt lds\ns_add_u32 m0, %[tb], 2048\ns_add_u32 s44, %[wo], 1949696\n" \
"buffer_load_dwordx4 v92, %[rw], s44 offen nt lds\ns_add_u32 m0, %[tb], 3072\ns_add_u32 s44, %[wo], 2007040\nbuffer_load_dwordx4 v92, %[rw], s44 offen nt lds\ns_add_u32 m0, %[tb], 4096\ns_add_u32 s44, %[wo], 2064384\nbuffer_load_dwordx4 v92, %[rw], s44 offen nt lds\ns_add_u32 m0, %[tb], 5120\n" \
"s_add_u32 s44, %[wo], 2121728\nbuffer_load_dwordx4 v92, %[rw], s44 offen nt lds\ns_add_u32 m0, %[tb], 6144\ns_add_u32 s44, %[wo], 2179072\nbuffer_load_dwordx4 v92, %[rw], s44 offen nt lds\ns_add_u32 m0, %[tb], 7168\ns_add_u32 s44, %[wo], 2236416\nbuffer_load_dwordx4 v92, %[rw], s44 offen nt lds\n" \
"s_add_u32 m0, %[tb], 8192\ns_add_u32 s44, %[wo], 3670016\nbuffer_load_dwordx4 v92, %[rw], s44 offen nt lds\ns_add_u32 m0, %[tb], 9216\ns_add_u32 s44, %[wo], 3727360\nbuffer_load_dwordx4 v92, %[rw], s44 offen nt lds\ns_add_u32 m0, %[tb], 10240\ns_add_u32 s44, %[wo], 3784704\n" \
"buffer_load_dwordx4 v92, %[rw], s44 offen nt lds\ns_add_u32 m0, %[tb], 11264\ns_add_u32 s44, %[wo], 3842048\nbuffer_load_dwordx4 v92, %[rw], s44 offen nt lds\ns_add_u32 m0, %[lw], 0\ns_add_u32 s44, %[wo], 3899392\nbuffer_load_dwordx4 v92, %[rw], s44 offen nt lds\ns_add_u32 m0, %[lw], 1024\n" \
"s_add_u32 s44, %[wo], 3956736\nbuffer_load_dwordx4 v92, %[rw], s44 offen nt lds\ns_add_u32 m0, %[lw], 2048\ns_add_u32 s44, %[wo], 4014080\nbuffer_load_dwordx4 v92, %[rw], s44 offen nt lds\ns_add_u32 m0, %[lw], 3072\ns_add_u32 s44, %[wo], 4071424\nbuffer_load_dwordx4 v92, %[rw], s44 offen nt lds\n" \
"s_add_u32 m0, %[lw], 4096\ns_add_u32 s44, %[wo], 5505024\nbuffer_load_dwordx4 v92, %[rw], s44 offen nt lds\ns_add_u32 m0, %[lw], 5120\ns_add_u32 s44, %[wo], 5562368\nbuffer_load_dwordx4 v92, %[rw], s44 offen nt lds\ns_add_u32 m0, %[lw], 6144\ns_add_u32 s44, %[wo], 5619712\n" \
"buffer_load_dwordx4 v92, %[rw], s44 offen nt lds\ns_add_u32 m0, %[lw], 7168\ns_add_u32 s44, %[wo], 5677056\nbuffer_load_dwordx4 v92, %[rw], s44 offen nt lds\ns_add_u32 m0, %[lw], 8192\ns_add_u32 s44, %[wo], 5734400\nbuffer_load_dwordx4 v92, %[rw], s44 offen nt lds\ns_add_u32 m0, %[lw], 9216\n" \
"s_add_u32 s44, %[wo], 5791744\nbuffer_load_dwordx4 v92, %[rw], s44 offen nt lds\ns_add_u32 m0, %[lw], 10240\ns_add_u32 s44, %[wo], 5849088\nbuffer_load_dwordx4 v92, %[rw], s44 offen nt lds\ns_add_u32 m0, %[lw], 11264\ns_add_u32 s44, %[wo], 5906432\nbuffer_load_dwordx4 v92, %[rw], s44 offen nt lds\n" \
"s_waitcnt vmcnt(28)\ns_barrier\ns_add_u32 m0, %[lw], 12288\ns_add_u32 s44, %[wo], 458752\nbuffer_load_dwordx4 v92, %[rw], s44 offen nt lds\ns_add_u32 m0, %[lw], 13312\ns_add_u32 s44, %[wo], 516096\nbuffer_load_dwordx4 v92, %[rw], s44 offen nt lds\n" \
"s_add_u32 m0, %[lw], 14336\ns_add_u32 s44, %[wo], 573440\nbuffer_load_dwordx4 v92, %[rw], s44 offen nt lds\ns_add_u32 m0, %[lw], 15360\ns_add_u32 s44, %[wo], 630784\nbuffer_load_dwordx4 v92, %[rw], s44 offen nt lds\nds_read_b64_tr_b16 a[0:1], v94 offset:16384\nds_read_b64_tr_b16 a[2:3], v94 offset:16896\n" \
"ds_read_b64_tr_b16 a[8:9], v94 offset:16392\nds_read_b64_tr_b16 a[10:11], v94 offset:16904\nds_read_b64_tr_b16 a[16:17], v94 offset:40960\nds_read_b64_tr_b16 a[18:19], v94 offset:41472\nds_read_b64_tr_b16 a[24:25], v94 offset:40968\nds_read_b64_tr_b16 a[26:27], v94 offset:41480\ns_waitcnt lgkmcnt(0)\nv_mfma_f32_16x16x16_bf16 v[8:11], a[0:1], v[6:7], 0\n" \
"v_mfma_f32_16x16x16_bf16 v[12:15], a[8:9], v[6:7], 0\nv_mfma_f32_16x16x16_bf16 v[16:19], a[16:17], v[6:7], 0\nv_mfma_f32_16x16x16_bf16 v[20:23], a[24:25], v[6:7], 0\nv_mfma_f32_16x16x16_bf16 v[8:11], a[2:3], v[6:7], v[8:11]\nv_mfma_f32_16x16x16_bf16 v[12:15], a[10:11], v[6:7], v[12:15]\nv_mfma_f32_16x16x16_bf16 v[16:19], a[18:19], v[6:7], v[16:19]\nv_mfma_f32_16x16x16_bf16 v[20:23], a[26:27], v[6:7], v[20:23]\ns_waitcnt vmcnt(28)\n" \
"s_barrier\ns_add_u32 m0, %[lw], 16384\ns_add_u32 s44, %[wo], 688128\nbuffer_load_dwordx4 v92, %[rw], s44 offen nt lds\ns_add_u32 m0, %[lw], 17408\ns_add_u32 s44, %[wo], 745472\nbuffer_load_dwordx4 v92, %[rw], s44 offen nt lds\ns_add_u32 m0, %[lw], 18432\n" \
"s_add_u32 s44, %[wo], 802816\nbuffer_load_dwordx4 v92, %[rw], s44 offen nt lds\ns_add_u32 m0, %[lw], 19456\ns_add_u32 s44, %[wo], 860160\nbuffer_load_dwordx4 v92, %[rw], s44 offen nt lds\nds_read_b64_tr_b16 a[4:5], v94 offset:20480\nds_read_b64_tr_b16 a[6:7], v94 offset:20992\nds_read_b64_tr_b16 a[12:13], v94 offset:20488\n" \
"ds_read_b64_tr_b16 a[14:15], v94 offset:21000\nds_read_b64_tr_b16 a[20:21], v94 offset:45056\nds_read_b64_tr_b16 a[22:23], v94 offset:45568\nds_read_b64_tr_b16 a[28:29], v94 offset:45064\nds_read_b64_tr_b16 a[30:31], v94 offset:45576\ns_waitcnt lgkmcnt(0)\nv_mfma_f32_16x16x16_bf16 v[8:11], a[4:5], v[6:7], v[8:11]\nv_mfma_f32_16x16x16_bf16 v[12:15], a[12:13], v[6:7], v[12:15]\n" \
"v_mfma_f32_16x16x16_bf16 v[16:19], a[20:21], v[6:7], v[16:19]\nv_mfma_f32_16x16x16_bf16 v[20:23], a[28:29], v[6:7], v[20:23]\nv_mfma_f32_16x16x16_bf16 v[8:11], a[6:7], v[6:7], v[8:11]\nv_mfma_f32_16x16x16_bf16 v[12:15], a[14:15], v[6:7], v[12:15]\nv_mfma_f32_16x16x16_bf16 v[16:19], a[22:23], v[6:7], v[16:19]\nv_mfma_f32_16x16x16_bf16 v[20:23], a[30:31], v[6:7], v[20:23]\ns_waitcnt vmcnt(28)\ns_barrier\n" \
"s_add_u32 m0, %[lw], 20480\ns_add_u32 s44, %[wo], 917504\nbuffer_load_dwordx4 v92, %[rw], s44 offen nt lds\ns_add_u32 m0, %[lw], 21504\ns_add_u32 s44, %[wo], 974848\nbuffer_load_dwordx4 v92, %[rw], s44 offen nt lds\ns_add_u32 m0, %[lw], 22528\ns_add_u32 s44, %[wo], 1032192\n" \
"buffer_load_dwordx4 v92, %[rw], s44 offen nt lds\ns_add_u32 m0, %[lw], 23552\ns_add_u32 s44, %[wo], 1089536\nbuffer_load_dwordx4 v92, %[rw], s44 offen nt lds\nds_read_b64_tr_b16 a[104:105], v93 offset:0\nds_read_b64_tr_b16 a[106:107], v93 offset:512\nds_read_b64_tr_b16 a[112:113], v93 offset:8\nds_read_b64_tr_b16 a[114:115], v93 offset:520\n" \
"ds_read_b64_tr_b16 a[120:121], v93 offset:12288\nds_read_b64_tr_b16 a[122:123], v93 offset:12800\nds_read_b64_tr_b16 a[128:129], v93 offset:12296\nds_read_b64_tr_b16 a[130:131], v93 offset:12808\ns_waitcnt lgkmcnt(0)\nv_mfma_f32_16x16x16_bf16 v[8:11], a[104:105], v[6:7], v[8:11]\nv_mfma_f32_16x16x16_bf16 v[12:15], a[112:113], v[6:7], v[12:15]\nv_mfma_f32_16x16x16_bf16 v[16:19], a[120:121], v[6:7], v[16:19]\n" \
"v_mfma_f32_16x16x16_bf16 v[20:23], a[128:129], v[6:7], v[20:23]\nv_mfma_f32_16x16x16_bf16 v[8:11], a[106:107], v[6:7], v[8:11]\nv_mfma_f32_16x16x16_bf16 v[12:15], a[114:115], v[6:7], v[12:15]\nv_mfma_f32_16x16x16_bf16 v[16:19], a[122:123], v[6:7], v[16:19]\nv_mfma_f32_16x16x16_bf16 v[20:23], a[130:131], v[6:7], v[20:23]\ns_waitcnt vmcnt(28)\ns_barrier\ns_add_u32 m0, %[tb], 0\n" \
"s_add_u32 s44, %[wo], 1146880\nbuffer_load_dwordx4 v92, %[rw], s44 offen nt lds\ns_add_u32 m0, %[tb], 1024\ns_add_u32 s44, %[wo], 1204224\nbuffer_load_dwordx4 v92, %[rw], s44 offen nt lds\ns_add_u32 m0, %[tb], 2048\ns_add_u32 s44, %[wo], 1261568\nbuffer_load_dwordx4 v92, %[rw], s44 offen nt lds\n" \
"s_add_u32 m0, %[tb], 3072\ns_add_u32 s44, %[wo], 1318912\nbuffer_load_dwordx4 v92, %[rw], s44 offen nt lds\nds_read_b64_tr_b16 a[108:109], v93 offset:4096\nds_read_b64_tr_b16 a[110:111], v93 offset:4608\nds_read_b64_tr_b16 a[116:117], v93 offset:4104\nds_read_b64_tr_b16 a[118:119], v93 offset:4616\nds_read_b64_tr_b16 a[124:125], v93 offset:16384\n" \
"ds_read_b64_tr_b16 a[126:127], v93 offset:16896\nds_read_b64_tr_b16 a[132:133], v93 offset:16392\nds_read_b64_tr_b16 a[134:135], v93 offset:16904\ns_waitcnt lgkmcnt(0)\nv_mfma_f32_16x16x16_bf16 v[8:11], a[108:109], v[6:7], v[8:11]\nv_mfma_f32_16x16x16_bf16 v[12:15], a[116:117], v[6:7], v[12:15]\nv_mfma_f32_16x16x16_bf16 v[16:19], a[124:125], v[6:7], v[16:19]\nv_mfma_f32_16x16x16_bf16 v[20:23], a[132:133], v[6:7], v[20:23]\n" \
"v_mfma_f32_16x16x16_bf16 v[8:11], a[110:111], v[6:7], v[8:11]\nv_mfma_f32_16x16x16_bf16 v[12:15], a[118:119], v[6:7], v[12:15]\nv_mfma_f32_16x16x16_bf16 v[16:19], a[126:127], v[6:7], v[16:19]\nv_mfma_f32_16x16x16_bf16 v[20:23], a[134:135], v[6:7], v[20:23]\ns_waitcnt vmcnt(28)\ns_barrier\ns_add_u32 m0, %[tb], 4096\ns_add_u32 s44, %[wo], 1376256\n" \
"buffer_load_dwordx4 v92, %[rw], s44 offen nt lds\ns_add_u32 m0, %[tb], 5120\ns_add_u32 s44, %[wo], 1433600\nbuffer_load_dwordx4 v92, %[rw], s44 offen nt lds\ns_add_u32 m0, %[tb], 6144\ns_add_u32 s44, %[wo], 1490944\nbuffer_load_dwordx4 v92, %[rw], s44 offen nt lds\ns_add_u32 m0, %[tb], 7168\n" \
"s_add_u32 s44, %[wo], 1548288\nbuffer_load_dwordx4 v92, %[rw], s44 offen nt lds\nds_read_b64_tr_b16 a[208:209], v93 offset:8192\nds_read_b64_tr_b16 a[210:211], v93 offset:8704\nds_read_b64_tr_b16 a[216:217], v93 offset:8200\nds_read_b64_tr_b16 a[218:219], v93 offset:8712\nds_read_b64_tr_b16 a[224:225], v93 offset:20480\nds_read_b64_tr_b16 a[226:227], v93 offset:20992\n" \
"ds_read_b64_tr_b16 a[232:233], v93 offset:20488\nds_read_b64_tr_b16 a[234:235], v93 offset:21000\ns_waitcnt lgkmcnt(0)\nv_mfma_f32_16x16x16_bf16 v[8:11], a[208:209], v[6:7], v[8:11]\nv_mfma_f32_16x16x16_bf16 v[12:15], a[216:217], v[6:7], v[12:15]\nv_mfma_f32_16x16x16_bf16 v[16:19], a[224:225], v[6:7], v[16:19]\nv_mfma_f32_16x16x16_bf16 v[20:23], a[232:233], v[6:7], v[20:23]\nv_mfma_f32_16x16x16_bf16 v[8:11], a[210:211], v[6:7], v[8:11]\n" \
"v_mfma_f32_16x16x16_bf16 v[12:15], a[218:219], v[6:7], v[12:15]\nv_mfma_f32_16x16x16_bf16 v[16:19], a[226:227], v[6:7], v[16:19]\nv_mfma_f32_16x16x16_bf16 v[20:23], a[234:235], v[6:7], v[20:23]\ns_waitcnt vmcnt(28)\ns_barrier\ns_add_u32 m0, %[tb], 8192\ns_add_u32 s44, %[wo], 1605632\nbuffer_load_dwordx4 v92, %[rw], s44 offen nt lds\n" \
"s_add_u32 m0, %[tb], 9216\ns_add_u32 s44, %[wo], 1662976\nbuffer_load_dwordx4 v92, %[rw], s44 offen nt lds\ns_add_u32 m0, %[tb], 10240\ns_add_u32 s44, %[wo], 1720320\nbuffer_load_dwordx4 v92, %[rw], s44 offen nt lds\ns_add_u32 m0, %[tb], 11264\ns_add_u32 s44, %[wo], 1777664\n" \
"buffer_load_dwordx4 v92, %[rw], s44 offen nt lds\nds_read_b64_tr_b16 a[212:213], v94 offset:0\nds_read_b64_tr_b16 a[214:215], v94 offset:512\nds_read_b64_tr_b16 a[220:221], v94 offset:8\nds_read_b64_tr_b16 a[222:223], v94 offset:520\nds_read_b64_tr_b16 a[228:229], v94 offset:24576\nds_read_b64_tr_b16 a[230:231], v94 offset:25088\nds_read_b64_tr_b16 a[236:237], v94 offset:24584\n" \
"ds_read_b64_tr_b16 a[238:239], v94 offset:25096\ns_waitcnt lgkmcnt(0)\nv_mfma_f32_16x16x16_bf16 v[8:11], a[212:213], v[6:7], v[8:11]\nv_mfma_f32_16x16x16_bf16 v[12:15], a[220:221], v[6:7], v[12:15]\nv_mfma_f32_16x16x16_bf16 v[16:19], a[228:229], v[6:7], v[16:19]\nv_mfma_f32_16x16x16_bf16 v[20:23], a[236:237], v[6:7], v[20:23]\nv_mfma_f32_16x16x16_bf16 v[8:11], a[214:215], v[6:7], v[8:11]\nv_mfma_f32_16x16x16_bf16 v[12:15], a[222:223], v[6:7], v[12:15]\n" \
"v_mfma_f32_16x16x16_bf16 v[16:19], a[230:231], v[6:7], v[16:19]\nv_mfma_f32_16x16x16_bf16 v[20:23], a[238:239], v[6:7], v[20:23]\ns_waitcnt vmcnt(28)\ns_barrier\ns_add_u32 m0, %[lw], 0\ns_add_u32 s44, %[wo], 2293760\nbuffer_load_dwordx4 v92, %[rw], s44 offen nt lds\ns_add_u32 m0, %[lw], 1024\n" \
"s_add_u32 s44, %[wo], 2351104\nbuffer_load_dwordx4 v92, %[rw], s44 offen nt lds\ns_add_u32 m0, %[lw], 2048\ns_add_u32 s44, %[wo], 2408448\nbuffer_load_dwordx4 v92, %[rw], s44 offen nt lds\ns_add_u32 m0, %[lw], 3072\ns_add_u32 s44, %[wo], 2465792\nbuffer_load_dwordx4 v92, %[rw], s44 offen nt lds\n" \
"ds_read_b64_tr_b16 v[152:153], v94 offset:4096\nds_read_b64_tr_b16 v[154:155], v94 offset:4608\nds_read_b64_tr_b16 v[160:161], v94 offset:4104\nds_read_b64_tr_b16 v[162:163], v94 offset:4616\nds_read_b64_tr_b16 v[168:169], v94 offset:28672\nds_read_b64_tr_b16 v[170:171], v94 offset:29184\nds_read_b64_tr_b16 v[176:177], v94 offset:28680\nds_read_b64_tr_b16 v[178:179], v94 offset:29192\n" \
"s_waitcnt lgkmcnt(0)\nv_mfma_f32_16x16x16_bf16 v[8:11], v[152:153], v[6:7], v[8:11]\nv_mfma_f32_16x16x16_bf16 v[12:15], v[160:161], v[6:7], v[12:15]\nv_mfma_f32_16x16x16_bf16 v[16:19], v[168:169], v[6:7], v[16:19]\nv_mfma_f32_16x16x16_bf16 v[20:23], v[176:177], v[6:7], v[20:23]\nv_mfma_f32_16x16x16_bf16 v[8:11], v[154:155], v[6:7], v[8:11]\nv_mfma_f32_16x16x16_bf16 v[12:15], v[162:163], v[6:7], v[12:15]\nv_mfma_f32_16x16x16_bf16 v[16:19], v[170:171], v[6:7], v[16:19]\n" \
"v_mfma_f32_16x16x16_bf16 v[20:23], v[178:179], v[6:7], v[20:23]\ns_waitcnt vmcnt(28)\ns_barrier\ns_add_u32 m0, %[lw], 4096\ns_add_u32 s44, %[wo], 2523136\nbuffer_load_dwordx4 v92, %[rw], s44 offen nt lds\ns_add_u32 m0, %[lw], 5120\ns_add_u32 s44, %[wo], 2580480\n" \
"buffer_load_dwordx4 v92, %[rw], s44 offen nt lds\ns_add_u32 m0, %[lw], 6144\ns_add_u32 s44, %[wo], 2637824\nbuffer_load_dwordx4 v92, %[rw], s44 offen nt lds\ns_add_u32 m0, %[lw], 7168\ns_add_u32 s44, %[wo], 2695168\nbuffer_load_dwordx4 v92, %[rw], s44 offen nt lds\nds_read_b64_tr_b16 v[156:157], v94 offset:8192\n" \
"ds_read_b64_tr_b16 v[158:159], v94 offset:8704\nds_read_b64_tr_b16 v[164:165], v94 offset:8200\nds_read_b64_tr_b16 v[166:167], v94 offset:8712\nds_read_b64_tr_b16 v[172:173], v94 offset:32768\nds_read_b64_tr_b16 v[174:175], v94 offset:33280\nds_read_b64_tr_b16 v[180:181], v94 offset:32776\nds_read_b64_tr_b16 v[182:183], v94 offset:33288\ns_waitcnt lgkmcnt(0)\n" \
"v_mfma_f32_16x16x16_bf16 v[8:11], v[156:157], v[6:7], v[8:11]\nv_mfma_f32_16x16x16_bf16 v[12:15], v[164:165], v[6:7], v[12:15]\nv_mfma_f32_16x16x16_bf16 v[16:19], v[172:173], v[6:7], v[16:19]\nv_mfma_f32_16x16x16_bf16 v[20:23], v[180:181], v[6:7], v[20:23]\nv_mfma_f32_16x16x16_bf16 v[8:11], v[158:159], v[6:7], v[8:11]\nv_mfma_f32_16x16x16_bf16 v[12:15], v[166:167], v[6:7], v[12:15]\nv_mfma_f32_16x16x16_bf16 v[16:19], v[174:175], v[6:7], v[16:19]\nv_mfma_f32_16x16x16_bf16 v[20:23], v[182:183], v[6:7], v[20:23]\n" \
"s_waitcnt vmcnt(28)\ns_barrier\ns_add_u32 m0, %[lw], 8192\ns_add_u32 s44, %[wo], 2752512\nbuffer_load_dwordx4 v92, %[rw], s44 offen nt lds\ns_add_u32 m0, %[lw], 9216\ns_add_u32 s44, %[wo], 2809856\nbuffer_load_dwordx4 v92, %[rw], s44 offen nt lds\n" \
"s_add_u32 m0, %[lw], 10240\ns_add_u32 s44, %[wo], 2867200\nbuffer_load_dwordx4 v92, %[rw], s44 offen nt lds\ns_add_u32 m0, %[lw], 11264\ns_add_u32 s44, %[wo], 2924544\nbuffer_load_dwordx4 v92, %[rw], s44 offen nt lds\nds_read_b64_tr_b16 a[32:33], v94 offset:12288\nds_read_b64_tr_b16 a[34:35], v94 offset:12800\n" \
"ds_read_b64_tr_b16 v[88:89], v94 offset:12296\nds_read_b64_tr_b16 v[90:91], v94 offset:12808\nds_read_b64_tr_b16 a[40:41], v94 offset:36864\nds_read_b64_tr_b16 a[42:43], v94 offset:37376\nds_read_b64_tr_b16 a[48:49], v94 offset:36872\nds_read_b64_tr_b16 a[50:51], v94 offset:37384\ns_waitcnt lgkmcnt(0)\nv_mfma_f32_16x16x16_bf16 v[8:11], a[32:33], v[6:7], v[8:11]\n" \
"v_mfma_f32_16x16x16_bf16 v[12:15], v[88:89], v[6:7], v[12:15]\nv_mfma_f32_16x16x16_bf16 v[16:19], a[40:41], v[6:7], v[16:19]\nv_mfma_f32_16x16x16_bf16 v[20:23], a[48:49], v[6:7], v[20:23]\nv_mfma_f32_16x16x16_bf16 v[8:11], a[34:35], v[6:7], v[8:11]\nv_mfma_f32_16x16x16_bf16 v[12:15], v[90:91], v[6:7], v[12:15]\nv_mfma_f32_16x16x16_bf16 v[16:19], a[42:43], v[6:7], v[16:19]\nv_mfma_f32_16x16x16_bf16 v[20:23], a[50:51], v[6:7], v[20:23]\ns_waitcnt vmcnt(28)\n" \
"s_barrier\ns_add_u32 m0, %[lw], 12288\ns_add_u32 s44, %[wo], 2981888\nbuffer_load_dwordx4 v92, %[rw], s44 offen nt lds\ns_add_u32 m0, %[lw], 13312\ns_add_u32 s44, %[wo], 3039232\nbuffer_load_dwordx4 v92, %[rw], s44 offen nt lds\ns_add_u32 m0, %[lw], 14336\n" \
"s_add_u32 s44, %[wo], 3096576\nbuffer_load_dwordx4 v92, %[rw], s44 offen nt lds\ns_add_u32 m0, %[lw], 15360\ns_add_u32 s44, %[wo], 3153920\nbuffer_load_dwordx4 v92, %[rw], s44 offen nt lds\nds_read_b64_tr_b16 a[36:37], v94 offset:16384\nds_read_b64_tr_b16 a[38:39], v94 offset:16896\nds_read_b64_tr_b16 v[84:85], v94 offset:16392\n" \
"ds_read_b64_tr_b16 v[86:87], v94 offset:16904\nds_read_b64_tr_b16 a[44:45], v94 offset:40960\nds_read_b64_tr_b16 a[46:47], v94 offset:41472\nds_read_b64_tr_b16 a[52:53], v94 offset:40968\nds_read_b64_tr_b16 a[54:55], v94 offset:41480\ns_waitcnt lgkmcnt(0)\nv_mfma_f32_16x16x16_bf16 v[8:11], a[36:37], v[6:7], v[8:11]\nv_mfma_f32_16x16x16_bf16 v[12:15], v[84:85], v[6:7], v[12:15]\n" \
"v_mfma_f32_16x16x16_bf16 v[16:19], a[44:45], v[6:7], v[16:19]\nv_mfma_f32_16x16x16_bf16 v[20:23], a[52:53], v[6:7], v[20:23]\nv_mfma_f32_16x16x16_bf16 v[8:11], a[38:39], v[6:7], v[8:11]\nv_mfma_f32_16x16x16_bf16 v[12:15], v[86:87], v[6:7], v[12:15]\nv_mfma_f32_16x16x16_bf16 v[16:19], a[46:47], v[6:7], v[16:19]\nv_mfma_f32_16x16x16_bf16 v[20:23], a[54:55], v[6:7], v[20:23]\ns_waitcnt vmcnt(28)\ns_barrier\n" \
"s_add_u32 m0, %[lw], 16384\ns_add_u32 s44, %[wo], 3211264\nbuffer_load_dwordx4 v92, %[rw], s44 offen nt lds\ns_add_u32 m0, %[lw], 17408\ns_add_u32 s44, %[wo], 3268608\nbuffer_load_dwordx4 v92, %[rw], s44 offen nt lds\ns_add_u32 m0, %[lw], 18432\ns_add_u32 s44, %[wo], 3325952\n" \
"buffer_load_dwordx4 v92, %[rw], s44 offen nt lds\ns_add_u32 m0, %[lw], 19456\ns_add_u32 s44, %[wo], 3383296\nbuffer_load_dwordx4 v92, %[rw], s44 offen nt lds\nds_read_b64_tr_b16 a[56:57], v94 offset:20480\nds_read_b64_tr_b16 a[58:59], v94 offset:20992\nds_read_b64_tr_b16 a[64:65], v94 offset:20488\nds_read_b64_tr_b16 a[66:67], v94 offset:21000\n" \
"ds_read_b64_tr_b16 v[80:81], v94 offset:45056\nds_read_b64_tr_b16 v[82:83], v94 offset:45568\nds_read_b64_tr_b16 a[72:73], v94 offset:45064\nds_read_b64_tr_b16 a[74:75], v94 offset:45576\ns_waitcnt lgkmcnt(0)\nv_mfma_f32_16x16x16_bf16 v[8:11], a[56:57], v[6:7], v[8:11]\nv_mfma_f32_16x16x16_bf16 v[12:15], a[64:65], v[6:7], v[12:15]\nv_mfma_f32_16x16x16_bf16 v[16:19], v[80:81], v[6:7], v[16:19]\n" \
"v_mfma_f32_16x16x16_bf16 v[20:23], a[72:73], v[6:7], v[20:23]\nv_mfma_f32_16x16x16_bf16 v[8:11], a[58:59], v[6:7], v[8:11]\nv_mfma_f32_16x16x16_bf16 v[12:15], a[66:67], v[6:7], v[12:15]\nv_mfma_f32_16x16x16_bf16 v[16:19], v[82:83], v[6:7], v[16:19]\nv_mfma_f32_16x16x16_bf16 v[20:23], a[74:75], v[6:7], v[20:23]\ns_waitcnt vmcnt(28)\ns_barrier\ns_add_u32 m0, %[lw], 20480\n" \
"s_add_u32 s44, %[wo], 3440640\nbuffer_load_dwordx4 v92, %[rw], s44 offen nt lds\ns_add_u32 m0, %[lw], 21504\ns_add_u32 s44, %[wo], 3497984\nbuffer_load_dwordx4 v92, %[rw], s44 offen nt lds\ns_add_u32 m0, %[lw], 22528\ns_add_u32 s44, %[wo], 3555328\nbuffer_load_dwordx4 v92, %[rw], s44 offen nt lds\n" \
"s_add_u32 m0, %[lw], 23552\ns_add_u32 s44, %[wo], 3612672\nbuffer_load_dwordx4 v92, %[rw], s44 offen nt lds\nds_read_b64_tr_b16 a[60:61], v93 offset:0\nds_read_b64_tr_b16 a[62:63], v93 offset:512\nds_read_b64_tr_b16 a[68:69], v93 offset:8\nds_read_b64_tr_b16 a[70:71], v93 offset:520\nds_read_b64_tr_b16 v[76:77], v93 offset:12288\n" \
"ds_read_b64_tr_b16 v[78:79], v93 offset:12800\nds_read_b64_tr_b16 a[76:77], v93 offset:12296\nds_read_b64_tr_b16 a[78:79], v93 offset:12808\ns_waitcnt lgkmcnt(0)\nv_mfma_f32_16x16x16_bf16 v[8:11], a[60:61], v[6:7], v[8:11]\nv_mfma_f32_16x16x16_bf16 v[12:15], a[68:69], v[6:7], v[12:15]\nv_mfma_f32_16x16x16_bf16 v[16:19], v[76:77], v[6:7], v[16:19]\nv_mfma_f32_16x16x16_bf16 v[20:23], a[76:77], v[6:7], v[20:23]\n" \
"v_mfma_f32_16x16x16_bf16 v[8:11], a[62:63], v[6:7], v[8:11]\nv_mfma_f32_16x16x16_bf16 v[12:15], a[70:71], v[6:7], v[12:15]\nv_mfma_f32_16x16x16_bf16 v[16:19], v[78:79], v[6:7], v[16:19]\nv_mfma_f32_16x16x16_bf16 v[20:23], a[78:79], v[6:7], v[20:23]\ns_waitcnt vmcnt(28)\ns_barrier\ns_add_u32 m0, %[tb], 0\ns_add_u32 s44, %[wo], 4128768\n" \
"buffer_load_dwordx4 v92, %[rw], s44 offen nt lds\ns_add_u32 m0, %[tb], 1024\ns_add_u32 s44, %[wo], 4186112\nbuffer_load_dwordx4 v92, %[rw], s44 offen nt lds\ns_add_u32 m0, %[tb], 2048\ns_add_u32 s44, %[wo], 4243456\nbuffer_load_dwordx4 v92, %[rw], s44 offen nt lds\ns_add_u32 m0, %[tb], 3072\n" \
"s_add_u32 s44, %[wo], 4300800\nbuffer_load_dwordx4 v92, %[rw], s44 offen nt lds\nds_read_b64_tr_b16 a[80:81], v93 offset:4096\nds_read_b64_tr_b16 a[82:83], v93 offset:4608\nds_read_b64_tr_b16 a[88:89], v93 offset:4104\nds_read_b64_tr_b16 a[90:91], v93 offset:4616\nds_read_b64_tr_b16 a[96:97], v93 offset:16384\nds_read_b64_tr_b16 a[98:99], v93 offset:16896\n" \
"ds_read_b64_tr_b16 v[72:73], v93 offset:16392\nds_read_b64_tr_b16 v[74:75], v93 offset:16904\ns_waitcnt lgkmcnt(0)\nv_mfma_f32_16x16x16_bf16 v[8:11], a[80:81], v[6:7], v[8:11]\nv_mfma_f32_16x16x16_bf16 v[12:15], a[88:89], v[6:7], v[12:15]\nv_mfma_f32_16x16x16_bf16 v[16:19], a[96:97], v[6:7], v[16:19]\nv_mfma_f32_16x16x16_bf16 v[20:23], v[72:73], v[6:7], v[20:23]\nv_mfma_f32_16x16x16_bf16 v[8:11], a[82:83], v[6:7], v[8:11]\n" \
"v_mfma_f32_16x16x16_bf16 v[12:15], a[90:91], v[6:7], v[12:15]\nv_mfma_f32_16x16x16_bf16 v[16:19], a[98:99], v[6:7], v[16:19]\nv_mfma_f32_16x16x16_bf16 v[20:23], v[74:75], v[6:7], v[20:23]\ns_waitcnt vmcnt(28)\ns_barrier\ns_add_u32 m0, %[tb], 4096\ns_add_u32 s44, %[wo], 4358144\nbuffer_load_dwordx4 v92, %[rw], s44 offen nt lds\n" \
"s_add_u32 m0, %[tb], 5120\ns_add_u32 s44, %[wo], 4415488\nbuffer_load_dwordx4 v92, %[rw], s44 offen nt lds\ns_add_u32 m0, %[tb], 6144\ns_add_u32 s44, %[wo], 4472832\nbuffer_load_dwordx4 v92, %[rw], s44 offen nt lds\ns_add_u32 m0, %[tb], 7168\ns_add_u32 s44, %[wo], 4530176\n" \
"buffer_load_dwordx4 v92, %[rw], s44 offen nt lds\nds_read_b64_tr_b16 a[84:85], v93 offset:8192\nds_read_b64_tr_b16 a[86:87], v93 offset:8704\nds_read_b64_tr_b16 a[92:93], v93 offset:8200\nds_read_b64_tr_b16 a[94:95], v93 offset:8712\nds_read_b64_tr_b16 a[100:101], v93 offset:20480\nds_read_b64_tr_b16 a[102:103], v93 offset:20992\nds_read_b64_tr_b16 v[68:69], v93 offset:20488\n" \
"ds_read_b64_tr_b16 v[70:71], v93 offset:21000\ns_waitcnt lgkmcnt(0)\nv_mfma_f32_16x16x16_bf16 v[8:11], a[84:85], v[6:7], v[8:11]\nv_mfma_f32_16x16x16_bf16 v[12:15], a[92:93], v[6:7], v[12:15]\nv_mfma_f32_16x16x16_bf16 v[16:19], a[100:101], v[6:7], v[16:19]\nv_mfma_f32_16x16x16_bf16 v[20:23], v[68:69], v[6:7], v[20:23]\nv_mfma_f32_16x16x16_bf16 v[8:11], a[86:87], v[6:7], v[8:11]\nv_mfma_f32_16x16x16_bf16 v[12:15], a[94:95], v[6:7], v[12:15]\n" \
"v_mfma_f32_16x16x16_bf16 v[16:19], a[102:103], v[6:7], v[16:19]\nv_mfma_f32_16x16x16_bf16 v[20:23], v[70:71], v[6:7], v[20:23]\ns_waitcnt vmcnt(28)\ns_barrier\ns_add_u32 m0, %[tb], 8192\ns_add_u32 s44, %[wo], 4587520\nbuffer_load_dwordx4 v92, %[rw], s44 offen nt lds\ns_add_u32 m0, %[tb], 9216\n" \
"s_add_u32 s44, %[wo], 4644864\nbuffer_load_dwordx4 v92, %[rw], s44 offen nt lds\ns_add_u32 m0, %[tb], 10240\ns_add_u32 s44, %[wo], 4702208\nbuffer_load_dwordx4 v92, %[rw], s44 offen nt lds\ns_add_u32 m0, %[tb], 11264\ns_add_u32 s44, %[wo], 4759552\nbuffer_load_dwordx4 v92, %[rw], s44 offen nt lds\n" \
"ds_read_b64_tr_b16 a[136:137], v94 offset:0\nds_read_b64_tr_b16 a[138:139], v94 offset:512\nds_read_b64_tr_b16 v[64:65], v94 offset:8\nds_read_b64_tr_b16 v[66:67], v94 offset:520\nds_read_b64_tr_b16 a[144:145], v94 offset:24576\nds_read_b64_tr_b16 a[146:147], v94 offset:25088\nds_read_b64_tr_b16 a[152:153], v94 offset:24584\nds_read_b64_tr_b16 a[154:155], v94 offset:25096\n" \
"s_waitcnt lgkmcnt(0)\nv_mfma_f32_16x16x16_bf16 v[8:11], a[136:137], v[6:7], v[8:11]\nv_mfma_f32_16x16x16_bf16 v[12:15], v[64:65], v[6:7], v[12:15]\nv_mfma_f32_16x16x16_bf16 v[16:19], a[144:145], v[6:7], v[16:19]\nv_mfma_f32_16x16x16_bf16 v[20:23], a[152:153], v[6:7], v[20:23]\nv_mfma_f32_16x16x16_bf16 v[8:11], a[138:139], v[6:7], v[8:11]\nv_mfma_f32_16x16x16_bf16 v[12:15], v[66:67], v[6:7], v[12:15]\nv_mfma_f32_16x16x16_bf16 v[16:19], a[146:147], v[6:7], v[16:19]\n" \
"v_mfma_f32_16x16x16_bf16 v[20:23], a[154:155], v[6:7], v[20:23]\ns_waitcnt vmcnt(28)\ns_barrier\ns_add_u32 m0, %[lw], 0\ns_add_u32 s44, %[wo], 4816896\nbuffer_load_dwordx4 v92, %[rw], s44 offen nt lds\ns_add_u32 m0, %[lw], 1024\ns_add_u32 s44, %[wo], 4874240\n" \
"buffer_load_dwordx4 v92, %[rw], s44 offen nt lds\ns_add_u32 m0, %[lw], 2048\ns_add_u32 s44, %[wo], 4931584\nbuffer_load_dwordx4 v92, %[rw], s44 offen nt lds\ns_add_u32 m0, %[lw], 3072\ns_add_u32 s44, %[wo], 4988928\nbuffer_load_dwordx4 v92, %[rw], s44 offen nt lds\nds_read_b64_tr_b16 a[140:141], v94 offset:4096\n" \
"ds_read_b64_tr_b16 a[142:143], v94 offset:4608\nds_read_b64_tr_b16 v[60:61], v94 offset:4104\nds_read_b64_tr_b16 v[62:63], v94 offset:4616\nds_read_b64_tr_b16 a[148:149], v94 offset:28672\nds_read_b64_tr_b16 a[150:151], v94 offset:29184\nds_read_b64_tr_b16 a[156:157], v94 offset:28680\nds_read_b64_tr_b16 a[158:159], v94 offset:29192\ns_waitcnt lgkmcnt(0)\n" \
"v_mfma_f32_16x16x16_bf16 v[8:11], a[140:141], v[6:7], v[8:11]\nv_mfma_f32_16x16x16_bf16 v[12:15], v[60:61], v[6:7], v[12:15]\nv_mfma_f32_16x16x16_bf16 v[16:19], a[148:149], v[6:7], v[16:19]\nv_mfma_f32_16x16x16_bf16 v[20:23], a[156:157], v[6:7], v[20:23]\nv_mfma_f32_16x16x16_bf16 v[8:11], a[142:143], v[6:7], v[8:11]\nv_mfma_f32_16x16x16_bf16 v[12:15], v[62:63], v[6:7], v[12:15]\nv_mfma_f32_16x16x16_bf16 v[16:19], a[150:151], v[6:7], v[16:19]\nv_mfma_f32_16x16x16_bf16 v[20:23], a[158:159], v[6:7], v[20:23]\n" \
"s_waitcnt vmcnt(28)\ns_barrier\ns_add_u32 m0, %[lw], 4096\ns_add_u32 s44, %[wo], 5046272\nbuffer_load_dwordx4 v92, %[rw], s44 offen nt lds\ns_add_u32 m0, %[lw], 5120\ns_add_u32 s44, %[wo], 5103616\nbuffer_load_dwordx4 v92, %[rw], s44 offen nt lds\n" \
"s_add_u32 m0, %[lw], 6144\ns_add_u32 s44, %[wo], 5160960\nbuffer_load_dwordx4 v92, %[rw], s44 offen nt lds\ns_add_u32 m0, %[lw], 7168\ns_add_u32 s44, %[wo], 5218304\nbuffer_load_dwordx4 v92, %[rw], s44 offen nt lds\nds_read_b64_tr_b16 a[160:161], v94 offset:8192\nds_read_b64_tr_b16 a[162:163], v94 offset:8704\n" \
"ds_read_b64_tr_b16 a[168:169], v94 offset:8200\nds_read_b64_tr_b16 a[170:171], v94 offset:8712\nds_read_b64_tr_b16 v[56:57], v94 offset:32768\nds_read_b64_tr_b16 v[58:59], v94 offset:33280\nds_read_b64_tr_b16 a[176:177], v94 offset:32776\nds_read_b64_tr_b16 a[178:179], v94 offset:33288\ns_waitcnt lgkmcnt(0)\nv_mfma_f32_16x16x16_bf16 v[8:11], a[160:161], v[6:7], v[8:11]\n" \
"v_mfma_f32_16x16x16_bf16 v[12:15], a[168:169], v[6:7], v[12:15]\nv_mfma_f32_16x16x16_bf16 v[16:19], v[56:57], v[6:7], v[16:19]\nv_mfma_f32_16x16x16_bf16 v[20:23], a[176:177], v[6:7], v[20:23]\nv_mfma_f32_16x16x16_bf16 v[8:11], a[162:163], v[6:7], v[8:11]\nv_mfma_f32_16x16x16_bf16 v[12:15], a[170:171], v[6:7], v[12:15]\nv_mfma_f32_16x16x16_bf16 v[16:19], v[58:59], v[6:7], v[16:19]\nv_mfma_f32_16x16x16_bf16 v[20:23], a[178:179], v[6:7], v[20:23]\ns_waitcnt vmcnt(28)\n" \
"s_barrier\ns_add_u32 m0, %[lw], 8192\ns_add_u32 s44, %[wo], 5275648\nbuffer_load_dwordx4 v92, %[rw], s44 offen nt lds\ns_add_u32 m0, %[lw], 9216\ns_add_u32 s44, %[wo], 5332992\nbuffer_load_dwordx4 v92, %[rw], s44 offen nt lds\ns_add_u32 m0, %[lw], 10240\n" \
"s_add_u32 s44, %[wo], 5390336\nbuffer_load_dwordx4 v92, %[rw], s44 offen nt lds\ns_add_u32 m0, %[lw], 11264\ns_add_u32 s44, %[wo], 5447680\nbuffer_load_dwordx4 v92, %[rw], s44 offen nt lds\nds_read_b64_tr_b16 a[164:165], v94 offset:12288\nds_read_b64_tr_b16 a[166:167], v94 offset:12800\nds_read_b64_tr_b16 a[172:173], v94 offset:12296\n" \
"ds_read_b64_tr_b16 a[174:175], v94 offset:12808\nds_read_b64_tr_b16 v[52:53], v94 offset:36864\nds_read_b64_tr_b16 v[54:55], v94 offset:37376\nds_read_b64_tr_b16 a[180:181], v94 offset:36872\nds_read_b64_tr_b16 a[182:183], v94 offset:37384\ns_waitcnt lgkmcnt(0)\nv_mfma_f32_16x16x16_bf16 v[8:11], a[164:165], v[6:7], v[8:11]\nv_mfma_f32_16x16x16_bf16 v[12:15], a[172:173], v[6:7], v[12:15]\n" \
"v_mfma_f32_16x16x16_bf16 v[16:19], v[52:53], v[6:7], v[16:19]\nv_mfma_f32_16x16x16_bf16 v[20:23], a[180:181], v[6:7], v[20:23]\nv_mfma_f32_16x16x16_bf16 v[8:11], a[166:167], v[6:7], v[8:11]\nv_mfma_f32_16x16x16_bf16 v[12:15], a[174:175], v[6:7], v[12:15]\nv_mfma_f32_16x16x16_bf16 v[16:19], v[54:55], v[6:7], v[16:19]\nv_mfma_f32_16x16x16_bf16 v[20:23], a[182:183], v[6:7], v[20:23]\ns_waitcnt vmcnt(28)\ns_barrier\n" \
"s_add_u32 m0, %[lw], 12288\ns_add_u32 s44, %[wo], 5963776\nbuffer_load_dwordx4 v92, %[rw], s44 offen nt lds\ns_add_u32 m0, %[lw], 13312\ns_add_u32 s44, %[wo], 6021120\nbuffer_load_dwordx4 v92, %[rw], s44 offen nt lds\ns_add_u32 m0, %[lw], 14336\ns_add_u32 s44, %[wo], 6078464\n" \
"buffer_load_dwordx4 v92, %[rw], s44 offen nt lds\ns_add_u32 m0, %[lw], 15360\ns_add_u32 s44, %[wo], 6135808\nbuffer_load_dwordx4 v92, %[rw], s44 offen nt lds\nds_read_b64_tr_b16 a[184:185], v94 offset:16384\nds_read_b64_tr_b16 a[186:187], v94 offset:16896\nds_read_b64_tr_b16 a[192:193], v94 offset:16392\nds_read_b64_tr_b16 a[194:195], v94 offset:16904\n" \
"ds_read_b64_tr_b16 a[200:201], v94 offset:40960\nds_read_b64_tr_b16 a[202:203], v94 offset:41472\nds_read_b64_tr_b16 v[48:49], v94 offset:40968\nds_read_b64_tr_b16 v[50:51], v94 offset:41480\ns_waitcnt lgkmcnt(0)\nv_mfma_f32_16x16x16_bf16 v[8:11], a[184:185], v[6:7], v[8:11]\nv_mfma_f32_16x16x16_bf16 v[12:15], a[192:193], v[6:7], v[12:15]\nv_mfma_f32_16x16x16_bf16 v[16:19], a[200:201], v[6:7], v[16:19]\n" \
"v_mfma_f32_16x16x16_bf16 v[20:23], v[48:49], v[6:7], v[20:23]\nv_mfma_f32_16x16x16_bf16 v[8:11], a[186:187], v[6:7], v[8:11]\nv_mfma_f32_16x16x16_bf16 v[12:15], a[194:195], v[6:7], v[12:15]\nv_mfma_f32_16x16x16_bf16 v[16:19], a[202:203], v[6:7], v[16:19]\nv_mfma_f32_16x16x16_bf16 v[20:23], v[50:51], v[6:7], v[20:23]\ns_waitcnt vmcnt(28)\ns_barrier\ns_add_u32 m0, %[lw], 16384\n" \
"s_add_u32 s44, %[wo], 6193152\nbuffer_load_dwordx4 v92, %[rw], s44 offen nt lds\ns_add_u32 m0, %[lw], 17408\ns_add_u32 s44, %[wo], 6250496\nbuffer_load_dwordx4 v92, %[rw], s44 offen nt lds\ns_add_u32 m0, %[lw], 18432\ns_add_u32 s44, %[wo], 6307840\nbuffer_load_dwordx4 v92, %[rw], s44 offen nt lds\n" \
"s_add_u32 m0, %[lw], 19456\ns_add_u32 s44, %[wo], 6365184\nbuffer_load_dwordx4 v92, %[rw], s44 offen nt lds\nds_read_b64_tr_b16 a[188:189], v94 offset:20480\nds_read_b64_tr_b16 a[190:191], v94 offset:20992\nds_read_b64_tr_b16 a[196:197], v94 offset:20488\nds_read_b64_tr_b16 a[198:199], v94 offset:21000\nds_read_b64_tr_b16 a[204:205], v94 offset:45056\n" \
"ds_read_b64_tr_b16 a[206:207], v94 offset:45568\nds_read_b64_tr_b16 v[44:45], v94 offset:45064\nds_read_b64_tr_b16 v[46:47], v94 offset:45576\ns_waitcnt lgkmcnt(0)\nv_mfma_f32_16x16x16_bf16 v[8:11], a[188:189], v[6:7], v[8:11]\nv_mfma_f32_16x16x16_bf16 v[12:15], a[196:197], v[6:7], v[12:15]\nv_mfma_f32_16x16x16_bf16 v[16:19], a[204:205], v[6:7], v[16:19]\nv_mfma_f32_16x16x16_bf16 v[20:23], v[44:45], v[6:7], v[20:23]\n" \
"v_mfma_f32_16x16x16_bf16 v[8:11], a[190:191], v[6:7], v[8:11]\nv_mfma_f32_16x16x16_bf16 v[12:15], a[198:199], v[6:7], v[12:15]\nv_mfma_f32_16x16x16_bf16 v[16:19], a[206:207], v[6:7], v[16:19]\nv_mfma_f32_16x16x16_bf16 v[20:23], v[46:47], v[6:7], v[20:23]\ns_waitcnt vmcnt(28)\ns_barrier\ns_add_u32 m0, %[lw], 20480\ns_add_u32 s44, %[wo], 6422528\n" \
"buffer_load_dwordx4 v92, %[rw], s44 offen nt lds\ns_add_u32 m0, %[lw], 21504\ns_add_u32 s44, %[wo], 6479872\nbuffer_load_dwordx4 v92, %[rw], s44 offen nt lds\ns_add_u32 m0, %[lw], 22528\ns_add_u32 s44, %[wo], 6537216\nbuffer_load_dwordx4 v92, %[rw], s44 offen nt lds\ns_add_u32 m0, %[lw], 23552\n" \
"s_add_u32 s44, %[wo], 6594560\nbuffer_load_dwordx4 v92, %[rw], s44 offen nt lds\nds_read_b64_tr_b16 a[240:241], v93 offset:0\nds_read_b64_tr_b16 a[242:243], v93 offset:512\nds_read_b64_tr_b16 v[40:41], v93 offset:8\nds_read_b64_tr_b16 v[42:43], v93 offset:520\nds_read_b64_tr_b16 a[248:249], v93 offset:12288\nds_read_b64_tr_b16 a[250:251], v93 offset:12800\n" \
"ds_read_b64_tr_b16 v[96:97], v93 offset:12296\nds_read_b64_tr_b16 v[98:99], v93 offset:12808\ns_waitcnt lgkmcnt(0)\nv_mfma_f32_16x16x16_bf16 v[8:11], a[240:241], v[6:7], v[8:11]\nv_mfma_f32_16x16x16_bf16 v[12:15], v[40:41], v[6:7], v[12:15]\nv_mfma_f32_16x16x16_bf16 v[16:19], a[248:249], v[6:7], v[16:19]\nv_mfma_f32_16x16x16_bf16 v[20:23], v[96:97], v[6:7], v[20:23]\nv_mfma_f32_16x16x16_bf16 v[8:11], a[242:243], v[6:7], v[8:11]\n" \
"v_mfma_f32_16x16x16_bf16 v[12:15], v[42:43], v[6:7], v[12:15]\nv_mfma_f32_16x16x16_bf16 v[16:19], a[250:251], v[6:7], v[16:19]\nv_mfma_f32_16x16x16_bf16 v[20:23], v[98:99], v[6:7], v[20:23]\ns_waitcnt vmcnt(28)\ns_barrier\ns_add_u32 m0, %[tb], 0\ns_add_u32 s44, %[wo], 6651904\nbuffer_load_dwordx4 v92, %[rw], s44 offen nt lds\n" \
"s_add_u32 m0, %[tb], 1024\ns_add_u32 s44, %[wo], 6709248\nbuffer_load_dwordx4 v92, %[rw], s44 offen nt lds\ns_add_u32 m0, %[tb], 2048\ns_add_u32 s44, %[wo], 6766592\nbuffer_load_dwordx4 v92, %[rw], s44 offen nt lds\ns_add_u32 m0, %[tb], 3072\ns_add_u32 s44, %[wo], 6823936\n" \
"buffer_load_dwordx4 v92, %[rw], s44 offen nt lds\nds_read_b64_tr_b16 a[244:245], v93 offset:4096\nds_read_b64_tr_b16 a[246:247], v93 offset:4608\nds_read_b64_tr_b16 v[36:37], v93 offset:4104\nds_read_b64_tr_b16 v[38:39], v93 offset:4616\nds_read_b64_tr_b16 a[252:253], v93 offset:16384\nds_read_b64_tr_b16 a[254:255], v93 offset:16896\nds_read_b64_tr_b16 v[100:101], v93 offset:16392\n" \
"ds_read_b64_tr_b16 v[102:103], v93 offset:16904\ns_waitcnt lgkmcnt(0)\nv_mfma_f32_16x16x16_bf16 v[8:11], a[244:245], v[6:7], v[8:11]\nv_mfma_f32_16x16x16_bf16 v[12:15], v[36:37], v[6:7], v[12:15]\nv_mfma_f32_16x16x16_bf16 v[16:19], a[252:253], v[6:7], v[16:19]\nv_mfma_f32_16x16x16_bf16 v[20:23], v[100:101], v[6:7], v[20:23]\nv_mfma_f32_16x16x16_bf16 v[8:11], a[246:247], v[6:7], v[8:11]\nv_mfma_f32_16x16x16_bf16 v[12:15], v[38:39], v[6:7], v[12:15]\n" \
"v_mfma_f32_16x16x16_bf16 v[16:19], a[254:255], v[6:7], v[16:19]\nv_mfma_f32_16x16x16_bf16 v[20:23], v[102:103], v[6:7], v[20:23]\ns_waitcnt vmcnt(28)\ns_barrier\ns_add_u32 m0, %[tb], 4096\ns_add_u32 s44, %[wo], 6881280\nbuffer_load_dwordx4 v92, %[rw], s44 offen nt lds\ns_add_u32 m0, %[tb], 5120\n" \
"s_add_u32 s44, %[wo], 6938624\nbuffer_load_dwordx4 v92, %[rw], s44 offen nt lds\ns_add_u32 m0, %[tb], 6144\ns_add_u32 s44, %[wo], 6995968\nbuffer_load_dwordx4 v92, %[rw], s44 offen nt lds\ns_add_u32 m0, %[tb], 7168\ns_add_u32 s44, %[wo], 7053312\nbuffer_load_dwordx4 v92, %[rw], s44 offen nt lds\n" \
"ds_read_b64_tr_b16 v[104:105], v93 offset:8192\nds_read_b64_tr_b16 v[106:107], v93 offset:8704\nds_read_b64_tr_b16 v[112:113], v93 offset:8200\nds_read_b64_tr_b16 v[114:115], v93 offset:8712\nds_read_b64_tr_b16 v[32:33], v93 offset:20480\nds_read_b64_tr_b16 v[34:35], v93 offset:20992\nds_read_b64_tr_b16 v[120:121], v93 offset:20488\nds_read_b64_tr_b16 v[122:123], v93 offset:21000\n" \
"s_waitcnt lgkmcnt(0)\nv_mfma_f32_16x16x16_bf16 v[8:11], v[104:105], v[6:7], v[8:11]\nv_mfma_f32_16x16x16_bf16 v[12:15], v[112:113], v[6:7], v[12:15]\nv_mfma_f32_16x16x16_bf16 v[16:19], v[32:33], v[6:7], v[16:19]\nv_mfma_f32_16x16x16_bf16 v[20:23], v[120:121], v[6:7], v[20:23]\nv_mfma_f32_16x16x16_bf16 v[8:11], v[106:107], v[6:7], v[8:11]\nv_mfma_f32_16x16x16_bf16 v[12:15], v[114:115], v[6:7], v[12:15]\nv_mfma_f32_16x16x16_bf16 v[16:19], v[34:35], v[6:7], v[16:19]\n" \
"v_mfma_f32_16x16x16_bf16 v[20:23], v[122:123], v[6:7], v[20:23]\ns_waitcnt vmcnt(28)\ns_barrier\ns_add_u32 m0, %[tb], 8192\ns_add_u32 s44, %[wo], 7110656\nbuffer_load_dwordx4 v92, %[rw], s44 offen nt lds\ns_add_u32 m0, %[tb], 9216\ns_add_u32 s44, %[wo], 7168000\n" \
"buffer_load_dwordx4 v92, %[rw], s44 offen nt lds\ns_add_u32 m0, %[tb], 10240\ns_add_u32 s44, %[wo], 7225344\nbuffer_load_dwordx4 v92, %[rw], s44 offen nt lds\ns_add_u32 m0, %[tb], 11264\ns_add_u32 s44, %[wo], 7282688\nbuffer_load_dwordx4 v92, %[rw], s44 offen nt lds\nds_read_b64_tr_b16 v[108:109], v94 offset:0\n" \
"ds_read_b64_tr_b16 v[110:111], v94 offset:512\nds_read_b64_tr_b16 v[116:117], v94 offset:8\nds_read_b64_tr_b16 v[118:119], v94 offset:520\nds_read_b64_tr_b16 v[28:29], v94 offset:24576\nds_read_b64_tr_b16 v[30:31], v94 offset:25088\nds_read_b64_tr_b16 v[124:125], v94 offset:24584\nds_read_b64_tr_b16 v[126:127], v94 offset:25096\ns_waitcnt lgkmcnt(0)\n" \
"v_mfma_f32_16x16x16_bf16 v[8:11], v[108:109], v[6:7], v[8:11]\nv_mfma_f32_16x16x16_bf16 v[12:15], v[116:117], v[6:7], v[12:15]\nv_mfma_f32_16x16x16_bf16 v[16:19], v[28:29], v[6:7], v[16:19]\nv_mfma_f32_16x16x16_bf16 v[20:23], v[124:125], v[6:7], v[20:23]\nv_mfma_f32_16x16x16_bf16 v[8:11], v[110:111], v[6:7], v[8:11]\nv_mfma_f32_16x16x16_bf16 v[12:15], v[118:119], v[6:7], v[12:15]\nv_mfma_f32_16x16x16_bf16 v[16:19], v[30:31], v[6:7], v[16:19]\nv_mfma_f32_16x16x16_bf16 v[20:23], v[126:127], v[6:7], v[20:23]\n" \
"s_waitcnt vmcnt(28)\ns_barrier\nds_read_b64_tr_b16 v[128:129], v94 offset:4096\nds_read_b64_tr_b16 v[130:131], v94 offset:4608\nds_read_b64_tr_b16 v[136:137], v94 offset:4104\nds_read_b64_tr_b16 v[138:139], v94 offset:4616\nds_read_b64_tr_b16 v[144:145], v94 offset:28672\nds_read_b64_tr_b16 v[146:147], v94 offset:29184\n" \
"ds_read_b64_tr_b16 v[24:25], v94 offset:28680\nds_read_b64_tr_b16 v[26:27], v94 offset:29192\ns_waitcnt lgkmcnt(0)\nv_mfma_f32_16x16x16_bf16 v[8:11], v[128:129], v[6:7], v[8:11]\nv_mfma_f32_16x16x16_bf16 v[12:15], v[136:137], v[6:7], v[12:15]\nv_mfma_f32_16x16x16_bf16 v[16:19], v[144:145], v[6:7], v[16:19]\nv_mfma_f32_16x16x16_bf16 v[20:23], v[24:25], v[6:7], v[20:23]\nv_mfma_f32_16x16x16_bf16 v[8:11], v[130:131], v[6:7], v[8:11]\n" \
"v_mfma_f32_16x16x16_bf16 v[12:15], v[138:139], v[6:7], v[12:15]\nv_mfma_f32_16x16x16_bf16 v[16:19], v[146:147], v[6:7], v[16:19]\nv_mfma_f32_16x16x16_bf16 v[20:23], v[26:27], v[6:7], v[20:23]\ns_waitcnt vmcnt(24)\nds_write_b128 v1, v[88:91] offset:0\nds_write_b128 v1, v[84:87] offset:1024\nds_write_b128 v1, v[80:83] offset:2048\nds_write_b128 v1, v[76:79] offset:3072\n" \
"s_barrier\nds_read_b64_tr_b16 v[132:133], v94 offset:8192\nds_read_b64_tr_b16 v[134:135], v94 offset:8704\nds_read_b64_tr_b16 v[140:141], v94 offset:8200\nds_read_b64_tr_b16 v[142:143], v94 offset:8712\nds_read_b64_tr_b16 v[148:149], v94 offset:32768\nds_read_b64_tr_b16 v[150:151], v94 offset:33280\nds_read_b64_tr_b16 v[76:77], v94 offset:32776\n" \
"ds_read_b64_tr_b16 v[78:79], v94 offset:33288\ns_waitcnt lgkmcnt(0)\nv_mfma_f32_16x16x16_bf16 v[8:11], v[132:133], v[6:7], v[8:11]\nv_mfma_f32_16x16x16_bf16 v[12:15], v[140:141], v[6:7], v[12:15]\nv_mfma_f32_16x16x16_bf16 v[16:19], v[148:149], v[6:7], v[16:19]\nv_mfma_f32_16x16x16_bf16 v[20:23], v[76:77], v[6:7], v[20:23]\nv_mfma_f32_16x16x16_bf16 v[8:11], v[134:135], v[6:7], v[8:11]\nv_mfma_f32_16x16x16_bf16 v[12:15], v[142:143], v[6:7], v[12:15]\n" \
"v_mfma_f32_16x16x16_bf16 v[16:19], v[150:151], v[6:7], v[16:19]\nv_mfma_f32_16x16x16_bf16 v[20:23], v[78:79], v[6:7], v[20:23]\ns_waitcnt vmcnt(20)\nds_write_b128 v1, v[72:75] offset:4096\nds_write_b128 v1, v[68:71] offset:5120\nds_write_b128 v1, v[64:67] offset:6144\nds_write_b128 v1, v[60:63] offset:7168\ns_barrier\n" \
"ds_read_b64_tr_b16 v[184:185], v94 offset:12288\nds_read_b64_tr_b16 v[186:187], v94 offset:12800\nds_read_b64_tr_b16 v[60:61], v94 offset:12296\nds_read_b64_tr_b16 v[62:63], v94 offset:12808\nds_read_b64_tr_b16 v[192:193], v94 offset:36864\nds_read_b64_tr_b16 v[194:195], v94 offset:37376\nds_read_b64_tr_b16 v[200:201], v94 offset:36872\nds_read_b64_tr_b16 v[202:203], v94 offset:37384\n" \
"s_waitcnt lgkmcnt(0)\nv_mfma_f32_16x16x16_bf16 v[8:11], v[184:185], v[6:7], v[8:11]\nv_mfma_f32_16x16x16_bf16 v[12:15], v[60:61], v[6:7], v[12:15]\nv_mfma_f32_16x16x16_bf16 v[16:19], v[192:193], v[6:7], v[16:19]\nv_mfma_f32_16x16x16_bf16 v[20:23], v[200:201], v[6:7], v[20:23]\nv_mfma_f32_16x16x16_bf16 v[8:11], v[186:187], v[6:7], v[8:11]\nv_mfma_f32_16x16x16_bf16 v[12:15], v[62:63], v[6:7], v[12:15]\nv_mfma_f32_16x16x16_bf16 v[16:19], v[194:195], v[6:7], v[16:19]\n" \
"v_mfma_f32_16x16x16_bf16 v[20:23], v[202:203], v[6:7], v[20:23]\ns_waitcnt vmcnt(16)\nds_write_b128 v1, v[56:59] offset:8192\nds_write_b128 v1, v[52:55] offset:9216\nds_write_b128 v1, v[48:51] offset:10240\nds_write_b128 v1, v[44:47] offset:11264\ns_barrier\nds_read_b64_tr_b16 v[188:189], v94 offset:16384\n" \
"ds_read_b64_tr_b16 v[190:191], v94 offset:16896\nds_read_b64_tr_b16 v[44:45], v94 offset:16392\nds_read_b64_tr_b16 v[46:47], v94 offset:16904\nds_read_b64_tr_b16 v[196:197], v94 offset:40960\nds_read_b64_tr_b16 v[198:199], v94 offset:41472\nds_read_b64_tr_b16 v[204:205], v94 offset:40968\nds_read_b64_tr_b16 v[206:207], v94 offset:41480\ns_waitcnt lgkmcnt(0)\n" \
"v_mfma_f32_16x16x16_bf16 v[8:11], v[188:189], v[6:7], v[8:11]\nv_mfma_f32_16x16x16_bf16 v[12:15], v[44:45], v[6:7], v[12:15]\nv_mfma_f32_16x16x16_bf16 v[16:19], v[196:197], v[6:7], v[16:19]\nv_mfma_f32_16x16x16_bf16 v[20:23], v[204:205], v[6:7], v[20:23]\nv_mfma_f32_16x16x16_bf16 v[8:11], v[190:191], v[6:7], v[8:11]\nv_mfma_f32_16x16x16_bf16 v[12:15], v[46:47], v[6:7], v[12:15]\nv_mfma_f32_16x16x16_bf16 v[16:19], v[198:199], v[6:7], v[16:19]\nv_mfma_f32_16x16x16_bf16 v[20:23], v[206:207], v[6:7], v[20:23]\n" \
"s_waitcnt vmcnt(12)\nds_write_b128 v1, v[40:43] offset:12288\nds_write_b128 v1, v[36:39] offset:13312\nds_write_b128 v1, v[32:35] offset:14336\nds_write_b128 v1, v[28:31] offset:15360\ns_barrier\nds_read_b64_tr_b16 v[208:209], v94 offset:20480\nds_read_b64_tr_b16 v[210:211], v94 offset:20992\n" \
"ds_read_b64_tr_b16 v[216:217], v94 offset:20488\nds_read_b64_tr_b16 v[218:219], v94 offset:21000\nds_read_b64_tr_b16 v[28:29], v94 offset:45056\nds_read_b64_tr_b16 v[30:31], v94 offset:45568\nds_read_b64_tr_b16 v[224:225], v94 offset:45064\nds_read_b64_tr_b16 v[226:227], v94 offset:45576\ns_waitcnt lgkmcnt(0)\nv_mfma_f32_16x16x16_bf16 v[8:11], v[208:209], v[6:7], v[8:11]\n" \
"v_mfma_f32_16x16x16_bf16 v[12:15], v[216:217], v[6:7], v[12:15]\nv_mfma_f32_16x16x16_bf16 v[16:19], v[28:29], v[6:7], v[16:19]\nv_mfma_f32_16x16x16_bf16 v[20:23], v[224:225], v[6:7], v[20:23]\nv_mfma_f32_16x16x16_bf16 v[8:11], v[210:211], v[6:7], v[8:11]\nv_mfma_f32_16x16x16_bf16 v[12:15], v[218:219], v[6:7], v[12:15]\nv_mfma_f32_16x16x16_bf16 v[16:19], v[30:31], v[6:7], v[16:19]\nv_mfma_f32_16x16x16_bf16 v[20:23], v[226:227], v[6:7], v[20:23]\ns_waitcnt vmcnt(8)\n" \
"ds_write_b128 v1, v[24:27] offset:16384\nds_write_b128 v1, v[76:79] offset:17408\nds_write_b128 v1, v[60:63] offset:18432\nds_write_b128 v1, v[44:47] offset:19456\ns_barrier\nds_read_b64_tr_b16 v[212:213], v93 offset:0\nds_read_b64_tr_b16 v[214:215], v93 offset:512\nds_read_b64_tr_b16 v[220:221], v93 offset:8\n" \
"ds_read_b64_tr_b16 v[222:223], v93 offset:520\nds_read_b64_tr_b16 v[44:45], v93 offset:12288\nds_read_b64_tr_b16 v[46:47], v93 offset:12800\nds_read_b64_tr_b16 v[228:229], v93 offset:12296\nds_read_b64_tr_b16 v[230:231], v93 offset:12808\ns_waitcnt lgkmcnt(0)\nv_mfma_f32_16x16x16_bf16 v[8:11], v[212:213], v[6:7], v[8:11]\nv_mfma_f32_16x16x16_bf16 v[12:15], v[220:221], v[6:7], v[12:15]\n" \
"v_mfma_f32_16x16x16_bf16 v[16:19], v[44:45], v[6:7], v[16:19]\nv_mfma_f32_16x16x16_bf16 v[20:23], v[228:229], v[6:7], v[20:23]\nv_mfma_f32_16x16x16_bf16 v[8:11], v[214:215], v[6:7], v[8:11]\nv_mfma_f32_16x16x16_bf16 v[12:15], v[222:223], v[6:7], v[12:15]\nv_mfma_f32_16x16x16_bf16 v[16:19], v[46:47], v[6:7], v[16:19]\nv_mfma_f32_16x16x16_bf16 v[20:23], v[230:231], v[6:7], v[20:23]\ns_waitcnt vmcnt(4)\nds_write_b128 v1, v[28:31] offset:20480\n" \
"ds_write_b128 v1, v[44:47] offset:21504\ns_barrier\nds_read_b64_tr_b16 v[232:233], v93 offset:4096\nds_read_b64_tr_b16 v[234:235], v93 offset:4608\nds_read_b64_tr_b16 v[240:241], v93 offset:4104\nds_read_b64_tr_b16 v[242:243], v93 offset:4616\nds_read_b64_tr_b16 v[248:249], v93 offset:16384\nds_read_b64_tr_b16 v[250:251], v93 offset:16896\n" \
"ds_read_b64_tr_b16 v[44:45], v93 offset:16392\nds_read_b64_tr_b16 v[46:47], v93 offset:16904\ns_waitcnt lgkmcnt(0)\nv_mfma_f32_16x16x16_bf16 v[8:11], v[232:233], v[6:7], v[8:11]\nv_mfma_f32_16x16x16_bf16 v[12:15], v[240:241], v[6:7], v[12:15]\nv_mfma_f32_16x16x16_bf16 v[16:19], v[248:249], v[6:7], v[16:19]\nv_mfma_f32_16x16x16_bf16 v[20:23], v[44:45], v[6:7], v[20:23]\nv_mfma_f32_16x16x16_bf16 v[8:11], v[234:235], v[6:7], v[8:11]\n" \
"v_mfma_f32_16x16x16_bf16 v[12:15], v[242:243], v[6:7], v[12:15]\nv_mfma_f32_16x16x16_bf16 v[16:19], v[250:251], v[6:7], v[16:19]\nv_mfma_f32_16x16x16_bf16 v[20:23], v[46:47], v[6:7], v[20:23]\ns_waitcnt vmcnt(0)\nds_write_b128 v1, v[44:47] offset:22528\ns_barrier\nds_read_b64_tr_b16 v[236:237], v93 offset:8192\nds_read_b64_tr_b16 v[238:239], v93 offset:8704\n" \
"ds_read_b64_tr_b16 v[244:245], v93 offset:8200\nds_read_b64_tr_b16 v[246:247], v93 offset:8712\nds_read_b64_tr_b16 v[252:253], v93 offset:20480\nds_read_b64_tr_b16 v[254:255], v93 offset:20992\nds_read_b64_tr_b16 v[44:45], v93 offset:20488\nds_read_b64_tr_b16 v[46:47], v93 offset:21000\ns_waitcnt lgkmcnt(0)\nv_mfma_f32_16x16x16_bf16 v[8:11], v[236:237], v[6:7], v[8:11]\n" \
"v_mfma_f32_16x16x16_bf16 v[12:15], v[244:245], v[6:7], v[12:15]\nv_mfma_f32_16x16x16_bf16 v[16:19], v[252:253], v[6:7], v[16:19]\nv_mfma_f32_16x16x16_bf16 v[20:23], v[44:45], v[6:7], v[20:23]\nv_mfma_f32_16x16x16_bf16 v[8:11], v[238:239], v[6:7], v[8:11]\nv_mfma_f32_16x16x16_bf16 v[12:15], v[246:247], v[6:7], v[12:15]\nv_mfma_f32_16x16x16_bf16 v[16:19], v[254:255], v[6:7], v[16:19]\nv_mfma_f32_16x16x16_bf16 v[20:23], v[46:47], v[6:7], v[20:23]\nds_write_b128 v1, v[44:47] offset:23552\n" \
"s_waitcnt lgkmcnt(0)\ns_barrier\ns_mov_b32 s40, 0\ns_mov_b32 s43, 0\ns_lshl_b32 s57, %[w], 12\ns_mov_b32 s58, 0xbeeff558\ns_mov_b32 s59, 0x409d30fc\ns_mov_b32 s56, -2147483648\n" \
".Lgg_next%=:\ns_cmp_lg_u32 %[w], 0\ns_cbranch_scc1 .Lgg_w1%=\nbuffer_load_dword v56, v3, %[rs], 0 offen sc1\ns_waitcnt vmcnt(0)\ns_barrier\nbuffer_store_dword v90, off, %[rs], s56 sc1\n.Lgg_poll%=:\n" \
"v_cmp_gt_i32 vcc, s40, v56\ns_cbranch_vccz .Lgg_go%=\ns_cmp_ge_u32 s40, 62\ns_cbranch_scc1 .Lgg_ns1050%=\ns_sleep 12\n.Lgg_ns1050%=:\ns_sleep 1\nbuffer_load_dword v56, v3, %[rs], 0 offen sc1\n" \
"s_waitcnt vmcnt(0)\ns_add_u32 s43, s43, 1\ns_cmp_lt_u32 s43, 262144\ns_cbranch_scc1 .Lgg_poll%=\ns_branch .Lgg_abort%=\n.Lgg_w1%=:\ns_waitcnt vmcnt(0)\ns_barrier\n" \
".Lgg_go%=:\ns_add_u32 s44, s40, 0\ns_lshl_b32 s44, s44, 17\ns_add_u32 s44, s44, %[xn]\ns_lshr_b32 s45, s44, 8\ns_and_b32 s45, s45, 0x2000\ns_xor_b32 s41, s44, s45\ns_add_u32 s61, s41, %[xw]\n" \
"s_barrier\ns_add_u32 m0, s57, 0\ns_add_u32 s60, s61, 0\nbuffer_load_dwordx4 v0, %[rs], s60 offen offset:0 sc1 lds\nbuffer_load_dwordx4 v0, %[rs], s60 offen offset:1024 sc1 lds\nbuffer_load_dwordx4 v0, %[rs], s60 offen offset:2048 sc1 lds\nbuffer_load_dwordx4 v0, %[rs], s60 offen offset:3072 sc1 lds\ns_add_u32 m0, s57, 16384\n" \
"s_add_u32 s60, s61, 32768\nbuffer_load_dwordx4 v0, %[rs], s60 offen offset:0 sc1 lds\nbuffer_load_dwordx4 v0, %[rs], s60 offen offset:1024 sc1 lds\nbuffer_load_dwordx4 v0, %[rs], s60 offen offset:2048 sc1 lds\nbuffer_load_dwordx4 v0, %[rs], s60 offen offset:3072 sc1 lds\nbuffer_load_dwordx4 v[40:43], v5, %[rs], s41 offen offset:0 sc1\nbuffer_load_dwordx4 v[44:47], v5, %[rs], s41 offen offset:256 sc1\ns_lshl_b32 s44, s40, 19\n" \
"s_add_u32 s44, s44, %[ho]\ns_lshr_b32 s45, s44, 8\ns_and_b32 s45, s45, 0x2000\ns_xor_b32 s55, s44, s45\ns_waitcnt vmcnt(6)\ns_barrier\nds_read_b128 v[56:59], v2 offset:0\nds_read_b128 v[60:63], v2 offset:2048\n" \
"ds_read_b128 v[64:67], v2 offset:4096\ns_waitcnt lgkmcnt(2)\nv_mfma_f32_16x16x32_bf16 v[24:27], a[0:3], v[56:59], 0\nds_read_b128 v[68:71], v2 offset:6144\nds_read_b128 v[80:83], v1 offset:0\nv_mfma_f32_16x16x32_bf16 v[28:31], a[8:11], v[56:59], 0\nbuffer_load_dwordx4 v[48:51], v5, %[rs], s41 offen offset:512 sc1\nv_mfma_f32_16x16x32_bf16 v[32:35], a[16:19], v[56:59], 0\n" \
"v_mfma_f32_16x16x32_bf16 v[36:39], a[24:27], v[56:59], 0\ns_waitcnt lgkmcnt(3)\nds_read_b128 v[72:75], v1 offset:1024\nv_mfma_f32_16x16x32_bf16 v[24:27], a[4:7], v[60:63], v[24:27]\nds_read_b128 v[56:59], v2 offset:8192\nv_mfma_f32_16x16x32_bf16 v[28:31], a[12:15], v[60:63], v[28:31]\nbuffer_load_dwordx4 v[52:55], v5, %[rs], s41 offen offset:768 sc1\nv_mfma_f32_16x16x32_bf16 v[32:35], a[20:23], v[60:63], v[32:35]\n" \
"v_mfma_f32_16x16x32_bf16 v[36:39], a[28:31], v[60:63], v[36:39]\ns_waitcnt lgkmcnt(4)\nds_read_b128 v[76:79], v1 offset:2048\nv_mfma_f32_16x16x32_bf16 v[24:27], a[32:35], v[64:67], v[24:27]\ns_waitcnt lgkmcnt(3)\ns_add_u32 m0, s57, 32768\nds_read_b128 v[60:63], v2 offset:10240\nv_mfma_f32_16x16x32_bf16 v[28:31], v[80:83], v[64:67], v[28:31]\n" \
"s_add_u32 s60, s61, 65536\nbuffer_load_dwordx4 v0, %[rs], s60 offen offset:0 sc1 lds\nv_mfma_f32_16x16x32_bf16 v[32:35], a[40:43], v[64:67], v[32:35]\nv_mfma_f32_16x16x32_bf16 v[36:39], a[48:51], v[64:67], v[36:39]\nds_read_b128 v[80:83], v1 offset:3072\nv_mfma_f32_16x16x32_bf16 v[24:27], a[36:39], v[68:71], v[24:27]\ns_waitcnt lgkmcnt(4)\nds_read_b128 v[64:67], v2 offset:12288\n" \
"v_mfma_f32_16x16x32_bf16 v[28:31], v[72:75], v[68:71], v[28:31]\nbuffer_load_dwordx4 v0, %[rs], s60 offen offset:1024 sc1 lds\nv_mfma_f32_16x16x32_bf16 v[32:35], a[44:47], v[68:71], v[32:35]\nv_mfma_f32_16x16x32_bf16 v[36:39], a[52:55], v[68:71], v[36:39]\ns_waitcnt lgkmcnt(4)\nds_read_b128 v[72:75], v1 offset:4096\nv_mfma_f32_16x16x32_bf16 v[24:27], a[56:59], v[56:59], v[24:27]\nds_read_b128 v[68:71], v2 offset:14336\n" \
"v_mfma_f32_16x16x32_bf16 v[28:31], a[64:67], v[56:59], v[28:31]\ns_waitcnt lgkmcnt(5)\nbuffer_load_dwordx4 v0, %[rs], s60 offen offset:2048 sc1 lds\nv_mfma_f32_16x16x32_bf16 v[32:35], v[76:79], v[56:59], v[32:35]\nv_mfma_f32_16x16x32_bf16 v[36:39], a[72:75], v[56:59], v[36:39]\ns_waitcnt lgkmcnt(4)\nds_read_b128 v[76:79], v1 offset:5120\nv_mfma_f32_16x16x32_bf16 v[24:27], a[60:63], v[60:63], v[24:27]\n" \
"s_waitcnt vmcnt(7)\ns_barrier\nds_read_b128 v[56:59], v2 offset:16384\nv_mfma_f32_16x16x32_bf16 v[28:31], a[68:71], v[60:63], v[28:31]\ns_waitcnt lgkmcnt(5)\nbuffer_load_dwordx4 v0, %[rs], s60 offen offset:3072 sc1 lds\nv_mfma_f32_16x16x32_bf16 v[32:35], v[80:83], v[60:63], v[32:35]\nv_mfma_f32_16x16x32_bf16 v[36:39], a[76:79], v[60:63], v[36:39]\n" \
"s_waitcnt lgkmcnt(4)\nv_mfma_f32_16x16x32_bf16 v[24:27], a[80:83], v[64:67], v[24:27]\ns_add_u32 m0, s57, 49152\nds_read_b128 v[60:63], v2 offset:18432\nv_mfma_f32_16x16x32_bf16 v[28:31], a[88:91], v[64:67], v[28:31]\ns_add_u32 s60, s61, 98304\nbuffer_load_dwordx4 v0, %[rs], s60 offen offset:0 sc1 lds\nv_mfma_f32_16x16x32_bf16 v[32:35], a[96:99], v[64:67], v[32:35]\n" \
"s_waitcnt lgkmcnt(4)\nv_mfma_f32_16x16x32_bf16 v[36:39], v[72:75], v[64:67], v[36:39]\ns_waitcnt lgkmcnt(3)\nv_mfma_f32_16x16x32_bf16 v[24:27], a[84:87], v[68:71], v[24:27]\nds_read_b128 v[64:67], v2 offset:20480\nv_mfma_f32_16x16x32_bf16 v[28:31], a[92:95], v[68:71], v[28:31]\nbuffer_load_dwordx4 v0, %[rs], s60 offen offset:1024 sc1 lds\nv_mfma_f32_16x16x32_bf16 v[32:35], a[100:103], v[68:71], v[32:35]\n" \
"s_waitcnt lgkmcnt(3)\nv_mfma_f32_16x16x32_bf16 v[36:39], v[76:79], v[68:71], v[36:39]\ns_waitcnt lgkmcnt(2)\nds_read_b128 v[76:79], v1 offset:6144\nv_mfma_f32_16x16x32_bf16 v[24:27], a[104:107], v[56:59], v[24:27]\nds_read_b128 v[68:71], v2 offset:22528\nv_mfma_f32_16x16x32_bf16 v[28:31], a[112:115], v[56:59], v[28:31]\nbuffer_load_dwordx4 v0, %[rs], s60 offen offset:2048 sc1 lds\n" \
"v_mfma_f32_16x16x32_bf16 v[32:35], a[120:123], v[56:59], v[32:35]\nv_mfma_f32_16x16x32_bf16 v[36:39], a[128:131], v[56:59], v[36:39]\ns_waitcnt lgkmcnt(3)\nds_read_b128 v[80:83], v1 offset:7168\nv_mfma_f32_16x16x32_bf16 v[24:27], a[108:111], v[60:63], v[24:27]\nds_read_b128 v[56:59], v2 offset:24576\nv_mfma_f32_16x16x32_bf16 v[28:31], a[116:119], v[60:63], v[28:31]\nbuffer_load_dwordx4 v0, %[rs], s60 offen offset:3072 sc1 lds\n" \
"v_mfma_f32_16x16x32_bf16 v[32:35], a[124:127], v[60:63], v[32:35]\nv_mfma_f32_16x16x32_bf16 v[36:39], a[132:135], v[60:63], v[36:39]\ns_waitcnt lgkmcnt(4)\nds_read_b128 v[72:75], v1 offset:8192\nv_mfma_f32_16x16x32_bf16 v[24:27], a[136:139], v[64:67], v[24:27]\ns_waitcnt vmcnt(8)\ns_waitcnt lgkmcnt(4)\nds_read_b128 v[60:63], v2 offset:26624\n" \
"v_add_f32 v84, v40, v44\nv_mfma_f32_16x16x32_bf16 v[28:31], v[76:79], v[64:67], v[28:31]\nv_add_f32 v85, v48, v52\nv_add_f32 v84, v84, v85\nv_mfma_f32_16x16x32_bf16 v[32:35], a[144:147], v[64:67], v[32:35]\nv_mov_b32 v85, v84\nv_mfma_f32_16x16x32_bf16 v[36:39], a[152:155], v[64:67], v[36:39]\ns_waitcnt lgkmcnt(4)\n" \
"ds_read_b128 v[76:79], v1 offset:9216\nv_mfma_f32_16x16x32_bf16 v[24:27], a[140:143], v[68:71], v[24:27]\ns_waitcnt lgkmcnt(4)\nds_read_b128 v[64:67], v2 offset:28672\nv_mfma_f32_16x16x32_bf16 v[28:31], v[80:83], v[68:71], v[28:31]\nv_permlane16_swap_b32 v84, v85\nv_mfma_f32_16x16x32_bf16 v[32:35], a[148:151], v[68:71], v[32:35]\nv_add_f32 v84, v84, v85\n" \
"v_mov_b32 v85, v84\nv_mfma_f32_16x16x32_bf16 v[36:39], a[156:159], v[68:71], v[36:39]\ns_waitcnt lgkmcnt(4)\nds_read_b128 v[80:83], v1 offset:10240\nv_mfma_f32_16x16x32_bf16 v[24:27], a[160:163], v[56:59], v[24:27]\nds_read_b128 v[68:71], v2 offset:30720\nv_mfma_f32_16x16x32_bf16 v[28:31], a[168:171], v[56:59], v[28:31]\ns_waitcnt lgkmcnt(5)\n" \
"v_permlane32_swap_b32 v84, v85\nv_mfma_f32_16x16x32_bf16 v[32:35], v[72:75], v[56:59], v[32:35]\nv_add_f32 v84, v84, v85\nv_mul_f32 v84, 0xbd800000, v84\nv_mfma_f32_16x16x32_bf16 v[36:39], a[176:179], v[56:59], v[36:39]\ns_waitcnt lgkmcnt(4)\nds_read_b128 v[72:75], v1 offset:11264\nv_add_f32 v40, v40, v84\n" \
"v_mfma_f32_16x16x32_bf16 v[24:27], a[164:167], v[60:63], v[24:27]\ns_waitcnt vmcnt(4)\ns_barrier\nds_read_b128 v[56:59], v2 offset:32768\nv_mfma_f32_16x16x32_bf16 v[28:31], a[172:175], v[60:63], v[28:31]\ns_waitcnt lgkmcnt(5)\nv_mul_f32 v40, 0x41000000, v40\nv_fma_f32 v41, v40, v40, v41\n" \
"v_mfma_f32_16x16x32_bf16 v[32:35], v[76:79], v[60:63], v[32:35]\nv_add_f32 v44, v44, v84\nv_mul_f32 v44, 0x41000000, v44\nv_mfma_f32_16x16x32_bf16 v[36:39], a[180:183], v[60:63], v[36:39]\ns_waitcnt lgkmcnt(4)\nv_fma_f32 v45, v44, v44, v45\nv_add_f32 v48, v48, v84\nv_mfma_f32_16x16x32_bf16 v[24:27], a[184:187], v[64:67], v[24:27]\n" \
"ds_read_b128 v[60:63], v2 offset:34816\nv_mul_f32 v48, 0x41000000, v48\nv_mfma_f32_16x16x32_bf16 v[28:31], a[192:195], v[64:67], v[28:31]\nv_fma_f32 v49, v48, v48, v49\nv_add_f32 v52, v52, v84\nv_mfma_f32_16x16x32_bf16 v[32:35], a[200:203], v[64:67], v[32:35]\ns_waitcnt lgkmcnt(4)\nv_mul_f32 v52, 0x41000000, v52\n" \
"v_fma_f32 v53, v52, v52, v53\nv_mfma_f32_16x16x32_bf16 v[36:39], v[80:83], v[64:67], v[36:39]\ns_waitcnt lgkmcnt(3)\nv_add_f32 v85, v41, v45\nv_add_f32 v86, v49, v53\nv_mfma_f32_16x16x32_bf16 v[24:27], a[188:191], v[68:71], v[24:27]\nds_read_b128 v[64:67], v2 offset:36864\nv_add_f32 v85, v85, v86\n" \
"v_mfma_f32_16x16x32_bf16 v[28:31], a[196:199], v[68:71], v[28:31]\nv_mov_b32 v86, v85\nv_mfma_f32_16x16x32_bf16 v[32:35], a[204:207], v[68:71], v[32:35]\ns_waitcnt lgkmcnt(3)\nv_permlane16_swap_b32 v85, v86\nv_mfma_f32_16x16x32_bf16 v[36:39], v[72:75], v[68:71], v[36:39]\ns_waitcnt lgkmcnt(2)\nds_read_b128 v[72:75], v1 offset:12288\n" \
"v_add_f32 v85, v85, v86\nv_mfma_f32_16x16x32_bf16 v[24:27], a[208:211], v[56:59], v[24:27]\nds_read_b128 v[68:71], v2 offset:38912\nv_mov_b32 v86, v85\nv_mfma_f32_16x16x32_bf16 v[28:31], a[216:219], v[56:59], v[28:31]\ns_nop 0\nv_permlane32_swap_b32 v85, v86\nv_mfma_f32_16x16x32_bf16 v[32:35], a[224:227], v[56:59], v[32:35]\n" \
"v_add_f32 v85, v85, v86\nv_mul_f32 v85, 0x3a800000, v85\nv_mfma_f32_16x16x32_bf16 v[36:39], a[232:235], v[56:59], v[36:39]\ns_waitcnt lgkmcnt(3)\nds_read_b128 v[76:79], v1 offset:13312\nv_cvt_pk_bf16_f32 v85, v85, v85\nv_mfma_f32_16x16x32_bf16 v[24:27], a[212:215], v[60:63], v[24:27]\nds_read_b128 v[56:59], v2 offset:40960\n" \
"v_and_b32 v85, 0xffff0000, v85\nv_mfma_f32_16x16x32_bf16 v[28:31], a[220:223], v[60:63], v[28:31]\nv_add_f32 v85, 0x3727c5ac, v85\nv_cvt_pk_bf16_f32 v85, v85, v85\nv_mfma_f32_16x16x32_bf16 v[32:35], a[228:231], v[60:63], v[32:35]\nv_and_b32 v85, 0xffff0000, v85\nv_cvt_pk_bf16_f32 v84, v84, v84\nv_mfma_f32_16x16x32_bf16 v[36:39], a[236:239], v[60:63], v[36:39]\n" \
"s_waitcnt lgkmcnt(4)\nds_read_b128 v[80:83], v1 offset:14336\nv_and_b32 v84, 0xffff0000, v84\nv_mfma_f32_16x16x32_bf16 v[24:27], a[240:243], v[64:67], v[24:27]\ns_waitcnt lgkmcnt(4)\nds_read_b128 v[60:63], v2 offset:43008\nv_mfma_f32_16x16x32_bf16 v[28:31], v[72:75], v[64:67], v[28:31]\nv_sqrt_f32 v85, v85\n" \
"v_mfma_f32_16x16x32_bf16 v[32:35], a[248:251], v[64:67], v[32:35]\nv_cvt_pk_bf16_f32 v85, v85, v85\nv_and_b32 v85, 0xffff0000, v85\nv_mfma_f32_16x16x32_bf16 v[36:39], v[96:99], v[64:67], v[36:39]\ns_waitcnt lgkmcnt(4)\nds_read_b128 v[72:75], v1 offset:15360\nv_mul_f32 v86, s58, v85\nv_mfma_f32_16x16x32_bf16 v[24:27], a[244:247], v[68:71], v[24:27]\n" \
"s_waitcnt lgkmcnt(4)\nds_read_b128 v[64:67], v2 offset:45056\nv_add_f32 v87, v42, v46\nv_mfma_f32_16x16x32_bf16 v[28:31], v[76:79], v[68:71], v[28:31]\nv_rcp_f32 v85, v85\nv_mfma_f32_16x16x32_bf16 v[32:35], a[252:255], v[68:71], v[32:35]\nv_mul_f32 v85, s58, v85\nv_add_f32 v88, v50, v54\n" \
"v_mfma_f32_16x16x32_bf16 v[36:39], v[100:103], v[68:71], v[36:39]\ns_waitcnt lgkmcnt(4)\nds_read_b128 v[76:79], v1 offset:16384\nv_add_f32 v87, v87, v88\nv_mfma_f32_16x16x32_bf16 v[24:27], v[104:107], v[56:59], v[24:27]\nds_read_b128 v[68:71], v2 offset:47104\nv_mov_b32 v88, v87\nv_mfma_f32_16x16x32_bf16 v[28:31], v[112:115], v[56:59], v[28:31]\n" \
"s_waitcnt lgkmcnt(5)\nv_permlane16_swap_b32 v87, v88\nv_mfma_f32_16x16x32_bf16 v[32:35], v[80:83], v[56:59], v[32:35]\nv_add_f32 v87, v87, v88\nv_mov_b32 v88, v87\nv_mfma_f32_16x16x32_bf16 v[36:39], v[120:123], v[56:59], v[36:39]\ns_waitcnt lgkmcnt(4)\nds_read_b128 v[80:83], v1 offset:17408\n" \
"v_mfma_f32_16x16x32_bf16 v[24:27], v[108:111], v[60:63], v[24:27]\ns_waitcnt vmcnt(0)\ns_barrier\nds_read_b128 v[56:59], v2 offset:49152\nv_mfma_f32_16x16x32_bf16 v[28:31], v[116:119], v[60:63], v[28:31]\ns_waitcnt lgkmcnt(5)\nv_permlane32_swap_b32 v87, v88\nv_mfma_f32_16x16x32_bf16 v[32:35], v[72:75], v[60:63], v[32:35]\n" \
"v_add_f32 v87, v87, v88\nv_mul_f32 v87, 0xbd800000, v87\nv_mfma_f32_16x16x32_bf16 v[36:39], v[124:127], v[60:63], v[36:39]\ns_waitcnt lgkmcnt(4)\nv_add_f32 v42, v42, v87\nv_mul_f32 v42, 0x41000000, v42\nv_mfma_f32_16x16x32_bf16 v[24:27], v[128:131], v[64:67], v[24:27]\nds_read_b128 v[60:63], v2 offset:51200\n" \
"v_fma_f32 v43, v42, v42, v43\nv_mfma_f32_16x16x32_bf16 v[28:31], v[136:139], v[64:67], v[28:31]\nv_add_f32 v46, v46, v87\nv_mul_f32 v46, 0x41000000, v46\nv_mfma_f32_16x16x32_bf16 v[32:35], v[144:147], v[64:67], v[32:35]\ns_waitcnt lgkmcnt(4)\nv_fma_f32 v47, v46, v46, v47\nv_add_f32 v50, v50, v87\n" \
"v_mfma_f32_16x16x32_bf16 v[36:39], v[76:79], v[64:67], v[36:39]\ns_waitcnt lgkmcnt(3)\nv_mul_f32 v50, 0x41000000, v50\nv_fma_f32 v51, v50, v50, v51\nv_mfma_f32_16x16x32_bf16 v[24:27], v[132:135], v[68:71], v[24:27]\nds_read_b128 v[64:67], v2 offset:53248\nv_add_f32 v54, v54, v87\nv_mfma_f32_16x16x32_bf16 v[28:31], v[140:143], v[68:71], v[28:31]\n" \
"v_mul_f32 v54, 0x41000000, v54\nv_fma_f32 v55, v54, v54, v55\nv_mfma_f32_16x16x32_bf16 v[32:35], v[148:151], v[68:71], v[32:35]\ns_waitcnt lgkmcnt(3)\nv_add_f32 v88, v43, v47\nv_add_f32 v89, v51, v55\nv_mfma_f32_16x16x32_bf16 v[36:39], v[80:83], v[68:71], v[36:39]\ns_waitcnt lgkmcnt(2)\n" \
"ds_read_b128 v[80:83], v1 offset:18432\nv_add_f32 v88, v88, v89\nv_mfma_f32_16x16x32_bf16 v[24:27], v[152:155], v[56:59], v[24:27]\nds_read_b128 v[68:71], v2 offset:55296\nv_mov_b32 v89, v88\nv_mfma_f32_16x16x32_bf16 v[28:31], v[160:163], v[56:59], v[28:31]\ns_nop 0\nv_permlane16_swap_b32 v88, v89\n" \
"v_mfma_f32_16x16x32_bf16 v[32:35], v[168:171], v[56:59], v[32:35]\nv_add_f32 v88, v88, v89\nv_mov_b32 v89, v88\nv_mfma_f32_16x16x32_bf16 v[36:39], v[176:179], v[56:59], v[36:39]\ns_waitcnt lgkmcnt(3)\nds_read_b128 v[72:75], v1 offset:19456\nv_mfma_f32_16x16x32_bf16 v[24:27], v[156:159], v[60:63], v[24:27]\nds_read_b128 v[56:59], v2 offset:57344\n" \
"v_mfma_f32_16x16x32_bf16 v[28:31], v[164:167], v[60:63], v[28:31]\nv_permlane32_swap_b32 v88, v89\nv_mfma_f32_16x16x32_bf16 v[32:35], v[172:175], v[60:63], v[32:35]\nv_add_f32 v88, v88, v89\nv_mul_f32 v88, 0x3a800000, v88\nv_mfma_f32_16x16x32_bf16 v[36:39], v[180:183], v[60:63], v[36:39]\ns_waitcnt lgkmcnt(4)\nds_read_b128 v[76:79], v1 offset:20480\n" \
"v_cvt_pk_bf16_f32 v88, v88, v88\nv_mfma_f32_16x16x32_bf16 v[24:27], v[184:187], v[64:67], v[24:27]\ns_waitcnt lgkmcnt(4)\nds_read_b128 v[60:63], v2 offset:59392\nv_and_b32 v88, 0xffff0000, v88\nv_mfma_f32_16x16x32_bf16 v[28:31], v[80:83], v[64:67], v[28:31]\nv_add_f32 v88, 0x3727c5ac, v88\nv_cvt_pk_bf16_f32 v88, v88, v88\n" \
"v_mfma_f32_16x16x32_bf16 v[32:35], v[192:195], v[64:67], v[32:35]\nv_and_b32 v88, 0xffff0000, v88\nv_cvt_pk_bf16_f32 v87, v87, v87\nv_mfma_f32_16x16x32_bf16 v[36:39], v[200:203], v[64:67], v[36:39]\ns_waitcnt lgkmcnt(4)\nds_read_b128 v[80:83], v1 offset:21504\nv_and_b32 v87, 0xffff0000, v87\nv_mfma_f32_16x16x32_bf16 v[24:27], v[188:191], v[68:71], v[24:27]\n" \
"s_waitcnt lgkmcnt(4)\nds_read_b128 v[64:67], v2 offset:61440\nv_mfma_f32_16x16x32_bf16 v[28:31], v[72:75], v[68:71], v[28:31]\nv_sqrt_f32 v88, v88\nv_mfma_f32_16x16x32_bf16 v[32:35], v[196:199], v[68:71], v[32:35]\nv_cvt_pk_bf16_f32 v88, v88, v88\nv_and_b32 v88, 0xffff0000, v88\nv_mfma_f32_16x16x32_bf16 v[36:39], v[204:207], v[68:71], v[36:39]\n" \
"s_waitcnt lgkmcnt(4)\nds_read_b128 v[72:75], v1 offset:22528\nv_mul_f32 v89, s58, v88\nv_mfma_f32_16x16x32_bf16 v[24:27], v[208:211], v[56:59], v[24:27]\nds_read_b128 v[68:71], v2 offset:63488\nv_mfma_f32_16x16x32_bf16 v[28:31], v[216:219], v[56:59], v[28:31]\ns_waitcnt lgkmcnt(5)\nv_rcp_f32 v88, v88\n" \
"v_mfma_f32_16x16x32_bf16 v[32:35], v[76:79], v[56:59], v[32:35]\nv_mul_f32 v88, s58, v88\nv_mfma_f32_16x16x32_bf16 v[36:39], v[224:227], v[56:59], v[36:39]\ns_waitcnt lgkmcnt(4)\nds_read_b128 v[76:79], v1 offset:23552\nv_mfma_f32_16x16x32_bf16 v[24:27], v[212:215], v[60:63], v[24:27]\nds_read_b128 v[56:59], v2 offset:256\nv_mfma_f32_16x16x32_bf16 v[28:31], v[220:223], v[60:63], v[28:31]\n" \
"s_waitcnt lgkmcnt(5)\nv_mfma_f32_16x16x32_bf16 v[32:35], v[80:83], v[60:63], v[32:35]\nv_mfma_f32_16x16x32_bf16 v[36:39], v[228:231], v[60:63], v[36:39]\ns_waitcnt lgkmcnt(4)\nv_mfma_f32_16x16x32_bf16 v[24:27], v[232:235], v[64:67], v[24:27]\nds_read_b128 v[60:63], v2 offset:2304\nv_mfma_f32_16x16x32_bf16 v[28:31], v[240:243], v[64:67], v[28:31]\nv_mfma_f32_16x16x32_bf16 v[32:35], v[248:251], v[64:67], v[32:35]\n" \
"s_waitcnt lgkmcnt(4)\nv_mfma_f32_16x16x32_bf16 v[36:39], v[72:75], v[64:67], v[36:39]\ns_waitcnt lgkmcnt(3)\nv_mfma_f32_16x16x32_bf16 v[24:27], v[236:239], v[68:71], v[24:27]\nds_read_b128 v[64:67], v2 offset:4352\nv_mfma_f32_16x16x32_bf16 v[28:31], v[244:247], v[68:71], v[28:31]\nv_mfma_f32_16x16x32_bf16 v[32:35], v[252:255], v[68:71], v[32:35]\ns_waitcnt lgkmcnt(3)\n" \
"v_mfma_f32_16x16x32_bf16 v[36:39], v[76:79], v[68:71], v[36:39]\ns_waitcnt lgkmcnt(2)\nds_read_b128 v[76:79], v1 offset:0\nv_mfma_f32_16x16x32_bf16 v[40:43], a[0:3], v[56:59], 0\nds_read_b128 v[68:71], v2 offset:6400\nv_mfma_f32_16x16x32_bf16 v[44:47], a[8:11], v[56:59], 0\ns_lshl_b32 s44, s40, 1\nv_mov_b32 v90, s44\n" \
"v_mfma_f32_16x16x32_bf16 v[48:51], a[16:19], v[56:59], 0\nv_mfma_f32_16x16x32_bf16 v[52:55], a[24:27], v[56:59], 0\ns_waitcnt lgkmcnt(3)\nds_read_b128 v[80:83], v1 offset:1024\nv_fma_f32 v24, v8, v84, v24\nv_mfma_f32_16x16x32_bf16 v[40:43], a[4:7], v[60:63], v[40:43]\nds_read_b128 v[56:59], v2 offset:8448\nv_fma_f32 v25, v9, v84, v25\n" \
"v_mfma_f32_16x16x32_bf16 v[44:47], a[12:15], v[60:63], v[44:47]\nv_fma_f32 v32, v16, v84, v32\nv_fma_f32 v33, v17, v84, v33\nv_mfma_f32_16x16x32_bf16 v[48:51], a[20:23], v[60:63], v[48:51]\nv_mul_f32 v24, v24, v85\nv_mul_f32 v25, v25, v85\nv_mfma_f32_16x16x32_bf16 v[52:55], a[28:31], v[60:63], v[52:55]\ns_waitcnt lgkmcnt(4)\n" \
"ds_read_b128 v[72:75], v1 offset:2048\nv_fma_f32 v91, v24, v24, s59\nv_mfma_f32_16x16x32_bf16 v[40:43], a[32:35], v[64:67], v[40:43]\ns_waitcnt lgkmcnt(4)\nds_read_b128 v[60:63], v2 offset:10496\nv_fma_f32 v92, v25, v25, s59\nv_mfma_f32_16x16x32_bf16 v[44:47], v[76:79], v[64:67], v[44:47]\nv_mul_f32 v91, v91, v24\n" \
"v_mul_f32 v92, v92, v25\nv_mfma_f32_16x16x32_bf16 v[48:51], a[40:43], v[64:67], v[48:51]\nv_exp_f32 v91, v91\nv_mfma_f32_16x16x32_bf16 v[52:55], a[48:51], v[64:67], v[52:55]\ns_waitcnt lgkmcnt(4)\nds_read_b128 v[76:79], v1 offset:3072\nv_mul_f32 v24, v24, v32\nv_mfma_f32_16x16x32_bf16 v[40:43], a[36:39], v[68:71], v[40:43]\n" \
"s_waitcnt lgkmcnt(4)\nds_read_b128 v[64:67], v2 offset:12544\nv_mul_f32 v25, v25, v33\nv_mfma_f32_16x16x32_bf16 v[44:47], v[80:83], v[68:71], v[44:47]\nv_exp_f32 v92, v92\nv_mfma_f32_16x16x32_bf16 v[48:51], a[44:47], v[68:71], v[48:51]\nv_fma_f32 v91, v91, v86, v86\nv_fma_f32 v92, v92, v86, v86\n" \
"v_mfma_f32_16x16x32_bf16 v[52:55], a[52:55], v[68:71], v[52:55]\ns_waitcnt lgkmcnt(4)\nds_read_b128 v[80:83], v1 offset:4096\nv_fma_f32 v26, v10, v84, v26\nv_mfma_f32_16x16x32_bf16 v[40:43], a[56:59], v[56:59], v[40:43]\nds_read_b128 v[68:71], v2 offset:14592\nv_fma_f32 v27, v11, v84, v27\nv_mfma_f32_16x16x32_bf16 v[44:47], a[64:67], v[56:59], v[44:47]\n" \
"s_waitcnt lgkmcnt(5)\nv_rcp_f32 v91, v91\nv_mfma_f32_16x16x32_bf16 v[48:51], v[72:75], v[56:59], v[48:51]\nv_rcp_f32 v92, v92\nv_mfma_f32_16x16x32_bf16 v[52:55], a[72:75], v[56:59], v[52:55]\ns_waitcnt lgkmcnt(4)\nds_read_b128 v[72:75], v1 offset:5120\nv_mul_f32 v24, v24, v91\n" \
"v_mfma_f32_16x16x32_bf16 v[40:43], a[60:63], v[60:63], v[40:43]\nds_read_b128 v[56:59], v2 offset:16640\nv_mul_f32 v25, v25, v92\nv_mfma_f32_16x16x32_bf16 v[44:47], a[68:71], v[60:63], v[44:47]\ns_waitcnt lgkmcnt(5)\nv_cvt_pk_bf16_f32 v32, v24, v25\nv_fma_f32 v34, v18, v84, v34\nv_mfma_f32_16x16x32_bf16 v[48:51], v[76:79], v[60:63], v[48:51]\n" \
"v_fma_f32 v35, v19, v84, v35\nv_mul_f32 v26, v26, v85\nv_mfma_f32_16x16x32_bf16 v[52:55], a[76:79], v[60:63], v[52:55]\ns_waitcnt lgkmcnt(4)\nv_mul_f32 v27, v27, v85\nv_fma_f32 v91, v26, v26, s59\nv_mfma_f32_16x16x32_bf16 v[40:43], a[80:83], v[64:67], v[40:43]\nds_read_b128 v[60:63], v2 offset:18688\n" \
"v_fma_f32 v92, v27, v27, s59\nv_mfma_f32_16x16x32_bf16 v[44:47], a[88:91], v[64:67], v[44:47]\nv_mul_f32 v91, v91, v26\nv_mul_f32 v92, v92, v27\nv_mfma_f32_16x16x32_bf16 v[48:51], a[96:99], v[64:67], v[48:51]\ns_waitcnt lgkmcnt(4)\nv_exp_f32 v91, v91\nv_mfma_f32_16x16x32_bf16 v[52:55], v[80:83], v[64:67], v[52:55]\n" \
"s_waitcnt lgkmcnt(3)\nv_exp_f32 v92, v92\nv_mfma_f32_16x16x32_bf16 v[40:43], a[84:87], v[68:71], v[40:43]\nds_read_b128 v[64:67], v2 offset:20736\nv_mul_f32 v26, v26, v34\nv_mfma_f32_16x16x32_bf16 v[44:47], a[92:95], v[68:71], v[44:47]\nv_mul_f32 v27, v27, v35\nv_fma_f32 v91, v91, v86, v86\n" \
"v_mfma_f32_16x16x32_bf16 v[48:51], a[100:103], v[68:71], v[48:51]\ns_waitcnt lgkmcnt(3)\nv_fma_f32 v92, v92, v86, v86\nv_fma_f32 v28, v12, v84, v28\nv_mfma_f32_16x16x32_bf16 v[52:55], v[72:75], v[68:71], v[52:55]\ns_waitcnt lgkmcnt(2)\nds_read_b128 v[72:75], v1 offset:6144\nv_fma_f32 v29, v13, v84, v29\n" \
"v_mfma_f32_16x16x32_bf16 v[40:43], a[104:107], v[56:59], v[40:43]\nds_read_b128 v[68:71], v2 offset:22784\nv_fma_f32 v36, v20, v84, v36\nv_mfma_f32_16x16x32_bf16 v[44:47], a[112:115], v[56:59], v[44:47]\nv_rcp_f32 v91, v91\nv_mfma_f32_16x16x32_bf16 v[48:51], a[120:123], v[56:59], v[48:51]\nv_rcp_f32 v92, v92\nv_mfma_f32_16x16x32_bf16 v[52:55], a[128:131], v[56:59], v[52:55]\n" \
"s_waitcnt lgkmcnt(3)\nds_read_b128 v[76:79], v1 offset:7168\nv_mul_f32 v26, v26, v91\nv_mfma_f32_16x16x32_bf16 v[40:43], a[108:111], v[60:63], v[40:43]\nds_read_b128 v[56:59], v2 offset:24832\nv_mul_f32 v27, v27, v92\nv_mfma_f32_16x16x32_bf16 v[44:47], a[116:119], v[60:63], v[44:47]\nv_cvt_pk_bf16_f32 v33, v26, v27\n" \
"v_fma_f32 v37, v21, v84, v37\nv_mfma_f32_16x16x32_bf16 v[48:51], a[124:127], v[60:63], v[48:51]\nv_mul_f32 v28, v28, v85\nv_mul_f32 v29, v29, v85\nv_mfma_f32_16x16x32_bf16 v[52:55], a[132:135], v[60:63], v[52:55]\ns_waitcnt lgkmcnt(4)\nds_read_b128 v[80:83], v1 offset:8192\nv_fma_f32 v91, v28, v28, s59\n" \
"v_mfma_f32_16x16x32_bf16 v[40:43], a[136:139], v[64:67], v[40:43]\ns_waitcnt lgkmcnt(4)\nds_read_b128 v[60:63], v2 offset:26880\nv_fma_f32 v92, v29, v29, s59\nv_mfma_f32_16x16x32_bf16 v[44:47], v[72:75], v[64:67], v[44:47]\nv_mul_f32 v91, v91, v28\nv_mul_f32 v92, v92, v29\nv_mfma_f32_16x16x32_bf16 v[48:51], a[144:147], v[64:67], v[48:51]\n" \
"v_exp_f32 v91, v91\nv_mfma_f32_16x16x32_bf16 v[52:55], a[152:155], v[64:67], v[52:55]\ns_waitcnt lgkmcnt(4)\nds_read_b128 v[72:75], v1 offset:9216\nv_mul_f32 v28, v28, v36\nv_mfma_f32_16x16x32_bf16 v[40:43], a[140:143], v[68:71], v[40:43]\ns_waitcnt lgkmcnt(4)\nds_read_b128 v[64:67], v2 offset:28928\n" \
"v_mul_f32 v29, v29, v37\nv_mfma_f32_16x16x32_bf16 v[44:47], v[76:79], v[68:71], v[44:47]\nv_exp_f32 v92, v92\nv_mfma_f32_16x16x32_bf16 v[48:51], a[148:151], v[68:71], v[48:51]\nv_fma_f32 v91, v91, v86, v86\nv_fma_f32 v92, v92, v86, v86\nv_mfma_f32_16x16x32_bf16 v[52:55], a[156:159], v[68:71], v[52:55]\ns_waitcnt lgkmcnt(4)\n" \
"ds_read_b128 v[76:79], v1 offset:10240\nv_fma_f32 v30, v14, v84, v30\nv_mfma_f32_16x16x32_bf16 v[40:43], a[160:163], v[56:59], v[40:43]\nds_read_b128 v[68:71], v2 offset:30976\nv_fma_f32 v31, v15, v84, v31\nv_mfma_f32_16x16x32_bf16 v[44:47], a[168:171], v[56:59], v[44:47]\ns_waitcnt lgkmcnt(5)\nv_rcp_f32 v91, v91\n" \
"v_mfma_f32_16x16x32_bf16 v[48:51], v[80:83], v[56:59], v[48:51]\nv_rcp_f32 v92, v92\nv_mfma_f32_16x16x32_bf16 v[52:55], a[176:179], v[56:59], v[52:55]\ns_waitcnt lgkmcnt(4)\nds_read_b128 v[80:83], v1 offset:11264\nv_mul_f32 v28, v28, v91\nv_mfma_f32_16x16x32_bf16 v[40:43], a[164:167], v[60:63], v[40:43]\nds_read_b128 v[56:59], v2 offset:33024\n" \
"v_mul_f32 v29, v29, v92\nv_mfma_f32_16x16x32_bf16 v[44:47], a[172:175], v[60:63], v[44:47]\ns_waitcnt lgkmcnt(5)\nv_cvt_pk_bf16_f32 v34, v28, v29\nv_fma_f32 v38, v22, v84, v38\nv_mfma_f32_16x16x32_bf16 v[48:51], v[72:75], v[60:63], v[48:51]\nv_fma_f32 v39, v23, v84, v39\nv_mul_f32 v30, v30, v85\n" \
"v_mfma_f32_16x16x32_bf16 v[52:55], a[180:183], v[60:63], v[52:55]\ns_waitcnt lgkmcnt(4)\nv_mul_f32 v31, v31, v85\nv_fma_f32 v91, v30, v30, s59\nv_mfma_f32_16x16x32_bf16 v[40:43], a[184:187], v[64:67], v[40:43]\nds_read_b128 v[60:63], v2 offset:35072\nv_fma_f32 v92, v31, v31, s59\nv_mfma_f32_16x16x32_bf16 v[44:47], a[192:195], v[64:67], v[44:47]\n" \
"v_mul_f32 v91, v91, v30\nv_mul_f32 v92, v92, v31\nv_mfma_f32_16x16x32_bf16 v[48:51], a[200:203], v[64:67], v[48:51]\ns_waitcnt lgkmcnt(4)\nv_exp_f32 v91, v91\nv_mfma_f32_16x16x32_bf16 v[52:55], v[76:79], v[64:67], v[52:55]\ns_waitcnt lgkmcnt(3)\nv_exp_f32 v92, v92\n" \
"v_mfma_f32_16x16x32_bf16 v[40:43], a[188:191], v[68:71], v[40:43]\nds_read_b128 v[64:67], v2 offset:37120\nv_mul_f32 v30, v30, v38\nv_mfma_f32_16x16x32_bf16 v[44:47], a[196:199], v[68:71], v[44:47]\nv_mul_f32 v31, v31, v39\nv_fma_f32 v91, v91, v86, v86\nv_mfma_f32_16x16x32_bf16 v[48:51], a[204:207], v[68:71], v[48:51]\ns_waitcnt lgkmcnt(3)\n" \
"v_fma_f32 v92, v92, v86, v86\nv_mfma_f32_16x16x32_bf16 v[52:55], v[80:83], v[68:71], v[52:55]\ns_waitcnt lgkmcnt(2)\nds_read_b128 v[80:83], v1 offset:12288\nv_mfma_f32_16x16x32_bf16 v[40:43], a[208:211], v[56:59], v[40:43]\nds_read_b128 v[68:71], v2 offset:39168\nv_mfma_f32_16x16x32_bf16 v[44:47], a[216:219], v[56:59], v[44:47]\nv_rcp_f32 v91, v91\n" \
"v_mfma_f32_16x16x32_bf16 v[48:51], a[224:227], v[56:59], v[48:51]\nv_rcp_f32 v92, v92\nv_mfma_f32_16x16x32_bf16 v[52:55], a[232:235], v[56:59], v[52:55]\ns_waitcnt lgkmcnt(3)\nds_read_b128 v[72:75], v1 offset:13312\nv_mul_f32 v30, v30, v91\nv_mfma_f32_16x16x32_bf16 v[40:43], a[212:215], v[60:63], v[40:43]\nds_read_b128 v[56:59], v2 offset:41216\n" \
"v_mul_f32 v31, v31, v92\nv_mfma_f32_16x16x32_bf16 v[44:47], a[220:223], v[60:63], v[44:47]\nv_cvt_pk_bf16_f32 v35, v30, v31\nv_mfma_f32_16x16x32_bf16 v[48:51], a[228:231], v[60:63], v[48:51]\nbuffer_store_dwordx4 v[32:35], v4, %[rs], s55 offen offset:0 sc1\nv_mfma_f32_16x16x32_bf16 v[52:55], a[236:239], v[60:63], v[52:55]\ns_waitcnt lgkmcnt(4)\nds_read_b128 v[76:79], v1 offset:14336\n" \
"v_mfma_f32_16x16x32_bf16 v[40:43], a[240:243], v[64:67], v[40:43]\ns_waitcnt lgkmcnt(4)\nds_read_b128 v[60:63], v2 offset:43264\nv_mfma_f32_16x16x32_bf16 v[44:47], v[80:83], v[64:67], v[44:47]\nv_mfma_f32_16x16x32_bf16 v[48:51], a[248:251], v[64:67], v[48:51]\nv_mfma_f32_16x16x32_bf16 v[52:55], v[96:99], v[64:67], v[52:55]\ns_waitcnt lgkmcnt(4)\nds_read_b128 v[80:83], v1 offset:15360\n" \
"v_mfma_f32_16x16x32_bf16 v[40:43], a[244:247], v[68:71], v[40:43]\ns_waitcnt lgkmcnt(4)\nds_read_b128 v[64:67], v2 offset:45312\nv_mfma_f32_16x16x32_bf16 v[44:47], v[72:75], v[68:71], v[44:47]\nv_mfma_f32_16x16x32_bf16 v[48:51], a[252:255], v[68:71], v[48:51]\nv_mfma_f32_16x16x32_bf16 v[52:55], v[100:103], v[68:71], v[52:55]\ns_waitcnt lgkmcnt(4)\nds_read_b128 v[72:75], v1 offset:16384\n" \
"v_mfma_f32_16x16x32_bf16 v[40:43], v[104:107], v[56:59], v[40:43]\nds_read_b128 v[68:71], v2 offset:47360\nv_mfma_f32_16x16x32_bf16 v[44:47], v[112:115], v[56:59], v[44:47]\ns_waitcnt lgkmcnt(5)\nv_mfma_f32_16x16x32_bf16 v[48:51], v[76:79], v[56:59], v[48:51]\nv_mfma_f32_16x16x32_bf16 v[52:55], v[120:123], v[56:59], v[52:55]\ns_waitcnt lgkmcnt(4)\nds_read_b128 v[76:79], v1 offset:17408\n" \
"v_mfma_f32_16x16x32_bf16 v[40:43], v[108:111], v[60:63], v[40:43]\nds_read_b128 v[56:59], v2 offset:49408\nv_mfma_f32_16x16x32_bf16 v[44:47], v[116:119], v[60:63], v[44:47]\ns_waitcnt lgkmcnt(5)\nv_mfma_f32_16x16x32_bf16 v[48:51], v[80:83], v[60:63], v[48:51]\nv_mfma_f32_16x16x32_bf16 v[52:55], v[124:127], v[60:63], v[52:55]\ns_waitcnt lgkmcnt(4)\nv_mfma_f32_16x16x32_bf16 v[40:43], v[128:131], v[64:67], v[40:43]\n" \
"ds_read_b128 v[60:63], v2 offset:51456\nv_mfma_f32_16x16x32_bf16 v[44:47], v[136:139], v[64:67], v[44:47]\nv_mfma_f32_16x16x32_bf16 v[48:51], v[144:147], v[64:67], v[48:51]\ns_waitcnt lgkmcnt(4)\nv_mfma_f32_16x16x32_bf16 v[52:55], v[72:75], v[64:67], v[52:55]\ns_waitcnt lgkmcnt(3)\nv_mfma_f32_16x16x32_bf16 v[40:43], v[132:135], v[68:71], v[40:43]\nds_read_b128 v[64:67], v2 offset:53504\n" \
"v_mfma_f32_16x16x32_bf16 v[44:47], v[140:143], v[68:71], v[44:47]\nv_mfma_f32_16x16x32_bf16 v[48:51], v[148:151], v[68:71], v[48:51]\ns_waitcnt lgkmcnt(3)\nv_mfma_f32_16x16x32_bf16 v[52:55], v[76:79], v[68:71], v[52:55]\ns_waitcnt lgkmcnt(2)\nds_read_b128 v[76:79], v1 offset:18432\nv_mfma_f32_16x16x32_bf16 v[40:43], v[152:155], v[56:59], v[40:43]\nds_read_b128 v[68:71], v2 offset:55552\n" \
"v_mfma_f32_16x16x32_bf16 v[44:47], v[160:163], v[56:59], v[44:47]\nv_mfma_f32_16x16x32_bf16 v[48:51], v[168:171], v[56:59], v[48:51]\nv_mfma_f32_16x16x32_bf16 v[52:55], v[176:179], v[56:59], v[52:55]\ns_waitcnt lgkmcnt(3)\nds_read_b128 v[80:83], v1 offset:19456\nv_mfma_f32_16x16x32_bf16 v[40:43], v[156:159], v[60:63], v[40:43]\nds_read_b128 v[56:59], v2 offset:57600\nv_mfma_f32_16x16x32_bf16 v[44:47], v[164:167], v[60:63], v[44:47]\n" \
"v_mfma_f32_16x16x32_bf16 v[48:51], v[172:175], v[60:63], v[48:51]\nv_mfma_f32_16x16x32_bf16 v[52:55], v[180:183], v[60:63], v[52:55]\ns_waitcnt lgkmcnt(4)\nds_read_b128 v[72:75], v1 offset:20480\nv_mfma_f32_16x16x32_bf16 v[40:43], v[184:187], v[64:67], v[40:43]\ns_waitcnt lgkmcnt(4)\nds_read_b128 v[60:63], v2 offset:59648\nv_mfma_f32_16x16x32_bf16 v[44:47], v[76:79], v[64:67], v[44:47]\n" \
"v_mfma_f32_16x16x32_bf16 v[48:51], v[192:195], v[64:67], v[48:51]\nv_mfma_f32_16x16x32_bf16 v[52:55], v[200:203], v[64:67], v[52:55]\ns_waitcnt lgkmcnt(4)\nds_read_b128 v[76:79], v1 offset:21504\nv_mfma_f32_16x16x32_bf16 v[40:43], v[188:191], v[68:71], v[40:43]\ns_waitcnt lgkmcnt(4)\nds_read_b128 v[64:67], v2 offset:61696\nv_mfma_f32_16x16x32_bf16 v[44:47], v[80:83], v[68:71], v[44:47]\n" \
"v_mfma_f32_16x16x32_bf16 v[48:51], v[196:199], v[68:71], v[48:51]\nv_mfma_f32_16x16x32_bf16 v[52:55], v[204:207], v[68:71], v[52:55]\ns_waitcnt lgkmcnt(4)\nds_read_b128 v[80:83], v1 offset:22528\nv_mfma_f32_16x16x32_bf16 v[40:43], v[208:211], v[56:59], v[40:43]\nds_read_b128 v[68:71], v2 offset:63744\nv_mfma_f32_16x16x32_bf16 v[44:47], v[216:219], v[56:59], v[44:47]\ns_waitcnt lgkmcnt(5)\n" \
"v_mfma_f32_16x16x32_bf16 v[48:51], v[72:75], v[56:59], v[48:51]\nv_mfma_f32_16x16x32_bf16 v[52:55], v[224:227], v[56:59], v[52:55]\ns_waitcnt lgkmcnt(4)\nds_read_b128 v[72:75], v1 offset:23552\nv_mfma_f32_16x16x32_bf16 v[40:43], v[212:215], v[60:63], v[40:43]\nv_mfma_f32_16x16x32_bf16 v[44:47], v[220:223], v[60:63], v[44:47]\ns_waitcnt lgkmcnt(4)\nv_mfma_f32_16x16x32_bf16 v[48:51], v[76:79], v[60:63], v[48:51]\n" \
"v_mfma_f32_16x16x32_bf16 v[52:55], v[228:231], v[60:63], v[52:55]\ns_waitcnt lgkmcnt(3)\nv_mfma_f32_16x16x32_bf16 v[40:43], v[232:235], v[64:67], v[40:43]\nv_mfma_f32_16x16x32_bf16 v[44:47], v[240:243], v[64:67], v[44:47]\nv_mfma_f32_16x16x32_bf16 v[48:51], v[248:251], v[64:67], v[48:51]\ns_waitcnt lgkmcnt(2)\nv_mfma_f32_16x16x32_bf16 v[52:55], v[80:83], v[64:67], v[52:55]\ns_waitcnt lgkmcnt(1)\n" \
"v_mfma_f32_16x16x32_bf16 v[40:43], v[236:239], v[68:71], v[40:43]\nv_mfma_f32_16x16x32_bf16 v[44:47], v[244:247], v[68:71], v[44:47]\nv_mfma_f32_16x16x32_bf16 v[48:51], v[252:255], v[68:71], v[48:51]\ns_waitcnt lgkmcnt(0)\nv_mfma_f32_16x16x32_bf16 v[52:55], v[72:75], v[68:71], v[52:55]\nv_fma_f32 v40, v8, v87, v40\nv_fma_f32 v41, v9, v87, v41\ns_nop 1\n" \
"v_fma_f32 v48, v16, v87, v48\nv_fma_f32 v49, v17, v87, v49\nv_mul_f32 v40, v40, v88\nv_mul_f32 v41, v41, v88\nv_fma_f32 v91, v40, v40, s59\nv_fma_f32 v92, v41, v41, s59\nv_mul_f32 v91, v91, v40\nv_mul_f32 v92, v92, v41\n" \
"v_exp_f32 v91, v91\nv_exp_f32 v92, v92\nv_mul_f32 v40, v40, v48\nv_mul_f32 v41, v41, v49\nv_fma_f32 v91, v91, v89, v89\nv_fma_f32 v92, v92, v89, v89\nv_rcp_f32 v91, v91\nv_rcp_f32 v92, v92\n" \
"v_mul_f32 v40, v40, v91\nv_mul_f32 v41, v41, v92\nv_cvt_pk_bf16_f32 v48, v40, v41\nv_fma_f32 v42, v10, v87, v42\nv_fma_f32 v43, v11, v87, v43\nv_fma_f32 v50, v18, v87, v50\nv_fma_f32 v51, v19, v87, v51\nv_mul_f32 v42, v42, v88\n" \
"v_mul_f32 v43, v43, v88\nv_fma_f32 v91, v42, v42, s59\nv_fma_f32 v92, v43, v43, s59\nv_mul_f32 v91, v91, v42\nv_mul_f32 v92, v92, v43\nv_exp_f32 v91, v91\nv_exp_f32 v92, v92\nv_mul_f32 v42, v42, v50\n" \
"v_mul_f32 v43, v43, v51\nv_fma_f32 v91, v91, v89, v89\nv_fma_f32 v92, v92, v89, v89\nv_rcp_f32 v91, v91\nv_rcp_f32 v92, v92\nv_mul_f32 v42, v42, v91\nv_mul_f32 v43, v43, v92\nv_cvt_pk_bf16_f32 v49, v42, v43\n" \
"v_fma_f32 v44, v12, v87, v44\nv_fma_f32 v45, v13, v87, v45\nv_fma_f32 v52, v20, v87, v52\nv_fma_f32 v53, v21, v87, v53\nv_mul_f32 v44, v44, v88\nv_mul_f32 v45, v45, v88\nv_fma_f32 v91, v44, v44, s59\nv_fma_f32 v92, v45, v45, s59\n" \
"v_mul_f32 v91, v91, v44\nv_mul_f32 v92, v92, v45\nv_exp_f32 v91, v91\nv_exp_f32 v92, v92\nv_mul_f32 v44, v44, v52\nv_mul_f32 v45, v45, v53\nv_fma_f32 v91, v91, v89, v89\nv_fma_f32 v92, v92, v89, v89\n" \
"v_rcp_f32 v91, v91\nv_rcp_f32 v92, v92\nv_mul_f32 v44, v44, v91\nv_mul_f32 v45, v45, v92\nv_cvt_pk_bf16_f32 v50, v44, v45\nv_fma_f32 v46, v14, v87, v46\nv_fma_f32 v47, v15, v87, v47\nv_fma_f32 v54, v22, v87, v54\n" \
"v_fma_f32 v55, v23, v87, v55\nv_mul_f32 v46, v46, v88\nv_mul_f32 v47, v47, v88\nv_fma_f32 v91, v46, v46, s59\nv_fma_f32 v92, v47, v47, s59\nv_mul_f32 v91, v91, v46\nv_mul_f32 v92, v92, v47\nv_exp_f32 v91, v91\n" \
"v_exp_f32 v92, v92\nv_mul_f32 v46, v46, v54\nv_mul_f32 v47, v47, v55\nv_fma_f32 v91, v91, v89, v89\nv_fma_f32 v92, v92, v89, v89\nv_rcp_f32 v91, v91\nv_rcp_f32 v92, v92\nv_mul_f32 v46, v46, v91\n" \
"v_mul_f32 v47, v47, v92\nv_cvt_pk_bf16_f32 v51, v46, v47\nbuffer_store_dwordx4 v[48:51], v4, %[rs], s55 offen offset:1024 sc1\nv_add_u32 v90, 1, v90\ns_mov_b32 s56, %[fh]\ns_add_u32 s40, s40, 1\ns_cmp_lt_u32 s40, 64\ns_cbranch_scc1 .Lgg_next%=\n" \
"s_waitcnt vmcnt(0)\ns_barrier\nbuffer_store_dword v90, off, %[rs], s56 sc1\ns_branch .Lgg_end%=\n.Lgg_abort%=:\ns_mov_b64 exec, -1\nv_mov_b32 v16, 0\ns_movk_i32 s44, 6400\n" \
"buffer_store_dword v16, off, %[rs], s44 sc1\n.Lgg_end%=:\ns_waitcnt vmcnt(0) lgkmcnt(0)\n" \
""
#define DD_ASM \
"v_mbcnt_lo_u32_b32 v255, -1, 0\nv_mbcnt_hi_u32_b32 v255, -1, v255\nv_lshlrev_b32 v255, 2, v255\nv_add_u32 v255, %[tb], v255\nds_read_b32 v0, v255 offset:0\nds_read_b32 v1, v255 offset:256\nds_read_b32 v2, v255 offset:512\nds_read_b32 v3, v255 offset:768\n" \
"ds_read_b32 v4, v255 offset:1024\nds_read_b32 v5, v255 offset:1280\nds_read_b32 v6, v255 offset:1536\nds_read_b32 v7, v255 offset:1792\nds_read_b32 v125, v255 offset:2048\nds_read_b32 v126, v255 offset:2304\nds_read_b32 v127, v255 offset:2560\ns_waitcnt lgkmcnt(0)\n" \
"s_add_u32 m0, %[tb], 8192\ns_add_u32 s44, %[wo], 0\nbuffer_load_dwordx4 v125, %[rw], s44 offen nt lds\ns_add_u32 m0, %[tb], 9216\ns_add_u32 s44, %[wo], 16384\nbuffer_load_dwordx4 v125, %[rw], s44 offen nt lds\ns_add_u32 m0, %[tb], 10240\ns_add_u32 s44, %[wo], 32768\n" \
"buffer_load_dwordx4 v125, %[rw], s44 offen nt lds\ns_add_u32 m0, %[tb], 11264\ns_add_u32 s44, %[wo], 49152\nbuffer_load_dwordx4 v125, %[rw], s44 offen nt lds\ns_add_u32 m0, %[lw], 0\ns_add_u32 s44, %[wo], 65536\nbuffer_load_dwordx4 v125, %[rw], s44 offen nt lds\ns_add_u32 m0, %[lw], 1024\n" \
"s_add_u32 s44, %[wo], 81920\nbuffer_load_dwordx4 v125, %[rw], s44 offen nt lds\ns_add_u32 m0, %[lw], 2048\ns_add_u32 s44, %[wo], 98304\nbuffer_load_dwordx4 v125, %[rw], s44 offen nt lds\ns_add_u32 m0, %[lw], 3072\ns_add_u32 s44, %[wo], 114688\nbuffer_load_dwordx4 v125, %[rw], s44 offen nt lds\n" \
"s_add_u32 m0, %[lw], 4096\ns_add_u32 s44, %[wo], 131072\nbuffer_load_dwordx4 v125, %[rw], s44 offen nt lds\ns_add_u32 m0, %[lw], 5120\ns_add_u32 s44, %[wo], 147456\nbuffer_load_dwordx4 v125, %[rw], s44 offen nt lds\ns_add_u32 m0, %[lw], 6144\ns_add_u32 s44, %[wo], 163840\n" \
"buffer_load_dwordx4 v125, %[rw], s44 offen nt lds\ns_add_u32 m0, %[lw], 7168\ns_add_u32 s44, %[wo], 180224\nbuffer_load_dwordx4 v125, %[rw], s44 offen nt lds\ns_add_u32 m0, %[lw], 8192\ns_add_u32 s44, %[wo], 196608\nbuffer_load_dwordx4 v125, %[rw], s44 offen nt lds\ns_add_u32 m0, %[lw], 9216\n" \
"s_add_u32 s44, %[wo], 212992\nbuffer_load_dwordx4 v125, %[rw], s44 offen nt lds\ns_add_u32 m0, %[lw], 10240\ns_add_u32 s44, %[wo], 229376\nbuffer_load_dwordx4 v125, %[rw], s44 offen nt lds\ns_add_u32 m0, %[lw], 11264\ns_add_u32 s44, %[wo], 245760\nbuffer_load_dwordx4 v125, %[rw], s44 offen nt lds\n" \
"s_add_u32 m0, %[lw], 12288\ns_add_u32 s44, %[wo], 262144\nbuffer_load_dwordx4 v125, %[rw], s44 offen nt lds\ns_add_u32 m0, %[lw], 13312\ns_add_u32 s44, %[wo], 278528\nbuffer_load_dwordx4 v125, %[rw], s44 offen nt lds\ns_add_u32 m0, %[lw], 14336\ns_add_u32 s44, %[wo], 294912\n" \
"buffer_load_dwordx4 v125, %[rw], s44 offen nt lds\ns_add_u32 m0, %[lw], 15360\ns_add_u32 s44, %[wo], 311296\nbuffer_load_dwordx4 v125, %[rw], s44 offen nt lds\ns_add_u32 m0, %[lw], 16384\ns_add_u32 s44, %[wo], 327680\nbuffer_load_dwordx4 v125, %[rw], s44 offen nt lds\ns_add_u32 m0, %[lw], 17408\n" \
"s_add_u32 s44, %[wo], 344064\nbuffer_load_dwordx4 v125, %[rw], s44 offen nt lds\ns_add_u32 m0, %[lw], 18432\ns_add_u32 s44, %[wo], 360448\nbuffer_load_dwordx4 v125, %[rw], s44 offen nt lds\ns_add_u32 m0, %[lw], 19456\ns_add_u32 s44, %[wo], 376832\nbuffer_load_dwordx4 v125, %[rw], s44 offen nt lds\n" \
"s_add_u32 m0, %[lw], 20480\ns_add_u32 s44, %[wo], 393216\nbuffer_load_dwordx4 v125, %[rw], s44 offen nt lds\ns_add_u32 m0, %[lw], 21504\ns_add_u32 s44, %[wo], 409600\nbuffer_load_dwordx4 v125, %[rw], s44 offen nt lds\ns_add_u32 m0, %[lw], 22528\ns_add_u32 s44, %[wo], 425984\n" \
"buffer_load_dwordx4 v125, %[rw], s44 offen nt lds\ns_add_u32 m0, %[lw], 23552\ns_add_u32 s44, %[wo], 442368\nbuffer_load_dwordx4 v125, %[rw], s44 offen nt lds\ns_add_u32 m0, %[tb], 0\ns_add_u32 s44, %[wo], 458752\nbuffer_load_dwordx4 v125, %[rw], s44 offen nt lds\ns_add_u32 m0, %[tb], 1024\n" \
"s_add_u32 s44, %[wo], 475136\nbuffer_load_dwordx4 v125, %[rw], s44 offen nt lds\ns_add_u32 m0, %[tb], 2048\ns_add_u32 s44, %[wo], 491520\nbuffer_load_dwordx4 v125, %[rw], s44 offen nt lds\ns_add_u32 m0, %[tb], 3072\ns_add_u32 s44, %[wo], 507904\nbuffer_load_dwordx4 v125, %[rw], s44 offen nt lds\n" \
"s_add_u32 m0, %[tb], 4096\ns_add_u32 s44, %[wo], 524288\nbuffer_load_dwordx4 v125, %[rw], s44 offen nt lds\ns_add_u32 m0, %[tb], 5120\ns_add_u32 s44, %[wo], 540672\nbuffer_load_dwordx4 v125, %[rw], s44 offen nt lds\ns_add_u32 m0, %[tb], 6144\ns_add_u32 s44, %[wo], 557056\n" \
"buffer_load_dwordx4 v125, %[rw], s44 offen nt lds\ns_add_u32 m0, %[tb], 7168\ns_add_u32 s44, %[wo], 573440\nbuffer_load_dwordx4 v125, %[rw], s44 offen nt lds\ns_waitcnt vmcnt(32)\nds_read_b64_tr_b16 a[0:1], v126 offset:8192\nds_read_b64_tr_b16 a[2:3], v126 offset:8704\nds_read_b64_tr_b16 a[8:9], v126 offset:8224\n" \
"ds_read_b64_tr_b16 a[10:11], v126 offset:8736\nds_read_b64_tr_b16 a[16:17], v126 offset:8256\nds_read_b64_tr_b16 a[18:19], v126 offset:8768\nds_read_b64_tr_b16 a[24:25], v126 offset:8288\nds_read_b64_tr_b16 a[26:27], v126 offset:8800\ns_waitcnt lgkmcnt(0)\ns_add_u32 m0, %[tb], 8192\ns_add_u32 s44, %[wo], 589824\n" \
"buffer_load_dwordx4 v125, %[rw], s44 offen nt lds\ns_add_u32 m0, %[tb], 9216\ns_add_u32 s44, %[wo], 606208\nbuffer_load_dwordx4 v125, %[rw], s44 offen nt lds\ns_add_u32 m0, %[tb], 10240\ns_add_u32 s44, %[wo], 622592\nbuffer_load_dwordx4 v125, %[rw], s44 offen nt lds\ns_add_u32 m0, %[tb], 11264\n" \
"s_add_u32 s44, %[wo], 638976\nbuffer_load_dwordx4 v125, %[rw], s44 offen nt lds\ns_waitcnt vmcnt(32)\nds_read_b64_tr_b16 a[4:5], v127 offset:0\nds_read_b64_tr_b16 a[6:7], v127 offset:512\nds_read_b64_tr_b16 a[12:13], v127 offset:32\nds_read_b64_tr_b16 a[14:15], v127 offset:544\nds_read_b64_tr_b16 a[20:21], v127 offset:64\n" \
"ds_read_b64_tr_b16 a[22:23], v127 offset:576\nds_read_b64_tr_b16 a[28:29], v127 offset:96\nds_read_b64_tr_b16 a[30:31], v127 offset:608\ns_waitcnt lgkmcnt(0)\ns_add_u32 m0, %[lw], 0\ns_add_u32 s44, %[wo], 655360\nbuffer_load_dwordx4 v125, %[rw], s44 offen nt lds\ns_add_u32 m0, %[lw], 1024\n" \
"s_add_u32 s44, %[wo], 671744\nbuffer_load_dwordx4 v125, %[rw], s44 offen nt lds\ns_add_u32 m0, %[lw], 2048\ns_add_u32 s44, %[wo], 688128\nbuffer_load_dwordx4 v125, %[rw], s44 offen nt lds\ns_add_u32 m0, %[lw], 3072\ns_add_u32 s44, %[wo], 704512\nbuffer_load_dwordx4 v125, %[rw], s44 offen nt lds\n" \
"s_waitcnt vmcnt(32)\nds_read_b64_tr_b16 a[32:33], v127 offset:4096\nds_read_b64_tr_b16 a[34:35], v127 offset:4608\nds_read_b64_tr_b16 v[92:93], v127 offset:4128\nds_read_b64_tr_b16 v[94:95], v127 offset:4640\nds_read_b64_tr_b16 a[40:41], v127 offset:4160\nds_read_b64_tr_b16 a[42:43], v127 offset:4672\nds_read_b64_tr_b16 a[48:49], v127 offset:4192\n" \
"ds_read_b64_tr_b16 a[50:51], v127 offset:4704\ns_waitcnt lgkmcnt(0)\ns_add_u32 m0, %[lw], 4096\ns_add_u32 s44, %[wo], 720896\nbuffer_load_dwordx4 v125, %[rw], s44 offen nt lds\ns_add_u32 m0, %[lw], 5120\ns_add_u32 s44, %[wo], 737280\nbuffer_load_dwordx4 v125, %[rw], s44 offen nt lds\n" \
"s_add_u32 m0, %[lw], 6144\ns_add_u32 s44, %[wo], 753664\nbuffer_load_dwordx4 v125, %[rw], s44 offen nt lds\ns_add_u32 m0, %[lw], 7168\ns_add_u32 s44, %[wo], 770048\nbuffer_load_dwordx4 v125, %[rw], s44 offen nt lds\ns_waitcnt vmcnt(32)\nds_read_b64_tr_b16 a[36:37], v127 offset:8192\n" \
"ds_read_b64_tr_b16 a[38:39], v127 offset:8704\nds_read_b64_tr_b16 v[88:89], v127 offset:8224\nds_read_b64_tr_b16 v[90:91], v127 offset:8736\nds_read_b64_tr_b16 a[44:45], v127 offset:8256\nds_read_b64_tr_b16 a[46:47], v127 offset:8768\nds_read_b64_tr_b16 a[52:53], v127 offset:8288\nds_read_b64_tr_b16 a[54:55], v127 offset:8800\ns_waitcnt lgkmcnt(0)\n" \
"s_add_u32 m0, %[lw], 8192\ns_add_u32 s44, %[wo], 786432\nbuffer_load_dwordx4 v125, %[rw], s44 offen nt lds\ns_add_u32 m0, %[lw], 9216\ns_add_u32 s44, %[wo], 802816\nbuffer_load_dwordx4 v125, %[rw], s44 offen nt lds\ns_add_u32 m0, %[lw], 10240\ns_add_u32 s44, %[wo], 819200\n" \
"buffer_load_dwordx4 v125, %[rw], s44 offen nt lds\ns_add_u32 m0, %[lw], 11264\ns_add_u32 s44, %[wo], 835584\nbuffer_load_dwordx4 v125, %[rw], s44 offen nt lds\ns_waitcnt vmcnt(32)\nds_read_b64_tr_b16 a[56:57], v127 offset:12288\nds_read_b64_tr_b16 a[58:59], v127 offset:12800\nds_read_b64_tr_b16 a[64:65], v127 offset:12320\n" \
"ds_read_b64_tr_b16 a[66:67], v127 offset:12832\nds_read_b64_tr_b16 a[72:73], v127 offset:12352\nds_read_b64_tr_b16 a[74:75], v127 offset:12864\nds_read_b64_tr_b16 a[80:81], v127 offset:12384\nds_read_b64_tr_b16 a[82:83], v127 offset:12896\ns_waitcnt lgkmcnt(0)\ns_add_u32 m0, %[lw], 12288\ns_add_u32 s44, %[wo], 851968\n" \
"buffer_load_dwordx4 v125, %[rw], s44 offen nt lds\ns_add_u32 m0, %[lw], 13312\ns_add_u32 s44, %[wo], 868352\nbuffer_load_dwordx4 v125, %[rw], s44 offen nt lds\ns_add_u32 m0, %[lw], 14336\ns_add_u32 s44, %[wo], 884736\nbuffer_load_dwordx4 v125, %[rw], s44 offen nt lds\ns_add_u32 m0, %[lw], 15360\n" \
"s_add_u32 s44, %[wo], 901120\nbuffer_load_dwordx4 v125, %[rw], s44 offen nt lds\ns_waitcnt vmcnt(32)\nds_read_b64_tr_b16 a[60:61], v127 offset:16384\nds_read_b64_tr_b16 a[62:63], v127 offset:16896\nds_read_b64_tr_b16 a[68:69], v127 offset:16416\nds_read_b64_tr_b16 a[70:71], v127 offset:16928\nds_read_b64_tr_b16 a[76:77], v127 offset:16448\n" \
"ds_read_b64_tr_b16 a[78:79], v127 offset:16960\nds_read_b64_tr_b16 a[84:85], v127 offset:16480\nds_read_b64_tr_b16 a[86:87], v127 offset:16992\ns_waitcnt lgkmcnt(0)\ns_add_u32 m0, %[lw], 16384\ns_add_u32 s44, %[wo], 917504\nbuffer_load_dwordx4 v125, %[rw], s44 offen nt lds\ns_add_u32 m0, %[lw], 17408\n" \
"s_add_u32 s44, %[wo], 933888\nbuffer_load_dwordx4 v125, %[rw], s44 offen nt lds\ns_add_u32 m0, %[lw], 18432\ns_add_u32 s44, %[wo], 950272\nbuffer_load_dwordx4 v125, %[rw], s44 offen nt lds\ns_add_u32 m0, %[lw], 19456\ns_add_u32 s44, %[wo], 966656\nbuffer_load_dwordx4 v125, %[rw], s44 offen nt lds\n" \
"s_waitcnt vmcnt(32)\nds_read_b64_tr_b16 a[88:89], v127 offset:20480\nds_read_b64_tr_b16 a[90:91], v127 offset:20992\nds_read_b64_tr_b16 a[96:97], v127 offset:20512\nds_read_b64_tr_b16 a[98:99], v127 offset:21024\nds_read_b64_tr_b16 a[104:105], v127 offset:20544\nds_read_b64_tr_b16 a[106:107], v127 offset:21056\nds_read_b64_tr_b16 v[84:85], v127 offset:20576\n" \
"ds_read_b64_tr_b16 v[86:87], v127 offset:21088\ns_waitcnt lgkmcnt(0)\ns_add_u32 m0, %[lw], 20480\ns_add_u32 s44, %[wo], 983040\nbuffer_load_dwordx4 v125, %[rw], s44 offen nt lds\ns_add_u32 m0, %[lw], 21504\ns_add_u32 s44, %[wo], 999424\nbuffer_load_dwordx4 v125, %[rw], s44 offen nt lds\n" \
"s_add_u32 m0, %[lw], 22528\ns_add_u32 s44, %[wo], 1015808\nbuffer_load_dwordx4 v125, %[rw], s44 offen nt lds\ns_add_u32 m0, %[lw], 23552\ns_add_u32 s44, %[wo], 1032192\nbuffer_load_dwordx4 v125, %[rw], s44 offen nt lds\ns_waitcnt vmcnt(32)\nds_read_b64_tr_b16 a[92:93], v126 offset:0\n" \
"ds_read_b64_tr_b16 a[94:95], v126 offset:512\nds_read_b64_tr_b16 a[100:101], v126 offset:32\nds_read_b64_tr_b16 a[102:103], v126 offset:544\nds_read_b64_tr_b16 a[108:109], v126 offset:64\nds_read_b64_tr_b16 a[110:111], v126 offset:576\nds_read_b64_tr_b16 v[80:81], v126 offset:96\nds_read_b64_tr_b16 v[82:83], v126 offset:608\ns_waitcnt lgkmcnt(0)\n" \
"s_add_u32 m0, %[tb], 0\ns_add_u32 s44, %[wo], 1048576\nbuffer_load_dwordx4 v125, %[rw], s44 offen nt lds\ns_add_u32 m0, %[tb], 1024\ns_add_u32 s44, %[wo], 1064960\nbuffer_load_dwordx4 v125, %[rw], s44 offen nt lds\ns_add_u32 m0, %[tb], 2048\ns_add_u32 s44, %[wo], 1081344\n" \
"buffer_load_dwordx4 v125, %[rw], s44 offen nt lds\ns_add_u32 m0, %[tb], 3072\ns_add_u32 s44, %[wo], 1097728\nbuffer_load_dwordx4 v125, %[rw], s44 offen nt lds\ns_waitcnt vmcnt(32)\nds_read_b64_tr_b16 a[112:113], v126 offset:4096\nds_read_b64_tr_b16 a[114:115], v126 offset:4608\nds_read_b64_tr_b16 a[120:121], v126 offset:4128\n" \
"ds_read_b64_tr_b16 a[122:123], v126 offset:4640\nds_read_b64_tr_b16 a[128:129], v126 offset:4160\nds_read_b64_tr_b16 a[130:131], v126 offset:4672\nds_read_b64_tr_b16 a[136:137], v126 offset:4192\nds_read_b64_tr_b16 a[138:139], v126 offset:4704\ns_waitcnt lgkmcnt(0)\ns_add_u32 m0, %[tb], 4096\ns_add_u32 s44, %[wo], 1114112\n" \
"buffer_load_dwordx4 v125, %[rw], s44 offen nt lds\ns_add_u32 m0, %[tb], 5120\ns_add_u32 s44, %[wo], 1130496\nbuffer_load_dwordx4 v125, %[rw], s44 offen nt lds\ns_add_u32 m0, %[tb], 6144\ns_add_u32 s44, %[wo], 1146880\nbuffer_load_dwordx4 v125, %[rw], s44 offen nt lds\ns_add_u32 m0, %[tb], 7168\n" \
"s_add_u32 s44, %[wo], 1163264\nbuffer_load_dwordx4 v125, %[rw], s44 offen nt lds\ns_waitcnt vmcnt(32)\nds_read_b64_tr_b16 a[116:117], v126 offset:8192\nds_read_b64_tr_b16 a[118:119], v126 offset:8704\nds_read_b64_tr_b16 a[124:125], v126 offset:8224\nds_read_b64_tr_b16 a[126:127], v126 offset:8736\nds_read_b64_tr_b16 a[132:133], v126 offset:8256\n" \
"ds_read_b64_tr_b16 a[134:135], v126 offset:8768\nds_read_b64_tr_b16 a[140:141], v126 offset:8288\nds_read_b64_tr_b16 a[142:143], v126 offset:8800\ns_waitcnt lgkmcnt(0)\ns_add_u32 m0, %[tb], 8192\ns_add_u32 s44, %[wo], 1179648\nbuffer_load_dwordx4 v125, %[rw], s44 offen nt lds\ns_add_u32 m0, %[tb], 9216\n" \
"s_add_u32 s44, %[wo], 1196032\nbuffer_load_dwordx4 v125, %[rw], s44 offen nt lds\ns_add_u32 m0, %[tb], 10240\ns_add_u32 s44, %[wo], 1212416\nbuffer_load_dwordx4 v125, %[rw], s44 offen nt lds\ns_add_u32 m0, %[tb], 11264\ns_add_u32 s44, %[wo], 1228800\nbuffer_load_dwordx4 v125, %[rw], s44 offen nt lds\n" \
"s_waitcnt vmcnt(32)\nds_read_b64_tr_b16 a[144:145], v127 offset:0\nds_read_b64_tr_b16 a[146:147], v127 offset:512\nds_read_b64_tr_b16 v[76:77], v127 offset:32\nds_read_b64_tr_b16 v[78:79], v127 offset:544\nds_read_b64_tr_b16 a[152:153], v127 offset:64\nds_read_b64_tr_b16 a[154:155], v127 offset:576\nds_read_b64_tr_b16 a[160:161], v127 offset:96\n" \
"ds_read_b64_tr_b16 a[162:163], v127 offset:608\ns_waitcnt lgkmcnt(0)\ns_add_u32 m0, %[lw], 0\ns_add_u32 s44, %[wo], 1245184\nbuffer_load_dwordx4 v125, %[rw], s44 offen nt lds\ns_add_u32 m0, %[lw], 1024\ns_add_u32 s44, %[wo], 1261568\nbuffer_load_dwordx4 v125, %[rw], s44 offen nt lds\n" \
"s_add_u32 m0, %[lw], 2048\ns_add_u32 s44, %[wo], 1277952\nbuffer_load_dwordx4 v125, %[rw], s44 offen nt lds\ns_add_u32 m0, %[lw], 3072\ns_add_u32 s44, %[wo], 1294336\nbuffer_load_dwordx4 v125, %[rw], s44 offen nt lds\ns_waitcnt vmcnt(32)\nds_read_b64_tr_b16 a[148:149], v127 offset:4096\n" \
"ds_read_b64_tr_b16 a[150:151], v127 offset:4608\nds_read_b64_tr_b16 v[72:73], v127 offset:4128\nds_read_b64_tr_b16 v[74:75], v127 offset:4640\nds_read_b64_tr_b16 a[156:157], v127 offset:4160\nds_read_b64_tr_b16 a[158:159], v127 offset:4672\nds_read_b64_tr_b16 a[164:165], v127 offset:4192\nds_read_b64_tr_b16 a[166:167], v127 offset:4704\ns_waitcnt lgkmcnt(0)\n" \
"s_add_u32 m0, %[lw], 4096\ns_add_u32 s44, %[wo], 1310720\nbuffer_load_dwordx4 v125, %[rw], s44 offen nt lds\ns_add_u32 m0, %[lw], 5120\ns_add_u32 s44, %[wo], 1327104\nbuffer_load_dwordx4 v125, %[rw], s44 offen nt lds\ns_add_u32 m0, %[lw], 6144\ns_add_u32 s44, %[wo], 1343488\n" \
"buffer_load_dwordx4 v125, %[rw], s44 offen nt lds\ns_add_u32 m0, %[lw], 7168\ns_add_u32 s44, %[wo], 1359872\nbuffer_load_dwordx4 v125, %[rw], s44 offen nt lds\ns_waitcnt vmcnt(32)\nds_read_b64_tr_b16 a[168:169], v127 offset:8192\nds_read_b64_tr_b16 a[170:171], v127 offset:8704\nds_read_b64_tr_b16 a[176:177], v127 offset:8224\n" \
"ds_read_b64_tr_b16 a[178:179], v127 offset:8736\nds_read_b64_tr_b16 v[68:69], v127 offset:8256\nds_read_b64_tr_b16 v[70:71], v127 offset:8768\nds_read_b64_tr_b16 a[184:185], v127 offset:8288\nds_read_b64_tr_b16 a[186:187], v127 offset:8800\ns_waitcnt lgkmcnt(0)\ns_add_u32 m0, %[lw], 8192\ns_add_u32 s44, %[wo], 1376256\n" \
"buffer_load_dwordx4 v125, %[rw], s44 offen nt lds\ns_add_u32 m0, %[lw], 9216\ns_add_u32 s44, %[wo], 1392640\nbuffer_load_dwordx4 v125, %[rw], s44 offen nt lds\ns_add_u32 m0, %[lw], 10240\ns_add_u32 s44, %[wo], 1409024\nbuffer_load_dwordx4 v125, %[rw], s44 offen nt lds\ns_add_u32 m0, %[lw], 11264\n" \
"s_add_u32 s44, %[wo], 1425408\nbuffer_load_dwordx4 v125, %[rw], s44 offen nt lds\ns_waitcnt vmcnt(32)\nds_read_b64_tr_b16 a[172:173], v127 offset:12288\nds_read_b64_tr_b16 a[174:175], v127 offset:12800\nds_read_b64_tr_b16 a[180:181], v127 offset:12320\nds_read_b64_tr_b16 a[182:183], v127 offset:12832\nds_read_b64_tr_b16 v[64:65], v127 offset:12352\n" \
"ds_read_b64_tr_b16 v[66:67], v127 offset:12864\nds_read_b64_tr_b16 a[188:189], v127 offset:12384\nds_read_b64_tr_b16 a[190:191], v127 offset:12896\ns_waitcnt lgkmcnt(0)\ns_add_u32 m0, %[lw], 12288\ns_add_u32 s44, %[wo], 1441792\nbuffer_load_dwordx4 v125, %[rw], s44 offen nt lds\ns_add_u32 m0, %[lw], 13312\n" \
"s_add_u32 s44, %[wo], 1458176\nbuffer_load_dwordx4 v125, %[rw], s44 offen nt lds\ns_add_u32 m0, %[lw], 14336\ns_add_u32 s44, %[wo], 1474560\nbuffer_load_dwordx4 v125, %[rw], s44 offen nt lds\ns_add_u32 m0, %[lw], 15360\ns_add_u32 s44, %[wo], 1490944\nbuffer_load_dwordx4 v125, %[rw], s44 offen nt lds\n" \
"s_waitcnt vmcnt(32)\nds_read_b64_tr_b16 a[192:193], v127 offset:16384\nds_read_b64_tr_b16 a[194:195], v127 offset:16896\nds_read_b64_tr_b16 a[200:201], v127 offset:16416\nds_read_b64_tr_b16 a[202:203], v127 offset:16928\nds_read_b64_tr_b16 a[208:209], v127 offset:16448\nds_read_b64_tr_b16 a[210:211], v127 offset:16960\nds_read_b64_tr_b16 a[216:217], v127 offset:16480\n" \
"ds_read_b64_tr_b16 a[218:219], v127 offset:16992\ns_waitcnt lgkmcnt(0)\ns_add_u32 m0, %[lw], 16384\ns_add_u32 s44, %[wo], 1507328\nbuffer_load_dwordx4 v125, %[rw], s44 offen nt lds\ns_add_u32 m0, %[lw], 17408\ns_add_u32 s44, %[wo], 1523712\nbuffer_load_dwordx4 v125, %[rw], s44 offen nt lds\n" \
"s_add_u32 m0, %[lw], 18432\ns_add_u32 s44, %[wo], 1540096\nbuffer_load_dwordx4 v125, %[rw], s44 offen nt lds\ns_add_u32 m0, %[lw], 19456\ns_add_u32 s44, %[wo], 1556480\nbuffer_load_dwordx4 v125, %[rw], s44 offen nt lds\ns_waitcnt vmcnt(32)\nds_read_b64_tr_b16 a[196:197], v127 offset:20480\n" \
"ds_read_b64_tr_b16 a[198:199], v127 offset:20992\nds_read_b64_tr_b16 a[204:205], v127 offset:20512\nds_read_b64_tr_b16 a[206:207], v127 offset:21024\nds_read_b64_tr_b16 a[212:213], v127 offset:20544\nds_read_b64_tr_b16 a[214:215], v127 offset:21056\nds_read_b64_tr_b16 a[220:221], v127 offset:20576\nds_read_b64_tr_b16 a[222:223], v127 offset:21088\ns_waitcnt lgkmcnt(0)\n" \
"s_add_u32 m0, %[lw], 20480\ns_add_u32 s44, %[wo], 1572864\nbuffer_load_dwordx4 v125, %[rw], s44 offen nt lds\ns_add_u32 m0, %[lw], 21504\ns_add_u32 s44, %[wo], 1589248\nbuffer_load_dwordx4 v125, %[rw], s44 offen nt lds\ns_add_u32 m0, %[lw], 22528\ns_add_u32 s44, %[wo], 1605632\n" \
"buffer_load_dwordx4 v125, %[rw], s44 offen nt lds\ns_add_u32 m0, %[lw], 23552\ns_add_u32 s44, %[wo], 1622016\nbuffer_load_dwordx4 v125, %[rw], s44 offen nt lds\ns_waitcnt vmcnt(32)\nds_read_b64_tr_b16 v[60:61], v126 offset:0\nds_read_b64_tr_b16 v[62:63], v126 offset:512\nds_read_b64_tr_b16 a[224:225], v126 offset:32\n" \
"ds_read_b64_tr_b16 a[226:227], v126 offset:544\nds_read_b64_tr_b16 a[232:233], v126 offset:64\nds_read_b64_tr_b16 a[234:235], v126 offset:576\nds_read_b64_tr_b16 a[240:241], v126 offset:96\nds_read_b64_tr_b16 a[242:243], v126 offset:608\ns_waitcnt lgkmcnt(0)\ns_add_u32 m0, %[tb], 0\ns_add_u32 s44, %[wo], 1638400\n" \
"buffer_load_dwordx4 v125, %[rw], s44 offen nt lds\ns_add_u32 m0, %[tb], 1024\ns_add_u32 s44, %[wo], 1654784\nbuffer_load_dwordx4 v125, %[rw], s44 offen nt lds\ns_add_u32 m0, %[tb], 2048\ns_add_u32 s44, %[wo], 1671168\nbuffer_load_dwordx4 v125, %[rw], s44 offen nt lds\ns_add_u32 m0, %[tb], 3072\n" \
"s_add_u32 s44, %[wo], 1687552\nbuffer_load_dwordx4 v125, %[rw], s44 offen nt lds\ns_waitcnt vmcnt(32)\nds_read_b64_tr_b16 v[56:57], v126 offset:4096\nds_read_b64_tr_b16 v[58:59], v126 offset:4608\nds_read_b64_tr_b16 a[228:229], v126 offset:4128\nds_read_b64_tr_b16 a[230:231], v126 offset:4640\nds_read_b64_tr_b16 a[236:237], v126 offset:4160\n" \
"ds_read_b64_tr_b16 a[238:239], v126 offset:4672\nds_read_b64_tr_b16 a[244:245], v126 offset:4192\nds_read_b64_tr_b16 a[246:247], v126 offset:4704\ns_waitcnt lgkmcnt(0)\ns_add_u32 m0, %[tb], 4096\ns_add_u32 s44, %[wo], 1703936\nbuffer_load_dwordx4 v125, %[rw], s44 offen nt lds\ns_add_u32 m0, %[tb], 5120\n" \
"s_add_u32 s44, %[wo], 1720320\nbuffer_load_dwordx4 v125, %[rw], s44 offen nt lds\ns_add_u32 m0, %[tb], 6144\ns_add_u32 s44, %[wo], 1736704\nbuffer_load_dwordx4 v125, %[rw], s44 offen nt lds\ns_add_u32 m0, %[tb], 7168\ns_add_u32 s44, %[wo], 1753088\nbuffer_load_dwordx4 v125, %[rw], s44 offen nt lds\n" \
"s_waitcnt vmcnt(32)\nds_read_b64_tr_b16 a[248:249], v126 offset:8192\nds_read_b64_tr_b16 a[250:251], v126 offset:8704\nds_read_b64_tr_b16 v[128:129], v126 offset:8224\nds_read_b64_tr_b16 v[130:131], v126 offset:8736\nds_read_b64_tr_b16 v[136:137], v126 offset:8256\nds_read_b64_tr_b16 v[138:139], v126 offset:8768\nds_read_b64_tr_b16 v[144:145], v126 offset:8288\n" \
"ds_read_b64_tr_b16 v[146:147], v126 offset:8800\ns_waitcnt lgkmcnt(0)\ns_add_u32 m0, %[tb], 8192\ns_add_u32 s44, %[wo], 1769472\nbuffer_load_dwordx4 v125, %[rw], s44 offen nt lds\ns_add_u32 m0, %[tb], 9216\ns_add_u32 s44, %[wo], 1785856\nbuffer_load_dwordx4 v125, %[rw], s44 offen nt lds\n" \
"s_add_u32 m0, %[tb], 10240\ns_add_u32 s44, %[wo], 1802240\nbuffer_load_dwordx4 v125, %[rw], s44 offen nt lds\ns_add_u32 m0, %[tb], 11264\ns_add_u32 s44, %[wo], 1818624\nbuffer_load_dwordx4 v125, %[rw], s44 offen nt lds\ns_waitcnt vmcnt(32)\nds_read_b64_tr_b16 a[252:253], v127 offset:0\n" \
"ds_read_b64_tr_b16 a[254:255], v127 offset:512\nds_read_b64_tr_b16 v[132:133], v127 offset:32\nds_read_b64_tr_b16 v[134:135], v127 offset:544\nds_read_b64_tr_b16 v[140:141], v127 offset:64\nds_read_b64_tr_b16 v[142:143], v127 offset:576\nds_read_b64_tr_b16 v[148:149], v127 offset:96\nds_read_b64_tr_b16 v[150:151], v127 offset:608\ns_waitcnt lgkmcnt(0)\n" \
"ds_write_b128 v1, v[92:95] offset:0\nds_write_b128 v1, v[88:91] offset:1024\nds_write_b128 v1, v[84:87] offset:2048\nds_write_b128 v1, v[80:83] offset:3072\ns_waitcnt vmcnt(28)\nds_read_b64_tr_b16 v[152:153], v127 offset:4096\nds_read_b64_tr_b16 v[154:155], v127 offset:4608\nds_read_b64_tr_b16 v[160:161], v127 offset:4128\n" \
"ds_read_b64_tr_b16 v[162:163], v127 offset:4640\nds_read_b64_tr_b16 v[80:81], v127 offset:4160\nds_read_b64_tr_b16 v[82:83], v127 offset:4672\nds_read_b64_tr_b16 v[168:169], v127 offset:4192\nds_read_b64_tr_b16 v[170:171], v127 offset:4704\ns_waitcnt lgkmcnt(0)\nds_write_b128 v1, v[76:79] offset:4096\nds_write_b128 v1, v[72:75] offset:5120\n" \
"ds_write_b128 v1, v[68:71] offset:6144\nds_write_b128 v1, v[64:67] offset:7168\ns_waitcnt vmcnt(24)\nds_read_b64_tr_b16 v[156:157], v127 offset:8192\nds_read_b64_tr_b16 v[158:159], v127 offset:8704\nds_read_b64_tr_b16 v[164:165], v127 offset:8224\nds_read_b64_tr_b16 v[166:167], v127 offset:8736\nds_read_b64_tr_b16 v[64:65], v127 offset:8256\n" \
"ds_read_b64_tr_b16 v[66:67], v127 offset:8768\nds_read_b64_tr_b16 v[172:173], v127 offset:8288\nds_read_b64_tr_b16 v[174:175], v127 offset:8800\ns_waitcnt lgkmcnt(0)\nds_write_b128 v1, v[60:63] offset:8192\nds_write_b128 v1, v[56:59] offset:9216\nds_write_b128 v1, v[80:83] offset:10240\nds_write_b128 v1, v[64:67] offset:11264\n" \
"s_waitcnt vmcnt(20)\nds_read_b64_tr_b16 v[176:177], v127 offset:12288\nds_read_b64_tr_b16 v[178:179], v127 offset:12800\nds_read_b64_tr_b16 v[184:185], v127 offset:12320\nds_read_b64_tr_b16 v[186:187], v127 offset:12832\nds_read_b64_tr_b16 v[192:193], v127 offset:12352\nds_read_b64_tr_b16 v[194:195], v127 offset:12864\nds_read_b64_tr_b16 v[200:201], v127 offset:12384\n" \
"ds_read_b64_tr_b16 v[202:203], v127 offset:12896\ns_waitcnt lgkmcnt(0)\ns_waitcnt vmcnt(16)\nds_read_b64_tr_b16 v[180:181], v127 offset:16384\nds_read_b64_tr_b16 v[182:183], v127 offset:16896\nds_read_b64_tr_b16 v[188:189], v127 offset:16416\nds_read_b64_tr_b16 v[190:191], v127 offset:16928\nds_read_b64_tr_b16 v[196:197], v127 offset:16448\n" \
"ds_read_b64_tr_b16 v[198:199], v127 offset:16960\nds_read_b64_tr_b16 v[204:205], v127 offset:16480\nds_read_b64_tr_b16 v[206:207], v127 offset:16992\ns_waitcnt lgkmcnt(0)\ns_waitcnt vmcnt(12)\nds_read_b64_tr_b16 v[64:65], v127 offset:20480\nds_read_b64_tr_b16 v[66:67], v127 offset:20992\nds_read_b64_tr_b16 v[208:209], v127 offset:20512\n" \
"ds_read_b64_tr_b16 v[210:211], v127 offset:21024\nds_read_b64_tr_b16 v[216:217], v127 offset:20544\nds_read_b64_tr_b16 v[218:219], v127 offset:21056\nds_read_b64_tr_b16 v[224:225], v127 offset:20576\nds_read_b64_tr_b16 v[226:227], v127 offset:21088\ns_waitcnt lgkmcnt(0)\nds_write_b128 v1, v[64:67] offset:12288\ns_waitcnt vmcnt(8)\n" \
"ds_read_b64_tr_b16 v[64:65], v126 offset:0\nds_read_b64_tr_b16 v[66:67], v126 offset:512\nds_read_b64_tr_b16 v[212:213], v126 offset:32\nds_read_b64_tr_b16 v[214:215], v126 offset:544\nds_read_b64_tr_b16 v[220:221], v126 offset:64\nds_read_b64_tr_b16 v[222:223], v126 offset:576\nds_read_b64_tr_b16 v[228:229], v126 offset:96\nds_read_b64_tr_b16 v[230:231], v126 offset:608\n" \
"s_waitcnt lgkmcnt(0)\nds_write_b128 v1, v[64:67] offset:13312\ns_waitcnt vmcnt(4)\nds_read_b64_tr_b16 v[232:233], v126 offset:4096\nds_read_b64_tr_b16 v[234:235], v126 offset:4608\nds_read_b64_tr_b16 v[64:65], v126 offset:4128\nds_read_b64_tr_b16 v[66:67], v126 offset:4640\nds_read_b64_tr_b16 v[240:241], v126 offset:4160\n" \
"ds_read_b64_tr_b16 v[242:243], v126 offset:4672\nds_read_b64_tr_b16 v[248:249], v126 offset:4192\nds_read_b64_tr_b16 v[250:251], v126 offset:4704\ns_waitcnt lgkmcnt(0)\nds_write_b128 v1, v[64:67] offset:14336\ns_waitcnt vmcnt(0)\nds_read_b64_tr_b16 v[236:237], v126 offset:8192\nds_read_b64_tr_b16 v[238:239], v126 offset:8704\n" \
"ds_read_b64_tr_b16 v[64:65], v126 offset:8224\nds_read_b64_tr_b16 v[66:67], v126 offset:8736\nds_read_b64_tr_b16 v[244:245], v126 offset:8256\nds_read_b64_tr_b16 v[246:247], v126 offset:8768\nds_read_b64_tr_b16 v[252:253], v126 offset:8288\nds_read_b64_tr_b16 v[254:255], v126 offset:8800\ns_waitcnt lgkmcnt(0)\nds_write_b128 v1, v[64:67] offset:15360\n" \
"s_waitcnt lgkmcnt(0)\ns_barrier\ns_mov_b32 s40, 0\ns_mov_b32 s43, 0\ns_mov_b32 s56, -2147483648\n.Ldd_next%=:\nbuffer_load_dword v8, v7, %[rs], 0 offen sc1\ns_lshl_b32 s58, s40, 1\n" \
"s_lshl_b32 s44, s40, 19\ns_add_u32 s44, s44, %[ho]\ns_lshr_b32 s45, s44, 8\ns_and_b32 s45, s45, 0x2000\ns_xor_b32 s55, s44, s45\ns_waitcnt vmcnt(0)\nbuffer_store_dword v9, off, %[rs], s56 sc1\n.Ldd_w0%=:\n" \
"v_cmp_gt_i32 vcc, s58, v8\ns_cbranch_vccz .Ldd_w0go%=\ns_cmp_ge_u32 s40, 62\ns_cbranch_scc1 .Ldd_ns666%=\ns_sleep 16\n.Ldd_ns666%=:\ns_sleep 1\nbuffer_load_dword v8, v7, %[rs], 0 offen sc1\n" \
"s_waitcnt vmcnt(0)\ns_add_u32 s43, s43, 1\ns_cmp_lt_u32 s43, 262144\ns_cbranch_scc1 .Ldd_w0%=\ns_branch .Ldd_abort%=\n.Ldd_w0go%=:\ns_add_u32 s60, s55, 0\nbuffer_load_dwordx4 v[48:51], v0, %[rs], s60 offen offset:0 sc1\n" \
"buffer_load_dwordx4 v[52:55], v0, %[rs], s60 offen offset:2048 sc1\ns_add_u32 s60, s55, 4096\nbuffer_load_dwordx4 v[56:59], v0, %[rs], s60 offen offset:0 sc1\nbuffer_load_dwordx4 v[60:63], v0, %[rs], s60 offen offset:2048 sc1\ns_add_u32 s60, s55, 16384\nbuffer_load_dwordx4 v[64:67], v0, %[rs], s60 offen offset:0 sc1\nbuffer_load_dwordx4 v[68:71], v0, %[rs], s60 offen offset:2048 sc1\ns_add_u32 s60, s55, 20480\n" \
"buffer_load_dwordx4 v[72:75], v0, %[rs], s60 offen offset:0 sc1\nbuffer_load_dwordx4 v[76:79], v0, %[rs], s60 offen offset:2048 sc1\ns_add_u32 s60, s55, 32768\nbuffer_load_dwordx4 v[80:83], v0, %[rs], s60 offen offset:0 sc1\nbuffer_load_dwordx4 v[84:87], v0, %[rs], s60 offen offset:2048 sc1\ns_add_u32 s60, s55, 36864\nbuffer_load_dwordx4 v[88:91], v0, %[rs], s60 offen offset:0 sc1\nbuffer_load_dwordx4 v[92:95], v0, %[rs], s60 offen offset:2048 sc1\n" \
"s_add_u32 s60, s55, 49152\nbuffer_load_dwordx4 v[96:99], v0, %[rs], s60 offen offset:0 sc1\nbuffer_load_dwordx4 v[100:103], v0, %[rs], s60 offen offset:2048 sc1\ns_add_u32 s60, s55, 53248\nbuffer_load_dwordx4 v[104:107], v0, %[rs], s60 offen offset:0 sc1\nbuffer_load_dwordx4 v[108:111], v0, %[rs], s60 offen offset:2048 sc1\ns_add_u32 s44, s40, 0\ns_lshl_b32 s44, s44, 17\n" \
"s_add_u32 s44, s44, %[xn]\ns_lshr_b32 s45, s44, 8\ns_and_b32 s45, s45, 0x2000\ns_xor_b32 s41, s44, s45\ns_lshl_b32 s44, s40, 16\ns_cmp_eq_u32 %[l], 0\ns_cselect_b32 s42, s41, s44\ns_add_u32 s58, s58, 1\n" \
"ds_read_b128 v[120:123], v1 offset:0\ns_waitcnt vmcnt(15)\nv_mfma_f32_16x16x32_bf16 v[16:19], a[0:3], v[48:51], 0\nds_read_b128 v[124:127], v1 offset:1024\nv_mfma_f32_16x16x32_bf16 v[24:27], a[8:11], v[48:51], 0\nbuffer_load_dword v8, v7, %[rs], 0 offen sc1\nv_mfma_f32_16x16x32_bf16 v[32:35], a[16:19], v[48:51], 0\ns_add_u32 s60, s55, 65536\n" \
"v_mfma_f32_16x16x32_bf16 v[40:43], a[24:27], v[48:51], 0\ns_waitcnt vmcnt(15)\nbuffer_load_dwordx4 v[48:51], v0, %[rs], s60 offen offset:0 sc1\nv_mfma_f32_16x16x32_bf16 v[16:19], a[4:7], v[52:55], v[16:19]\nv_mfma_f32_16x16x32_bf16 v[24:27], a[12:15], v[52:55], v[24:27]\nv_mfma_f32_16x16x32_bf16 v[32:35], a[20:23], v[52:55], v[32:35]\nv_mfma_f32_16x16x32_bf16 v[40:43], a[28:31], v[52:55], v[40:43]\ns_waitcnt vmcnt(15)\n" \
"buffer_load_dwordx4 v[52:55], v0, %[rs], s60 offen offset:2048 sc1\nv_mfma_f32_16x16x32_bf16 v[16:19], a[32:35], v[56:59], v[16:19]\ns_waitcnt lgkmcnt(1)\nv_mfma_f32_16x16x32_bf16 v[24:27], v[120:123], v[56:59], v[24:27]\nv_mfma_f32_16x16x32_bf16 v[32:35], a[40:43], v[56:59], v[32:35]\ns_add_u32 s60, s55, 69632\nv_mfma_f32_16x16x32_bf16 v[40:43], a[48:51], v[56:59], v[40:43]\ns_waitcnt vmcnt(15)\n" \
"buffer_load_dwordx4 v[56:59], v0, %[rs], s60 offen offset:0 sc1\nv_mfma_f32_16x16x32_bf16 v[16:19], a[36:39], v[60:63], v[16:19]\ns_waitcnt lgkmcnt(0)\nds_read_b128 v[120:123], v1 offset:2048\nv_mfma_f32_16x16x32_bf16 v[24:27], v[124:127], v[60:63], v[24:27]\nv_mfma_f32_16x16x32_bf16 v[32:35], a[44:47], v[60:63], v[32:35]\nv_mfma_f32_16x16x32_bf16 v[40:43], a[52:55], v[60:63], v[40:43]\ns_waitcnt vmcnt(15)\n" \
"buffer_load_dwordx4 v[60:63], v0, %[rs], s60 offen offset:2048 sc1\nv_mfma_f32_16x16x32_bf16 v[16:19], a[56:59], v[64:67], v[16:19]\nds_read_b128 v[124:127], v1 offset:3072\nv_mfma_f32_16x16x32_bf16 v[24:27], a[64:67], v[64:67], v[24:27]\nv_mfma_f32_16x16x32_bf16 v[32:35], a[72:75], v[64:67], v[32:35]\ns_add_u32 s60, s55, 81920\nv_mfma_f32_16x16x32_bf16 v[40:43], a[80:83], v[64:67], v[40:43]\ns_waitcnt vmcnt(15)\n" \
"buffer_load_dwordx4 v[64:67], v0, %[rs], s60 offen offset:0 sc1\nv_mfma_f32_16x16x32_bf16 v[16:19], a[60:63], v[68:71], v[16:19]\nv_mfma_f32_16x16x32_bf16 v[24:27], a[68:71], v[68:71], v[24:27]\nv_mfma_f32_16x16x32_bf16 v[32:35], a[76:79], v[68:71], v[32:35]\nv_mfma_f32_16x16x32_bf16 v[40:43], a[84:87], v[68:71], v[40:43]\ns_waitcnt vmcnt(15)\nbuffer_load_dwordx4 v[68:71], v0, %[rs], s60 offen offset:2048 sc1\nv_mfma_f32_16x16x32_bf16 v[16:19], a[88:91], v[72:75], v[16:19]\n" \
"v_mfma_f32_16x16x32_bf16 v[24:27], a[96:99], v[72:75], v[24:27]\nv_mfma_f32_16x16x32_bf16 v[32:35], a[104:107], v[72:75], v[32:35]\ns_waitcnt lgkmcnt(1)\ns_add_u32 s60, s55, 86016\nv_mfma_f32_16x16x32_bf16 v[40:43], v[120:123], v[72:75], v[40:43]\ns_waitcnt vmcnt(15)\nbuffer_load_dwordx4 v[72:75], v0, %[rs], s60 offen offset:0 sc1\nv_mfma_f32_16x16x32_bf16 v[16:19], a[92:95], v[76:79], v[16:19]\n" \
"ds_read_b128 v[120:123], v1 offset:4096\nv_mfma_f32_16x16x32_bf16 v[24:27], a[100:103], v[76:79], v[24:27]\nv_mfma_f32_16x16x32_bf16 v[32:35], a[108:111], v[76:79], v[32:35]\ns_waitcnt lgkmcnt(1)\nv_mfma_f32_16x16x32_bf16 v[40:43], v[124:127], v[76:79], v[40:43]\ns_waitcnt vmcnt(15)\nbuffer_load_dwordx4 v[76:79], v0, %[rs], s60 offen offset:2048 sc1\nv_mfma_f32_16x16x32_bf16 v[16:19], a[112:115], v[80:83], v[16:19]\n" \
"ds_read_b128 v[124:127], v1 offset:5120\nv_mfma_f32_16x16x32_bf16 v[24:27], a[120:123], v[80:83], v[24:27]\nv_mfma_f32_16x16x32_bf16 v[32:35], a[128:131], v[80:83], v[32:35]\ns_add_u32 s60, s55, 98304\nv_mfma_f32_16x16x32_bf16 v[40:43], a[136:139], v[80:83], v[40:43]\ns_waitcnt vmcnt(15)\nbuffer_load_dwordx4 v[80:83], v0, %[rs], s60 offen offset:0 sc1\nv_mfma_f32_16x16x32_bf16 v[16:19], a[116:119], v[84:87], v[16:19]\n" \
"ds_read_b128 v[112:115], v1 offset:6144\nv_mfma_f32_16x16x32_bf16 v[24:27], a[124:127], v[84:87], v[24:27]\nv_mfma_f32_16x16x32_bf16 v[32:35], a[132:135], v[84:87], v[32:35]\nv_mfma_f32_16x16x32_bf16 v[40:43], a[140:143], v[84:87], v[40:43]\ns_waitcnt vmcnt(15)\nbuffer_load_dwordx4 v[84:87], v0, %[rs], s60 offen offset:2048 sc1\nv_mfma_f32_16x16x32_bf16 v[16:19], a[144:147], v[88:91], v[16:19]\ns_waitcnt lgkmcnt(2)\n" \
"ds_read_b128 v[116:119], v1 offset:7168\nv_mfma_f32_16x16x32_bf16 v[24:27], v[120:123], v[88:91], v[24:27]\nv_mfma_f32_16x16x32_bf16 v[32:35], a[152:155], v[88:91], v[32:35]\ns_add_u32 s60, s55, 102400\nv_mfma_f32_16x16x32_bf16 v[40:43], a[160:163], v[88:91], v[40:43]\ns_waitcnt vmcnt(15)\nbuffer_load_dwordx4 v[88:91], v0, %[rs], s60 offen offset:0 sc1\nv_mfma_f32_16x16x32_bf16 v[16:19], a[148:151], v[92:95], v[16:19]\n" \
"s_waitcnt lgkmcnt(2)\nv_mfma_f32_16x16x32_bf16 v[24:27], v[124:127], v[92:95], v[24:27]\ns_waitcnt vmcnt(11)\nv_cmp_gt_i32 vcc, s58, v8\nv_mfma_f32_16x16x32_bf16 v[32:35], a[156:159], v[92:95], v[32:35]\ns_cmp_eq_u64 vcc, 0\ns_cselect_b32 s61, s55, -2147483648\nv_mfma_f32_16x16x32_bf16 v[40:43], a[164:167], v[92:95], v[40:43]\n" \
"buffer_load_dwordx4 v[92:95], v0, %[rs], s60 offen offset:2048 sc1\nv_mfma_f32_16x16x32_bf16 v[16:19], a[168:171], v[96:99], v[16:19]\nv_mfma_f32_16x16x32_bf16 v[24:27], a[176:179], v[96:99], v[24:27]\ns_waitcnt lgkmcnt(1)\nv_mfma_f32_16x16x32_bf16 v[32:35], v[112:115], v[96:99], v[32:35]\nv_mfma_f32_16x16x32_bf16 v[40:43], a[184:187], v[96:99], v[40:43]\ns_add_u32 s60, s61, 0\nbuffer_load_dwordx4 v[96:99], v0, %[rs], s60 offen offset:1024 sc1\n" \
"v_mfma_f32_16x16x32_bf16 v[16:19], a[172:175], v[100:103], v[16:19]\nds_read_b128 v[112:115], v1 offset:8192\nv_mfma_f32_16x16x32_bf16 v[24:27], a[180:183], v[100:103], v[24:27]\ns_waitcnt lgkmcnt(1)\nv_mfma_f32_16x16x32_bf16 v[32:35], v[116:119], v[100:103], v[32:35]\nv_mfma_f32_16x16x32_bf16 v[40:43], a[188:191], v[100:103], v[40:43]\nbuffer_load_dwordx4 v[100:103], v0, %[rs], s60 offen offset:3072 sc1\nv_mfma_f32_16x16x32_bf16 v[16:19], a[192:195], v[104:107], v[16:19]\n" \
"ds_read_b128 v[116:119], v1 offset:9216\nv_mfma_f32_16x16x32_bf16 v[24:27], a[200:203], v[104:107], v[24:27]\nv_mfma_f32_16x16x32_bf16 v[32:35], a[208:211], v[104:107], v[32:35]\nv_mfma_f32_16x16x32_bf16 v[40:43], a[216:219], v[104:107], v[40:43]\ns_add_u32 s60, s61, 4096\nbuffer_load_dwordx4 v[104:107], v0, %[rs], s60 offen offset:1024 sc1\nv_mfma_f32_16x16x32_bf16 v[16:19], a[196:199], v[108:111], v[16:19]\nv_mfma_f32_16x16x32_bf16 v[24:27], a[204:207], v[108:111], v[24:27]\n" \
"v_mfma_f32_16x16x32_bf16 v[32:35], a[212:215], v[108:111], v[32:35]\nv_mfma_f32_16x16x32_bf16 v[40:43], a[220:223], v[108:111], v[40:43]\ns_waitcnt vmcnt(14)\ns_waitcnt lgkmcnt(1)\nbuffer_load_dwordx4 v[108:111], v0, %[rs], s60 offen offset:3072 sc1\nv_mfma_f32_16x16x32_bf16 v[16:19], v[112:115], v[48:51], v[16:19]\nv_mfma_f32_16x16x32_bf16 v[24:27], a[224:227], v[48:51], v[24:27]\nv_mfma_f32_16x16x32_bf16 v[32:35], a[232:235], v[48:51], v[32:35]\n" \
"s_add_u32 s60, s61, 16384\nv_mfma_f32_16x16x32_bf16 v[40:43], a[240:243], v[48:51], v[40:43]\ns_waitcnt vmcnt(14)\ns_waitcnt lgkmcnt(0)\nbuffer_load_dwordx4 v[48:51], v0, %[rs], s60 offen offset:1024 sc1\nv_mfma_f32_16x16x32_bf16 v[16:19], v[116:119], v[52:55], v[16:19]\nds_read_b128 v[112:115], v1 offset:10240\nv_mfma_f32_16x16x32_bf16 v[24:27], a[228:231], v[52:55], v[24:27]\n" \
"v_mfma_f32_16x16x32_bf16 v[32:35], a[236:239], v[52:55], v[32:35]\nv_mfma_f32_16x16x32_bf16 v[40:43], a[244:247], v[52:55], v[40:43]\ns_waitcnt vmcnt(14)\nbuffer_load_dwordx4 v[52:55], v0, %[rs], s60 offen offset:3072 sc1\nv_mfma_f32_16x16x32_bf16 v[16:19], a[248:251], v[56:59], v[16:19]\nds_read_b128 v[116:119], v1 offset:11264\nv_mfma_f32_16x16x32_bf16 v[24:27], v[128:131], v[56:59], v[24:27]\nv_mfma_f32_16x16x32_bf16 v[32:35], v[136:139], v[56:59], v[32:35]\n" \
"s_add_u32 s60, s61, 20480\nv_mfma_f32_16x16x32_bf16 v[40:43], v[144:147], v[56:59], v[40:43]\ns_waitcnt vmcnt(14)\nbuffer_load_dwordx4 v[56:59], v0, %[rs], s60 offen offset:1024 sc1\nv_mfma_f32_16x16x32_bf16 v[16:19], a[252:255], v[60:63], v[16:19]\nv_mfma_f32_16x16x32_bf16 v[24:27], v[132:135], v[60:63], v[24:27]\nv_mfma_f32_16x16x32_bf16 v[32:35], v[140:143], v[60:63], v[32:35]\nv_mfma_f32_16x16x32_bf16 v[40:43], v[148:151], v[60:63], v[40:43]\n" \
"s_waitcnt vmcnt(14)\nbuffer_load_dwordx4 v[60:63], v0, %[rs], s60 offen offset:3072 sc1\nv_mfma_f32_16x16x32_bf16 v[16:19], v[152:155], v[64:67], v[16:19]\nv_mfma_f32_16x16x32_bf16 v[24:27], v[160:163], v[64:67], v[24:27]\ns_waitcnt lgkmcnt(1)\nv_mfma_f32_16x16x32_bf16 v[32:35], v[112:115], v[64:67], v[32:35]\ns_add_u32 s60, s61, 32768\nv_mfma_f32_16x16x32_bf16 v[40:43], v[168:171], v[64:67], v[40:43]\n" \
"s_waitcnt vmcnt(14)\nbuffer_load_dwordx4 v[64:67], v0, %[rs], s60 offen offset:1024 sc1\nv_mfma_f32_16x16x32_bf16 v[16:19], v[156:159], v[68:71], v[16:19]\nds_read_b128 v[112:115], v1 offset:12288\nv_mfma_f32_16x16x32_bf16 v[24:27], v[164:167], v[68:71], v[24:27]\ns_waitcnt lgkmcnt(1)\nv_mfma_f32_16x16x32_bf16 v[32:35], v[116:119], v[68:71], v[32:35]\nv_mfma_f32_16x16x32_bf16 v[40:43], v[172:175], v[68:71], v[40:43]\n" \
"s_waitcnt vmcnt(14)\nbuffer_load_dwordx4 v[68:71], v0, %[rs], s60 offen offset:3072 sc1\nv_mfma_f32_16x16x32_bf16 v[16:19], v[176:179], v[72:75], v[16:19]\nds_read_b128 v[116:119], v1 offset:13312\nv_mfma_f32_16x16x32_bf16 v[24:27], v[184:187], v[72:75], v[24:27]\nv_mfma_f32_16x16x32_bf16 v[32:35], v[192:195], v[72:75], v[32:35]\ns_add_u32 s60, s61, 36864\nv_mfma_f32_16x16x32_bf16 v[40:43], v[200:203], v[72:75], v[40:43]\n" \
"s_waitcnt vmcnt(14)\nbuffer_load_dwordx4 v[72:75], v0, %[rs], s60 offen offset:1024 sc1\nv_mfma_f32_16x16x32_bf16 v[16:19], v[180:183], v[76:79], v[16:19]\nds_read_b128 v[120:123], v1 offset:14336\nv_mfma_f32_16x16x32_bf16 v[24:27], v[188:191], v[76:79], v[24:27]\nv_mfma_f32_16x16x32_bf16 v[32:35], v[196:199], v[76:79], v[32:35]\nv_mfma_f32_16x16x32_bf16 v[40:43], v[204:207], v[76:79], v[40:43]\ns_waitcnt vmcnt(14)\n" \
"s_waitcnt lgkmcnt(2)\nbuffer_load_dwordx4 v[76:79], v0, %[rs], s60 offen offset:3072 sc1\nv_mfma_f32_16x16x32_bf16 v[16:19], v[112:115], v[80:83], v[16:19]\nds_read_b128 v[124:127], v1 offset:15360\nv_mfma_f32_16x16x32_bf16 v[24:27], v[208:211], v[80:83], v[24:27]\nv_mfma_f32_16x16x32_bf16 v[32:35], v[216:219], v[80:83], v[32:35]\ns_add_u32 s60, s61, 49152\nv_mfma_f32_16x16x32_bf16 v[40:43], v[224:227], v[80:83], v[40:43]\n" \
"s_waitcnt vmcnt(14)\ns_waitcnt lgkmcnt(2)\nbuffer_load_dwordx4 v[80:83], v0, %[rs], s60 offen offset:1024 sc1\nv_mfma_f32_16x16x32_bf16 v[16:19], v[116:119], v[84:87], v[16:19]\nv_mfma_f32_16x16x32_bf16 v[24:27], v[212:215], v[84:87], v[24:27]\nv_mfma_f32_16x16x32_bf16 v[32:35], v[220:223], v[84:87], v[32:35]\nv_mfma_f32_16x16x32_bf16 v[40:43], v[228:231], v[84:87], v[40:43]\ns_waitcnt vmcnt(14)\n" \
"buffer_load_dwordx4 v[84:87], v0, %[rs], s60 offen offset:3072 sc1\nv_mfma_f32_16x16x32_bf16 v[16:19], v[232:235], v[88:91], v[16:19]\ns_waitcnt lgkmcnt(1)\nv_mfma_f32_16x16x32_bf16 v[24:27], v[120:123], v[88:91], v[24:27]\nv_mfma_f32_16x16x32_bf16 v[32:35], v[240:243], v[88:91], v[32:35]\ns_add_u32 s60, s61, 53248\nv_mfma_f32_16x16x32_bf16 v[40:43], v[248:251], v[88:91], v[40:43]\ns_waitcnt vmcnt(14)\n" \
"buffer_load_dwordx4 v[88:91], v0, %[rs], s60 offen offset:1024 sc1\nv_mfma_f32_16x16x32_bf16 v[16:19], v[236:239], v[92:95], v[16:19]\ns_waitcnt lgkmcnt(0)\nds_read_b128 v[120:123], v1 offset:0\nv_mfma_f32_16x16x32_bf16 v[24:27], v[124:127], v[92:95], v[24:27]\nv_mfma_f32_16x16x32_bf16 v[32:35], v[244:247], v[92:95], v[32:35]\nv_mfma_f32_16x16x32_bf16 v[40:43], v[252:255], v[92:95], v[40:43]\nbuffer_load_dwordx4 v[92:95], v0, %[rs], s60 offen offset:3072 sc1\n" \
"s_cbranch_vccnz .Ldd_slow%=\n.Ldd_slowr%=:\ns_waitcnt vmcnt(15)\nv_mfma_f32_16x16x32_bf16 v[20:23], a[0:3], v[96:99], 0\nds_read_b128 v[124:127], v1 offset:1024\nv_mfma_f32_16x16x32_bf16 v[28:31], a[8:11], v[96:99], 0\nv_mfma_f32_16x16x32_bf16 v[36:39], a[16:19], v[96:99], 0\ns_add_u32 s60, s55, 65536\n" \
"v_mfma_f32_16x16x32_bf16 v[44:47], a[24:27], v[96:99], 0\ns_waitcnt vmcnt(14)\nbuffer_load_dwordx4 v[96:99], v0, %[rs], s60 offen offset:1024 sc1\nv_mfma_f32_16x16x32_bf16 v[20:23], a[4:7], v[100:103], v[20:23]\nv_mfma_f32_16x16x32_bf16 v[28:31], a[12:15], v[100:103], v[28:31]\nv_mfma_f32_16x16x32_bf16 v[36:39], a[20:23], v[100:103], v[36:39]\nv_mfma_f32_16x16x32_bf16 v[44:47], a[28:31], v[100:103], v[44:47]\ns_waitcnt vmcnt(14)\n" \
"buffer_load_dwordx4 v[100:103], v0, %[rs], s60 offen offset:3072 sc1\nv_mfma_f32_16x16x32_bf16 v[20:23], a[32:35], v[104:107], v[20:23]\ns_waitcnt lgkmcnt(1)\nv_mfma_f32_16x16x32_bf16 v[28:31], v[120:123], v[104:107], v[28:31]\nds_write_b128 v2, v[16:19] offset:0\nv_mfma_f32_16x16x32_bf16 v[36:39], a[40:43], v[104:107], v[36:39]\ns_add_u32 s60, s55, 69632\nv_mfma_f32_16x16x32_bf16 v[44:47], a[48:51], v[104:107], v[44:47]\n" \
"s_waitcnt vmcnt(14)\nbuffer_load_dwordx4 v[104:107], v0, %[rs], s60 offen offset:1024 sc1\nv_mfma_f32_16x16x32_bf16 v[20:23], a[36:39], v[108:111], v[20:23]\ns_waitcnt lgkmcnt(1)\nds_read_b128 v[120:123], v1 offset:2048\nv_mfma_f32_16x16x32_bf16 v[28:31], v[124:127], v[108:111], v[28:31]\nv_mfma_f32_16x16x32_bf16 v[36:39], a[44:47], v[108:111], v[36:39]\nv_mfma_f32_16x16x32_bf16 v[44:47], a[52:55], v[108:111], v[44:47]\n" \
"s_waitcnt vmcnt(14)\nbuffer_load_dwordx4 v[108:111], v0, %[rs], s60 offen offset:3072 sc1\nv_mfma_f32_16x16x32_bf16 v[20:23], a[56:59], v[48:51], v[20:23]\nds_read_b128 v[124:127], v1 offset:3072\nv_mfma_f32_16x16x32_bf16 v[28:31], a[64:67], v[48:51], v[28:31]\nds_write_b128 v2, v[24:27] offset:2048\nv_mfma_f32_16x16x32_bf16 v[36:39], a[72:75], v[48:51], v[36:39]\ns_add_u32 s60, s55, 81920\n" \
"v_mfma_f32_16x16x32_bf16 v[44:47], a[80:83], v[48:51], v[44:47]\ns_waitcnt vmcnt(14)\nbuffer_load_dwordx4 v[48:51], v0, %[rs], s60 offen offset:1024 sc1\nv_mfma_f32_16x16x32_bf16 v[20:23], a[60:63], v[52:55], v[20:23]\nv_mfma_f32_16x16x32_bf16 v[28:31], a[68:71], v[52:55], v[28:31]\nv_mfma_f32_16x16x32_bf16 v[36:39], a[76:79], v[52:55], v[36:39]\nv_mfma_f32_16x16x32_bf16 v[44:47], a[84:87], v[52:55], v[44:47]\ns_waitcnt vmcnt(14)\n" \
"buffer_load_dwordx4 v[52:55], v0, %[rs], s60 offen offset:3072 sc1\nv_mfma_f32_16x16x32_bf16 v[20:23], a[88:91], v[56:59], v[20:23]\nv_mfma_f32_16x16x32_bf16 v[28:31], a[96:99], v[56:59], v[28:31]\nds_write_b128 v2, v[32:35] offset:4096\nv_mfma_f32_16x16x32_bf16 v[36:39], a[104:107], v[56:59], v[36:39]\ns_waitcnt lgkmcnt(3)\ns_add_u32 s60, s55, 86016\nv_mfma_f32_16x16x32_bf16 v[44:47], v[120:123], v[56:59], v[44:47]\n" \
"s_waitcnt vmcnt(14)\nbuffer_load_dwordx4 v[56:59], v0, %[rs], s60 offen offset:1024 sc1\nv_mfma_f32_16x16x32_bf16 v[20:23], a[92:95], v[60:63], v[20:23]\nds_read_b128 v[120:123], v1 offset:4096\nv_mfma_f32_16x16x32_bf16 v[28:31], a[100:103], v[60:63], v[28:31]\nv_mfma_f32_16x16x32_bf16 v[36:39], a[108:111], v[60:63], v[36:39]\ns_waitcnt lgkmcnt(3)\nv_mfma_f32_16x16x32_bf16 v[44:47], v[124:127], v[60:63], v[44:47]\n" \
"s_waitcnt vmcnt(14)\nbuffer_load_dwordx4 v[60:63], v0, %[rs], s60 offen offset:3072 sc1\nv_mfma_f32_16x16x32_bf16 v[20:23], a[112:115], v[64:67], v[20:23]\nds_read_b128 v[124:127], v1 offset:5120\nv_mfma_f32_16x16x32_bf16 v[28:31], a[120:123], v[64:67], v[28:31]\nds_write_b128 v2, v[40:43] offset:6144\nv_mfma_f32_16x16x32_bf16 v[36:39], a[128:131], v[64:67], v[36:39]\ns_add_u32 s60, s55, 98304\n" \
"v_mfma_f32_16x16x32_bf16 v[44:47], a[136:139], v[64:67], v[44:47]\ns_waitcnt vmcnt(14)\nbuffer_load_dwordx4 v[64:67], v0, %[rs], s60 offen offset:1024 sc1\nv_mfma_f32_16x16x32_bf16 v[20:23], a[116:119], v[68:71], v[20:23]\nds_read_b128 v[112:115], v1 offset:6144\nv_mfma_f32_16x16x32_bf16 v[28:31], a[124:127], v[68:71], v[28:31]\nv_mfma_f32_16x16x32_bf16 v[36:39], a[132:135], v[68:71], v[36:39]\nv_mfma_f32_16x16x32_bf16 v[44:47], a[140:143], v[68:71], v[44:47]\n" \
"s_waitcnt vmcnt(14)\nbuffer_load_dwordx4 v[68:71], v0, %[rs], s60 offen offset:3072 sc1\nv_mfma_f32_16x16x32_bf16 v[20:23], a[144:147], v[72:75], v[20:23]\ns_waitcnt lgkmcnt(3)\nds_read_b128 v[116:119], v1 offset:7168\nv_mfma_f32_16x16x32_bf16 v[28:31], v[120:123], v[72:75], v[28:31]\nv_mfma_f32_16x16x32_bf16 v[36:39], a[152:155], v[72:75], v[36:39]\ns_add_u32 s60, s55, 102400\n" \
"v_mfma_f32_16x16x32_bf16 v[44:47], a[160:163], v[72:75], v[44:47]\ns_waitcnt vmcnt(14)\nbuffer_load_dwordx4 v[72:75], v0, %[rs], s60 offen offset:1024 sc1\nv_mfma_f32_16x16x32_bf16 v[20:23], a[148:151], v[76:79], v[20:23]\ns_waitcnt lgkmcnt(3)\nv_mfma_f32_16x16x32_bf16 v[28:31], v[124:127], v[76:79], v[28:31]\nv_mfma_f32_16x16x32_bf16 v[36:39], a[156:159], v[76:79], v[36:39]\nv_mfma_f32_16x16x32_bf16 v[44:47], a[164:167], v[76:79], v[44:47]\n" \
"s_waitcnt vmcnt(14)\nbuffer_load_dwordx4 v[76:79], v0, %[rs], s60 offen offset:3072 sc1\nv_mfma_f32_16x16x32_bf16 v[20:23], a[168:171], v[80:83], v[20:23]\nv_mfma_f32_16x16x32_bf16 v[28:31], a[176:179], v[80:83], v[28:31]\ns_waitcnt lgkmcnt(1)\nv_mfma_f32_16x16x32_bf16 v[36:39], v[112:115], v[80:83], v[36:39]\nv_mfma_f32_16x16x32_bf16 v[44:47], a[184:187], v[80:83], v[44:47]\ns_waitcnt vmcnt(14)\n" \
"buffer_load_dwordx4 v[12:15], v4, %[rs], s41 offen sc1\nv_mfma_f32_16x16x32_bf16 v[20:23], a[172:175], v[84:87], v[20:23]\nds_read_b128 v[112:115], v1 offset:8192\nv_mfma_f32_16x16x32_bf16 v[28:31], a[180:183], v[84:87], v[28:31]\ns_waitcnt lgkmcnt(1)\nv_mfma_f32_16x16x32_bf16 v[36:39], v[116:119], v[84:87], v[36:39]\nv_mfma_f32_16x16x32_bf16 v[44:47], a[188:191], v[84:87], v[44:47]\ns_waitcnt vmcnt(14)\n" \
"v_mfma_f32_16x16x32_bf16 v[20:23], a[192:195], v[88:91], v[20:23]\nds_read_b128 v[116:119], v1 offset:9216\nv_mfma_f32_16x16x32_bf16 v[28:31], a[200:203], v[88:91], v[28:31]\nv_mfma_f32_16x16x32_bf16 v[36:39], a[208:211], v[88:91], v[36:39]\nv_mfma_f32_16x16x32_bf16 v[44:47], a[216:219], v[88:91], v[44:47]\ns_waitcnt vmcnt(13)\nv_mfma_f32_16x16x32_bf16 v[20:23], a[196:199], v[92:95], v[20:23]\nv_mfma_f32_16x16x32_bf16 v[28:31], a[204:207], v[92:95], v[28:31]\n" \
"v_mfma_f32_16x16x32_bf16 v[36:39], a[212:215], v[92:95], v[36:39]\nv_mfma_f32_16x16x32_bf16 v[44:47], a[220:223], v[92:95], v[44:47]\ns_waitcnt vmcnt(12)\ns_waitcnt lgkmcnt(1)\nv_mfma_f32_16x16x32_bf16 v[20:23], v[112:115], v[96:99], v[20:23]\nv_mfma_f32_16x16x32_bf16 v[28:31], a[224:227], v[96:99], v[28:31]\nv_mfma_f32_16x16x32_bf16 v[36:39], a[232:235], v[96:99], v[36:39]\nv_mfma_f32_16x16x32_bf16 v[44:47], a[240:243], v[96:99], v[44:47]\n" \
"s_waitcnt vmcnt(11)\ns_waitcnt lgkmcnt(0)\nv_mfma_f32_16x16x32_bf16 v[20:23], v[116:119], v[100:103], v[20:23]\nds_read_b128 v[112:115], v1 offset:10240\nv_mfma_f32_16x16x32_bf16 v[28:31], a[228:231], v[100:103], v[28:31]\nv_mfma_f32_16x16x32_bf16 v[36:39], a[236:239], v[100:103], v[36:39]\nv_mfma_f32_16x16x32_bf16 v[44:47], a[244:247], v[100:103], v[44:47]\ns_waitcnt vmcnt(10)\n" \
"v_mfma_f32_16x16x32_bf16 v[20:23], a[248:251], v[104:107], v[20:23]\nds_read_b128 v[116:119], v1 offset:11264\nv_mfma_f32_16x16x32_bf16 v[28:31], v[128:131], v[104:107], v[28:31]\nv_mfma_f32_16x16x32_bf16 v[36:39], v[136:139], v[104:107], v[36:39]\nv_mfma_f32_16x16x32_bf16 v[44:47], v[144:147], v[104:107], v[44:47]\ns_waitcnt vmcnt(9)\nv_mfma_f32_16x16x32_bf16 v[20:23], a[252:255], v[108:111], v[20:23]\nv_mfma_f32_16x16x32_bf16 v[28:31], v[132:135], v[108:111], v[28:31]\n" \
"v_mfma_f32_16x16x32_bf16 v[36:39], v[140:143], v[108:111], v[36:39]\nv_mfma_f32_16x16x32_bf16 v[44:47], v[148:151], v[108:111], v[44:47]\ns_waitcnt vmcnt(8)\nv_mfma_f32_16x16x32_bf16 v[20:23], v[152:155], v[48:51], v[20:23]\nv_mfma_f32_16x16x32_bf16 v[28:31], v[160:163], v[48:51], v[28:31]\ns_waitcnt lgkmcnt(1)\nv_mfma_f32_16x16x32_bf16 v[36:39], v[112:115], v[48:51], v[36:39]\nv_mfma_f32_16x16x32_bf16 v[44:47], v[168:171], v[48:51], v[44:47]\n" \
"s_waitcnt vmcnt(7)\nv_mfma_f32_16x16x32_bf16 v[20:23], v[156:159], v[52:55], v[20:23]\nds_read_b128 v[112:115], v1 offset:12288\nv_mfma_f32_16x16x32_bf16 v[28:31], v[164:167], v[52:55], v[28:31]\ns_waitcnt lgkmcnt(1)\nv_mfma_f32_16x16x32_bf16 v[36:39], v[116:119], v[52:55], v[36:39]\nv_mfma_f32_16x16x32_bf16 v[44:47], v[172:175], v[52:55], v[44:47]\ns_waitcnt vmcnt(6)\n" \
"v_mfma_f32_16x16x32_bf16 v[20:23], v[176:179], v[56:59], v[20:23]\nds_read_b128 v[116:119], v1 offset:13312\nv_mfma_f32_16x16x32_bf16 v[28:31], v[184:187], v[56:59], v[28:31]\nv_mfma_f32_16x16x32_bf16 v[36:39], v[192:195], v[56:59], v[36:39]\nv_mfma_f32_16x16x32_bf16 v[44:47], v[200:203], v[56:59], v[44:47]\ns_waitcnt vmcnt(5)\nv_mfma_f32_16x16x32_bf16 v[20:23], v[180:183], v[60:63], v[20:23]\nds_read_b128 v[120:123], v1 offset:14336\n" \
"v_mfma_f32_16x16x32_bf16 v[28:31], v[188:191], v[60:63], v[28:31]\nv_mfma_f32_16x16x32_bf16 v[36:39], v[196:199], v[60:63], v[36:39]\nv_mfma_f32_16x16x32_bf16 v[44:47], v[204:207], v[60:63], v[44:47]\ns_waitcnt vmcnt(4)\ns_waitcnt lgkmcnt(2)\nv_mfma_f32_16x16x32_bf16 v[20:23], v[112:115], v[64:67], v[20:23]\nds_read_b128 v[124:127], v1 offset:15360\nv_mfma_f32_16x16x32_bf16 v[28:31], v[208:211], v[64:67], v[28:31]\n" \
"v_mfma_f32_16x16x32_bf16 v[36:39], v[216:219], v[64:67], v[36:39]\nv_mfma_f32_16x16x32_bf16 v[44:47], v[224:227], v[64:67], v[44:47]\ns_waitcnt vmcnt(3)\ns_waitcnt lgkmcnt(2)\nv_mfma_f32_16x16x32_bf16 v[20:23], v[116:119], v[68:71], v[20:23]\nv_mfma_f32_16x16x32_bf16 v[28:31], v[212:215], v[68:71], v[28:31]\nv_mfma_f32_16x16x32_bf16 v[36:39], v[220:223], v[68:71], v[36:39]\nv_mfma_f32_16x16x32_bf16 v[44:47], v[228:231], v[68:71], v[44:47]\n" \
"s_waitcnt vmcnt(2)\nv_mfma_f32_16x16x32_bf16 v[20:23], v[232:235], v[72:75], v[20:23]\ns_waitcnt lgkmcnt(1)\nv_mfma_f32_16x16x32_bf16 v[28:31], v[120:123], v[72:75], v[28:31]\nv_mfma_f32_16x16x32_bf16 v[36:39], v[240:243], v[72:75], v[36:39]\nv_mfma_f32_16x16x32_bf16 v[44:47], v[248:251], v[72:75], v[44:47]\ns_waitcnt vmcnt(1)\nv_mfma_f32_16x16x32_bf16 v[20:23], v[236:239], v[76:79], v[20:23]\n" \
"s_waitcnt vmcnt(0)\ns_waitcnt lgkmcnt(0)\nv_lshlrev_b32 v80, 16, v12\nv_and_b32 v81, 0xffff0000, v12\nv_mfma_f32_16x16x32_bf16 v[28:31], v[124:127], v[76:79], v[28:31]\nv_lshlrev_b32 v82, 16, v13\nv_and_b32 v83, 0xffff0000, v13\nv_mfma_f32_16x16x32_bf16 v[36:39], v[244:247], v[76:79], v[36:39]\n" \
"v_lshlrev_b32 v84, 16, v14\nv_and_b32 v85, 0xffff0000, v14\nv_mfma_f32_16x16x32_bf16 v[44:47], v[252:255], v[76:79], v[44:47]\ns_mul_i32 s44, s40, %[l]\nds_write_b128 v2, v[20:23] offset:1024\nds_write_b128 v2, v[28:31] offset:3072\ns_nop 0\nds_write_b128 v2, v[36:39] offset:5120\n" \
"s_nop 2\nds_write_b128 v2, v[44:47] offset:7168\nv_lshlrev_b32 v86, 16, v15\nv_and_b32 v87, 0xffff0000, v15\nv_mov_b32 v9, s40\ns_cmp_eq_u32 s44, 0\ns_cselect_b32 s56, %[fd], -2147483648\ns_add_u32 s40, s40, 1\n" \
"s_waitcnt lgkmcnt(0)\ns_barrier\nds_read_b128 v[16:19], v3 offset:0\nds_read_b128 v[20:23], v3 offset:256\nds_read_b128 v[24:27], v3 offset:8192\nds_read_b128 v[28:31], v3 offset:8448\nds_read_b128 v[32:35], v3 offset:16384\nds_read_b128 v[36:39], v3 offset:16640\n" \
"ds_read_b128 v[40:43], v3 offset:24576\nds_read_b128 v[44:47], v3 offset:24832\ns_waitcnt lgkmcnt(6)\nv_pk_add_f32 v[16:17], v[16:17], v[80:81]\nv_pk_add_f32 v[18:19], v[18:19], v[82:83]\nv_pk_add_f32 v[20:21], v[20:21], v[84:85]\nv_pk_add_f32 v[22:23], v[22:23], v[86:87]\ns_waitcnt lgkmcnt(4)\n" \
"v_pk_add_f32 v[16:17], v[16:17], v[24:25]\nv_pk_add_f32 v[18:19], v[18:19], v[26:27]\nv_pk_add_f32 v[20:21], v[20:21], v[28:29]\nv_pk_add_f32 v[22:23], v[22:23], v[30:31]\ns_waitcnt lgkmcnt(2)\nv_pk_add_f32 v[16:17], v[16:17], v[32:33]\nv_pk_add_f32 v[18:19], v[18:19], v[34:35]\nv_pk_add_f32 v[20:21], v[20:21], v[36:37]\n" \
"v_pk_add_f32 v[22:23], v[22:23], v[38:39]\ns_waitcnt lgkmcnt(0)\nv_pk_add_f32 v[16:17], v[16:17], v[40:41]\nv_pk_add_f32 v[18:19], v[18:19], v[42:43]\nv_pk_add_f32 v[20:21], v[20:21], v[44:45]\nv_pk_add_f32 v[22:23], v[22:23], v[46:47]\nv_cvt_pk_bf16_f32 v12, v16, v17\nv_cvt_pk_bf16_f32 v13, v18, v19\n" \
"v_cvt_pk_bf16_f32 v14, v20, v21\nv_cvt_pk_bf16_f32 v15, v22, v23\nbuffer_store_dwordx4 v[12:15], v5, %[ro], s42 offen sc1\nv_pk_add_f32 v[24:25], v[16:17], v[18:19]\nv_pk_add_f32 v[26:27], v[20:21], v[22:23]\nv_pk_add_f32 v[24:25], v[24:25], v[26:27]\nv_add_f32 v24, v24, v25\ns_nop 1\n" \
"v_add_f32_dpp v25, v24, v24 row_ror:8 row_mask:0xf bank_mask:0xf\nv_add_f32_dpp v26, v24, v24 row_ror:8 row_mask:0xf bank_mask:0xf\ns_nop 1\nv_permlane16_swap_b32 v25, v26\nv_add_f32 v24, v25, v26\nv_add_f32 v25, v25, v26\ns_nop 1\nv_permlane32_swap_b32 v24, v25\n" \
"v_add_f32 v24, v24, v25\nv_mul_f32 v24, 0x3c800000, v24\nv_pk_add_f32 v[16:17], v[16:17], v[24:25] op_sel_hi:[1,0] neg_lo:[0,1] neg_hi:[0,1]\nv_pk_add_f32 v[18:19], v[18:19], v[24:25] op_sel_hi:[1,0] neg_lo:[0,1] neg_hi:[0,1]\nv_pk_add_f32 v[20:21], v[20:21], v[24:25] op_sel_hi:[1,0] neg_lo:[0,1] neg_hi:[0,1]\nv_pk_add_f32 v[22:23], v[22:23], v[24:25] op_sel_hi:[1,0] neg_lo:[0,1] neg_hi:[0,1]\nv_pk_mul_f32 v[28:29], v[16:17], v[16:17]\nv_pk_mul_f32 v[30:31], v[18:19], v[18:19]\n" \
"v_pk_fma_f32 v[28:29], v[20:21], v[20:21], v[28:29]\nv_pk_fma_f32 v[30:31], v[22:23], v[22:23], v[30:31]\nv_pk_add_f32 v[28:29], v[28:29], v[30:31]\nv_add_f32 v28, v28, v29\ns_nop 1\nv_add_f32_dpp v29, v28, v28 row_ror:8 row_mask:0xf bank_mask:0xf\nv_add_f32_dpp v30, v28, v28 row_ror:8 row_mask:0xf bank_mask:0xf\ns_nop 1\n" \
"v_permlane16_swap_b32 v29, v30\nv_add_f32 v28, v29, v30\nv_add_f32 v29, v29, v30\ns_nop 1\nv_permlane32_swap_b32 v28, v29\nv_add_f32 v28, v28, v29\nv_mov_b32 v25, v28\nbuffer_store_dwordx2 v[24:25], v6, %[rs], s41 offen sc1\n" \
"v_xor_b32 v2, 0x8000, v2\nv_xor_b32 v3, 0x8000, v3\ns_cmp_lt_u32 s40, 64\ns_cbranch_scc1 .Ldd_next%=\ns_waitcnt vmcnt(0)\nbuffer_store_dword v9, off, %[rs], s56 sc1\ns_branch .Ldd_end%=\n.Ldd_slow%=:\n" \
".Ldd_w1%=:\nv_cmp_gt_i32 vcc, s58, v8\ns_cbranch_vccz .Ldd_w1go%=\ns_cmp_ge_u32 s40, 62\ns_cbranch_scc1 .Ldd_ns1219%=\ns_sleep 16\n.Ldd_ns1219%=:\ns_sleep 1\n" \
"buffer_load_dword v8, v7, %[rs], 0 offen sc1\ns_waitcnt vmcnt(0)\ns_add_u32 s43, s43, 1\ns_cmp_lt_u32 s43, 262144\ns_cbranch_scc1 .Ldd_w1%=\ns_branch .Ldd_abort%=\n.Ldd_w1go%=:\ns_add_u32 s60, s55, 0\n" \
"buffer_load_dwordx4 v[96:99], v0, %[rs], s60 offen offset:1024 sc1\nbuffer_load_dwordx4 v[100:103], v0, %[rs], s60 offen offset:3072 sc1\ns_add_u32 s60, s55, 4096\nbuffer_load_dwordx4 v[104:107], v0, %[rs], s60 offen offset:1024 sc1\nbuffer_load_dwordx4 v[108:111], v0, %[rs], s60 offen offset:3072 sc1\ns_add_u32 s60, s55, 16384\nbuffer_load_dwordx4 v[48:51], v0, %[rs], s60 offen offset:1024 sc1\nbuffer_load_dwordx4 v[52:55], v0, %[rs], s60 offen offset:3072 sc1\n" \
"s_add_u32 s60, s55, 20480\nbuffer_load_dwordx4 v[56:59], v0, %[rs], s60 offen offset:1024 sc1\nbuffer_load_dwordx4 v[60:63], v0, %[rs], s60 offen offset:3072 sc1\ns_add_u32 s60, s55, 32768\nbuffer_load_dwordx4 v[64:67], v0, %[rs], s60 offen offset:1024 sc1\nbuffer_load_dwordx4 v[68:71], v0, %[rs], s60 offen offset:3072 sc1\ns_add_u32 s60, s55, 36864\nbuffer_load_dwordx4 v[72:75], v0, %[rs], s60 offen offset:1024 sc1\n" \
"buffer_load_dwordx4 v[76:79], v0, %[rs], s60 offen offset:3072 sc1\ns_add_u32 s60, s55, 49152\nbuffer_load_dwordx4 v[80:83], v0, %[rs], s60 offen offset:1024 sc1\nbuffer_load_dwordx4 v[84:87], v0, %[rs], s60 offen offset:3072 sc1\ns_add_u32 s60, s55, 53248\nbuffer_load_dwordx4 v[88:91], v0, %[rs], s60 offen offset:1024 sc1\nbuffer_load_dwordx4 v[92:95], v0, %[rs], s60 offen offset:3072 sc1\ns_branch .Ldd_slowr%=\n" \
".Ldd_abort%=:\ns_mov_b64 exec, -1\nv_mov_b32 v16, 0\ns_movk_i32 s44, 6400\nbuffer_store_dword v16, off, %[rs], s44 sc1\n.Ldd_end%=:\ns_waitcnt vmcnt(0) lgkmcnt(0)\n" \
""
#define G1F 1
#define A1F 0

#define V10(p, x) p #x "0", p #x "1", p #x "2", p #x "3", p #x "4", p #x "5", p #x "6", p #x "7", p #x "8", p #x "9"
#define V256(p) V10(p, ), V10(p, 1), V10(p, 2), V10(p, 3), V10(p, 4), V10(p, 5), V10(p, 6), V10(p, 7), V10(p, 8), V10(p, 9), V10(p, 10), V10(p, 11), V10(p, 12), V10(p, 13), V10(p, 14), V10(p, 15), V10(p, 16), \
    V10(p, 17), V10(p, 18), V10(p, 19), V10(p, 20), V10(p, 21), V10(p, 22), V10(p, 23), V10(p, 24), p "250", p "251", p "252", p "253", p "254", p "255"
#define CLOB "memory", "vcc", "scc", V256("v"), V256("a"), V10("s", 4), V10("s", 5), V10("s", 6), V10("s", 7), V10("s", 8)
template <int N> DEVI void table(lds_char* p, const unsigned (&t)[N]) {
#pragma unroll
    for (int i = 0; i < N; ++i) *(l1*)(p + i * 256) = t[i];
    WL0;
}
DEVI unsigned prof_at(int cu, int w) { return O_PROF + (262144 + cu * 2560 + w * 8) * 4; }
DEVI unsigned sp_idx(int lane) { return (lane & 2) ? 0xEEEEu : 0x4444u; }
DEVI int sp_row(int r, int hf) { return 4 * (r >> 1) + 2 * hf + (r & 1); }

DEVI void qk_role(const Args& a, const int w, lds_char* smem, int l, int h) {
    const int lane = lane_id(), g = lane >> 4, n = lane & 15, b = n & 3, p = n >> 2;
    const unsigned l0 = (unsigned)(size_t)smem, tb = l0 + w * 12288, lw = l0 + 55296 + w * 24576, ls = l * SD, IMG = l0 + 32768, MIM = l0 + 49152;
    const unsigned xw = (w >> 1) * 16384 + (w & 1) * 4096, oc = (h >> 3) * 16384 + ls, tr = (8 * g + p) * 128 + b * 16;
    const unsigned cm[4] = {4 * g > n ? 0xF149F2CAu : 0u, 4 * g + 1 > n ? 0xF149F2CAu : 0u, 4 * g + 2 > n ? 0xF149F2CAu : 0u, 4 * g + 3 > n ? 0xF149F2CAu : 0u};
    const unsigned t[19] = {
        l0 + lane * 16, lw + lane * 16, l0 + (g >> 1) * 1024 + (g & 1) * 512 + n * 16, O_E1 + ls + xw + lane * 16, sp_idx(lane),
        IMG + (w >> 1) * 8192 + ((w & 1) * 8 + 2 * g) * 512 + n * 16,
        MIM + (w * 8 + 2 * p) * 144 + g * 32 + b * 4,
        IMG + g * 512 + (w ? n * 16 : 8192 + (16 * (b >> 1) + 8 * (p >> 1) + 2 * (p & 1) + (b & 1)) * 16),
        MIM + g * 144 + (n & 7) * 16, MIM + 2304 + g * 144 + (2 * p + (b & 1)) * 16,
        w ? O_PT + (h >> 3) * 32768 + ls + lane * 16 + (w == 1 ? 16384 + ((h & 7) >> 1) * 1024 + (h & 1) * 8 : (h & 7) * 1024)
          : O_PM + oc + ((h >> 1) & 3) * 1024 + ((2 * (h & 1) + (g >> 1)) * 16 + sp_row(n & 7, g & 1)) * 16 + (n >> 3) * 8,
        w ? O_PT + (h >> 3) * 32768 + ls + 20480 + ((h >> 2) & 1) * 2048 + (w - 1) * 1024 + (h & 3) * 256 + n * 16 + g * 4
          : n >> 3 ? OOB : O_PM + oc + 4096 + ((h & 7) >> 1) * 256 + sp_row(n & 7, g & 1) * 16 + (h & 1) * 8 + (g >> 1) * 4,
        w == 3 ? (l ? F_X + SD + p * 128 + (X1F ? 0 : b * 4) : F_X0 + b * 4) : cm[0], cm[1], cm[2], cm[3],
        (lane >> 3) * 8192u + (lane & 7) * 16, tb + tr, lw + tr};
    table(smem + w * 12288 + lane * 4, t);
    asm volatile(QK_ASM : : [ws] "s"(a.ws), [sz] "n"(WS_RS), [rw] "s"(mkrs(w < 2 ? a.Wq : a.Wk, 8u << 20)), [wo] "s"(h * 256 + (w & 1) * 128), [tb] "s"(tb), [lw] "s"(lw), [l0] "s"(l0), [w] "s"(w),
        [xn] "s"(O_XN + ls), [xw] "s"(xw), [fs] "s"(l ? 0 : 128), [sg] "s"(F_Y0 + 4096 + ls + h * 128 + 16), [pr] "s"(prof_at(l * 80 + h, w)) : CLOB, "s36", "s37", "s38", "s39");
    __builtin_amdgcn_endpgm();
}
DEVI void vu_role(const Args& a, const int w, lds_char* smem, int l, int h) {
    const int lane = lane_id(), g = lane >> 4, n = lane & 15, b = n & 3, p = n >> 2;
    const unsigned l0 = (unsigned)(size_t)smem, tb = l0 + w * 12288, lw = l0 + 61440 + w * 24576, ls = l * SD;
    const unsigned xw = (w >> 1) * 16384 + (w & 1) * 4096, tv = (8 * g + p) * 128 + b * 16 + (w & 1) * 64, to = (4 * g + (p & 1)) * 128 + (p >> 1) * 32 + b * 8;
    const unsigned t[18] = {
        l0 + lane * 16, lw + lane * 16, l0 + (g >> 1) * 1024 + (g & 1) * 512 + n * 16, O_E1 + ls + xw + lane * 16, sp_idx(lane),
        l0 + 57344 + (2 * w + (n >> 3)) * 256 + g * 32 + (n & 7) * 2,
        O_UM + ls + (4 * w + (g & 1)) * 65536 + (h >> 3) * 16384 + (g >> 1) * 2048 + (h & 7) * 256 + n * 16,
        sd(O_Y + h * 131072 + ls) + w * 32768 + (g >> 1) * 1024 + (g & 1) * 256 + n * 16,
        l ? F_X + SD + p * 128 + (X1F ? 0 : b * 4) : F_X0 + b * 4, l0 + 49152 + (w >> 1) * 2048 + (2 * (w & 1) + (g >> 1)) * 256 + n * 16 + (g & 1) * 8, sp_idx(lane >> 2), l0 + g * 256 + (n & 7) * 16,
        (lane >> 3) * 8192u + (lane & 7) * 16, (8 * (lane >> 5) + ((lane >> 3) & 3)) * 2048u + (lane & 7) * 16, tb + tv - (w & 1) * 12288, lw + tv - (w & 1) * 24576, tb + to, lw + to};
    table(smem + w * 12288 + lane * 4, t);
    asm volatile(VU_ASM : : [ws] "s"(a.ws), [sz] "n"(WS_RS), [rw] "s"(mkrs(a.Wv, 8u << 20)), [ro] "s"(mkrs(a.Wo, 8u << 20)), [wo] "s"(h * 256 + (w >> 1) * 128 + (w & 1) * 262144), [w2] "s"(h * 262144 + w * 512), [tb] "s"(tb), [lw] "s"(lw), [w] "s"(w),
        [xn] "s"(O_XN + ls), [xw] "s"(xw), [fs] "s"(l ? 0 : 128), [xt] "s"(w * 16384 - xw), [fy] "s"(F_Y0 + ls + h * 128 + w * 4), [sg] "s"(w == 1 ? F_Y0 + ls + h * 128 + 16 : OOB), [pr] "s"(prof_at(l * 80 + 32 + h, w) - 128) : CLOB, "s36", "s37", "s38", "s39");
    __builtin_amdgcn_endpgm();
}
DEVI void rd_role(const Args& a, const int w, lds_char* smem, int l, int c) {
    const int lane = lane_id(), g = lane >> 4, n = lane & 15, r = 2 * (n >> 2) + (n & 1), hf = (n >> 1) & 1;
    const rsrc_t rs = mkrs(a.ws, WS_RS);
    const unsigned l0 = (unsigned)(size_t)smem, tb = l0 + 65536 + w * 4096, ls = l * SD, vp = O_E1 + ls + (c >> 3) * 16384 + (c & 7) * 1024 + w * 256 + n * 16;
    {
        const v4u ff = {SENT, SENT, SENT, SENT};
#pragma unroll
        for (int i = 0; i < 8; ++i) st16(ff, rs, O_UM + ls + c * 65536 + w * 16384 + i * 1024 + lane * 16);
#pragma unroll
        for (int i = 0; i < 2; ++i) st16(ff, rs, O_PT + ls + c * 16384 + w * 2048 + i * 1024 + lane * 16);
        st16(ff, rs, c < 8 ? O_PM + ls + w * 16384 + c * 1024 + lane * 16 : OOB);
        st16(ff, rs, g ? vp + g * 32768 : OOB);
        WV0; BAR;
    }
    st4(g & 1 ? *(const unsigned*)(a.mem0 + r * 1024 + c * 64 + w * 16 + 4 * g + 2 * hf) : nosent(*(const unsigned*)(a.mem0 + r * 1024 + c * 64 + w * 16 + 4 * g + 2 * hf)), rs, g & 1 ? vp + g * 4 : OOB, 0);
    WV0;
    st4(nosent(*(const unsigned*)(a.mem0 + r * 1024 + c * 64 + w * 16 + 4 * g + 2 * hf)), rs, g & 1 ? OOB : vp + g * 4, 0);
    const int gi = lane >> 3, r8 = lane & 7;
    const unsigned t[12] = {
        (unsigned)lane * 16, sp_idx(lane), l0 + w * 4096 + g * 576 + ((n >> 3) * 16 + hf * 4 + 2 * ((n >> 2) & 1) + (n & 1)) * 4,
        l0 + (r >> 1) * 576 + w * 128 + (g >> 1) * 64 + (r & 1) * 32 + hf * 16,
        (vp ^ ((g & 1) * 65536)) + (g >> 1) * 8, g & 1 ? SENT : 0u,
        l0 + 16384 + w * 8192 + lane * 16, l0 + 16384 + ((gi >> 1) * 2 + (w >> 1)) * 1024 + ((gi & 1) * 32 + (w & 1) * 8 + r8) * 16,
        (c >> 1) * 16384u + (c & 1) * 4096 + gi * 512 + (w * 8 + r8) * 16,
        gi ? OOB : O_ST - O_XR + c * 256 + ((w & 1) * 8 + r8) * 16 + (w >> 1) * 8,
        F_Y0 + ls + (w * 8 + r8) * 128 + (c >> 2) * 4, F_Y0 + ls + (gi & 1) * 4096 + (w * 8 + r8) * 128 + 16};
    table(smem + 65536 + w * 4096 + lane * 4, t);
    asm volatile(RD_ASM : : [rs] "s"(rs), [tb] "s"(tb), [su] "s"(O_UM + ls + c * 65536 + w * 16384), [sp] "s"(O_PM + ls + w * 16384), [st] "s"(O_PT + ls + w * 32768),
        [sy] "s"(sd(O_Y + w * (1u << 20) + ls) + (c >> 1) * 16384 + (c & 1) * 4096), [at] "s"(O_XR + ls), [du] "s"((c & 2) * 5 / 2), [fa] "s"(A1F && w ? OOB : F_A + ls + c * 128 + w * 4), [pr] "s"(prof_at(l * 80 + 64 + c, w) + 128) : CLOB);
    __builtin_amdgcn_endpgm();
}
DEVI void gg_role(const Args& a, const int w, lds_char* smem, int l, int gi) {
    const int lane = lane_id(), g = lane >> 4, n = lane & 15, b = n & 3, p = n >> 2;
    const unsigned l0 = (unsigned)(size_t)smem, tb = l0 + w * 12288, lw = l0 + 65536 + w * 24576, ls = l * SD, tv = (8 * g + p) * 128 + b * 16 + (w & 1) * 64, fh = (g >> 1) * 1024 + (g & 1) * 512 + n * 16;
    const unsigned t[9] = {l0 + lane * 16, lw + lane * 16, l0 + fh, F_A + ls + (lane >> 2) * 128 + (A1F ? 0 : (lane & 3) * 4), w * 2048u + lane * 16, O_ST - O_XR + g * 1024 + n * 16, (lane >> 3) * 7168u + (lane & 7) * 16, tb + tv - (w & 1) * 12288, lw + tv - (w & 1) * 24576};
    table(smem + w * 12288 + lane * 4, t);
    asm volatile(GG_ASM : : [rs] "s"(mkrs(a.ws, WS_RS)), [rw] "s"(mkrs(w & 1 ? a.Wu : a.Wg, 3584 * 2048)), [wo] "s"(gi * 256 + (w >> 1) * 128), [tb] "s"(tb), [lw] "s"(lw), [w] "s"(w),
        [xn] "s"(O_XR + ls), [xw] "s"((w >> 1) * 16384 + (w & 1) * 4096), [ho] "s"(O_H + ls + gi * 16384), [fh] "s"(F_H + ls + (gi / 7) * 1024 + (gi % 7) * 128 + w * 4), [pr] "s"(prof_at(160 + l * 28 + gi, w) + 128) : CLOB);
    __builtin_amdgcn_endpgm();
}
DEVI void dd_role(const Args& a, const int w, lds_char* smem, int l, int c) {
    const int lane = lane_id(), g = lane >> 4, b = lane & 3, p = (lane >> 2) & 3, gi = lane >> 3, r8 = lane & 7;
    const unsigned l0 = (unsigned)(size_t)smem, tb = l0 + w * 12288, lw = l0 + 65536 + w * 24576, ls = l * SD, tr = (8 * g + p) * 128 + b * 8;
    const unsigned ta = (c >> 1) * 16384u + (c & 1) * 4096 + gi * 512 + (w * 8 + r8) * 16;
    const rsrc_t rs = mkrs(a.ws, WS_RS);
    const unsigned t[11] = {(unsigned)lane * 16, lw + lane * 16, l0 + w * 8192 + lane * 16, l0 + ((gi >> 1) * 2 + (w >> 1)) * 1024 + ((gi & 1) * 32 + (w & 1) * 8 + r8) * 16,
        ta, l ? (w * 8 + r8) * 2048 + c * 128 + gi * 16 : ta, gi || l ? OOB : O_ST - O_XR + 4096 + c * 256 + (w * 8 + r8) * 8,
        F_H + ls + w * 1024 + ((lane >> 2) % 7) * 128 + (G1F ? 0 : b * 4), gi * 2048u + r8 * 16, tb + tr, lw + tr};
    table(smem + w * 12288 + lane * 4, t);
    asm volatile(DD_ASM : : [rs] "s"(rs), [rw] "s"(mkrs(a.Wd, 3584 * 2048)), [ro] "s"(mkrs(l ? (char*)a.out : a.ws, l ? 2048 * 2048 : WS_RS)), [wo] "s"(w * (896 * 2048) + c * 128), [tb] "s"(tb), [lw] "s"(lw), [l] "s"(l),
        [xn] "s"(O_XR + ls), [ho] "s"(O_H + ls + w * 7 * 16384), [fd] "s"(D1F && w ? OOB : F_D + ls + c * 128 + w * 4), [lt] "s"(F_D + ls + c * 128 + w * 4 + 64), [pr] "s"(prof_at(216 + l * 16 + c, w) + 128) : CLOB);
    __builtin_amdgcn_endpgm();
}

#define CPF 74
#define CPJ 0
#define LNW 0
#define DLQ 0
#define DLS 400
#define SPL 0
#define PLAN 3
#define PFA 0
#define PFB 2
#define PFL 1
#define VUX 1
#define LNS 40
#define LN1 700
#define DLG 1100
#define DLD 1500
#define DL1 1500
#define DLG1 2600
#define DLD1 3000
DEVI Args args(unsigned sh) {
    auto p = (const __attribute__((address_space(4))) Args*)__builtin_amdgcn_kernarg_segment_ptr();
    asm volatile("" : "+s"(p));
    Args a;
    a.x = p->x; a.mem0 = p->mem0; a.Wq = p->Wq; a.Wk = p->Wk; a.Wv = p->Wv; a.Wo = p->Wo; a.Wg = p->Wg; a.Wu = p->Wu; a.Wd = p->Wd; a.ws = p->ws + sh; a.out = p->out;
    return a;
}
DEVI unsigned beat_wait(unsigned x0) {
    const rsrc_t r0 = mkrs(args(0).ws, 2u << 20);
    for (unsigned spins = 0; spins < SPIN_CAP; ++spins) {
        CB;
        const v2u t = __builtin_amdgcn_raw_buffer_load_b64(r0, 0, O_HB, AUX_SC1);
        if (RFL(t[0]) != x0) return RFL(t[1]) & (2u << 20);
        __builtin_amdgcn_s_sleep(8);
    }
    __builtin_amdgcn_endpgm();
}
DEVI void leader(const Args& a, const int w, lds_char* smem, unsigned x0) {
    const int lane = lane_id();
    const rsrc_t r0 = mkrs(a.ws, 4u << 20);
    unsigned t[2] = {~0u, ~0u};
    for (int i = 0; i < 2 * NPRB; ++i) {
        const unsigned t0 = (unsigned)__builtin_amdgcn_s_memtime();
        WL0;
        if (lane == 0) st4(0u, r0, 0, O_PRB + (i & 1) * SD + w * 1024 + (i >> 1) * 128);
        WV0;
        const unsigned d = (unsigned)__builtin_amdgcn_s_memtime() - t0;
        if (i & 1) t[1] = min(t[1], d); else t[0] = min(t[0], d);
    }
    *(l2*)(smem + w * 8) = v2u{t[0], t[1]};
    WL0; BAR;
    const v4u p = *(l4*)smem, q = *(l4*)(smem + 16);
    const unsigned sh = RFL(min(min(p[0], p[2]), min(q[0], q[2])) < min(min(p[1], p[3]), min(q[1], q[3])) ? 2u << 20 : 0u);
    const unsigned o = sh + w * 1024 + lane * 16;
    const v4u ff = {SENT, SENT, SENT, SENT};
#pragma unroll
    for (int i = 0; i < 8; ++i) st16(ff, r0, o + O_E1 + i * 4096);
#pragma unroll
    for (int i = 1; i < 16; ++i) if (i != 2 && i != 11) st16(ff, r0, o + i * 4096);
    WV0; BAR;
    if (w) return;
    for (unsigned spins = 0; spins < SPIN_CAP; ++spins) {
        if (lane == 0) st8(v2u{++x0, sh}, r0, 0, O_HB);
        if (__ballot((int)ld4(r0, sh + F_D + SD + (lane >> 2) * 128 + (D1F ? 0 : (lane & 3) * 4), 0) < 0) == 0ull) break;
        __builtin_amdgcn_s_sleep(16);
    }
}
__global__ __launch_bounds__(256, 1) void sys_kernel(Args) {
    extern __shared__ __attribute__((aligned(16))) char smem_[];
    lds_char* smem = (lds_char*)smem_;
    const int x = blockIdx.x & 7, j = blockIdx.x >> 3, wv = RFL(threadIdx.x >> 6), l = x >> 2, xx = x & 3;
    const unsigned t0 = (unsigned)__builtin_amdgcn_s_memrealtime();
    auto until = [&](unsigned dl) AI { while ((unsigned)__builtin_amdgcn_s_memrealtime() - t0 < dl) __builtin_amdgcn_s_sleep(8); };
    unsigned x0 = 0;
    if (l || j >= 16) x0 = RFL(ld4(mkrs(args(0).ws, 2u << 20), 0, O_HB));
    if (j < 16) {
        if (l) { until(j ? LN1 + j * LNS : 0); ln0(args(0), wv, j * 4 + xx, [&]() AI { return beat_wait(x0); }); until(DL1); }
        if (DLQ && !l && j < 8) until(DLQ);
        if (j < 8) qk_role(args(0), wv, smem, l, xx * 8 + j); else vu_role(args(0), wv, smem, l, (xx ^ VUX) * 8 + j - 8);
        return;
    }
    if (j >= 16 + CPJ && j < 20 + CPJ) {
        unsigned long long pc; unsigned n;
        asm volatile("s_getpc_b64 %0\n\ts_mov_b32 %1, .-_Z10sys_kernel4Args" : "=s"(pc), "=s"(n));
        const rsrc_t rc = mkrs((const void*)(pc - n), CPF * 1024);
        __builtin_amdgcn_raw_ptr_buffer_load_lds(rc, smem + LDSB - 1024 + wv * 256, 4, lane_id() * 128, ((j - 16 - CPJ) * 4 + wv) * 8192, 0, 0);
    }
    {
        const int lane = lane_id(), q = (j - 16) * 4 + wv, m = SPL && !l ? ((xx & 1) != (SPL & 1)) * 2 + (q >> 5) : q >> 4;
        const Args a = args(0);
        const u16 *W0 = a.Wq, *W1 = a.Wk, *W2 = a.Wv, *W3 = a.Wo;
        asm volatile("" : "+s"(W0), "+s"(W1), "+s"(W2), "+s"(W3));
        const u16* W = m == 0 ? W0 : m == 1 ? W1 : m == 2 ? W2 : W3;
        const rsrc_t rw = mkrs(W, 8u << 20);
        if (l) {
#pragma unroll
            for (int i = 0; i < PFA; ++i) __builtin_amdgcn_raw_ptr_buffer_load_lds(rw, smem + LDSB - 1024 + wv * 256, 4, lane * 256, i * (1u << 20) + (xx * 16 + (q & 15)) * 16384, 0, 0);
        } else {
            const unsigned po = (0x100u - (unsigned)(size_t)W) & 0x300u, hx = SPL ? (xx & 2) + ((q >> 4) & 1) : m < 2 ? xx : xx ^ VUX;
            const unsigned vo = m == 3 ? (lane >> 1) * 1024 + (lane & 1) * 128 : (lane >> 2) * 8192 + ((lane >> 1) & 1) * 1024 + (lane & 1) * 128;
            const unsigned so = po + (m == 3 ? hx * (2u << 20) + (q & 15) * 32768 : hx * 2048 + (q & 15) * 131072);
#pragma unroll
            for (int i = 0; i < PFB; ++i) __builtin_amdgcn_raw_ptr_buffer_load_lds(rw, smem + LDSB - 1024 + wv * 256, 4, vo, so + i * (m == 3 ? 524288 : 2u << 20), 0, 0);
            const unsigned pl = (0x1F00u - (unsigned)(size_t)W) & 0x1F00u;
#pragma unroll
            for (int i = 0; i < PFL; ++i) __builtin_amdgcn_raw_ptr_buffer_load_lds(rw, smem + LDSB - 1024 + wv * 256, 4, (lane >> 1) * 8192 + (lane & 1) * 128, (pl >> 11) == hx && m < 3 ? pl + ((q & 15) + 16 * i) * 262144 : OOB, 0, 0);
        }
    }
    int ro, ix;
    if ((xx & 1) == (PLAN & 1)) { if (j < 24) { ro = 0; ix = (xx >> 1) * 8 + j - 16; } else if (j < 26) { ro = 3; ix = (xx >> 1) * 2 + j - 24; } else { ro = 1; ix = (xx >> 1) * 14 + j - 26; } }
    else if (j < 24) { ro = 1; ix = (xx >> 1) * 14 + j - 10; } else { ro = 2; ix = (xx >> 1) * 8 + j - 24; }
    if (ro == 3 && l) { if (ix == 0) leader(args(0), wv, smem, x0); return; }
    const unsigned sh = beat_wait(x0);
    if (ro == 0) { rd_role(args(sh), wv, smem, l, ix); return; }
    if (ro == 1) { const Args a = args(sh); until(l ? DLG1 : ((((unsigned)(size_t)a.Wg >> 8) + ix) & 3) == 1 || ((((unsigned)(size_t)a.Wu >> 8) + ix) & 3) == 1 ? DLG - DLS : DLG); WV0; BAR; gg_role(a, wv, smem, l, ix); return; }
    if (ro == 2) { const Args a = args(sh); until((l ? DLD1 : DLD) - (((((unsigned)(size_t)a.Wd >> 8) + (ix >> 1)) & 3) == 1 ? DLS + 200 : 0)); WV0; BAR; dd_role(a, wv, smem, l, ix); return; }
    r_role(args(sh), wv, ix);
}

extern "C" void kernel_launch(void* const* d_in, const int* in_sizes, int n_in, void* d_out, int out_size, void* d_ws, size_t ws_size, hipStream_t stream) {
    (void)in_sizes; (void)n_in; (void)out_size; (void)ws_size;
    Args a;
    a.x = (const u16*)d_in[0]; a.mem0 = (const u16*)d_in[1]; a.Wq = (const u16*)d_in[2]; a.Wk = (const u16*)d_in[3]; a.Wv = (const u16*)d_in[4]; a.Wo = (const u16*)d_in[5];
    a.Wg = (const u16*)d_in[8]; a.Wu = (const u16*)d_in[9]; a.Wd = (const u16*)d_in[10];
    a.ws = (char*)(((uintptr_t)d_ws + (2u << 20) - 1) & ~(uintptr_t)((2u << 20) - 1)); a.out = (u16*)d_out;
    (void)hipFuncSetAttribute((const void*)sys_kernel, hipFuncAttributeMaxDynamicSharedMemorySize, LDSB);
    hipLaunchKernelGGL(sys_kernel, dim3(256), dim3(256), LDSB, stream, a);
}
```

```cpp
#include <hip/hip_runtime.h>
#include <stdint.h>

typedef unsigned short u16;
typedef unsigned long long u64;
typedef __attribute__((ext_vector_type(8))) short bf16x8;
typedef __attribute__((ext_vector_type(4))) short s16x4;
typedef __attribute__((ext_vector_type(4))) float f32x4;
typedef __attribute__((ext_vector_type(16))) float f32x16;
typedef __attribute__((ext_vector_type(2))) float f32x2;
typedef __attribute__((ext_vector_type(4))) unsigned v4u;
typedef __attribute__((ext_vector_type(2))) unsigned v2u;
typedef __amdgpu_buffer_rsrc_t rsrc_t;
typedef __attribute__((address_space(3))) char lds_char;
typedef __attribute__((address_space(3))) bf16x8 l8;
typedef __attribute__((address_space(3))) v4u l4;
typedef __attribute__((address_space(3))) v2u l2;
typedef __attribute__((address_space(3))) unsigned l1;
typedef __attribute__((address_space(3))) f32x4 lf4;
typedef __attribute__((address_space(3))) float lf1;
#define DEVI __device__ __forceinline__
#define AI __attribute__((always_inline))

constexpr int NSEG = 64;
constexpr float ATT_SCALE = 0.08838834764831845f;
constexpr float LOG2E = 1.4426950408889634f;
#define NPRB 4
constexpr unsigned SD = 8192;
DEVI unsigned sd(unsigned o) { return o ^ ((o >> 8) & SD); }
constexpr unsigned F_X0 = 32768;
constexpr unsigned F_X = 4096;
constexpr unsigned F_A = 16384;
constexpr unsigned F_H = F_A + 2048;
constexpr unsigned F_D = F_H + 4096;
constexpr unsigned F_Y0 = 49152;
constexpr unsigned F_ABORT = 6400;
constexpr unsigned O_PM = 65536;
constexpr unsigned O_E1 = 131072;
constexpr unsigned O_PT = 262144;
constexpr unsigned O_UM = 524288;
constexpr unsigned CTRL_END = O_UM + (1u << 20);
constexpr unsigned O_PRB = 0x1F0000, O_HB = 0x1FFF00;
constexpr unsigned O_XN = 4u << 20;
constexpr unsigned O_XR = O_XN + (8u << 20);
constexpr unsigned O_ST = O_XR + (8u << 20);
constexpr unsigned O_H = O_ST + (8u << 20);
constexpr unsigned O_Y = O_H + (36u << 20);
constexpr unsigned WS_NEED = O_Y + (8u << 20);
constexpr unsigned O_PROF = WS_NEED;
constexpr unsigned WS_RS = WS_NEED;
static_assert(CTRL_END <= O_PRB && CTRL_END % 16384 == 0 && O_Y % (8u << 20) == 0, "workspace");
constexpr unsigned OOB = 0x80000000u, OOB2 = 0x90000000u, SENT = 0xFFFFFFFFu;

struct Args { const u16 *x, *mem0, *Wq, *Wk, *Wv, *Wo, *Wg, *Wu, *Wd; char* ws; u16* out; };

typedef __attribute__((ext_vector_type(2))) __bf16 bf16x2_t;
DEVI unsigned pack2(float lo, float hi) { f32x2 v = {lo, hi}; bf16x2_t b = __builtin_convertvector(v, bf16x2_t); return __builtin_bit_cast(unsigned, b); }
DEVI float bflo(unsigned u) { return __uint_as_float(u << 16); }
DEVI float bfhi(unsigned u) { return __uint_as_float(u & 0xffff0000u); }
DEVI float rbf(float x) { return bflo(pack2(x, x)); }
DEVI bf16x8 as8(v4u v) { return __builtin_bit_cast(bf16x8, v); }
DEVI f32x4 mfma16(bf16x8 a, bf16x8 b, f32x4 c) { return __builtin_amdgcn_mfma_f32_16x16x32_bf16(a, b, c, 0, 0, 0); }
DEVI f32x16 mfma32(bf16x8 a, bf16x8 b, f32x16 c) { return __builtin_amdgcn_mfma_f32_32x32x16_bf16(a, b, c, 0, 0, 0); }
constexpr int AUX_SC1 = 16;
DEVI v4u ld16(rsrc_t r, unsigned vo, unsigned so) { return __builtin_amdgcn_raw_buffer_load_b128(r, vo, so, AUX_SC1); }
DEVI unsigned ld4(rsrc_t r, unsigned vo, unsigned so) { return __builtin_amdgcn_raw_buffer_load_b32(r, vo, so, AUX_SC1); }
DEVI void st16(v4u v, rsrc_t r, unsigned vo) { __builtin_amdgcn_raw_buffer_store_b128(v, r, vo, 0, AUX_SC1); }
DEVI void st8(v2u v, rsrc_t r, unsigned vo, unsigned so) { __builtin_amdgcn_raw_buffer_store_b64(v, r, vo, so, AUX_SC1); }
DEVI void st4(unsigned v, rsrc_t r, unsigned vo, unsigned so) { __builtin_amdgcn_raw_buffer_store_b32(v, r, vo, so, AUX_SC1); }
DEVI v2u trd(lds_char* p) { return __builtin_bit_cast(v2u, __builtin_amdgcn_ds_read_tr16_b64_v4i16((__attribute__((address_space(3))) s16x4*)p)); }
template <int O1, int O2> DEVI void tra(v2u& lo, v2u& hi, unsigned a) { asm volatile("ds_read_b64_tr_b16 %0, %2 offset:%3\n\tds_read_b64_tr_b16 %1, %2 offset:%4" : "=&v"(lo), "=&v"(hi) : "v"(a), "n"(O1), "n"(O2) : "memory"); }
DEVI void trw(v2u& a, v2u& b, v2u& c, v2u& d) { asm volatile("s_waitcnt lgkmcnt(0)" : "+v"(a), "+v"(b), "+v"(c), "+v"(d)::"memory"); }
DEVI rsrc_t mkrs(const void* p, unsigned n) { return __builtin_amdgcn_make_buffer_rsrc((void*)p, 0, (int)n, 0x00020000); }
#define CB asm volatile("" ::: "memory")
#define SB __builtin_amdgcn_sched_barrier(0)
#define WL0 asm volatile("s_waitcnt lgkmcnt(0)" ::: "memory")
#define WV0 asm volatile("s_waitcnt vmcnt(0)" ::: "memory")
#define WV(n) asm volatile("s_waitcnt vmcnt(%0)" ::"n"(n) : "memory")
#define BAR do { __builtin_amdgcn_s_barrier(); CB; } while (0)
#define RFL(x) __builtin_amdgcn_readfirstlane(x)
DEVI int lane_id() { int x; asm volatile("v_mbcnt_lo_u32_b32 %0, -1, 0\n\tv_mbcnt_hi_u32_b32 %0, -1, %0" : "=v"(x)); return x; }
template <int I> struct IC { static constexpr int v = I; };
template <int B, int E_, class F> DEVI void sfor(F&& f) { if constexpr (B < E_) { f(IC<B>{}); sfor<B + 1, E_>(f); } }
DEVI float red_x32(float x, bool mx) { unsigned u = __float_as_uint(x); auto r = __builtin_amdgcn_permlane32_swap(u, u, false, false); float a = __uint_as_float(r[0]), b = __uint_as_float(r[1]); return mx ? fmaxf(a, b) : a + b; }
DEVI float red_x16(float x, bool mx) { unsigned u = __float_as_uint(x); auto r = __builtin_amdgcn_permlane16_swap(u, u, false, false); float a = __uint_as_float(r[0]), b = __uint_as_float(r[1]); return mx ? fmaxf(a, b) : a + b; }
DEVI unsigned vmax4(v4u d) { return max(max(d[0], d[1]), max(d[2], d[3])); }
DEVI unsigned nosent(unsigned v) { return min(v, SENT - 1u); }
constexpr unsigned SPIN_CAP = 1u << 18;
DEVI bool spin_bail(unsigned& spins, rsrc_t rs) {
    if (++spins > SPIN_CAP) { st4(0u, rs, 0, F_ABORT); return true; }
    return (spins & 255u) == 0u && RFL(ld4(rs, 0, F_ABORT)) != SENT;
}
#define TSTAMP(idx)
constexpr int LDSB = 160 * 1024;
DEVI void idle(unsigned dl) { const unsigned t0 = (unsigned)__builtin_amdgcn_s_memrealtime(); while ((unsigned)__builtin_amdgcn_s_memrealtime() - t0 < dl) __builtin_amdgcn_s_sleep(8); }
#define D1F 0
#define X1F 0

DEVI float red_rows(float s) {
    s += __uint_as_float(__builtin_amdgcn_update_dpp(0, __float_as_uint(s), 0x128, 0xf, 0xf, false));
    s = red_x16(s, false); return red_x32(s, false);
}
DEVI f32x2 up2(unsigned u) { return f32x2{bflo(u), bfhi(u)}; }
DEVI float ln_inv(float var) { return __builtin_amdgcn_rcpf(rbf(__builtin_amdgcn_sqrtf(rbf(rbf(fmaxf(var, 0.f)) + 1e-5f)))); }
template <class F> DEVI void ln0(const Args& a, const int w, int b, F&& base) {
    const int lane = lane_id(), row = w * 8 + (lane & 7);
    const rsrc_t rx = mkrs(a.x, 2048 * 2048);
    v4u u[16];
#pragma unroll
    for (int i = 0; i < 16; ++i) u[i] = __builtin_amdgcn_raw_buffer_load_b128(rx, row * 2048 + (lane >> 3) * 16 + i * 128, b * 65536, 0);
    const float k0 = __shfl(bflo(u[0][0]), lane & 7);
    f32x2 s1 = {0.f, 0.f}, s2 = {0.f, 0.f};
#pragma unroll
    for (int i = 0; i < 16; ++i)
#pragma unroll
        for (int e = 0; e < 4; ++e) { const f32x2 d = up2(u[i][e]) - k0; s1 += d; s2 += d * d; }
    const float s = red_rows(s1[0] + s1[1]) * (1.0f / 1024.0f), q = red_rows(s2[0] + s2[1]) * (1.0f / 1024.0f);
    float inv = ln_inv(q - s * s), c = -rbf(k0 + s) * inv;
    asm volatile("" : "+v"(inv), "+v"(c));
    const rsrc_t rs = mkrs(a.ws + base(), WS_RS);
    const unsigned o = sd(O_XN + b * 131072) + (lane >> 3) * 512 + row * 16;
#pragma unroll
    for (int i = 0; i < 16; ++i) {
        v4u r;
#pragma unroll
        for (int e = 0; e < 4; ++e) { const f32x2 z = up2(u[i][e]) * inv + c; r[e] = pack2(z[0], z[1]); }
        st16(r, rs, o + (i >> 1) * 16384 + (i & 1) * 4096);
    }
#pragma unroll
    for (int i = 0; i < 16; ++i) st16(u[i], rs, o + (O_XR - O_XN) + (i >> 1) * 16384 + (i & 1) * 4096);
    st16(u[0], rs, sd(O_ST + b * 131072) + w * 1024 + lane * 16); st16(u[0], rs, sd(O_ST + b * 131072) + 4096 + w * 1024 + lane * 16); st16(u[0], rs, sd(O_ST + SD + b * 131072) + w * 1024 + lane * 16);
    WV0;
    st4(b, rs, 0, F_X0 + b * 128 + w * 4);
}
#define RSLP 8
DEVI void r_role(const Args& a, const int w, int rq) {
    const int lane = lane_id(), row = (w >> 1) * 16 + (rq & 1) * 8 + (lane & 7), cq = (rq >> 1) * 2 + (w & 1);
    const rsrc_t rs = mkrs(a.ws, WS_RS);
    const unsigned fo = cq * 32768 + (lane >> 3) * 512 + row * 16, so = O_ST - O_XR + 4096 + (lane >> 3) * 512 + row * 8;
    auto io = [](int i) -> unsigned { return (i >> 1) * 16384 + (i & 1) * 4096; };
    const unsigned fd = F_D + (lane & 15) * 128 + (D1F ? 0 : (row >> 3) * 4);
    for (int b = 0; b < NSEG; ++b) {
        unsigned spins = 0;
        for (;;) {
            CB;
            if (__ballot((int)ld4(rs, fd, 0) < b) == 0ull) break;
            if (spin_bail(spins, rs)) return;
            __builtin_amdgcn_s_sleep(RSLP);
        }
        const unsigned sx = sd(O_XR + b * 131072);
        const v2u s0 = __builtin_amdgcn_raw_buffer_load_b64(rs, so, sx, AUX_SC1), s1 = __builtin_amdgcn_raw_buffer_load_b64(rs, so + 256, sx, AUX_SC1);
        v4u u[4];
#pragma unroll
        for (int i = 0; i < 4; ++i) u[i] = ld16(rs, fo + io(i), sx);
        const float m0 = __uint_as_float(s0[0]), m1 = __uint_as_float(s1[0]), mean = red_rows(m0 + m1) * 0.0625f;
        const float var = red_rows(__uint_as_float(s0[1]) + __uint_as_float(s1[1]) + 64.f * ((m0 - mean) * (m0 - mean) + (m1 - mean) * (m1 - mean))) * (1.0f / 1024.0f);
        const float inv = ln_inv(var), c = -rbf(mean) * inv;
#pragma unroll
        for (int i = 0; i < 4; ++i) {
            v4u o;
#pragma unroll
            for (int e = 0; e < 4; ++e) { const f32x2 z = up2(u[i][e]) * inv + c; o[e] = pack2(z[0], z[1]); }
            st16(o, rs, fo + io(i) + ((sx ^ SD) - (O_XR - O_XN)));
        }
#pragma unroll
        for (int i = 0; i < 4; ++i) st16(u[i], rs, fo + io(i) + (sx ^ SD));
        WV0;
        if (X1F) BAR;
        st4(b, rs, 0, X1F && w ? OOB : F_X + SD + rq * 128 + w * 4);
        if (rq == 0 && w == 0) TSTAMP(200000 + 64 + b);
    }
}
#define QK_ASM \
"v_mbcnt_lo_u32_b32 v255, -1, 0\nv_mbcnt_hi_u32_b32 v255, -1, v255\nv_lshlrev_b32 v255, 2, v255\nv_add_u32 v255, %[tb], v255\nds_read_b32 v0, v255 offset:0\nds_read_b32 v1, v255 offset:256\nds_read_b32 v2, v255 offset:512\nds_read_b32 v3, v255 offset:768\n" \
"ds_read_b32 v4, v255 offset:1024\nds_read_b32 v5, v255 offset:1280\nds_read_b32 v6, v255 offset:1536\nds_read_b32 v7, v255 offset:1792\nds_read_b32 v8, v255 offset:2048\nds_read_b32 v9, v255 offset:2304\nds_read_b32 v10, v255 offset:2560\nds_read_b32 v11, v255 offset:2816\n" \
"ds_read_b32 v12, v255 offset:3072\nds_read_b32 v13, v255 offset:3328\nds_read_b32 v14, v255 offset:3584\nds_read_b32 v15, v255 offset:3840\nds_read_b32 v92, v255 offset:4096\nds_read_b32 v93, v255 offset:4352\nds_read_b32 v94, v255 offset:4608\ns_waitcnt lgkmcnt(0)\n" \
"s_mov_b64 s[36:37], %[ws]\ns_mov_b32 s38, %[sz]\ns_mov_b32 s39, 0x20000\ns_mov_b32 s45, 2096896\nbuffer_load_dword v95, off, s[36:39], s45 sc1\ns_add_u32 m0, %[lw], 16384\ns_add_u32 s44, %[wo], 0\nbuffer_load_dwordx4 v92, %[rw], s44 offen nt lds\n" \
"s_add_u32 m0, %[lw], 17408\ns_add_u32 s44, %[wo], 65536\nbuffer_load_dwordx4 v92, %[rw], s44 offen nt lds\ns_add_u32 m0, %[lw], 18432\ns_add_u32 s44, %[wo], 131072\nbuffer_load_dwordx4 v92, %[rw], s44 offen nt lds\ns_add_u32 m0, %[lw], 19456\ns_add_u32 s44, %[wo], 196608\n" \
"buffer_load_dwordx4 v92, %[rw], s44 offen nt lds\ns_add_u32 m0, %[lw], 20480\ns_add_u32 s44, %[wo], 262144\nbuffer_load_dwordx4 v92, %[rw], s44 offen nt lds\ns_add_u32 m0, %[lw], 21504\ns_add_u32 s44, %[wo], 327680\nbuffer_load_dwordx4 v92, %[rw], s44 offen nt lds\ns_add_u32 m0, %[lw], 22528\n" \
"s_add_u32 s44, %[wo], 393216\nbuffer_load_dwordx4 v92, %[rw], s44 offen nt lds\ns_add_u32 m0, %[lw], 23552\ns_add_u32 s44, %[wo], 458752\nbuffer_load_dwordx4 v92, %[rw], s44 offen nt lds\ns_add_u32 m0, %[tb], 0\ns_add_u32 s44, %[wo], 524288\nbuffer_load_dwordx4 v92, %[rw], s44 offen nt lds\n" \
"s_add_u32 m0, %[tb], 1024\ns_add_u32 s44, %[wo], 589824\nbuffer_load_dwordx4 v92, %[rw], s44 offen nt lds\ns_add_u32 m0, %[tb], 2048\ns_add_u32 s44, %[wo], 655360\nbuffer_load_dwordx4 v92, %[rw], s44 offen nt lds\ns_add_u32 m0, %[tb], 3072\ns_add_u32 s44, %[wo], 720896\n" \
"buffer_load_dwordx4 v92, %[rw], s44 offen nt lds\ns_add_u32 m0, %[tb], 4096\ns_add_u32 s44, %[wo], 786432\nbuffer_load_dwordx4 v92, %[rw], s44 offen nt lds\ns_add_u32 m0, %[tb], 5120\ns_add_u32 s44, %[wo], 851968\nbuffer_load_dwordx4 v92, %[rw], s44 offen nt lds\ns_add_u32 m0, %[tb], 6144\n" \
"s_add_u32 s44, %[wo], 917504\nbuffer_load_dwordx4 v92, %[rw], s44 offen nt lds\ns_add_u32 m0, %[tb], 7168\ns_add_u32 s44, %[wo], 983040\nbuffer_load_dwordx4 v92, %[rw], s44 offen nt lds\ns_add_u32 m0, %[tb], 8192\ns_add_u32 s44, %[wo], 1048576\nbuffer_load_dwordx4 v92, %[rw], s44 offen nt lds\n" \
"s_add_u32 m0, %[tb], 9216\ns_add_u32 s44, %[wo], 1114112\nbuffer_load_dwordx4 v92, %[rw], s44 offen nt lds\ns_add_u32 m0, %[tb], 10240\ns_add_u32 s44, %[wo], 1179648\nbuffer_load_dwordx4 v92, %[rw], s44 offen nt lds\ns_add_u32 m0, %[tb], 11264\ns_add_u32 s44, %[wo], 1245184\n" \
"buffer_load_dwordx4 v92, %[rw], s44 offen nt lds\ns_add_u32 m0, %[lw], 0\ns_add_u32 s44, %[wo], 1310720\nbuffer_load_dwordx4 v92, %[rw], s44 offen nt lds\ns_add_u32 m0, %[lw], 1024\ns_add_u32 s44, %[wo], 1376256\nbuffer_load_dwordx4 v92, %[rw], s44 offen nt lds\ns_add_u32 m0, %[lw], 2048\n" \
"s_add_u32 s44, %[wo], 1441792\nbuffer_load_dwordx4 v92, %[rw], s44 offen nt lds\ns_add_u32 m0, %[lw], 3072\ns_add_u32 s44, %[wo], 1507328\nbuffer_load_dwordx4 v92, %[rw], s44 offen nt lds\ns_add_u32 m0, %[lw], 4096\ns_add_u32 s44, %[wo], 1572864\nbuffer_load_dwordx4 v92, %[rw], s44 offen nt lds\n" \
"s_add_u32 m0, %[lw], 5120\ns_add_u32 s44, %[wo], 1638400\nbuffer_load_dwordx4 v92, %[rw], s44 offen nt lds\ns_add_u32 m0, %[lw], 6144\ns_add_u32 s44, %[wo], 1703936\nbuffer_load_dwordx4 v92, %[rw], s44 offen nt lds\ns_add_u32 m0, %[lw], 7168\ns_add_u32 s44, %[wo], 1769472\n" \
"buffer_load_dwordx4 v92, %[rw], s44 offen nt lds\ns_add_u32 m0, %[lw], 8192\ns_add_u32 s44, %[wo], 1835008\nbuffer_load_dwordx4 v92, %[rw], s44 offen nt lds\ns_add_u32 m0, %[lw], 9216\ns_add_u32 s44, %[wo], 1900544\nbuffer_load_dwordx4 v92, %[rw], s44 offen nt lds\ns_add_u32 m0, %[lw], 10240\n" \
"s_add_u32 s44, %[wo], 1966080\nbuffer_load_dwordx4 v92, %[rw], s44 offen nt lds\ns_add_u32 m0, %[lw], 11264\ns_add_u32 s44, %[wo], 2031616\nbuffer_load_dwordx4 v92, %[rw], s44 offen nt lds\ns_add_u32 m0, %[lw], 12288\ns_add_u32 s44, %[wo], 2097152\nbuffer_load_dwordx4 v92, %[rw], s44 offen nt lds\n" \
"s_add_u32 m0, %[lw], 13312\ns_add_u32 s44, %[wo], 2162688\nbuffer_load_dwordx4 v92, %[rw], s44 offen nt lds\ns_add_u32 m0, %[lw], 14336\ns_add_u32 s44, %[wo], 2228224\nbuffer_load_dwordx4 v92, %[rw], s44 offen nt lds\ns_add_u32 m0, %[lw], 15360\ns_add_u32 s44, %[wo], 2293760\n" \
"buffer_load_dwordx4 v92, %[rw], s44 offen nt lds\ns_waitcnt vmcnt(32)\nds_read_b64_tr_b16 a[0:1], v94 offset:16384\nds_read_b64_tr_b16 a[2:3], v94 offset:16896\nds_read_b64_tr_b16 a[8:9], v94 offset:16392\nds_read_b64_tr_b16 a[10:11], v94 offset:16904\nds_read_b64_tr_b16 a[16:17], v94 offset:16448\nds_read_b64_tr_b16 a[18:19], v94 offset:16960\n" \
"ds_read_b64_tr_b16 a[24:25], v94 offset:16456\nds_read_b64_tr_b16 a[26:27], v94 offset:16968\ns_waitcnt lgkmcnt(0)\ns_add_u32 m0, %[lw], 16384\ns_add_u32 s44, %[wo], 2359296\nbuffer_load_dwordx4 v92, %[rw], s44 offen nt lds\ns_add_u32 m0, %[lw], 17408\ns_add_u32 s44, %[wo], 2424832\n" \
"buffer_load_dwordx4 v92, %[rw], s44 offen nt lds\ns_add_u32 m0, %[lw], 18432\ns_add_u32 s44, %[wo], 2490368\nbuffer_load_dwordx4 v92, %[rw], s44 offen nt lds\ns_add_u32 m0, %[lw], 19456\ns_add_u32 s44, %[wo], 2555904\nbuffer_load_dwordx4 v92, %[rw], s44 offen nt lds\ns_waitcnt vmcnt(32)\n" \
"ds_read_b64_tr_b16 a[4:5], v94 offset:20480\nds_read_b64_tr_b16 a[6:7], v94 offset:20992\nds_read_b64_tr_b16 a[12:13], v94 offset:20488\nds_read_b64_tr_b16 a[14:15], v94 offset:21000\nds_read_b64_tr_b16 a[20:21], v94 offset:20544\nds_read_b64_tr_b16 a[22:23], v94 offset:21056\nds_read_b64_tr_b16 a[28:29], v94 offset:20552\nds_read_b64_tr_b16 a[30:31], v94 offset:21064\n" \
"s_waitcnt lgkmcnt(0)\ns_add_u32 m0, %[lw], 20480\ns_add_u32 s44, %[wo], 2621440\nbuffer_load_dwordx4 v92, %[rw], s44 offen nt lds\ns_add_u32 m0, %[lw], 21504\ns_add_u32 s44, %[wo], 2686976\nbuffer_load_dwordx4 v92, %[rw], s44 offen nt lds\ns_add_u32 m0, %[lw], 22528\n" \
"s_add_u32 s44, %[wo], 2752512\nbuffer_load_dwordx4 v92, %[rw], s44 offen nt lds\ns_add_u32 m0, %[lw], 23552\ns_add_u32 s44, %[wo], 2818048\nbuffer_load_dwordx4 v92, %[rw], s44 offen nt lds\ns_waitcnt vmcnt(32)\nds_read_b64_tr_b16 a[32:33], v93 offset:0\nds_read_b64_tr_b16 a[34:35], v93 offset:512\n" \
"ds_read_b64_tr_b16 v[84:85], v93 offset:8\nds_read_b64_tr_b16 v[86:87], v93 offset:520\nds_read_b64_tr_b16 a[40:41], v93 offset:64\nds_read_b64_tr_b16 a[42:43], v93 offset:576\nds_read_b64_tr_b16 a[48:49], v93 offset:72\nds_read_b64_tr_b16 a[50:51], v93 offset:584\ns_waitcnt lgkmcnt(0)\ns_add_u32 m0, %[tb], 0\n" \
"s_add_u32 s44, %[wo], 2883584\nbuffer_load_dwordx4 v92, %[rw], s44 offen nt lds\ns_add_u32 m0, %[tb], 1024\ns_add_u32 s44, %[wo], 2949120\nbuffer_load_dwordx4 v92, %[rw], s44 offen nt lds\ns_add_u32 m0, %[tb], 2048\ns_add_u32 s44, %[wo], 3014656\nbuffer_load_dwordx4 v92, %[rw], s44 offen nt lds\n" \
"s_add_u32 m0, %[tb], 3072\ns_add_u32 s44, %[wo], 3080192\nbuffer_load_dwordx4 v92, %[rw], s44 offen nt lds\ns_waitcnt vmcnt(32)\nds_read_b64_tr_b16 a[36:37], v93 offset:4096\nds_read_b64_tr_b16 a[38:39], v93 offset:4608\nds_read_b64_tr_b16 v[80:81], v93 offset:4104\nds_read_b64_tr_b16 v[82:83], v93 offset:4616\n" \
"ds_read_b64_tr_b16 a[44:45], v93 offset:4160\nds_read_b64_tr_b16 a[46:47], v93 offset:4672\nds_read_b64_tr_b16 a[52:53], v93 offset:4168\nds_read_b64_tr_b16 a[54:55], v93 offset:4680\ns_waitcnt lgkmcnt(0)\ns_add_u32 m0, %[tb], 4096\ns_add_u32 s44, %[wo], 3145728\nbuffer_load_dwordx4 v92, %[rw], s44 offen nt lds\n" \
"s_add_u32 m0, %[tb], 5120\ns_add_u32 s44, %[wo], 3211264\nbuffer_load_dwordx4 v92, %[rw], s44 offen nt lds\ns_add_u32 m0, %[tb], 6144\ns_add_u32 s44, %[wo], 3276800\nbuffer_load_dwordx4 v92, %[rw], s44 offen nt lds\ns_add_u32 m0, %[tb], 7168\ns_add_u32 s44, %[wo], 3342336\n" \
"buffer_load_dwordx4 v92, %[rw], s44 offen nt lds\ns_waitcnt vmcnt(32)\nds_read_b64_tr_b16 a[56:57], v93 offset:8192\nds_read_b64_tr_b16 a[58:59], v93 offset:8704\nds_read_b64_tr_b16 a[64:65], v93 offset:8200\nds_read_b64_tr_b16 a[66:67], v93 offset:8712\nds_read_b64_tr_b16 v[76:77], v93 offset:8256\nds_read_b64_tr_b16 v[78:79], v93 offset:8768\n" \
"ds_read_b64_tr_b16 a[72:73], v93 offset:8264\nds_read_b64_tr_b16 a[74:75], v93 offset:8776\ns_waitcnt lgkmcnt(0)\ns_add_u32 m0, %[tb], 8192\ns_add_u32 s44, %[wo], 3407872\nbuffer_load_dwordx4 v92, %[rw], s44 offen nt lds\ns_add_u32 m0, %[tb], 9216\ns_add_u32 s44, %[wo], 3473408\n" \
"buffer_load_dwordx4 v92, %[rw], s44 offen nt lds\ns_add_u32 m0, %[tb], 10240\ns_add_u32 s44, %[wo], 3538944\nbuffer_load_dwordx4 v92, %[rw], s44 offen nt lds\ns_add_u32 m0, %[tb], 11264\ns_add_u32 s44, %[wo], 3604480\nbuffer_load_dwordx4 v92, %[rw], s44 offen nt lds\ns_waitcnt vmcnt(32)\n" \
"ds_read_b64_tr_b16 a[60:61], v94 offset:0\nds_read_b64_tr_b16 a[62:63], v94 offset:512\nds_read_b64_tr_b16 a[68:69], v94 offset:8\nds_read_b64_tr_b16 a[70:71], v94 offset:520\nds_read_b64_tr_b16 v[72:73], v94 offset:64\nds_read_b64_tr_b16 v[74:75], v94 offset:576\nds_read_b64_tr_b16 a[76:77], v94 offset:72\nds_read_b64_tr_b16 a[78:79], v94 offset:584\n" \
"s_waitcnt lgkmcnt(0)\ns_add_u32 m0, %[lw], 0\ns_add_u32 s44, %[wo], 3670016\nbuffer_load_dwordx4 v92, %[rw], s44 offen nt lds\ns_add_u32 m0, %[lw], 1024\ns_add_u32 s44, %[wo], 3735552\nbuffer_load_dwordx4 v92, %[rw], s44 offen nt lds\ns_add_u32 m0, %[lw], 2048\n" \
"s_add_u32 s44, %[wo], 3801088\nbuffer_load_dwordx4 v92, %[rw], s44 offen nt lds\ns_add_u32 m0, %[lw], 3072\ns_add_u32 s44, %[wo], 3866624\nbuffer_load_dwordx4 v92, %[rw], s44 offen nt lds\ns_waitcnt vmcnt(32)\nds_read_b64_tr_b16 a[80:81], v94 offset:4096\nds_read_b64_tr_b16 a[82:83], v94 offset:4608\n" \
"ds_read_b64_tr_b16 a[88:89], v94 offset:4104\nds_read_b64_tr_b16 a[90:91], v94 offset:4616\nds_read_b64_tr_b16 a[96:97], v94 offset:4160\nds_read_b64_tr_b16 a[98:99], v94 offset:4672\nds_read_b64_tr_b16 v[68:69], v94 offset:4168\nds_read_b64_tr_b16 v[70:71], v94 offset:4680\ns_waitcnt lgkmcnt(0)\ns_add_u32 m0, %[lw], 4096\n" \
"s_add_u32 s44, %[wo], 3932160\nbuffer_load_dwordx4 v92, %[rw], s44 offen nt lds\ns_add_u32 m0, %[lw], 5120\ns_add_u32 s44, %[wo], 3997696\nbuffer_load_dwordx4 v92, %[rw], s44 offen nt lds\ns_add_u32 m0, %[lw], 6144\ns_add_u32 s44, %[wo], 4063232\nbuffer_load_dwordx4 v92, %[rw], s44 offen nt lds\n" \
"s_add_u32 m0, %[lw], 7168\ns_add_u32 s44, %[wo], 4128768\nbuffer_load_dwordx4 v92, %[rw], s44 offen nt lds\ns_waitcnt vmcnt(32)\nds_read_b64_tr_b16 a[84:85], v94 offset:8192\nds_read_b64_tr_b16 a[86:87], v94 offset:8704\nds_read_b64_tr_b16 a[92:93], v94 offset:8200\nds_read_b64_tr_b16 a[94:95], v94 offset:8712\n" \
"ds_read_b64_tr_b16 a[100:101], v94 offset:8256\nds_read_b64_tr_b16 a[102:103], v94 offset:8768\nds_read_b64_tr_b16 v[64:65], v94 offset:8264\nds_read_b64_tr_b16 v[66:67], v94 offset:8776\ns_waitcnt lgkmcnt(0)\ns_add_u32 m0, %[lw], 8192\ns_add_u32 s44, %[wo], 4194304\nbuffer_load_dwordx4 v92, %[rw], s44 offen nt lds\n" \
"s_add_u32 m0, %[lw], 9216\ns_add_u32 s44, %[wo], 4259840\nbuffer_load_dwordx4 v92, %[rw], s44 offen nt lds\ns_add_u32 m0, %[lw], 10240\ns_add_u32 s44, %[wo], 4325376\nbuffer_load_dwordx4 v92, %[rw], s44 offen nt lds\ns_add_u32 m0, %[lw], 11264\ns_add_u32 s44, %[wo], 4390912\n" \
"buffer_load_dwordx4 v92, %[rw], s44 offen nt lds\ns_waitcnt vmcnt(32)\nds_read_b64_tr_b16 a[104:105], v94 offset:12288\nds_read_b64_tr_b16 a[106:107], v94 offset:12800\nds_read_b64_tr_b16 a[112:113], v94 offset:12296\nds_read_b64_tr_b16 a[114:115], v94 offset:12808\nds_read_b64_tr_b16 a[120:121], v94 offset:12352\nds_read_b64_tr_b16 a[122:123], v94 offset:12864\n" \
"ds_read_b64_tr_b16 a[128:129], v94 offset:12360\nds_read_b64_tr_b16 a[130:131], v94 offset:12872\ns_waitcnt lgkmcnt(0)\ns_add_u32 m0, %[lw], 12288\ns_add_u32 s44, %[wo], 4456448\nbuffer_load_dwordx4 v92, %[rw], s44 offen nt lds\ns_add_u32 m0, %[lw], 13312\ns_add_u32 s44, %[wo], 4521984\n" \
"buffer_load_dwordx4 v92, %[rw], s44 offen nt lds\ns_add_u32 m0, %[lw], 14336\ns_add_u32 s44, %[wo], 4587520\nbuffer_load_dwordx4 v92, %[rw], s44 offen nt lds\ns_add_u32 m0, %[lw], 15360\ns_add_u32 s44, %[wo], 4653056\nbuffer_load_dwordx4 v92, %[rw], s44 offen nt lds\ns_waitcnt vmcnt(32)\n" \
"ds_read_b64_tr_b16 a[108:109], v94 offset:16384\nds_read_b64_tr_b16 a[110:111], v94 offset:16896\nds_read_b64_tr_b16 a[116:117], v94 offset:16392\nds_read_b64_tr_b16 a[118:119], v94 offset:16904\nds_read_b64_tr_b16 a[124:125], v94 offset:16448\nds_read_b64_tr_b16 a[126:127], v94 offset:16960\nds_read_b64_tr_b16 a[132:133], v94 offset:16456\nds_read_b64_tr_b16 a[134:135], v94 offset:16968\n" \
"s_waitcnt lgkmcnt(0)\ns_add_u32 m0, %[lw], 16384\ns_add_u32 s44, %[wo], 4718592\nbuffer_load_dwordx4 v92, %[rw], s44 offen nt lds\ns_add_u32 m0, %[lw], 17408\ns_add_u32 s44, %[wo], 4784128\nbuffer_load_dwordx4 v92, %[rw], s44 offen nt lds\ns_add_u32 m0, %[lw], 18432\n" \
"s_add_u32 s44, %[wo], 4849664\nbuffer_load_dwordx4 v92, %[rw], s44 offen nt lds\ns_add_u32 m0, %[lw], 19456\ns_add_u32 s44, %[wo], 4915200\nbuffer_load_dwordx4 v92, %[rw], s44 offen nt lds\ns_waitcnt vmcnt(32)\nds_read_b64_tr_b16 a[136:137], v94 offset:20480\nds_read_b64_tr_b16 a[138:139], v94 offset:20992\n" \
"ds_read_b64_tr_b16 v[60:61], v94 offset:20488\nds_read_b64_tr_b16 v[62:63], v94 offset:21000\nds_read_b64_tr_b16 a[144:145], v94 offset:20544\nds_read_b64_tr_b16 a[146:147], v94 offset:21056\nds_read_b64_tr_b16 a[152:153], v94 offset:20552\nds_read_b64_tr_b16 a[154:155], v94 offset:21064\ns_waitcnt lgkmcnt(0)\ns_add_u32 m0, %[lw], 20480\n" \
"s_add_u32 s44, %[wo], 4980736\nbuffer_load_dwordx4 v92, %[rw], s44 offen nt lds\ns_add_u32 m0, %[lw], 21504\ns_add_u32 s44, %[wo], 5046272\nbuffer_load_dwordx4 v92, %[rw], s44 offen nt lds\ns_add_u32 m0, %[lw], 22528\ns_add_u32 s44, %[wo], 5111808\nbuffer_load_dwordx4 v92, %[rw], s44 offen nt lds\n" \
"s_add_u32 m0, %[lw], 23552\ns_add_u32 s44, %[wo], 5177344\nbuffer_load_dwordx4 v92, %[rw], s44 offen nt lds\ns_waitcnt vmcnt(32)\nds_read_b64_tr_b16 a[140:141], v93 offset:0\nds_read_b64_tr_b16 a[142:143], v93 offset:512\nds_read_b64_tr_b16 v[56:57], v93 offset:8\nds_read_b64_tr_b16 v[58:59], v93 offset:520\n" \
"ds_read_b64_tr_b16 a[148:149], v93 offset:64\nds_read_b64_tr_b16 a[150:151], v93 offset:576\nds_read_b64_tr_b16 a[156:157], v93 offset:72\nds_read_b64_tr_b16 a[158:159], v93 offset:584\ns_waitcnt lgkmcnt(0)\ns_add_u32 m0, %[tb], 0\ns_add_u32 s44, %[wo], 5242880\nbuffer_load_dwordx4 v92, %[rw], s44 offen nt lds\n" \
"s_add_u32 m0, %[tb], 1024\ns_add_u32 s44, %[wo], 5308416\nbuffer_load_dwordx4 v92, %[rw], s44 offen nt lds\ns_add_u32 m0, %[tb], 2048\ns_add_u32 s44, %[wo], 5373952\nbuffer_load_dwordx4 v92, %[rw], s44 offen nt lds\ns_add_u32 m0, %[tb], 3072\ns_add_u32 s44, %[wo], 5439488\n" \
"buffer_load_dwordx4 v92, %[rw], s44 offen nt lds\ns_waitcnt vmcnt(32)\nds_read_b64_tr_b16 a[160:161], v93 offset:4096\nds_read_b64_tr_b16 a[162:163], v93 offset:4608\nds_read_b64_tr_b16 a[168:169], v93 offset:4104\nds_read_b64_tr_b16 a[170:171], v93 offset:4616\nds_read_b64_tr_b16 v[52:53], v93 offset:4160\nds_read_b64_tr_b16 v[54:55], v93 offset:4672\n" \
"ds_read_b64_tr_b16 a[176:177], v93 offset:4168\nds_read_b64_tr_b16 a[178:179], v93 offset:4680\ns_waitcnt lgkmcnt(0)\ns_add_u32 m0, %[tb], 4096\ns_add_u32 s44, %[wo], 5505024\nbuffer_load_dwordx4 v92, %[rw], s44 offen nt lds\ns_add_u32 m0, %[tb], 5120\ns_add_u32 s44, %[wo], 5570560\n" \
"buffer_load_dwordx4 v92, %[rw], s44 offen nt lds\ns_add_u32 m0, %[tb], 6144\ns_add_u32 s44, %[wo], 5636096\nbuffer_load_dwordx4 v92, %[rw], s44 offen nt lds\ns_add_u32 m0, %[tb], 7168\ns_add_u32 s44, %[wo], 5701632\nbuffer_load_dwordx4 v92, %[rw], s44 offen nt lds\ns_waitcnt vmcnt(32)\n" \
"ds_read_b64_tr_b16 a[164:165], v93 offset:8192\nds_read_b64_tr_b16 a[166:167], v93 offset:8704\nds_read_b64_tr_b16 a[172:173], v93 offset:8200\nds_read_b64_tr_b16 a[174:175], v93 offset:8712\nds_read_b64_tr_b16 v[48:49], v93 offset:8256\nds_read_b64_tr_b16 v[50:51], v93 offset:8768\nds_read_b64_tr_b16 a[180:181], v93 offset:8264\nds_read_b64_tr_b16 a[182:183], v93 offset:8776\n" \
"s_waitcnt lgkmcnt(0)\ns_add_u32 m0, %[tb], 8192\ns_add_u32 s44, %[wo], 5767168\nbuffer_load_dwordx4 v92, %[rw], s44 offen nt lds\ns_add_u32 m0, %[tb], 9216\ns_add_u32 s44, %[wo], 5832704\nbuffer_load_dwordx4 v92, %[rw], s44 offen nt lds\ns_add_u32 m0, %[tb], 10240\n" \
"s_add_u32 s44, %[wo], 5898240\nbuffer_load_dwordx4 v92, %[rw], s44 offen nt lds\ns_add_u32 m0, %[tb], 11264\ns_add_u32 s44, %[wo], 5963776\nbuffer_load_dwordx4 v92, %[rw], s44 offen nt lds\ns_waitcnt vmcnt(32)\nds_read_b64_tr_b16 a[184:185], v94 offset:0\nds_read_b64_tr_b16 a[186:187], v94 offset:512\n" \
"ds_read_b64_tr_b16 a[192:193], v94 offset:8\nds_read_b64_tr_b16 a[194:195], v94 offset:520\nds_read_b64_tr_b16 a[200:201], v94 offset:64\nds_read_b64_tr_b16 a[202:203], v94 offset:576\nds_read_b64_tr_b16 v[44:45], v94 offset:72\nds_read_b64_tr_b16 v[46:47], v94 offset:584\ns_waitcnt lgkmcnt(0)\ns_add_u32 m0, %[lw], 0\n" \
"s_add_u32 s44, %[wo], 6029312\nbuffer_load_dwordx4 v92, %[rw], s44 offen nt lds\ns_add_u32 m0, %[lw], 1024\ns_add_u32 s44, %[wo], 6094848\nbuffer_load_dwordx4 v92, %[rw], s44 offen nt lds\ns_add_u32 m0, %[lw], 2048\ns_add_u32 s44, %[wo], 6160384\nbuffer_load_dwordx4 v92, %[rw], s44 offen nt lds\n" \
"s_add_u32 m0, %[lw], 3072\ns_add_u32 s44, %[wo], 6225920\nbuffer_load_dwordx4 v92, %[rw], s44 offen nt lds\ns_waitcnt vmcnt(32)\nds_read_b64_tr_b16 a[188:189], v94 offset:4096\nds_read_b64_tr_b16 a[190:191], v94 offset:4608\nds_read_b64_tr_b16 a[196:197], v94 offset:4104\nds_read_b64_tr_b16 a[198:199], v94 offset:4616\n" \
"ds_read_b64_tr_b16 a[204:205], v94 offset:4160\nds_read_b64_tr_b16 a[206:207], v94 offset:4672\nds_read_b64_tr_b16 v[40:41], v94 offset:4168\nds_read_b64_tr_b16 v[42:43], v94 offset:4680\ns_waitcnt lgkmcnt(0)\ns_add_u32 m0, %[lw], 4096\ns_add_u32 s44, %[wo], 6291456\nbuffer_load_dwordx4 v92, %[rw], s44 offen nt lds\n" \
"s_add_u32 m0, %[lw], 5120\ns_add_u32 s44, %[wo], 6356992\nbuffer_load_dwordx4 v92, %[rw], s44 offen nt lds\ns_add_u32 m0, %[lw], 6144\ns_add_u32 s44, %[wo], 6422528\nbuffer_load_dwordx4 v92, %[rw], s44 offen nt lds\ns_add_u32 m0, %[lw], 7168\ns_add_u32 s44, %[wo], 6488064\n" \
"buffer_load_dwordx4 v92, %[rw], s44 offen nt lds\ns_waitcnt vmcnt(32)\nds_read_b64_tr_b16 a[208:209], v94 offset:8192\nds_read_b64_tr_b16 a[210:211], v94 offset:8704\nds_read_b64_tr_b16 a[216:217], v94 offset:8200\nds_read_b64_tr_b16 a[218:219], v94 offset:8712\nds_read_b64_tr_b16 a[224:225], v94 offset:8256\nds_read_b64_tr_b16 a[226:227], v94 offset:8768\n" \
"ds_read_b64_tr_b16 a[232:233], v94 offset:8264\nds_read_b64_tr_b16 a[234:235], v94 offset:8776\ns_waitcnt lgkmcnt(0)\ns_add_u32 m0, %[lw], 8192\ns_add_u32 s44, %[wo], 6553600\nbuffer_load_dwordx4 v92, %[rw], s44 offen nt lds\ns_add_u32 m0, %[lw], 9216\ns_add_u32 s44, %[wo], 6619136\n" \
"buffer_load_dwordx4 v92, %[rw], s44 offen nt lds\ns_add_u32 m0, %[lw], 10240\ns_add_u32 s44, %[wo], 6684672\nbuffer_load_dwordx4 v92, %[rw], s44 offen nt lds\ns_add_u32 m0, %[lw], 11264\ns_add_u32 s44, %[wo], 6750208\nbuffer_load_dwordx4 v92, %[rw], s44 offen nt lds\ns_waitcnt vmcnt(32)\n" \
"ds_read_b64_tr_b16 a[212:213], v94 offset:12288\nds_read_b64_tr_b16 a[214:215], v94 offset:12800\nds_read_b64_tr_b16 a[220:221], v94 offset:12296\nds_read_b64_tr_b16 a[222:223], v94 offset:12808\nds_read_b64_tr_b16 a[228:229], v94 offset:12352\nds_read_b64_tr_b16 a[230:231], v94 offset:12864\nds_read_b64_tr_b16 a[236:237], v94 offset:12360\nds_read_b64_tr_b16 a[238:239], v94 offset:12872\n" \
"s_waitcnt lgkmcnt(0)\ns_add_u32 m0, %[lw], 12288\ns_add_u32 s44, %[wo], 6815744\nbuffer_load_dwordx4 v92, %[rw], s44 offen nt lds\ns_add_u32 m0, %[lw], 13312\ns_add_u32 s44, %[wo], 6881280\nbuffer_load_dwordx4 v92, %[rw], s44 offen nt lds\ns_add_u32 m0, %[lw], 14336\n" \
"s_add_u32 s44, %[wo], 6946816\nbuffer_load_dwordx4 v92, %[rw], s44 offen nt lds\ns_add_u32 m0, %[lw], 15360\ns_add_u32 s44, %[wo], 7012352\nbuffer_load_dwordx4 v92, %[rw], s44 offen nt lds\ns_waitcnt vmcnt(32)\nds_read_b64_tr_b16 a[240:241], v94 offset:16384\nds_read_b64_tr_b16 a[242:243], v94 offset:16896\n" \
"ds_read_b64_tr_b16 v[36:37], v94 offset:16392\nds_read_b64_tr_b16 v[38:39], v94 offset:16904\nds_read_b64_tr_b16 a[248:249], v94 offset:16448\nds_read_b64_tr_b16 a[250:251], v94 offset:16960\nds_read_b64_tr_b16 v[96:97], v94 offset:16456\nds_read_b64_tr_b16 v[98:99], v94 offset:16968\ns_waitcnt lgkmcnt(0)\ns_add_u32 m0, %[lw], 16384\n" \
"s_add_u32 s44, %[wo], 7077888\nbuffer_load_dwordx4 v92, %[rw], s44 offen nt lds\ns_add_u32 m0, %[lw], 17408\ns_add_u32 s44, %[wo], 7143424\nbuffer_load_dwordx4 v92, %[rw], s44 offen nt lds\ns_add_u32 m0, %[lw], 18432\ns_add_u32 s44, %[wo], 7208960\nbuffer_load_dwordx4 v92, %[rw], s44 offen nt lds\n" \
"s_add_u32 m0, %[lw], 19456\ns_add_u32 s44, %[wo], 7274496\nbuffer_load_dwordx4 v92, %[rw], s44 offen nt lds\ns_waitcnt vmcnt(32)\nds_read_b64_tr_b16 a[244:245], v94 offset:20480\nds_read_b64_tr_b16 a[246:247], v94 offset:20992\nds_read_b64_tr_b16 v[32:33], v94 offset:20488\nds_read_b64_tr_b16 v[34:35], v94 offset:21000\n" \
"ds_read_b64_tr_b16 a[252:253], v94 offset:20544\nds_read_b64_tr_b16 a[254:255], v94 offset:21056\nds_read_b64_tr_b16 v[100:101], v94 offset:20552\nds_read_b64_tr_b16 v[102:103], v94 offset:21064\ns_waitcnt lgkmcnt(0)\ns_add_u32 m0, %[lw], 20480\ns_add_u32 s44, %[wo], 7340032\nbuffer_load_dwordx4 v92, %[rw], s44 offen nt lds\n" \
"s_add_u32 m0, %[lw], 21504\ns_add_u32 s44, %[wo], 7405568\nbuffer_load_dwordx4 v92, %[rw], s44 offen nt lds\ns_add_u32 m0, %[lw], 22528\ns_add_u32 s44, %[wo], 7471104\nbuffer_load_dwordx4 v92, %[rw], s44 offen nt lds\ns_add_u32 m0, %[lw], 23552\ns_add_u32 s44, %[wo], 7536640\n" \
"buffer_load_dwordx4 v92, %[rw], s44 offen nt lds\ns_waitcnt vmcnt(32)\nds_read_b64_tr_b16 v[104:105], v93 offset:0\nds_read_b64_tr_b16 v[106:107], v93 offset:512\nds_read_b64_tr_b16 v[112:113], v93 offset:8\nds_read_b64_tr_b16 v[114:115], v93 offset:520\nds_read_b64_tr_b16 v[28:29], v93 offset:64\nds_read_b64_tr_b16 v[30:31], v93 offset:576\n" \
"ds_read_b64_tr_b16 v[120:121], v93 offset:72\nds_read_b64_tr_b16 v[122:123], v93 offset:584\ns_waitcnt lgkmcnt(0)\ns_add_u32 m0, %[tb], 0\ns_add_u32 s44, %[wo], 7602176\nbuffer_load_dwordx4 v92, %[rw], s44 offen nt lds\ns_add_u32 m0, %[tb], 1024\ns_add_u32 s44, %[wo], 7667712\n" \
"buffer_load_dwordx4 v92, %[rw], s44 offen nt lds\ns_add_u32 m0, %[tb], 2048\ns_add_u32 s44, %[wo], 7733248\nbuffer_load_dwordx4 v92, %[rw], s44 offen nt lds\ns_add_u32 m0, %[tb], 3072\ns_add_u32 s44, %[wo], 7798784\nbuffer_load_dwordx4 v92, %[rw], s44 offen nt lds\ns_waitcnt vmcnt(32)\n" \
"ds_read_b64_tr_b16 v[108:109], v93 offset:4096\nds_read_b64_tr_b16 v[110:111], v93 offset:4608\nds_read_b64_tr_b16 v[116:117], v93 offset:4104\nds_read_b64_tr_b16 v[118:119], v93 offset:4616\nds_read_b64_tr_b16 v[24:25], v93 offset:4160\nds_read_b64_tr_b16 v[26:27], v93 offset:4672\nds_read_b64_tr_b16 v[124:125], v93 offset:4168\nds_read_b64_tr_b16 v[126:127], v93 offset:4680\n" \
"s_waitcnt lgkmcnt(0)\nbuffer_load_dwordx2 v[88:89], off, s[36:39], s45 sc1\ns_add_u32 m0, %[tb], 4096\ns_add_u32 s44, %[wo], 7864320\nbuffer_load_dwordx4 v92, %[rw], s44 offen nt lds\ns_add_u32 m0, %[tb], 5120\ns_add_u32 s44, %[wo], 7929856\nbuffer_load_dwordx4 v92, %[rw], s44 offen nt lds\n" \
"s_add_u32 m0, %[tb], 6144\ns_add_u32 s44, %[wo], 7995392\nbuffer_load_dwordx4 v92, %[rw], s44 offen nt lds\ns_add_u32 m0, %[tb], 7168\ns_add_u32 s44, %[wo], 8060928\nbuffer_load_dwordx4 v92, %[rw], s44 offen nt lds\ns_waitcnt vmcnt(33)\nds_read_b64_tr_b16 v[128:129], v93 offset:8192\n" \
"ds_read_b64_tr_b16 v[130:131], v93 offset:8704\nds_read_b64_tr_b16 v[136:137], v93 offset:8200\nds_read_b64_tr_b16 v[138:139], v93 offset:8712\nds_read_b64_tr_b16 v[144:145], v93 offset:8256\nds_read_b64_tr_b16 v[146:147], v93 offset:8768\nds_read_b64_tr_b16 v[20:21], v93 offset:8264\nds_read_b64_tr_b16 v[22:23], v93 offset:8776\ns_waitcnt lgkmcnt(0)\n" \
"s_add_u32 m0, %[tb], 8192\ns_add_u32 s44, %[wo], 8126464\nbuffer_load_dwordx4 v92, %[rw], s44 offen nt lds\ns_add_u32 m0, %[tb], 9216\ns_add_u32 s44, %[wo], 8192000\nbuffer_load_dwordx4 v92, %[rw], s44 offen nt lds\ns_add_u32 m0, %[tb], 10240\ns_add_u32 s44, %[wo], 8257536\n" \
"buffer_load_dwordx4 v92, %[rw], s44 offen nt lds\ns_add_u32 m0, %[tb], 11264\ns_add_u32 s44, %[wo], 8323072\nbuffer_load_dwordx4 v92, %[rw], s44 offen nt lds\ns_waitcnt vmcnt(33)\nds_read_b64_tr_b16 v[132:133], v94 offset:0\nds_read_b64_tr_b16 v[134:135], v94 offset:512\nds_read_b64_tr_b16 v[140:141], v94 offset:8\n" \
"ds_read_b64_tr_b16 v[142:143], v94 offset:520\nds_read_b64_tr_b16 v[148:149], v94 offset:64\nds_read_b64_tr_b16 v[150:151], v94 offset:576\nds_read_b64_tr_b16 v[16:17], v94 offset:72\nds_read_b64_tr_b16 v[18:19], v94 offset:584\ns_waitcnt lgkmcnt(0)\nds_write_b128 v1, v[84:87] offset:0\nds_write_b128 v1, v[80:83] offset:1024\n" \
"ds_write_b128 v1, v[76:79] offset:2048\nds_write_b128 v1, v[72:75] offset:3072\ns_waitcnt vmcnt(29)\nds_read_b64_tr_b16 v[152:153], v94 offset:4096\nds_read_b64_tr_b16 v[154:155], v94 offset:4608\nds_read_b64_tr_b16 v[160:161], v94 offset:4104\nds_read_b64_tr_b16 v[162:163], v94 offset:4616\nds_read_b64_tr_b16 v[168:169], v94 offset:4160\n" \
"ds_read_b64_tr_b16 v[170:171], v94 offset:4672\nds_read_b64_tr_b16 v[176:177], v94 offset:4168\nds_read_b64_tr_b16 v[178:179], v94 offset:4680\ns_waitcnt lgkmcnt(0)\nds_write_b128 v1, v[68:71] offset:4096\nds_write_b128 v1, v[64:67] offset:5120\nds_write_b128 v1, v[60:63] offset:6144\nds_write_b128 v1, v[56:59] offset:7168\n" \
"s_waitcnt vmcnt(25)\nds_read_b64_tr_b16 v[156:157], v94 offset:8192\nds_read_b64_tr_b16 v[158:159], v94 offset:8704\nds_read_b64_tr_b16 v[164:165], v94 offset:8200\nds_read_b64_tr_b16 v[166:167], v94 offset:8712\nds_read_b64_tr_b16 v[172:173], v94 offset:8256\nds_read_b64_tr_b16 v[174:175], v94 offset:8768\nds_read_b64_tr_b16 v[180:181], v94 offset:8264\n" \
"ds_read_b64_tr_b16 v[182:183], v94 offset:8776\ns_waitcnt lgkmcnt(0)\nds_write_b128 v1, v[52:55] offset:8192\nds_write_b128 v1, v[48:51] offset:9216\nds_write_b128 v1, v[44:47] offset:10240\nds_write_b128 v1, v[40:43] offset:11264\ns_waitcnt vmcnt(21)\nds_read_b64_tr_b16 v[184:185], v94 offset:12288\n" \
"ds_read_b64_tr_b16 v[186:187], v94 offset:12800\nds_read_b64_tr_b16 v[40:41], v94 offset:12296\nds_read_b64_tr_b16 v[42:43], v94 offset:12808\nds_read_b64_tr_b16 v[192:193], v94 offset:12352\nds_read_b64_tr_b16 v[194:195], v94 offset:12864\nds_read_b64_tr_b16 v[200:201], v94 offset:12360\nds_read_b64_tr_b16 v[202:203], v94 offset:12872\ns_waitcnt lgkmcnt(0)\n" \
"ds_write_b128 v1, v[36:39] offset:12288\nds_write_b128 v1, v[32:35] offset:13312\nds_write_b128 v1, v[28:31] offset:14336\nds_write_b128 v1, v[24:27] offset:15360\ns_waitcnt vmcnt(17)\nds_read_b64_tr_b16 v[188:189], v94 offset:16384\nds_read_b64_tr_b16 v[190:191], v94 offset:16896\nds_read_b64_tr_b16 v[24:25], v94 offset:16392\n" \
"ds_read_b64_tr_b16 v[26:27], v94 offset:16904\nds_read_b64_tr_b16 v[196:197], v94 offset:16448\nds_read_b64_tr_b16 v[198:199], v94 offset:16960\nds_read_b64_tr_b16 v[204:205], v94 offset:16456\nds_read_b64_tr_b16 v[206:207], v94 offset:16968\ns_waitcnt lgkmcnt(0)\nds_write_b128 v1, v[20:23] offset:16384\nds_write_b128 v1, v[16:19] offset:17408\n" \
"ds_write_b128 v1, v[40:43] offset:18432\nds_write_b128 v1, v[24:27] offset:19456\ns_waitcnt vmcnt(13)\nds_read_b64_tr_b16 v[208:209], v94 offset:20480\nds_read_b64_tr_b16 v[210:211], v94 offset:20992\nds_read_b64_tr_b16 v[216:217], v94 offset:20488\nds_read_b64_tr_b16 v[218:219], v94 offset:21000\nds_read_b64_tr_b16 v[24:25], v94 offset:20544\n" \
"ds_read_b64_tr_b16 v[26:27], v94 offset:21056\nds_read_b64_tr_b16 v[224:225], v94 offset:20552\nds_read_b64_tr_b16 v[226:227], v94 offset:21064\ns_waitcnt lgkmcnt(0)\nds_write_b128 v1, v[24:27] offset:20480\ns_waitcnt vmcnt(9)\nds_read_b64_tr_b16 v[212:213], v93 offset:0\nds_read_b64_tr_b16 v[214:215], v93 offset:512\n" \
"ds_read_b64_tr_b16 v[220:221], v93 offset:8\nds_read_b64_tr_b16 v[222:223], v93 offset:520\nds_read_b64_tr_b16 v[24:25], v93 offset:64\nds_read_b64_tr_b16 v[26:27], v93 offset:576\nds_read_b64_tr_b16 v[228:229], v93 offset:72\nds_read_b64_tr_b16 v[230:231], v93 offset:584\ns_waitcnt lgkmcnt(0)\nds_write_b128 v1, v[24:27] offset:21504\n" \
"s_waitcnt vmcnt(4)\nds_read_b64_tr_b16 v[232:233], v93 offset:4096\nds_read_b64_tr_b16 v[234:235], v93 offset:4608\nds_read_b64_tr_b16 v[240:241], v93 offset:4104\nds_read_b64_tr_b16 v[242:243], v93 offset:4616\nds_read_b64_tr_b16 v[248:249], v93 offset:4160\nds_read_b64_tr_b16 v[250:251], v93 offset:4672\nds_read_b64_tr_b16 v[24:25], v93 offset:4168\n" \
"ds_read_b64_tr_b16 v[26:27], v93 offset:4680\ns_waitcnt lgkmcnt(0)\nds_write_b128 v1, v[24:27] offset:22528\ns_waitcnt vmcnt(0)\nds_read_b64_tr_b16 v[236:237], v93 offset:8192\nds_read_b64_tr_b16 v[238:239], v93 offset:8704\nds_read_b64_tr_b16 v[244:245], v93 offset:8200\nds_read_b64_tr_b16 v[246:247], v93 offset:8712\n" \
"ds_read_b64_tr_b16 v[252:253], v93 offset:8256\nds_read_b64_tr_b16 v[254:255], v93 offset:8768\nds_read_b64_tr_b16 v[24:25], v93 offset:8264\nds_read_b64_tr_b16 v[26:27], v93 offset:8776\ns_waitcnt lgkmcnt(0)\ns_mov_b32 s43, 0\ns_mov_b32 s86, 110\ns_mov_b32 s89, 0\n" \
".Lqk_hb%=:\nv_cmp_ne_u32 vcc, v95, v88\ns_cbranch_vccnz .Lqk_hb1%=\ns_sleep 4\nbuffer_load_dwordx2 v[88:89], off, s[36:39], s45 sc1\ns_waitcnt vmcnt(0)\ns_add_u32 s43, s43, 1\ns_cmp_lt_u32 s43, 262144\n" \
"s_cbranch_scc1 .Lqk_hb%=\ns_branch .Lqk_abort%=\n.Lqk_hb1%=:\nv_readfirstlane_b32 s44, v89\ns_and_b32 s44, s44, 0x200000\ns_add_u32 s36, s36, s44\ns_addc_u32 s37, s37, 0\nds_write_b128 v1, v[24:27] offset:23552\n" \
"s_waitcnt lgkmcnt(0)\ns_barrier\ns_mov_b32 s40, -1\ns_mov_b32 s43, 0\ns_mov_b32 s61, 0x7fff7fff\ns_mov_b32 s58, 0x3e0293ee\ns_mov_b32 s59, 0xbe0293ee\ns_lshl_b32 s57, %[w], 12\n" \
"s_mov_b32 s62, 0xff00ff00\ns_mov_b32 s63, 0xff00ff00\ns_mov_b32 s56, 0\ns_cmp_eq_u32 %[w], 3\ns_cbranch_scc1 .Lqk_fetch0%=\n.Lqk_pwait%=:\ns_barrier\nds_read_b128 v[80:83], v2 offset:0\n" \
"ds_read_b128 v[84:87], v2 offset:256\nds_read_b128 v[88:91], v2 offset:2048\n.Lqk_proj%=:\ns_waitcnt lgkmcnt(2)\nv_mfma_f32_16x16x32_bf16 v[16:19], a[0:3], v[80:83], 0\nds_read_b128 v[92:95], v2 offset:2304\nv_mfma_f32_16x16x32_bf16 v[20:23], a[8:11], v[80:83], 0\nds_read_b128 v[72:75], v1 offset:0\n" \
"v_mfma_f32_16x16x32_bf16 v[24:27], a[16:19], v[80:83], 0\nv_mfma_f32_16x16x32_bf16 v[28:31], a[24:27], v[80:83], 0\ns_waitcnt lgkmcnt(3)\nv_mfma_f32_16x16x32_bf16 v[32:35], a[0:3], v[84:87], 0\nds_read_b128 v[80:83], v2 offset:4096\nv_mfma_f32_16x16x32_bf16 v[36:39], a[8:11], v[84:87], 0\nv_mfma_f32_16x16x32_bf16 v[40:43], a[16:19], v[84:87], 0\nv_mfma_f32_16x16x32_bf16 v[44:47], a[24:27], v[84:87], 0\n" \
"s_waitcnt lgkmcnt(3)\nv_mfma_f32_16x16x32_bf16 v[16:19], a[4:7], v[88:91], v[16:19]\nds_read_b128 v[84:87], v2 offset:4352\nv_mfma_f32_16x16x32_bf16 v[20:23], a[12:15], v[88:91], v[20:23]\nds_read_b128 v[76:79], v1 offset:1024\nv_mfma_f32_16x16x32_bf16 v[24:27], a[20:23], v[88:91], v[24:27]\nv_mfma_f32_16x16x32_bf16 v[28:31], a[28:31], v[88:91], v[28:31]\ns_waitcnt lgkmcnt(4)\n" \
"v_mfma_f32_16x16x32_bf16 v[32:35], a[4:7], v[92:95], v[32:35]\nds_read_b128 v[88:91], v2 offset:6144\nv_mfma_f32_16x16x32_bf16 v[36:39], a[12:15], v[92:95], v[36:39]\nv_mfma_f32_16x16x32_bf16 v[40:43], a[20:23], v[92:95], v[40:43]\nv_mfma_f32_16x16x32_bf16 v[44:47], a[28:31], v[92:95], v[44:47]\ns_waitcnt lgkmcnt(3)\nv_mfma_f32_16x16x32_bf16 v[16:19], a[32:35], v[80:83], v[16:19]\nds_read_b128 v[92:95], v2 offset:6400\n" \
"v_mfma_f32_16x16x32_bf16 v[20:23], v[72:75], v[80:83], v[20:23]\nds_read_b128 v[64:67], v1 offset:2048\nv_mfma_f32_16x16x32_bf16 v[24:27], a[40:43], v[80:83], v[24:27]\nv_mfma_f32_16x16x32_bf16 v[28:31], a[48:51], v[80:83], v[28:31]\ns_waitcnt lgkmcnt(4)\nv_mfma_f32_16x16x32_bf16 v[32:35], a[32:35], v[84:87], v[32:35]\nds_read_b128 v[80:83], v2 offset:8192\nv_mfma_f32_16x16x32_bf16 v[36:39], v[72:75], v[84:87], v[36:39]\n" \
"v_mfma_f32_16x16x32_bf16 v[40:43], a[40:43], v[84:87], v[40:43]\nv_mfma_f32_16x16x32_bf16 v[44:47], a[48:51], v[84:87], v[44:47]\ns_waitcnt lgkmcnt(3)\nv_mfma_f32_16x16x32_bf16 v[16:19], a[36:39], v[88:91], v[16:19]\nds_read_b128 v[84:87], v2 offset:8448\nv_mfma_f32_16x16x32_bf16 v[20:23], v[76:79], v[88:91], v[20:23]\nds_read_b128 v[68:71], v1 offset:3072\nv_mfma_f32_16x16x32_bf16 v[24:27], a[44:47], v[88:91], v[24:27]\n" \
"v_mfma_f32_16x16x32_bf16 v[28:31], a[52:55], v[88:91], v[28:31]\ns_waitcnt lgkmcnt(4)\nv_mfma_f32_16x16x32_bf16 v[32:35], a[36:39], v[92:95], v[32:35]\nds_read_b128 v[88:91], v2 offset:10240\nv_mfma_f32_16x16x32_bf16 v[36:39], v[76:79], v[92:95], v[36:39]\nv_mfma_f32_16x16x32_bf16 v[40:43], a[44:47], v[92:95], v[40:43]\nv_mfma_f32_16x16x32_bf16 v[44:47], a[52:55], v[92:95], v[44:47]\ns_waitcnt lgkmcnt(3)\n" \
"v_mfma_f32_16x16x32_bf16 v[16:19], a[56:59], v[80:83], v[16:19]\nds_read_b128 v[92:95], v2 offset:10496\nv_mfma_f32_16x16x32_bf16 v[20:23], a[64:67], v[80:83], v[20:23]\nds_read_b128 v[72:75], v1 offset:4096\nv_mfma_f32_16x16x32_bf16 v[24:27], v[64:67], v[80:83], v[24:27]\nv_mfma_f32_16x16x32_bf16 v[28:31], a[72:75], v[80:83], v[28:31]\ns_waitcnt lgkmcnt(4)\nv_mfma_f32_16x16x32_bf16 v[32:35], a[56:59], v[84:87], v[32:35]\n" \
"ds_read_b128 v[80:83], v2 offset:12288\nv_mfma_f32_16x16x32_bf16 v[36:39], a[64:67], v[84:87], v[36:39]\nv_mfma_f32_16x16x32_bf16 v[40:43], v[64:67], v[84:87], v[40:43]\nv_mfma_f32_16x16x32_bf16 v[44:47], a[72:75], v[84:87], v[44:47]\ns_waitcnt lgkmcnt(3)\nv_mfma_f32_16x16x32_bf16 v[16:19], a[60:63], v[88:91], v[16:19]\nds_read_b128 v[84:87], v2 offset:12544\nv_mfma_f32_16x16x32_bf16 v[20:23], a[68:71], v[88:91], v[20:23]\n" \
"ds_read_b128 v[76:79], v1 offset:5120\nv_mfma_f32_16x16x32_bf16 v[24:27], v[68:71], v[88:91], v[24:27]\nv_mfma_f32_16x16x32_bf16 v[28:31], a[76:79], v[88:91], v[28:31]\ns_waitcnt lgkmcnt(4)\nv_mfma_f32_16x16x32_bf16 v[32:35], a[60:63], v[92:95], v[32:35]\nds_read_b128 v[88:91], v2 offset:14336\nv_mfma_f32_16x16x32_bf16 v[36:39], a[68:71], v[92:95], v[36:39]\nv_mfma_f32_16x16x32_bf16 v[40:43], v[68:71], v[92:95], v[40:43]\n" \
"v_mfma_f32_16x16x32_bf16 v[44:47], a[76:79], v[92:95], v[44:47]\n.Lqk_pj%=:\ns_waitcnt lgkmcnt(3)\nv_mfma_f32_16x16x32_bf16 v[16:19], a[80:83], v[80:83], v[16:19]\nds_read_b128 v[92:95], v2 offset:14592\nv_mfma_f32_16x16x32_bf16 v[20:23], a[88:91], v[80:83], v[20:23]\ns_add_u32 s40, s40, 1\nv_mfma_f32_16x16x32_bf16 v[24:27], a[96:99], v[80:83], v[24:27]\n" \
"s_lshl_b32 s44, s40, 17\nv_mfma_f32_16x16x32_bf16 v[28:31], v[72:75], v[80:83], v[28:31]\ns_add_u32 s44, s44, %[xn]\ns_waitcnt lgkmcnt(3)\nv_mfma_f32_16x16x32_bf16 v[32:35], a[80:83], v[84:87], v[32:35]\ns_barrier\nds_read_b128 v[80:83], v2 offset:16384\nv_mfma_f32_16x16x32_bf16 v[36:39], a[88:91], v[84:87], v[36:39]\n" \
"s_lshr_b32 s45, s44, 8\nv_mfma_f32_16x16x32_bf16 v[40:43], a[96:99], v[84:87], v[40:43]\ns_and_b32 s45, s45, 0x2000\nv_mfma_f32_16x16x32_bf16 v[44:47], v[72:75], v[84:87], v[44:47]\ns_xor_b32 s41, s44, s45\ns_waitcnt lgkmcnt(2)\nv_mfma_f32_16x16x32_bf16 v[16:19], a[84:87], v[88:91], v[16:19]\nds_read_b128 v[84:87], v2 offset:16640\n" \
"v_mfma_f32_16x16x32_bf16 v[20:23], a[92:95], v[88:91], v[20:23]\ns_add_u32 s41, s41, %[xw]\ns_and_b32 s42, s40, 3\ns_add_u32 m0, s57, 32768\nv_mfma_f32_16x16x32_bf16 v[24:27], a[100:103], v[88:91], v[24:27]\ns_add_u32 s60, s41, 65536\nbuffer_load_dwordx4 v0, s[36:39], s60 offen offset:0 sc1 lds\nv_mfma_f32_16x16x32_bf16 v[28:31], v[76:79], v[88:91], v[28:31]\n" \
"s_lshl_b32 s42, s42, 15\ns_waitcnt lgkmcnt(2)\nv_mfma_f32_16x16x32_bf16 v[32:35], a[84:87], v[92:95], v[32:35]\nds_read_b128 v[88:91], v2 offset:18432\nv_mfma_f32_16x16x32_bf16 v[36:39], a[92:95], v[92:95], v[36:39]\ns_and_b32 s44, s40, 1\nv_mfma_f32_16x16x32_bf16 v[40:43], a[100:103], v[92:95], v[40:43]\ns_lshl_b32 s55, s44, 17\n" \
"buffer_load_dwordx4 v0, s[36:39], s60 offen offset:1024 sc1 lds\nv_mfma_f32_16x16x32_bf16 v[44:47], v[76:79], v[92:95], v[44:47]\ns_add_u32 s56, s56, %[fs]\ns_waitcnt lgkmcnt(2)\nv_mfma_f32_16x16x32_bf16 v[16:19], a[104:107], v[80:83], v[16:19]\nds_read_b128 v[92:95], v2 offset:18688\nv_mfma_f32_16x16x32_bf16 v[20:23], a[112:115], v[80:83], v[20:23]\nds_read_b128 v[72:75], v1 offset:6144\n" \
"v_mfma_f32_16x16x32_bf16 v[24:27], a[120:123], v[80:83], v[24:27]\nbuffer_load_dwordx4 v0, s[36:39], s60 offen offset:2048 sc1 lds\nv_mfma_f32_16x16x32_bf16 v[28:31], a[128:131], v[80:83], v[28:31]\ns_waitcnt lgkmcnt(3)\nv_mfma_f32_16x16x32_bf16 v[32:35], a[104:107], v[84:87], v[32:35]\nds_read_b128 v[80:83], v2 offset:20480\nv_mfma_f32_16x16x32_bf16 v[36:39], a[112:115], v[84:87], v[36:39]\nv_mfma_f32_16x16x32_bf16 v[40:43], a[120:123], v[84:87], v[40:43]\n" \
"buffer_load_dwordx4 v0, s[36:39], s60 offen offset:3072 sc1 lds\nv_mfma_f32_16x16x32_bf16 v[44:47], a[128:131], v[84:87], v[44:47]\ns_waitcnt lgkmcnt(3)\nv_mfma_f32_16x16x32_bf16 v[16:19], a[108:111], v[88:91], v[16:19]\nds_read_b128 v[84:87], v2 offset:20736\nv_mfma_f32_16x16x32_bf16 v[20:23], a[116:119], v[88:91], v[20:23]\nds_read_b128 v[76:79], v1 offset:7168\nv_mfma_f32_16x16x32_bf16 v[24:27], a[124:127], v[88:91], v[24:27]\n" \
"v_mfma_f32_16x16x32_bf16 v[28:31], a[132:135], v[88:91], v[28:31]\ns_waitcnt lgkmcnt(4)\nv_mfma_f32_16x16x32_bf16 v[32:35], a[108:111], v[92:95], v[32:35]\nds_read_b128 v[88:91], v2 offset:22528\nv_mfma_f32_16x16x32_bf16 v[36:39], a[116:119], v[92:95], v[36:39]\nv_mfma_f32_16x16x32_bf16 v[40:43], a[124:127], v[92:95], v[40:43]\nv_mfma_f32_16x16x32_bf16 v[44:47], a[132:135], v[92:95], v[44:47]\ns_waitcnt lgkmcnt(3)\n" \
"v_mfma_f32_16x16x32_bf16 v[16:19], a[136:139], v[80:83], v[16:19]\nds_read_b128 v[92:95], v2 offset:22784\nv_mfma_f32_16x16x32_bf16 v[20:23], v[72:75], v[80:83], v[20:23]\nds_read_b128 v[64:67], v1 offset:8192\nv_mfma_f32_16x16x32_bf16 v[24:27], a[144:147], v[80:83], v[24:27]\nv_mfma_f32_16x16x32_bf16 v[28:31], a[152:155], v[80:83], v[28:31]\ns_waitcnt lgkmcnt(4)\nv_mfma_f32_16x16x32_bf16 v[32:35], a[136:139], v[84:87], v[32:35]\n" \
"ds_read_b128 v[80:83], v2 offset:24576\nv_mfma_f32_16x16x32_bf16 v[36:39], v[72:75], v[84:87], v[36:39]\nv_mfma_f32_16x16x32_bf16 v[40:43], a[144:147], v[84:87], v[40:43]\nv_mfma_f32_16x16x32_bf16 v[44:47], a[152:155], v[84:87], v[44:47]\ns_waitcnt lgkmcnt(3)\nv_mfma_f32_16x16x32_bf16 v[16:19], a[140:143], v[88:91], v[16:19]\nds_read_b128 v[84:87], v2 offset:24832\nv_mfma_f32_16x16x32_bf16 v[20:23], v[76:79], v[88:91], v[20:23]\n" \
"ds_read_b128 v[68:71], v1 offset:9216\nv_mfma_f32_16x16x32_bf16 v[24:27], a[148:151], v[88:91], v[24:27]\nv_mfma_f32_16x16x32_bf16 v[28:31], a[156:159], v[88:91], v[28:31]\ns_waitcnt lgkmcnt(4)\nv_mfma_f32_16x16x32_bf16 v[32:35], a[140:143], v[92:95], v[32:35]\nds_read_b128 v[88:91], v2 offset:26624\nv_mfma_f32_16x16x32_bf16 v[36:39], v[76:79], v[92:95], v[36:39]\nv_mfma_f32_16x16x32_bf16 v[40:43], a[148:151], v[92:95], v[40:43]\n" \
"v_mfma_f32_16x16x32_bf16 v[44:47], a[156:159], v[92:95], v[44:47]\ns_waitcnt lgkmcnt(3)\nv_mfma_f32_16x16x32_bf16 v[16:19], a[160:163], v[80:83], v[16:19]\nds_read_b128 v[92:95], v2 offset:26880\nv_mfma_f32_16x16x32_bf16 v[20:23], a[168:171], v[80:83], v[20:23]\nds_read_b128 v[72:75], v1 offset:10240\nv_mfma_f32_16x16x32_bf16 v[24:27], v[64:67], v[80:83], v[24:27]\nv_mfma_f32_16x16x32_bf16 v[28:31], a[176:179], v[80:83], v[28:31]\n" \
"s_waitcnt lgkmcnt(4)\nv_mfma_f32_16x16x32_bf16 v[32:35], a[160:163], v[84:87], v[32:35]\nds_read_b128 v[80:83], v2 offset:28672\nv_mfma_f32_16x16x32_bf16 v[36:39], a[168:171], v[84:87], v[36:39]\nv_mfma_f32_16x16x32_bf16 v[40:43], v[64:67], v[84:87], v[40:43]\nv_mfma_f32_16x16x32_bf16 v[44:47], a[176:179], v[84:87], v[44:47]\ns_waitcnt lgkmcnt(3)\nv_mfma_f32_16x16x32_bf16 v[16:19], a[164:167], v[88:91], v[16:19]\n" \
"ds_read_b128 v[84:87], v2 offset:28928\nv_mfma_f32_16x16x32_bf16 v[20:23], a[172:175], v[88:91], v[20:23]\nds_read_b128 v[76:79], v1 offset:11264\nv_mfma_f32_16x16x32_bf16 v[24:27], v[68:71], v[88:91], v[24:27]\nv_mfma_f32_16x16x32_bf16 v[28:31], a[180:183], v[88:91], v[28:31]\ns_waitcnt lgkmcnt(4)\nv_mfma_f32_16x16x32_bf16 v[32:35], a[164:167], v[92:95], v[32:35]\nds_read_b128 v[88:91], v2 offset:30720\n" \
"v_mfma_f32_16x16x32_bf16 v[36:39], a[172:175], v[92:95], v[36:39]\nv_mfma_f32_16x16x32_bf16 v[40:43], v[68:71], v[92:95], v[40:43]\nv_mfma_f32_16x16x32_bf16 v[44:47], a[180:183], v[92:95], v[44:47]\ns_waitcnt lgkmcnt(3)\nv_mfma_f32_16x16x32_bf16 v[16:19], a[184:187], v[80:83], v[16:19]\nds_read_b128 v[92:95], v2 offset:30976\nv_mfma_f32_16x16x32_bf16 v[20:23], a[192:195], v[80:83], v[20:23]\nv_mfma_f32_16x16x32_bf16 v[24:27], a[200:203], v[80:83], v[24:27]\n" \
"v_mfma_f32_16x16x32_bf16 v[28:31], v[72:75], v[80:83], v[28:31]\ns_waitcnt lgkmcnt(3)\nv_mfma_f32_16x16x32_bf16 v[32:35], a[184:187], v[84:87], v[32:35]\ns_waitcnt vmcnt(0)\ns_barrier\nds_read_b128 v[48:51], v2 offset:32768\nv_mfma_f32_16x16x32_bf16 v[36:39], a[192:195], v[84:87], v[36:39]\ns_add_u32 m0, s57, 0\n" \
"v_mfma_f32_16x16x32_bf16 v[40:43], a[200:203], v[84:87], v[40:43]\ns_add_u32 s60, s41, 98304\nbuffer_load_dwordx4 v0, s[36:39], s60 offen offset:0 sc1 lds\nv_mfma_f32_16x16x32_bf16 v[44:47], v[72:75], v[84:87], v[44:47]\ns_waitcnt lgkmcnt(2)\nv_mfma_f32_16x16x32_bf16 v[16:19], a[188:191], v[88:91], v[16:19]\nds_read_b128 v[52:55], v2 offset:33024\nv_mfma_f32_16x16x32_bf16 v[20:23], a[196:199], v[88:91], v[20:23]\n" \
"v_mfma_f32_16x16x32_bf16 v[24:27], a[204:207], v[88:91], v[24:27]\nbuffer_load_dwordx4 v0, s[36:39], s60 offen offset:1024 sc1 lds\nv_mfma_f32_16x16x32_bf16 v[28:31], v[76:79], v[88:91], v[28:31]\ns_waitcnt lgkmcnt(2)\nv_mfma_f32_16x16x32_bf16 v[32:35], a[188:191], v[92:95], v[32:35]\nds_read_b128 v[56:59], v2 offset:34816\nv_mfma_f32_16x16x32_bf16 v[36:39], a[196:199], v[92:95], v[36:39]\nv_mfma_f32_16x16x32_bf16 v[40:43], a[204:207], v[92:95], v[40:43]\n" \
"buffer_load_dwordx4 v0, s[36:39], s60 offen offset:2048 sc1 lds\nv_mfma_f32_16x16x32_bf16 v[44:47], v[76:79], v[92:95], v[44:47]\ns_waitcnt lgkmcnt(2)\nv_mfma_f32_16x16x32_bf16 v[16:19], a[208:211], v[48:51], v[16:19]\nds_read_b128 v[60:63], v2 offset:35072\nv_mfma_f32_16x16x32_bf16 v[20:23], a[216:219], v[48:51], v[20:23]\nds_read_b128 v[72:75], v1 offset:12288\nv_mfma_f32_16x16x32_bf16 v[24:27], a[224:227], v[48:51], v[24:27]\n" \
"buffer_load_dwordx4 v0, s[36:39], s60 offen offset:3072 sc1 lds\nv_mfma_f32_16x16x32_bf16 v[28:31], a[232:235], v[48:51], v[28:31]\ns_waitcnt lgkmcnt(3)\nv_mfma_f32_16x16x32_bf16 v[32:35], a[208:211], v[52:55], v[32:35]\nds_read_b128 v[48:51], v2 offset:36864\nv_mfma_f32_16x16x32_bf16 v[36:39], a[216:219], v[52:55], v[36:39]\nv_mfma_f32_16x16x32_bf16 v[40:43], a[224:227], v[52:55], v[40:43]\nv_mfma_f32_16x16x32_bf16 v[44:47], a[232:235], v[52:55], v[44:47]\n" \
"s_waitcnt lgkmcnt(3)\nv_mfma_f32_16x16x32_bf16 v[16:19], a[212:215], v[56:59], v[16:19]\nds_read_b128 v[52:55], v2 offset:37120\nv_mfma_f32_16x16x32_bf16 v[20:23], a[220:223], v[56:59], v[20:23]\nds_read_b128 v[76:79], v1 offset:13312\nv_mfma_f32_16x16x32_bf16 v[24:27], a[228:231], v[56:59], v[24:27]\nv_mfma_f32_16x16x32_bf16 v[28:31], a[236:239], v[56:59], v[28:31]\ns_waitcnt lgkmcnt(4)\n" \
"v_mfma_f32_16x16x32_bf16 v[32:35], a[212:215], v[60:63], v[32:35]\nds_read_b128 v[56:59], v2 offset:38912\nv_mfma_f32_16x16x32_bf16 v[36:39], a[220:223], v[60:63], v[36:39]\nv_mfma_f32_16x16x32_bf16 v[40:43], a[228:231], v[60:63], v[40:43]\nv_mfma_f32_16x16x32_bf16 v[44:47], a[236:239], v[60:63], v[44:47]\ns_waitcnt lgkmcnt(3)\nv_mfma_f32_16x16x32_bf16 v[16:19], a[240:243], v[48:51], v[16:19]\nds_read_b128 v[60:63], v2 offset:39168\n" \
"v_mfma_f32_16x16x32_bf16 v[20:23], v[72:75], v[48:51], v[20:23]\nds_read_b128 v[64:67], v1 offset:14336\nv_mfma_f32_16x16x32_bf16 v[24:27], a[248:251], v[48:51], v[24:27]\nv_mfma_f32_16x16x32_bf16 v[28:31], v[96:99], v[48:51], v[28:31]\ns_waitcnt lgkmcnt(4)\nv_mfma_f32_16x16x32_bf16 v[32:35], a[240:243], v[52:55], v[32:35]\nds_read_b128 v[48:51], v2 offset:40960\nv_mfma_f32_16x16x32_bf16 v[36:39], v[72:75], v[52:55], v[36:39]\n" \
"v_mfma_f32_16x16x32_bf16 v[40:43], a[248:251], v[52:55], v[40:43]\nv_mfma_f32_16x16x32_bf16 v[44:47], v[96:99], v[52:55], v[44:47]\ns_waitcnt lgkmcnt(3)\nv_mfma_f32_16x16x32_bf16 v[16:19], a[244:247], v[56:59], v[16:19]\nds_read_b128 v[52:55], v2 offset:41216\nv_mfma_f32_16x16x32_bf16 v[20:23], v[76:79], v[56:59], v[20:23]\nds_read_b128 v[68:71], v1 offset:15360\nv_mfma_f32_16x16x32_bf16 v[24:27], a[252:255], v[56:59], v[24:27]\n" \
"v_mfma_f32_16x16x32_bf16 v[28:31], v[100:103], v[56:59], v[28:31]\ns_waitcnt lgkmcnt(4)\nv_mfma_f32_16x16x32_bf16 v[32:35], a[244:247], v[60:63], v[32:35]\nds_read_b128 v[56:59], v2 offset:43008\nv_mfma_f32_16x16x32_bf16 v[36:39], v[76:79], v[60:63], v[36:39]\nv_mfma_f32_16x16x32_bf16 v[40:43], a[252:255], v[60:63], v[40:43]\nv_mfma_f32_16x16x32_bf16 v[44:47], v[100:103], v[60:63], v[44:47]\ns_waitcnt lgkmcnt(3)\n" \
"v_mfma_f32_16x16x32_bf16 v[16:19], v[104:107], v[48:51], v[16:19]\nds_read_b128 v[60:63], v2 offset:43264\nv_mfma_f32_16x16x32_bf16 v[20:23], v[112:115], v[48:51], v[20:23]\nds_read_b128 v[72:75], v1 offset:16384\nv_mfma_f32_16x16x32_bf16 v[24:27], v[64:67], v[48:51], v[24:27]\nv_mfma_f32_16x16x32_bf16 v[28:31], v[120:123], v[48:51], v[28:31]\ns_waitcnt lgkmcnt(4)\nv_mfma_f32_16x16x32_bf16 v[32:35], v[104:107], v[52:55], v[32:35]\n" \
"ds_read_b128 v[48:51], v2 offset:45056\nv_mfma_f32_16x16x32_bf16 v[36:39], v[112:115], v[52:55], v[36:39]\nv_mfma_f32_16x16x32_bf16 v[40:43], v[64:67], v[52:55], v[40:43]\nv_mfma_f32_16x16x32_bf16 v[44:47], v[120:123], v[52:55], v[44:47]\ns_waitcnt lgkmcnt(3)\nv_mfma_f32_16x16x32_bf16 v[16:19], v[108:111], v[56:59], v[16:19]\nds_read_b128 v[52:55], v2 offset:45312\nv_mfma_f32_16x16x32_bf16 v[20:23], v[116:119], v[56:59], v[20:23]\n" \
"ds_read_b128 v[76:79], v1 offset:17408\nv_mfma_f32_16x16x32_bf16 v[24:27], v[68:71], v[56:59], v[24:27]\nv_mfma_f32_16x16x32_bf16 v[28:31], v[124:127], v[56:59], v[28:31]\ns_waitcnt lgkmcnt(4)\nv_mfma_f32_16x16x32_bf16 v[32:35], v[108:111], v[60:63], v[32:35]\nds_read_b128 v[56:59], v2 offset:47104\nv_mfma_f32_16x16x32_bf16 v[36:39], v[116:119], v[60:63], v[36:39]\nv_mfma_f32_16x16x32_bf16 v[40:43], v[68:71], v[60:63], v[40:43]\n" \
"v_mfma_f32_16x16x32_bf16 v[44:47], v[124:127], v[60:63], v[44:47]\ns_waitcnt lgkmcnt(3)\nv_mfma_f32_16x16x32_bf16 v[16:19], v[128:131], v[48:51], v[16:19]\nds_read_b128 v[60:63], v2 offset:47360\nv_mfma_f32_16x16x32_bf16 v[20:23], v[136:139], v[48:51], v[20:23]\nv_mfma_f32_16x16x32_bf16 v[24:27], v[144:147], v[48:51], v[24:27]\nv_mfma_f32_16x16x32_bf16 v[28:31], v[72:75], v[48:51], v[28:31]\ns_waitcnt lgkmcnt(3)\n" \
"v_mfma_f32_16x16x32_bf16 v[32:35], v[128:131], v[52:55], v[32:35]\ns_waitcnt lgkmcnt(0)\ns_waitcnt vmcnt(0)\ns_barrier\nds_read_b128 v[48:51], v2 offset:0\nv_mfma_f32_16x16x32_bf16 v[36:39], v[136:139], v[52:55], v[36:39]\nv_mfma_f32_16x16x32_bf16 v[40:43], v[144:147], v[52:55], v[40:43]\nv_mfma_f32_16x16x32_bf16 v[44:47], v[72:75], v[52:55], v[44:47]\n" \
"v_mfma_f32_16x16x32_bf16 v[16:19], v[132:135], v[56:59], v[16:19]\nds_read_b128 v[52:55], v2 offset:256\nv_mfma_f32_16x16x32_bf16 v[20:23], v[140:143], v[56:59], v[20:23]\nv_mfma_f32_16x16x32_bf16 v[24:27], v[148:151], v[56:59], v[24:27]\nv_mfma_f32_16x16x32_bf16 v[28:31], v[76:79], v[56:59], v[28:31]\nv_mfma_f32_16x16x32_bf16 v[32:35], v[132:135], v[60:63], v[32:35]\nds_read_b128 v[56:59], v2 offset:2048\nv_mfma_f32_16x16x32_bf16 v[36:39], v[140:143], v[60:63], v[36:39]\n" \
"v_mfma_f32_16x16x32_bf16 v[40:43], v[148:151], v[60:63], v[40:43]\nv_mfma_f32_16x16x32_bf16 v[44:47], v[76:79], v[60:63], v[44:47]\ns_waitcnt lgkmcnt(2)\nv_mfma_f32_16x16x32_bf16 v[16:19], v[152:155], v[48:51], v[16:19]\nds_read_b128 v[60:63], v2 offset:2304\nv_mfma_f32_16x16x32_bf16 v[20:23], v[160:163], v[48:51], v[20:23]\nds_read_b128 v[72:75], v1 offset:18432\nv_mfma_f32_16x16x32_bf16 v[24:27], v[168:171], v[48:51], v[24:27]\n" \
"v_mfma_f32_16x16x32_bf16 v[28:31], v[176:179], v[48:51], v[28:31]\ns_waitcnt lgkmcnt(3)\nv_mfma_f32_16x16x32_bf16 v[32:35], v[152:155], v[52:55], v[32:35]\nds_read_b128 v[48:51], v2 offset:4096\nv_mfma_f32_16x16x32_bf16 v[36:39], v[160:163], v[52:55], v[36:39]\nv_mfma_f32_16x16x32_bf16 v[40:43], v[168:171], v[52:55], v[40:43]\nv_mfma_f32_16x16x32_bf16 v[44:47], v[176:179], v[52:55], v[44:47]\ns_waitcnt lgkmcnt(3)\n" \
"v_mfma_f32_16x16x32_bf16 v[16:19], v[156:159], v[56:59], v[16:19]\nds_read_b128 v[52:55], v2 offset:4352\nv_mfma_f32_16x16x32_bf16 v[20:23], v[164:167], v[56:59], v[20:23]\nds_read_b128 v[76:79], v1 offset:19456\nv_mfma_f32_16x16x32_bf16 v[24:27], v[172:175], v[56:59], v[24:27]\nv_mfma_f32_16x16x32_bf16 v[28:31], v[180:183], v[56:59], v[28:31]\ns_waitcnt lgkmcnt(4)\nv_mfma_f32_16x16x32_bf16 v[32:35], v[156:159], v[60:63], v[32:35]\n" \
"ds_read_b128 v[56:59], v2 offset:6144\nv_mfma_f32_16x16x32_bf16 v[36:39], v[164:167], v[60:63], v[36:39]\nv_mfma_f32_16x16x32_bf16 v[40:43], v[172:175], v[60:63], v[40:43]\nv_mfma_f32_16x16x32_bf16 v[44:47], v[180:183], v[60:63], v[44:47]\ns_waitcnt lgkmcnt(3)\nv_mfma_f32_16x16x32_bf16 v[16:19], v[184:187], v[48:51], v[16:19]\nds_read_b128 v[60:63], v2 offset:6400\nv_mfma_f32_16x16x32_bf16 v[20:23], v[72:75], v[48:51], v[20:23]\n" \
"ds_read_b128 v[64:67], v1 offset:20480\nv_mfma_f32_16x16x32_bf16 v[24:27], v[192:195], v[48:51], v[24:27]\nv_mfma_f32_16x16x32_bf16 v[28:31], v[200:203], v[48:51], v[28:31]\ns_waitcnt lgkmcnt(4)\nv_mfma_f32_16x16x32_bf16 v[32:35], v[184:187], v[52:55], v[32:35]\nds_read_b128 v[48:51], v2 offset:8192\nv_mfma_f32_16x16x32_bf16 v[36:39], v[72:75], v[52:55], v[36:39]\nv_mfma_f32_16x16x32_bf16 v[40:43], v[192:195], v[52:55], v[40:43]\n" \
"v_mfma_f32_16x16x32_bf16 v[44:47], v[200:203], v[52:55], v[44:47]\ns_waitcnt lgkmcnt(3)\nv_mfma_f32_16x16x32_bf16 v[16:19], v[188:191], v[56:59], v[16:19]\nds_read_b128 v[52:55], v2 offset:10240\nv_mfma_f32_16x16x32_bf16 v[20:23], v[76:79], v[56:59], v[20:23]\nds_read_b128 v[68:71], v1 offset:21504\nv_mfma_f32_16x16x32_bf16 v[24:27], v[196:199], v[56:59], v[24:27]\nbuffer_load_dwordx4 v[80:83], v3, s[36:39], s42 offen offset:0 sc1\n" \
"v_mfma_f32_16x16x32_bf16 v[28:31], v[204:207], v[56:59], v[28:31]\ns_waitcnt lgkmcnt(4)\nv_mfma_f32_16x16x32_bf16 v[32:35], v[188:191], v[60:63], v[32:35]\nds_read_b128 v[56:59], v2 offset:12288\nv_mfma_f32_16x16x32_bf16 v[36:39], v[76:79], v[60:63], v[36:39]\nds_read_b128 v[72:75], v1 offset:22528\nv_mfma_f32_16x16x32_bf16 v[40:43], v[196:199], v[60:63], v[40:43]\nbuffer_load_dwordx4 v[84:87], v3, s[36:39], s42 offen offset:1024 sc1\n" \
"v_mfma_f32_16x16x32_bf16 v[44:47], v[204:207], v[60:63], v[44:47]\ns_waitcnt lgkmcnt(4)\nv_mfma_f32_16x16x32_bf16 v[16:19], v[208:211], v[48:51], v[16:19]\nds_read_b128 v[60:63], v2 offset:14336\nv_mfma_f32_16x16x32_bf16 v[20:23], v[216:219], v[48:51], v[20:23]\nds_read_b128 v[76:79], v1 offset:23552\nv_mfma_f32_16x16x32_bf16 v[24:27], v[64:67], v[48:51], v[24:27]\nbuffer_load_dwordx4 v[88:91], v3, s[36:39], s42 offen offset:2048 sc1\n" \
"v_mfma_f32_16x16x32_bf16 v[28:31], v[224:227], v[48:51], v[28:31]\ns_waitcnt lgkmcnt(5)\nv_mfma_f32_16x16x32_bf16 v[16:19], v[212:215], v[52:55], v[16:19]\nds_read_b128 v[48:51], v2 offset:8448\nv_mfma_f32_16x16x32_bf16 v[20:23], v[220:223], v[52:55], v[20:23]\ns_waitcnt lgkmcnt(5)\nv_mfma_f32_16x16x32_bf16 v[24:27], v[68:71], v[52:55], v[24:27]\nbuffer_load_dwordx4 v[92:95], v3, s[36:39], s42 offen offset:3072 sc1\n" \
"v_mfma_f32_16x16x32_bf16 v[28:31], v[228:231], v[52:55], v[28:31]\ns_waitcnt lgkmcnt(4)\nv_mfma_f32_16x16x32_bf16 v[16:19], v[232:235], v[56:59], v[16:19]\nds_read_b128 v[52:55], v2 offset:10496\nv_mfma_f32_16x16x32_bf16 v[20:23], v[240:243], v[56:59], v[20:23]\nv_mfma_f32_16x16x32_bf16 v[24:27], v[248:251], v[56:59], v[24:27]\ns_waitcnt lgkmcnt(4)\nv_mfma_f32_16x16x32_bf16 v[28:31], v[72:75], v[56:59], v[28:31]\n" \
"s_waitcnt lgkmcnt(3)\nv_mfma_f32_16x16x32_bf16 v[16:19], v[236:239], v[60:63], v[16:19]\nds_read_b128 v[56:59], v2 offset:12544\nv_mfma_f32_16x16x32_bf16 v[20:23], v[244:247], v[60:63], v[20:23]\nv_mfma_f32_16x16x32_bf16 v[24:27], v[252:255], v[60:63], v[24:27]\ns_waitcnt lgkmcnt(3)\nv_mfma_f32_16x16x32_bf16 v[28:31], v[76:79], v[60:63], v[28:31]\ns_waitcnt lgkmcnt(2)\n" \
"v_mfma_f32_16x16x32_bf16 v[32:35], v[208:211], v[48:51], v[32:35]\nds_read_b128 v[60:63], v2 offset:14592\nv_mfma_f32_16x16x32_bf16 v[36:39], v[216:219], v[48:51], v[36:39]\nv_cvt_pk_bf16_f32 v20, v16, v20\nv_cvt_pk_bf16_f32 v21, v17, v21\nv_mfma_f32_16x16x32_bf16 v[40:43], v[64:67], v[48:51], v[40:43]\nv_cvt_pk_bf16_f32 v22, v18, v22\nv_cvt_pk_bf16_f32 v23, v19, v23\n" \
"v_mfma_f32_16x16x32_bf16 v[44:47], v[224:227], v[48:51], v[44:47]\ns_waitcnt lgkmcnt(2)\nds_write_b128 v5, v[20:23] offset:0\nv_mfma_f32_16x16x32_bf16 v[32:35], v[212:215], v[52:55], v[32:35]\nv_cvt_pk_bf16_f32 v28, v24, v28\nv_cvt_pk_bf16_f32 v29, v25, v29\nv_mfma_f32_16x16x32_bf16 v[36:39], v[220:223], v[52:55], v[36:39]\nv_cvt_pk_bf16_f32 v30, v26, v30\n" \
"v_cvt_pk_bf16_f32 v31, v27, v31\nv_mfma_f32_16x16x32_bf16 v[40:43], v[68:71], v[52:55], v[40:43]\nv_mov_b64 v[48:49], 0\nv_mov_b64 v[50:51], 0\nv_mfma_f32_16x16x32_bf16 v[44:47], v[228:231], v[52:55], v[44:47]\ns_cmp_eq_u32 %[w], 1\nds_write_b128 v5, v[28:31] offset:512\ns_cbranch_scc1 .Lqk_c1%=\n" \
"s_cmp_eq_u32 %[w], 2\ns_cbranch_scc1 .Lqk_c2%=\ns_cmp_eq_u32 %[w], 3\ns_cbranch_scc1 .Lqk_c3%=\n.Lqk_c0%=:\ns_waitcnt lgkmcnt(3)\nv_mfma_f32_16x16x32_bf16 v[32:35], v[232:235], v[56:59], v[32:35]\nv_mov_b64 v[52:53], 0\n" \
"v_mov_b64 v[54:55], 0\nv_mfma_f32_16x16x32_bf16 v[36:39], v[240:243], v[56:59], v[36:39]\ns_waitcnt lgkmcnt(2)\nds_read_b128 v[64:67], v1 offset:0\nv_mfma_f32_16x16x32_bf16 v[32:35], v[236:239], v[60:63], v[32:35]\nds_read_b128 v[68:71], v1 offset:1024\nv_mfma_f32_16x16x32_bf16 v[36:39], v[244:247], v[60:63], v[36:39]\ns_bfe_i32 s54, s40, 0x10000\n" \
"v_mfma_f32_16x16x32_bf16 v[40:43], v[248:251], v[56:59], v[40:43]\nv_mfma_f32_16x16x32_bf16 v[44:47], v[72:75], v[56:59], v[44:47]\nv_cvt_pk_bf16_f32 v36, v32, v36\nv_cvt_pk_bf16_f32 v37, v33, v37\nv_mfma_f32_16x16x32_bf16 v[40:43], v[252:255], v[60:63], v[40:43]\nv_cvt_pk_bf16_f32 v38, v34, v38\nv_cvt_pk_bf16_f32 v39, v35, v39\nv_mfma_f32_16x16x32_bf16 v[44:47], v[76:79], v[60:63], v[44:47]\n" \
"ds_write_b128 v5, v[36:39] offset:256\nv_mov_b64 v[56:57], 0\nv_mov_b64 v[58:59], 0\nv_mov_b64 v[60:61], 0\nv_mov_b64 v[62:63], 0\n.Lqk_poll0%=:\ns_waitcnt vmcnt(0)\nv_smfmac_f32_16x16x64_bf16 v[48:51], v[80:83], a[0:7], v4\n" \
"ds_write_b128 v0, v[80:83] offset:16384\nv_max3_u32 v16, v80, v82, v84\nv_smfmac_f32_16x16x64_bf16 v[52:55], v[80:83], a[8:15], v4\nds_read_b128 v[72:75], v1 offset:2048\nds_read_b128 v[76:79], v1 offset:3072\nv_max3_u32 v16, v16, v86, v88\nv_smfmac_f32_16x16x64_bf16 v[56:59], v[80:83], a[16:23], v4\nds_write_b128 v0, v[84:87] offset:17408\n" \
"v_max3_u32 v16, v16, v90, v92\nv_max_u32 v16, v16, v94\nv_smfmac_f32_16x16x64_bf16 v[60:63], v[80:83], a[24:31], v4\nv_cmp_eq_u32 vcc, -1, v16\nv_smfmac_f32_16x16x64_bf16 v[48:51], v[84:87], a[32:39], v4\nds_write_b128 v0, v[88:91] offset:18432\ns_cbranch_vccnz .Lqk_re0%=\ns_waitcnt lgkmcnt(6)\n" \
"v_smfmac_f32_16x16x64_bf16 v[52:55], v[84:87], v[64:71], v4\nds_read_b128 v[64:67], v1 offset:4096\nds_read_b128 v[68:71], v1 offset:5120\nv_smfmac_f32_16x16x64_bf16 v[56:59], v[84:87], a[40:47], v4\nds_write_b128 v0, v[92:95] offset:19456\nv_smfmac_f32_16x16x64_bf16 v[60:63], v[84:87], a[48:55], v4\nv_smfmac_f32_16x16x64_bf16 v[48:51], v[88:91], a[56:63], v4\nv_smfmac_f32_16x16x64_bf16 v[52:55], v[88:91], a[64:71], v4\n" \
"s_waitcnt lgkmcnt(0)\ns_barrier\ns_memrealtime s[68:69]\nds_read_b128 v[80:83], v0 offset:20480\nds_read_b128 v[84:87], v0 offset:21504\nv_mov_b32 v24, s40\nv_smfmac_f32_16x16x64_bf16 v[56:59], v[88:91], v[72:79], v4\nds_read_b128 v[72:75], v1 offset:6144\n" \
"ds_read_b128 v[76:79], v1 offset:7168\nv_cvt_pk_bf16_f32 v44, v40, v44\nv_smfmac_f32_16x16x64_bf16 v[60:63], v[88:91], a[72:79], v4\nds_read_b128 v[88:91], v0 offset:22528\nv_cvt_pk_bf16_f32 v45, v41, v45\nv_smfmac_f32_16x16x64_bf16 v[48:51], v[92:95], a[80:87], v4\nv_cvt_pk_bf16_f32 v46, v42, v46\nv_smfmac_f32_16x16x64_bf16 v[52:55], v[92:95], a[88:95], v4\n" \
"v_cvt_pk_bf16_f32 v47, v43, v47\nv_smfmac_f32_16x16x64_bf16 v[56:59], v[92:95], a[96:103], v4\nv_smfmac_f32_16x16x64_bf16 v[60:63], v[92:95], v[64:71], v4\ns_waitcnt lgkmcnt(4)\nds_write_b128 v5, v[44:47] offset:768\nds_read_b128 v[92:95], v0 offset:23552\nds_read_b128 v[64:67], v1 offset:8192\nds_read_b128 v[68:71], v1 offset:9216\n" \
"v_smfmac_f32_16x16x64_bf16 v[48:51], v[80:83], a[104:111], v4\ns_sub_u32 s88, s86, s89\ns_min_u32 s88, s88, 150\nv_smfmac_f32_16x16x64_bf16 v[52:55], v[80:83], a[112:119], v4\ns_add_u32 s88, s88, s68\ns_mov_b32 s89, s68\nv_mov_b32 v25, s88\nv_smfmac_f32_16x16x64_bf16 v[56:59], v[80:83], a[120:127], v4\n" \
"buffer_store_dwordx2 v[24:25], off, s[36:39], %[sg] sc1\nv_smfmac_f32_16x16x64_bf16 v[60:63], v[80:83], a[128:135], v4\ns_waitcnt lgkmcnt(7)\nds_read_b128 v[80:83], v0 offset:24576\nv_smfmac_f32_16x16x64_bf16 v[48:51], v[84:87], a[136:143], v4\ns_waitcnt lgkmcnt(6)\nv_smfmac_f32_16x16x64_bf16 v[52:55], v[84:87], v[72:79], v4\nds_read_b128 v[72:75], v1 offset:10240\n" \
"ds_read_b128 v[76:79], v1 offset:11264\nv_smfmac_f32_16x16x64_bf16 v[56:59], v[84:87], a[144:151], v4\nv_smfmac_f32_16x16x64_bf16 v[60:63], v[84:87], a[152:159], v4\ns_waitcnt lgkmcnt(7)\nds_read_b128 v[84:87], v0 offset:25600\nv_smfmac_f32_16x16x64_bf16 v[48:51], v[88:91], a[160:167], v4\ns_waitcnt lgkmcnt(7)\ns_barrier\n" \
"v_smfmac_f32_16x16x64_bf16 v[52:55], v[88:91], a[168:175], v4\ns_waitcnt lgkmcnt(4)\nv_smfmac_f32_16x16x64_bf16 v[56:59], v[88:91], v[64:71], v4\nds_read_b128 v[64:67], v1 offset:12288\nds_read_b128 v[68:71], v1 offset:13312\nv_smfmac_f32_16x16x64_bf16 v[60:63], v[88:91], a[176:183], v4\nds_read_b128 v[88:91], v0 offset:26624\nv_smfmac_f32_16x16x64_bf16 v[48:51], v[92:95], a[184:191], v4\n" \
"ds_read_b128 v[16:19], v7 offset:0\nv_smfmac_f32_16x16x64_bf16 v[52:55], v[92:95], a[192:199], v4\nv_smfmac_f32_16x16x64_bf16 v[56:59], v[92:95], a[200:207], v4\ns_waitcnt lgkmcnt(5)\nds_read_b128 v[20:23], v7 offset:64\nv_smfmac_f32_16x16x64_bf16 v[60:63], v[92:95], v[72:79], v4\nds_read_b128 v[92:95], v0 offset:27648\nds_read_b128 v[72:75], v1 offset:14336\n" \
"ds_read_b128 v[76:79], v1 offset:15360\nv_smfmac_f32_16x16x64_bf16 v[48:51], v[80:83], a[208:215], v4\nds_read_b128 v[24:27], v7 offset:2048\nv_smfmac_f32_16x16x64_bf16 v[52:55], v[80:83], a[216:223], v4\nv_smfmac_f32_16x16x64_bf16 v[56:59], v[80:83], a[224:231], v4\nds_read_b128 v[28:31], v7 offset:2112\nv_smfmac_f32_16x16x64_bf16 v[60:63], v[80:83], a[232:239], v4\ns_waitcnt lgkmcnt(10)\n" \
"ds_read_b128 v[80:83], v0 offset:28672\nv_smfmac_f32_16x16x64_bf16 v[48:51], v[84:87], a[240:247], v4\ns_waitcnt lgkmcnt(9)\nds_read_b128 v[32:35], v7 offset:4096\nv_smfmac_f32_16x16x64_bf16 v[52:55], v[84:87], v[64:71], v4\nds_read_b128 v[64:67], v1 offset:16384\nds_read_b128 v[68:71], v1 offset:17408\nv_smfmac_f32_16x16x64_bf16 v[56:59], v[84:87], a[248:255], v4\n" \
"ds_read_b128 v[36:39], v7 offset:4160\nv_smfmac_f32_16x16x64_bf16 v[60:63], v[84:87], v[96:103], v4\ns_waitcnt lgkmcnt(12)\nds_read_b128 v[84:87], v0 offset:29696\nv_smfmac_f32_16x16x64_bf16 v[48:51], v[88:91], v[104:111], v4\nds_read_b128 v[40:43], v7 offset:6144\nv_smfmac_f32_16x16x64_bf16 v[52:55], v[88:91], v[112:119], v4\ns_waitcnt lgkmcnt(9)\n" \
"v_smfmac_f32_16x16x64_bf16 v[56:59], v[88:91], v[72:79], v4\nds_read_b128 v[72:75], v1 offset:18432\nds_read_b128 v[76:79], v1 offset:19456\nv_smfmac_f32_16x16x64_bf16 v[60:63], v[88:91], v[120:127], v4\nds_read_b128 v[88:91], v0 offset:30720\nv_smfmac_f32_16x16x64_bf16 v[48:51], v[92:95], v[128:135], v4\nds_read_b128 v[44:47], v7 offset:6208\nv_smfmac_f32_16x16x64_bf16 v[52:55], v[92:95], v[136:143], v4\n" \
"v_smfmac_f32_16x16x64_bf16 v[56:59], v[92:95], v[144:151], v4\ns_waitcnt lgkmcnt(7)\nv_smfmac_f32_16x16x64_bf16 v[60:63], v[92:95], v[64:71], v4\nds_read_b128 v[92:95], v0 offset:31744\nds_read_b128 v[64:67], v1 offset:20480\nds_read_b128 v[68:71], v1 offset:21504\nv_smfmac_f32_16x16x64_bf16 v[48:51], v[80:83], v[152:159], v4\nv_smfmac_f32_16x16x64_bf16 v[52:55], v[80:83], v[160:167], v4\n" \
"v_smfmac_f32_16x16x64_bf16 v[56:59], v[80:83], v[168:175], v4\nv_smfmac_f32_16x16x64_bf16 v[60:63], v[80:83], v[176:183], v4\ns_waitcnt lgkmcnt(8)\nv_smfmac_f32_16x16x64_bf16 v[48:51], v[84:87], v[184:191], v4\ns_waitcnt lgkmcnt(5)\nv_smfmac_f32_16x16x64_bf16 v[52:55], v[84:87], v[72:79], v4\nds_read_b128 v[72:75], v1 offset:22528\nds_read_b128 v[76:79], v1 offset:23552\n" \
"v_smfmac_f32_16x16x64_bf16 v[56:59], v[84:87], v[192:199], v4\nv_smfmac_f32_16x16x64_bf16 v[60:63], v[84:87], v[200:207], v4\ns_waitcnt lgkmcnt(6)\nv_smfmac_f32_16x16x64_bf16 v[48:51], v[88:91], v[208:215], v4\nv_smfmac_f32_16x16x64_bf16 v[52:55], v[88:91], v[216:223], v4\ns_waitcnt lgkmcnt(4)\nv_smfmac_f32_16x16x64_bf16 v[48:51], v[92:95], v[232:239], v4\nv_smfmac_f32_16x16x64_bf16 v[52:55], v[92:95], v[240:247], v4\n" \
"s_waitcnt lgkmcnt(2)\nv_smfmac_f32_16x16x64_bf16 v[56:59], v[88:91], v[64:71], v4\nv_add_f32 v48, v48, v50\nv_smfmac_f32_16x16x64_bf16 v[60:63], v[88:91], v[224:231], v4\nv_add_f32 v52, v52, v54\nv_add_f32 v49, v49, v51\nv_smfmac_f32_16x16x64_bf16 v[56:59], v[92:95], v[248:255], v4\ns_waitcnt lgkmcnt(0)\n" \
"v_add_f32 v53, v53, v55\nv_cvt_pk_bf16_f32 v50, v48, v52\nv_smfmac_f32_16x16x64_bf16 v[60:63], v[92:95], v[72:79], v4\nv_cvt_pk_bf16_f32 v51, v49, v53\nds_write2_b32 v6, v50, v51 offset0:0 offset1:4\ns_nop 1\nv_add_f32 v56, v56, v58\nv_add_f32 v57, v57, v59\n" \
"s_nop 1\nv_add_f32 v60, v60, v62\nv_add_f32 v61, v61, v63\nv_cvt_pk_bf16_f32 v62, v56, v60\nv_cvt_pk_bf16_f32 v63, v57, v61\nds_write2_b32 v6, v62, v63 offset0:36 offset1:40\nv_mov_b32 v61, s54\ns_waitcnt lgkmcnt(0)\n" \
"s_barrier\nds_read_b128 v[80:83], v8 offset:0\nds_read_b128 v[84:87], v8 offset:576\nds_read_b128 v[88:91], v8 offset:1152\nds_read_b128 v[92:95], v8 offset:1728\nds_read_b128 v[64:67], v9 offset:0\nds_read_b128 v[68:71], v9 offset:576\nds_read_b128 v[72:75], v9 offset:1152\n" \
"ds_read_b128 v[76:79], v9 offset:1728\nds_read_b32 v60, v0 offset:53760\ns_waitcnt lgkmcnt(8)\nv_mfma_f32_16x16x32_bf16 v[52:55], v[16:19], v[80:83], 0\nv_mfma_f32_16x16x32_bf16 v[56:59], v[20:23], v[80:83], 0\ns_waitcnt lgkmcnt(7)\nv_mfma_f32_16x16x32_bf16 v[52:55], v[24:27], v[84:87], v[52:55]\nv_mfma_f32_16x16x32_bf16 v[56:59], v[28:31], v[84:87], v[56:59]\n" \
"s_waitcnt lgkmcnt(6)\nv_mfma_f32_16x16x32_bf16 v[52:55], v[32:35], v[88:91], v[52:55]\nv_mfma_f32_16x16x32_bf16 v[56:59], v[36:39], v[88:91], v[56:59]\ns_waitcnt lgkmcnt(5)\nv_mfma_f32_16x16x32_bf16 v[52:55], v[40:43], v[92:95], v[52:55]\nv_mfma_f32_16x16x32_bf16 v[56:59], v[44:47], v[92:95], v[56:59]\ns_waitcnt lgkmcnt(4)\nv_mfma_f32_16x16x32_bf16 v[48:51], v[64:67], v[80:83], 0\n" \
"s_waitcnt lgkmcnt(3)\nv_mfma_f32_16x16x32_bf16 v[48:51], v[68:71], v[84:87], v[48:51]\ns_waitcnt lgkmcnt(2)\nds_read_b128 v[80:83], v2 offset:0\nv_mfma_f32_16x16x32_bf16 v[48:51], v[72:75], v[88:91], v[48:51]\ns_waitcnt lgkmcnt(2)\nds_read_b128 v[84:87], v2 offset:256\nv_mfma_f32_16x16x32_bf16 v[48:51], v[76:79], v[92:95], v[48:51]\n" \
"ds_read_b128 v[88:91], v2 offset:2048\nv_max3_f32 v62, v52, v53, v54\nv_max3_f32 v63, v55, v56, v57\nv_max3_f32 v62, v62, v58, v59\nv_cndmask_b32 v52, v52, v56, s[62:63]\nv_cndmask_b32 v53, v53, v57, s[62:63]\nv_cndmask_b32 v54, v54, v58, s[62:63]\nv_cndmask_b32 v55, v55, v59, s[62:63]\n" \
"v_max_f32 v62, v62, v63\nv_max3_f32 v63, v62, v48, v49\nv_max3_f32 v62, v62, v48, v49\ns_waitcnt lgkmcnt(2)\nv_mfma_f32_16x16x32_bf16 v[16:19], a[0:3], v[80:83], 0\nds_read_b128 v[92:95], v2 offset:2304\nv_permlane16_swap_b32 v62, v63\nv_mfma_f32_16x16x32_bf16 v[20:23], a[8:11], v[80:83], 0\n" \
"ds_read_b128 v[72:75], v1 offset:0\nv_max_f32 v50, v62, v63\nv_max_f32 v62, v62, v63\nv_mfma_f32_16x16x32_bf16 v[24:27], a[16:19], v[80:83], 0\ns_nop 0\nv_permlane32_swap_b32 v62, v50\nv_max_f32 v62, v62, v50\nv_mfma_f32_16x16x32_bf16 v[28:31], a[24:27], v[80:83], 0\n" \
"s_waitcnt lgkmcnt(3)\nv_mul_f32 v62, s59, v62\nv_fma_f32 v52, v52, s58, v62\nv_fma_f32 v53, v53, s58, v62\nv_mfma_f32_16x16x32_bf16 v[32:35], a[0:3], v[84:87], 0\nds_read_b128 v[80:83], v2 offset:4096\nv_exp_f32 v52, v52\nv_mfma_f32_16x16x32_bf16 v[36:39], a[8:11], v[84:87], 0\n" \
"v_fma_f32 v54, v54, s58, v62\nv_exp_f32 v53, v53\nv_mfma_f32_16x16x32_bf16 v[40:43], a[16:19], v[84:87], 0\nv_fma_f32 v55, v55, s58, v62\nv_exp_f32 v54, v54\nv_mfma_f32_16x16x32_bf16 v[44:47], a[24:27], v[84:87], 0\ns_waitcnt lgkmcnt(3)\nv_add_f32 v63, v52, v53\n" \
"v_fma_f32 v48, v48, s58, v62\nv_add_f32 v63, v63, v54\nv_mfma_f32_16x16x32_bf16 v[16:19], a[4:7], v[88:91], v[16:19]\nds_read_b128 v[84:87], v2 offset:4352\nv_exp_f32 v55, v55\nv_mfma_f32_16x16x32_bf16 v[20:23], a[12:15], v[88:91], v[20:23]\nds_read_b128 v[76:79], v1 offset:1024\nv_fma_f32 v49, v49, s58, v62\n" \
"v_add_f32 v63, v63, v55\nv_mfma_f32_16x16x32_bf16 v[24:27], a[20:23], v[88:91], v[24:27]\nv_exp_f32 v48, v48\nv_mfma_f32_16x16x32_bf16 v[28:31], a[28:31], v[88:91], v[28:31]\ns_waitcnt lgkmcnt(4)\nv_exp_f32 v49, v49\nv_add_f32 v50, v48, v63\nv_mfma_f32_16x16x32_bf16 v[32:35], a[4:7], v[92:95], v[32:35]\n" \
"ds_read_b128 v[88:91], v2 offset:6144\nv_add_f32 v50, v50, v49\nv_mfma_f32_16x16x32_bf16 v[36:39], a[12:15], v[92:95], v[36:39]\ns_nop 0\nv_add_f32_dpp v62, v63, v50 row_ror:8 row_mask:0xf bank_mask:0xf\nv_add_f32_dpp v50, v63, v50 row_ror:8 row_mask:0xf bank_mask:0xf\nv_mfma_f32_16x16x32_bf16 v[40:43], a[20:23], v[92:95], v[40:43]\ns_nop 0\n" \
"v_permlane16_swap_b32 v62, v50\nv_add_f32 v63, v62, v50\nv_mfma_f32_16x16x32_bf16 v[44:47], a[28:31], v[92:95], v[44:47]\ns_waitcnt lgkmcnt(3)\nv_add_f32 v50, v62, v50\nv_mfma_f32_16x16x32_bf16 v[16:19], a[32:35], v[80:83], v[16:19]\nds_read_b128 v[92:95], v2 offset:6400\nv_permlane32_swap_b32 v63, v50\n" \
"v_mfma_f32_16x16x32_bf16 v[20:23], v[72:75], v[80:83], v[20:23]\nds_read_b128 v[64:67], v1 offset:2048\nv_add_f32 v63, v63, v50\nv_mfma_f32_16x16x32_bf16 v[24:27], a[40:43], v[80:83], v[24:27]\nv_rcp_f32 v63, v63\nv_mfma_f32_16x16x32_bf16 v[28:31], a[48:51], v[80:83], v[28:31]\ns_waitcnt lgkmcnt(4)\nv_mul_f32 v52, v52, v63\n" \
"v_mul_f32 v53, v53, v63\nv_mul_f32 v54, v54, v63\nv_mfma_f32_16x16x32_bf16 v[32:35], a[32:35], v[84:87], v[32:35]\nds_read_b128 v[80:83], v2 offset:8192\nv_mul_f32 v55, v55, v63\nv_mul_f32 v48, v48, v63\nv_mfma_f32_16x16x32_bf16 v[36:39], v[72:75], v[84:87], v[36:39]\nv_mul_f32 v49, v49, v63\n" \
"v_cvt_pk_bf16_f32 v52, v52, v53\nv_cvt_pk_bf16_f32 v53, v54, v55\nv_mfma_f32_16x16x32_bf16 v[40:43], a[40:43], v[84:87], v[40:43]\nv_cvt_pk_bf16_f32 v48, v48, v49\nv_bfi_b32 v52, s61, v52, v61\nv_bfi_b32 v53, s61, v53, v61\nv_mfma_f32_16x16x32_bf16 v[44:47], a[48:51], v[84:87], v[44:47]\ns_waitcnt lgkmcnt(3)\n" \
"v_bfi_b32 v48, s61, v48, v61\nv_mfma_f32_16x16x32_bf16 v[16:19], a[36:39], v[88:91], v[16:19]\nds_read_b128 v[84:87], v2 offset:8448\nv_mfma_f32_16x16x32_bf16 v[20:23], v[76:79], v[88:91], v[20:23]\nds_read_b128 v[68:71], v1 offset:3072\nv_mfma_f32_16x16x32_bf16 v[24:27], a[44:47], v[88:91], v[24:27]\nbuffer_store_dwordx2 v[52:53], v10, s[36:39], 0 offen sc1\nv_mfma_f32_16x16x32_bf16 v[28:31], a[52:55], v[88:91], v[28:31]\n" \
"s_memrealtime s[86:87]\ns_waitcnt lgkmcnt(4)\nbuffer_store_dword v48, v11, s[36:39], 0 offen sc1\nv_cmp_ne_u32 vcc, 0, v60\nv_mfma_f32_16x16x32_bf16 v[32:35], a[36:39], v[92:95], v[32:35]\nds_read_b128 v[88:91], v2 offset:10240\nv_mfma_f32_16x16x32_bf16 v[36:39], v[76:79], v[92:95], v[36:39]\nv_mfma_f32_16x16x32_bf16 v[40:43], a[44:47], v[92:95], v[40:43]\n" \
"v_mfma_f32_16x16x32_bf16 v[44:47], a[52:55], v[92:95], v[44:47]\ns_waitcnt lgkmcnt(3)\nv_mfma_f32_16x16x32_bf16 v[16:19], a[56:59], v[80:83], v[16:19]\nds_read_b128 v[92:95], v2 offset:10496\nv_mfma_f32_16x16x32_bf16 v[20:23], a[64:67], v[80:83], v[20:23]\nds_read_b128 v[72:75], v1 offset:4096\nv_mfma_f32_16x16x32_bf16 v[24:27], v[64:67], v[80:83], v[24:27]\nv_mfma_f32_16x16x32_bf16 v[28:31], a[72:75], v[80:83], v[28:31]\n" \
"s_waitcnt lgkmcnt(4)\nv_mfma_f32_16x16x32_bf16 v[32:35], a[56:59], v[84:87], v[32:35]\nds_read_b128 v[80:83], v2 offset:12288\nv_mfma_f32_16x16x32_bf16 v[36:39], a[64:67], v[84:87], v[36:39]\nv_mfma_f32_16x16x32_bf16 v[40:43], v[64:67], v[84:87], v[40:43]\nv_mfma_f32_16x16x32_bf16 v[44:47], a[72:75], v[84:87], v[44:47]\ns_waitcnt lgkmcnt(3)\nv_mfma_f32_16x16x32_bf16 v[16:19], a[60:63], v[88:91], v[16:19]\n" \
"ds_read_b128 v[84:87], v2 offset:12544\nv_mfma_f32_16x16x32_bf16 v[20:23], a[68:71], v[88:91], v[20:23]\nds_read_b128 v[76:79], v1 offset:5120\nv_mfma_f32_16x16x32_bf16 v[24:27], v[68:71], v[88:91], v[24:27]\nv_mfma_f32_16x16x32_bf16 v[28:31], a[76:79], v[88:91], v[28:31]\ns_waitcnt lgkmcnt(4)\nv_mfma_f32_16x16x32_bf16 v[32:35], a[60:63], v[92:95], v[32:35]\nds_read_b128 v[88:91], v2 offset:14336\n" \
"v_mfma_f32_16x16x32_bf16 v[36:39], a[68:71], v[92:95], v[36:39]\nv_mfma_f32_16x16x32_bf16 v[40:43], v[68:71], v[92:95], v[40:43]\nv_mfma_f32_16x16x32_bf16 v[44:47], a[76:79], v[92:95], v[44:47]\ns_cbranch_vccnz .Lqk_pj%=\ns_cmp_lt_i32 s40, 63\ns_waitcnt lgkmcnt(0)\ns_cbranch_scc1 .Lqk_pwait%=\ns_branch .Lqk_end%=\n" \
".Lqk_re0%=:\nv_max_u32 v17, v80, v82\nv_cmp_eq_u32 vcc, -1, v17\ns_cbranch_vccz .Lqk_sk1614%=\ns_mov_b64 exec, vcc\nbuffer_load_dwordx4 v[80:83], v3, s[36:39], s42 offen offset:0 sc1\ns_mov_b64 exec, -1\n.Lqk_sk1614%=:\n" \
"v_max_u32 v17, v84, v86\nv_cmp_eq_u32 vcc, -1, v17\ns_cbranch_vccz .Lqk_sk1621%=\ns_mov_b64 exec, vcc\nbuffer_load_dwordx4 v[84:87], v3, s[36:39], s42 offen offset:1024 sc1\ns_mov_b64 exec, -1\n.Lqk_sk1621%=:\nv_max_u32 v17, v88, v90\n" \
"v_cmp_eq_u32 vcc, -1, v17\ns_cbranch_vccz .Lqk_sk1628%=\ns_mov_b64 exec, vcc\nbuffer_load_dwordx4 v[88:91], v3, s[36:39], s42 offen offset:2048 sc1\ns_mov_b64 exec, -1\n.Lqk_sk1628%=:\nv_max_u32 v17, v92, v94\nv_cmp_eq_u32 vcc, -1, v17\n" \
"s_cbranch_vccz .Lqk_sk1635%=\ns_mov_b64 exec, vcc\nbuffer_load_dwordx4 v[92:95], v3, s[36:39], s42 offen offset:3072 sc1\ns_mov_b64 exec, -1\n.Lqk_sk1635%=:\nv_mov_b64 v[48:49], 0\nv_mov_b64 v[50:51], 0\nv_mov_b64 v[52:53], 0\n" \
"v_mov_b64 v[54:55], 0\nv_mov_b64 v[56:57], 0\nv_mov_b64 v[58:59], 0\nv_mov_b64 v[60:61], 0\nv_mov_b64 v[62:63], 0\ns_add_u32 s43, s43, 1\ns_cmp_lt_u32 s43, 262144\ns_cbranch_scc1 .Lqk_poll0%=\n" \
"s_branch .Lqk_abort%=\n.Lqk_c1%=:\ns_waitcnt lgkmcnt(3)\nv_mfma_f32_16x16x32_bf16 v[32:35], v[232:235], v[56:59], v[32:35]\nv_mov_b64 v[52:53], 0\nv_mov_b64 v[54:55], 0\nv_mfma_f32_16x16x32_bf16 v[36:39], v[240:243], v[56:59], v[36:39]\ns_waitcnt lgkmcnt(2)\n" \
"ds_read_b128 v[64:67], v1 offset:6144\nv_mfma_f32_16x16x32_bf16 v[32:35], v[236:239], v[60:63], v[32:35]\nds_read_b128 v[68:71], v1 offset:7168\nv_mfma_f32_16x16x32_bf16 v[36:39], v[244:247], v[60:63], v[36:39]\ns_bfe_i32 s54, s40, 0x10001\nv_mfma_f32_16x16x32_bf16 v[40:43], v[248:251], v[56:59], v[40:43]\nv_mfma_f32_16x16x32_bf16 v[44:47], v[72:75], v[56:59], v[44:47]\nv_cvt_pk_bf16_f32 v36, v32, v36\n" \
"v_cvt_pk_bf16_f32 v37, v33, v37\nv_mfma_f32_16x16x32_bf16 v[40:43], v[252:255], v[60:63], v[40:43]\nv_cvt_pk_bf16_f32 v38, v34, v38\nv_cvt_pk_bf16_f32 v39, v35, v39\nv_mfma_f32_16x16x32_bf16 v[44:47], v[76:79], v[60:63], v[44:47]\nds_write_b128 v5, v[36:39] offset:256\nv_mov_b64 v[56:57], 0\nv_mov_b64 v[58:59], 0\n" \
"v_mov_b64 v[60:61], 0\nv_mov_b64 v[62:63], 0\n.Lqk_poll1%=:\ns_waitcnt vmcnt(0)\nv_smfmac_f32_16x16x64_bf16 v[48:51], v[80:83], a[104:111], v4\nds_write_b128 v0, v[80:83] offset:20480\nv_max3_u32 v16, v80, v82, v84\nv_smfmac_f32_16x16x64_bf16 v[52:55], v[80:83], a[112:119], v4\n" \
"ds_read_b128 v[72:75], v1 offset:8192\nds_read_b128 v[76:79], v1 offset:9216\nv_max3_u32 v16, v16, v86, v88\nv_smfmac_f32_16x16x64_bf16 v[56:59], v[80:83], a[120:127], v4\nds_write_b128 v0, v[84:87] offset:21504\nv_max3_u32 v16, v16, v90, v92\nv_max_u32 v16, v16, v94\nv_smfmac_f32_16x16x64_bf16 v[60:63], v[80:83], a[128:135], v4\n" \
"v_cmp_eq_u32 vcc, -1, v16\nv_smfmac_f32_16x16x64_bf16 v[48:51], v[84:87], a[136:143], v4\nds_write_b128 v0, v[88:91] offset:22528\ns_cbranch_vccnz .Lqk_re1%=\ns_waitcnt lgkmcnt(6)\nv_smfmac_f32_16x16x64_bf16 v[52:55], v[84:87], v[64:71], v4\nds_read_b128 v[64:67], v1 offset:10240\nds_read_b128 v[68:71], v1 offset:11264\n" \
"v_smfmac_f32_16x16x64_bf16 v[56:59], v[84:87], a[144:151], v4\nds_write_b128 v0, v[92:95] offset:23552\nv_smfmac_f32_16x16x64_bf16 v[60:63], v[84:87], a[152:159], v4\nv_smfmac_f32_16x16x64_bf16 v[48:51], v[88:91], a[160:167], v4\nv_smfmac_f32_16x16x64_bf16 v[52:55], v[88:91], a[168:175], v4\ns_waitcnt lgkmcnt(0)\ns_barrier\nds_read_b128 v[80:83], v0 offset:24576\n" \
"ds_read_b128 v[84:87], v0 offset:25600\nv_smfmac_f32_16x16x64_bf16 v[56:59], v[88:91], v[72:79], v4\nds_read_b128 v[72:75], v1 offset:12288\nds_read_b128 v[76:79], v1 offset:13312\nv_cvt_pk_bf16_f32 v44, v40, v44\nv_smfmac_f32_16x16x64_bf16 v[60:63], v[88:91], a[176:183], v4\nds_read_b128 v[88:91], v0 offset:26624\nv_cvt_pk_bf16_f32 v45, v41, v45\n" \
"v_smfmac_f32_16x16x64_bf16 v[48:51], v[92:95], a[184:191], v4\nv_cvt_pk_bf16_f32 v46, v42, v46\nv_smfmac_f32_16x16x64_bf16 v[52:55], v[92:95], a[192:199], v4\nv_cvt_pk_bf16_f32 v47, v43, v47\nv_smfmac_f32_16x16x64_bf16 v[56:59], v[92:95], a[200:207], v4\nv_smfmac_f32_16x16x64_bf16 v[60:63], v[92:95], v[64:71], v4\ns_waitcnt lgkmcnt(4)\nds_write_b128 v5, v[44:47] offset:768\n" \
"ds_read_b128 v[92:95], v0 offset:27648\nds_read_b128 v[64:67], v1 offset:14336\nds_read_b128 v[68:71], v1 offset:15360\nv_smfmac_f32_16x16x64_bf16 v[48:51], v[80:83], a[208:215], v4\nv_smfmac_f32_16x16x64_bf16 v[52:55], v[80:83], a[216:223], v4\nv_smfmac_f32_16x16x64_bf16 v[56:59], v[80:83], a[224:231], v4\nv_smfmac_f32_16x16x64_bf16 v[60:63], v[80:83], a[232:239], v4\ns_waitcnt lgkmcnt(7)\n" \
"ds_read_b128 v[80:83], v0 offset:28672\nv_smfmac_f32_16x16x64_bf16 v[48:51], v[84:87], a[240:247], v4\ns_waitcnt lgkmcnt(6)\nv_smfmac_f32_16x16x64_bf16 v[52:55], v[84:87], v[72:79], v4\nds_read_b128 v[72:75], v1 offset:16384\nds_read_b128 v[76:79], v1 offset:17408\nv_smfmac_f32_16x16x64_bf16 v[56:59], v[84:87], a[248:255], v4\nv_smfmac_f32_16x16x64_bf16 v[60:63], v[84:87], v[96:103], v4\n" \
"s_waitcnt lgkmcnt(7)\nds_read_b128 v[84:87], v0 offset:29696\nv_smfmac_f32_16x16x64_bf16 v[48:51], v[88:91], v[104:111], v4\ns_waitcnt lgkmcnt(7)\ns_barrier\nv_smfmac_f32_16x16x64_bf16 v[52:55], v[88:91], v[112:119], v4\ns_waitcnt lgkmcnt(4)\nv_smfmac_f32_16x16x64_bf16 v[56:59], v[88:91], v[64:71], v4\n" \
"ds_read_b128 v[64:67], v1 offset:18432\nds_read_b128 v[68:71], v1 offset:19456\nv_smfmac_f32_16x16x64_bf16 v[60:63], v[88:91], v[120:127], v4\nds_read_b128 v[88:91], v0 offset:30720\nv_smfmac_f32_16x16x64_bf16 v[48:51], v[92:95], v[128:135], v4\nds_read_b128 v[16:19], v7 offset:0\nv_smfmac_f32_16x16x64_bf16 v[52:55], v[92:95], v[136:143], v4\nv_smfmac_f32_16x16x64_bf16 v[56:59], v[92:95], v[144:151], v4\n" \
"s_waitcnt lgkmcnt(5)\nds_read_b128 v[20:23], v7 offset:2048\nv_smfmac_f32_16x16x64_bf16 v[60:63], v[92:95], v[72:79], v4\nds_read_b128 v[92:95], v0 offset:31744\nds_read_b128 v[72:75], v1 offset:20480\nds_read_b128 v[76:79], v1 offset:21504\nv_smfmac_f32_16x16x64_bf16 v[48:51], v[80:83], v[152:159], v4\nds_read_b128 v[24:27], v7 offset:4096\n" \
"v_smfmac_f32_16x16x64_bf16 v[52:55], v[80:83], v[160:167], v4\nv_smfmac_f32_16x16x64_bf16 v[56:59], v[80:83], v[168:175], v4\nds_read_b128 v[28:31], v7 offset:6144\nv_smfmac_f32_16x16x64_bf16 v[60:63], v[80:83], v[176:183], v4\ns_waitcnt lgkmcnt(10)\nds_read_b128 v[80:83], v0 offset:16384\nv_smfmac_f32_16x16x64_bf16 v[48:51], v[84:87], v[184:191], v4\ns_waitcnt lgkmcnt(9)\n" \
"ds_read_b128 v[32:35], v7 offset:8192\nv_smfmac_f32_16x16x64_bf16 v[52:55], v[84:87], v[64:71], v4\nds_read_b128 v[64:67], v1 offset:22528\nds_read_b128 v[68:71], v1 offset:23552\nv_smfmac_f32_16x16x64_bf16 v[56:59], v[84:87], v[192:199], v4\nds_read_b128 v[36:39], v7 offset:10240\nv_smfmac_f32_16x16x64_bf16 v[60:63], v[84:87], v[200:207], v4\ns_waitcnt lgkmcnt(12)\n" \
"ds_read_b128 v[84:87], v0 offset:17408\nv_smfmac_f32_16x16x64_bf16 v[48:51], v[88:91], v[208:215], v4\nds_read_b128 v[40:43], v7 offset:12288\nv_smfmac_f32_16x16x64_bf16 v[52:55], v[88:91], v[216:223], v4\ns_waitcnt lgkmcnt(9)\nv_smfmac_f32_16x16x64_bf16 v[56:59], v[88:91], v[72:79], v4\nds_read_b128 v[72:75], v1 offset:0\nds_read_b128 v[76:79], v1 offset:1024\n" \
"v_smfmac_f32_16x16x64_bf16 v[60:63], v[88:91], v[224:231], v4\nds_read_b128 v[88:91], v0 offset:18432\nv_smfmac_f32_16x16x64_bf16 v[48:51], v[92:95], v[232:239], v4\nds_read_b128 v[44:47], v7 offset:14336\nv_smfmac_f32_16x16x64_bf16 v[52:55], v[92:95], v[240:247], v4\nv_smfmac_f32_16x16x64_bf16 v[56:59], v[92:95], v[248:255], v4\ns_waitcnt lgkmcnt(7)\nv_smfmac_f32_16x16x64_bf16 v[60:63], v[92:95], v[64:71], v4\n" \
"ds_read_b128 v[92:95], v0 offset:19456\nds_read_b128 v[64:67], v1 offset:2048\nds_read_b128 v[68:71], v1 offset:3072\nv_smfmac_f32_16x16x64_bf16 v[48:51], v[80:83], a[0:7], v4\nv_smfmac_f32_16x16x64_bf16 v[52:55], v[80:83], a[8:15], v4\nv_smfmac_f32_16x16x64_bf16 v[56:59], v[80:83], a[16:23], v4\nv_smfmac_f32_16x16x64_bf16 v[60:63], v[80:83], a[24:31], v4\ns_waitcnt lgkmcnt(8)\n" \
"v_smfmac_f32_16x16x64_bf16 v[48:51], v[84:87], a[32:39], v4\ns_waitcnt lgkmcnt(5)\nv_smfmac_f32_16x16x64_bf16 v[52:55], v[84:87], v[72:79], v4\nds_read_b128 v[72:75], v1 offset:4096\nds_read_b128 v[76:79], v1 offset:5120\nv_smfmac_f32_16x16x64_bf16 v[56:59], v[84:87], a[40:47], v4\nv_smfmac_f32_16x16x64_bf16 v[60:63], v[84:87], a[48:55], v4\ns_waitcnt lgkmcnt(6)\n" \
"v_smfmac_f32_16x16x64_bf16 v[48:51], v[88:91], a[56:63], v4\nv_smfmac_f32_16x16x64_bf16 v[52:55], v[88:91], a[64:71], v4\ns_waitcnt lgkmcnt(4)\nv_smfmac_f32_16x16x64_bf16 v[48:51], v[92:95], a[80:87], v4\nv_smfmac_f32_16x16x64_bf16 v[52:55], v[92:95], a[88:95], v4\ns_waitcnt lgkmcnt(2)\nv_smfmac_f32_16x16x64_bf16 v[56:59], v[88:91], v[64:71], v4\nv_add_f32 v48, v48, v50\n" \
"v_smfmac_f32_16x16x64_bf16 v[60:63], v[88:91], a[72:79], v4\nv_add_f32 v52, v52, v54\nv_add_f32 v49, v49, v51\nv_smfmac_f32_16x16x64_bf16 v[56:59], v[92:95], a[96:103], v4\ns_waitcnt lgkmcnt(0)\nv_add_f32 v53, v53, v55\nv_cvt_pk_bf16_f32 v50, v48, v52\nv_smfmac_f32_16x16x64_bf16 v[60:63], v[92:95], v[72:79], v4\n" \
"v_cvt_pk_bf16_f32 v51, v49, v53\nds_write2_b32 v6, v50, v51 offset0:0 offset1:4\ns_nop 1\nv_add_f32 v56, v56, v58\nv_add_f32 v57, v57, v59\ns_nop 1\nv_add_f32 v60, v60, v62\nv_add_f32 v61, v61, v63\n" \
"v_cvt_pk_bf16_f32 v62, v56, v60\nv_cvt_pk_bf16_f32 v63, v57, v61\nds_write2_b32 v6, v62, v63 offset0:36 offset1:40\nv_mov_b32 v61, s54\nv_mfma_f32_16x16x32_bf16 v[52:55], v[32:35], v[16:19], v[12:15]\nv_mfma_f32_16x16x32_bf16 v[52:55], v[36:39], v[20:23], v[52:55]\nv_mfma_f32_16x16x32_bf16 v[52:55], v[40:43], v[24:27], v[52:55]\nv_mfma_f32_16x16x32_bf16 v[52:55], v[44:47], v[28:31], v[52:55]\n" \
"s_waitcnt lgkmcnt(0)\ns_barrier\nds_read_b128 v[32:35], v9 offset:0\nds_read_b128 v[36:39], v9 offset:576\nds_read_b128 v[40:43], v9 offset:1152\nds_read_b128 v[44:47], v9 offset:1728\nds_read_b32 v60, v0 offset:53760\nds_read_b128 v[80:83], v2 offset:0\n" \
"ds_read_b128 v[84:87], v2 offset:256\nds_read_b128 v[88:91], v2 offset:2048\ns_waitcnt lgkmcnt(7)\nv_mfma_f32_16x16x32_bf16 v[48:51], v[32:35], v[16:19], 0\ns_waitcnt lgkmcnt(6)\nv_mfma_f32_16x16x32_bf16 v[48:51], v[36:39], v[20:23], v[48:51]\ns_waitcnt lgkmcnt(5)\nv_mfma_f32_16x16x32_bf16 v[48:51], v[40:43], v[24:27], v[48:51]\n" \
"s_waitcnt lgkmcnt(4)\nv_mfma_f32_16x16x32_bf16 v[48:51], v[44:47], v[28:31], v[48:51]\ns_waitcnt lgkmcnt(2)\nv_mfma_f32_16x16x32_bf16 v[16:19], a[0:3], v[80:83], 0\nds_read_b128 v[92:95], v2 offset:2304\nv_max3_f32 v62, v52, v53, v54\nv_mfma_f32_16x16x32_bf16 v[20:23], a[8:11], v[80:83], 0\nds_read_b128 v[72:75], v1 offset:0\n" \
"v_max_f32 v62, v62, v55\nv_mfma_f32_16x16x32_bf16 v[24:27], a[16:19], v[80:83], 0\nv_max3_f32 v63, v62, v48, v49\nv_max3_f32 v62, v62, v48, v49\nv_mfma_f32_16x16x32_bf16 v[28:31], a[24:27], v[80:83], 0\ns_waitcnt lgkmcnt(3)\nv_permlane16_swap_b32 v62, v63\nv_mfma_f32_16x16x32_bf16 v[32:35], a[0:3], v[84:87], 0\n" \
"ds_read_b128 v[80:83], v2 offset:4096\nv_max_f32 v50, v62, v63\nv_mfma_f32_16x16x32_bf16 v[36:39], a[8:11], v[84:87], 0\nv_max_f32 v62, v62, v63\nv_mfma_f32_16x16x32_bf16 v[40:43], a[16:19], v[84:87], 0\ns_nop 0\nv_permlane32_swap_b32 v62, v50\nv_mfma_f32_16x16x32_bf16 v[44:47], a[24:27], v[84:87], 0\n" \
"s_waitcnt lgkmcnt(3)\nv_max_f32 v62, v62, v50\nv_mul_f32 v62, s59, v62\nv_mfma_f32_16x16x32_bf16 v[16:19], a[4:7], v[88:91], v[16:19]\nds_read_b128 v[84:87], v2 offset:4352\nv_fma_f32 v52, v52, s58, v62\nv_mfma_f32_16x16x32_bf16 v[20:23], a[12:15], v[88:91], v[20:23]\nds_read_b128 v[76:79], v1 offset:1024\n" \
"v_fma_f32 v53, v53, s58, v62\nv_mfma_f32_16x16x32_bf16 v[24:27], a[20:23], v[88:91], v[24:27]\nv_exp_f32 v52, v52\nv_mfma_f32_16x16x32_bf16 v[28:31], a[28:31], v[88:91], v[28:31]\ns_waitcnt lgkmcnt(4)\nv_fma_f32 v54, v54, s58, v62\nv_fma_f32 v55, v55, s58, v62\nv_mfma_f32_16x16x32_bf16 v[32:35], a[4:7], v[92:95], v[32:35]\n" \
"ds_read_b128 v[88:91], v2 offset:6144\nv_fma_f32 v48, v48, s58, v62\nv_mfma_f32_16x16x32_bf16 v[36:39], a[12:15], v[92:95], v[36:39]\nv_exp_f32 v53, v53\nv_mfma_f32_16x16x32_bf16 v[40:43], a[20:23], v[92:95], v[40:43]\nv_exp_f32 v54, v54\nv_mfma_f32_16x16x32_bf16 v[44:47], a[28:31], v[92:95], v[44:47]\ns_waitcnt lgkmcnt(3)\n" \
"v_add_f32 v63, v52, v53\nv_add_f32 v63, v63, v54\nv_mfma_f32_16x16x32_bf16 v[16:19], a[32:35], v[80:83], v[16:19]\nds_read_b128 v[92:95], v2 offset:6400\nv_fma_f32 v49, v49, s58, v62\nv_mfma_f32_16x16x32_bf16 v[20:23], v[72:75], v[80:83], v[20:23]\nds_read_b128 v[64:67], v1 offset:2048\nv_mfma_f32_16x16x32_bf16 v[24:27], a[40:43], v[80:83], v[24:27]\n" \
"v_exp_f32 v55, v55\nv_mfma_f32_16x16x32_bf16 v[28:31], a[48:51], v[80:83], v[28:31]\ns_waitcnt lgkmcnt(4)\nv_exp_f32 v48, v48\nv_mfma_f32_16x16x32_bf16 v[32:35], a[32:35], v[84:87], v[32:35]\nds_read_b128 v[80:83], v2 offset:8192\nv_add_f32 v63, v63, v55\nv_mfma_f32_16x16x32_bf16 v[36:39], v[72:75], v[84:87], v[36:39]\n" \
"v_exp_f32 v49, v49\nv_mfma_f32_16x16x32_bf16 v[40:43], a[40:43], v[84:87], v[40:43]\nv_add_f32 v63, v63, v48\nv_add_f32 v62, v63, v49\nv_mfma_f32_16x16x32_bf16 v[44:47], a[48:51], v[84:87], v[44:47]\ns_waitcnt lgkmcnt(3)\nv_add_f32 v50, v63, v49\nv_mfma_f32_16x16x32_bf16 v[16:19], a[36:39], v[88:91], v[16:19]\n" \
"ds_read_b128 v[84:87], v2 offset:8448\nv_mfma_f32_16x16x32_bf16 v[20:23], v[76:79], v[88:91], v[20:23]\nds_read_b128 v[68:71], v1 offset:3072\nv_mfma_f32_16x16x32_bf16 v[24:27], a[44:47], v[88:91], v[24:27]\nv_permlane16_swap_b32 v62, v50\nv_mfma_f32_16x16x32_bf16 v[28:31], a[52:55], v[88:91], v[28:31]\ns_waitcnt lgkmcnt(4)\nv_add_f32 v63, v62, v50\n" \
"v_add_f32 v50, v62, v50\nv_mfma_f32_16x16x32_bf16 v[32:35], a[36:39], v[92:95], v[32:35]\nds_read_b128 v[88:91], v2 offset:10240\nv_mfma_f32_16x16x32_bf16 v[36:39], v[76:79], v[92:95], v[36:39]\nv_permlane32_swap_b32 v63, v50\nv_mfma_f32_16x16x32_bf16 v[40:43], a[44:47], v[92:95], v[40:43]\nv_add_f32 v63, v63, v50\nv_mfma_f32_16x16x32_bf16 v[44:47], a[52:55], v[92:95], v[44:47]\n" \
"s_waitcnt lgkmcnt(3)\nv_rcp_f32 v63, v63\nv_mfma_f32_16x16x32_bf16 v[16:19], a[56:59], v[80:83], v[16:19]\nds_read_b128 v[92:95], v2 offset:10496\nv_mul_f32 v52, v52, v63\nv_mfma_f32_16x16x32_bf16 v[20:23], a[64:67], v[80:83], v[20:23]\nds_read_b128 v[72:75], v1 offset:4096\nv_mul_f32 v53, v53, v63\n" \
"v_mfma_f32_16x16x32_bf16 v[24:27], v[64:67], v[80:83], v[24:27]\nv_mul_f32 v54, v54, v63\nv_mul_f32 v55, v55, v63\nv_mfma_f32_16x16x32_bf16 v[28:31], a[72:75], v[80:83], v[28:31]\ns_waitcnt lgkmcnt(4)\nv_mul_f32 v48, v48, v63\nv_mul_f32 v49, v49, v63\nv_mfma_f32_16x16x32_bf16 v[32:35], a[56:59], v[84:87], v[32:35]\n" \
"ds_read_b128 v[80:83], v2 offset:12288\nv_cvt_pk_bf16_f32 v52, v52, v53\nv_mfma_f32_16x16x32_bf16 v[36:39], a[64:67], v[84:87], v[36:39]\nv_cvt_pk_bf16_f32 v53, v54, v55\nv_cvt_pk_bf16_f32 v48, v48, v49\nv_mfma_f32_16x16x32_bf16 v[40:43], v[64:67], v[84:87], v[40:43]\nv_bfi_b32 v52, s61, v52, v61\nv_bfi_b32 v53, s61, v53, v61\n" \
"v_mfma_f32_16x16x32_bf16 v[44:47], a[72:75], v[84:87], v[44:47]\ns_waitcnt lgkmcnt(3)\nv_bfi_b32 v48, s61, v48, v61\nv_mfma_f32_16x16x32_bf16 v[16:19], a[60:63], v[88:91], v[16:19]\nds_read_b128 v[84:87], v2 offset:12544\nv_mfma_f32_16x16x32_bf16 v[20:23], a[68:71], v[88:91], v[20:23]\nds_read_b128 v[76:79], v1 offset:5120\nv_mfma_f32_16x16x32_bf16 v[24:27], v[68:71], v[88:91], v[24:27]\n" \
"buffer_store_dwordx2 v[52:53], v10, s[36:39], s55 offen sc1\nv_mfma_f32_16x16x32_bf16 v[28:31], a[76:79], v[88:91], v[28:31]\ns_waitcnt lgkmcnt(4)\nbuffer_store_dword v48, v11, s[36:39], s55 offen sc1\nv_mfma_f32_16x16x32_bf16 v[32:35], a[60:63], v[92:95], v[32:35]\nds_read_b128 v[88:91], v2 offset:14336\nv_cmp_ne_u32 vcc, 0, v60\nv_mfma_f32_16x16x32_bf16 v[36:39], a[68:71], v[92:95], v[36:39]\n" \
"v_mfma_f32_16x16x32_bf16 v[40:43], v[68:71], v[92:95], v[40:43]\nv_mfma_f32_16x16x32_bf16 v[44:47], a[76:79], v[92:95], v[44:47]\ns_cbranch_vccnz .Lqk_pj%=\ns_cmp_lt_i32 s40, 63\ns_waitcnt lgkmcnt(0)\ns_cbranch_scc1 .Lqk_pwait%=\ns_branch .Lqk_end%=\n.Lqk_re1%=:\n" \
"v_max_u32 v17, v80, v82\nv_cmp_eq_u32 vcc, -1, v17\ns_cbranch_vccz .Lqk_sk1994%=\ns_mov_b64 exec, vcc\nbuffer_load_dwordx4 v[80:83], v3, s[36:39], s42 offen offset:0 sc1\ns_mov_b64 exec, -1\n.Lqk_sk1994%=:\nv_max_u32 v17, v84, v86\n" \
"v_cmp_eq_u32 vcc, -1, v17\ns_cbranch_vccz .Lqk_sk2001%=\ns_mov_b64 exec, vcc\nbuffer_load_dwordx4 v[84:87], v3, s[36:39], s42 offen offset:1024 sc1\ns_mov_b64 exec, -1\n.Lqk_sk2001%=:\nv_max_u32 v17, v88, v90\nv_cmp_eq_u32 vcc, -1, v17\n" \
"s_cbranch_vccz .Lqk_sk2008%=\ns_mov_b64 exec, vcc\nbuffer_load_dwordx4 v[88:91], v3, s[36:39], s42 offen offset:2048 sc1\ns_mov_b64 exec, -1\n.Lqk_sk2008%=:\nv_max_u32 v17, v92, v94\nv_cmp_eq_u32 vcc, -1, v17\ns_cbranch_vccz .Lqk_sk2015%=\n" \
"s_mov_b64 exec, vcc\nbuffer_load_dwordx4 v[92:95], v3, s[36:39], s42 offen offset:3072 sc1\ns_mov_b64 exec, -1\n.Lqk_sk2015%=:\nv_mov_b64 v[48:49], 0\nv_mov_b64 v[50:51], 0\nv_mov_b64 v[52:53], 0\nv_mov_b64 v[54:55], 0\n" \
"v_mov_b64 v[56:57], 0\nv_mov_b64 v[58:59], 0\nv_mov_b64 v[60:61], 0\nv_mov_b64 v[62:63], 0\ns_add_u32 s43, s43, 1\ns_cmp_lt_u32 s43, 262144\ns_cbranch_scc1 .Lqk_poll1%=\ns_branch .Lqk_abort%=\n" \
".Lqk_c2%=:\ns_waitcnt lgkmcnt(3)\nv_mfma_f32_16x16x32_bf16 v[32:35], v[232:235], v[56:59], v[32:35]\nv_mov_b64 v[52:53], 0\nv_mov_b64 v[54:55], 0\nv_mfma_f32_16x16x32_bf16 v[36:39], v[240:243], v[56:59], v[36:39]\ns_waitcnt lgkmcnt(2)\nds_read_b128 v[64:67], v1 offset:12288\n" \
"v_mfma_f32_16x16x32_bf16 v[32:35], v[236:239], v[60:63], v[32:35]\nds_read_b128 v[68:71], v1 offset:13312\nv_mfma_f32_16x16x32_bf16 v[36:39], v[244:247], v[60:63], v[36:39]\ns_bfe_i32 s54, s40, 0x10001\nv_mfma_f32_16x16x32_bf16 v[40:43], v[248:251], v[56:59], v[40:43]\nv_mfma_f32_16x16x32_bf16 v[44:47], v[72:75], v[56:59], v[44:47]\nv_cvt_pk_bf16_f32 v36, v32, v36\nv_cvt_pk_bf16_f32 v37, v33, v37\n" \
"v_mfma_f32_16x16x32_bf16 v[40:43], v[252:255], v[60:63], v[40:43]\nv_cvt_pk_bf16_f32 v38, v34, v38\nv_cvt_pk_bf16_f32 v39, v35, v39\nv_mfma_f32_16x16x32_bf16 v[44:47], v[76:79], v[60:63], v[44:47]\nds_write_b128 v5, v[36:39] offset:256\nv_mov_b64 v[56:57], 0\nv_mov_b64 v[58:59], 0\nv_mov_b64 v[60:61], 0\n" \
"v_mov_b64 v[62:63], 0\n.Lqk_poll2%=:\ns_waitcnt vmcnt(0)\nv_smfmac_f32_16x16x64_bf16 v[48:51], v[80:83], a[208:215], v4\nds_write_b128 v0, v[80:83] offset:24576\nv_max3_u32 v16, v80, v82, v84\nv_smfmac_f32_16x16x64_bf16 v[52:55], v[80:83], a[216:223], v4\nds_read_b128 v[72:75], v1 offset:14336\n" \
"ds_read_b128 v[76:79], v1 offset:15360\nv_max3_u32 v16, v16, v86, v88\nv_smfmac_f32_16x16x64_bf16 v[56:59], v[80:83], a[224:231], v4\nds_write_b128 v0, v[84:87] offset:25600\nv_max3_u32 v16, v16, v90, v92\nv_max_u32 v16, v16, v94\nv_smfmac_f32_16x16x64_bf16 v[60:63], v[80:83], a[232:239], v4\nv_cmp_eq_u32 vcc, -1, v16\n" \
"v_smfmac_f32_16x16x64_bf16 v[48:51], v[84:87], a[240:247], v4\nds_write_b128 v0, v[88:91] offset:26624\ns_cbranch_vccnz .Lqk_re2%=\ns_waitcnt lgkmcnt(6)\nv_smfmac_f32_16x16x64_bf16 v[52:55], v[84:87], v[64:71], v4\nds_read_b128 v[64:67], v1 offset:16384\nds_read_b128 v[68:71], v1 offset:17408\nv_smfmac_f32_16x16x64_bf16 v[56:59], v[84:87], a[248:255], v4\n" \
"ds_write_b128 v0, v[92:95] offset:27648\nv_smfmac_f32_16x16x64_bf16 v[60:63], v[84:87], v[96:103], v4\nv_smfmac_f32_16x16x64_bf16 v[48:51], v[88:91], v[104:111], v4\nv_smfmac_f32_16x16x64_bf16 v[52:55], v[88:91], v[112:119], v4\ns_waitcnt lgkmcnt(0)\ns_barrier\nds_read_b128 v[80:83], v0 offset:28672\nds_read_b128 v[84:87], v0 offset:29696\n" \
"v_smfmac_f32_16x16x64_bf16 v[56:59], v[88:91], v[72:79], v4\nds_read_b128 v[72:75], v1 offset:18432\nds_read_b128 v[76:79], v1 offset:19456\nv_cvt_pk_bf16_f32 v44, v40, v44\nv_smfmac_f32_16x16x64_bf16 v[60:63], v[88:91], v[120:127], v4\nds_read_b128 v[88:91], v0 offset:30720\nv_cvt_pk_bf16_f32 v45, v41, v45\nv_smfmac_f32_16x16x64_bf16 v[48:51], v[92:95], v[128:135], v4\n" \
"v_cvt_pk_bf16_f32 v46, v42, v46\nv_smfmac_f32_16x16x64_bf16 v[52:55], v[92:95], v[136:143], v4\nv_cvt_pk_bf16_f32 v47, v43, v47\nv_smfmac_f32_16x16x64_bf16 v[56:59], v[92:95], v[144:151], v4\nv_smfmac_f32_16x16x64_bf16 v[60:63], v[92:95], v[64:71], v4\ns_waitcnt lgkmcnt(4)\nds_write_b128 v5, v[44:47] offset:768\nds_read_b128 v[92:95], v0 offset:31744\n" \
"ds_read_b128 v[64:67], v1 offset:20480\nds_read_b128 v[68:71], v1 offset:21504\nv_smfmac_f32_16x16x64_bf16 v[48:51], v[80:83], v[152:159], v4\nv_smfmac_f32_16x16x64_bf16 v[52:55], v[80:83], v[160:167], v4\nv_smfmac_f32_16x16x64_bf16 v[56:59], v[80:83], v[168:175], v4\nv_smfmac_f32_16x16x64_bf16 v[60:63], v[80:83], v[176:183], v4\ns_waitcnt lgkmcnt(7)\nds_read_b128 v[80:83], v0 offset:16384\n" \
"v_smfmac_f32_16x16x64_bf16 v[48:51], v[84:87], v[184:191], v4\ns_waitcnt lgkmcnt(6)\nv_smfmac_f32_16x16x64_bf16 v[52:55], v[84:87], v[72:79], v4\nds_read_b128 v[72:75], v1 offset:22528\nds_read_b128 v[76:79], v1 offset:23552\nv_smfmac_f32_16x16x64_bf16 v[56:59], v[84:87], v[192:199], v4\nv_smfmac_f32_16x16x64_bf16 v[60:63], v[84:87], v[200:207], v4\ns_waitcnt lgkmcnt(7)\n" \
"ds_read_b128 v[84:87], v0 offset:17408\nv_smfmac_f32_16x16x64_bf16 v[48:51], v[88:91], v[208:215], v4\ns_waitcnt lgkmcnt(7)\ns_barrier\nv_smfmac_f32_16x16x64_bf16 v[52:55], v[88:91], v[216:223], v4\ns_waitcnt lgkmcnt(4)\nv_smfmac_f32_16x16x64_bf16 v[56:59], v[88:91], v[64:71], v4\nds_read_b128 v[64:67], v1 offset:0\n" \
"ds_read_b128 v[68:71], v1 offset:1024\nv_smfmac_f32_16x16x64_bf16 v[60:63], v[88:91], v[224:231], v4\nds_read_b128 v[88:91], v0 offset:18432\nv_smfmac_f32_16x16x64_bf16 v[48:51], v[92:95], v[232:239], v4\nds_read_b128 v[16:19], v7 offset:256\nv_smfmac_f32_16x16x64_bf16 v[52:55], v[92:95], v[240:247], v4\nv_smfmac_f32_16x16x64_bf16 v[56:59], v[92:95], v[248:255], v4\ns_waitcnt lgkmcnt(5)\n" \
"ds_read_b128 v[20:23], v7 offset:2304\nv_smfmac_f32_16x16x64_bf16 v[60:63], v[92:95], v[72:79], v4\nds_read_b128 v[92:95], v0 offset:19456\nds_read_b128 v[72:75], v1 offset:2048\nds_read_b128 v[76:79], v1 offset:3072\nv_smfmac_f32_16x16x64_bf16 v[48:51], v[80:83], a[0:7], v4\nds_read_b128 v[24:27], v7 offset:4352\nv_smfmac_f32_16x16x64_bf16 v[52:55], v[80:83], a[8:15], v4\n" \
"v_smfmac_f32_16x16x64_bf16 v[56:59], v[80:83], a[16:23], v4\nds_read_b128 v[28:31], v7 offset:6400\nv_smfmac_f32_16x16x64_bf16 v[60:63], v[80:83], a[24:31], v4\ns_waitcnt lgkmcnt(10)\nds_read_b128 v[80:83], v0 offset:20480\nv_smfmac_f32_16x16x64_bf16 v[48:51], v[84:87], a[32:39], v4\ns_waitcnt lgkmcnt(9)\nds_read_b128 v[32:35], v7 offset:8192\n" \
"v_smfmac_f32_16x16x64_bf16 v[52:55], v[84:87], v[64:71], v4\nds_read_b128 v[64:67], v1 offset:4096\nds_read_b128 v[68:71], v1 offset:5120\nv_smfmac_f32_16x16x64_bf16 v[56:59], v[84:87], a[40:47], v4\nds_read_b128 v[36:39], v7 offset:10240\nv_smfmac_f32_16x16x64_bf16 v[60:63], v[84:87], a[48:55], v4\ns_waitcnt lgkmcnt(12)\nds_read_b128 v[84:87], v0 offset:21504\n" \
"v_smfmac_f32_16x16x64_bf16 v[48:51], v[88:91], a[56:63], v4\nds_read_b128 v[40:43], v7 offset:12288\nv_smfmac_f32_16x16x64_bf16 v[52:55], v[88:91], a[64:71], v4\ns_waitcnt lgkmcnt(9)\nv_smfmac_f32_16x16x64_bf16 v[56:59], v[88:91], v[72:79], v4\nds_read_b128 v[72:75], v1 offset:6144\nds_read_b128 v[76:79], v1 offset:7168\nv_smfmac_f32_16x16x64_bf16 v[60:63], v[88:91], a[72:79], v4\n" \
"ds_read_b128 v[88:91], v0 offset:22528\nv_smfmac_f32_16x16x64_bf16 v[48:51], v[92:95], a[80:87], v4\nds_read_b128 v[44:47], v7 offset:14336\nv_smfmac_f32_16x16x64_bf16 v[52:55], v[92:95], a[88:95], v4\nv_smfmac_f32_16x16x64_bf16 v[56:59], v[92:95], a[96:103], v4\ns_waitcnt lgkmcnt(7)\nv_smfmac_f32_16x16x64_bf16 v[60:63], v[92:95], v[64:71], v4\nds_read_b128 v[92:95], v0 offset:23552\n" \
"ds_read_b128 v[64:67], v1 offset:8192\nds_read_b128 v[68:71], v1 offset:9216\nv_smfmac_f32_16x16x64_bf16 v[48:51], v[80:83], a[104:111], v4\nv_smfmac_f32_16x16x64_bf16 v[52:55], v[80:83], a[112:119], v4\nv_smfmac_f32_16x16x64_bf16 v[56:59], v[80:83], a[120:127], v4\nv_smfmac_f32_16x16x64_bf16 v[60:63], v[80:83], a[128:135], v4\ns_waitcnt lgkmcnt(8)\nv_smfmac_f32_16x16x64_bf16 v[48:51], v[84:87], a[136:143], v4\n" \
"s_waitcnt lgkmcnt(5)\nds_read_b128 v[80:83], v7 offset:8448\nv_smfmac_f32_16x16x64_bf16 v[52:55], v[84:87], v[72:79], v4\nds_read_b128 v[72:75], v1 offset:10240\nds_read_b128 v[76:79], v1 offset:11264\nv_smfmac_f32_16x16x64_bf16 v[56:59], v[84:87], a[144:151], v4\nv_smfmac_f32_16x16x64_bf16 v[60:63], v[84:87], a[152:159], v4\ns_waitcnt lgkmcnt(7)\n" \
"v_smfmac_f32_16x16x64_bf16 v[48:51], v[88:91], a[160:167], v4\nds_read_b128 v[84:87], v7 offset:10496\nv_smfmac_f32_16x16x64_bf16 v[52:55], v[88:91], a[168:175], v4\ns_waitcnt lgkmcnt(6)\nv_smfmac_f32_16x16x64_bf16 v[48:51], v[92:95], a[184:191], v4\nv_smfmac_f32_16x16x64_bf16 v[52:55], v[92:95], a[192:199], v4\ns_waitcnt lgkmcnt(4)\nv_smfmac_f32_16x16x64_bf16 v[56:59], v[88:91], v[64:71], v4\n" \
"ds_read_b128 v[64:67], v7 offset:12544\nv_add_f32 v48, v48, v50\nv_smfmac_f32_16x16x64_bf16 v[60:63], v[88:91], a[176:183], v4\nds_read_b128 v[68:71], v7 offset:14592\nv_add_f32 v52, v52, v54\nv_add_f32 v49, v49, v51\nv_smfmac_f32_16x16x64_bf16 v[56:59], v[92:95], a[200:207], v4\ns_waitcnt lgkmcnt(3)\n" \
"v_add_f32 v53, v53, v55\nv_cvt_pk_bf16_f32 v50, v48, v52\nv_smfmac_f32_16x16x64_bf16 v[60:63], v[92:95], v[72:79], v4\nv_cvt_pk_bf16_f32 v51, v49, v53\nds_write2_b32 v6, v50, v51 offset0:0 offset1:4\ns_nop 1\nv_add_f32 v56, v56, v58\nv_add_f32 v57, v57, v59\n" \
"s_nop 1\nv_add_f32 v60, v60, v62\nv_add_f32 v61, v61, v63\nv_cvt_pk_bf16_f32 v62, v56, v60\nv_cvt_pk_bf16_f32 v63, v57, v61\nds_write2_b32 v6, v62, v63 offset0:36 offset1:40\nv_mov_b32 v61, s54\nv_mfma_f32_16x16x32_bf16 v[52:55], v[32:35], v[16:19], 0\n" \
"v_mfma_f32_16x16x32_bf16 v[52:55], v[36:39], v[20:23], v[52:55]\nv_mfma_f32_16x16x32_bf16 v[52:55], v[40:43], v[24:27], v[52:55]\nv_mfma_f32_16x16x32_bf16 v[52:55], v[44:47], v[28:31], v[52:55]\ns_waitcnt lgkmcnt(0)\ns_barrier\nds_read_b128 v[32:35], v9 offset:0\nds_read_b128 v[36:39], v9 offset:576\nds_read_b128 v[40:43], v9 offset:1152\n" \
"ds_read_b128 v[44:47], v9 offset:1728\nds_read_b32 v60, v0 offset:53760\nv_mfma_f32_16x16x32_bf16 v[56:59], v[80:83], v[16:19], v[12:15]\nv_mfma_f32_16x16x32_bf16 v[56:59], v[84:87], v[20:23], v[56:59]\nv_mfma_f32_16x16x32_bf16 v[56:59], v[64:67], v[24:27], v[56:59]\nv_mfma_f32_16x16x32_bf16 v[56:59], v[68:71], v[28:31], v[56:59]\ns_waitcnt lgkmcnt(4)\nds_read_b128 v[80:83], v2 offset:0\n" \
"ds_read_b128 v[84:87], v2 offset:256\nds_read_b128 v[88:91], v2 offset:2048\nv_mfma_f32_16x16x32_bf16 v[48:51], v[32:35], v[16:19], 0\ns_waitcnt lgkmcnt(6)\nv_mfma_f32_16x16x32_bf16 v[48:51], v[36:39], v[20:23], v[48:51]\ns_waitcnt lgkmcnt(5)\nv_mfma_f32_16x16x32_bf16 v[48:51], v[40:43], v[24:27], v[48:51]\ns_waitcnt lgkmcnt(4)\n" \
"v_mfma_f32_16x16x32_bf16 v[48:51], v[44:47], v[28:31], v[48:51]\ns_waitcnt lgkmcnt(2)\nv_mfma_f32_16x16x32_bf16 v[16:19], a[0:3], v[80:83], 0\nds_read_b128 v[92:95], v2 offset:2304\nv_max3_f32 v62, v52, v53, v54\nv_max3_f32 v62, v62, v56, v57\nv_mfma_f32_16x16x32_bf16 v[20:23], a[8:11], v[80:83], 0\nds_read_b128 v[72:75], v1 offset:0\n" \
"v_max3_f32 v62, v62, v58, v59\nv_max_f32 v62, v62, v55\nv_mfma_f32_16x16x32_bf16 v[24:27], a[16:19], v[80:83], 0\nv_max3_f32 v63, v62, v48, v49\nv_max3_f32 v62, v62, v48, v49\nv_mfma_f32_16x16x32_bf16 v[28:31], a[24:27], v[80:83], 0\ns_waitcnt lgkmcnt(3)\nv_permlane16_swap_b32 v62, v63\n" \
"v_max_f32 v50, v62, v63\nv_mfma_f32_16x16x32_bf16 v[32:35], a[0:3], v[84:87], 0\nds_read_b128 v[80:83], v2 offset:4096\nv_max_f32 v62, v62, v63\nv_mfma_f32_16x16x32_bf16 v[36:39], a[8:11], v[84:87], 0\ns_nop 0\nv_permlane32_swap_b32 v62, v50\nv_max_f32 v62, v62, v50\n" \
"v_mfma_f32_16x16x32_bf16 v[40:43], a[16:19], v[84:87], 0\nv_mul_f32 v62, s59, v62\nv_fma_f32 v52, v52, s58, v62\nv_fma_f32 v53, v53, s58, v62\nv_mfma_f32_16x16x32_bf16 v[44:47], a[24:27], v[84:87], 0\ns_waitcnt lgkmcnt(3)\nv_exp_f32 v52, v52\nv_fma_f32 v54, v54, s58, v62\n" \
"v_mfma_f32_16x16x32_bf16 v[16:19], a[4:7], v[88:91], v[16:19]\nds_read_b128 v[84:87], v2 offset:4352\nv_exp_f32 v53, v53\nv_mfma_f32_16x16x32_bf16 v[20:23], a[12:15], v[88:91], v[20:23]\nds_read_b128 v[76:79], v1 offset:1024\nv_fma_f32 v55, v55, s58, v62\nv_add_f32 v63, v52, v53\nv_mfma_f32_16x16x32_bf16 v[24:27], a[20:23], v[88:91], v[24:27]\n" \
"v_exp_f32 v54, v54\nv_fma_f32 v56, v56, s58, v62\nv_mfma_f32_16x16x32_bf16 v[28:31], a[28:31], v[88:91], v[28:31]\ns_waitcnt lgkmcnt(4)\nv_exp_f32 v55, v55\nv_add_f32 v63, v63, v54\nv_mfma_f32_16x16x32_bf16 v[32:35], a[4:7], v[92:95], v[32:35]\nds_read_b128 v[88:91], v2 offset:6144\n" \
"v_fma_f32 v57, v57, s58, v62\nv_add_f32 v63, v63, v55\nv_mfma_f32_16x16x32_bf16 v[36:39], a[12:15], v[92:95], v[36:39]\nv_exp_f32 v56, v56\nv_fma_f32 v58, v58, s58, v62\nv_mfma_f32_16x16x32_bf16 v[40:43], a[20:23], v[92:95], v[40:43]\nv_exp_f32 v57, v57\nv_add_f32 v63, v63, v56\n" \
"v_mfma_f32_16x16x32_bf16 v[44:47], a[28:31], v[92:95], v[44:47]\ns_waitcnt lgkmcnt(3)\nv_fma_f32 v59, v59, s58, v62\nv_exp_f32 v58, v58\nv_mfma_f32_16x16x32_bf16 v[16:19], a[32:35], v[80:83], v[16:19]\nds_read_b128 v[92:95], v2 offset:6400\nv_add_f32 v63, v63, v57\nv_fma_f32 v48, v48, s58, v62\n" \
"v_mfma_f32_16x16x32_bf16 v[20:23], v[72:75], v[80:83], v[20:23]\nds_read_b128 v[64:67], v1 offset:2048\nv_exp_f32 v59, v59\nv_mfma_f32_16x16x32_bf16 v[24:27], a[40:43], v[80:83], v[24:27]\nv_add_f32 v63, v63, v58\nv_fma_f32 v49, v49, s58, v62\nv_add_f32 v63, v63, v59\nv_mfma_f32_16x16x32_bf16 v[28:31], a[48:51], v[80:83], v[28:31]\n" \
"s_waitcnt lgkmcnt(4)\nv_exp_f32 v48, v48\nv_mfma_f32_16x16x32_bf16 v[32:35], a[32:35], v[84:87], v[32:35]\nds_read_b128 v[80:83], v2 offset:8192\nv_exp_f32 v49, v49\nv_mfma_f32_16x16x32_bf16 v[36:39], v[72:75], v[84:87], v[36:39]\nv_add_f32 v63, v63, v48\nv_add_f32 v62, v63, v49\n" \
"v_add_f32 v50, v63, v49\nv_mfma_f32_16x16x32_bf16 v[40:43], a[40:43], v[84:87], v[40:43]\ns_nop 0\nv_permlane16_swap_b32 v62, v50\nv_add_f32 v63, v62, v50\nv_mfma_f32_16x16x32_bf16 v[44:47], a[48:51], v[84:87], v[44:47]\ns_waitcnt lgkmcnt(3)\nv_add_f32 v50, v62, v50\n" \
"v_mfma_f32_16x16x32_bf16 v[16:19], a[36:39], v[88:91], v[16:19]\nds_read_b128 v[84:87], v2 offset:8448\nv_permlane32_swap_b32 v63, v50\nv_mfma_f32_16x16x32_bf16 v[20:23], v[76:79], v[88:91], v[20:23]\nds_read_b128 v[68:71], v1 offset:3072\nv_add_f32 v63, v63, v50\nv_mfma_f32_16x16x32_bf16 v[24:27], a[44:47], v[88:91], v[24:27]\nv_rcp_f32 v63, v63\n" \
"v_mfma_f32_16x16x32_bf16 v[28:31], a[52:55], v[88:91], v[28:31]\ns_waitcnt lgkmcnt(4)\nv_mul_f32 v52, v52, v63\nv_mul_f32 v53, v53, v63\nv_mul_f32 v54, v54, v63\nv_mfma_f32_16x16x32_bf16 v[32:35], a[36:39], v[92:95], v[32:35]\nds_read_b128 v[88:91], v2 offset:10240\nv_mul_f32 v55, v55, v63\n" \
"v_mul_f32 v56, v56, v63\nv_mfma_f32_16x16x32_bf16 v[36:39], v[76:79], v[92:95], v[36:39]\nv_mul_f32 v57, v57, v63\nv_mul_f32 v58, v58, v63\nv_mul_f32 v59, v59, v63\nv_mfma_f32_16x16x32_bf16 v[40:43], a[44:47], v[92:95], v[40:43]\nv_mul_f32 v48, v48, v63\nv_mul_f32 v49, v49, v63\n" \
"v_cvt_pk_bf16_f32 v52, v52, v53\nv_mfma_f32_16x16x32_bf16 v[44:47], a[52:55], v[92:95], v[44:47]\ns_waitcnt lgkmcnt(3)\nv_cvt_pk_bf16_f32 v53, v54, v55\nv_cvt_pk_bf16_f32 v48, v48, v49\nv_cvt_pk_bf16_f32 v54, v56, v57\nv_mfma_f32_16x16x32_bf16 v[16:19], a[56:59], v[80:83], v[16:19]\nds_read_b128 v[92:95], v2 offset:10496\n" \
"v_cvt_pk_bf16_f32 v55, v58, v59\nv_bfi_b32 v52, s61, v52, v61\nv_mfma_f32_16x16x32_bf16 v[20:23], a[64:67], v[80:83], v[20:23]\nds_read_b128 v[72:75], v1 offset:4096\nv_bfi_b32 v53, s61, v53, v61\nv_bfi_b32 v54, s61, v54, v61\nv_mfma_f32_16x16x32_bf16 v[24:27], v[64:67], v[80:83], v[24:27]\nv_bfi_b32 v55, s61, v55, v61\n" \
"v_bfi_b32 v48, s61, v48, v61\nv_mfma_f32_16x16x32_bf16 v[28:31], a[72:75], v[80:83], v[28:31]\ns_waitcnt lgkmcnt(4)\nbuffer_store_dwordx4 v[52:55], v10, s[36:39], s55 offen sc1\nv_mfma_f32_16x16x32_bf16 v[32:35], a[56:59], v[84:87], v[32:35]\nds_read_b128 v[80:83], v2 offset:12288\nv_mfma_f32_16x16x32_bf16 v[36:39], a[64:67], v[84:87], v[36:39]\nbuffer_store_dword v48, v11, s[36:39], s55 offen sc1\n" \
"v_cmp_ne_u32 vcc, 0, v60\nv_mfma_f32_16x16x32_bf16 v[40:43], v[64:67], v[84:87], v[40:43]\nv_mfma_f32_16x16x32_bf16 v[44:47], a[72:75], v[84:87], v[44:47]\ns_waitcnt lgkmcnt(3)\nv_mfma_f32_16x16x32_bf16 v[16:19], a[60:63], v[88:91], v[16:19]\nds_read_b128 v[84:87], v2 offset:12544\nv_mfma_f32_16x16x32_bf16 v[20:23], a[68:71], v[88:91], v[20:23]\nds_read_b128 v[76:79], v1 offset:5120\n" \
"v_mfma_f32_16x16x32_bf16 v[24:27], v[68:71], v[88:91], v[24:27]\nv_mfma_f32_16x16x32_bf16 v[28:31], a[76:79], v[88:91], v[28:31]\ns_waitcnt lgkmcnt(4)\nv_mfma_f32_16x16x32_bf16 v[32:35], a[60:63], v[92:95], v[32:35]\nds_read_b128 v[88:91], v2 offset:14336\nv_mfma_f32_16x16x32_bf16 v[36:39], a[68:71], v[92:95], v[36:39]\nv_mfma_f32_16x16x32_bf16 v[40:43], v[68:71], v[92:95], v[40:43]\nv_mfma_f32_16x16x32_bf16 v[44:47], a[76:79], v[92:95], v[44:47]\n" \
"s_cbranch_vccnz .Lqk_pj%=\ns_cmp_lt_i32 s40, 63\ns_waitcnt lgkmcnt(0)\ns_cbranch_scc1 .Lqk_pwait%=\ns_branch .Lqk_end%=\n.Lqk_re2%=:\nv_max_u32 v17, v80, v82\nv_cmp_eq_u32 vcc, -1, v17\n" \
"s_cbranch_vccz .Lqk_sk2404%=\ns_mov_b64 exec, vcc\nbuffer_load_dwordx4 v[80:83], v3, s[36:39], s42 offen offset:0 sc1\ns_mov_b64 exec, -1\n.Lqk_sk2404%=:\nv_max_u32 v17, v84, v86\nv_cmp_eq_u32 vcc, -1, v17\ns_cbranch_vccz .Lqk_sk2411%=\n" \
"s_mov_b64 exec, vcc\nbuffer_load_dwordx4 v[84:87], v3, s[36:39], s42 offen offset:1024 sc1\ns_mov_b64 exec, -1\n.Lqk_sk2411%=:\nv_max_u32 v17, v88, v90\nv_cmp_eq_u32 vcc, -1, v17\ns_cbranch_vccz .Lqk_sk2418%=\ns_mov_b64 exec, vcc\n" \
"buffer_load_dwordx4 v[88:91], v3, s[36:39], s42 offen offset:2048 sc1\ns_mov_b64 exec, -1\n.Lqk_sk2418%=:\nv_max_u32 v17, v92, v94\nv_cmp_eq_u32 vcc, -1, v17\ns_cbranch_vccz .Lqk_sk2425%=\ns_mov_b64 exec, vcc\nbuffer_load_dwordx4 v[92:95], v3, s[36:39], s42 offen offset:3072 sc1\n" \
"s_mov_b64 exec, -1\n.Lqk_sk2425%=:\nv_mov_b64 v[48:49], 0\nv_mov_b64 v[50:51], 0\nv_mov_b64 v[52:53], 0\nv_mov_b64 v[54:55], 0\nv_mov_b64 v[56:57], 0\nv_mov_b64 v[58:59], 0\n" \
"v_mov_b64 v[60:61], 0\nv_mov_b64 v[62:63], 0\ns_add_u32 s43, s43, 1\ns_cmp_lt_u32 s43, 262144\ns_cbranch_scc1 .Lqk_poll2%=\ns_branch .Lqk_abort%=\n.Lqk_c3%=:\ns_waitcnt lgkmcnt(3)\n" \
"v_mfma_f32_16x16x32_bf16 v[32:35], v[232:235], v[56:59], v[32:35]\ns_add_u32 s44, s40, 1\nv_mov_b64 v[52:53], 0\nv_mov_b64 v[54:55], 0\nv_mfma_f32_16x16x32_bf16 v[36:39], v[240:243], v[56:59], v[36:39]\ns_lshl_b32 s44, s44, 17\ns_waitcnt lgkmcnt(2)\nds_read_b128 v[64:67], v1 offset:18432\n" \
"v_mfma_f32_16x16x32_bf16 v[32:35], v[236:239], v[60:63], v[32:35]\ns_add_u32 s44, s44, %[xn]\ns_lshr_b32 s45, s44, 8\nds_read_b128 v[68:71], v1 offset:19456\nv_mfma_f32_16x16x32_bf16 v[36:39], v[244:247], v[60:63], v[36:39]\ns_and_b32 s45, s45, 0x2000\ns_xor_b32 s41, s44, s45\nv_mfma_f32_16x16x32_bf16 v[40:43], v[248:251], v[56:59], v[40:43]\n" \
"buffer_load_dword v16, v12, s[36:39], s56 offen sc1\nv_mfma_f32_16x16x32_bf16 v[44:47], v[72:75], v[56:59], v[44:47]\nv_cvt_pk_bf16_f32 v36, v32, v36\nv_cvt_pk_bf16_f32 v37, v33, v37\nv_mfma_f32_16x16x32_bf16 v[40:43], v[252:255], v[60:63], v[40:43]\nv_cvt_pk_bf16_f32 v38, v34, v38\nv_cvt_pk_bf16_f32 v39, v35, v39\nv_mfma_f32_16x16x32_bf16 v[44:47], v[76:79], v[60:63], v[44:47]\n" \
"ds_write_b128 v5, v[36:39] offset:256\nv_mov_b64 v[56:57], 0\nv_mov_b64 v[58:59], 0\nv_mov_b64 v[60:61], 0\nv_mov_b64 v[62:63], 0\n.Lqk_poll3%=:\ns_waitcnt vmcnt(1)\nv_smfmac_f32_16x16x64_bf16 v[48:51], v[80:83], v[152:159], v4\n" \
"ds_write_b128 v0, v[80:83] offset:28672\nv_max3_u32 v17, v80, v82, v84\nv_smfmac_f32_16x16x64_bf16 v[52:55], v[80:83], v[160:167], v4\nds_read_b128 v[72:75], v1 offset:20480\nds_read_b128 v[76:79], v1 offset:21504\nv_max3_u32 v17, v17, v86, v88\nv_smfmac_f32_16x16x64_bf16 v[56:59], v[80:83], v[168:175], v4\nds_write_b128 v0, v[84:87] offset:29696\n" \
"v_max3_u32 v17, v17, v90, v92\nv_max_u32 v17, v17, v94\nv_smfmac_f32_16x16x64_bf16 v[60:63], v[80:83], v[176:183], v4\nv_cmp_eq_u32 vcc, -1, v17\nv_smfmac_f32_16x16x64_bf16 v[48:51], v[84:87], v[184:191], v4\nds_write_b128 v0, v[88:91] offset:30720\ns_cbranch_vccnz .Lqk_re3%=\ns_waitcnt lgkmcnt(6)\n" \
"v_smfmac_f32_16x16x64_bf16 v[52:55], v[84:87], v[64:71], v4\nds_read_b128 v[64:67], v1 offset:22528\nds_read_b128 v[68:71], v1 offset:23552\nv_smfmac_f32_16x16x64_bf16 v[56:59], v[84:87], v[192:199], v4\nds_write_b128 v0, v[92:95] offset:31744\nv_smfmac_f32_16x16x64_bf16 v[60:63], v[84:87], v[200:207], v4\nv_smfmac_f32_16x16x64_bf16 v[48:51], v[88:91], v[208:215], v4\nv_smfmac_f32_16x16x64_bf16 v[52:55], v[88:91], v[216:223], v4\n" \
"s_waitcnt lgkmcnt(0)\ns_barrier\nds_read_b128 v[80:83], v0 offset:16384\nds_read_b128 v[84:87], v0 offset:17408\nv_smfmac_f32_16x16x64_bf16 v[56:59], v[88:91], v[72:79], v4\ns_waitcnt vmcnt(0)\nds_read_b128 v[72:75], v1 offset:0\nds_read_b128 v[76:79], v1 offset:1024\n" \
"v_cvt_pk_bf16_f32 v44, v40, v44\nv_cmp_ge_i32 vcc, s40, v16\nv_smfmac_f32_16x16x64_bf16 v[60:63], v[88:91], v[224:231], v4\ns_cmp_eq_u64 vcc, 0\ns_cselect_b32 s61, s41, -2147483648\ns_cselect_b32 s44, 1, 0\nds_read_b128 v[88:91], v0 offset:18432\nv_cvt_pk_bf16_f32 v45, v41, v45\n" \
"v_smfmac_f32_16x16x64_bf16 v[48:51], v[92:95], v[232:239], v4\ns_add_u32 m0, %[l0], 0\ns_add_u32 s60, s61, 0\nbuffer_load_dwordx4 v0, s[36:39], s60 offen offset:0 sc1 lds\nv_cvt_pk_bf16_f32 v46, v42, v46\nv_smfmac_f32_16x16x64_bf16 v[52:55], v[92:95], v[240:247], v4\nv_cvt_pk_bf16_f32 v47, v43, v47\nv_mov_b32 v16, s44\n" \
"v_smfmac_f32_16x16x64_bf16 v[56:59], v[92:95], v[248:255], v4\nbuffer_load_dwordx4 v0, s[36:39], s60 offen offset:1024 sc1 lds\nv_smfmac_f32_16x16x64_bf16 v[60:63], v[92:95], v[64:71], v4\ns_waitcnt lgkmcnt(4)\nds_write_b128 v5, v[44:47] offset:768\nds_read_b128 v[92:95], v0 offset:19456\nds_read_b128 v[64:67], v1 offset:2048\nds_read_b128 v[68:71], v1 offset:3072\n" \
"ds_write_b32 v0, v16 offset:53760\nv_smfmac_f32_16x16x64_bf16 v[48:51], v[80:83], a[0:7], v4\nbuffer_load_dwordx4 v0, s[36:39], s60 offen offset:2048 sc1 lds\nv_smfmac_f32_16x16x64_bf16 v[52:55], v[80:83], a[8:15], v4\nv_smfmac_f32_16x16x64_bf16 v[56:59], v[80:83], a[16:23], v4\nbuffer_load_dwordx4 v0, s[36:39], s60 offen offset:3072 sc1 lds\nv_smfmac_f32_16x16x64_bf16 v[60:63], v[80:83], a[24:31], v4\ns_waitcnt lgkmcnt(8)\n" \
"s_add_u32 m0, %[l0], 4096\nds_read_b128 v[80:83], v0 offset:20480\nv_smfmac_f32_16x16x64_bf16 v[48:51], v[84:87], a[32:39], v4\ns_add_u32 s60, s61, 4096\ns_waitcnt lgkmcnt(7)\nbuffer_load_dwordx4 v0, s[36:39], s60 offen offset:0 sc1 lds\nv_smfmac_f32_16x16x64_bf16 v[52:55], v[84:87], v[72:79], v4\nds_read_b128 v[72:75], v1 offset:4096\n" \
"ds_read_b128 v[76:79], v1 offset:5120\nv_smfmac_f32_16x16x64_bf16 v[56:59], v[84:87], a[40:47], v4\nbuffer_load_dwordx4 v0, s[36:39], s60 offen offset:1024 sc1 lds\nv_smfmac_f32_16x16x64_bf16 v[60:63], v[84:87], a[48:55], v4\ns_waitcnt lgkmcnt(8)\nds_read_b128 v[84:87], v0 offset:21504\nv_smfmac_f32_16x16x64_bf16 v[48:51], v[88:91], a[56:63], v4\ns_waitcnt lgkmcnt(8)\n" \
"s_barrier\nbuffer_load_dwordx4 v0, s[36:39], s60 offen offset:2048 sc1 lds\nv_smfmac_f32_16x16x64_bf16 v[52:55], v[88:91], a[64:71], v4\ns_waitcnt lgkmcnt(5)\nv_smfmac_f32_16x16x64_bf16 v[56:59], v[88:91], v[64:71], v4\nds_read_b128 v[64:67], v1 offset:6144\nds_read_b128 v[68:71], v1 offset:7168\nbuffer_load_dwordx4 v0, s[36:39], s60 offen offset:3072 sc1 lds\n" \
"v_smfmac_f32_16x16x64_bf16 v[60:63], v[88:91], a[72:79], v4\ns_add_u32 m0, %[l0], 8192\nds_read_b128 v[88:91], v0 offset:22528\nv_smfmac_f32_16x16x64_bf16 v[48:51], v[92:95], a[80:87], v4\ns_add_u32 s60, s61, 16384\nbuffer_load_dwordx4 v0, s[36:39], s60 offen offset:0 sc1 lds\nv_smfmac_f32_16x16x64_bf16 v[52:55], v[92:95], a[88:95], v4\nv_smfmac_f32_16x16x64_bf16 v[56:59], v[92:95], a[96:103], v4\n" \
"s_waitcnt lgkmcnt(4)\nbuffer_load_dwordx4 v0, s[36:39], s60 offen offset:1024 sc1 lds\nv_smfmac_f32_16x16x64_bf16 v[60:63], v[92:95], v[72:79], v4\nds_read_b128 v[92:95], v0 offset:23552\nds_read_b128 v[72:75], v1 offset:8192\nds_read_b128 v[76:79], v1 offset:9216\nv_smfmac_f32_16x16x64_bf16 v[48:51], v[80:83], a[104:111], v4\nbuffer_load_dwordx4 v0, s[36:39], s60 offen offset:2048 sc1 lds\n" \
"v_smfmac_f32_16x16x64_bf16 v[52:55], v[80:83], a[112:119], v4\nv_smfmac_f32_16x16x64_bf16 v[56:59], v[80:83], a[120:127], v4\nbuffer_load_dwordx4 v0, s[36:39], s60 offen offset:3072 sc1 lds\nv_smfmac_f32_16x16x64_bf16 v[60:63], v[80:83], a[128:135], v4\ns_waitcnt lgkmcnt(6)\ns_add_u32 m0, %[l0], 12288\nds_read_b128 v[80:83], v0 offset:24576\nv_smfmac_f32_16x16x64_bf16 v[48:51], v[84:87], a[136:143], v4\n" \
"s_add_u32 s60, s61, 20480\ns_waitcnt lgkmcnt(5)\nbuffer_load_dwordx4 v0, s[36:39], s60 offen offset:0 sc1 lds\nv_smfmac_f32_16x16x64_bf16 v[52:55], v[84:87], v[64:71], v4\nds_read_b128 v[64:67], v1 offset:10240\nds_read_b128 v[68:71], v1 offset:11264\nv_smfmac_f32_16x16x64_bf16 v[56:59], v[84:87], a[144:151], v4\nbuffer_load_dwordx4 v0, s[36:39], s60 offen offset:1024 sc1 lds\n" \
"v_smfmac_f32_16x16x64_bf16 v[60:63], v[84:87], a[152:159], v4\ns_waitcnt lgkmcnt(6)\nds_read_b128 v[84:87], v0 offset:25600\nv_smfmac_f32_16x16x64_bf16 v[48:51], v[88:91], a[160:167], v4\nbuffer_load_dwordx4 v0, s[36:39], s60 offen offset:2048 sc1 lds\nv_smfmac_f32_16x16x64_bf16 v[52:55], v[88:91], a[168:175], v4\ns_waitcnt lgkmcnt(4)\nv_smfmac_f32_16x16x64_bf16 v[56:59], v[88:91], v[72:79], v4\n" \
"ds_read_b128 v[72:75], v1 offset:12288\nds_read_b128 v[76:79], v1 offset:13312\nbuffer_load_dwordx4 v0, s[36:39], s60 offen offset:3072 sc1 lds\nv_smfmac_f32_16x16x64_bf16 v[60:63], v[88:91], a[176:183], v4\nds_read_b128 v[88:91], v0 offset:26624\nv_smfmac_f32_16x16x64_bf16 v[48:51], v[92:95], a[184:191], v4\nv_smfmac_f32_16x16x64_bf16 v[52:55], v[92:95], a[192:199], v4\nv_smfmac_f32_16x16x64_bf16 v[56:59], v[92:95], a[200:207], v4\n" \
"s_waitcnt lgkmcnt(4)\nv_smfmac_f32_16x16x64_bf16 v[60:63], v[92:95], v[64:71], v4\nds_read_b128 v[92:95], v0 offset:27648\nds_read_b128 v[64:67], v1 offset:14336\nds_read_b128 v[68:71], v1 offset:15360\nv_smfmac_f32_16x16x64_bf16 v[48:51], v[80:83], a[208:215], v4\nv_smfmac_f32_16x16x64_bf16 v[52:55], v[80:83], a[216:223], v4\nv_smfmac_f32_16x16x64_bf16 v[56:59], v[80:83], a[224:231], v4\n" \
"v_smfmac_f32_16x16x64_bf16 v[60:63], v[80:83], a[232:239], v4\ns_waitcnt lgkmcnt(6)\nv_smfmac_f32_16x16x64_bf16 v[48:51], v[84:87], a[240:247], v4\ns_waitcnt lgkmcnt(4)\nv_smfmac_f32_16x16x64_bf16 v[52:55], v[84:87], v[72:79], v4\nds_read_b128 v[72:75], v1 offset:16384\nds_read_b128 v[76:79], v1 offset:17408\nv_smfmac_f32_16x16x64_bf16 v[56:59], v[84:87], a[248:255], v4\n" \
"v_smfmac_f32_16x16x64_bf16 v[60:63], v[84:87], v[96:103], v4\ns_waitcnt lgkmcnt(5)\nv_smfmac_f32_16x16x64_bf16 v[48:51], v[88:91], v[104:111], v4\nv_smfmac_f32_16x16x64_bf16 v[52:55], v[88:91], v[112:119], v4\ns_waitcnt lgkmcnt(4)\nv_smfmac_f32_16x16x64_bf16 v[48:51], v[92:95], v[128:135], v4\nv_smfmac_f32_16x16x64_bf16 v[52:55], v[92:95], v[136:143], v4\ns_waitcnt lgkmcnt(2)\n" \
"v_smfmac_f32_16x16x64_bf16 v[56:59], v[88:91], v[64:71], v4\nv_add_f32 v48, v48, v50\nv_smfmac_f32_16x16x64_bf16 v[60:63], v[88:91], v[120:127], v4\nv_add_f32 v52, v52, v54\nv_add_f32 v49, v49, v51\nv_smfmac_f32_16x16x64_bf16 v[56:59], v[92:95], v[144:151], v4\ns_waitcnt lgkmcnt(0)\nv_add_f32 v53, v53, v55\n" \
"v_cvt_pk_bf16_f32 v50, v48, v52\nv_smfmac_f32_16x16x64_bf16 v[60:63], v[92:95], v[72:79], v4\nv_cvt_pk_bf16_f32 v51, v49, v53\nds_write2_b32 v6, v50, v51 offset0:0 offset1:4\ns_nop 1\nv_add_f32 v56, v56, v58\nv_add_f32 v57, v57, v59\ns_nop 1\n" \
"v_add_f32 v60, v60, v62\nv_add_f32 v61, v61, v63\nv_cvt_pk_bf16_f32 v62, v56, v60\nv_cvt_pk_bf16_f32 v63, v57, v61\nds_write2_b32 v6, v62, v63 offset0:36 offset1:40\ns_waitcnt vmcnt(0)\ns_waitcnt lgkmcnt(0)\ns_barrier\n" \
"s_cbranch_vccnz .Lqk_slow%=\nds_read_b128 v[80:83], v2 offset:0\nds_read_b128 v[84:87], v2 offset:256\nds_read_b128 v[88:91], v2 offset:2048\ns_add_u32 m0, %[l0], 16384\ns_add_u32 s60, s41, 32768\nbuffer_load_dwordx4 v0, s[36:39], s60 offen offset:0 sc1 lds\nbuffer_load_dwordx4 v0, s[36:39], s60 offen offset:1024 sc1 lds\n" \
"buffer_load_dwordx4 v0, s[36:39], s60 offen offset:2048 sc1 lds\nbuffer_load_dwordx4 v0, s[36:39], s60 offen offset:3072 sc1 lds\ns_add_u32 m0, %[l0], 20480\ns_add_u32 s60, s41, 36864\nbuffer_load_dwordx4 v0, s[36:39], s60 offen offset:0 sc1 lds\nbuffer_load_dwordx4 v0, s[36:39], s60 offen offset:1024 sc1 lds\nbuffer_load_dwordx4 v0, s[36:39], s60 offen offset:2048 sc1 lds\nbuffer_load_dwordx4 v0, s[36:39], s60 offen offset:3072 sc1 lds\n" \
"s_add_u32 m0, %[l0], 24576\ns_add_u32 s60, s41, 49152\nbuffer_load_dwordx4 v0, s[36:39], s60 offen offset:0 sc1 lds\nbuffer_load_dwordx4 v0, s[36:39], s60 offen offset:1024 sc1 lds\nbuffer_load_dwordx4 v0, s[36:39], s60 offen offset:2048 sc1 lds\nbuffer_load_dwordx4 v0, s[36:39], s60 offen offset:3072 sc1 lds\ns_add_u32 m0, %[l0], 28672\ns_add_u32 s60, s41, 53248\n" \
"buffer_load_dwordx4 v0, s[36:39], s60 offen offset:0 sc1 lds\nbuffer_load_dwordx4 v0, s[36:39], s60 offen offset:1024 sc1 lds\nbuffer_load_dwordx4 v0, s[36:39], s60 offen offset:2048 sc1 lds\nbuffer_load_dwordx4 v0, s[36:39], s60 offen offset:3072 sc1 lds\ns_waitcnt lgkmcnt(2)\nv_mfma_f32_16x16x32_bf16 v[16:19], a[0:3], v[80:83], 0\nds_read_b128 v[92:95], v2 offset:2304\nv_mfma_f32_16x16x32_bf16 v[20:23], a[8:11], v[80:83], 0\n" \
"ds_read_b128 v[72:75], v1 offset:0\nv_mfma_f32_16x16x32_bf16 v[24:27], a[16:19], v[80:83], 0\nv_mfma_f32_16x16x32_bf16 v[28:31], a[24:27], v[80:83], 0\ns_waitcnt lgkmcnt(3)\nv_mfma_f32_16x16x32_bf16 v[32:35], a[0:3], v[84:87], 0\nds_read_b128 v[80:83], v2 offset:4096\nv_mfma_f32_16x16x32_bf16 v[36:39], a[8:11], v[84:87], 0\nv_mfma_f32_16x16x32_bf16 v[40:43], a[16:19], v[84:87], 0\n" \
"v_mfma_f32_16x16x32_bf16 v[44:47], a[24:27], v[84:87], 0\ns_waitcnt lgkmcnt(3)\nv_mfma_f32_16x16x32_bf16 v[16:19], a[4:7], v[88:91], v[16:19]\nds_read_b128 v[84:87], v2 offset:4352\nv_mfma_f32_16x16x32_bf16 v[20:23], a[12:15], v[88:91], v[20:23]\nds_read_b128 v[76:79], v1 offset:1024\nv_mfma_f32_16x16x32_bf16 v[24:27], a[20:23], v[88:91], v[24:27]\nv_mfma_f32_16x16x32_bf16 v[28:31], a[28:31], v[88:91], v[28:31]\n" \
"s_waitcnt lgkmcnt(4)\nv_mfma_f32_16x16x32_bf16 v[32:35], a[4:7], v[92:95], v[32:35]\nds_read_b128 v[88:91], v2 offset:6144\nv_mfma_f32_16x16x32_bf16 v[36:39], a[12:15], v[92:95], v[36:39]\nv_mfma_f32_16x16x32_bf16 v[40:43], a[20:23], v[92:95], v[40:43]\nv_mfma_f32_16x16x32_bf16 v[44:47], a[28:31], v[92:95], v[44:47]\ns_waitcnt lgkmcnt(3)\nv_mfma_f32_16x16x32_bf16 v[16:19], a[32:35], v[80:83], v[16:19]\n" \
"ds_read_b128 v[92:95], v2 offset:6400\nv_mfma_f32_16x16x32_bf16 v[20:23], v[72:75], v[80:83], v[20:23]\nds_read_b128 v[64:67], v1 offset:2048\nv_mfma_f32_16x16x32_bf16 v[24:27], a[40:43], v[80:83], v[24:27]\nv_mfma_f32_16x16x32_bf16 v[28:31], a[48:51], v[80:83], v[28:31]\ns_waitcnt lgkmcnt(4)\nv_mfma_f32_16x16x32_bf16 v[32:35], a[32:35], v[84:87], v[32:35]\nds_read_b128 v[80:83], v2 offset:8192\n" \
"v_mfma_f32_16x16x32_bf16 v[36:39], v[72:75], v[84:87], v[36:39]\nv_mfma_f32_16x16x32_bf16 v[40:43], a[40:43], v[84:87], v[40:43]\nv_mfma_f32_16x16x32_bf16 v[44:47], a[48:51], v[84:87], v[44:47]\ns_waitcnt lgkmcnt(3)\nv_mfma_f32_16x16x32_bf16 v[16:19], a[36:39], v[88:91], v[16:19]\nds_read_b128 v[84:87], v2 offset:8448\nv_mfma_f32_16x16x32_bf16 v[20:23], v[76:79], v[88:91], v[20:23]\nds_read_b128 v[68:71], v1 offset:3072\n" \
"v_mfma_f32_16x16x32_bf16 v[24:27], a[44:47], v[88:91], v[24:27]\nv_mfma_f32_16x16x32_bf16 v[28:31], a[52:55], v[88:91], v[28:31]\ns_waitcnt lgkmcnt(4)\nv_mfma_f32_16x16x32_bf16 v[32:35], a[36:39], v[92:95], v[32:35]\nds_read_b128 v[88:91], v2 offset:10240\nv_mfma_f32_16x16x32_bf16 v[36:39], v[76:79], v[92:95], v[36:39]\nv_mfma_f32_16x16x32_bf16 v[40:43], a[44:47], v[92:95], v[40:43]\nv_mfma_f32_16x16x32_bf16 v[44:47], a[52:55], v[92:95], v[44:47]\n" \
"s_waitcnt lgkmcnt(3)\nv_mfma_f32_16x16x32_bf16 v[16:19], a[56:59], v[80:83], v[16:19]\nds_read_b128 v[92:95], v2 offset:10496\nv_mfma_f32_16x16x32_bf16 v[20:23], a[64:67], v[80:83], v[20:23]\nds_read_b128 v[72:75], v1 offset:4096\nv_mfma_f32_16x16x32_bf16 v[24:27], v[64:67], v[80:83], v[24:27]\nv_mfma_f32_16x16x32_bf16 v[28:31], a[72:75], v[80:83], v[28:31]\ns_waitcnt lgkmcnt(4)\n" \
"v_mfma_f32_16x16x32_bf16 v[32:35], a[56:59], v[84:87], v[32:35]\nds_read_b128 v[80:83], v2 offset:12288\nv_mfma_f32_16x16x32_bf16 v[36:39], a[64:67], v[84:87], v[36:39]\nv_mfma_f32_16x16x32_bf16 v[40:43], v[64:67], v[84:87], v[40:43]\nv_mfma_f32_16x16x32_bf16 v[44:47], a[72:75], v[84:87], v[44:47]\ns_waitcnt lgkmcnt(3)\nv_mfma_f32_16x16x32_bf16 v[16:19], a[60:63], v[88:91], v[16:19]\nds_read_b128 v[84:87], v2 offset:12544\n" \
"v_mfma_f32_16x16x32_bf16 v[20:23], a[68:71], v[88:91], v[20:23]\nds_read_b128 v[76:79], v1 offset:5120\nv_mfma_f32_16x16x32_bf16 v[24:27], v[68:71], v[88:91], v[24:27]\nv_mfma_f32_16x16x32_bf16 v[28:31], a[76:79], v[88:91], v[28:31]\ns_waitcnt lgkmcnt(4)\nv_mfma_f32_16x16x32_bf16 v[32:35], a[60:63], v[92:95], v[32:35]\nds_read_b128 v[88:91], v2 offset:14336\nv_mfma_f32_16x16x32_bf16 v[36:39], a[68:71], v[92:95], v[36:39]\n" \
"v_mfma_f32_16x16x32_bf16 v[40:43], v[68:71], v[92:95], v[40:43]\nv_mfma_f32_16x16x32_bf16 v[44:47], a[76:79], v[92:95], v[44:47]\ns_waitcnt vmcnt(0)\ns_branch .Lqk_pj%=\n.Lqk_slow%=:\ns_cmp_ge_i32 s40, 63\ns_cbranch_scc1 .Lqk_end%=\n.Lqk_fetch0%=:\n" \
"buffer_load_dword v16, v12, s[36:39], s56 offen sc1\ns_waitcnt vmcnt(0)\nv_cmp_ge_i32 vcc, s40, v16\ns_cbranch_vccz .Lqk_fok%=\ns_add_u32 s43, s43, 1\ns_cmp_lt_u32 s43, 262144\ns_cbranch_scc1 .Lqk_fetch0%=\ns_branch .Lqk_abort%=\n" \
".Lqk_fok%=:\ns_add_u32 s44, s40, 1\ns_lshl_b32 s44, s44, 17\ns_add_u32 s44, s44, %[xn]\ns_lshr_b32 s45, s44, 8\ns_and_b32 s45, s45, 0x2000\ns_xor_b32 s41, s44, s45\ns_add_u32 m0, %[l0], 0\n" \
"s_add_u32 s60, s41, 0\nbuffer_load_dwordx4 v0, s[36:39], s60 offen offset:0 sc1 lds\nbuffer_load_dwordx4 v0, s[36:39], s60 offen offset:1024 sc1 lds\nbuffer_load_dwordx4 v0, s[36:39], s60 offen offset:2048 sc1 lds\nbuffer_load_dwordx4 v0, s[36:39], s60 offen offset:3072 sc1 lds\ns_add_u32 m0, %[l0], 4096\ns_add_u32 s60, s41, 4096\nbuffer_load_dwordx4 v0, s[36:39], s60 offen offset:0 sc1 lds\n" \
"buffer_load_dwordx4 v0, s[36:39], s60 offen offset:1024 sc1 lds\nbuffer_load_dwordx4 v0, s[36:39], s60 offen offset:2048 sc1 lds\nbuffer_load_dwordx4 v0, s[36:39], s60 offen offset:3072 sc1 lds\ns_add_u32 m0, %[l0], 8192\ns_add_u32 s60, s41, 16384\nbuffer_load_dwordx4 v0, s[36:39], s60 offen offset:0 sc1 lds\nbuffer_load_dwordx4 v0, s[36:39], s60 offen offset:1024 sc1 lds\nbuffer_load_dwordx4 v0, s[36:39], s60 offen offset:2048 sc1 lds\n" \
"buffer_load_dwordx4 v0, s[36:39], s60 offen offset:3072 sc1 lds\ns_add_u32 m0, %[l0], 12288\ns_add_u32 s60, s41, 20480\nbuffer_load_dwordx4 v0, s[36:39], s60 offen offset:0 sc1 lds\nbuffer_load_dwordx4 v0, s[36:39], s60 offen offset:1024 sc1 lds\nbuffer_load_dwordx4 v0, s[36:39], s60 offen offset:2048 sc1 lds\nbuffer_load_dwordx4 v0, s[36:39], s60 offen offset:3072 sc1 lds\ns_add_u32 m0, %[l0], 16384\n" \
"s_add_u32 s60, s41, 32768\nbuffer_load_dwordx4 v0, s[36:39], s60 offen offset:0 sc1 lds\nbuffer_load_dwordx4 v0, s[36:39], s60 offen offset:1024 sc1 lds\nbuffer_load_dwordx4 v0, s[36:39], s60 offen offset:2048 sc1 lds\nbuffer_load_dwordx4 v0, s[36:39], s60 offen offset:3072 sc1 lds\ns_add_u32 m0, %[l0], 20480\ns_add_u32 s60, s41, 36864\nbuffer_load_dwordx4 v0, s[36:39], s60 offen offset:0 sc1 lds\n" \
"buffer_load_dwordx4 v0, s[36:39], s60 offen offset:1024 sc1 lds\nbuffer_load_dwordx4 v0, s[36:39], s60 offen offset:2048 sc1 lds\nbuffer_load_dwordx4 v0, s[36:39], s60 offen offset:3072 sc1 lds\ns_add_u32 m0, %[l0], 24576\ns_add_u32 s60, s41, 49152\nbuffer_load_dwordx4 v0, s[36:39], s60 offen offset:0 sc1 lds\nbuffer_load_dwordx4 v0, s[36:39], s60 offen offset:1024 sc1 lds\nbuffer_load_dwordx4 v0, s[36:39], s60 offen offset:2048 sc1 lds\n" \
"buffer_load_dwordx4 v0, s[36:39], s60 offen offset:3072 sc1 lds\ns_add_u32 m0, %[l0], 28672\ns_add_u32 s60, s41, 53248\nbuffer_load_dwordx4 v0, s[36:39], s60 offen offset:0 sc1 lds\nbuffer_load_dwordx4 v0, s[36:39], s60 offen offset:1024 sc1 lds\nbuffer_load_dwordx4 v0, s[36:39], s60 offen offset:2048 sc1 lds\nbuffer_load_dwordx4 v0, s[36:39], s60 offen offset:3072 sc1 lds\ns_waitcnt vmcnt(0)\n" \
"s_branch .Lqk_pwait%=\n.Lqk_re3%=:\nv_max_u32 v17, v80, v82\nv_cmp_eq_u32 vcc, -1, v17\ns_cbranch_vccz .Lqk_sk2838%=\ns_mov_b64 exec, vcc\nbuffer_load_dwordx4 v[80:83], v3, s[36:39], s42 offen offset:0 sc1\ns_mov_b64 exec, -1\n" \
".Lqk_sk2838%=:\nv_max_u32 v17, v84, v86\nv_cmp_eq_u32 vcc, -1, v17\ns_cbranch_vccz .Lqk_sk2845%=\ns_mov_b64 exec, vcc\nbuffer_load_dwordx4 v[84:87], v3, s[36:39], s42 offen offset:1024 sc1\ns_mov_b64 exec, -1\n.Lqk_sk2845%=:\n" \
"v_max_u32 v17, v88, v90\nv_cmp_eq_u32 vcc, -1, v17\ns_cbranch_vccz .Lqk_sk2852%=\ns_mov_b64 exec, vcc\nbuffer_load_dwordx4 v[88:91], v3, s[36:39], s42 offen offset:2048 sc1\ns_mov_b64 exec, -1\n.Lqk_sk2852%=:\nv_max_u32 v17, v92, v94\n" \
"v_cmp_eq_u32 vcc, -1, v17\ns_cbranch_vccz .Lqk_sk2859%=\ns_mov_b64 exec, vcc\nbuffer_load_dwordx4 v[92:95], v3, s[36:39], s42 offen offset:3072 sc1\ns_mov_b64 exec, -1\n.Lqk_sk2859%=:\nbuffer_load_dword v16, v12, s[36:39], s56 offen sc1\nv_mov_b64 v[48:49], 0\n" \
"v_mov_b64 v[50:51], 0\nv_mov_b64 v[52:53], 0\nv_mov_b64 v[54:55], 0\nv_mov_b64 v[56:57], 0\nv_mov_b64 v[58:59], 0\nv_mov_b64 v[60:61], 0\nv_mov_b64 v[62:63], 0\ns_add_u32 s43, s43, 1\n" \
"s_cmp_lt_u32 s43, 262144\ns_cbranch_scc1 .Lqk_poll3%=\ns_branch .Lqk_abort%=\n.Lqk_abort%=:\ns_mov_b64 exec, -1\nv_mov_b32 v16, 0\ns_movk_i32 s44, 6400\nbuffer_store_dword v16, off, s[36:39], s44 sc1\n" \
".Lqk_end%=:\ns_waitcnt vmcnt(0) lgkmcnt(0)\n" \
""
#define VU_ASM \
"v_mbcnt_lo_u32_b32 v255, -1, 0\nv_mbcnt_hi_u32_b32 v255, -1, v255\nv_lshlrev_b32 v255, 2, v255\nv_add_u32 v255, %[tb], v255\nds_read_b32 v0, v255 offset:0\nds_read_b32 v1, v255 offset:256\nds_read_b32 v2, v255 offset:512\nds_read_b32 v3, v255 offset:768\n" \
"ds_read_b32 v4, v255 offset:1024\nds_read_b32 v5, v255 offset:1280\nds_read_b32 v6, v255 offset:1536\nds_read_b32 v7, v255 offset:1792\nds_read_b32 v8, v255 offset:2048\nds_read_b32 v9, v255 offset:2304\nds_read_b32 v10, v255 offset:2560\nds_read_b32 v11, v255 offset:2816\n" \
"ds_read_b32 v90, v255 offset:3072\nds_read_b32 v91, v255 offset:3328\nds_read_b32 v92, v255 offset:3584\nds_read_b32 v93, v255 offset:3840\nds_read_b32 v94, v255 offset:4096\nds_read_b32 v95, v255 offset:4352\ns_waitcnt lgkmcnt(0)\ns_mov_b64 s[36:37], %[ws]\n" \
"s_mov_b32 s38, %[sz]\ns_mov_b32 s39, 0x20000\ns_mov_b32 s45, 2096896\nbuffer_load_dword v15, off, s[36:39], s45 sc1\ns_add_u32 m0, %[lw], 16384\ns_add_u32 s44, %[wo], 0\nbuffer_load_dwordx4 v90, %[rw], s44 offen nt lds\ns_add_u32 m0, %[lw], 17408\n" \
"s_add_u32 s44, %[wo], 65536\nbuffer_load_dwordx4 v90, %[rw], s44 offen nt lds\ns_add_u32 m0, %[lw], 18432\ns_add_u32 s44, %[wo], 131072\nbuffer_load_dwordx4 v90, %[rw], s44 offen nt lds\ns_add_u32 m0, %[lw], 19456\ns_add_u32 s44, %[wo], 196608\nbuffer_load_dwordx4 v90, %[rw], s44 offen nt lds\n" \
"s_add_u32 m0, %[lw], 20480\ns_add_u32 s44, %[wo], 524288\nbuffer_load_dwordx4 v90, %[rw], s44 offen nt lds\ns_add_u32 m0, %[lw], 21504\ns_add_u32 s44, %[wo], 589824\nbuffer_load_dwordx4 v90, %[rw], s44 offen nt lds\ns_add_u32 m0, %[lw], 22528\ns_add_u32 s44, %[wo], 655360\n" \
"buffer_load_dwordx4 v90, %[rw], s44 offen nt lds\ns_add_u32 m0, %[lw], 23552\ns_add_u32 s44, %[wo], 720896\nbuffer_load_dwordx4 v90, %[rw], s44 offen nt lds\ns_add_u32 m0, %[tb], 0\ns_add_u32 s44, %[wo], 1048576\nbuffer_load_dwordx4 v90, %[rw], s44 offen nt lds\ns_add_u32 m0, %[tb], 1024\n" \
"s_add_u32 s44, %[wo], 1114112\nbuffer_load_dwordx4 v90, %[rw], s44 offen nt lds\ns_add_u32 m0, %[tb], 2048\ns_add_u32 s44, %[wo], 1179648\nbuffer_load_dwordx4 v90, %[rw], s44 offen nt lds\ns_add_u32 m0, %[tb], 3072\ns_add_u32 s44, %[wo], 1245184\nbuffer_load_dwordx4 v90, %[rw], s44 offen nt lds\n" \
"s_add_u32 m0, %[tb], 4096\ns_add_u32 s44, %[wo], 1572864\nbuffer_load_dwordx4 v90, %[rw], s44 offen nt lds\ns_add_u32 m0, %[tb], 5120\ns_add_u32 s44, %[wo], 1638400\nbuffer_load_dwordx4 v90, %[rw], s44 offen nt lds\ns_add_u32 m0, %[tb], 6144\ns_add_u32 s44, %[wo], 1703936\n" \
"buffer_load_dwordx4 v90, %[rw], s44 offen nt lds\ns_add_u32 m0, %[tb], 7168\ns_add_u32 s44, %[wo], 1769472\nbuffer_load_dwordx4 v90, %[rw], s44 offen nt lds\ns_add_u32 m0, %[tb], 8192\ns_add_u32 s44, %[wo], 2097152\nbuffer_load_dwordx4 v90, %[rw], s44 offen nt lds\ns_add_u32 m0, %[tb], 9216\n" \
"s_add_u32 s44, %[wo], 2162688\nbuffer_load_dwordx4 v90, %[rw], s44 offen nt lds\ns_add_u32 m0, %[tb], 10240\ns_add_u32 s44, %[wo], 2228224\nbuffer_load_dwordx4 v90, %[rw], s44 offen nt lds\ns_add_u32 m0, %[tb], 11264\ns_add_u32 s44, %[wo], 2293760\nbuffer_load_dwordx4 v90, %[rw], s44 offen nt lds\n" \
"s_add_u32 m0, %[lw], 0\ns_add_u32 s44, %[wo], 2621440\nbuffer_load_dwordx4 v90, %[rw], s44 offen nt lds\ns_add_u32 m0, %[lw], 1024\ns_add_u32 s44, %[wo], 2686976\nbuffer_load_dwordx4 v90, %[rw], s44 offen nt lds\ns_add_u32 m0, %[lw], 2048\ns_add_u32 s44, %[wo], 2752512\n" \
"buffer_load_dwordx4 v90, %[rw], s44 offen nt lds\ns_add_u32 m0, %[lw], 3072\ns_add_u32 s44, %[wo], 2818048\nbuffer_load_dwordx4 v90, %[rw], s44 offen nt lds\ns_add_u32 m0, %[lw], 4096\ns_add_u32 s44, %[wo], 3145728\nbuffer_load_dwordx4 v90, %[rw], s44 offen nt lds\ns_add_u32 m0, %[lw], 5120\n" \
"s_add_u32 s44, %[wo], 3211264\nbuffer_load_dwordx4 v90, %[rw], s44 offen nt lds\ns_add_u32 m0, %[lw], 6144\ns_add_u32 s44, %[wo], 3276800\nbuffer_load_dwordx4 v90, %[rw], s44 offen nt lds\ns_add_u32 m0, %[lw], 7168\ns_add_u32 s44, %[wo], 3342336\nbuffer_load_dwordx4 v90, %[rw], s44 offen nt lds\n" \
"s_add_u32 m0, %[lw], 8192\ns_add_u32 s44, %[wo], 3670016\nbuffer_load_dwordx4 v90, %[rw], s44 offen nt lds\ns_add_u32 m0, %[lw], 9216\ns_add_u32 s44, %[wo], 3735552\nbuffer_load_dwordx4 v90, %[rw], s44 offen nt lds\ns_add_u32 m0, %[lw], 10240\ns_add_u32 s44, %[wo], 3801088\n" \
"buffer_load_dwordx4 v90, %[rw], s44 offen nt lds\ns_add_u32 m0, %[lw], 11264\ns_add_u32 s44, %[wo], 3866624\nbuffer_load_dwordx4 v90, %[rw], s44 offen nt lds\ns_waitcnt vmcnt(28)\ns_barrier\ns_add_u32 m0, %[lw], 12288\ns_add_u32 s44, %[wo], 4194304\n" \
"buffer_load_dwordx4 v90, %[rw], s44 offen nt lds\ns_add_u32 m0, %[lw], 13312\ns_add_u32 s44, %[wo], 4259840\nbuffer_load_dwordx4 v90, %[rw], s44 offen nt lds\ns_add_u32 m0, %[lw], 14336\ns_add_u32 s44, %[wo], 4325376\nbuffer_load_dwordx4 v90, %[rw], s44 offen nt lds\ns_add_u32 m0, %[lw], 15360\n" \
"s_add_u32 s44, %[wo], 4390912\nbuffer_load_dwordx4 v90, %[rw], s44 offen nt lds\nds_read_b64_tr_b16 a[0:1], v93 offset:16384\nds_read_b64_tr_b16 a[2:3], v93 offset:16896\nds_read_b64_tr_b16 a[16:17], v93 offset:16392\nds_read_b64_tr_b16 a[18:19], v93 offset:16904\nds_read_b64_tr_b16 a[4:5], v93 offset:40960\nds_read_b64_tr_b16 a[6:7], v93 offset:41472\n" \
"ds_read_b64_tr_b16 a[20:21], v93 offset:40968\nds_read_b64_tr_b16 a[22:23], v93 offset:41480\ns_waitcnt lgkmcnt(0)\ns_waitcnt vmcnt(28)\ns_barrier\ns_add_u32 m0, %[lw], 16384\ns_add_u32 s44, %[wo], 4718592\nbuffer_load_dwordx4 v90, %[rw], s44 offen nt lds\n" \
"s_add_u32 m0, %[lw], 17408\ns_add_u32 s44, %[wo], 4784128\nbuffer_load_dwordx4 v90, %[rw], s44 offen nt lds\ns_add_u32 m0, %[lw], 18432\ns_add_u32 s44, %[wo], 4849664\nbuffer_load_dwordx4 v90, %[rw], s44 offen nt lds\ns_add_u32 m0, %[lw], 19456\ns_add_u32 s44, %[wo], 4915200\n" \
"buffer_load_dwordx4 v90, %[rw], s44 offen nt lds\nds_read_b64_tr_b16 a[24:25], v93 offset:20480\nds_read_b64_tr_b16 a[26:27], v93 offset:20992\nds_read_b64_tr_b16 a[40:41], v93 offset:20488\nds_read_b64_tr_b16 a[42:43], v93 offset:21000\nds_read_b64_tr_b16 a[28:29], v93 offset:45056\nds_read_b64_tr_b16 a[30:31], v93 offset:45568\nds_read_b64_tr_b16 a[44:45], v93 offset:45064\n" \
"ds_read_b64_tr_b16 a[46:47], v93 offset:45576\ns_waitcnt lgkmcnt(0)\ns_waitcnt vmcnt(28)\ns_barrier\ns_add_u32 m0, %[lw], 20480\ns_add_u32 s44, %[wo], 5242880\nbuffer_load_dwordx4 v90, %[rw], s44 offen nt lds\ns_add_u32 m0, %[lw], 21504\n" \
"s_add_u32 s44, %[wo], 5308416\nbuffer_load_dwordx4 v90, %[rw], s44 offen nt lds\ns_add_u32 m0, %[lw], 22528\ns_add_u32 s44, %[wo], 5373952\nbuffer_load_dwordx4 v90, %[rw], s44 offen nt lds\ns_add_u32 m0, %[lw], 23552\ns_add_u32 s44, %[wo], 5439488\nbuffer_load_dwordx4 v90, %[rw], s44 offen nt lds\n" \
"ds_read_b64_tr_b16 a[56:57], v92 offset:0\nds_read_b64_tr_b16 a[58:59], v92 offset:512\nds_read_b64_tr_b16 a[72:73], v92 offset:8\nds_read_b64_tr_b16 a[74:75], v92 offset:520\nds_read_b64_tr_b16 a[60:61], v92 offset:12288\nds_read_b64_tr_b16 a[62:63], v92 offset:12800\nds_read_b64_tr_b16 a[76:77], v92 offset:12296\nds_read_b64_tr_b16 a[78:79], v92 offset:12808\n" \
"s_waitcnt lgkmcnt(0)\ns_waitcnt vmcnt(28)\ns_barrier\ns_add_u32 m0, %[tb], 0\ns_add_u32 s44, %[wo], 5767168\nbuffer_load_dwordx4 v90, %[rw], s44 offen nt lds\ns_add_u32 m0, %[tb], 1024\ns_add_u32 s44, %[wo], 5832704\n" \
"buffer_load_dwordx4 v90, %[rw], s44 offen nt lds\ns_add_u32 m0, %[tb], 2048\ns_add_u32 s44, %[wo], 5898240\nbuffer_load_dwordx4 v90, %[rw], s44 offen nt lds\ns_add_u32 m0, %[tb], 3072\ns_add_u32 s44, %[wo], 5963776\nbuffer_load_dwordx4 v90, %[rw], s44 offen nt lds\nds_read_b64_tr_b16 v[84:85], v92 offset:4096\n" \
"ds_read_b64_tr_b16 v[86:87], v92 offset:4608\nds_read_b64_tr_b16 a[88:89], v92 offset:4104\nds_read_b64_tr_b16 a[90:91], v92 offset:4616\nds_read_b64_tr_b16 v[80:81], v92 offset:16384\nds_read_b64_tr_b16 v[82:83], v92 offset:16896\nds_read_b64_tr_b16 a[92:93], v92 offset:16392\nds_read_b64_tr_b16 a[94:95], v92 offset:16904\ns_waitcnt lgkmcnt(0)\n" \
"s_waitcnt vmcnt(28)\ns_barrier\ns_add_u32 m0, %[tb], 4096\ns_add_u32 s44, %[wo], 6291456\nbuffer_load_dwordx4 v90, %[rw], s44 offen nt lds\ns_add_u32 m0, %[tb], 5120\ns_add_u32 s44, %[wo], 6356992\nbuffer_load_dwordx4 v90, %[rw], s44 offen nt lds\n" \
"s_add_u32 m0, %[tb], 6144\ns_add_u32 s44, %[wo], 6422528\nbuffer_load_dwordx4 v90, %[rw], s44 offen nt lds\ns_add_u32 m0, %[tb], 7168\ns_add_u32 s44, %[wo], 6488064\nbuffer_load_dwordx4 v90, %[rw], s44 offen nt lds\nds_read_b64_tr_b16 a[104:105], v92 offset:8192\nds_read_b64_tr_b16 a[106:107], v92 offset:8704\n" \
"ds_read_b64_tr_b16 a[120:121], v92 offset:8200\nds_read_b64_tr_b16 a[122:123], v92 offset:8712\nds_read_b64_tr_b16 a[108:109], v92 offset:20480\nds_read_b64_tr_b16 a[110:111], v92 offset:20992\nds_read_b64_tr_b16 a[124:125], v92 offset:20488\nds_read_b64_tr_b16 a[126:127], v92 offset:21000\ns_waitcnt lgkmcnt(0)\ns_waitcnt vmcnt(28)\n" \
"s_barrier\ns_add_u32 m0, %[tb], 8192\ns_add_u32 s44, %[wo], 6815744\nbuffer_load_dwordx4 v90, %[rw], s44 offen nt lds\ns_add_u32 m0, %[tb], 9216\ns_add_u32 s44, %[wo], 6881280\nbuffer_load_dwordx4 v90, %[rw], s44 offen nt lds\ns_add_u32 m0, %[tb], 10240\n" \
"s_add_u32 s44, %[wo], 6946816\nbuffer_load_dwordx4 v90, %[rw], s44 offen nt lds\ns_add_u32 m0, %[tb], 11264\ns_add_u32 s44, %[wo], 7012352\nbuffer_load_dwordx4 v90, %[rw], s44 offen nt lds\nds_read_b64_tr_b16 a[128:129], v93 offset:0\nds_read_b64_tr_b16 a[130:131], v93 offset:512\nds_read_b64_tr_b16 a[144:145], v93 offset:8\n" \
"ds_read_b64_tr_b16 a[146:147], v93 offset:520\nds_read_b64_tr_b16 a[132:133], v93 offset:24576\nds_read_b64_tr_b16 a[134:135], v93 offset:25088\nds_read_b64_tr_b16 a[148:149], v93 offset:24584\nds_read_b64_tr_b16 a[150:151], v93 offset:25096\ns_waitcnt lgkmcnt(0)\ns_waitcnt vmcnt(28)\ns_barrier\n" \
"s_add_u32 m0, %[lw], 0\ns_add_u32 s44, %[wo], 7340032\nbuffer_load_dwordx4 v90, %[rw], s44 offen nt lds\ns_add_u32 m0, %[lw], 1024\ns_add_u32 s44, %[wo], 7405568\nbuffer_load_dwordx4 v90, %[rw], s44 offen nt lds\ns_add_u32 m0, %[lw], 2048\ns_add_u32 s44, %[wo], 7471104\n" \
"buffer_load_dwordx4 v90, %[rw], s44 offen nt lds\ns_add_u32 m0, %[lw], 3072\ns_add_u32 s44, %[wo], 7536640\nbuffer_load_dwordx4 v90, %[rw], s44 offen nt lds\nds_read_b64_tr_b16 a[160:161], v93 offset:4096\nds_read_b64_tr_b16 a[162:163], v93 offset:4608\nds_read_b64_tr_b16 v[76:77], v93 offset:4104\nds_read_b64_tr_b16 v[78:79], v93 offset:4616\n" \
"ds_read_b64_tr_b16 a[164:165], v93 offset:28672\nds_read_b64_tr_b16 a[166:167], v93 offset:29184\nds_read_b64_tr_b16 v[72:73], v93 offset:28680\nds_read_b64_tr_b16 v[74:75], v93 offset:29192\ns_waitcnt lgkmcnt(0)\ns_waitcnt vmcnt(28)\ns_barrier\ns_add_u32 m0, %[lw], 4096\n" \
"s_add_u32 s44, %[wo], 7864320\nbuffer_load_dwordx4 v90, %[rw], s44 offen nt lds\ns_add_u32 m0, %[lw], 5120\ns_add_u32 s44, %[wo], 7929856\nbuffer_load_dwordx4 v90, %[rw], s44 offen nt lds\ns_add_u32 m0, %[lw], 6144\ns_add_u32 s44, %[wo], 7995392\nbuffer_load_dwordx4 v90, %[rw], s44 offen nt lds\n" \
"s_add_u32 m0, %[lw], 7168\ns_add_u32 s44, %[wo], 8060928\nbuffer_load_dwordx4 v90, %[rw], s44 offen nt lds\nds_read_b64_tr_b16 a[184:185], v93 offset:8192\nds_read_b64_tr_b16 a[186:187], v93 offset:8704\nds_read_b64_tr_b16 a[192:193], v93 offset:8200\nds_read_b64_tr_b16 a[194:195], v93 offset:8712\nds_read_b64_tr_b16 a[188:189], v93 offset:32768\n" \
"ds_read_b64_tr_b16 a[190:191], v93 offset:33280\nds_read_b64_tr_b16 a[196:197], v93 offset:32776\nds_read_b64_tr_b16 a[198:199], v93 offset:33288\ns_waitcnt lgkmcnt(0)\ns_waitcnt vmcnt(28)\ns_barrier\ns_add_u32 m0, %[lw], 8192\ns_add_u32 s44, %[w2], 0\n" \
"buffer_load_dwordx4 v91, %[ro], s44 offen nt lds\ns_add_u32 m0, %[lw], 9216\ns_add_u32 s44, %[w2], 32768\nbuffer_load_dwordx4 v91, %[ro], s44 offen nt lds\ns_add_u32 m0, %[lw], 10240\ns_add_u32 s44, %[w2], 65536\nbuffer_load_dwordx4 v91, %[ro], s44 offen nt lds\ns_add_u32 m0, %[lw], 11264\n" \
"s_add_u32 s44, %[w2], 98304\nbuffer_load_dwordx4 v91, %[ro], s44 offen nt lds\nds_read_b64_tr_b16 a[208:209], v93 offset:12288\nds_read_b64_tr_b16 a[210:211], v93 offset:12800\nds_read_b64_tr_b16 a[224:225], v93 offset:12296\nds_read_b64_tr_b16 a[226:227], v93 offset:12808\nds_read_b64_tr_b16 a[212:213], v93 offset:36864\nds_read_b64_tr_b16 a[214:215], v93 offset:37376\n" \
"ds_read_b64_tr_b16 a[228:229], v93 offset:36872\nds_read_b64_tr_b16 a[230:231], v93 offset:37384\ns_waitcnt lgkmcnt(0)\ns_waitcnt vmcnt(28)\ns_barrier\ns_add_u32 m0, %[lw], 12288\ns_add_u32 s44, %[w2], 128\nbuffer_load_dwordx4 v91, %[ro], s44 offen nt lds\n" \
"s_add_u32 m0, %[lw], 13312\ns_add_u32 s44, %[w2], 32896\nbuffer_load_dwordx4 v91, %[ro], s44 offen nt lds\ns_add_u32 m0, %[lw], 14336\ns_add_u32 s44, %[w2], 65664\nbuffer_load_dwordx4 v91, %[ro], s44 offen nt lds\ns_add_u32 m0, %[lw], 15360\ns_add_u32 s44, %[w2], 98432\n" \
"buffer_load_dwordx4 v91, %[ro], s44 offen nt lds\nds_read_b64_tr_b16 v[68:69], v93 offset:16384\nds_read_b64_tr_b16 v[70:71], v93 offset:16896\nds_read_b64_tr_b16 a[240:241], v93 offset:16392\nds_read_b64_tr_b16 a[242:243], v93 offset:16904\nds_read_b64_tr_b16 v[64:65], v93 offset:40960\nds_read_b64_tr_b16 v[66:67], v93 offset:41472\nds_read_b64_tr_b16 a[244:245], v93 offset:40968\n" \
"ds_read_b64_tr_b16 a[246:247], v93 offset:41480\ns_waitcnt lgkmcnt(0)\ns_waitcnt vmcnt(28)\ns_barrier\ns_add_u32 m0, %[lw], 16384\ns_add_u32 s44, %[w2], 256\nbuffer_load_dwordx4 v91, %[ro], s44 offen nt lds\ns_add_u32 m0, %[lw], 17408\n" \
"s_add_u32 s44, %[w2], 33024\nbuffer_load_dwordx4 v91, %[ro], s44 offen nt lds\ns_add_u32 m0, %[lw], 18432\ns_add_u32 s44, %[w2], 65792\nbuffer_load_dwordx4 v91, %[ro], s44 offen nt lds\ns_add_u32 m0, %[lw], 19456\ns_add_u32 s44, %[w2], 98560\nbuffer_load_dwordx4 v91, %[ro], s44 offen nt lds\n" \
"ds_read_b64_tr_b16 v[96:97], v93 offset:20480\nds_read_b64_tr_b16 v[98:99], v93 offset:20992\nds_read_b64_tr_b16 v[112:113], v93 offset:20488\nds_read_b64_tr_b16 v[114:115], v93 offset:21000\nds_read_b64_tr_b16 v[100:101], v93 offset:45056\nds_read_b64_tr_b16 v[102:103], v93 offset:45568\nds_read_b64_tr_b16 v[116:117], v93 offset:45064\nds_read_b64_tr_b16 v[118:119], v93 offset:45576\n" \
"s_waitcnt lgkmcnt(0)\ns_waitcnt vmcnt(28)\ns_barrier\ns_add_u32 m0, %[lw], 20480\ns_add_u32 s44, %[w2], 384\nbuffer_load_dwordx4 v91, %[ro], s44 offen nt lds\ns_add_u32 m0, %[lw], 21504\ns_add_u32 s44, %[w2], 33152\n" \
"buffer_load_dwordx4 v91, %[ro], s44 offen nt lds\ns_add_u32 m0, %[lw], 22528\ns_add_u32 s44, %[w2], 65920\nbuffer_load_dwordx4 v91, %[ro], s44 offen nt lds\ns_add_u32 m0, %[lw], 23552\ns_add_u32 s44, %[w2], 98688\nbuffer_load_dwordx4 v91, %[ro], s44 offen nt lds\nds_read_b64_tr_b16 v[128:129], v92 offset:0\n" \
"ds_read_b64_tr_b16 v[130:131], v92 offset:512\nds_read_b64_tr_b16 v[136:137], v92 offset:8\nds_read_b64_tr_b16 v[138:139], v92 offset:520\nds_read_b64_tr_b16 v[132:133], v92 offset:12288\nds_read_b64_tr_b16 v[134:135], v92 offset:12800\nds_read_b64_tr_b16 v[140:141], v92 offset:12296\nds_read_b64_tr_b16 v[142:143], v92 offset:12808\ns_waitcnt lgkmcnt(0)\n" \
"s_waitcnt vmcnt(28)\ns_barrier\ns_add_u32 m0, %[tb], 0\ns_add_u32 s44, %[w2], 139264\nbuffer_load_dwordx4 v91, %[ro], s44 offen nt lds\ns_add_u32 m0, %[tb], 1024\ns_add_u32 s44, %[w2], 172032\nbuffer_load_dwordx4 v91, %[ro], s44 offen nt lds\n" \
"s_add_u32 m0, %[tb], 2048\ns_add_u32 s44, %[w2], 204800\nbuffer_load_dwordx4 v91, %[ro], s44 offen nt lds\ns_add_u32 m0, %[tb], 3072\ns_add_u32 s44, %[w2], 237568\nbuffer_load_dwordx4 v91, %[ro], s44 offen nt lds\nds_read_b64_tr_b16 v[152:153], v92 offset:4096\nds_read_b64_tr_b16 v[154:155], v92 offset:4608\n" \
"ds_read_b64_tr_b16 v[60:61], v92 offset:4104\nds_read_b64_tr_b16 v[62:63], v92 offset:4616\nds_read_b64_tr_b16 v[156:157], v92 offset:16384\nds_read_b64_tr_b16 v[158:159], v92 offset:16896\nds_read_b64_tr_b16 v[56:57], v92 offset:16392\nds_read_b64_tr_b16 v[58:59], v92 offset:16904\ns_waitcnt lgkmcnt(0)\ns_waitcnt vmcnt(28)\n" \
"s_barrier\ns_add_u32 m0, %[tb], 4096\ns_add_u32 s44, %[w2], 139392\nbuffer_load_dwordx4 v91, %[ro], s44 offen nt lds\ns_add_u32 m0, %[tb], 5120\ns_add_u32 s44, %[w2], 172160\nbuffer_load_dwordx4 v91, %[ro], s44 offen nt lds\ns_add_u32 m0, %[tb], 6144\n" \
"s_add_u32 s44, %[w2], 204928\nbuffer_load_dwordx4 v91, %[ro], s44 offen nt lds\ns_add_u32 m0, %[tb], 7168\ns_add_u32 s44, %[w2], 237696\nbuffer_load_dwordx4 v91, %[ro], s44 offen nt lds\nds_read_b64_tr_b16 v[168:169], v92 offset:8192\nds_read_b64_tr_b16 v[170:171], v92 offset:8704\nds_read_b64_tr_b16 v[184:185], v92 offset:8200\n" \
"ds_read_b64_tr_b16 v[186:187], v92 offset:8712\nds_read_b64_tr_b16 v[172:173], v92 offset:20480\nds_read_b64_tr_b16 v[174:175], v92 offset:20992\nds_read_b64_tr_b16 v[188:189], v92 offset:20488\nds_read_b64_tr_b16 v[190:191], v92 offset:21000\ns_waitcnt lgkmcnt(0)\ns_waitcnt vmcnt(28)\ns_barrier\n" \
"s_add_u32 m0, %[tb], 8192\ns_add_u32 s44, %[w2], 139520\nbuffer_load_dwordx4 v91, %[ro], s44 offen nt lds\ns_add_u32 m0, %[tb], 9216\ns_add_u32 s44, %[w2], 172288\nbuffer_load_dwordx4 v91, %[ro], s44 offen nt lds\ns_add_u32 m0, %[tb], 10240\ns_add_u32 s44, %[w2], 205056\n" \
"buffer_load_dwordx4 v91, %[ro], s44 offen nt lds\ns_add_u32 m0, %[tb], 11264\ns_add_u32 s44, %[w2], 237824\nbuffer_load_dwordx4 v91, %[ro], s44 offen nt lds\nds_read_b64_tr_b16 v[200:201], v93 offset:0\nds_read_b64_tr_b16 v[202:203], v93 offset:512\nds_read_b64_tr_b16 v[216:217], v93 offset:8\nds_read_b64_tr_b16 v[218:219], v93 offset:520\n" \
"ds_read_b64_tr_b16 v[204:205], v93 offset:24576\nds_read_b64_tr_b16 v[206:207], v93 offset:25088\nds_read_b64_tr_b16 v[220:221], v93 offset:24584\nds_read_b64_tr_b16 v[222:223], v93 offset:25096\ns_waitcnt lgkmcnt(0)\ns_waitcnt vmcnt(28)\ns_barrier\ns_add_u32 m0, %[lw], 0\n" \
"s_add_u32 s44, %[w2], 139648\nbuffer_load_dwordx4 v91, %[ro], s44 offen nt lds\ns_add_u32 m0, %[lw], 1024\ns_add_u32 s44, %[w2], 172416\nbuffer_load_dwordx4 v91, %[ro], s44 offen nt lds\ns_add_u32 m0, %[lw], 2048\ns_add_u32 s44, %[w2], 205184\nbuffer_load_dwordx4 v91, %[ro], s44 offen nt lds\n" \
"s_add_u32 m0, %[lw], 3072\ns_add_u32 s44, %[w2], 237952\nbuffer_load_dwordx4 v91, %[ro], s44 offen nt lds\nds_read_b64_tr_b16 v[232:233], v93 offset:4096\nds_read_b64_tr_b16 v[234:235], v93 offset:4608\nds_read_b64_tr_b16 v[240:241], v93 offset:4104\nds_read_b64_tr_b16 v[242:243], v93 offset:4616\nds_read_b64_tr_b16 v[236:237], v93 offset:28672\n" \
"ds_read_b64_tr_b16 v[238:239], v93 offset:29184\nds_read_b64_tr_b16 v[244:245], v93 offset:28680\nds_read_b64_tr_b16 v[246:247], v93 offset:29192\ns_waitcnt lgkmcnt(0)\ns_waitcnt vmcnt(28)\ns_barrier\ns_add_u32 m0, %[lw], 4096\ns_add_u32 s44, %[w2], 131072\n" \
"buffer_load_dwordx4 v91, %[ro], s44 offen nt lds\ns_add_u32 m0, %[lw], 5120\ns_add_u32 s44, %[w2], 163840\nbuffer_load_dwordx4 v91, %[ro], s44 offen nt lds\ns_add_u32 m0, %[lw], 6144\ns_add_u32 s44, %[w2], 196608\nbuffer_load_dwordx4 v91, %[ro], s44 offen nt lds\ns_add_u32 m0, %[lw], 7168\n" \
"s_add_u32 s44, %[w2], 229376\nbuffer_load_dwordx4 v91, %[ro], s44 offen nt lds\nds_read_b64_tr_b16 a[8:9], v95 offset:8192\nds_read_b64_tr_b16 a[10:11], v95 offset:8448\nds_read_b64_tr_b16 a[12:13], v95 offset:10240\nds_read_b64_tr_b16 a[14:15], v95 offset:10496\nds_read_b64_tr_b16 a[216:217], v95 offset:8256\nds_read_b64_tr_b16 a[218:219], v95 offset:8512\n" \
"ds_read_b64_tr_b16 a[220:221], v95 offset:10304\nds_read_b64_tr_b16 a[222:223], v95 offset:10560\ns_waitcnt lgkmcnt(0)\ns_waitcnt vmcnt(28)\ns_barrier\ns_add_u32 m0, %[lw], 8192\ns_add_u32 s44, %[w2], 131200\nbuffer_load_dwordx4 v91, %[ro], s44 offen nt lds\n" \
"s_add_u32 m0, %[lw], 9216\ns_add_u32 s44, %[w2], 163968\nbuffer_load_dwordx4 v91, %[ro], s44 offen nt lds\ns_add_u32 m0, %[lw], 10240\ns_add_u32 s44, %[w2], 196736\nbuffer_load_dwordx4 v91, %[ro], s44 offen nt lds\ns_add_u32 m0, %[lw], 11264\ns_add_u32 s44, %[w2], 229504\n" \
"buffer_load_dwordx4 v91, %[ro], s44 offen nt lds\nds_read_b64_tr_b16 a[64:65], v95 offset:12288\nds_read_b64_tr_b16 a[66:67], v95 offset:12544\nds_read_b64_tr_b16 a[68:69], v95 offset:14336\nds_read_b64_tr_b16 a[70:71], v95 offset:14592\nds_read_b64_tr_b16 v[104:105], v95 offset:12352\nds_read_b64_tr_b16 v[106:107], v95 offset:12608\nds_read_b64_tr_b16 v[108:109], v95 offset:14400\n" \
"ds_read_b64_tr_b16 v[110:111], v95 offset:14656\ns_waitcnt lgkmcnt(0)\ns_waitcnt vmcnt(28)\ns_barrier\ns_add_u32 m0, %[lw], 12288\ns_add_u32 s44, %[w2], 131328\nbuffer_load_dwordx4 v91, %[ro], s44 offen nt lds\ns_add_u32 m0, %[lw], 13312\n" \
"s_add_u32 s44, %[w2], 164096\nbuffer_load_dwordx4 v91, %[ro], s44 offen nt lds\ns_add_u32 m0, %[lw], 14336\ns_add_u32 s44, %[w2], 196864\nbuffer_load_dwordx4 v91, %[ro], s44 offen nt lds\ns_add_u32 m0, %[lw], 15360\ns_add_u32 s44, %[w2], 229632\nbuffer_load_dwordx4 v91, %[ro], s44 offen nt lds\n" \
"ds_read_b64_tr_b16 a[112:113], v95 offset:16384\nds_read_b64_tr_b16 a[114:115], v95 offset:16640\nds_read_b64_tr_b16 a[116:117], v95 offset:18432\nds_read_b64_tr_b16 a[118:119], v95 offset:18688\nds_read_b64_tr_b16 v[160:161], v95 offset:16448\nds_read_b64_tr_b16 v[162:163], v95 offset:16704\nds_read_b64_tr_b16 v[164:165], v95 offset:18496\nds_read_b64_tr_b16 v[166:167], v95 offset:18752\n" \
"s_waitcnt lgkmcnt(0)\ns_waitcnt vmcnt(28)\ns_barrier\ns_add_u32 m0, %[lw], 16384\ns_add_u32 s44, %[w2], 131456\nbuffer_load_dwordx4 v91, %[ro], s44 offen nt lds\ns_add_u32 m0, %[lw], 17408\ns_add_u32 s44, %[w2], 164224\n" \
"buffer_load_dwordx4 v91, %[ro], s44 offen nt lds\ns_add_u32 m0, %[lw], 18432\ns_add_u32 s44, %[w2], 196992\nbuffer_load_dwordx4 v91, %[ro], s44 offen nt lds\ns_add_u32 m0, %[lw], 19456\ns_add_u32 s44, %[w2], 229760\nbuffer_load_dwordx4 v91, %[ro], s44 offen nt lds\nds_read_b64_tr_b16 a[168:169], v95 offset:20480\n" \
"ds_read_b64_tr_b16 a[170:171], v95 offset:20736\nds_read_b64_tr_b16 a[172:173], v95 offset:22528\nds_read_b64_tr_b16 a[174:175], v95 offset:22784\nds_read_b64_tr_b16 v[208:209], v95 offset:20544\nds_read_b64_tr_b16 v[210:211], v95 offset:20800\nds_read_b64_tr_b16 v[212:213], v95 offset:22592\nds_read_b64_tr_b16 v[214:215], v95 offset:22848\ns_waitcnt lgkmcnt(0)\n" \
"s_waitcnt vmcnt(28)\ns_barrier\ns_add_u32 m0, %[lw], 20480\ns_add_u32 s44, %[w2], 8192\nbuffer_load_dwordx4 v91, %[ro], s44 offen nt lds\ns_add_u32 m0, %[lw], 21504\ns_add_u32 s44, %[w2], 40960\nbuffer_load_dwordx4 v91, %[ro], s44 offen nt lds\n" \
"s_add_u32 m0, %[lw], 22528\ns_add_u32 s44, %[w2], 73728\nbuffer_load_dwordx4 v91, %[ro], s44 offen nt lds\ns_add_u32 m0, %[lw], 23552\ns_add_u32 s44, %[w2], 106496\nbuffer_load_dwordx4 v91, %[ro], s44 offen nt lds\nds_read_b64_tr_b16 a[48:49], v94 offset:0\nds_read_b64_tr_b16 a[50:51], v94 offset:256\n" \
"ds_read_b64_tr_b16 a[52:53], v94 offset:2048\nds_read_b64_tr_b16 a[54:55], v94 offset:2304\nds_read_b64_tr_b16 a[248:249], v94 offset:64\nds_read_b64_tr_b16 a[250:251], v94 offset:320\nds_read_b64_tr_b16 a[252:253], v94 offset:2112\nds_read_b64_tr_b16 a[254:255], v94 offset:2368\ns_waitcnt lgkmcnt(0)\ns_waitcnt vmcnt(28)\n" \
"s_barrier\ns_add_u32 m0, %[tb], 0\ns_add_u32 s44, %[w2], 8320\nbuffer_load_dwordx4 v91, %[ro], s44 offen nt lds\ns_add_u32 m0, %[tb], 1024\ns_add_u32 s44, %[w2], 41088\nbuffer_load_dwordx4 v91, %[ro], s44 offen nt lds\ns_add_u32 m0, %[tb], 2048\n" \
"s_add_u32 s44, %[w2], 73856\nbuffer_load_dwordx4 v91, %[ro], s44 offen nt lds\ns_add_u32 m0, %[tb], 3072\ns_add_u32 s44, %[w2], 106624\nbuffer_load_dwordx4 v91, %[ro], s44 offen nt lds\nds_read_b64_tr_b16 a[96:97], v94 offset:4096\nds_read_b64_tr_b16 a[98:99], v94 offset:4352\nds_read_b64_tr_b16 a[100:101], v94 offset:6144\n" \
"ds_read_b64_tr_b16 a[102:103], v94 offset:6400\nds_read_b64_tr_b16 v[144:145], v94 offset:4160\nds_read_b64_tr_b16 v[146:147], v94 offset:4416\nds_read_b64_tr_b16 v[148:149], v94 offset:6208\nds_read_b64_tr_b16 v[150:151], v94 offset:6464\ns_waitcnt lgkmcnt(0)\nbuffer_load_dwordx2 v[12:13], off, s[36:39], s45 sc1\ns_waitcnt vmcnt(29)\n" \
"s_barrier\ns_add_u32 m0, %[tb], 4096\ns_add_u32 s44, %[w2], 8448\nbuffer_load_dwordx4 v91, %[ro], s44 offen nt lds\ns_add_u32 m0, %[tb], 5120\ns_add_u32 s44, %[w2], 41216\nbuffer_load_dwordx4 v91, %[ro], s44 offen nt lds\ns_add_u32 m0, %[tb], 6144\n" \
"s_add_u32 s44, %[w2], 73984\nbuffer_load_dwordx4 v91, %[ro], s44 offen nt lds\ns_add_u32 m0, %[tb], 7168\ns_add_u32 s44, %[w2], 106752\nbuffer_load_dwordx4 v91, %[ro], s44 offen nt lds\nds_read_b64_tr_b16 a[152:153], v94 offset:8192\nds_read_b64_tr_b16 a[154:155], v94 offset:8448\nds_read_b64_tr_b16 a[156:157], v94 offset:10240\n" \
"ds_read_b64_tr_b16 a[158:159], v94 offset:10496\nds_read_b64_tr_b16 v[192:193], v94 offset:8256\nds_read_b64_tr_b16 v[194:195], v94 offset:8512\nds_read_b64_tr_b16 v[196:197], v94 offset:10304\nds_read_b64_tr_b16 v[198:199], v94 offset:10560\ns_waitcnt lgkmcnt(0)\ns_waitcnt vmcnt(29)\ns_barrier\n" \
"s_add_u32 m0, %[tb], 8192\ns_add_u32 s44, %[w2], 8576\nbuffer_load_dwordx4 v91, %[ro], s44 offen nt lds\ns_add_u32 m0, %[tb], 9216\ns_add_u32 s44, %[w2], 41344\nbuffer_load_dwordx4 v91, %[ro], s44 offen nt lds\ns_add_u32 m0, %[tb], 10240\ns_add_u32 s44, %[w2], 74112\n" \
"buffer_load_dwordx4 v91, %[ro], s44 offen nt lds\ns_add_u32 m0, %[tb], 11264\ns_add_u32 s44, %[w2], 106880\nbuffer_load_dwordx4 v91, %[ro], s44 offen nt lds\nds_read_b64_tr_b16 a[200:201], v95 offset:0\nds_read_b64_tr_b16 a[202:203], v95 offset:256\nds_read_b64_tr_b16 a[204:205], v95 offset:2048\nds_read_b64_tr_b16 a[206:207], v95 offset:2304\n" \
"ds_read_b64_tr_b16 v[248:249], v95 offset:64\nds_read_b64_tr_b16 v[250:251], v95 offset:320\nds_read_b64_tr_b16 v[252:253], v95 offset:2112\nds_read_b64_tr_b16 v[254:255], v95 offset:2368\ns_waitcnt lgkmcnt(0)\ns_waitcnt vmcnt(29)\ns_barrier\nds_read_b64_tr_b16 v[52:53], v95 offset:4096\n" \
"ds_read_b64_tr_b16 v[54:55], v95 offset:4352\nds_read_b64_tr_b16 v[48:49], v95 offset:6144\nds_read_b64_tr_b16 v[50:51], v95 offset:6400\nds_read_b64_tr_b16 v[44:45], v95 offset:4160\nds_read_b64_tr_b16 v[46:47], v95 offset:4416\nds_read_b64_tr_b16 v[40:41], v95 offset:6208\nds_read_b64_tr_b16 v[42:43], v95 offset:6464\ns_waitcnt lgkmcnt(0)\n" \
"ds_write_b128 v1, v[84:87] offset:0\nds_write_b128 v1, v[80:83] offset:1024\nds_write_b128 v1, v[76:79] offset:2048\nds_write_b128 v1, v[72:75] offset:3072\ns_waitcnt vmcnt(25)\ns_barrier\nds_read_b64_tr_b16 a[80:81], v95 offset:8192\nds_read_b64_tr_b16 a[82:83], v95 offset:8448\n" \
"ds_read_b64_tr_b16 a[84:85], v95 offset:10240\nds_read_b64_tr_b16 a[86:87], v95 offset:10496\nds_read_b64_tr_b16 v[120:121], v95 offset:8256\nds_read_b64_tr_b16 v[122:123], v95 offset:8512\nds_read_b64_tr_b16 v[124:125], v95 offset:10304\nds_read_b64_tr_b16 v[126:127], v95 offset:10560\ns_waitcnt lgkmcnt(0)\nds_write_b128 v1, v[68:71] offset:4096\n" \
"ds_write_b128 v1, v[64:67] offset:5120\nds_write_b128 v1, v[60:63] offset:6144\nds_write_b128 v1, v[56:59] offset:7168\ns_waitcnt vmcnt(21)\ns_barrier\nds_read_b64_tr_b16 v[56:57], v95 offset:12288\nds_read_b64_tr_b16 v[58:59], v95 offset:12544\nds_read_b64_tr_b16 v[60:61], v95 offset:14336\n" \
"ds_read_b64_tr_b16 v[62:63], v95 offset:14592\nds_read_b64_tr_b16 v[64:65], v95 offset:12352\nds_read_b64_tr_b16 v[66:67], v95 offset:12608\nds_read_b64_tr_b16 v[68:69], v95 offset:14400\nds_read_b64_tr_b16 v[70:71], v95 offset:14656\ns_waitcnt lgkmcnt(0)\nds_write_b128 v1, v[52:55] offset:8192\nds_write_b128 v1, v[48:51] offset:9216\n" \
"ds_write_b128 v1, v[44:47] offset:10240\nds_write_b128 v1, v[40:43] offset:11264\ns_waitcnt vmcnt(17)\ns_barrier\nds_read_b64_tr_b16 a[176:177], v95 offset:16384\nds_read_b64_tr_b16 a[178:179], v95 offset:16640\nds_read_b64_tr_b16 a[180:181], v95 offset:18432\nds_read_b64_tr_b16 a[182:183], v95 offset:18688\n" \
"ds_read_b64_tr_b16 v[224:225], v95 offset:16448\nds_read_b64_tr_b16 v[226:227], v95 offset:16704\nds_read_b64_tr_b16 v[228:229], v95 offset:18496\nds_read_b64_tr_b16 v[230:231], v95 offset:18752\ns_waitcnt lgkmcnt(0)\nds_write_b128 v1, v[56:59] offset:12288\nds_write_b128 v1, v[60:63] offset:13312\nds_write_b128 v1, v[64:67] offset:14336\n" \
"ds_write_b128 v1, v[68:71] offset:15360\ns_waitcnt vmcnt(13)\ns_barrier\nds_read_b64_tr_b16 a[32:33], v95 offset:20480\nds_read_b64_tr_b16 a[34:35], v95 offset:20736\nds_read_b64_tr_b16 a[36:37], v95 offset:22528\nds_read_b64_tr_b16 a[38:39], v95 offset:22784\nds_read_b64_tr_b16 a[232:233], v95 offset:20544\n" \
"ds_read_b64_tr_b16 a[234:235], v95 offset:20800\nds_read_b64_tr_b16 a[236:237], v95 offset:22592\nds_read_b64_tr_b16 a[238:239], v95 offset:22848\ns_waitcnt lgkmcnt(0)\ns_waitcnt vmcnt(9)\ns_barrier\nds_read_b64_tr_b16 v[68:69], v94 offset:0\nds_read_b64_tr_b16 v[70:71], v94 offset:256\n" \
"ds_read_b64_tr_b16 v[64:65], v94 offset:2048\nds_read_b64_tr_b16 v[66:67], v94 offset:2304\nds_read_b64_tr_b16 v[60:61], v94 offset:64\nds_read_b64_tr_b16 v[62:63], v94 offset:320\nds_read_b64_tr_b16 v[56:57], v94 offset:2112\nds_read_b64_tr_b16 v[58:59], v94 offset:2368\ns_waitcnt lgkmcnt(0)\nds_write_b128 v1, v[68:71] offset:16384\n" \
"ds_write_b128 v1, v[64:67] offset:17408\nds_write_b128 v1, v[60:63] offset:18432\nds_write_b128 v1, v[56:59] offset:19456\ns_waitcnt vmcnt(4)\ns_barrier\nds_read_b64_tr_b16 a[136:137], v94 offset:4096\nds_read_b64_tr_b16 a[138:139], v94 offset:4352\nds_read_b64_tr_b16 a[140:141], v94 offset:6144\n" \
"ds_read_b64_tr_b16 a[142:143], v94 offset:6400\nds_read_b64_tr_b16 v[176:177], v94 offset:4160\nds_read_b64_tr_b16 v[178:179], v94 offset:4416\nds_read_b64_tr_b16 v[180:181], v94 offset:6208\nds_read_b64_tr_b16 v[182:183], v94 offset:6464\ns_waitcnt lgkmcnt(0)\ns_waitcnt vmcnt(0)\ns_barrier\n" \
"ds_read_b64_tr_b16 v[56:57], v94 offset:8192\nds_read_b64_tr_b16 v[58:59], v94 offset:8448\nds_read_b64_tr_b16 v[60:61], v94 offset:10240\nds_read_b64_tr_b16 v[62:63], v94 offset:10496\nds_read_b64_tr_b16 v[64:65], v94 offset:8256\nds_read_b64_tr_b16 v[66:67], v94 offset:8512\nds_read_b64_tr_b16 v[68:69], v94 offset:10304\nds_read_b64_tr_b16 v[70:71], v94 offset:10560\n" \
"s_waitcnt lgkmcnt(0)\ns_mov_b32 s43, 0\ns_mov_b32 s86, 110\ns_mov_b32 s89, 0\n.Lvu_hb%=:\nv_cmp_ne_u32 vcc, v15, v12\ns_cbranch_vccnz .Lvu_hb1%=\ns_sleep 4\n" \
"buffer_load_dwordx2 v[12:13], off, s[36:39], s45 sc1\ns_waitcnt vmcnt(0)\ns_add_u32 s43, s43, 1\ns_cmp_lt_u32 s43, 262144\ns_cbranch_scc1 .Lvu_hb%=\ns_branch .Lvu_abort%=\n.Lvu_hb1%=:\nv_readfirstlane_b32 s44, v13\n" \
"s_and_b32 s44, s44, 0x200000\ns_add_u32 s36, s36, s44\ns_addc_u32 s37, s37, 0\nds_write_b128 v1, v[56:59] offset:20480\nds_write_b128 v1, v[60:63] offset:21504\nds_write_b128 v1, v[64:67] offset:22528\nds_write_b128 v1, v[68:71] offset:23552\ns_waitcnt lgkmcnt(0)\n" \
"s_barrier\ns_mov_b32 s40, -1\ns_mov_b32 s43, 0\ns_lshl_b32 s57, %[w], 12\ns_mov_b32 s56, 0\ns_mov_b32 s42, 0\ns_mov_b32 s60, -2147483648\ns_mov_b32 s54, -2147483648\n" \
"s_add_u32 s44, s40, 1\ns_lshl_b32 s44, s44, 17\ns_add_u32 s44, s44, %[xn]\ns_lshr_b32 s45, s44, 8\ns_and_b32 s45, s45, 0x2000\ns_xor_b32 s41, s44, s45\ns_add_u32 s41, s41, %[xw]\n.Lvu_f0%=:\n" \
"buffer_load_dword v14, v8, s[36:39], s56 offen sc1\ns_waitcnt vmcnt(0)\nv_cmp_ge_i32 vcc, s40, v14\ns_cbranch_vccz .Lvu_f0fok%=\ns_add_u32 s43, s43, 1\ns_cmp_lt_u32 s43, 262144\ns_cbranch_scc1 .Lvu_f0%=\ns_branch .Lvu_abort%=\n" \
".Lvu_f0fok%=:\ns_add_u32 m0, s57, 0\ns_add_u32 s58, s41, 0\nbuffer_load_dwordx4 v0, s[36:39], s58 offen offset:0 sc1 lds\nbuffer_load_dwordx4 v0, s[36:39], s58 offen offset:1024 sc1 lds\nbuffer_load_dwordx4 v0, s[36:39], s58 offen offset:2048 sc1 lds\nbuffer_load_dwordx4 v0, s[36:39], s58 offen offset:3072 sc1 lds\ns_add_u32 m0, s57, 32768\n" \
"s_add_u32 s58, s41, 32768\nbuffer_load_dwordx4 v0, s[36:39], s58 offen offset:0 sc1 lds\nbuffer_load_dwordx4 v0, s[36:39], s58 offen offset:1024 sc1 lds\nbuffer_load_dwordx4 v0, s[36:39], s58 offen offset:2048 sc1 lds\nbuffer_load_dwordx4 v0, s[36:39], s58 offen offset:3072 sc1 lds\ns_waitcnt vmcnt(0)\ns_barrier\nds_read_b128 v[32:35], v2 offset:0\n" \
"ds_read_b128 v[36:39], v2 offset:256\nds_read_b128 v[40:43], v2 offset:2048\nds_read_b128 v[44:47], v2 offset:2304\n.Lvu_tok%=:\nds_read_b128 v[64:67], v2 offset:4096\nds_read_b128 v[68:71], v2 offset:4352\ns_waitcnt lgkmcnt(5)\nv_mfma_f32_16x16x32_bf16 v[16:19], a[0:3], v[32:35], 0\n" \
"v_cvt_pk_bf16_f32 v92, v80, v81\nv_cvt_pk_bf16_f32 v93, v82, v83\nv_mfma_f32_16x16x32_bf16 v[20:23], a[16:19], v[32:35], 0\ns_waitcnt lgkmcnt(4)\nds_read_b128 v[32:35], v2 offset:6144\nv_cvt_pk_bf16_f32 v94, v84, v85\nv_cvt_pk_bf16_f32 v95, v86, v87\nv_mfma_f32_16x16x32_bf16 v[24:27], a[0:3], v[36:39], 0\n" \
"v_and_or_b32 v92, v92, -2, v13\nv_and_or_b32 v94, v94, -2, v13\nv_mfma_f32_16x16x32_bf16 v[28:31], a[16:19], v[36:39], 0\ns_waitcnt lgkmcnt(4)\nbuffer_store_dwordx4 v[92:95], v7, s[36:39], s60 offen offset:2560 sc1\nds_read_b128 v[36:39], v2 offset:6400\nv_mfma_f32_16x16x32_bf16 v[16:19], a[4:7], v[40:43], v[16:19]\nv_mfma_f32_16x16x32_bf16 v[20:23], a[20:23], v[40:43], v[20:23]\n" \
"s_waitcnt lgkmcnt(4)\ns_add_u32 m0, s57, 16384\nds_read_b128 v[40:43], v2 offset:8192\nv_mfma_f32_16x16x32_bf16 v[24:27], a[4:7], v[44:47], v[24:27]\ns_add_u32 s60, s41, 65536\nbuffer_load_dwordx4 v0, s[36:39], s60 offen offset:0 sc1 lds\nv_mfma_f32_16x16x32_bf16 v[28:31], a[20:23], v[44:47], v[28:31]\ns_waitcnt lgkmcnt(4)\n" \
"ds_read_b128 v[44:47], v2 offset:8448\nv_mfma_f32_16x16x32_bf16 v[16:19], a[24:27], v[64:67], v[16:19]\nds_read_b128 v[56:59], v1 offset:0\nds_read_b128 v[60:63], v1 offset:1024\nv_mfma_f32_16x16x32_bf16 v[20:23], a[40:43], v[64:67], v[20:23]\ns_waitcnt lgkmcnt(6)\nds_read_b128 v[64:67], v2 offset:10240\nv_mfma_f32_16x16x32_bf16 v[24:27], a[24:27], v[68:71], v[24:27]\n" \
"buffer_load_dwordx4 v0, s[36:39], s60 offen offset:1024 sc1 lds\nv_mfma_f32_16x16x32_bf16 v[28:31], a[40:43], v[68:71], v[28:31]\ns_waitcnt lgkmcnt(6)\nds_read_b128 v[68:71], v2 offset:10496\nv_mfma_f32_16x16x32_bf16 v[16:19], a[28:31], v[32:35], v[16:19]\nv_mfma_f32_16x16x32_bf16 v[20:23], a[44:47], v[32:35], v[20:23]\ns_waitcnt lgkmcnt(6)\nds_read_b128 v[32:35], v2 offset:12288\n" \
"v_mfma_f32_16x16x32_bf16 v[24:27], a[28:31], v[36:39], v[24:27]\nbuffer_load_dwordx4 v0, s[36:39], s60 offen offset:2048 sc1 lds\nv_mfma_f32_16x16x32_bf16 v[28:31], a[44:47], v[36:39], v[28:31]\ns_waitcnt lgkmcnt(6)\nds_read_b128 v[36:39], v2 offset:12544\nv_mfma_f32_16x16x32_bf16 v[16:19], a[56:59], v[40:43], v[16:19]\nv_mfma_f32_16x16x32_bf16 v[20:23], a[72:75], v[40:43], v[20:23]\ns_waitcnt lgkmcnt(6)\n" \
"ds_read_b128 v[40:43], v2 offset:14336\nv_mfma_f32_16x16x32_bf16 v[24:27], a[56:59], v[44:47], v[24:27]\nbuffer_load_dwordx4 v0, s[36:39], s60 offen offset:3072 sc1 lds\nv_mfma_f32_16x16x32_bf16 v[28:31], a[72:75], v[44:47], v[28:31]\ns_waitcnt lgkmcnt(4)\nds_read_b128 v[44:47], v2 offset:14592\nv_mfma_f32_16x16x32_bf16 v[16:19], a[60:63], v[64:67], v[16:19]\ns_add_u32 s56, s56, %[fs]\n" \
"s_add_u32 s62, s40, 1\ns_lshr_b32 m0, s57, 4\nv_mfma_f32_16x16x32_bf16 v[20:23], a[76:79], v[64:67], v[20:23]\ns_add_u32 m0, m0, 159744\ns_waitcnt lgkmcnt(4)\nds_read_b128 v[64:67], v2 offset:32768\nv_mfma_f32_16x16x32_bf16 v[24:27], a[60:63], v[68:71], v[24:27]\nbuffer_load_dword v8, s[36:39], s56 offen sc1 lds\n" \
"v_lshrrev_b32 v14, 2, v0\nv_mfma_f32_16x16x32_bf16 v[28:31], a[76:79], v[68:71], v[28:31]\ns_waitcnt lgkmcnt(4)\nds_read_b128 v[68:71], v2 offset:33024\nv_mfma_f32_16x16x32_bf16 v[16:19], v[56:59], v[32:35], v[16:19]\nv_add_u32 v14, 159744, v14\nv_mfma_f32_16x16x32_bf16 v[20:23], a[88:91], v[32:35], v[20:23]\ns_waitcnt lgkmcnt(4)\n" \
"ds_read_b128 v[32:35], v2 offset:34816\nv_mfma_f32_16x16x32_bf16 v[24:27], v[56:59], v[36:39], v[24:27]\nv_mfma_f32_16x16x32_bf16 v[28:31], a[88:91], v[36:39], v[28:31]\ns_waitcnt lgkmcnt(4)\nds_read_b128 v[36:39], v2 offset:35072\nv_mfma_f32_16x16x32_bf16 v[16:19], v[60:63], v[40:43], v[16:19]\nv_mfma_f32_16x16x32_bf16 v[20:23], a[92:95], v[40:43], v[20:23]\ns_waitcnt lgkmcnt(4)\n" \
"ds_read_b128 v[40:43], v2 offset:36864\nv_mfma_f32_16x16x32_bf16 v[24:27], v[60:63], v[44:47], v[24:27]\nv_mfma_f32_16x16x32_bf16 v[28:31], a[92:95], v[44:47], v[28:31]\ns_waitcnt lgkmcnt(4)\nds_read_b128 v[44:47], v2 offset:37120\nv_mfma_f32_16x16x32_bf16 v[16:19], a[104:107], v[64:67], v[16:19]\nds_read_b128 v[56:59], v1 offset:2048\nds_read_b128 v[60:63], v1 offset:3072\n" \
"v_mfma_f32_16x16x32_bf16 v[20:23], a[120:123], v[64:67], v[20:23]\ns_waitcnt lgkmcnt(6)\nds_read_b128 v[64:67], v2 offset:38912\nv_mfma_f32_16x16x32_bf16 v[24:27], a[104:107], v[68:71], v[24:27]\ns_barrier\nv_mfma_f32_16x16x32_bf16 v[28:31], a[120:123], v[68:71], v[28:31]\ns_waitcnt lgkmcnt(6)\ns_add_u32 m0, s57, 0\n" \
"ds_read_b128 v[68:71], v2 offset:39168\nv_mfma_f32_16x16x32_bf16 v[16:19], a[108:111], v[32:35], v[16:19]\ns_add_u32 s60, s41, 98304\nbuffer_load_dwordx4 v0, s[36:39], s60 offen offset:0 sc1 lds\nv_mfma_f32_16x16x32_bf16 v[20:23], a[124:127], v[32:35], v[20:23]\ns_waitcnt lgkmcnt(6)\nds_read_b128 v[32:35], v2 offset:40960\nv_mfma_f32_16x16x32_bf16 v[24:27], a[108:111], v[36:39], v[24:27]\n" \
"v_mfma_f32_16x16x32_bf16 v[28:31], a[124:127], v[36:39], v[28:31]\ns_waitcnt lgkmcnt(6)\nds_read_b128 v[36:39], v2 offset:41216\nv_mfma_f32_16x16x32_bf16 v[16:19], a[128:131], v[40:43], v[16:19]\nbuffer_load_dwordx4 v0, s[36:39], s60 offen offset:1024 sc1 lds\nv_mfma_f32_16x16x32_bf16 v[20:23], a[144:147], v[40:43], v[20:23]\ns_waitcnt lgkmcnt(6)\nds_read_b128 v[40:43], v2 offset:43008\n" \
"v_mfma_f32_16x16x32_bf16 v[24:27], a[128:131], v[44:47], v[24:27]\nv_mfma_f32_16x16x32_bf16 v[28:31], a[144:147], v[44:47], v[28:31]\ns_waitcnt lgkmcnt(4)\nds_read_b128 v[44:47], v2 offset:43264\nv_mfma_f32_16x16x32_bf16 v[16:19], a[132:135], v[64:67], v[16:19]\nbuffer_load_dwordx4 v0, s[36:39], s60 offen offset:2048 sc1 lds\nv_mfma_f32_16x16x32_bf16 v[20:23], a[148:151], v[64:67], v[20:23]\ns_waitcnt lgkmcnt(4)\n" \
"ds_read_b128 v[64:67], v2 offset:45056\nv_mfma_f32_16x16x32_bf16 v[24:27], a[132:135], v[68:71], v[24:27]\nv_mfma_f32_16x16x32_bf16 v[28:31], a[148:151], v[68:71], v[28:31]\ns_waitcnt lgkmcnt(4)\nds_read_b128 v[68:71], v2 offset:45312\nv_mfma_f32_16x16x32_bf16 v[16:19], a[160:163], v[32:35], v[16:19]\nbuffer_load_dwordx4 v0, s[36:39], s60 offen offset:3072 sc1 lds\nv_mfma_f32_16x16x32_bf16 v[20:23], v[56:59], v[32:35], v[20:23]\n" \
"s_waitcnt lgkmcnt(4)\nds_read_b128 v[32:35], v2 offset:47104\nv_mfma_f32_16x16x32_bf16 v[24:27], a[160:163], v[36:39], v[24:27]\ns_add_u32 s44, s40, 2\ns_lshl_b32 s44, s44, 17\nv_mfma_f32_16x16x32_bf16 v[28:31], v[56:59], v[36:39], v[28:31]\ns_waitcnt lgkmcnt(4)\nds_read_b128 v[36:39], v2 offset:47360\n" \
"v_mfma_f32_16x16x32_bf16 v[16:19], a[164:167], v[40:43], v[16:19]\ns_add_u32 s44, s44, %[xn]\ns_lshr_b32 s45, s44, 8\nv_mfma_f32_16x16x32_bf16 v[20:23], v[60:63], v[40:43], v[20:23]\ns_waitcnt lgkmcnt(4)\nv_mfma_f32_16x16x32_bf16 v[24:27], a[164:167], v[44:47], v[24:27]\ns_and_b32 s45, s45, 0x2000\ns_xor_b32 s41, s44, s45\n" \
"v_mfma_f32_16x16x32_bf16 v[28:31], v[60:63], v[44:47], v[28:31]\ns_waitcnt lgkmcnt(0)\ns_waitcnt vmcnt(4)\ns_barrier\nds_read_b128 v[40:43], v2 offset:16384\nds_read_b128 v[44:47], v2 offset:16640\nv_mfma_f32_16x16x32_bf16 v[16:19], a[184:187], v[64:67], v[16:19]\nds_read_b32 v14, v14\n" \
"v_mfma_f32_16x16x32_bf16 v[20:23], a[192:195], v[64:67], v[20:23]\nds_read_b128 v[64:67], v2 offset:18432\nv_mfma_f32_16x16x32_bf16 v[24:27], a[184:187], v[68:71], v[24:27]\ns_add_u32 s41, s41, %[xw]\nv_mfma_f32_16x16x32_bf16 v[28:31], a[192:195], v[68:71], v[28:31]\nds_read_b128 v[68:71], v2 offset:18688\nv_mfma_f32_16x16x32_bf16 v[16:19], a[188:191], v[32:35], v[16:19]\nds_read_b128 v[56:59], v1 offset:4096\n" \
"ds_read_b128 v[60:63], v1 offset:5120\nv_mfma_f32_16x16x32_bf16 v[20:23], a[196:199], v[32:35], v[20:23]\nds_read_b128 v[32:35], v2 offset:20480\nv_mfma_f32_16x16x32_bf16 v[24:27], a[188:191], v[36:39], v[24:27]\ns_waitcnt lgkmcnt(5)\nv_cmp_ge_i32 s[64:65], s62, v14\nv_mfma_f32_16x16x32_bf16 v[28:31], a[196:199], v[36:39], v[28:31]\ns_cmp_eq_u64 s[64:65], 0\n" \
"s_cselect_b32 s61, s41, -2147483648\nds_read_b128 v[36:39], v2 offset:20736\nv_mfma_f32_16x16x32_bf16 v[16:19], a[208:211], v[40:43], v[16:19]\ns_waitcnt vmcnt(4)\nbuffer_store_dword v15, off, s[36:39], s54 sc1\nv_mfma_f32_16x16x32_bf16 v[20:23], a[224:227], v[40:43], v[20:23]\ns_add_u32 m0, s57, 32768\nds_read_b128 v[40:43], v2 offset:22528\n" \
"v_mfma_f32_16x16x32_bf16 v[24:27], a[208:211], v[44:47], v[24:27]\ns_add_u32 s58, s61, 32768\nbuffer_load_dwordx4 v0, s[36:39], s58 offen offset:0 sc1 lds\nv_mfma_f32_16x16x32_bf16 v[28:31], a[224:227], v[44:47], v[28:31]\ns_waitcnt lgkmcnt(6)\nds_read_b128 v[44:47], v2 offset:22784\nv_mfma_f32_16x16x32_bf16 v[16:19], a[212:215], v[64:67], v[16:19]\nv_mfma_f32_16x16x32_bf16 v[20:23], a[228:231], v[64:67], v[20:23]\n" \
"s_waitcnt lgkmcnt(6)\nds_read_b128 v[64:67], v2 offset:24576\nv_mfma_f32_16x16x32_bf16 v[24:27], a[212:215], v[68:71], v[24:27]\nbuffer_load_dwordx4 v0, s[36:39], s58 offen offset:1024 sc1 lds\nv_mfma_f32_16x16x32_bf16 v[28:31], a[228:231], v[68:71], v[28:31]\ns_waitcnt lgkmcnt(4)\nds_read_b128 v[68:71], v2 offset:24832\nv_mfma_f32_16x16x32_bf16 v[16:19], v[56:59], v[32:35], v[16:19]\n" \
"v_mfma_f32_16x16x32_bf16 v[20:23], a[240:243], v[32:35], v[20:23]\ns_waitcnt lgkmcnt(4)\nds_read_b128 v[32:35], v2 offset:26624\nv_mfma_f32_16x16x32_bf16 v[24:27], v[56:59], v[36:39], v[24:27]\nbuffer_load_dwordx4 v0, s[36:39], s58 offen offset:2048 sc1 lds\nv_mfma_f32_16x16x32_bf16 v[28:31], a[240:243], v[36:39], v[28:31]\ns_waitcnt lgkmcnt(4)\nds_read_b128 v[36:39], v2 offset:26880\n" \
"v_mfma_f32_16x16x32_bf16 v[16:19], v[60:63], v[40:43], v[16:19]\nv_mfma_f32_16x16x32_bf16 v[20:23], a[244:247], v[40:43], v[20:23]\ns_waitcnt lgkmcnt(4)\nds_read_b128 v[40:43], v2 offset:28672\nv_mfma_f32_16x16x32_bf16 v[24:27], v[60:63], v[44:47], v[24:27]\nbuffer_load_dwordx4 v0, s[36:39], s58 offen offset:3072 sc1 lds\nv_mfma_f32_16x16x32_bf16 v[28:31], a[244:247], v[44:47], v[28:31]\ns_waitcnt lgkmcnt(4)\n" \
"ds_read_b128 v[44:47], v2 offset:28928\nv_mfma_f32_16x16x32_bf16 v[16:19], v[96:99], v[64:67], v[16:19]\nds_read_b128 v[56:59], v1 offset:6144\nds_read_b128 v[60:63], v1 offset:7168\nv_mfma_f32_16x16x32_bf16 v[20:23], v[112:115], v[64:67], v[20:23]\ns_waitcnt lgkmcnt(6)\nds_read_b128 v[64:67], v2 offset:30720\nv_mfma_f32_16x16x32_bf16 v[24:27], v[96:99], v[68:71], v[24:27]\n" \
"v_mfma_f32_16x16x32_bf16 v[28:31], v[112:115], v[68:71], v[28:31]\ns_waitcnt lgkmcnt(6)\nds_read_b128 v[68:71], v2 offset:30976\nv_mfma_f32_16x16x32_bf16 v[16:19], v[100:103], v[32:35], v[16:19]\nv_mfma_f32_16x16x32_bf16 v[20:23], v[116:119], v[32:35], v[20:23]\ns_waitcnt lgkmcnt(6)\nv_mfma_f32_16x16x32_bf16 v[24:27], v[100:103], v[36:39], v[24:27]\nv_mfma_f32_16x16x32_bf16 v[28:31], v[116:119], v[36:39], v[28:31]\n" \
"s_waitcnt lgkmcnt(0)\ns_waitcnt vmcnt(5)\ns_barrier\nds_read_b128 v[32:35], v2 offset:0\nds_read_b128 v[36:39], v2 offset:256\nv_mfma_f32_16x16x32_bf16 v[16:19], v[128:131], v[40:43], v[16:19]\ns_add_u32 m0, s57, 16384\ns_nop 0\n" \
"buffer_load_dwordx4 v3, s[36:39], s42 offen offset:0 sc1 lds\nv_mfma_f32_16x16x32_bf16 v[20:23], v[136:139], v[40:43], v[20:23]\nds_read_b128 v[40:43], v2 offset:2048\nv_mfma_f32_16x16x32_bf16 v[24:27], v[128:131], v[44:47], v[24:27]\nv_mfma_f32_16x16x32_bf16 v[28:31], v[136:139], v[44:47], v[28:31]\nds_read_b128 v[44:47], v2 offset:2304\nv_mfma_f32_16x16x32_bf16 v[16:19], v[132:135], v[64:67], v[16:19]\nbuffer_load_dwordx4 v3, s[36:39], s42 offen offset:1024 sc1 lds\n" \
"v_mfma_f32_16x16x32_bf16 v[20:23], v[140:143], v[64:67], v[20:23]\nds_read_b128 v[64:67], v2 offset:4096\nv_mfma_f32_16x16x32_bf16 v[24:27], v[132:135], v[68:71], v[24:27]\nv_mfma_f32_16x16x32_bf16 v[28:31], v[140:143], v[68:71], v[28:31]\ns_waitcnt lgkmcnt(4)\nds_read_b128 v[68:71], v2 offset:4352\nv_mfma_f32_16x16x32_bf16 v[16:19], v[152:155], v[32:35], v[16:19]\nbuffer_load_dwordx4 v3, s[36:39], s42 offen offset:2048 sc1 lds\n" \
"v_mfma_f32_16x16x32_bf16 v[20:23], v[56:59], v[32:35], v[20:23]\ns_waitcnt lgkmcnt(4)\nds_read_b128 v[32:35], v2 offset:6144\nv_mfma_f32_16x16x32_bf16 v[24:27], v[152:155], v[36:39], v[24:27]\nv_mfma_f32_16x16x32_bf16 v[28:31], v[56:59], v[36:39], v[28:31]\ns_waitcnt lgkmcnt(4)\nds_read_b128 v[36:39], v2 offset:6400\nv_mfma_f32_16x16x32_bf16 v[16:19], v[156:159], v[40:43], v[16:19]\n" \
"buffer_load_dwordx4 v3, s[36:39], s42 offen offset:3072 sc1 lds\nv_add_u32 v14, s57, v0\nv_mfma_f32_16x16x32_bf16 v[20:23], v[60:63], v[40:43], v[20:23]\ns_waitcnt lgkmcnt(4)\nds_read_b128 v[40:43], v2 offset:8192\nv_mfma_f32_16x16x32_bf16 v[24:27], v[156:159], v[44:47], v[24:27]\nv_mov_b64 v[48:49], 0\nv_mfma_f32_16x16x32_bf16 v[28:31], v[60:63], v[44:47], v[28:31]\n" \
"s_waitcnt lgkmcnt(4)\nds_read_b128 v[44:47], v2 offset:10240\nv_mov_b64 v[50:51], 0\nv_mfma_f32_16x16x32_bf16 v[16:19], v[168:171], v[64:67], v[16:19]\nv_mov_b64 v[52:53], 0\nv_mfma_f32_16x16x32_bf16 v[20:23], v[184:187], v[64:67], v[20:23]\ns_waitcnt lgkmcnt(4)\nds_read_b128 v[64:67], v2 offset:12288\n" \
"v_mov_b64 v[54:55], 0\nv_mfma_f32_16x16x32_bf16 v[24:27], v[168:171], v[68:71], v[24:27]\nds_read_b128 v[56:59], v1 offset:0\nds_read_b128 v[60:63], v1 offset:1024\nv_mfma_f32_16x16x32_bf16 v[28:31], v[184:187], v[68:71], v[28:31]\ns_waitcnt lgkmcnt(6)\nds_read_b128 v[68:71], v2 offset:14336\nv_mfma_f32_16x16x32_bf16 v[16:19], v[172:175], v[32:35], v[16:19]\n" \
"v_mfma_f32_16x16x32_bf16 v[20:23], v[188:191], v[32:35], v[20:23]\ns_waitcnt lgkmcnt(6)\nds_read_b128 v[32:35], v2 offset:8448\nv_mfma_f32_16x16x32_bf16 v[24:27], v[172:175], v[36:39], v[24:27]\nv_mfma_f32_16x16x32_bf16 v[28:31], v[188:191], v[36:39], v[28:31]\ns_waitcnt lgkmcnt(6)\nds_read_b128 v[36:39], v2 offset:10496\nv_mfma_f32_16x16x32_bf16 v[16:19], v[200:203], v[40:43], v[16:19]\n" \
"s_waitcnt vmcnt(0)\nds_read_b128 v[80:83], v14 offset:16384\nds_read_b128 v[84:87], v14 offset:17408\nv_mfma_f32_16x16x32_bf16 v[20:23], v[216:219], v[40:43], v[20:23]\ns_waitcnt lgkmcnt(8)\nds_read_b128 v[40:43], v2 offset:12544\nv_mfma_f32_16x16x32_bf16 v[16:19], v[204:207], v[44:47], v[16:19]\nds_read_b128 v[88:91], v14 offset:18432\n" \
"ds_read_b128 v[92:95], v14 offset:19456\nv_mfma_f32_16x16x32_bf16 v[20:23], v[220:223], v[44:47], v[20:23]\ns_waitcnt lgkmcnt(10)\nds_read_b128 v[44:47], v2 offset:14592\nv_mfma_f32_16x16x32_bf16 v[16:19], v[232:235], v[64:67], v[16:19]\ns_waitcnt lgkmcnt(4)\nv_max3_u32 v72, v80, v82, v84\nv_mfma_f32_16x16x32_bf16 v[20:23], v[240:243], v[64:67], v[20:23]\n" \
"s_waitcnt lgkmcnt(2)\nv_max3_u32 v72, v72, v86, v88\nv_mfma_f32_16x16x32_bf16 v[16:19], v[236:239], v[68:71], v[16:19]\ns_waitcnt lgkmcnt(1)\nv_max3_u32 v72, v72, v90, v92\nv_max_u32 v72, v72, v94\nv_mfma_f32_16x16x32_bf16 v[20:23], v[244:247], v[68:71], v[20:23]\nv_cmp_eq_u32 vcc, -1, v72\n" \
"v_mfma_f32_16x16x32_bf16 v[24:27], v[200:203], v[32:35], v[24:27]\ns_cbranch_vccnz .Lvu_re%=\n.Lvu_rer%=:\ns_barrier\ns_memrealtime s[68:69]\nds_read_b128 v[80:83], v0 offset:16384\nv_mfma_f32_16x16x32_bf16 v[28:31], v[216:219], v[32:35], v[28:31]\nds_read_b128 v[84:87], v0 offset:17408\n" \
"v_mfma_f32_16x16x32_bf16 v[24:27], v[204:207], v[36:39], v[24:27]\nds_read_b128 v[88:91], v0 offset:18432\nv_cvt_pk_bf16_f32 v72, v16, v17\nv_cvt_pk_bf16_f32 v73, v18, v19\nv_mfma_f32_16x16x32_bf16 v[28:31], v[220:223], v[36:39], v[28:31]\nds_read_b128 v[92:95], v0 offset:19456\nv_cvt_pk_bf16_f32 v74, v20, v21\nv_cvt_pk_bf16_f32 v75, v22, v23\n" \
"v_mfma_f32_16x16x32_bf16 v[24:27], v[232:235], v[40:43], v[24:27]\nds_read_b128 v[16:19], v0 offset:20480\nv_mfma_f32_16x16x32_bf16 v[28:31], v[240:243], v[40:43], v[28:31]\ns_waitcnt lgkmcnt(5)\nds_read_b128 v[20:23], v0 offset:21504\nv_mfma_f32_16x16x32_bf16 v[24:27], v[236:239], v[44:47], v[24:27]\nds_read_b128 v[32:35], v0 offset:22528\nv_mfma_f32_16x16x32_bf16 v[28:31], v[244:247], v[44:47], v[28:31]\n" \
"s_cmp_lt_i32 s40, 0\nds_read_b128 v[36:39], v0 offset:23552\nds_write2st64_b64 v9, v[72:73], v[74:75] offset0:0 offset1:8\ns_cbranch_scc1 .Lvu_first%=\n.Lvu_crit%=:\ns_waitcnt lgkmcnt(8)\nv_smfmac_f32_16x16x64_bf16 v[48:51], v[80:83], a[0:7], v4\ns_waitcnt lgkmcnt(7)\n" \
"ds_read_b128 v[64:67], v1 offset:4096\nds_read_b128 v[40:43], v0 offset:24576\nv_mov_b32 v14, s40\nv_smfmac_f32_16x16x64_bf16 v[48:51], v[84:87], a[24:31], v4\ns_waitcnt lgkmcnt(8)\nds_read_b128 v[68:71], v1 offset:5120\nds_read_b128 v[44:47], v0 offset:25600\nv_cvt_pk_bf16_f32 v76, v24, v25\n" \
"v_smfmac_f32_16x16x64_bf16 v[48:51], v[88:91], a[56:63], v4\ns_sub_u32 s88, s86, s89\ns_min_u32 s88, s88, 150\ns_waitcnt lgkmcnt(9)\nds_read_b128 v[80:83], v0 offset:28672\nv_cvt_pk_bf16_f32 v77, v26, v27\nv_smfmac_f32_16x16x64_bf16 v[48:51], v[92:95], v[56:63], v4\ns_waitcnt lgkmcnt(9)\n" \
"ds_read_b128 v[84:87], v0 offset:29696\nv_cvt_pk_bf16_f32 v78, v28, v29\nv_smfmac_f32_16x16x64_bf16 v[48:51], v[16:19], a[104:111], v4\ns_add_u32 s88, s88, s68\ns_mov_b32 s89, s68\ns_waitcnt lgkmcnt(9)\nds_read_b128 v[88:91], v0 offset:30720\nv_cvt_pk_bf16_f32 v79, v30, v31\n" \
"v_smfmac_f32_16x16x64_bf16 v[48:51], v[20:23], a[128:135], v4\ns_waitcnt lgkmcnt(9)\nds_read_b128 v[92:95], v0 offset:31744\nv_mov_b32 v15, s88\nv_smfmac_f32_16x16x64_bf16 v[48:51], v[32:35], a[160:167], v4\ns_waitcnt lgkmcnt(9)\nds_read_b128 v[24:27], v0 offset:26624\nbuffer_store_dwordx2 v[14:15], off, s[36:39], %[sg] sc1\n" \
"v_smfmac_f32_16x16x64_bf16 v[48:51], v[36:39], a[184:191], v4\ns_waitcnt lgkmcnt(7)\nds_read_b128 v[28:31], v0 offset:27648\nv_smfmac_f32_16x16x64_bf16 v[48:51], v[40:43], a[208:215], v4\ns_waitcnt lgkmcnt(6)\nds_read_b128 v[56:59], v1 offset:6144\nv_smfmac_f32_16x16x64_bf16 v[48:51], v[44:47], v[64:71], v4\ns_waitcnt lgkmcnt(6)\n" \
"ds_read_b128 v[60:63], v1 offset:7168\nv_smfmac_f32_16x16x64_bf16 v[48:51], v[80:83], v[152:159], v4\ns_waitcnt lgkmcnt(6)\nds_write2st64_b64 v9, v[76:77], v[78:79] offset0:2 offset1:10\nv_smfmac_f32_16x16x64_bf16 v[48:51], v[84:87], v[168:175], v4\ns_waitcnt lgkmcnt(6)\nv_smfmac_f32_16x16x64_bf16 v[48:51], v[88:91], v[200:207], v4\ns_waitcnt lgkmcnt(5)\n" \
"v_smfmac_f32_16x16x64_bf16 v[48:51], v[92:95], v[232:239], v4\ns_waitcnt lgkmcnt(4)\nv_smfmac_f32_16x16x64_bf16 v[48:51], v[24:27], v[96:103], v4\ns_waitcnt lgkmcnt(3)\nds_read_b128 v[64:67], v1 offset:2048\nv_smfmac_f32_16x16x64_bf16 v[48:51], v[28:31], v[128:135], v4\ns_waitcnt lgkmcnt(2)\nds_read_b128 v[68:71], v1 offset:3072\n" \
"v_smfmac_f32_16x16x64_bf16 v[52:55], v[80:83], v[56:63], v4\nds_read_b128 v[56:59], v1 offset:8192\nv_smfmac_f32_16x16x64_bf16 v[52:55], v[84:87], v[184:191], v4\nds_read_b128 v[60:63], v1 offset:9216\nv_add_f32 v48, v48, v50\nv_smfmac_f32_16x16x64_bf16 v[52:55], v[88:91], v[216:223], v4\nds_read_b128 v[80:83], v0 offset:16384\nv_add_f32 v49, v49, v51\n" \
"v_smfmac_f32_16x16x64_bf16 v[52:55], v[92:95], v[240:247], v4\nds_read_b128 v[84:87], v0 offset:17408\nv_cvt_pk_bf16_f32 v14, v48, v49\nv_smfmac_f32_16x16x64_bf16 v[52:55], v[16:19], a[120:127], v4\nds_write_b16 v5, v14 offset:0\nds_write_b16_d16_hi v5, v14 offset:16\nv_smfmac_f32_16x16x64_bf16 v[52:55], v[20:23], a[144:151], v4\nds_read_b128 v[88:91], v0 offset:18432\n" \
"v_mov_b64 v[16:17], 0\nv_smfmac_f32_16x16x64_bf16 v[52:55], v[24:27], v[112:119], v4\nds_read_b128 v[92:95], v0 offset:19456\nv_mov_b64 v[18:19], 0\nv_smfmac_f32_16x16x64_bf16 v[52:55], v[28:31], v[136:143], v4\ns_waitcnt lgkmcnt(8)\nv_mov_b64 v[20:21], 0\nv_smfmac_f32_16x16x64_bf16 v[52:55], v[32:35], v[64:71], v4\n" \
"s_waitcnt lgkmcnt(2)\nds_read_b128 v[64:67], v1 offset:12288\ns_barrier\nds_read_b128 v[72:75], v11 offset:57344\nv_smfmac_f32_16x16x64_bf16 v[52:55], v[36:39], a[192:199], v4\nds_read_b128 v[68:71], v1 offset:13312\nds_read_b128 v[76:79], v11 offset:58368\nv_mov_b64 v[22:23], 0\n" \
"v_smfmac_f32_16x16x64_bf16 v[52:55], v[40:43], a[224:231], v4\nv_mov_b64 v[24:25], 0\nv_smfmac_f32_16x16x64_bf16 v[52:55], v[44:47], a[240:247], v4\nv_mov_b64 v[26:27], 0\nv_smfmac_f32_16x16x64_bf16 v[52:55], v[80:83], a[16:23], v4\nv_mov_b64 v[28:29], 0\nv_smfmac_f32_16x16x64_bf16 v[52:55], v[84:87], a[40:47], v4\ns_waitcnt lgkmcnt(5)\n" \
"v_mov_b64 v[30:31], 0\nv_smfmac_f32_16x16x64_bf16 v[52:55], v[88:91], a[72:79], v4\ns_waitcnt lgkmcnt(4)\nv_mov_b64 v[32:33], 0\nv_smfmac_f32_16x16x64_bf16 v[52:55], v[92:95], a[88:95], v4\ns_waitcnt lgkmcnt(2)\nv_mov_b64 v[34:35], 0\nv_smfmac_f32_16x16x64_bf16 v[16:19], v[72:75], a[8:15], v10\n" \
"s_waitcnt lgkmcnt(0)\nv_mov_b64 v[36:37], 0\nv_smfmac_f32_16x16x64_bf16 v[16:19], v[76:79], v[56:63], v10\nds_read_b128 v[56:59], v1 offset:10240\nv_add_f32 v52, v52, v54\nv_smfmac_f32_16x16x64_bf16 v[20:23], v[72:75], a[64:71], v10\nds_read_b128 v[60:63], v1 offset:11264\nv_add_f32 v53, v53, v55\n" \
"v_smfmac_f32_16x16x64_bf16 v[20:23], v[76:79], a[80:87], v10\nv_cvt_pk_bf16_f32 v14, v52, v53\nv_smfmac_f32_16x16x64_bf16 v[24:27], v[72:75], a[112:119], v10\nds_write_b16 v5, v14 offset:2048\nds_write_b16_d16_hi v5, v14 offset:2064\nv_smfmac_f32_16x16x64_bf16 v[24:27], v[76:79], v[64:71], v10\nds_read_b128 v[64:67], v1 offset:14336\nv_mov_b64 v[38:39], 0\n" \
"v_smfmac_f32_16x16x64_bf16 v[28:31], v[72:75], a[168:175], v10\nds_read_b128 v[68:71], v1 offset:15360\nv_mov_b64 v[40:41], 0\nv_smfmac_f32_16x16x64_bf16 v[28:31], v[76:79], a[176:183], v10\nv_mov_b64 v[42:43], 0\nv_smfmac_f32_16x16x64_bf16 v[32:35], v[72:75], a[216:223], v10\ns_waitcnt lgkmcnt(2)\ns_barrier\n" \
"s_mov_b32 s59, 0\nds_read_b128 v[80:83], v11 offset:59392\nv_smfmac_f32_16x16x64_bf16 v[32:35], v[76:79], v[56:63], v10\ns_add_u32 m0, s57, 0\ns_add_u32 s58, s61, 0\nds_read_b128 v[56:59], v1 offset:16384\nbuffer_load_dwordx4 v0, s[36:39], s58 offen offset:0 sc1 lds\nds_read_b128 v[84:87], v11 offset:60416\n" \
"v_smfmac_f32_16x16x64_bf16 v[36:39], v[72:75], v[104:111], v10\nds_read_b128 v[60:63], v1 offset:17408\nv_mov_b64 v[44:45], 0\nv_smfmac_f32_16x16x64_bf16 v[36:39], v[76:79], v[120:127], v10\nv_mov_b64 v[46:47], 0\nv_smfmac_f32_16x16x64_bf16 v[40:43], v[72:75], v[160:167], v10\ns_waitcnt lgkmcnt(4)\nbuffer_load_dwordx4 v0, s[36:39], s58 offen offset:1024 sc1 lds\n" \
"v_smfmac_f32_16x16x64_bf16 v[40:43], v[76:79], v[64:71], v10\nds_read_b128 v[64:67], v1 offset:20480\nv_mov_b64 v[48:49], 0\nv_smfmac_f32_16x16x64_bf16 v[44:47], v[72:75], v[208:215], v10\nds_read_b128 v[68:71], v1 offset:21504\nv_mov_b64 v[50:51], 0\nv_smfmac_f32_16x16x64_bf16 v[44:47], v[76:79], v[224:231], v10\ns_waitcnt lgkmcnt(5)\n" \
"buffer_load_dwordx4 v0, s[36:39], s58 offen offset:2048 sc1 lds\nv_smfmac_f32_16x16x64_bf16 v[16:19], v[80:83], a[32:39], v10\ns_waitcnt lgkmcnt(3)\nds_read_b128 v[72:75], v11 offset:49152\nv_mov_b64 v[52:53], 0\nv_smfmac_f32_16x16x64_bf16 v[16:19], v[84:87], a[48:55], v10\ns_waitcnt lgkmcnt(3)\nds_read_b128 v[76:79], v11 offset:50176\n" \
"v_mov_b64 v[54:55], 0\nv_smfmac_f32_16x16x64_bf16 v[20:23], v[80:83], v[56:63], v10\nds_read_b128 v[56:59], v1 offset:18432\nbuffer_load_dwordx4 v0, s[36:39], s58 offen offset:3072 sc1 lds\nv_smfmac_f32_16x16x64_bf16 v[20:23], v[84:87], a[96:103], v10\nds_read_b128 v[60:63], v1 offset:19456\nv_cvt_pk_bf16_f32 v88, v16, v17\nv_smfmac_f32_16x16x64_bf16 v[24:27], v[80:83], a[136:143], v10\n" \
"v_and_or_b32 v88, v88, -2, v12\nv_cvt_pk_bf16_f32 v89, v18, v19\nv_smfmac_f32_16x16x64_bf16 v[24:27], v[84:87], a[152:159], v10\ns_waitcnt lgkmcnt(4)\nv_cvt_pk_bf16_f32 v90, v20, v21\nv_cvt_pk_bf16_f32 v91, v22, v23\nv_and_or_b32 v90, v90, -2, v12\nv_smfmac_f32_16x16x64_bf16 v[28:31], v[80:83], v[64:71], v10\n" \
"ds_read_b128 v[64:67], v1 offset:22528\nv_permlane16_swap_b32 v89, v91\nv_smfmac_f32_16x16x64_bf16 v[28:31], v[84:87], a[200:207], v10\nds_read_b128 v[68:71], v1 offset:23552\nv_permlane16_swap_b32 v88, v90\nv_smfmac_f32_16x16x64_bf16 v[32:35], v[80:83], a[232:239], v10\ns_add_u32 s63, s59, 0\nds_read_b128 v[16:19], v11 offset:51200\n" \
"v_cvt_pk_bf16_f32 v92, v24, v25\nv_and_or_b32 v92, v92, -2, v12\nv_smfmac_f32_16x16x64_bf16 v[32:35], v[84:87], a[248:255], v10\ns_waitcnt lgkmcnt(3)\nds_read_b128 v[20:23], v11 offset:52224\nbuffer_store_dwordx4 v[88:91], v6, s[36:39], s63 offen sc1\nv_cvt_pk_bf16_f32 v93, v26, v27\nv_cvt_pk_bf16_f32 v94, v28, v29\n" \
"v_cvt_pk_bf16_f32 v95, v30, v31\nv_and_or_b32 v94, v94, -2, v12\nv_smfmac_f32_16x16x64_bf16 v[36:39], v[80:83], v[56:63], v10\nds_read_b128 v[56:59], v1 offset:8192\nv_permlane16_swap_b32 v93, v95\nv_smfmac_f32_16x16x64_bf16 v[36:39], v[84:87], v[144:151], v10\nds_read_b128 v[60:63], v1 offset:9216\nv_permlane16_swap_b32 v92, v94\n" \
"v_smfmac_f32_16x16x64_bf16 v[40:43], v[80:83], v[176:183], v10\ns_add_u32 s63, s59, 131072\nds_read_b128 v[24:27], v11 offset:53248\nv_cvt_pk_bf16_f32 v88, v32, v33\nv_and_or_b32 v88, v88, -2, v12\nv_smfmac_f32_16x16x64_bf16 v[40:43], v[84:87], v[192:199], v10\ns_waitcnt lgkmcnt(5)\nds_read_b128 v[28:31], v11 offset:54272\n" \
"buffer_store_dwordx4 v[92:95], v6, s[36:39], s63 offen sc1\nv_cvt_pk_bf16_f32 v89, v34, v35\nv_cvt_pk_bf16_f32 v90, v36, v37\nv_cvt_pk_bf16_f32 v91, v38, v39\nv_and_or_b32 v90, v90, -2, v12\nv_smfmac_f32_16x16x64_bf16 v[44:47], v[80:83], v[64:71], v10\nds_read_b128 v[64:67], v1 offset:16384\nv_permlane16_swap_b32 v89, v91\n" \
"v_smfmac_f32_16x16x64_bf16 v[44:47], v[84:87], v[248:255], v10\ns_cmp_ge_i32 s40, 63\nds_read_b128 v[68:71], v1 offset:17408\nv_permlane16_swap_b32 v88, v90\ns_cbranch_scc1 .Lvu_last%=\n.Lvu_y%=:\nv_smfmac_f32_16x16x64_bf16 v[48:51], v[72:75], a[8:15], v10\ns_add_u32 s63, s59, 4096\n" \
"ds_read_b128 v[32:35], v11 offset:55296\nds_read_b128 v[36:39], v11 offset:56320\nv_cvt_pk_bf16_f32 v92, v40, v41\nv_and_or_b32 v92, v92, -2, v12\nv_smfmac_f32_16x16x64_bf16 v[52:55], v[76:79], a[8:15], v10\ns_waitcnt lgkmcnt(9)\ns_waitcnt lgkmcnt(6)\nbuffer_store_dwordx4 v[88:91], v6, s[36:39], s63 offen sc1\n" \
"v_cvt_pk_bf16_f32 v93, v42, v43\nv_cvt_pk_bf16_f32 v94, v44, v45\nv_cvt_pk_bf16_f32 v95, v46, v47\nv_and_or_b32 v94, v94, -2, v12\nv_smfmac_f32_16x16x64_bf16 v[48:51], v[16:19], v[56:63], v10\ns_add_u32 s40, s40, 1\ns_and_b32 s44, s40, 1\nv_permlane16_swap_b32 v93, v95\n" \
"v_smfmac_f32_16x16x64_bf16 v[52:55], v[20:23], v[56:63], v10\ns_lshl_b32 s55, s44, 22\ns_waitcnt lgkmcnt(5)\nv_permlane16_swap_b32 v92, v94\nv_smfmac_f32_16x16x64_bf16 v[48:51], v[24:27], a[32:39], v10\ns_waitcnt lgkmcnt(4)\ns_add_u32 s63, s59, 135168\nds_read_b128 v[56:59], v1 offset:12288\n" \
"v_smfmac_f32_16x16x64_bf16 v[52:55], v[28:31], a[32:39], v10\ns_waitcnt lgkmcnt(2)\nds_read_b128 v[60:63], v1 offset:13312\nbuffer_store_dwordx4 v[92:95], v6, s[36:39], s63 offen sc1\nv_mov_b64 v[80:81], 0\nv_mov_b64 v[82:83], 0\nv_mov_b64 v[84:85], 0\nv_mov_b64 v[86:87], 0\n" \
"v_smfmac_f32_16x16x64_bf16 v[48:51], v[32:35], a[48:55], v10\ns_memrealtime s[86:87]\ns_bfe_u32 s45, s40, 0x10001\ns_waitcnt lgkmcnt(2)\nv_smfmac_f32_16x16x64_bf16 v[52:55], v[36:39], a[48:55], v10\nv_mov_b32 v13, s45\nv_smfmac_f32_16x16x64_bf16 v[80:83], v[72:75], a[64:71], v10\nv_cvt_pk_bf16_f32 v88, v48, v49\n" \
"v_smfmac_f32_16x16x64_bf16 v[84:87], v[76:79], a[64:71], v10\nv_cvt_pk_bf16_f32 v89, v50, v51\nv_smfmac_f32_16x16x64_bf16 v[80:83], v[16:19], a[80:87], v10\nv_cvt_pk_bf16_f32 v90, v52, v53\nv_smfmac_f32_16x16x64_bf16 v[84:87], v[20:23], a[80:87], v10\ns_add_u32 s60, s55, 0\nv_cvt_pk_bf16_f32 v91, v54, v55\nv_smfmac_f32_16x16x64_bf16 v[80:83], v[24:27], v[64:71], v10\n" \
"v_and_or_b32 v88, v88, -2, v13\nv_smfmac_f32_16x16x64_bf16 v[84:87], v[28:31], v[64:71], v10\ns_and_b32 s42, s40, 3\ns_lshl_b32 s42, s42, 15\nv_and_or_b32 v90, v90, -2, v13\nv_smfmac_f32_16x16x64_bf16 v[80:83], v[32:35], a[96:103], v10\nbuffer_store_dwordx4 v[88:91], v7, s[36:39], s60 offen offset:0 sc1\nv_mov_b64 v[48:49], 0\n" \
"v_mov_b64 v[50:51], 0\nv_mov_b64 v[52:53], 0\nv_mov_b64 v[54:55], 0\nv_smfmac_f32_16x16x64_bf16 v[84:87], v[36:39], a[96:103], v10\nds_read_b128 v[64:67], v1 offset:20480\nv_mov_b32 v12, s44\nv_smfmac_f32_16x16x64_bf16 v[48:51], v[72:75], a[112:119], v10\nds_read_b128 v[68:71], v1 offset:21504\n" \
"v_cvt_pk_bf16_f32 v92, v80, v81\nv_smfmac_f32_16x16x64_bf16 v[52:55], v[76:79], a[112:119], v10\ns_waitcnt lgkmcnt(2)\nv_cvt_pk_bf16_f32 v93, v82, v83\nv_smfmac_f32_16x16x64_bf16 v[48:51], v[16:19], v[56:63], v10\nv_cvt_pk_bf16_f32 v94, v84, v85\nv_smfmac_f32_16x16x64_bf16 v[52:55], v[20:23], v[56:63], v10\ns_add_u32 s60, s55, 4096\n" \
"v_cvt_pk_bf16_f32 v95, v86, v87\nv_smfmac_f32_16x16x64_bf16 v[48:51], v[24:27], a[136:143], v10\nds_read_b128 v[56:59], v1 offset:10240\nv_and_or_b32 v92, v92, -2, v13\nv_smfmac_f32_16x16x64_bf16 v[52:55], v[28:31], a[136:143], v10\nds_read_b128 v[60:63], v1 offset:11264\nv_and_or_b32 v94, v94, -2, v13\nv_smfmac_f32_16x16x64_bf16 v[48:51], v[32:35], a[152:159], v10\n" \
"buffer_store_dwordx4 v[92:95], v7, s[36:39], s60 offen offset:0 sc1\nv_mov_b64 v[80:81], 0\nv_mov_b64 v[82:83], 0\nv_mov_b64 v[84:85], 0\nv_mov_b64 v[86:87], 0\nv_smfmac_f32_16x16x64_bf16 v[52:55], v[36:39], a[152:159], v10\ns_waitcnt vmcnt(6)\ns_barrier\n" \
"v_smfmac_f32_16x16x64_bf16 v[80:83], v[72:75], a[168:175], v10\nv_cvt_pk_bf16_f32 v88, v48, v49\nv_smfmac_f32_16x16x64_bf16 v[84:87], v[76:79], a[168:175], v10\nv_cvt_pk_bf16_f32 v89, v50, v51\nv_smfmac_f32_16x16x64_bf16 v[80:83], v[16:19], a[176:183], v10\ns_cmp_lt_u32 s40, 2\ns_cselect_b32 s54, %[fy], -2147483648\nv_cvt_pk_bf16_f32 v90, v52, v53\n" \
"v_smfmac_f32_16x16x64_bf16 v[84:87], v[20:23], a[176:183], v10\ns_add_u32 s60, s55, 16384\ns_waitcnt lgkmcnt(2)\nv_cvt_pk_bf16_f32 v91, v54, v55\nv_smfmac_f32_16x16x64_bf16 v[80:83], v[24:27], v[64:71], v10\nv_and_or_b32 v88, v88, -2, v13\nv_smfmac_f32_16x16x64_bf16 v[84:87], v[28:31], v[64:71], v10\nv_and_or_b32 v90, v90, -2, v13\n" \
"v_smfmac_f32_16x16x64_bf16 v[80:83], v[32:35], a[200:207], v10\nbuffer_store_dwordx4 v[88:91], v7, s[36:39], s60 offen offset:0 sc1\nv_mov_b64 v[48:49], 0\nv_mov_b64 v[50:51], 0\nv_mov_b64 v[52:53], 0\nv_mov_b64 v[54:55], 0\nv_smfmac_f32_16x16x64_bf16 v[84:87], v[36:39], a[200:207], v10\nds_read_b128 v[64:67], v1 offset:18432\n" \
"v_smfmac_f32_16x16x64_bf16 v[48:51], v[72:75], a[216:223], v10\nds_read_b128 v[68:71], v1 offset:19456\nv_cvt_pk_bf16_f32 v92, v80, v81\nv_smfmac_f32_16x16x64_bf16 v[52:55], v[76:79], a[216:223], v10\ns_waitcnt lgkmcnt(2)\nv_cvt_pk_bf16_f32 v93, v82, v83\nv_smfmac_f32_16x16x64_bf16 v[48:51], v[16:19], v[56:63], v10\nv_cvt_pk_bf16_f32 v94, v84, v85\n" \
"v_smfmac_f32_16x16x64_bf16 v[52:55], v[20:23], v[56:63], v10\ns_add_u32 s60, s55, 20480\nv_cvt_pk_bf16_f32 v95, v86, v87\nv_smfmac_f32_16x16x64_bf16 v[48:51], v[24:27], a[232:239], v10\nds_read_b128 v[56:59], v1 offset:14336\nv_and_or_b32 v92, v92, -2, v13\nv_smfmac_f32_16x16x64_bf16 v[52:55], v[28:31], a[232:239], v10\nds_read_b128 v[60:63], v1 offset:15360\n" \
"v_and_or_b32 v94, v94, -2, v13\nv_smfmac_f32_16x16x64_bf16 v[48:51], v[32:35], a[248:255], v10\nbuffer_store_dwordx4 v[92:95], v7, s[36:39], s60 offen offset:0 sc1\nv_mov_b64 v[80:81], 0\nv_mov_b64 v[82:83], 0\nv_mov_b64 v[84:85], 0\nv_mov_b64 v[86:87], 0\nv_smfmac_f32_16x16x64_bf16 v[52:55], v[36:39], a[248:255], v10\n" \
"v_smfmac_f32_16x16x64_bf16 v[80:83], v[72:75], v[104:111], v10\nv_cvt_pk_bf16_f32 v88, v48, v49\nv_smfmac_f32_16x16x64_bf16 v[84:87], v[76:79], v[104:111], v10\nv_cvt_pk_bf16_f32 v89, v50, v51\nv_smfmac_f32_16x16x64_bf16 v[80:83], v[16:19], v[120:127], v10\nv_cvt_pk_bf16_f32 v90, v52, v53\nv_smfmac_f32_16x16x64_bf16 v[84:87], v[20:23], v[120:127], v10\ns_add_u32 s60, s55, 0\n" \
"s_waitcnt lgkmcnt(2)\nv_cvt_pk_bf16_f32 v91, v54, v55\nv_smfmac_f32_16x16x64_bf16 v[80:83], v[24:27], v[64:71], v10\nv_and_or_b32 v88, v88, -2, v13\nv_smfmac_f32_16x16x64_bf16 v[84:87], v[28:31], v[64:71], v10\nv_and_or_b32 v90, v90, -2, v13\nv_smfmac_f32_16x16x64_bf16 v[80:83], v[32:35], v[144:151], v10\nbuffer_store_dwordx4 v[88:91], v7, s[36:39], s60 offen offset:2048 sc1\n" \
"v_mov_b64 v[48:49], 0\nv_mov_b64 v[50:51], 0\nv_mov_b64 v[52:53], 0\nv_mov_b64 v[54:55], 0\nv_smfmac_f32_16x16x64_bf16 v[84:87], v[36:39], v[144:151], v10\nds_read_b128 v[64:67], v1 offset:22528\nv_smfmac_f32_16x16x64_bf16 v[48:51], v[72:75], v[160:167], v10\nds_read_b128 v[68:71], v1 offset:23552\n" \
"v_cvt_pk_bf16_f32 v92, v80, v81\nv_smfmac_f32_16x16x64_bf16 v[52:55], v[76:79], v[160:167], v10\ns_waitcnt lgkmcnt(2)\nv_cvt_pk_bf16_f32 v93, v82, v83\nv_smfmac_f32_16x16x64_bf16 v[48:51], v[16:19], v[56:63], v10\nds_read_b128 v[40:43], v11 offset:49280\nv_cvt_pk_bf16_f32 v94, v84, v85\nv_smfmac_f32_16x16x64_bf16 v[52:55], v[20:23], v[56:63], v10\n" \
"s_add_u32 s60, s55, 4096\nv_cvt_pk_bf16_f32 v95, v86, v87\nv_smfmac_f32_16x16x64_bf16 v[48:51], v[24:27], v[176:183], v10\nds_read_b128 v[56:59], v1 offset:8192\nds_read_b128 v[44:47], v11 offset:50304\nv_and_or_b32 v92, v92, -2, v13\nv_smfmac_f32_16x16x64_bf16 v[52:55], v[28:31], v[176:183], v10\nds_read_b128 v[60:63], v1 offset:9216\n" \
"v_and_or_b32 v94, v94, -2, v13\nv_smfmac_f32_16x16x64_bf16 v[48:51], v[32:35], v[192:199], v10\nbuffer_store_dwordx4 v[92:95], v7, s[36:39], s60 offen offset:2048 sc1\nv_mov_b64 v[80:81], 0\nv_mov_b64 v[82:83], 0\nv_mov_b64 v[84:85], 0\nv_mov_b64 v[86:87], 0\nv_smfmac_f32_16x16x64_bf16 v[52:55], v[36:39], v[192:199], v10\n" \
"v_smfmac_f32_16x16x64_bf16 v[80:83], v[72:75], v[208:215], v10\nds_read_b128 v[72:75], v11 offset:51328\nv_cvt_pk_bf16_f32 v88, v48, v49\nv_smfmac_f32_16x16x64_bf16 v[84:87], v[76:79], v[208:215], v10\nds_read_b128 v[76:79], v11 offset:52352\nv_cvt_pk_bf16_f32 v89, v50, v51\nv_smfmac_f32_16x16x64_bf16 v[80:83], v[16:19], v[224:231], v10\ns_add_u32 s60, s55, 16384\n" \
"ds_read_b128 v[16:19], v11 offset:53376\nv_cvt_pk_bf16_f32 v90, v52, v53\nv_smfmac_f32_16x16x64_bf16 v[84:87], v[20:23], v[224:231], v10\ns_waitcnt lgkmcnt(7)\nds_read_b128 v[20:23], v11 offset:54400\nv_cvt_pk_bf16_f32 v91, v54, v55\nv_smfmac_f32_16x16x64_bf16 v[80:83], v[24:27], v[64:71], v10\nds_read_b128 v[24:27], v11 offset:55424\n" \
"v_and_or_b32 v88, v88, -2, v13\nv_smfmac_f32_16x16x64_bf16 v[84:87], v[28:31], v[64:71], v10\nds_read_b128 v[28:31], v11 offset:56448\nv_and_or_b32 v90, v90, -2, v13\nv_smfmac_f32_16x16x64_bf16 v[80:83], v[32:35], v[248:255], v10\nbuffer_store_dwordx4 v[88:91], v7, s[36:39], s60 offen offset:2048 sc1\nv_mov_b64 v[48:49], 0\nv_mov_b64 v[50:51], 0\n" \
"v_mov_b64 v[52:53], 0\nv_mov_b64 v[54:55], 0\nv_smfmac_f32_16x16x64_bf16 v[84:87], v[36:39], v[248:255], v10\ns_waitcnt lgkmcnt(9)\nds_read_b128 v[64:67], v1 offset:16384\nv_smfmac_f32_16x16x64_bf16 v[48:51], v[40:43], a[8:15], v10\ns_waitcnt lgkmcnt(8)\nds_read_b128 v[68:71], v1 offset:17408\n" \
"v_cvt_pk_bf16_f32 v92, v80, v81\nv_smfmac_f32_16x16x64_bf16 v[52:55], v[44:47], a[8:15], v10\ns_waitcnt lgkmcnt(7)\nv_cvt_pk_bf16_f32 v93, v82, v83\nv_smfmac_f32_16x16x64_bf16 v[48:51], v[72:75], v[56:63], v10\ns_waitcnt lgkmcnt(6)\nv_cvt_pk_bf16_f32 v94, v84, v85\nv_smfmac_f32_16x16x64_bf16 v[52:55], v[76:79], v[56:63], v10\n" \
"s_add_u32 s60, s55, 20480\ns_waitcnt lgkmcnt(5)\nv_cvt_pk_bf16_f32 v95, v86, v87\nv_smfmac_f32_16x16x64_bf16 v[48:51], v[16:19], a[32:39], v10\ns_waitcnt lgkmcnt(4)\nds_read_b128 v[56:59], v1 offset:12288\nv_and_or_b32 v92, v92, -2, v13\nv_smfmac_f32_16x16x64_bf16 v[52:55], v[20:23], a[32:39], v10\n" \
"s_waitcnt lgkmcnt(4)\nds_read_b128 v[60:63], v1 offset:13312\nv_and_or_b32 v94, v94, -2, v13\nv_smfmac_f32_16x16x64_bf16 v[48:51], v[24:27], a[48:55], v10\ns_waitcnt lgkmcnt(4)\nbuffer_store_dwordx4 v[92:95], v7, s[36:39], s60 offen offset:2048 sc1\nv_mov_b64 v[80:81], 0\nv_mov_b64 v[82:83], 0\n" \
"v_mov_b64 v[84:85], 0\nv_mov_b64 v[86:87], 0\nv_smfmac_f32_16x16x64_bf16 v[52:55], v[28:31], a[48:55], v10\nv_smfmac_f32_16x16x64_bf16 v[80:83], v[40:43], a[64:71], v10\nv_cvt_pk_bf16_f32 v88, v48, v49\nv_smfmac_f32_16x16x64_bf16 v[84:87], v[44:47], a[64:71], v10\nv_cvt_pk_bf16_f32 v89, v50, v51\nv_smfmac_f32_16x16x64_bf16 v[80:83], v[72:75], a[80:87], v10\n" \
"v_cvt_pk_bf16_f32 v90, v52, v53\nv_smfmac_f32_16x16x64_bf16 v[84:87], v[76:79], a[80:87], v10\ns_add_u32 s60, s55, 0\ns_waitcnt lgkmcnt(2)\nv_cvt_pk_bf16_f32 v91, v54, v55\nv_smfmac_f32_16x16x64_bf16 v[80:83], v[16:19], v[64:71], v10\nv_and_or_b32 v88, v88, -2, v13\nv_smfmac_f32_16x16x64_bf16 v[84:87], v[20:23], v[64:71], v10\n" \
"v_and_or_b32 v90, v90, -2, v13\nv_smfmac_f32_16x16x64_bf16 v[80:83], v[24:27], a[96:103], v10\nbuffer_store_dwordx4 v[88:91], v7, s[36:39], s60 offen offset:512 sc1\nv_mov_b64 v[48:49], 0\nv_mov_b64 v[50:51], 0\nv_mov_b64 v[52:53], 0\nv_mov_b64 v[54:55], 0\nv_smfmac_f32_16x16x64_bf16 v[84:87], v[28:31], a[96:103], v10\n" \
"ds_read_b128 v[64:67], v1 offset:20480\nv_smfmac_f32_16x16x64_bf16 v[48:51], v[40:43], a[112:119], v10\nds_read_b128 v[68:71], v1 offset:21504\nv_cvt_pk_bf16_f32 v92, v80, v81\nv_smfmac_f32_16x16x64_bf16 v[52:55], v[44:47], a[112:119], v10\ns_waitcnt lgkmcnt(2)\nv_cvt_pk_bf16_f32 v93, v82, v83\nv_smfmac_f32_16x16x64_bf16 v[48:51], v[72:75], v[56:63], v10\n" \
"v_cvt_pk_bf16_f32 v94, v84, v85\nv_smfmac_f32_16x16x64_bf16 v[52:55], v[76:79], v[56:63], v10\ns_add_u32 s60, s55, 4096\nv_cvt_pk_bf16_f32 v95, v86, v87\nv_smfmac_f32_16x16x64_bf16 v[48:51], v[16:19], a[136:143], v10\nds_read_b128 v[56:59], v1 offset:10240\nv_and_or_b32 v92, v92, -2, v13\nv_smfmac_f32_16x16x64_bf16 v[52:55], v[20:23], a[136:143], v10\n" \
"ds_read_b128 v[60:63], v1 offset:11264\nv_and_or_b32 v94, v94, -2, v13\nv_smfmac_f32_16x16x64_bf16 v[48:51], v[24:27], a[152:159], v10\nbuffer_store_dwordx4 v[92:95], v7, s[36:39], s60 offen offset:512 sc1\nv_mov_b64 v[80:81], 0\nv_mov_b64 v[82:83], 0\nv_mov_b64 v[84:85], 0\nv_mov_b64 v[86:87], 0\n" \
"v_smfmac_f32_16x16x64_bf16 v[52:55], v[28:31], a[152:159], v10\nv_smfmac_f32_16x16x64_bf16 v[80:83], v[40:43], a[168:175], v10\nv_cvt_pk_bf16_f32 v88, v48, v49\nv_smfmac_f32_16x16x64_bf16 v[84:87], v[44:47], a[168:175], v10\nv_cvt_pk_bf16_f32 v89, v50, v51\nv_smfmac_f32_16x16x64_bf16 v[80:83], v[72:75], a[176:183], v10\nv_cvt_pk_bf16_f32 v90, v52, v53\nv_smfmac_f32_16x16x64_bf16 v[84:87], v[76:79], a[176:183], v10\n" \
"s_add_u32 s60, s55, 16384\ns_waitcnt lgkmcnt(2)\nv_cvt_pk_bf16_f32 v91, v54, v55\nv_smfmac_f32_16x16x64_bf16 v[80:83], v[16:19], v[64:71], v10\nv_and_or_b32 v88, v88, -2, v13\nv_smfmac_f32_16x16x64_bf16 v[84:87], v[20:23], v[64:71], v10\nv_and_or_b32 v90, v90, -2, v13\nv_smfmac_f32_16x16x64_bf16 v[80:83], v[24:27], a[200:207], v10\n" \
"buffer_store_dwordx4 v[88:91], v7, s[36:39], s60 offen offset:512 sc1\nv_mov_b64 v[48:49], 0\nv_mov_b64 v[50:51], 0\nv_mov_b64 v[52:53], 0\nv_mov_b64 v[54:55], 0\nv_smfmac_f32_16x16x64_bf16 v[84:87], v[28:31], a[200:207], v10\nds_read_b128 v[64:67], v1 offset:18432\nv_smfmac_f32_16x16x64_bf16 v[48:51], v[40:43], a[216:223], v10\n" \
"ds_read_b128 v[68:71], v1 offset:19456\nv_cvt_pk_bf16_f32 v92, v80, v81\nv_smfmac_f32_16x16x64_bf16 v[52:55], v[44:47], a[216:223], v10\ns_waitcnt lgkmcnt(2)\nv_cvt_pk_bf16_f32 v93, v82, v83\nv_smfmac_f32_16x16x64_bf16 v[48:51], v[72:75], v[56:63], v10\nv_cvt_pk_bf16_f32 v94, v84, v85\nv_smfmac_f32_16x16x64_bf16 v[52:55], v[76:79], v[56:63], v10\n" \
"s_add_u32 s60, s55, 20480\nv_cvt_pk_bf16_f32 v95, v86, v87\nv_smfmac_f32_16x16x64_bf16 v[48:51], v[16:19], a[232:239], v10\nds_read_b128 v[56:59], v1 offset:14336\nv_and_or_b32 v92, v92, -2, v13\nv_smfmac_f32_16x16x64_bf16 v[52:55], v[20:23], a[232:239], v10\nds_read_b128 v[60:63], v1 offset:15360\nv_and_or_b32 v94, v94, -2, v13\n" \
"v_smfmac_f32_16x16x64_bf16 v[48:51], v[24:27], a[248:255], v10\nbuffer_store_dwordx4 v[92:95], v7, s[36:39], s60 offen offset:512 sc1\nv_mov_b64 v[80:81], 0\nv_mov_b64 v[82:83], 0\nv_mov_b64 v[84:85], 0\nv_mov_b64 v[86:87], 0\nv_smfmac_f32_16x16x64_bf16 v[52:55], v[28:31], a[248:255], v10\nv_smfmac_f32_16x16x64_bf16 v[80:83], v[40:43], v[104:111], v10\n" \
"v_cvt_pk_bf16_f32 v88, v48, v49\nv_smfmac_f32_16x16x64_bf16 v[84:87], v[44:47], v[104:111], v10\nv_cvt_pk_bf16_f32 v89, v50, v51\nv_smfmac_f32_16x16x64_bf16 v[80:83], v[72:75], v[120:127], v10\nv_cvt_pk_bf16_f32 v90, v52, v53\nv_smfmac_f32_16x16x64_bf16 v[84:87], v[76:79], v[120:127], v10\ns_add_u32 s60, s55, 0\ns_waitcnt lgkmcnt(2)\n" \
"v_cvt_pk_bf16_f32 v91, v54, v55\nv_smfmac_f32_16x16x64_bf16 v[80:83], v[16:19], v[64:71], v10\nv_and_or_b32 v88, v88, -2, v13\nv_smfmac_f32_16x16x64_bf16 v[84:87], v[20:23], v[64:71], v10\nv_and_or_b32 v90, v90, -2, v13\nv_smfmac_f32_16x16x64_bf16 v[80:83], v[24:27], v[144:151], v10\nbuffer_store_dwordx4 v[88:91], v7, s[36:39], s60 offen offset:2560 sc1\nv_mov_b64 v[48:49], 0\n" \
"v_mov_b64 v[50:51], 0\nv_mov_b64 v[52:53], 0\nv_mov_b64 v[54:55], 0\nv_smfmac_f32_16x16x64_bf16 v[84:87], v[28:31], v[144:151], v10\nds_read_b128 v[64:67], v1 offset:22528\nv_smfmac_f32_16x16x64_bf16 v[48:51], v[40:43], v[160:167], v10\nds_read_b128 v[68:71], v1 offset:23552\nv_cvt_pk_bf16_f32 v92, v80, v81\n" \
"v_smfmac_f32_16x16x64_bf16 v[52:55], v[44:47], v[160:167], v10\ns_waitcnt lgkmcnt(2)\nv_cvt_pk_bf16_f32 v93, v82, v83\nv_smfmac_f32_16x16x64_bf16 v[48:51], v[72:75], v[56:63], v10\nv_cvt_pk_bf16_f32 v94, v84, v85\nv_smfmac_f32_16x16x64_bf16 v[52:55], v[76:79], v[56:63], v10\ns_add_u32 s60, s55, 4096\nv_cvt_pk_bf16_f32 v95, v86, v87\n" \
"v_smfmac_f32_16x16x64_bf16 v[48:51], v[16:19], v[176:183], v10\nv_and_or_b32 v92, v92, -2, v13\nv_smfmac_f32_16x16x64_bf16 v[52:55], v[20:23], v[176:183], v10\nv_and_or_b32 v94, v94, -2, v13\nv_smfmac_f32_16x16x64_bf16 v[48:51], v[24:27], v[192:199], v10\nbuffer_store_dwordx4 v[92:95], v7, s[36:39], s60 offen offset:2560 sc1\nv_mov_b64 v[80:81], 0\nv_mov_b64 v[82:83], 0\n" \
"v_mov_b64 v[84:85], 0\nv_mov_b64 v[86:87], 0\nv_smfmac_f32_16x16x64_bf16 v[52:55], v[28:31], v[192:199], v10\nds_read_b128 v[32:35], v2 offset:0\nv_smfmac_f32_16x16x64_bf16 v[80:83], v[40:43], v[208:215], v10\nds_read_b128 v[36:39], v2 offset:256\nv_cvt_pk_bf16_f32 v88, v48, v49\nv_smfmac_f32_16x16x64_bf16 v[84:87], v[44:47], v[208:215], v10\n" \
"v_cvt_pk_bf16_f32 v89, v50, v51\nv_smfmac_f32_16x16x64_bf16 v[80:83], v[72:75], v[224:231], v10\ns_add_u32 s60, s55, 16384\nds_read_b128 v[40:43], v2 offset:2048\nv_cvt_pk_bf16_f32 v90, v52, v53\nv_smfmac_f32_16x16x64_bf16 v[84:87], v[76:79], v[224:231], v10\ns_waitcnt lgkmcnt(3)\nds_read_b128 v[44:47], v2 offset:2304\n" \
"v_cvt_pk_bf16_f32 v91, v54, v55\nv_smfmac_f32_16x16x64_bf16 v[80:83], v[16:19], v[64:71], v10\nv_and_or_b32 v88, v88, -2, v13\nv_smfmac_f32_16x16x64_bf16 v[84:87], v[20:23], v[64:71], v10\nv_and_or_b32 v90, v90, -2, v13\nv_smfmac_f32_16x16x64_bf16 v[80:83], v[24:27], v[248:255], v10\nbuffer_store_dwordx4 v[88:91], v7, s[36:39], s60 offen offset:2560 sc1\nv_mov_b64 v[48:49], 0\n" \
"v_mov_b64 v[50:51], 0\nv_mov_b64 v[52:53], 0\nv_mov_b64 v[54:55], 0\nv_smfmac_f32_16x16x64_bf16 v[84:87], v[28:31], v[248:255], v10\ns_add_u32 s60, s55, 20480\ns_cmp_eq_u64 s[64:65], 0\nv_mov_b32 v15, s40\ns_cbranch_scc1 .Lvu_tok%=\n" \
"v_cvt_pk_bf16_f32 v92, v80, v81\nv_cvt_pk_bf16_f32 v93, v82, v83\ns_nop 1\nv_cvt_pk_bf16_f32 v94, v84, v85\nv_cvt_pk_bf16_f32 v95, v86, v87\nv_and_or_b32 v92, v92, -2, v13\nv_and_or_b32 v94, v94, -2, v13\nbuffer_store_dwordx4 v[92:95], v7, s[36:39], s60 offen offset:2560 sc1\n" \
"s_mov_b32 s60, -2147483648\ns_cmp_lt_i32 s40, 63\ns_cbranch_scc1 .Lvu_f0%=\ns_mov_b32 s61, -2147483648\nv_add_u32 v14, s57, v0\ns_waitcnt lgkmcnt(0)\n.Lvu_nov%=:\ns_add_u32 m0, s57, 16384\n" \
"s_nop 0\nbuffer_load_dwordx4 v3, s[36:39], s42 offen offset:0 sc1 lds\nbuffer_load_dwordx4 v3, s[36:39], s42 offen offset:1024 sc1 lds\nbuffer_load_dwordx4 v3, s[36:39], s42 offen offset:2048 sc1 lds\nbuffer_load_dwordx4 v3, s[36:39], s42 offen offset:3072 sc1 lds\ns_waitcnt vmcnt(0)\nds_read_b128 v[80:83], v14 offset:16384\nds_read_b128 v[84:87], v14 offset:17408\n" \
"ds_read_b128 v[88:91], v14 offset:18432\nds_read_b128 v[92:95], v14 offset:19456\ns_waitcnt lgkmcnt(2)\nv_max3_u32 v72, v80, v82, v84\ns_waitcnt lgkmcnt(1)\nv_max3_u32 v72, v72, v86, v88\ns_waitcnt lgkmcnt(0)\nv_max3_u32 v72, v72, v90, v92\n" \
"v_max_u32 v72, v72, v94\nv_cmp_eq_u32 vcc, -1, v72\ns_cbranch_vccz .Lvu_novfok%=\ns_add_u32 s43, s43, 1\ns_cmp_lt_u32 s43, 262144\ns_cbranch_scc1 .Lvu_nov%=\ns_branch .Lvu_abort%=\n.Lvu_novfok%=:\n" \
"s_waitcnt lgkmcnt(0)\ns_barrier\ns_memrealtime s[68:69]\nds_read_b128 v[56:59], v1 offset:0\nds_read_b128 v[60:63], v1 offset:1024\nds_read_b128 v[80:83], v0 offset:16384\nds_read_b128 v[84:87], v0 offset:17408\nds_read_b128 v[88:91], v0 offset:18432\n" \
"ds_read_b128 v[92:95], v0 offset:19456\nds_read_b128 v[16:19], v0 offset:20480\nds_read_b128 v[20:23], v0 offset:21504\nds_read_b128 v[32:35], v0 offset:22528\nds_read_b128 v[36:39], v0 offset:23552\ns_waitcnt lgkmcnt(0)\ns_branch .Lvu_crit%=\n.Lvu_re%=:\n" \
".Lvu_re2%=:\ns_add_u32 m0, s57, 16384\nv_max_u32 v72, v80, v82\nv_cmp_eq_u32 vcc, -1, v72\ns_cbranch_vccz .Lvu_sk1887%=\ns_mov_b64 exec, vcc\nbuffer_load_dwordx4 v3, s[36:39], s42 offen offset:0 sc1 lds\ns_mov_b64 exec, -1\n" \
".Lvu_sk1887%=:\nv_max_u32 v72, v84, v86\nv_cmp_eq_u32 vcc, -1, v72\ns_cbranch_vccz .Lvu_sk1894%=\ns_mov_b64 exec, vcc\nbuffer_load_dwordx4 v3, s[36:39], s42 offen offset:1024 sc1 lds\ns_mov_b64 exec, -1\n.Lvu_sk1894%=:\n" \
"v_max_u32 v72, v88, v90\nv_cmp_eq_u32 vcc, -1, v72\ns_cbranch_vccz .Lvu_sk1901%=\ns_mov_b64 exec, vcc\nbuffer_load_dwordx4 v3, s[36:39], s42 offen offset:2048 sc1 lds\ns_mov_b64 exec, -1\n.Lvu_sk1901%=:\nv_max_u32 v72, v92, v94\n" \
"v_cmp_eq_u32 vcc, -1, v72\ns_cbranch_vccz .Lvu_sk1908%=\ns_mov_b64 exec, vcc\nbuffer_load_dwordx4 v3, s[36:39], s42 offen offset:3072 sc1 lds\ns_mov_b64 exec, -1\n.Lvu_sk1908%=:\n.Lvu_skf%=:\ns_waitcnt vmcnt(0)\n" \
"ds_read_b128 v[80:83], v14 offset:16384\nds_read_b128 v[84:87], v14 offset:17408\nds_read_b128 v[88:91], v14 offset:18432\nds_read_b128 v[92:95], v14 offset:19456\ns_waitcnt lgkmcnt(2)\nv_max3_u32 v72, v80, v82, v84\ns_waitcnt lgkmcnt(1)\nv_max3_u32 v72, v72, v86, v88\n" \
"s_waitcnt lgkmcnt(0)\nv_max3_u32 v72, v72, v90, v92\nv_max_u32 v72, v72, v94\nv_cmp_eq_u32 vcc, -1, v72\ns_cbranch_vccz .Lvu_rer%=\ns_add_u32 s43, s43, 1\ns_cmp_lt_u32 s43, 262144\ns_cbranch_scc1 .Lvu_re2%=\n" \
"s_branch .Lvu_abort%=\n.Lvu_rew%=:\nds_read_b128 v[80:83], v14 offset:16384\nds_read_b128 v[84:87], v14 offset:17408\nds_read_b128 v[88:91], v14 offset:18432\nds_read_b128 v[92:95], v14 offset:19456\ns_waitcnt lgkmcnt(0)\ns_branch .Lvu_rer%=\n" \
".Lvu_first%=:\ns_nop 7\nv_cvt_pk_bf16_f32 v76, v24, v25\nv_cvt_pk_bf16_f32 v77, v26, v27\nv_cvt_pk_bf16_f32 v78, v28, v29\nv_cvt_pk_bf16_f32 v79, v30, v31\nds_write2st64_b64 v9, v[76:77], v[78:79] offset0:2 offset1:10\ns_waitcnt vmcnt(0) lgkmcnt(0)\n" \
"s_barrier\nds_read_b128 v[56:59], v1 offset:8192\nds_read_b128 v[60:63], v1 offset:9216\nds_read_b128 v[64:67], v1 offset:16384\nds_read_b128 v[68:71], v1 offset:17408\nds_read_b128 v[72:75], v11 offset:49152\nds_read_b128 v[76:79], v11 offset:50176\nds_read_b128 v[16:19], v11 offset:51200\n" \
"ds_read_b128 v[20:23], v11 offset:52224\nds_read_b128 v[24:27], v11 offset:53248\nds_read_b128 v[28:31], v11 offset:54272\nds_read_b128 v[32:35], v11 offset:55296\nds_read_b128 v[36:39], v11 offset:56320\ns_waitcnt lgkmcnt(0)\ns_add_u32 m0, s57, 0\ns_add_u32 s58, s61, 0\n" \
"buffer_load_dwordx4 v0, s[36:39], s58 offen offset:0 sc1 lds\nbuffer_load_dwordx4 v0, s[36:39], s58 offen offset:1024 sc1 lds\nbuffer_load_dwordx4 v0, s[36:39], s58 offen offset:2048 sc1 lds\nbuffer_load_dwordx4 v0, s[36:39], s58 offen offset:3072 sc1 lds\ns_mov_b32 s59, -2147483648\nbuffer_store_dword v14, off, s[36:39], s59\nbuffer_store_dword v14, off, s[36:39], s59\nbuffer_store_dword v14, off, s[36:39], s59\n" \
"s_branch .Lvu_y%=\n.Lvu_last%=:\ns_nop 7\nv_cvt_pk_bf16_f32 v92, v40, v41\nv_and_or_b32 v92, v92, -2, v12\ns_add_u32 s63, s59, 4096\nbuffer_store_dwordx4 v[88:91], v6, s[36:39], s63 offen sc1\nv_cvt_pk_bf16_f32 v93, v42, v43\n" \
"v_cvt_pk_bf16_f32 v94, v44, v45\nv_cvt_pk_bf16_f32 v95, v46, v47\nv_and_or_b32 v94, v94, -2, v12\ns_nop 0\nv_permlane16_swap_b32 v93, v95\nv_permlane16_swap_b32 v92, v94\ns_add_u32 s63, s59, 135168\nbuffer_store_dwordx4 v[92:95], v6, s[36:39], s63 offen sc1\n" \
"s_branch .Lvu_end%=\n.Lvu_abort%=:\ns_mov_b64 exec, -1\nv_mov_b32 v16, 0\ns_movk_i32 s44, 6400\nbuffer_store_dword v16, off, s[36:39], s44 sc1\n.Lvu_end%=:\ns_waitcnt vmcnt(0) lgkmcnt(0)\n" \
""
#define RD_ASM \
"v_mbcnt_lo_u32_b32 v255, -1, 0\nv_mbcnt_hi_u32_b32 v255, -1, v255\nv_lshlrev_b32 v255, 2, v255\nv_add_u32 v255, %[tb], v255\nds_read_b32 v196, v255 offset:0\nds_read_b32 v197, v255 offset:256\nds_read_b32 v198, v255 offset:512\nds_read_b32 v199, v255 offset:768\n" \
"ds_read_b32 v200, v255 offset:1024\nds_read_b32 v201, v255 offset:1280\nds_read_b32 v202, v255 offset:1536\nds_read_b32 v203, v255 offset:1792\nds_read_b32 v204, v255 offset:2048\nds_read_b32 v205, v255 offset:2304\nds_read_b32 v206, v255 offset:2560\nds_read_b32 v207, v255 offset:2816\n" \
"s_waitcnt lgkmcnt(0)\nv_accvgpr_write_b32 a32, v206\nv_accvgpr_write_b32 a33, v207\ns_mov_b32 s40, 0\ns_mov_b32 s43, 0\ns_add_u32 s57, %[su], 4096\ns_add_u32 s56, %[sp], 4096\ns_mov_b32 s41, %[sy]\n" \
"s_mov_b32 s59, -2147483648\n.Lrd_yflag%=:\nv_accvgpr_read_b32 v206, a32\nbuffer_load_dword v208, v206, %[rs], 0 offen sc1\ns_waitcnt vmcnt(0)\nv_cmp_gt_i32 vcc, s40, v208\ns_cbranch_vccz .Lrd_yld%=\ns_add_u32 s43, s43, 1\n" \
"s_cmp_lt_u32 s43, 262144\ns_cbranch_scc1 .Lrd_yflag%=\ns_branch .Lrd_abort%=\n.Lrd_yld%=:\ns_add_u32 s60, s41, 0\nbuffer_load_dwordx4 v[0:3], v196, %[rs], s60 offen offset:0 sc1\ns_add_u32 s60, s41, 0\nbuffer_load_dwordx4 v[32:35], v196, %[rs], s60 offen offset:1024 sc1\n" \
"s_add_u32 s60, s41, 0\nbuffer_load_dwordx4 v[64:67], v196, %[rs], s60 offen offset:2048 sc1\ns_add_u32 s60, s41, 131072\nbuffer_load_dwordx4 v[4:7], v196, %[rs], s60 offen offset:0 sc1\ns_add_u32 s60, s41, 131072\nbuffer_load_dwordx4 v[36:39], v196, %[rs], s60 offen offset:1024 sc1\ns_add_u32 s60, s41, 131072\nbuffer_load_dwordx4 v[68:71], v196, %[rs], s60 offen offset:2048 sc1\n" \
"s_add_u32 s60, s41, 262144\nbuffer_load_dwordx4 v[8:11], v196, %[rs], s60 offen offset:0 sc1\ns_add_u32 s60, s41, 262144\nbuffer_load_dwordx4 v[40:43], v196, %[rs], s60 offen offset:1024 sc1\ns_add_u32 s60, s41, 262144\nbuffer_load_dwordx4 v[72:75], v196, %[rs], s60 offen offset:2048 sc1\ns_add_u32 s60, s41, 393216\nbuffer_load_dwordx4 v[12:15], v196, %[rs], s60 offen offset:0 sc1\n" \
"s_add_u32 s60, s41, 393216\nbuffer_load_dwordx4 v[44:47], v196, %[rs], s60 offen offset:1024 sc1\ns_add_u32 s60, s41, 393216\nbuffer_load_dwordx4 v[76:79], v196, %[rs], s60 offen offset:2048 sc1\ns_add_u32 s60, s41, 524288\nbuffer_load_dwordx4 v[16:19], v196, %[rs], s60 offen offset:0 sc1\ns_add_u32 s60, s41, 524288\nbuffer_load_dwordx4 v[48:51], v196, %[rs], s60 offen offset:1024 sc1\n" \
"s_add_u32 s60, s41, 524288\nbuffer_load_dwordx4 v[80:83], v196, %[rs], s60 offen offset:2048 sc1\ns_add_u32 s60, s41, 655360\nbuffer_load_dwordx4 v[20:23], v196, %[rs], s60 offen offset:0 sc1\ns_add_u32 s60, s41, 655360\nbuffer_load_dwordx4 v[52:55], v196, %[rs], s60 offen offset:1024 sc1\ns_add_u32 s60, s41, 655360\nbuffer_load_dwordx4 v[84:87], v196, %[rs], s60 offen offset:2048 sc1\n" \
"s_add_u32 s60, s41, 786432\nbuffer_load_dwordx4 v[24:27], v196, %[rs], s60 offen offset:0 sc1\ns_add_u32 s60, s41, 786432\nbuffer_load_dwordx4 v[56:59], v196, %[rs], s60 offen offset:1024 sc1\ns_add_u32 s60, s41, 786432\nbuffer_load_dwordx4 v[88:91], v196, %[rs], s60 offen offset:2048 sc1\ns_add_u32 s60, s41, 917504\nbuffer_load_dwordx4 v[28:31], v196, %[rs], s60 offen offset:0 sc1\n" \
"s_add_u32 s60, s41, 917504\nbuffer_load_dwordx4 v[60:63], v196, %[rs], s60 offen offset:1024 sc1\ns_add_u32 s60, s41, 917504\nbuffer_load_dwordx4 v[92:95], v196, %[rs], s60 offen offset:2048 sc1\ns_add_u32 s60, s41, 0\nbuffer_load_dwordx4 v[96:99], v196, %[rs], s60 offen offset:3072 sc1\ns_add_u32 s60, s41, 131072\nbuffer_load_dwordx4 v[100:103], v196, %[rs], s60 offen offset:3072 sc1\n" \
"s_add_u32 s60, s41, 262144\nbuffer_load_dwordx4 v[104:107], v196, %[rs], s60 offen offset:3072 sc1\ns_add_u32 s60, s41, 393216\nbuffer_load_dwordx4 v[108:111], v196, %[rs], s60 offen offset:3072 sc1\ns_add_u32 s60, s41, 524288\nbuffer_load_dwordx4 v[112:115], v196, %[rs], s60 offen offset:3072 sc1\ns_add_u32 s60, s41, 655360\nbuffer_load_dwordx4 v[116:119], v196, %[rs], s60 offen offset:3072 sc1\n" \
"s_add_u32 s60, s41, 786432\nbuffer_load_dwordx4 v[120:123], v196, %[rs], s60 offen offset:3072 sc1\ns_add_u32 s60, s41, 917504\nbuffer_load_dwordx4 v[124:127], v196, %[rs], s60 offen offset:3072 sc1\ns_mov_b64 exec, -1\nv_accvgpr_read_b32 v212, a33\nbuffer_load_dwordx2 v[214:215], v212, %[rs], 0 offen sc1\ns_waitcnt vmcnt(0)\n" \
"s_branch .Lrd_ychk%=\n.Lrd_u2%=:\nbuffer_load_dwordx4 v[136:139], v196, %[rs], %[su] offen offset:2048 sc1\nbuffer_load_dwordx4 v[140:143], v196, %[rs], %[su] offen offset:3072 sc1\ns_add_u32 s43, s43, 1\ns_cmp_ge_u32 s43, 262144\ns_cbranch_scc1 .Lrd_abort%=\ns_waitcnt vmcnt(0)\n" \
"v_bitop3_b32 v208, v136, v138, s54 bitop3:0x7e\nv_bitop3_b32 v209, v140, v142, s54 bitop3:0x7e\nv_bitop3_b32 v208, v208, 1, v209 bitop3:0xc8\nv_cmp_ne_u32 vcc, 0, v208\ns_cbranch_vccnz .Lrd_u2%=\n.Lrd_u3%=:\nbuffer_load_dwordx4 v[128:131], v196, %[rs], %[su] offen offset:0 sc1\nbuffer_load_dwordx4 v[132:135], v196, %[rs], %[su] offen offset:1024 sc1\n" \
"buffer_load_dwordx4 v[144:147], v196, %[rs], s57 offen offset:0 sc1\nbuffer_load_dwordx4 v[148:151], v196, %[rs], s57 offen offset:1024 sc1\nbuffer_load_dwordx4 v[152:155], v196, %[rs], s57 offen offset:2048 sc1\nbuffer_load_dwordx4 v[156:159], v196, %[rs], s57 offen offset:3072 sc1\nbuffer_load_dwordx4 v[160:163], v196, %[rs], %[sp] offen offset:0 sc1\nbuffer_load_dwordx4 v[164:167], v196, %[rs], %[sp] offen offset:1024 sc1\nbuffer_load_dwordx4 v[168:171], v196, %[rs], %[sp] offen offset:2048 sc1\nbuffer_load_dwordx4 v[172:175], v196, %[rs], %[sp] offen offset:3072 sc1\n" \
"buffer_load_dwordx4 v[176:179], v196, %[rs], s56 offen offset:0 sc1\n.Lrd_ra%=:\ns_waitcnt vmcnt(5)\nv_bitop3_b32 v208, v128, v130, s54 bitop3:0x7e\nv_bitop3_b32 v209, v136, v138, s54 bitop3:0x7e\nv_bitop3_b32 v208, v208, 1, v209 bitop3:0xc8\nv_cmp_ne_u32 s[86:87], 0, v208\nv_bitop3_b32 v208, v144, v146, s54 bitop3:0x7e\n" \
"v_bitop3_b32 v209, v152, v154, s54 bitop3:0x7e\nv_bitop3_b32 v208, v208, 1, v209 bitop3:0xc8\nv_cmp_ne_u32 s[88:89], 0, v208\nv_bitop3_b32 v208, v132, v134, s54 bitop3:0x7e\nv_bitop3_b32 v209, v140, v142, s54 bitop3:0x7e\nv_bitop3_b32 v208, v208, 1, v209 bitop3:0xc8\nv_cmp_ne_u32 s[68:69], 0, v208\nv_bitop3_b32 v208, v148, v150, s54 bitop3:0x7e\n" \
"v_bitop3_b32 v209, v156, v158, s54 bitop3:0x7e\nv_bitop3_b32 v208, v208, 1, v209 bitop3:0xc8\nv_cmp_ne_u32 s[62:63], 0, v208\ns_or_b64 vcc, s[86:87], s[88:89]\ns_or_b64 vcc, vcc, s[68:69]\ns_or_b64 vcc, vcc, s[62:63]\ns_waitcnt vmcnt(4)\nv_smfmac_f32_16x16x64_bf16 v[180:183], v[160:163], v[0:7], v197\n" \
"v_bitop3_b32 v208, v160, v162, s54 bitop3:0x7e\nv_smfmac_f32_16x16x64_bf16 v[184:187], v[160:163], v[32:39], v197\nv_cmp_gt_i32 s[46:47], 0, v208\nv_smfmac_f32_16x16x64_bf16 v[188:191], v[160:163], v[64:71], v197\nv_smfmac_f32_16x16x64_bf16 v[192:195], v[160:163], v[96:103], v197\ns_or_b64 vcc, vcc, s[46:47]\ns_waitcnt vmcnt(3)\nv_smfmac_f32_16x16x64_bf16 v[180:183], v[164:167], v[8:15], v197\n" \
"v_bitop3_b32 v208, v164, v166, s54 bitop3:0x7e\nv_smfmac_f32_16x16x64_bf16 v[184:187], v[164:167], v[40:47], v197\nv_cmp_gt_i32 s[48:49], 0, v208\nv_smfmac_f32_16x16x64_bf16 v[188:191], v[164:167], v[72:79], v197\nv_smfmac_f32_16x16x64_bf16 v[192:195], v[164:167], v[104:111], v197\ns_or_b64 vcc, vcc, s[48:49]\ns_waitcnt vmcnt(2)\nv_smfmac_f32_16x16x64_bf16 v[180:183], v[168:171], v[16:23], v197\n" \
"v_bitop3_b32 v208, v168, v170, s54 bitop3:0x7e\nv_smfmac_f32_16x16x64_bf16 v[184:187], v[168:171], v[48:55], v197\nv_cmp_gt_i32 s[50:51], 0, v208\nv_smfmac_f32_16x16x64_bf16 v[188:191], v[168:171], v[80:87], v197\nv_smfmac_f32_16x16x64_bf16 v[192:195], v[168:171], v[112:119], v197\ns_or_b64 vcc, vcc, s[50:51]\ns_waitcnt vmcnt(1)\nv_smfmac_f32_16x16x64_bf16 v[180:183], v[172:175], v[24:31], v197\n" \
"s_waitcnt vmcnt(0)\nv_bitop3_b32 v208, v172, v174, s54 bitop3:0x7e\nv_smfmac_f32_16x16x64_bf16 v[180:183], v[176:179], v[128:135], v197\nv_bitop3_b32 v209, v176, v178, s54 bitop3:0x7e\nv_smfmac_f32_16x16x64_bf16 v[184:187], v[172:175], v[56:63], v197\nv_bitop3_b32 v210, v177, v179, s54 bitop3:0x7e\nv_smfmac_f32_16x16x64_bf16 v[184:187], v[176:179], v[136:143], v197\nv_cmp_gt_i32 s[52:53], 0, v208\n" \
"v_smfmac_f32_16x16x64_bf16 v[188:191], v[172:175], v[88:95], v197\nv_or_b32 v209, v209, v210\nv_smfmac_f32_16x16x64_bf16 v[188:191], v[176:179], v[144:151], v197\ns_or_b64 vcc, vcc, s[52:53]\nv_cmp_gt_i32 s[64:65], 0, v209\nv_smfmac_f32_16x16x64_bf16 v[192:195], v[172:175], v[120:127], v197\nv_smfmac_f32_16x16x64_bf16 v[192:195], v[176:179], v[152:159], v197\ns_or_b64 vcc, vcc, s[64:65]\n" \
"s_cbranch_scc1 .Lrd_rb%=\ns_add_u32 s60, s58, 16384\nv_add_f32 v180, v180, v182\nv_add_f32 v181, v181, v183\nds_write2_b32 v198, v180, v181 offset0:0 offset1:8\nbuffer_load_dwordx4 v[212:215], v196, %[rs], s60 offen offset:0 sc1\nv_add_f32 v184, v184, v186\nv_add_f32 v185, v185, v187\n" \
"ds_write2_b32 v198, v184, v185 offset0:32 offset1:40\ns_add_u32 s60, s58, 0\nbuffer_load_dwordx4 v[216:219], v196, %[rs], s60 offen offset:0 sc1\nv_add_f32 v188, v188, v190\nv_add_f32 v189, v189, v191\nds_write2_b32 v198, v188, v189 offset0:64 offset1:72\ns_add_u32 s60, s58, 0\nbuffer_load_dwordx4 v[220:223], v196, %[rs], s60 offen offset:1024 sc1\n" \
"v_add_f32 v192, v192, v194\nv_add_f32 v193, v193, v195\nds_write2_b32 v198, v192, v193 offset0:96 offset1:104\ns_add_u32 s60, s58, 16384\nbuffer_load_dwordx4 v[224:227], v196, %[rs], s60 offen offset:1024 sc1\ns_waitcnt lgkmcnt(0)\ns_barrier\nds_read_b128 v[180:183], v199 offset:0\n" \
"ds_read_b128 v[184:187], v199 offset:4096\nds_read_b128 v[188:191], v199 offset:8192\nds_read_b128 v[192:195], v199 offset:12288\ns_and_b32 s44, s54, 0x80008000\ns_add_u32 s60, s58, 0\nbuffer_load_dwordx4 v[228:231], v196, %[rs], s60 offen offset:2048 sc1\ns_waitcnt lgkmcnt(2)\nv_pk_add_f32 v[182:183], v[182:183], v[186:187]\n" \
"v_pk_add_f32 v[180:181], v[180:181], v[184:185]\ns_waitcnt lgkmcnt(0)\nv_pk_add_f32 v[186:187], v[188:189], v[192:193]\nv_pk_add_f32 v[184:185], v[190:191], v[194:195]\nv_pk_add_f32 v[180:181], v[180:181], v[186:187]\nv_pk_add_f32 v[182:183], v[182:183], v[184:185]\nv_cvt_pk_bf16_f32 v180, v180, v181\nv_xor_b32 v180, s44, v180\n" \
"v_cvt_pk_bf16_f32 v181, v182, v183\nv_min_u32 v180, -2, v180\nv_xor_b32 v209, s44, v181\nv_or_b32 v208, v180, v201\nbuffer_store_dwordx2 v[208:209], v206, %[rs], 0 offen sc1\ns_add_u32 s60, s58, 0\nbuffer_load_dwordx4 v[232:235], v196, %[rs], s60 offen offset:3072 sc1\ns_add_u32 s60, s58, 16384\n" \
"buffer_load_dwordx4 v[236:239], v196, %[rs], s60 offen offset:2048 sc1\ns_add_u32 s60, s58, 4096\nbuffer_load_dwordx4 v[240:243], v196, %[rs], s60 offen offset:0 sc1\ns_add_u32 s60, s58, 4096\nbuffer_load_dwordx4 v[244:247], v196, %[rs], s60 offen offset:1024 sc1\ns_add_u32 s60, s58, 16384\nbuffer_load_dwordx4 v[248:251], v196, %[rs], s60 offen offset:3072 sc1\ns_xor_b32 s41, s41, 4194304\n" \
"v_mov_b32 v207, s40\ns_waitcnt vmcnt(10)\nv_bitop3_b32 v209, v212, v214, s61 bitop3:0x7e\nv_cmp_gt_i32 vcc, 0, v209\ns_cbranch_vccnz .Lrd_tre0%=\n.Lrd_tl0%=:\nv_mfma_f32_16x16x16_bf16 a[0:3], v[0:1], v[212:213], 0\ns_add_u32 s60, s58, 20480\n" \
"buffer_load_dwordx4 v[252:255], v196, %[rs], s60 offen offset:0 sc1\nv_mfma_f32_16x16x16_bf16 a[8:11], v[32:33], v[212:213], 0\nv_mfma_f32_16x16x16_bf16 a[16:19], v[64:65], v[212:213], 0\nv_mfma_f32_16x16x16_bf16 a[24:27], v[96:97], v[212:213], 0\ns_waitcnt vmcnt(10)\nv_mfma_f32_16x16x16_bf16 a[0:3], v[4:5], v[214:215], a[0:3]\ns_add_u32 s60, s58, 20480\nbuffer_load_dwordx4 v[160:163], v196, %[rs], s60 offen offset:2048 sc1\n" \
"v_bitop3_b32 v209, v216, v218, s61 bitop3:0x7e\nv_mfma_f32_16x16x16_bf16 a[8:11], v[36:37], v[214:215], a[8:11]\ns_lshl_b32 s42, s40, 17\ns_add_u32 s42, s42, %[at]\nv_mfma_f32_16x16x16_bf16 a[16:19], v[68:69], v[214:215], a[16:19]\nv_cmp_gt_i32 vcc, 0, v209\nv_mfma_f32_16x16x16_bf16 a[24:27], v[100:101], v[214:215], a[24:27]\ns_cbranch_vccnz .Lrd_tre1%=\n" \
".Lrd_tl1%=:\ns_waitcnt vmcnt(10)\nv_mfma_f32_16x16x32_bf16 a[4:7], v[0:3], v[216:219], 0\ns_add_u32 s60, s58, 4096\nbuffer_load_dwordx4 v[164:167], v196, %[rs], s60 offen offset:2048 sc1\nv_bitop3_b32 v209, v220, v222, s61 bitop3:0x7e\nv_mfma_f32_16x16x32_bf16 a[12:15], v[32:35], v[216:219], 0\ns_lshr_b32 s45, s42, 8\n" \
"s_and_b32 s45, s45, 0x2000\nv_mfma_f32_16x16x32_bf16 a[20:23], v[64:67], v[216:219], 0\nv_cmp_gt_i32 vcc, 0, v209\nv_mfma_f32_16x16x32_bf16 a[28:31], v[96:99], v[216:219], 0\ns_cbranch_vccnz .Lrd_tre2%=\n.Lrd_tl2%=:\ns_waitcnt vmcnt(10)\nv_mfma_f32_16x16x32_bf16 a[4:7], v[4:7], v[220:223], a[4:7]\n" \
"s_add_u32 s60, s58, 4096\nbuffer_load_dwordx4 v[168:171], v196, %[rs], s60 offen offset:3072 sc1\nv_bitop3_b32 v209, v224, v226, s61 bitop3:0x7e\nv_mfma_f32_16x16x32_bf16 a[12:15], v[36:39], v[220:223], a[12:15]\ns_xor_b32 s42, s42, s45\ns_add_u32 s40, s40, 1\nv_mfma_f32_16x16x32_bf16 a[20:23], v[68:71], v[220:223], a[20:23]\nv_cmp_gt_i32 vcc, 0, v209\n" \
"v_mfma_f32_16x16x32_bf16 a[28:31], v[100:103], v[220:223], a[28:31]\ns_cbranch_vccnz .Lrd_tre3%=\n.Lrd_tl3%=:\nv_mfma_f32_16x16x16_bf16 a[0:3], v[8:9], v[224:225], a[0:3]\ns_add_u32 s60, s58, 20480\nbuffer_load_dwordx4 v[172:175], v196, %[rs], s60 offen offset:1024 sc1\nv_mfma_f32_16x16x16_bf16 a[8:11], v[40:41], v[224:225], a[8:11]\nv_mfma_f32_16x16x16_bf16 a[16:19], v[72:73], v[224:225], a[16:19]\n" \
"buffer_load_dwordx4 v[212:215], v204, %[rs], s42 offen sc1\nv_mfma_f32_16x16x16_bf16 a[24:27], v[104:105], v[224:225], a[24:27]\ns_waitcnt vmcnt(12)\nv_mfma_f32_16x16x16_bf16 a[0:3], v[12:13], v[226:227], a[0:3]\ns_add_u32 s60, s58, 20480\nbuffer_load_dwordx4 v[176:179], v196, %[rs], s60 offen offset:3072 sc1\nv_bitop3_b32 v209, v228, v230, s61 bitop3:0x7e\nv_mfma_f32_16x16x16_bf16 a[8:11], v[44:45], v[226:227], a[8:11]\n" \
"s_lshl_b32 s62, s55, 31\ns_or_b32 s62, s62, 1.0\ns_mov_b32 s63, s62\nv_mfma_f32_16x16x16_bf16 a[16:19], v[76:77], v[226:227], a[16:19]\nv_cmp_gt_i32 vcc, 0, v209\nv_mfma_f32_16x16x16_bf16 a[24:27], v[108:109], v[226:227], a[24:27]\ns_cbranch_vccnz .Lrd_tre4%=\n.Lrd_tl4%=:\n" \
"s_waitcnt vmcnt(11)\nv_mfma_f32_16x16x32_bf16 a[4:7], v[8:11], v[228:231], a[4:7]\ns_cmp_eq_u32 s40, 1\ns_cbranch_scc1 .Lrd_y1%=\n.Lrd_y1r%=:\ns_add_u32 s60, s41, 0\nbuffer_load_dwordx4 v[0:3], v196, %[rs], s60 offen offset:0 sc1\nv_bitop3_b32 v209, v232, v234, s61 bitop3:0x7e\n" \
"v_mfma_f32_16x16x32_bf16 a[12:15], v[40:43], v[228:231], a[12:15]\nv_mfma_f32_16x16x32_bf16 a[20:23], v[72:75], v[228:231], a[20:23]\nv_cmp_gt_i32 vcc, 0, v209\nv_mfma_f32_16x16x32_bf16 a[28:31], v[104:107], v[228:231], a[28:31]\ns_cbranch_vccnz .Lrd_tre5%=\n.Lrd_tl5%=:\ns_waitcnt vmcnt(11)\nv_mfma_f32_16x16x32_bf16 a[4:7], v[12:15], v[232:235], a[4:7]\n" \
"s_add_u32 s60, s41, 0\nbuffer_load_dwordx4 v[32:35], v196, %[rs], s60 offen offset:1024 sc1\nv_bitop3_b32 v209, v236, v238, s61 bitop3:0x7e\nv_mfma_f32_16x16x32_bf16 a[12:15], v[44:47], v[232:235], a[12:15]\nv_mfma_f32_16x16x32_bf16 a[20:23], v[76:79], v[232:235], a[20:23]\nv_cmp_gt_i32 vcc, 0, v209\nv_mfma_f32_16x16x32_bf16 a[28:31], v[108:111], v[232:235], a[28:31]\ns_cbranch_vccnz .Lrd_tre6%=\n" \
".Lrd_tl6%=:\nv_mfma_f32_16x16x16_bf16 a[0:3], v[16:17], v[236:237], a[0:3]\ns_add_u32 s60, s41, 0\nbuffer_load_dwordx4 v[64:67], v196, %[rs], s60 offen offset:2048 sc1\nv_mfma_f32_16x16x16_bf16 a[8:11], v[48:49], v[236:237], a[8:11]\nv_mfma_f32_16x16x16_bf16 a[16:19], v[80:81], v[236:237], a[16:19]\nv_mfma_f32_16x16x16_bf16 a[24:27], v[112:113], v[236:237], a[24:27]\ns_waitcnt vmcnt(12)\n" \
"v_mfma_f32_16x16x16_bf16 a[0:3], v[20:21], v[238:239], a[0:3]\ns_add_u32 s60, s41, 131072\nbuffer_load_dwordx4 v[4:7], v196, %[rs], s60 offen offset:0 sc1\nv_bitop3_b32 v209, v240, v242, s61 bitop3:0x7e\nv_mfma_f32_16x16x16_bf16 a[8:11], v[52:53], v[238:239], a[8:11]\nv_mfma_f32_16x16x16_bf16 a[16:19], v[84:85], v[238:239], a[16:19]\nv_cmp_gt_i32 vcc, 0, v209\nv_mfma_f32_16x16x16_bf16 a[24:27], v[116:117], v[238:239], a[24:27]\n" \
"s_cbranch_vccnz .Lrd_tre7%=\n.Lrd_tl7%=:\ns_waitcnt vmcnt(12)\nv_mfma_f32_16x16x32_bf16 a[4:7], v[16:19], v[240:243], a[4:7]\ns_add_u32 s60, s41, 131072\nbuffer_load_dwordx4 v[36:39], v196, %[rs], s60 offen offset:1024 sc1\nv_bitop3_b32 v209, v244, v246, s61 bitop3:0x7e\nv_mfma_f32_16x16x32_bf16 a[12:15], v[48:51], v[240:243], a[12:15]\n" \
"v_mfma_f32_16x16x32_bf16 a[20:23], v[80:83], v[240:243], a[20:23]\nv_cmp_gt_i32 vcc, 0, v209\nv_mfma_f32_16x16x32_bf16 a[28:31], v[112:115], v[240:243], a[28:31]\ns_cbranch_vccnz .Lrd_tre8%=\n.Lrd_tl8%=:\ns_waitcnt vmcnt(12)\nv_mfma_f32_16x16x32_bf16 a[4:7], v[20:23], v[244:247], a[4:7]\ns_add_u32 s60, s41, 131072\n" \
"buffer_load_dwordx4 v[68:71], v196, %[rs], s60 offen offset:2048 sc1\nv_bitop3_b32 v209, v248, v250, s61 bitop3:0x7e\nv_mfma_f32_16x16x32_bf16 a[12:15], v[52:55], v[244:247], a[12:15]\nv_mfma_f32_16x16x32_bf16 a[20:23], v[84:87], v[244:247], a[20:23]\nv_cmp_gt_i32 vcc, 0, v209\nv_mfma_f32_16x16x32_bf16 a[28:31], v[116:119], v[244:247], a[28:31]\ns_cbranch_vccnz .Lrd_tre9%=\n.Lrd_tl9%=:\n" \
"v_mfma_f32_16x16x16_bf16 a[0:3], v[24:25], v[248:249], a[0:3]\ns_add_u32 s60, s41, 262144\nbuffer_load_dwordx4 v[8:11], v196, %[rs], s60 offen offset:0 sc1\nv_mfma_f32_16x16x16_bf16 a[8:11], v[56:57], v[248:249], a[8:11]\nv_mfma_f32_16x16x16_bf16 a[16:19], v[88:89], v[248:249], a[16:19]\nv_mfma_f32_16x16x16_bf16 a[24:27], v[120:121], v[248:249], a[24:27]\ns_waitcnt vmcnt(13)\nv_mfma_f32_16x16x16_bf16 a[0:3], v[28:29], v[250:251], a[0:3]\n" \
"s_add_u32 s60, s41, 262144\nbuffer_load_dwordx4 v[40:43], v196, %[rs], s60 offen offset:1024 sc1\nv_bitop3_b32 v209, v252, v254, s61 bitop3:0x7e\nv_mfma_f32_16x16x16_bf16 a[8:11], v[60:61], v[250:251], a[8:11]\nv_bitop3_b32 v210, v253, v255, s61 bitop3:0x7e\nv_or_b32 v209, v209, v210\nv_mfma_f32_16x16x16_bf16 a[16:19], v[92:93], v[250:251], a[16:19]\nv_cmp_gt_i32 vcc, 0, v209\n" \
"v_mfma_f32_16x16x16_bf16 a[24:27], v[124:125], v[250:251], a[24:27]\ns_cbranch_vccnz .Lrd_tre10%=\n.Lrd_tl10%=:\ns_waitcnt vmcnt(13)\nv_mfma_f32_16x16x32_bf16 a[0:3], v[128:131], v[252:255], a[0:3]\ns_add_u32 s60, s41, 262144\nbuffer_load_dwordx4 v[72:75], v196, %[rs], s60 offen offset:2048 sc1\nv_bitop3_b32 v209, v160, v162, s61 bitop3:0x7e\n" \
"v_mfma_f32_16x16x32_bf16 a[8:11], v[136:139], v[252:255], a[8:11]\nv_bitop3_b32 v210, v161, v163, s61 bitop3:0x7e\nv_or_b32 v209, v209, v210\nv_mfma_f32_16x16x32_bf16 a[16:19], v[144:147], v[252:255], a[16:19]\nv_cmp_gt_i32 vcc, 0, v209\nv_mfma_f32_16x16x32_bf16 a[24:27], v[152:155], v[252:255], a[24:27]\ns_cbranch_vccnz .Lrd_tre11%=\n.Lrd_tl11%=:\n" \
"s_waitcnt vmcnt(13)\nv_mfma_f32_16x16x32_bf16 a[0:3], v[132:135], v[160:163], a[0:3]\ns_add_u32 s60, s41, 393216\nbuffer_load_dwordx4 v[12:15], v196, %[rs], s60 offen offset:0 sc1\nv_bitop3_b32 v209, v164, v166, s61 bitop3:0x7e\nv_mfma_f32_16x16x32_bf16 a[8:11], v[140:143], v[160:163], a[8:11]\nv_mfma_f32_16x16x32_bf16 a[16:19], v[148:151], v[160:163], a[16:19]\nv_cmp_gt_i32 vcc, 0, v209\n" \
"v_mfma_f32_16x16x32_bf16 a[24:27], v[156:159], v[160:163], a[24:27]\ns_cbranch_vccnz .Lrd_tre12%=\n.Lrd_tl12%=:\ns_waitcnt vmcnt(13)\nv_mfma_f32_16x16x32_bf16 a[4:7], v[24:27], v[164:167], a[4:7]\ns_add_u32 s60, s41, 393216\nbuffer_load_dwordx4 v[44:47], v196, %[rs], s60 offen offset:1024 sc1\nv_bitop3_b32 v209, v168, v170, s61 bitop3:0x7e\n" \
"v_mfma_f32_16x16x32_bf16 a[12:15], v[56:59], v[164:167], a[12:15]\nv_mfma_f32_16x16x32_bf16 a[20:23], v[88:91], v[164:167], a[20:23]\nds_write_b128 v202, a[0:3] offset:0\nv_cmp_gt_i32 vcc, 0, v209\nv_mfma_f32_16x16x32_bf16 a[28:31], v[120:123], v[164:167], a[28:31]\ns_cbranch_vccnz .Lrd_tre13%=\n.Lrd_tl13%=:\ns_waitcnt vmcnt(13)\n" \
"v_mfma_f32_16x16x32_bf16 a[4:7], v[28:31], v[168:171], a[4:7]\ns_add_u32 s60, s41, 393216\nbuffer_load_dwordx4 v[76:79], v196, %[rs], s60 offen offset:2048 sc1\nv_bitop3_b32 v209, v172, v174, s61 bitop3:0x7e\nv_mfma_f32_16x16x32_bf16 a[12:15], v[60:63], v[168:171], a[12:15]\nv_bitop3_b32 v210, v173, v175, s61 bitop3:0x7e\nv_or_b32 v209, v209, v210\nv_mfma_f32_16x16x32_bf16 a[20:23], v[92:95], v[168:171], a[20:23]\n" \
"ds_write_b128 v202, a[8:11] offset:2048\nv_cmp_gt_i32 vcc, 0, v209\nv_mfma_f32_16x16x32_bf16 a[28:31], v[124:127], v[168:171], a[28:31]\ns_cbranch_vccnz .Lrd_tre14%=\n.Lrd_tl14%=:\ns_waitcnt vmcnt(12)\nv_mfma_f32_16x16x32_bf16 a[4:7], v[128:131], v[172:175], a[4:7]\ns_add_u32 s60, s41, 524288\n" \
"buffer_load_dwordx4 v[16:19], v196, %[rs], s60 offen offset:0 sc1\nv_bitop3_b32 v209, v176, v178, s61 bitop3:0x7e\nv_mfma_f32_16x16x32_bf16 a[12:15], v[136:139], v[172:175], a[12:15]\nv_bitop3_b32 v210, v177, v179, s61 bitop3:0x7e\nv_or_b32 v209, v209, v210\nv_mfma_f32_16x16x32_bf16 a[20:23], v[144:147], v[172:175], a[20:23]\nds_write_b128 v202, a[16:19] offset:4096\nv_cmp_gt_i32 vcc, 0, v209\n" \
"v_mfma_f32_16x16x32_bf16 a[28:31], v[152:155], v[172:175], a[28:31]\ns_cbranch_vccnz .Lrd_tre15%=\n.Lrd_tl15%=:\nv_mfma_f32_16x16x32_bf16 a[4:7], v[132:135], v[176:179], a[4:7]\ns_add_u32 s60, s41, 524288\nbuffer_load_dwordx4 v[48:51], v196, %[rs], s60 offen offset:1024 sc1\nv_lshlrev_b32 v216, 16, v212\nv_and_b32 v217, 0xffff0000, v212\n" \
"v_mfma_f32_16x16x32_bf16 a[12:15], v[140:143], v[176:179], a[12:15]\nv_lshlrev_b32 v218, 16, v213\nv_and_b32 v219, 0xffff0000, v213\nv_mfma_f32_16x16x32_bf16 a[20:23], v[148:151], v[176:179], a[20:23]\nds_write_b128 v202, a[24:27] offset:6144\nv_lshlrev_b32 v220, 16, v214\nv_and_b32 v221, 0xffff0000, v214\nv_mfma_f32_16x16x32_bf16 a[28:31], v[156:159], v[176:179], a[28:31]\n" \
"s_add_u32 s60, s41, 524288\nbuffer_load_dwordx4 v[80:83], v196, %[rs], s60 offen offset:2048 sc1\ns_add_u32 s60, s41, 655360\nds_write_b128 v202, a[4:7] offset:1024\nbuffer_load_dwordx4 v[20:23], v196, %[rs], s60 offen offset:0 sc1\nds_write_b128 v202, a[12:15] offset:3072\nv_lshlrev_b32 v222, 16, v215\nv_and_b32 v223, 0xffff0000, v215\n" \
"s_add_u32 s60, s41, 655360\nbuffer_load_dwordx4 v[52:55], v196, %[rs], s60 offen offset:1024 sc1\nds_write_b128 v202, a[20:23] offset:5120\ns_add_u32 s60, s41, 655360\nbuffer_load_dwordx4 v[84:87], v196, %[rs], s60 offen offset:2048 sc1\nds_write_b128 v202, a[28:31] offset:7168\ns_waitcnt lgkmcnt(0)\ns_barrier\n" \
"ds_read_b128 v[224:227], v203 offset:0\nds_read_b128 v[228:231], v203 offset:256\nds_read_b128 v[232:235], v203 offset:8192\nds_read_b128 v[236:239], v203 offset:8448\nds_read_b128 v[240:243], v203 offset:16384\nds_read_b128 v[244:247], v203 offset:16640\nds_read_b128 v[248:251], v203 offset:24576\nds_read_b128 v[252:255], v203 offset:24832\n" \
"s_add_u32 s60, s41, 786432\nbuffer_load_dwordx4 v[24:27], v196, %[rs], s60 offen offset:0 sc1\ns_waitcnt lgkmcnt(4)\nv_pk_add_f32 v[224:225], v[224:225], v[232:233]\nv_pk_add_f32 v[226:227], v[226:227], v[234:235]\nv_pk_add_f32 v[228:229], v[228:229], v[236:237]\nv_pk_add_f32 v[230:231], v[230:231], v[238:239]\ns_add_u32 s60, s41, 786432\n" \
"buffer_load_dwordx4 v[56:59], v196, %[rs], s60 offen offset:1024 sc1\ns_waitcnt lgkmcnt(2)\nv_pk_add_f32 v[224:225], v[224:225], v[240:241]\nv_pk_add_f32 v[226:227], v[226:227], v[242:243]\nv_pk_add_f32 v[228:229], v[228:229], v[244:245]\nv_pk_add_f32 v[230:231], v[230:231], v[246:247]\ns_add_u32 s60, s41, 786432\nbuffer_load_dwordx4 v[88:91], v196, %[rs], s60 offen offset:2048 sc1\n" \
"s_waitcnt lgkmcnt(0)\nv_pk_add_f32 v[224:225], v[224:225], v[248:249]\nv_pk_add_f32 v[226:227], v[226:227], v[250:251]\nv_pk_add_f32 v[228:229], v[228:229], v[252:253]\nv_pk_add_f32 v[230:231], v[230:231], v[254:255]\nv_pk_fma_f32 v[224:225], v[224:225], s[62:63], v[216:217]\nv_pk_fma_f32 v[226:227], v[226:227], s[62:63], v[218:219]\nv_pk_fma_f32 v[228:229], v[228:229], s[62:63], v[220:221]\n" \
"v_pk_fma_f32 v[230:231], v[230:231], s[62:63], v[222:223]\nv_cvt_pk_bf16_f32 v208, v224, v225\nv_cvt_pk_bf16_f32 v209, v226, v227\nv_cvt_pk_bf16_f32 v210, v228, v229\nv_cvt_pk_bf16_f32 v211, v230, v231\nbuffer_store_dwordx4 v[208:211], v204, %[rs], s42 offen sc1\nv_pk_add_f32 v[232:233], v[224:225], v[226:227]\nv_pk_add_f32 v[234:235], v[228:229], v[230:231]\n" \
"v_pk_add_f32 v[232:233], v[232:233], v[234:235]\ns_add_u32 s60, s41, 917504\nbuffer_load_dwordx4 v[28:31], v196, %[rs], s60 offen offset:0 sc1\ns_add_u32 s60, s41, 917504\nbuffer_load_dwordx4 v[60:63], v196, %[rs], s60 offen offset:1024 sc1\nv_add_f32 v232, v232, v233\ns_nop 1\nv_add_f32_dpp v233, v232, v232 row_ror:8 row_mask:0xf bank_mask:0xf\n" \
"v_add_f32_dpp v234, v232, v232 row_ror:8 row_mask:0xf bank_mask:0xf\ns_nop 1\nv_permlane16_swap_b32 v233, v234\nv_add_f32 v232, v233, v234\nv_add_f32 v233, v233, v234\ns_nop 1\nv_permlane32_swap_b32 v232, v233\nv_add_f32 v232, v232, v233\n" \
"v_mul_f32 v232, 0x3c800000, v232\nv_pk_add_f32 v[224:225], v[224:225], v[232:233] op_sel_hi:[1,0] neg_lo:[0,1] neg_hi:[0,1]\nv_pk_add_f32 v[226:227], v[226:227], v[232:233] op_sel_hi:[1,0] neg_lo:[0,1] neg_hi:[0,1]\nv_pk_add_f32 v[228:229], v[228:229], v[232:233] op_sel_hi:[1,0] neg_lo:[0,1] neg_hi:[0,1]\nv_pk_add_f32 v[230:231], v[230:231], v[232:233] op_sel_hi:[1,0] neg_lo:[0,1] neg_hi:[0,1]\ns_add_u32 s60, s41, 917504\nbuffer_load_dwordx4 v[92:95], v196, %[rs], s60 offen offset:2048 sc1\ns_add_u32 s60, s41, 0\n" \
"buffer_load_dwordx4 v[96:99], v196, %[rs], s60 offen offset:3072 sc1\nv_pk_mul_f32 v[236:237], v[224:225], v[224:225]\nv_pk_mul_f32 v[238:239], v[226:227], v[226:227]\nv_pk_fma_f32 v[236:237], v[228:229], v[228:229], v[236:237]\nv_pk_fma_f32 v[238:239], v[230:231], v[230:231], v[238:239]\nv_pk_add_f32 v[236:237], v[236:237], v[238:239]\nv_add_f32 v236, v236, v237\ns_nop 1\n" \
"v_add_f32_dpp v237, v236, v236 row_ror:8 row_mask:0xf bank_mask:0xf\nv_add_f32_dpp v238, v236, v236 row_ror:8 row_mask:0xf bank_mask:0xf\ns_nop 1\nv_permlane16_swap_b32 v237, v238\nv_add_f32 v236, v237, v238\nv_add_f32 v237, v237, v238\ns_nop 1\nv_permlane32_swap_b32 v236, v237\n" \
"v_add_f32 v236, v236, v237\nv_mov_b32 v233, v236\nbuffer_store_dwordx2 v[232:233], v205, %[rs], s42 offen sc1\ns_mov_b32 s59, %[fa]\ns_add_u32 s60, s41, 131072\nbuffer_load_dwordx4 v[100:103], v196, %[rs], s60 offen offset:3072 sc1\nv_accvgpr_read_b32 v212, a33\nbuffer_load_dwordx2 v[214:215], v212, %[rs], 0 offen sc1\n" \
"s_add_u32 s60, s41, 262144\nbuffer_load_dwordx4 v[104:107], v196, %[rs], s60 offen offset:3072 sc1\ns_add_u32 s60, s41, 393216\nbuffer_load_dwordx4 v[108:111], v196, %[rs], s60 offen offset:3072 sc1\ns_add_u32 s60, s41, 524288\nbuffer_load_dwordx4 v[112:115], v196, %[rs], s60 offen offset:3072 sc1\ns_add_u32 s60, s41, 655360\nbuffer_load_dwordx4 v[116:119], v196, %[rs], s60 offen offset:3072 sc1\n" \
"s_add_u32 s60, s41, 786432\nbuffer_load_dwordx4 v[120:123], v196, %[rs], s60 offen offset:3072 sc1\ns_add_u32 s60, s41, 917504\nbuffer_load_dwordx4 v[124:127], v196, %[rs], s60 offen offset:3072 sc1\ns_cmp_ge_u32 s40, 64\ns_cbranch_scc1 .Lrd_fin%=\n.Lrd_ychk%=:\ns_and_b32 s44, s40, 1\n" \
"s_bfe_u32 s55, s40, 0x10001\ns_mul_i32 s54, s44, 0x80008001\ns_mul_i32 s61, s55, 0x80008000\ns_lshl_b32 s58, s44, 17\ns_add_u32 s58, s58, %[st]\ns_add_u32 s44, s40, 1\ns_and_b32 s44, s44, 3\ns_lshl_b32 s44, s44, 15\n" \
"v_xor_b32 v206, s44, v200\nv_mov_b64 v[180:181], 0\nv_mov_b64 v[182:183], 0\nv_mov_b64 v[184:185], 0\nv_mov_b64 v[186:187], 0\nv_mov_b64 v[188:189], 0\nv_mov_b64 v[190:191], 0\nv_mov_b64 v[192:193], 0\n" \
"v_mov_b64 v[194:195], 0\ns_waitcnt vmcnt(6)\n.Lrd_sg0%=:\ns_memrealtime s[68:69]\nv_cmp_ne_u32 vcc, s40, v214\ns_cbranch_vccnz .Lrd_sg1%=\nv_max_u32_dpp v215, v215, v215 quad_perm:[1,0,3,2] row_mask:0xf bank_mask:0xf\ns_nop 1\n" \
"v_max_u32_dpp v215, v215, v215 quad_perm:[2,3,0,1] row_mask:0xf bank_mask:0xf\ns_nop 1\nv_max_u32_dpp v215, v215, v215 row_half_mirror row_mask:0xf bank_mask:0xf\ns_nop 0\nv_readlane_b32 s86, v215, 0\nv_readlane_b32 s87, v215, 8\ns_add_u32 s86, s86, %[du]\n.Lrd_tw622%=:\n" \
"s_waitcnt lgkmcnt(0)\ns_sub_u32 s44, s86, s68\ns_add_u32 s44, s44, 11\ns_cmp_lt_u32 s44, 250\ns_cbranch_scc1 .Lrd_tw622x%=\nbuffer_load_dwordx4 v[128:131], v196, %[rs], %[su] offen offset:0 sc1\nbuffer_load_dwordx4 v[132:135], v196, %[rs], %[su] offen offset:1024 sc1\ns_memrealtime s[68:69]\n" \
"buffer_load_dwordx4 v[136:139], v196, %[rs], %[su] offen offset:2048 sc1\nbuffer_load_dwordx4 v[140:143], v196, %[rs], %[su] offen offset:3072 sc1\n.Lrd_tw633%=:\ns_waitcnt lgkmcnt(0)\ns_sub_u32 s44, s86, s68\ns_add_u32 s44, s44, 21\ns_cmp_lt_u32 s44, 250\ns_cbranch_scc1 .Lrd_tw633x%=\n" \
"buffer_load_dwordx4 v[144:147], v196, %[rs], s57 offen offset:0 sc1\nbuffer_load_dwordx4 v[148:151], v196, %[rs], s57 offen offset:1024 sc1\ns_memrealtime s[68:69]\nbuffer_load_dwordx4 v[152:155], v196, %[rs], s57 offen offset:2048 sc1\nbuffer_load_dwordx4 v[156:159], v196, %[rs], s57 offen offset:3072 sc1\nbuffer_store_dword v207, off, %[rs], s59 sc1\ns_mov_b32 s59, -2147483648\n.Lrd_tw646%=:\n" \
"s_waitcnt lgkmcnt(0)\ns_sub_u32 s44, s87, s68\ns_add_u32 s44, s44, 24\ns_cmp_lt_u32 s44, 250\ns_cbranch_scc1 .Lrd_tw646x%=\nbuffer_load_dwordx4 v[160:163], v196, %[rs], %[sp] offen offset:0 sc1\nbuffer_load_dwordx4 v[164:167], v196, %[rs], %[sp] offen offset:1024 sc1\nbuffer_load_dwordx4 v[168:171], v196, %[rs], %[sp] offen offset:2048 sc1\n" \
"buffer_load_dwordx4 v[172:175], v196, %[rs], %[sp] offen offset:3072 sc1\nbuffer_load_dwordx4 v[176:179], v196, %[rs], s56 offen offset:0 sc1\nv_bitop3_b32 v209, v0, v2, s55 bitop3:0x7e\nv_bitop3_b32 v210, v32, v34, s55 bitop3:0x7e\nv_or3_b32 v208, v209, v210, 0\nv_bitop3_b32 v209, v64, v66, s55 bitop3:0x7e\nv_bitop3_b32 v210, v4, v6, s55 bitop3:0x7e\nv_or3_b32 v208, v209, v210, v208\n" \
"v_bitop3_b32 v209, v36, v38, s55 bitop3:0x7e\nv_bitop3_b32 v210, v68, v70, s55 bitop3:0x7e\nv_or3_b32 v208, v209, v210, v208\nv_bitop3_b32 v209, v8, v10, s55 bitop3:0x7e\nv_bitop3_b32 v210, v40, v42, s55 bitop3:0x7e\nv_or3_b32 v208, v209, v210, v208\nv_bitop3_b32 v209, v72, v74, s55 bitop3:0x7e\nv_bitop3_b32 v210, v12, v14, s55 bitop3:0x7e\n" \
"v_or3_b32 v208, v209, v210, v208\nv_bitop3_b32 v209, v44, v46, s55 bitop3:0x7e\nv_bitop3_b32 v210, v76, v78, s55 bitop3:0x7e\nv_or3_b32 v208, v209, v210, v208\nv_bitop3_b32 v209, v16, v18, s55 bitop3:0x7e\nv_bitop3_b32 v210, v48, v50, s55 bitop3:0x7e\nv_or3_b32 v208, v209, v210, v208\nv_bitop3_b32 v209, v80, v82, s55 bitop3:0x7e\n" \
"v_bitop3_b32 v210, v20, v22, s55 bitop3:0x7e\nv_or3_b32 v208, v209, v210, v208\nv_bitop3_b32 v209, v52, v54, s55 bitop3:0x7e\nv_bitop3_b32 v210, v84, v86, s55 bitop3:0x7e\nv_or3_b32 v208, v209, v210, v208\nv_bitop3_b32 v209, v24, v26, s55 bitop3:0x7e\nv_bitop3_b32 v210, v56, v58, s55 bitop3:0x7e\nv_or3_b32 v208, v209, v210, v208\n" \
"v_bitop3_b32 v209, v88, v90, s55 bitop3:0x7e\nv_bitop3_b32 v210, v28, v30, s55 bitop3:0x7e\nv_or3_b32 v208, v209, v210, v208\nv_bitop3_b32 v209, v60, v62, s55 bitop3:0x7e\nv_bitop3_b32 v210, v92, v94, s55 bitop3:0x7e\nv_or3_b32 v208, v209, v210, v208\ns_waitcnt vmcnt(18)\nv_bitop3_b32 v209, v96, v98, s55 bitop3:0x7e\n" \
"v_bitop3_b32 v210, v100, v102, s55 bitop3:0x7e\nv_or3_b32 v208, v209, v210, v208\nv_bitop3_b32 v209, v104, v106, s55 bitop3:0x7e\nv_bitop3_b32 v210, v108, v110, s55 bitop3:0x7e\nv_or3_b32 v208, v209, v210, v208\ns_waitcnt vmcnt(14)\nv_bitop3_b32 v209, v112, v114, s55 bitop3:0x7e\nv_bitop3_b32 v210, v116, v118, s55 bitop3:0x7e\n" \
"v_or3_b32 v208, v209, v210, v208\nv_bitop3_b32 v209, v120, v122, s55 bitop3:0x7e\nv_bitop3_b32 v210, v124, v126, s55 bitop3:0x7e\nv_or3_b32 v208, v209, v210, v208\nv_and_b32 v208, 1, v208\nv_cmp_ne_u32 vcc, 0, v208\ns_cbranch_vccnz .Lrd_yre%=\ns_branch .Lrd_ra%=\n" \
".Lrd_sg1%=:\nbuffer_load_dwordx2 v[214:215], v212, %[rs], 0 offen sc1\ns_waitcnt vmcnt(0)\nbuffer_store_dword v207, off, %[rs], s59 sc1\ns_mov_b32 s59, -2147483648\ns_add_u32 s43, s43, 1\ns_cmp_lt_u32 s43, 262144\ns_cbranch_scc1 .Lrd_sg0%=\n" \
"s_branch .Lrd_abort%=\n.Lrd_tw622x%=:\ns_memrealtime s[68:69]\ns_branch .Lrd_tw622%=\n.Lrd_tw633x%=:\ns_memrealtime s[68:69]\ns_branch .Lrd_tw633%=\n.Lrd_tw646x%=:\n" \
"s_memrealtime s[68:69]\ns_branch .Lrd_tw646%=\n.Lrd_fin%=:\ns_waitcnt vmcnt(0)\nbuffer_store_dword v207, off, %[rs], s59 sc1\ns_mov_b32 s59, -2147483648\ns_branch .Lrd_end%=\n.Lrd_yre%=:\n" \
"s_mov_b64 exec, vcc\ns_waitcnt vmcnt(0)\ns_add_u32 s43, s43, 1\ns_cmp_lt_u32 s43, 262144\ns_cbranch_scc1 .Lrd_yld%=\ns_branch .Lrd_abort%=\n.Lrd_rb%=:\ns_mov_b64 exec, s[46:47]\n" \
"s_cbranch_execz .Lrd_rs0%=\nbuffer_load_dwordx4 v[160:163], v196, %[rs], %[sp] offen offset:0 sc1\n.Lrd_rs0%=:\ns_mov_b64 exec, s[48:49]\ns_cbranch_execz .Lrd_rs1%=\nbuffer_load_dwordx4 v[164:167], v196, %[rs], %[sp] offen offset:1024 sc1\n.Lrd_rs1%=:\ns_mov_b64 exec, s[50:51]\n" \
"s_cbranch_execz .Lrd_rs2%=\nbuffer_load_dwordx4 v[168:171], v196, %[rs], %[sp] offen offset:2048 sc1\n.Lrd_rs2%=:\ns_mov_b64 exec, s[52:53]\ns_cbranch_execz .Lrd_rs3%=\nbuffer_load_dwordx4 v[172:175], v196, %[rs], %[sp] offen offset:3072 sc1\n.Lrd_rs3%=:\ns_mov_b64 exec, s[64:65]\n" \
"s_cbranch_execz .Lrd_rs4%=\nbuffer_load_dwordx4 v[176:179], v196, %[rs], s56 offen offset:0 sc1\n.Lrd_rs4%=:\ns_mov_b64 exec, s[86:87]\ns_cbranch_execz .Lrd_rs5%=\nbuffer_load_dwordx4 v[128:131], v196, %[rs], %[su] offen offset:0 sc1\nbuffer_load_dwordx4 v[136:139], v196, %[rs], %[su] offen offset:2048 sc1\n.Lrd_rs5%=:\n" \
"s_mov_b64 exec, s[88:89]\ns_cbranch_execz .Lrd_rs6%=\nbuffer_load_dwordx4 v[144:147], v196, %[rs], s57 offen offset:0 sc1\nbuffer_load_dwordx4 v[152:155], v196, %[rs], s57 offen offset:2048 sc1\n.Lrd_rs6%=:\ns_mov_b64 exec, s[68:69]\ns_cbranch_execz .Lrd_rs7%=\nbuffer_load_dwordx4 v[132:135], v196, %[rs], %[su] offen offset:1024 sc1\n" \
"buffer_load_dwordx4 v[140:143], v196, %[rs], %[su] offen offset:3072 sc1\n.Lrd_rs7%=:\ns_mov_b64 exec, s[62:63]\ns_cbranch_execz .Lrd_rs8%=\nbuffer_load_dwordx4 v[148:151], v196, %[rs], s57 offen offset:1024 sc1\nbuffer_load_dwordx4 v[156:159], v196, %[rs], s57 offen offset:3072 sc1\n.Lrd_rs8%=:\ns_mov_b64 exec, -1\n" \
"v_mov_b64 v[180:181], 0\nv_mov_b64 v[182:183], 0\nv_mov_b64 v[184:185], 0\nv_mov_b64 v[186:187], 0\nv_mov_b64 v[188:189], 0\nv_mov_b64 v[190:191], 0\nv_mov_b64 v[192:193], 0\nv_mov_b64 v[194:195], 0\n" \
"s_waitcnt vmcnt(0)\ns_add_u32 s43, s43, 1\ns_cmp_lt_u32 s43, 262144\ns_cbranch_scc1 .Lrd_ra%=\ns_branch .Lrd_abort%=\n.Lrd_y1%=:\nv_accvgpr_read_b32 v206, a32\nbuffer_load_dword v208, v206, %[rs], 0 offen sc1\n" \
"s_waitcnt vmcnt(0)\nv_cmp_gt_i32 vcc, 1, v208\ns_cbranch_vccz .Lrd_y1r%=\ns_add_u32 s43, s43, 1\ns_cmp_lt_u32 s43, 262144\ns_cbranch_scc1 .Lrd_y1%=\ns_branch .Lrd_abort%=\n.Lrd_tre0%=:\n" \
"s_add_u32 s60, s58, 16384\nbuffer_load_dwordx4 v[212:215], v196, %[rs], s60 offen offset:0 sc1\ns_waitcnt vmcnt(0)\nv_bitop3_b32 v209, v212, v214, s61 bitop3:0x7e\nv_cmp_gt_i32 vcc, 0, v209\ns_cbranch_vccz .Lrd_tl0%=\ns_add_u32 s43, s43, 1\ns_cmp_lt_u32 s43, 262144\n" \
"s_cbranch_scc1 .Lrd_tre0%=\ns_branch .Lrd_abort%=\n.Lrd_tre1%=:\ns_add_u32 s60, s58, 0\nbuffer_load_dwordx4 v[216:219], v196, %[rs], s60 offen offset:0 sc1\ns_waitcnt vmcnt(0)\nv_bitop3_b32 v209, v216, v218, s61 bitop3:0x7e\nv_cmp_gt_i32 vcc, 0, v209\n" \
"s_cbranch_vccz .Lrd_tl1%=\ns_add_u32 s43, s43, 1\ns_cmp_lt_u32 s43, 262144\ns_cbranch_scc1 .Lrd_tre1%=\ns_branch .Lrd_abort%=\n.Lrd_tre2%=:\ns_add_u32 s60, s58, 0\nbuffer_load_dwordx4 v[220:223], v196, %[rs], s60 offen offset:1024 sc1\n" \
"s_waitcnt vmcnt(0)\nv_bitop3_b32 v209, v220, v222, s61 bitop3:0x7e\nv_cmp_gt_i32 vcc, 0, v209\ns_cbranch_vccz .Lrd_tl2%=\ns_add_u32 s43, s43, 1\ns_cmp_lt_u32 s43, 262144\ns_cbranch_scc1 .Lrd_tre2%=\ns_branch .Lrd_abort%=\n" \
".Lrd_tre3%=:\ns_add_u32 s60, s58, 16384\nbuffer_load_dwordx4 v[224:227], v196, %[rs], s60 offen offset:1024 sc1\ns_waitcnt vmcnt(0)\nv_bitop3_b32 v209, v224, v226, s61 bitop3:0x7e\nv_cmp_gt_i32 vcc, 0, v209\ns_cbranch_vccz .Lrd_tl3%=\ns_add_u32 s43, s43, 1\n" \
"s_cmp_lt_u32 s43, 262144\ns_cbranch_scc1 .Lrd_tre3%=\ns_branch .Lrd_abort%=\n.Lrd_tre4%=:\ns_add_u32 s60, s58, 0\nbuffer_load_dwordx4 v[228:231], v196, %[rs], s60 offen offset:2048 sc1\ns_waitcnt vmcnt(0)\nv_bitop3_b32 v209, v228, v230, s61 bitop3:0x7e\n" \
"v_cmp_gt_i32 vcc, 0, v209\ns_cbranch_vccz .Lrd_tl4%=\ns_add_u32 s43, s43, 1\ns_cmp_lt_u32 s43, 262144\ns_cbranch_scc1 .Lrd_tre4%=\ns_branch .Lrd_abort%=\n.Lrd_tre5%=:\ns_add_u32 s60, s58, 0\n" \
"buffer_load_dwordx4 v[232:235], v196, %[rs], s60 offen offset:3072 sc1\ns_waitcnt vmcnt(0)\nv_bitop3_b32 v209, v232, v234, s61 bitop3:0x7e\nv_cmp_gt_i32 vcc, 0, v209\ns_cbranch_vccz .Lrd_tl5%=\ns_add_u32 s43, s43, 1\ns_cmp_lt_u32 s43, 262144\ns_cbranch_scc1 .Lrd_tre5%=\n" \
"s_branch .Lrd_abort%=\n.Lrd_tre6%=:\ns_add_u32 s60, s58, 16384\nbuffer_load_dwordx4 v[236:239], v196, %[rs], s60 offen offset:2048 sc1\ns_waitcnt vmcnt(0)\nv_bitop3_b32 v209, v236, v238, s61 bitop3:0x7e\nv_cmp_gt_i32 vcc, 0, v209\ns_cbranch_vccz .Lrd_tl6%=\n" \
"s_add_u32 s43, s43, 1\ns_cmp_lt_u32 s43, 262144\ns_cbranch_scc1 .Lrd_tre6%=\ns_branch .Lrd_abort%=\n.Lrd_tre7%=:\ns_add_u32 s60, s58, 4096\nbuffer_load_dwordx4 v[240:243], v196, %[rs], s60 offen offset:0 sc1\ns_waitcnt vmcnt(0)\n" \
"v_bitop3_b32 v209, v240, v242, s61 bitop3:0x7e\nv_cmp_gt_i32 vcc, 0, v209\ns_cbranch_vccz .Lrd_tl7%=\ns_add_u32 s43, s43, 1\ns_cmp_lt_u32 s43, 262144\ns_cbranch_scc1 .Lrd_tre7%=\ns_branch .Lrd_abort%=\n.Lrd_tre8%=:\n" \
"s_add_u32 s60, s58, 4096\nbuffer_load_dwordx4 v[244:247], v196, %[rs], s60 offen offset:1024 sc1\ns_waitcnt vmcnt(0)\nv_bitop3_b32 v209, v244, v246, s61 bitop3:0x7e\nv_cmp_gt_i32 vcc, 0, v209\ns_cbranch_vccz .Lrd_tl8%=\ns_add_u32 s43, s43, 1\ns_cmp_lt_u32 s43, 262144\n" \
"s_cbranch_scc1 .Lrd_tre8%=\ns_branch .Lrd_abort%=\n.Lrd_tre9%=:\ns_add_u32 s60, s58, 16384\nbuffer_load_dwordx4 v[248:251], v196, %[rs], s60 offen offset:3072 sc1\ns_waitcnt vmcnt(0)\nv_bitop3_b32 v209, v248, v250, s61 bitop3:0x7e\nv_cmp_gt_i32 vcc, 0, v209\n" \
"s_cbranch_vccz .Lrd_tl9%=\ns_add_u32 s43, s43, 1\ns_cmp_lt_u32 s43, 262144\ns_cbranch_scc1 .Lrd_tre9%=\ns_branch .Lrd_abort%=\n.Lrd_tre10%=:\ns_add_u32 s60, s58, 20480\nbuffer_load_dwordx4 v[252:255], v196, %[rs], s60 offen offset:0 sc1\n" \
"s_waitcnt vmcnt(0)\nv_bitop3_b32 v209, v252, v254, s61 bitop3:0x7e\nv_bitop3_b32 v210, v253, v255, s61 bitop3:0x7e\nv_or_b32 v209, v209, v210\nv_cmp_gt_i32 vcc, 0, v209\ns_cbranch_vccz .Lrd_tl10%=\ns_add_u32 s43, s43, 1\ns_cmp_lt_u32 s43, 262144\n" \
"s_cbranch_scc1 .Lrd_tre10%=\ns_branch .Lrd_abort%=\n.Lrd_tre11%=:\ns_add_u32 s60, s58, 20480\nbuffer_load_dwordx4 v[160:163], v196, %[rs], s60 offen offset:2048 sc1\ns_waitcnt vmcnt(0)\nv_bitop3_b32 v209, v160, v162, s61 bitop3:0x7e\nv_bitop3_b32 v210, v161, v163, s61 bitop3:0x7e\n" \
"v_or_b32 v209, v209, v210\nv_cmp_gt_i32 vcc, 0, v209\ns_cbranch_vccz .Lrd_tl11%=\ns_add_u32 s43, s43, 1\ns_cmp_lt_u32 s43, 262144\ns_cbranch_scc1 .Lrd_tre11%=\ns_branch .Lrd_abort%=\n.Lrd_tre12%=:\n" \
"s_add_u32 s60, s58, 4096\nbuffer_load_dwordx4 v[164:167], v196, %[rs], s60 offen offset:2048 sc1\ns_waitcnt vmcnt(0)\nv_bitop3_b32 v209, v164, v166, s61 bitop3:0x7e\nv_cmp_gt_i32 vcc, 0, v209\ns_cbranch_vccz .Lrd_tl12%=\ns_add_u32 s43, s43, 1\ns_cmp_lt_u32 s43, 262144\n" \
"s_cbranch_scc1 .Lrd_tre12%=\ns_branch .Lrd_abort%=\n.Lrd_tre13%=:\ns_add_u32 s60, s58, 4096\nbuffer_load_dwordx4 v[168:171], v196, %[rs], s60 offen offset:3072 sc1\ns_waitcnt vmcnt(0)\nv_bitop3_b32 v209, v168, v170, s61 bitop3:0x7e\nv_cmp_gt_i32 vcc, 0, v209\n" \
"s_cbranch_vccz .Lrd_tl13%=\ns_add_u32 s43, s43, 1\ns_cmp_lt_u32 s43, 262144\ns_cbranch_scc1 .Lrd_tre13%=\ns_branch .Lrd_abort%=\n.Lrd_tre14%=:\ns_add_u32 s60, s58, 20480\nbuffer_load_dwordx4 v[172:175], v196, %[rs], s60 offen offset:1024 sc1\n" \
"s_waitcnt vmcnt(0)\nv_bitop3_b32 v209, v172, v174, s61 bitop3:0x7e\nv_bitop3_b32 v210, v173, v175, s61 bitop3:0x7e\nv_or_b32 v209, v209, v210\nv_cmp_gt_i32 vcc, 0, v209\ns_cbranch_vccz .Lrd_tl14%=\ns_add_u32 s43, s43, 1\ns_cmp_lt_u32 s43, 262144\n" \
"s_cbranch_scc1 .Lrd_tre14%=\ns_branch .Lrd_abort%=\n.Lrd_tre15%=:\ns_add_u32 s60, s58, 20480\nbuffer_load_dwordx4 v[176:179], v196, %[rs], s60 offen offset:3072 sc1\ns_waitcnt vmcnt(0)\nv_bitop3_b32 v209, v176, v178, s61 bitop3:0x7e\nv_bitop3_b32 v210, v177, v179, s61 bitop3:0x7e\n" \
"v_or_b32 v209, v209, v210\nv_cmp_gt_i32 vcc, 0, v209\ns_cbranch_vccz .Lrd_tl15%=\ns_add_u32 s43, s43, 1\ns_cmp_lt_u32 s43, 262144\ns_cbranch_scc1 .Lrd_tre15%=\ns_branch .Lrd_abort%=\n.Lrd_abort%=:\n" \
"s_mov_b64 exec, -1\nv_mov_b32 v16, 0\ns_movk_i32 s44, 6400\nbuffer_store_dword v16, off, %[rs], s44 sc1\n.Lrd_end%=:\ns_waitcnt vmcnt(0) lgkmcnt(0)\n" \
""
#define GG_ASM \
"v_mbcnt_lo_u32_b32 v255, -1, 0\nv_mbcnt_hi_u32_b32 v255, -1, v255\nv_lshlrev_b32 v255, 2, v255\nv_add_u32 v255, %[tb], v255\nds_read_b32 v0, v255 offset:0\nds_read_b32 v1, v255 offset:256\nds_read_b32 v2, v255 offset:512\nds_read_b32 v3, v255 offset:768\n" \
"ds_read_b32 v4, v255 offset:1024\nds_read_b32 v5, v255 offset:1280\nds_read_b32 v92, v255 offset:1536\nds_read_b32 v93, v255 offset:1792\nds_read_b32 v94, v255 offset:2048\ns_waitcnt lgkmcnt(0)\nv_mov_b32 v6, 0x3f803f80\nv_mov_b32 v7, 0x3f803f80\n" \
"s_add_u32 m0, %[lw], 16384\ns_add_u32 s44, %[wo], 0\nbuffer_load_dwordx4 v92, %[rw], s44 offen nt lds\ns_add_u32 m0, %[lw], 17408\ns_add_u32 s44, %[wo], 57344\nbuffer_load_dwordx4 v92, %[rw], s44 offen nt lds\ns_add_u32 m0, %[lw], 18432\ns_add_u32 s44, %[wo], 114688\n" \
"buffer_load_dwordx4 v92, %[rw], s44 offen nt lds\ns_add_u32 m0, %[lw], 19456\ns_add_u32 s44, %[wo], 172032\nbuffer_load_dwordx4 v92, %[rw], s44 offen nt lds\ns_add_u32 m0, %[lw], 20480\ns_add_u32 s44, %[wo], 229376\nbuffer_load_dwordx4 v92, %[rw], s44 offen nt lds\ns_add_u32 m0, %[lw], 21504\n" \
"s_add_u32 s44, %[wo], 286720\nbuffer_load_dwordx4 v92, %[rw], s44 offen nt lds\ns_add_u32 m0, %[lw], 22528\ns_add_u32 s44, %[wo], 344064\nbuffer_load_dwordx4 v92, %[rw], s44 offen nt lds\ns_add_u32 m0, %[lw], 23552\ns_add_u32 s44, %[wo], 401408\nbuffer_load_dwordx4 v92, %[rw], s44 offen nt lds\n" \
"s_add_u32 m0, %[tb], 0\ns_add_u32 s44, %[wo], 1835008\nbuffer_load_dwordx4 v92, %[rw], s44 offen nt lds\ns_add_u32 m0, %[tb], 1024\ns_add_u32 s44, %[wo], 1892352\nbuffer_load_dwordx4 v92, %[rw], s44 offen nt lds\ns_add_u32 m0, %[tb], 2048\ns_add_u32 s44, %[wo], 1949696\n" \
"buffer_load_dwordx4 v92, %[rw], s44 offen nt lds\ns_add_u32 m0, %[tb], 3072\ns_add_u32 s44, %[wo], 2007040\nbuffer_load_dwordx4 v92, %[rw], s44 offen nt lds\ns_add_u32 m0, %[tb], 4096\ns_add_u32 s44, %[wo], 2064384\nbuffer_load_dwordx4 v92, %[rw], s44 offen nt lds\ns_add_u32 m0, %[tb], 5120\n" \
"s_add_u32 s44, %[wo], 2121728\nbuffer_load_dwordx4 v92, %[rw], s44 offen nt lds\ns_add_u32 m0, %[tb], 6144\ns_add_u32 s44, %[wo], 2179072\nbuffer_load_dwordx4 v92, %[rw], s44 offen nt lds\ns_add_u32 m0, %[tb], 7168\ns_add_u32 s44, %[wo], 2236416\nbuffer_load_dwordx4 v92, %[rw], s44 offen nt lds\n" \
"s_add_u32 m0, %[tb], 8192\ns_add_u32 s44, %[wo], 3670016\nbuffer_load_dwordx4 v92, %[rw], s44 offen nt lds\ns_add_u32 m0, %[tb], 9216\ns_add_u32 s44, %[wo], 3727360\nbuffer_load_dwordx4 v92, %[rw], s44 offen nt lds\ns_add_u32 m0, %[tb], 10240\ns_add_u32 s44, %[wo], 3784704\n" \
"buffer_load_dwordx4 v92, %[rw], s44 offen nt lds\ns_add_u32 m0, %[tb], 11264\ns_add_u32 s44, %[wo], 3842048\nbuffer_load_dwordx4 v92, %[rw], s44 offen nt lds\ns_add_u32 m0, %[lw], 0\ns_add_u32 s44, %[wo], 3899392\nbuffer_load_dwordx4 v92, %[rw], s44 offen nt lds\ns_add_u32 m0, %[lw], 1024\n" \
"s_add_u32 s44, %[wo], 3956736\nbuffer_load_dwordx4 v92, %[rw], s44 offen nt lds\ns_add_u32 m0, %[lw], 2048\ns_add_u32 s44, %[wo], 4014080\nbuffer_load_dwordx4 v92, %[rw], s44 offen nt lds\ns_add_u32 m0, %[lw], 3072\ns_add_u32 s44, %[wo], 4071424\nbuffer_load_dwordx4 v92, %[rw], s44 offen nt lds\n" \
"s_add_u32 m0, %[lw], 4096\ns_add_u32 s44, %[wo], 5505024\nbuffer_load_dwordx4 v92, %[rw], s44 offen nt lds\ns_add_u32 m0, %[lw], 5120\ns_add_u32 s44, %[wo], 5562368\nbuffer_load_dwordx4 v92, %[rw], s44 offen nt lds\ns_add_u32 m0, %[lw], 6144\ns_add_u32 s44, %[wo], 5619712\n" \
"buffer_load_dwordx4 v92, %[rw], s44 offen nt lds\ns_add_u32 m0, %[lw], 7168\ns_add_u32 s44, %[wo], 5677056\nbuffer_load_dwordx4 v92, %[rw], s44 offen nt lds\ns_add_u32 m0, %[lw], 8192\ns_add_u32 s44, %[wo], 5734400\nbuffer_load_dwordx4 v92, %[rw], s44 offen nt lds\ns_add_u32 m0, %[lw], 9216\n" \
"s_add_u32 s44, %[wo], 5791744\nbuffer_load_dwordx4 v92, %[rw], s44 offen nt lds\ns_add_u32 m0, %[lw], 10240\ns_add_u32 s44, %[wo], 5849088\nbuffer_load_dwordx4 v92, %[rw], s44 offen nt lds\ns_add_u32 m0, %[lw], 11264\ns_add_u32 s44, %[wo], 5906432\nbuffer_load_dwordx4 v92, %[rw], s44 offen nt lds\n" \
"s_waitcnt vmcnt(28)\ns_barrier\ns_add_u32 m0, %[lw], 12288\ns_add_u32 s44, %[wo], 458752\nbuffer_load_dwordx4 v92, %[rw], s44 offen nt lds\ns_add_u32 m0, %[lw], 13312\ns_add_u32 s44, %[wo], 516096\nbuffer_load_dwordx4 v92, %[rw], s44 offen nt lds\n" \
"s_add_u32 m0, %[lw], 14336\ns_add_u32 s44, %[wo], 573440\nbuffer_load_dwordx4 v92, %[rw], s44 offen nt lds\ns_add_u32 m0, %[lw], 15360\ns_add_u32 s44, %[wo], 630784\nbuffer_load_dwordx4 v92, %[rw], s44 offen nt lds\nds_read_b64_tr_b16 a[0:1], v94 offset:16384\nds_read_b64_tr_b16 a[2:3], v94 offset:16896\n" \
"ds_read_b64_tr_b16 a[8:9], v94 offset:16392\nds_read_b64_tr_b16 a[10:11], v94 offset:16904\nds_read_b64_tr_b16 a[16:17], v94 offset:40960\nds_read_b64_tr_b16 a[18:19], v94 offset:41472\nds_read_b64_tr_b16 a[24:25], v94 offset:40968\nds_read_b64_tr_b16 a[26:27], v94 offset:41480\ns_waitcnt lgkmcnt(0)\nv_mfma_f32_16x16x16_bf16 v[8:11], a[0:1], v[6:7], 0\n" \
"v_mfma_f32_16x16x16_bf16 v[12:15], a[8:9], v[6:7], 0\nv_mfma_f32_16x16x16_bf16 v[16:19], a[16:17], v[6:7], 0\nv_mfma_f32_16x16x16_bf16 v[20:23], a[24:25], v[6:7], 0\nv_mfma_f32_16x16x16_bf16 v[8:11], a[2:3], v[6:7], v[8:11]\nv_mfma_f32_16x16x16_bf16 v[12:15], a[10:11], v[6:7], v[12:15]\nv_mfma_f32_16x16x16_bf16 v[16:19], a[18:19], v[6:7], v[16:19]\nv_mfma_f32_16x16x16_bf16 v[20:23], a[26:27], v[6:7], v[20:23]\ns_waitcnt vmcnt(28)\n" \
"s_barrier\ns_add_u32 m0, %[lw], 16384\ns_add_u32 s44, %[wo], 688128\nbuffer_load_dwordx4 v92, %[rw], s44 offen nt lds\ns_add_u32 m0, %[lw], 17408\ns_add_u32 s44, %[wo], 745472\nbuffer_load_dwordx4 v92, %[rw], s44 offen nt lds\ns_add_u32 m0, %[lw], 18432\n" \
"s_add_u32 s44, %[wo], 802816\nbuffer_load_dwordx4 v92, %[rw], s44 offen nt lds\ns_add_u32 m0, %[lw], 19456\ns_add_u32 s44, %[wo], 860160\nbuffer_load_dwordx4 v92, %[rw], s44 offen nt lds\nds_read_b64_tr_b16 a[4:5], v94 offset:20480\nds_read_b64_tr_b16 a[6:7], v94 offset:20992\nds_read_b64_tr_b16 a[12:13], v94 offset:20488\n" \
"ds_read_b64_tr_b16 a[14:15], v94 offset:21000\nds_read_b64_tr_b16 a[20:21], v94 offset:45056\nds_read_b64_tr_b16 a[22:23], v94 offset:45568\nds_read_b64_tr_b16 a[28:29], v94 offset:45064\nds_read_b64_tr_b16 a[30:31], v94 offset:45576\ns_waitcnt lgkmcnt(0)\nv_mfma_f32_16x16x16_bf16 v[8:11], a[4:5], v[6:7], v[8:11]\nv_mfma_f32_16x16x16_bf16 v[12:15], a[12:13], v[6:7], v[12:15]\n" \
"v_mfma_f32_16x16x16_bf16 v[16:19], a[20:21], v[6:7], v[16:19]\nv_mfma_f32_16x16x16_bf16 v[20:23], a[28:29], v[6:7], v[20:23]\nv_mfma_f32_16x16x16_bf16 v[8:11], a[6:7], v[6:7], v[8:11]\nv_mfma_f32_16x16x16_bf16 v[12:15], a[14:15], v[6:7], v[12:15]\nv_mfma_f32_16x16x16_bf16 v[16:19], a[22:23], v[6:7], v[16:19]\nv_mfma_f32_16x16x16_bf16 v[20:23], a[30:31], v[6:7], v[20:23]\ns_waitcnt vmcnt(28)\ns_barrier\n" \
"s_add_u32 m0, %[lw], 20480\ns_add_u32 s44, %[wo], 917504\nbuffer_load_dwordx4 v92, %[rw], s44 offen nt lds\ns_add_u32 m0, %[lw], 21504\ns_add_u32 s44, %[wo], 974848\nbuffer_load_dwordx4 v92, %[rw], s44 offen nt lds\ns_add_u32 m0, %[lw], 22528\ns_add_u32 s44, %[wo], 1032192\n" \
"buffer_load_dwordx4 v92, %[rw], s44 offen nt lds\ns_add_u32 m0, %[lw], 23552\ns_add_u32 s44, %[wo], 1089536\nbuffer_load_dwordx4 v92, %[rw], s44 offen nt lds\nds_read_b64_tr_b16 a[104:105], v93 offset:0\nds_read_b64_tr_b16 a[106:107], v93 offset:512\nds_read_b64_tr_b16 a[112:113], v93 offset:8\nds_read_b64_tr_b16 a[114:115], v93 offset:520\n" \
"ds_read_b64_tr_b16 a[120:121], v93 offset:12288\nds_read_b64_tr_b16 a[122:123], v93 offset:12800\nds_read_b64_tr_b16 a[128:129], v93 offset:12296\nds_read_b64_tr_b16 a[130:131], v93 offset:12808\ns_waitcnt lgkmcnt(0)\nv_mfma_f32_16x16x16_bf16 v[8:11], a[104:105], v[6:7], v[8:11]\nv_mfma_f32_16x16x16_bf16 v[12:15], a[112:113], v[6:7], v[12:15]\nv_mfma_f32_16x16x16_bf16 v[16:19], a[120:121], v[6:7], v[16:19]\n" \
"v_mfma_f32_16x16x16_bf16 v[20:23], a[128:129], v[6:7], v[20:23]\nv_mfma_f32_16x16x16_bf16 v[8:11], a[106:107], v[6:7], v[8:11]\nv_mfma_f32_16x16x16_bf16 v[12:15], a[114:115], v[6:7], v[12:15]\nv_mfma_f32_16x16x16_bf16 v[16:19], a[122:123], v[6:7], v[16:19]\nv_mfma_f32_16x16x16_bf16 v[20:23], a[130:131], v[6:7], v[20:23]\ns_waitcnt vmcnt(28)\ns_barrier\ns_add_u32 m0, %[tb], 0\n" \
"s_add_u32 s44, %[wo], 1146880\nbuffer_load_dwordx4 v92, %[rw], s44 offen nt lds\ns_add_u32 m0, %[tb], 1024\ns_add_u32 s44, %[wo], 1204224\nbuffer_load_dwordx4 v92, %[rw], s44 offen nt lds\ns_add_u32 m0, %[tb], 2048\ns_add_u32 s44, %[wo], 1261568\nbuffer_load_dwordx4 v92, %[rw], s44 offen nt lds\n" \
"s_add_u32 m0, %[tb], 3072\ns_add_u32 s44, %[wo], 1318912\nbuffer_load_dwordx4 v92, %[rw], s44 offen nt lds\nds_read_b64_tr_b16 a[108:109], v93 offset:4096\nds_read_b64_tr_b16 a[110:111], v93 offset:4608\nds_read_b64_tr_b16 a[116:117], v93 offset:4104\nds_read_b64_tr_b16 a[118:119], v93 offset:4616\nds_read_b64_tr_b16 a[124:125], v93 offset:16384\n" \
"ds_read_b64_tr_b16 a[126:127], v93 offset:16896\nds_read_b64_tr_b16 a[132:133], v93 offset:16392\nds_read_b64_tr_b16 a[134:135], v93 offset:16904\ns_waitcnt lgkmcnt(0)\nv_mfma_f32_16x16x16_bf16 v[8:11], a[108:109], v[6:7], v[8:11]\nv_mfma_f32_16x16x16_bf16 v[12:15], a[116:117], v[6:7], v[12:15]\nv_mfma_f32_16x16x16_bf16 v[16:19], a[124:125], v[6:7], v[16:19]\nv_mfma_f32_16x16x16_bf16 v[20:23], a[132:133], v[6:7], v[20:23]\n" \
"v_mfma_f32_16x16x16_bf16 v[8:11], a[110:111], v[6:7], v[8:11]\nv_mfma_f32_16x16x16_bf16 v[12:15], a[118:119], v[6:7], v[12:15]\nv_mfma_f32_16x16x16_bf16 v[16:19], a[126:127], v[6:7], v[16:19]\nv_mfma_f32_16x16x16_bf16 v[20:23], a[134:135], v[6:7], v[20:23]\ns_waitcnt vmcnt(28)\ns_barrier\ns_add_u32 m0, %[tb], 4096\ns_add_u32 s44, %[wo], 1376256\n" \
"buffer_load_dwordx4 v92, %[rw], s44 offen nt lds\ns_add_u32 m0, %[tb], 5120\ns_add_u32 s44, %[wo], 1433600\nbuffer_load_dwordx4 v92, %[rw], s44 offen nt lds\ns_add_u32 m0, %[tb], 6144\ns_add_u32 s44, %[wo], 1490944\nbuffer_load_dwordx4 v92, %[rw], s44 offen nt lds\ns_add_u32 m0, %[tb], 7168\n" \
"s_add_u32 s44, %[wo], 1548288\nbuffer_load_dwordx4 v92, %[rw], s44 offen nt lds\nds_read_b64_tr_b16 a[208:209], v93 offset:8192\nds_read_b64_tr_b16 a[210:211], v93 offset:8704\nds_read_b64_tr_b16 a[216:217], v93 offset:8200\nds_read_b64_tr_b16 a[218:219], v93 offset:8712\nds_read_b64_tr_b16 a[224:225], v93 offset:20480\nds_read_b64_tr_b16 a[226:227], v93 offset:20992\n" \
"ds_read_b64_tr_b16 a[232:233], v93 offset:20488\nds_read_b64_tr_b16 a[234:235], v93 offset:21000\ns_waitcnt lgkmcnt(0)\nv_mfma_f32_16x16x16_bf16 v[8:11], a[208:209], v[6:7], v[8:11]\nv_mfma_f32_16x16x16_bf16 v[12:15], a[216:217], v[6:7], v[12:15]\nv_mfma_f32_16x16x16_bf16 v[16:19], a[224:225], v[6:7], v[16:19]\nv_mfma_f32_16x16x16_bf16 v[20:23], a[232:233], v[6:7], v[20:23]\nv_mfma_f32_16x16x16_bf16 v[8:11], a[210:211], v[6:7], v[8:11]\n" \
"v_mfma_f32_16x16x16_bf16 v[12:15], a[218:219], v[6:7], v[12:15]\nv_mfma_f32_16x16x16_bf16 v[16:19], a[226:227], v[6:7], v[16:19]\nv_mfma_f32_16x16x16_bf16 v[20:23], a[234:235], v[6:7], v[20:23]\ns_waitcnt vmcnt(28)\ns_barrier\ns_add_u32 m0, %[tb], 8192\ns_add_u32 s44, %[wo], 1605632\nbuffer_load_dwordx4 v92, %[rw], s44 offen nt lds\n" \
"s_add_u32 m0, %[tb], 9216\ns_add_u32 s44, %[wo], 1662976\nbuffer_load_dwordx4 v92, %[rw], s44 offen nt lds\ns_add_u32 m0, %[tb], 10240\ns_add_u32 s44, %[wo], 1720320\nbuffer_load_dwordx4 v92, %[rw], s44 offen nt lds\ns_add_u32 m0, %[tb], 11264\ns_add_u32 s44, %[wo], 1777664\n" \
"buffer_load_dwordx4 v92, %[rw], s44 offen nt lds\nds_read_b64_tr_b16 a[212:213], v94 offset:0\nds_read_b64_tr_b16 a[214:215], v94 offset:512\nds_read_b64_tr_b16 a[220:221], v94 offset:8\nds_read_b64_tr_b16 a[222:223], v94 offset:520\nds_read_b64_tr_b16 a[228:229], v94 offset:24576\nds_read_b64_tr_b16 a[230:231], v94 offset:25088\nds_read_b64_tr_b16 a[236:237], v94 offset:24584\n" \
"ds_read_b64_tr_b16 a[238:239], v94 offset:25096\ns_waitcnt lgkmcnt(0)\nv_mfma_f32_16x16x16_bf16 v[8:11], a[212:213], v[6:7], v[8:11]\nv_mfma_f32_16x16x16_bf16 v[12:15], a[220:221], v[6:7], v[12:15]\nv_mfma_f32_16x16x16_bf16 v[16:19], a[228:229], v[6:7], v[16:19]\nv_mfma_f32_16x16x16_bf16 v[20:23], a[236:237], v[6:7], v[20:23]\nv_mfma_f32_16x16x16_bf16 v[8:11], a[214:215], v[6:7], v[8:11]\nv_mfma_f32_16x16x16_bf16 v[12:15], a[222:223], v[6:7], v[12:15]\n" \
"v_mfma_f32_16x16x16_bf16 v[16:19], a[230:231], v[6:7], v[16:19]\nv_mfma_f32_16x16x16_bf16 v[20:23], a[238:239], v[6:7], v[20:23]\ns_waitcnt vmcnt(28)\ns_barrier\ns_add_u32 m0, %[lw], 0\ns_add_u32 s44, %[wo], 2293760\nbuffer_load_dwordx4 v92, %[rw], s44 offen nt lds\ns_add_u32 m0, %[lw], 1024\n" \
"s_add_u32 s44, %[wo], 2351104\nbuffer_load_dwordx4 v92, %[rw], s44 offen nt lds\ns_add_u32 m0, %[lw], 2048\ns_add_u32 s44, %[wo], 2408448\nbuffer_load_dwordx4 v92, %[rw], s44 offen nt lds\ns_add_u32 m0, %[lw], 3072\ns_add_u32 s44, %[wo], 2465792\nbuffer_load_dwordx4 v92, %[rw], s44 offen nt lds\n" \
"ds_read_b64_tr_b16 v[152:153], v94 offset:4096\nds_read_b64_tr_b16 v[154:155], v94 offset:4608\nds_read_b64_tr_b16 v[160:161], v94 offset:4104\nds_read_b64_tr_b16 v[162:163], v94 offset:4616\nds_read_b64_tr_b16 v[168:169], v94 offset:28672\nds_read_b64_tr_b16 v[170:171], v94 offset:29184\nds_read_b64_tr_b16 v[176:177], v94 offset:28680\nds_read_b64_tr_b16 v[178:179], v94 offset:29192\n" \
"s_waitcnt lgkmcnt(0)\nv_mfma_f32_16x16x16_bf16 v[8:11], v[152:153], v[6:7], v[8:11]\nv_mfma_f32_16x16x16_bf16 v[12:15], v[160:161], v[6:7], v[12:15]\nv_mfma_f32_16x16x16_bf16 v[16:19], v[168:169], v[6:7], v[16:19]\nv_mfma_f32_16x16x16_bf16 v[20:23], v[176:177], v[6:7], v[20:23]\nv_mfma_f32_16x16x16_bf16 v[8:11], v[154:155], v[6:7], v[8:11]\nv_mfma_f32_16x16x16_bf16 v[12:15], v[162:163], v[6:7], v[12:15]\nv_mfma_f32_16x16x16_bf16 v[16:19], v[170:171], v[6:7], v[16:19]\n" \
"v_mfma_f32_16x16x16_bf16 v[20:23], v[178:179], v[6:7], v[20:23]\ns_waitcnt vmcnt(28)\ns_barrier\ns_add_u32 m0, %[lw], 4096\ns_add_u32 s44, %[wo], 2523136\nbuffer_load_dwordx4 v92, %[rw], s44 offen nt lds\ns_add_u32 m0, %[lw], 5120\ns_add_u32 s44, %[wo], 2580480\n" \
"buffer_load_dwordx4 v92, %[rw], s44 offen nt lds\ns_add_u32 m0, %[lw], 6144\ns_add_u32 s44, %[wo], 2637824\nbuffer_load_dwordx4 v92, %[rw], s44 offen nt lds\ns_add_u32 m0, %[lw], 7168\ns_add_u32 s44, %[wo], 2695168\nbuffer_load_dwordx4 v92, %[rw], s44 offen nt lds\nds_read_b64_tr_b16 v[156:157], v94 offset:8192\n" \
"ds_read_b64_tr_b16 v[158:159], v94 offset:8704\nds_read_b64_tr_b16 v[164:165], v94 offset:8200\nds_read_b64_tr_b16 v[166:167], v94 offset:8712\nds_read_b64_tr_b16 v[172:173], v94 offset:32768\nds_read_b64_tr_b16 v[174:175], v94 offset:33280\nds_read_b64_tr_b16 v[180:181], v94 offset:32776\nds_read_b64_tr_b16 v[182:183], v94 offset:33288\ns_waitcnt lgkmcnt(0)\n" \
"v_mfma_f32_16x16x16_bf16 v[8:11], v[156:157], v[6:7], v[8:11]\nv_mfma_f32_16x16x16_bf16 v[12:15], v[164:165], v[6:7], v[12:15]\nv_mfma_f32_16x16x16_bf16 v[16:19], v[172:173], v[6:7], v[16:19]\nv_mfma_f32_16x16x16_bf16 v[20:23], v[180:181], v[6:7], v[20:23]\nv_mfma_f32_16x16x16_bf16 v[8:11], v[158:159], v[6:7], v[8:11]\nv_mfma_f32_16x16x16_bf16 v[12:15], v[166:167], v[6:7], v[12:15]\nv_mfma_f32_16x16x16_bf16 v[16:19], v[174:175], v[6:7], v[16:19]\nv_mfma_f32_16x16x16_bf16 v[20:23], v[182:183], v[6:7], v[20:23]\n" \
"s_waitcnt vmcnt(28)\ns_barrier\ns_add_u32 m0, %[lw], 8192\ns_add_u32 s44, %[wo], 2752512\nbuffer_load_dwordx4 v92, %[rw], s44 offen nt lds\ns_add_u32 m0, %[lw], 9216\ns_add_u32 s44, %[wo], 2809856\nbuffer_load_dwordx4 v92, %[rw], s44 offen nt lds\n" \
"s_add_u32 m0, %[lw], 10240\ns_add_u32 s44, %[wo], 2867200\nbuffer_load_dwordx4 v92, %[rw], s44 offen nt lds\ns_add_u32 m0, %[lw], 11264\ns_add_u32 s44, %[wo], 2924544\nbuffer_load_dwordx4 v92, %[rw], s44 offen nt lds\nds_read_b64_tr_b16 a[32:33], v94 offset:12288\nds_read_b64_tr_b16 a[34:35], v94 offset:12800\n" \
"ds_read_b64_tr_b16 v[88:89], v94 offset:12296\nds_read_b64_tr_b16 v[90:91], v94 offset:12808\nds_read_b64_tr_b16 a[40:41], v94 offset:36864\nds_read_b64_tr_b16 a[42:43], v94 offset:37376\nds_read_b64_tr_b16 a[48:49], v94 offset:36872\nds_read_b64_tr_b16 a[50:51], v94 offset:37384\ns_waitcnt lgkmcnt(0)\nv_mfma_f32_16x16x16_bf16 v[8:11], a[32:33], v[6:7], v[8:11]\n" \
"v_mfma_f32_16x16x16_bf16 v[12:15], v[88:89], v[6:7], v[12:15]\nv_mfma_f32_16x16x16_bf16 v[16:19], a[40:41], v[6:7], v[16:19]\nv_mfma_f32_16x16x16_bf16 v[20:23], a[48:49], v[6:7], v[20:23]\nv_mfma_f32_16x16x16_bf16 v[8:11], a[34:35], v[6:7], v[8:11]\nv_mfma_f32_16x16x16_bf16 v[12:15], v[90:91], v[6:7], v[12:15]\nv_mfma_f32_16x16x16_bf16 v[16:19], a[42:43], v[6:7], v[16:19]\nv_mfma_f32_16x16x16_bf16 v[20:23], a[50:51], v[6:7], v[20:23]\ns_waitcnt vmcnt(28)\n" \
"s_barrier\ns_add_u32 m0, %[lw], 12288\ns_add_u32 s44, %[wo], 2981888\nbuffer_load_dwordx4 v92, %[rw], s44 offen nt lds\ns_add_u32 m0, %[lw], 13312\ns_add_u32 s44, %[wo], 3039232\nbuffer_load_dwordx4 v92, %[rw], s44 offen nt lds\ns_add_u32 m0, %[lw], 14336\n" \
"s_add_u32 s44, %[wo], 3096576\nbuffer_load_dwordx4 v92, %[rw], s44 offen nt lds\ns_add_u32 m0, %[lw], 15360\ns_add_u32 s44, %[wo], 3153920\nbuffer_load_dwordx4 v92, %[rw], s44 offen nt lds\nds_read_b64_tr_b16 a[36:37], v94 offset:16384\nds_read_b64_tr_b16 a[38:39], v94 offset:16896\nds_read_b64_tr_b16 v[84:85], v94 offset:16392\n" \
"ds_read_b64_tr_b16 v[86:87], v94 offset:16904\nds_read_b64_tr_b16 a[44:45], v94 offset:40960\nds_read_b64_tr_b16 a[46:47], v94 offset:41472\nds_read_b64_tr_b16 a[52:53], v94 offset:40968\nds_read_b64_tr_b16 a[54:55], v94 offset:41480\ns_waitcnt lgkmcnt(0)\nv_mfma_f32_16x16x16_bf16 v[8:11], a[36:37], v[6:7], v[8:11]\nv_mfma_f32_16x16x16_bf16 v[12:15], v[84:85], v[6:7], v[12:15]\n" \
"v_mfma_f32_16x16x16_bf16 v[16:19], a[44:45], v[6:7], v[16:19]\nv_mfma_f32_16x16x16_bf16 v[20:23], a[52:53], v[6:7], v[20:23]\nv_mfma_f32_16x16x16_bf16 v[8:11], a[38:39], v[6:7], v[8:11]\nv_mfma_f32_16x16x16_bf16 v[12:15], v[86:87], v[6:7], v[12:15]\nv_mfma_f32_16x16x16_bf16 v[16:19], a[46:47], v[6:7], v[16:19]\nv_mfma_f32_16x16x16_bf16 v[20:23], a[54:55], v[6:7], v[20:23]\ns_waitcnt vmcnt(28)\ns_barrier\n" \
"s_add_u32 m0, %[lw], 16384\ns_add_u32 s44, %[wo], 3211264\nbuffer_load_dwordx4 v92, %[rw], s44 offen nt lds\ns_add_u32 m0, %[lw], 17408\ns_add_u32 s44, %[wo], 3268608\nbuffer_load_dwordx4 v92, %[rw], s44 offen nt lds\ns_add_u32 m0, %[lw], 18432\ns_add_u32 s44, %[wo], 3325952\n" \
"buffer_load_dwordx4 v92, %[rw], s44 offen nt lds\ns_add_u32 m0, %[lw], 19456\ns_add_u32 s44, %[wo], 3383296\nbuffer_load_dwordx4 v92, %[rw], s44 offen nt lds\nds_read_b64_tr_b16 a[56:57], v94 offset:20480\nds_read_b64_tr_b16 a[58:59], v94 offset:20992\nds_read_b64_tr_b16 a[64:65], v94 offset:20488\nds_read_b64_tr_b16 a[66:67], v94 offset:21000\n" \
"ds_read_b64_tr_b16 v[80:81], v94 offset:45056\nds_read_b64_tr_b16 v[82:83], v94 offset:45568\nds_read_b64_tr_b16 a[72:73], v94 offset:45064\nds_read_b64_tr_b16 a[74:75], v94 offset:45576\ns_waitcnt lgkmcnt(0)\nv_mfma_f32_16x16x16_bf16 v[8:11], a[56:57], v[6:7], v[8:11]\nv_mfma_f32_16x16x16_bf16 v[12:15], a[64:65], v[6:7], v[12:15]\nv_mfma_f32_16x16x16_bf16 v[16:19], v[80:81], v[6:7], v[16:19]\n" \
"v_mfma_f32_16x16x16_bf16 v[20:23], a[72:73], v[6:7], v[20:23]\nv_mfma_f32_16x16x16_bf16 v[8:11], a[58:59], v[6:7], v[8:11]\nv_mfma_f32_16x16x16_bf16 v[12:15], a[66:67], v[6:7], v[12:15]\nv_mfma_f32_16x16x16_bf16 v[16:19], v[82:83], v[6:7], v[16:19]\nv_mfma_f32_16x16x16_bf16 v[20:23], a[74:75], v[6:7], v[20:23]\ns_waitcnt vmcnt(28)\ns_barrier\ns_add_u32 m0, %[lw], 20480\n" \
"s_add_u32 s44, %[wo], 3440640\nbuffer_load_dwordx4 v92, %[rw], s44 offen nt lds\ns_add_u32 m0, %[lw], 21504\ns_add_u32 s44, %[wo], 3497984\nbuffer_load_dwordx4 v92, %[rw], s44 offen nt lds\ns_add_u32 m0, %[lw], 22528\ns_add_u32 s44, %[wo], 3555328\nbuffer_load_dwordx4 v92, %[rw], s44 offen nt lds\n" \
"s_add_u32 m0, %[lw], 23552\ns_add_u32 s44, %[wo], 3612672\nbuffer_load_dwordx4 v92, %[rw], s44 offen nt lds\nds_read_b64_tr_b16 a[60:61], v93 offset:0\nds_read_b64_tr_b16 a[62:63], v93 offset:512\nds_read_b64_tr_b16 a[68:69], v93 offset:8\nds_read_b64_tr_b16 a[70:71], v93 offset:520\nds_read_b64_tr_b16 v[76:77], v93 offset:12288\n" \
"ds_read_b64_tr_b16 v[78:79], v93 offset:12800\nds_read_b64_tr_b16 a[76:77], v93 offset:12296\nds_read_b64_tr_b16 a[78:79], v93 offset:12808\ns_waitcnt lgkmcnt(0)\nv_mfma_f32_16x16x16_bf16 v[8:11], a[60:61], v[6:7], v[8:11]\nv_mfma_f32_16x16x16_bf16 v[12:15], a[68:69], v[6:7], v[12:15]\nv_mfma_f32_16x16x16_bf16 v[16:19], v[76:77], v[6:7], v[16:19]\nv_mfma_f32_16x16x16_bf16 v[20:23], a[76:77], v[6:7], v[20:23]\n" \
"v_mfma_f32_16x16x16_bf16 v[8:11], a[62:63], v[6:7], v[8:11]\nv_mfma_f32_16x16x16_bf16 v[12:15], a[70:71], v[6:7], v[12:15]\nv_mfma_f32_16x16x16_bf16 v[16:19], v[78:79], v[6:7], v[16:19]\nv_mfma_f32_16x16x16_bf16 v[20:23], a[78:79], v[6:7], v[20:23]\ns_waitcnt vmcnt(28)\ns_barrier\ns_add_u32 m0, %[tb], 0\ns_add_u32 s44, %[wo], 4128768\n" \
"buffer_load_dwordx4 v92, %[rw], s44 offen nt lds\ns_add_u32 m0, %[tb], 1024\ns_add_u32 s44, %[wo], 4186112\nbuffer_load_dwordx4 v92, %[rw], s44 offen nt lds\ns_add_u32 m0, %[tb], 2048\ns_add_u32 s44, %[wo], 4243456\nbuffer_load_dwordx4 v92, %[rw], s44 offen nt lds\ns_add_u32 m0, %[tb], 3072\n" \
"s_add_u32 s44, %[wo], 4300800\nbuffer_load_dwordx4 v92, %[rw], s44 offen nt lds\nds_read_b64_tr_b16 a[80:81], v93 offset:4096\nds_read_b64_tr_b16 a[82:83], v93 offset:4608\nds_read_b64_tr_b16 a[88:89], v93 offset:4104\nds_read_b64_tr_b16 a[90:91], v93 offset:4616\nds_read_b64_tr_b16 a[96:97], v93 offset:16384\nds_read_b64_tr_b16 a[98:99], v93 offset:16896\n" \
"ds_read_b64_tr_b16 v[72:73], v93 offset:16392\nds_read_b64_tr_b16 v[74:75], v93 offset:16904\ns_waitcnt lgkmcnt(0)\nv_mfma_f32_16x16x16_bf16 v[8:11], a[80:81], v[6:7], v[8:11]\nv_mfma_f32_16x16x16_bf16 v[12:15], a[88:89], v[6:7], v[12:15]\nv_mfma_f32_16x16x16_bf16 v[16:19], a[96:97], v[6:7], v[16:19]\nv_mfma_f32_16x16x16_bf16 v[20:23], v[72:73], v[6:7], v[20:23]\nv_mfma_f32_16x16x16_bf16 v[8:11], a[82:83], v[6:7], v[8:11]\n" \
"v_mfma_f32_16x16x16_bf16 v[12:15], a[90:91], v[6:7], v[12:15]\nv_mfma_f32_16x16x16_bf16 v[16:19], a[98:99], v[6:7], v[16:19]\nv_mfma_f32_16x16x16_bf16 v[20:23], v[74:75], v[6:7], v[20:23]\ns_waitcnt vmcnt(28)\ns_barrier\ns_add_u32 m0, %[tb], 4096\ns_add_u32 s44, %[wo], 4358144\nbuffer_load_dwordx4 v92, %[rw], s44 offen nt lds\n" \
"s_add_u32 m0, %[tb], 5120\ns_add_u32 s44, %[wo], 4415488\nbuffer_load_dwordx4 v92, %[rw], s44 offen nt lds\ns_add_u32 m0, %[tb], 6144\ns_add_u32 s44, %[wo], 4472832\nbuffer_load_dwordx4 v92, %[rw], s44 offen nt lds\ns_add_u32 m0, %[tb], 7168\ns_add_u32 s44, %[wo], 4530176\n" \
"buffer_load_dwordx4 v92, %[rw], s44 offen nt lds\nds_read_b64_tr_b16 a[84:85], v93 offset:8192\nds_read_b64_tr_b16 a[86:87], v93 offset:8704\nds_read_b64_tr_b16 a[92:93], v93 offset:8200\nds_read_b64_tr_b16 a[94:95], v93 offset:8712\nds_read_b64_tr_b16 a[100:101], v93 offset:20480\nds_read_b64_tr_b16 a[102:103], v93 offset:20992\nds_read_b64_tr_b16 v[68:69], v93 offset:20488\n" \
"ds_read_b64_tr_b16 v[70:71], v93 offset:21000\ns_waitcnt lgkmcnt(0)\nv_mfma_f32_16x16x16_bf16 v[8:11], a[84:85], v[6:7], v[8:11]\nv_mfma_f32_16x16x16_bf16 v[12:15], a[92:93], v[6:7], v[12:15]\nv_mfma_f32_16x16x16_bf16 v[16:19], a[100:101], v[6:7], v[16:19]\nv_mfma_f32_16x16x16_bf16 v[20:23], v[68:69], v[6:7], v[20:23]\nv_mfma_f32_16x16x16_bf16 v[8:11], a[86:87], v[6:7], v[8:11]\nv_mfma_f32_16x16x16_bf16 v[12:15], a[94:95], v[6:7], v[12:15]\n" \
"v_mfma_f32_16x16x16_bf16 v[16:19], a[102:103], v[6:7], v[16:19]\nv_mfma_f32_16x16x16_bf16 v[20:23], v[70:71], v[6:7], v[20:23]\ns_waitcnt vmcnt(28)\ns_barrier\ns_add_u32 m0, %[tb], 8192\ns_add_u32 s44, %[wo], 4587520\nbuffer_load_dwordx4 v92, %[rw], s44 offen nt lds\ns_add_u32 m0, %[tb], 9216\n" \
"s_add_u32 s44, %[wo], 4644864\nbuffer_load_dwordx4 v92, %[rw], s44 offen nt lds\ns_add_u32 m0, %[tb], 10240\ns_add_u32 s44, %[wo], 4702208\nbuffer_load_dwordx4 v92, %[rw], s44 offen nt lds\ns_add_u32 m0, %[tb], 11264\ns_add_u32 s44, %[wo], 4759552\nbuffer_load_dwordx4 v92, %[rw], s44 offen nt lds\n" \
"ds_read_b64_tr_b16 a[136:137], v94 offset:0\nds_read_b64_tr_b16 a[138:139], v94 offset:512\nds_read_b64_tr_b16 v[64:65], v94 offset:8\nds_read_b64_tr_b16 v[66:67], v94 offset:520\nds_read_b64_tr_b16 a[144:145], v94 offset:24576\nds_read_b64_tr_b16 a[146:147], v94 offset:25088\nds_read_b64_tr_b16 a[152:153], v94 offset:24584\nds_read_b64_tr_b16 a[154:155], v94 offset:25096\n" \
"s_waitcnt lgkmcnt(0)\nv_mfma_f32_16x16x16_bf16 v[8:11], a[136:137], v[6:7], v[8:11]\nv_mfma_f32_16x16x16_bf16 v[12:15], v[64:65], v[6:7], v[12:15]\nv_mfma_f32_16x16x16_bf16 v[16:19], a[144:145], v[6:7], v[16:19]\nv_mfma_f32_16x16x16_bf16 v[20:23], a[152:153], v[6:7], v[20:23]\nv_mfma_f32_16x16x16_bf16 v[8:11], a[138:139], v[6:7], v[8:11]\nv_mfma_f32_16x16x16_bf16 v[12:15], v[66:67], v[6:7], v[12:15]\nv_mfma_f32_16x16x16_bf16 v[16:19], a[146:147], v[6:7], v[16:19]\n" \
"v_mfma_f32_16x16x16_bf16 v[20:23], a[154:155], v[6:7], v[20:23]\ns_waitcnt vmcnt(28)\ns_barrier\ns_add_u32 m0, %[lw], 0\ns_add_u32 s44, %[wo], 4816896\nbuffer_load_dwordx4 v92, %[rw], s44 offen nt lds\ns_add_u32 m0, %[lw], 1024\ns_add_u32 s44, %[wo], 4874240\n" \
"buffer_load_dwordx4 v92, %[rw], s44 offen nt lds\ns_add_u32 m0, %[lw], 2048\ns_add_u32 s44, %[wo], 4931584\nbuffer_load_dwordx4 v92, %[rw], s44 offen nt lds\ns_add_u32 m0, %[lw], 3072\ns_add_u32 s44, %[wo], 4988928\nbuffer_load_dwordx4 v92, %[rw], s44 offen nt lds\nds_read_b64_tr_b16 a[140:141], v94 offset:4096\n" \
"ds_read_b64_tr_b16 a[142:143], v94 offset:4608\nds_read_b64_tr_b16 v[60:61], v94 offset:4104\nds_read_b64_tr_b16 v[62:63], v94 offset:4616\nds_read_b64_tr_b16 a[148:149], v94 offset:28672\nds_read_b64_tr_b16 a[150:151], v94 offset:29184\nds_read_b64_tr_b16 a[156:157], v94 offset:28680\nds_read_b64_tr_b16 a[158:159], v94 offset:29192\ns_waitcnt lgkmcnt(0)\n" \
"v_mfma_f32_16x16x16_bf16 v[8:11], a[140:141], v[6:7], v[8:11]\nv_mfma_f32_16x16x16_bf16 v[12:15], v[60:61], v[6:7], v[12:15]\nv_mfma_f32_16x16x16_bf16 v[16:19], a[148:149], v[6:7], v[16:19]\nv_mfma_f32_16x16x16_bf16 v[20:23], a[156:157], v[6:7], v[20:23]\nv_mfma_f32_16x16x16_bf16 v[8:11], a[142:143], v[6:7], v[8:11]\nv_mfma_f32_16x16x16_bf16 v[12:15], v[62:63], v[6:7], v[12:15]\nv_mfma_f32_16x16x16_bf16 v[16:19], a[150:151], v[6:7], v[16:19]\nv_mfma_f32_16x16x16_bf16 v[20:23], a[158:159], v[6:7], v[20:23]\n" \
"s_waitcnt vmcnt(28)\ns_barrier\ns_add_u32 m0, %[lw], 4096\ns_add_u32 s44, %[wo], 5046272\nbuffer_load_dwordx4 v92, %[rw], s44 offen nt lds\ns_add_u32 m0, %[lw], 5120\ns_add_u32 s44, %[wo], 5103616\nbuffer_load_dwordx4 v92, %[rw], s44 offen nt lds\n" \
"s_add_u32 m0, %[lw], 6144\ns_add_u32 s44, %[wo], 5160960\nbuffer_load_dwordx4 v92, %[rw], s44 offen nt lds\ns_add_u32 m0, %[lw], 7168\ns_add_u32 s44, %[wo], 5218304\nbuffer_load_dwordx4 v92, %[rw], s44 offen nt lds\nds_read_b64_tr_b16 a[160:161], v94 offset:8192\nds_read_b64_tr_b16 a[162:163], v94 offset:8704\n" \
"ds_read_b64_tr_b16 a[168:169], v94 offset:8200\nds_read_b64_tr_b16 a[170:171], v94 offset:8712\nds_read_b64_tr_b16 v[56:57], v94 offset:32768\nds_read_b64_tr_b16 v[58:59], v94 offset:33280\nds_read_b64_tr_b16 a[176:177], v94 offset:32776\nds_read_b64_tr_b16 a[178:179], v94 offset:33288\ns_waitcnt lgkmcnt(0)\nv_mfma_f32_16x16x16_bf16 v[8:11], a[160:161], v[6:7], v[8:11]\n" \
"v_mfma_f32_16x16x16_bf16 v[12:15], a[168:169], v[6:7], v[12:15]\nv_mfma_f32_16x16x16_bf16 v[16:19], v[56:57], v[6:7], v[16:19]\nv_mfma_f32_16x16x16_bf16 v[20:23], a[176:177], v[6:7], v[20:23]\nv_mfma_f32_16x16x16_bf16 v[8:11], a[162:163], v[6:7], v[8:11]\nv_mfma_f32_16x16x16_bf16 v[12:15], a[170:171], v[6:7], v[12:15]\nv_mfma_f32_16x16x16_bf16 v[16:19], v[58:59], v[6:7], v[16:19]\nv_mfma_f32_16x16x16_bf16 v[20:23], a[178:179], v[6:7], v[20:23]\ns_waitcnt vmcnt(28)\n" \
"s_barrier\ns_add_u32 m0, %[lw], 8192\ns_add_u32 s44, %[wo], 5275648\nbuffer_load_dwordx4 v92, %[rw], s44 offen nt lds\ns_add_u32 m0, %[lw], 9216\ns_add_u32 s44, %[wo], 5332992\nbuffer_load_dwordx4 v92, %[rw], s44 offen nt lds\ns_add_u32 m0, %[lw], 10240\n" \
"s_add_u32 s44, %[wo], 5390336\nbuffer_load_dwordx4 v92, %[rw], s44 offen nt lds\ns_add_u32 m0, %[lw], 11264\ns_add_u32 s44, %[wo], 5447680\nbuffer_load_dwordx4 v92, %[rw], s44 offen nt lds\nds_read_b64_tr_b16 a[164:165], v94 offset:12288\nds_read_b64_tr_b16 a[166:167], v94 offset:12800\nds_read_b64_tr_b16 a[172:173], v94 offset:12296\n" \
"ds_read_b64_tr_b16 a[174:175], v94 offset:12808\nds_read_b64_tr_b16 v[52:53], v94 offset:36864\nds_read_b64_tr_b16 v[54:55], v94 offset:37376\nds_read_b64_tr_b16 a[180:181], v94 offset:36872\nds_read_b64_tr_b16 a[182:183], v94 offset:37384\ns_waitcnt lgkmcnt(0)\nv_mfma_f32_16x16x16_bf16 v[8:11], a[164:165], v[6:7], v[8:11]\nv_mfma_f32_16x16x16_bf16 v[12:15], a[172:173], v[6:7], v[12:15]\n" \
"v_mfma_f32_16x16x16_bf16 v[16:19], v[52:53], v[6:7], v[16:19]\nv_mfma_f32_16x16x16_bf16 v[20:23], a[180:181], v[6:7], v[20:23]\nv_mfma_f32_16x16x16_bf16 v[8:11], a[166:167], v[6:7], v[8:11]\nv_mfma_f32_16x16x16_bf16 v[12:15], a[174:175], v[6:7], v[12:15]\nv_mfma_f32_16x16x16_bf16 v[16:19], v[54:55], v[6:7], v[16:19]\nv_mfma_f32_16x16x16_bf16 v[20:23], a[182:183], v[6:7], v[20:23]\ns_waitcnt vmcnt(28)\ns_barrier\n" \
"s_add_u32 m0, %[lw], 12288\ns_add_u32 s44, %[wo], 5963776\nbuffer_load_dwordx4 v92, %[rw], s44 offen nt lds\ns_add_u32 m0, %[lw], 13312\ns_add_u32 s44, %[wo], 6021120\nbuffer_load_dwordx4 v92, %[rw], s44 offen nt lds\ns_add_u32 m0, %[lw], 14336\ns_add_u32 s44, %[wo], 6078464\n" \
"buffer_load_dwordx4 v92, %[rw], s44 offen nt lds\ns_add_u32 m0, %[lw], 15360\ns_add_u32 s44, %[wo], 6135808\nbuffer_load_dwordx4 v92, %[rw], s44 offen nt lds\nds_read_b64_tr_b16 a[184:185], v94 offset:16384\nds_read_b64_tr_b16 a[186:187], v94 offset:16896\nds_read_b64_tr_b16 a[192:193], v94 offset:16392\nds_read_b64_tr_b16 a[194:195], v94 offset:16904\n" \
"ds_read_b64_tr_b16 a[200:201], v94 offset:40960\nds_read_b64_tr_b16 a[202:203], v94 offset:41472\nds_read_b64_tr_b16 v[48:49], v94 offset:40968\nds_read_b64_tr_b16 v[50:51], v94 offset:41480\ns_waitcnt lgkmcnt(0)\nv_mfma_f32_16x16x16_bf16 v[8:11], a[184:185], v[6:7], v[8:11]\nv_mfma_f32_16x16x16_bf16 v[12:15], a[192:193], v[6:7], v[12:15]\nv_mfma_f32_16x16x16_bf16 v[16:19], a[200:201], v[6:7], v[16:19]\n" \
"v_mfma_f32_16x16x16_bf16 v[20:23], v[48:49], v[6:7], v[20:23]\nv_mfma_f32_16x16x16_bf16 v[8:11], a[186:187], v[6:7], v[8:11]\nv_mfma_f32_16x16x16_bf16 v[12:15], a[194:195], v[6:7], v[12:15]\nv_mfma_f32_16x16x16_bf16 v[16:19], a[202:203], v[6:7], v[16:19]\nv_mfma_f32_16x16x16_bf16 v[20:23], v[50:51], v[6:7], v[20:23]\ns_waitcnt vmcnt(28)\ns_barrier\ns_add_u32 m0, %[lw], 16384\n" \
"s_add_u32 s44, %[wo], 6193152\nbuffer_load_dwordx4 v92, %[rw], s44 offen nt lds\ns_add_u32 m0, %[lw], 17408\ns_add_u32 s44, %[wo], 6250496\nbuffer_load_dwordx4 v92, %[rw], s44 offen nt lds\ns_add_u32 m0, %[lw], 18432\ns_add_u32 s44, %[wo], 6307840\nbuffer_load_dwordx4 v92, %[rw], s44 offen nt lds\n" \
"s_add_u32 m0, %[lw], 19456\ns_add_u32 s44, %[wo], 6365184\nbuffer_load_dwordx4 v92, %[rw], s44 offen nt lds\nds_read_b64_tr_b16 a[188:189], v94 offset:20480\nds_read_b64_tr_b16 a[190:191], v94 offset:20992\nds_read_b64_tr_b16 a[196:197], v94 offset:20488\nds_read_b64_tr_b16 a[198:199], v94 offset:21000\nds_read_b64_tr_b16 a[204:205], v94 offset:45056\n" \
"ds_read_b64_tr_b16 a[206:207], v94 offset:45568\nds_read_b64_tr_b16 v[44:45], v94 offset:45064\nds_read_b64_tr_b16 v[46:47], v94 offset:45576\ns_waitcnt lgkmcnt(0)\nv_mfma_f32_16x16x16_bf16 v[8:11], a[188:189], v[6:7], v[8:11]\nv_mfma_f32_16x16x16_bf16 v[12:15], a[196:197], v[6:7], v[12:15]\nv_mfma_f32_16x16x16_bf16 v[16:19], a[204:205], v[6:7], v[16:19]\nv_mfma_f32_16x16x16_bf16 v[20:23], v[44:45], v[6:7], v[20:23]\n" \
"v_mfma_f32_16x16x16_bf16 v[8:11], a[190:191], v[6:7], v[8:11]\nv_mfma_f32_16x16x16_bf16 v[12:15], a[198:199], v[6:7], v[12:15]\nv_mfma_f32_16x16x16_bf16 v[16:19], a[206:207], v[6:7], v[16:19]\nv_mfma_f32_16x16x16_bf16 v[20:23], v[46:47], v[6:7], v[20:23]\ns_waitcnt vmcnt(28)\ns_barrier\ns_add_u32 m0, %[lw], 20480\ns_add_u32 s44, %[wo], 6422528\n" \
"buffer_load_dwordx4 v92, %[rw], s44 offen nt lds\ns_add_u32 m0, %[lw], 21504\ns_add_u32 s44, %[wo], 6479872\nbuffer_load_dwordx4 v92, %[rw], s44 offen nt lds\ns_add_u32 m0, %[lw], 22528\ns_add_u32 s44, %[wo], 6537216\nbuffer_load_dwordx4 v92, %[rw], s44 offen nt lds\ns_add_u32 m0, %[lw], 23552\n" \
"s_add_u32 s44, %[wo], 6594560\nbuffer_load_dwordx4 v92, %[rw], s44 offen nt lds\nds_read_b64_tr_b16 a[240:241], v93 offset:0\nds_read_b64_tr_b16 a[242:243], v93 offset:512\nds_read_b64_tr_b16 v[40:41], v93 offset:8\nds_read_b64_tr_b16 v[42:43], v93 offset:520\nds_read_b64_tr_b16 a[248:249], v93 offset:12288\nds_read_b64_tr_b16 a[250:251], v93 offset:12800\n" \
"ds_read_b64_tr_b16 v[96:97], v93 offset:12296\nds_read_b64_tr_b16 v[98:99], v93 offset:12808\ns_waitcnt lgkmcnt(0)\nv_mfma_f32_16x16x16_bf16 v[8:11], a[240:241], v[6:7], v[8:11]\nv_mfma_f32_16x16x16_bf16 v[12:15], v[40:41], v[6:7], v[12:15]\nv_mfma_f32_16x16x16_bf16 v[16:19], a[248:249], v[6:7], v[16:19]\nv_mfma_f32_16x16x16_bf16 v[20:23], v[96:97], v[6:7], v[20:23]\nv_mfma_f32_16x16x16_bf16 v[8:11], a[242:243], v[6:7], v[8:11]\n" \
"v_mfma_f32_16x16x16_bf16 v[12:15], v[42:43], v[6:7], v[12:15]\nv_mfma_f32_16x16x16_bf16 v[16:19], a[250:251], v[6:7], v[16:19]\nv_mfma_f32_16x16x16_bf16 v[20:23], v[98:99], v[6:7], v[20:23]\ns_waitcnt vmcnt(28)\ns_barrier\ns_add_u32 m0, %[tb], 0\ns_add_u32 s44, %[wo], 6651904\nbuffer_load_dwordx4 v92, %[rw], s44 offen nt lds\n" \
"s_add_u32 m0, %[tb], 1024\ns_add_u32 s44, %[wo], 6709248\nbuffer_load_dwordx4 v92, %[rw], s44 offen nt lds\ns_add_u32 m0, %[tb], 2048\ns_add_u32 s44, %[wo], 6766592\nbuffer_load_dwordx4 v92, %[rw], s44 offen nt lds\ns_add_u32 m0, %[tb], 3072\ns_add_u32 s44, %[wo], 6823936\n" \
"buffer_load_dwordx4 v92, %[rw], s44 offen nt lds\nds_read_b64_tr_b16 a[244:245], v93 offset:4096\nds_read_b64_tr_b16 a[246:247], v93 offset:4608\nds_read_b64_tr_b16 v[36:37], v93 offset:4104\nds_read_b64_tr_b16 v[38:39], v93 offset:4616\nds_read_b64_tr_b16 a[252:253], v93 offset:16384\nds_read_b64_tr_b16 a[254:255], v93 offset:16896\nds_read_b64_tr_b16 v[100:101], v93 offset:16392\n" \
"ds_read_b64_tr_b16 v[102:103], v93 offset:16904\ns_waitcnt lgkmcnt(0)\nv_mfma_f32_16x16x16_bf16 v[8:11], a[244:245], v[6:7], v[8:11]\nv_mfma_f32_16x16x16_bf16 v[12:15], v[36:37], v[6:7], v[12:15]\nv_mfma_f32_16x16x16_bf16 v[16:19], a[252:253], v[6:7], v[16:19]\nv_mfma_f32_16x16x16_bf16 v[20:23], v[100:101], v[6:7], v[20:23]\nv_mfma_f32_16x16x16_bf16 v[8:11], a[246:247], v[6:7], v[8:11]\nv_mfma_f32_16x16x16_bf16 v[12:15], v[38:39], v[6:7], v[12:15]\n" \
"v_mfma_f32_16x16x16_bf16 v[16:19], a[254:255], v[6:7], v[16:19]\nv_mfma_f32_16x16x16_bf16 v[20:23], v[102:103], v[6:7], v[20:23]\ns_waitcnt vmcnt(28)\ns_barrier\ns_add_u32 m0, %[tb], 4096\ns_add_u32 s44, %[wo], 6881280\nbuffer_load_dwordx4 v92, %[rw], s44 offen nt lds\ns_add_u32 m0, %[tb], 5120\n" \
"s_add_u32 s44, %[wo], 6938624\nbuffer_load_dwordx4 v92, %[rw], s44 offen nt lds\ns_add_u32 m0, %[tb], 6144\ns_add_u32 s44, %[wo], 6995968\nbuffer_load_dwordx4 v92, %[rw], s44 offen nt lds\ns_add_u32 m0, %[tb], 7168\ns_add_u32 s44, %[wo], 7053312\nbuffer_load_dwordx4 v92, %[rw], s44 offen nt lds\n" \
"ds_read_b64_tr_b16 v[104:105], v93 offset:8192\nds_read_b64_tr_b16 v[106:107], v93 offset:8704\nds_read_b64_tr_b16 v[112:113], v93 offset:8200\nds_read_b64_tr_b16 v[114:115], v93 offset:8712\nds_read_b64_tr_b16 v[32:33], v93 offset:20480\nds_read_b64_tr_b16 v[34:35], v93 offset:20992\nds_read_b64_tr_b16 v[120:121], v93 offset:20488\nds_read_b64_tr_b16 v[122:123], v93 offset:21000\n" \
"s_waitcnt lgkmcnt(0)\nv_mfma_f32_16x16x16_bf16 v[8:11], v[104:105], v[6:7], v[8:11]\nv_mfma_f32_16x16x16_bf16 v[12:15], v[112:113], v[6:7], v[12:15]\nv_mfma_f32_16x16x16_bf16 v[16:19], v[32:33], v[6:7], v[16:19]\nv_mfma_f32_16x16x16_bf16 v[20:23], v[120:121], v[6:7], v[20:23]\nv_mfma_f32_16x16x16_bf16 v[8:11], v[106:107], v[6:7], v[8:11]\nv_mfma_f32_16x16x16_bf16 v[12:15], v[114:115], v[6:7], v[12:15]\nv_mfma_f32_16x16x16_bf16 v[16:19], v[34:35], v[6:7], v[16:19]\n" \
"v_mfma_f32_16x16x16_bf16 v[20:23], v[122:123], v[6:7], v[20:23]\ns_waitcnt vmcnt(28)\ns_barrier\ns_add_u32 m0, %[tb], 8192\ns_add_u32 s44, %[wo], 7110656\nbuffer_load_dwordx4 v92, %[rw], s44 offen nt lds\ns_add_u32 m0, %[tb], 9216\ns_add_u32 s44, %[wo], 7168000\n" \
"buffer_load_dwordx4 v92, %[rw], s44 offen nt lds\ns_add_u32 m0, %[tb], 10240\ns_add_u32 s44, %[wo], 7225344\nbuffer_load_dwordx4 v92, %[rw], s44 offen nt lds\ns_add_u32 m0, %[tb], 11264\ns_add_u32 s44, %[wo], 7282688\nbuffer_load_dwordx4 v92, %[rw], s44 offen nt lds\nds_read_b64_tr_b16 v[108:109], v94 offset:0\n" \
"ds_read_b64_tr_b16 v[110:111], v94 offset:512\nds_read_b64_tr_b16 v[116:117], v94 offset:8\nds_read_b64_tr_b16 v[118:119], v94 offset:520\nds_read_b64_tr_b16 v[28:29], v94 offset:24576\nds_read_b64_tr_b16 v[30:31], v94 offset:25088\nds_read_b64_tr_b16 v[124:125], v94 offset:24584\nds_read_b64_tr_b16 v[126:127], v94 offset:25096\ns_waitcnt lgkmcnt(0)\n" \
"v_mfma_f32_16x16x16_bf16 v[8:11], v[108:109], v[6:7], v[8:11]\nv_mfma_f32_16x16x16_bf16 v[12:15], v[116:117], v[6:7], v[12:15]\nv_mfma_f32_16x16x16_bf16 v[16:19], v[28:29], v[6:7], v[16:19]\nv_mfma_f32_16x16x16_bf16 v[20:23], v[124:125], v[6:7], v[20:23]\nv_mfma_f32_16x16x16_bf16 v[8:11], v[110:111], v[6:7], v[8:11]\nv_mfma_f32_16x16x16_bf16 v[12:15], v[118:119], v[6:7], v[12:15]\nv_mfma_f32_16x16x16_bf16 v[16:19], v[30:31], v[6:7], v[16:19]\nv_mfma_f32_16x16x16_bf16 v[20:23], v[126:127], v[6:7], v[20:23]\n" \
"s_waitcnt vmcnt(28)\ns_barrier\nds_read_b64_tr_b16 v[128:129], v94 offset:4096\nds_read_b64_tr_b16 v[130:131], v94 offset:4608\nds_read_b64_tr_b16 v[136:137], v94 offset:4104\nds_read_b64_tr_b16 v[138:139], v94 offset:4616\nds_read_b64_tr_b16 v[144:145], v94 offset:28672\nds_read_b64_tr_b16 v[146:147], v94 offset:29184\n" \
"ds_read_b64_tr_b16 v[24:25], v94 offset:28680\nds_read_b64_tr_b16 v[26:27], v94 offset:29192\ns_waitcnt lgkmcnt(0)\nv_mfma_f32_16x16x16_bf16 v[8:11], v[128:129], v[6:7], v[8:11]\nv_mfma_f32_16x16x16_bf16 v[12:15], v[136:137], v[6:7], v[12:15]\nv_mfma_f32_16x16x16_bf16 v[16:19], v[144:145], v[6:7], v[16:19]\nv_mfma_f32_16x16x16_bf16 v[20:23], v[24:25], v[6:7], v[20:23]\nv_mfma_f32_16x16x16_bf16 v[8:11], v[130:131], v[6:7], v[8:11]\n" \
"v_mfma_f32_16x16x16_bf16 v[12:15], v[138:139], v[6:7], v[12:15]\nv_mfma_f32_16x16x16_bf16 v[16:19], v[146:147], v[6:7], v[16:19]\nv_mfma_f32_16x16x16_bf16 v[20:23], v[26:27], v[6:7], v[20:23]\ns_waitcnt vmcnt(24)\nds_write_b128 v1, v[88:91] offset:0\nds_write_b128 v1, v[84:87] offset:1024\nds_write_b128 v1, v[80:83] offset:2048\nds_write_b128 v1, v[76:79] offset:3072\n" \
"s_barrier\nds_read_b64_tr_b16 v[132:133], v94 offset:8192\nds_read_b64_tr_b16 v[134:135], v94 offset:8704\nds_read_b64_tr_b16 v[140:141], v94 offset:8200\nds_read_b64_tr_b16 v[142:143], v94 offset:8712\nds_read_b64_tr_b16 v[148:149], v94 offset:32768\nds_read_b64_tr_b16 v[150:151], v94 offset:33280\nds_read_b64_tr_b16 v[76:77], v94 offset:32776\n" \
"ds_read_b64_tr_b16 v[78:79], v94 offset:33288\ns_waitcnt lgkmcnt(0)\nv_mfma_f32_16x16x16_bf16 v[8:11], v[132:133], v[6:7], v[8:11]\nv_mfma_f32_16x16x16_bf16 v[12:15], v[140:141], v[6:7], v[12:15]\nv_mfma_f32_16x16x16_bf16 v[16:19], v[148:149], v[6:7], v[16:19]\nv_mfma_f32_16x16x16_bf16 v[20:23], v[76:77], v[6:7], v[20:23]\nv_mfma_f32_16x16x16_bf16 v[8:11], v[134:135], v[6:7], v[8:11]\nv_mfma_f32_16x16x16_bf16 v[12:15], v[142:143], v[6:7], v[12:15]\n" \
"v_mfma_f32_16x16x16_bf16 v[16:19], v[150:151], v[6:7], v[16:19]\nv_mfma_f32_16x16x16_bf16 v[20:23], v[78:79], v[6:7], v[20:23]\ns_waitcnt vmcnt(20)\nds_write_b128 v1, v[72:75] offset:4096\nds_write_b128 v1, v[68:71] offset:5120\nds_write_b128 v1, v[64:67] offset:6144\nds_write_b128 v1, v[60:63] offset:7168\ns_barrier\n" \
"ds_read_b64_tr_b16 v[184:185], v94 offset:12288\nds_read_b64_tr_b16 v[186:187], v94 offset:12800\nds_read_b64_tr_b16 v[60:61], v94 offset:12296\nds_read_b64_tr_b16 v[62:63], v94 offset:12808\nds_read_b64_tr_b16 v[192:193], v94 offset:36864\nds_read_b64_tr_b16 v[194:195], v94 offset:37376\nds_read_b64_tr_b16 v[200:201], v94 offset:36872\nds_read_b64_tr_b16 v[202:203], v94 offset:37384\n" \
"s_waitcnt lgkmcnt(0)\nv_mfma_f32_16x16x16_bf16 v[8:11], v[184:185], v[6:7], v[8:11]\nv_mfma_f32_16x16x16_bf16 v[12:15], v[60:61], v[6:7], v[12:15]\nv_mfma_f32_16x16x16_bf16 v[16:19], v[192:193], v[6:7], v[16:19]\nv_mfma_f32_16x16x16_bf16 v[20:23], v[200:201], v[6:7], v[20:23]\nv_mfma_f32_16x16x16_bf16 v[8:11], v[186:187], v[6:7], v[8:11]\nv_mfma_f32_16x16x16_bf16 v[12:15], v[62:63], v[6:7], v[12:15]\nv_mfma_f32_16x16x16_bf16 v[16:19], v[194:195], v[6:7], v[16:19]\n" \
"v_mfma_f32_16x16x16_bf16 v[20:23], v[202:203], v[6:7], v[20:23]\ns_waitcnt vmcnt(16)\nds_write_b128 v1, v[56:59] offset:8192\nds_write_b128 v1, v[52:55] offset:9216\nds_write_b128 v1, v[48:51] offset:10240\nds_write_b128 v1, v[44:47] offset:11264\ns_barrier\nds_read_b64_tr_b16 v[188:189], v94 offset:16384\n" \
"ds_read_b64_tr_b16 v[190:191], v94 offset:16896\nds_read_b64_tr_b16 v[44:45], v94 offset:16392\nds_read_b64_tr_b16 v[46:47], v94 offset:16904\nds_read_b64_tr_b16 v[196:197], v94 offset:40960\nds_read_b64_tr_b16 v[198:199], v94 offset:41472\nds_read_b64_tr_b16 v[204:205], v94 offset:40968\nds_read_b64_tr_b16 v[206:207], v94 offset:41480\ns_waitcnt lgkmcnt(0)\n" \
"v_mfma_f32_16x16x16_bf16 v[8:11], v[188:189], v[6:7], v[8:11]\nv_mfma_f32_16x16x16_bf16 v[12:15], v[44:45], v[6:7], v[12:15]\nv_mfma_f32_16x16x16_bf16 v[16:19], v[196:197], v[6:7], v[16:19]\nv_mfma_f32_16x16x16_bf16 v[20:23], v[204:205], v[6:7], v[20:23]\nv_mfma_f32_16x16x16_bf16 v[8:11], v[190:191], v[6:7], v[8:11]\nv_mfma_f32_16x16x16_bf16 v[12:15], v[46:47], v[6:7], v[12:15]\nv_mfma_f32_16x16x16_bf16 v[16:19], v[198:199], v[6:7], v[16:19]\nv_mfma_f32_16x16x16_bf16 v[20:23], v[206:207], v[6:7], v[20:23]\n" \
"s_waitcnt vmcnt(12)\nds_write_b128 v1, v[40:43] offset:12288\nds_write_b128 v1, v[36:39] offset:13312\nds_write_b128 v1, v[32:35] offset:14336\nds_write_b128 v1, v[28:31] offset:15360\ns_barrier\nds_read_b64_tr_b16 v[208:209], v94 offset:20480\nds_read_b64_tr_b16 v[210:211], v94 offset:20992\n" \
"ds_read_b64_tr_b16 v[216:217], v94 offset:20488\nds_read_b64_tr_b16 v[218:219], v94 offset:21000\nds_read_b64_tr_b16 v[28:29], v94 offset:45056\nds_read_b64_tr_b16 v[30:31], v94 offset:45568\nds_read_b64_tr_b16 v[224:225], v94 offset:45064\nds_read_b64_tr_b16 v[226:227], v94 offset:45576\ns_waitcnt lgkmcnt(0)\nv_mfma_f32_16x16x16_bf16 v[8:11], v[208:209], v[6:7], v[8:11]\n" \
"v_mfma_f32_16x16x16_bf16 v[12:15], v[216:217], v[6:7], v[12:15]\nv_mfma_f32_16x16x16_bf16 v[16:19], v[28:29], v[6:7], v[16:19]\nv_mfma_f32_16x16x16_bf16 v[20:23], v[224:225], v[6:7], v[20:23]\nv_mfma_f32_16x16x16_bf16 v[8:11], v[210:211], v[6:7], v[8:11]\nv_mfma_f32_16x16x16_bf16 v[12:15], v[218:219], v[6:7], v[12:15]\nv_mfma_f32_16x16x16_bf16 v[16:19], v[30:31], v[6:7], v[16:19]\nv_mfma_f32_16x16x16_bf16 v[20:23], v[226:227], v[6:7], v[20:23]\ns_waitcnt vmcnt(8)\n" \
"ds_write_b128 v1, v[24:27] offset:16384\nds_write_b128 v1, v[76:79] offset:17408\nds_write_b128 v1, v[60:63] offset:18432\nds_write_b128 v1, v[44:47] offset:19456\ns_barrier\nds_read_b64_tr_b16 v[212:213], v93 offset:0\nds_read_b64_tr_b16 v[214:215], v93 offset:512\nds_read_b64_tr_b16 v[220:221], v93 offset:8\n" \
"ds_read_b64_tr_b16 v[222:223], v93 offset:520\nds_read_b64_tr_b16 v[44:45], v93 offset:12288\nds_read_b64_tr_b16 v[46:47], v93 offset:12800\nds_read_b64_tr_b16 v[228:229], v93 offset:12296\nds_read_b64_tr_b16 v[230:231], v93 offset:12808\ns_waitcnt lgkmcnt(0)\nv_mfma_f32_16x16x16_bf16 v[8:11], v[212:213], v[6:7], v[8:11]\nv_mfma_f32_16x16x16_bf16 v[12:15], v[220:221], v[6:7], v[12:15]\n" \
"v_mfma_f32_16x16x16_bf16 v[16:19], v[44:45], v[6:7], v[16:19]\nv_mfma_f32_16x16x16_bf16 v[20:23], v[228:229], v[6:7], v[20:23]\nv_mfma_f32_16x16x16_bf16 v[8:11], v[214:215], v[6:7], v[8:11]\nv_mfma_f32_16x16x16_bf16 v[12:15], v[222:223], v[6:7], v[12:15]\nv_mfma_f32_16x16x16_bf16 v[16:19], v[46:47], v[6:7], v[16:19]\nv_mfma_f32_16x16x16_bf16 v[20:23], v[230:231], v[6:7], v[20:23]\ns_waitcnt vmcnt(4)\nds_write_b128 v1, v[28:31] offset:20480\n" \
"ds_write_b128 v1, v[44:47] offset:21504\ns_barrier\nds_read_b64_tr_b16 v[232:233], v93 offset:4096\nds_read_b64_tr_b16 v[234:235], v93 offset:4608\nds_read_b64_tr_b16 v[240:241], v93 offset:4104\nds_read_b64_tr_b16 v[242:243], v93 offset:4616\nds_read_b64_tr_b16 v[248:249], v93 offset:16384\nds_read_b64_tr_b16 v[250:251], v93 offset:16896\n" \
"ds_read_b64_tr_b16 v[44:45], v93 offset:16392\nds_read_b64_tr_b16 v[46:47], v93 offset:16904\ns_waitcnt lgkmcnt(0)\nv_mfma_f32_16x16x16_bf16 v[8:11], v[232:233], v[6:7], v[8:11]\nv_mfma_f32_16x16x16_bf16 v[12:15], v[240:241], v[6:7], v[12:15]\nv_mfma_f32_16x16x16_bf16 v[16:19], v[248:249], v[6:7], v[16:19]\nv_mfma_f32_16x16x16_bf16 v[20:23], v[44:45], v[6:7], v[20:23]\nv_mfma_f32_16x16x16_bf16 v[8:11], v[234:235], v[6:7], v[8:11]\n" \
"v_mfma_f32_16x16x16_bf16 v[12:15], v[242:243], v[6:7], v[12:15]\nv_mfma_f32_16x16x16_bf16 v[16:19], v[250:251], v[6:7], v[16:19]\nv_mfma_f32_16x16x16_bf16 v[20:23], v[46:47], v[6:7], v[20:23]\ns_waitcnt vmcnt(0)\nds_write_b128 v1, v[44:47] offset:22528\ns_barrier\nds_read_b64_tr_b16 v[236:237], v93 offset:8192\nds_read_b64_tr_b16 v[238:239], v93 offset:8704\n" \
"ds_read_b64_tr_b16 v[244:245], v93 offset:8200\nds_read_b64_tr_b16 v[246:247], v93 offset:8712\nds_read_b64_tr_b16 v[252:253], v93 offset:20480\nds_read_b64_tr_b16 v[254:255], v93 offset:20992\nds_read_b64_tr_b16 v[44:45], v93 offset:20488\nds_read_b64_tr_b16 v[46:47], v93 offset:21000\ns_waitcnt lgkmcnt(0)\nv_mfma_f32_16x16x16_bf16 v[8:11], v[236:237], v[6:7], v[8:11]\n" \
"v_mfma_f32_16x16x16_bf16 v[12:15], v[244:245], v[6:7], v[12:15]\nv_mfma_f32_16x16x16_bf16 v[16:19], v[252:253], v[6:7], v[16:19]\nv_mfma_f32_16x16x16_bf16 v[20:23], v[44:45], v[6:7], v[20:23]\nv_mfma_f32_16x16x16_bf16 v[8:11], v[238:239], v[6:7], v[8:11]\nv_mfma_f32_16x16x16_bf16 v[12:15], v[246:247], v[6:7], v[12:15]\nv_mfma_f32_16x16x16_bf16 v[16:19], v[254:255], v[6:7], v[16:19]\nv_mfma_f32_16x16x16_bf16 v[20:23], v[46:47], v[6:7], v[20:23]\nds_write_b128 v1, v[44:47] offset:23552\n" \
"s_waitcnt lgkmcnt(0)\ns_barrier\ns_mov_b32 s40, 0\ns_mov_b32 s43, 0\ns_lshl_b32 s57, %[w], 12\ns_mov_b32 s58, 0xbeeff558\ns_mov_b32 s59, 0x409d30fc\ns_mov_b32 s56, -2147483648\n" \
".Lgg_next%=:\ns_cmp_lg_u32 %[w], 0\ns_cbranch_scc1 .Lgg_w1%=\nbuffer_load_dword v56, v3, %[rs], 0 offen sc1\ns_waitcnt vmcnt(0)\ns_barrier\nbuffer_store_dword v90, off, %[rs], s56 sc1\n.Lgg_poll%=:\n" \
"v_cmp_gt_i32 vcc, s40, v56\ns_cbranch_vccz .Lgg_go%=\ns_cmp_ge_u32 s40, 62\ns_cbranch_scc1 .Lgg_ns1050%=\ns_sleep 12\n.Lgg_ns1050%=:\ns_sleep 1\nbuffer_load_dword v56, v3, %[rs], 0 offen sc1\n" \
"s_waitcnt vmcnt(0)\ns_add_u32 s43, s43, 1\ns_cmp_lt_u32 s43, 262144\ns_cbranch_scc1 .Lgg_poll%=\ns_branch .Lgg_abort%=\n.Lgg_w1%=:\ns_waitcnt vmcnt(0)\ns_barrier\n" \
".Lgg_go%=:\ns_add_u32 s44, s40, 0\ns_lshl_b32 s44, s44, 17\ns_add_u32 s44, s44, %[xn]\ns_lshr_b32 s45, s44, 8\ns_and_b32 s45, s45, 0x2000\ns_xor_b32 s41, s44, s45\ns_add_u32 s61, s41, %[xw]\n" \
"s_barrier\ns_add_u32 m0, s57, 0\ns_add_u32 s60, s61, 0\nbuffer_load_dwordx4 v0, %[rs], s60 offen offset:0 sc1 lds\nbuffer_load_dwordx4 v0, %[rs], s60 offen offset:1024 sc1 lds\nbuffer_load_dwordx4 v0, %[rs], s60 offen offset:2048 sc1 lds\nbuffer_load_dwordx4 v0, %[rs], s60 offen offset:3072 sc1 lds\ns_add_u32 m0, s57, 16384\n" \
"s_add_u32 s60, s61, 32768\nbuffer_load_dwordx4 v0, %[rs], s60 offen offset:0 sc1 lds\nbuffer_load_dwordx4 v0, %[rs], s60 offen offset:1024 sc1 lds\nbuffer_load_dwordx4 v0, %[rs], s60 offen offset:2048 sc1 lds\nbuffer_load_dwordx4 v0, %[rs], s60 offen offset:3072 sc1 lds\nbuffer_load_dwordx4 v[40:43], v5, %[rs], s41 offen offset:0 sc1\nbuffer_load_dwordx4 v[44:47], v5, %[rs], s41 offen offset:256 sc1\ns_lshl_b32 s44, s40, 19\n" \
"s_add_u32 s44, s44, %[ho]\ns_lshr_b32 s45, s44, 8\ns_and_b32 s45, s45, 0x2000\ns_xor_b32 s55, s44, s45\ns_waitcnt vmcnt(6)\ns_barrier\nds_read_b128 v[56:59], v2 offset:0\nds_read_b128 v[60:63], v2 offset:2048\n" \
"ds_read_b128 v[64:67], v2 offset:4096\ns_waitcnt lgkmcnt(2)\nv_mfma_f32_16x16x32_bf16 v[24:27], a[0:3], v[56:59], 0\nds_read_b128 v[68:71], v2 offset:6144\nds_read_b128 v[80:83], v1 offset:0\nv_mfma_f32_16x16x32_bf16 v[28:31], a[8:11], v[56:59], 0\nbuffer_load_dwordx4 v[48:51], v5, %[rs], s41 offen offset:512 sc1\nv_mfma_f32_16x16x32_bf16 v[32:35], a[16:19], v[56:59], 0\n" \
"v_mfma_f32_16x16x32_bf16 v[36:39], a[24:27], v[56:59], 0\ns_waitcnt lgkmcnt(3)\nds_read_b128 v[72:75], v1 offset:1024\nv_mfma_f32_16x16x32_bf16 v[24:27], a[4:7], v[60:63], v[24:27]\nds_read_b128 v[56:59], v2 offset:8192\nv_mfma_f32_16x16x32_bf16 v[28:31], a[12:15], v[60:63], v[28:31]\nbuffer_load_dwordx4 v[52:55], v5, %[rs], s41 offen offset:768 sc1\nv_mfma_f32_16x16x32_bf16 v[32:35], a[20:23], v[60:63], v[32:35]\n" \
"v_mfma_f32_16x16x32_bf16 v[36:39], a[28:31], v[60:63], v[36:39]\ns_waitcnt lgkmcnt(4)\nds_read_b128 v[76:79], v1 offset:2048\nv_mfma_f32_16x16x32_bf16 v[24:27], a[32:35], v[64:67], v[24:27]\ns_waitcnt lgkmcnt(3)\ns_add_u32 m0, s57, 32768\nds_read_b128 v[60:63], v2 offset:10240\nv_mfma_f32_16x16x32_bf16 v[28:31], v[80:83], v[64:67], v[28:31]\n" \
"s_add_u32 s60, s61, 65536\nbuffer_load_dwordx4 v0, %[rs], s60 offen offset:0 sc1 lds\nv_mfma_f32_16x16x32_bf16 v[32:35], a[40:43], v[64:67], v[32:35]\nv_mfma_f32_16x16x32_bf16 v[36:39], a[48:51], v[64:67], v[36:39]\nds_read_b128 v[80:83], v1 offset:3072\nv_mfma_f32_16x16x32_bf16 v[24:27], a[36:39], v[68:71], v[24:27]\ns_waitcnt lgkmcnt(4)\nds_read_b128 v[64:67], v2 offset:12288\n" \
"v_mfma_f32_16x16x32_bf16 v[28:31], v[72:75], v[68:71], v[28:31]\nbuffer_load_dwordx4 v0, %[rs], s60 offen offset:1024 sc1 lds\nv_mfma_f32_16x16x32_bf16 v[32:35], a[44:47], v[68:71], v[32:35]\nv_mfma_f32_16x16x32_bf16 v[36:39], a[52:55], v[68:71], v[36:39]\ns_waitcnt lgkmcnt(4)\nds_read_b128 v[72:75], v1 offset:4096\nv_mfma_f32_16x16x32_bf16 v[24:27], a[56:59], v[56:59], v[24:27]\nds_read_b128 v[68:71], v2 offset:14336\n" \
"v_mfma_f32_16x16x32_bf16 v[28:31], a[64:67], v[56:59], v[28:31]\ns_waitcnt lgkmcnt(5)\nbuffer_load_dwordx4 v0, %[rs], s60 offen offset:2048 sc1 lds\nv_mfma_f32_16x16x32_bf16 v[32:35], v[76:79], v[56:59], v[32:35]\nv_mfma_f32_16x16x32_bf16 v[36:39], a[72:75], v[56:59], v[36:39]\ns_waitcnt lgkmcnt(4)\nds_read_b128 v[76:79], v1 offset:5120\nv_mfma_f32_16x16x32_bf16 v[24:27], a[60:63], v[60:63], v[24:27]\n" \
"s_waitcnt vmcnt(7)\ns_barrier\nds_read_b128 v[56:59], v2 offset:16384\nv_mfma_f32_16x16x32_bf16 v[28:31], a[68:71], v[60:63], v[28:31]\ns_waitcnt lgkmcnt(5)\nbuffer_load_dwordx4 v0, %[rs], s60 offen offset:3072 sc1 lds\nv_mfma_f32_16x16x32_bf16 v[32:35], v[80:83], v[60:63], v[32:35]\nv_mfma_f32_16x16x32_bf16 v[36:39], a[76:79], v[60:63], v[36:39]\n" \
"s_waitcnt lgkmcnt(4)\nv_mfma_f32_16x16x32_bf16 v[24:27], a[80:83], v[64:67], v[24:27]\ns_add_u32 m0, s57, 49152\nds_read_b128 v[60:63], v2 offset:18432\nv_mfma_f32_16x16x32_bf16 v[28:31], a[88:91], v[64:67], v[28:31]\ns_add_u32 s60, s61, 98304\nbuffer_load_dwordx4 v0, %[rs], s60 offen offset:0 sc1 lds\nv_mfma_f32_16x16x32_bf16 v[32:35], a[96:99], v[64:67], v[32:35]\n" \
"s_waitcnt lgkmcnt(4)\nv_mfma_f32_16x16x32_bf16 v[36:39], v[72:75], v[64:67], v[36:39]\ns_waitcnt lgkmcnt(3)\nv_mfma_f32_16x16x32_bf16 v[24:27], a[84:87], v[68:71], v[24:27]\nds_read_b128 v[64:67], v2 offset:20480\nv_mfma_f32_16x16x32_bf16 v[28:31], a[92:95], v[68:71], v[28:31]\nbuffer_load_dwordx4 v0, %[rs], s60 offen offset:1024 sc1 lds\nv_mfma_f32_16x16x32_bf16 v[32:35], a[100:103], v[68:71], v[32:35]\n" \
"s_waitcnt lgkmcnt(3)\nv_mfma_f32_16x16x32_bf16 v[36:39], v[76:79], v[68:71], v[36:39]\ns_waitcnt lgkmcnt(2)\nds_read_b128 v[76:79], v1 offset:6144\nv_mfma_f32_16x16x32_bf16 v[24:27], a[104:107], v[56:59], v[24:27]\nds_read_b128 v[68:71], v2 offset:22528\nv_mfma_f32_16x16x32_bf16 v[28:31], a[112:115], v[56:59], v[28:31]\nbuffer_load_dwordx4 v0, %[rs], s60 offen offset:2048 sc1 lds\n" \
"v_mfma_f32_16x16x32_bf16 v[32:35], a[120:123], v[56:59], v[32:35]\nv_mfma_f32_16x16x32_bf16 v[36:39], a[128:131], v[56:59], v[36:39]\ns_waitcnt lgkmcnt(3)\nds_read_b128 v[80:83], v1 offset:7168\nv_mfma_f32_16x16x32_bf16 v[24:27], a[108:111], v[60:63], v[24:27]\nds_read_b128 v[56:59], v2 offset:24576\nv_mfma_f32_16x16x32_bf16 v[28:31], a[116:119], v[60:63], v[28:31]\nbuffer_load_dwordx4 v0, %[rs], s60 offen offset:3072 sc1 lds\n" \
"v_mfma_f32_16x16x32_bf16 v[32:35], a[124:127], v[60:63], v[32:35]\nv_mfma_f32_16x16x32_bf16 v[36:39], a[132:135], v[60:63], v[36:39]\ns_waitcnt lgkmcnt(4)\nds_read_b128 v[72:75], v1 offset:8192\nv_mfma_f32_16x16x32_bf16 v[24:27], a[136:139], v[64:67], v[24:27]\ns_waitcnt vmcnt(8)\ns_waitcnt lgkmcnt(4)\nds_read_b128 v[60:63], v2 offset:26624\n" \
"v_add_f32 v84, v40, v44\nv_mfma_f32_16x16x32_bf16 v[28:31], v[76:79], v[64:67], v[28:31]\nv_add_f32 v85, v48, v52\nv_add_f32 v84, v84, v85\nv_mfma_f32_16x16x32_bf16 v[32:35], a[144:147], v[64:67], v[32:35]\nv_mov_b32 v85, v84\nv_mfma_f32_16x16x32_bf16 v[36:39], a[152:155], v[64:67], v[36:39]\ns_waitcnt lgkmcnt(4)\n" \
"ds_read_b128 v[76:79], v1 offset:9216\nv_mfma_f32_16x16x32_bf16 v[24:27], a[140:143], v[68:71], v[24:27]\ns_waitcnt lgkmcnt(4)\nds_read_b128 v[64:67], v2 offset:28672\nv_mfma_f32_16x16x32_bf16 v[28:31], v[80:83], v[68:71], v[28:31]\nv_permlane16_swap_b32 v84, v85\nv_mfma_f32_16x16x32_bf16 v[32:35], a[148:151], v[68:71], v[32:35]\nv_add_f32 v84, v84, v85\n" \
"v_mov_b32 v85, v84\nv_mfma_f32_16x16x32_bf16 v[36:39], a[156:159], v[68:71], v[36:39]\ns_waitcnt lgkmcnt(4)\nds_read_b128 v[80:83], v1 offset:10240\nv_mfma_f32_16x16x32_bf16 v[24:27], a[160:163], v[56:59], v[24:27]\nds_read_b128 v[68:71], v2 offset:30720\nv_mfma_f32_16x16x32_bf16 v[28:31], a[168:171], v[56:59], v[28:31]\ns_waitcnt lgkmcnt(5)\n" \
"v_permlane32_swap_b32 v84, v85\nv_mfma_f32_16x16x32_bf16 v[32:35], v[72:75], v[56:59], v[32:35]\nv_add_f32 v84, v84, v85\nv_mul_f32 v84, 0xbd800000, v84\nv_mfma_f32_16x16x32_bf16 v[36:39], a[176:179], v[56:59], v[36:39]\ns_waitcnt lgkmcnt(4)\nds_read_b128 v[72:75], v1 offset:11264\nv_add_f32 v40, v40, v84\n" \
"v_mfma_f32_16x16x32_bf16 v[24:27], a[164:167], v[60:63], v[24:27]\ns_waitcnt vmcnt(4)\ns_barrier\nds_read_b128 v[56:59], v2 offset:32768\nv_mfma_f32_16x16x32_bf16 v[28:31], a[172:175], v[60:63], v[28:31]\ns_waitcnt lgkmcnt(5)\nv_mul_f32 v40, 0x41000000, v40\nv_fma_f32 v41, v40, v40, v41\n" \
"v_mfma_f32_16x16x32_bf16 v[32:35], v[76:79], v[60:63], v[32:35]\nv_add_f32 v44, v44, v84\nv_mul_f32 v44, 0x41000000, v44\nv_mfma_f32_16x16x32_bf16 v[36:39], a[180:183], v[60:63], v[36:39]\ns_waitcnt lgkmcnt(4)\nv_fma_f32 v45, v44, v44, v45\nv_add_f32 v48, v48, v84\nv_mfma_f32_16x16x32_bf16 v[24:27], a[184:187], v[64:67], v[24:27]\n" \
"ds_read_b128 v[60:63], v2 offset:34816\nv_mul_f32 v48, 0x41000000, v48\nv_mfma_f32_16x16x32_bf16 v[28:31], a[192:195], v[64:67], v[28:31]\nv_fma_f32 v49, v48, v48, v49\nv_add_f32 v52, v52, v84\nv_mfma_f32_16x16x32_bf16 v[32:35], a[200:203], v[64:67], v[32:35]\ns_waitcnt lgkmcnt(4)\nv_mul_f32 v52, 0x41000000, v52\n" \
"v_fma_f32 v53, v52, v52, v53\nv_mfma_f32_16x16x32_bf16 v[36:39], v[80:83], v[64:67], v[36:39]\ns_waitcnt lgkmcnt(3)\nv_add_f32 v85, v41, v45\nv_add_f32 v86, v49, v53\nv_mfma_f32_16x16x32_bf16 v[24:27], a[188:191], v[68:71], v[24:27]\nds_read_b128 v[64:67], v2 offset:36864\nv_add_f32 v85, v85, v86\n" \
"v_mfma_f32_16x16x32_bf16 v[28:31], a[196:199], v[68:71], v[28:31]\nv_mov_b32 v86, v85\nv_mfma_f32_16x16x32_bf16 v[32:35], a[204:207], v[68:71], v[32:35]\ns_waitcnt lgkmcnt(3)\nv_permlane16_swap_b32 v85, v86\nv_mfma_f32_16x16x32_bf16 v[36:39], v[72:75], v[68:71], v[36:39]\ns_waitcnt lgkmcnt(2)\nds_read_b128 v[72:75], v1 offset:12288\n" \
"v_add_f32 v85, v85, v86\nv_mfma_f32_16x16x32_bf16 v[24:27], a[208:211], v[56:59], v[24:27]\nds_read_b128 v[68:71], v2 offset:38912\nv_mov_b32 v86, v85\nv_mfma_f32_16x16x32_bf16 v[28:31], a[216:219], v[56:59], v[28:31]\ns_nop 0\nv_permlane32_swap_b32 v85, v86\nv_mfma_f32_16x16x32_bf16 v[32:35], a[224:227], v[56:59], v[32:35]\n" \
"v_add_f32 v85, v85, v86\nv_mul_f32 v85, 0x3a800000, v85\nv_mfma_f32_16x16x32_bf16 v[36:39], a[232:235], v[56:59], v[36:39]\ns_waitcnt lgkmcnt(3)\nds_read_b128 v[76:79], v1 offset:13312\nv_cvt_pk_bf16_f32 v85, v85, v85\nv_mfma_f32_16x16x32_bf16 v[24:27], a[212:215], v[60:63], v[24:27]\nds_read_b128 v[56:59], v2 offset:40960\n" \
"v_and_b32 v85, 0xffff0000, v85\nv_mfma_f32_16x16x32_bf16 v[28:31], a[220:223], v[60:63], v[28:31]\nv_add_f32 v85, 0x3727c5ac, v85\nv_cvt_pk_bf16_f32 v85, v85, v85\nv_mfma_f32_16x16x32_bf16 v[32:35], a[228:231], v[60:63], v[32:35]\nv_and_b32 v85, 0xffff0000, v85\nv_cvt_pk_bf16_f32 v84, v84, v84\nv_mfma_f32_16x16x32_bf16 v[36:39], a[236:239], v[60:63], v[36:39]\n" \
"s_waitcnt lgkmcnt(4)\nds_read_b128 v[80:83], v1 offset:14336\nv_and_b32 v84, 0xffff0000, v84\nv_mfma_f32_16x16x32_bf16 v[24:27], a[240:243], v[64:67], v[24:27]\ns_waitcnt lgkmcnt(4)\nds_read_b128 v[60:63], v2 offset:43008\nv_mfma_f32_16x16x32_bf16 v[28:31], v[72:75], v[64:67], v[28:31]\nv_sqrt_f32 v85, v85\n" \
"v_mfma_f32_16x16x32_bf16 v[32:35], a[248:251], v[64:67], v[32:35]\nv_cvt_pk_bf16_f32 v85, v85, v85\nv_and_b32 v85, 0xffff0000, v85\nv_mfma_f32_16x16x32_bf16 v[36:39], v[96:99], v[64:67], v[36:39]\ns_waitcnt lgkmcnt(4)\nds_read_b128 v[72:75], v1 offset:15360\nv_mul_f32 v86, s58, v85\nv_mfma_f32_16x16x32_bf16 v[24:27], a[244:247], v[68:71], v[24:27]\n" \
"s_waitcnt lgkmcnt(4)\nds_read_b128 v[64:67], v2 offset:45056\nv_add_f32 v87, v42, v46\nv_mfma_f32_16x16x32_bf16 v[28:31], v[76:79], v[68:71], v[28:31]\nv_rcp_f32 v85, v85\nv_mfma_f32_16x16x32_bf16 v[32:35], a[252:255], v[68:71], v[32:35]\nv_mul_f32 v85, s58, v85\nv_add_f32 v88, v50, v54\n" \
"v_mfma_f32_16x16x32_bf16 v[36:39], v[100:103], v[68:71], v[36:39]\ns_waitcnt lgkmcnt(4)\nds_read_b128 v[76:79], v1 offset:16384\nv_add_f32 v87, v87, v88\nv_mfma_f32_16x16x32_bf16 v[24:27], v[104:107], v[56:59], v[24:27]\nds_read_b128 v[68:71], v2 offset:47104\nv_mov_b32 v88, v87\nv_mfma_f32_16x16x32_bf16 v[28:31], v[112:115], v[56:59], v[28:31]\n" \
"s_waitcnt lgkmcnt(5)\nv_permlane16_swap_b32 v87, v88\nv_mfma_f32_16x16x32_bf16 v[32:35], v[80:83], v[56:59], v[32:35]\nv_add_f32 v87, v87, v88\nv_mov_b32 v88, v87\nv_mfma_f32_16x16x32_bf16 v[36:39], v[120:123], v[56:59], v[36:39]\ns_waitcnt lgkmcnt(4)\nds_read_b128 v[80:83], v1 offset:17408\n" \
"v_mfma_f32_16x16x32_bf16 v[24:27], v[108:111], v[60:63], v[24:27]\ns_waitcnt vmcnt(0)\ns_barrier\nds_read_b128 v[56:59], v2 offset:49152\nv_mfma_f32_16x16x32_bf16 v[28:31], v[116:119], v[60:63], v[28:31]\ns_waitcnt lgkmcnt(5)\nv_permlane32_swap_b32 v87, v88\nv_mfma_f32_16x16x32_bf16 v[32:35], v[72:75], v[60:63], v[32:35]\n" \
"v_add_f32 v87, v87, v88\nv_mul_f32 v87, 0xbd800000, v87\nv_mfma_f32_16x16x32_bf16 v[36:39], v[124:127], v[60:63], v[36:39]\ns_waitcnt lgkmcnt(4)\nv_add_f32 v42, v42, v87\nv_mul_f32 v42, 0x41000000, v42\nv_mfma_f32_16x16x32_bf16 v[24:27], v[128:131], v[64:67], v[24:27]\nds_read_b128 v[60:63], v2 offset:51200\n" \
"v_fma_f32 v43, v42, v42, v43\nv_mfma_f32_16x16x32_bf16 v[28:31], v[136:139], v[64:67], v[28:31]\nv_add_f32 v46, v46, v87\nv_mul_f32 v46, 0x41000000, v46\nv_mfma_f32_16x16x32_bf16 v[32:35], v[144:147], v[64:67], v[32:35]\ns_waitcnt lgkmcnt(4)\nv_fma_f32 v47, v46, v46, v47\nv_add_f32 v50, v50, v87\n" \
"v_mfma_f32_16x16x32_bf16 v[36:39], v[76:79], v[64:67], v[36:39]\ns_waitcnt lgkmcnt(3)\nv_mul_f32 v50, 0x41000000, v50\nv_fma_f32 v51, v50, v50, v51\nv_mfma_f32_16x16x32_bf16 v[24:27], v[132:135], v[68:71], v[24:27]\nds_read_b128 v[64:67], v2 offset:53248\nv_add_f32 v54, v54, v87\nv_mfma_f32_16x16x32_bf16 v[28:31], v[140:143], v[68:71], v[28:31]\n" \
"v_mul_f32 v54, 0x41000000, v54\nv_fma_f32 v55, v54, v54, v55\nv_mfma_f32_16x16x32_bf16 v[32:35], v[148:151], v[68:71], v[32:35]\ns_waitcnt lgkmcnt(3)\nv_add_f32 v88, v43, v47\nv_add_f32 v89, v51, v55\nv_mfma_f32_16x16x32_bf16 v[36:39], v[80:83], v[68:71], v[36:39]\ns_waitcnt lgkmcnt(2)\n" \
"ds_read_b128 v[80:83], v1 offset:18432\nv_add_f32 v88, v88, v89\nv_mfma_f32_16x16x32_bf16 v[24:27], v[152:155], v[56:59], v[24:27]\nds_read_b128 v[68:71], v2 offset:55296\nv_mov_b32 v89, v88\nv_mfma_f32_16x16x32_bf16 v[28:31], v[160:163], v[56:59], v[28:31]\ns_nop 0\nv_permlane16_swap_b32 v88, v89\n" \
"v_mfma_f32_16x16x32_bf16 v[32:35], v[168:171], v[56:59], v[32:35]\nv_add_f32 v88, v88, v89\nv_mov_b32 v89, v88\nv_mfma_f32_16x16x32_bf16 v[36:39], v[176:179], v[56:59], v[36:39]\ns_waitcnt lgkmcnt(3)\nds_read_b128 v[72:75], v1 offset:19456\nv_mfma_f32_16x16x32_bf16 v[24:27], v[156:159], v[60:63], v[24:27]\nds_read_b128 v[56:59], v2 offset:57344\n" \
"v_mfma_f32_16x16x32_bf16 v[28:31], v[164:167], v[60:63], v[28:31]\nv_permlane32_swap_b32 v88, v89\nv_mfma_f32_16x16x32_bf16 v[32:35], v[172:175], v[60:63], v[32:35]\nv_add_f32 v88, v88, v89\nv_mul_f32 v88, 0x3a800000, v88\nv_mfma_f32_16x16x32_bf16 v[36:39], v[180:183], v[60:63], v[36:39]\ns_waitcnt lgkmcnt(4)\nds_read_b128 v[76:79], v1 offset:20480\n" \
"v_cvt_pk_bf16_f32 v88, v88, v88\nv_mfma_f32_16x16x32_bf16 v[24:27], v[184:187], v[64:67], v[24:27]\ns_waitcnt lgkmcnt(4)\nds_read_b128 v[60:63], v2 offset:59392\nv_and_b32 v88, 0xffff0000, v88\nv_mfma_f32_16x16x32_bf16 v[28:31], v[80:83], v[64:67], v[28:31]\nv_add_f32 v88, 0x3727c5ac, v88\nv_cvt_pk_bf16_f32 v88, v88, v88\n" \
"v_mfma_f32_16x16x32_bf16 v[32:35], v[192:195], v[64:67], v[32:35]\nv_and_b32 v88, 0xffff0000, v88\nv_cvt_pk_bf16_f32 v87, v87, v87\nv_mfma_f32_16x16x32_bf16 v[36:39], v[200:203], v[64:67], v[36:39]\ns_waitcnt lgkmcnt(4)\nds_read_b128 v[80:83], v1 offset:21504\nv_and_b32 v87, 0xffff0000, v87\nv_mfma_f32_16x16x32_bf16 v[24:27], v[188:191], v[68:71], v[24:27]\n" \
"s_waitcnt lgkmcnt(4)\nds_read_b128 v[64:67], v2 offset:61440\nv_mfma_f32_16x16x32_bf16 v[28:31], v[72:75], v[68:71], v[28:31]\nv_sqrt_f32 v88, v88\nv_mfma_f32_16x16x32_bf16 v[32:35], v[196:199], v[68:71], v[32:35]\nv_cvt_pk_bf16_f32 v88, v88, v88\nv_and_b32 v88, 0xffff0000, v88\nv_mfma_f32_16x16x32_bf16 v[36:39], v[204:207], v[68:71], v[36:39]\n" \
"s_waitcnt lgkmcnt(4)\nds_read_b128 v[72:75], v1 offset:22528\nv_mul_f32 v89, s58, v88\nv_mfma_f32_16x16x32_bf16 v[24:27], v[208:211], v[56:59], v[24:27]\nds_read_b128 v[68:71], v2 offset:63488\nv_mfma_f32_16x16x32_bf16 v[28:31], v[216:219], v[56:59], v[28:31]\ns_waitcnt lgkmcnt(5)\nv_rcp_f32 v88, v88\n" \
"v_mfma_f32_16x16x32_bf16 v[32:35], v[76:79], v[56:59], v[32:35]\nv_mul_f32 v88, s58, v88\nv_mfma_f32_16x16x32_bf16 v[36:39], v[224:227], v[56:59], v[36:39]\ns_waitcnt lgkmcnt(4)\nds_read_b128 v[76:79], v1 offset:23552\nv_mfma_f32_16x16x32_bf16 v[24:27], v[212:215], v[60:63], v[24:27]\nds_read_b128 v[56:59], v2 offset:256\nv_mfma_f32_16x16x32_bf16 v[28:31], v[220:223], v[60:63], v[28:31]\n" \
"s_waitcnt lgkmcnt(5)\nv_mfma_f32_16x16x32_bf16 v[32:35], v[80:83], v[60:63], v[32:35]\nv_mfma_f32_16x16x32_bf16 v[36:39], v[228:231], v[60:63], v[36:39]\ns_waitcnt lgkmcnt(4)\nv_mfma_f32_16x16x32_bf16 v[24:27], v[232:235], v[64:67], v[24:27]\nds_read_b128 v[60:63], v2 offset:2304\nv_mfma_f32_16x16x32_bf16 v[28:31], v[240:243], v[64:67], v[28:31]\nv_mfma_f32_16x16x32_bf16 v[32:35], v[248:251], v[64:67], v[32:35]\n" \
"s_waitcnt lgkmcnt(4)\nv_mfma_f32_16x16x32_bf16 v[36:39], v[72:75], v[64:67], v[36:39]\ns_waitcnt lgkmcnt(3)\nv_mfma_f32_16x16x32_bf16 v[24:27], v[236:239], v[68:71], v[24:27]\nds_read_b128 v[64:67], v2 offset:4352\nv_mfma_f32_16x16x32_bf16 v[28:31], v[244:247], v[68:71], v[28:31]\nv_mfma_f32_16x16x32_bf16 v[32:35], v[252:255], v[68:71], v[32:35]\ns_waitcnt lgkmcnt(3)\n" \
"v_mfma_f32_16x16x32_bf16 v[36:39], v[76:79], v[68:71], v[36:39]\ns_waitcnt lgkmcnt(2)\nds_read_b128 v[76:79], v1 offset:0\nv_mfma_f32_16x16x32_bf16 v[40:43], a[0:3], v[56:59], 0\nds_read_b128 v[68:71], v2 offset:6400\nv_mfma_f32_16x16x32_bf16 v[44:47], a[8:11], v[56:59], 0\ns_lshl_b32 s44, s40, 1\nv_mov_b32 v90, s44\n" \
"v_mfma_f32_16x16x32_bf16 v[48:51], a[16:19], v[56:59], 0\nv_mfma_f32_16x16x32_bf16 v[52:55], a[24:27], v[56:59], 0\ns_waitcnt lgkmcnt(3)\nds_read_b128 v[80:83], v1 offset:1024\nv_fma_f32 v24, v8, v84, v24\nv_mfma_f32_16x16x32_bf16 v[40:43], a[4:7], v[60:63], v[40:43]\nds_read_b128 v[56:59], v2 offset:8448\nv_fma_f32 v25, v9, v84, v25\n" \
"v_mfma_f32_16x16x32_bf16 v[44:47], a[12:15], v[60:63], v[44:47]\nv_fma_f32 v32, v16, v84, v32\nv_fma_f32 v33, v17, v84, v33\nv_mfma_f32_16x16x32_bf16 v[48:51], a[20:23], v[60:63], v[48:51]\nv_mul_f32 v24, v24, v85\nv_mul_f32 v25, v25, v85\nv_mfma_f32_16x16x32_bf16 v[52:55], a[28:31], v[60:63], v[52:55]\ns_waitcnt lgkmcnt(4)\n" \
"ds_read_b128 v[72:75], v1 offset:2048\nv_fma_f32 v91, v24, v24, s59\nv_mfma_f32_16x16x32_bf16 v[40:43], a[32:35], v[64:67], v[40:43]\ns_waitcnt lgkmcnt(4)\nds_read_b128 v[60:63], v2 offset:10496\nv_fma_f32 v92, v25, v25, s59\nv_mfma_f32_16x16x32_bf16 v[44:47], v[76:79], v[64:67], v[44:47]\nv_mul_f32 v91, v91, v24\n" \
"v_mul_f32 v92, v92, v25\nv_mfma_f32_16x16x32_bf16 v[48:51], a[40:43], v[64:67], v[48:51]\nv_exp_f32 v91, v91\nv_mfma_f32_16x16x32_bf16 v[52:55], a[48:51], v[64:67], v[52:55]\ns_waitcnt lgkmcnt(4)\nds_read_b128 v[76:79], v1 offset:3072\nv_mul_f32 v24, v24, v32\nv_mfma_f32_16x16x32_bf16 v[40:43], a[36:39], v[68:71], v[40:43]\n" \
"s_waitcnt lgkmcnt(4)\nds_read_b128 v[64:67], v2 offset:12544\nv_mul_f32 v25, v25, v33\nv_mfma_f32_16x16x32_bf16 v[44:47], v[80:83], v[68:71], v[44:47]\nv_exp_f32 v92, v92\nv_mfma_f32_16x16x32_bf16 v[48:51], a[44:47], v[68:71], v[48:51]\nv_fma_f32 v91, v91, v86, v86\nv_fma_f32 v92, v92, v86, v86\n" \
"v_mfma_f32_16x16x32_bf16 v[52:55], a[52:55], v[68:71], v[52:55]\ns_waitcnt lgkmcnt(4)\nds_read_b128 v[80:83], v1 offset:4096\nv_fma_f32 v26, v10, v84, v26\nv_mfma_f32_16x16x32_bf16 v[40:43], a[56:59], v[56:59], v[40:43]\nds_read_b128 v[68:71], v2 offset:14592\nv_fma_f32 v27, v11, v84, v27\nv_mfma_f32_16x16x32_bf16 v[44:47], a[64:67], v[56:59], v[44:47]\n" \
"s_waitcnt lgkmcnt(5)\nv_rcp_f32 v91, v91\nv_mfma_f32_16x16x32_bf16 v[48:51], v[72:75], v[56:59], v[48:51]\nv_rcp_f32 v92, v92\nv_mfma_f32_16x16x32_bf16 v[52:55], a[72:75], v[56:59], v[52:55]\ns_waitcnt lgkmcnt(4)\nds_read_b128 v[72:75], v1 offset:5120\nv_mul_f32 v24, v24, v91\n" \
"v_mfma_f32_16x16x32_bf16 v[40:43], a[60:63], v[60:63], v[40:43]\nds_read_b128 v[56:59], v2 offset:16640\nv_mul_f32 v25, v25, v92\nv_mfma_f32_16x16x32_bf16 v[44:47], a[68:71], v[60:63], v[44:47]\ns_waitcnt lgkmcnt(5)\nv_cvt_pk_bf16_f32 v32, v24, v25\nv_fma_f32 v34, v18, v84, v34\nv_mfma_f32_16x16x32_bf16 v[48:51], v[76:79], v[60:63], v[48:51]\n" \
"v_fma_f32 v35, v19, v84, v35\nv_mul_f32 v26, v26, v85\nv_mfma_f32_16x16x32_bf16 v[52:55], a[76:79], v[60:63], v[52:55]\ns_waitcnt lgkmcnt(4)\nv_mul_f32 v27, v27, v85\nv_fma_f32 v91, v26, v26, s59\nv_mfma_f32_16x16x32_bf16 v[40:43], a[80:83], v[64:67], v[40:43]\nds_read_b128 v[60:63], v2 offset:18688\n" \
"v_fma_f32 v92, v27, v27, s59\nv_mfma_f32_16x16x32_bf16 v[44:47], a[88:91], v[64:67], v[44:47]\nv_mul_f32 v91, v91, v26\nv_mul_f32 v92, v92, v27\nv_mfma_f32_16x16x32_bf16 v[48:51], a[96:99], v[64:67], v[48:51]\ns_waitcnt lgkmcnt(4)\nv_exp_f32 v91, v91\nv_mfma_f32_16x16x32_bf16 v[52:55], v[80:83], v[64:67], v[52:55]\n" \
"s_waitcnt lgkmcnt(3)\nv_exp_f32 v92, v92\nv_mfma_f32_16x16x32_bf16 v[40:43], a[84:87], v[68:71], v[40:43]\nds_read_b128 v[64:67], v2 offset:20736\nv_mul_f32 v26, v26, v34\nv_mfma_f32_16x16x32_bf16 v[44:47], a[92:95], v[68:71], v[44:47]\nv_mul_f32 v27, v27, v35\nv_fma_f32 v91, v91, v86, v86\n" \
"v_mfma_f32_16x16x32_bf16 v[48:51], a[100:103], v[68:71], v[48:51]\ns_waitcnt lgkmcnt(3)\nv_fma_f32 v92, v92, v86, v86\nv_fma_f32 v28, v12, v84, v28\nv_mfma_f32_16x16x32_bf16 v[52:55], v[72:75], v[68:71], v[52:55]\ns_waitcnt lgkmcnt(2)\nds_read_b128 v[72:75], v1 offset:6144\nv_fma_f32 v29, v13, v84, v29\n" \
"v_mfma_f32_16x16x32_bf16 v[40:43], a[104:107], v[56:59], v[40:43]\nds_read_b128 v[68:71], v2 offset:22784\nv_fma_f32 v36, v20, v84, v36\nv_mfma_f32_16x16x32_bf16 v[44:47], a[112:115], v[56:59], v[44:47]\nv_rcp_f32 v91, v91\nv_mfma_f32_16x16x32_bf16 v[48:51], a[120:123], v[56:59], v[48:51]\nv_rcp_f32 v92, v92\nv_mfma_f32_16x16x32_bf16 v[52:55], a[128:131], v[56:59], v[52:55]\n" \
"s_waitcnt lgkmcnt(3)\nds_read_b128 v[76:79], v1 offset:7168\nv_mul_f32 v26, v26, v91\nv_mfma_f32_16x16x32_bf16 v[40:43], a[108:111], v[60:63], v[40:43]\nds_read_b128 v[56:59], v2 offset:24832\nv_mul_f32 v27, v27, v92\nv_mfma_f32_16x16x32_bf16 v[44:47], a[116:119], v[60:63], v[44:47]\nv_cvt_pk_bf16_f32 v33, v26, v27\n" \
"v_fma_f32 v37, v21, v84, v37\nv_mfma_f32_16x16x32_bf16 v[48:51], a[124:127], v[60:63], v[48:51]\nv_mul_f32 v28, v28, v85\nv_mul_f32 v29, v29, v85\nv_mfma_f32_16x16x32_bf16 v[52:55], a[132:135], v[60:63], v[52:55]\ns_waitcnt lgkmcnt(4)\nds_read_b128 v[80:83], v1 offset:8192\nv_fma_f32 v91, v28, v28, s59\n" \
"v_mfma_f32_16x16x32_bf16 v[40:43], a[136:139], v[64:67], v[40:43]\ns_waitcnt lgkmcnt(4)\nds_read_b128 v[60:63], v2 offset:26880\nv_fma_f32 v92, v29, v29, s59\nv_mfma_f32_16x16x32_bf16 v[44:47], v[72:75], v[64:67], v[44:47]\nv_mul_f32 v91, v91, v28\nv_mul_f32 v92, v92, v29\nv_mfma_f32_16x16x32_bf16 v[48:51], a[144:147], v[64:67], v[48:51]\n" \
"v_exp_f32 v91, v91\nv_mfma_f32_16x16x32_bf16 v[52:55], a[152:155], v[64:67], v[52:55]\ns_waitcnt lgkmcnt(4)\nds_read_b128 v[72:75], v1 offset:9216\nv_mul_f32 v28, v28, v36\nv_mfma_f32_16x16x32_bf16 v[40:43], a[140:143], v[68:71], v[40:43]\ns_waitcnt lgkmcnt(4)\nds_read_b128 v[64:67], v2 offset:28928\n" \
"v_mul_f32 v29, v29, v37\nv_mfma_f32_16x16x32_bf16 v[44:47], v[76:79], v[68:71], v[44:47]\nv_exp_f32 v92, v92\nv_mfma_f32_16x16x32_bf16 v[48:51], a[148:151], v[68:71], v[48:51]\nv_fma_f32 v91, v91, v86, v86\nv_fma_f32 v92, v92, v86, v86\nv_mfma_f32_16x16x32_bf16 v[52:55], a[156:159], v[68:71], v[52:55]\ns_waitcnt lgkmcnt(4)\n" \
"ds_read_b128 v[76:79], v1 offset:10240\nv_fma_f32 v30, v14, v84, v30\nv_mfma_f32_16x16x32_bf16 v[40:43], a[160:163], v[56:59], v[40:43]\nds_read_b128 v[68:71], v2 offset:30976\nv_fma_f32 v31, v15, v84, v31\nv_mfma_f32_16x16x32_bf16 v[44:47], a[168:171], v[56:59], v[44:47]\ns_waitcnt lgkmcnt(5)\nv_rcp_f32 v91, v91\n" \
"v_mfma_f32_16x16x32_bf16 v[48:51], v[80:83], v[56:59], v[48:51]\nv_rcp_f32 v92, v92\nv_mfma_f32_16x16x32_bf16 v[52:55], a[176:179], v[56:59], v[52:55]\ns_waitcnt lgkmcnt(4)\nds_read_b128 v[80:83], v1 offset:11264\nv_mul_f32 v28, v28, v91\nv_mfma_f32_16x16x32_bf16 v[40:43], a[164:167], v[60:63], v[40:43]\nds_read_b128 v[56:59], v2 offset:33024\n" \
"v_mul_f32 v29, v29, v92\nv_mfma_f32_16x16x32_bf16 v[44:47], a[172:175], v[60:63], v[44:47]\ns_waitcnt lgkmcnt(5)\nv_cvt_pk_bf16_f32 v34, v28, v29\nv_fma_f32 v38, v22, v84, v38\nv_mfma_f32_16x16x32_bf16 v[48:51], v[72:75], v[60:63], v[48:51]\nv_fma_f32 v39, v23, v84, v39\nv_mul_f32 v30, v30, v85\n" \
"v_mfma_f32_16x16x32_bf16 v[52:55], a[180:183], v[60:63], v[52:55]\ns_waitcnt lgkmcnt(4)\nv_mul_f32 v31, v31, v85\nv_fma_f32 v91, v30, v30, s59\nv_mfma_f32_16x16x32_bf16 v[40:43], a[184:187], v[64:67], v[40:43]\nds_read_b128 v[60:63], v2 offset:35072\nv_fma_f32 v92, v31, v31, s59\nv_mfma_f32_16x16x32_bf16 v[44:47], a[192:195], v[64:67], v[44:47]\n" \
"v_mul_f32 v91, v91, v30\nv_mul_f32 v92, v92, v31\nv_mfma_f32_16x16x32_bf16 v[48:51], a[200:203], v[64:67], v[48:51]\ns_waitcnt lgkmcnt(4)\nv_exp_f32 v91, v91\nv_mfma_f32_16x16x32_bf16 v[52:55], v[76:79], v[64:67], v[52:55]\ns_waitcnt lgkmcnt(3)\nv_exp_f32 v92, v92\n" \
"v_mfma_f32_16x16x32_bf16 v[40:43], a[188:191], v[68:71], v[40:43]\nds_read_b128 v[64:67], v2 offset:37120\nv_mul_f32 v30, v30, v38\nv_mfma_f32_16x16x32_bf16 v[44:47], a[196:199], v[68:71], v[44:47]\nv_mul_f32 v31, v31, v39\nv_fma_f32 v91, v91, v86, v86\nv_mfma_f32_16x16x32_bf16 v[48:51], a[204:207], v[68:71], v[48:51]\ns_waitcnt lgkmcnt(3)\n" \
"v_fma_f32 v92, v92, v86, v86\nv_mfma_f32_16x16x32_bf16 v[52:55], v[80:83], v[68:71], v[52:55]\ns_waitcnt lgkmcnt(2)\nds_read_b128 v[80:83], v1 offset:12288\nv_mfma_f32_16x16x32_bf16 v[40:43], a[208:211], v[56:59], v[40:43]\nds_read_b128 v[68:71], v2 offset:39168\nv_mfma_f32_16x16x32_bf16 v[44:47], a[216:219], v[56:59], v[44:47]\nv_rcp_f32 v91, v91\n" \
"v_mfma_f32_16x16x32_bf16 v[48:51], a[224:227], v[56:59], v[48:51]\nv_rcp_f32 v92, v92\nv_mfma_f32_16x16x32_bf16 v[52:55], a[232:235], v[56:59], v[52:55]\ns_waitcnt lgkmcnt(3)\nds_read_b128 v[72:75], v1 offset:13312\nv_mul_f32 v30, v30, v91\nv_mfma_f32_16x16x32_bf16 v[40:43], a[212:215], v[60:63], v[40:43]\nds_read_b128 v[56:59], v2 offset:41216\n" \
"v_mul_f32 v31, v31, v92\nv_mfma_f32_16x16x32_bf16 v[44:47], a[220:223], v[60:63], v[44:47]\nv_cvt_pk_bf16_f32 v35, v30, v31\nv_mfma_f32_16x16x32_bf16 v[48:51], a[228:231], v[60:63], v[48:51]\nbuffer_store_dwordx4 v[32:35], v4, %[rs], s55 offen offset:0 sc1\nv_mfma_f32_16x16x32_bf16 v[52:55], a[236:239], v[60:63], v[52:55]\ns_waitcnt lgkmcnt(4)\nds_read_b128 v[76:79], v1 offset:14336\n" \
"v_mfma_f32_16x16x32_bf16 v[40:43], a[240:243], v[64:67], v[40:43]\ns_waitcnt lgkmcnt(4)\nds_read_b128 v[60:63], v2 offset:43264\nv_mfma_f32_16x16x32_bf16 v[44:47], v[80:83], v[64:67], v[44:47]\nv_mfma_f32_16x16x32_bf16 v[48:51], a[248:251], v[64:67], v[48:51]\nv_mfma_f32_16x16x32_bf16 v[52:55], v[96:99], v[64:67], v[52:55]\ns_waitcnt lgkmcnt(4)\nds_read_b128 v[80:83], v1 offset:15360\n" \
"v_mfma_f32_16x16x32_bf16 v[40:43], a[244:247], v[68:71], v[40:43]\ns_waitcnt lgkmcnt(4)\nds_read_b128 v[64:67], v2 offset:45312\nv_mfma_f32_16x16x32_bf16 v[44:47], v[72:75], v[68:71], v[44:47]\nv_mfma_f32_16x16x32_bf16 v[48:51], a[252:255], v[68:71], v[48:51]\nv_mfma_f32_16x16x32_bf16 v[52:55], v[100:103], v[68:71], v[52:55]\ns_waitcnt lgkmcnt(4)\nds_read_b128 v[72:75], v1 offset:16384\n" \
"v_mfma_f32_16x16x32_bf16 v[40:43], v[104:107], v[56:59], v[40:43]\nds_read_b128 v[68:71], v2 offset:47360\nv_mfma_f32_16x16x32_bf16 v[44:47], v[112:115], v[56:59], v[44:47]\ns_waitcnt lgkmcnt(5)\nv_mfma_f32_16x16x32_bf16 v[48:51], v[76:79], v[56:59], v[48:51]\nv_mfma_f32_16x16x32_bf16 v[52:55], v[120:123], v[56:59], v[52:55]\ns_waitcnt lgkmcnt(4)\nds_read_b128 v[76:79], v1 offset:17408\n" \
"v_mfma_f32_16x16x32_bf16 v[40:43], v[108:111], v[60:63], v[40:43]\nds_read_b128 v[56:59], v2 offset:49408\nv_mfma_f32_16x16x32_bf16 v[44:47], v[116:119], v[60:63], v[44:47]\ns_waitcnt lgkmcnt(5)\nv_mfma_f32_16x16x32_bf16 v[48:51], v[80:83], v[60:63], v[48:51]\nv_mfma_f32_16x16x32_bf16 v[52:55], v[124:127], v[60:63], v[52:55]\ns_waitcnt lgkmcnt(4)\nv_mfma_f32_16x16x32_bf16 v[40:43], v[128:131], v[64:67], v[40:43]\n" \
"ds_read_b128 v[60:63], v2 offset:51456\nv_mfma_f32_16x16x32_bf16 v[44:47], v[136:139], v[64:67], v[44:47]\nv_mfma_f32_16x16x32_bf16 v[48:51], v[144:147], v[64:67], v[48:51]\ns_waitcnt lgkmcnt(4)\nv_mfma_f32_16x16x32_bf16 v[52:55], v[72:75], v[64:67], v[52:55]\ns_waitcnt lgkmcnt(3)\nv_mfma_f32_16x16x32_bf16 v[40:43], v[132:135], v[68:71], v[40:43]\nds_read_b128 v[64:67], v2 offset:53504\n" \
"v_mfma_f32_16x16x32_bf16 v[44:47], v[140:143], v[68:71], v[44:47]\nv_mfma_f32_16x16x32_bf16 v[48:51], v[148:151], v[68:71], v[48:51]\ns_waitcnt lgkmcnt(3)\nv_mfma_f32_16x16x32_bf16 v[52:55], v[76:79], v[68:71], v[52:55]\ns_waitcnt lgkmcnt(2)\nds_read_b128 v[76:79], v1 offset:18432\nv_mfma_f32_16x16x32_bf16 v[40:43], v[152:155], v[56:59], v[40:43]\nds_read_b128 v[68:71], v2 offset:55552\n" \
"v_mfma_f32_16x16x32_bf16 v[44:47], v[160:163], v[56:59], v[44:47]\nv_mfma_f32_16x16x32_bf16 v[48:51], v[168:171], v[56:59], v[48:51]\nv_mfma_f32_16x16x32_bf16 v[52:55], v[176:179], v[56:59], v[52:55]\ns_waitcnt lgkmcnt(3)\nds_read_b128 v[80:83], v1 offset:19456\nv_mfma_f32_16x16x32_bf16 v[40:43], v[156:159], v[60:63], v[40:43]\nds_read_b128 v[56:59], v2 offset:57600\nv_mfma_f32_16x16x32_bf16 v[44:47], v[164:167], v[60:63], v[44:47]\n" \
"v_mfma_f32_16x16x32_bf16 v[48:51], v[172:175], v[60:63], v[48:51]\nv_mfma_f32_16x16x32_bf16 v[52:55], v[180:183], v[60:63], v[52:55]\ns_waitcnt lgkmcnt(4)\nds_read_b128 v[72:75], v1 offset:20480\nv_mfma_f32_16x16x32_bf16 v[40:43], v[184:187], v[64:67], v[40:43]\ns_waitcnt lgkmcnt(4)\nds_read_b128 v[60:63], v2 offset:59648\nv_mfma_f32_16x16x32_bf16 v[44:47], v[76:79], v[64:67], v[44:47]\n" \
"v_mfma_f32_16x16x32_bf16 v[48:51], v[192:195], v[64:67], v[48:51]\nv_mfma_f32_16x16x32_bf16 v[52:55], v[200:203], v[64:67], v[52:55]\ns_waitcnt lgkmcnt(4)\nds_read_b128 v[76:79], v1 offset:21504\nv_mfma_f32_16x16x32_bf16 v[40:43], v[188:191], v[68:71], v[40:43]\ns_waitcnt lgkmcnt(4)\nds_read_b128 v[64:67], v2 offset:61696\nv_mfma_f32_16x16x32_bf16 v[44:47], v[80:83], v[68:71], v[44:47]\n" \
"v_mfma_f32_16x16x32_bf16 v[48:51], v[196:199], v[68:71], v[48:51]\nv_mfma_f32_16x16x32_bf16 v[52:55], v[204:207], v[68:71], v[52:55]\ns_waitcnt lgkmcnt(4)\nds_read_b128 v[80:83], v1 offset:22528\nv_mfma_f32_16x16x32_bf16 v[40:43], v[208:211], v[56:59], v[40:43]\nds_read_b128 v[68:71], v2 offset:63744\nv_mfma_f32_16x16x32_bf16 v[44:47], v[216:219], v[56:59], v[44:47]\ns_waitcnt lgkmcnt(5)\n" \
"v_mfma_f32_16x16x32_bf16 v[48:51], v[72:75], v[56:59], v[48:51]\nv_mfma_f32_16x16x32_bf16 v[52:55], v[224:227], v[56:59], v[52:55]\ns_waitcnt lgkmcnt(4)\nds_read_b128 v[72:75], v1 offset:23552\nv_mfma_f32_16x16x32_bf16 v[40:43], v[212:215], v[60:63], v[40:43]\nv_mfma_f32_16x16x32_bf16 v[44:47], v[220:223], v[60:63], v[44:47]\ns_waitcnt lgkmcnt(4)\nv_mfma_f32_16x16x32_bf16 v[48:51], v[76:79], v[60:63], v[48:51]\n" \
"v_mfma_f32_16x16x32_bf16 v[52:55], v[228:231], v[60:63], v[52:55]\ns_waitcnt lgkmcnt(3)\nv_mfma_f32_16x16x32_bf16 v[40:43], v[232:235], v[64:67], v[40:43]\nv_mfma_f32_16x16x32_bf16 v[44:47], v[240:243], v[64:67], v[44:47]\nv_mfma_f32_16x16x32_bf16 v[48:51], v[248:251], v[64:67], v[48:51]\ns_waitcnt lgkmcnt(2)\nv_mfma_f32_16x16x32_bf16 v[52:55], v[80:83], v[64:67], v[52:55]\ns_waitcnt lgkmcnt(1)\n" \
"v_mfma_f32_16x16x32_bf16 v[40:43], v[236:239], v[68:71], v[40:43]\nv_mfma_f32_16x16x32_bf16 v[44:47], v[244:247], v[68:71], v[44:47]\nv_mfma_f32_16x16x32_bf16 v[48:51], v[252:255], v[68:71], v[48:51]\ns_waitcnt lgkmcnt(0)\nv_mfma_f32_16x16x32_bf16 v[52:55], v[72:75], v[68:71], v[52:55]\nv_fma_f32 v40, v8, v87, v40\nv_fma_f32 v41, v9, v87, v41\ns_nop 1\n" \
"v_fma_f32 v48, v16, v87, v48\nv_fma_f32 v49, v17, v87, v49\nv_mul_f32 v40, v40, v88\nv_mul_f32 v41, v41, v88\nv_fma_f32 v91, v40, v40, s59\nv_fma_f32 v92, v41, v41, s59\nv_mul_f32 v91, v91, v40\nv_mul_f32 v92, v92, v41\n" \
"v_exp_f32 v91, v91\nv_exp_f32 v92, v92\nv_mul_f32 v40, v40, v48\nv_mul_f32 v41, v41, v49\nv_fma_f32 v91, v91, v89, v89\nv_fma_f32 v92, v92, v89, v89\nv_rcp_f32 v91, v91\nv_rcp_f32 v92, v92\n" \
"v_mul_f32 v40, v40, v91\nv_mul_f32 v41, v41, v92\nv_cvt_pk_bf16_f32 v48, v40, v41\nv_fma_f32 v42, v10, v87, v42\nv_fma_f32 v43, v11, v87, v43\nv_fma_f32 v50, v18, v87, v50\nv_fma_f32 v51, v19, v87, v51\nv_mul_f32 v42, v42, v88\n" \
"v_mul_f32 v43, v43, v88\nv_fma_f32 v91, v42, v42, s59\nv_fma_f32 v92, v43, v43, s59\nv_mul_f32 v91, v91, v42\nv_mul_f32 v92, v92, v43\nv_exp_f32 v91, v91\nv_exp_f32 v92, v92\nv_mul_f32 v42, v42, v50\n" \
"v_mul_f32 v43, v43, v51\nv_fma_f32 v91, v91, v89, v89\nv_fma_f32 v92, v92, v89, v89\nv_rcp_f32 v91, v91\nv_rcp_f32 v92, v92\nv_mul_f32 v42, v42, v91\nv_mul_f32 v43, v43, v92\nv_cvt_pk_bf16_f32 v49, v42, v43\n" \
"v_fma_f32 v44, v12, v87, v44\nv_fma_f32 v45, v13, v87, v45\nv_fma_f32 v52, v20, v87, v52\nv_fma_f32 v53, v21, v87, v53\nv_mul_f32 v44, v44, v88\nv_mul_f32 v45, v45, v88\nv_fma_f32 v91, v44, v44, s59\nv_fma_f32 v92, v45, v45, s59\n" \
"v_mul_f32 v91, v91, v44\nv_mul_f32 v92, v92, v45\nv_exp_f32 v91, v91\nv_exp_f32 v92, v92\nv_mul_f32 v44, v44, v52\nv_mul_f32 v45, v45, v53\nv_fma_f32 v91, v91, v89, v89\nv_fma_f32 v92, v92, v89, v89\n" \
"v_rcp_f32 v91, v91\nv_rcp_f32 v92, v92\nv_mul_f32 v44, v44, v91\nv_mul_f32 v45, v45, v92\nv_cvt_pk_bf16_f32 v50, v44, v45\nv_fma_f32 v46, v14, v87, v46\nv_fma_f32 v47, v15, v87, v47\nv_fma_f32 v54, v22, v87, v54\n" \
"v_fma_f32 v55, v23, v87, v55\nv_mul_f32 v46, v46, v88\nv_mul_f32 v47, v47, v88\nv_fma_f32 v91, v46, v46, s59\nv_fma_f32 v92, v47, v47, s59\nv_mul_f32 v91, v91, v46\nv_mul_f32 v92, v92, v47\nv_exp_f32 v91, v91\n" \
"v_exp_f32 v92, v92\nv_mul_f32 v46, v46, v54\nv_mul_f32 v47, v47, v55\nv_fma_f32 v91, v91, v89, v89\nv_fma_f32 v92, v92, v89, v89\nv_rcp_f32 v91, v91\nv_rcp_f32 v92, v92\nv_mul_f32 v46, v46, v91\n" \
"v_mul_f32 v47, v47, v92\nv_cvt_pk_bf16_f32 v51, v46, v47\nbuffer_store_dwordx4 v[48:51], v4, %[rs], s55 offen offset:1024 sc1\nv_add_u32 v90, 1, v90\ns_mov_b32 s56, %[fh]\ns_add_u32 s40, s40, 1\ns_cmp_lt_u32 s40, 64\ns_cbranch_scc1 .Lgg_next%=\n" \
"s_waitcnt vmcnt(0)\ns_barrier\nbuffer_store_dword v90, off, %[rs], s56 sc1\ns_branch .Lgg_end%=\n.Lgg_abort%=:\ns_mov_b64 exec, -1\nv_mov_b32 v16, 0\ns_movk_i32 s44, 6400\n" \
"buffer_store_dword v16, off, %[rs], s44 sc1\n.Lgg_end%=:\ns_waitcnt vmcnt(0) lgkmcnt(0)\n" \
""
#define DD_ASM \
"v_mbcnt_lo_u32_b32 v255, -1, 0\nv_mbcnt_hi_u32_b32 v255, -1, v255\nv_lshlrev_b32 v255, 2, v255\nv_add_u32 v255, %[tb], v255\nds_read_b32 v0, v255 offset:0\nds_read_b32 v1, v255 offset:256\nds_read_b32 v2, v255 offset:512\nds_read_b32 v3, v255 offset:768\n" \
"ds_read_b32 v4, v255 offset:1024\nds_read_b32 v5, v255 offset:1280\nds_read_b32 v6, v255 offset:1536\nds_read_b32 v7, v255 offset:1792\nds_read_b32 v125, v255 offset:2048\nds_read_b32 v126, v255 offset:2304\nds_read_b32 v127, v255 offset:2560\ns_waitcnt lgkmcnt(0)\n" \
"s_add_u32 m0, %[tb], 8192\ns_add_u32 s44, %[wo], 0\nbuffer_load_dwordx4 v125, %[rw], s44 offen nt lds\ns_add_u32 m0, %[tb], 9216\ns_add_u32 s44, %[wo], 16384\nbuffer_load_dwordx4 v125, %[rw], s44 offen nt lds\ns_add_u32 m0, %[tb], 10240\ns_add_u32 s44, %[wo], 32768\n" \
"buffer_load_dwordx4 v125, %[rw], s44 offen nt lds\ns_add_u32 m0, %[tb], 11264\ns_add_u32 s44, %[wo], 49152\nbuffer_load_dwordx4 v125, %[rw], s44 offen nt lds\ns_add_u32 m0, %[lw], 0\ns_add_u32 s44, %[wo], 65536\nbuffer_load_dwordx4 v125, %[rw], s44 offen nt lds\ns_add_u32 m0, %[lw], 1024\n" \
"s_add_u32 s44, %[wo], 81920\nbuffer_load_dwordx4 v125, %[rw], s44 offen nt lds\ns_add_u32 m0, %[lw], 2048\ns_add_u32 s44, %[wo], 98304\nbuffer_load_dwordx4 v125, %[rw], s44 offen nt lds\ns_add_u32 m0, %[lw], 3072\ns_add_u32 s44, %[wo], 114688\nbuffer_load_dwordx4 v125, %[rw], s44 offen nt lds\n" \
"s_add_u32 m0, %[lw], 4096\ns_add_u32 s44, %[wo], 131072\nbuffer_load_dwordx4 v125, %[rw], s44 offen nt lds\ns_add_u32 m0, %[lw], 5120\ns_add_u32 s44, %[wo], 147456\nbuffer_load_dwordx4 v125, %[rw], s44 offen nt lds\ns_add_u32 m0, %[lw], 6144\ns_add_u32 s44, %[wo], 163840\n" \
"buffer_load_dwordx4 v125, %[rw], s44 offen nt lds\ns_add_u32 m0, %[lw], 7168\ns_add_u32 s44, %[wo], 180224\nbuffer_load_dwordx4 v125, %[rw], s44 offen nt lds\ns_add_u32 m0, %[lw], 8192\ns_add_u32 s44, %[wo], 196608\nbuffer_load_dwordx4 v125, %[rw], s44 offen nt lds\ns_add_u32 m0, %[lw], 9216\n" \
"s_add_u32 s44, %[wo], 212992\nbuffer_load_dwordx4 v125, %[rw], s44 offen nt lds\ns_add_u32 m0, %[lw], 10240\ns_add_u32 s44, %[wo], 229376\nbuffer_load_dwordx4 v125, %[rw], s44 offen nt lds\ns_add_u32 m0, %[lw], 11264\ns_add_u32 s44, %[wo], 245760\nbuffer_load_dwordx4 v125, %[rw], s44 offen nt lds\n" \
"s_add_u32 m0, %[lw], 12288\ns_add_u32 s44, %[wo], 262144\nbuffer_load_dwordx4 v125, %[rw], s44 offen nt lds\ns_add_u32 m0, %[lw], 13312\ns_add_u32 s44, %[wo], 278528\nbuffer_load_dwordx4 v125, %[rw], s44 offen nt lds\ns_add_u32 m0, %[lw], 14336\ns_add_u32 s44, %[wo], 294912\n" \
"buffer_load_dwordx4 v125, %[rw], s44 offen nt lds\ns_add_u32 m0, %[lw], 15360\ns_add_u32 s44, %[wo], 311296\nbuffer_load_dwordx4 v125, %[rw], s44 offen nt lds\ns_add_u32 m0, %[lw], 16384\ns_add_u32 s44, %[wo], 327680\nbuffer_load_dwordx4 v125, %[rw], s44 offen nt lds\ns_add_u32 m0, %[lw], 17408\n" \
"s_add_u32 s44, %[wo], 344064\nbuffer_load_dwordx4 v125, %[rw], s44 offen nt lds\ns_add_u32 m0, %[lw], 18432\ns_add_u32 s44, %[wo], 360448\nbuffer_load_dwordx4 v125, %[rw], s44 offen nt lds\ns_add_u32 m0, %[lw], 19456\ns_add_u32 s44, %[wo], 376832\nbuffer_load_dwordx4 v125, %[rw], s44 offen nt lds\n" \
"s_add_u32 m0, %[lw], 20480\ns_add_u32 s44, %[wo], 393216\nbuffer_load_dwordx4 v125, %[rw], s44 offen nt lds\ns_add_u32 m0, %[lw], 21504\ns_add_u32 s44, %[wo], 409600\nbuffer_load_dwordx4 v125, %[rw], s44 offen nt lds\ns_add_u32 m0, %[lw], 22528\ns_add_u32 s44, %[wo], 425984\n" \
"buffer_load_dwordx4 v125, %[rw], s44 offen nt lds\ns_add_u32 m0, %[lw], 23552\ns_add_u32 s44, %[wo], 442368\nbuffer_load_dwordx4 v125, %[rw], s44 offen nt lds\ns_add_u32 m0, %[tb], 0\ns_add_u32 s44, %[wo], 458752\nbuffer_load_dwordx4 v125, %[rw], s44 offen nt lds\ns_add_u32 m0, %[tb], 1024\n" \
"s_add_u32 s44, %[wo], 475136\nbuffer_load_dwordx4 v125, %[rw], s44 offen nt lds\ns_add_u32 m0, %[tb], 2048\ns_add_u32 s44, %[wo], 491520\nbuffer_load_dwordx4 v125, %[rw], s44 offen nt lds\ns_add_u32 m0, %[tb], 3072\ns_add_u32 s44, %[wo], 507904\nbuffer_load_dwordx4 v125, %[rw], s44 offen nt lds\n" \
"s_add_u32 m0, %[tb], 4096\ns_add_u32 s44, %[wo], 524288\nbuffer_load_dwordx4 v125, %[rw], s44 offen nt lds\ns_add_u32 m0, %[tb], 5120\ns_add_u32 s44, %[wo], 540672\nbuffer_load_dwordx4 v125, %[rw], s44 offen nt lds\ns_add_u32 m0, %[tb], 6144\ns_add_u32 s44, %[wo], 557056\n" \
"buffer_load_dwordx4 v125, %[rw], s44 offen nt lds\ns_add_u32 m0, %[tb], 7168\ns_add_u32 s44, %[wo], 573440\nbuffer_load_dwordx4 v125, %[rw], s44 offen nt lds\ns_waitcnt vmcnt(32)\nds_read_b64_tr_b16 a[0:1], v126 offset:8192\nds_read_b64_tr_b16 a[2:3], v126 offset:8704\nds_read_b64_tr_b16 a[8:9], v126 offset:8224\n" \
"ds_read_b64_tr_b16 a[10:11], v126 offset:8736\nds_read_b64_tr_b16 a[16:17], v126 offset:8256\nds_read_b64_tr_b16 a[18:19], v126 offset:8768\nds_read_b64_tr_b16 a[24:25], v126 offset:8288\nds_read_b64_tr_b16 a[26:27], v126 offset:8800\ns_waitcnt lgkmcnt(0)\ns_add_u32 m0, %[tb], 8192\ns_add_u32 s44, %[wo], 589824\n" \
"buffer_load_dwordx4 v125, %[rw], s44 offen nt lds\ns_add_u32 m0, %[tb], 9216\ns_add_u32 s44, %[wo], 606208\nbuffer_load_dwordx4 v125, %[rw], s44 offen nt lds\ns_add_u32 m0, %[tb], 10240\ns_add_u32 s44, %[wo], 622592\nbuffer_load_dwordx4 v125, %[rw], s44 offen nt lds\ns_add_u32 m0, %[tb], 11264\n" \
"s_add_u32 s44, %[wo], 638976\nbuffer_load_dwordx4 v125, %[rw], s44 offen nt lds\ns_waitcnt vmcnt(32)\nds_read_b64_tr_b16 a[4:5], v127 offset:0\nds_read_b64_tr_b16 a[6:7], v127 offset:512\nds_read_b64_tr_b16 a[12:13], v127 offset:32\nds_read_b64_tr_b16 a[14:15], v127 offset:544\nds_read_b64_tr_b16 a[20:21], v127 offset:64\n" \
"ds_read_b64_tr_b16 a[22:23], v127 offset:576\nds_read_b64_tr_b16 a[28:29], v127 offset:96\nds_read_b64_tr_b16 a[30:31], v127 offset:608\ns_waitcnt lgkmcnt(0)\ns_add_u32 m0, %[lw], 0\ns_add_u32 s44, %[wo], 655360\nbuffer_load_dwordx4 v125, %[rw], s44 offen nt lds\ns_add_u32 m0, %[lw], 1024\n" \
"s_add_u32 s44, %[wo], 671744\nbuffer_load_dwordx4 v125, %[rw], s44 offen nt lds\ns_add_u32 m0, %[lw], 2048\ns_add_u32 s44, %[wo], 688128\nbuffer_load_dwordx4 v125, %[rw], s44 offen nt lds\ns_add_u32 m0, %[lw], 3072\ns_add_u32 s44, %[wo], 704512\nbuffer_load_dwordx4 v125, %[rw], s44 offen nt lds\n" \
"s_waitcnt vmcnt(32)\nds_read_b64_tr_b16 a[32:33], v127 offset:4096\nds_read_b64_tr_b16 a[34:35], v127 offset:4608\nds_read_b64_tr_b16 v[92:93], v127 offset:4128\nds_read_b64_tr_b16 v[94:95], v127 offset:4640\nds_read_b64_tr_b16 a[40:41], v127 offset:4160\nds_read_b64_tr_b16 a[42:43], v127 offset:4672\nds_read_b64_tr_b16 a[48:49], v127 offset:4192\n" \
"ds_read_b64_tr_b16 a[50:51], v127 offset:4704\ns_waitcnt lgkmcnt(0)\ns_add_u32 m0, %[lw], 4096\ns_add_u32 s44, %[wo], 720896\nbuffer_load_dwordx4 v125, %[rw], s44 offen nt lds\ns_add_u32 m0, %[lw], 5120\ns_add_u32 s44, %[wo], 737280\nbuffer_load_dwordx4 v125, %[rw], s44 offen nt lds\n" \
"s_add_u32 m0, %[lw], 6144\ns_add_u32 s44, %[wo], 753664\nbuffer_load_dwordx4 v125, %[rw], s44 offen nt lds\ns_add_u32 m0, %[lw], 7168\ns_add_u32 s44, %[wo], 770048\nbuffer_load_dwordx4 v125, %[rw], s44 offen nt lds\ns_waitcnt vmcnt(32)\nds_read_b64_tr_b16 a[36:37], v127 offset:8192\n" \
"ds_read_b64_tr_b16 a[38:39], v127 offset:8704\nds_read_b64_tr_b16 v[88:89], v127 offset:8224\nds_read_b64_tr_b16 v[90:91], v127 offset:8736\nds_read_b64_tr_b16 a[44:45], v127 offset:8256\nds_read_b64_tr_b16 a[46:47], v127 offset:8768\nds_read_b64_tr_b16 a[52:53], v127 offset:8288\nds_read_b64_tr_b16 a[54:55], v127 offset:8800\ns_waitcnt lgkmcnt(0)\n" \
"s_add_u32 m0, %[lw], 8192\ns_add_u32 s44, %[wo], 786432\nbuffer_load_dwordx4 v125, %[rw], s44 offen nt lds\ns_add_u32 m0, %[lw], 9216\ns_add_u32 s44, %[wo], 802816\nbuffer_load_dwordx4 v125, %[rw], s44 offen nt lds\ns_add_u32 m0, %[lw], 10240\ns_add_u32 s44, %[wo], 819200\n" \
"buffer_load_dwordx4 v125, %[rw], s44 offen nt lds\ns_add_u32 m0, %[lw], 11264\ns_add_u32 s44, %[wo], 835584\nbuffer_load_dwordx4 v125, %[rw], s44 offen nt lds\ns_waitcnt vmcnt(32)\nds_read_b64_tr_b16 a[56:57], v127 offset:12288\nds_read_b64_tr_b16 a[58:59], v127 offset:12800\nds_read_b64_tr_b16 a[64:65], v127 offset:12320\n" \
"ds_read_b64_tr_b16 a[66:67], v127 offset:12832\nds_read_b64_tr_b16 a[72:73], v127 offset:12352\nds_read_b64_tr_b16 a[74:75], v127 offset:12864\nds_read_b64_tr_b16 a[80:81], v127 offset:12384\nds_read_b64_tr_b16 a[82:83], v127 offset:12896\ns_waitcnt lgkmcnt(0)\ns_add_u32 m0, %[lw], 12288\ns_add_u32 s44, %[wo], 851968\n" \
"buffer_load_dwordx4 v125, %[rw], s44 offen nt lds\ns_add_u32 m0, %[lw], 13312\ns_add_u32 s44, %[wo], 868352\nbuffer_load_dwordx4 v125, %[rw], s44 offen nt lds\ns_add_u32 m0, %[lw], 14336\ns_add_u32 s44, %[wo], 884736\nbuffer_load_dwordx4 v125, %[rw], s44 offen nt lds\ns_add_u32 m0, %[lw], 15360\n" \
"s_add_u32 s44, %[wo], 901120\nbuffer_load_dwordx4 v125, %[rw], s44 offen nt lds\ns_waitcnt vmcnt(32)\nds_read_b64_tr_b16 a[60:61], v127 offset:16384\nds_read_b64_tr_b16 a[62:63], v127 offset:16896\nds_read_b64_tr_b16 a[68:69], v127 offset:16416\nds_read_b64_tr_b16 a[70:71], v127 offset:16928\nds_read_b64_tr_b16 a[76:77], v127 offset:16448\n" \
"ds_read_b64_tr_b16 a[78:79], v127 offset:16960\nds_read_b64_tr_b16 a[84:85], v127 offset:16480\nds_read_b64_tr_b16 a[86:87], v127 offset:16992\ns_waitcnt lgkmcnt(0)\ns_add_u32 m0, %[lw], 16384\ns_add_u32 s44, %[wo], 917504\nbuffer_load_dwordx4 v125, %[rw], s44 offen nt lds\ns_add_u32 m0, %[lw], 17408\n" \
"s_add_u32 s44, %[wo], 933888\nbuffer_load_dwordx4 v125, %[rw], s44 offen nt lds\ns_add_u32 m0, %[lw], 18432\ns_add_u32 s44, %[wo], 950272\nbuffer_load_dwordx4 v125, %[rw], s44 offen nt lds\ns_add_u32 m0, %[lw], 19456\ns_add_u32 s44, %[wo], 966656\nbuffer_load_dwordx4 v125, %[rw], s44 offen nt lds\n" \
"s_waitcnt vmcnt(32)\nds_read_b64_tr_b16 a[88:89], v127 offset:20480\nds_read_b64_tr_b16 a[90:91], v127 offset:20992\nds_read_b64_tr_b16 a[96:97], v127 offset:20512\nds_read_b64_tr_b16 a[98:99], v127 offset:21024\nds_read_b64_tr_b16 a[104:105], v127 offset:20544\nds_read_b64_tr_b16 a[106:107], v127 offset:21056\nds_read_b64_tr_b16 v[84:85], v127 offset:20576\n" \
"ds_read_b64_tr_b16 v[86:87], v127 offset:21088\ns_waitcnt lgkmcnt(0)\ns_add_u32 m0, %[lw], 20480\ns_add_u32 s44, %[wo], 983040\nbuffer_load_dwordx4 v125, %[rw], s44 offen nt lds\ns_add_u32 m0, %[lw], 21504\ns_add_u32 s44, %[wo], 999424\nbuffer_load_dwordx4 v125, %[rw], s44 offen nt lds\n" \
"s_add_u32 m0, %[lw], 22528\ns_add_u32 s44, %[wo], 1015808\nbuffer_load_dwordx4 v125, %[rw], s44 offen nt lds\ns_add_u32 m0, %[lw], 23552\ns_add_u32 s44, %[wo], 1032192\nbuffer_load_dwordx4 v125, %[rw], s44 offen nt lds\ns_waitcnt vmcnt(32)\nds_read_b64_tr_b16 a[92:93], v126 offset:0\n" \
"ds_read_b64_tr_b16 a[94:95], v126 offset:512\nds_read_b64_tr_b16 a[100:101], v126 offset:32\nds_read_b64_tr_b16 a[102:103], v126 offset:544\nds_read_b64_tr_b16 a[108:109], v126 offset:64\nds_read_b64_tr_b16 a[110:111], v126 offset:576\nds_read_b64_tr_b16 v[80:81], v126 offset:96\nds_read_b64_tr_b16 v[82:83], v126 offset:608\ns_waitcnt lgkmcnt(0)\n" \
"s_add_u32 m0, %[tb], 0\ns_add_u32 s44, %[wo], 1048576\nbuffer_load_dwordx4 v125, %[rw], s44 offen nt lds\ns_add_u32 m0, %[tb], 1024\ns_add_u32 s44, %[wo], 1064960\nbuffer_load_dwordx4 v125, %[rw], s44 offen nt lds\ns_add_u32 m0, %[tb], 2048\ns_add_u32 s44, %[wo], 1081344\n" \
"buffer_load_dwordx4 v125, %[rw], s44 offen nt lds\ns_add_u32 m0, %[tb], 3072\ns_add_u32 s44, %[wo], 1097728\nbuffer_load_dwordx4 v125, %[rw], s44 offen nt lds\ns_waitcnt vmcnt(32)\nds_read_b64_tr_b16 a[112:113], v126 offset:4096\nds_read_b64_tr_b16 a[114:115], v126 offset:4608\nds_read_b64_tr_b16 a[120:121], v126 offset:4128\n" \
"ds_read_b64_tr_b16 a[122:123], v126 offset:4640\nds_read_b64_tr_b16 a[128:129], v126 offset:4160\nds_read_b64_tr_b16 a[130:131], v126 offset:4672\nds_read_b64_tr_b16 a[136:137], v126 offset:4192\nds_read_b64_tr_b16 a[138:139], v126 offset:4704\ns_waitcnt lgkmcnt(0)\ns_add_u32 m0, %[tb], 4096\ns_add_u32 s44, %[wo], 1114112\n" \
"buffer_load_dwordx4 v125, %[rw], s44 offen nt lds\ns_add_u32 m0, %[tb], 5120\ns_add_u32 s44, %[wo], 1130496\nbuffer_load_dwordx4 v125, %[rw], s44 offen nt lds\ns_add_u32 m0, %[tb], 6144\ns_add_u32 s44, %[wo], 1146880\nbuffer_load_dwordx4 v125, %[rw], s44 offen nt lds\ns_add_u32 m0, %[tb], 7168\n" \
"s_add_u32 s44, %[wo], 1163264\nbuffer_load_dwordx4 v125, %[rw], s44 offen nt lds\ns_waitcnt vmcnt(32)\nds_read_b64_tr_b16 a[116:117], v126 offset:8192\nds_read_b64_tr_b16 a[118:119], v126 offset:8704\nds_read_b64_tr_b16 a[124:125], v126 offset:8224\nds_read_b64_tr_b16 a[126:127], v126 offset:8736\nds_read_b64_tr_b16 a[132:133], v126 offset:8256\n" \
"ds_read_b64_tr_b16 a[134:135], v126 offset:8768\nds_read_b64_tr_b16 a[140:141], v126 offset:8288\nds_read_b64_tr_b16 a[142:143], v126 offset:8800\ns_waitcnt lgkmcnt(0)\ns_add_u32 m0, %[tb], 8192\ns_add_u32 s44, %[wo], 1179648\nbuffer_load_dwordx4 v125, %[rw], s44 offen nt lds\ns_add_u32 m0, %[tb], 9216\n" \
"s_add_u32 s44, %[wo], 1196032\nbuffer_load_dwordx4 v125, %[rw], s44 offen nt lds\ns_add_u32 m0, %[tb], 10240\ns_add_u32 s44, %[wo], 1212416\nbuffer_load_dwordx4 v125, %[rw], s44 offen nt lds\ns_add_u32 m0, %[tb], 11264\ns_add_u32 s44, %[wo], 1228800\nbuffer_load_dwordx4 v125, %[rw], s44 offen nt lds\n" \
"s_waitcnt vmcnt(32)\nds_read_b64_tr_b16 a[144:145], v127 offset:0\nds_read_b64_tr_b16 a[146:147], v127 offset:512\nds_read_b64_tr_b16 v[76:77], v127 offset:32\nds_read_b64_tr_b16 v[78:79], v127 offset:544\nds_read_b64_tr_b16 a[152:153], v127 offset:64\nds_read_b64_tr_b16 a[154:155], v127 offset:576\nds_read_b64_tr_b16 a[160:161], v127 offset:96\n" \
"ds_read_b64_tr_b16 a[162:163], v127 offset:608\ns_waitcnt lgkmcnt(0)\ns_add_u32 m0, %[lw], 0\ns_add_u32 s44, %[wo], 1245184\nbuffer_load_dwordx4 v125, %[rw], s44 offen nt lds\ns_add_u32 m0, %[lw], 1024\ns_add_u32 s44, %[wo], 1261568\nbuffer_load_dwordx4 v125, %[rw], s44 offen nt lds\n" \
"s_add_u32 m0, %[lw], 2048\ns_add_u32 s44, %[wo], 1277952\nbuffer_load_dwordx4 v125, %[rw], s44 offen nt lds\ns_add_u32 m0, %[lw], 3072\ns_add_u32 s44, %[wo], 1294336\nbuffer_load_dwordx4 v125, %[rw], s44 offen nt lds\ns_waitcnt vmcnt(32)\nds_read_b64_tr_b16 a[148:149], v127 offset:4096\n" \
"ds_read_b64_tr_b16 a[150:151], v127 offset:4608\nds_read_b64_tr_b16 v[72:73], v127 offset:4128\nds_read_b64_tr_b16 v[74:75], v127 offset:4640\nds_read_b64_tr_b16 a[156:157], v127 offset:4160\nds_read_b64_tr_b16 a[158:159], v127 offset:4672\nds_read_b64_tr_b16 a[164:165], v127 offset:4192\nds_read_b64_tr_b16 a[166:167], v127 offset:4704\ns_waitcnt lgkmcnt(0)\n" \
"s_add_u32 m0, %[lw], 4096\ns_add_u32 s44, %[wo], 1310720\nbuffer_load_dwordx4 v125, %[rw], s44 offen nt lds\ns_add_u32 m0, %[lw], 5120\ns_add_u32 s44, %[wo], 1327104\nbuffer_load_dwordx4 v125, %[rw], s44 offen nt lds\ns_add_u32 m0, %[lw], 6144\ns_add_u32 s44, %[wo], 1343488\n" \
"buffer_load_dwordx4 v125, %[rw], s44 offen nt lds\ns_add_u32 m0, %[lw], 7168\ns_add_u32 s44, %[wo], 1359872\nbuffer_load_dwordx4 v125, %[rw], s44 offen nt lds\ns_waitcnt vmcnt(32)\nds_read_b64_tr_b16 a[168:169], v127 offset:8192\nds_read_b64_tr_b16 a[170:171], v127 offset:8704\nds_read_b64_tr_b16 a[176:177], v127 offset:8224\n" \
"ds_read_b64_tr_b16 a[178:179], v127 offset:8736\nds_read_b64_tr_b16 v[68:69], v127 offset:8256\nds_read_b64_tr_b16 v[70:71], v127 offset:8768\nds_read_b64_tr_b16 a[184:185], v127 offset:8288\nds_read_b64_tr_b16 a[186:187], v127 offset:8800\ns_waitcnt lgkmcnt(0)\ns_add_u32 m0, %[lw], 8192\ns_add_u32 s44, %[wo], 1376256\n" \
"buffer_load_dwordx4 v125, %[rw], s44 offen nt lds\ns_add_u32 m0, %[lw], 9216\ns_add_u32 s44, %[wo], 1392640\nbuffer_load_dwordx4 v125, %[rw], s44 offen nt lds\ns_add_u32 m0, %[lw], 10240\ns_add_u32 s44, %[wo], 1409024\nbuffer_load_dwordx4 v125, %[rw], s44 offen nt lds\ns_add_u32 m0, %[lw], 11264\n" \
"s_add_u32 s44, %[wo], 1425408\nbuffer_load_dwordx4 v125, %[rw], s44 offen nt lds\ns_waitcnt vmcnt(32)\nds_read_b64_tr_b16 a[172:173], v127 offset:12288\nds_read_b64_tr_b16 a[174:175], v127 offset:12800\nds_read_b64_tr_b16 a[180:181], v127 offset:12320\nds_read_b64_tr_b16 a[182:183], v127 offset:12832\nds_read_b64_tr_b16 v[64:65], v127 offset:12352\n" \
"ds_read_b64_tr_b16 v[66:67], v127 offset:12864\nds_read_b64_tr_b16 a[188:189], v127 offset:12384\nds_read_b64_tr_b16 a[190:191], v127 offset:12896\ns_waitcnt lgkmcnt(0)\ns_add_u32 m0, %[lw], 12288\ns_add_u32 s44, %[wo], 1441792\nbuffer_load_dwordx4 v125, %[rw], s44 offen nt lds\ns_add_u32 m0, %[lw], 13312\n" \
"s_add_u32 s44, %[wo], 1458176\nbuffer_load_dwordx4 v125, %[rw], s44 offen nt lds\ns_add_u32 m0, %[lw], 14336\ns_add_u32 s44, %[wo], 1474560\nbuffer_load_dwordx4 v125, %[rw], s44 offen nt lds\ns_add_u32 m0, %[lw], 15360\ns_add_u32 s44, %[wo], 1490944\nbuffer_load_dwordx4 v125, %[rw], s44 offen nt lds\n" \
"s_waitcnt vmcnt(32)\nds_read_b64_tr_b16 a[192:193], v127 offset:16384\nds_read_b64_tr_b16 a[194:195], v127 offset:16896\nds_read_b64_tr_b16 a[200:201], v127 offset:16416\nds_read_b64_tr_b16 a[202:203], v127 offset:16928\nds_read_b64_tr_b16 a[208:209], v127 offset:16448\nds_read_b64_tr_b16 a[210:211], v127 offset:16960\nds_read_b64_tr_b16 a[216:217], v127 offset:16480\n" \
"ds_read_b64_tr_b16 a[218:219], v127 offset:16992\ns_waitcnt lgkmcnt(0)\ns_add_u32 m0, %[lw], 16384\ns_add_u32 s44, %[wo], 1507328\nbuffer_load_dwordx4 v125, %[rw], s44 offen nt lds\ns_add_u32 m0, %[lw], 17408\ns_add_u32 s44, %[wo], 1523712\nbuffer_load_dwordx4 v125, %[rw], s44 offen nt lds\n" \
"s_add_u32 m0, %[lw], 18432\ns_add_u32 s44, %[wo], 1540096\nbuffer_load_dwordx4 v125, %[rw], s44 offen nt lds\ns_add_u32 m0, %[lw], 19456\ns_add_u32 s44, %[wo], 1556480\nbuffer_load_dwordx4 v125, %[rw], s44 offen nt lds\ns_waitcnt vmcnt(32)\nds_read_b64_tr_b16 a[196:197], v127 offset:20480\n" \
"ds_read_b64_tr_b16 a[198:199], v127 offset:20992\nds_read_b64_tr_b16 a[204:205], v127 offset:20512\nds_read_b64_tr_b16 a[206:207], v127 offset:21024\nds_read_b64_tr_b16 a[212:213], v127 offset:20544\nds_read_b64_tr_b16 a[214:215], v127 offset:21056\nds_read_b64_tr_b16 a[220:221], v127 offset:20576\nds_read_b64_tr_b16 a[222:223], v127 offset:21088\ns_waitcnt lgkmcnt(0)\n" \
"s_add_u32 m0, %[lw], 20480\ns_add_u32 s44, %[wo], 1572864\nbuffer_load_dwordx4 v125, %[rw], s44 offen nt lds\ns_add_u32 m0, %[lw], 21504\ns_add_u32 s44, %[wo], 1589248\nbuffer_load_dwordx4 v125, %[rw], s44 offen nt lds\ns_add_u32 m0, %[lw], 22528\ns_add_u32 s44, %[wo], 1605632\n" \
"buffer_load_dwordx4 v125, %[rw], s44 offen nt lds\ns_add_u32 m0, %[lw], 23552\ns_add_u32 s44, %[wo], 1622016\nbuffer_load_dwordx4 v125, %[rw], s44 offen nt lds\ns_waitcnt vmcnt(32)\nds_read_b64_tr_b16 v[60:61], v126 offset:0\nds_read_b64_tr_b16 v[62:63], v126 offset:512\nds_read_b64_tr_b16 a[224:225], v126 offset:32\n" \
"ds_read_b64_tr_b16 a[226:227], v126 offset:544\nds_read_b64_tr_b16 a[232:233], v126 offset:64\nds_read_b64_tr_b16 a[234:235], v126 offset:576\nds_read_b64_tr_b16 a[240:241], v126 offset:96\nds_read_b64_tr_b16 a[242:243], v126 offset:608\ns_waitcnt lgkmcnt(0)\ns_add_u32 m0, %[tb], 0\ns_add_u32 s44, %[wo], 1638400\n" \
"buffer_load_dwordx4 v125, %[rw], s44 offen nt lds\ns_add_u32 m0, %[tb], 1024\ns_add_u32 s44, %[wo], 1654784\nbuffer_load_dwordx4 v125, %[rw], s44 offen nt lds\ns_add_u32 m0, %[tb], 2048\ns_add_u32 s44, %[wo], 1671168\nbuffer_load_dwordx4 v125, %[rw], s44 offen nt lds\ns_add_u32 m0, %[tb], 3072\n" \
"s_add_u32 s44, %[wo], 1687552\nbuffer_load_dwordx4 v125, %[rw], s44 offen nt lds\ns_waitcnt vmcnt(32)\nds_read_b64_tr_b16 v[56:57], v126 offset:4096\nds_read_b64_tr_b16 v[58:59], v126 offset:4608\nds_read_b64_tr_b16 a[228:229], v126 offset:4128\nds_read_b64_tr_b16 a[230:231], v126 offset:4640\nds_read_b64_tr_b16 a[236:237], v126 offset:4160\n" \
"ds_read_b64_tr_b16 a[238:239], v126 offset:4672\nds_read_b64_tr_b16 a[244:245], v126 offset:4192\nds_read_b64_tr_b16 a[246:247], v126 offset:4704\ns_waitcnt lgkmcnt(0)\ns_add_u32 m0, %[tb], 4096\ns_add_u32 s44, %[wo], 1703936\nbuffer_load_dwordx4 v125, %[rw], s44 offen nt lds\ns_add_u32 m0, %[tb], 5120\n" \
"s_add_u32 s44, %[wo], 1720320\nbuffer_load_dwordx4 v125, %[rw], s44 offen nt lds\ns_add_u32 m0, %[tb], 6144\ns_add_u32 s44, %[wo], 1736704\nbuffer_load_dwordx4 v125, %[rw], s44 offen nt lds\ns_add_u32 m0, %[tb], 7168\ns_add_u32 s44, %[wo], 1753088\nbuffer_load_dwordx4 v125, %[rw], s44 offen nt lds\n" \
"s_waitcnt vmcnt(32)\nds_read_b64_tr_b16 a[248:249], v126 offset:8192\nds_read_b64_tr_b16 a[250:251], v126 offset:8704\nds_read_b64_tr_b16 v[128:129], v126 offset:8224\nds_read_b64_tr_b16 v[130:131], v126 offset:8736\nds_read_b64_tr_b16 v[136:137], v126 offset:8256\nds_read_b64_tr_b16 v[138:139], v126 offset:8768\nds_read_b64_tr_b16 v[144:145], v126 offset:8288\n" \
"ds_read_b64_tr_b16 v[146:147], v126 offset:8800\ns_waitcnt lgkmcnt(0)\ns_add_u32 m0, %[tb], 8192\ns_add_u32 s44, %[wo], 1769472\nbuffer_load_dwordx4 v125, %[rw], s44 offen nt lds\ns_add_u32 m0, %[tb], 9216\ns_add_u32 s44, %[wo], 1785856\nbuffer_load_dwordx4 v125, %[rw], s44 offen nt lds\n" \
"s_add_u32 m0, %[tb], 10240\ns_add_u32 s44, %[wo], 1802240\nbuffer_load_dwordx4 v125, %[rw], s44 offen nt lds\ns_add_u32 m0, %[tb], 11264\ns_add_u32 s44, %[wo], 1818624\nbuffer_load_dwordx4 v125, %[rw], s44 offen nt lds\ns_waitcnt vmcnt(32)\nds_read_b64_tr_b16 a[252:253], v127 offset:0\n" \
"ds_read_b64_tr_b16 a[254:255], v127 offset:512\nds_read_b64_tr_b16 v[132:133], v127 offset:32\nds_read_b64_tr_b16 v[134:135], v127 offset:544\nds_read_b64_tr_b16 v[140:141], v127 offset:64\nds_read_b64_tr_b16 v[142:143], v127 offset:576\nds_read_b64_tr_b16 v[148:149], v127 offset:96\nds_read_b64_tr_b16 v[150:151], v127 offset:608\ns_waitcnt lgkmcnt(0)\n" \
"ds_write_b128 v1, v[92:95] offset:0\nds_write_b128 v1, v[88:91] offset:1024\nds_write_b128 v1, v[84:87] offset:2048\nds_write_b128 v1, v[80:83] offset:3072\ns_waitcnt vmcnt(28)\nds_read_b64_tr_b16 v[152:153], v127 offset:4096\nds_read_b64_tr_b16 v[154:155], v127 offset:4608\nds_read_b64_tr_b16 v[160:161], v127 offset:4128\n" \
"ds_read_b64_tr_b16 v[162:163], v127 offset:4640\nds_read_b64_tr_b16 v[80:81], v127 offset:4160\nds_read_b64_tr_b16 v[82:83], v127 offset:4672\nds_read_b64_tr_b16 v[168:169], v127 offset:4192\nds_read_b64_tr_b16 v[170:171], v127 offset:4704\ns_waitcnt lgkmcnt(0)\nds_write_b128 v1, v[76:79] offset:4096\nds_write_b128 v1, v[72:75] offset:5120\n" \
"ds_write_b128 v1, v[68:71] offset:6144\nds_write_b128 v1, v[64:67] offset:7168\ns_waitcnt vmcnt(24)\nds_read_b64_tr_b16 v[156:157], v127 offset:8192\nds_read_b64_tr_b16 v[158:159], v127 offset:8704\nds_read_b64_tr_b16 v[164:165], v127 offset:8224\nds_read_b64_tr_b16 v[166:167], v127 offset:8736\nds_read_b64_tr_b16 v[64:65], v127 offset:8256\n" \
"ds_read_b64_tr_b16 v[66:67], v127 offset:8768\nds_read_b64_tr_b16 v[172:173], v127 offset:8288\nds_read_b64_tr_b16 v[174:175], v127 offset:8800\ns_waitcnt lgkmcnt(0)\nds_write_b128 v1, v[60:63] offset:8192\nds_write_b128 v1, v[56:59] offset:9216\nds_write_b128 v1, v[80:83] offset:10240\nds_write_b128 v1, v[64:67] offset:11264\n" \
"s_waitcnt vmcnt(20)\nds_read_b64_tr_b16 v[176:177], v127 offset:12288\nds_read_b64_tr_b16 v[178:179], v127 offset:12800\nds_read_b64_tr_b16 v[184:185], v127 offset:12320\nds_read_b64_tr_b16 v[186:187], v127 offset:12832\nds_read_b64_tr_b16 v[192:193], v127 offset:12352\nds_read_b64_tr_b16 v[194:195], v127 offset:12864\nds_read_b64_tr_b16 v[200:201], v127 offset:12384\n" \
"ds_read_b64_tr_b16 v[202:203], v127 offset:12896\ns_waitcnt lgkmcnt(0)\ns_waitcnt vmcnt(16)\nds_read_b64_tr_b16 v[180:181], v127 offset:16384\nds_read_b64_tr_b16 v[182:183], v127 offset:16896\nds_read_b64_tr_b16 v[188:189], v127 offset:16416\nds_read_b64_tr_b16 v[190:191], v127 offset:16928\nds_read_b64_tr_b16 v[196:197], v127 offset:16448\n" \
"ds_read_b64_tr_b16 v[198:199], v127 offset:16960\nds_read_b64_tr_b16 v[204:205], v127 offset:16480\nds_read_b64_tr_b16 v[206:207], v127 offset:16992\ns_waitcnt lgkmcnt(0)\ns_waitcnt vmcnt(12)\nds_read_b64_tr_b16 v[64:65], v127 offset:20480\nds_read_b64_tr_b16 v[66:67], v127 offset:20992\nds_read_b64_tr_b16 v[208:209], v127 offset:20512\n" \
"ds_read_b64_tr_b16 v[210:211], v127 offset:21024\nds_read_b64_tr_b16 v[216:217], v127 offset:20544\nds_read_b64_tr_b16 v[218:219], v127 offset:21056\nds_read_b64_tr_b16 v[224:225], v127 offset:20576\nds_read_b64_tr_b16 v[226:227], v127 offset:21088\ns_waitcnt lgkmcnt(0)\nds_write_b128 v1, v[64:67] offset:12288\ns_waitcnt vmcnt(8)\n" \
"ds_read_b64_tr_b16 v[64:65], v126 offset:0\nds_read_b64_tr_b16 v[66:67], v126 offset:512\nds_read_b64_tr_b16 v[212:213], v126 offset:32\nds_read_b64_tr_b16 v[214:215], v126 offset:544\nds_read_b64_tr_b16 v[220:221], v126 offset:64\nds_read_b64_tr_b16 v[222:223], v126 offset:576\nds_read_b64_tr_b16 v[228:229], v126 offset:96\nds_read_b64_tr_b16 v[230:231], v126 offset:608\n" \
"s_waitcnt lgkmcnt(0)\nds_write_b128 v1, v[64:67] offset:13312\ns_waitcnt vmcnt(4)\nds_read_b64_tr_b16 v[232:233], v126 offset:4096\nds_read_b64_tr_b16 v[234:235], v126 offset:4608\nds_read_b64_tr_b16 v[64:65], v126 offset:4128\nds_read_b64_tr_b16 v[66:67], v126 offset:4640\nds_read_b64_tr_b16 v[240:241], v126 offset:4160\n" \
"ds_read_b64_tr_b16 v[242:243], v126 offset:4672\nds_read_b64_tr_b16 v[248:249], v126 offset:4192\nds_read_b64_tr_b16 v[250:251], v126 offset:4704\ns_waitcnt lgkmcnt(0)\nds_write_b128 v1, v[64:67] offset:14336\ns_waitcnt vmcnt(0)\nds_read_b64_tr_b16 v[236:237], v126 offset:8192\nds_read_b64_tr_b16 v[238:239], v126 offset:8704\n" \
"ds_read_b64_tr_b16 v[64:65], v126 offset:8224\nds_read_b64_tr_b16 v[66:67], v126 offset:8736\nds_read_b64_tr_b16 v[244:245], v126 offset:8256\nds_read_b64_tr_b16 v[246:247], v126 offset:8768\nds_read_b64_tr_b16 v[252:253], v126 offset:8288\nds_read_b64_tr_b16 v[254:255], v126 offset:8800\ns_waitcnt lgkmcnt(0)\nds_write_b128 v1, v[64:67] offset:15360\n" \
"s_waitcnt lgkmcnt(0)\ns_barrier\ns_mov_b32 s40, 0\ns_mov_b32 s43, 0\ns_mov_b32 s56, -2147483648\n.Ldd_next%=:\nbuffer_load_dword v8, v7, %[rs], 0 offen sc1\ns_lshl_b32 s58, s40, 1\n" \
"s_lshl_b32 s44, s40, 19\ns_add_u32 s44, s44, %[ho]\ns_lshr_b32 s45, s44, 8\ns_and_b32 s45, s45, 0x2000\ns_xor_b32 s55, s44, s45\ns_waitcnt vmcnt(0)\nbuffer_store_dword v9, off, %[rs], s56 sc1\n.Ldd_w0%=:\n" \
"v_cmp_gt_i32 vcc, s58, v8\ns_cbranch_vccz .Ldd_w0go%=\ns_cmp_ge_u32 s40, 62\ns_cbranch_scc1 .Ldd_ns666%=\ns_sleep 16\n.Ldd_ns666%=:\ns_sleep 1\nbuffer_load_dword v8, v7, %[rs], 0 offen sc1\n" \
"s_waitcnt vmcnt(0)\ns_add_u32 s43, s43, 1\ns_cmp_lt_u32 s43, 262144\ns_cbranch_scc1 .Ldd_w0%=\ns_branch .Ldd_abort%=\n.Ldd_w0go%=:\ns_add_u32 s60, s55, 0\nbuffer_load_dwordx4 v[48:51], v0, %[rs], s60 offen offset:0 sc1\n" \
"buffer_load_dwordx4 v[52:55], v0, %[rs], s60 offen offset:2048 sc1\ns_add_u32 s60, s55, 4096\nbuffer_load_dwordx4 v[56:59], v0, %[rs], s60 offen offset:0 sc1\nbuffer_load_dwordx4 v[60:63], v0, %[rs], s60 offen offset:2048 sc1\ns_add_u32 s60, s55, 16384\nbuffer_load_dwordx4 v[64:67], v0, %[rs], s60 offen offset:0 sc1\nbuffer_load_dwordx4 v[68:71], v0, %[rs], s60 offen offset:2048 sc1\ns_add_u32 s60, s55, 20480\n" \
"buffer_load_dwordx4 v[72:75], v0, %[rs], s60 offen offset:0 sc1\nbuffer_load_dwordx4 v[76:79], v0, %[rs], s60 offen offset:2048 sc1\ns_add_u32 s60, s55, 32768\nbuffer_load_dwordx4 v[80:83], v0, %[rs], s60 offen offset:0 sc1\nbuffer_load_dwordx4 v[84:87], v0, %[rs], s60 offen offset:2048 sc1\ns_add_u32 s60, s55, 36864\nbuffer_load_dwordx4 v[88:91], v0, %[rs], s60 offen offset:0 sc1\nbuffer_load_dwordx4 v[92:95], v0, %[rs], s60 offen offset:2048 sc1\n" \
"s_add_u32 s60, s55, 49152\nbuffer_load_dwordx4 v[96:99], v0, %[rs], s60 offen offset:0 sc1\nbuffer_load_dwordx4 v[100:103], v0, %[rs], s60 offen offset:2048 sc1\ns_add_u32 s60, s55, 53248\nbuffer_load_dwordx4 v[104:107], v0, %[rs], s60 offen offset:0 sc1\nbuffer_load_dwordx4 v[108:111], v0, %[rs], s60 offen offset:2048 sc1\ns_add_u32 s44, s40, 0\ns_lshl_b32 s44, s44, 17\n" \
"s_add_u32 s44, s44, %[xn]\ns_lshr_b32 s45, s44, 8\ns_and_b32 s45, s45, 0x2000\ns_xor_b32 s41, s44, s45\ns_lshl_b32 s44, s40, 16\ns_cmp_eq_u32 %[l], 0\ns_cselect_b32 s42, s41, s44\ns_add_u32 s58, s58, 1\n" \
"ds_read_b128 v[120:123], v1 offset:0\ns_waitcnt vmcnt(15)\nv_mfma_f32_16x16x32_bf16 v[16:19], a[0:3], v[48:51], 0\nds_read_b128 v[124:127], v1 offset:1024\nv_mfma_f32_16x16x32_bf16 v[24:27], a[8:11], v[48:51], 0\nbuffer_load_dword v8, v7, %[rs], 0 offen sc1\nv_mfma_f32_16x16x32_bf16 v[32:35], a[16:19], v[48:51], 0\ns_add_u32 s60, s55, 65536\n" \
"v_mfma_f32_16x16x32_bf16 v[40:43], a[24:27], v[48:51], 0\ns_waitcnt vmcnt(15)\nbuffer_load_dwordx4 v[48:51], v0, %[rs], s60 offen offset:0 sc1\nv_mfma_f32_16x16x32_bf16 v[16:19], a[4:7], v[52:55], v[16:19]\nv_mfma_f32_16x16x32_bf16 v[24:27], a[12:15], v[52:55], v[24:27]\nv_mfma_f32_16x16x32_bf16 v[32:35], a[20:23], v[52:55], v[32:35]\nv_mfma_f32_16x16x32_bf16 v[40:43], a[28:31], v[52:55], v[40:43]\ns_waitcnt vmcnt(15)\n" \
"buffer_load_dwordx4 v[52:55], v0, %[rs], s60 offen offset:2048 sc1\nv_mfma_f32_16x16x32_bf16 v[16:19], a[32:35], v[56:59], v[16:19]\ns_waitcnt lgkmcnt(1)\nv_mfma_f32_16x16x32_bf16 v[24:27], v[120:123], v[56:59], v[24:27]\nv_mfma_f32_16x16x32_bf16 v[32:35], a[40:43], v[56:59], v[32:35]\ns_add_u32 s60, s55, 69632\nv_mfma_f32_16x16x32_bf16 v[40:43], a[48:51], v[56:59], v[40:43]\ns_waitcnt vmcnt(15)\n" \
"buffer_load_dwordx4 v[56:59], v0, %[rs], s60 offen offset:0 sc1\nv_mfma_f32_16x16x32_bf16 v[16:19], a[36:39], v[60:63], v[16:19]\ns_waitcnt lgkmcnt(0)\nds_read_b128 v[120:123], v1 offset:2048\nv_mfma_f32_16x16x32_bf16 v[24:27], v[124:127], v[60:63], v[24:27]\nv_mfma_f32_16x16x32_bf16 v[32:35], a[44:47], v[60:63], v[32:35]\nv_mfma_f32_16x16x32_bf16 v[40:43], a[52:55], v[60:63], v[40:43]\ns_waitcnt vmcnt(15)\n" \
"buffer_load_dwordx4 v[60:63], v0, %[rs], s60 offen offset:2048 sc1\nv_mfma_f32_16x16x32_bf16 v[16:19], a[56:59], v[64:67], v[16:19]\nds_read_b128 v[124:127], v1 offset:3072\nv_mfma_f32_16x16x32_bf16 v[24:27], a[64:67], v[64:67], v[24:27]\nv_mfma_f32_16x16x32_bf16 v[32:35], a[72:75], v[64:67], v[32:35]\ns_add_u32 s60, s55, 81920\nv_mfma_f32_16x16x32_bf16 v[40:43], a[80:83], v[64:67], v[40:43]\ns_waitcnt vmcnt(15)\n" \
"buffer_load_dwordx4 v[64:67], v0, %[rs], s60 offen offset:0 sc1\nv_mfma_f32_16x16x32_bf16 v[16:19], a[60:63], v[68:71], v[16:19]\nv_mfma_f32_16x16x32_bf16 v[24:27], a[68:71], v[68:71], v[24:27]\nv_mfma_f32_16x16x32_bf16 v[32:35], a[76:79], v[68:71], v[32:35]\nv_mfma_f32_16x16x32_bf16 v[40:43], a[84:87], v[68:71], v[40:43]\ns_waitcnt vmcnt(15)\nbuffer_load_dwordx4 v[68:71], v0, %[rs], s60 offen offset:2048 sc1\nv_mfma_f32_16x16x32_bf16 v[16:19], a[88:91], v[72:75], v[16:19]\n" \
"v_mfma_f32_16x16x32_bf16 v[24:27], a[96:99], v[72:75], v[24:27]\nv_mfma_f32_16x16x32_bf16 v[32:35], a[104:107], v[72:75], v[32:35]\ns_waitcnt lgkmcnt(1)\ns_add_u32 s60, s55, 86016\nv_mfma_f32_16x16x32_bf16 v[40:43], v[120:123], v[72:75], v[40:43]\ns_waitcnt vmcnt(15)\nbuffer_load_dwordx4 v[72:75], v0, %[rs], s60 offen offset:0 sc1\nv_mfma_f32_16x16x32_bf16 v[16:19], a[92:95], v[76:79], v[16:19]\n" \
"ds_read_b128 v[120:123], v1 offset:4096\nv_mfma_f32_16x16x32_bf16 v[24:27], a[100:103], v[76:79], v[24:27]\nv_mfma_f32_16x16x32_bf16 v[32:35], a[108:111], v[76:79], v[32:35]\ns_waitcnt lgkmcnt(1)\nv_mfma_f32_16x16x32_bf16 v[40:43], v[124:127], v[76:79], v[40:43]\ns_waitcnt vmcnt(15)\nbuffer_load_dwordx4 v[76:79], v0, %[rs], s60 offen offset:2048 sc1\nv_mfma_f32_16x16x32_bf16 v[16:19], a[112:115], v[80:83], v[16:19]\n" \
"ds_read_b128 v[124:127], v1 offset:5120\nv_mfma_f32_16x16x32_bf16 v[24:27], a[120:123], v[80:83], v[24:27]\nv_mfma_f32_16x16x32_bf16 v[32:35], a[128:131], v[80:83], v[32:35]\ns_add_u32 s60, s55, 98304\nv_mfma_f32_16x16x32_bf16 v[40:43], a[136:139], v[80:83], v[40:43]\ns_waitcnt vmcnt(15)\nbuffer_load_dwordx4 v[80:83], v0, %[rs], s60 offen offset:0 sc1\nv_mfma_f32_16x16x32_bf16 v[16:19], a[116:119], v[84:87], v[16:19]\n" \
"ds_read_b128 v[112:115], v1 offset:6144\nv_mfma_f32_16x16x32_bf16 v[24:27], a[124:127], v[84:87], v[24:27]\nv_mfma_f32_16x16x32_bf16 v[32:35], a[132:135], v[84:87], v[32:35]\nv_mfma_f32_16x16x32_bf16 v[40:43], a[140:143], v[84:87], v[40:43]\ns_waitcnt vmcnt(15)\nbuffer_load_dwordx4 v[84:87], v0, %[rs], s60 offen offset:2048 sc1\nv_mfma_f32_16x16x32_bf16 v[16:19], a[144:147], v[88:91], v[16:19]\ns_waitcnt lgkmcnt(2)\n" \
"ds_read_b128 v[116:119], v1 offset:7168\nv_mfma_f32_16x16x32_bf16 v[24:27], v[120:123], v[88:91], v[24:27]\nv_mfma_f32_16x16x32_bf16 v[32:35], a[152:155], v[88:91], v[32:35]\ns_add_u32 s60, s55, 102400\nv_mfma_f32_16x16x32_bf16 v[40:43], a[160:163], v[88:91], v[40:43]\ns_waitcnt vmcnt(15)\nbuffer_load_dwordx4 v[88:91], v0, %[rs], s60 offen offset:0 sc1\nv_mfma_f32_16x16x32_bf16 v[16:19], a[148:151], v[92:95], v[16:19]\n" \
"s_waitcnt lgkmcnt(2)\nv_mfma_f32_16x16x32_bf16 v[24:27], v[124:127], v[92:95], v[24:27]\ns_waitcnt vmcnt(11)\nv_cmp_gt_i32 vcc, s58, v8\nv_mfma_f32_16x16x32_bf16 v[32:35], a[156:159], v[92:95], v[32:35]\ns_cmp_eq_u64 vcc, 0\ns_cselect_b32 s61, s55, -2147483648\nv_mfma_f32_16x16x32_bf16 v[40:43], a[164:167], v[92:95], v[40:43]\n" \
"buffer_load_dwordx4 v[92:95], v0, %[rs], s60 offen offset:2048 sc1\nv_mfma_f32_16x16x32_bf16 v[16:19], a[168:171], v[96:99], v[16:19]\nv_mfma_f32_16x16x32_bf16 v[24:27], a[176:179], v[96:99], v[24:27]\ns_waitcnt lgkmcnt(1)\nv_mfma_f32_16x16x32_bf16 v[32:35], v[112:115], v[96:99], v[32:35]\nv_mfma_f32_16x16x32_bf16 v[40:43], a[184:187], v[96:99], v[40:43]\ns_add_u32 s60, s61, 0\nbuffer_load_dwordx4 v[96:99], v0, %[rs], s60 offen offset:1024 sc1\n" \
"v_mfma_f32_16x16x32_bf16 v[16:19], a[172:175], v[100:103], v[16:19]\nds_read_b128 v[112:115], v1 offset:8192\nv_mfma_f32_16x16x32_bf16 v[24:27], a[180:183], v[100:103], v[24:27]\ns_waitcnt lgkmcnt(1)\nv_mfma_f32_16x16x32_bf16 v[32:35], v[116:119], v[100:103], v[32:35]\nv_mfma_f32_16x16x32_bf16 v[40:43], a[188:191], v[100:103], v[40:43]\nbuffer_load_dwordx4 v[100:103], v0, %[rs], s60 offen offset:3072 sc1\nv_mfma_f32_16x16x32_bf16 v[16:19], a[192:195], v[104:107], v[16:19]\n" \
"ds_read_b128 v[116:119], v1 offset:9216\nv_mfma_f32_16x16x32_bf16 v[24:27], a[200:203], v[104:107], v[24:27]\nv_mfma_f32_16x16x32_bf16 v[32:35], a[208:211], v[104:107], v[32:35]\nv_mfma_f32_16x16x32_bf16 v[40:43], a[216:219], v[104:107], v[40:43]\ns_add_u32 s60, s61, 4096\nbuffer_load_dwordx4 v[104:107], v0, %[rs], s60 offen offset:1024 sc1\nv_mfma_f32_16x16x32_bf16 v[16:19], a[196:199], v[108:111], v[16:19]\nv_mfma_f32_16x16x32_bf16 v[24:27], a[204:207], v[108:111], v[24:27]\n" \
"v_mfma_f32_16x16x32_bf16 v[32:35], a[212:215], v[108:111], v[32:35]\nv_mfma_f32_16x16x32_bf16 v[40:43], a[220:223], v[108:111], v[40:43]\ns_waitcnt vmcnt(14)\ns_waitcnt lgkmcnt(1)\nbuffer_load_dwordx4 v[108:111], v0, %[rs], s60 offen offset:3072 sc1\nv_mfma_f32_16x16x32_bf16 v[16:19], v[112:115], v[48:51], v[16:19]\nv_mfma_f32_16x16x32_bf16 v[24:27], a[224:227], v[48:51], v[24:27]\nv_mfma_f32_16x16x32_bf16 v[32:35], a[232:235], v[48:51], v[32:35]\n" \
"s_add_u32 s60, s61, 16384\nv_mfma_f32_16x16x32_bf16 v[40:43], a[240:243], v[48:51], v[40:43]\ns_waitcnt vmcnt(14)\ns_waitcnt lgkmcnt(0)\nbuffer_load_dwordx4 v[48:51], v0, %[rs], s60 offen offset:1024 sc1\nv_mfma_f32_16x16x32_bf16 v[16:19], v[116:119], v[52:55], v[16:19]\nds_read_b128 v[112:115], v1 offset:10240\nv_mfma_f32_16x16x32_bf16 v[24:27], a[228:231], v[52:55], v[24:27]\n" \
"v_mfma_f32_16x16x32_bf16 v[32:35], a[236:239], v[52:55], v[32:35]\nv_mfma_f32_16x16x32_bf16 v[40:43], a[244:247], v[52:55], v[40:43]\ns_waitcnt vmcnt(14)\nbuffer_load_dwordx4 v[52:55], v0, %[rs], s60 offen offset:3072 sc1\nv_mfma_f32_16x16x32_bf16 v[16:19], a[248:251], v[56:59], v[16:19]\nds_read_b128 v[116:119], v1 offset:11264\nv_mfma_f32_16x16x32_bf16 v[24:27], v[128:131], v[56:59], v[24:27]\nv_mfma_f32_16x16x32_bf16 v[32:35], v[136:139], v[56:59], v[32:35]\n" \
"s_add_u32 s60, s61, 20480\nv_mfma_f32_16x16x32_bf16 v[40:43], v[144:147], v[56:59], v[40:43]\ns_waitcnt vmcnt(14)\nbuffer_load_dwordx4 v[56:59], v0, %[rs], s60 offen offset:1024 sc1\nv_mfma_f32_16x16x32_bf16 v[16:19], a[252:255], v[60:63], v[16:19]\nv_mfma_f32_16x16x32_bf16 v[24:27], v[132:135], v[60:63], v[24:27]\nv_mfma_f32_16x16x32_bf16 v[32:35], v[140:143], v[60:63], v[32:35]\nv_mfma_f32_16x16x32_bf16 v[40:43], v[148:151], v[60:63], v[40:43]\n" \
"s_waitcnt vmcnt(14)\nbuffer_load_dwordx4 v[60:63], v0, %[rs], s60 offen offset:3072 sc1\nv_mfma_f32_16x16x32_bf16 v[16:19], v[152:155], v[64:67], v[16:19]\nv_mfma_f32_16x16x32_bf16 v[24:27], v[160:163], v[64:67], v[24:27]\ns_waitcnt lgkmcnt(1)\nv_mfma_f32_16x16x32_bf16 v[32:35], v[112:115], v[64:67], v[32:35]\ns_add_u32 s60, s61, 32768\nv_mfma_f32_16x16x32_bf16 v[40:43], v[168:171], v[64:67], v[40:43]\n" \
"s_waitcnt vmcnt(14)\nbuffer_load_dwordx4 v[64:67], v0, %[rs], s60 offen offset:1024 sc1\nv_mfma_f32_16x16x32_bf16 v[16:19], v[156:159], v[68:71], v[16:19]\nds_read_b128 v[112:115], v1 offset:12288\nv_mfma_f32_16x16x32_bf16 v[24:27], v[164:167], v[68:71], v[24:27]\ns_waitcnt lgkmcnt(1)\nv_mfma_f32_16x16x32_bf16 v[32:35], v[116:119], v[68:71], v[32:35]\nv_mfma_f32_16x16x32_bf16 v[40:43], v[172:175], v[68:71], v[40:43]\n" \
"s_waitcnt vmcnt(14)\nbuffer_load_dwordx4 v[68:71], v0, %[rs], s60 offen offset:3072 sc1\nv_mfma_f32_16x16x32_bf16 v[16:19], v[176:179], v[72:75], v[16:19]\nds_read_b128 v[116:119], v1 offset:13312\nv_mfma_f32_16x16x32_bf16 v[24:27], v[184:187], v[72:75], v[24:27]\nv_mfma_f32_16x16x32_bf16 v[32:35], v[192:195], v[72:75], v[32:35]\ns_add_u32 s60, s61, 36864\nv_mfma_f32_16x16x32_bf16 v[40:43], v[200:203], v[72:75], v[40:43]\n" \
"s_waitcnt vmcnt(14)\nbuffer_load_dwordx4 v[72:75], v0, %[rs], s60 offen offset:1024 sc1\nv_mfma_f32_16x16x32_bf16 v[16:19], v[180:183], v[76:79], v[16:19]\nds_read_b128 v[120:123], v1 offset:14336\nv_mfma_f32_16x16x32_bf16 v[24:27], v[188:191], v[76:79], v[24:27]\nv_mfma_f32_16x16x32_bf16 v[32:35], v[196:199], v[76:79], v[32:35]\nv_mfma_f32_16x16x32_bf16 v[40:43], v[204:207], v[76:79], v[40:43]\ns_waitcnt vmcnt(14)\n" \
"s_waitcnt lgkmcnt(2)\nbuffer_load_dwordx4 v[76:79], v0, %[rs], s60 offen offset:3072 sc1\nv_mfma_f32_16x16x32_bf16 v[16:19], v[112:115], v[80:83], v[16:19]\nds_read_b128 v[124:127], v1 offset:15360\nv_mfma_f32_16x16x32_bf16 v[24:27], v[208:211], v[80:83], v[24:27]\nv_mfma_f32_16x16x32_bf16 v[32:35], v[216:219], v[80:83], v[32:35]\ns_add_u32 s60, s61, 49152\nv_mfma_f32_16x16x32_bf16 v[40:43], v[224:227], v[80:83], v[40:43]\n" \
"s_waitcnt vmcnt(14)\ns_waitcnt lgkmcnt(2)\nbuffer_load_dwordx4 v[80:83], v0, %[rs], s60 offen offset:1024 sc1\nv_mfma_f32_16x16x32_bf16 v[16:19], v[116:119], v[84:87], v[16:19]\nv_mfma_f32_16x16x32_bf16 v[24:27], v[212:215], v[84:87], v[24:27]\nv_mfma_f32_16x16x32_bf16 v[32:35], v[220:223], v[84:87], v[32:35]\nv_mfma_f32_16x16x32_bf16 v[40:43], v[228:231], v[84:87], v[40:43]\ns_waitcnt vmcnt(14)\n" \
"buffer_load_dwordx4 v[84:87], v0, %[rs], s60 offen offset:3072 sc1\nv_mfma_f32_16x16x32_bf16 v[16:19], v[232:235], v[88:91], v[16:19]\ns_waitcnt lgkmcnt(1)\nv_mfma_f32_16x16x32_bf16 v[24:27], v[120:123], v[88:91], v[24:27]\nv_mfma_f32_16x16x32_bf16 v[32:35], v[240:243], v[88:91], v[32:35]\ns_add_u32 s60, s61, 53248\nv_mfma_f32_16x16x32_bf16 v[40:43], v[248:251], v[88:91], v[40:43]\ns_waitcnt vmcnt(14)\n" \
"buffer_load_dwordx4 v[88:91], v0, %[rs], s60 offen offset:1024 sc1\nv_mfma_f32_16x16x32_bf16 v[16:19], v[236:239], v[92:95], v[16:19]\ns_waitcnt lgkmcnt(0)\nds_read_b128 v[120:123], v1 offset:0\nv_mfma_f32_16x16x32_bf16 v[24:27], v[124:127], v[92:95], v[24:27]\nv_mfma_f32_16x16x32_bf16 v[32:35], v[244:247], v[92:95], v[32:35]\nv_mfma_f32_16x16x32_bf16 v[40:43], v[252:255], v[92:95], v[40:43]\nbuffer_load_dwordx4 v[92:95], v0, %[rs], s60 offen offset:3072 sc1\n" \
"s_cbranch_vccnz .Ldd_slow%=\n.Ldd_slowr%=:\ns_waitcnt vmcnt(15)\nv_mfma_f32_16x16x32_bf16 v[20:23], a[0:3], v[96:99], 0\nds_read_b128 v[124:127], v1 offset:1024\nv_mfma_f32_16x16x32_bf16 v[28:31], a[8:11], v[96:99], 0\nv_mfma_f32_16x16x32_bf16 v[36:39], a[16:19], v[96:99], 0\ns_add_u32 s60, s55, 65536\n" \
"v_mfma_f32_16x16x32_bf16 v[44:47], a[24:27], v[96:99], 0\ns_waitcnt vmcnt(14)\nbuffer_load_dwordx4 v[96:99], v0, %[rs], s60 offen offset:1024 sc1\nv_mfma_f32_16x16x32_bf16 v[20:23], a[4:7], v[100:103], v[20:23]\nv_mfma_f32_16x16x32_bf16 v[28:31], a[12:15], v[100:103], v[28:31]\nv_mfma_f32_16x16x32_bf16 v[36:39], a[20:23], v[100:103], v[36:39]\nv_mfma_f32_16x16x32_bf16 v[44:47], a[28:31], v[100:103], v[44:47]\ns_waitcnt vmcnt(14)\n" \
"buffer_load_dwordx4 v[100:103], v0, %[rs], s60 offen offset:3072 sc1\nv_mfma_f32_16x16x32_bf16 v[20:23], a[32:35], v[104:107], v[20:23]\ns_waitcnt lgkmcnt(1)\nv_mfma_f32_16x16x32_bf16 v[28:31], v[120:123], v[104:107], v[28:31]\nds_write_b128 v2, v[16:19] offset:0\nv_mfma_f32_16x16x32_bf16 v[36:39], a[40:43], v[104:107], v[36:39]\ns_add_u32 s60, s55, 69632\nv_mfma_f32_16x16x32_bf16 v[44:47], a[48:51], v[104:107], v[44:47]\n" \
"s_waitcnt vmcnt(14)\nbuffer_load_dwordx4 v[104:107], v0, %[rs], s60 offen offset:1024 sc1\nv_mfma_f32_16x16x32_bf16 v[20:23], a[36:39], v[108:111], v[20:23]\ns_waitcnt lgkmcnt(1)\nds_read_b128 v[120:123], v1 offset:2048\nv_mfma_f32_16x16x32_bf16 v[28:31], v[124:127], v[108:111], v[28:31]\nv_mfma_f32_16x16x32_bf16 v[36:39], a[44:47], v[108:111], v[36:39]\nv_mfma_f32_16x16x32_bf16 v[44:47], a[52:55], v[108:111], v[44:47]\n" \
"s_waitcnt vmcnt(14)\nbuffer_load_dwordx4 v[108:111], v0, %[rs], s60 offen offset:3072 sc1\nv_mfma_f32_16x16x32_bf16 v[20:23], a[56:59], v[48:51], v[20:23]\nds_read_b128 v[124:127], v1 offset:3072\nv_mfma_f32_16x16x32_bf16 v[28:31], a[64:67], v[48:51], v[28:31]\nds_write_b128 v2, v[24:27] offset:2048\nv_mfma_f32_16x16x32_bf16 v[36:39], a[72:75], v[48:51], v[36:39]\ns_add_u32 s60, s55, 81920\n" \
"v_mfma_f32_16x16x32_bf16 v[44:47], a[80:83], v[48:51], v[44:47]\ns_waitcnt vmcnt(14)\nbuffer_load_dwordx4 v[48:51], v0, %[rs], s60 offen offset:1024 sc1\nv_mfma_f32_16x16x32_bf16 v[20:23], a[60:63], v[52:55], v[20:23]\nv_mfma_f32_16x16x32_bf16 v[28:31], a[68:71], v[52:55], v[28:31]\nv_mfma_f32_16x16x32_bf16 v[36:39], a[76:79], v[52:55], v[36:39]\nv_mfma_f32_16x16x32_bf16 v[44:47], a[84:87], v[52:55], v[44:47]\ns_waitcnt vmcnt(14)\n" \
"buffer_load_dwordx4 v[52:55], v0, %[rs], s60 offen offset:3072 sc1\nv_mfma_f32_16x16x32_bf16 v[20:23], a[88:91], v[56:59], v[20:23]\nv_mfma_f32_16x16x32_bf16 v[28:31], a[96:99], v[56:59], v[28:31]\nds_write_b128 v2, v[32:35] offset:4096\nv_mfma_f32_16x16x32_bf16 v[36:39], a[104:107], v[56:59], v[36:39]\ns_waitcnt lgkmcnt(3)\ns_add_u32 s60, s55, 86016\nv_mfma_f32_16x16x32_bf16 v[44:47], v[120:123], v[56:59], v[44:47]\n" \
"s_waitcnt vmcnt(14)\nbuffer_load_dwordx4 v[56:59], v0, %[rs], s60 offen offset:1024 sc1\nv_mfma_f32_16x16x32_bf16 v[20:23], a[92:95], v[60:63], v[20:23]\nds_read_b128 v[120:123], v1 offset:4096\nv_mfma_f32_16x16x32_bf16 v[28:31], a[100:103], v[60:63], v[28:31]\nv_mfma_f32_16x16x32_bf16 v[36:39], a[108:111], v[60:63], v[36:39]\ns_waitcnt lgkmcnt(3)\nv_mfma_f32_16x16x32_bf16 v[44:47], v[124:127], v[60:63], v[44:47]\n" \
"s_waitcnt vmcnt(14)\nbuffer_load_dwordx4 v[60:63], v0, %[rs], s60 offen offset:3072 sc1\nv_mfma_f32_16x16x32_bf16 v[20:23], a[112:115], v[64:67], v[20:23]\nds_read_b128 v[124:127], v1 offset:5120\nv_mfma_f32_16x16x32_bf16 v[28:31], a[120:123], v[64:67], v[28:31]\nds_write_b128 v2, v[40:43] offset:6144\nv_mfma_f32_16x16x32_bf16 v[36:39], a[128:131], v[64:67], v[36:39]\ns_add_u32 s60, s55, 98304\n" \
"v_mfma_f32_16x16x32_bf16 v[44:47], a[136:139], v[64:67], v[44:47]\ns_waitcnt vmcnt(14)\nbuffer_load_dwordx4 v[64:67], v0, %[rs], s60 offen offset:1024 sc1\nv_mfma_f32_16x16x32_bf16 v[20:23], a[116:119], v[68:71], v[20:23]\nds_read_b128 v[112:115], v1 offset:6144\nv_mfma_f32_16x16x32_bf16 v[28:31], a[124:127], v[68:71], v[28:31]\nv_mfma_f32_16x16x32_bf16 v[36:39], a[132:135], v[68:71], v[36:39]\nv_mfma_f32_16x16x32_bf16 v[44:47], a[140:143], v[68:71], v[44:47]\n" \
"s_waitcnt vmcnt(14)\nbuffer_load_dwordx4 v[68:71], v0, %[rs], s60 offen offset:3072 sc1\nv_mfma_f32_16x16x32_bf16 v[20:23], a[144:147], v[72:75], v[20:23]\ns_waitcnt lgkmcnt(3)\nds_read_b128 v[116:119], v1 offset:7168\nv_mfma_f32_16x16x32_bf16 v[28:31], v[120:123], v[72:75], v[28:31]\nv_mfma_f32_16x16x32_bf16 v[36:39], a[152:155], v[72:75], v[36:39]\ns_add_u32 s60, s55, 102400\n" \
"v_mfma_f32_16x16x32_bf16 v[44:47], a[160:163], v[72:75], v[44:47]\ns_waitcnt vmcnt(14)\nbuffer_load_dwordx4 v[72:75], v0, %[rs], s60 offen offset:1024 sc1\nv_mfma_f32_16x16x32_bf16 v[20:23], a[148:151], v[76:79], v[20:23]\ns_waitcnt lgkmcnt(3)\nv_mfma_f32_16x16x32_bf16 v[28:31], v[124:127], v[76:79], v[28:31]\nv_mfma_f32_16x16x32_bf16 v[36:39], a[156:159], v[76:79], v[36:39]\nv_mfma_f32_16x16x32_bf16 v[44:47], a[164:167], v[76:79], v[44:47]\n" \
"s_waitcnt vmcnt(14)\nbuffer_load_dwordx4 v[76:79], v0, %[rs], s60 offen offset:3072 sc1\nv_mfma_f32_16x16x32_bf16 v[20:23], a[168:171], v[80:83], v[20:23]\nv_mfma_f32_16x16x32_bf16 v[28:31], a[176:179], v[80:83], v[28:31]\ns_waitcnt lgkmcnt(1)\nv_mfma_f32_16x16x32_bf16 v[36:39], v[112:115], v[80:83], v[36:39]\nv_mfma_f32_16x16x32_bf16 v[44:47], a[184:187], v[80:83], v[44:47]\ns_waitcnt vmcnt(14)\n" \
"buffer_load_dwordx4 v[12:15], v4, %[rs], s41 offen sc1\nv_mfma_f32_16x16x32_bf16 v[20:23], a[172:175], v[84:87], v[20:23]\nds_read_b128 v[112:115], v1 offset:8192\nv_mfma_f32_16x16x32_bf16 v[28:31], a[180:183], v[84:87], v[28:31]\ns_waitcnt lgkmcnt(1)\nv_mfma_f32_16x16x32_bf16 v[36:39], v[116:119], v[84:87], v[36:39]\nv_mfma_f32_16x16x32_bf16 v[44:47], a[188:191], v[84:87], v[44:47]\ns_waitcnt vmcnt(14)\n" \
"v_mfma_f32_16x16x32_bf16 v[20:23], a[192:195], v[88:91], v[20:23]\nds_read_b128 v[116:119], v1 offset:9216\nv_mfma_f32_16x16x32_bf16 v[28:31], a[200:203], v[88:91], v[28:31]\nv_mfma_f32_16x16x32_bf16 v[36:39], a[208:211], v[88:91], v[36:39]\nv_mfma_f32_16x16x32_bf16 v[44:47], a[216:219], v[88:91], v[44:47]\ns_waitcnt vmcnt(13)\nv_mfma_f32_16x16x32_bf16 v[20:23], a[196:199], v[92:95], v[20:23]\nv_mfma_f32_16x16x32_bf16 v[28:31], a[204:207], v[92:95], v[28:31]\n" \
"v_mfma_f32_16x16x32_bf16 v[36:39], a[212:215], v[92:95], v[36:39]\nv_mfma_f32_16x16x32_bf16 v[44:47], a[220:223], v[92:95], v[44:47]\ns_waitcnt vmcnt(12)\ns_waitcnt lgkmcnt(1)\nv_mfma_f32_16x16x32_bf16 v[20:23], v[112:115], v[96:99], v[20:23]\nv_mfma_f32_16x16x32_bf16 v[28:31], a[224:227], v[96:99], v[28:31]\nv_mfma_f32_16x16x32_bf16 v[36:39], a[232:235], v[96:99], v[36:39]\nv_mfma_f32_16x16x32_bf16 v[44:47], a[240:243], v[96:99], v[44:47]\n" \
"s_waitcnt vmcnt(11)\ns_waitcnt lgkmcnt(0)\nv_mfma_f32_16x16x32_bf16 v[20:23], v[116:119], v[100:103], v[20:23]\nds_read_b128 v[112:115], v1 offset:10240\nv_mfma_f32_16x16x32_bf16 v[28:31], a[228:231], v[100:103], v[28:31]\nv_mfma_f32_16x16x32_bf16 v[36:39], a[236:239], v[100:103], v[36:39]\nv_mfma_f32_16x16x32_bf16 v[44:47], a[244:247], v[100:103], v[44:47]\ns_waitcnt vmcnt(10)\n" \
"v_mfma_f32_16x16x32_bf16 v[20:23], a[248:251], v[104:107], v[20:23]\nds_read_b128 v[116:119], v1 offset:11264\nv_mfma_f32_16x16x32_bf16 v[28:31], v[128:131], v[104:107], v[28:31]\nv_mfma_f32_16x16x32_bf16 v[36:39], v[136:139], v[104:107], v[36:39]\nv_mfma_f32_16x16x32_bf16 v[44:47], v[144:147], v[104:107], v[44:47]\ns_waitcnt vmcnt(9)\nv_mfma_f32_16x16x32_bf16 v[20:23], a[252:255], v[108:111], v[20:23]\nv_mfma_f32_16x16x32_bf16 v[28:31], v[132:135], v[108:111], v[28:31]\n" \
"v_mfma_f32_16x16x32_bf16 v[36:39], v[140:143], v[108:111], v[36:39]\nv_mfma_f32_16x16x32_bf16 v[44:47], v[148:151], v[108:111], v[44:47]\ns_waitcnt vmcnt(8)\nv_mfma_f32_16x16x32_bf16 v[20:23], v[152:155], v[48:51], v[20:23]\nv_mfma_f32_16x16x32_bf16 v[28:31], v[160:163], v[48:51], v[28:31]\ns_waitcnt lgkmcnt(1)\nv_mfma_f32_16x16x32_bf16 v[36:39], v[112:115], v[48:51], v[36:39]\nv_mfma_f32_16x16x32_bf16 v[44:47], v[168:171], v[48:51], v[44:47]\n" \
"s_waitcnt vmcnt(7)\nv_mfma_f32_16x16x32_bf16 v[20:23], v[156:159], v[52:55], v[20:23]\nds_read_b128 v[112:115], v1 offset:12288\nv_mfma_f32_16x16x32_bf16 v[28:31], v[164:167], v[52:55], v[28:31]\ns_waitcnt lgkmcnt(1)\nv_mfma_f32_16x16x32_bf16 v[36:39], v[116:119], v[52:55], v[36:39]\nv_mfma_f32_16x16x32_bf16 v[44:47], v[172:175], v[52:55], v[44:47]\ns_waitcnt vmcnt(6)\n" \
"v_mfma_f32_16x16x32_bf16 v[20:23], v[176:179], v[56:59], v[20:23]\nds_read_b128 v[116:119], v1 offset:13312\nv_mfma_f32_16x16x32_bf16 v[28:31], v[184:187], v[56:59], v[28:31]\nv_mfma_f32_16x16x32_bf16 v[36:39], v[192:195], v[56:59], v[36:39]\nv_mfma_f32_16x16x32_bf16 v[44:47], v[200:203], v[56:59], v[44:47]\ns_waitcnt vmcnt(5)\nv_mfma_f32_16x16x32_bf16 v[20:23], v[180:183], v[60:63], v[20:23]\nds_read_b128 v[120:123], v1 offset:14336\n" \
"v_mfma_f32_16x16x32_bf16 v[28:31], v[188:191], v[60:63], v[28:31]\nv_mfma_f32_16x16x32_bf16 v[36:39], v[196:199], v[60:63], v[36:39]\nv_mfma_f32_16x16x32_bf16 v[44:47], v[204:207], v[60:63], v[44:47]\ns_waitcnt vmcnt(4)\ns_waitcnt lgkmcnt(2)\nv_mfma_f32_16x16x32_bf16 v[20:23], v[112:115], v[64:67], v[20:23]\nds_read_b128 v[124:127], v1 offset:15360\nv_mfma_f32_16x16x32_bf16 v[28:31], v[208:211], v[64:67], v[28:31]\n" \
"v_mfma_f32_16x16x32_bf16 v[36:39], v[216:219], v[64:67], v[36:39]\nv_mfma_f32_16x16x32_bf16 v[44:47], v[224:227], v[64:67], v[44:47]\ns_waitcnt vmcnt(3)\ns_waitcnt lgkmcnt(2)\nv_mfma_f32_16x16x32_bf16 v[20:23], v[116:119], v[68:71], v[20:23]\nv_mfma_f32_16x16x32_bf16 v[28:31], v[212:215], v[68:71], v[28:31]\nv_mfma_f32_16x16x32_bf16 v[36:39], v[220:223], v[68:71], v[36:39]\nv_mfma_f32_16x16x32_bf16 v[44:47], v[228:231], v[68:71], v[44:47]\n" \
"s_waitcnt vmcnt(2)\nv_mfma_f32_16x16x32_bf16 v[20:23], v[232:235], v[72:75], v[20:23]\ns_waitcnt lgkmcnt(1)\nv_mfma_f32_16x16x32_bf16 v[28:31], v[120:123], v[72:75], v[28:31]\nv_mfma_f32_16x16x32_bf16 v[36:39], v[240:243], v[72:75], v[36:39]\nv_mfma_f32_16x16x32_bf16 v[44:47], v[248:251], v[72:75], v[44:47]\ns_waitcnt vmcnt(1)\nv_mfma_f32_16x16x32_bf16 v[20:23], v[236:239], v[76:79], v[20:23]\n" \
"s_waitcnt vmcnt(0)\ns_waitcnt lgkmcnt(0)\nv_lshlrev_b32 v80, 16, v12\nv_and_b32 v81, 0xffff0000, v12\nv_mfma_f32_16x16x32_bf16 v[28:31], v[124:127], v[76:79], v[28:31]\nv_lshlrev_b32 v82, 16, v13\nv_and_b32 v83, 0xffff0000, v13\nv_mfma_f32_16x16x32_bf16 v[36:39], v[244:247], v[76:79], v[36:39]\n" \
"v_lshlrev_b32 v84, 16, v14\nv_and_b32 v85, 0xffff0000, v14\nv_mfma_f32_16x16x32_bf16 v[44:47], v[252:255], v[76:79], v[44:47]\ns_mul_i32 s44, s40, %[l]\nds_write_b128 v2, v[20:23] offset:1024\nds_write_b128 v2, v[28:31] offset:3072\ns_nop 0\nds_write_b128 v2, v[36:39] offset:5120\n" \
"s_nop 2\nds_write_b128 v2, v[44:47] offset:7168\nv_lshlrev_b32 v86, 16, v15\nv_and_b32 v87, 0xffff0000, v15\nv_mov_b32 v9, s40\ns_cmp_eq_u32 s44, 0\ns_cselect_b32 s56, %[fd], -2147483648\ns_add_u32 s40, s40, 1\n" \
"s_waitcnt lgkmcnt(0)\ns_barrier\nds_read_b128 v[16:19], v3 offset:0\nds_read_b128 v[20:23], v3 offset:256\nds_read_b128 v[24:27], v3 offset:8192\nds_read_b128 v[28:31], v3 offset:8448\nds_read_b128 v[32:35], v3 offset:16384\nds_read_b128 v[36:39], v3 offset:16640\n" \
"ds_read_b128 v[40:43], v3 offset:24576\nds_read_b128 v[44:47], v3 offset:24832\ns_waitcnt lgkmcnt(6)\nv_pk_add_f32 v[16:17], v[16:17], v[80:81]\nv_pk_add_f32 v[18:19], v[18:19], v[82:83]\nv_pk_add_f32 v[20:21], v[20:21], v[84:85]\nv_pk_add_f32 v[22:23], v[22:23], v[86:87]\ns_waitcnt lgkmcnt(4)\n" \
"v_pk_add_f32 v[16:17], v[16:17], v[24:25]\nv_pk_add_f32 v[18:19], v[18:19], v[26:27]\nv_pk_add_f32 v[20:21], v[20:21], v[28:29]\nv_pk_add_f32 v[22:23], v[22:23], v[30:31]\ns_waitcnt lgkmcnt(2)\nv_pk_add_f32 v[16:17], v[16:17], v[32:33]\nv_pk_add_f32 v[18:19], v[18:19], v[34:35]\nv_pk_add_f32 v[20:21], v[20:21], v[36:37]\n" \
"v_pk_add_f32 v[22:23], v[22:23], v[38:39]\ns_waitcnt lgkmcnt(0)\nv_pk_add_f32 v[16:17], v[16:17], v[40:41]\nv_pk_add_f32 v[18:19], v[18:19], v[42:43]\nv_pk_add_f32 v[20:21], v[20:21], v[44:45]\nv_pk_add_f32 v[22:23], v[22:23], v[46:47]\nv_cvt_pk_bf16_f32 v12, v16, v17\nv_cvt_pk_bf16_f32 v13, v18, v19\n" \
"v_cvt_pk_bf16_f32 v14, v20, v21\nv_cvt_pk_bf16_f32 v15, v22, v23\nbuffer_store_dwordx4 v[12:15], v5, %[ro], s42 offen sc1\nv_pk_add_f32 v[24:25], v[16:17], v[18:19]\nv_pk_add_f32 v[26:27], v[20:21], v[22:23]\nv_pk_add_f32 v[24:25], v[24:25], v[26:27]\nv_add_f32 v24, v24, v25\ns_nop 1\n" \
"v_add_f32_dpp v25, v24, v24 row_ror:8 row_mask:0xf bank_mask:0xf\nv_add_f32_dpp v26, v24, v24 row_ror:8 row_mask:0xf bank_mask:0xf\ns_nop 1\nv_permlane16_swap_b32 v25, v26\nv_add_f32 v24, v25, v26\nv_add_f32 v25, v25, v26\ns_nop 1\nv_permlane32_swap_b32 v24, v25\n" \
"v_add_f32 v24, v24, v25\nv_mul_f32 v24, 0x3c800000, v24\nv_pk_add_f32 v[16:17], v[16:17], v[24:25] op_sel_hi:[1,0] neg_lo:[0,1] neg_hi:[0,1]\nv_pk_add_f32 v[18:19], v[18:19], v[24:25] op_sel_hi:[1,0] neg_lo:[0,1] neg_hi:[0,1]\nv_pk_add_f32 v[20:21], v[20:21], v[24:25] op_sel_hi:[1,0] neg_lo:[0,1] neg_hi:[0,1]\nv_pk_add_f32 v[22:23], v[22:23], v[24:25] op_sel_hi:[1,0] neg_lo:[0,1] neg_hi:[0,1]\nv_pk_mul_f32 v[28:29], v[16:17], v[16:17]\nv_pk_mul_f32 v[30:31], v[18:19], v[18:19]\n" \
"v_pk_fma_f32 v[28:29], v[20:21], v[20:21], v[28:29]\nv_pk_fma_f32 v[30:31], v[22:23], v[22:23], v[30:31]\nv_pk_add_f32 v[28:29], v[28:29], v[30:31]\nv_add_f32 v28, v28, v29\ns_nop 1\nv_add_f32_dpp v29, v28, v28 row_ror:8 row_mask:0xf bank_mask:0xf\nv_add_f32_dpp v30, v28, v28 row_ror:8 row_mask:0xf bank_mask:0xf\ns_nop 1\n" \
"v_permlane16_swap_b32 v29, v30\nv_add_f32 v28, v29, v30\nv_add_f32 v29, v29, v30\ns_nop 1\nv_permlane32_swap_b32 v28, v29\nv_add_f32 v28, v28, v29\nv_mov_b32 v25, v28\nbuffer_store_dwordx2 v[24:25], v6, %[rs], s41 offen sc1\n" \
"v_xor_b32 v2, 0x8000, v2\nv_xor_b32 v3, 0x8000, v3\ns_cmp_lt_u32 s40, 64\ns_cbranch_scc1 .Ldd_next%=\ns_waitcnt vmcnt(0)\nbuffer_store_dword v9, off, %[rs], s56 sc1\ns_branch .Ldd_end%=\n.Ldd_slow%=:\n" \
".Ldd_w1%=:\nv_cmp_gt_i32 vcc, s58, v8\ns_cbranch_vccz .Ldd_w1go%=\ns_cmp_ge_u32 s40, 62\ns_cbranch_scc1 .Ldd_ns1219%=\ns_sleep 16\n.Ldd_ns1219%=:\ns_sleep 1\n" \
"buffer_load_dword v8, v7, %[rs], 0 offen sc1\ns_waitcnt vmcnt(0)\ns_add_u32 s43, s43, 1\ns_cmp_lt_u32 s43, 262144\ns_cbranch_scc1 .Ldd_w1%=\ns_branch .Ldd_abort%=\n.Ldd_w1go%=:\ns_add_u32 s60, s55, 0\n" \
"buffer_load_dwordx4 v[96:99], v0, %[rs], s60 offen offset:1024 sc1\nbuffer_load_dwordx4 v[100:103], v0, %[rs], s60 offen offset:3072 sc1\ns_add_u32 s60, s55, 4096\nbuffer_load_dwordx4 v[104:107], v0, %[rs], s60 offen offset:1024 sc1\nbuffer_load_dwordx4 v[108:111], v0, %[rs], s60 offen offset:3072 sc1\ns_add_u32 s60, s55, 16384\nbuffer_load_dwordx4 v[48:51], v0, %[rs], s60 offen offset:1024 sc1\nbuffer_load_dwordx4 v[52:55], v0, %[rs], s60 offen offset:3072 sc1\n" \
"s_add_u32 s60, s55, 20480\nbuffer_load_dwordx4 v[56:59], v0, %[rs], s60 offen offset:1024 sc1\nbuffer_load_dwordx4 v[60:63], v0, %[rs], s60 offen offset:3072 sc1\ns_add_u32 s60, s55, 32768\nbuffer_load_dwordx4 v[64:67], v0, %[rs], s60 offen offset:1024 sc1\nbuffer_load_dwordx4 v[68:71], v0, %[rs], s60 offen offset:3072 sc1\ns_add_u32 s60, s55, 36864\nbuffer_load_dwordx4 v[72:75], v0, %[rs], s60 offen offset:1024 sc1\n" \
"buffer_load_dwordx4 v[76:79], v0, %[rs], s60 offen offset:3072 sc1\ns_add_u32 s60, s55, 49152\nbuffer_load_dwordx4 v[80:83], v0, %[rs], s60 offen offset:1024 sc1\nbuffer_load_dwordx4 v[84:87], v0, %[rs], s60 offen offset:3072 sc1\ns_add_u32 s60, s55, 53248\nbuffer_load_dwordx4 v[88:91], v0, %[rs], s60 offen offset:1024 sc1\nbuffer_load_dwordx4 v[92:95], v0, %[rs], s60 offen offset:3072 sc1\ns_branch .Ldd_slowr%=\n" \
".Ldd_abort%=:\ns_mov_b64 exec, -1\nv_mov_b32 v16, 0\ns_movk_i32 s44, 6400\nbuffer_store_dword v16, off, %[rs], s44 sc1\n.Ldd_end%=:\ns_waitcnt vmcnt(0) lgkmcnt(0)\n" \
""
#define G1F 1
#define A1F 0

#define V10(p, x) p #x "0", p #x "1", p #x "2", p #x "3", p #x "4", p #x "5", p #x "6", p #x "7", p #x "8", p #x "9"
#define V256(p) V10(p, ), V10(p, 1), V10(p, 2), V10(p, 3), V10(p, 4), V10(p, 5), V10(p, 6), V10(p, 7), V10(p, 8), V10(p, 9), V10(p, 10), V10(p, 11), V10(p, 12), V10(p, 13), V10(p, 14), V10(p, 15), V10(p, 16), \
    V10(p, 17), V10(p, 18), V10(p, 19), V10(p, 20), V10(p, 21), V10(p, 22), V10(p, 23), V10(p, 24), p "250", p "251", p "252", p "253", p "254", p "255"
#define CLOB "memory", "vcc", "scc", V256("v"), V256("a"), V10("s", 4), V10("s", 5), V10("s", 6), V10("s", 7), V10("s", 8)
template <int N> DEVI void table(lds_char* p, const unsigned (&t)[N]) {
#pragma unroll
    for (int i = 0; i < N; ++i) *(l1*)(p + i * 256) = t[i];
    WL0;
}
DEVI unsigned prof_at(int cu, int w) { return O_PROF + (262144 + cu * 2560 + w * 8) * 4; }
DEVI unsigned sp_idx(int lane) { return (lane & 2) ? 0xEEEEu : 0x4444u; }
DEVI int sp_row(int r, int hf) { return 4 * (r >> 1) + 2 * hf + (r & 1); }

DEVI void qk_role(const Args& a, const int w, lds_char* smem, int l, int h) {
    const int lane = lane_id(), g = lane >> 4, n = lane & 15, b = n & 3, p = n >> 2;
    const unsigned l0 = (unsigned)(size_t)smem, tb = l0 + w * 12288, lw = l0 + 55296 + w * 24576, ls = l * SD, IMG = l0 + 32768, MIM = l0 + 49152;
    const unsigned xw = (w >> 1) * 16384 + (w & 1) * 4096, oc = (h >> 3) * 16384 + ls, tr = (8 * g + p) * 128 + b * 16;
    const unsigned cm[4] = {4 * g > n ? 0xF149F2CAu : 0u, 4 * g + 1 > n ? 0xF149F2CAu : 0u, 4 * g + 2 > n ? 0xF149F2CAu : 0u, 4 * g + 3 > n ? 0xF149F2CAu : 0u};
    const unsigned t[19] = {
        l0 + lane * 16, lw + lane * 16, l0 + (g >> 1) * 1024 + (g & 1) * 512 + n * 16, O_E1 + ls + xw + lane * 16, sp_idx(lane),
        IMG + (w >> 1) * 8192 + ((w & 1) * 8 + 2 * g) * 512 + n * 16,
        MIM + (w * 8 + 2 * p) * 144 + g * 32 + b * 4,
        IMG + g * 512 + (w ? n * 16 : 8192 + (16 * (b >> 1) + 8 * (p >> 1) + 2 * (p & 1) + (b & 1)) * 16),
        MIM + g * 144 + (n & 7) * 16, MIM + 2304 + g * 144 + (2 * p + (b & 1)) * 16,
        w ? O_PT + (h >> 3) * 32768 + ls + lane * 16 + (w == 1 ? 16384 + ((h & 7) >> 1) * 1024 + (h & 1) * 8 : (h & 7) * 1024)
          : O_PM + oc + ((h >> 1) & 3) * 1024 + ((2 * (h & 1) + (g >> 1)) * 16 + sp_row(n & 7, g & 1)) * 16 + (n >> 3) * 8,
        w ? O_PT + (h >> 3) * 32768 + ls + 20480 + ((h >> 2) & 1) * 2048 + (w - 1) * 1024 + (h & 3) * 256 + n * 16 + g * 4
          : n >> 3 ? OOB : O_PM + oc + 4096 + ((h & 7) >> 1) * 256 + sp_row(n & 7, g & 1) * 16 + (h & 1) * 8 + (g >> 1) * 4,
        w == 3 ? (l ? F_X + SD + p * 128 + (X1F ? 0 : b * 4) : F_X0 + b * 4) : cm[0], cm[1], cm[2], cm[3],
        (lane >> 3) * 8192u + (lane & 7) * 16, tb + tr, lw + tr};
    table(smem + w * 12288 + lane * 4, t);
    asm volatile(QK_ASM : : [ws] "s"(a.ws), [sz] "n"(WS_RS), [rw] "s"(mkrs(w < 2 ? a.Wq : a.Wk, 8u << 20)), [wo] "s"(h * 256 + (w & 1) * 128), [tb] "s"(tb), [lw] "s"(lw), [l0] "s"(l0), [w] "s"(w),
        [xn] "s"(O_XN + ls), [xw] "s"(xw), [fs] "s"(l ? 0 : 128), [sg] "s"(F_Y0 + 4096 + ls + h * 128 + 16), [pr] "s"(prof_at(l * 80 + h, w)) : CLOB, "s36", "s37", "s38", "s39");
    __builtin_amdgcn_endpgm();
}
DEVI void vu_role(const Args& a, const int w, lds_char* smem, int l, int h) {
    const int lane = lane_id(), g = lane >> 4, n = lane & 15, b = n & 3, p = n >> 2;
    const unsigned l0 = (unsigned)(size_t)smem, tb = l0 + w * 12288, lw = l0 + 61440 + w * 24576, ls = l * SD;
    const unsigned xw = (w >> 1) * 16384 + (w & 1) * 4096, tv = (8 * g + p) * 128 + b * 16 + (w & 1) * 64, to = (4 * g + (p & 1)) * 128 + (p >> 1) * 32 + b * 8;
    const unsigned t[18] = {
        l0 + lane * 16, lw + lane * 16, l0 + (g >> 1) * 1024 + (g & 1) * 512 + n * 16, O_E1 + ls + xw + lane * 16, sp_idx(lane),
        l0 + 57344 + (2 * w + (n >> 3)) * 256 + g * 32 + (n & 7) * 2,
        O_UM + ls + (4 * w + (g & 1)) * 65536 + (h >> 3) * 16384 + (g >> 1) * 2048 + (h & 7) * 256 + n * 16,
        sd(O_Y + h * 131072 + ls) + w * 32768 + (g >> 1) * 1024 + (g & 1) * 256 + n * 16,
        l ? F_X + SD + p * 128 + (X1F ? 0 : b * 4) : F_X0 + b * 4, l0 + 49152 + (w >> 1) * 2048 + (2 * (w & 1) + (g >> 1)) * 256 + n * 16 + (g & 1) * 8, sp_idx(lane >> 2), l0 + g * 256 + (n & 7) * 16,
        (lane >> 3) * 8192u + (lane & 7) * 16, (8 * (lane >> 5) + ((lane >> 3) & 3)) * 2048u + (lane & 7) * 16, tb + tv - (w & 1) * 12288, lw + tv - (w & 1) * 24576, tb + to, lw + to};
    table(smem + w * 12288 + lane * 4, t);
    asm volatile(VU_ASM : : [ws] "s"(a.ws), [sz] "n"(WS_RS), [rw] "s"(mkrs(a.Wv, 8u << 20)), [ro] "s"(mkrs(a.Wo, 8u << 20)), [wo] "s"(h * 256 + (w >> 1) * 128 + (w & 1) * 262144), [w2] "s"(h * 262144 + w * 512), [tb] "s"(tb), [lw] "s"(lw), [w] "s"(w),
        [xn] "s"(O_XN + ls), [xw] "s"(xw), [fs] "s"(l ? 0 : 128), [xt] "s"(w * 16384 - xw), [fy] "s"(F_Y0 + ls + h * 128 + w * 4), [sg] "s"(w == 1 ? F_Y0 + ls + h * 128 + 16 : OOB), [pr] "s"(prof_at(l * 80 + 32 + h, w) - 128) : CLOB, "s36", "s37", "s38", "s39");
    __builtin_amdgcn_endpgm();
}
DEVI void rd_role(const Args& a, const int w, lds_char* smem, int l, int c) {
    const int lane = lane_id(), g = lane >> 4, n = lane & 15, r = 2 * (n >> 2) + (n & 1), hf = (n >> 1) & 1;
    const rsrc_t rs = mkrs(a.ws, WS_RS);
    const unsigned l0 = (unsigned)(size_t)smem, tb = l0 + 65536 + w * 4096, ls = l * SD, vp = O_E1 + ls + (c >> 3) * 16384 + (c & 7) * 1024 + w * 256 + n * 16;
    {
        const v4u ff = {SENT, SENT, SENT, SENT};
#pragma unroll
        for (int i = 0; i < 8; ++i) st16(ff, rs, O_UM + ls + c * 65536 + w * 16384 + i * 1024 + lane * 16);
#pragma unroll
        for (int i = 0; i < 2; ++i) st16(ff, rs, O_PT + ls + c * 16384 + w * 2048 + i * 1024 + lane * 16);
        st16(ff, rs, c < 8 ? O_PM + ls + w * 16384 + c * 1024 + lane * 16 : OOB);
        st16(ff, rs, g ? vp + g * 32768 : OOB);
        WV0; BAR;
    }
    st4(g & 1 ? *(const unsigned*)(a.mem0 + r * 1024 + c * 64 + w * 16 + 4 * g + 2 * hf) : nosent(*(const unsigned*)(a.mem0 + r * 1024 + c * 64 + w * 16 + 4 * g + 2 * hf)), rs, g & 1 ? vp + g * 4 : OOB, 0);
    WV0;
    st4(nosent(*(const unsigned*)(a.mem0 + r * 1024 + c * 64 + w * 16 + 4 * g + 2 * hf)), rs, g & 1 ? OOB : vp + g * 4, 0);
    const int gi = lane >> 3, r8 = lane & 7;
    const unsigned t[12] = {
        (unsigned)lane * 16, sp_idx(lane), l0 + w * 4096 + g * 576 + ((n >> 3) * 16 + hf * 4 + 2 * ((n >> 2) & 1) + (n & 1)) * 4,
        l0 + (r >> 1) * 576 + w * 128 + (g >> 1) * 64 + (r & 1) * 32 + hf * 16,
        (vp ^ ((g & 1) * 65536)) + (g >> 1) * 8, g & 1 ? SENT : 0u,
        l0 + 16384 + w * 8192 + lane * 16, l0 + 16384 + ((gi >> 1) * 2 + (w >> 1)) * 1024 + ((gi & 1) * 32 + (w & 1) * 8 + r8) * 16,
        (c >> 1) * 16384u + (c & 1) * 4096 + gi * 512 + (w * 8 + r8) * 16,
        gi ? OOB : O_ST - O_XR + c * 256 + ((w & 1) * 8 + r8) * 16 + (w >> 1) * 8,
        F_Y0 + ls + (w * 8 + r8) * 128 + (c >> 2) * 4, F_Y0 + ls + (gi & 1) * 4096 + (w * 8 + r8) * 128 + 16};
    table(smem + 65536 + w * 4096 + lane * 4, t);
    asm volatile(RD_ASM : : [rs] "s"(rs), [tb] "s"(tb), [su] "s"(O_UM + ls + c * 65536 + w * 16384), [sp] "s"(O_PM + ls + w * 16384), [st] "s"(O_PT + ls + w * 32768),
        [sy] "s"(sd(O_Y + w * (1u << 20) + ls) + (c >> 1) * 16384 + (c & 1) * 4096), [at] "s"(O_XR + ls), [du] "s"((c & 2) * 5 / 2), [fa] "s"(A1F && w ? OOB : F_A + ls + c * 128 + w * 4), [pr] "s"(prof_at(l * 80 + 64 + c, w) + 128) : CLOB);
    __builtin_amdgcn_endpgm();
}
DEVI void gg_role(const Args& a, const int w, lds_char* smem, int l, int gi) {
    const int lane = lane_id(), g = lane >> 4, n = lane & 15, b = n & 3, p = n >> 2;
    const unsigned l0 = (unsigned)(size_t)smem, tb = l0 + w * 12288, lw = l0 + 65536 + w * 24576, ls = l * SD, tv = (8 * g + p) * 128 + b * 16 + (w & 1) * 64, fh = (g >> 1) * 1024 + (g & 1) * 512 + n * 16;
    const unsigned t[9] = {l0 + lane * 16, lw + lane * 16, l0 + fh, F_A + ls + (lane >> 2) * 128 + (A1F ? 0 : (lane & 3) * 4), w * 2048u + lane * 16, O_ST - O_XR + g * 1024 + n * 16, (lane >> 3) * 7168u + (lane & 7) * 16, tb + tv - (w & 1) * 12288, lw + tv - (w & 1) * 24576};
    table(smem + w * 12288 + lane * 4, t);
    asm volatile(GG_ASM : : [rs] "s"(mkrs(a.ws, WS_RS)), [rw] "s"(mkrs(w & 1 ? a.Wu : a.Wg, 3584 * 2048)), [wo] "s"(gi * 256 + (w >> 1) * 128), [tb] "s"(tb), [lw] "s"(lw), [w] "s"(w),
        [xn] "s"(O_XR + ls), [xw] "s"((w >> 1) * 16384 + (w & 1) * 4096), [ho] "s"(O_H + ls + gi * 16384), [fh] "s"(F_H + ls + (gi / 7) * 1024 + (gi % 7) * 128 + w * 4), [pr] "s"(prof_at(160 + l * 28 + gi, w) + 128) : CLOB);
    __builtin_amdgcn_endpgm();
}
DEVI void dd_role(const Args& a, const int w, lds_char* smem, int l, int c) {
    const int lane = lane_id(), g = lane >> 4, b = lane & 3, p = (lane >> 2) & 3, gi = lane >> 3, r8 = lane & 7;
    const unsigned l0 = (unsigned)(size_t)smem, tb = l0 + w * 12288, lw = l0 + 65536 + w * 24576, ls = l * SD, tr = (8 * g + p) * 128 + b * 8;
    const unsigned ta = (c >> 1) * 16384u + (c & 1) * 4096 + gi * 512 + (w * 8 + r8) * 16;
    const rsrc_t rs = mkrs(a.ws, WS_RS);
    const unsigned t[11] = {(unsigned)lane * 16, lw + lane * 16, l0 + w * 8192 + lane * 16, l0 + ((gi >> 1) * 2 + (w >> 1)) * 1024 + ((gi & 1) * 32 + (w & 1) * 8 + r8) * 16,
        ta, l ? (w * 8 + r8) * 2048 + c * 128 + gi * 16 : ta, gi || l ? OOB : O_ST - O_XR + 4096 + c * 256 + (w * 8 + r8) * 8,
        F_H + ls + w * 1024 + ((lane >> 2) % 7) * 128 + (G1F ? 0 : b * 4), gi * 2048u + r8 * 16, tb + tr, lw + tr};
    table(smem + w * 12288 + lane * 4, t);
    asm volatile(DD_ASM : : [rs] "s"(rs), [rw] "s"(mkrs(a.Wd, 3584 * 2048)), [ro] "s"(mkrs(l ? (char*)a.out : a.ws, l ? 2048 * 2048 : WS_RS)), [wo] "s"(w * (896 * 2048) + c * 128), [tb] "s"(tb), [lw] "s"(lw), [l] "s"(l),
        [xn] "s"(O_XR + ls), [ho] "s"(O_H + ls + w * 7 * 16384), [fd] "s"(D1F && w ? OOB : F_D + ls + c * 128 + w * 4), [lt] "s"(F_D + ls + c * 128 + w * 4 + 64), [pr] "s"(prof_at(216 + l * 16 + c, w) + 128) : CLOB);
    __builtin_amdgcn_endpgm();
}

#define CPF 74
#define CPJ 0
#define LNW 0
#define DLQ 150
#define DLS 400
#define SPL 0
#define PLAN 3
#define PFA 0
#define PFB 3
#define PFL 1
#define VUX 1
#define LNS 40
#define LN1 700
#define DLG 1100
#define DLD 1500
#define DL1 1500
#define DLG1 2600
#define DLD1 3000
DEVI Args args(unsigned sh) {
    auto p = (const __attribute__((address_space(4))) Args*)__builtin_amdgcn_kernarg_segment_ptr();
    asm volatile("" : "+s"(p));
    Args a;
    a.x = p->x; a.mem0 = p->mem0; a.Wq = p->Wq; a.Wk = p->Wk; a.Wv = p->Wv; a.Wo = p->Wo; a.Wg = p->Wg; a.Wu = p->Wu; a.Wd = p->Wd; a.ws = p->ws + sh; a.out = p->out;
    return a;
}
DEVI unsigned beat_wait(unsigned x0) {
    const rsrc_t r0 = mkrs(args(0).ws, 2u << 20);
    for (unsigned spins = 0; spins < SPIN_CAP; ++spins) {
        CB;
        const v2u t = __builtin_amdgcn_raw_buffer_load_b64(r0, 0, O_HB, AUX_SC1);
        if (RFL(t[0]) != x0) return RFL(t[1]) & (2u << 20);
        __builtin_amdgcn_s_sleep(8);
    }
    __builtin_amdgcn_endpgm();
}
#define MPF 0
DEVI void mpf(rsrc_t rs, unsigned o) {
    o += O_E1 + lane_id() * 128;
    unsigned spins = 0;
    for (int s = 0; s < NSEG && spins < SPIN_CAP; ++spins) {
        CB;
        if (__ballot(ld4(rs, o, (s & 3) * 32768) == SENT) == 0ull) { ++s; for (int i = 0; i < MPF; ++i) __builtin_amdgcn_s_sleep(8); }
    }
}
DEVI void leader(const Args& a, const int w, lds_char* smem, unsigned x0) {
    const int lane = lane_id();
    const rsrc_t r0 = mkrs(a.ws, 4u << 20);
    unsigned t[2] = {~0u, ~0u};
    for (int i = 0; i < 2 * NPRB; ++i) {
        const unsigned t0 = (unsigned)__builtin_amdgcn_s_memtime();
        WL0;
        if (lane == 0) st4(0u, r0, 0, O_PRB + (i & 1) * SD + w * 1024 + (i >> 1) * 128);
        WV0;
        const unsigned d = (unsigned)__builtin_amdgcn_s_memtime() - t0;
        if (i & 1) t[1] = min(t[1], d); else t[0] = min(t[0], d);
    }
    *(l2*)(smem + w * 8) = v2u{t[0], t[1]};
    WL0; BAR;
    const v4u p = *(l4*)smem, q = *(l4*)(smem + 16);
    const unsigned sh = RFL(min(min(p[0], p[2]), min(q[0], q[2])) < min(min(p[1], p[3]), min(q[1], q[3])) ? 2u << 20 : 0u);
    const unsigned o = sh + w * 1024 + lane * 16;
    const v4u ff = {SENT, SENT, SENT, SENT};
#pragma unroll
    for (int i = 0; i < 8; ++i) st16(ff, r0, o + O_E1 + i * 4096);
#pragma unroll
    for (int i = 1; i < 16; ++i) if (i != 2 && i != 11) st16(ff, r0, o + i * 4096);
    WV0; BAR;
    if (w) { if (MPF && w > 1) mpf(r0, sh + SD + (w & 1) * 16384); return; }
    for (unsigned spins = 0; spins < SPIN_CAP; ++spins) {
        if (lane == 0) st8(v2u{++x0, sh}, r0, 0, O_HB);
        if (__ballot((int)ld4(r0, sh + F_D + SD + (lane >> 2) * 128 + (D1F ? 0 : (lane & 3) * 4), 0) < 0) == 0ull) break;
        __builtin_amdgcn_s_sleep(16);
    }
}
__global__ __launch_bounds__(256, 1) void sys_kernel(Args) {
    extern __shared__ __attribute__((aligned(16))) char smem_[];
    lds_char* smem = (lds_char*)smem_;
    const int x = blockIdx.x & 7, j = blockIdx.x >> 3, wv = RFL(threadIdx.x >> 6), l = x >> 2, xx = x & 3;
    const unsigned t0 = (unsigned)__builtin_amdgcn_s_memrealtime();
    auto until = [&](unsigned dl) AI { while ((unsigned)__builtin_amdgcn_s_memrealtime() - t0 < dl) __builtin_amdgcn_s_sleep(8); };
    unsigned x0 = 0;
    if (l || j >= 16) x0 = RFL(ld4(mkrs(args(0).ws, 2u << 20), 0, O_HB));
    if (j < 16) {
        if (l) { until(j ? LN1 + j * LNS : 0); ln0(args(0), wv, j * 4 + xx, [&]() AI { return beat_wait(x0); }); until(DL1); }
        if (DLQ && !l && j < 8) until(DLQ);
        if (j < 8) qk_role(args(0), wv, smem, l, xx * 8 + j); else vu_role(args(0), wv, smem, l, (xx ^ VUX) * 8 + j - 8);
        return;
    }
    if (j >= 16 + CPJ && j < 20 + CPJ) {
        unsigned long long pc; unsigned n;
        asm volatile("s_getpc_b64 %0\n\ts_mov_b32 %1, .-_Z10sys_kernel4Args" : "=s"(pc), "=s"(n));
        const rsrc_t rc = mkrs((const void*)(pc - n), CPF * 1024);
        __builtin_amdgcn_raw_ptr_buffer_load_lds(rc, smem + LDSB - 1024 + wv * 256, 4, lane_id() * 128, ((j - 16 - CPJ) * 4 + wv) * 8192, 0, 0);
    }
    {
        const int lane = lane_id(), q = (j - 16) * 4 + wv, m = SPL && !l ? ((xx & 1) != (SPL & 1)) * 2 + (q >> 5) : q >> 4;
        const Args a = args(0);
        const u16 *W0 = a.Wq, *W1 = a.Wk, *W2 = a.Wv, *W3 = a.Wo;
        asm volatile("" : "+s"(W0), "+s"(W1), "+s"(W2), "+s"(W3));
        const u16* W = m == 0 ? W0 : m == 1 ? W1 : m == 2 ? W2 : W3;
        const rsrc_t rw = mkrs(W, 8u << 20);
        if (l) {
#pragma unroll
            for (int i = 0; i < PFA; ++i) __builtin_amdgcn_raw_ptr_buffer_load_lds(rw, smem + LDSB - 1024 + wv * 256, 4, lane * 256, i * (1u << 20) + (xx * 16 + (q & 15)) * 16384, 0, 0);
        } else {
            const unsigned po = (0x100u - (unsigned)(size_t)W) & 0x300u, hx = SPL ? (xx & 2) + ((q >> 4) & 1) : m < 2 ? xx : xx ^ VUX;
            const unsigned vo = m == 3 ? (lane >> 1) * 1024 + (lane & 1) * 128 : (lane >> 2) * 8192 + ((lane >> 1) & 1) * 1024 + (lane & 1) * 128;
            const unsigned so = po + (m == 3 ? hx * (2u << 20) + (q & 15) * 32768 : hx * 2048 + (q & 15) * 131072);
#pragma unroll
            for (int i = 0; i < PFB; ++i) __builtin_amdgcn_raw_ptr_buffer_load_lds(rw, smem + LDSB - 1024 + wv * 256, 4, vo, so + i * (m == 3 ? 524288 : 2u << 20), 0, 0);
            const unsigned pl = (0x1F00u - (unsigned)(size_t)W) & 0x1F00u;
#pragma unroll
            for (int i = 0; i < PFL; ++i) __builtin_amdgcn_raw_ptr_buffer_load_lds(rw, smem + LDSB - 1024 + wv * 256, 4, (lane >> 1) * 8192 + (lane & 1) * 128, (pl >> 11) == hx && m < 3 ? pl + ((q & 15) + 16 * i) * 262144 : OOB, 0, 0);
        }
    }
    int ro, ix;
    if ((xx & 1) == (PLAN & 1)) { if (j < 24) { ro = 0; ix = (xx >> 1) * 8 + j - 16; } else if (j < 26) { ro = 3; ix = (xx >> 1) * 2 + j - 24; } else { ro = 1; ix = (xx >> 1) * 14 + j - 26; } }
    else if (j < 24) { ro = 1; ix = (xx >> 1) * 14 + j - 10; } else { ro = 2; ix = (xx >> 1) * 8 + j - 24; }
    if (ro == 3 && l) { if (ix == 0) leader(args(0), wv, smem, x0); else if (MPF && wv > 1) mpf(mkrs(args(0).ws, 4u << 20), beat_wait(x0) + SD + (wv & 1) * 16384); return; }
    const unsigned sh = beat_wait(x0);
    if (ro == 0) { rd_role(args(sh), wv, smem, l, ix); return; }
    if (ro == 1) { const Args a = args(sh); until(l ? DLG1 : ((((unsigned)(size_t)a.Wg >> 8) + ix) & 3) == 1 || ((((unsigned)(size_t)a.Wu >> 8) + ix) & 3) == 1 ? DLG - DLS : DLG); WV0; BAR; gg_role(a, wv, smem, l, ix); return; }
    if (ro == 2) { const Args a = args(sh); until((l ? DLD1 : DLD) - (((((unsigned)(size_t)a.Wd >> 8) + (ix >> 1)) & 3) == 1 ? DLS + 200 : 0)); WV0; BAR; dd_role(a, wv, smem, l, ix); return; }
    r_role(args(sh), wv, ix);
}

extern "C" void kernel_launch(void* const* d_in, const int* in_sizes, int n_in, void* d_out, int out_size, void* d_ws, size_t ws_size, hipStream_t stream) {
    (void)in_sizes; (void)n_in; (void)out_size; (void)ws_size;
    Args a;
    a.x = (const u16*)d_in[0]; a.mem0 = (const u16*)d_in[1]; a.Wq = (const u16*)d_in[2]; a.Wk = (const u16*)d_in[3]; a.Wv = (const u16*)d_in[4]; a.Wo = (const u16*)d_in[5];
    a.Wg = (const u16*)d_in[8]; a.Wu = (const u16*)d_in[9]; a.Wd = (const u16*)d_in[10];
    a.ws = (char*)(((uintptr_t)d_ws + (2u << 20) - 1) & ~(uintptr_t)((2u << 20) - 1)); a.out = (u16*)d_out;
    (void)hipFuncSetAttribute((const void*)sys_kernel, hipFuncAttributeMaxDynamicSharedMemorySize, LDSB);
    hipLaunchKernelGGL(sys_kernel, dim3(256), dim3(256), LDSB, stream, a);
}
```

```cpp
#include <hip/hip_runtime.h>
#include <stdint.h>

typedef unsigned short u16;
typedef unsigned long long u64;
typedef __attribute__((ext_vector_type(8))) short bf16x8;
typedef __attribute__((ext_vector_type(4))) short s16x4;
typedef __attribute__((ext_vector_type(4))) float f32x4;
typedef __attribute__((ext_vector_type(16))) float f32x16;
typedef __attribute__((ext_vector_type(2))) float f32x2;
typedef __attribute__((ext_vector_type(4))) unsigned v4u;
typedef __attribute__((ext_vector_type(2))) unsigned v2u;
typedef __amdgpu_buffer_rsrc_t rsrc_t;
typedef __attribute__((address_space(3))) char lds_char;
typedef __attribute__((address_space(3))) bf16x8 l8;
typedef __attribute__((address_space(3))) v4u l4;
typedef __attribute__((address_space(3))) v2u l2;
typedef __attribute__((address_space(3))) unsigned l1;
typedef __attribute__((address_space(3))) f32x4 lf4;
typedef __attribute__((address_space(3))) float lf1;
#define DEVI __device__ __forceinline__
#define AI __attribute__((always_inline))

constexpr int NSEG = 64;
constexpr float ATT_SCALE = 0.08838834764831845f;
constexpr float LOG2E = 1.4426950408889634f;
#define NPRB 4
constexpr unsigned SD = 8192;
DEVI unsigned sd(unsigned o) { return o ^ ((o >> 8) & SD); }
constexpr unsigned F_X0 = 32768;
constexpr unsigned F_X = 4096;
constexpr unsigned F_A = 16384;
constexpr unsigned F_H = F_A + 2048;
constexpr unsigned F_D = F_H + 4096;
constexpr unsigned F_Y0 = 49152;
constexpr unsigned F_ABORT = 6400;
constexpr unsigned O_PM = 65536;
constexpr unsigned O_E1 = 131072;
constexpr unsigned O_PT = 262144;
constexpr unsigned O_UM = 524288;
constexpr unsigned CTRL_END = O_UM + (1u << 20);
constexpr unsigned O_PRB = 0x1F0000, O_HB = 0x1FFF00;
constexpr unsigned O_XN = 4u << 20;
constexpr unsigned O_XR = O_XN + (8u << 20);
constexpr unsigned O_ST = O_XR + (8u << 20);
constexpr unsigned O_H = O_ST + (8u << 20);
constexpr unsigned O_Y = O_H + (36u << 20);
constexpr unsigned WS_NEED = O_Y + (8u << 20);
constexpr unsigned O_PROF = WS_NEED;
constexpr unsigned WS_RS = WS_NEED;
static_assert(CTRL_END <= O_PRB && CTRL_END % 16384 == 0 && O_Y % (8u << 20) == 0, "workspace");
constexpr unsigned OOB = 0x80000000u, OOB2 = 0x90000000u, SENT = 0xFFFFFFFFu;

struct Args { const u16 *x, *mem0, *Wq, *Wk, *Wv, *Wo, *Wg, *Wu, *Wd; char* ws; u16* out; };

typedef __attribute__((ext_vector_type(2))) __bf16 bf16x2_t;
DEVI unsigned pack2(float lo, float hi) { f32x2 v = {lo, hi}; bf16x2_t b = __builtin_convertvector(v, bf16x2_t); return __builtin_bit_cast(unsigned, b); }
DEVI float bflo(unsigned u) { return __uint_as_float(u << 16); }
DEVI float bfhi(unsigned u) { return __uint_as_float(u & 0xffff0000u); }
DEVI float rbf(float x) { return bflo(pack2(x, x)); }
DEVI bf16x8 as8(v4u v) { return __builtin_bit_cast(bf16x8, v); }
DEVI f32x4 mfma16(bf16x8 a, bf16x8 b, f32x4 c) { return __builtin_amdgcn_mfma_f32_16x16x32_bf16(a, b, c, 0, 0, 0); }
DEVI f32x16 mfma32(bf16x8 a, bf16x8 b, f32x16 c) { return __builtin_amdgcn_mfma_f32_32x32x16_bf16(a, b, c, 0, 0, 0); }
constexpr int AUX_SC1 = 16;
DEVI v4u ld16(rsrc_t r, unsigned vo, unsigned so) { return __builtin_amdgcn_raw_buffer_load_b128(r, vo, so, AUX_SC1); }
DEVI unsigned ld4(rsrc_t r, unsigned vo, unsigned so) { return __builtin_amdgcn_raw_buffer_load_b32(r, vo, so, AUX_SC1); }
DEVI void st16(v4u v, rsrc_t r, unsigned vo) { __builtin_amdgcn_raw_buffer_store_b128(v, r, vo, 0, AUX_SC1); }
DEVI void st8(v2u v, rsrc_t r, unsigned vo, unsigned so) { __builtin_amdgcn_raw_buffer_store_b64(v, r, vo, so, AUX_SC1); }
DEVI void st4(unsigned v, rsrc_t r, unsigned vo, unsigned so) { __builtin_amdgcn_raw_buffer_store_b32(v, r, vo, so, AUX_SC1); }
DEVI v2u trd(lds_char* p) { return __builtin_bit_cast(v2u, __builtin_amdgcn_ds_read_tr16_b64_v4i16((__attribute__((address_space(3))) s16x4*)p)); }
template <int O1, int O2> DEVI void tra(v2u& lo, v2u& hi, unsigned a) { asm volatile("ds_read_b64_tr_b16 %0, %2 offset:%3\n\tds_read_b64_tr_b16 %1, %2 offset:%4" : "=&v"(lo), "=&v"(hi) : "v"(a), "n"(O1), "n"(O2) : "memory"); }
DEVI void trw(v2u& a, v2u& b, v2u& c, v2u& d) { asm volatile("s_waitcnt lgkmcnt(0)" : "+v"(a), "+v"(b), "+v"(c), "+v"(d)::"memory"); }
DEVI rsrc_t mkrs(const void* p, unsigned n) { return __builtin_amdgcn_make_buffer_rsrc((void*)p, 0, (int)n, 0x00020000); }
#define CB asm volatile("" ::: "memory")
#define SB __builtin_amdgcn_sched_barrier(0)
#define WL0 asm volatile("s_waitcnt lgkmcnt(0)" ::: "memory")
#define WV0 asm volatile("s_waitcnt vmcnt(0)" ::: "memory")
#define WV(n) asm volatile("s_waitcnt vmcnt(%0)" ::"n"(n) : "memory")
#define BAR do { __builtin_amdgcn_s_barrier(); CB; } while (0)
#define RFL(x) __builtin_amdgcn_readfirstlane(x)
DEVI int lane_id() { int x; asm volatile("v_mbcnt_lo_u32_b32 %0, -1, 0\n\tv_mbcnt_hi_u32_b32 %0, -1, %0" : "=v"(x)); return x; }
template <int I> struct IC { static constexpr int v = I; };
template <int B, int E_, class F> DEVI void sfor(F&& f) { if constexpr (B < E_) { f(IC<B>{}); sfor<B + 1, E_>(f); } }
DEVI float red_x32(float x, bool mx) { unsigned u = __float_as_uint(x); auto r = __builtin_amdgcn_permlane32_swap(u, u, false, false); float a = __uint_as_float(r[0]), b = __uint_as_float(r[1]); return mx ? fmaxf(a, b) : a + b; }
DEVI float red_x16(float x, bool mx) { unsigned u = __float_as_uint(x); auto r = __builtin_amdgcn_permlane16_swap(u, u, false, false); float a = __uint_as_float(r[0]), b = __uint_as_float(r[1]); return mx ? fmaxf(a, b) : a + b; }
DEVI unsigned vmax4(v4u d) { return max(max(d[0], d[1]), max(d[2], d[3])); }
DEVI unsigned nosent(unsigned v) { return min(v, SENT - 1u); }
constexpr unsigned SPIN_CAP = 1u << 18;
DEVI bool spin_bail(unsigned& spins, rsrc_t rs) {
    if (++spins > SPIN_CAP) { st4(0u, rs, 0, F_ABORT); return true; }
    return (spins & 255u) == 0u && RFL(ld4(rs, 0, F_ABORT)) != SENT;
}
#define TSTAMP(idx)
constexpr int LDSB = 160 * 1024;
DEVI void idle(unsigned dl) { const unsigned t0 = (unsigned)__builtin_amdgcn_s_memrealtime(); while ((unsigned)__builtin_amdgcn_s_memrealtime() - t0 < dl) __builtin_amdgcn_s_sleep(8); }
#define D1F 0
#define X1F 0

DEVI float red_rows(float s) {
    s += __uint_as_float(__builtin_amdgcn_update_dpp(0, __float_as_uint(s), 0x128, 0xf, 0xf, false));
    s = red_x16(s, false); return red_x32(s, false);
}
DEVI f32x2 up2(unsigned u) { return f32x2{bflo(u), bfhi(u)}; }
DEVI float ln_inv(float var) { return __builtin_amdgcn_rcpf(rbf(__builtin_amdgcn_sqrtf(rbf(rbf(fmaxf(var, 0.f)) + 1e-5f)))); }
template <class F> DEVI void ln0(const Args& a, const int w, int b, F&& base) {
    const int lane = lane_id(), row = w * 8 + (lane & 7);
    const rsrc_t rx = mkrs(a.x, 2048 * 2048);
    v4u u[16];
#pragma unroll
    for (int i = 0; i < 16; ++i) u[i] = __builtin_amdgcn_raw_buffer_load_b128(rx, row * 2048 + (lane >> 3) * 16 + i * 128, b * 65536, 0);
    const float k0 = __shfl(bflo(u[0][0]), lane & 7);
    f32x2 s1 = {0.f, 0.f}, s2 = {0.f, 0.f};
#pragma unroll
    for (int i = 0; i < 16; ++i)
#pragma unroll
        for (int e = 0; e < 4; ++e) { const f32x2 d = up2(u[i][e]) - k0; s1 += d; s2 += d * d; }
    const float s = red_rows(s1[0] + s1[1]) * (1.0f / 1024.0f), q = red_rows(s2[0] + s2[1]) * (1.0f / 1024.0f);
    float inv = ln_inv(q - s * s), c = -rbf(k0 + s) * inv;
    asm volatile("" : "+v"(inv), "+v"(c));
    const rsrc_t rs = mkrs(a.ws + base(), WS_RS);
    const unsigned o = sd(O_XN + b * 131072) + (lane >> 3) * 512 + row * 16;
#pragma unroll
    for (int i = 0; i < 16; ++i) {
        v4u r;
#pragma unroll
        for (int e = 0; e < 4; ++e) { const f32x2 z = up2(u[i][e]) * inv + c; r[e] = pack2(z[0], z[1]); }
        st16(r, rs, o + (i >> 1) * 16384 + (i & 1) * 4096);
    }
#pragma unroll
    for (int i = 0; i < 16; ++i) st16(u[i], rs, o + (O_XR - O_XN) + (i >> 1) * 16384 + (i & 1) * 4096);
    st16(u[0], rs, sd(O_ST + b * 131072) + w * 1024 + lane * 16); st16(u[0], rs, sd(O_ST + b * 131072) + 4096 + w * 1024 + lane * 16); st16(u[0], rs, sd(O_ST + SD + b * 131072) + w * 1024 + lane * 16);
    WV0;
    st4(b, rs, 0, F_X0 + b * 128 + w * 4);
}
#define RSLP 8
DEVI void r_role(const Args& a, const int w, int rq) {
    const int lane = lane_id(), row = (w >> 1) * 16 + (rq & 1) * 8 + (lane & 7), cq = (rq >> 1) * 2 + (w & 1);
    const rsrc_t rs = mkrs(a.ws, WS_RS);
    const unsigned fo = cq * 32768 + (lane >> 3) * 512 + row * 16, so = O_ST - O_XR + 4096 + (lane >> 3) * 512 + row * 8;
    auto io = [](int i) -> unsigned { return (i >> 1) * 16384 + (i & 1) * 4096; };
    const unsigned fd = F_D + (lane & 15) * 128 + (D1F ? 0 : (row >> 3) * 4);
    for (int b = 0; b < NSEG; ++b) {
        unsigned spins = 0;
        for (;;) {
            CB;
            if (__ballot((int)ld4(rs, fd, 0) < b) == 0ull) break;
            if (spin_bail(spins, rs)) return;
            __builtin_amdgcn_s_sleep(RSLP);
        }
        const unsigned sx = sd(O_XR + b * 131072);
        const v2u s0 = __builtin_amdgcn_raw_buffer_load_b64(rs, so, sx, AUX_SC1), s1 = __builtin_amdgcn_raw_buffer_load_b64(rs, so + 256, sx, AUX_SC1);
        v4u u[4];
#pragma unroll
        for (int i = 0; i < 4; ++i) u[i] = ld16(rs, fo + io(i), sx);
        const float m0 = __uint_as_float(s0[0]), m1 = __uint_as_float(s1[0]), mean = red_rows(m0 + m1) * 0.0625f;
        const float var = red_rows(__uint_as_float(s0[1]) + __uint_as_float(s1[1]) + 64.f * ((m0 - mean) * (m0 - mean) + (m1 - mean) * (m1 - mean))) * (1.0f / 1024.0f);
        const float inv = ln_inv(var), c = -rbf(mean) * inv;
#pragma unroll
        for (int i = 0; i < 4; ++i) {
            v4u o;
#pragma unroll
            for (int e = 0; e < 4; ++e) { const f32x2 z = up2(u[i][e]) * inv + c; o[e] = pack2(z[0], z[1]); }
            st16(o, rs, fo + io(i) + ((sx ^ SD) - (O_XR - O_XN)));
        }
#pragma unroll
        for (int i = 0; i < 4; ++i) st16(u[i], rs, fo + io(i) + (sx ^ SD));
        WV0;
        if (X1F) BAR;
        st4(b, rs, 0, X1F && w ? OOB : F_X + SD + rq * 128 + w * 4);
        if (rq == 0 && w == 0) TSTAMP(200000 + 64 + b);
    }
}
#define QK_ASM \
"v_mbcnt_lo_u32_b32 v255, -1, 0\nv_mbcnt_hi_u32_b32 v255, -1, v255\nv_lshlrev_b32 v255, 2, v255\nv_add_u32 v255, %[tb], v255\nds_read_b32 v0, v255 offset:0\nds_read_b32 v1, v255 offset:256\nds_read_b32 v2, v255 offset:512\nds_read_b32 v3, v255 offset:768\n" \
"ds_read_b32 v4, v255 offset:1024\nds_read_b32 v5, v255 offset:1280\nds_read_b32 v6, v255 offset:1536\nds_read_b32 v7, v255 offset:1792\nds_read_b32 v8, v255 offset:2048\nds_read_b32 v9, v255 offset:2304\nds_read_b32 v10, v255 offset:2560\nds_read_b32 v11, v255 offset:2816\n" \
"ds_read_b32 v12, v255 offset:3072\nds_read_b32 v13, v255 offset:3328\nds_read_b32 v14, v255 offset:3584\nds_read_b32 v15, v255 offset:3840\nds_read_b32 v92, v255 offset:4096\nds_read_b32 v93, v255 offset:4352\nds_read_b32 v94, v255 offset:4608\ns_waitcnt lgkmcnt(0)\n" \
"s_mov_b64 s[36:37], %[ws]\ns_mov_b32 s38, %[sz]\ns_mov_b32 s39, 0x20000\ns_mov_b32 s45, 2096896\nbuffer_load_dword v95, off, s[36:39], s45 sc1\ns_add_u32 m0, %[lw], 16384\ns_add_u32 s44, %[wo], 0\nbuffer_load_dwordx4 v92, %[rw], s44 offen nt lds\n" \
"s_add_u32 m0, %[lw], 17408\ns_add_u32 s44, %[wo], 65536\nbuffer_load_dwordx4 v92, %[rw], s44 offen nt lds\ns_add_u32 m0, %[lw], 18432\ns_add_u32 s44, %[wo], 131072\nbuffer_load_dwordx4 v92, %[rw], s44 offen nt lds\ns_add_u32 m0, %[lw], 19456\ns_add_u32 s44, %[wo], 196608\n" \
"buffer_load_dwordx4 v92, %[rw], s44 offen nt lds\ns_add_u32 m0, %[lw], 20480\ns_add_u32 s44, %[wo], 262144\nbuffer_load_dwordx4 v92, %[rw], s44 offen nt lds\ns_add_u32 m0, %[lw], 21504\ns_add_u32 s44, %[wo], 327680\nbuffer_load_dwordx4 v92, %[rw], s44 offen nt lds\ns_add_u32 m0, %[lw], 22528\n" \
"s_add_u32 s44, %[wo], 393216\nbuffer_load_dwordx4 v92, %[rw], s44 offen nt lds\ns_add_u32 m0, %[lw], 23552\ns_add_u32 s44, %[wo], 458752\nbuffer_load_dwordx4 v92, %[rw], s44 offen nt lds\ns_add_u32 m0, %[tb], 0\ns_add_u32 s44, %[wo], 524288\nbuffer_load_dwordx4 v92, %[rw], s44 offen nt lds\n" \
"s_add_u32 m0, %[tb], 1024\ns_add_u32 s44, %[wo], 589824\nbuffer_load_dwordx4 v92, %[rw], s44 offen nt lds\ns_add_u32 m0, %[tb], 2048\ns_add_u32 s44, %[wo], 655360\nbuffer_load_dwordx4 v92, %[rw], s44 offen nt lds\ns_add_u32 m0, %[tb], 3072\ns_add_u32 s44, %[wo], 720896\n" \
"buffer_load_dwordx4 v92, %[rw], s44 offen nt lds\ns_add_u32 m0, %[tb], 4096\ns_add_u32 s44, %[wo], 786432\nbuffer_load_dwordx4 v92, %[rw], s44 offen nt lds\ns_add_u32 m0, %[tb], 5120\ns_add_u32 s44, %[wo], 851968\nbuffer_load_dwordx4 v92, %[rw], s44 offen nt lds\ns_add_u32 m0, %[tb], 6144\n" \
"s_add_u32 s44, %[wo], 917504\nbuffer_load_dwordx4 v92, %[rw], s44 offen nt lds\ns_add_u32 m0, %[tb], 7168\ns_add_u32 s44, %[wo], 983040\nbuffer_load_dwordx4 v92, %[rw], s44 offen nt lds\ns_add_u32 m0, %[tb], 8192\ns_add_u32 s44, %[wo], 1048576\nbuffer_load_dwordx4 v92, %[rw], s44 offen nt lds\n" \
"s_add_u32 m0, %[tb], 9216\ns_add_u32 s44, %[wo], 1114112\nbuffer_load_dwordx4 v92, %[rw], s44 offen nt lds\ns_add_u32 m0, %[tb], 10240\ns_add_u32 s44, %[wo], 1179648\nbuffer_load_dwordx4 v92, %[rw], s44 offen nt lds\ns_add_u32 m0, %[tb], 11264\ns_add_u32 s44, %[wo], 1245184\n" \
"buffer_load_dwordx4 v92, %[rw], s44 offen nt lds\ns_add_u32 m0, %[lw], 0\ns_add_u32 s44, %[wo], 1310720\nbuffer_load_dwordx4 v92, %[rw], s44 offen nt lds\ns_add_u32 m0, %[lw], 1024\ns_add_u32 s44, %[wo], 1376256\nbuffer_load_dwordx4 v92, %[rw], s44 offen nt lds\ns_add_u32 m0, %[lw], 2048\n" \
"s_add_u32 s44, %[wo], 1441792\nbuffer_load_dwordx4 v92, %[rw], s44 offen nt lds\ns_add_u32 m0, %[lw], 3072\ns_add_u32 s44, %[wo], 1507328\nbuffer_load_dwordx4 v92, %[rw], s44 offen nt lds\ns_add_u32 m0, %[lw], 4096\ns_add_u32 s44, %[wo], 1572864\nbuffer_load_dwordx4 v92, %[rw], s44 offen nt lds\n" \
"s_add_u32 m0, %[lw], 5120\ns_add_u32 s44, %[wo], 1638400\nbuffer_load_dwordx4 v92, %[rw], s44 offen nt lds\ns_add_u32 m0, %[lw], 6144\ns_add_u32 s44, %[wo], 1703936\nbuffer_load_dwordx4 v92, %[rw], s44 offen nt lds\ns_add_u32 m0, %[lw], 7168\ns_add_u32 s44, %[wo], 1769472\n" \
"buffer_load_dwordx4 v92, %[rw], s44 offen nt lds\ns_add_u32 m0, %[lw], 8192\ns_add_u32 s44, %[wo], 1835008\nbuffer_load_dwordx4 v92, %[rw], s44 offen nt lds\ns_add_u32 m0, %[lw], 9216\ns_add_u32 s44, %[wo], 1900544\nbuffer_load_dwordx4 v92, %[rw], s44 offen nt lds\ns_add_u32 m0, %[lw], 10240\n" \
"s_add_u32 s44, %[wo], 1966080\nbuffer_load_dwordx4 v92, %[rw], s44 offen nt lds\ns_add_u32 m0, %[lw], 11264\ns_add_u32 s44, %[wo], 2031616\nbuffer_load_dwordx4 v92, %[rw], s44 offen nt lds\ns_add_u32 m0, %[lw], 12288\ns_add_u32 s44, %[wo], 2097152\nbuffer_load_dwordx4 v92, %[rw], s44 offen nt lds\n" \
"s_add_u32 m0, %[lw], 13312\ns_add_u32 s44, %[wo], 2162688\nbuffer_load_dwordx4 v92, %[rw], s44 offen nt lds\ns_add_u32 m0, %[lw], 14336\ns_add_u32 s44, %[wo], 2228224\nbuffer_load_dwordx4 v92, %[rw], s44 offen nt lds\ns_add_u32 m0, %[lw], 15360\ns_add_u32 s44, %[wo], 2293760\n" \
"buffer_load_dwordx4 v92, %[rw], s44 offen nt lds\ns_waitcnt vmcnt(32)\nds_read_b64_tr_b16 a[0:1], v94 offset:16384\nds_read_b64_tr_b16 a[2:3], v94 offset:16896\nds_read_b64_tr_b16 a[8:9], v94 offset:16392\nds_read_b64_tr_b16 a[10:11], v94 offset:16904\nds_read_b64_tr_b16 a[16:17], v94 offset:16448\nds_read_b64_tr_b16 a[18:19], v94 offset:16960\n" \
"ds_read_b64_tr_b16 a[24:25], v94 offset:16456\nds_read_b64_tr_b16 a[26:27], v94 offset:16968\ns_waitcnt lgkmcnt(0)\ns_add_u32 m0, %[lw], 16384\ns_add_u32 s44, %[wo], 2359296\nbuffer_load_dwordx4 v92, %[rw], s44 offen nt lds\ns_add_u32 m0, %[lw], 17408\ns_add_u32 s44, %[wo], 2424832\n" \
"buffer_load_dwordx4 v92, %[rw], s44 offen nt lds\ns_add_u32 m0, %[lw], 18432\ns_add_u32 s44, %[wo], 2490368\nbuffer_load_dwordx4 v92, %[rw], s44 offen nt lds\ns_add_u32 m0, %[lw], 19456\ns_add_u32 s44, %[wo], 2555904\nbuffer_load_dwordx4 v92, %[rw], s44 offen nt lds\ns_waitcnt vmcnt(32)\n" \
"ds_read_b64_tr_b16 a[4:5], v94 offset:20480\nds_read_b64_tr_b16 a[6:7], v94 offset:20992\nds_read_b64_tr_b16 a[12:13], v94 offset:20488\nds_read_b64_tr_b16 a[14:15], v94 offset:21000\nds_read_b64_tr_b16 a[20:21], v94 offset:20544\nds_read_b64_tr_b16 a[22:23], v94 offset:21056\nds_read_b64_tr_b16 a[28:29], v94 offset:20552\nds_read_b64_tr_b16 a[30:31], v94 offset:21064\n" \
"s_waitcnt lgkmcnt(0)\ns_add_u32 m0, %[lw], 20480\ns_add_u32 s44, %[wo], 2621440\nbuffer_load_dwordx4 v92, %[rw], s44 offen nt lds\ns_add_u32 m0, %[lw], 21504\ns_add_u32 s44, %[wo], 2686976\nbuffer_load_dwordx4 v92, %[rw], s44 offen nt lds\ns_add_u32 m0, %[lw], 22528\n" \
"s_add_u32 s44, %[wo], 2752512\nbuffer_load_dwordx4 v92, %[rw], s44 offen nt lds\ns_add_u32 m0, %[lw], 23552\ns_add_u32 s44, %[wo], 2818048\nbuffer_load_dwordx4 v92, %[rw], s44 offen nt lds\ns_waitcnt vmcnt(32)\nds_read_b64_tr_b16 a[32:33], v93 offset:0\nds_read_b64_tr_b16 a[34:35], v93 offset:512\n" \
"ds_read_b64_tr_b16 v[84:85], v93 offset:8\nds_read_b64_tr_b16 v[86:87], v93 offset:520\nds_read_b64_tr_b16 a[40:41], v93 offset:64\nds_read_b64_tr_b16 a[42:43], v93 offset:576\nds_read_b64_tr_b16 a[48:49], v93 offset:72\nds_read_b64_tr_b16 a[50:51], v93 offset:584\ns_waitcnt lgkmcnt(0)\ns_add_u32 m0, %[tb], 0\n" \
"s_add_u32 s44, %[wo], 2883584\nbuffer_load_dwordx4 v92, %[rw], s44 offen nt lds\ns_add_u32 m0, %[tb], 1024\ns_add_u32 s44, %[wo], 2949120\nbuffer_load_dwordx4 v92, %[rw], s44 offen nt lds\ns_add_u32 m0, %[tb], 2048\ns_add_u32 s44, %[wo], 3014656\nbuffer_load_dwordx4 v92, %[rw], s44 offen nt lds\n" \
"s_add_u32 m0, %[tb], 3072\ns_add_u32 s44, %[wo], 3080192\nbuffer_load_dwordx4 v92, %[rw], s44 offen nt lds\ns_waitcnt vmcnt(32)\nds_read_b64_tr_b16 a[36:37], v93 offset:4096\nds_read_b64_tr_b16 a[38:39], v93 offset:4608\nds_read_b64_tr_b16 v[80:81], v93 offset:4104\nds_read_b64_tr_b16 v[82:83], v93 offset:4616\n" \
"ds_read_b64_tr_b16 a[44:45], v93 offset:4160\nds_read_b64_tr_b16 a[46:47], v93 offset:4672\nds_read_b64_tr_b16 a[52:53], v93 offset:4168\nds_read_b64_tr_b16 a[54:55], v93 offset:4680\ns_waitcnt lgkmcnt(0)\ns_add_u32 m0, %[tb], 4096\ns_add_u32 s44, %[wo], 3145728\nbuffer_load_dwordx4 v92, %[rw], s44 offen nt lds\n" \
"s_add_u32 m0, %[tb], 5120\ns_add_u32 s44, %[wo], 3211264\nbuffer_load_dwordx4 v92, %[rw], s44 offen nt lds\ns_add_u32 m0, %[tb], 6144\ns_add_u32 s44, %[wo], 3276800\nbuffer_load_dwordx4 v92, %[rw], s44 offen nt lds\ns_add_u32 m0, %[tb], 7168\ns_add_u32 s44, %[wo], 3342336\n" \
"buffer_load_dwordx4 v92, %[rw], s44 offen nt lds\ns_waitcnt vmcnt(32)\nds_read_b64_tr_b16 a[56:57], v93 offset:8192\nds_read_b64_tr_b16 a[58:59], v93 offset:8704\nds_read_b64_tr_b16 a[64:65], v93 offset:8200\nds_read_b64_tr_b16 a[66:67], v93 offset:8712\nds_read_b64_tr_b16 v[76:77], v93 offset:8256\nds_read_b64_tr_b16 v[78:79], v93 offset:8768\n" \
"ds_read_b64_tr_b16 a[72:73], v93 offset:8264\nds_read_b64_tr_b16 a[74:75], v93 offset:8776\ns_waitcnt lgkmcnt(0)\ns_add_u32 m0, %[tb], 8192\ns_add_u32 s44, %[wo], 3407872\nbuffer_load_dwordx4 v92, %[rw], s44 offen nt lds\ns_add_u32 m0, %[tb], 9216\ns_add_u32 s44, %[wo], 3473408\n" \
"buffer_load_dwordx4 v92, %[rw], s44 offen nt lds\ns_add_u32 m0, %[tb], 10240\ns_add_u32 s44, %[wo], 3538944\nbuffer_load_dwordx4 v92, %[rw], s44 offen nt lds\ns_add_u32 m0, %[tb], 11264\ns_add_u32 s44, %[wo], 3604480\nbuffer_load_dwordx4 v92, %[rw], s44 offen nt lds\ns_waitcnt vmcnt(32)\n" \
"ds_read_b64_tr_b16 a[60:61], v94 offset:0\nds_read_b64_tr_b16 a[62:63], v94 offset:512\nds_read_b64_tr_b16 a[68:69], v94 offset:8\nds_read_b64_tr_b16 a[70:71], v94 offset:520\nds_read_b64_tr_b16 v[72:73], v94 offset:64\nds_read_b64_tr_b16 v[74:75], v94 offset:576\nds_read_b64_tr_b16 a[76:77], v94 offset:72\nds_read_b64_tr_b16 a[78:79], v94 offset:584\n" \
"s_waitcnt lgkmcnt(0)\ns_add_u32 m0, %[lw], 0\ns_add_u32 s44, %[wo], 3670016\nbuffer_load_dwordx4 v92, %[rw], s44 offen nt lds\ns_add_u32 m0, %[lw], 1024\ns_add_u32 s44, %[wo], 3735552\nbuffer_load_dwordx4 v92, %[rw], s44 offen nt lds\ns_add_u32 m0, %[lw], 2048\n" \
"s_add_u32 s44, %[wo], 3801088\nbuffer_load_dwordx4 v92, %[rw], s44 offen nt lds\ns_add_u32 m0, %[lw], 3072\ns_add_u32 s44, %[wo], 3866624\nbuffer_load_dwordx4 v92, %[rw], s44 offen nt lds\ns_waitcnt vmcnt(32)\nds_read_b64_tr_b16 a[80:81], v94 offset:4096\nds_read_b64_tr_b16 a[82:83], v94 offset:4608\n" \
"ds_read_b64_tr_b16 a[88:89], v94 offset:4104\nds_read_b64_tr_b16 a[90:91], v94 offset:4616\nds_read_b64_tr_b16 a[96:97], v94 offset:4160\nds_read_b64_tr_b16 a[98:99], v94 offset:4672\nds_read_b64_tr_b16 v[68:69], v94 offset:4168\nds_read_b64_tr_b16 v[70:71], v94 offset:4680\ns_waitcnt lgkmcnt(0)\ns_add_u32 m0, %[lw], 4096\n" \
"s_add_u32 s44, %[wo], 3932160\nbuffer_load_dwordx4 v92, %[rw], s44 offen nt lds\ns_add_u32 m0, %[lw], 5120\ns_add_u32 s44, %[wo], 3997696\nbuffer_load_dwordx4 v92, %[rw], s44 offen nt lds\ns_add_u32 m0, %[lw], 6144\ns_add_u32 s44, %[wo], 4063232\nbuffer_load_dwordx4 v92, %[rw], s44 offen nt lds\n" \
"s_add_u32 m0, %[lw], 7168\ns_add_u32 s44, %[wo], 4128768\nbuffer_load_dwordx4 v92, %[rw], s44 offen nt lds\ns_waitcnt vmcnt(32)\nds_read_b64_tr_b16 a[84:85], v94 offset:8192\nds_read_b64_tr_b16 a[86:87], v94 offset:8704\nds_read_b64_tr_b16 a[92:93], v94 offset:8200\nds_read_b64_tr_b16 a[94:95], v94 offset:8712\n" \
"ds_read_b64_tr_b16 a[100:101], v94 offset:8256\nds_read_b64_tr_b16 a[102:103], v94 offset:8768\nds_read_b64_tr_b16 v[64:65], v94 offset:8264\nds_read_b64_tr_b16 v[66:67], v94 offset:8776\ns_waitcnt lgkmcnt(0)\ns_add_u32 m0, %[lw], 8192\ns_add_u32 s44, %[wo], 4194304\nbuffer_load_dwordx4 v92, %[rw], s44 offen nt lds\n" \
"s_add_u32 m0, %[lw], 9216\ns_add_u32 s44, %[wo], 4259840\nbuffer_load_dwordx4 v92, %[rw], s44 offen nt lds\ns_add_u32 m0, %[lw], 10240\ns_add_u32 s44, %[wo], 4325376\nbuffer_load_dwordx4 v92, %[rw], s44 offen nt lds\ns_add_u32 m0, %[lw], 11264\ns_add_u32 s44, %[wo], 4390912\n" \
"buffer_load_dwordx4 v92, %[rw], s44 offen nt lds\ns_waitcnt vmcnt(32)\nds_read_b64_tr_b16 a[104:105], v94 offset:12288\nds_read_b64_tr_b16 a[106:107], v94 offset:12800\nds_read_b64_tr_b16 a[112:113], v94 offset:12296\nds_read_b64_tr_b16 a[114:115], v94 offset:12808\nds_read_b64_tr_b16 a[120:121], v94 offset:12352\nds_read_b64_tr_b16 a[122:123], v94 offset:12864\n" \
"ds_read_b64_tr_b16 a[128:129], v94 offset:12360\nds_read_b64_tr_b16 a[130:131], v94 offset:12872\ns_waitcnt lgkmcnt(0)\ns_add_u32 m0, %[lw], 12288\ns_add_u32 s44, %[wo], 4456448\nbuffer_load_dwordx4 v92, %[rw], s44 offen nt lds\ns_add_u32 m0, %[lw], 13312\ns_add_u32 s44, %[wo], 4521984\n" \
"buffer_load_dwordx4 v92, %[rw], s44 offen nt lds\ns_add_u32 m0, %[lw], 14336\ns_add_u32 s44, %[wo], 4587520\nbuffer_load_dwordx4 v92, %[rw], s44 offen nt lds\ns_add_u32 m0, %[lw], 15360\ns_add_u32 s44, %[wo], 4653056\nbuffer_load_dwordx4 v92, %[rw], s44 offen nt lds\ns_waitcnt vmcnt(32)\n" \
"ds_read_b64_tr_b16 a[108:109], v94 offset:16384\nds_read_b64_tr_b16 a[110:111], v94 offset:16896\nds_read_b64_tr_b16 a[116:117], v94 offset:16392\nds_read_b64_tr_b16 a[118:119], v94 offset:16904\nds_read_b64_tr_b16 a[124:125], v94 offset:16448\nds_read_b64_tr_b16 a[126:127], v94 offset:16960\nds_read_b64_tr_b16 a[132:133], v94 offset:16456\nds_read_b64_tr_b16 a[134:135], v94 offset:16968\n" \
"s_waitcnt lgkmcnt(0)\ns_add_u32 m0, %[lw], 16384\ns_add_u32 s44, %[wo], 4718592\nbuffer_load_dwordx4 v92, %[rw], s44 offen nt lds\ns_add_u32 m0, %[lw], 17408\ns_add_u32 s44, %[wo], 4784128\nbuffer_load_dwordx4 v92, %[rw], s44 offen nt lds\ns_add_u32 m0, %[lw], 18432\n" \
"s_add_u32 s44, %[wo], 4849664\nbuffer_load_dwordx4 v92, %[rw], s44 offen nt lds\ns_add_u32 m0, %[lw], 19456\ns_add_u32 s44, %[wo], 4915200\nbuffer_load_dwordx4 v92, %[rw], s44 offen nt lds\ns_waitcnt vmcnt(32)\nds_read_b64_tr_b16 a[136:137], v94 offset:20480\nds_read_b64_tr_b16 a[138:139], v94 offset:20992\n" \
"ds_read_b64_tr_b16 v[60:61], v94 offset:20488\nds_read_b64_tr_b16 v[62:63], v94 offset:21000\nds_read_b64_tr_b16 a[144:145], v94 offset:20544\nds_read_b64_tr_b16 a[146:147], v94 offset:21056\nds_read_b64_tr_b16 a[152:153], v94 offset:20552\nds_read_b64_tr_b16 a[154:155], v94 offset:21064\ns_waitcnt lgkmcnt(0)\ns_add_u32 m0, %[lw], 20480\n" \
"s_add_u32 s44, %[wo], 4980736\nbuffer_load_dwordx4 v92, %[rw], s44 offen nt lds\ns_add_u32 m0, %[lw], 21504\ns_add_u32 s44, %[wo], 5046272\nbuffer_load_dwordx4 v92, %[rw], s44 offen nt lds\ns_add_u32 m0, %[lw], 22528\ns_add_u32 s44, %[wo], 5111808\nbuffer_load_dwordx4 v92, %[rw], s44 offen nt lds\n" \
"s_add_u32 m0, %[lw], 23552\ns_add_u32 s44, %[wo], 5177344\nbuffer_load_dwordx4 v92, %[rw], s44 offen nt lds\ns_waitcnt vmcnt(32)\nds_read_b64_tr_b16 a[140:141], v93 offset:0\nds_read_b64_tr_b16 a[142:143], v93 offset:512\nds_read_b64_tr_b16 v[56:57], v93 offset:8\nds_read_b64_tr_b16 v[58:59], v93 offset:520\n" \
"ds_read_b64_tr_b16 a[148:149], v93 offset:64\nds_read_b64_tr_b16 a[150:151], v93 offset:576\nds_read_b64_tr_b16 a[156:157], v93 offset:72\nds_read_b64_tr_b16 a[158:159], v93 offset:584\ns_waitcnt lgkmcnt(0)\ns_add_u32 m0, %[tb], 0\ns_add_u32 s44, %[wo], 5242880\nbuffer_load_dwordx4 v92, %[rw], s44 offen nt lds\n" \
"s_add_u32 m0, %[tb], 1024\ns_add_u32 s44, %[wo], 5308416\nbuffer_load_dwordx4 v92, %[rw], s44 offen nt lds\ns_add_u32 m0, %[tb], 2048\ns_add_u32 s44, %[wo], 5373952\nbuffer_load_dwordx4 v92, %[rw], s44 offen nt lds\ns_add_u32 m0, %[tb], 3072\ns_add_u32 s44, %[wo], 5439488\n" \
"buffer_load_dwordx4 v92, %[rw], s44 offen nt lds\ns_waitcnt vmcnt(32)\nds_read_b64_tr_b16 a[160:161], v93 offset:4096\nds_read_b64_tr_b16 a[162:163], v93 offset:4608\nds_read_b64_tr_b16 a[168:169], v93 offset:4104\nds_read_b64_tr_b16 a[170:171], v93 offset:4616\nds_read_b64_tr_b16 v[52:53], v93 offset:4160\nds_read_b64_tr_b16 v[54:55], v93 offset:4672\n" \
"ds_read_b64_tr_b16 a[176:177], v93 offset:4168\nds_read_b64_tr_b16 a[178:179], v93 offset:4680\ns_waitcnt lgkmcnt(0)\ns_add_u32 m0, %[tb], 4096\ns_add_u32 s44, %[wo], 5505024\nbuffer_load_dwordx4 v92, %[rw], s44 offen nt lds\ns_add_u32 m0, %[tb], 5120\ns_add_u32 s44, %[wo], 5570560\n" \
"buffer_load_dwordx4 v92, %[rw], s44 offen nt lds\ns_add_u32 m0, %[tb], 6144\ns_add_u32 s44, %[wo], 5636096\nbuffer_load_dwordx4 v92, %[rw], s44 offen nt lds\ns_add_u32 m0, %[tb], 7168\ns_add_u32 s44, %[wo], 5701632\nbuffer_load_dwordx4 v92, %[rw], s44 offen nt lds\ns_waitcnt vmcnt(32)\n" \
"ds_read_b64_tr_b16 a[164:165], v93 offset:8192\nds_read_b64_tr_b16 a[166:167], v93 offset:8704\nds_read_b64_tr_b16 a[172:173], v93 offset:8200\nds_read_b64_tr_b16 a[174:175], v93 offset:8712\nds_read_b64_tr_b16 v[48:49], v93 offset:8256\nds_read_b64_tr_b16 v[50:51], v93 offset:8768\nds_read_b64_tr_b16 a[180:181], v93 offset:8264\nds_read_b64_tr_b16 a[182:183], v93 offset:8776\n" \
"s_waitcnt lgkmcnt(0)\ns_add_u32 m0, %[tb], 8192\ns_add_u32 s44, %[wo], 5767168\nbuffer_load_dwordx4 v92, %[rw], s44 offen nt lds\ns_add_u32 m0, %[tb], 9216\ns_add_u32 s44, %[wo], 5832704\nbuffer_load_dwordx4 v92, %[rw], s44 offen nt lds\ns_add_u32 m0, %[tb], 10240\n" \
"s_add_u32 s44, %[wo], 5898240\nbuffer_load_dwordx4 v92, %[rw], s44 offen nt lds\ns_add_u32 m0, %[tb], 11264\ns_add_u32 s44, %[wo], 5963776\nbuffer_load_dwordx4 v92, %[rw], s44 offen nt lds\ns_waitcnt vmcnt(32)\nds_read_b64_tr_b16 a[184:185], v94 offset:0\nds_read_b64_tr_b16 a[186:187], v94 offset:512\n" \
"ds_read_b64_tr_b16 a[192:193], v94 offset:8\nds_read_b64_tr_b16 a[194:195], v94 offset:520\nds_read_b64_tr_b16 a[200:201], v94 offset:64\nds_read_b64_tr_b16 a[202:203], v94 offset:576\nds_read_b64_tr_b16 v[44:45], v94 offset:72\nds_read_b64_tr_b16 v[46:47], v94 offset:584\ns_waitcnt lgkmcnt(0)\ns_add_u32 m0, %[lw], 0\n" \
"s_add_u32 s44, %[wo], 6029312\nbuffer_load_dwordx4 v92, %[rw], s44 offen nt lds\ns_add_u32 m0, %[lw], 1024\ns_add_u32 s44, %[wo], 6094848\nbuffer_load_dwordx4 v92, %[rw], s44 offen nt lds\ns_add_u32 m0, %[lw], 2048\ns_add_u32 s44, %[wo], 6160384\nbuffer_load_dwordx4 v92, %[rw], s44 offen nt lds\n" \
"s_add_u32 m0, %[lw], 3072\ns_add_u32 s44, %[wo], 6225920\nbuffer_load_dwordx4 v92, %[rw], s44 offen nt lds\ns_waitcnt vmcnt(32)\nds_read_b64_tr_b16 a[188:189], v94 offset:4096\nds_read_b64_tr_b16 a[190:191], v94 offset:4608\nds_read_b64_tr_b16 a[196:197], v94 offset:4104\nds_read_b64_tr_b16 a[198:199], v94 offset:4616\n" \
"ds_read_b64_tr_b16 a[204:205], v94 offset:4160\nds_read_b64_tr_b16 a[206:207], v94 offset:4672\nds_read_b64_tr_b16 v[40:41], v94 offset:4168\nds_read_b64_tr_b16 v[42:43], v94 offset:4680\ns_waitcnt lgkmcnt(0)\ns_add_u32 m0, %[lw], 4096\ns_add_u32 s44, %[wo], 6291456\nbuffer_load_dwordx4 v92, %[rw], s44 offen nt lds\n" \
"s_add_u32 m0, %[lw], 5120\ns_add_u32 s44, %[wo], 6356992\nbuffer_load_dwordx4 v92, %[rw], s44 offen nt lds\ns_add_u32 m0, %[lw], 6144\ns_add_u32 s44, %[wo], 6422528\nbuffer_load_dwordx4 v92, %[rw], s44 offen nt lds\ns_add_u32 m0, %[lw], 7168\ns_add_u32 s44, %[wo], 6488064\n" \
"buffer_load_dwordx4 v92, %[rw], s44 offen nt lds\ns_waitcnt vmcnt(32)\nds_read_b64_tr_b16 a[208:209], v94 offset:8192\nds_read_b64_tr_b16 a[210:211], v94 offset:8704\nds_read_b64_tr_b16 a[216:217], v94 offset:8200\nds_read_b64_tr_b16 a[218:219], v94 offset:8712\nds_read_b64_tr_b16 a[224:225], v94 offset:8256\nds_read_b64_tr_b16 a[226:227], v94 offset:8768\n" \
"ds_read_b64_tr_b16 a[232:233], v94 offset:8264\nds_read_b64_tr_b16 a[234:235], v94 offset:8776\ns_waitcnt lgkmcnt(0)\ns_add_u32 m0, %[lw], 8192\ns_add_u32 s44, %[wo], 6553600\nbuffer_load_dwordx4 v92, %[rw], s44 offen nt lds\ns_add_u32 m0, %[lw], 9216\ns_add_u32 s44, %[wo], 6619136\n" \
"buffer_load_dwordx4 v92, %[rw], s44 offen nt lds\ns_add_u32 m0, %[lw], 10240\ns_add_u32 s44, %[wo], 6684672\nbuffer_load_dwordx4 v92, %[rw], s44 offen nt lds\ns_add_u32 m0, %[lw], 11264\ns_add_u32 s44, %[wo], 6750208\nbuffer_load_dwordx4 v92, %[rw], s44 offen nt lds\ns_waitcnt vmcnt(32)\n" \
"ds_read_b64_tr_b16 a[212:213], v94 offset:12288\nds_read_b64_tr_b16 a[214:215], v94 offset:12800\nds_read_b64_tr_b16 a[220:221], v94 offset:12296\nds_read_b64_tr_b16 a[222:223], v94 offset:12808\nds_read_b64_tr_b16 a[228:229], v94 offset:12352\nds_read_b64_tr_b16 a[230:231], v94 offset:12864\nds_read_b64_tr_b16 a[236:237], v94 offset:12360\nds_read_b64_tr_b16 a[238:239], v94 offset:12872\n" \
"s_waitcnt lgkmcnt(0)\ns_add_u32 m0, %[lw], 12288\ns_add_u32 s44, %[wo], 6815744\nbuffer_load_dwordx4 v92, %[rw], s44 offen nt lds\ns_add_u32 m0, %[lw], 13312\ns_add_u32 s44, %[wo], 6881280\nbuffer_load_dwordx4 v92, %[rw], s44 offen nt lds\ns_add_u32 m0, %[lw], 14336\n" \
"s_add_u32 s44, %[wo], 6946816\nbuffer_load_dwordx4 v92, %[rw], s44 offen nt lds\ns_add_u32 m0, %[lw], 15360\ns_add_u32 s44, %[wo], 7012352\nbuffer_load_dwordx4 v92, %[rw], s44 offen nt lds\ns_waitcnt vmcnt(32)\nds_read_b64_tr_b16 a[240:241], v94 offset:16384\nds_read_b64_tr_b16 a[242:243], v94 offset:16896\n" \
"ds_read_b64_tr_b16 v[36:37], v94 offset:16392\nds_read_b64_tr_b16 v[38:39], v94 offset:16904\nds_read_b64_tr_b16 a[248:249], v94 offset:16448\nds_read_b64_tr_b16 a[250:251], v94 offset:16960\nds_read_b64_tr_b16 v[96:97], v94 offset:16456\nds_read_b64_tr_b16 v[98:99], v94 offset:16968\ns_waitcnt lgkmcnt(0)\ns_add_u32 m0, %[lw], 16384\n" \
"s_add_u32 s44, %[wo], 7077888\nbuffer_load_dwordx4 v92, %[rw], s44 offen nt lds\ns_add_u32 m0, %[lw], 17408\ns_add_u32 s44, %[wo], 7143424\nbuffer_load_dwordx4 v92, %[rw], s44 offen nt lds\ns_add_u32 m0, %[lw], 18432\ns_add_u32 s44, %[wo], 7208960\nbuffer_load_dwordx4 v92, %[rw], s44 offen nt lds\n" \
"s_add_u32 m0, %[lw], 19456\ns_add_u32 s44, %[wo], 7274496\nbuffer_load_dwordx4 v92, %[rw], s44 offen nt lds\ns_waitcnt vmcnt(32)\nds_read_b64_tr_b16 a[244:245], v94 offset:20480\nds_read_b64_tr_b16 a[246:247], v94 offset:20992\nds_read_b64_tr_b16 v[32:33], v94 offset:20488\nds_read_b64_tr_b16 v[34:35], v94 offset:21000\n" \
"ds_read_b64_tr_b16 a[252:253], v94 offset:20544\nds_read_b64_tr_b16 a[254:255], v94 offset:21056\nds_read_b64_tr_b16 v[100:101], v94 offset:20552\nds_read_b64_tr_b16 v[102:103], v94 offset:21064\ns_waitcnt lgkmcnt(0)\ns_add_u32 m0, %[lw], 20480\ns_add_u32 s44, %[wo], 7340032\nbuffer_load_dwordx4 v92, %[rw], s44 offen nt lds\n" \
"s_add_u32 m0, %[lw], 21504\ns_add_u32 s44, %[wo], 7405568\nbuffer_load_dwordx4 v92, %[rw], s44 offen nt lds\ns_add_u32 m0, %[lw], 22528\ns_add_u32 s44, %[wo], 7471104\nbuffer_load_dwordx4 v92, %[rw], s44 offen nt lds\ns_add_u32 m0, %[lw], 23552\ns_add_u32 s44, %[wo], 7536640\n" \
"buffer_load_dwordx4 v92, %[rw], s44 offen nt lds\ns_waitcnt vmcnt(32)\nds_read_b64_tr_b16 v[104:105], v93 offset:0\nds_read_b64_tr_b16 v[106:107], v93 offset:512\nds_read_b64_tr_b16 v[112:113], v93 offset:8\nds_read_b64_tr_b16 v[114:115], v93 offset:520\nds_read_b64_tr_b16 v[28:29], v93 offset:64\nds_read_b64_tr_b16 v[30:31], v93 offset:576\n" \
"ds_read_b64_tr_b16 v[120:121], v93 offset:72\nds_read_b64_tr_b16 v[122:123], v93 offset:584\ns_waitcnt lgkmcnt(0)\ns_add_u32 m0, %[tb], 0\ns_add_u32 s44, %[wo], 7602176\nbuffer_load_dwordx4 v92, %[rw], s44 offen nt lds\ns_add_u32 m0, %[tb], 1024\ns_add_u32 s44, %[wo], 7667712\n" \
"buffer_load_dwordx4 v92, %[rw], s44 offen nt lds\ns_add_u32 m0, %[tb], 2048\ns_add_u32 s44, %[wo], 7733248\nbuffer_load_dwordx4 v92, %[rw], s44 offen nt lds\ns_add_u32 m0, %[tb], 3072\ns_add_u32 s44, %[wo], 7798784\nbuffer_load_dwordx4 v92, %[rw], s44 offen nt lds\ns_waitcnt vmcnt(32)\n" \
"ds_read_b64_tr_b16 v[108:109], v93 offset:4096\nds_read_b64_tr_b16 v[110:111], v93 offset:4608\nds_read_b64_tr_b16 v[116:117], v93 offset:4104\nds_read_b64_tr_b16 v[118:119], v93 offset:4616\nds_read_b64_tr_b16 v[24:25], v93 offset:4160\nds_read_b64_tr_b16 v[26:27], v93 offset:4672\nds_read_b64_tr_b16 v[124:125], v93 offset:4168\nds_read_b64_tr_b16 v[126:127], v93 offset:4680\n" \
"s_waitcnt lgkmcnt(0)\nbuffer_load_dwordx2 v[88:89], off, s[36:39], s45 sc1\ns_add_u32 m0, %[tb], 4096\ns_add_u32 s44, %[wo], 7864320\nbuffer_load_dwordx4 v92, %[rw], s44 offen nt lds\ns_add_u32 m0, %[tb], 5120\ns_add_u32 s44, %[wo], 7929856\nbuffer_load_dwordx4 v92, %[rw], s44 offen nt lds\n" \
"s_add_u32 m0, %[tb], 6144\ns_add_u32 s44, %[wo], 7995392\nbuffer_load_dwordx4 v92, %[rw], s44 offen nt lds\ns_add_u32 m0, %[tb], 7168\ns_add_u32 s44, %[wo], 8060928\nbuffer_load_dwordx4 v92, %[rw], s44 offen nt lds\ns_waitcnt vmcnt(33)\nds_read_b64_tr_b16 v[128:129], v93 offset:8192\n" \
"ds_read_b64_tr_b16 v[130:131], v93 offset:8704\nds_read_b64_tr_b16 v[136:137], v93 offset:8200\nds_read_b64_tr_b16 v[138:139], v93 offset:8712\nds_read_b64_tr_b16 v[144:145], v93 offset:8256\nds_read_b64_tr_b16 v[146:147], v93 offset:8768\nds_read_b64_tr_b16 v[20:21], v93 offset:8264\nds_read_b64_tr_b16 v[22:23], v93 offset:8776\ns_waitcnt lgkmcnt(0)\n" \
"s_add_u32 m0, %[tb], 8192\ns_add_u32 s44, %[wo], 8126464\nbuffer_load_dwordx4 v92, %[rw], s44 offen nt lds\ns_add_u32 m0, %[tb], 9216\ns_add_u32 s44, %[wo], 8192000\nbuffer_load_dwordx4 v92, %[rw], s44 offen nt lds\ns_add_u32 m0, %[tb], 10240\ns_add_u32 s44, %[wo], 8257536\n" \
"buffer_load_dwordx4 v92, %[rw], s44 offen nt lds\ns_add_u32 m0, %[tb], 11264\ns_add_u32 s44, %[wo], 8323072\nbuffer_load_dwordx4 v92, %[rw], s44 offen nt lds\ns_waitcnt vmcnt(33)\nds_read_b64_tr_b16 v[132:133], v94 offset:0\nds_read_b64_tr_b16 v[134:135], v94 offset:512\nds_read_b64_tr_b16 v[140:141], v94 offset:8\n" \
"ds_read_b64_tr_b16 v[142:143], v94 offset:520\nds_read_b64_tr_b16 v[148:149], v94 offset:64\nds_read_b64_tr_b16 v[150:151], v94 offset:576\nds_read_b64_tr_b16 v[16:17], v94 offset:72\nds_read_b64_tr_b16 v[18:19], v94 offset:584\ns_waitcnt lgkmcnt(0)\nds_write_b128 v1, v[84:87] offset:0\nds_write_b128 v1, v[80:83] offset:1024\n" \
"ds_write_b128 v1, v[76:79] offset:2048\nds_write_b128 v1, v[72:75] offset:3072\ns_waitcnt vmcnt(29)\nds_read_b64_tr_b16 v[152:153], v94 offset:4096\nds_read_b64_tr_b16 v[154:155], v94 offset:4608\nds_read_b64_tr_b16 v[160:161], v94 offset:4104\nds_read_b64_tr_b16 v[162:163], v94 offset:4616\nds_read_b64_tr_b16 v[168:169], v94 offset:4160\n" \
"ds_read_b64_tr_b16 v[170:171], v94 offset:4672\nds_read_b64_tr_b16 v[176:177], v94 offset:4168\nds_read_b64_tr_b16 v[178:179], v94 offset:4680\ns_waitcnt lgkmcnt(0)\nds_write_b128 v1, v[68:71] offset:4096\nds_write_b128 v1, v[64:67] offset:5120\nds_write_b128 v1, v[60:63] offset:6144\nds_write_b128 v1, v[56:59] offset:7168\n" \
"s_waitcnt vmcnt(25)\nds_read_b64_tr_b16 v[156:157], v94 offset:8192\nds_read_b64_tr_b16 v[158:159], v94 offset:8704\nds_read_b64_tr_b16 v[164:165], v94 offset:8200\nds_read_b64_tr_b16 v[166:167], v94 offset:8712\nds_read_b64_tr_b16 v[172:173], v94 offset:8256\nds_read_b64_tr_b16 v[174:175], v94 offset:8768\nds_read_b64_tr_b16 v[180:181], v94 offset:8264\n" \
"ds_read_b64_tr_b16 v[182:183], v94 offset:8776\ns_waitcnt lgkmcnt(0)\nds_write_b128 v1, v[52:55] offset:8192\nds_write_b128 v1, v[48:51] offset:9216\nds_write_b128 v1, v[44:47] offset:10240\nds_write_b128 v1, v[40:43] offset:11264\ns_waitcnt vmcnt(21)\nds_read_b64_tr_b16 v[184:185], v94 offset:12288\n" \
"ds_read_b64_tr_b16 v[186:187], v94 offset:12800\nds_read_b64_tr_b16 v[40:41], v94 offset:12296\nds_read_b64_tr_b16 v[42:43], v94 offset:12808\nds_read_b64_tr_b16 v[192:193], v94 offset:12352\nds_read_b64_tr_b16 v[194:195], v94 offset:12864\nds_read_b64_tr_b16 v[200:201], v94 offset:12360\nds_read_b64_tr_b16 v[202:203], v94 offset:12872\ns_waitcnt lgkmcnt(0)\n" \
"ds_write_b128 v1, v[36:39] offset:12288\nds_write_b128 v1, v[32:35] offset:13312\nds_write_b128 v1, v[28:31] offset:14336\nds_write_b128 v1, v[24:27] offset:15360\ns_waitcnt vmcnt(17)\nds_read_b64_tr_b16 v[188:189], v94 offset:16384\nds_read_b64_tr_b16 v[190:191], v94 offset:16896\nds_read_b64_tr_b16 v[24:25], v94 offset:16392\n" \
"ds_read_b64_tr_b16 v[26:27], v94 offset:16904\nds_read_b64_tr_b16 v[196:197], v94 offset:16448\nds_read_b64_tr_b16 v[198:199], v94 offset:16960\nds_read_b64_tr_b16 v[204:205], v94 offset:16456\nds_read_b64_tr_b16 v[206:207], v94 offset:16968\ns_waitcnt lgkmcnt(0)\nds_write_b128 v1, v[20:23] offset:16384\nds_write_b128 v1, v[16:19] offset:17408\n" \
"ds_write_b128 v1, v[40:43] offset:18432\nds_write_b128 v1, v[24:27] offset:19456\ns_waitcnt vmcnt(13)\nds_read_b64_tr_b16 v[208:209], v94 offset:20480\nds_read_b64_tr_b16 v[210:211], v94 offset:20992\nds_read_b64_tr_b16 v[216:217], v94 offset:20488\nds_read_b64_tr_b16 v[218:219], v94 offset:21000\nds_read_b64_tr_b16 v[24:25], v94 offset:20544\n" \
"ds_read_b64_tr_b16 v[26:27], v94 offset:21056\nds_read_b64_tr_b16 v[224:225], v94 offset:20552\nds_read_b64_tr_b16 v[226:227], v94 offset:21064\ns_waitcnt lgkmcnt(0)\nds_write_b128 v1, v[24:27] offset:20480\ns_waitcnt vmcnt(9)\nds_read_b64_tr_b16 v[212:213], v93 offset:0\nds_read_b64_tr_b16 v[214:215], v93 offset:512\n" \
"ds_read_b64_tr_b16 v[220:221], v93 offset:8\nds_read_b64_tr_b16 v[222:223], v93 offset:520\nds_read_b64_tr_b16 v[24:25], v93 offset:64\nds_read_b64_tr_b16 v[26:27], v93 offset:576\nds_read_b64_tr_b16 v[228:229], v93 offset:72\nds_read_b64_tr_b16 v[230:231], v93 offset:584\ns_waitcnt lgkmcnt(0)\nds_write_b128 v1, v[24:27] offset:21504\n" \
"s_waitcnt vmcnt(4)\nds_read_b64_tr_b16 v[232:233], v93 offset:4096\nds_read_b64_tr_b16 v[234:235], v93 offset:4608\nds_read_b64_tr_b16 v[240:241], v93 offset:4104\nds_read_b64_tr_b16 v[242:243], v93 offset:4616\nds_read_b64_tr_b16 v[248:249], v93 offset:4160\nds_read_b64_tr_b16 v[250:251], v93 offset:4672\nds_read_b64_tr_b16 v[24:25], v93 offset:4168\n" \
"ds_read_b64_tr_b16 v[26:27], v93 offset:4680\ns_waitcnt lgkmcnt(0)\nds_write_b128 v1, v[24:27] offset:22528\ns_waitcnt vmcnt(0)\nds_read_b64_tr_b16 v[236:237], v93 offset:8192\nds_read_b64_tr_b16 v[238:239], v93 offset:8704\nds_read_b64_tr_b16 v[244:245], v93 offset:8200\nds_read_b64_tr_b16 v[246:247], v93 offset:8712\n" \
"ds_read_b64_tr_b16 v[252:253], v93 offset:8256\nds_read_b64_tr_b16 v[254:255], v93 offset:8768\nds_read_b64_tr_b16 v[24:25], v93 offset:8264\nds_read_b64_tr_b16 v[26:27], v93 offset:8776\ns_waitcnt lgkmcnt(0)\ns_mov_b32 s43, 0\ns_mov_b32 s86, 110\ns_mov_b32 s89, 0\n" \
".Lqk_hb%=:\nv_cmp_ne_u32 vcc, v95, v88\ns_cbranch_vccnz .Lqk_hb1%=\ns_sleep 4\nbuffer_load_dwordx2 v[88:89], off, s[36:39], s45 sc1\ns_waitcnt vmcnt(0)\ns_add_u32 s43, s43, 1\ns_cmp_lt_u32 s43, 262144\n" \
"s_cbranch_scc1 .Lqk_hb%=\ns_branch .Lqk_abort%=\n.Lqk_hb1%=:\nv_readfirstlane_b32 s44, v89\ns_and_b32 s44, s44, 0x200000\ns_add_u32 s36, s36, s44\ns_addc_u32 s37, s37, 0\nds_write_b128 v1, v[24:27] offset:23552\n" \
"s_waitcnt lgkmcnt(0)\ns_barrier\ns_mov_b32 s40, -1\ns_mov_b32 s43, 0\ns_mov_b32 s61, 0x7fff7fff\ns_mov_b32 s58, 0x3e0293ee\ns_mov_b32 s59, 0xbe0293ee\ns_lshl_b32 s57, %[w], 12\n" \
"s_mov_b32 s62, 0xff00ff00\ns_mov_b32 s63, 0xff00ff00\ns_mov_b32 s56, 0\ns_cmp_eq_u32 %[w], 3\ns_cbranch_scc1 .Lqk_fetch0%=\n.Lqk_pwait%=:\ns_barrier\nds_read_b128 v[80:83], v2 offset:0\n" \
"ds_read_b128 v[84:87], v2 offset:256\nds_read_b128 v[88:91], v2 offset:2048\n.Lqk_proj%=:\ns_waitcnt lgkmcnt(2)\nv_mfma_f32_16x16x32_bf16 v[16:19], a[0:3], v[80:83], 0\nds_read_b128 v[92:95], v2 offset:2304\nv_mfma_f32_16x16x32_bf16 v[20:23], a[8:11], v[80:83], 0\nds_read_b128 v[72:75], v1 offset:0\n" \
"v_mfma_f32_16x16x32_bf16 v[24:27], a[16:19], v[80:83], 0\nv_mfma_f32_16x16x32_bf16 v[28:31], a[24:27], v[80:83], 0\ns_waitcnt lgkmcnt(3)\nv_mfma_f32_16x16x32_bf16 v[32:35], a[0:3], v[84:87], 0\nds_read_b128 v[80:83], v2 offset:4096\nv_mfma_f32_16x16x32_bf16 v[36:39], a[8:11], v[84:87], 0\nv_mfma_f32_16x16x32_bf16 v[40:43], a[16:19], v[84:87], 0\nv_mfma_f32_16x16x32_bf16 v[44:47], a[24:27], v[84:87], 0\n" \
"s_waitcnt lgkmcnt(3)\nv_mfma_f32_16x16x32_bf16 v[16:19], a[4:7], v[88:91], v[16:19]\nds_read_b128 v[84:87], v2 offset:4352\nv_mfma_f32_16x16x32_bf16 v[20:23], a[12:15], v[88:91], v[20:23]\nds_read_b128 v[76:79], v1 offset:1024\nv_mfma_f32_16x16x32_bf16 v[24:27], a[20:23], v[88:91], v[24:27]\nv_mfma_f32_16x16x32_bf16 v[28:31], a[28:31], v[88:91], v[28:31]\ns_waitcnt lgkmcnt(4)\n" \
"v_mfma_f32_16x16x32_bf16 v[32:35], a[4:7], v[92:95], v[32:35]\nds_read_b128 v[88:91], v2 offset:6144\nv_mfma_f32_16x16x32_bf16 v[36:39], a[12:15], v[92:95], v[36:39]\nv_mfma_f32_16x16x32_bf16 v[40:43], a[20:23], v[92:95], v[40:43]\nv_mfma_f32_16x16x32_bf16 v[44:47], a[28:31], v[92:95], v[44:47]\ns_waitcnt lgkmcnt(3)\nv_mfma_f32_16x16x32_bf16 v[16:19], a[32:35], v[80:83], v[16:19]\nds_read_b128 v[92:95], v2 offset:6400\n" \
"v_mfma_f32_16x16x32_bf16 v[20:23], v[72:75], v[80:83], v[20:23]\nds_read_b128 v[64:67], v1 offset:2048\nv_mfma_f32_16x16x32_bf16 v[24:27], a[40:43], v[80:83], v[24:27]\nv_mfma_f32_16x16x32_bf16 v[28:31], a[48:51], v[80:83], v[28:31]\ns_waitcnt lgkmcnt(4)\nv_mfma_f32_16x16x32_bf16 v[32:35], a[32:35], v[84:87], v[32:35]\nds_read_b128 v[80:83], v2 offset:8192\nv_mfma_f32_16x16x32_bf16 v[36:39], v[72:75], v[84:87], v[36:39]\n" \
"v_mfma_f32_16x16x32_bf16 v[40:43], a[40:43], v[84:87], v[40:43]\nv_mfma_f32_16x16x32_bf16 v[44:47], a[48:51], v[84:87], v[44:47]\ns_waitcnt lgkmcnt(3)\nv_mfma_f32_16x16x32_bf16 v[16:19], a[36:39], v[88:91], v[16:19]\nds_read_b128 v[84:87], v2 offset:8448\nv_mfma_f32_16x16x32_bf16 v[20:23], v[76:79], v[88:91], v[20:23]\nds_read_b128 v[68:71], v1 offset:3072\nv_mfma_f32_16x16x32_bf16 v[24:27], a[44:47], v[88:91], v[24:27]\n" \
"v_mfma_f32_16x16x32_bf16 v[28:31], a[52:55], v[88:91], v[28:31]\ns_waitcnt lgkmcnt(4)\nv_mfma_f32_16x16x32_bf16 v[32:35], a[36:39], v[92:95], v[32:35]\nds_read_b128 v[88:91], v2 offset:10240\nv_mfma_f32_16x16x32_bf16 v[36:39], v[76:79], v[92:95], v[36:39]\nv_mfma_f32_16x16x32_bf16 v[40:43], a[44:47], v[92:95], v[40:43]\nv_mfma_f32_16x16x32_bf16 v[44:47], a[52:55], v[92:95], v[44:47]\ns_waitcnt lgkmcnt(3)\n" \
"v_mfma_f32_16x16x32_bf16 v[16:19], a[56:59], v[80:83], v[16:19]\nds_read_b128 v[92:95], v2 offset:10496\nv_mfma_f32_16x16x32_bf16 v[20:23], a[64:67], v[80:83], v[20:23]\nds_read_b128 v[72:75], v1 offset:4096\nv_mfma_f32_16x16x32_bf16 v[24:27], v[64:67], v[80:83], v[24:27]\nv_mfma_f32_16x16x32_bf16 v[28:31], a[72:75], v[80:83], v[28:31]\ns_waitcnt lgkmcnt(4)\nv_mfma_f32_16x16x32_bf16 v[32:35], a[56:59], v[84:87], v[32:35]\n" \
"ds_read_b128 v[80:83], v2 offset:12288\nv_mfma_f32_16x16x32_bf16 v[36:39], a[64:67], v[84:87], v[36:39]\nv_mfma_f32_16x16x32_bf16 v[40:43], v[64:67], v[84:87], v[40:43]\nv_mfma_f32_16x16x32_bf16 v[44:47], a[72:75], v[84:87], v[44:47]\ns_waitcnt lgkmcnt(3)\nv_mfma_f32_16x16x32_bf16 v[16:19], a[60:63], v[88:91], v[16:19]\nds_read_b128 v[84:87], v2 offset:12544\nv_mfma_f32_16x16x32_bf16 v[20:23], a[68:71], v[88:91], v[20:23]\n" \
"ds_read_b128 v[76:79], v1 offset:5120\nv_mfma_f32_16x16x32_bf16 v[24:27], v[68:71], v[88:91], v[24:27]\nv_mfma_f32_16x16x32_bf16 v[28:31], a[76:79], v[88:91], v[28:31]\ns_waitcnt lgkmcnt(4)\nv_mfma_f32_16x16x32_bf16 v[32:35], a[60:63], v[92:95], v[32:35]\nds_read_b128 v[88:91], v2 offset:14336\nv_mfma_f32_16x16x32_bf16 v[36:39], a[68:71], v[92:95], v[36:39]\nv_mfma_f32_16x16x32_bf16 v[40:43], v[68:71], v[92:95], v[40:43]\n" \
"v_mfma_f32_16x16x32_bf16 v[44:47], a[76:79], v[92:95], v[44:47]\n.Lqk_pj%=:\ns_waitcnt lgkmcnt(3)\nv_mfma_f32_16x16x32_bf16 v[16:19], a[80:83], v[80:83], v[16:19]\nds_read_b128 v[92:95], v2 offset:14592\nv_mfma_f32_16x16x32_bf16 v[20:23], a[88:91], v[80:83], v[20:23]\ns_add_u32 s40, s40, 1\nv_mfma_f32_16x16x32_bf16 v[24:27], a[96:99], v[80:83], v[24:27]\n" \
"s_lshl_b32 s44, s40, 17\nv_mfma_f32_16x16x32_bf16 v[28:31], v[72:75], v[80:83], v[28:31]\ns_add_u32 s44, s44, %[xn]\ns_waitcnt lgkmcnt(3)\nv_mfma_f32_16x16x32_bf16 v[32:35], a[80:83], v[84:87], v[32:35]\ns_barrier\nds_read_b128 v[80:83], v2 offset:16384\nv_mfma_f32_16x16x32_bf16 v[36:39], a[88:91], v[84:87], v[36:39]\n" \
"s_lshr_b32 s45, s44, 8\nv_mfma_f32_16x16x32_bf16 v[40:43], a[96:99], v[84:87], v[40:43]\ns_and_b32 s45, s45, 0x2000\nv_mfma_f32_16x16x32_bf16 v[44:47], v[72:75], v[84:87], v[44:47]\ns_xor_b32 s41, s44, s45\ns_waitcnt lgkmcnt(2)\nv_mfma_f32_16x16x32_bf16 v[16:19], a[84:87], v[88:91], v[16:19]\nds_read_b128 v[84:87], v2 offset:16640\n" \
"v_mfma_f32_16x16x32_bf16 v[20:23], a[92:95], v[88:91], v[20:23]\ns_add_u32 s41, s41, %[xw]\ns_and_b32 s42, s40, 3\ns_add_u32 m0, s57, 32768\nv_mfma_f32_16x16x32_bf16 v[24:27], a[100:103], v[88:91], v[24:27]\ns_add_u32 s60, s41, 65536\nbuffer_load_dwordx4 v0, s[36:39], s60 offen offset:0 sc1 lds\nv_mfma_f32_16x16x32_bf16 v[28:31], v[76:79], v[88:91], v[28:31]\n" \
"s_lshl_b32 s42, s42, 15\ns_waitcnt lgkmcnt(2)\nv_mfma_f32_16x16x32_bf16 v[32:35], a[84:87], v[92:95], v[32:35]\nds_read_b128 v[88:91], v2 offset:18432\nv_mfma_f32_16x16x32_bf16 v[36:39], a[92:95], v[92:95], v[36:39]\ns_and_b32 s44, s40, 1\nv_mfma_f32_16x16x32_bf16 v[40:43], a[100:103], v[92:95], v[40:43]\ns_lshl_b32 s55, s44, 17\n" \
"buffer_load_dwordx4 v0, s[36:39], s60 offen offset:1024 sc1 lds\nv_mfma_f32_16x16x32_bf16 v[44:47], v[76:79], v[92:95], v[44:47]\ns_add_u32 s56, s56, %[fs]\ns_waitcnt lgkmcnt(2)\nv_mfma_f32_16x16x32_bf16 v[16:19], a[104:107], v[80:83], v[16:19]\nds_read_b128 v[92:95], v2 offset:18688\nv_mfma_f32_16x16x32_bf16 v[20:23], a[112:115], v[80:83], v[20:23]\nds_read_b128 v[72:75], v1 offset:6144\n" \
"v_mfma_f32_16x16x32_bf16 v[24:27], a[120:123], v[80:83], v[24:27]\nbuffer_load_dwordx4 v0, s[36:39], s60 offen offset:2048 sc1 lds\nv_mfma_f32_16x16x32_bf16 v[28:31], a[128:131], v[80:83], v[28:31]\ns_waitcnt lgkmcnt(3)\nv_mfma_f32_16x16x32_bf16 v[32:35], a[104:107], v[84:87], v[32:35]\nds_read_b128 v[80:83], v2 offset:20480\nv_mfma_f32_16x16x32_bf16 v[36:39], a[112:115], v[84:87], v[36:39]\nv_mfma_f32_16x16x32_bf16 v[40:43], a[120:123], v[84:87], v[40:43]\n" \
"buffer_load_dwordx4 v0, s[36:39], s60 offen offset:3072 sc1 lds\nv_mfma_f32_16x16x32_bf16 v[44:47], a[128:131], v[84:87], v[44:47]\ns_waitcnt lgkmcnt(3)\nv_mfma_f32_16x16x32_bf16 v[16:19], a[108:111], v[88:91], v[16:19]\nds_read_b128 v[84:87], v2 offset:20736\nv_mfma_f32_16x16x32_bf16 v[20:23], a[116:119], v[88:91], v[20:23]\nds_read_b128 v[76:79], v1 offset:7168\nv_mfma_f32_16x16x32_bf16 v[24:27], a[124:127], v[88:91], v[24:27]\n" \
"v_mfma_f32_16x16x32_bf16 v[28:31], a[132:135], v[88:91], v[28:31]\ns_waitcnt lgkmcnt(4)\nv_mfma_f32_16x16x32_bf16 v[32:35], a[108:111], v[92:95], v[32:35]\nds_read_b128 v[88:91], v2 offset:22528\nv_mfma_f32_16x16x32_bf16 v[36:39], a[116:119], v[92:95], v[36:39]\nv_mfma_f32_16x16x32_bf16 v[40:43], a[124:127], v[92:95], v[40:43]\nv_mfma_f32_16x16x32_bf16 v[44:47], a[132:135], v[92:95], v[44:47]\ns_waitcnt lgkmcnt(3)\n" \
"v_mfma_f32_16x16x32_bf16 v[16:19], a[136:139], v[80:83], v[16:19]\nds_read_b128 v[92:95], v2 offset:22784\nv_mfma_f32_16x16x32_bf16 v[20:23], v[72:75], v[80:83], v[20:23]\nds_read_b128 v[64:67], v1 offset:8192\nv_mfma_f32_16x16x32_bf16 v[24:27], a[144:147], v[80:83], v[24:27]\nv_mfma_f32_16x16x32_bf16 v[28:31], a[152:155], v[80:83], v[28:31]\ns_waitcnt lgkmcnt(4)\nv_mfma_f32_16x16x32_bf16 v[32:35], a[136:139], v[84:87], v[32:35]\n" \
"ds_read_b128 v[80:83], v2 offset:24576\nv_mfma_f32_16x16x32_bf16 v[36:39], v[72:75], v[84:87], v[36:39]\nv_mfma_f32_16x16x32_bf16 v[40:43], a[144:147], v[84:87], v[40:43]\nv_mfma_f32_16x16x32_bf16 v[44:47], a[152:155], v[84:87], v[44:47]\ns_waitcnt lgkmcnt(3)\nv_mfma_f32_16x16x32_bf16 v[16:19], a[140:143], v[88:91], v[16:19]\nds_read_b128 v[84:87], v2 offset:24832\nv_mfma_f32_16x16x32_bf16 v[20:23], v[76:79], v[88:91], v[20:23]\n" \
"ds_read_b128 v[68:71], v1 offset:9216\nv_mfma_f32_16x16x32_bf16 v[24:27], a[148:151], v[88:91], v[24:27]\nv_mfma_f32_16x16x32_bf16 v[28:31], a[156:159], v[88:91], v[28:31]\ns_waitcnt lgkmcnt(4)\nv_mfma_f32_16x16x32_bf16 v[32:35], a[140:143], v[92:95], v[32:35]\nds_read_b128 v[88:91], v2 offset:26624\nv_mfma_f32_16x16x32_bf16 v[36:39], v[76:79], v[92:95], v[36:39]\nv_mfma_f32_16x16x32_bf16 v[40:43], a[148:151], v[92:95], v[40:43]\n" \
"v_mfma_f32_16x16x32_bf16 v[44:47], a[156:159], v[92:95], v[44:47]\ns_waitcnt lgkmcnt(3)\nv_mfma_f32_16x16x32_bf16 v[16:19], a[160:163], v[80:83], v[16:19]\nds_read_b128 v[92:95], v2 offset:26880\nv_mfma_f32_16x16x32_bf16 v[20:23], a[168:171], v[80:83], v[20:23]\nds_read_b128 v[72:75], v1 offset:10240\nv_mfma_f32_16x16x32_bf16 v[24:27], v[64:67], v[80:83], v[24:27]\nv_mfma_f32_16x16x32_bf16 v[28:31], a[176:179], v[80:83], v[28:31]\n" \
"s_waitcnt lgkmcnt(4)\nv_mfma_f32_16x16x32_bf16 v[32:35], a[160:163], v[84:87], v[32:35]\nds_read_b128 v[80:83], v2 offset:28672\nv_mfma_f32_16x16x32_bf16 v[36:39], a[168:171], v[84:87], v[36:39]\nv_mfma_f32_16x16x32_bf16 v[40:43], v[64:67], v[84:87], v[40:43]\nv_mfma_f32_16x16x32_bf16 v[44:47], a[176:179], v[84:87], v[44:47]\ns_waitcnt lgkmcnt(3)\nv_mfma_f32_16x16x32_bf16 v[16:19], a[164:167], v[88:91], v[16:19]\n" \
"ds_read_b128 v[84:87], v2 offset:28928\nv_mfma_f32_16x16x32_bf16 v[20:23], a[172:175], v[88:91], v[20:23]\nds_read_b128 v[76:79], v1 offset:11264\nv_mfma_f32_16x16x32_bf16 v[24:27], v[68:71], v[88:91], v[24:27]\nv_mfma_f32_16x16x32_bf16 v[28:31], a[180:183], v[88:91], v[28:31]\ns_waitcnt lgkmcnt(4)\nv_mfma_f32_16x16x32_bf16 v[32:35], a[164:167], v[92:95], v[32:35]\nds_read_b128 v[88:91], v2 offset:30720\n" \
"v_mfma_f32_16x16x32_bf16 v[36:39], a[172:175], v[92:95], v[36:39]\nv_mfma_f32_16x16x32_bf16 v[40:43], v[68:71], v[92:95], v[40:43]\nv_mfma_f32_16x16x32_bf16 v[44:47], a[180:183], v[92:95], v[44:47]\ns_waitcnt lgkmcnt(3)\nv_mfma_f32_16x16x32_bf16 v[16:19], a[184:187], v[80:83], v[16:19]\nds_read_b128 v[92:95], v2 offset:30976\nv_mfma_f32_16x16x32_bf16 v[20:23], a[192:195], v[80:83], v[20:23]\nv_mfma_f32_16x16x32_bf16 v[24:27], a[200:203], v[80:83], v[24:27]\n" \
"v_mfma_f32_16x16x32_bf16 v[28:31], v[72:75], v[80:83], v[28:31]\ns_waitcnt lgkmcnt(3)\nv_mfma_f32_16x16x32_bf16 v[32:35], a[184:187], v[84:87], v[32:35]\ns_waitcnt vmcnt(0)\ns_barrier\nds_read_b128 v[48:51], v2 offset:32768\nv_mfma_f32_16x16x32_bf16 v[36:39], a[192:195], v[84:87], v[36:39]\ns_add_u32 m0, s57, 0\n" \
"v_mfma_f32_16x16x32_bf16 v[40:43], a[200:203], v[84:87], v[40:43]\ns_add_u32 s60, s41, 98304\nbuffer_load_dwordx4 v0, s[36:39], s60 offen offset:0 sc1 lds\nv_mfma_f32_16x16x32_bf16 v[44:47], v[72:75], v[84:87], v[44:47]\ns_waitcnt lgkmcnt(2)\nv_mfma_f32_16x16x32_bf16 v[16:19], a[188:191], v[88:91], v[16:19]\nds_read_b128 v[52:55], v2 offset:33024\nv_mfma_f32_16x16x32_bf16 v[20:23], a[196:199], v[88:91], v[20:23]\n" \
"v_mfma_f32_16x16x32_bf16 v[24:27], a[204:207], v[88:91], v[24:27]\nbuffer_load_dwordx4 v0, s[36:39], s60 offen offset:1024 sc1 lds\nv_mfma_f32_16x16x32_bf16 v[28:31], v[76:79], v[88:91], v[28:31]\ns_waitcnt lgkmcnt(2)\nv_mfma_f32_16x16x32_bf16 v[32:35], a[188:191], v[92:95], v[32:35]\nds_read_b128 v[56:59], v2 offset:34816\nv_mfma_f32_16x16x32_bf16 v[36:39], a[196:199], v[92:95], v[36:39]\nv_mfma_f32_16x16x32_bf16 v[40:43], a[204:207], v[92:95], v[40:43]\n" \
"buffer_load_dwordx4 v0, s[36:39], s60 offen offset:2048 sc1 lds\nv_mfma_f32_16x16x32_bf16 v[44:47], v[76:79], v[92:95], v[44:47]\ns_waitcnt lgkmcnt(2)\nv_mfma_f32_16x16x32_bf16 v[16:19], a[208:211], v[48:51], v[16:19]\nds_read_b128 v[60:63], v2 offset:35072\nv_mfma_f32_16x16x32_bf16 v[20:23], a[216:219], v[48:51], v[20:23]\nds_read_b128 v[72:75], v1 offset:12288\nv_mfma_f32_16x16x32_bf16 v[24:27], a[224:227], v[48:51], v[24:27]\n" \
"buffer_load_dwordx4 v0, s[36:39], s60 offen offset:3072 sc1 lds\nv_mfma_f32_16x16x32_bf16 v[28:31], a[232:235], v[48:51], v[28:31]\ns_waitcnt lgkmcnt(3)\nv_mfma_f32_16x16x32_bf16 v[32:35], a[208:211], v[52:55], v[32:35]\nds_read_b128 v[48:51], v2 offset:36864\nv_mfma_f32_16x16x32_bf16 v[36:39], a[216:219], v[52:55], v[36:39]\nv_mfma_f32_16x16x32_bf16 v[40:43], a[224:227], v[52:55], v[40:43]\nv_mfma_f32_16x16x32_bf16 v[44:47], a[232:235], v[52:55], v[44:47]\n" \
"s_waitcnt lgkmcnt(3)\nv_mfma_f32_16x16x32_bf16 v[16:19], a[212:215], v[56:59], v[16:19]\nds_read_b128 v[52:55], v2 offset:37120\nv_mfma_f32_16x16x32_bf16 v[20:23], a[220:223], v[56:59], v[20:23]\nds_read_b128 v[76:79], v1 offset:13312\nv_mfma_f32_16x16x32_bf16 v[24:27], a[228:231], v[56:59], v[24:27]\nv_mfma_f32_16x16x32_bf16 v[28:31], a[236:239], v[56:59], v[28:31]\ns_waitcnt lgkmcnt(4)\n" \
"v_mfma_f32_16x16x32_bf16 v[32:35], a[212:215], v[60:63], v[32:35]\nds_read_b128 v[56:59], v2 offset:38912\nv_mfma_f32_16x16x32_bf16 v[36:39], a[220:223], v[60:63], v[36:39]\nv_mfma_f32_16x16x32_bf16 v[40:43], a[228:231], v[60:63], v[40:43]\nv_mfma_f32_16x16x32_bf16 v[44:47], a[236:239], v[60:63], v[44:47]\ns_waitcnt lgkmcnt(3)\nv_mfma_f32_16x16x32_bf16 v[16:19], a[240:243], v[48:51], v[16:19]\nds_read_b128 v[60:63], v2 offset:39168\n" \
"v_mfma_f32_16x16x32_bf16 v[20:23], v[72:75], v[48:51], v[20:23]\nds_read_b128 v[64:67], v1 offset:14336\nv_mfma_f32_16x16x32_bf16 v[24:27], a[248:251], v[48:51], v[24:27]\nv_mfma_f32_16x16x32_bf16 v[28:31], v[96:99], v[48:51], v[28:31]\ns_waitcnt lgkmcnt(4)\nv_mfma_f32_16x16x32_bf16 v[32:35], a[240:243], v[52:55], v[32:35]\nds_read_b128 v[48:51], v2 offset:40960\nv_mfma_f32_16x16x32_bf16 v[36:39], v[72:75], v[52:55], v[36:39]\n" \
"v_mfma_f32_16x16x32_bf16 v[40:43], a[248:251], v[52:55], v[40:43]\nv_mfma_f32_16x16x32_bf16 v[44:47], v[96:99], v[52:55], v[44:47]\ns_waitcnt lgkmcnt(3)\nv_mfma_f32_16x16x32_bf16 v[16:19], a[244:247], v[56:59], v[16:19]\nds_read_b128 v[52:55], v2 offset:41216\nv_mfma_f32_16x16x32_bf16 v[20:23], v[76:79], v[56:59], v[20:23]\nds_read_b128 v[68:71], v1 offset:15360\nv_mfma_f32_16x16x32_bf16 v[24:27], a[252:255], v[56:59], v[24:27]\n" \
"v_mfma_f32_16x16x32_bf16 v[28:31], v[100:103], v[56:59], v[28:31]\ns_waitcnt lgkmcnt(4)\nv_mfma_f32_16x16x32_bf16 v[32:35], a[244:247], v[60:63], v[32:35]\nds_read_b128 v[56:59], v2 offset:43008\nv_mfma_f32_16x16x32_bf16 v[36:39], v[76:79], v[60:63], v[36:39]\nv_mfma_f32_16x16x32_bf16 v[40:43], a[252:255], v[60:63], v[40:43]\nv_mfma_f32_16x16x32_bf16 v[44:47], v[100:103], v[60:63], v[44:47]\ns_waitcnt lgkmcnt(3)\n" \
"v_mfma_f32_16x16x32_bf16 v[16:19], v[104:107], v[48:51], v[16:19]\nds_read_b128 v[60:63], v2 offset:43264\nv_mfma_f32_16x16x32_bf16 v[20:23], v[112:115], v[48:51], v[20:23]\nds_read_b128 v[72:75], v1 offset:16384\nv_mfma_f32_16x16x32_bf16 v[24:27], v[64:67], v[48:51], v[24:27]\nv_mfma_f32_16x16x32_bf16 v[28:31], v[120:123], v[48:51], v[28:31]\ns_waitcnt lgkmcnt(4)\nv_mfma_f32_16x16x32_bf16 v[32:35], v[104:107], v[52:55], v[32:35]\n" \
"ds_read_b128 v[48:51], v2 offset:45056\nv_mfma_f32_16x16x32_bf16 v[36:39], v[112:115], v[52:55], v[36:39]\nv_mfma_f32_16x16x32_bf16 v[40:43], v[64:67], v[52:55], v[40:43]\nv_mfma_f32_16x16x32_bf16 v[44:47], v[120:123], v[52:55], v[44:47]\ns_waitcnt lgkmcnt(3)\nv_mfma_f32_16x16x32_bf16 v[16:19], v[108:111], v[56:59], v[16:19]\nds_read_b128 v[52:55], v2 offset:45312\nv_mfma_f32_16x16x32_bf16 v[20:23], v[116:119], v[56:59], v[20:23]\n" \
"ds_read_b128 v[76:79], v1 offset:17408\nv_mfma_f32_16x16x32_bf16 v[24:27], v[68:71], v[56:59], v[24:27]\nv_mfma_f32_16x16x32_bf16 v[28:31], v[124:127], v[56:59], v[28:31]\ns_waitcnt lgkmcnt(4)\nv_mfma_f32_16x16x32_bf16 v[32:35], v[108:111], v[60:63], v[32:35]\nds_read_b128 v[56:59], v2 offset:47104\nv_mfma_f32_16x16x32_bf16 v[36:39], v[116:119], v[60:63], v[36:39]\nv_mfma_f32_16x16x32_bf16 v[40:43], v[68:71], v[60:63], v[40:43]\n" \
"v_mfma_f32_16x16x32_bf16 v[44:47], v[124:127], v[60:63], v[44:47]\ns_waitcnt lgkmcnt(3)\nv_mfma_f32_16x16x32_bf16 v[16:19], v[128:131], v[48:51], v[16:19]\nds_read_b128 v[60:63], v2 offset:47360\nv_mfma_f32_16x16x32_bf16 v[20:23], v[136:139], v[48:51], v[20:23]\nv_mfma_f32_16x16x32_bf16 v[24:27], v[144:147], v[48:51], v[24:27]\nv_mfma_f32_16x16x32_bf16 v[28:31], v[72:75], v[48:51], v[28:31]\ns_waitcnt lgkmcnt(3)\n" \
"v_mfma_f32_16x16x32_bf16 v[32:35], v[128:131], v[52:55], v[32:35]\ns_waitcnt lgkmcnt(0)\ns_waitcnt vmcnt(0)\ns_barrier\nds_read_b128 v[48:51], v2 offset:0\nv_mfma_f32_16x16x32_bf16 v[36:39], v[136:139], v[52:55], v[36:39]\nv_mfma_f32_16x16x32_bf16 v[40:43], v[144:147], v[52:55], v[40:43]\nv_mfma_f32_16x16x32_bf16 v[44:47], v[72:75], v[52:55], v[44:47]\n" \
"v_mfma_f32_16x16x32_bf16 v[16:19], v[132:135], v[56:59], v[16:19]\nds_read_b128 v[52:55], v2 offset:256\nv_mfma_f32_16x16x32_bf16 v[20:23], v[140:143], v[56:59], v[20:23]\nv_mfma_f32_16x16x32_bf16 v[24:27], v[148:151], v[56:59], v[24:27]\nv_mfma_f32_16x16x32_bf16 v[28:31], v[76:79], v[56:59], v[28:31]\nv_mfma_f32_16x16x32_bf16 v[32:35], v[132:135], v[60:63], v[32:35]\nds_read_b128 v[56:59], v2 offset:2048\nv_mfma_f32_16x16x32_bf16 v[36:39], v[140:143], v[60:63], v[36:39]\n" \
"v_mfma_f32_16x16x32_bf16 v[40:43], v[148:151], v[60:63], v[40:43]\nv_mfma_f32_16x16x32_bf16 v[44:47], v[76:79], v[60:63], v[44:47]\ns_waitcnt lgkmcnt(2)\nv_mfma_f32_16x16x32_bf16 v[16:19], v[152:155], v[48:51], v[16:19]\nds_read_b128 v[60:63], v2 offset:2304\nv_mfma_f32_16x16x32_bf16 v[20:23], v[160:163], v[48:51], v[20:23]\nds_read_b128 v[72:75], v1 offset:18432\nv_mfma_f32_16x16x32_bf16 v[24:27], v[168:171], v[48:51], v[24:27]\n" \
"v_mfma_f32_16x16x32_bf16 v[28:31], v[176:179], v[48:51], v[28:31]\ns_waitcnt lgkmcnt(3)\nv_mfma_f32_16x16x32_bf16 v[32:35], v[152:155], v[52:55], v[32:35]\nds_read_b128 v[48:51], v2 offset:4096\nv_mfma_f32_16x16x32_bf16 v[36:39], v[160:163], v[52:55], v[36:39]\nv_mfma_f32_16x16x32_bf16 v[40:43], v[168:171], v[52:55], v[40:43]\nv_mfma_f32_16x16x32_bf16 v[44:47], v[176:179], v[52:55], v[44:47]\ns_waitcnt lgkmcnt(3)\n" \
"v_mfma_f32_16x16x32_bf16 v[16:19], v[156:159], v[56:59], v[16:19]\nds_read_b128 v[52:55], v2 offset:4352\nv_mfma_f32_16x16x32_bf16 v[20:23], v[164:167], v[56:59], v[20:23]\nds_read_b128 v[76:79], v1 offset:19456\nv_mfma_f32_16x16x32_bf16 v[24:27], v[172:175], v[56:59], v[24:27]\nv_mfma_f32_16x16x32_bf16 v[28:31], v[180:183], v[56:59], v[28:31]\ns_waitcnt lgkmcnt(4)\nv_mfma_f32_16x16x32_bf16 v[32:35], v[156:159], v[60:63], v[32:35]\n" \
"ds_read_b128 v[56:59], v2 offset:6144\nv_mfma_f32_16x16x32_bf16 v[36:39], v[164:167], v[60:63], v[36:39]\nv_mfma_f32_16x16x32_bf16 v[40:43], v[172:175], v[60:63], v[40:43]\nv_mfma_f32_16x16x32_bf16 v[44:47], v[180:183], v[60:63], v[44:47]\ns_waitcnt lgkmcnt(3)\nv_mfma_f32_16x16x32_bf16 v[16:19], v[184:187], v[48:51], v[16:19]\nds_read_b128 v[60:63], v2 offset:6400\nv_mfma_f32_16x16x32_bf16 v[20:23], v[72:75], v[48:51], v[20:23]\n" \
"ds_read_b128 v[64:67], v1 offset:20480\nv_mfma_f32_16x16x32_bf16 v[24:27], v[192:195], v[48:51], v[24:27]\nv_mfma_f32_16x16x32_bf16 v[28:31], v[200:203], v[48:51], v[28:31]\ns_waitcnt lgkmcnt(4)\nv_mfma_f32_16x16x32_bf16 v[32:35], v[184:187], v[52:55], v[32:35]\nds_read_b128 v[48:51], v2 offset:8192\nv_mfma_f32_16x16x32_bf16 v[36:39], v[72:75], v[52:55], v[36:39]\nv_mfma_f32_16x16x32_bf16 v[40:43], v[192:195], v[52:55], v[40:43]\n" \
"v_mfma_f32_16x16x32_bf16 v[44:47], v[200:203], v[52:55], v[44:47]\ns_waitcnt lgkmcnt(3)\nv_mfma_f32_16x16x32_bf16 v[16:19], v[188:191], v[56:59], v[16:19]\nds_read_b128 v[52:55], v2 offset:10240\nv_mfma_f32_16x16x32_bf16 v[20:23], v[76:79], v[56:59], v[20:23]\nds_read_b128 v[68:71], v1 offset:21504\nv_mfma_f32_16x16x32_bf16 v[24:27], v[196:199], v[56:59], v[24:27]\nbuffer_load_dwordx4 v[80:83], v3, s[36:39], s42 offen offset:0 sc1\n" \
"v_mfma_f32_16x16x32_bf16 v[28:31], v[204:207], v[56:59], v[28:31]\ns_waitcnt lgkmcnt(4)\nv_mfma_f32_16x16x32_bf16 v[32:35], v[188:191], v[60:63], v[32:35]\nds_read_b128 v[56:59], v2 offset:12288\nv_mfma_f32_16x16x32_bf16 v[36:39], v[76:79], v[60:63], v[36:39]\nds_read_b128 v[72:75], v1 offset:22528\nv_mfma_f32_16x16x32_bf16 v[40:43], v[196:199], v[60:63], v[40:43]\nbuffer_load_dwordx4 v[84:87], v3, s[36:39], s42 offen offset:1024 sc1\n" \
"v_mfma_f32_16x16x32_bf16 v[44:47], v[204:207], v[60:63], v[44:47]\ns_waitcnt lgkmcnt(4)\nv_mfma_f32_16x16x32_bf16 v[16:19], v[208:211], v[48:51], v[16:19]\nds_read_b128 v[60:63], v2 offset:14336\nv_mfma_f32_16x16x32_bf16 v[20:23], v[216:219], v[48:51], v[20:23]\nds_read_b128 v[76:79], v1 offset:23552\nv_mfma_f32_16x16x32_bf16 v[24:27], v[64:67], v[48:51], v[24:27]\nbuffer_load_dwordx4 v[88:91], v3, s[36:39], s42 offen offset:2048 sc1\n" \
"v_mfma_f32_16x16x32_bf16 v[28:31], v[224:227], v[48:51], v[28:31]\ns_waitcnt lgkmcnt(5)\nv_mfma_f32_16x16x32_bf16 v[16:19], v[212:215], v[52:55], v[16:19]\nds_read_b128 v[48:51], v2 offset:8448\nv_mfma_f32_16x16x32_bf16 v[20:23], v[220:223], v[52:55], v[20:23]\ns_waitcnt lgkmcnt(5)\nv_mfma_f32_16x16x32_bf16 v[24:27], v[68:71], v[52:55], v[24:27]\nbuffer_load_dwordx4 v[92:95], v3, s[36:39], s42 offen offset:3072 sc1\n" \
"v_mfma_f32_16x16x32_bf16 v[28:31], v[228:231], v[52:55], v[28:31]\ns_waitcnt lgkmcnt(4)\nv_mfma_f32_16x16x32_bf16 v[16:19], v[232:235], v[56:59], v[16:19]\nds_read_b128 v[52:55], v2 offset:10496\nv_mfma_f32_16x16x32_bf16 v[20:23], v[240:243], v[56:59], v[20:23]\nv_mfma_f32_16x16x32_bf16 v[24:27], v[248:251], v[56:59], v[24:27]\ns_waitcnt lgkmcnt(4)\nv_mfma_f32_16x16x32_bf16 v[28:31], v[72:75], v[56:59], v[28:31]\n" \
"s_waitcnt lgkmcnt(3)\nv_mfma_f32_16x16x32_bf16 v[16:19], v[236:239], v[60:63], v[16:19]\nds_read_b128 v[56:59], v2 offset:12544\nv_mfma_f32_16x16x32_bf16 v[20:23], v[244:247], v[60:63], v[20:23]\nv_mfma_f32_16x16x32_bf16 v[24:27], v[252:255], v[60:63], v[24:27]\ns_waitcnt lgkmcnt(3)\nv_mfma_f32_16x16x32_bf16 v[28:31], v[76:79], v[60:63], v[28:31]\ns_waitcnt lgkmcnt(2)\n" \
"v_mfma_f32_16x16x32_bf16 v[32:35], v[208:211], v[48:51], v[32:35]\nds_read_b128 v[60:63], v2 offset:14592\nv_mfma_f32_16x16x32_bf16 v[36:39], v[216:219], v[48:51], v[36:39]\nv_cvt_pk_bf16_f32 v20, v16, v20\nv_cvt_pk_bf16_f32 v21, v17, v21\nv_mfma_f32_16x16x32_bf16 v[40:43], v[64:67], v[48:51], v[40:43]\nv_cvt_pk_bf16_f32 v22, v18, v22\nv_cvt_pk_bf16_f32 v23, v19, v23\n" \
"v_mfma_f32_16x16x32_bf16 v[44:47], v[224:227], v[48:51], v[44:47]\ns_waitcnt lgkmcnt(2)\nds_write_b128 v5, v[20:23] offset:0\nv_mfma_f32_16x16x32_bf16 v[32:35], v[212:215], v[52:55], v[32:35]\nv_cvt_pk_bf16_f32 v28, v24, v28\nv_cvt_pk_bf16_f32 v29, v25, v29\nv_mfma_f32_16x16x32_bf16 v[36:39], v[220:223], v[52:55], v[36:39]\nv_cvt_pk_bf16_f32 v30, v26, v30\n" \
"v_cvt_pk_bf16_f32 v31, v27, v31\nv_mfma_f32_16x16x32_bf16 v[40:43], v[68:71], v[52:55], v[40:43]\nv_mov_b64 v[48:49], 0\nv_mov_b64 v[50:51], 0\nv_mfma_f32_16x16x32_bf16 v[44:47], v[228:231], v[52:55], v[44:47]\ns_cmp_eq_u32 %[w], 1\nds_write_b128 v5, v[28:31] offset:512\ns_cbranch_scc1 .Lqk_c1%=\n" \
"s_cmp_eq_u32 %[w], 2\ns_cbranch_scc1 .Lqk_c2%=\ns_cmp_eq_u32 %[w], 3\ns_cbranch_scc1 .Lqk_c3%=\n.Lqk_c0%=:\ns_waitcnt lgkmcnt(3)\nv_mfma_f32_16x16x32_bf16 v[32:35], v[232:235], v[56:59], v[32:35]\nv_mov_b64 v[52:53], 0\n" \
"v_mov_b64 v[54:55], 0\nv_mfma_f32_16x16x32_bf16 v[36:39], v[240:243], v[56:59], v[36:39]\ns_waitcnt lgkmcnt(2)\nds_read_b128 v[64:67], v1 offset:0\nv_mfma_f32_16x16x32_bf16 v[32:35], v[236:239], v[60:63], v[32:35]\nds_read_b128 v[68:71], v1 offset:1024\nv_mfma_f32_16x16x32_bf16 v[36:39], v[244:247], v[60:63], v[36:39]\ns_bfe_i32 s54, s40, 0x10000\n" \
"v_mfma_f32_16x16x32_bf16 v[40:43], v[248:251], v[56:59], v[40:43]\nv_mfma_f32_16x16x32_bf16 v[44:47], v[72:75], v[56:59], v[44:47]\nv_cvt_pk_bf16_f32 v36, v32, v36\nv_cvt_pk_bf16_f32 v37, v33, v37\nv_mfma_f32_16x16x32_bf16 v[40:43], v[252:255], v[60:63], v[40:43]\nv_cvt_pk_bf16_f32 v38, v34, v38\nv_cvt_pk_bf16_f32 v39, v35, v39\nv_mfma_f32_16x16x32_bf16 v[44:47], v[76:79], v[60:63], v[44:47]\n" \
"ds_read_b128 v[72:75], v1 offset:2048\nds_read_b128 v[76:79], v1 offset:3072\nv_mov_b64 v[56:57], 0\nv_mov_b64 v[58:59], 0\nv_mov_b64 v[60:61], 0\nv_mov_b64 v[62:63], 0\n.Lqk_poll0%=:\ns_waitcnt vmcnt(0)\n" \
"v_smfmac_f32_16x16x64_bf16 v[48:51], v[80:83], a[0:7], v4\nds_write_b128 v0, v[80:83] offset:16384\nv_max3_u32 v16, v80, v82, v84\nv_smfmac_f32_16x16x64_bf16 v[52:55], v[80:83], a[8:15], v4\nv_max3_u32 v16, v16, v86, v88\nv_smfmac_f32_16x16x64_bf16 v[56:59], v[80:83], a[16:23], v4\nds_write_b128 v0, v[84:87] offset:17408\nv_max3_u32 v16, v16, v90, v92\n" \
"v_max_u32 v16, v16, v94\nv_smfmac_f32_16x16x64_bf16 v[60:63], v[80:83], a[24:31], v4\nv_cmp_eq_u32 vcc, -1, v16\nv_smfmac_f32_16x16x64_bf16 v[48:51], v[84:87], a[32:39], v4\nds_write_b128 v0, v[88:91] offset:18432\ns_cbranch_vccnz .Lqk_re0%=\ns_waitcnt lgkmcnt(5)\nv_smfmac_f32_16x16x64_bf16 v[52:55], v[84:87], v[64:71], v4\n" \
"v_smfmac_f32_16x16x64_bf16 v[56:59], v[84:87], a[40:47], v4\nds_write_b128 v0, v[92:95] offset:19456\nv_smfmac_f32_16x16x64_bf16 v[60:63], v[84:87], a[48:55], v4\nv_smfmac_f32_16x16x64_bf16 v[48:51], v[88:91], a[56:63], v4\nv_smfmac_f32_16x16x64_bf16 v[52:55], v[88:91], a[64:71], v4\ns_waitcnt lgkmcnt(0)\n.Lqk_jn0%=:\ns_barrier\n" \
"s_memrealtime s[68:69]\nds_read_b128 v[80:83], v0 offset:20480\nds_read_b128 v[64:67], v1 offset:4096\nds_read_b128 v[68:71], v1 offset:5120\nv_mov_b32 v24, s40\nv_smfmac_f32_16x16x64_bf16 v[56:59], v[88:91], v[72:79], v4\nds_read_b128 v[72:75], v1 offset:6144\nds_read_b128 v[76:79], v1 offset:7168\n" \
"v_smfmac_f32_16x16x64_bf16 v[60:63], v[88:91], a[72:79], v4\nds_read_b128 v[84:87], v0 offset:21504\nds_read_b128 v[88:91], v0 offset:22528\nv_smfmac_f32_16x16x64_bf16 v[48:51], v[92:95], a[80:87], v4\nds_write_b128 v5, v[36:39] offset:256\nv_cvt_pk_bf16_f32 v44, v40, v44\nv_cvt_pk_bf16_f32 v45, v41, v45\nv_cvt_pk_bf16_f32 v46, v42, v46\n" \
"v_cvt_pk_bf16_f32 v47, v43, v47\nv_smfmac_f32_16x16x64_bf16 v[52:55], v[92:95], a[88:95], v4\nv_smfmac_f32_16x16x64_bf16 v[56:59], v[92:95], a[96:103], v4\ns_waitcnt lgkmcnt(5)\nv_smfmac_f32_16x16x64_bf16 v[60:63], v[92:95], v[64:71], v4\ns_sub_u32 s88, s86, s89\nds_read_b128 v[92:95], v0 offset:23552\nds_read_b128 v[64:67], v1 offset:8192\n" \
"ds_read_b128 v[68:71], v1 offset:9216\nv_smfmac_f32_16x16x64_bf16 v[48:51], v[80:83], a[104:111], v4\ns_min_u32 s88, s88, 150\nds_write_b128 v5, v[44:47] offset:768\nv_smfmac_f32_16x16x64_bf16 v[52:55], v[80:83], a[112:119], v4\ns_add_u32 s88, s88, s68\ns_mov_b32 s89, s68\nv_mov_b32 v25, s88\n" \
"v_smfmac_f32_16x16x64_bf16 v[56:59], v[80:83], a[120:127], v4\nbuffer_store_dwordx2 v[24:25], off, s[36:39], %[sg] sc1\nv_smfmac_f32_16x16x64_bf16 v[60:63], v[80:83], a[128:135], v4\ns_waitcnt lgkmcnt(6)\nds_read_b128 v[80:83], v0 offset:24576\nv_smfmac_f32_16x16x64_bf16 v[48:51], v[84:87], a[136:143], v4\nv_smfmac_f32_16x16x64_bf16 v[52:55], v[84:87], v[72:79], v4\nds_read_b128 v[72:75], v1 offset:10240\n" \
"ds_read_b128 v[76:79], v1 offset:11264\nv_smfmac_f32_16x16x64_bf16 v[56:59], v[84:87], a[144:151], v4\nv_smfmac_f32_16x16x64_bf16 v[60:63], v[84:87], a[152:159], v4\ns_waitcnt lgkmcnt(8)\nds_read_b128 v[84:87], v0 offset:25600\nv_smfmac_f32_16x16x64_bf16 v[48:51], v[88:91], a[160:167], v4\ns_waitcnt lgkmcnt(4)\ns_barrier\n" \
"v_smfmac_f32_16x16x64_bf16 v[52:55], v[88:91], a[168:175], v4\nv_smfmac_f32_16x16x64_bf16 v[56:59], v[88:91], v[64:71], v4\nds_read_b128 v[64:67], v1 offset:12288\nds_read_b128 v[68:71], v1 offset:13312\nv_smfmac_f32_16x16x64_bf16 v[60:63], v[88:91], a[176:183], v4\nds_read_b128 v[88:91], v0 offset:26624\nv_smfmac_f32_16x16x64_bf16 v[48:51], v[92:95], a[184:191], v4\nds_read_b128 v[16:19], v7 offset:0\n" \
"v_smfmac_f32_16x16x64_bf16 v[52:55], v[92:95], a[192:199], v4\nv_smfmac_f32_16x16x64_bf16 v[56:59], v[92:95], a[200:207], v4\ns_waitcnt lgkmcnt(5)\nds_read_b128 v[20:23], v7 offset:64\nv_smfmac_f32_16x16x64_bf16 v[60:63], v[92:95], v[72:79], v4\nds_read_b128 v[92:95], v0 offset:27648\nds_read_b128 v[72:75], v1 offset:14336\nds_read_b128 v[76:79], v1 offset:15360\n" \
"v_smfmac_f32_16x16x64_bf16 v[48:51], v[80:83], a[208:215], v4\nds_read_b128 v[24:27], v7 offset:2048\nv_smfmac_f32_16x16x64_bf16 v[52:55], v[80:83], a[216:223], v4\nv_smfmac_f32_16x16x64_bf16 v[56:59], v[80:83], a[224:231], v4\nds_read_b128 v[28:31], v7 offset:2112\nv_smfmac_f32_16x16x64_bf16 v[60:63], v[80:83], a[232:239], v4\ns_waitcnt lgkmcnt(10)\nds_read_b128 v[80:83], v0 offset:28672\n" \
"v_smfmac_f32_16x16x64_bf16 v[48:51], v[84:87], a[240:247], v4\ns_waitcnt lgkmcnt(9)\nds_read_b128 v[32:35], v7 offset:4096\nv_smfmac_f32_16x16x64_bf16 v[52:55], v[84:87], v[64:71], v4\nds_read_b128 v[64:67], v1 offset:16384\nds_read_b128 v[68:71], v1 offset:17408\nv_smfmac_f32_16x16x64_bf16 v[56:59], v[84:87], a[248:255], v4\nds_read_b128 v[36:39], v7 offset:4160\n" \
"v_smfmac_f32_16x16x64_bf16 v[60:63], v[84:87], v[96:103], v4\ns_waitcnt lgkmcnt(12)\nds_read_b128 v[84:87], v0 offset:29696\nv_smfmac_f32_16x16x64_bf16 v[48:51], v[88:91], v[104:111], v4\nds_read_b128 v[40:43], v7 offset:6144\nv_smfmac_f32_16x16x64_bf16 v[52:55], v[88:91], v[112:119], v4\ns_waitcnt lgkmcnt(9)\nv_smfmac_f32_16x16x64_bf16 v[56:59], v[88:91], v[72:79], v4\n" \
"ds_read_b128 v[72:75], v1 offset:18432\nds_read_b128 v[76:79], v1 offset:19456\nv_smfmac_f32_16x16x64_bf16 v[60:63], v[88:91], v[120:127], v4\nds_read_b128 v[88:91], v0 offset:30720\nv_smfmac_f32_16x16x64_bf16 v[48:51], v[92:95], v[128:135], v4\nds_read_b128 v[44:47], v7 offset:6208\nv_smfmac_f32_16x16x64_bf16 v[52:55], v[92:95], v[136:143], v4\nv_smfmac_f32_16x16x64_bf16 v[56:59], v[92:95], v[144:151], v4\n" \
"s_waitcnt lgkmcnt(7)\nv_smfmac_f32_16x16x64_bf16 v[60:63], v[92:95], v[64:71], v4\nds_read_b128 v[92:95], v0 offset:31744\nds_read_b128 v[64:67], v1 offset:20480\nds_read_b128 v[68:71], v1 offset:21504\nv_smfmac_f32_16x16x64_bf16 v[48:51], v[80:83], v[152:159], v4\nv_smfmac_f32_16x16x64_bf16 v[52:55], v[80:83], v[160:167], v4\nv_smfmac_f32_16x16x64_bf16 v[56:59], v[80:83], v[168:175], v4\n" \
"v_smfmac_f32_16x16x64_bf16 v[60:63], v[80:83], v[176:183], v4\ns_waitcnt lgkmcnt(8)\nv_smfmac_f32_16x16x64_bf16 v[48:51], v[84:87], v[184:191], v4\ns_waitcnt lgkmcnt(5)\nv_smfmac_f32_16x16x64_bf16 v[52:55], v[84:87], v[72:79], v4\nds_read_b128 v[72:75], v1 offset:22528\nds_read_b128 v[76:79], v1 offset:23552\nv_smfmac_f32_16x16x64_bf16 v[56:59], v[84:87], v[192:199], v4\n" \
"v_smfmac_f32_16x16x64_bf16 v[60:63], v[84:87], v[200:207], v4\ns_waitcnt lgkmcnt(6)\nv_smfmac_f32_16x16x64_bf16 v[48:51], v[88:91], v[208:215], v4\nv_smfmac_f32_16x16x64_bf16 v[52:55], v[88:91], v[216:223], v4\ns_waitcnt lgkmcnt(4)\nv_smfmac_f32_16x16x64_bf16 v[48:51], v[92:95], v[232:239], v4\nv_smfmac_f32_16x16x64_bf16 v[52:55], v[92:95], v[240:247], v4\ns_waitcnt lgkmcnt(2)\n" \
"v_smfmac_f32_16x16x64_bf16 v[56:59], v[88:91], v[64:71], v4\nv_add_f32 v48, v48, v50\nv_smfmac_f32_16x16x64_bf16 v[60:63], v[88:91], v[224:231], v4\nv_add_f32 v52, v52, v54\nv_add_f32 v49, v49, v51\nv_smfmac_f32_16x16x64_bf16 v[56:59], v[92:95], v[248:255], v4\ns_waitcnt lgkmcnt(0)\nv_add_f32 v53, v53, v55\n" \
"v_cvt_pk_bf16_f32 v50, v48, v52\nv_smfmac_f32_16x16x64_bf16 v[60:63], v[92:95], v[72:79], v4\nv_cvt_pk_bf16_f32 v51, v49, v53\nds_write2_b32 v6, v50, v51 offset0:0 offset1:4\ns_nop 1\nv_add_f32 v56, v56, v58\nv_add_f32 v57, v57, v59\ns_nop 1\n" \
"v_add_f32 v60, v60, v62\nv_add_f32 v61, v61, v63\nv_cvt_pk_bf16_f32 v62, v56, v60\nv_cvt_pk_bf16_f32 v63, v57, v61\nds_write2_b32 v6, v62, v63 offset0:36 offset1:40\nv_mov_b32 v61, s54\ns_waitcnt lgkmcnt(0)\ns_barrier\n" \
"ds_read_b128 v[80:83], v8 offset:0\nds_read_b128 v[84:87], v8 offset:576\nds_read_b128 v[88:91], v8 offset:1152\nds_read_b128 v[92:95], v8 offset:1728\nds_read_b128 v[64:67], v9 offset:0\nds_read_b128 v[68:71], v9 offset:576\nds_read_b128 v[72:75], v9 offset:1152\nds_read_b128 v[76:79], v9 offset:1728\n" \
"ds_read_b32 v60, v0 offset:53760\ns_waitcnt lgkmcnt(8)\nv_mfma_f32_16x16x32_bf16 v[52:55], v[16:19], v[80:83], 0\nv_mfma_f32_16x16x32_bf16 v[56:59], v[20:23], v[80:83], 0\ns_waitcnt lgkmcnt(7)\nv_mfma_f32_16x16x32_bf16 v[52:55], v[24:27], v[84:87], v[52:55]\nv_mfma_f32_16x16x32_bf16 v[56:59], v[28:31], v[84:87], v[56:59]\ns_waitcnt lgkmcnt(6)\n" \
"v_mfma_f32_16x16x32_bf16 v[52:55], v[32:35], v[88:91], v[52:55]\nv_mfma_f32_16x16x32_bf16 v[56:59], v[36:39], v[88:91], v[56:59]\ns_waitcnt lgkmcnt(5)\nv_mfma_f32_16x16x32_bf16 v[52:55], v[40:43], v[92:95], v[52:55]\nv_mfma_f32_16x16x32_bf16 v[56:59], v[44:47], v[92:95], v[56:59]\ns_waitcnt lgkmcnt(4)\nv_mfma_f32_16x16x32_bf16 v[48:51], v[64:67], v[80:83], 0\ns_waitcnt lgkmcnt(3)\n" \
"v_mfma_f32_16x16x32_bf16 v[48:51], v[68:71], v[84:87], v[48:51]\ns_waitcnt lgkmcnt(2)\nds_read_b128 v[80:83], v2 offset:0\nv_mfma_f32_16x16x32_bf16 v[48:51], v[72:75], v[88:91], v[48:51]\ns_waitcnt lgkmcnt(2)\nds_read_b128 v[84:87], v2 offset:256\nv_mfma_f32_16x16x32_bf16 v[48:51], v[76:79], v[92:95], v[48:51]\nds_read_b128 v[88:91], v2 offset:2048\n" \
"v_max3_f32 v62, v52, v53, v54\nv_max3_f32 v63, v55, v56, v57\nv_max3_f32 v62, v62, v58, v59\nv_cndmask_b32 v52, v52, v56, s[62:63]\nv_cndmask_b32 v53, v53, v57, s[62:63]\nv_cndmask_b32 v54, v54, v58, s[62:63]\nv_cndmask_b32 v55, v55, v59, s[62:63]\nv_max_f32 v62, v62, v63\n" \
"v_max3_f32 v63, v62, v48, v49\nv_max3_f32 v62, v62, v48, v49\ns_waitcnt lgkmcnt(2)\nv_mfma_f32_16x16x32_bf16 v[16:19], a[0:3], v[80:83], 0\nds_read_b128 v[92:95], v2 offset:2304\nv_permlane16_swap_b32 v62, v63\nv_mfma_f32_16x16x32_bf16 v[20:23], a[8:11], v[80:83], 0\nds_read_b128 v[72:75], v1 offset:0\n" \
"v_max_f32 v50, v62, v63\nv_max_f32 v62, v62, v63\nv_mfma_f32_16x16x32_bf16 v[24:27], a[16:19], v[80:83], 0\ns_nop 0\nv_permlane32_swap_b32 v62, v50\nv_max_f32 v62, v62, v50\nv_mfma_f32_16x16x32_bf16 v[28:31], a[24:27], v[80:83], 0\ns_waitcnt lgkmcnt(3)\n" \
"v_mul_f32 v62, s59, v62\nv_fma_f32 v52, v52, s58, v62\nv_fma_f32 v53, v53, s58, v62\nv_mfma_f32_16x16x32_bf16 v[32:35], a[0:3], v[84:87], 0\nds_read_b128 v[80:83], v2 offset:4096\nv_exp_f32 v52, v52\nv_mfma_f32_16x16x32_bf16 v[36:39], a[8:11], v[84:87], 0\nv_fma_f32 v54, v54, s58, v62\n" \
"v_exp_f32 v53, v53\nv_mfma_f32_16x16x32_bf16 v[40:43], a[16:19], v[84:87], 0\nv_fma_f32 v55, v55, s58, v62\nv_exp_f32 v54, v54\nv_mfma_f32_16x16x32_bf16 v[44:47], a[24:27], v[84:87], 0\ns_waitcnt lgkmcnt(3)\nv_add_f32 v63, v52, v53\nv_fma_f32 v48, v48, s58, v62\n" \
"v_add_f32 v63, v63, v54\nv_mfma_f32_16x16x32_bf16 v[16:19], a[4:7], v[88:91], v[16:19]\nds_read_b128 v[84:87], v2 offset:4352\nv_exp_f32 v55, v55\nv_mfma_f32_16x16x32_bf16 v[20:23], a[12:15], v[88:91], v[20:23]\nds_read_b128 v[76:79], v1 offset:1024\nv_fma_f32 v49, v49, s58, v62\nv_add_f32 v63, v63, v55\n" \
"v_mfma_f32_16x16x32_bf16 v[24:27], a[20:23], v[88:91], v[24:27]\nv_exp_f32 v48, v48\nv_mfma_f32_16x16x32_bf16 v[28:31], a[28:31], v[88:91], v[28:31]\ns_waitcnt lgkmcnt(4)\nv_exp_f32 v49, v49\nv_add_f32 v50, v48, v63\nv_mfma_f32_16x16x32_bf16 v[32:35], a[4:7], v[92:95], v[32:35]\nds_read_b128 v[88:91], v2 offset:6144\n" \
"v_add_f32 v50, v50, v49\nv_mfma_f32_16x16x32_bf16 v[36:39], a[12:15], v[92:95], v[36:39]\ns_nop 0\nv_add_f32_dpp v62, v63, v50 row_ror:8 row_mask:0xf bank_mask:0xf\nv_add_f32_dpp v50, v63, v50 row_ror:8 row_mask:0xf bank_mask:0xf\nv_mfma_f32_16x16x32_bf16 v[40:43], a[20:23], v[92:95], v[40:43]\ns_nop 0\nv_permlane16_swap_b32 v62, v50\n" \
"v_add_f32 v63, v62, v50\nv_mfma_f32_16x16x32_bf16 v[44:47], a[28:31], v[92:95], v[44:47]\ns_waitcnt lgkmcnt(3)\nv_add_f32 v50, v62, v50\nv_mfma_f32_16x16x32_bf16 v[16:19], a[32:35], v[80:83], v[16:19]\nds_read_b128 v[92:95], v2 offset:6400\nv_permlane32_swap_b32 v63, v50\nv_mfma_f32_16x16x32_bf16 v[20:23], v[72:75], v[80:83], v[20:23]\n" \
"ds_read_b128 v[64:67], v1 offset:2048\nv_add_f32 v63, v63, v50\nv_mfma_f32_16x16x32_bf16 v[24:27], a[40:43], v[80:83], v[24:27]\nv_rcp_f32 v63, v63\nv_mfma_f32_16x16x32_bf16 v[28:31], a[48:51], v[80:83], v[28:31]\ns_waitcnt lgkmcnt(4)\nv_mul_f32 v52, v52, v63\nv_mul_f32 v53, v53, v63\n" \
"v_mul_f32 v54, v54, v63\nv_mfma_f32_16x16x32_bf16 v[32:35], a[32:35], v[84:87], v[32:35]\nds_read_b128 v[80:83], v2 offset:8192\nv_mul_f32 v55, v55, v63\nv_mul_f32 v48, v48, v63\nv_mfma_f32_16x16x32_bf16 v[36:39], v[72:75], v[84:87], v[36:39]\nv_mul_f32 v49, v49, v63\nv_cvt_pk_bf16_f32 v52, v52, v53\n" \
"v_cvt_pk_bf16_f32 v53, v54, v55\nv_mfma_f32_16x16x32_bf16 v[40:43], a[40:43], v[84:87], v[40:43]\nv_cvt_pk_bf16_f32 v48, v48, v49\nv_bfi_b32 v52, s61, v52, v61\nv_bfi_b32 v53, s61, v53, v61\nv_mfma_f32_16x16x32_bf16 v[44:47], a[48:51], v[84:87], v[44:47]\ns_waitcnt lgkmcnt(3)\nv_bfi_b32 v48, s61, v48, v61\n" \
"v_mov_b32_dpp v54, v52 row_ror:8 row_mask:0xf bank_mask:0xf\nv_mov_b32_dpp v55, v53 row_ror:8 row_mask:0xf bank_mask:0xf\nv_mfma_f32_16x16x32_bf16 v[16:19], a[36:39], v[88:91], v[16:19]\nds_read_b128 v[84:87], v2 offset:8448\nv_mfma_f32_16x16x32_bf16 v[20:23], v[76:79], v[88:91], v[20:23]\nds_read_b128 v[68:71], v1 offset:3072\nv_mfma_f32_16x16x32_bf16 v[24:27], a[44:47], v[88:91], v[24:27]\nbuffer_store_dwordx4 v[52:55], v10, s[36:39], 0 offen sc1\n" \
"v_mov_b32 v49, v48\nv_mfma_f32_16x16x32_bf16 v[28:31], a[52:55], v[88:91], v[28:31]\ns_waitcnt lgkmcnt(4)\nv_permlane32_swap_b32 v48, v49\nv_mfma_f32_16x16x32_bf16 v[32:35], a[36:39], v[92:95], v[32:35]\nds_read_b128 v[88:91], v2 offset:10240\nv_mfma_f32_16x16x32_bf16 v[36:39], v[76:79], v[92:95], v[36:39]\ns_memrealtime s[86:87]\n" \
"buffer_store_dwordx2 v[48:49], v11, s[36:39], 0 offen sc1\nv_cmp_ne_u32 vcc, 0, v60\nv_mfma_f32_16x16x32_bf16 v[40:43], a[44:47], v[92:95], v[40:43]\nv_mfma_f32_16x16x32_bf16 v[44:47], a[52:55], v[92:95], v[44:47]\ns_waitcnt lgkmcnt(3)\nv_mfma_f32_16x16x32_bf16 v[16:19], a[56:59], v[80:83], v[16:19]\nds_read_b128 v[92:95], v2 offset:10496\nv_mfma_f32_16x16x32_bf16 v[20:23], a[64:67], v[80:83], v[20:23]\n" \
"ds_read_b128 v[72:75], v1 offset:4096\nv_mfma_f32_16x16x32_bf16 v[24:27], v[64:67], v[80:83], v[24:27]\nv_mfma_f32_16x16x32_bf16 v[28:31], a[72:75], v[80:83], v[28:31]\ns_waitcnt lgkmcnt(4)\nv_mfma_f32_16x16x32_bf16 v[32:35], a[56:59], v[84:87], v[32:35]\nds_read_b128 v[80:83], v2 offset:12288\nv_mfma_f32_16x16x32_bf16 v[36:39], a[64:67], v[84:87], v[36:39]\nv_mfma_f32_16x16x32_bf16 v[40:43], v[64:67], v[84:87], v[40:43]\n" \
"v_mfma_f32_16x16x32_bf16 v[44:47], a[72:75], v[84:87], v[44:47]\ns_waitcnt lgkmcnt(3)\nv_mfma_f32_16x16x32_bf16 v[16:19], a[60:63], v[88:91], v[16:19]\nds_read_b128 v[84:87], v2 offset:12544\nv_mfma_f32_16x16x32_bf16 v[20:23], a[68:71], v[88:91], v[20:23]\nds_read_b128 v[76:79], v1 offset:5120\nv_mfma_f32_16x16x32_bf16 v[24:27], v[68:71], v[88:91], v[24:27]\nv_mfma_f32_16x16x32_bf16 v[28:31], a[76:79], v[88:91], v[28:31]\n" \
"s_waitcnt lgkmcnt(4)\nv_mfma_f32_16x16x32_bf16 v[32:35], a[60:63], v[92:95], v[32:35]\nds_read_b128 v[88:91], v2 offset:14336\nv_mfma_f32_16x16x32_bf16 v[36:39], a[68:71], v[92:95], v[36:39]\nv_mfma_f32_16x16x32_bf16 v[40:43], v[68:71], v[92:95], v[40:43]\nv_mfma_f32_16x16x32_bf16 v[44:47], a[76:79], v[92:95], v[44:47]\ns_cbranch_vccnz .Lqk_pj%=\ns_cmp_lt_i32 s40, 63\n" \
"s_waitcnt lgkmcnt(0)\ns_cbranch_scc1 .Lqk_pwait%=\ns_branch .Lqk_end%=\n.Lqk_re0%=:\nv_max_u32 v17, v80, v82\nv_cmp_eq_u32 vcc, -1, v17\ns_cbranch_vccz .Lqk_sk1617%=\ns_mov_b64 exec, vcc\n" \
"buffer_load_dwordx4 v[80:83], v3, s[36:39], s42 offen offset:0 sc1\ns_mov_b64 exec, -1\n.Lqk_sk1617%=:\nv_max_u32 v17, v84, v86\nv_cmp_eq_u32 vcc, -1, v17\ns_cbranch_vccz .Lqk_sk1624%=\ns_mov_b64 exec, vcc\nbuffer_load_dwordx4 v[84:87], v3, s[36:39], s42 offen offset:1024 sc1\n" \
"s_mov_b64 exec, -1\n.Lqk_sk1624%=:\nv_max_u32 v17, v88, v90\nv_cmp_eq_u32 vcc, -1, v17\ns_cbranch_vccz .Lqk_sk1631%=\ns_mov_b64 exec, vcc\nbuffer_load_dwordx4 v[88:91], v3, s[36:39], s42 offen offset:2048 sc1\ns_mov_b64 exec, -1\n" \
".Lqk_sk1631%=:\nv_max_u32 v17, v92, v94\nv_cmp_eq_u32 vcc, -1, v17\ns_cbranch_vccz .Lqk_sk1638%=\ns_mov_b64 exec, vcc\nbuffer_load_dwordx4 v[92:95], v3, s[36:39], s42 offen offset:3072 sc1\ns_mov_b64 exec, -1\n.Lqk_sk1638%=:\n" \
"v_mov_b64 v[48:49], 0\nv_mov_b64 v[50:51], 0\nv_mov_b64 v[52:53], 0\nv_mov_b64 v[54:55], 0\nv_mov_b64 v[56:57], 0\nv_mov_b64 v[58:59], 0\nv_mov_b64 v[60:61], 0\nv_mov_b64 v[62:63], 0\n" \
"s_add_u32 s43, s43, 1\ns_cmp_lt_u32 s43, 262144\ns_cbranch_scc1 .Lqk_poll0%=\ns_branch .Lqk_abort%=\n.Lqk_c1%=:\ns_waitcnt lgkmcnt(3)\nv_mfma_f32_16x16x32_bf16 v[32:35], v[232:235], v[56:59], v[32:35]\nv_mov_b64 v[52:53], 0\n" \
"v_mov_b64 v[54:55], 0\nv_mfma_f32_16x16x32_bf16 v[36:39], v[240:243], v[56:59], v[36:39]\ns_waitcnt lgkmcnt(2)\nds_read_b128 v[64:67], v1 offset:6144\nv_mfma_f32_16x16x32_bf16 v[32:35], v[236:239], v[60:63], v[32:35]\nds_read_b128 v[68:71], v1 offset:7168\nv_mfma_f32_16x16x32_bf16 v[36:39], v[244:247], v[60:63], v[36:39]\ns_bfe_i32 s54, s40, 0x10001\n" \
"v_mfma_f32_16x16x32_bf16 v[40:43], v[248:251], v[56:59], v[40:43]\nv_mfma_f32_16x16x32_bf16 v[44:47], v[72:75], v[56:59], v[44:47]\nv_cvt_pk_bf16_f32 v36, v32, v36\nv_cvt_pk_bf16_f32 v37, v33, v37\nv_mfma_f32_16x16x32_bf16 v[40:43], v[252:255], v[60:63], v[40:43]\nv_cvt_pk_bf16_f32 v38, v34, v38\nv_cvt_pk_bf16_f32 v39, v35, v39\nv_mfma_f32_16x16x32_bf16 v[44:47], v[76:79], v[60:63], v[44:47]\n" \
"ds_read_b128 v[72:75], v1 offset:8192\nds_read_b128 v[76:79], v1 offset:9216\nv_mov_b64 v[56:57], 0\nv_mov_b64 v[58:59], 0\nv_mov_b64 v[60:61], 0\nv_mov_b64 v[62:63], 0\n.Lqk_poll1%=:\ns_waitcnt vmcnt(0)\n" \
"v_smfmac_f32_16x16x64_bf16 v[48:51], v[80:83], a[104:111], v4\nds_write_b128 v0, v[80:83] offset:20480\nv_max3_u32 v16, v80, v82, v84\nv_smfmac_f32_16x16x64_bf16 v[52:55], v[80:83], a[112:119], v4\nv_max3_u32 v16, v16, v86, v88\nv_smfmac_f32_16x16x64_bf16 v[56:59], v[80:83], a[120:127], v4\nds_write_b128 v0, v[84:87] offset:21504\nv_max3_u32 v16, v16, v90, v92\n" \
"v_max_u32 v16, v16, v94\nv_smfmac_f32_16x16x64_bf16 v[60:63], v[80:83], a[128:135], v4\nv_cmp_eq_u32 vcc, -1, v16\nv_smfmac_f32_16x16x64_bf16 v[48:51], v[84:87], a[136:143], v4\nds_write_b128 v0, v[88:91] offset:22528\ns_cbranch_vccnz .Lqk_re1%=\ns_waitcnt lgkmcnt(5)\nv_smfmac_f32_16x16x64_bf16 v[52:55], v[84:87], v[64:71], v4\n" \
"v_smfmac_f32_16x16x64_bf16 v[56:59], v[84:87], a[144:151], v4\nds_write_b128 v0, v[92:95] offset:23552\nv_smfmac_f32_16x16x64_bf16 v[60:63], v[84:87], a[152:159], v4\nv_smfmac_f32_16x16x64_bf16 v[48:51], v[88:91], a[160:167], v4\nv_smfmac_f32_16x16x64_bf16 v[52:55], v[88:91], a[168:175], v4\ns_waitcnt lgkmcnt(0)\n.Lqk_jn1%=:\ns_barrier\n" \
"ds_read_b128 v[80:83], v0 offset:24576\nds_read_b128 v[64:67], v1 offset:10240\nds_read_b128 v[68:71], v1 offset:11264\nv_smfmac_f32_16x16x64_bf16 v[56:59], v[88:91], v[72:79], v4\nds_read_b128 v[72:75], v1 offset:12288\nds_read_b128 v[76:79], v1 offset:13312\nv_smfmac_f32_16x16x64_bf16 v[60:63], v[88:91], a[176:183], v4\nds_read_b128 v[84:87], v0 offset:25600\n" \
"ds_read_b128 v[88:91], v0 offset:26624\nv_smfmac_f32_16x16x64_bf16 v[48:51], v[92:95], a[184:191], v4\nds_write_b128 v5, v[36:39] offset:256\nv_cvt_pk_bf16_f32 v44, v40, v44\nv_cvt_pk_bf16_f32 v45, v41, v45\nv_cvt_pk_bf16_f32 v46, v42, v46\nv_cvt_pk_bf16_f32 v47, v43, v47\nv_smfmac_f32_16x16x64_bf16 v[52:55], v[92:95], a[192:199], v4\n" \
"v_smfmac_f32_16x16x64_bf16 v[56:59], v[92:95], a[200:207], v4\ns_waitcnt lgkmcnt(5)\nv_smfmac_f32_16x16x64_bf16 v[60:63], v[92:95], v[64:71], v4\nds_read_b128 v[92:95], v0 offset:27648\nds_read_b128 v[64:67], v1 offset:14336\nds_read_b128 v[68:71], v1 offset:15360\nv_smfmac_f32_16x16x64_bf16 v[48:51], v[80:83], a[208:215], v4\nds_write_b128 v5, v[44:47] offset:768\n" \
"v_smfmac_f32_16x16x64_bf16 v[52:55], v[80:83], a[216:223], v4\nv_smfmac_f32_16x16x64_bf16 v[56:59], v[80:83], a[224:231], v4\nv_smfmac_f32_16x16x64_bf16 v[60:63], v[80:83], a[232:239], v4\ns_waitcnt lgkmcnt(6)\nds_read_b128 v[80:83], v0 offset:28672\nv_smfmac_f32_16x16x64_bf16 v[48:51], v[84:87], a[240:247], v4\nv_smfmac_f32_16x16x64_bf16 v[52:55], v[84:87], v[72:79], v4\nds_read_b128 v[72:75], v1 offset:16384\n" \
"ds_read_b128 v[76:79], v1 offset:17408\nv_smfmac_f32_16x16x64_bf16 v[56:59], v[84:87], a[248:255], v4\nv_smfmac_f32_16x16x64_bf16 v[60:63], v[84:87], v[96:103], v4\ns_waitcnt lgkmcnt(8)\nds_read_b128 v[84:87], v0 offset:29696\nv_smfmac_f32_16x16x64_bf16 v[48:51], v[88:91], v[104:111], v4\ns_waitcnt lgkmcnt(4)\ns_barrier\n" \
"v_smfmac_f32_16x16x64_bf16 v[52:55], v[88:91], v[112:119], v4\nv_smfmac_f32_16x16x64_bf16 v[56:59], v[88:91], v[64:71], v4\nds_read_b128 v[64:67], v1 offset:18432\nds_read_b128 v[68:71], v1 offset:19456\nv_smfmac_f32_16x16x64_bf16 v[60:63], v[88:91], v[120:127], v4\nds_read_b128 v[88:91], v0 offset:30720\nv_smfmac_f32_16x16x64_bf16 v[48:51], v[92:95], v[128:135], v4\nds_read_b128 v[16:19], v7 offset:0\n" \
"v_smfmac_f32_16x16x64_bf16 v[52:55], v[92:95], v[136:143], v4\nv_smfmac_f32_16x16x64_bf16 v[56:59], v[92:95], v[144:151], v4\ns_waitcnt lgkmcnt(5)\nds_read_b128 v[20:23], v7 offset:2048\nv_smfmac_f32_16x16x64_bf16 v[60:63], v[92:95], v[72:79], v4\nds_read_b128 v[92:95], v0 offset:31744\nds_read_b128 v[72:75], v1 offset:20480\nds_read_b128 v[76:79], v1 offset:21504\n" \
"v_smfmac_f32_16x16x64_bf16 v[48:51], v[80:83], v[152:159], v4\nds_read_b128 v[24:27], v7 offset:4096\nv_smfmac_f32_16x16x64_bf16 v[52:55], v[80:83], v[160:167], v4\nv_smfmac_f32_16x16x64_bf16 v[56:59], v[80:83], v[168:175], v4\nds_read_b128 v[28:31], v7 offset:6144\nv_smfmac_f32_16x16x64_bf16 v[60:63], v[80:83], v[176:183], v4\ns_waitcnt lgkmcnt(10)\nds_read_b128 v[80:83], v0 offset:16384\n" \
"v_smfmac_f32_16x16x64_bf16 v[48:51], v[84:87], v[184:191], v4\ns_waitcnt lgkmcnt(9)\nds_read_b128 v[32:35], v7 offset:8192\nv_smfmac_f32_16x16x64_bf16 v[52:55], v[84:87], v[64:71], v4\nds_read_b128 v[64:67], v1 offset:22528\nds_read_b128 v[68:71], v1 offset:23552\nv_smfmac_f32_16x16x64_bf16 v[56:59], v[84:87], v[192:199], v4\nds_read_b128 v[36:39], v7 offset:10240\n" \
"v_smfmac_f32_16x16x64_bf16 v[60:63], v[84:87], v[200:207], v4\ns_waitcnt lgkmcnt(12)\nds_read_b128 v[84:87], v0 offset:17408\nv_smfmac_f32_16x16x64_bf16 v[48:51], v[88:91], v[208:215], v4\nds_read_b128 v[40:43], v7 offset:12288\nv_smfmac_f32_16x16x64_bf16 v[52:55], v[88:91], v[216:223], v4\ns_waitcnt lgkmcnt(9)\nv_smfmac_f32_16x16x64_bf16 v[56:59], v[88:91], v[72:79], v4\n" \
"ds_read_b128 v[72:75], v1 offset:0\nds_read_b128 v[76:79], v1 offset:1024\nv_smfmac_f32_16x16x64_bf16 v[60:63], v[88:91], v[224:231], v4\nds_read_b128 v[88:91], v0 offset:18432\nv_smfmac_f32_16x16x64_bf16 v[48:51], v[92:95], v[232:239], v4\nds_read_b128 v[44:47], v7 offset:14336\nv_smfmac_f32_16x16x64_bf16 v[52:55], v[92:95], v[240:247], v4\nv_smfmac_f32_16x16x64_bf16 v[56:59], v[92:95], v[248:255], v4\n" \
"s_waitcnt lgkmcnt(7)\nv_smfmac_f32_16x16x64_bf16 v[60:63], v[92:95], v[64:71], v4\nds_read_b128 v[92:95], v0 offset:19456\nds_read_b128 v[64:67], v1 offset:2048\nds_read_b128 v[68:71], v1 offset:3072\nv_smfmac_f32_16x16x64_bf16 v[48:51], v[80:83], a[0:7], v4\nv_smfmac_f32_16x16x64_bf16 v[52:55], v[80:83], a[8:15], v4\nv_smfmac_f32_16x16x64_bf16 v[56:59], v[80:83], a[16:23], v4\n" \
"v_smfmac_f32_16x16x64_bf16 v[60:63], v[80:83], a[24:31], v4\ns_waitcnt lgkmcnt(8)\nv_smfmac_f32_16x16x64_bf16 v[48:51], v[84:87], a[32:39], v4\ns_waitcnt lgkmcnt(5)\nv_smfmac_f32_16x16x64_bf16 v[52:55], v[84:87], v[72:79], v4\nds_read_b128 v[72:75], v1 offset:4096\nds_read_b128 v[76:79], v1 offset:5120\nv_smfmac_f32_16x16x64_bf16 v[56:59], v[84:87], a[40:47], v4\n" \
"v_smfmac_f32_16x16x64_bf16 v[60:63], v[84:87], a[48:55], v4\ns_waitcnt lgkmcnt(6)\nv_smfmac_f32_16x16x64_bf16 v[48:51], v[88:91], a[56:63], v4\nv_smfmac_f32_16x16x64_bf16 v[52:55], v[88:91], a[64:71], v4\ns_waitcnt lgkmcnt(4)\nv_smfmac_f32_16x16x64_bf16 v[48:51], v[92:95], a[80:87], v4\nv_smfmac_f32_16x16x64_bf16 v[52:55], v[92:95], a[88:95], v4\ns_waitcnt lgkmcnt(2)\n" \
"v_smfmac_f32_16x16x64_bf16 v[56:59], v[88:91], v[64:71], v4\nv_add_f32 v48, v48, v50\nv_smfmac_f32_16x16x64_bf16 v[60:63], v[88:91], a[72:79], v4\nv_add_f32 v52, v52, v54\nv_add_f32 v49, v49, v51\nv_smfmac_f32_16x16x64_bf16 v[56:59], v[92:95], a[96:103], v4\ns_waitcnt lgkmcnt(0)\nv_add_f32 v53, v53, v55\n" \
"v_cvt_pk_bf16_f32 v50, v48, v52\nv_smfmac_f32_16x16x64_bf16 v[60:63], v[92:95], v[72:79], v4\nv_cvt_pk_bf16_f32 v51, v49, v53\nds_write2_b32 v6, v50, v51 offset0:0 offset1:4\ns_nop 1\nv_add_f32 v56, v56, v58\nv_add_f32 v57, v57, v59\ns_nop 1\n" \
"v_add_f32 v60, v60, v62\nv_add_f32 v61, v61, v63\nv_cvt_pk_bf16_f32 v62, v56, v60\nv_cvt_pk_bf16_f32 v63, v57, v61\nds_write2_b32 v6, v62, v63 offset0:36 offset1:40\nv_mov_b32 v61, s54\nv_mfma_f32_16x16x32_bf16 v[52:55], v[32:35], v[16:19], v[12:15]\nv_mfma_f32_16x16x32_bf16 v[52:55], v[36:39], v[20:23], v[52:55]\n" \
"v_mfma_f32_16x16x32_bf16 v[52:55], v[40:43], v[24:27], v[52:55]\nv_mfma_f32_16x16x32_bf16 v[52:55], v[44:47], v[28:31], v[52:55]\ns_waitcnt lgkmcnt(0)\ns_barrier\nds_read_b128 v[32:35], v9 offset:0\nds_read_b128 v[36:39], v9 offset:576\nds_read_b128 v[40:43], v9 offset:1152\nds_read_b128 v[44:47], v9 offset:1728\n" \
"ds_read_b32 v60, v0 offset:53760\nds_read_b128 v[80:83], v2 offset:0\nds_read_b128 v[84:87], v2 offset:256\nds_read_b128 v[88:91], v2 offset:2048\ns_waitcnt lgkmcnt(7)\nv_mfma_f32_16x16x32_bf16 v[48:51], v[32:35], v[16:19], 0\ns_waitcnt lgkmcnt(6)\nv_mfma_f32_16x16x32_bf16 v[48:51], v[36:39], v[20:23], v[48:51]\n" \
"s_waitcnt lgkmcnt(5)\nv_mfma_f32_16x16x32_bf16 v[48:51], v[40:43], v[24:27], v[48:51]\ns_waitcnt lgkmcnt(4)\nv_mfma_f32_16x16x32_bf16 v[48:51], v[44:47], v[28:31], v[48:51]\ns_waitcnt lgkmcnt(2)\nv_mfma_f32_16x16x32_bf16 v[16:19], a[0:3], v[80:83], 0\nds_read_b128 v[92:95], v2 offset:2304\nv_max3_f32 v62, v52, v53, v54\n" \
"v_max_f32 v62, v62, v55\nv_mfma_f32_16x16x32_bf16 v[20:23], a[8:11], v[80:83], 0\nds_read_b128 v[72:75], v1 offset:0\nv_max3_f32 v63, v62, v48, v49\nv_max3_f32 v62, v62, v48, v49\nv_mfma_f32_16x16x32_bf16 v[24:27], a[16:19], v[80:83], 0\ns_nop 0\nv_permlane16_swap_b32 v62, v63\n" \
"v_max_f32 v50, v62, v63\nv_mfma_f32_16x16x32_bf16 v[28:31], a[24:27], v[80:83], 0\ns_waitcnt lgkmcnt(3)\nv_max_f32 v62, v62, v63\nv_mfma_f32_16x16x32_bf16 v[32:35], a[0:3], v[84:87], 0\nds_read_b128 v[80:83], v2 offset:4096\nv_permlane32_swap_b32 v62, v50\nv_mfma_f32_16x16x32_bf16 v[36:39], a[8:11], v[84:87], 0\n" \
"v_max_f32 v62, v62, v50\nv_mul_f32 v62, s59, v62\nv_fma_f32 v52, v52, s58, v62\nv_mfma_f32_16x16x32_bf16 v[40:43], a[16:19], v[84:87], 0\nv_fma_f32 v53, v53, s58, v62\nv_exp_f32 v52, v52\nv_mfma_f32_16x16x32_bf16 v[44:47], a[24:27], v[84:87], 0\ns_waitcnt lgkmcnt(3)\n" \
"v_fma_f32 v54, v54, s58, v62\nv_exp_f32 v53, v53\nv_mfma_f32_16x16x32_bf16 v[16:19], a[4:7], v[88:91], v[16:19]\nds_read_b128 v[84:87], v2 offset:4352\nv_fma_f32 v55, v55, s58, v62\nv_add_f32 v63, v52, v53\nv_mfma_f32_16x16x32_bf16 v[20:23], a[12:15], v[88:91], v[20:23]\nds_read_b128 v[76:79], v1 offset:1024\n" \
"v_exp_f32 v54, v54\nv_mfma_f32_16x16x32_bf16 v[24:27], a[20:23], v[88:91], v[24:27]\nv_fma_f32 v48, v48, s58, v62\nv_exp_f32 v55, v55\nv_mfma_f32_16x16x32_bf16 v[28:31], a[28:31], v[88:91], v[28:31]\ns_waitcnt lgkmcnt(4)\nv_add_f32 v63, v63, v54\nv_fma_f32 v49, v49, s58, v62\n" \
"v_add_f32 v63, v63, v55\nv_mfma_f32_16x16x32_bf16 v[32:35], a[4:7], v[92:95], v[32:35]\nds_read_b128 v[88:91], v2 offset:6144\nv_exp_f32 v48, v48\nv_mfma_f32_16x16x32_bf16 v[36:39], a[12:15], v[92:95], v[36:39]\nv_exp_f32 v49, v49\nv_add_f32 v63, v63, v48\nv_mfma_f32_16x16x32_bf16 v[40:43], a[20:23], v[92:95], v[40:43]\n" \
"v_add_f32 v62, v63, v49\nv_add_f32 v50, v63, v49\nv_mfma_f32_16x16x32_bf16 v[44:47], a[28:31], v[92:95], v[44:47]\ns_waitcnt lgkmcnt(3)\nv_permlane16_swap_b32 v62, v50\nv_add_f32 v63, v62, v50\nv_mfma_f32_16x16x32_bf16 v[16:19], a[32:35], v[80:83], v[16:19]\nds_read_b128 v[92:95], v2 offset:6400\n" \
"v_add_f32 v50, v62, v50\nv_mfma_f32_16x16x32_bf16 v[20:23], v[72:75], v[80:83], v[20:23]\nds_read_b128 v[64:67], v1 offset:2048\nv_permlane32_swap_b32 v63, v50\nv_mfma_f32_16x16x32_bf16 v[24:27], a[40:43], v[80:83], v[24:27]\nv_add_f32 v63, v63, v50\nv_rcp_f32 v63, v63\nv_mfma_f32_16x16x32_bf16 v[28:31], a[48:51], v[80:83], v[28:31]\n" \
"s_waitcnt lgkmcnt(4)\nv_mul_f32 v52, v52, v63\nv_mul_f32 v53, v53, v63\nv_mul_f32 v54, v54, v63\nv_mfma_f32_16x16x32_bf16 v[32:35], a[32:35], v[84:87], v[32:35]\nds_read_b128 v[80:83], v2 offset:8192\nv_mul_f32 v55, v55, v63\nv_mul_f32 v48, v48, v63\n" \
"v_mfma_f32_16x16x32_bf16 v[36:39], v[72:75], v[84:87], v[36:39]\nv_mul_f32 v49, v49, v63\nv_cvt_pk_bf16_f32 v52, v52, v53\nv_cvt_pk_bf16_f32 v53, v54, v55\nv_mfma_f32_16x16x32_bf16 v[40:43], a[40:43], v[84:87], v[40:43]\nv_cvt_pk_bf16_f32 v48, v48, v49\nv_bfi_b32 v52, s61, v52, v61\nv_bfi_b32 v53, s61, v53, v61\n" \
"v_mfma_f32_16x16x32_bf16 v[44:47], a[48:51], v[84:87], v[44:47]\ns_waitcnt lgkmcnt(3)\nv_bfi_b32 v48, s61, v48, v61\nv_mfma_f32_16x16x32_bf16 v[16:19], a[36:39], v[88:91], v[16:19]\nds_read_b128 v[84:87], v2 offset:8448\nv_mfma_f32_16x16x32_bf16 v[20:23], v[76:79], v[88:91], v[20:23]\nds_read_b128 v[68:71], v1 offset:3072\nv_mfma_f32_16x16x32_bf16 v[24:27], a[44:47], v[88:91], v[24:27]\n" \
"buffer_store_dwordx2 v[52:53], v10, s[36:39], s55 offen sc1\nv_mov_b32 v49, v48\nv_mfma_f32_16x16x32_bf16 v[28:31], a[52:55], v[88:91], v[28:31]\ns_waitcnt lgkmcnt(4)\nv_permlane16_swap_b32 v48, v49\nv_mov_b32 v50, v48\nv_mfma_f32_16x16x32_bf16 v[32:35], a[36:39], v[92:95], v[32:35]\nds_read_b128 v[88:91], v2 offset:10240\n" \
"v_mov_b32 v51, v49\nv_mfma_f32_16x16x32_bf16 v[36:39], v[76:79], v[92:95], v[36:39]\nv_permlane32_swap_b32 v48, v50\nv_mfma_f32_16x16x32_bf16 v[40:43], a[44:47], v[92:95], v[40:43]\nv_permlane32_swap_b32 v49, v51\nv_mfma_f32_16x16x32_bf16 v[44:47], a[52:55], v[92:95], v[44:47]\ns_waitcnt lgkmcnt(3)\nbuffer_store_dwordx4 v[48:51], v11, s[36:39], s55 offen sc1\n" \
"v_cmp_ne_u32 vcc, 0, v60\nv_mfma_f32_16x16x32_bf16 v[16:19], a[56:59], v[80:83], v[16:19]\nds_read_b128 v[92:95], v2 offset:10496\nv_mfma_f32_16x16x32_bf16 v[20:23], a[64:67], v[80:83], v[20:23]\nds_read_b128 v[72:75], v1 offset:4096\nv_mfma_f32_16x16x32_bf16 v[24:27], v[64:67], v[80:83], v[24:27]\nv_mfma_f32_16x16x32_bf16 v[28:31], a[72:75], v[80:83], v[28:31]\ns_waitcnt lgkmcnt(4)\n" \
"v_mfma_f32_16x16x32_bf16 v[32:35], a[56:59], v[84:87], v[32:35]\nds_read_b128 v[80:83], v2 offset:12288\nv_mfma_f32_16x16x32_bf16 v[36:39], a[64:67], v[84:87], v[36:39]\nv_mfma_f32_16x16x32_bf16 v[40:43], v[64:67], v[84:87], v[40:43]\nv_mfma_f32_16x16x32_bf16 v[44:47], a[72:75], v[84:87], v[44:47]\ns_waitcnt lgkmcnt(3)\nv_mfma_f32_16x16x32_bf16 v[16:19], a[60:63], v[88:91], v[16:19]\nds_read_b128 v[84:87], v2 offset:12544\n" \
"v_mfma_f32_16x16x32_bf16 v[20:23], a[68:71], v[88:91], v[20:23]\nds_read_b128 v[76:79], v1 offset:5120\nv_mfma_f32_16x16x32_bf16 v[24:27], v[68:71], v[88:91], v[24:27]\nv_mfma_f32_16x16x32_bf16 v[28:31], a[76:79], v[88:91], v[28:31]\ns_waitcnt lgkmcnt(4)\nv_mfma_f32_16x16x32_bf16 v[32:35], a[60:63], v[92:95], v[32:35]\nds_read_b128 v[88:91], v2 offset:14336\nv_mfma_f32_16x16x32_bf16 v[36:39], a[68:71], v[92:95], v[36:39]\n" \
"v_mfma_f32_16x16x32_bf16 v[40:43], v[68:71], v[92:95], v[40:43]\nv_mfma_f32_16x16x32_bf16 v[44:47], a[76:79], v[92:95], v[44:47]\ns_cbranch_vccnz .Lqk_pj%=\ns_cmp_lt_i32 s40, 63\ns_waitcnt lgkmcnt(0)\ns_cbranch_scc1 .Lqk_pwait%=\ns_branch .Lqk_end%=\n.Lqk_re1%=:\n" \
"v_max_u32 v17, v80, v82\nv_cmp_eq_u32 vcc, -1, v17\ns_cbranch_vccz .Lqk_sk2002%=\ns_mov_b64 exec, vcc\nbuffer_load_dwordx4 v[80:83], v3, s[36:39], s42 offen offset:0 sc1\ns_mov_b64 exec, -1\n.Lqk_sk2002%=:\nv_max_u32 v17, v84, v86\n" \
"v_cmp_eq_u32 vcc, -1, v17\ns_cbranch_vccz .Lqk_sk2009%=\ns_mov_b64 exec, vcc\nbuffer_load_dwordx4 v[84:87], v3, s[36:39], s42 offen offset:1024 sc1\ns_mov_b64 exec, -1\n.Lqk_sk2009%=:\nv_max_u32 v17, v88, v90\nv_cmp_eq_u32 vcc, -1, v17\n" \
"s_cbranch_vccz .Lqk_sk2016%=\ns_mov_b64 exec, vcc\nbuffer_load_dwordx4 v[88:91], v3, s[36:39], s42 offen offset:2048 sc1\ns_mov_b64 exec, -1\n.Lqk_sk2016%=:\nv_max_u32 v17, v92, v94\nv_cmp_eq_u32 vcc, -1, v17\ns_cbranch_vccz .Lqk_sk2023%=\n" \
"s_mov_b64 exec, vcc\nbuffer_load_dwordx4 v[92:95], v3, s[36:39], s42 offen offset:3072 sc1\ns_mov_b64 exec, -1\n.Lqk_sk2023%=:\nv_mov_b64 v[48:49], 0\nv_mov_b64 v[50:51], 0\nv_mov_b64 v[52:53], 0\nv_mov_b64 v[54:55], 0\n" \
"v_mov_b64 v[56:57], 0\nv_mov_b64 v[58:59], 0\nv_mov_b64 v[60:61], 0\nv_mov_b64 v[62:63], 0\ns_add_u32 s43, s43, 1\ns_cmp_lt_u32 s43, 262144\ns_cbranch_scc1 .Lqk_poll1%=\ns_branch .Lqk_abort%=\n" \
".Lqk_c2%=:\ns_waitcnt lgkmcnt(3)\nv_mfma_f32_16x16x32_bf16 v[32:35], v[232:235], v[56:59], v[32:35]\nv_mov_b64 v[52:53], 0\nv_mov_b64 v[54:55], 0\nv_mfma_f32_16x16x32_bf16 v[36:39], v[240:243], v[56:59], v[36:39]\ns_waitcnt lgkmcnt(2)\nds_read_b128 v[64:67], v1 offset:12288\n" \
"v_mfma_f32_16x16x32_bf16 v[32:35], v[236:239], v[60:63], v[32:35]\nds_read_b128 v[68:71], v1 offset:13312\nv_mfma_f32_16x16x32_bf16 v[36:39], v[244:247], v[60:63], v[36:39]\ns_bfe_i32 s54, s40, 0x10001\nv_mfma_f32_16x16x32_bf16 v[40:43], v[248:251], v[56:59], v[40:43]\nv_mfma_f32_16x16x32_bf16 v[44:47], v[72:75], v[56:59], v[44:47]\nv_cvt_pk_bf16_f32 v36, v32, v36\nv_cvt_pk_bf16_f32 v37, v33, v37\n" \
"v_mfma_f32_16x16x32_bf16 v[40:43], v[252:255], v[60:63], v[40:43]\nv_cvt_pk_bf16_f32 v38, v34, v38\nv_cvt_pk_bf16_f32 v39, v35, v39\nv_mfma_f32_16x16x32_bf16 v[44:47], v[76:79], v[60:63], v[44:47]\nds_read_b128 v[72:75], v1 offset:14336\nds_read_b128 v[76:79], v1 offset:15360\nv_mov_b64 v[56:57], 0\nv_mov_b64 v[58:59], 0\n" \
"v_mov_b64 v[60:61], 0\nv_mov_b64 v[62:63], 0\n.Lqk_poll2%=:\ns_waitcnt vmcnt(0)\nv_smfmac_f32_16x16x64_bf16 v[48:51], v[80:83], a[208:215], v4\nds_write_b128 v0, v[80:83] offset:24576\nv_max3_u32 v16, v80, v82, v84\nv_smfmac_f32_16x16x64_bf16 v[52:55], v[80:83], a[216:223], v4\n" \
"v_max3_u32 v16, v16, v86, v88\nv_smfmac_f32_16x16x64_bf16 v[56:59], v[80:83], a[224:231], v4\nds_write_b128 v0, v[84:87] offset:25600\nv_max3_u32 v16, v16, v90, v92\nv_max_u32 v16, v16, v94\nv_smfmac_f32_16x16x64_bf16 v[60:63], v[80:83], a[232:239], v4\nv_cmp_eq_u32 vcc, -1, v16\nv_smfmac_f32_16x16x64_bf16 v[48:51], v[84:87], a[240:247], v4\n" \
"ds_write_b128 v0, v[88:91] offset:26624\ns_cbranch_vccnz .Lqk_re2%=\ns_waitcnt lgkmcnt(5)\nv_smfmac_f32_16x16x64_bf16 v[52:55], v[84:87], v[64:71], v4\nv_smfmac_f32_16x16x64_bf16 v[56:59], v[84:87], a[248:255], v4\nds_write_b128 v0, v[92:95] offset:27648\nv_smfmac_f32_16x16x64_bf16 v[60:63], v[84:87], v[96:103], v4\nv_smfmac_f32_16x16x64_bf16 v[48:51], v[88:91], v[104:111], v4\n" \
"v_smfmac_f32_16x16x64_bf16 v[52:55], v[88:91], v[112:119], v4\ns_waitcnt lgkmcnt(0)\n.Lqk_jn2%=:\ns_barrier\nds_read_b128 v[80:83], v0 offset:28672\nds_read_b128 v[64:67], v1 offset:16384\nds_read_b128 v[68:71], v1 offset:17408\nv_smfmac_f32_16x16x64_bf16 v[56:59], v[88:91], v[72:79], v4\n" \
"ds_read_b128 v[72:75], v1 offset:18432\nds_read_b128 v[76:79], v1 offset:19456\nv_smfmac_f32_16x16x64_bf16 v[60:63], v[88:91], v[120:127], v4\nds_read_b128 v[84:87], v0 offset:29696\nds_read_b128 v[88:91], v0 offset:30720\nv_smfmac_f32_16x16x64_bf16 v[48:51], v[92:95], v[128:135], v4\nds_write_b128 v5, v[36:39] offset:256\nv_cvt_pk_bf16_f32 v44, v40, v44\n" \
"v_cvt_pk_bf16_f32 v45, v41, v45\nv_cvt_pk_bf16_f32 v46, v42, v46\nv_cvt_pk_bf16_f32 v47, v43, v47\nv_smfmac_f32_16x16x64_bf16 v[52:55], v[92:95], v[136:143], v4\nv_smfmac_f32_16x16x64_bf16 v[56:59], v[92:95], v[144:151], v4\ns_waitcnt lgkmcnt(5)\nv_smfmac_f32_16x16x64_bf16 v[60:63], v[92:95], v[64:71], v4\nds_read_b128 v[92:95], v0 offset:31744\n" \
"ds_read_b128 v[64:67], v1 offset:20480\nds_read_b128 v[68:71], v1 offset:21504\nv_smfmac_f32_16x16x64_bf16 v[48:51], v[80:83], v[152:159], v4\nds_write_b128 v5, v[44:47] offset:768\nv_smfmac_f32_16x16x64_bf16 v[52:55], v[80:83], v[160:167], v4\nv_smfmac_f32_16x16x64_bf16 v[56:59], v[80:83], v[168:175], v4\nv_smfmac_f32_16x16x64_bf16 v[60:63], v[80:83], v[176:183], v4\ns_waitcnt lgkmcnt(6)\n" \
"ds_read_b128 v[80:83], v0 offset:16384\nv_smfmac_f32_16x16x64_bf16 v[48:51], v[84:87], v[184:191], v4\nv_smfmac_f32_16x16x64_bf16 v[52:55], v[84:87], v[72:79], v4\nds_read_b128 v[72:75], v1 offset:22528\nds_read_b128 v[76:79], v1 offset:23552\nv_smfmac_f32_16x16x64_bf16 v[56:59], v[84:87], v[192:199], v4\nv_smfmac_f32_16x16x64_bf16 v[60:63], v[84:87], v[200:207], v4\ns_waitcnt lgkmcnt(8)\n" \
"ds_read_b128 v[84:87], v0 offset:17408\nv_smfmac_f32_16x16x64_bf16 v[48:51], v[88:91], v[208:215], v4\ns_waitcnt lgkmcnt(4)\ns_barrier\nv_smfmac_f32_16x16x64_bf16 v[52:55], v[88:91], v[216:223], v4\nv_smfmac_f32_16x16x64_bf16 v[56:59], v[88:91], v[64:71], v4\nds_read_b128 v[64:67], v1 offset:0\nds_read_b128 v[68:71], v1 offset:1024\n" \
"v_smfmac_f32_16x16x64_bf16 v[60:63], v[88:91], v[224:231], v4\nds_read_b128 v[88:91], v0 offset:18432\nv_smfmac_f32_16x16x64_bf16 v[48:51], v[92:95], v[232:239], v4\nds_read_b128 v[16:19], v7 offset:256\nv_smfmac_f32_16x16x64_bf16 v[52:55], v[92:95], v[240:247], v4\nv_smfmac_f32_16x16x64_bf16 v[56:59], v[92:95], v[248:255], v4\ns_waitcnt lgkmcnt(5)\nds_read_b128 v[20:23], v7 offset:2304\n" \
"v_smfmac_f32_16x16x64_bf16 v[60:63], v[92:95], v[72:79], v4\nds_read_b128 v[92:95], v0 offset:19456\nds_read_b128 v[72:75], v1 offset:2048\nds_read_b128 v[76:79], v1 offset:3072\nv_smfmac_f32_16x16x64_bf16 v[48:51], v[80:83], a[0:7], v4\nds_read_b128 v[24:27], v7 offset:4352\nv_smfmac_f32_16x16x64_bf16 v[52:55], v[80:83], a[8:15], v4\nv_smfmac_f32_16x16x64_bf16 v[56:59], v[80:83], a[16:23], v4\n" \
"ds_read_b128 v[28:31], v7 offset:6400\nv_smfmac_f32_16x16x64_bf16 v[60:63], v[80:83], a[24:31], v4\ns_waitcnt lgkmcnt(10)\nds_read_b128 v[80:83], v0 offset:20480\nv_smfmac_f32_16x16x64_bf16 v[48:51], v[84:87], a[32:39], v4\ns_waitcnt lgkmcnt(9)\nds_read_b128 v[32:35], v7 offset:8192\nv_smfmac_f32_16x16x64_bf16 v[52:55], v[84:87], v[64:71], v4\n" \
"ds_read_b128 v[64:67], v1 offset:4096\nds_read_b128 v[68:71], v1 offset:5120\nv_smfmac_f32_16x16x64_bf16 v[56:59], v[84:87], a[40:47], v4\nds_read_b128 v[36:39], v7 offset:10240\nv_smfmac_f32_16x16x64_bf16 v[60:63], v[84:87], a[48:55], v4\ns_waitcnt lgkmcnt(12)\nds_read_b128 v[84:87], v0 offset:21504\nv_smfmac_f32_16x16x64_bf16 v[48:51], v[88:91], a[56:63], v4\n" \
"ds_read_b128 v[40:43], v7 offset:12288\nv_smfmac_f32_16x16x64_bf16 v[52:55], v[88:91], a[64:71], v4\ns_waitcnt lgkmcnt(9)\nv_smfmac_f32_16x16x64_bf16 v[56:59], v[88:91], v[72:79], v4\nds_read_b128 v[72:75], v1 offset:6144\nds_read_b128 v[76:79], v1 offset:7168\nv_smfmac_f32_16x16x64_bf16 v[60:63], v[88:91], a[72:79], v4\nds_read_b128 v[88:91], v0 offset:22528\n" \
"v_smfmac_f32_16x16x64_bf16 v[48:51], v[92:95], a[80:87], v4\nds_read_b128 v[44:47], v7 offset:14336\nv_smfmac_f32_16x16x64_bf16 v[52:55], v[92:95], a[88:95], v4\nv_smfmac_f32_16x16x64_bf16 v[56:59], v[92:95], a[96:103], v4\ns_waitcnt lgkmcnt(7)\nv_smfmac_f32_16x16x64_bf16 v[60:63], v[92:95], v[64:71], v4\nds_read_b128 v[92:95], v0 offset:23552\nds_read_b128 v[64:67], v1 offset:8192\n" \
"ds_read_b128 v[68:71], v1 offset:9216\nv_smfmac_f32_16x16x64_bf16 v[48:51], v[80:83], a[104:111], v4\nv_smfmac_f32_16x16x64_bf16 v[52:55], v[80:83], a[112:119], v4\nv_smfmac_f32_16x16x64_bf16 v[56:59], v[80:83], a[120:127], v4\nv_smfmac_f32_16x16x64_bf16 v[60:63], v[80:83], a[128:135], v4\ns_waitcnt lgkmcnt(8)\nv_smfmac_f32_16x16x64_bf16 v[48:51], v[84:87], a[136:143], v4\ns_waitcnt lgkmcnt(5)\n" \
"ds_read_b128 v[80:83], v7 offset:8448\nv_smfmac_f32_16x16x64_bf16 v[52:55], v[84:87], v[72:79], v4\nds_read_b128 v[72:75], v1 offset:10240\nds_read_b128 v[76:79], v1 offset:11264\nv_smfmac_f32_16x16x64_bf16 v[56:59], v[84:87], a[144:151], v4\nv_smfmac_f32_16x16x64_bf16 v[60:63], v[84:87], a[152:159], v4\ns_waitcnt lgkmcnt(7)\nv_smfmac_f32_16x16x64_bf16 v[48:51], v[88:91], a[160:167], v4\n" \
"ds_read_b128 v[84:87], v7 offset:10496\nv_smfmac_f32_16x16x64_bf16 v[52:55], v[88:91], a[168:175], v4\ns_waitcnt lgkmcnt(6)\nv_smfmac_f32_16x16x64_bf16 v[48:51], v[92:95], a[184:191], v4\nv_smfmac_f32_16x16x64_bf16 v[52:55], v[92:95], a[192:199], v4\ns_waitcnt lgkmcnt(4)\nv_smfmac_f32_16x16x64_bf16 v[56:59], v[88:91], v[64:71], v4\nds_read_b128 v[64:67], v7 offset:12544\n" \
"v_add_f32 v48, v48, v50\nv_smfmac_f32_16x16x64_bf16 v[60:63], v[88:91], a[176:183], v4\nds_read_b128 v[68:71], v7 offset:14592\nv_add_f32 v52, v52, v54\nv_add_f32 v49, v49, v51\nv_smfmac_f32_16x16x64_bf16 v[56:59], v[92:95], a[200:207], v4\ns_waitcnt lgkmcnt(3)\nv_add_f32 v53, v53, v55\n" \
"v_cvt_pk_bf16_f32 v50, v48, v52\nv_smfmac_f32_16x16x64_bf16 v[60:63], v[92:95], v[72:79], v4\nv_cvt_pk_bf16_f32 v51, v49, v53\nds_write2_b32 v6, v50, v51 offset0:0 offset1:4\ns_nop 1\nv_add_f32 v56, v56, v58\nv_add_f32 v57, v57, v59\ns_nop 1\n" \
"v_add_f32 v60, v60, v62\nv_add_f32 v61, v61, v63\nv_cvt_pk_bf16_f32 v62, v56, v60\nv_cvt_pk_bf16_f32 v63, v57, v61\nds_write2_b32 v6, v62, v63 offset0:36 offset1:40\nv_mov_b32 v61, s54\nv_mfma_f32_16x16x32_bf16 v[52:55], v[32:35], v[16:19], 0\nv_mfma_f32_16x16x32_bf16 v[52:55], v[36:39], v[20:23], v[52:55]\n" \
"v_mfma_f32_16x16x32_bf16 v[52:55], v[40:43], v[24:27], v[52:55]\nv_mfma_f32_16x16x32_bf16 v[52:55], v[44:47], v[28:31], v[52:55]\ns_waitcnt lgkmcnt(0)\ns_barrier\nds_read_b128 v[32:35], v9 offset:0\nds_read_b128 v[36:39], v9 offset:576\nds_read_b128 v[40:43], v9 offset:1152\nds_read_b128 v[44:47], v9 offset:1728\n" \
"ds_read_b32 v60, v0 offset:53760\nv_mfma_f32_16x16x32_bf16 v[56:59], v[80:83], v[16:19], v[12:15]\nv_mfma_f32_16x16x32_bf16 v[56:59], v[84:87], v[20:23], v[56:59]\nv_mfma_f32_16x16x32_bf16 v[56:59], v[64:67], v[24:27], v[56:59]\nv_mfma_f32_16x16x32_bf16 v[56:59], v[68:71], v[28:31], v[56:59]\ns_waitcnt lgkmcnt(4)\nds_read_b128 v[80:83], v2 offset:0\nds_read_b128 v[84:87], v2 offset:256\n" \
"ds_read_b128 v[88:91], v2 offset:2048\nv_mfma_f32_16x16x32_bf16 v[48:51], v[32:35], v[16:19], 0\ns_waitcnt lgkmcnt(6)\nv_mfma_f32_16x16x32_bf16 v[48:51], v[36:39], v[20:23], v[48:51]\ns_waitcnt lgkmcnt(5)\nv_mfma_f32_16x16x32_bf16 v[48:51], v[40:43], v[24:27], v[48:51]\ns_waitcnt lgkmcnt(4)\nv_mfma_f32_16x16x32_bf16 v[48:51], v[44:47], v[28:31], v[48:51]\n" \
"s_waitcnt lgkmcnt(2)\nv_mfma_f32_16x16x32_bf16 v[16:19], a[0:3], v[80:83], 0\nds_read_b128 v[92:95], v2 offset:2304\nv_max3_f32 v62, v52, v53, v54\nv_max3_f32 v62, v62, v56, v57\nv_mfma_f32_16x16x32_bf16 v[20:23], a[8:11], v[80:83], 0\nds_read_b128 v[72:75], v1 offset:0\nv_max3_f32 v62, v62, v58, v59\n" \
"v_max_f32 v62, v62, v55\nv_mfma_f32_16x16x32_bf16 v[24:27], a[16:19], v[80:83], 0\nv_max3_f32 v63, v62, v48, v49\nv_max3_f32 v62, v62, v48, v49\nv_mfma_f32_16x16x32_bf16 v[28:31], a[24:27], v[80:83], 0\ns_waitcnt lgkmcnt(3)\nv_permlane16_swap_b32 v62, v63\nv_max_f32 v50, v62, v63\n" \
"v_mfma_f32_16x16x32_bf16 v[32:35], a[0:3], v[84:87], 0\nds_read_b128 v[80:83], v2 offset:4096\nv_max_f32 v62, v62, v63\nv_mfma_f32_16x16x32_bf16 v[36:39], a[8:11], v[84:87], 0\ns_nop 0\nv_permlane32_swap_b32 v62, v50\nv_max_f32 v62, v62, v50\nv_mfma_f32_16x16x32_bf16 v[40:43], a[16:19], v[84:87], 0\n" \
"v_mul_f32 v62, s59, v62\nv_fma_f32 v52, v52, s58, v62\nv_fma_f32 v53, v53, s58, v62\nv_mfma_f32_16x16x32_bf16 v[44:47], a[24:27], v[84:87], 0\ns_waitcnt lgkmcnt(3)\nv_exp_f32 v52, v52\nv_fma_f32 v54, v54, s58, v62\nv_mfma_f32_16x16x32_bf16 v[16:19], a[4:7], v[88:91], v[16:19]\n" \
"ds_read_b128 v[84:87], v2 offset:4352\nv_exp_f32 v53, v53\nv_mfma_f32_16x16x32_bf16 v[20:23], a[12:15], v[88:91], v[20:23]\nds_read_b128 v[76:79], v1 offset:1024\nv_fma_f32 v55, v55, s58, v62\nv_add_f32 v63, v52, v53\nv_mfma_f32_16x16x32_bf16 v[24:27], a[20:23], v[88:91], v[24:27]\nv_exp_f32 v54, v54\n" \
"v_fma_f32 v56, v56, s58, v62\nv_mfma_f32_16x16x32_bf16 v[28:31], a[28:31], v[88:91], v[28:31]\ns_waitcnt lgkmcnt(4)\nv_exp_f32 v55, v55\nv_add_f32 v63, v63, v54\nv_mfma_f32_16x16x32_bf16 v[32:35], a[4:7], v[92:95], v[32:35]\nds_read_b128 v[88:91], v2 offset:6144\nv_fma_f32 v57, v57, s58, v62\n" \
"v_add_f32 v63, v63, v55\nv_mfma_f32_16x16x32_bf16 v[36:39], a[12:15], v[92:95], v[36:39]\nv_exp_f32 v56, v56\nv_fma_f32 v58, v58, s58, v62\nv_mfma_f32_16x16x32_bf16 v[40:43], a[20:23], v[92:95], v[40:43]\nv_exp_f32 v57, v57\nv_add_f32 v63, v63, v56\nv_mfma_f32_16x16x32_bf16 v[44:47], a[28:31], v[92:95], v[44:47]\n" \
"s_waitcnt lgkmcnt(3)\nv_fma_f32 v59, v59, s58, v62\nv_exp_f32 v58, v58\nv_mfma_f32_16x16x32_bf16 v[16:19], a[32:35], v[80:83], v[16:19]\nds_read_b128 v[92:95], v2 offset:6400\nv_add_f32 v63, v63, v57\nv_fma_f32 v48, v48, s58, v62\nv_mfma_f32_16x16x32_bf16 v[20:23], v[72:75], v[80:83], v[20:23]\n" \
"ds_read_b128 v[64:67], v1 offset:2048\nv_exp_f32 v59, v59\nv_mfma_f32_16x16x32_bf16 v[24:27], a[40:43], v[80:83], v[24:27]\nv_add_f32 v63, v63, v58\nv_fma_f32 v49, v49, s58, v62\nv_add_f32 v63, v63, v59\nv_mfma_f32_16x16x32_bf16 v[28:31], a[48:51], v[80:83], v[28:31]\ns_waitcnt lgkmcnt(4)\n" \
"v_exp_f32 v48, v48\nv_mfma_f32_16x16x32_bf16 v[32:35], a[32:35], v[84:87], v[32:35]\nds_read_b128 v[80:83], v2 offset:8192\nv_exp_f32 v49, v49\nv_mfma_f32_16x16x32_bf16 v[36:39], v[72:75], v[84:87], v[36:39]\nv_add_f32 v63, v63, v48\nv_add_f32 v62, v63, v49\nv_add_f32 v50, v63, v49\n" \
"v_mfma_f32_16x16x32_bf16 v[40:43], a[40:43], v[84:87], v[40:43]\ns_nop 0\nv_permlane16_swap_b32 v62, v50\nv_add_f32 v63, v62, v50\nv_mfma_f32_16x16x32_bf16 v[44:47], a[48:51], v[84:87], v[44:47]\ns_waitcnt lgkmcnt(3)\nv_add_f32 v50, v62, v50\nv_mfma_f32_16x16x32_bf16 v[16:19], a[36:39], v[88:91], v[16:19]\n" \
"ds_read_b128 v[84:87], v2 offset:8448\nv_permlane32_swap_b32 v63, v50\nv_mfma_f32_16x16x32_bf16 v[20:23], v[76:79], v[88:91], v[20:23]\nds_read_b128 v[68:71], v1 offset:3072\nv_add_f32 v63, v63, v50\nv_mfma_f32_16x16x32_bf16 v[24:27], a[44:47], v[88:91], v[24:27]\nv_rcp_f32 v63, v63\nv_mfma_f32_16x16x32_bf16 v[28:31], a[52:55], v[88:91], v[28:31]\n" \
"s_waitcnt lgkmcnt(4)\nv_mul_f32 v52, v52, v63\nv_mul_f32 v53, v53, v63\nv_mul_f32 v54, v54, v63\nv_mfma_f32_16x16x32_bf16 v[32:35], a[36:39], v[92:95], v[32:35]\nds_read_b128 v[88:91], v2 offset:10240\nv_mul_f32 v55, v55, v63\nv_mul_f32 v56, v56, v63\n" \
"v_mfma_f32_16x16x32_bf16 v[36:39], v[76:79], v[92:95], v[36:39]\nv_mul_f32 v57, v57, v63\nv_mul_f32 v58, v58, v63\nv_mul_f32 v59, v59, v63\nv_mfma_f32_16x16x32_bf16 v[40:43], a[44:47], v[92:95], v[40:43]\nv_mul_f32 v48, v48, v63\nv_mul_f32 v49, v49, v63\nv_cvt_pk_bf16_f32 v52, v52, v53\n" \
"v_mfma_f32_16x16x32_bf16 v[44:47], a[52:55], v[92:95], v[44:47]\ns_waitcnt lgkmcnt(3)\nv_cvt_pk_bf16_f32 v53, v54, v55\nv_cvt_pk_bf16_f32 v48, v48, v49\nv_cvt_pk_bf16_f32 v54, v56, v57\nv_mfma_f32_16x16x32_bf16 v[16:19], a[56:59], v[80:83], v[16:19]\nds_read_b128 v[92:95], v2 offset:10496\nv_cvt_pk_bf16_f32 v55, v58, v59\n" \
"v_bfi_b32 v52, s61, v52, v61\nv_mfma_f32_16x16x32_bf16 v[20:23], a[64:67], v[80:83], v[20:23]\nds_read_b128 v[72:75], v1 offset:4096\nv_bfi_b32 v53, s61, v53, v61\nv_bfi_b32 v54, s61, v54, v61\nv_mfma_f32_16x16x32_bf16 v[24:27], v[64:67], v[80:83], v[24:27]\nv_bfi_b32 v55, s61, v55, v61\nv_bfi_b32 v48, s61, v48, v61\n" \
"v_mfma_f32_16x16x32_bf16 v[28:31], a[72:75], v[80:83], v[28:31]\ns_waitcnt lgkmcnt(4)\nbuffer_store_dwordx4 v[52:55], v10, s[36:39], s55 offen sc1\nv_mov_b32 v49, v48\nv_mfma_f32_16x16x32_bf16 v[32:35], a[56:59], v[84:87], v[32:35]\nds_read_b128 v[80:83], v2 offset:12288\nv_permlane16_swap_b32 v48, v49\nv_mfma_f32_16x16x32_bf16 v[36:39], a[64:67], v[84:87], v[36:39]\n" \
"v_mov_b32 v50, v48\nv_mov_b32 v51, v49\nv_mfma_f32_16x16x32_bf16 v[40:43], v[64:67], v[84:87], v[40:43]\nv_permlane32_swap_b32 v48, v50\nv_mfma_f32_16x16x32_bf16 v[44:47], a[72:75], v[84:87], v[44:47]\ns_waitcnt lgkmcnt(3)\nv_permlane32_swap_b32 v49, v51\nv_mfma_f32_16x16x32_bf16 v[16:19], a[60:63], v[88:91], v[16:19]\n" \
"ds_read_b128 v[84:87], v2 offset:12544\nv_mfma_f32_16x16x32_bf16 v[20:23], a[68:71], v[88:91], v[20:23]\nds_read_b128 v[76:79], v1 offset:5120\nv_mfma_f32_16x16x32_bf16 v[24:27], v[68:71], v[88:91], v[24:27]\nbuffer_store_dwordx4 v[48:51], v11, s[36:39], s55 offen sc1\nv_cmp_ne_u32 vcc, 0, v60\nv_mfma_f32_16x16x32_bf16 v[28:31], a[76:79], v[88:91], v[28:31]\ns_waitcnt lgkmcnt(4)\n" \
"v_mfma_f32_16x16x32_bf16 v[32:35], a[60:63], v[92:95], v[32:35]\nds_read_b128 v[88:91], v2 offset:14336\nv_mfma_f32_16x16x32_bf16 v[36:39], a[68:71], v[92:95], v[36:39]\nv_mfma_f32_16x16x32_bf16 v[40:43], v[68:71], v[92:95], v[40:43]\nv_mfma_f32_16x16x32_bf16 v[44:47], a[76:79], v[92:95], v[44:47]\ns_cbranch_vccnz .Lqk_pj%=\ns_cmp_lt_i32 s40, 63\ns_waitcnt lgkmcnt(0)\n" \
"s_cbranch_scc1 .Lqk_pwait%=\ns_branch .Lqk_end%=\n.Lqk_re2%=:\nv_max_u32 v17, v80, v82\nv_cmp_eq_u32 vcc, -1, v17\ns_cbranch_vccz .Lqk_sk2417%=\ns_mov_b64 exec, vcc\nbuffer_load_dwordx4 v[80:83], v3, s[36:39], s42 offen offset:0 sc1\n" \
"s_mov_b64 exec, -1\n.Lqk_sk2417%=:\nv_max_u32 v17, v84, v86\nv_cmp_eq_u32 vcc, -1, v17\ns_cbranch_vccz .Lqk_sk2424%=\ns_mov_b64 exec, vcc\nbuffer_load_dwordx4 v[84:87], v3, s[36:39], s42 offen offset:1024 sc1\ns_mov_b64 exec, -1\n" \
".Lqk_sk2424%=:\nv_max_u32 v17, v88, v90\nv_cmp_eq_u32 vcc, -1, v17\ns_cbranch_vccz .Lqk_sk2431%=\ns_mov_b64 exec, vcc\nbuffer_load_dwordx4 v[88:91], v3, s[36:39], s42 offen offset:2048 sc1\ns_mov_b64 exec, -1\n.Lqk_sk2431%=:\n" \
"v_max_u32 v17, v92, v94\nv_cmp_eq_u32 vcc, -1, v17\ns_cbranch_vccz .Lqk_sk2438%=\ns_mov_b64 exec, vcc\nbuffer_load_dwordx4 v[92:95], v3, s[36:39], s42 offen offset:3072 sc1\ns_mov_b64 exec, -1\n.Lqk_sk2438%=:\nv_mov_b64 v[48:49], 0\n" \
"v_mov_b64 v[50:51], 0\nv_mov_b64 v[52:53], 0\nv_mov_b64 v[54:55], 0\nv_mov_b64 v[56:57], 0\nv_mov_b64 v[58:59], 0\nv_mov_b64 v[60:61], 0\nv_mov_b64 v[62:63], 0\ns_add_u32 s43, s43, 1\n" \
"s_cmp_lt_u32 s43, 262144\ns_cbranch_scc1 .Lqk_poll2%=\ns_branch .Lqk_abort%=\n.Lqk_c3%=:\ns_waitcnt lgkmcnt(3)\nv_mfma_f32_16x16x32_bf16 v[32:35], v[232:235], v[56:59], v[32:35]\ns_add_u32 s44, s40, 1\nv_mov_b64 v[52:53], 0\n" \
"v_mov_b64 v[54:55], 0\nv_mfma_f32_16x16x32_bf16 v[36:39], v[240:243], v[56:59], v[36:39]\ns_lshl_b32 s44, s44, 17\ns_waitcnt lgkmcnt(2)\nds_read_b128 v[64:67], v1 offset:18432\nv_mfma_f32_16x16x32_bf16 v[32:35], v[236:239], v[60:63], v[32:35]\ns_add_u32 s44, s44, %[xn]\ns_lshr_b32 s45, s44, 8\n" \
"ds_read_b128 v[68:71], v1 offset:19456\nv_mfma_f32_16x16x32_bf16 v[36:39], v[244:247], v[60:63], v[36:39]\ns_and_b32 s45, s45, 0x2000\ns_xor_b32 s41, s44, s45\nv_mfma_f32_16x16x32_bf16 v[40:43], v[248:251], v[56:59], v[40:43]\nbuffer_load_dword v16, v12, s[36:39], s56 offen sc1\nv_mfma_f32_16x16x32_bf16 v[44:47], v[72:75], v[56:59], v[44:47]\nv_cvt_pk_bf16_f32 v36, v32, v36\n" \
"v_cvt_pk_bf16_f32 v37, v33, v37\nv_mfma_f32_16x16x32_bf16 v[40:43], v[252:255], v[60:63], v[40:43]\nv_cvt_pk_bf16_f32 v38, v34, v38\nv_cvt_pk_bf16_f32 v39, v35, v39\nv_mfma_f32_16x16x32_bf16 v[44:47], v[76:79], v[60:63], v[44:47]\nds_read_b128 v[72:75], v1 offset:20480\nds_read_b128 v[76:79], v1 offset:21504\nv_mov_b64 v[56:57], 0\n" \
"v_mov_b64 v[58:59], 0\nv_mov_b64 v[60:61], 0\nv_mov_b64 v[62:63], 0\n.Lqk_poll3%=:\ns_waitcnt vmcnt(1)\nv_smfmac_f32_16x16x64_bf16 v[48:51], v[80:83], v[152:159], v4\nds_write_b128 v0, v[80:83] offset:28672\nv_max3_u32 v17, v80, v82, v84\n" \
"v_smfmac_f32_16x16x64_bf16 v[52:55], v[80:83], v[160:167], v4\nv_max3_u32 v17, v17, v86, v88\nv_smfmac_f32_16x16x64_bf16 v[56:59], v[80:83], v[168:175], v4\nds_write_b128 v0, v[84:87] offset:29696\nv_max3_u32 v17, v17, v90, v92\nv_max_u32 v17, v17, v94\nv_smfmac_f32_16x16x64_bf16 v[60:63], v[80:83], v[176:183], v4\nv_cmp_eq_u32 vcc, -1, v17\n" \
"v_smfmac_f32_16x16x64_bf16 v[48:51], v[84:87], v[184:191], v4\nds_write_b128 v0, v[88:91] offset:30720\ns_cbranch_vccnz .Lqk_re3%=\ns_waitcnt lgkmcnt(5)\nv_smfmac_f32_16x16x64_bf16 v[52:55], v[84:87], v[64:71], v4\nv_smfmac_f32_16x16x64_bf16 v[56:59], v[84:87], v[192:199], v4\nds_write_b128 v0, v[92:95] offset:31744\nv_smfmac_f32_16x16x64_bf16 v[60:63], v[84:87], v[200:207], v4\n" \
"v_smfmac_f32_16x16x64_bf16 v[48:51], v[88:91], v[208:215], v4\nv_smfmac_f32_16x16x64_bf16 v[52:55], v[88:91], v[216:223], v4\ns_waitcnt lgkmcnt(0)\n.Lqk_jn3%=:\ns_barrier\nds_read_b128 v[80:83], v0 offset:16384\nds_read_b128 v[64:67], v1 offset:22528\nds_read_b128 v[68:71], v1 offset:23552\n" \
"v_smfmac_f32_16x16x64_bf16 v[56:59], v[88:91], v[72:79], v4\ns_waitcnt vmcnt(0)\nds_read_b128 v[72:75], v1 offset:0\nds_read_b128 v[76:79], v1 offset:1024\nv_cmp_ge_i32 vcc, s40, v16\nv_smfmac_f32_16x16x64_bf16 v[60:63], v[88:91], v[224:231], v4\ns_cmp_eq_u64 vcc, 0\ns_cselect_b32 s61, s41, -2147483648\n" \
"s_cselect_b32 s44, 1, 0\nds_read_b128 v[84:87], v0 offset:17408\nds_read_b128 v[88:91], v0 offset:18432\nv_smfmac_f32_16x16x64_bf16 v[48:51], v[92:95], v[232:239], v4\ns_add_u32 m0, %[l0], 0\ns_add_u32 s60, s61, 0\nds_write_b128 v5, v[36:39] offset:256\nbuffer_load_dwordx4 v0, s[36:39], s60 offen offset:0 sc1 lds\n" \
"v_cvt_pk_bf16_f32 v44, v40, v44\nv_cvt_pk_bf16_f32 v45, v41, v45\nv_cvt_pk_bf16_f32 v46, v42, v46\nv_cvt_pk_bf16_f32 v47, v43, v47\nv_smfmac_f32_16x16x64_bf16 v[52:55], v[92:95], v[240:247], v4\nv_mov_b32 v16, s44\nv_smfmac_f32_16x16x64_bf16 v[56:59], v[92:95], v[248:255], v4\ns_waitcnt lgkmcnt(5)\n" \
"buffer_load_dwordx4 v0, s[36:39], s60 offen offset:1024 sc1 lds\nv_smfmac_f32_16x16x64_bf16 v[60:63], v[92:95], v[64:71], v4\nds_read_b128 v[92:95], v0 offset:19456\nds_read_b128 v[64:67], v1 offset:2048\nds_read_b128 v[68:71], v1 offset:3072\nds_write_b32 v0, v16 offset:53760\nv_smfmac_f32_16x16x64_bf16 v[48:51], v[80:83], a[0:7], v4\nds_write_b128 v5, v[44:47] offset:768\n" \
"buffer_load_dwordx4 v0, s[36:39], s60 offen offset:2048 sc1 lds\nv_smfmac_f32_16x16x64_bf16 v[52:55], v[80:83], a[8:15], v4\nv_smfmac_f32_16x16x64_bf16 v[56:59], v[80:83], a[16:23], v4\nbuffer_load_dwordx4 v0, s[36:39], s60 offen offset:3072 sc1 lds\nv_smfmac_f32_16x16x64_bf16 v[60:63], v[80:83], a[24:31], v4\ns_waitcnt lgkmcnt(7)\ns_add_u32 m0, %[l0], 4096\nds_read_b128 v[80:83], v0 offset:20480\n" \
"v_smfmac_f32_16x16x64_bf16 v[48:51], v[84:87], a[32:39], v4\ns_add_u32 s60, s61, 4096\nbuffer_load_dwordx4 v0, s[36:39], s60 offen offset:0 sc1 lds\nv_smfmac_f32_16x16x64_bf16 v[52:55], v[84:87], v[72:79], v4\nds_read_b128 v[72:75], v1 offset:4096\nds_read_b128 v[76:79], v1 offset:5120\nv_smfmac_f32_16x16x64_bf16 v[56:59], v[84:87], a[40:47], v4\nbuffer_load_dwordx4 v0, s[36:39], s60 offen offset:1024 sc1 lds\n" \
"v_smfmac_f32_16x16x64_bf16 v[60:63], v[84:87], a[48:55], v4\ns_waitcnt lgkmcnt(9)\nds_read_b128 v[84:87], v0 offset:21504\nv_smfmac_f32_16x16x64_bf16 v[48:51], v[88:91], a[56:63], v4\ns_waitcnt lgkmcnt(4)\ns_barrier\nbuffer_load_dwordx4 v0, s[36:39], s60 offen offset:2048 sc1 lds\nv_smfmac_f32_16x16x64_bf16 v[52:55], v[88:91], a[64:71], v4\n" \
"v_smfmac_f32_16x16x64_bf16 v[56:59], v[88:91], v[64:71], v4\nds_read_b128 v[64:67], v1 offset:6144\nds_read_b128 v[68:71], v1 offset:7168\nbuffer_load_dwordx4 v0, s[36:39], s60 offen offset:3072 sc1 lds\nv_smfmac_f32_16x16x64_bf16 v[60:63], v[88:91], a[72:79], v4\ns_add_u32 m0, %[l0], 8192\nds_read_b128 v[88:91], v0 offset:22528\nv_smfmac_f32_16x16x64_bf16 v[48:51], v[92:95], a[80:87], v4\n" \
"s_add_u32 s60, s61, 16384\nbuffer_load_dwordx4 v0, s[36:39], s60 offen offset:0 sc1 lds\nv_smfmac_f32_16x16x64_bf16 v[52:55], v[92:95], a[88:95], v4\nv_smfmac_f32_16x16x64_bf16 v[56:59], v[92:95], a[96:103], v4\ns_waitcnt lgkmcnt(4)\nbuffer_load_dwordx4 v0, s[36:39], s60 offen offset:1024 sc1 lds\nv_smfmac_f32_16x16x64_bf16 v[60:63], v[92:95], v[72:79], v4\nds_read_b128 v[92:95], v0 offset:23552\n" \
"ds_read_b128 v[72:75], v1 offset:8192\nds_read_b128 v[76:79], v1 offset:9216\nv_smfmac_f32_16x16x64_bf16 v[48:51], v[80:83], a[104:111], v4\nbuffer_load_dwordx4 v0, s[36:39], s60 offen offset:2048 sc1 lds\nv_smfmac_f32_16x16x64_bf16 v[52:55], v[80:83], a[112:119], v4\nv_smfmac_f32_16x16x64_bf16 v[56:59], v[80:83], a[120:127], v4\nbuffer_load_dwordx4 v0, s[36:39], s60 offen offset:3072 sc1 lds\nv_smfmac_f32_16x16x64_bf16 v[60:63], v[80:83], a[128:135], v4\n" \
"s_waitcnt lgkmcnt(6)\ns_add_u32 m0, %[l0], 12288\nds_read_b128 v[80:83], v0 offset:24576\nv_smfmac_f32_16x16x64_bf16 v[48:51], v[84:87], a[136:143], v4\ns_add_u32 s60, s61, 20480\ns_waitcnt lgkmcnt(5)\nbuffer_load_dwordx4 v0, s[36:39], s60 offen offset:0 sc1 lds\nv_smfmac_f32_16x16x64_bf16 v[52:55], v[84:87], v[64:71], v4\n" \
"ds_read_b128 v[64:67], v1 offset:10240\nds_read_b128 v[68:71], v1 offset:11264\nv_smfmac_f32_16x16x64_bf16 v[56:59], v[84:87], a[144:151], v4\nbuffer_load_dwordx4 v0, s[36:39], s60 offen offset:1024 sc1 lds\nv_smfmac_f32_16x16x64_bf16 v[60:63], v[84:87], a[152:159], v4\ns_waitcnt lgkmcnt(6)\nds_read_b128 v[84:87], v0 offset:25600\nv_smfmac_f32_16x16x64_bf16 v[48:51], v[88:91], a[160:167], v4\n" \
"buffer_load_dwordx4 v0, s[36:39], s60 offen offset:2048 sc1 lds\nv_smfmac_f32_16x16x64_bf16 v[52:55], v[88:91], a[168:175], v4\ns_waitcnt lgkmcnt(4)\nv_smfmac_f32_16x16x64_bf16 v[56:59], v[88:91], v[72:79], v4\nds_read_b128 v[72:75], v1 offset:12288\nds_read_b128 v[76:79], v1 offset:13312\nbuffer_load_dwordx4 v0, s[36:39], s60 offen offset:3072 sc1 lds\nv_smfmac_f32_16x16x64_bf16 v[60:63], v[88:91], a[176:183], v4\n" \
"ds_read_b128 v[88:91], v0 offset:26624\nv_smfmac_f32_16x16x64_bf16 v[48:51], v[92:95], a[184:191], v4\nv_smfmac_f32_16x16x64_bf16 v[52:55], v[92:95], a[192:199], v4\nv_smfmac_f32_16x16x64_bf16 v[56:59], v[92:95], a[200:207], v4\ns_waitcnt lgkmcnt(4)\nv_smfmac_f32_16x16x64_bf16 v[60:63], v[92:95], v[64:71], v4\nds_read_b128 v[92:95], v0 offset:27648\nds_read_b128 v[64:67], v1 offset:14336\n" \
"ds_read_b128 v[68:71], v1 offset:15360\nv_smfmac_f32_16x16x64_bf16 v[48:51], v[80:83], a[208:215], v4\nv_smfmac_f32_16x16x64_bf16 v[52:55], v[80:83], a[216:223], v4\nv_smfmac_f32_16x16x64_bf16 v[56:59], v[80:83], a[224:231], v4\nv_smfmac_f32_16x16x64_bf16 v[60:63], v[80:83], a[232:239], v4\ns_waitcnt lgkmcnt(6)\nv_smfmac_f32_16x16x64_bf16 v[48:51], v[84:87], a[240:247], v4\ns_waitcnt lgkmcnt(4)\n" \
"v_smfmac_f32_16x16x64_bf16 v[52:55], v[84:87], v[72:79], v4\nds_read_b128 v[72:75], v1 offset:16384\nds_read_b128 v[76:79], v1 offset:17408\nv_smfmac_f32_16x16x64_bf16 v[56:59], v[84:87], a[248:255], v4\nv_smfmac_f32_16x16x64_bf16 v[60:63], v[84:87], v[96:103], v4\ns_waitcnt lgkmcnt(5)\nv_smfmac_f32_16x16x64_bf16 v[48:51], v[88:91], v[104:111], v4\nv_smfmac_f32_16x16x64_bf16 v[52:55], v[88:91], v[112:119], v4\n" \
"s_waitcnt lgkmcnt(4)\nv_smfmac_f32_16x16x64_bf16 v[48:51], v[92:95], v[128:135], v4\nv_smfmac_f32_16x16x64_bf16 v[52:55], v[92:95], v[136:143], v4\ns_waitcnt lgkmcnt(2)\nv_smfmac_f32_16x16x64_bf16 v[56:59], v[88:91], v[64:71], v4\nv_add_f32 v48, v48, v50\nv_smfmac_f32_16x16x64_bf16 v[60:63], v[88:91], v[120:127], v4\nv_add_f32 v52, v52, v54\n" \
"v_add_f32 v49, v49, v51\nv_smfmac_f32_16x16x64_bf16 v[56:59], v[92:95], v[144:151], v4\ns_waitcnt lgkmcnt(0)\nv_add_f32 v53, v53, v55\nv_cvt_pk_bf16_f32 v50, v48, v52\nv_smfmac_f32_16x16x64_bf16 v[60:63], v[92:95], v[72:79], v4\nv_cvt_pk_bf16_f32 v51, v49, v53\nds_write2_b32 v6, v50, v51 offset0:0 offset1:4\n" \
"s_nop 1\nv_add_f32 v56, v56, v58\nv_add_f32 v57, v57, v59\ns_nop 1\nv_add_f32 v60, v60, v62\nv_add_f32 v61, v61, v63\nv_cvt_pk_bf16_f32 v62, v56, v60\nv_cvt_pk_bf16_f32 v63, v57, v61\n" \
"ds_write2_b32 v6, v62, v63 offset0:36 offset1:40\ns_waitcnt vmcnt(0)\ns_waitcnt lgkmcnt(0)\ns_barrier\ns_cbranch_vccnz .Lqk_slow%=\nds_read_b128 v[80:83], v2 offset:0\nds_read_b128 v[84:87], v2 offset:256\nds_read_b128 v[88:91], v2 offset:2048\n" \
"s_add_u32 m0, %[l0], 16384\ns_add_u32 s60, s41, 32768\nbuffer_load_dwordx4 v0, s[36:39], s60 offen offset:0 sc1 lds\nbuffer_load_dwordx4 v0, s[36:39], s60 offen offset:1024 sc1 lds\nbuffer_load_dwordx4 v0, s[36:39], s60 offen offset:2048 sc1 lds\nbuffer_load_dwordx4 v0, s[36:39], s60 offen offset:3072 sc1 lds\ns_add_u32 m0, %[l0], 20480\ns_add_u32 s60, s41, 36864\n" \
"buffer_load_dwordx4 v0, s[36:39], s60 offen offset:0 sc1 lds\nbuffer_load_dwordx4 v0, s[36:39], s60 offen offset:1024 sc1 lds\nbuffer_load_dwordx4 v0, s[36:39], s60 offen offset:2048 sc1 lds\nbuffer_load_dwordx4 v0, s[36:39], s60 offen offset:3072 sc1 lds\ns_add_u32 m0, %[l0], 24576\ns_add_u32 s60, s41, 49152\nbuffer_load_dwordx4 v0, s[36:39], s60 offen offset:0 sc1 lds\nbuffer_load_dwordx4 v0, s[36:39], s60 offen offset:1024 sc1 lds\n" \
"buffer_load_dwordx4 v0, s[36:39], s60 offen offset:2048 sc1 lds\nbuffer_load_dwordx4 v0, s[36:39], s60 offen offset:3072 sc1 lds\ns_add_u32 m0, %[l0], 28672\ns_add_u32 s60, s41, 53248\nbuffer_load_dwordx4 v0, s[36:39], s60 offen offset:0 sc1 lds\nbuffer_load_dwordx4 v0, s[36:39], s60 offen offset:1024 sc1 lds\nbuffer_load_dwordx4 v0, s[36:39], s60 offen offset:2048 sc1 lds\nbuffer_load_dwordx4 v0, s[36:39], s60 offen offset:3072 sc1 lds\n" \
"s_waitcnt lgkmcnt(2)\nv_mfma_f32_16x16x32_bf16 v[16:19], a[0:3], v[80:83], 0\nds_read_b128 v[92:95], v2 offset:2304\nv_mfma_f32_16x16x32_bf16 v[20:23], a[8:11], v[80:83], 0\nds_read_b128 v[72:75], v1 offset:0\nv_mfma_f32_16x16x32_bf16 v[24:27], a[16:19], v[80:83], 0\nv_mfma_f32_16x16x32_bf16 v[28:31], a[24:27], v[80:83], 0\ns_waitcnt lgkmcnt(3)\n" \
"v_mfma_f32_16x16x32_bf16 v[32:35], a[0:3], v[84:87], 0\nds_read_b128 v[80:83], v2 offset:4096\nv_mfma_f32_16x16x32_bf16 v[36:39], a[8:11], v[84:87], 0\nv_mfma_f32_16x16x32_bf16 v[40:43], a[16:19], v[84:87], 0\nv_mfma_f32_16x16x32_bf16 v[44:47], a[24:27], v[84:87], 0\ns_waitcnt lgkmcnt(3)\nv_mfma_f32_16x16x32_bf16 v[16:19], a[4:7], v[88:91], v[16:19]\nds_read_b128 v[84:87], v2 offset:4352\n" \
"v_mfma_f32_16x16x32_bf16 v[20:23], a[12:15], v[88:91], v[20:23]\nds_read_b128 v[76:79], v1 offset:1024\nv_mfma_f32_16x16x32_bf16 v[24:27], a[20:23], v[88:91], v[24:27]\nv_mfma_f32_16x16x32_bf16 v[28:31], a[28:31], v[88:91], v[28:31]\ns_waitcnt lgkmcnt(4)\nv_mfma_f32_16x16x32_bf16 v[32:35], a[4:7], v[92:95], v[32:35]\nds_read_b128 v[88:91], v2 offset:6144\nv_mfma_f32_16x16x32_bf16 v[36:39], a[12:15], v[92:95], v[36:39]\n" \
"v_mfma_f32_16x16x32_bf16 v[40:43], a[20:23], v[92:95], v[40:43]\nv_mfma_f32_16x16x32_bf16 v[44:47], a[28:31], v[92:95], v[44:47]\ns_waitcnt lgkmcnt(3)\nv_mfma_f32_16x16x32_bf16 v[16:19], a[32:35], v[80:83], v[16:19]\nds_read_b128 v[92:95], v2 offset:6400\nv_mfma_f32_16x16x32_bf16 v[20:23], v[72:75], v[80:83], v[20:23]\nds_read_b128 v[64:67], v1 offset:2048\nv_mfma_f32_16x16x32_bf16 v[24:27], a[40:43], v[80:83], v[24:27]\n" \
"v_mfma_f32_16x16x32_bf16 v[28:31], a[48:51], v[80:83], v[28:31]\ns_waitcnt lgkmcnt(4)\nv_mfma_f32_16x16x32_bf16 v[32:35], a[32:35], v[84:87], v[32:35]\nds_read_b128 v[80:83], v2 offset:8192\nv_mfma_f32_16x16x32_bf16 v[36:39], v[72:75], v[84:87], v[36:39]\nv_mfma_f32_16x16x32_bf16 v[40:43], a[40:43], v[84:87], v[40:43]\nv_mfma_f32_16x16x32_bf16 v[44:47], a[48:51], v[84:87], v[44:47]\ns_waitcnt lgkmcnt(3)\n" \
"v_mfma_f32_16x16x32_bf16 v[16:19], a[36:39], v[88:91], v[16:19]\nds_read_b128 v[84:87], v2 offset:8448\nv_mfma_f32_16x16x32_bf16 v[20:23], v[76:79], v[88:91], v[20:23]\nds_read_b128 v[68:71], v1 offset:3072\nv_mfma_f32_16x16x32_bf16 v[24:27], a[44:47], v[88:91], v[24:27]\nv_mfma_f32_16x16x32_bf16 v[28:31], a[52:55], v[88:91], v[28:31]\ns_waitcnt lgkmcnt(4)\nv_mfma_f32_16x16x32_bf16 v[32:35], a[36:39], v[92:95], v[32:35]\n" \
"ds_read_b128 v[88:91], v2 offset:10240\nv_mfma_f32_16x16x32_bf16 v[36:39], v[76:79], v[92:95], v[36:39]\nv_mfma_f32_16x16x32_bf16 v[40:43], a[44:47], v[92:95], v[40:43]\nv_mfma_f32_16x16x32_bf16 v[44:47], a[52:55], v[92:95], v[44:47]\ns_waitcnt lgkmcnt(3)\nv_mfma_f32_16x16x32_bf16 v[16:19], a[56:59], v[80:83], v[16:19]\nds_read_b128 v[92:95], v2 offset:10496\nv_mfma_f32_16x16x32_bf16 v[20:23], a[64:67], v[80:83], v[20:23]\n" \
"ds_read_b128 v[72:75], v1 offset:4096\nv_mfma_f32_16x16x32_bf16 v[24:27], v[64:67], v[80:83], v[24:27]\nv_mfma_f32_16x16x32_bf16 v[28:31], a[72:75], v[80:83], v[28:31]\ns_waitcnt lgkmcnt(4)\nv_mfma_f32_16x16x32_bf16 v[32:35], a[56:59], v[84:87], v[32:35]\nds_read_b128 v[80:83], v2 offset:12288\nv_mfma_f32_16x16x32_bf16 v[36:39], a[64:67], v[84:87], v[36:39]\nv_mfma_f32_16x16x32_bf16 v[40:43], v[64:67], v[84:87], v[40:43]\n" \
"v_mfma_f32_16x16x32_bf16 v[44:47], a[72:75], v[84:87], v[44:47]\ns_waitcnt lgkmcnt(3)\nv_mfma_f32_16x16x32_bf16 v[16:19], a[60:63], v[88:91], v[16:19]\nds_read_b128 v[84:87], v2 offset:12544\nv_mfma_f32_16x16x32_bf16 v[20:23], a[68:71], v[88:91], v[20:23]\nds_read_b128 v[76:79], v1 offset:5120\nv_mfma_f32_16x16x32_bf16 v[24:27], v[68:71], v[88:91], v[24:27]\nv_mfma_f32_16x16x32_bf16 v[28:31], a[76:79], v[88:91], v[28:31]\n" \
"s_waitcnt lgkmcnt(4)\nv_mfma_f32_16x16x32_bf16 v[32:35], a[60:63], v[92:95], v[32:35]\nds_read_b128 v[88:91], v2 offset:14336\nv_mfma_f32_16x16x32_bf16 v[36:39], a[68:71], v[92:95], v[36:39]\nv_mfma_f32_16x16x32_bf16 v[40:43], v[68:71], v[92:95], v[40:43]\nv_mfma_f32_16x16x32_bf16 v[44:47], a[76:79], v[92:95], v[44:47]\ns_waitcnt vmcnt(0)\ns_branch .Lqk_pj%=\n" \
".Lqk_slow%=:\ns_cmp_ge_i32 s40, 63\ns_cbranch_scc1 .Lqk_end%=\n.Lqk_fetch0%=:\nbuffer_load_dword v16, v12, s[36:39], s56 offen sc1\ns_waitcnt vmcnt(0)\nv_cmp_ge_i32 vcc, s40, v16\ns_cbranch_vccz .Lqk_fok%=\n" \
"s_add_u32 s43, s43, 1\ns_cmp_lt_u32 s43, 262144\ns_cbranch_scc1 .Lqk_fetch0%=\ns_branch .Lqk_abort%=\n.Lqk_fok%=:\ns_add_u32 s44, s40, 1\ns_lshl_b32 s44, s44, 17\ns_add_u32 s44, s44, %[xn]\n" \
"s_lshr_b32 s45, s44, 8\ns_and_b32 s45, s45, 0x2000\ns_xor_b32 s41, s44, s45\ns_add_u32 m0, %[l0], 0\ns_add_u32 s60, s41, 0\nbuffer_load_dwordx4 v0, s[36:39], s60 offen offset:0 sc1 lds\nbuffer_load_dwordx4 v0, s[36:39], s60 offen offset:1024 sc1 lds\nbuffer_load_dwordx4 v0, s[36:39], s60 offen offset:2048 sc1 lds\n" \
"buffer_load_dwordx4 v0, s[36:39], s60 offen offset:3072 sc1 lds\ns_add_u32 m0, %[l0], 4096\ns_add_u32 s60, s41, 4096\nbuffer_load_dwordx4 v0, s[36:39], s60 offen offset:0 sc1 lds\nbuffer_load_dwordx4 v0, s[36:39], s60 offen offset:1024 sc1 lds\nbuffer_load_dwordx4 v0, s[36:39], s60 offen offset:2048 sc1 lds\nbuffer_load_dwordx4 v0, s[36:39], s60 offen offset:3072 sc1 lds\ns_add_u32 m0, %[l0], 8192\n" \
"s_add_u32 s60, s41, 16384\nbuffer_load_dwordx4 v0, s[36:39], s60 offen offset:0 sc1 lds\nbuffer_load_dwordx4 v0, s[36:39], s60 offen offset:1024 sc1 lds\nbuffer_load_dwordx4 v0, s[36:39], s60 offen offset:2048 sc1 lds\nbuffer_load_dwordx4 v0, s[36:39], s60 offen offset:3072 sc1 lds\ns_add_u32 m0, %[l0], 12288\ns_add_u32 s60, s41, 20480\nbuffer_load_dwordx4 v0, s[36:39], s60 offen offset:0 sc1 lds\n" \
"buffer_load_dwordx4 v0, s[36:39], s60 offen offset:1024 sc1 lds\nbuffer_load_dwordx4 v0, s[36:39], s60 offen offset:2048 sc1 lds\nbuffer_load_dwordx4 v0, s[36:39], s60 offen offset:3072 sc1 lds\ns_add_u32 m0, %[l0], 16384\ns_add_u32 s60, s41, 32768\nbuffer_load_dwordx4 v0, s[36:39], s60 offen offset:0 sc1 lds\nbuffer_load_dwordx4 v0, s[36:39], s60 offen offset:1024 sc1 lds\nbuffer_load_dwordx4 v0, s[36:39], s60 offen offset:2048 sc1 lds\n" \
"buffer_load_dwordx4 v0, s[36:39], s60 offen offset:3072 sc1 lds\ns_add_u32 m0, %[l0], 20480\ns_add_u32 s60, s41, 36864\nbuffer_load_dwordx4 v0, s[36:39], s60 offen offset:0 sc1 lds\nbuffer_load_dwordx4 v0, s[36:39], s60 offen offset:1024 sc1 lds\nbuffer_load_dwordx4 v0, s[36:39], s60 offen offset:2048 sc1 lds\nbuffer_load_dwordx4 v0, s[36:39], s60 offen offset:3072 sc1 lds\ns_add_u32 m0, %[l0], 24576\n" \
"s_add_u32 s60, s41, 49152\nbuffer_load_dwordx4 v0, s[36:39], s60 offen offset:0 sc1 lds\nbuffer_load_dwordx4 v0, s[36:39], s60 offen offset:1024 sc1 lds\nbuffer_load_dwordx4 v0, s[36:39], s60 offen offset:2048 sc1 lds\nbuffer_load_dwordx4 v0, s[36:39], s60 offen offset:3072 sc1 lds\ns_add_u32 m0, %[l0], 28672\ns_add_u32 s60, s41, 53248\nbuffer_load_dwordx4 v0, s[36:39], s60 offen offset:0 sc1 lds\n" \
"buffer_load_dwordx4 v0, s[36:39], s60 offen offset:1024 sc1 lds\nbuffer_load_dwordx4 v0, s[36:39], s60 offen offset:2048 sc1 lds\nbuffer_load_dwordx4 v0, s[36:39], s60 offen offset:3072 sc1 lds\ns_waitcnt vmcnt(0)\ns_branch .Lqk_pwait%=\n.Lqk_re3%=:\nv_max_u32 v17, v80, v82\nv_cmp_eq_u32 vcc, -1, v17\n" \
"s_cbranch_vccz .Lqk_sk2850%=\ns_mov_b64 exec, vcc\nbuffer_load_dwordx4 v[80:83], v3, s[36:39], s42 offen offset:0 sc1\ns_mov_b64 exec, -1\n.Lqk_sk2850%=:\nv_max_u32 v17, v84, v86\nv_cmp_eq_u32 vcc, -1, v17\ns_cbranch_vccz .Lqk_sk2857%=\n" \
"s_mov_b64 exec, vcc\nbuffer_load_dwordx4 v[84:87], v3, s[36:39], s42 offen offset:1024 sc1\ns_mov_b64 exec, -1\n.Lqk_sk2857%=:\nv_max_u32 v17, v88, v90\nv_cmp_eq_u32 vcc, -1, v17\ns_cbranch_vccz .Lqk_sk2864%=\ns_mov_b64 exec, vcc\n" \
"buffer_load_dwordx4 v[88:91], v3, s[36:39], s42 offen offset:2048 sc1\ns_mov_b64 exec, -1\n.Lqk_sk2864%=:\nv_max_u32 v17, v92, v94\nv_cmp_eq_u32 vcc, -1, v17\ns_cbranch_vccz .Lqk_sk2871%=\ns_mov_b64 exec, vcc\nbuffer_load_dwordx4 v[92:95], v3, s[36:39], s42 offen offset:3072 sc1\n" \
"s_mov_b64 exec, -1\n.Lqk_sk2871%=:\nbuffer_load_dword v16, v12, s[36:39], s56 offen sc1\nv_mov_b64 v[48:49], 0\nv_mov_b64 v[50:51], 0\nv_mov_b64 v[52:53], 0\nv_mov_b64 v[54:55], 0\nv_mov_b64 v[56:57], 0\n" \
"v_mov_b64 v[58:59], 0\nv_mov_b64 v[60:61], 0\nv_mov_b64 v[62:63], 0\ns_add_u32 s43, s43, 1\ns_cmp_lt_u32 s43, 262144\ns_cbranch_scc1 .Lqk_poll3%=\ns_branch .Lqk_abort%=\n.Lqk_abort%=:\n" \
"s_mov_b64 exec, -1\nv_mov_b32 v16, 0\ns_movk_i32 s44, 6400\nbuffer_store_dword v16, off, s[36:39], s44 sc1\n.Lqk_end%=:\ns_waitcnt vmcnt(0) lgkmcnt(0)\n" \
""
#define VU_ASM \
"v_mbcnt_lo_u32_b32 v255, -1, 0\nv_mbcnt_hi_u32_b32 v255, -1, v255\nv_lshlrev_b32 v255, 2, v255\nv_add_u32 v255, %[tb], v255\nds_read_b32 v0, v255 offset:0\nds_read_b32 v1, v255 offset:256\nds_read_b32 v2, v255 offset:512\nds_read_b32 v3, v255 offset:768\n" \
"ds_read_b32 v4, v255 offset:1024\nds_read_b32 v5, v255 offset:1280\nds_read_b32 v6, v255 offset:1536\nds_read_b32 v7, v255 offset:1792\nds_read_b32 v8, v255 offset:2048\nds_read_b32 v9, v255 offset:2304\nds_read_b32 v10, v255 offset:2560\nds_read_b32 v11, v255 offset:2816\n" \
"ds_read_b32 v90, v255 offset:3072\nds_read_b32 v91, v255 offset:3328\nds_read_b32 v92, v255 offset:3584\nds_read_b32 v93, v255 offset:3840\nds_read_b32 v94, v255 offset:4096\nds_read_b32 v95, v255 offset:4352\ns_waitcnt lgkmcnt(0)\ns_mov_b64 s[36:37], %[ws]\n" \
"s_mov_b32 s38, %[sz]\ns_mov_b32 s39, 0x20000\ns_mov_b32 s45, 2096896\nbuffer_load_dword v15, off, s[36:39], s45 sc1\ns_add_u32 m0, %[lw], 16384\ns_add_u32 s44, %[wo], 0\nbuffer_load_dwordx4 v90, %[rw], s44 offen nt lds\ns_add_u32 m0, %[lw], 17408\n" \
"s_add_u32 s44, %[wo], 65536\nbuffer_load_dwordx4 v90, %[rw], s44 offen nt lds\ns_add_u32 m0, %[lw], 18432\ns_add_u32 s44, %[wo], 131072\nbuffer_load_dwordx4 v90, %[rw], s44 offen nt lds\ns_add_u32 m0, %[lw], 19456\ns_add_u32 s44, %[wo], 196608\nbuffer_load_dwordx4 v90, %[rw], s44 offen nt lds\n" \
"s_add_u32 m0, %[lw], 20480\ns_add_u32 s44, %[wo], 524288\nbuffer_load_dwordx4 v90, %[rw], s44 offen nt lds\ns_add_u32 m0, %[lw], 21504\ns_add_u32 s44, %[wo], 589824\nbuffer_load_dwordx4 v90, %[rw], s44 offen nt lds\ns_add_u32 m0, %[lw], 22528\ns_add_u32 s44, %[wo], 655360\n" \
"buffer_load_dwordx4 v90, %[rw], s44 offen nt lds\ns_add_u32 m0, %[lw], 23552\ns_add_u32 s44, %[wo], 720896\nbuffer_load_dwordx4 v90, %[rw], s44 offen nt lds\ns_add_u32 m0, %[tb], 0\ns_add_u32 s44, %[wo], 1048576\nbuffer_load_dwordx4 v90, %[rw], s44 offen nt lds\ns_add_u32 m0, %[tb], 1024\n" \
"s_add_u32 s44, %[wo], 1114112\nbuffer_load_dwordx4 v90, %[rw], s44 offen nt lds\ns_add_u32 m0, %[tb], 2048\ns_add_u32 s44, %[wo], 1179648\nbuffer_load_dwordx4 v90, %[rw], s44 offen nt lds\ns_add_u32 m0, %[tb], 3072\ns_add_u32 s44, %[wo], 1245184\nbuffer_load_dwordx4 v90, %[rw], s44 offen nt lds\n" \
"s_add_u32 m0, %[tb], 4096\ns_add_u32 s44, %[wo], 1572864\nbuffer_load_dwordx4 v90, %[rw], s44 offen nt lds\ns_add_u32 m0, %[tb], 5120\ns_add_u32 s44, %[wo], 1638400\nbuffer_load_dwordx4 v90, %[rw], s44 offen nt lds\ns_add_u32 m0, %[tb], 6144\ns_add_u32 s44, %[wo], 1703936\n" \
"buffer_load_dwordx4 v90, %[rw], s44 offen nt lds\ns_add_u32 m0, %[tb], 7168\ns_add_u32 s44, %[wo], 1769472\nbuffer_load_dwordx4 v90, %[rw], s44 offen nt lds\ns_add_u32 m0, %[tb], 8192\ns_add_u32 s44, %[wo], 2097152\nbuffer_load_dwordx4 v90, %[rw], s44 offen nt lds\ns_add_u32 m0, %[tb], 9216\n" \
"s_add_u32 s44, %[wo], 2162688\nbuffer_load_dwordx4 v90, %[rw], s44 offen nt lds\ns_add_u32 m0, %[tb], 10240\ns_add_u32 s44, %[wo], 2228224\nbuffer_load_dwordx4 v90, %[rw], s44 offen nt lds\ns_add_u32 m0, %[tb], 11264\ns_add_u32 s44, %[wo], 2293760\nbuffer_load_dwordx4 v90, %[rw], s44 offen nt lds\n" \
"s_add_u32 m0, %[lw], 0\ns_add_u32 s44, %[wo], 2621440\nbuffer_load_dwordx4 v90, %[rw], s44 offen nt lds\ns_add_u32 m0, %[lw], 1024\ns_add_u32 s44, %[wo], 2686976\nbuffer_load_dwordx4 v90, %[rw], s44 offen nt lds\ns_add_u32 m0, %[lw], 2048\ns_add_u32 s44, %[wo], 2752512\n" \
"buffer_load_dwordx4 v90, %[rw], s44 offen nt lds\ns_add_u32 m0, %[lw], 3072\ns_add_u32 s44, %[wo], 2818048\nbuffer_load_dwordx4 v90, %[rw], s44 offen nt lds\ns_add_u32 m0, %[lw], 4096\ns_add_u32 s44, %[wo], 3145728\nbuffer_load_dwordx4 v90, %[rw], s44 offen nt lds\ns_add_u32 m0, %[lw], 5120\n" \
"s_add_u32 s44, %[wo], 3211264\nbuffer_load_dwordx4 v90, %[rw], s44 offen nt lds\ns_add_u32 m0, %[lw], 6144\ns_add_u32 s44, %[wo], 3276800\nbuffer_load_dwordx4 v90, %[rw], s44 offen nt lds\ns_add_u32 m0, %[lw], 7168\ns_add_u32 s44, %[wo], 3342336\nbuffer_load_dwordx4 v90, %[rw], s44 offen nt lds\n" \
"s_add_u32 m0, %[lw], 8192\ns_add_u32 s44, %[wo], 3670016\nbuffer_load_dwordx4 v90, %[rw], s44 offen nt lds\ns_add_u32 m0, %[lw], 9216\ns_add_u32 s44, %[wo], 3735552\nbuffer_load_dwordx4 v90, %[rw], s44 offen nt lds\ns_add_u32 m0, %[lw], 10240\ns_add_u32 s44, %[wo], 3801088\n" \
"buffer_load_dwordx4 v90, %[rw], s44 offen nt lds\ns_add_u32 m0, %[lw], 11264\ns_add_u32 s44, %[wo], 3866624\nbuffer_load_dwordx4 v90, %[rw], s44 offen nt lds\ns_waitcnt vmcnt(28)\ns_barrier\ns_add_u32 m0, %[lw], 12288\ns_add_u32 s44, %[wo], 4194304\n" \
"buffer_load_dwordx4 v90, %[rw], s44 offen nt lds\ns_add_u32 m0, %[lw], 13312\ns_add_u32 s44, %[wo], 4259840\nbuffer_load_dwordx4 v90, %[rw], s44 offen nt lds\ns_add_u32 m0, %[lw], 14336\ns_add_u32 s44, %[wo], 4325376\nbuffer_load_dwordx4 v90, %[rw], s44 offen nt lds\ns_add_u32 m0, %[lw], 15360\n" \
"s_add_u32 s44, %[wo], 4390912\nbuffer_load_dwordx4 v90, %[rw], s44 offen nt lds\nds_read_b64_tr_b16 a[0:1], v93 offset:16384\nds_read_b64_tr_b16 a[2:3], v93 offset:16896\nds_read_b64_tr_b16 a[16:17], v93 offset:16392\nds_read_b64_tr_b16 a[18:19], v93 offset:16904\nds_read_b64_tr_b16 a[4:5], v93 offset:40960\nds_read_b64_tr_b16 a[6:7], v93 offset:41472\n" \
"ds_read_b64_tr_b16 a[20:21], v93 offset:40968\nds_read_b64_tr_b16 a[22:23], v93 offset:41480\ns_waitcnt lgkmcnt(0)\ns_waitcnt vmcnt(28)\ns_barrier\ns_add_u32 m0, %[lw], 16384\ns_add_u32 s44, %[wo], 4718592\nbuffer_load_dwordx4 v90, %[rw], s44 offen nt lds\n" \
"s_add_u32 m0, %[lw], 17408\ns_add_u32 s44, %[wo], 4784128\nbuffer_load_dwordx4 v90, %[rw], s44 offen nt lds\ns_add_u32 m0, %[lw], 18432\ns_add_u32 s44, %[wo], 4849664\nbuffer_load_dwordx4 v90, %[rw], s44 offen nt lds\ns_add_u32 m0, %[lw], 19456\ns_add_u32 s44, %[wo], 4915200\n" \
"buffer_load_dwordx4 v90, %[rw], s44 offen nt lds\nds_read_b64_tr_b16 a[24:25], v93 offset:20480\nds_read_b64_tr_b16 a[26:27], v93 offset:20992\nds_read_b64_tr_b16 a[40:41], v93 offset:20488\nds_read_b64_tr_b16 a[42:43], v93 offset:21000\nds_read_b64_tr_b16 a[28:29], v93 offset:45056\nds_read_b64_tr_b16 a[30:31], v93 offset:45568\nds_read_b64_tr_b16 a[44:45], v93 offset:45064\n" \
"ds_read_b64_tr_b16 a[46:47], v93 offset:45576\ns_waitcnt lgkmcnt(0)\ns_waitcnt vmcnt(28)\ns_barrier\ns_add_u32 m0, %[lw], 20480\ns_add_u32 s44, %[wo], 5242880\nbuffer_load_dwordx4 v90, %[rw], s44 offen nt lds\ns_add_u32 m0, %[lw], 21504\n" \
"s_add_u32 s44, %[wo], 5308416\nbuffer_load_dwordx4 v90, %[rw], s44 offen nt lds\ns_add_u32 m0, %[lw], 22528\ns_add_u32 s44, %[wo], 5373952\nbuffer_load_dwordx4 v90, %[rw], s44 offen nt lds\ns_add_u32 m0, %[lw], 23552\ns_add_u32 s44, %[wo], 5439488\nbuffer_load_dwordx4 v90, %[rw], s44 offen nt lds\n" \
"ds_read_b64_tr_b16 v[84:85], v92 offset:0\nds_read_b64_tr_b16 v[86:87], v92 offset:512\nds_read_b64_tr_b16 a[64:65], v92 offset:8\nds_read_b64_tr_b16 a[66:67], v92 offset:520\nds_read_b64_tr_b16 v[80:81], v92 offset:12288\nds_read_b64_tr_b16 v[82:83], v92 offset:12800\nds_read_b64_tr_b16 a[68:69], v92 offset:12296\nds_read_b64_tr_b16 a[70:71], v92 offset:12808\n" \
"s_waitcnt lgkmcnt(0)\ns_waitcnt vmcnt(28)\ns_barrier\ns_add_u32 m0, %[tb], 0\ns_add_u32 s44, %[wo], 5767168\nbuffer_load_dwordx4 v90, %[rw], s44 offen nt lds\ns_add_u32 m0, %[tb], 1024\ns_add_u32 s44, %[wo], 5832704\n" \
"buffer_load_dwordx4 v90, %[rw], s44 offen nt lds\ns_add_u32 m0, %[tb], 2048\ns_add_u32 s44, %[wo], 5898240\nbuffer_load_dwordx4 v90, %[rw], s44 offen nt lds\ns_add_u32 m0, %[tb], 3072\ns_add_u32 s44, %[wo], 5963776\nbuffer_load_dwordx4 v90, %[rw], s44 offen nt lds\nds_read_b64_tr_b16 a[80:81], v92 offset:4096\n" \
"ds_read_b64_tr_b16 a[82:83], v92 offset:4608\nds_read_b64_tr_b16 a[88:89], v92 offset:4104\nds_read_b64_tr_b16 a[90:91], v92 offset:4616\nds_read_b64_tr_b16 a[84:85], v92 offset:16384\nds_read_b64_tr_b16 a[86:87], v92 offset:16896\nds_read_b64_tr_b16 a[92:93], v92 offset:16392\nds_read_b64_tr_b16 a[94:95], v92 offset:16904\ns_waitcnt lgkmcnt(0)\n" \
"s_waitcnt vmcnt(28)\ns_barrier\ns_add_u32 m0, %[tb], 4096\ns_add_u32 s44, %[wo], 6291456\nbuffer_load_dwordx4 v90, %[rw], s44 offen nt lds\ns_add_u32 m0, %[tb], 5120\ns_add_u32 s44, %[wo], 6356992\nbuffer_load_dwordx4 v90, %[rw], s44 offen nt lds\n" \
"s_add_u32 m0, %[tb], 6144\ns_add_u32 s44, %[wo], 6422528\nbuffer_load_dwordx4 v90, %[rw], s44 offen nt lds\ns_add_u32 m0, %[tb], 7168\ns_add_u32 s44, %[wo], 6488064\nbuffer_load_dwordx4 v90, %[rw], s44 offen nt lds\nds_read_b64_tr_b16 a[104:105], v92 offset:8192\nds_read_b64_tr_b16 a[106:107], v92 offset:8704\n" \
"ds_read_b64_tr_b16 a[120:121], v92 offset:8200\nds_read_b64_tr_b16 a[122:123], v92 offset:8712\nds_read_b64_tr_b16 a[108:109], v92 offset:20480\nds_read_b64_tr_b16 a[110:111], v92 offset:20992\nds_read_b64_tr_b16 a[124:125], v92 offset:20488\nds_read_b64_tr_b16 a[126:127], v92 offset:21000\ns_waitcnt lgkmcnt(0)\ns_waitcnt vmcnt(28)\n" \
"s_barrier\ns_add_u32 m0, %[tb], 8192\ns_add_u32 s44, %[wo], 6815744\nbuffer_load_dwordx4 v90, %[rw], s44 offen nt lds\ns_add_u32 m0, %[tb], 9216\ns_add_u32 s44, %[wo], 6881280\nbuffer_load_dwordx4 v90, %[rw], s44 offen nt lds\ns_add_u32 m0, %[tb], 10240\n" \
"s_add_u32 s44, %[wo], 6946816\nbuffer_load_dwordx4 v90, %[rw], s44 offen nt lds\ns_add_u32 m0, %[tb], 11264\ns_add_u32 s44, %[wo], 7012352\nbuffer_load_dwordx4 v90, %[rw], s44 offen nt lds\nds_read_b64_tr_b16 a[128:129], v93 offset:0\nds_read_b64_tr_b16 a[130:131], v93 offset:512\nds_read_b64_tr_b16 v[76:77], v93 offset:8\n" \
"ds_read_b64_tr_b16 v[78:79], v93 offset:520\nds_read_b64_tr_b16 a[132:133], v93 offset:24576\nds_read_b64_tr_b16 a[134:135], v93 offset:25088\nds_read_b64_tr_b16 v[72:73], v93 offset:24584\nds_read_b64_tr_b16 v[74:75], v93 offset:25096\ns_waitcnt lgkmcnt(0)\ns_waitcnt vmcnt(28)\ns_barrier\n" \
"s_add_u32 m0, %[lw], 0\ns_add_u32 s44, %[wo], 7340032\nbuffer_load_dwordx4 v90, %[rw], s44 offen nt lds\ns_add_u32 m0, %[lw], 1024\ns_add_u32 s44, %[wo], 7405568\nbuffer_load_dwordx4 v90, %[rw], s44 offen nt lds\ns_add_u32 m0, %[lw], 2048\ns_add_u32 s44, %[wo], 7471104\n" \
"buffer_load_dwordx4 v90, %[rw], s44 offen nt lds\ns_add_u32 m0, %[lw], 3072\ns_add_u32 s44, %[wo], 7536640\nbuffer_load_dwordx4 v90, %[rw], s44 offen nt lds\nds_read_b64_tr_b16 a[152:153], v93 offset:4096\nds_read_b64_tr_b16 a[154:155], v93 offset:4608\nds_read_b64_tr_b16 a[168:169], v93 offset:4104\nds_read_b64_tr_b16 a[170:171], v93 offset:4616\n" \
"ds_read_b64_tr_b16 a[156:157], v93 offset:28672\nds_read_b64_tr_b16 a[158:159], v93 offset:29184\nds_read_b64_tr_b16 a[172:173], v93 offset:28680\nds_read_b64_tr_b16 a[174:175], v93 offset:29192\ns_waitcnt lgkmcnt(0)\ns_waitcnt vmcnt(28)\ns_barrier\ns_add_u32 m0, %[lw], 4096\n" \
"s_add_u32 s44, %[wo], 7864320\nbuffer_load_dwordx4 v90, %[rw], s44 offen nt lds\ns_add_u32 m0, %[lw], 5120\ns_add_u32 s44, %[wo], 7929856\nbuffer_load_dwordx4 v90, %[rw], s44 offen nt lds\ns_add_u32 m0, %[lw], 6144\ns_add_u32 s44, %[wo], 7995392\nbuffer_load_dwordx4 v90, %[rw], s44 offen nt lds\n" \
"s_add_u32 m0, %[lw], 7168\ns_add_u32 s44, %[wo], 8060928\nbuffer_load_dwordx4 v90, %[rw], s44 offen nt lds\nds_read_b64_tr_b16 a[184:185], v93 offset:8192\nds_read_b64_tr_b16 a[186:187], v93 offset:8704\nds_read_b64_tr_b16 a[192:193], v93 offset:8200\nds_read_b64_tr_b16 a[194:195], v93 offset:8712\nds_read_b64_tr_b16 a[188:189], v93 offset:32768\n" \
"ds_read_b64_tr_b16 a[190:191], v93 offset:33280\nds_read_b64_tr_b16 a[196:197], v93 offset:32776\nds_read_b64_tr_b16 a[198:199], v93 offset:33288\ns_waitcnt lgkmcnt(0)\ns_waitcnt vmcnt(28)\ns_barrier\ns_add_u32 m0, %[lw], 8192\ns_add_u32 s44, %[w2], 0\n" \
"buffer_load_dwordx4 v91, %[ro], s44 offen nt lds\ns_add_u32 m0, %[lw], 9216\ns_add_u32 s44, %[w2], 32768\nbuffer_load_dwordx4 v91, %[ro], s44 offen nt lds\ns_add_u32 m0, %[lw], 10240\ns_add_u32 s44, %[w2], 65536\nbuffer_load_dwordx4 v91, %[ro], s44 offen nt lds\ns_add_u32 m0, %[lw], 11264\n" \
"s_add_u32 s44, %[w2], 98304\nbuffer_load_dwordx4 v91, %[ro], s44 offen nt lds\nds_read_b64_tr_b16 v[68:69], v93 offset:12288\nds_read_b64_tr_b16 v[70:71], v93 offset:12800\nds_read_b64_tr_b16 a[216:217], v93 offset:12296\nds_read_b64_tr_b16 a[218:219], v93 offset:12808\nds_read_b64_tr_b16 v[64:65], v93 offset:36864\nds_read_b64_tr_b16 v[66:67], v93 offset:37376\n" \
"ds_read_b64_tr_b16 a[220:221], v93 offset:36872\nds_read_b64_tr_b16 a[222:223], v93 offset:37384\ns_waitcnt lgkmcnt(0)\ns_waitcnt vmcnt(28)\ns_barrier\ns_add_u32 m0, %[lw], 12288\ns_add_u32 s44, %[w2], 128\nbuffer_load_dwordx4 v91, %[ro], s44 offen nt lds\n" \
"s_add_u32 m0, %[lw], 13312\ns_add_u32 s44, %[w2], 32896\nbuffer_load_dwordx4 v91, %[ro], s44 offen nt lds\ns_add_u32 m0, %[lw], 14336\ns_add_u32 s44, %[w2], 65664\nbuffer_load_dwordx4 v91, %[ro], s44 offen nt lds\ns_add_u32 m0, %[lw], 15360\ns_add_u32 s44, %[w2], 98432\n" \
"buffer_load_dwordx4 v91, %[ro], s44 offen nt lds\nds_read_b64_tr_b16 a[224:225], v93 offset:16384\nds_read_b64_tr_b16 a[226:227], v93 offset:16896\nds_read_b64_tr_b16 a[240:241], v93 offset:16392\nds_read_b64_tr_b16 a[242:243], v93 offset:16904\nds_read_b64_tr_b16 a[228:229], v93 offset:40960\nds_read_b64_tr_b16 a[230:231], v93 offset:41472\nds_read_b64_tr_b16 a[244:245], v93 offset:40968\n" \
"ds_read_b64_tr_b16 a[246:247], v93 offset:41480\ns_waitcnt lgkmcnt(0)\ns_waitcnt vmcnt(28)\ns_barrier\ns_add_u32 m0, %[lw], 16384\ns_add_u32 s44, %[w2], 256\nbuffer_load_dwordx4 v91, %[ro], s44 offen nt lds\ns_add_u32 m0, %[lw], 17408\n" \
"s_add_u32 s44, %[w2], 33024\nbuffer_load_dwordx4 v91, %[ro], s44 offen nt lds\ns_add_u32 m0, %[lw], 18432\ns_add_u32 s44, %[w2], 65792\nbuffer_load_dwordx4 v91, %[ro], s44 offen nt lds\ns_add_u32 m0, %[lw], 19456\ns_add_u32 s44, %[w2], 98560\nbuffer_load_dwordx4 v91, %[ro], s44 offen nt lds\n" \
"ds_read_b64_tr_b16 v[96:97], v93 offset:20480\nds_read_b64_tr_b16 v[98:99], v93 offset:20992\nds_read_b64_tr_b16 v[112:113], v93 offset:20488\nds_read_b64_tr_b16 v[114:115], v93 offset:21000\nds_read_b64_tr_b16 v[100:101], v93 offset:45056\nds_read_b64_tr_b16 v[102:103], v93 offset:45568\nds_read_b64_tr_b16 v[116:117], v93 offset:45064\nds_read_b64_tr_b16 v[118:119], v93 offset:45576\n" \
"s_waitcnt lgkmcnt(0)\ns_waitcnt vmcnt(28)\ns_barrier\ns_add_u32 m0, %[lw], 20480\ns_add_u32 s44, %[w2], 384\nbuffer_load_dwordx4 v91, %[ro], s44 offen nt lds\ns_add_u32 m0, %[lw], 21504\ns_add_u32 s44, %[w2], 33152\n" \
"buffer_load_dwordx4 v91, %[ro], s44 offen nt lds\ns_add_u32 m0, %[lw], 22528\ns_add_u32 s44, %[w2], 65920\nbuffer_load_dwordx4 v91, %[ro], s44 offen nt lds\ns_add_u32 m0, %[lw], 23552\ns_add_u32 s44, %[w2], 98688\nbuffer_load_dwordx4 v91, %[ro], s44 offen nt lds\nds_read_b64_tr_b16 v[128:129], v92 offset:0\n" \
"ds_read_b64_tr_b16 v[130:131], v92 offset:512\nds_read_b64_tr_b16 v[60:61], v92 offset:8\nds_read_b64_tr_b16 v[62:63], v92 offset:520\nds_read_b64_tr_b16 v[132:133], v92 offset:12288\nds_read_b64_tr_b16 v[134:135], v92 offset:12800\nds_read_b64_tr_b16 v[56:57], v92 offset:12296\nds_read_b64_tr_b16 v[58:59], v92 offset:12808\ns_waitcnt lgkmcnt(0)\n" \
"s_waitcnt vmcnt(28)\ns_barrier\ns_add_u32 m0, %[tb], 0\ns_add_u32 s44, %[w2], 139264\nbuffer_load_dwordx4 v91, %[ro], s44 offen nt lds\ns_add_u32 m0, %[tb], 1024\ns_add_u32 s44, %[w2], 172032\nbuffer_load_dwordx4 v91, %[ro], s44 offen nt lds\n" \
"s_add_u32 m0, %[tb], 2048\ns_add_u32 s44, %[w2], 204800\nbuffer_load_dwordx4 v91, %[ro], s44 offen nt lds\ns_add_u32 m0, %[tb], 3072\ns_add_u32 s44, %[w2], 237568\nbuffer_load_dwordx4 v91, %[ro], s44 offen nt lds\nds_read_b64_tr_b16 v[144:145], v92 offset:4096\nds_read_b64_tr_b16 v[146:147], v92 offset:4608\n" \
"ds_read_b64_tr_b16 v[160:161], v92 offset:4104\nds_read_b64_tr_b16 v[162:163], v92 offset:4616\nds_read_b64_tr_b16 v[148:149], v92 offset:16384\nds_read_b64_tr_b16 v[150:151], v92 offset:16896\nds_read_b64_tr_b16 v[164:165], v92 offset:16392\nds_read_b64_tr_b16 v[166:167], v92 offset:16904\ns_waitcnt lgkmcnt(0)\ns_waitcnt vmcnt(28)\n" \
"s_barrier\ns_add_u32 m0, %[tb], 4096\ns_add_u32 s44, %[w2], 139392\nbuffer_load_dwordx4 v91, %[ro], s44 offen nt lds\ns_add_u32 m0, %[tb], 5120\ns_add_u32 s44, %[w2], 172160\nbuffer_load_dwordx4 v91, %[ro], s44 offen nt lds\ns_add_u32 m0, %[tb], 6144\n" \
"s_add_u32 s44, %[w2], 204928\nbuffer_load_dwordx4 v91, %[ro], s44 offen nt lds\ns_add_u32 m0, %[tb], 7168\ns_add_u32 s44, %[w2], 237696\nbuffer_load_dwordx4 v91, %[ro], s44 offen nt lds\nds_read_b64_tr_b16 v[168:169], v92 offset:8192\nds_read_b64_tr_b16 v[170:171], v92 offset:8704\nds_read_b64_tr_b16 v[184:185], v92 offset:8200\n" \
"ds_read_b64_tr_b16 v[186:187], v92 offset:8712\nds_read_b64_tr_b16 v[172:173], v92 offset:20480\nds_read_b64_tr_b16 v[174:175], v92 offset:20992\nds_read_b64_tr_b16 v[188:189], v92 offset:20488\nds_read_b64_tr_b16 v[190:191], v92 offset:21000\ns_waitcnt lgkmcnt(0)\ns_waitcnt vmcnt(28)\ns_barrier\n" \
"s_add_u32 m0, %[tb], 8192\ns_add_u32 s44, %[w2], 139520\nbuffer_load_dwordx4 v91, %[ro], s44 offen nt lds\ns_add_u32 m0, %[tb], 9216\ns_add_u32 s44, %[w2], 172288\nbuffer_load_dwordx4 v91, %[ro], s44 offen nt lds\ns_add_u32 m0, %[tb], 10240\ns_add_u32 s44, %[w2], 205056\n" \
"buffer_load_dwordx4 v91, %[ro], s44 offen nt lds\ns_add_u32 m0, %[tb], 11264\ns_add_u32 s44, %[w2], 237824\nbuffer_load_dwordx4 v91, %[ro], s44 offen nt lds\nds_read_b64_tr_b16 v[200:201], v93 offset:0\nds_read_b64_tr_b16 v[202:203], v93 offset:512\nds_read_b64_tr_b16 v[216:217], v93 offset:8\nds_read_b64_tr_b16 v[218:219], v93 offset:520\n" \
"ds_read_b64_tr_b16 v[204:205], v93 offset:24576\nds_read_b64_tr_b16 v[206:207], v93 offset:25088\nds_read_b64_tr_b16 v[220:221], v93 offset:24584\nds_read_b64_tr_b16 v[222:223], v93 offset:25096\ns_waitcnt lgkmcnt(0)\ns_waitcnt vmcnt(28)\ns_barrier\ns_add_u32 m0, %[lw], 0\n" \
"s_add_u32 s44, %[w2], 139648\nbuffer_load_dwordx4 v91, %[ro], s44 offen nt lds\ns_add_u32 m0, %[lw], 1024\ns_add_u32 s44, %[w2], 172416\nbuffer_load_dwordx4 v91, %[ro], s44 offen nt lds\ns_add_u32 m0, %[lw], 2048\ns_add_u32 s44, %[w2], 205184\nbuffer_load_dwordx4 v91, %[ro], s44 offen nt lds\n" \
"s_add_u32 m0, %[lw], 3072\ns_add_u32 s44, %[w2], 237952\nbuffer_load_dwordx4 v91, %[ro], s44 offen nt lds\nds_read_b64_tr_b16 v[232:233], v93 offset:4096\nds_read_b64_tr_b16 v[234:235], v93 offset:4608\nds_read_b64_tr_b16 v[240:241], v93 offset:4104\nds_read_b64_tr_b16 v[242:243], v93 offset:4616\nds_read_b64_tr_b16 v[236:237], v93 offset:28672\n" \
"ds_read_b64_tr_b16 v[238:239], v93 offset:29184\nds_read_b64_tr_b16 v[244:245], v93 offset:28680\nds_read_b64_tr_b16 v[246:247], v93 offset:29192\ns_waitcnt lgkmcnt(0)\ns_waitcnt vmcnt(28)\ns_barrier\ns_add_u32 m0, %[lw], 4096\ns_add_u32 s44, %[w2], 131072\n" \
"buffer_load_dwordx4 v91, %[ro], s44 offen nt lds\ns_add_u32 m0, %[lw], 5120\ns_add_u32 s44, %[w2], 163840\nbuffer_load_dwordx4 v91, %[ro], s44 offen nt lds\ns_add_u32 m0, %[lw], 6144\ns_add_u32 s44, %[w2], 196608\nbuffer_load_dwordx4 v91, %[ro], s44 offen nt lds\ns_add_u32 m0, %[lw], 7168\n" \
"s_add_u32 s44, %[w2], 229376\nbuffer_load_dwordx4 v91, %[ro], s44 offen nt lds\nds_read_b64_tr_b16 a[8:9], v95 offset:8192\nds_read_b64_tr_b16 a[10:11], v95 offset:8448\nds_read_b64_tr_b16 a[12:13], v95 offset:10240\nds_read_b64_tr_b16 a[14:15], v95 offset:10496\nds_read_b64_tr_b16 a[208:209], v95 offset:8256\nds_read_b64_tr_b16 a[210:211], v95 offset:8512\n" \
"ds_read_b64_tr_b16 a[212:213], v95 offset:10304\nds_read_b64_tr_b16 a[214:215], v95 offset:10560\ns_waitcnt lgkmcnt(0)\ns_waitcnt vmcnt(28)\ns_barrier\ns_add_u32 m0, %[lw], 8192\ns_add_u32 s44, %[w2], 131200\nbuffer_load_dwordx4 v91, %[ro], s44 offen nt lds\n" \
"s_add_u32 m0, %[lw], 9216\ns_add_u32 s44, %[w2], 163968\nbuffer_load_dwordx4 v91, %[ro], s44 offen nt lds\ns_add_u32 m0, %[lw], 10240\ns_add_u32 s44, %[w2], 196736\nbuffer_load_dwordx4 v91, %[ro], s44 offen nt lds\ns_add_u32 m0, %[lw], 11264\ns_add_u32 s44, %[w2], 229504\n" \
"buffer_load_dwordx4 v91, %[ro], s44 offen nt lds\nds_read_b64_tr_b16 a[56:57], v95 offset:12288\nds_read_b64_tr_b16 a[58:59], v95 offset:12544\nds_read_b64_tr_b16 a[60:61], v95 offset:14336\nds_read_b64_tr_b16 a[62:63], v95 offset:14592\nds_read_b64_tr_b16 v[104:105], v95 offset:12352\nds_read_b64_tr_b16 v[106:107], v95 offset:12608\nds_read_b64_tr_b16 v[108:109], v95 offset:14400\n" \
"ds_read_b64_tr_b16 v[110:111], v95 offset:14656\ns_waitcnt lgkmcnt(0)\ns_waitcnt vmcnt(28)\ns_barrier\ns_add_u32 m0, %[lw], 12288\ns_add_u32 s44, %[w2], 131328\nbuffer_load_dwordx4 v91, %[ro], s44 offen nt lds\ns_add_u32 m0, %[lw], 13312\n" \
"s_add_u32 s44, %[w2], 164096\nbuffer_load_dwordx4 v91, %[ro], s44 offen nt lds\ns_add_u32 m0, %[lw], 14336\ns_add_u32 s44, %[w2], 196864\nbuffer_load_dwordx4 v91, %[ro], s44 offen nt lds\ns_add_u32 m0, %[lw], 15360\ns_add_u32 s44, %[w2], 229632\nbuffer_load_dwordx4 v91, %[ro], s44 offen nt lds\n" \
"ds_read_b64_tr_b16 a[112:113], v95 offset:16384\nds_read_b64_tr_b16 a[114:115], v95 offset:16640\nds_read_b64_tr_b16 a[116:117], v95 offset:18432\nds_read_b64_tr_b16 a[118:119], v95 offset:18688\nds_read_b64_tr_b16 v[152:153], v95 offset:16448\nds_read_b64_tr_b16 v[154:155], v95 offset:16704\nds_read_b64_tr_b16 v[156:157], v95 offset:18496\nds_read_b64_tr_b16 v[158:159], v95 offset:18752\n" \
"s_waitcnt lgkmcnt(0)\ns_waitcnt vmcnt(28)\ns_barrier\ns_add_u32 m0, %[lw], 16384\ns_add_u32 s44, %[w2], 131456\nbuffer_load_dwordx4 v91, %[ro], s44 offen nt lds\ns_add_u32 m0, %[lw], 17408\ns_add_u32 s44, %[w2], 164224\n" \
"buffer_load_dwordx4 v91, %[ro], s44 offen nt lds\ns_add_u32 m0, %[lw], 18432\ns_add_u32 s44, %[w2], 196992\nbuffer_load_dwordx4 v91, %[ro], s44 offen nt lds\ns_add_u32 m0, %[lw], 19456\ns_add_u32 s44, %[w2], 229760\nbuffer_load_dwordx4 v91, %[ro], s44 offen nt lds\nds_read_b64_tr_b16 a[160:161], v95 offset:20480\n" \
"ds_read_b64_tr_b16 a[162:163], v95 offset:20736\nds_read_b64_tr_b16 a[164:165], v95 offset:22528\nds_read_b64_tr_b16 a[166:167], v95 offset:22784\nds_read_b64_tr_b16 v[208:209], v95 offset:20544\nds_read_b64_tr_b16 v[210:211], v95 offset:20800\nds_read_b64_tr_b16 v[212:213], v95 offset:22592\nds_read_b64_tr_b16 v[214:215], v95 offset:22848\ns_waitcnt lgkmcnt(0)\n" \
"s_waitcnt vmcnt(28)\ns_barrier\ns_add_u32 m0, %[lw], 20480\ns_add_u32 s44, %[w2], 8192\nbuffer_load_dwordx4 v91, %[ro], s44 offen nt lds\ns_add_u32 m0, %[lw], 21504\ns_add_u32 s44, %[w2], 40960\nbuffer_load_dwordx4 v91, %[ro], s44 offen nt lds\n" \
"s_add_u32 m0, %[lw], 22528\ns_add_u32 s44, %[w2], 73728\nbuffer_load_dwordx4 v91, %[ro], s44 offen nt lds\ns_add_u32 m0, %[lw], 23552\ns_add_u32 s44, %[w2], 106496\nbuffer_load_dwordx4 v91, %[ro], s44 offen nt lds\nds_read_b64_tr_b16 a[48:49], v94 offset:0\nds_read_b64_tr_b16 a[50:51], v94 offset:256\n" \
"ds_read_b64_tr_b16 a[52:53], v94 offset:2048\nds_read_b64_tr_b16 a[54:55], v94 offset:2304\nds_read_b64_tr_b16 a[248:249], v94 offset:64\nds_read_b64_tr_b16 a[250:251], v94 offset:320\nds_read_b64_tr_b16 a[252:253], v94 offset:2112\nds_read_b64_tr_b16 a[254:255], v94 offset:2368\ns_waitcnt lgkmcnt(0)\ns_waitcnt vmcnt(28)\n" \
"s_barrier\ns_add_u32 m0, %[tb], 0\ns_add_u32 s44, %[w2], 8320\nbuffer_load_dwordx4 v91, %[ro], s44 offen nt lds\ns_add_u32 m0, %[tb], 1024\ns_add_u32 s44, %[w2], 41088\nbuffer_load_dwordx4 v91, %[ro], s44 offen nt lds\ns_add_u32 m0, %[tb], 2048\n" \
"s_add_u32 s44, %[w2], 73856\nbuffer_load_dwordx4 v91, %[ro], s44 offen nt lds\ns_add_u32 m0, %[tb], 3072\ns_add_u32 s44, %[w2], 106624\nbuffer_load_dwordx4 v91, %[ro], s44 offen nt lds\nds_read_b64_tr_b16 a[96:97], v94 offset:4096\nds_read_b64_tr_b16 a[98:99], v94 offset:4352\nds_read_b64_tr_b16 a[100:101], v94 offset:6144\n" \
"ds_read_b64_tr_b16 a[102:103], v94 offset:6400\nds_read_b64_tr_b16 v[136:137], v94 offset:4160\nds_read_b64_tr_b16 v[138:139], v94 offset:4416\nds_read_b64_tr_b16 v[140:141], v94 offset:6208\nds_read_b64_tr_b16 v[142:143], v94 offset:6464\ns_waitcnt lgkmcnt(0)\nbuffer_load_dwordx2 v[12:13], off, s[36:39], s45 sc1\ns_waitcnt vmcnt(29)\n" \
"s_barrier\ns_add_u32 m0, %[tb], 4096\ns_add_u32 s44, %[w2], 8448\nbuffer_load_dwordx4 v91, %[ro], s44 offen nt lds\ns_add_u32 m0, %[tb], 5120\ns_add_u32 s44, %[w2], 41216\nbuffer_load_dwordx4 v91, %[ro], s44 offen nt lds\ns_add_u32 m0, %[tb], 6144\n" \
"s_add_u32 s44, %[w2], 73984\nbuffer_load_dwordx4 v91, %[ro], s44 offen nt lds\ns_add_u32 m0, %[tb], 7168\ns_add_u32 s44, %[w2], 106752\nbuffer_load_dwordx4 v91, %[ro], s44 offen nt lds\nds_read_b64_tr_b16 a[144:145], v94 offset:8192\nds_read_b64_tr_b16 a[146:147], v94 offset:8448\nds_read_b64_tr_b16 a[148:149], v94 offset:10240\n" \
"ds_read_b64_tr_b16 a[150:151], v94 offset:10496\nds_read_b64_tr_b16 v[192:193], v94 offset:8256\nds_read_b64_tr_b16 v[194:195], v94 offset:8512\nds_read_b64_tr_b16 v[196:197], v94 offset:10304\nds_read_b64_tr_b16 v[198:199], v94 offset:10560\ns_waitcnt lgkmcnt(0)\ns_waitcnt vmcnt(29)\ns_barrier\n" \
"s_add_u32 m0, %[tb], 8192\ns_add_u32 s44, %[w2], 8576\nbuffer_load_dwordx4 v91, %[ro], s44 offen nt lds\ns_add_u32 m0, %[tb], 9216\ns_add_u32 s44, %[w2], 41344\nbuffer_load_dwordx4 v91, %[ro], s44 offen nt lds\ns_add_u32 m0, %[tb], 10240\ns_add_u32 s44, %[w2], 74112\n" \
"buffer_load_dwordx4 v91, %[ro], s44 offen nt lds\ns_add_u32 m0, %[tb], 11264\ns_add_u32 s44, %[w2], 106880\nbuffer_load_dwordx4 v91, %[ro], s44 offen nt lds\nds_read_b64_tr_b16 a[200:201], v95 offset:0\nds_read_b64_tr_b16 a[202:203], v95 offset:256\nds_read_b64_tr_b16 a[204:205], v95 offset:2048\nds_read_b64_tr_b16 a[206:207], v95 offset:2304\n" \
"ds_read_b64_tr_b16 v[248:249], v95 offset:64\nds_read_b64_tr_b16 v[250:251], v95 offset:320\nds_read_b64_tr_b16 v[252:253], v95 offset:2112\nds_read_b64_tr_b16 v[254:255], v95 offset:2368\ns_waitcnt lgkmcnt(0)\ns_waitcnt vmcnt(29)\ns_barrier\nds_read_b64_tr_b16 v[52:53], v95 offset:4096\n" \
"ds_read_b64_tr_b16 v[54:55], v95 offset:4352\nds_read_b64_tr_b16 v[48:49], v95 offset:6144\nds_read_b64_tr_b16 v[50:51], v95 offset:6400\nds_read_b64_tr_b16 v[44:45], v95 offset:4160\nds_read_b64_tr_b16 v[46:47], v95 offset:4416\nds_read_b64_tr_b16 v[40:41], v95 offset:6208\nds_read_b64_tr_b16 v[42:43], v95 offset:6464\ns_waitcnt lgkmcnt(0)\n" \
"ds_write_b128 v1, v[84:87] offset:0\nds_write_b128 v1, v[80:83] offset:1024\nds_write_b128 v1, v[76:79] offset:2048\nds_write_b128 v1, v[72:75] offset:3072\ns_waitcnt vmcnt(25)\ns_barrier\nds_read_b64_tr_b16 a[72:73], v95 offset:8192\nds_read_b64_tr_b16 a[74:75], v95 offset:8448\n" \
"ds_read_b64_tr_b16 a[76:77], v95 offset:10240\nds_read_b64_tr_b16 a[78:79], v95 offset:10496\nds_read_b64_tr_b16 v[120:121], v95 offset:8256\nds_read_b64_tr_b16 v[122:123], v95 offset:8512\nds_read_b64_tr_b16 v[124:125], v95 offset:10304\nds_read_b64_tr_b16 v[126:127], v95 offset:10560\ns_waitcnt lgkmcnt(0)\nds_write_b128 v1, v[68:71] offset:4096\n" \
"ds_write_b128 v1, v[64:67] offset:5120\nds_write_b128 v1, v[60:63] offset:6144\nds_write_b128 v1, v[56:59] offset:7168\ns_waitcnt vmcnt(21)\ns_barrier\nds_read_b64_tr_b16 v[56:57], v95 offset:12288\nds_read_b64_tr_b16 v[58:59], v95 offset:12544\nds_read_b64_tr_b16 v[60:61], v95 offset:14336\n" \
"ds_read_b64_tr_b16 v[62:63], v95 offset:14592\nds_read_b64_tr_b16 v[64:65], v95 offset:12352\nds_read_b64_tr_b16 v[66:67], v95 offset:12608\nds_read_b64_tr_b16 v[68:69], v95 offset:14400\nds_read_b64_tr_b16 v[70:71], v95 offset:14656\ns_waitcnt lgkmcnt(0)\nds_write_b128 v1, v[52:55] offset:8192\nds_write_b128 v1, v[48:51] offset:9216\n" \
"ds_write_b128 v1, v[44:47] offset:10240\nds_write_b128 v1, v[40:43] offset:11264\ns_waitcnt vmcnt(17)\ns_barrier\nds_read_b64_tr_b16 a[176:177], v95 offset:16384\nds_read_b64_tr_b16 a[178:179], v95 offset:16640\nds_read_b64_tr_b16 a[180:181], v95 offset:18432\nds_read_b64_tr_b16 a[182:183], v95 offset:18688\n" \
"ds_read_b64_tr_b16 v[224:225], v95 offset:16448\nds_read_b64_tr_b16 v[226:227], v95 offset:16704\nds_read_b64_tr_b16 v[228:229], v95 offset:18496\nds_read_b64_tr_b16 v[230:231], v95 offset:18752\ns_waitcnt lgkmcnt(0)\nds_write_b128 v1, v[56:59] offset:12288\nds_write_b128 v1, v[60:63] offset:13312\nds_write_b128 v1, v[64:67] offset:14336\n" \
"ds_write_b128 v1, v[68:71] offset:15360\ns_waitcnt vmcnt(13)\ns_barrier\nds_read_b64_tr_b16 a[32:33], v95 offset:20480\nds_read_b64_tr_b16 a[34:35], v95 offset:20736\nds_read_b64_tr_b16 a[36:37], v95 offset:22528\nds_read_b64_tr_b16 a[38:39], v95 offset:22784\nds_read_b64_tr_b16 a[232:233], v95 offset:20544\n" \
"ds_read_b64_tr_b16 a[234:235], v95 offset:20800\nds_read_b64_tr_b16 a[236:237], v95 offset:22592\nds_read_b64_tr_b16 a[238:239], v95 offset:22848\ns_waitcnt lgkmcnt(0)\ns_waitcnt vmcnt(9)\ns_barrier\nds_read_b64_tr_b16 v[68:69], v94 offset:0\nds_read_b64_tr_b16 v[70:71], v94 offset:256\n" \
"ds_read_b64_tr_b16 v[64:65], v94 offset:2048\nds_read_b64_tr_b16 v[66:67], v94 offset:2304\nds_read_b64_tr_b16 v[60:61], v94 offset:64\nds_read_b64_tr_b16 v[62:63], v94 offset:320\nds_read_b64_tr_b16 v[56:57], v94 offset:2112\nds_read_b64_tr_b16 v[58:59], v94 offset:2368\ns_waitcnt lgkmcnt(0)\nds_write_b128 v1, v[68:71] offset:16384\n" \
"ds_write_b128 v1, v[64:67] offset:17408\nds_write_b128 v1, v[60:63] offset:18432\nds_write_b128 v1, v[56:59] offset:19456\ns_waitcnt vmcnt(4)\ns_barrier\nds_read_b64_tr_b16 a[136:137], v94 offset:4096\nds_read_b64_tr_b16 a[138:139], v94 offset:4352\nds_read_b64_tr_b16 a[140:141], v94 offset:6144\n" \
"ds_read_b64_tr_b16 a[142:143], v94 offset:6400\nds_read_b64_tr_b16 v[176:177], v94 offset:4160\nds_read_b64_tr_b16 v[178:179], v94 offset:4416\nds_read_b64_tr_b16 v[180:181], v94 offset:6208\nds_read_b64_tr_b16 v[182:183], v94 offset:6464\ns_waitcnt lgkmcnt(0)\ns_waitcnt vmcnt(0)\ns_barrier\n" \
"ds_read_b64_tr_b16 v[56:57], v94 offset:8192\nds_read_b64_tr_b16 v[58:59], v94 offset:8448\nds_read_b64_tr_b16 v[60:61], v94 offset:10240\nds_read_b64_tr_b16 v[62:63], v94 offset:10496\nds_read_b64_tr_b16 v[64:65], v94 offset:8256\nds_read_b64_tr_b16 v[66:67], v94 offset:8512\nds_read_b64_tr_b16 v[68:69], v94 offset:10304\nds_read_b64_tr_b16 v[70:71], v94 offset:10560\n" \
"s_waitcnt lgkmcnt(0)\ns_mov_b32 s43, 0\ns_mov_b32 s86, 110\ns_mov_b32 s89, 0\n.Lvu_hb%=:\nv_cmp_ne_u32 vcc, v15, v12\ns_cbranch_vccnz .Lvu_hb1%=\ns_sleep 4\n" \
"buffer_load_dwordx2 v[12:13], off, s[36:39], s45 sc1\ns_waitcnt vmcnt(0)\ns_add_u32 s43, s43, 1\ns_cmp_lt_u32 s43, 262144\ns_cbranch_scc1 .Lvu_hb%=\ns_branch .Lvu_abort%=\n.Lvu_hb1%=:\nv_readfirstlane_b32 s44, v13\n" \
"s_and_b32 s44, s44, 0x200000\ns_add_u32 s36, s36, s44\ns_addc_u32 s37, s37, 0\nds_write_b128 v1, v[56:59] offset:20480\nds_write_b128 v1, v[60:63] offset:21504\nds_write_b128 v1, v[64:67] offset:22528\nds_write_b128 v1, v[68:71] offset:23552\ns_waitcnt lgkmcnt(0)\n" \
"s_barrier\ns_mov_b32 s40, -1\ns_mov_b32 s43, 0\ns_lshl_b32 s57, %[w], 12\ns_mov_b32 s56, 0\ns_mov_b32 s42, 0\ns_mov_b32 s60, -2147483648\ns_mov_b32 s54, -2147483648\n" \
"s_add_u32 s44, s40, 1\ns_lshl_b32 s44, s44, 17\ns_add_u32 s44, s44, %[xn]\ns_lshr_b32 s45, s44, 8\ns_and_b32 s45, s45, 0x2000\ns_xor_b32 s41, s44, s45\ns_add_u32 s41, s41, %[xw]\n.Lvu_f0%=:\n" \
"buffer_load_dword v14, v8, s[36:39], s56 offen sc1\ns_waitcnt vmcnt(0)\nv_cmp_ge_i32 vcc, s40, v14\ns_cbranch_vccz .Lvu_f0fok%=\ns_add_u32 s43, s43, 1\ns_cmp_lt_u32 s43, 262144\ns_cbranch_scc1 .Lvu_f0%=\ns_branch .Lvu_abort%=\n" \
".Lvu_f0fok%=:\ns_add_u32 m0, s57, 0\ns_add_u32 s58, s41, 0\nbuffer_load_dwordx4 v0, s[36:39], s58 offen offset:0 sc1 lds\nbuffer_load_dwordx4 v0, s[36:39], s58 offen offset:1024 sc1 lds\nbuffer_load_dwordx4 v0, s[36:39], s58 offen offset:2048 sc1 lds\nbuffer_load_dwordx4 v0, s[36:39], s58 offen offset:3072 sc1 lds\ns_add_u32 m0, s57, 32768\n" \
"s_add_u32 s58, s41, 32768\nbuffer_load_dwordx4 v0, s[36:39], s58 offen offset:0 sc1 lds\nbuffer_load_dwordx4 v0, s[36:39], s58 offen offset:1024 sc1 lds\nbuffer_load_dwordx4 v0, s[36:39], s58 offen offset:2048 sc1 lds\nbuffer_load_dwordx4 v0, s[36:39], s58 offen offset:3072 sc1 lds\ns_waitcnt vmcnt(0)\ns_barrier\nds_read_b128 v[32:35], v2 offset:0\n" \
"ds_read_b128 v[36:39], v2 offset:256\nds_read_b128 v[40:43], v2 offset:2048\nds_read_b128 v[44:47], v2 offset:2304\n.Lvu_tok%=:\nds_read_b128 v[64:67], v2 offset:4096\nds_read_b128 v[68:71], v2 offset:4352\ns_waitcnt lgkmcnt(5)\nv_mfma_f32_16x16x32_bf16 v[16:19], a[0:3], v[32:35], 0\n" \
"v_cvt_pk_bf16_f32 v92, v80, v81\nv_cvt_pk_bf16_f32 v93, v82, v83\nv_mfma_f32_16x16x32_bf16 v[20:23], a[16:19], v[32:35], 0\ns_waitcnt lgkmcnt(4)\nds_read_b128 v[32:35], v2 offset:6144\nv_cvt_pk_bf16_f32 v94, v84, v85\nv_cvt_pk_bf16_f32 v95, v86, v87\nv_mfma_f32_16x16x32_bf16 v[24:27], a[0:3], v[36:39], 0\n" \
"v_and_or_b32 v92, v92, -2, v13\nv_and_or_b32 v94, v94, -2, v13\nv_mfma_f32_16x16x32_bf16 v[28:31], a[16:19], v[36:39], 0\ns_waitcnt lgkmcnt(4)\nbuffer_store_dwordx4 v[92:95], v7, s[36:39], s60 offen offset:2560 sc1\nds_read_b128 v[36:39], v2 offset:6400\nv_mfma_f32_16x16x32_bf16 v[16:19], a[4:7], v[40:43], v[16:19]\nds_read_b128 v[56:59], v1 offset:0\n" \
"ds_read_b128 v[60:63], v1 offset:1024\nv_mfma_f32_16x16x32_bf16 v[20:23], a[20:23], v[40:43], v[20:23]\ns_waitcnt lgkmcnt(6)\ns_add_u32 m0, s57, 16384\nds_read_b128 v[40:43], v2 offset:8192\nv_mfma_f32_16x16x32_bf16 v[24:27], a[4:7], v[44:47], v[24:27]\ns_add_u32 s60, s41, 65536\nbuffer_load_dwordx4 v0, s[36:39], s60 offen offset:0 sc1 lds\n" \
"v_mfma_f32_16x16x32_bf16 v[28:31], a[20:23], v[44:47], v[28:31]\ns_waitcnt lgkmcnt(6)\nds_read_b128 v[44:47], v2 offset:8448\nv_mfma_f32_16x16x32_bf16 v[16:19], a[24:27], v[64:67], v[16:19]\nv_mfma_f32_16x16x32_bf16 v[20:23], a[40:43], v[64:67], v[20:23]\ns_waitcnt lgkmcnt(6)\nds_read_b128 v[64:67], v2 offset:10240\nv_mfma_f32_16x16x32_bf16 v[24:27], a[24:27], v[68:71], v[24:27]\n" \
"buffer_load_dwordx4 v0, s[36:39], s60 offen offset:1024 sc1 lds\nv_mfma_f32_16x16x32_bf16 v[28:31], a[40:43], v[68:71], v[28:31]\ns_waitcnt lgkmcnt(6)\nds_read_b128 v[68:71], v2 offset:10496\nv_mfma_f32_16x16x32_bf16 v[16:19], a[28:31], v[32:35], v[16:19]\nv_mfma_f32_16x16x32_bf16 v[20:23], a[44:47], v[32:35], v[20:23]\ns_waitcnt lgkmcnt(6)\nds_read_b128 v[32:35], v2 offset:12288\n" \
"v_mfma_f32_16x16x32_bf16 v[24:27], a[28:31], v[36:39], v[24:27]\nbuffer_load_dwordx4 v0, s[36:39], s60 offen offset:2048 sc1 lds\nv_mfma_f32_16x16x32_bf16 v[28:31], a[44:47], v[36:39], v[28:31]\ns_waitcnt lgkmcnt(4)\nds_read_b128 v[36:39], v2 offset:12544\nv_mfma_f32_16x16x32_bf16 v[16:19], v[56:59], v[40:43], v[16:19]\nv_mfma_f32_16x16x32_bf16 v[20:23], a[64:67], v[40:43], v[20:23]\ns_waitcnt lgkmcnt(4)\n" \
"ds_read_b128 v[40:43], v2 offset:14336\nv_mfma_f32_16x16x32_bf16 v[24:27], v[56:59], v[44:47], v[24:27]\nbuffer_load_dwordx4 v0, s[36:39], s60 offen offset:3072 sc1 lds\nv_mfma_f32_16x16x32_bf16 v[28:31], a[64:67], v[44:47], v[28:31]\ns_waitcnt lgkmcnt(4)\nds_read_b128 v[44:47], v2 offset:14592\nv_mfma_f32_16x16x32_bf16 v[16:19], v[60:63], v[64:67], v[16:19]\ns_add_u32 s56, s56, %[fs]\n" \
"s_add_u32 s62, s40, 1\ns_lshr_b32 m0, s57, 4\nv_mfma_f32_16x16x32_bf16 v[20:23], a[68:71], v[64:67], v[20:23]\ns_add_u32 m0, m0, 159744\ns_waitcnt lgkmcnt(4)\nds_read_b128 v[64:67], v2 offset:32768\nv_mfma_f32_16x16x32_bf16 v[24:27], v[60:63], v[68:71], v[24:27]\nbuffer_load_dword v8, s[36:39], s56 offen sc1 lds\n" \
"v_lshrrev_b32 v14, 2, v0\nv_mfma_f32_16x16x32_bf16 v[28:31], a[68:71], v[68:71], v[28:31]\ns_waitcnt lgkmcnt(4)\nds_read_b128 v[68:71], v2 offset:33024\nv_mfma_f32_16x16x32_bf16 v[16:19], a[80:83], v[32:35], v[16:19]\nv_add_u32 v14, 159744, v14\nv_mfma_f32_16x16x32_bf16 v[20:23], a[88:91], v[32:35], v[20:23]\ns_waitcnt lgkmcnt(4)\n" \
"ds_read_b128 v[32:35], v2 offset:34816\nv_mfma_f32_16x16x32_bf16 v[24:27], a[80:83], v[36:39], v[24:27]\nds_read_b128 v[56:59], v1 offset:2048\nds_read_b128 v[60:63], v1 offset:3072\nv_mfma_f32_16x16x32_bf16 v[28:31], a[88:91], v[36:39], v[28:31]\ns_waitcnt lgkmcnt(6)\nds_read_b128 v[36:39], v2 offset:35072\nv_mfma_f32_16x16x32_bf16 v[16:19], a[84:87], v[40:43], v[16:19]\n" \
"v_mfma_f32_16x16x32_bf16 v[20:23], a[92:95], v[40:43], v[20:23]\ns_waitcnt lgkmcnt(6)\nds_read_b128 v[40:43], v2 offset:36864\nv_mfma_f32_16x16x32_bf16 v[24:27], a[84:87], v[44:47], v[24:27]\nv_mfma_f32_16x16x32_bf16 v[28:31], a[92:95], v[44:47], v[28:31]\ns_waitcnt lgkmcnt(6)\nds_read_b128 v[44:47], v2 offset:37120\nv_mfma_f32_16x16x32_bf16 v[16:19], a[104:107], v[64:67], v[16:19]\n" \
"v_mfma_f32_16x16x32_bf16 v[20:23], a[120:123], v[64:67], v[20:23]\ns_waitcnt lgkmcnt(6)\nds_read_b128 v[64:67], v2 offset:38912\nv_mfma_f32_16x16x32_bf16 v[24:27], a[104:107], v[68:71], v[24:27]\ns_barrier\nv_mfma_f32_16x16x32_bf16 v[28:31], a[120:123], v[68:71], v[28:31]\ns_waitcnt lgkmcnt(6)\ns_add_u32 m0, s57, 0\n" \
"ds_read_b128 v[68:71], v2 offset:39168\nv_mfma_f32_16x16x32_bf16 v[16:19], a[108:111], v[32:35], v[16:19]\ns_add_u32 s60, s41, 98304\nbuffer_load_dwordx4 v0, s[36:39], s60 offen offset:0 sc1 lds\nv_mfma_f32_16x16x32_bf16 v[20:23], a[124:127], v[32:35], v[20:23]\ns_waitcnt lgkmcnt(4)\nds_read_b128 v[32:35], v2 offset:40960\nv_mfma_f32_16x16x32_bf16 v[24:27], a[108:111], v[36:39], v[24:27]\n" \
"v_mfma_f32_16x16x32_bf16 v[28:31], a[124:127], v[36:39], v[28:31]\ns_waitcnt lgkmcnt(4)\nds_read_b128 v[36:39], v2 offset:41216\nv_mfma_f32_16x16x32_bf16 v[16:19], a[128:131], v[40:43], v[16:19]\nbuffer_load_dwordx4 v0, s[36:39], s60 offen offset:1024 sc1 lds\nv_mfma_f32_16x16x32_bf16 v[20:23], v[56:59], v[40:43], v[20:23]\ns_waitcnt lgkmcnt(4)\nds_read_b128 v[40:43], v2 offset:43008\n" \
"v_mfma_f32_16x16x32_bf16 v[24:27], a[128:131], v[44:47], v[24:27]\nv_mfma_f32_16x16x32_bf16 v[28:31], v[56:59], v[44:47], v[28:31]\ns_waitcnt lgkmcnt(4)\nds_read_b128 v[44:47], v2 offset:43264\nv_mfma_f32_16x16x32_bf16 v[16:19], a[132:135], v[64:67], v[16:19]\nbuffer_load_dwordx4 v0, s[36:39], s60 offen offset:2048 sc1 lds\nv_mfma_f32_16x16x32_bf16 v[20:23], v[60:63], v[64:67], v[20:23]\ns_waitcnt lgkmcnt(4)\n" \
"ds_read_b128 v[64:67], v2 offset:45056\nv_mfma_f32_16x16x32_bf16 v[24:27], a[132:135], v[68:71], v[24:27]\nv_mfma_f32_16x16x32_bf16 v[28:31], v[60:63], v[68:71], v[28:31]\ns_waitcnt lgkmcnt(4)\nds_read_b128 v[68:71], v2 offset:45312\nv_mfma_f32_16x16x32_bf16 v[16:19], a[152:155], v[32:35], v[16:19]\nbuffer_load_dwordx4 v0, s[36:39], s60 offen offset:3072 sc1 lds\nv_mfma_f32_16x16x32_bf16 v[20:23], a[168:171], v[32:35], v[20:23]\n" \
"s_waitcnt lgkmcnt(4)\ns_add_u32 s44, s40, 2\nds_read_b128 v[32:35], v2 offset:47104\nv_mfma_f32_16x16x32_bf16 v[24:27], a[152:155], v[36:39], v[24:27]\ns_lshl_b32 s44, s44, 17\nds_read_b128 v[56:59], v1 offset:4096\nds_read_b128 v[60:63], v1 offset:5120\nv_mfma_f32_16x16x32_bf16 v[28:31], a[168:171], v[36:39], v[28:31]\n" \
"s_waitcnt lgkmcnt(6)\nds_read_b128 v[36:39], v2 offset:47360\nv_mfma_f32_16x16x32_bf16 v[16:19], a[156:159], v[40:43], v[16:19]\ns_add_u32 s44, s44, %[xn]\ns_lshr_b32 s45, s44, 8\nv_mfma_f32_16x16x32_bf16 v[20:23], a[172:175], v[40:43], v[20:23]\ns_waitcnt lgkmcnt(6)\nv_mfma_f32_16x16x32_bf16 v[24:27], a[156:159], v[44:47], v[24:27]\n" \
"s_and_b32 s45, s45, 0x2000\ns_xor_b32 s41, s44, s45\nv_mfma_f32_16x16x32_bf16 v[28:31], a[172:175], v[44:47], v[28:31]\ns_waitcnt lgkmcnt(0)\ns_waitcnt vmcnt(4)\ns_barrier\nds_read_b128 v[40:43], v2 offset:16384\nds_read_b128 v[44:47], v2 offset:16640\n" \
"v_mfma_f32_16x16x32_bf16 v[16:19], a[184:187], v[64:67], v[16:19]\nds_read_b32 v14, v14\nv_mfma_f32_16x16x32_bf16 v[20:23], a[192:195], v[64:67], v[20:23]\nds_read_b128 v[64:67], v2 offset:18432\nv_mfma_f32_16x16x32_bf16 v[24:27], a[184:187], v[68:71], v[24:27]\ns_add_u32 s41, s41, %[xw]\nv_mfma_f32_16x16x32_bf16 v[28:31], a[192:195], v[68:71], v[28:31]\nds_read_b128 v[68:71], v2 offset:18688\n" \
"v_mfma_f32_16x16x32_bf16 v[16:19], a[188:191], v[32:35], v[16:19]\nv_mfma_f32_16x16x32_bf16 v[20:23], a[196:199], v[32:35], v[20:23]\nds_read_b128 v[32:35], v2 offset:20480\nv_mfma_f32_16x16x32_bf16 v[24:27], a[188:191], v[36:39], v[24:27]\ns_waitcnt lgkmcnt(3)\nv_cmp_ge_i32 s[64:65], s62, v14\nv_mfma_f32_16x16x32_bf16 v[28:31], a[196:199], v[36:39], v[28:31]\ns_cmp_eq_u64 s[64:65], 0\n" \
"s_cselect_b32 s61, s41, -2147483648\nds_read_b128 v[36:39], v2 offset:20736\nv_mfma_f32_16x16x32_bf16 v[16:19], v[56:59], v[40:43], v[16:19]\ns_waitcnt vmcnt(4)\nbuffer_store_dword v15, off, s[36:39], s54 sc1\nv_mfma_f32_16x16x32_bf16 v[20:23], a[216:219], v[40:43], v[20:23]\ns_add_u32 m0, s57, 32768\nds_read_b128 v[40:43], v2 offset:22528\n" \
"v_mfma_f32_16x16x32_bf16 v[24:27], v[56:59], v[44:47], v[24:27]\ns_add_u32 s58, s61, 32768\nbuffer_load_dwordx4 v0, s[36:39], s58 offen offset:0 sc1 lds\nv_mfma_f32_16x16x32_bf16 v[28:31], a[216:219], v[44:47], v[28:31]\ns_waitcnt lgkmcnt(4)\nds_read_b128 v[44:47], v2 offset:22784\nv_mfma_f32_16x16x32_bf16 v[16:19], v[60:63], v[64:67], v[16:19]\nv_mfma_f32_16x16x32_bf16 v[20:23], a[220:223], v[64:67], v[20:23]\n" \
"s_waitcnt lgkmcnt(4)\nds_read_b128 v[64:67], v2 offset:24576\nv_mfma_f32_16x16x32_bf16 v[24:27], v[60:63], v[68:71], v[24:27]\nbuffer_load_dwordx4 v0, s[36:39], s58 offen offset:1024 sc1 lds\nv_mfma_f32_16x16x32_bf16 v[28:31], a[220:223], v[68:71], v[28:31]\ns_waitcnt lgkmcnt(4)\nds_read_b128 v[68:71], v2 offset:24832\nv_mfma_f32_16x16x32_bf16 v[16:19], a[224:227], v[32:35], v[16:19]\n" \
"ds_read_b128 v[56:59], v1 offset:6144\nds_read_b128 v[60:63], v1 offset:7168\nv_mfma_f32_16x16x32_bf16 v[20:23], a[240:243], v[32:35], v[20:23]\ns_waitcnt lgkmcnt(6)\nds_read_b128 v[32:35], v2 offset:26624\nv_mfma_f32_16x16x32_bf16 v[24:27], a[224:227], v[36:39], v[24:27]\nbuffer_load_dwordx4 v0, s[36:39], s58 offen offset:2048 sc1 lds\nv_mfma_f32_16x16x32_bf16 v[28:31], a[240:243], v[36:39], v[28:31]\n" \
"s_waitcnt lgkmcnt(6)\nds_read_b128 v[36:39], v2 offset:26880\nv_mfma_f32_16x16x32_bf16 v[16:19], a[228:231], v[40:43], v[16:19]\nv_mfma_f32_16x16x32_bf16 v[20:23], a[244:247], v[40:43], v[20:23]\ns_waitcnt lgkmcnt(6)\nds_read_b128 v[40:43], v2 offset:28672\nv_mfma_f32_16x16x32_bf16 v[24:27], a[228:231], v[44:47], v[24:27]\nbuffer_load_dwordx4 v0, s[36:39], s58 offen offset:3072 sc1 lds\n" \
"v_mfma_f32_16x16x32_bf16 v[28:31], a[244:247], v[44:47], v[28:31]\ns_waitcnt lgkmcnt(6)\nds_read_b128 v[44:47], v2 offset:28928\nv_mfma_f32_16x16x32_bf16 v[16:19], v[96:99], v[64:67], v[16:19]\nv_mfma_f32_16x16x32_bf16 v[20:23], v[112:115], v[64:67], v[20:23]\ns_waitcnt lgkmcnt(6)\nds_read_b128 v[64:67], v2 offset:30720\nv_mfma_f32_16x16x32_bf16 v[24:27], v[96:99], v[68:71], v[24:27]\n" \
"v_mfma_f32_16x16x32_bf16 v[28:31], v[112:115], v[68:71], v[28:31]\ns_waitcnt lgkmcnt(4)\nds_read_b128 v[68:71], v2 offset:30976\nv_mfma_f32_16x16x32_bf16 v[16:19], v[100:103], v[32:35], v[16:19]\nv_mfma_f32_16x16x32_bf16 v[20:23], v[116:119], v[32:35], v[20:23]\ns_waitcnt lgkmcnt(4)\nv_mfma_f32_16x16x32_bf16 v[24:27], v[100:103], v[36:39], v[24:27]\nv_mfma_f32_16x16x32_bf16 v[28:31], v[116:119], v[36:39], v[28:31]\n" \
"s_waitcnt lgkmcnt(0)\ns_waitcnt vmcnt(5)\ns_barrier\nds_read_b128 v[32:35], v2 offset:0\nds_read_b128 v[36:39], v2 offset:2048\nv_mfma_f32_16x16x32_bf16 v[16:19], v[128:131], v[40:43], v[16:19]\ns_add_u32 m0, s57, 16384\ns_nop 0\n" \
"buffer_load_dwordx4 v3, s[36:39], s42 offen offset:0 sc1 lds\nv_mfma_f32_16x16x32_bf16 v[20:23], v[56:59], v[40:43], v[20:23]\nds_read_b128 v[40:43], v2 offset:4096\nv_mfma_f32_16x16x32_bf16 v[24:27], v[128:131], v[44:47], v[24:27]\nv_mfma_f32_16x16x32_bf16 v[28:31], v[56:59], v[44:47], v[28:31]\nds_read_b128 v[44:47], v2 offset:6144\nv_mfma_f32_16x16x32_bf16 v[16:19], v[132:135], v[64:67], v[16:19]\nbuffer_load_dwordx4 v3, s[36:39], s42 offen offset:1024 sc1 lds\n" \
"v_mfma_f32_16x16x32_bf16 v[20:23], v[60:63], v[64:67], v[20:23]\nds_read_b128 v[64:67], v2 offset:8192\nv_mfma_f32_16x16x32_bf16 v[24:27], v[132:135], v[68:71], v[24:27]\nv_mfma_f32_16x16x32_bf16 v[28:31], v[60:63], v[68:71], v[28:31]\ns_waitcnt lgkmcnt(4)\nds_read_b128 v[68:71], v2 offset:10240\nv_mfma_f32_16x16x32_bf16 v[16:19], v[144:147], v[32:35], v[16:19]\nbuffer_load_dwordx4 v3, s[36:39], s42 offen offset:2048 sc1 lds\n" \
"v_mfma_f32_16x16x32_bf16 v[20:23], v[160:163], v[32:35], v[20:23]\ns_waitcnt lgkmcnt(4)\nds_read_b128 v[32:35], v2 offset:12288\nv_mfma_f32_16x16x32_bf16 v[16:19], v[148:151], v[36:39], v[16:19]\nv_mfma_f32_16x16x32_bf16 v[20:23], v[164:167], v[36:39], v[20:23]\ns_waitcnt lgkmcnt(4)\nds_read_b128 v[36:39], v2 offset:14336\nv_mfma_f32_16x16x32_bf16 v[16:19], v[168:171], v[40:43], v[16:19]\n" \
"buffer_load_dwordx4 v3, s[36:39], s42 offen offset:3072 sc1 lds\nv_add_u32 v14, s57, v0\nv_mfma_f32_16x16x32_bf16 v[20:23], v[184:187], v[40:43], v[20:23]\ns_waitcnt lgkmcnt(4)\nds_read_b128 v[40:43], v2 offset:256\nv_mfma_f32_16x16x32_bf16 v[16:19], v[172:175], v[44:47], v[16:19]\nv_mov_b64 v[48:49], 0\nv_mfma_f32_16x16x32_bf16 v[20:23], v[188:191], v[44:47], v[20:23]\n" \
"s_waitcnt lgkmcnt(4)\nds_read_b128 v[44:47], v2 offset:2304\nv_mov_b64 v[50:51], 0\nv_mfma_f32_16x16x32_bf16 v[16:19], v[200:203], v[64:67], v[16:19]\nv_mov_b64 v[52:53], 0\nv_mfma_f32_16x16x32_bf16 v[20:23], v[216:219], v[64:67], v[20:23]\ns_waitcnt lgkmcnt(4)\nds_read_b128 v[64:67], v2 offset:4352\n" \
"v_mov_b64 v[54:55], 0\nv_mfma_f32_16x16x32_bf16 v[16:19], v[204:207], v[68:71], v[16:19]\nds_read_b128 v[56:59], v1 offset:0\nds_read_b128 v[60:63], v1 offset:1024\nv_mfma_f32_16x16x32_bf16 v[20:23], v[220:223], v[68:71], v[20:23]\ns_waitcnt lgkmcnt(6)\nds_read_b128 v[68:71], v2 offset:6400\nv_mfma_f32_16x16x32_bf16 v[16:19], v[232:235], v[32:35], v[16:19]\n" \
"s_waitcnt vmcnt(0)\nds_read_b128 v[80:83], v14 offset:16384\nds_read_b128 v[84:87], v14 offset:17408\nv_mfma_f32_16x16x32_bf16 v[20:23], v[240:243], v[32:35], v[20:23]\ns_waitcnt lgkmcnt(8)\nds_read_b128 v[32:35], v2 offset:8448\nv_mfma_f32_16x16x32_bf16 v[16:19], v[236:239], v[36:39], v[16:19]\nds_read_b128 v[88:91], v14 offset:18432\n" \
"ds_read_b128 v[92:95], v14 offset:19456\nv_mfma_f32_16x16x32_bf16 v[20:23], v[244:247], v[36:39], v[20:23]\ns_waitcnt lgkmcnt(10)\nds_read_b128 v[36:39], v2 offset:10496\nv_mfma_f32_16x16x32_bf16 v[24:27], v[144:147], v[40:43], v[24:27]\nv_mfma_f32_16x16x32_bf16 v[28:31], v[160:163], v[40:43], v[28:31]\ns_waitcnt lgkmcnt(10)\nds_read_b128 v[40:43], v2 offset:12544\n" \
"v_cvt_pk_bf16_f32 v72, v16, v17\nv_mfma_f32_16x16x32_bf16 v[24:27], v[148:151], v[44:47], v[24:27]\nv_cvt_pk_bf16_f32 v73, v18, v19\nv_mfma_f32_16x16x32_bf16 v[28:31], v[164:167], v[44:47], v[28:31]\ns_waitcnt lgkmcnt(10)\nds_read_b128 v[44:47], v2 offset:14592\nv_cvt_pk_bf16_f32 v74, v20, v21\nv_mfma_f32_16x16x32_bf16 v[24:27], v[168:171], v[64:67], v[24:27]\n" \
"s_waitcnt lgkmcnt(6)\nv_max3_u32 v76, v80, v82, v84\nv_cvt_pk_bf16_f32 v75, v22, v23\nv_mfma_f32_16x16x32_bf16 v[28:31], v[184:187], v[64:67], v[28:31]\ns_waitcnt lgkmcnt(4)\nds_write2st64_b64 v9, v[72:73], v[74:75] offset0:0 offset1:8\nv_max3_u32 v76, v76, v86, v88\nv_mfma_f32_16x16x32_bf16 v[24:27], v[172:175], v[68:71], v[24:27]\n" \
"s_waitcnt lgkmcnt(4)\nv_max3_u32 v76, v76, v90, v92\nv_max_u32 v76, v76, v94\nv_mfma_f32_16x16x32_bf16 v[28:31], v[188:191], v[68:71], v[28:31]\nv_cmp_eq_u32 vcc, -1, v76\nv_mfma_f32_16x16x32_bf16 v[24:27], v[200:203], v[32:35], v[24:27]\ns_cbranch_vccnz .Lvu_re%=\n.Lvu_rer%=:\n" \
"s_barrier\ns_memrealtime s[68:69]\nds_read_b128 v[80:83], v0 offset:16384\nv_mfma_f32_16x16x32_bf16 v[28:31], v[216:219], v[32:35], v[28:31]\ns_waitcnt lgkmcnt(4)\nds_read_b128 v[84:87], v0 offset:17408\nv_mfma_f32_16x16x32_bf16 v[24:27], v[204:207], v[36:39], v[24:27]\nds_read_b128 v[88:91], v0 offset:18432\n" \
"v_mfma_f32_16x16x32_bf16 v[28:31], v[220:223], v[36:39], v[28:31]\ns_waitcnt lgkmcnt(5)\nds_read_b128 v[92:95], v0 offset:19456\nv_mfma_f32_16x16x32_bf16 v[24:27], v[232:235], v[40:43], v[24:27]\nds_read_b128 v[16:19], v0 offset:20480\nv_mfma_f32_16x16x32_bf16 v[28:31], v[240:243], v[40:43], v[28:31]\ns_waitcnt lgkmcnt(6)\nds_read_b128 v[20:23], v0 offset:21504\n" \
"v_mfma_f32_16x16x32_bf16 v[24:27], v[236:239], v[44:47], v[24:27]\nds_read_b128 v[32:35], v0 offset:22528\nv_mfma_f32_16x16x32_bf16 v[28:31], v[244:247], v[44:47], v[28:31]\ns_cmp_lt_i32 s40, 0\nds_read_b128 v[36:39], v0 offset:23552\ns_cbranch_scc1 .Lvu_first%=\n.Lvu_crit%=:\ns_waitcnt lgkmcnt(7)\n" \
"v_smfmac_f32_16x16x64_bf16 v[48:51], v[80:83], a[0:7], v4\ns_waitcnt lgkmcnt(6)\nds_read_b128 v[64:67], v1 offset:4096\nds_read_b128 v[40:43], v0 offset:24576\nv_mov_b32 v14, s40\nv_smfmac_f32_16x16x64_bf16 v[48:51], v[84:87], a[24:31], v4\ns_waitcnt lgkmcnt(7)\nds_read_b128 v[68:71], v1 offset:5120\n" \
"ds_read_b128 v[44:47], v0 offset:25600\nv_cvt_pk_bf16_f32 v76, v24, v25\nv_smfmac_f32_16x16x64_bf16 v[48:51], v[88:91], v[56:63], v4\ns_sub_u32 s88, s86, s89\ns_min_u32 s88, s88, 150\ns_waitcnt lgkmcnt(8)\nds_read_b128 v[80:83], v0 offset:28672\nv_cvt_pk_bf16_f32 v77, v26, v27\n" \
"v_smfmac_f32_16x16x64_bf16 v[48:51], v[92:95], a[80:87], v4\ns_waitcnt lgkmcnt(8)\nds_read_b128 v[84:87], v0 offset:29696\nv_cvt_pk_bf16_f32 v78, v28, v29\nv_smfmac_f32_16x16x64_bf16 v[48:51], v[16:19], a[104:111], v4\ns_add_u32 s88, s88, s68\ns_mov_b32 s89, s68\ns_waitcnt lgkmcnt(8)\n" \
"ds_read_b128 v[88:91], v0 offset:30720\nv_cvt_pk_bf16_f32 v79, v30, v31\nv_smfmac_f32_16x16x64_bf16 v[48:51], v[20:23], a[128:135], v4\ns_waitcnt lgkmcnt(8)\nds_read_b128 v[92:95], v0 offset:31744\nv_mov_b32 v15, s88\nv_smfmac_f32_16x16x64_bf16 v[48:51], v[32:35], a[152:159], v4\ns_waitcnt lgkmcnt(8)\n" \
"ds_read_b128 v[24:27], v0 offset:26624\nbuffer_store_dwordx2 v[14:15], off, s[36:39], %[sg] sc1\nv_smfmac_f32_16x16x64_bf16 v[48:51], v[36:39], a[184:191], v4\ns_waitcnt lgkmcnt(7)\ns_waitcnt lgkmcnt(6)\nds_read_b128 v[28:31], v0 offset:27648\nv_smfmac_f32_16x16x64_bf16 v[48:51], v[40:43], v[64:71], v4\ns_waitcnt lgkmcnt(6)\n" \
"ds_read_b128 v[56:59], v1 offset:2048\nv_smfmac_f32_16x16x64_bf16 v[48:51], v[44:47], a[224:231], v4\ns_waitcnt lgkmcnt(6)\nds_read_b128 v[60:63], v1 offset:3072\nv_smfmac_f32_16x16x64_bf16 v[48:51], v[80:83], v[144:151], v4\ns_waitcnt lgkmcnt(6)\nds_write2st64_b64 v9, v[76:77], v[78:79] offset0:2 offset1:10\nv_smfmac_f32_16x16x64_bf16 v[48:51], v[84:87], v[168:175], v4\n" \
"s_waitcnt lgkmcnt(6)\nv_smfmac_f32_16x16x64_bf16 v[48:51], v[88:91], v[200:207], v4\ns_waitcnt lgkmcnt(5)\nv_smfmac_f32_16x16x64_bf16 v[48:51], v[92:95], v[232:239], v4\ns_waitcnt lgkmcnt(4)\nv_smfmac_f32_16x16x64_bf16 v[48:51], v[24:27], v[96:103], v4\ns_waitcnt lgkmcnt(3)\nds_read_b128 v[64:67], v1 offset:6144\n" \
"v_smfmac_f32_16x16x64_bf16 v[48:51], v[28:31], v[128:135], v4\nds_read_b128 v[68:71], v1 offset:7168\nv_smfmac_f32_16x16x64_bf16 v[52:55], v[80:83], v[160:167], v4\nv_smfmac_f32_16x16x64_bf16 v[52:55], v[84:87], v[184:191], v4\nv_add_f32 v48, v48, v50\nv_smfmac_f32_16x16x64_bf16 v[52:55], v[88:91], v[216:223], v4\nds_read_b128 v[80:83], v0 offset:16384\nv_add_f32 v49, v49, v51\n" \
"v_smfmac_f32_16x16x64_bf16 v[52:55], v[92:95], v[240:247], v4\nds_read_b128 v[84:87], v0 offset:17408\nv_cvt_pk_bf16_f32 v14, v48, v49\nv_smfmac_f32_16x16x64_bf16 v[52:55], v[16:19], a[120:127], v4\ns_waitcnt lgkmcnt(5)\nds_write_b16 v5, v14 offset:0\nds_write_b16_d16_hi v5, v14 offset:16\nv_smfmac_f32_16x16x64_bf16 v[52:55], v[20:23], v[56:63], v4\n" \
"ds_read_b128 v[88:91], v0 offset:18432\nv_mov_b64 v[16:17], 0\nv_smfmac_f32_16x16x64_bf16 v[52:55], v[24:27], v[112:119], v4\ns_waitcnt lgkmcnt(5)\nds_read_b128 v[92:95], v0 offset:19456\nv_mov_b64 v[18:19], 0\nv_smfmac_f32_16x16x64_bf16 v[52:55], v[28:31], v[64:71], v4\nds_read_b128 v[56:59], v1 offset:8192\n" \
"v_mov_b64 v[20:21], 0\nv_smfmac_f32_16x16x64_bf16 v[52:55], v[32:35], a[168:175], v4\ns_waitcnt lgkmcnt(3)\nds_read_b128 v[60:63], v1 offset:9216\ns_barrier\nds_read_b128 v[72:75], v11 offset:57344\nv_smfmac_f32_16x16x64_bf16 v[52:55], v[36:39], a[192:199], v4\nds_read_b128 v[76:79], v11 offset:58368\n" \
"v_mov_b64 v[22:23], 0\nv_smfmac_f32_16x16x64_bf16 v[52:55], v[40:43], a[216:223], v4\nv_mov_b64 v[24:25], 0\nv_smfmac_f32_16x16x64_bf16 v[52:55], v[44:47], a[240:247], v4\nds_read_b128 v[64:67], v1 offset:12288\nv_mov_b64 v[26:27], 0\nv_smfmac_f32_16x16x64_bf16 v[52:55], v[80:83], a[16:23], v4\nds_read_b128 v[68:71], v1 offset:13312\n" \
"v_mov_b64 v[28:29], 0\nv_smfmac_f32_16x16x64_bf16 v[52:55], v[84:87], a[40:47], v4\ns_waitcnt lgkmcnt(7)\nv_mov_b64 v[30:31], 0\nv_smfmac_f32_16x16x64_bf16 v[52:55], v[88:91], a[64:71], v4\ns_waitcnt lgkmcnt(6)\nv_mov_b64 v[32:33], 0\nv_smfmac_f32_16x16x64_bf16 v[52:55], v[92:95], a[88:95], v4\n" \
"s_waitcnt lgkmcnt(3)\nv_mov_b64 v[34:35], 0\nv_smfmac_f32_16x16x64_bf16 v[16:19], v[72:75], a[8:15], v10\ns_waitcnt lgkmcnt(2)\nv_mov_b64 v[36:37], 0\nv_smfmac_f32_16x16x64_bf16 v[16:19], v[76:79], v[56:63], v10\nds_read_b128 v[56:59], v1 offset:10240\nv_add_f32 v52, v52, v54\n" \
"v_smfmac_f32_16x16x64_bf16 v[20:23], v[72:75], a[56:63], v10\nds_read_b128 v[60:63], v1 offset:11264\nv_add_f32 v53, v53, v55\nv_smfmac_f32_16x16x64_bf16 v[20:23], v[76:79], a[72:79], v10\nv_cvt_pk_bf16_f32 v14, v52, v53\nv_smfmac_f32_16x16x64_bf16 v[24:27], v[72:75], a[112:119], v10\ns_waitcnt lgkmcnt(2)\nds_write_b16 v5, v14 offset:2048\n" \
"ds_write_b16_d16_hi v5, v14 offset:2064\nv_smfmac_f32_16x16x64_bf16 v[24:27], v[76:79], v[64:71], v10\nds_read_b128 v[64:67], v1 offset:14336\nv_mov_b64 v[38:39], 0\nv_smfmac_f32_16x16x64_bf16 v[28:31], v[72:75], a[160:167], v10\nds_read_b128 v[68:71], v1 offset:15360\nv_mov_b64 v[40:41], 0\nv_smfmac_f32_16x16x64_bf16 v[28:31], v[76:79], a[176:183], v10\n" \
"v_mov_b64 v[42:43], 0\nv_smfmac_f32_16x16x64_bf16 v[32:35], v[72:75], a[208:215], v10\ns_waitcnt lgkmcnt(2)\ns_barrier\ns_mov_b32 s59, 0\nds_read_b128 v[80:83], v11 offset:59392\nv_smfmac_f32_16x16x64_bf16 v[32:35], v[76:79], v[56:63], v10\ns_add_u32 m0, s57, 0\n" \
"s_add_u32 s58, s61, 0\nds_read_b128 v[56:59], v1 offset:16384\nbuffer_load_dwordx4 v0, s[36:39], s58 offen offset:0 sc1 lds\nds_read_b128 v[84:87], v11 offset:60416\nv_smfmac_f32_16x16x64_bf16 v[36:39], v[72:75], v[104:111], v10\nds_read_b128 v[60:63], v1 offset:17408\nv_mov_b64 v[44:45], 0\nv_smfmac_f32_16x16x64_bf16 v[36:39], v[76:79], v[120:127], v10\n" \
"v_mov_b64 v[46:47], 0\nv_smfmac_f32_16x16x64_bf16 v[40:43], v[72:75], v[152:159], v10\ns_waitcnt lgkmcnt(4)\nbuffer_load_dwordx4 v0, s[36:39], s58 offen offset:1024 sc1 lds\nv_smfmac_f32_16x16x64_bf16 v[40:43], v[76:79], v[64:71], v10\nds_read_b128 v[64:67], v1 offset:20480\nv_mov_b64 v[48:49], 0\nv_smfmac_f32_16x16x64_bf16 v[44:47], v[72:75], v[208:215], v10\n" \
"ds_read_b128 v[68:71], v1 offset:21504\nv_mov_b64 v[50:51], 0\nv_smfmac_f32_16x16x64_bf16 v[44:47], v[76:79], v[224:231], v10\ns_waitcnt lgkmcnt(5)\nbuffer_load_dwordx4 v0, s[36:39], s58 offen offset:2048 sc1 lds\nv_smfmac_f32_16x16x64_bf16 v[16:19], v[80:83], a[32:39], v10\ns_waitcnt lgkmcnt(3)\nds_read_b128 v[72:75], v11 offset:49152\n" \
"v_mov_b64 v[52:53], 0\nv_smfmac_f32_16x16x64_bf16 v[16:19], v[84:87], a[48:55], v10\ns_waitcnt lgkmcnt(3)\nds_read_b128 v[76:79], v11 offset:50176\nv_mov_b64 v[54:55], 0\nv_smfmac_f32_16x16x64_bf16 v[20:23], v[80:83], v[56:63], v10\nds_read_b128 v[56:59], v1 offset:18432\nbuffer_load_dwordx4 v0, s[36:39], s58 offen offset:3072 sc1 lds\n" \
"v_smfmac_f32_16x16x64_bf16 v[20:23], v[84:87], a[96:103], v10\nds_read_b128 v[60:63], v1 offset:19456\nv_cvt_pk_bf16_f32 v88, v16, v17\nv_smfmac_f32_16x16x64_bf16 v[24:27], v[80:83], a[136:143], v10\nv_and_or_b32 v88, v88, -2, v12\nv_cvt_pk_bf16_f32 v89, v18, v19\nv_smfmac_f32_16x16x64_bf16 v[24:27], v[84:87], a[144:151], v10\ns_waitcnt lgkmcnt(4)\n" \
"v_cvt_pk_bf16_f32 v90, v20, v21\nv_cvt_pk_bf16_f32 v91, v22, v23\nv_and_or_b32 v90, v90, -2, v12\nv_smfmac_f32_16x16x64_bf16 v[28:31], v[80:83], v[64:71], v10\nds_read_b128 v[64:67], v1 offset:22528\nv_permlane16_swap_b32 v89, v91\nv_smfmac_f32_16x16x64_bf16 v[28:31], v[84:87], a[200:207], v10\nds_read_b128 v[68:71], v1 offset:23552\n" \
"v_permlane16_swap_b32 v88, v90\nv_smfmac_f32_16x16x64_bf16 v[32:35], v[80:83], a[232:239], v10\ns_add_u32 s63, s59, 0\nds_read_b128 v[16:19], v11 offset:51200\nv_cvt_pk_bf16_f32 v92, v24, v25\nv_and_or_b32 v92, v92, -2, v12\nv_smfmac_f32_16x16x64_bf16 v[32:35], v[84:87], a[248:255], v10\ns_waitcnt lgkmcnt(3)\n" \
"ds_read_b128 v[20:23], v11 offset:52224\nbuffer_store_dwordx4 v[88:91], v6, s[36:39], s63 offen sc1\nv_cvt_pk_bf16_f32 v93, v26, v27\nv_cvt_pk_bf16_f32 v94, v28, v29\nv_cvt_pk_bf16_f32 v95, v30, v31\nv_and_or_b32 v94, v94, -2, v12\nv_smfmac_f32_16x16x64_bf16 v[36:39], v[80:83], v[56:63], v10\nds_read_b128 v[56:59], v1 offset:8192\n" \
"v_permlane16_swap_b32 v93, v95\nv_smfmac_f32_16x16x64_bf16 v[36:39], v[84:87], v[136:143], v10\nds_read_b128 v[60:63], v1 offset:9216\nv_permlane16_swap_b32 v92, v94\nv_smfmac_f32_16x16x64_bf16 v[40:43], v[80:83], v[176:183], v10\ns_add_u32 s63, s59, 131072\nds_read_b128 v[24:27], v11 offset:53248\nv_cvt_pk_bf16_f32 v88, v32, v33\n" \
"v_and_or_b32 v88, v88, -2, v12\nv_smfmac_f32_16x16x64_bf16 v[40:43], v[84:87], v[192:199], v10\ns_waitcnt lgkmcnt(5)\nds_read_b128 v[28:31], v11 offset:54272\nbuffer_store_dwordx4 v[92:95], v6, s[36:39], s63 offen sc1\nv_cvt_pk_bf16_f32 v89, v34, v35\nv_cvt_pk_bf16_f32 v90, v36, v37\nv_cvt_pk_bf16_f32 v91, v38, v39\n" \
"v_and_or_b32 v90, v90, -2, v12\nv_smfmac_f32_16x16x64_bf16 v[44:47], v[80:83], v[64:71], v10\nds_read_b128 v[64:67], v1 offset:16384\nv_permlane16_swap_b32 v89, v91\nv_smfmac_f32_16x16x64_bf16 v[44:47], v[84:87], v[248:255], v10\ns_cmp_ge_i32 s40, 63\nds_read_b128 v[68:71], v1 offset:17408\nv_permlane16_swap_b32 v88, v90\n" \
"s_cbranch_scc1 .Lvu_last%=\n.Lvu_y%=:\nv_smfmac_f32_16x16x64_bf16 v[48:51], v[72:75], a[8:15], v10\ns_add_u32 s63, s59, 4096\nds_read_b128 v[32:35], v11 offset:55296\nds_read_b128 v[36:39], v11 offset:56320\nv_cvt_pk_bf16_f32 v92, v40, v41\nv_and_or_b32 v92, v92, -2, v12\n" \
"v_smfmac_f32_16x16x64_bf16 v[52:55], v[76:79], a[8:15], v10\ns_waitcnt lgkmcnt(9)\ns_waitcnt lgkmcnt(6)\nbuffer_store_dwordx4 v[88:91], v6, s[36:39], s63 offen sc1\nv_cvt_pk_bf16_f32 v93, v42, v43\nv_cvt_pk_bf16_f32 v94, v44, v45\nv_cvt_pk_bf16_f32 v95, v46, v47\nv_and_or_b32 v94, v94, -2, v12\n" \
"v_smfmac_f32_16x16x64_bf16 v[48:51], v[16:19], v[56:63], v10\ns_add_u32 s40, s40, 1\ns_and_b32 s44, s40, 1\nv_permlane16_swap_b32 v93, v95\nv_smfmac_f32_16x16x64_bf16 v[52:55], v[20:23], v[56:63], v10\ns_lshl_b32 s55, s44, 22\ns_waitcnt lgkmcnt(5)\nv_permlane16_swap_b32 v92, v94\n" \
"v_smfmac_f32_16x16x64_bf16 v[48:51], v[24:27], a[32:39], v10\ns_waitcnt lgkmcnt(4)\ns_add_u32 s63, s59, 135168\nds_read_b128 v[56:59], v1 offset:12288\nv_smfmac_f32_16x16x64_bf16 v[52:55], v[28:31], a[32:39], v10\ns_waitcnt lgkmcnt(2)\nds_read_b128 v[60:63], v1 offset:13312\nbuffer_store_dwordx4 v[92:95], v6, s[36:39], s63 offen sc1\n" \
"v_mov_b64 v[80:81], 0\nv_mov_b64 v[82:83], 0\nv_mov_b64 v[84:85], 0\nv_mov_b64 v[86:87], 0\nv_smfmac_f32_16x16x64_bf16 v[48:51], v[32:35], a[48:55], v10\ns_memrealtime s[86:87]\ns_bfe_u32 s45, s40, 0x10001\ns_waitcnt lgkmcnt(2)\n" \
"v_smfmac_f32_16x16x64_bf16 v[52:55], v[36:39], a[48:55], v10\nv_mov_b32 v13, s45\nv_smfmac_f32_16x16x64_bf16 v[80:83], v[72:75], a[56:63], v10\nv_cvt_pk_bf16_f32 v88, v48, v49\nv_smfmac_f32_16x16x64_bf16 v[84:87], v[76:79], a[56:63], v10\nv_cvt_pk_bf16_f32 v89, v50, v51\nv_smfmac_f32_16x16x64_bf16 v[80:83], v[16:19], a[72:79], v10\nv_cvt_pk_bf16_f32 v90, v52, v53\n" \
"v_smfmac_f32_16x16x64_bf16 v[84:87], v[20:23], a[72:79], v10\ns_add_u32 s60, s55, 0\nv_cvt_pk_bf16_f32 v91, v54, v55\nv_smfmac_f32_16x16x64_bf16 v[80:83], v[24:27], v[64:71], v10\nv_and_or_b32 v88, v88, -2, v13\nv_smfmac_f32_16x16x64_bf16 v[84:87], v[28:31], v[64:71], v10\ns_and_b32 s42, s40, 3\ns_lshl_b32 s42, s42, 15\n" \
"v_and_or_b32 v90, v90, -2, v13\nv_smfmac_f32_16x16x64_bf16 v[80:83], v[32:35], a[96:103], v10\nbuffer_store_dwordx4 v[88:91], v7, s[36:39], s60 offen offset:0 sc1\nv_mov_b64 v[48:49], 0\nv_mov_b64 v[50:51], 0\nv_mov_b64 v[52:53], 0\nv_mov_b32 v12, s44\nv_smfmac_f32_16x16x64_bf16 v[84:87], v[36:39], a[96:103], v10\n" \
"ds_read_b128 v[64:67], v1 offset:20480\nv_mov_b64 v[54:55], 0\nv_smfmac_f32_16x16x64_bf16 v[48:51], v[72:75], a[112:119], v10\nds_read_b128 v[68:71], v1 offset:21504\nv_cvt_pk_bf16_f32 v92, v80, v81\nv_smfmac_f32_16x16x64_bf16 v[52:55], v[76:79], a[112:119], v10\ns_waitcnt lgkmcnt(2)\nv_cvt_pk_bf16_f32 v93, v82, v83\n" \
"v_smfmac_f32_16x16x64_bf16 v[48:51], v[16:19], v[56:63], v10\nv_cvt_pk_bf16_f32 v94, v84, v85\nv_smfmac_f32_16x16x64_bf16 v[52:55], v[20:23], v[56:63], v10\ns_add_u32 s60, s55, 4096\nv_cvt_pk_bf16_f32 v95, v86, v87\nv_smfmac_f32_16x16x64_bf16 v[48:51], v[24:27], a[136:143], v10\nds_read_b128 v[56:59], v1 offset:10240\nv_and_or_b32 v92, v92, -2, v13\n" \
"v_smfmac_f32_16x16x64_bf16 v[52:55], v[28:31], a[136:143], v10\nds_read_b128 v[60:63], v1 offset:11264\nv_and_or_b32 v94, v94, -2, v13\nv_smfmac_f32_16x16x64_bf16 v[48:51], v[32:35], a[144:151], v10\ns_waitcnt vmcnt(5)\nbuffer_store_dwordx4 v[92:95], v7, s[36:39], s60 offen offset:0 sc1\nv_mov_b64 v[80:81], 0\nv_mov_b64 v[82:83], 0\n" \
"v_mov_b64 v[84:85], 0\ns_barrier\nv_smfmac_f32_16x16x64_bf16 v[52:55], v[36:39], a[144:151], v10\nv_mov_b64 v[86:87], 0\nv_smfmac_f32_16x16x64_bf16 v[80:83], v[72:75], a[160:167], v10\nv_cvt_pk_bf16_f32 v88, v48, v49\nv_smfmac_f32_16x16x64_bf16 v[84:87], v[76:79], a[160:167], v10\nv_cvt_pk_bf16_f32 v89, v50, v51\n" \
"v_smfmac_f32_16x16x64_bf16 v[80:83], v[16:19], a[176:183], v10\ns_cmp_lt_u32 s40, 2\ns_cselect_b32 s54, %[fy], -2147483648\nv_cvt_pk_bf16_f32 v90, v52, v53\nv_smfmac_f32_16x16x64_bf16 v[84:87], v[20:23], a[176:183], v10\ns_add_u32 s60, s55, 16384\ns_waitcnt lgkmcnt(2)\nv_cvt_pk_bf16_f32 v91, v54, v55\n" \
"v_smfmac_f32_16x16x64_bf16 v[80:83], v[24:27], v[64:71], v10\nv_and_or_b32 v88, v88, -2, v13\nv_smfmac_f32_16x16x64_bf16 v[84:87], v[28:31], v[64:71], v10\nv_and_or_b32 v90, v90, -2, v13\nv_smfmac_f32_16x16x64_bf16 v[80:83], v[32:35], a[200:207], v10\nbuffer_store_dwordx4 v[88:91], v7, s[36:39], s60 offen offset:0 sc1\nv_mov_b64 v[48:49], 0\nv_mov_b64 v[50:51], 0\n" \
"v_mov_b64 v[52:53], 0\nv_smfmac_f32_16x16x64_bf16 v[84:87], v[36:39], a[200:207], v10\nds_read_b128 v[64:67], v1 offset:18432\nv_mov_b64 v[54:55], 0\nv_smfmac_f32_16x16x64_bf16 v[48:51], v[72:75], a[208:215], v10\nds_read_b128 v[68:71], v1 offset:19456\nv_cvt_pk_bf16_f32 v92, v80, v81\nv_smfmac_f32_16x16x64_bf16 v[52:55], v[76:79], a[208:215], v10\n" \
"s_waitcnt lgkmcnt(2)\nv_cvt_pk_bf16_f32 v93, v82, v83\nv_smfmac_f32_16x16x64_bf16 v[48:51], v[16:19], v[56:63], v10\nv_cvt_pk_bf16_f32 v94, v84, v85\nv_smfmac_f32_16x16x64_bf16 v[52:55], v[20:23], v[56:63], v10\ns_add_u32 s60, s55, 20480\nv_cvt_pk_bf16_f32 v95, v86, v87\nv_smfmac_f32_16x16x64_bf16 v[48:51], v[24:27], a[232:239], v10\n" \
"ds_read_b128 v[56:59], v1 offset:14336\nv_and_or_b32 v92, v92, -2, v13\nv_smfmac_f32_16x16x64_bf16 v[52:55], v[28:31], a[232:239], v10\nds_read_b128 v[60:63], v1 offset:15360\nv_and_or_b32 v94, v94, -2, v13\nv_smfmac_f32_16x16x64_bf16 v[48:51], v[32:35], a[248:255], v10\nbuffer_store_dwordx4 v[92:95], v7, s[36:39], s60 offen offset:0 sc1\nv_mov_b64 v[80:81], 0\n" \
"v_mov_b64 v[82:83], 0\nv_mov_b64 v[84:85], 0\nv_smfmac_f32_16x16x64_bf16 v[52:55], v[36:39], a[248:255], v10\nv_mov_b64 v[86:87], 0\nv_smfmac_f32_16x16x64_bf16 v[80:83], v[72:75], v[104:111], v10\nv_cvt_pk_bf16_f32 v88, v48, v49\nv_smfmac_f32_16x16x64_bf16 v[84:87], v[76:79], v[104:111], v10\nv_cvt_pk_bf16_f32 v89, v50, v51\n" \
"v_smfmac_f32_16x16x64_bf16 v[80:83], v[16:19], v[120:127], v10\nv_cvt_pk_bf16_f32 v90, v52, v53\nv_smfmac_f32_16x16x64_bf16 v[84:87], v[20:23], v[120:127], v10\ns_add_u32 s60, s55, 0\ns_waitcnt lgkmcnt(2)\nv_cvt_pk_bf16_f32 v91, v54, v55\nv_smfmac_f32_16x16x64_bf16 v[80:83], v[24:27], v[64:71], v10\nv_and_or_b32 v88, v88, -2, v13\n" \
"v_smfmac_f32_16x16x64_bf16 v[84:87], v[28:31], v[64:71], v10\nv_and_or_b32 v90, v90, -2, v13\nv_smfmac_f32_16x16x64_bf16 v[80:83], v[32:35], v[136:143], v10\nbuffer_store_dwordx4 v[88:91], v7, s[36:39], s60 offen offset:2048 sc1\nv_xor_b32 v11, 128, v11\nv_mov_b64 v[48:49], 0\nv_mov_b64 v[50:51], 0\nv_mov_b64 v[52:53], 0\n" \
"v_smfmac_f32_16x16x64_bf16 v[84:87], v[36:39], v[136:143], v10\nds_read_b128 v[64:67], v1 offset:22528\nv_mov_b64 v[54:55], 0\nv_smfmac_f32_16x16x64_bf16 v[48:51], v[72:75], v[152:159], v10\nds_read_b128 v[68:71], v1 offset:23552\nv_cvt_pk_bf16_f32 v92, v80, v81\nv_smfmac_f32_16x16x64_bf16 v[52:55], v[76:79], v[152:159], v10\ns_waitcnt lgkmcnt(2)\n" \
"ds_read_b128 v[40:43], v11 offset:49152\nv_cvt_pk_bf16_f32 v93, v82, v83\nv_smfmac_f32_16x16x64_bf16 v[48:51], v[16:19], v[56:63], v10\nds_read_b128 v[44:47], v11 offset:50176\nv_cvt_pk_bf16_f32 v94, v84, v85\nv_smfmac_f32_16x16x64_bf16 v[52:55], v[20:23], v[56:63], v10\ns_add_u32 s60, s55, 4096\nv_cvt_pk_bf16_f32 v95, v86, v87\n" \
"v_smfmac_f32_16x16x64_bf16 v[48:51], v[24:27], v[176:183], v10\nds_read_b128 v[56:59], v1 offset:8192\nv_and_or_b32 v92, v92, -2, v13\nv_smfmac_f32_16x16x64_bf16 v[52:55], v[28:31], v[176:183], v10\nds_read_b128 v[60:63], v1 offset:9216\nv_and_or_b32 v94, v94, -2, v13\nv_smfmac_f32_16x16x64_bf16 v[48:51], v[32:35], v[192:199], v10\nbuffer_store_dwordx4 v[92:95], v7, s[36:39], s60 offen offset:2048 sc1\n" \
"v_mov_b64 v[80:81], 0\nv_mov_b64 v[82:83], 0\nv_mov_b64 v[84:85], 0\nv_smfmac_f32_16x16x64_bf16 v[52:55], v[36:39], v[192:199], v10\nv_mov_b64 v[86:87], 0\nv_smfmac_f32_16x16x64_bf16 v[80:83], v[72:75], v[208:215], v10\nds_read_b128 v[72:75], v11 offset:51200\nv_cvt_pk_bf16_f32 v88, v48, v49\n" \
"v_smfmac_f32_16x16x64_bf16 v[84:87], v[76:79], v[208:215], v10\nds_read_b128 v[76:79], v11 offset:52224\nv_cvt_pk_bf16_f32 v89, v50, v51\nv_smfmac_f32_16x16x64_bf16 v[80:83], v[16:19], v[224:231], v10\ns_add_u32 s60, s55, 16384\nds_read_b128 v[16:19], v11 offset:53248\nv_cvt_pk_bf16_f32 v90, v52, v53\nv_smfmac_f32_16x16x64_bf16 v[84:87], v[20:23], v[224:231], v10\n" \
"s_waitcnt lgkmcnt(7)\nds_read_b128 v[20:23], v11 offset:54272\nv_cvt_pk_bf16_f32 v91, v54, v55\nv_smfmac_f32_16x16x64_bf16 v[80:83], v[24:27], v[64:71], v10\nds_read_b128 v[24:27], v11 offset:55296\nv_and_or_b32 v88, v88, -2, v13\nv_smfmac_f32_16x16x64_bf16 v[84:87], v[28:31], v[64:71], v10\nds_read_b128 v[28:31], v11 offset:56320\n" \
"v_and_or_b32 v90, v90, -2, v13\nv_smfmac_f32_16x16x64_bf16 v[80:83], v[32:35], v[248:255], v10\nbuffer_store_dwordx4 v[88:91], v7, s[36:39], s60 offen offset:2048 sc1\nv_mov_b64 v[48:49], 0\nv_mov_b64 v[50:51], 0\nv_mov_b64 v[52:53], 0\nv_smfmac_f32_16x16x64_bf16 v[84:87], v[36:39], v[248:255], v10\ns_waitcnt lgkmcnt(9)\n" \
"ds_read_b128 v[64:67], v1 offset:16384\nv_mov_b64 v[54:55], 0\nv_smfmac_f32_16x16x64_bf16 v[48:51], v[40:43], a[8:15], v10\ns_waitcnt lgkmcnt(9)\nds_read_b128 v[68:71], v1 offset:17408\nv_cvt_pk_bf16_f32 v92, v80, v81\nv_smfmac_f32_16x16x64_bf16 v[52:55], v[44:47], a[8:15], v10\ns_waitcnt lgkmcnt(7)\n" \
"v_cvt_pk_bf16_f32 v93, v82, v83\nv_smfmac_f32_16x16x64_bf16 v[48:51], v[72:75], v[56:63], v10\ns_waitcnt lgkmcnt(6)\nv_cvt_pk_bf16_f32 v94, v84, v85\nv_smfmac_f32_16x16x64_bf16 v[52:55], v[76:79], v[56:63], v10\ns_add_u32 s60, s55, 20480\ns_waitcnt lgkmcnt(5)\nv_cvt_pk_bf16_f32 v95, v86, v87\n" \
"v_smfmac_f32_16x16x64_bf16 v[48:51], v[16:19], a[32:39], v10\ns_waitcnt lgkmcnt(4)\nds_read_b128 v[56:59], v1 offset:12288\nv_and_or_b32 v92, v92, -2, v13\nv_smfmac_f32_16x16x64_bf16 v[52:55], v[20:23], a[32:39], v10\ns_waitcnt lgkmcnt(4)\nds_read_b128 v[60:63], v1 offset:13312\nv_and_or_b32 v94, v94, -2, v13\n" \
"v_smfmac_f32_16x16x64_bf16 v[48:51], v[24:27], a[48:55], v10\ns_waitcnt lgkmcnt(4)\nbuffer_store_dwordx4 v[92:95], v7, s[36:39], s60 offen offset:2048 sc1\nv_xor_b32 v11, 128, v11\nv_mov_b64 v[80:81], 0\nv_mov_b64 v[82:83], 0\nv_mov_b64 v[84:85], 0\nv_smfmac_f32_16x16x64_bf16 v[52:55], v[28:31], a[48:55], v10\n" \
"v_mov_b64 v[86:87], 0\nv_smfmac_f32_16x16x64_bf16 v[80:83], v[40:43], a[56:63], v10\nv_cvt_pk_bf16_f32 v88, v48, v49\nv_smfmac_f32_16x16x64_bf16 v[84:87], v[44:47], a[56:63], v10\nv_cvt_pk_bf16_f32 v89, v50, v51\nv_smfmac_f32_16x16x64_bf16 v[80:83], v[72:75], a[72:79], v10\nv_cvt_pk_bf16_f32 v90, v52, v53\nv_smfmac_f32_16x16x64_bf16 v[84:87], v[76:79], a[72:79], v10\n" \
"s_add_u32 s60, s55, 0\ns_waitcnt lgkmcnt(2)\nv_cvt_pk_bf16_f32 v91, v54, v55\nv_smfmac_f32_16x16x64_bf16 v[80:83], v[16:19], v[64:71], v10\nv_and_or_b32 v88, v88, -2, v13\nv_smfmac_f32_16x16x64_bf16 v[84:87], v[20:23], v[64:71], v10\nv_and_or_b32 v90, v90, -2, v13\nv_smfmac_f32_16x16x64_bf16 v[80:83], v[24:27], a[96:103], v10\n" \
"buffer_store_dwordx4 v[88:91], v7, s[36:39], s60 offen offset:512 sc1\nv_mov_b64 v[48:49], 0\nv_mov_b64 v[50:51], 0\nv_mov_b64 v[52:53], 0\nv_smfmac_f32_16x16x64_bf16 v[84:87], v[28:31], a[96:103], v10\nds_read_b128 v[64:67], v1 offset:20480\nv_mov_b64 v[54:55], 0\nv_smfmac_f32_16x16x64_bf16 v[48:51], v[40:43], a[112:119], v10\n" \
"ds_read_b128 v[68:71], v1 offset:21504\nv_cvt_pk_bf16_f32 v92, v80, v81\nv_smfmac_f32_16x16x64_bf16 v[52:55], v[44:47], a[112:119], v10\ns_waitcnt lgkmcnt(2)\nv_cvt_pk_bf16_f32 v93, v82, v83\nv_smfmac_f32_16x16x64_bf16 v[48:51], v[72:75], v[56:63], v10\nv_cvt_pk_bf16_f32 v94, v84, v85\nv_smfmac_f32_16x16x64_bf16 v[52:55], v[76:79], v[56:63], v10\n" \
"s_add_u32 s60, s55, 4096\nv_cvt_pk_bf16_f32 v95, v86, v87\nv_smfmac_f32_16x16x64_bf16 v[48:51], v[16:19], a[136:143], v10\nds_read_b128 v[56:59], v1 offset:10240\nv_and_or_b32 v92, v92, -2, v13\nv_smfmac_f32_16x16x64_bf16 v[52:55], v[20:23], a[136:143], v10\nds_read_b128 v[60:63], v1 offset:11264\nv_and_or_b32 v94, v94, -2, v13\n" \
"v_smfmac_f32_16x16x64_bf16 v[48:51], v[24:27], a[144:151], v10\nbuffer_store_dwordx4 v[92:95], v7, s[36:39], s60 offen offset:512 sc1\nv_mov_b64 v[80:81], 0\nv_mov_b64 v[82:83], 0\nv_mov_b64 v[84:85], 0\nv_smfmac_f32_16x16x64_bf16 v[52:55], v[28:31], a[144:151], v10\nv_mov_b64 v[86:87], 0\nv_smfmac_f32_16x16x64_bf16 v[80:83], v[40:43], a[160:167], v10\n" \
"v_cvt_pk_bf16_f32 v88, v48, v49\nv_smfmac_f32_16x16x64_bf16 v[84:87], v[44:47], a[160:167], v10\nv_cvt_pk_bf16_f32 v89, v50, v51\nv_smfmac_f32_16x16x64_bf16 v[80:83], v[72:75], a[176:183], v10\nv_cvt_pk_bf16_f32 v90, v52, v53\nv_smfmac_f32_16x16x64_bf16 v[84:87], v[76:79], a[176:183], v10\ns_add_u32 s60, s55, 16384\ns_waitcnt lgkmcnt(2)\n" \
"v_cvt_pk_bf16_f32 v91, v54, v55\nv_smfmac_f32_16x16x64_bf16 v[80:83], v[16:19], v[64:71], v10\nv_and_or_b32 v88, v88, -2, v13\nv_smfmac_f32_16x16x64_bf16 v[84:87], v[20:23], v[64:71], v10\nv_and_or_b32 v90, v90, -2, v13\nv_smfmac_f32_16x16x64_bf16 v[80:83], v[24:27], a[200:207], v10\nbuffer_store_dwordx4 v[88:91], v7, s[36:39], s60 offen offset:512 sc1\nv_mov_b64 v[48:49], 0\n" \
"v_mov_b64 v[50:51], 0\nv_mov_b64 v[52:53], 0\nv_smfmac_f32_16x16x64_bf16 v[84:87], v[28:31], a[200:207], v10\nds_read_b128 v[64:67], v1 offset:18432\nv_mov_b64 v[54:55], 0\nv_smfmac_f32_16x16x64_bf16 v[48:51], v[40:43], a[208:215], v10\nds_read_b128 v[68:71], v1 offset:19456\nv_cvt_pk_bf16_f32 v92, v80, v81\n" \
"v_smfmac_f32_16x16x64_bf16 v[52:55], v[44:47], a[208:215], v10\ns_waitcnt lgkmcnt(2)\nv_cvt_pk_bf16_f32 v93, v82, v83\nv_smfmac_f32_16x16x64_bf16 v[48:51], v[72:75], v[56:63], v10\nv_cvt_pk_bf16_f32 v94, v84, v85\nv_smfmac_f32_16x16x64_bf16 v[52:55], v[76:79], v[56:63], v10\ns_add_u32 s60, s55, 20480\nv_cvt_pk_bf16_f32 v95, v86, v87\n" \
"v_smfmac_f32_16x16x64_bf16 v[48:51], v[16:19], a[232:239], v10\nds_read_b128 v[56:59], v1 offset:14336\nv_and_or_b32 v92, v92, -2, v13\nv_smfmac_f32_16x16x64_bf16 v[52:55], v[20:23], a[232:239], v10\nds_read_b128 v[60:63], v1 offset:15360\nv_and_or_b32 v94, v94, -2, v13\nv_smfmac_f32_16x16x64_bf16 v[48:51], v[24:27], a[248:255], v10\nbuffer_store_dwordx4 v[92:95], v7, s[36:39], s60 offen offset:512 sc1\n" \
"v_mov_b64 v[80:81], 0\nv_mov_b64 v[82:83], 0\nv_mov_b64 v[84:85], 0\nv_smfmac_f32_16x16x64_bf16 v[52:55], v[28:31], a[248:255], v10\nv_mov_b64 v[86:87], 0\nv_smfmac_f32_16x16x64_bf16 v[80:83], v[40:43], v[104:111], v10\nv_cvt_pk_bf16_f32 v88, v48, v49\nv_smfmac_f32_16x16x64_bf16 v[84:87], v[44:47], v[104:111], v10\n" \
"v_cvt_pk_bf16_f32 v89, v50, v51\nv_smfmac_f32_16x16x64_bf16 v[80:83], v[72:75], v[120:127], v10\nv_cvt_pk_bf16_f32 v90, v52, v53\nv_smfmac_f32_16x16x64_bf16 v[84:87], v[76:79], v[120:127], v10\ns_add_u32 s60, s55, 0\ns_waitcnt lgkmcnt(2)\nv_cvt_pk_bf16_f32 v91, v54, v55\nv_smfmac_f32_16x16x64_bf16 v[80:83], v[16:19], v[64:71], v10\n" \
"v_and_or_b32 v88, v88, -2, v13\nv_smfmac_f32_16x16x64_bf16 v[84:87], v[20:23], v[64:71], v10\nv_and_or_b32 v90, v90, -2, v13\nv_smfmac_f32_16x16x64_bf16 v[80:83], v[24:27], v[136:143], v10\nbuffer_store_dwordx4 v[88:91], v7, s[36:39], s60 offen offset:2560 sc1\nv_mov_b64 v[48:49], 0\nv_mov_b64 v[50:51], 0\nv_mov_b64 v[52:53], 0\n" \
"v_smfmac_f32_16x16x64_bf16 v[84:87], v[28:31], v[136:143], v10\nds_read_b128 v[64:67], v1 offset:22528\nv_mov_b64 v[54:55], 0\nv_smfmac_f32_16x16x64_bf16 v[48:51], v[40:43], v[152:159], v10\nds_read_b128 v[68:71], v1 offset:23552\nv_cvt_pk_bf16_f32 v92, v80, v81\nv_smfmac_f32_16x16x64_bf16 v[52:55], v[44:47], v[152:159], v10\ns_waitcnt lgkmcnt(2)\n" \
"v_cvt_pk_bf16_f32 v93, v82, v83\nv_smfmac_f32_16x16x64_bf16 v[48:51], v[72:75], v[56:63], v10\nv_cvt_pk_bf16_f32 v94, v84, v85\nv_smfmac_f32_16x16x64_bf16 v[52:55], v[76:79], v[56:63], v10\ns_add_u32 s60, s55, 4096\nv_cvt_pk_bf16_f32 v95, v86, v87\nv_smfmac_f32_16x16x64_bf16 v[48:51], v[16:19], v[176:183], v10\nv_and_or_b32 v92, v92, -2, v13\n" \
"v_smfmac_f32_16x16x64_bf16 v[52:55], v[20:23], v[176:183], v10\nv_and_or_b32 v94, v94, -2, v13\nv_smfmac_f32_16x16x64_bf16 v[48:51], v[24:27], v[192:199], v10\nbuffer_store_dwordx4 v[92:95], v7, s[36:39], s60 offen offset:2560 sc1\nv_mov_b64 v[80:81], 0\nv_mov_b64 v[82:83], 0\nv_mov_b64 v[84:85], 0\nv_smfmac_f32_16x16x64_bf16 v[52:55], v[28:31], v[192:199], v10\n" \
"ds_read_b128 v[32:35], v2 offset:0\nv_mov_b64 v[86:87], 0\nv_smfmac_f32_16x16x64_bf16 v[80:83], v[40:43], v[208:215], v10\nds_read_b128 v[36:39], v2 offset:256\nv_cvt_pk_bf16_f32 v88, v48, v49\nv_smfmac_f32_16x16x64_bf16 v[84:87], v[44:47], v[208:215], v10\nv_cvt_pk_bf16_f32 v89, v50, v51\nv_smfmac_f32_16x16x64_bf16 v[80:83], v[72:75], v[224:231], v10\n" \
"s_add_u32 s60, s55, 16384\nds_read_b128 v[40:43], v2 offset:2048\nv_cvt_pk_bf16_f32 v90, v52, v53\nv_smfmac_f32_16x16x64_bf16 v[84:87], v[76:79], v[224:231], v10\ns_waitcnt lgkmcnt(3)\nds_read_b128 v[44:47], v2 offset:2304\nv_cvt_pk_bf16_f32 v91, v54, v55\nv_smfmac_f32_16x16x64_bf16 v[80:83], v[16:19], v[64:71], v10\n" \
"v_and_or_b32 v88, v88, -2, v13\nv_smfmac_f32_16x16x64_bf16 v[84:87], v[20:23], v[64:71], v10\nv_and_or_b32 v90, v90, -2, v13\nv_smfmac_f32_16x16x64_bf16 v[80:83], v[24:27], v[248:255], v10\nbuffer_store_dwordx4 v[88:91], v7, s[36:39], s60 offen offset:2560 sc1\nv_mov_b64 v[48:49], 0\nv_mov_b64 v[50:51], 0\nv_mov_b64 v[52:53], 0\n" \
"v_mov_b32 v15, s40\nv_smfmac_f32_16x16x64_bf16 v[84:87], v[28:31], v[248:255], v10\ns_add_u32 s60, s55, 20480\ns_cmp_eq_u64 s[64:65], 0\nv_mov_b64 v[54:55], 0\ns_cbranch_scc1 .Lvu_tok%=\nv_cvt_pk_bf16_f32 v92, v80, v81\nv_cvt_pk_bf16_f32 v93, v82, v83\n" \
"s_nop 1\nv_cvt_pk_bf16_f32 v94, v84, v85\nv_cvt_pk_bf16_f32 v95, v86, v87\nv_and_or_b32 v92, v92, -2, v13\nv_and_or_b32 v94, v94, -2, v13\nbuffer_store_dwordx4 v[92:95], v7, s[36:39], s60 offen offset:2560 sc1\ns_mov_b32 s60, -2147483648\ns_cmp_lt_i32 s40, 63\n" \
"s_cbranch_scc1 .Lvu_f0%=\ns_mov_b32 s61, -2147483648\nv_add_u32 v14, s57, v0\ns_waitcnt lgkmcnt(0)\n.Lvu_nov%=:\ns_add_u32 m0, s57, 16384\ns_nop 0\nbuffer_load_dwordx4 v3, s[36:39], s42 offen offset:0 sc1 lds\n" \
"buffer_load_dwordx4 v3, s[36:39], s42 offen offset:1024 sc1 lds\nbuffer_load_dwordx4 v3, s[36:39], s42 offen offset:2048 sc1 lds\nbuffer_load_dwordx4 v3, s[36:39], s42 offen offset:3072 sc1 lds\ns_waitcnt vmcnt(0)\nds_read_b128 v[80:83], v14 offset:16384\nds_read_b128 v[84:87], v14 offset:17408\nds_read_b128 v[88:91], v14 offset:18432\nds_read_b128 v[92:95], v14 offset:19456\n" \
"s_waitcnt lgkmcnt(2)\nv_max3_u32 v76, v80, v82, v84\ns_waitcnt lgkmcnt(1)\nv_max3_u32 v76, v76, v86, v88\ns_waitcnt lgkmcnt(0)\nv_max3_u32 v76, v76, v90, v92\nv_max_u32 v76, v76, v94\nv_cmp_eq_u32 vcc, -1, v76\n" \
"s_cbranch_vccz .Lvu_novfok%=\ns_add_u32 s43, s43, 1\ns_cmp_lt_u32 s43, 262144\ns_cbranch_scc1 .Lvu_nov%=\ns_branch .Lvu_abort%=\n.Lvu_novfok%=:\ns_waitcnt lgkmcnt(0)\ns_barrier\n" \
"s_memrealtime s[68:69]\nds_read_b128 v[56:59], v1 offset:0\nds_read_b128 v[60:63], v1 offset:1024\nds_read_b128 v[80:83], v0 offset:16384\nds_read_b128 v[84:87], v0 offset:17408\nds_read_b128 v[88:91], v0 offset:18432\nds_read_b128 v[92:95], v0 offset:19456\nds_read_b128 v[16:19], v0 offset:20480\n" \
"ds_read_b128 v[20:23], v0 offset:21504\nds_read_b128 v[32:35], v0 offset:22528\nds_read_b128 v[36:39], v0 offset:23552\ns_waitcnt lgkmcnt(0)\ns_branch .Lvu_crit%=\n.Lvu_re%=:\n.Lvu_re2%=:\ns_add_u32 m0, s57, 16384\n" \
"v_max_u32 v76, v80, v82\nv_cmp_eq_u32 vcc, -1, v76\ns_cbranch_vccz .Lvu_sk1893%=\ns_mov_b64 exec, vcc\nbuffer_load_dwordx4 v3, s[36:39], s42 offen offset:0 sc1 lds\ns_mov_b64 exec, -1\n.Lvu_sk1893%=:\nv_max_u32 v76, v84, v86\n" \
"v_cmp_eq_u32 vcc, -1, v76\ns_cbranch_vccz .Lvu_sk1900%=\ns_mov_b64 exec, vcc\nbuffer_load_dwordx4 v3, s[36:39], s42 offen offset:1024 sc1 lds\ns_mov_b64 exec, -1\n.Lvu_sk1900%=:\nv_max_u32 v76, v88, v90\nv_cmp_eq_u32 vcc, -1, v76\n" \
"s_cbranch_vccz .Lvu_sk1907%=\ns_mov_b64 exec, vcc\nbuffer_load_dwordx4 v3, s[36:39], s42 offen offset:2048 sc1 lds\ns_mov_b64 exec, -1\n.Lvu_sk1907%=:\nv_max_u32 v76, v92, v94\nv_cmp_eq_u32 vcc, -1, v76\ns_cbranch_vccz .Lvu_sk1914%=\n" \
"s_mov_b64 exec, vcc\nbuffer_load_dwordx4 v3, s[36:39], s42 offen offset:3072 sc1 lds\ns_mov_b64 exec, -1\n.Lvu_sk1914%=:\n.Lvu_skf%=:\ns_waitcnt vmcnt(0)\nds_read_b128 v[80:83], v14 offset:16384\nds_read_b128 v[84:87], v14 offset:17408\n" \
"ds_read_b128 v[88:91], v14 offset:18432\nds_read_b128 v[92:95], v14 offset:19456\ns_waitcnt lgkmcnt(2)\nv_max3_u32 v76, v80, v82, v84\ns_waitcnt lgkmcnt(1)\nv_max3_u32 v76, v76, v86, v88\ns_waitcnt lgkmcnt(0)\nv_max3_u32 v76, v76, v90, v92\n" \
"v_max_u32 v76, v76, v94\nv_cmp_eq_u32 vcc, -1, v76\ns_cbranch_vccz .Lvu_rer%=\ns_add_u32 s43, s43, 1\ns_cmp_lt_u32 s43, 262144\ns_cbranch_scc1 .Lvu_re2%=\ns_branch .Lvu_abort%=\n.Lvu_rew%=:\n" \
"ds_read_b128 v[80:83], v14 offset:16384\nds_read_b128 v[84:87], v14 offset:17408\nds_read_b128 v[88:91], v14 offset:18432\nds_read_b128 v[92:95], v14 offset:19456\ns_waitcnt lgkmcnt(0)\ns_branch .Lvu_rer%=\n.Lvu_first%=:\ns_nop 7\n" \
"v_cvt_pk_bf16_f32 v76, v24, v25\nv_cvt_pk_bf16_f32 v77, v26, v27\nv_cvt_pk_bf16_f32 v78, v28, v29\nv_cvt_pk_bf16_f32 v79, v30, v31\nds_write2st64_b64 v9, v[76:77], v[78:79] offset0:2 offset1:10\ns_waitcnt vmcnt(0) lgkmcnt(0)\ns_barrier\nds_read_b128 v[56:59], v1 offset:8192\n" \
"ds_read_b128 v[60:63], v1 offset:9216\nds_read_b128 v[64:67], v1 offset:16384\nds_read_b128 v[68:71], v1 offset:17408\nds_read_b128 v[72:75], v11 offset:49152\nds_read_b128 v[76:79], v11 offset:50176\nds_read_b128 v[16:19], v11 offset:51200\nds_read_b128 v[20:23], v11 offset:52224\nds_read_b128 v[24:27], v11 offset:53248\n" \
"ds_read_b128 v[28:31], v11 offset:54272\nds_read_b128 v[32:35], v11 offset:55296\nds_read_b128 v[36:39], v11 offset:56320\ns_waitcnt lgkmcnt(0)\ns_add_u32 m0, s57, 0\ns_add_u32 s58, s61, 0\nbuffer_load_dwordx4 v0, s[36:39], s58 offen offset:0 sc1 lds\nbuffer_load_dwordx4 v0, s[36:39], s58 offen offset:1024 sc1 lds\n" \
"buffer_load_dwordx4 v0, s[36:39], s58 offen offset:2048 sc1 lds\nbuffer_load_dwordx4 v0, s[36:39], s58 offen offset:3072 sc1 lds\ns_mov_b32 s59, -2147483648\nbuffer_store_dword v14, off, s[36:39], s59\nbuffer_store_dword v14, off, s[36:39], s59\nbuffer_store_dword v14, off, s[36:39], s59\ns_branch .Lvu_y%=\n.Lvu_last%=:\n" \
"s_nop 7\nv_cvt_pk_bf16_f32 v92, v40, v41\nv_and_or_b32 v92, v92, -2, v12\ns_add_u32 s63, s59, 4096\nbuffer_store_dwordx4 v[88:91], v6, s[36:39], s63 offen sc1\nv_cvt_pk_bf16_f32 v93, v42, v43\nv_cvt_pk_bf16_f32 v94, v44, v45\nv_cvt_pk_bf16_f32 v95, v46, v47\n" \
"v_and_or_b32 v94, v94, -2, v12\ns_nop 0\nv_permlane16_swap_b32 v93, v95\nv_permlane16_swap_b32 v92, v94\ns_add_u32 s63, s59, 135168\nbuffer_store_dwordx4 v[92:95], v6, s[36:39], s63 offen sc1\ns_branch .Lvu_end%=\n.Lvu_abort%=:\n" \
"s_mov_b64 exec, -1\nv_mov_b32 v16, 0\ns_movk_i32 s44, 6400\nbuffer_store_dword v16, off, s[36:39], s44 sc1\n.Lvu_end%=:\ns_waitcnt vmcnt(0) lgkmcnt(0)\n" \
""
#define RD_ASM \
"v_mbcnt_lo_u32_b32 v255, -1, 0\nv_mbcnt_hi_u32_b32 v255, -1, v255\nv_lshlrev_b32 v255, 2, v255\nv_add_u32 v255, %[tb], v255\nds_read_b32 v196, v255 offset:0\nds_read_b32 v197, v255 offset:256\nds_read_b32 v198, v255 offset:512\nds_read_b32 v199, v255 offset:768\n" \
"ds_read_b32 v200, v255 offset:1024\nds_read_b32 v201, v255 offset:1280\nds_read_b32 v202, v255 offset:1536\nds_read_b32 v203, v255 offset:1792\nds_read_b32 v204, v255 offset:2048\nds_read_b32 v205, v255 offset:2304\nds_read_b32 v206, v255 offset:2560\nds_read_b32 v207, v255 offset:2816\n" \
"s_waitcnt lgkmcnt(0)\nv_accvgpr_write_b32 a32, v206\nv_accvgpr_write_b32 a33, v207\ns_mov_b32 s40, 0\ns_mov_b32 s43, 0\ns_add_u32 s57, %[su], 4096\ns_add_u32 s56, %[sp], 4096\ns_mov_b32 s41, %[sy]\n" \
"s_mov_b32 s59, -2147483648\n.Lrd_yflag%=:\nv_accvgpr_read_b32 v206, a32\nbuffer_load_dword v208, v206, %[rs], 0 offen sc1\ns_waitcnt vmcnt(0)\nv_cmp_gt_i32 vcc, s40, v208\ns_cbranch_vccz .Lrd_yld%=\ns_add_u32 s43, s43, 1\n" \
"s_cmp_lt_u32 s43, 262144\ns_cbranch_scc1 .Lrd_yflag%=\ns_branch .Lrd_abort%=\n.Lrd_yld%=:\ns_add_u32 s60, s41, 0\nbuffer_load_dwordx4 v[0:3], v196, %[rs], s60 offen offset:0 sc1\ns_add_u32 s60, s41, 0\nbuffer_load_dwordx4 v[32:35], v196, %[rs], s60 offen offset:1024 sc1\n" \
"s_add_u32 s60, s41, 0\nbuffer_load_dwordx4 v[64:67], v196, %[rs], s60 offen offset:2048 sc1\ns_add_u32 s60, s41, 131072\nbuffer_load_dwordx4 v[4:7], v196, %[rs], s60 offen offset:0 sc1\ns_add_u32 s60, s41, 131072\nbuffer_load_dwordx4 v[36:39], v196, %[rs], s60 offen offset:1024 sc1\ns_add_u32 s60, s41, 131072\nbuffer_load_dwordx4 v[68:71], v196, %[rs], s60 offen offset:2048 sc1\n" \
"s_add_u32 s60, s41, 262144\nbuffer_load_dwordx4 v[8:11], v196, %[rs], s60 offen offset:0 sc1\ns_add_u32 s60, s41, 262144\nbuffer_load_dwordx4 v[40:43], v196, %[rs], s60 offen offset:1024 sc1\ns_add_u32 s60, s41, 262144\nbuffer_load_dwordx4 v[72:75], v196, %[rs], s60 offen offset:2048 sc1\ns_add_u32 s60, s41, 393216\nbuffer_load_dwordx4 v[12:15], v196, %[rs], s60 offen offset:0 sc1\n" \
"s_add_u32 s60, s41, 393216\nbuffer_load_dwordx4 v[44:47], v196, %[rs], s60 offen offset:1024 sc1\ns_add_u32 s60, s41, 393216\nbuffer_load_dwordx4 v[76:79], v196, %[rs], s60 offen offset:2048 sc1\ns_add_u32 s60, s41, 524288\nbuffer_load_dwordx4 v[16:19], v196, %[rs], s60 offen offset:0 sc1\ns_add_u32 s60, s41, 524288\nbuffer_load_dwordx4 v[48:51], v196, %[rs], s60 offen offset:1024 sc1\n" \
"s_add_u32 s60, s41, 524288\nbuffer_load_dwordx4 v[80:83], v196, %[rs], s60 offen offset:2048 sc1\ns_add_u32 s60, s41, 655360\nbuffer_load_dwordx4 v[20:23], v196, %[rs], s60 offen offset:0 sc1\ns_add_u32 s60, s41, 655360\nbuffer_load_dwordx4 v[52:55], v196, %[rs], s60 offen offset:1024 sc1\ns_add_u32 s60, s41, 655360\nbuffer_load_dwordx4 v[84:87], v196, %[rs], s60 offen offset:2048 sc1\n" \
"s_add_u32 s60, s41, 786432\nbuffer_load_dwordx4 v[24:27], v196, %[rs], s60 offen offset:0 sc1\ns_add_u32 s60, s41, 786432\nbuffer_load_dwordx4 v[56:59], v196, %[rs], s60 offen offset:1024 sc1\ns_add_u32 s60, s41, 786432\nbuffer_load_dwordx4 v[88:91], v196, %[rs], s60 offen offset:2048 sc1\ns_add_u32 s60, s41, 917504\nbuffer_load_dwordx4 v[28:31], v196, %[rs], s60 offen offset:0 sc1\n" \
"s_add_u32 s60, s41, 917504\nbuffer_load_dwordx4 v[60:63], v196, %[rs], s60 offen offset:1024 sc1\ns_add_u32 s60, s41, 917504\nbuffer_load_dwordx4 v[92:95], v196, %[rs], s60 offen offset:2048 sc1\ns_add_u32 s60, s41, 0\nbuffer_load_dwordx4 v[96:99], v196, %[rs], s60 offen offset:3072 sc1\ns_add_u32 s60, s41, 131072\nbuffer_load_dwordx4 v[100:103], v196, %[rs], s60 offen offset:3072 sc1\n" \
"s_add_u32 s60, s41, 262144\nbuffer_load_dwordx4 v[104:107], v196, %[rs], s60 offen offset:3072 sc1\ns_add_u32 s60, s41, 393216\nbuffer_load_dwordx4 v[108:111], v196, %[rs], s60 offen offset:3072 sc1\ns_add_u32 s60, s41, 524288\nbuffer_load_dwordx4 v[112:115], v196, %[rs], s60 offen offset:3072 sc1\ns_add_u32 s60, s41, 655360\nbuffer_load_dwordx4 v[116:119], v196, %[rs], s60 offen offset:3072 sc1\n" \
"s_add_u32 s60, s41, 786432\nbuffer_load_dwordx4 v[120:123], v196, %[rs], s60 offen offset:3072 sc1\ns_add_u32 s60, s41, 917504\nbuffer_load_dwordx4 v[124:127], v196, %[rs], s60 offen offset:3072 sc1\ns_mov_b64 exec, -1\nv_accvgpr_read_b32 v212, a33\nbuffer_load_dwordx2 v[214:215], v212, %[rs], 0 offen sc1\ns_waitcnt vmcnt(0)\n" \
"s_branch .Lrd_ychk%=\n.Lrd_u2%=:\nbuffer_load_dwordx4 v[136:139], v196, %[rs], %[su] offen offset:2048 sc1\nbuffer_load_dwordx4 v[140:143], v196, %[rs], %[su] offen offset:3072 sc1\ns_add_u32 s43, s43, 1\ns_cmp_ge_u32 s43, 262144\ns_cbranch_scc1 .Lrd_abort%=\ns_waitcnt vmcnt(0)\n" \
"v_bitop3_b32 v208, v136, v138, s54 bitop3:0x7e\nv_bitop3_b32 v209, v140, v142, s54 bitop3:0x7e\nv_bitop3_b32 v208, v208, 1, v209 bitop3:0xc8\nv_cmp_ne_u32 vcc, 0, v208\ns_cbranch_vccnz .Lrd_u2%=\n.Lrd_u3%=:\nbuffer_load_dwordx4 v[128:131], v196, %[rs], %[su] offen offset:0 sc1\nbuffer_load_dwordx4 v[132:135], v196, %[rs], %[su] offen offset:1024 sc1\n" \
"buffer_load_dwordx4 v[144:147], v196, %[rs], s57 offen offset:0 sc1\nbuffer_load_dwordx4 v[148:151], v196, %[rs], s57 offen offset:1024 sc1\nbuffer_load_dwordx4 v[152:155], v196, %[rs], s57 offen offset:2048 sc1\nbuffer_load_dwordx4 v[156:159], v196, %[rs], s57 offen offset:3072 sc1\nbuffer_load_dwordx4 v[160:163], v196, %[rs], %[sp] offen offset:0 sc1\nbuffer_load_dwordx4 v[164:167], v196, %[rs], %[sp] offen offset:1024 sc1\nbuffer_load_dwordx4 v[168:171], v196, %[rs], %[sp] offen offset:2048 sc1\nbuffer_load_dwordx4 v[172:175], v196, %[rs], %[sp] offen offset:3072 sc1\n" \
"buffer_load_dwordx4 v[176:179], v196, %[rs], s56 offen offset:0 sc1\n.Lrd_ra%=:\ns_waitcnt vmcnt(5)\nv_bitop3_b32 v208, v128, v130, s54 bitop3:0x7e\nv_bitop3_b32 v209, v136, v138, s54 bitop3:0x7e\nv_bitop3_b32 v208, v208, 1, v209 bitop3:0xc8\nv_cmp_ne_u32 s[86:87], 0, v208\nv_bitop3_b32 v208, v144, v146, s54 bitop3:0x7e\n" \
"v_bitop3_b32 v209, v152, v154, s54 bitop3:0x7e\nv_bitop3_b32 v208, v208, 1, v209 bitop3:0xc8\nv_cmp_ne_u32 s[88:89], 0, v208\nv_bitop3_b32 v208, v132, v134, s54 bitop3:0x7e\nv_bitop3_b32 v209, v140, v142, s54 bitop3:0x7e\nv_bitop3_b32 v208, v208, 1, v209 bitop3:0xc8\nv_cmp_ne_u32 s[68:69], 0, v208\nv_bitop3_b32 v208, v148, v150, s54 bitop3:0x7e\n" \
"v_bitop3_b32 v209, v156, v158, s54 bitop3:0x7e\nv_bitop3_b32 v208, v208, 1, v209 bitop3:0xc8\nv_cmp_ne_u32 s[62:63], 0, v208\ns_or_b64 vcc, s[86:87], s[88:89]\ns_or_b64 vcc, vcc, s[68:69]\ns_or_b64 vcc, vcc, s[62:63]\ns_waitcnt vmcnt(4)\nv_smfmac_f32_16x16x64_bf16 v[180:183], v[160:163], v[0:7], v197\n" \
"v_bitop3_b32 v208, v160, v162, s54 bitop3:0x7e\nv_smfmac_f32_16x16x64_bf16 v[184:187], v[160:163], v[32:39], v197\nv_cmp_gt_i32 s[46:47], 0, v208\nv_smfmac_f32_16x16x64_bf16 v[188:191], v[160:163], v[64:71], v197\nv_smfmac_f32_16x16x64_bf16 v[192:195], v[160:163], v[96:103], v197\ns_or_b64 vcc, vcc, s[46:47]\ns_waitcnt vmcnt(3)\nv_smfmac_f32_16x16x64_bf16 v[180:183], v[164:167], v[8:15], v197\n" \
"v_bitop3_b32 v208, v164, v166, s54 bitop3:0x7e\nv_smfmac_f32_16x16x64_bf16 v[184:187], v[164:167], v[40:47], v197\nv_cmp_gt_i32 s[48:49], 0, v208\nv_smfmac_f32_16x16x64_bf16 v[188:191], v[164:167], v[72:79], v197\nv_smfmac_f32_16x16x64_bf16 v[192:195], v[164:167], v[104:111], v197\ns_or_b64 vcc, vcc, s[48:49]\ns_waitcnt vmcnt(2)\nv_smfmac_f32_16x16x64_bf16 v[180:183], v[168:171], v[16:23], v197\n" \
"v_bitop3_b32 v208, v168, v170, s54 bitop3:0x7e\nv_smfmac_f32_16x16x64_bf16 v[184:187], v[168:171], v[48:55], v197\nv_cmp_gt_i32 s[50:51], 0, v208\nv_smfmac_f32_16x16x64_bf16 v[188:191], v[168:171], v[80:87], v197\nv_smfmac_f32_16x16x64_bf16 v[192:195], v[168:171], v[112:119], v197\ns_or_b64 vcc, vcc, s[50:51]\ns_waitcnt vmcnt(1)\nv_smfmac_f32_16x16x64_bf16 v[180:183], v[172:175], v[24:31], v197\n" \
"s_waitcnt vmcnt(0)\nv_bitop3_b32 v208, v172, v174, s54 bitop3:0x7e\nv_smfmac_f32_16x16x64_bf16 v[180:183], v[176:179], v[128:135], v197\nv_bitop3_b32 v209, v176, v178, s54 bitop3:0x7e\nv_smfmac_f32_16x16x64_bf16 v[184:187], v[172:175], v[56:63], v197\nv_bitop3_b32 v210, v177, v179, s54 bitop3:0x7e\nv_smfmac_f32_16x16x64_bf16 v[184:187], v[176:179], v[136:143], v197\nv_cmp_gt_i32 s[52:53], 0, v208\n" \
"v_smfmac_f32_16x16x64_bf16 v[188:191], v[172:175], v[88:95], v197\nv_or_b32 v209, v209, v210\nv_smfmac_f32_16x16x64_bf16 v[188:191], v[176:179], v[144:151], v197\ns_or_b64 vcc, vcc, s[52:53]\nv_cmp_gt_i32 s[64:65], 0, v209\nv_smfmac_f32_16x16x64_bf16 v[192:195], v[172:175], v[120:127], v197\nv_smfmac_f32_16x16x64_bf16 v[192:195], v[176:179], v[152:159], v197\ns_or_b64 vcc, vcc, s[64:65]\n" \
"s_cbranch_scc1 .Lrd_rb%=\ns_add_u32 s60, s58, 16384\nv_add_f32 v180, v180, v182\nv_add_f32 v181, v181, v183\nds_write2_b32 v198, v180, v181 offset0:0 offset1:8\nbuffer_load_dwordx4 v[212:215], v196, %[rs], s60 offen offset:0 sc1\nv_add_f32 v184, v184, v186\nv_add_f32 v185, v185, v187\n" \
"ds_write2_b32 v198, v184, v185 offset0:32 offset1:40\ns_add_u32 s60, s58, 0\nbuffer_load_dwordx4 v[216:219], v196, %[rs], s60 offen offset:0 sc1\nv_add_f32 v188, v188, v190\nv_add_f32 v189, v189, v191\nds_write2_b32 v198, v188, v189 offset0:64 offset1:72\ns_add_u32 s60, s58, 0\nbuffer_load_dwordx4 v[220:223], v196, %[rs], s60 offen offset:1024 sc1\n" \
"v_add_f32 v192, v192, v194\nv_add_f32 v193, v193, v195\nds_write2_b32 v198, v192, v193 offset0:96 offset1:104\ns_add_u32 s60, s58, 16384\nbuffer_load_dwordx4 v[224:227], v196, %[rs], s60 offen offset:1024 sc1\ns_waitcnt lgkmcnt(0)\ns_barrier\nds_read_b128 v[180:183], v199 offset:0\n" \
"ds_read_b128 v[184:187], v199 offset:4096\nds_read_b128 v[188:191], v199 offset:8192\nds_read_b128 v[192:195], v199 offset:12288\ns_and_b32 s44, s54, 0x80008000\ns_add_u32 s60, s58, 0\nbuffer_load_dwordx4 v[228:231], v196, %[rs], s60 offen offset:2048 sc1\ns_waitcnt lgkmcnt(2)\nv_pk_add_f32 v[182:183], v[182:183], v[186:187]\n" \
"v_pk_add_f32 v[180:181], v[180:181], v[184:185]\ns_waitcnt lgkmcnt(0)\nv_pk_add_f32 v[186:187], v[188:189], v[192:193]\nv_pk_add_f32 v[184:185], v[190:191], v[194:195]\nv_pk_add_f32 v[180:181], v[180:181], v[186:187]\nv_pk_add_f32 v[182:183], v[182:183], v[184:185]\nv_cvt_pk_bf16_f32 v180, v180, v181\nv_xor_b32 v180, s44, v180\n" \
"v_cvt_pk_bf16_f32 v181, v182, v183\nv_min_u32 v180, -2, v180\nv_xor_b32 v209, s44, v181\nv_or_b32 v208, v180, v201\nv_mov_b32 v211, v209\nv_or_b32 v210, v180, v201\ns_nop 0\nv_permlane32_swap_b32 v209, v211\n" \
"v_permlane32_swap_b32 v208, v210\nbuffer_store_dwordx4 v[208:211], v206, %[rs], 0 offen sc1\ns_add_u32 s60, s58, 0\nbuffer_load_dwordx4 v[232:235], v196, %[rs], s60 offen offset:3072 sc1\ns_add_u32 s60, s58, 16384\nbuffer_load_dwordx4 v[236:239], v196, %[rs], s60 offen offset:2048 sc1\ns_add_u32 s60, s58, 4096\nbuffer_load_dwordx4 v[240:243], v196, %[rs], s60 offen offset:0 sc1\n" \
"s_add_u32 s60, s58, 4096\nbuffer_load_dwordx4 v[244:247], v196, %[rs], s60 offen offset:1024 sc1\ns_add_u32 s60, s58, 16384\nbuffer_load_dwordx4 v[248:251], v196, %[rs], s60 offen offset:3072 sc1\ns_xor_b32 s41, s41, 4194304\nv_mov_b32 v207, s40\ns_waitcnt vmcnt(10)\nv_bitop3_b32 v209, v212, v214, s61 bitop3:0x7e\n" \
"v_cmp_gt_i32 vcc, 0, v209\ns_cbranch_vccnz .Lrd_tre0%=\n.Lrd_tl0%=:\nv_mfma_f32_16x16x16_bf16 a[0:3], v[0:1], v[212:213], 0\ns_add_u32 s60, s58, 20480\nbuffer_load_dwordx4 v[252:255], v196, %[rs], s60 offen offset:0 sc1\nv_mfma_f32_16x16x16_bf16 a[8:11], v[32:33], v[212:213], 0\nv_mfma_f32_16x16x16_bf16 a[16:19], v[64:65], v[212:213], 0\n" \
"v_mfma_f32_16x16x16_bf16 a[24:27], v[96:97], v[212:213], 0\ns_waitcnt vmcnt(10)\nv_mfma_f32_16x16x16_bf16 a[0:3], v[4:5], v[214:215], a[0:3]\ns_add_u32 s60, s58, 20480\nbuffer_load_dwordx4 v[160:163], v196, %[rs], s60 offen offset:2048 sc1\nv_bitop3_b32 v209, v216, v218, s61 bitop3:0x7e\nv_mfma_f32_16x16x16_bf16 a[8:11], v[36:37], v[214:215], a[8:11]\ns_lshl_b32 s42, s40, 17\n" \
"s_add_u32 s42, s42, %[at]\nv_mfma_f32_16x16x16_bf16 a[16:19], v[68:69], v[214:215], a[16:19]\nv_cmp_gt_i32 vcc, 0, v209\nv_mfma_f32_16x16x16_bf16 a[24:27], v[100:101], v[214:215], a[24:27]\ns_cbranch_vccnz .Lrd_tre1%=\n.Lrd_tl1%=:\ns_waitcnt vmcnt(10)\nv_mfma_f32_16x16x32_bf16 a[4:7], v[0:3], v[216:219], 0\n" \
"s_add_u32 s60, s58, 4096\nbuffer_load_dwordx4 v[164:167], v196, %[rs], s60 offen offset:2048 sc1\nv_bitop3_b32 v209, v220, v222, s61 bitop3:0x7e\nv_mfma_f32_16x16x32_bf16 a[12:15], v[32:35], v[216:219], 0\ns_lshr_b32 s45, s42, 8\ns_and_b32 s45, s45, 0x2000\nv_mfma_f32_16x16x32_bf16 a[20:23], v[64:67], v[216:219], 0\nv_cmp_gt_i32 vcc, 0, v209\n" \
"v_mfma_f32_16x16x32_bf16 a[28:31], v[96:99], v[216:219], 0\ns_cbranch_vccnz .Lrd_tre2%=\n.Lrd_tl2%=:\ns_waitcnt vmcnt(10)\nv_mfma_f32_16x16x32_bf16 a[4:7], v[4:7], v[220:223], a[4:7]\ns_add_u32 s60, s58, 4096\nbuffer_load_dwordx4 v[168:171], v196, %[rs], s60 offen offset:3072 sc1\nv_bitop3_b32 v209, v224, v226, s61 bitop3:0x7e\n" \
"v_mfma_f32_16x16x32_bf16 a[12:15], v[36:39], v[220:223], a[12:15]\ns_xor_b32 s42, s42, s45\ns_add_u32 s40, s40, 1\nv_mfma_f32_16x16x32_bf16 a[20:23], v[68:71], v[220:223], a[20:23]\nv_cmp_gt_i32 vcc, 0, v209\nv_mfma_f32_16x16x32_bf16 a[28:31], v[100:103], v[220:223], a[28:31]\ns_cbranch_vccnz .Lrd_tre3%=\n.Lrd_tl3%=:\n" \
"v_mfma_f32_16x16x16_bf16 a[0:3], v[8:9], v[224:225], a[0:3]\ns_add_u32 s60, s58, 20480\nbuffer_load_dwordx4 v[172:175], v196, %[rs], s60 offen offset:1024 sc1\nv_mfma_f32_16x16x16_bf16 a[8:11], v[40:41], v[224:225], a[8:11]\nv_mfma_f32_16x16x16_bf16 a[16:19], v[72:73], v[224:225], a[16:19]\nbuffer_load_dwordx4 v[212:215], v204, %[rs], s42 offen sc1\nv_mfma_f32_16x16x16_bf16 a[24:27], v[104:105], v[224:225], a[24:27]\ns_waitcnt vmcnt(12)\n" \
"v_mfma_f32_16x16x16_bf16 a[0:3], v[12:13], v[226:227], a[0:3]\ns_add_u32 s60, s58, 20480\nbuffer_load_dwordx4 v[176:179], v196, %[rs], s60 offen offset:3072 sc1\nv_bitop3_b32 v209, v228, v230, s61 bitop3:0x7e\nv_mfma_f32_16x16x16_bf16 a[8:11], v[44:45], v[226:227], a[8:11]\ns_lshl_b32 s62, s55, 31\ns_or_b32 s62, s62, 1.0\ns_mov_b32 s63, s62\n" \
"v_mfma_f32_16x16x16_bf16 a[16:19], v[76:77], v[226:227], a[16:19]\nv_cmp_gt_i32 vcc, 0, v209\nv_mfma_f32_16x16x16_bf16 a[24:27], v[108:109], v[226:227], a[24:27]\ns_cbranch_vccnz .Lrd_tre4%=\n.Lrd_tl4%=:\ns_waitcnt vmcnt(11)\nv_mfma_f32_16x16x32_bf16 a[4:7], v[8:11], v[228:231], a[4:7]\ns_cmp_eq_u32 s40, 1\n" \
"s_cbranch_scc1 .Lrd_y1%=\n.Lrd_y1r%=:\ns_add_u32 s60, s41, 0\nbuffer_load_dwordx4 v[0:3], v196, %[rs], s60 offen offset:0 sc1\nv_bitop3_b32 v209, v232, v234, s61 bitop3:0x7e\nv_mfma_f32_16x16x32_bf16 a[12:15], v[40:43], v[228:231], a[12:15]\nv_mfma_f32_16x16x32_bf16 a[20:23], v[72:75], v[228:231], a[20:23]\nv_cmp_gt_i32 vcc, 0, v209\n" \
"v_mfma_f32_16x16x32_bf16 a[28:31], v[104:107], v[228:231], a[28:31]\ns_cbranch_vccnz .Lrd_tre5%=\n.Lrd_tl5%=:\ns_waitcnt vmcnt(11)\nv_mfma_f32_16x16x32_bf16 a[4:7], v[12:15], v[232:235], a[4:7]\ns_add_u32 s60, s41, 0\nbuffer_load_dwordx4 v[32:35], v196, %[rs], s60 offen offset:1024 sc1\nv_bitop3_b32 v209, v236, v238, s61 bitop3:0x7e\n" \
"v_mfma_f32_16x16x32_bf16 a[12:15], v[44:47], v[232:235], a[12:15]\nv_mfma_f32_16x16x32_bf16 a[20:23], v[76:79], v[232:235], a[20:23]\nv_cmp_gt_i32 vcc, 0, v209\nv_mfma_f32_16x16x32_bf16 a[28:31], v[108:111], v[232:235], a[28:31]\ns_cbranch_vccnz .Lrd_tre6%=\n.Lrd_tl6%=:\nv_mfma_f32_16x16x16_bf16 a[0:3], v[16:17], v[236:237], a[0:3]\ns_add_u32 s60, s41, 0\n" \
"buffer_load_dwordx4 v[64:67], v196, %[rs], s60 offen offset:2048 sc1\nv_mfma_f32_16x16x16_bf16 a[8:11], v[48:49], v[236:237], a[8:11]\nv_mfma_f32_16x16x16_bf16 a[16:19], v[80:81], v[236:237], a[16:19]\nv_mfma_f32_16x16x16_bf16 a[24:27], v[112:113], v[236:237], a[24:27]\ns_waitcnt vmcnt(12)\nv_mfma_f32_16x16x16_bf16 a[0:3], v[20:21], v[238:239], a[0:3]\ns_add_u32 s60, s41, 131072\nbuffer_load_dwordx4 v[4:7], v196, %[rs], s60 offen offset:0 sc1\n" \
"v_bitop3_b32 v209, v240, v242, s61 bitop3:0x7e\nv_mfma_f32_16x16x16_bf16 a[8:11], v[52:53], v[238:239], a[8:11]\nv_mfma_f32_16x16x16_bf16 a[16:19], v[84:85], v[238:239], a[16:19]\nv_cmp_gt_i32 vcc, 0, v209\nv_mfma_f32_16x16x16_bf16 a[24:27], v[116:117], v[238:239], a[24:27]\ns_cbranch_vccnz .Lrd_tre7%=\n.Lrd_tl7%=:\ns_waitcnt vmcnt(12)\n" \
"v_mfma_f32_16x16x32_bf16 a[4:7], v[16:19], v[240:243], a[4:7]\ns_add_u32 s60, s41, 131072\nbuffer_load_dwordx4 v[36:39], v196, %[rs], s60 offen offset:1024 sc1\nv_bitop3_b32 v209, v244, v246, s61 bitop3:0x7e\nv_mfma_f32_16x16x32_bf16 a[12:15], v[48:51], v[240:243], a[12:15]\nv_mfma_f32_16x16x32_bf16 a[20:23], v[80:83], v[240:243], a[20:23]\nv_cmp_gt_i32 vcc, 0, v209\nv_mfma_f32_16x16x32_bf16 a[28:31], v[112:115], v[240:243], a[28:31]\n" \
"s_cbranch_vccnz .Lrd_tre8%=\n.Lrd_tl8%=:\ns_waitcnt vmcnt(12)\nv_mfma_f32_16x16x32_bf16 a[4:7], v[20:23], v[244:247], a[4:7]\ns_add_u32 s60, s41, 131072\nbuffer_load_dwordx4 v[68:71], v196, %[rs], s60 offen offset:2048 sc1\nv_bitop3_b32 v209, v248, v250, s61 bitop3:0x7e\nv_mfma_f32_16x16x32_bf16 a[12:15], v[52:55], v[244:247], a[12:15]\n" \
"v_mfma_f32_16x16x32_bf16 a[20:23], v[84:87], v[244:247], a[20:23]\nv_cmp_gt_i32 vcc, 0, v209\nv_mfma_f32_16x16x32_bf16 a[28:31], v[116:119], v[244:247], a[28:31]\ns_cbranch_vccnz .Lrd_tre9%=\n.Lrd_tl9%=:\nv_mfma_f32_16x16x16_bf16 a[0:3], v[24:25], v[248:249], a[0:3]\ns_add_u32 s60, s41, 262144\nbuffer_load_dwordx4 v[8:11], v196, %[rs], s60 offen offset:0 sc1\n" \
"v_mfma_f32_16x16x16_bf16 a[8:11], v[56:57], v[248:249], a[8:11]\nv_mfma_f32_16x16x16_bf16 a[16:19], v[88:89], v[248:249], a[16:19]\nv_mfma_f32_16x16x16_bf16 a[24:27], v[120:121], v[248:249], a[24:27]\ns_waitcnt vmcnt(13)\nv_mfma_f32_16x16x16_bf16 a[0:3], v[28:29], v[250:251], a[0:3]\ns_add_u32 s60, s41, 262144\nbuffer_load_dwordx4 v[40:43], v196, %[rs], s60 offen offset:1024 sc1\nv_bitop3_b32 v209, v252, v254, s61 bitop3:0x7e\n" \
"v_mfma_f32_16x16x16_bf16 a[8:11], v[60:61], v[250:251], a[8:11]\nv_bitop3_b32 v210, v253, v255, s61 bitop3:0x7e\nv_or_b32 v209, v209, v210\nv_mfma_f32_16x16x16_bf16 a[16:19], v[92:93], v[250:251], a[16:19]\nv_cmp_gt_i32 vcc, 0, v209\nv_mfma_f32_16x16x16_bf16 a[24:27], v[124:125], v[250:251], a[24:27]\ns_cbranch_vccnz .Lrd_tre10%=\n.Lrd_tl10%=:\n" \
"s_waitcnt vmcnt(13)\nv_mfma_f32_16x16x32_bf16 a[0:3], v[128:131], v[252:255], a[0:3]\ns_add_u32 s60, s41, 262144\nbuffer_load_dwordx4 v[72:75], v196, %[rs], s60 offen offset:2048 sc1\nv_bitop3_b32 v209, v160, v162, s61 bitop3:0x7e\nv_mfma_f32_16x16x32_bf16 a[8:11], v[136:139], v[252:255], a[8:11]\nv_bitop3_b32 v210, v161, v163, s61 bitop3:0x7e\nv_or_b32 v209, v209, v210\n" \
"v_mfma_f32_16x16x32_bf16 a[16:19], v[144:147], v[252:255], a[16:19]\nv_cmp_gt_i32 vcc, 0, v209\nv_mfma_f32_16x16x32_bf16 a[24:27], v[152:155], v[252:255], a[24:27]\ns_cbranch_vccnz .Lrd_tre11%=\n.Lrd_tl11%=:\ns_waitcnt vmcnt(13)\nv_mfma_f32_16x16x32_bf16 a[0:3], v[132:135], v[160:163], a[0:3]\ns_add_u32 s60, s41, 393216\n" \
"buffer_load_dwordx4 v[12:15], v196, %[rs], s60 offen offset:0 sc1\nv_bitop3_b32 v209, v164, v166, s61 bitop3:0x7e\nv_mfma_f32_16x16x32_bf16 a[8:11], v[140:143], v[160:163], a[8:11]\nv_mfma_f32_16x16x32_bf16 a[16:19], v[148:151], v[160:163], a[16:19]\nv_cmp_gt_i32 vcc, 0, v209\nv_mfma_f32_16x16x32_bf16 a[24:27], v[156:159], v[160:163], a[24:27]\ns_cbranch_vccnz .Lrd_tre12%=\n.Lrd_tl12%=:\n" \
"s_waitcnt vmcnt(13)\nv_mfma_f32_16x16x32_bf16 a[4:7], v[24:27], v[164:167], a[4:7]\ns_add_u32 s60, s41, 393216\nbuffer_load_dwordx4 v[44:47], v196, %[rs], s60 offen offset:1024 sc1\nv_bitop3_b32 v209, v168, v170, s61 bitop3:0x7e\nv_mfma_f32_16x16x32_bf16 a[12:15], v[56:59], v[164:167], a[12:15]\nv_mfma_f32_16x16x32_bf16 a[20:23], v[88:91], v[164:167], a[20:23]\nds_write_b128 v202, a[0:3] offset:0\n" \
"v_cmp_gt_i32 vcc, 0, v209\nv_mfma_f32_16x16x32_bf16 a[28:31], v[120:123], v[164:167], a[28:31]\ns_cbranch_vccnz .Lrd_tre13%=\n.Lrd_tl13%=:\ns_waitcnt vmcnt(13)\nv_mfma_f32_16x16x32_bf16 a[4:7], v[28:31], v[168:171], a[4:7]\ns_add_u32 s60, s41, 393216\nbuffer_load_dwordx4 v[76:79], v196, %[rs], s60 offen offset:2048 sc1\n" \
"v_bitop3_b32 v209, v172, v174, s61 bitop3:0x7e\nv_mfma_f32_16x16x32_bf16 a[12:15], v[60:63], v[168:171], a[12:15]\nv_bitop3_b32 v210, v173, v175, s61 bitop3:0x7e\nv_or_b32 v209, v209, v210\nv_mfma_f32_16x16x32_bf16 a[20:23], v[92:95], v[168:171], a[20:23]\nds_write_b128 v202, a[8:11] offset:2048\nv_cmp_gt_i32 vcc, 0, v209\nv_mfma_f32_16x16x32_bf16 a[28:31], v[124:127], v[168:171], a[28:31]\n" \
"s_cbranch_vccnz .Lrd_tre14%=\n.Lrd_tl14%=:\ns_waitcnt vmcnt(12)\nv_mfma_f32_16x16x32_bf16 a[4:7], v[128:131], v[172:175], a[4:7]\ns_add_u32 s60, s41, 524288\nbuffer_load_dwordx4 v[16:19], v196, %[rs], s60 offen offset:0 sc1\nv_bitop3_b32 v209, v176, v178, s61 bitop3:0x7e\nv_mfma_f32_16x16x32_bf16 a[12:15], v[136:139], v[172:175], a[12:15]\n" \
"v_bitop3_b32 v210, v177, v179, s61 bitop3:0x7e\nv_or_b32 v209, v209, v210\nv_mfma_f32_16x16x32_bf16 a[20:23], v[144:147], v[172:175], a[20:23]\nds_write_b128 v202, a[16:19] offset:4096\nv_cmp_gt_i32 vcc, 0, v209\nv_mfma_f32_16x16x32_bf16 a[28:31], v[152:155], v[172:175], a[28:31]\ns_cbranch_vccnz .Lrd_tre15%=\n.Lrd_tl15%=:\n" \
"v_mfma_f32_16x16x32_bf16 a[4:7], v[132:135], v[176:179], a[4:7]\ns_add_u32 s60, s41, 524288\nbuffer_load_dwordx4 v[48:51], v196, %[rs], s60 offen offset:1024 sc1\nv_lshlrev_b32 v216, 16, v212\nv_and_b32 v217, 0xffff0000, v212\nv_mfma_f32_16x16x32_bf16 a[12:15], v[140:143], v[176:179], a[12:15]\nv_lshlrev_b32 v218, 16, v213\nv_and_b32 v219, 0xffff0000, v213\n" \
"v_mfma_f32_16x16x32_bf16 a[20:23], v[148:151], v[176:179], a[20:23]\nds_write_b128 v202, a[24:27] offset:6144\nv_lshlrev_b32 v220, 16, v214\nv_and_b32 v221, 0xffff0000, v214\nv_mfma_f32_16x16x32_bf16 a[28:31], v[156:159], v[176:179], a[28:31]\ns_add_u32 s60, s41, 524288\nbuffer_load_dwordx4 v[80:83], v196, %[rs], s60 offen offset:2048 sc1\ns_add_u32 s60, s41, 655360\n" \
"ds_write_b128 v202, a[4:7] offset:1024\nbuffer_load_dwordx4 v[20:23], v196, %[rs], s60 offen offset:0 sc1\nds_write_b128 v202, a[12:15] offset:3072\nv_lshlrev_b32 v222, 16, v215\nv_and_b32 v223, 0xffff0000, v215\ns_add_u32 s60, s41, 655360\nbuffer_load_dwordx4 v[52:55], v196, %[rs], s60 offen offset:1024 sc1\nds_write_b128 v202, a[20:23] offset:5120\n" \
"s_add_u32 s60, s41, 655360\nbuffer_load_dwordx4 v[84:87], v196, %[rs], s60 offen offset:2048 sc1\nds_write_b128 v202, a[28:31] offset:7168\ns_waitcnt lgkmcnt(0)\ns_barrier\nds_read_b128 v[224:227], v203 offset:0\nds_read_b128 v[228:231], v203 offset:256\nds_read_b128 v[232:235], v203 offset:8192\n" \
"ds_read_b128 v[236:239], v203 offset:8448\nds_read_b128 v[240:243], v203 offset:16384\nds_read_b128 v[244:247], v203 offset:16640\nds_read_b128 v[248:251], v203 offset:24576\nds_read_b128 v[252:255], v203 offset:24832\ns_add_u32 s60, s41, 786432\nbuffer_load_dwordx4 v[24:27], v196, %[rs], s60 offen offset:0 sc1\ns_waitcnt lgkmcnt(4)\n" \
"v_pk_add_f32 v[224:225], v[224:225], v[232:233]\nv_pk_add_f32 v[226:227], v[226:227], v[234:235]\nv_pk_add_f32 v[228:229], v[228:229], v[236:237]\nv_pk_add_f32 v[230:231], v[230:231], v[238:239]\ns_add_u32 s60, s41, 786432\nbuffer_load_dwordx4 v[56:59], v196, %[rs], s60 offen offset:1024 sc1\ns_waitcnt lgkmcnt(2)\nv_pk_add_f32 v[224:225], v[224:225], v[240:241]\n" \
"v_pk_add_f32 v[226:227], v[226:227], v[242:243]\nv_pk_add_f32 v[228:229], v[228:229], v[244:245]\nv_pk_add_f32 v[230:231], v[230:231], v[246:247]\ns_add_u32 s60, s41, 786432\nbuffer_load_dwordx4 v[88:91], v196, %[rs], s60 offen offset:2048 sc1\ns_waitcnt lgkmcnt(0)\nv_pk_add_f32 v[224:225], v[224:225], v[248:249]\nv_pk_add_f32 v[226:227], v[226:227], v[250:251]\n" \
"v_pk_add_f32 v[228:229], v[228:229], v[252:253]\nv_pk_add_f32 v[230:231], v[230:231], v[254:255]\nv_pk_fma_f32 v[224:225], v[224:225], s[62:63], v[216:217]\nv_pk_fma_f32 v[226:227], v[226:227], s[62:63], v[218:219]\nv_pk_fma_f32 v[228:229], v[228:229], s[62:63], v[220:221]\nv_pk_fma_f32 v[230:231], v[230:231], s[62:63], v[222:223]\nv_cvt_pk_bf16_f32 v208, v224, v225\nv_cvt_pk_bf16_f32 v209, v226, v227\n" \
"v_cvt_pk_bf16_f32 v210, v228, v229\nv_cvt_pk_bf16_f32 v211, v230, v231\nbuffer_store_dwordx4 v[208:211], v204, %[rs], s42 offen sc1\nv_pk_add_f32 v[232:233], v[224:225], v[226:227]\nv_pk_add_f32 v[234:235], v[228:229], v[230:231]\nv_pk_add_f32 v[232:233], v[232:233], v[234:235]\ns_add_u32 s60, s41, 917504\nbuffer_load_dwordx4 v[28:31], v196, %[rs], s60 offen offset:0 sc1\n" \
"s_add_u32 s60, s41, 917504\nbuffer_load_dwordx4 v[60:63], v196, %[rs], s60 offen offset:1024 sc1\nv_add_f32 v232, v232, v233\ns_nop 1\nv_add_f32_dpp v233, v232, v232 row_ror:8 row_mask:0xf bank_mask:0xf\nv_add_f32_dpp v234, v232, v232 row_ror:8 row_mask:0xf bank_mask:0xf\ns_nop 1\nv_permlane16_swap_b32 v233, v234\n" \
"v_add_f32 v232, v233, v234\nv_add_f32 v233, v233, v234\ns_nop 1\nv_permlane32_swap_b32 v232, v233\nv_add_f32 v232, v232, v233\nv_mul_f32 v232, 0x3c800000, v232\nv_pk_add_f32 v[224:225], v[224:225], v[232:233] op_sel_hi:[1,0] neg_lo:[0,1] neg_hi:[0,1]\nv_pk_add_f32 v[226:227], v[226:227], v[232:233] op_sel_hi:[1,0] neg_lo:[0,1] neg_hi:[0,1]\n" \
"v_pk_add_f32 v[228:229], v[228:229], v[232:233] op_sel_hi:[1,0] neg_lo:[0,1] neg_hi:[0,1]\nv_pk_add_f32 v[230:231], v[230:231], v[232:233] op_sel_hi:[1,0] neg_lo:[0,1] neg_hi:[0,1]\ns_add_u32 s60, s41, 917504\nbuffer_load_dwordx4 v[92:95], v196, %[rs], s60 offen offset:2048 sc1\ns_add_u32 s60, s41, 0\nbuffer_load_dwordx4 v[96:99], v196, %[rs], s60 offen offset:3072 sc1\nv_pk_mul_f32 v[236:237], v[224:225], v[224:225]\nv_pk_mul_f32 v[238:239], v[226:227], v[226:227]\n" \
"v_pk_fma_f32 v[236:237], v[228:229], v[228:229], v[236:237]\nv_pk_fma_f32 v[238:239], v[230:231], v[230:231], v[238:239]\nv_pk_add_f32 v[236:237], v[236:237], v[238:239]\nv_add_f32 v236, v236, v237\ns_nop 1\nv_add_f32_dpp v237, v236, v236 row_ror:8 row_mask:0xf bank_mask:0xf\nv_add_f32_dpp v238, v236, v236 row_ror:8 row_mask:0xf bank_mask:0xf\ns_nop 1\n" \
"v_permlane16_swap_b32 v237, v238\nv_add_f32 v236, v237, v238\nv_add_f32 v237, v237, v238\ns_nop 1\nv_permlane32_swap_b32 v236, v237\nv_add_f32 v236, v236, v237\nv_mov_b32 v233, v236\nbuffer_store_dwordx2 v[232:233], v205, %[rs], s42 offen sc1\n" \
"s_mov_b32 s59, %[fa]\ns_add_u32 s60, s41, 131072\nbuffer_load_dwordx4 v[100:103], v196, %[rs], s60 offen offset:3072 sc1\nv_accvgpr_read_b32 v212, a33\nbuffer_load_dwordx2 v[214:215], v212, %[rs], 0 offen sc1\ns_add_u32 s60, s41, 262144\nbuffer_load_dwordx4 v[104:107], v196, %[rs], s60 offen offset:3072 sc1\ns_add_u32 s60, s41, 393216\n" \
"buffer_load_dwordx4 v[108:111], v196, %[rs], s60 offen offset:3072 sc1\ns_add_u32 s60, s41, 524288\nbuffer_load_dwordx4 v[112:115], v196, %[rs], s60 offen offset:3072 sc1\ns_add_u32 s60, s41, 655360\nbuffer_load_dwordx4 v[116:119], v196, %[rs], s60 offen offset:3072 sc1\ns_add_u32 s60, s41, 786432\nbuffer_load_dwordx4 v[120:123], v196, %[rs], s60 offen offset:3072 sc1\ns_add_u32 s60, s41, 917504\n" \
"buffer_load_dwordx4 v[124:127], v196, %[rs], s60 offen offset:3072 sc1\ns_cmp_ge_u32 s40, 64\ns_cbranch_scc1 .Lrd_fin%=\n.Lrd_ychk%=:\ns_and_b32 s44, s40, 1\ns_bfe_u32 s55, s40, 0x10001\ns_mul_i32 s54, s44, 0x80008001\ns_mul_i32 s61, s55, 0x80008000\n" \
"s_lshl_b32 s58, s44, 17\ns_add_u32 s58, s58, %[st]\ns_add_u32 s44, s40, 1\ns_and_b32 s44, s44, 3\ns_lshl_b32 s44, s44, 15\nv_xor_b32 v206, s44, v200\nv_mov_b64 v[180:181], 0\nv_mov_b64 v[182:183], 0\n" \
"v_mov_b64 v[184:185], 0\nv_mov_b64 v[186:187], 0\nv_mov_b64 v[188:189], 0\nv_mov_b64 v[190:191], 0\nv_mov_b64 v[192:193], 0\nv_mov_b64 v[194:195], 0\ns_waitcnt vmcnt(6)\n.Lrd_sg0%=:\n" \
"s_memrealtime s[68:69]\nv_cmp_ne_u32 vcc, s40, v214\ns_cbranch_vccnz .Lrd_sg1%=\nv_max_u32_dpp v215, v215, v215 quad_perm:[1,0,3,2] row_mask:0xf bank_mask:0xf\ns_nop 1\nv_max_u32_dpp v215, v215, v215 quad_perm:[2,3,0,1] row_mask:0xf bank_mask:0xf\ns_nop 1\nv_max_u32_dpp v215, v215, v215 row_half_mirror row_mask:0xf bank_mask:0xf\n" \
"s_nop 0\nv_readlane_b32 s86, v215, 0\nv_readlane_b32 s87, v215, 8\ns_add_u32 s86, s86, %[du]\n.Lrd_tw626%=:\ns_waitcnt lgkmcnt(0)\ns_sub_u32 s44, s86, s68\ns_add_u32 s44, s44, 11\n" \
"s_cmp_lt_u32 s44, 250\ns_cbranch_scc1 .Lrd_tw626x%=\nbuffer_load_dwordx4 v[128:131], v196, %[rs], %[su] offen offset:0 sc1\nbuffer_load_dwordx4 v[132:135], v196, %[rs], %[su] offen offset:1024 sc1\ns_memrealtime s[68:69]\nbuffer_load_dwordx4 v[136:139], v196, %[rs], %[su] offen offset:2048 sc1\nbuffer_load_dwordx4 v[140:143], v196, %[rs], %[su] offen offset:3072 sc1\n.Lrd_tw637%=:\n" \
"s_waitcnt lgkmcnt(0)\ns_sub_u32 s44, s86, s68\ns_add_u32 s44, s44, 21\ns_cmp_lt_u32 s44, 250\ns_cbranch_scc1 .Lrd_tw637x%=\nbuffer_load_dwordx4 v[144:147], v196, %[rs], s57 offen offset:0 sc1\nbuffer_load_dwordx4 v[148:151], v196, %[rs], s57 offen offset:1024 sc1\ns_memrealtime s[68:69]\n" \
"buffer_load_dwordx4 v[152:155], v196, %[rs], s57 offen offset:2048 sc1\nbuffer_load_dwordx4 v[156:159], v196, %[rs], s57 offen offset:3072 sc1\nbuffer_store_dword v207, off, %[rs], s59 sc1\ns_mov_b32 s59, -2147483648\n.Lrd_tw650%=:\ns_waitcnt lgkmcnt(0)\ns_sub_u32 s44, s87, s68\ns_add_u32 s44, s44, 24\n" \
"s_cmp_lt_u32 s44, 250\ns_cbranch_scc1 .Lrd_tw650x%=\nbuffer_load_dwordx4 v[160:163], v196, %[rs], %[sp] offen offset:0 sc1\nbuffer_load_dwordx4 v[164:167], v196, %[rs], %[sp] offen offset:1024 sc1\nbuffer_load_dwordx4 v[168:171], v196, %[rs], %[sp] offen offset:2048 sc1\nbuffer_load_dwordx4 v[172:175], v196, %[rs], %[sp] offen offset:3072 sc1\nbuffer_load_dwordx4 v[176:179], v196, %[rs], s56 offen offset:0 sc1\nv_bitop3_b32 v209, v0, v2, s55 bitop3:0x7e\n" \
"v_bitop3_b32 v210, v32, v34, s55 bitop3:0x7e\nv_or3_b32 v208, v209, v210, 0\nv_bitop3_b32 v209, v64, v66, s55 bitop3:0x7e\nv_bitop3_b32 v210, v4, v6, s55 bitop3:0x7e\nv_or3_b32 v208, v209, v210, v208\nv_bitop3_b32 v209, v36, v38, s55 bitop3:0x7e\nv_bitop3_b32 v210, v68, v70, s55 bitop3:0x7e\nv_or3_b32 v208, v209, v210, v208\n" \
"v_bitop3_b32 v209, v8, v10, s55 bitop3:0x7e\nv_bitop3_b32 v210, v40, v42, s55 bitop3:0x7e\nv_or3_b32 v208, v209, v210, v208\nv_bitop3_b32 v209, v72, v74, s55 bitop3:0x7e\nv_bitop3_b32 v210, v12, v14, s55 bitop3:0x7e\nv_or3_b32 v208, v209, v210, v208\nv_bitop3_b32 v209, v44, v46, s55 bitop3:0x7e\nv_bitop3_b32 v210, v76, v78, s55 bitop3:0x7e\n" \
"v_or3_b32 v208, v209, v210, v208\nv_bitop3_b32 v209, v16, v18, s55 bitop3:0x7e\nv_bitop3_b32 v210, v48, v50, s55 bitop3:0x7e\nv_or3_b32 v208, v209, v210, v208\nv_bitop3_b32 v209, v80, v82, s55 bitop3:0x7e\nv_bitop3_b32 v210, v20, v22, s55 bitop3:0x7e\nv_or3_b32 v208, v209, v210, v208\nv_bitop3_b32 v209, v52, v54, s55 bitop3:0x7e\n" \
"v_bitop3_b32 v210, v84, v86, s55 bitop3:0x7e\nv_or3_b32 v208, v209, v210, v208\nv_bitop3_b32 v209, v24, v26, s55 bitop3:0x7e\nv_bitop3_b32 v210, v56, v58, s55 bitop3:0x7e\nv_or3_b32 v208, v209, v210, v208\nv_bitop3_b32 v209, v88, v90, s55 bitop3:0x7e\nv_bitop3_b32 v210, v28, v30, s55 bitop3:0x7e\nv_or3_b32 v208, v209, v210, v208\n" \
"v_bitop3_b32 v209, v60, v62, s55 bitop3:0x7e\nv_bitop3_b32 v210, v92, v94, s55 bitop3:0x7e\nv_or3_b32 v208, v209, v210, v208\ns_waitcnt vmcnt(18)\nv_bitop3_b32 v209, v96, v98, s55 bitop3:0x7e\nv_bitop3_b32 v210, v100, v102, s55 bitop3:0x7e\nv_or3_b32 v208, v209, v210, v208\nv_bitop3_b32 v209, v104, v106, s55 bitop3:0x7e\n" \
"v_bitop3_b32 v210, v108, v110, s55 bitop3:0x7e\nv_or3_b32 v208, v209, v210, v208\ns_waitcnt vmcnt(14)\nv_bitop3_b32 v209, v112, v114, s55 bitop3:0x7e\nv_bitop3_b32 v210, v116, v118, s55 bitop3:0x7e\nv_or3_b32 v208, v209, v210, v208\nv_bitop3_b32 v209, v120, v122, s55 bitop3:0x7e\nv_bitop3_b32 v210, v124, v126, s55 bitop3:0x7e\n" \
"v_or3_b32 v208, v209, v210, v208\nv_and_b32 v208, 1, v208\nv_cmp_ne_u32 vcc, 0, v208\ns_cbranch_vccnz .Lrd_yre%=\ns_branch .Lrd_ra%=\n.Lrd_sg1%=:\nbuffer_load_dwordx2 v[214:215], v212, %[rs], 0 offen sc1\ns_waitcnt vmcnt(0)\n" \
"buffer_store_dword v207, off, %[rs], s59 sc1\ns_mov_b32 s59, -2147483648\ns_add_u32 s43, s43, 1\ns_cmp_lt_u32 s43, 262144\ns_cbranch_scc1 .Lrd_sg0%=\ns_branch .Lrd_abort%=\n.Lrd_tw626x%=:\ns_memrealtime s[68:69]\n" \
"s_branch .Lrd_tw626%=\n.Lrd_tw637x%=:\ns_memrealtime s[68:69]\ns_branch .Lrd_tw637%=\n.Lrd_tw650x%=:\ns_memrealtime s[68:69]\ns_branch .Lrd_tw650%=\n.Lrd_fin%=:\n" \
"s_waitcnt vmcnt(0)\nbuffer_store_dword v207, off, %[rs], s59 sc1\ns_mov_b32 s59, -2147483648\ns_branch .Lrd_end%=\n.Lrd_yre%=:\ns_mov_b64 exec, vcc\ns_waitcnt vmcnt(0)\ns_add_u32 s43, s43, 1\n" \
"s_cmp_lt_u32 s43, 262144\ns_cbranch_scc1 .Lrd_yld%=\ns_branch .Lrd_abort%=\n.Lrd_rb%=:\ns_mov_b64 exec, s[46:47]\ns_cbranch_execz .Lrd_rs0%=\nbuffer_load_dwordx4 v[160:163], v196, %[rs], %[sp] offen offset:0 sc1\n.Lrd_rs0%=:\n" \
"s_mov_b64 exec, s[48:49]\ns_cbranch_execz .Lrd_rs1%=\nbuffer_load_dwordx4 v[164:167], v196, %[rs], %[sp] offen offset:1024 sc1\n.Lrd_rs1%=:\ns_mov_b64 exec, s[50:51]\ns_cbranch_execz .Lrd_rs2%=\nbuffer_load_dwordx4 v[168:171], v196, %[rs], %[sp] offen offset:2048 sc1\n.Lrd_rs2%=:\n" \
"s_mov_b64 exec, s[52:53]\ns_cbranch_execz .Lrd_rs3%=\nbuffer_load_dwordx4 v[172:175], v196, %[rs], %[sp] offen offset:3072 sc1\n.Lrd_rs3%=:\ns_mov_b64 exec, s[64:65]\ns_cbranch_execz .Lrd_rs4%=\nbuffer_load_dwordx4 v[176:179], v196, %[rs], s56 offen offset:0 sc1\n.Lrd_rs4%=:\n" \
"s_mov_b64 exec, s[86:87]\ns_cbranch_execz .Lrd_rs5%=\nbuffer_load_dwordx4 v[128:131], v196, %[rs], %[su] offen offset:0 sc1\nbuffer_load_dwordx4 v[136:139], v196, %[rs], %[su] offen offset:2048 sc1\n.Lrd_rs5%=:\ns_mov_b64 exec, s[88:89]\ns_cbranch_execz .Lrd_rs6%=\nbuffer_load_dwordx4 v[144:147], v196, %[rs], s57 offen offset:0 sc1\n" \
"buffer_load_dwordx4 v[152:155], v196, %[rs], s57 offen offset:2048 sc1\n.Lrd_rs6%=:\ns_mov_b64 exec, s[68:69]\ns_cbranch_execz .Lrd_rs7%=\nbuffer_load_dwordx4 v[132:135], v196, %[rs], %[su] offen offset:1024 sc1\nbuffer_load_dwordx4 v[140:143], v196, %[rs], %[su] offen offset:3072 sc1\n.Lrd_rs7%=:\ns_mov_b64 exec, s[62:63]\n" \
"s_cbranch_execz .Lrd_rs8%=\nbuffer_load_dwordx4 v[148:151], v196, %[rs], s57 offen offset:1024 sc1\nbuffer_load_dwordx4 v[156:159], v196, %[rs], s57 offen offset:3072 sc1\n.Lrd_rs8%=:\ns_mov_b64 exec, -1\nv_mov_b64 v[180:181], 0\nv_mov_b64 v[182:183], 0\nv_mov_b64 v[184:185], 0\n" \
"v_mov_b64 v[186:187], 0\nv_mov_b64 v[188:189], 0\nv_mov_b64 v[190:191], 0\nv_mov_b64 v[192:193], 0\nv_mov_b64 v[194:195], 0\ns_waitcnt vmcnt(0)\ns_add_u32 s43, s43, 1\ns_cmp_lt_u32 s43, 262144\n" \
"s_cbranch_scc1 .Lrd_ra%=\ns_branch .Lrd_abort%=\n.Lrd_y1%=:\nv_accvgpr_read_b32 v206, a32\nbuffer_load_dword v208, v206, %[rs], 0 offen sc1\ns_waitcnt vmcnt(0)\nv_cmp_gt_i32 vcc, 1, v208\ns_cbranch_vccz .Lrd_y1r%=\n" \
"s_add_u32 s43, s43, 1\ns_cmp_lt_u32 s43, 262144\ns_cbranch_scc1 .Lrd_y1%=\ns_branch .Lrd_abort%=\n.Lrd_tre0%=:\ns_add_u32 s60, s58, 16384\nbuffer_load_dwordx4 v[212:215], v196, %[rs], s60 offen offset:0 sc1\ns_waitcnt vmcnt(0)\n" \
"v_bitop3_b32 v209, v212, v214, s61 bitop3:0x7e\nv_cmp_gt_i32 vcc, 0, v209\ns_cbranch_vccz .Lrd_tl0%=\ns_add_u32 s43, s43, 1\ns_cmp_lt_u32 s43, 262144\ns_cbranch_scc1 .Lrd_tre0%=\ns_branch .Lrd_abort%=\n.Lrd_tre1%=:\n" \
"s_add_u32 s60, s58, 0\nbuffer_load_dwordx4 v[216:219], v196, %[rs], s60 offen offset:0 sc1\ns_waitcnt vmcnt(0)\nv_bitop3_b32 v209, v216, v218, s61 bitop3:0x7e\nv_cmp_gt_i32 vcc, 0, v209\ns_cbranch_vccz .Lrd_tl1%=\ns_add_u32 s43, s43, 1\ns_cmp_lt_u32 s43, 262144\n" \
"s_cbranch_scc1 .Lrd_tre1%=\ns_branch .Lrd_abort%=\n.Lrd_tre2%=:\ns_add_u32 s60, s58, 0\nbuffer_load_dwordx4 v[220:223], v196, %[rs], s60 offen offset:1024 sc1\ns_waitcnt vmcnt(0)\nv_bitop3_b32 v209, v220, v222, s61 bitop3:0x7e\nv_cmp_gt_i32 vcc, 0, v209\n" \
"s_cbranch_vccz .Lrd_tl2%=\ns_add_u32 s43, s43, 1\ns_cmp_lt_u32 s43, 262144\ns_cbranch_scc1 .Lrd_tre2%=\ns_branch .Lrd_abort%=\n.Lrd_tre3%=:\ns_add_u32 s60, s58, 16384\nbuffer_load_dwordx4 v[224:227], v196, %[rs], s60 offen offset:1024 sc1\n" \
"s_waitcnt vmcnt(0)\nv_bitop3_b32 v209, v224, v226, s61 bitop3:0x7e\nv_cmp_gt_i32 vcc, 0, v209\ns_cbranch_vccz .Lrd_tl3%=\ns_add_u32 s43, s43, 1\ns_cmp_lt_u32 s43, 262144\ns_cbranch_scc1 .Lrd_tre3%=\ns_branch .Lrd_abort%=\n" \
".Lrd_tre4%=:\ns_add_u32 s60, s58, 0\nbuffer_load_dwordx4 v[228:231], v196, %[rs], s60 offen offset:2048 sc1\ns_waitcnt vmcnt(0)\nv_bitop3_b32 v209, v228, v230, s61 bitop3:0x7e\nv_cmp_gt_i32 vcc, 0, v209\ns_cbranch_vccz .Lrd_tl4%=\ns_add_u32 s43, s43, 1\n" \
"s_cmp_lt_u32 s43, 262144\ns_cbranch_scc1 .Lrd_tre4%=\ns_branch .Lrd_abort%=\n.Lrd_tre5%=:\ns_add_u32 s60, s58, 0\nbuffer_load_dwordx4 v[232:235], v196, %[rs], s60 offen offset:3072 sc1\ns_waitcnt vmcnt(0)\nv_bitop3_b32 v209, v232, v234, s61 bitop3:0x7e\n" \
"v_cmp_gt_i32 vcc, 0, v209\ns_cbranch_vccz .Lrd_tl5%=\ns_add_u32 s43, s43, 1\ns_cmp_lt_u32 s43, 262144\ns_cbranch_scc1 .Lrd_tre5%=\ns_branch .Lrd_abort%=\n.Lrd_tre6%=:\ns_add_u32 s60, s58, 16384\n" \
"buffer_load_dwordx4 v[236:239], v196, %[rs], s60 offen offset:2048 sc1\ns_waitcnt vmcnt(0)\nv_bitop3_b32 v209, v236, v238, s61 bitop3:0x7e\nv_cmp_gt_i32 vcc, 0, v209\ns_cbranch_vccz .Lrd_tl6%=\ns_add_u32 s43, s43, 1\ns_cmp_lt_u32 s43, 262144\ns_cbranch_scc1 .Lrd_tre6%=\n" \
"s_branch .Lrd_abort%=\n.Lrd_tre7%=:\ns_add_u32 s60, s58, 4096\nbuffer_load_dwordx4 v[240:243], v196, %[rs], s60 offen offset:0 sc1\ns_waitcnt vmcnt(0)\nv_bitop3_b32 v209, v240, v242, s61 bitop3:0x7e\nv_cmp_gt_i32 vcc, 0, v209\ns_cbranch_vccz .Lrd_tl7%=\n" \
"s_add_u32 s43, s43, 1\ns_cmp_lt_u32 s43, 262144\ns_cbranch_scc1 .Lrd_tre7%=\ns_branch .Lrd_abort%=\n.Lrd_tre8%=:\ns_add_u32 s60, s58, 4096\nbuffer_load_dwordx4 v[244:247], v196, %[rs], s60 offen offset:1024 sc1\ns_waitcnt vmcnt(0)\n" \
"v_bitop3_b32 v209, v244, v246, s61 bitop3:0x7e\nv_cmp_gt_i32 vcc, 0, v209\ns_cbranch_vccz .Lrd_tl8%=\ns_add_u32 s43, s43, 1\ns_cmp_lt_u32 s43, 262144\ns_cbranch_scc1 .Lrd_tre8%=\ns_branch .Lrd_abort%=\n.Lrd_tre9%=:\n" \
"s_add_u32 s60, s58, 16384\nbuffer_load_dwordx4 v[248:251], v196, %[rs], s60 offen offset:3072 sc1\ns_waitcnt vmcnt(0)\nv_bitop3_b32 v209, v248, v250, s61 bitop3:0x7e\nv_cmp_gt_i32 vcc, 0, v209\ns_cbranch_vccz .Lrd_tl9%=\ns_add_u32 s43, s43, 1\ns_cmp_lt_u32 s43, 262144\n" \
"s_cbranch_scc1 .Lrd_tre9%=\ns_branch .Lrd_abort%=\n.Lrd_tre10%=:\ns_add_u32 s60, s58, 20480\nbuffer_load_dwordx4 v[252:255], v196, %[rs], s60 offen offset:0 sc1\ns_waitcnt vmcnt(0)\nv_bitop3_b32 v209, v252, v254, s61 bitop3:0x7e\nv_bitop3_b32 v210, v253, v255, s61 bitop3:0x7e\n" \
"v_or_b32 v209, v209, v210\nv_cmp_gt_i32 vcc, 0, v209\ns_cbranch_vccz .Lrd_tl10%=\ns_add_u32 s43, s43, 1\ns_cmp_lt_u32 s43, 262144\ns_cbranch_scc1 .Lrd_tre10%=\ns_branch .Lrd_abort%=\n.Lrd_tre11%=:\n" \
"s_add_u32 s60, s58, 20480\nbuffer_load_dwordx4 v[160:163], v196, %[rs], s60 offen offset:2048 sc1\ns_waitcnt vmcnt(0)\nv_bitop3_b32 v209, v160, v162, s61 bitop3:0x7e\nv_bitop3_b32 v210, v161, v163, s61 bitop3:0x7e\nv_or_b32 v209, v209, v210\nv_cmp_gt_i32 vcc, 0, v209\ns_cbranch_vccz .Lrd_tl11%=\n" \
"s_add_u32 s43, s43, 1\ns_cmp_lt_u32 s43, 262144\ns_cbranch_scc1 .Lrd_tre11%=\ns_branch .Lrd_abort%=\n.Lrd_tre12%=:\ns_add_u32 s60, s58, 4096\nbuffer_load_dwordx4 v[164:167], v196, %[rs], s60 offen offset:2048 sc1\ns_waitcnt vmcnt(0)\n" \
"v_bitop3_b32 v209, v164, v166, s61 bitop3:0x7e\nv_cmp_gt_i32 vcc, 0, v209\ns_cbranch_vccz .Lrd_tl12%=\ns_add_u32 s43, s43, 1\ns_cmp_lt_u32 s43, 262144\ns_cbranch_scc1 .Lrd_tre12%=\ns_branch .Lrd_abort%=\n.Lrd_tre13%=:\n" \
"s_add_u32 s60, s58, 4096\nbuffer_load_dwordx4 v[168:171], v196, %[rs], s60 offen offset:3072 sc1\ns_waitcnt vmcnt(0)\nv_bitop3_b32 v209, v168, v170, s61 bitop3:0x7e\nv_cmp_gt_i32 vcc, 0, v209\ns_cbranch_vccz .Lrd_tl13%=\ns_add_u32 s43, s43, 1\ns_cmp_lt_u32 s43, 262144\n" \
"s_cbranch_scc1 .Lrd_tre13%=\ns_branch .Lrd_abort%=\n.Lrd_tre14%=:\ns_add_u32 s60, s58, 20480\nbuffer_load_dwordx4 v[172:175], v196, %[rs], s60 offen offset:1024 sc1\ns_waitcnt vmcnt(0)\nv_bitop3_b32 v209, v172, v174, s61 bitop3:0x7e\nv_bitop3_b32 v210, v173, v175, s61 bitop3:0x7e\n" \
"v_or_b32 v209, v209, v210\nv_cmp_gt_i32 vcc, 0, v209\ns_cbranch_vccz .Lrd_tl14%=\ns_add_u32 s43, s43, 1\ns_cmp_lt_u32 s43, 262144\ns_cbranch_scc1 .Lrd_tre14%=\ns_branch .Lrd_abort%=\n.Lrd_tre15%=:\n" \
"s_add_u32 s60, s58, 20480\nbuffer_load_dwordx4 v[176:179], v196, %[rs], s60 offen offset:3072 sc1\ns_waitcnt vmcnt(0)\nv_bitop3_b32 v209, v176, v178, s61 bitop3:0x7e\nv_bitop3_b32 v210, v177, v179, s61 bitop3:0x7e\nv_or_b32 v209, v209, v210\nv_cmp_gt_i32 vcc, 0, v209\ns_cbranch_vccz .Lrd_tl15%=\n" \
"s_add_u32 s43, s43, 1\ns_cmp_lt_u32 s43, 262144\ns_cbranch_scc1 .Lrd_tre15%=\ns_branch .Lrd_abort%=\n.Lrd_abort%=:\ns_mov_b64 exec, -1\nv_mov_b32 v16, 0\ns_movk_i32 s44, 6400\n" \
"buffer_store_dword v16, off, %[rs], s44 sc1\n.Lrd_end%=:\ns_waitcnt vmcnt(0) lgkmcnt(0)\n" \
""
#define GG_ASM \
"v_mbcnt_lo_u32_b32 v255, -1, 0\nv_mbcnt_hi_u32_b32 v255, -1, v255\nv_lshlrev_b32 v255, 2, v255\nv_add_u32 v255, %[tb], v255\nds_read_b32 v0, v255 offset:0\nds_read_b32 v1, v255 offset:256\nds_read_b32 v2, v255 offset:512\nds_read_b32 v3, v255 offset:768\n" \
"ds_read_b32 v4, v255 offset:1024\nds_read_b32 v5, v255 offset:1280\nds_read_b32 v92, v255 offset:1536\nds_read_b32 v93, v255 offset:1792\nds_read_b32 v94, v255 offset:2048\ns_waitcnt lgkmcnt(0)\nv_mov_b32 v6, 0x3f803f80\nv_mov_b32 v7, 0x3f803f80\n" \
"s_add_u32 m0, %[lw], 16384\ns_add_u32 s44, %[wo], 0\nbuffer_load_dwordx4 v92, %[rw], s44 offen nt lds\ns_add_u32 m0, %[lw], 17408\ns_add_u32 s44, %[wo], 57344\nbuffer_load_dwordx4 v92, %[rw], s44 offen nt lds\ns_add_u32 m0, %[lw], 18432\ns_add_u32 s44, %[wo], 114688\n" \
"buffer_load_dwordx4 v92, %[rw], s44 offen nt lds\ns_add_u32 m0, %[lw], 19456\ns_add_u32 s44, %[wo], 172032\nbuffer_load_dwordx4 v92, %[rw], s44 offen nt lds\ns_add_u32 m0, %[lw], 20480\ns_add_u32 s44, %[wo], 229376\nbuffer_load_dwordx4 v92, %[rw], s44 offen nt lds\ns_add_u32 m0, %[lw], 21504\n" \
"s_add_u32 s44, %[wo], 286720\nbuffer_load_dwordx4 v92, %[rw], s44 offen nt lds\ns_add_u32 m0, %[lw], 22528\ns_add_u32 s44, %[wo], 344064\nbuffer_load_dwordx4 v92, %[rw], s44 offen nt lds\ns_add_u32 m0, %[lw], 23552\ns_add_u32 s44, %[wo], 401408\nbuffer_load_dwordx4 v92, %[rw], s44 offen nt lds\n" \
"s_add_u32 m0, %[tb], 0\ns_add_u32 s44, %[wo], 1835008\nbuffer_load_dwordx4 v92, %[rw], s44 offen nt lds\ns_add_u32 m0, %[tb], 1024\ns_add_u32 s44, %[wo], 1892352\nbuffer_load_dwordx4 v92, %[rw], s44 offen nt lds\ns_add_u32 m0, %[tb], 2048\ns_add_u32 s44, %[wo], 1949696\n" \
"buffer_load_dwordx4 v92, %[rw], s44 offen nt lds\ns_add_u32 m0, %[tb], 3072\ns_add_u32 s44, %[wo], 2007040\nbuffer_load_dwordx4 v92, %[rw], s44 offen nt lds\ns_add_u32 m0, %[tb], 4096\ns_add_u32 s44, %[wo], 2064384\nbuffer_load_dwordx4 v92, %[rw], s44 offen nt lds\ns_add_u32 m0, %[tb], 5120\n" \
"s_add_u32 s44, %[wo], 2121728\nbuffer_load_dwordx4 v92, %[rw], s44 offen nt lds\ns_add_u32 m0, %[tb], 6144\ns_add_u32 s44, %[wo], 2179072\nbuffer_load_dwordx4 v92, %[rw], s44 offen nt lds\ns_add_u32 m0, %[tb], 7168\ns_add_u32 s44, %[wo], 2236416\nbuffer_load_dwordx4 v92, %[rw], s44 offen nt lds\n" \
"s_add_u32 m0, %[tb], 8192\ns_add_u32 s44, %[wo], 3670016\nbuffer_load_dwordx4 v92, %[rw], s44 offen nt lds\ns_add_u32 m0, %[tb], 9216\ns_add_u32 s44, %[wo], 3727360\nbuffer_load_dwordx4 v92, %[rw], s44 offen nt lds\ns_add_u32 m0, %[tb], 10240\ns_add_u32 s44, %[wo], 3784704\n" \
"buffer_load_dwordx4 v92, %[rw], s44 offen nt lds\ns_add_u32 m0, %[tb], 11264\ns_add_u32 s44, %[wo], 3842048\nbuffer_load_dwordx4 v92, %[rw], s44 offen nt lds\ns_add_u32 m0, %[lw], 0\ns_add_u32 s44, %[wo], 3899392\nbuffer_load_dwordx4 v92, %[rw], s44 offen nt lds\ns_add_u32 m0, %[lw], 1024\n" \
"s_add_u32 s44, %[wo], 3956736\nbuffer_load_dwordx4 v92, %[rw], s44 offen nt lds\ns_add_u32 m0, %[lw], 2048\ns_add_u32 s44, %[wo], 4014080\nbuffer_load_dwordx4 v92, %[rw], s44 offen nt lds\ns_add_u32 m0, %[lw], 3072\ns_add_u32 s44, %[wo], 4071424\nbuffer_load_dwordx4 v92, %[rw], s44 offen nt lds\n" \
"s_add_u32 m0, %[lw], 4096\ns_add_u32 s44, %[wo], 5505024\nbuffer_load_dwordx4 v92, %[rw], s44 offen nt lds\ns_add_u32 m0, %[lw], 5120\ns_add_u32 s44, %[wo], 5562368\nbuffer_load_dwordx4 v92, %[rw], s44 offen nt lds\ns_add_u32 m0, %[lw], 6144\ns_add_u32 s44, %[wo], 5619712\n" \
"buffer_load_dwordx4 v92, %[rw], s44 offen nt lds\ns_add_u32 m0, %[lw], 7168\ns_add_u32 s44, %[wo], 5677056\nbuffer_load_dwordx4 v92, %[rw], s44 offen nt lds\ns_add_u32 m0, %[lw], 8192\ns_add_u32 s44, %[wo], 5734400\nbuffer_load_dwordx4 v92, %[rw], s44 offen nt lds\ns_add_u32 m0, %[lw], 9216\n" \
"s_add_u32 s44, %[wo], 5791744\nbuffer_load_dwordx4 v92, %[rw], s44 offen nt lds\ns_add_u32 m0, %[lw], 10240\ns_add_u32 s44, %[wo], 5849088\nbuffer_load_dwordx4 v92, %[rw], s44 offen nt lds\ns_add_u32 m0, %[lw], 11264\ns_add_u32 s44, %[wo], 5906432\nbuffer_load_dwordx4 v92, %[rw], s44 offen nt lds\n" \
"s_waitcnt vmcnt(28)\ns_barrier\ns_add_u32 m0, %[lw], 12288\ns_add_u32 s44, %[wo], 458752\nbuffer_load_dwordx4 v92, %[rw], s44 offen nt lds\ns_add_u32 m0, %[lw], 13312\ns_add_u32 s44, %[wo], 516096\nbuffer_load_dwordx4 v92, %[rw], s44 offen nt lds\n" \
"s_add_u32 m0, %[lw], 14336\ns_add_u32 s44, %[wo], 573440\nbuffer_load_dwordx4 v92, %[rw], s44 offen nt lds\ns_add_u32 m0, %[lw], 15360\ns_add_u32 s44, %[wo], 630784\nbuffer_load_dwordx4 v92, %[rw], s44 offen nt lds\nds_read_b64_tr_b16 a[0:1], v94 offset:16384\nds_read_b64_tr_b16 a[2:3], v94 offset:16896\n" \
"ds_read_b64_tr_b16 a[8:9], v94 offset:16392\nds_read_b64_tr_b16 a[10:11], v94 offset:16904\nds_read_b64_tr_b16 a[16:17], v94 offset:40960\nds_read_b64_tr_b16 a[18:19], v94 offset:41472\nds_read_b64_tr_b16 a[24:25], v94 offset:40968\nds_read_b64_tr_b16 a[26:27], v94 offset:41480\ns_waitcnt lgkmcnt(0)\nv_mfma_f32_16x16x16_bf16 v[8:11], a[0:1], v[6:7], 0\n" \
"v_mfma_f32_16x16x16_bf16 v[12:15], a[8:9], v[6:7], 0\nv_mfma_f32_16x16x16_bf16 v[16:19], a[16:17], v[6:7], 0\nv_mfma_f32_16x16x16_bf16 v[20:23], a[24:25], v[6:7], 0\nv_mfma_f32_16x16x16_bf16 v[8:11], a[2:3], v[6:7], v[8:11]\nv_mfma_f32_16x16x16_bf16 v[12:15], a[10:11], v[6:7], v[12:15]\nv_mfma_f32_16x16x16_bf16 v[16:19], a[18:19], v[6:7], v[16:19]\nv_mfma_f32_16x16x16_bf16 v[20:23], a[26:27], v[6:7], v[20:23]\ns_waitcnt vmcnt(28)\n" \
"s_barrier\ns_add_u32 m0, %[lw], 16384\ns_add_u32 s44, %[wo], 688128\nbuffer_load_dwordx4 v92, %[rw], s44 offen nt lds\ns_add_u32 m0, %[lw], 17408\ns_add_u32 s44, %[wo], 745472\nbuffer_load_dwordx4 v92, %[rw], s44 offen nt lds\ns_add_u32 m0, %[lw], 18432\n" \
"s_add_u32 s44, %[wo], 802816\nbuffer_load_dwordx4 v92, %[rw], s44 offen nt lds\ns_add_u32 m0, %[lw], 19456\ns_add_u32 s44, %[wo], 860160\nbuffer_load_dwordx4 v92, %[rw], s44 offen nt lds\nds_read_b64_tr_b16 a[4:5], v94 offset:20480\nds_read_b64_tr_b16 a[6:7], v94 offset:20992\nds_read_b64_tr_b16 a[12:13], v94 offset:20488\n" \
"ds_read_b64_tr_b16 a[14:15], v94 offset:21000\nds_read_b64_tr_b16 a[20:21], v94 offset:45056\nds_read_b64_tr_b16 a[22:23], v94 offset:45568\nds_read_b64_tr_b16 a[28:29], v94 offset:45064\nds_read_b64_tr_b16 a[30:31], v94 offset:45576\ns_waitcnt lgkmcnt(0)\nv_mfma_f32_16x16x16_bf16 v[8:11], a[4:5], v[6:7], v[8:11]\nv_mfma_f32_16x16x16_bf16 v[12:15], a[12:13], v[6:7], v[12:15]\n" \
"v_mfma_f32_16x16x16_bf16 v[16:19], a[20:21], v[6:7], v[16:19]\nv_mfma_f32_16x16x16_bf16 v[20:23], a[28:29], v[6:7], v[20:23]\nv_mfma_f32_16x16x16_bf16 v[8:11], a[6:7], v[6:7], v[8:11]\nv_mfma_f32_16x16x16_bf16 v[12:15], a[14:15], v[6:7], v[12:15]\nv_mfma_f32_16x16x16_bf16 v[16:19], a[22:23], v[6:7], v[16:19]\nv_mfma_f32_16x16x16_bf16 v[20:23], a[30:31], v[6:7], v[20:23]\ns_waitcnt vmcnt(28)\ns_barrier\n" \
"s_add_u32 m0, %[lw], 20480\ns_add_u32 s44, %[wo], 917504\nbuffer_load_dwordx4 v92, %[rw], s44 offen nt lds\ns_add_u32 m0, %[lw], 21504\ns_add_u32 s44, %[wo], 974848\nbuffer_load_dwordx4 v92, %[rw], s44 offen nt lds\ns_add_u32 m0, %[lw], 22528\ns_add_u32 s44, %[wo], 1032192\n" \
"buffer_load_dwordx4 v92, %[rw], s44 offen nt lds\ns_add_u32 m0, %[lw], 23552\ns_add_u32 s44, %[wo], 1089536\nbuffer_load_dwordx4 v92, %[rw], s44 offen nt lds\nds_read_b64_tr_b16 a[104:105], v93 offset:0\nds_read_b64_tr_b16 a[106:107], v93 offset:512\nds_read_b64_tr_b16 a[112:113], v93 offset:8\nds_read_b64_tr_b16 a[114:115], v93 offset:520\n" \
"ds_read_b64_tr_b16 a[120:121], v93 offset:12288\nds_read_b64_tr_b16 a[122:123], v93 offset:12800\nds_read_b64_tr_b16 a[128:129], v93 offset:12296\nds_read_b64_tr_b16 a[130:131], v93 offset:12808\ns_waitcnt lgkmcnt(0)\nv_mfma_f32_16x16x16_bf16 v[8:11], a[104:105], v[6:7], v[8:11]\nv_mfma_f32_16x16x16_bf16 v[12:15], a[112:113], v[6:7], v[12:15]\nv_mfma_f32_16x16x16_bf16 v[16:19], a[120:121], v[6:7], v[16:19]\n" \
"v_mfma_f32_16x16x16_bf16 v[20:23], a[128:129], v[6:7], v[20:23]\nv_mfma_f32_16x16x16_bf16 v[8:11], a[106:107], v[6:7], v[8:11]\nv_mfma_f32_16x16x16_bf16 v[12:15], a[114:115], v[6:7], v[12:15]\nv_mfma_f32_16x16x16_bf16 v[16:19], a[122:123], v[6:7], v[16:19]\nv_mfma_f32_16x16x16_bf16 v[20:23], a[130:131], v[6:7], v[20:23]\ns_waitcnt vmcnt(28)\ns_barrier\ns_add_u32 m0, %[tb], 0\n" \
"s_add_u32 s44, %[wo], 1146880\nbuffer_load_dwordx4 v92, %[rw], s44 offen nt lds\ns_add_u32 m0, %[tb], 1024\ns_add_u32 s44, %[wo], 1204224\nbuffer_load_dwordx4 v92, %[rw], s44 offen nt lds\ns_add_u32 m0, %[tb], 2048\ns_add_u32 s44, %[wo], 1261568\nbuffer_load_dwordx4 v92, %[rw], s44 offen nt lds\n" \
"s_add_u32 m0, %[tb], 3072\ns_add_u32 s44, %[wo], 1318912\nbuffer_load_dwordx4 v92, %[rw], s44 offen nt lds\nds_read_b64_tr_b16 a[108:109], v93 offset:4096\nds_read_b64_tr_b16 a[110:111], v93 offset:4608\nds_read_b64_tr_b16 a[116:117], v93 offset:4104\nds_read_b64_tr_b16 a[118:119], v93 offset:4616\nds_read_b64_tr_b16 a[124:125], v93 offset:16384\n" \
"ds_read_b64_tr_b16 a[126:127], v93 offset:16896\nds_read_b64_tr_b16 a[132:133], v93 offset:16392\nds_read_b64_tr_b16 a[134:135], v93 offset:16904\ns_waitcnt lgkmcnt(0)\nv_mfma_f32_16x16x16_bf16 v[8:11], a[108:109], v[6:7], v[8:11]\nv_mfma_f32_16x16x16_bf16 v[12:15], a[116:117], v[6:7], v[12:15]\nv_mfma_f32_16x16x16_bf16 v[16:19], a[124:125], v[6:7], v[16:19]\nv_mfma_f32_16x16x16_bf16 v[20:23], a[132:133], v[6:7], v[20:23]\n" \
"v_mfma_f32_16x16x16_bf16 v[8:11], a[110:111], v[6:7], v[8:11]\nv_mfma_f32_16x16x16_bf16 v[12:15], a[118:119], v[6:7], v[12:15]\nv_mfma_f32_16x16x16_bf16 v[16:19], a[126:127], v[6:7], v[16:19]\nv_mfma_f32_16x16x16_bf16 v[20:23], a[134:135], v[6:7], v[20:23]\ns_waitcnt vmcnt(28)\ns_barrier\ns_add_u32 m0, %[tb], 4096\ns_add_u32 s44, %[wo], 1376256\n" \
"buffer_load_dwordx4 v92, %[rw], s44 offen nt lds\ns_add_u32 m0, %[tb], 5120\ns_add_u32 s44, %[wo], 1433600\nbuffer_load_dwordx4 v92, %[rw], s44 offen nt lds\ns_add_u32 m0, %[tb], 6144\ns_add_u32 s44, %[wo], 1490944\nbuffer_load_dwordx4 v92, %[rw], s44 offen nt lds\ns_add_u32 m0, %[tb], 7168\n" \
"s_add_u32 s44, %[wo], 1548288\nbuffer_load_dwordx4 v92, %[rw], s44 offen nt lds\nds_read_b64_tr_b16 a[208:209], v93 offset:8192\nds_read_b64_tr_b16 a[210:211], v93 offset:8704\nds_read_b64_tr_b16 a[216:217], v93 offset:8200\nds_read_b64_tr_b16 a[218:219], v93 offset:8712\nds_read_b64_tr_b16 a[224:225], v93 offset:20480\nds_read_b64_tr_b16 a[226:227], v93 offset:20992\n" \
"ds_read_b64_tr_b16 a[232:233], v93 offset:20488\nds_read_b64_tr_b16 a[234:235], v93 offset:21000\ns_waitcnt lgkmcnt(0)\nv_mfma_f32_16x16x16_bf16 v[8:11], a[208:209], v[6:7], v[8:11]\nv_mfma_f32_16x16x16_bf16 v[12:15], a[216:217], v[6:7], v[12:15]\nv_mfma_f32_16x16x16_bf16 v[16:19], a[224:225], v[6:7], v[16:19]\nv_mfma_f32_16x16x16_bf16 v[20:23], a[232:233], v[6:7], v[20:23]\nv_mfma_f32_16x16x16_bf16 v[8:11], a[210:211], v[6:7], v[8:11]\n" \
"v_mfma_f32_16x16x16_bf16 v[12:15], a[218:219], v[6:7], v[12:15]\nv_mfma_f32_16x16x16_bf16 v[16:19], a[226:227], v[6:7], v[16:19]\nv_mfma_f32_16x16x16_bf16 v[20:23], a[234:235], v[6:7], v[20:23]\ns_waitcnt vmcnt(28)\ns_barrier\ns_add_u32 m0, %[tb], 8192\ns_add_u32 s44, %[wo], 1605632\nbuffer_load_dwordx4 v92, %[rw], s44 offen nt lds\n" \
"s_add_u32 m0, %[tb], 9216\ns_add_u32 s44, %[wo], 1662976\nbuffer_load_dwordx4 v92, %[rw], s44 offen nt lds\ns_add_u32 m0, %[tb], 10240\ns_add_u32 s44, %[wo], 1720320\nbuffer_load_dwordx4 v92, %[rw], s44 offen nt lds\ns_add_u32 m0, %[tb], 11264\ns_add_u32 s44, %[wo], 1777664\n" \
"buffer_load_dwordx4 v92, %[rw], s44 offen nt lds\nds_read_b64_tr_b16 a[212:213], v94 offset:0\nds_read_b64_tr_b16 a[214:215], v94 offset:512\nds_read_b64_tr_b16 a[220:221], v94 offset:8\nds_read_b64_tr_b16 a[222:223], v94 offset:520\nds_read_b64_tr_b16 a[228:229], v94 offset:24576\nds_read_b64_tr_b16 a[230:231], v94 offset:25088\nds_read_b64_tr_b16 a[236:237], v94 offset:24584\n" \
"ds_read_b64_tr_b16 a[238:239], v94 offset:25096\ns_waitcnt lgkmcnt(0)\nv_mfma_f32_16x16x16_bf16 v[8:11], a[212:213], v[6:7], v[8:11]\nv_mfma_f32_16x16x16_bf16 v[12:15], a[220:221], v[6:7], v[12:15]\nv_mfma_f32_16x16x16_bf16 v[16:19], a[228:229], v[6:7], v[16:19]\nv_mfma_f32_16x16x16_bf16 v[20:23], a[236:237], v[6:7], v[20:23]\nv_mfma_f32_16x16x16_bf16 v[8:11], a[214:215], v[6:7], v[8:11]\nv_mfma_f32_16x16x16_bf16 v[12:15], a[222:223], v[6:7], v[12:15]\n" \
"v_mfma_f32_16x16x16_bf16 v[16:19], a[230:231], v[6:7], v[16:19]\nv_mfma_f32_16x16x16_bf16 v[20:23], a[238:239], v[6:7], v[20:23]\ns_waitcnt vmcnt(28)\ns_barrier\ns_add_u32 m0, %[lw], 0\ns_add_u32 s44, %[wo], 2293760\nbuffer_load_dwordx4 v92, %[rw], s44 offen nt lds\ns_add_u32 m0, %[lw], 1024\n" \
"s_add_u32 s44, %[wo], 2351104\nbuffer_load_dwordx4 v92, %[rw], s44 offen nt lds\ns_add_u32 m0, %[lw], 2048\ns_add_u32 s44, %[wo], 2408448\nbuffer_load_dwordx4 v92, %[rw], s44 offen nt lds\ns_add_u32 m0, %[lw], 3072\ns_add_u32 s44, %[wo], 2465792\nbuffer_load_dwordx4 v92, %[rw], s44 offen nt lds\n" \
"ds_read_b64_tr_b16 v[152:153], v94 offset:4096\nds_read_b64_tr_b16 v[154:155], v94 offset:4608\nds_read_b64_tr_b16 v[160:161], v94 offset:4104\nds_read_b64_tr_b16 v[162:163], v94 offset:4616\nds_read_b64_tr_b16 v[168:169], v94 offset:28672\nds_read_b64_tr_b16 v[170:171], v94 offset:29184\nds_read_b64_tr_b16 v[176:177], v94 offset:28680\nds_read_b64_tr_b16 v[178:179], v94 offset:29192\n" \
"s_waitcnt lgkmcnt(0)\nv_mfma_f32_16x16x16_bf16 v[8:11], v[152:153], v[6:7], v[8:11]\nv_mfma_f32_16x16x16_bf16 v[12:15], v[160:161], v[6:7], v[12:15]\nv_mfma_f32_16x16x16_bf16 v[16:19], v[168:169], v[6:7], v[16:19]\nv_mfma_f32_16x16x16_bf16 v[20:23], v[176:177], v[6:7], v[20:23]\nv_mfma_f32_16x16x16_bf16 v[8:11], v[154:155], v[6:7], v[8:11]\nv_mfma_f32_16x16x16_bf16 v[12:15], v[162:163], v[6:7], v[12:15]\nv_mfma_f32_16x16x16_bf16 v[16:19], v[170:171], v[6:7], v[16:19]\n" \
"v_mfma_f32_16x16x16_bf16 v[20:23], v[178:179], v[6:7], v[20:23]\ns_waitcnt vmcnt(28)\ns_barrier\ns_add_u32 m0, %[lw], 4096\ns_add_u32 s44, %[wo], 2523136\nbuffer_load_dwordx4 v92, %[rw], s44 offen nt lds\ns_add_u32 m0, %[lw], 5120\ns_add_u32 s44, %[wo], 2580480\n" \
"buffer_load_dwordx4 v92, %[rw], s44 offen nt lds\ns_add_u32 m0, %[lw], 6144\ns_add_u32 s44, %[wo], 2637824\nbuffer_load_dwordx4 v92, %[rw], s44 offen nt lds\ns_add_u32 m0, %[lw], 7168\ns_add_u32 s44, %[wo], 2695168\nbuffer_load_dwordx4 v92, %[rw], s44 offen nt lds\nds_read_b64_tr_b16 v[156:157], v94 offset:8192\n" \
"ds_read_b64_tr_b16 v[158:159], v94 offset:8704\nds_read_b64_tr_b16 v[164:165], v94 offset:8200\nds_read_b64_tr_b16 v[166:167], v94 offset:8712\nds_read_b64_tr_b16 v[172:173], v94 offset:32768\nds_read_b64_tr_b16 v[174:175], v94 offset:33280\nds_read_b64_tr_b16 v[180:181], v94 offset:32776\nds_read_b64_tr_b16 v[182:183], v94 offset:33288\ns_waitcnt lgkmcnt(0)\n" \
"v_mfma_f32_16x16x16_bf16 v[8:11], v[156:157], v[6:7], v[8:11]\nv_mfma_f32_16x16x16_bf16 v[12:15], v[164:165], v[6:7], v[12:15]\nv_mfma_f32_16x16x16_bf16 v[16:19], v[172:173], v[6:7], v[16:19]\nv_mfma_f32_16x16x16_bf16 v[20:23], v[180:181], v[6:7], v[20:23]\nv_mfma_f32_16x16x16_bf16 v[8:11], v[158:159], v[6:7], v[8:11]\nv_mfma_f32_16x16x16_bf16 v[12:15], v[166:167], v[6:7], v[12:15]\nv_mfma_f32_16x16x16_bf16 v[16:19], v[174:175], v[6:7], v[16:19]\nv_mfma_f32_16x16x16_bf16 v[20:23], v[182:183], v[6:7], v[20:23]\n" \
"s_waitcnt vmcnt(28)\ns_barrier\ns_add_u32 m0, %[lw], 8192\ns_add_u32 s44, %[wo], 2752512\nbuffer_load_dwordx4 v92, %[rw], s44 offen nt lds\ns_add_u32 m0, %[lw], 9216\ns_add_u32 s44, %[wo], 2809856\nbuffer_load_dwordx4 v92, %[rw], s44 offen nt lds\n" \
"s_add_u32 m0, %[lw], 10240\ns_add_u32 s44, %[wo], 2867200\nbuffer_load_dwordx4 v92, %[rw], s44 offen nt lds\ns_add_u32 m0, %[lw], 11264\ns_add_u32 s44, %[wo], 2924544\nbuffer_load_dwordx4 v92, %[rw], s44 offen nt lds\nds_read_b64_tr_b16 a[32:33], v94 offset:12288\nds_read_b64_tr_b16 a[34:35], v94 offset:12800\n" \
"ds_read_b64_tr_b16 v[88:89], v94 offset:12296\nds_read_b64_tr_b16 v[90:91], v94 offset:12808\nds_read_b64_tr_b16 a[40:41], v94 offset:36864\nds_read_b64_tr_b16 a[42:43], v94 offset:37376\nds_read_b64_tr_b16 a[48:49], v94 offset:36872\nds_read_b64_tr_b16 a[50:51], v94 offset:37384\ns_waitcnt lgkmcnt(0)\nv_mfma_f32_16x16x16_bf16 v[8:11], a[32:33], v[6:7], v[8:11]\n" \
"v_mfma_f32_16x16x16_bf16 v[12:15], v[88:89], v[6:7], v[12:15]\nv_mfma_f32_16x16x16_bf16 v[16:19], a[40:41], v[6:7], v[16:19]\nv_mfma_f32_16x16x16_bf16 v[20:23], a[48:49], v[6:7], v[20:23]\nv_mfma_f32_16x16x16_bf16 v[8:11], a[34:35], v[6:7], v[8:11]\nv_mfma_f32_16x16x16_bf16 v[12:15], v[90:91], v[6:7], v[12:15]\nv_mfma_f32_16x16x16_bf16 v[16:19], a[42:43], v[6:7], v[16:19]\nv_mfma_f32_16x16x16_bf16 v[20:23], a[50:51], v[6:7], v[20:23]\ns_waitcnt vmcnt(28)\n" \
"s_barrier\ns_add_u32 m0, %[lw], 12288\ns_add_u32 s44, %[wo], 2981888\nbuffer_load_dwordx4 v92, %[rw], s44 offen nt lds\ns_add_u32 m0, %[lw], 13312\ns_add_u32 s44, %[wo], 3039232\nbuffer_load_dwordx4 v92, %[rw], s44 offen nt lds\ns_add_u32 m0, %[lw], 14336\n" \
"s_add_u32 s44, %[wo], 3096576\nbuffer_load_dwordx4 v92, %[rw], s44 offen nt lds\ns_add_u32 m0, %[lw], 15360\ns_add_u32 s44, %[wo], 3153920\nbuffer_load_dwordx4 v92, %[rw], s44 offen nt lds\nds_read_b64_tr_b16 a[36:37], v94 offset:16384\nds_read_b64_tr_b16 a[38:39], v94 offset:16896\nds_read_b64_tr_b16 v[84:85], v94 offset:16392\n" \
"ds_read_b64_tr_b16 v[86:87], v94 offset:16904\nds_read_b64_tr_b16 a[44:45], v94 offset:40960\nds_read_b64_tr_b16 a[46:47], v94 offset:41472\nds_read_b64_tr_b16 a[52:53], v94 offset:40968\nds_read_b64_tr_b16 a[54:55], v94 offset:41480\ns_waitcnt lgkmcnt(0)\nv_mfma_f32_16x16x16_bf16 v[8:11], a[36:37], v[6:7], v[8:11]\nv_mfma_f32_16x16x16_bf16 v[12:15], v[84:85], v[6:7], v[12:15]\n" \
"v_mfma_f32_16x16x16_bf16 v[16:19], a[44:45], v[6:7], v[16:19]\nv_mfma_f32_16x16x16_bf16 v[20:23], a[52:53], v[6:7], v[20:23]\nv_mfma_f32_16x16x16_bf16 v[8:11], a[38:39], v[6:7], v[8:11]\nv_mfma_f32_16x16x16_bf16 v[12:15], v[86:87], v[6:7], v[12:15]\nv_mfma_f32_16x16x16_bf16 v[16:19], a[46:47], v[6:7], v[16:19]\nv_mfma_f32_16x16x16_bf16 v[20:23], a[54:55], v[6:7], v[20:23]\ns_waitcnt vmcnt(28)\ns_barrier\n" \
"s_add_u32 m0, %[lw], 16384\ns_add_u32 s44, %[wo], 3211264\nbuffer_load_dwordx4 v92, %[rw], s44 offen nt lds\ns_add_u32 m0, %[lw], 17408\ns_add_u32 s44, %[wo], 3268608\nbuffer_load_dwordx4 v92, %[rw], s44 offen nt lds\ns_add_u32 m0, %[lw], 18432\ns_add_u32 s44, %[wo], 3325952\n" \
"buffer_load_dwordx4 v92, %[rw], s44 offen nt lds\ns_add_u32 m0, %[lw], 19456\ns_add_u32 s44, %[wo], 3383296\nbuffer_load_dwordx4 v92, %[rw], s44 offen nt lds\nds_read_b64_tr_b16 a[56:57], v94 offset:20480\nds_read_b64_tr_b16 a[58:59], v94 offset:20992\nds_read_b64_tr_b16 a[64:65], v94 offset:20488\nds_read_b64_tr_b16 a[66:67], v94 offset:21000\n" \
"ds_read_b64_tr_b16 v[80:81], v94 offset:45056\nds_read_b64_tr_b16 v[82:83], v94 offset:45568\nds_read_b64_tr_b16 a[72:73], v94 offset:45064\nds_read_b64_tr_b16 a[74:75], v94 offset:45576\ns_waitcnt lgkmcnt(0)\nv_mfma_f32_16x16x16_bf16 v[8:11], a[56:57], v[6:7], v[8:11]\nv_mfma_f32_16x16x16_bf16 v[12:15], a[64:65], v[6:7], v[12:15]\nv_mfma_f32_16x16x16_bf16 v[16:19], v[80:81], v[6:7], v[16:19]\n" \
"v_mfma_f32_16x16x16_bf16 v[20:23], a[72:73], v[6:7], v[20:23]\nv_mfma_f32_16x16x16_bf16 v[8:11], a[58:59], v[6:7], v[8:11]\nv_mfma_f32_16x16x16_bf16 v[12:15], a[66:67], v[6:7], v[12:15]\nv_mfma_f32_16x16x16_bf16 v[16:19], v[82:83], v[6:7], v[16:19]\nv_mfma_f32_16x16x16_bf16 v[20:23], a[74:75], v[6:7], v[20:23]\ns_waitcnt vmcnt(28)\ns_barrier\ns_add_u32 m0, %[lw], 20480\n" \
"s_add_u32 s44, %[wo], 3440640\nbuffer_load_dwordx4 v92, %[rw], s44 offen nt lds\ns_add_u32 m0, %[lw], 21504\ns_add_u32 s44, %[wo], 3497984\nbuffer_load_dwordx4 v92, %[rw], s44 offen nt lds\ns_add_u32 m0, %[lw], 22528\ns_add_u32 s44, %[wo], 3555328\nbuffer_load_dwordx4 v92, %[rw], s44 offen nt lds\n" \
"s_add_u32 m0, %[lw], 23552\ns_add_u32 s44, %[wo], 3612672\nbuffer_load_dwordx4 v92, %[rw], s44 offen nt lds\nds_read_b64_tr_b16 a[60:61], v93 offset:0\nds_read_b64_tr_b16 a[62:63], v93 offset:512\nds_read_b64_tr_b16 a[68:69], v93 offset:8\nds_read_b64_tr_b16 a[70:71], v93 offset:520\nds_read_b64_tr_b16 v[76:77], v93 offset:12288\n" \
"ds_read_b64_tr_b16 v[78:79], v93 offset:12800\nds_read_b64_tr_b16 a[76:77], v93 offset:12296\nds_read_b64_tr_b16 a[78:79], v93 offset:12808\ns_waitcnt lgkmcnt(0)\nv_mfma_f32_16x16x16_bf16 v[8:11], a[60:61], v[6:7], v[8:11]\nv_mfma_f32_16x16x16_bf16 v[12:15], a[68:69], v[6:7], v[12:15]\nv_mfma_f32_16x16x16_bf16 v[16:19], v[76:77], v[6:7], v[16:19]\nv_mfma_f32_16x16x16_bf16 v[20:23], a[76:77], v[6:7], v[20:23]\n" \
"v_mfma_f32_16x16x16_bf16 v[8:11], a[62:63], v[6:7], v[8:11]\nv_mfma_f32_16x16x16_bf16 v[12:15], a[70:71], v[6:7], v[12:15]\nv_mfma_f32_16x16x16_bf16 v[16:19], v[78:79], v[6:7], v[16:19]\nv_mfma_f32_16x16x16_bf16 v[20:23], a[78:79], v[6:7], v[20:23]\ns_waitcnt vmcnt(28)\ns_barrier\ns_add_u32 m0, %[tb], 0\ns_add_u32 s44, %[wo], 4128768\n" \
"buffer_load_dwordx4 v92, %[rw], s44 offen nt lds\ns_add_u32 m0, %[tb], 1024\ns_add_u32 s44, %[wo], 4186112\nbuffer_load_dwordx4 v92, %[rw], s44 offen nt lds\ns_add_u32 m0, %[tb], 2048\ns_add_u32 s44, %[wo], 4243456\nbuffer_load_dwordx4 v92, %[rw], s44 offen nt lds\ns_add_u32 m0, %[tb], 3072\n" \
"s_add_u32 s44, %[wo], 4300800\nbuffer_load_dwordx4 v92, %[rw], s44 offen nt lds\nds_read_b64_tr_b16 a[80:81], v93 offset:4096\nds_read_b64_tr_b16 a[82:83], v93 offset:4608\nds_read_b64_tr_b16 a[88:89], v93 offset:4104\nds_read_b64_tr_b16 a[90:91], v93 offset:4616\nds_read_b64_tr_b16 a[96:97], v93 offset:16384\nds_read_b64_tr_b16 a[98:99], v93 offset:16896\n" \
"ds_read_b64_tr_b16 v[72:73], v93 offset:16392\nds_read_b64_tr_b16 v[74:75], v93 offset:16904\ns_waitcnt lgkmcnt(0)\nv_mfma_f32_16x16x16_bf16 v[8:11], a[80:81], v[6:7], v[8:11]\nv_mfma_f32_16x16x16_bf16 v[12:15], a[88:89], v[6:7], v[12:15]\nv_mfma_f32_16x16x16_bf16 v[16:19], a[96:97], v[6:7], v[16:19]\nv_mfma_f32_16x16x16_bf16 v[20:23], v[72:73], v[6:7], v[20:23]\nv_mfma_f32_16x16x16_bf16 v[8:11], a[82:83], v[6:7], v[8:11]\n" \
"v_mfma_f32_16x16x16_bf16 v[12:15], a[90:91], v[6:7], v[12:15]\nv_mfma_f32_16x16x16_bf16 v[16:19], a[98:99], v[6:7], v[16:19]\nv_mfma_f32_16x16x16_bf16 v[20:23], v[74:75], v[6:7], v[20:23]\ns_waitcnt vmcnt(28)\ns_barrier\ns_add_u32 m0, %[tb], 4096\ns_add_u32 s44, %[wo], 4358144\nbuffer_load_dwordx4 v92, %[rw], s44 offen nt lds\n" \
"s_add_u32 m0, %[tb], 5120\ns_add_u32 s44, %[wo], 4415488\nbuffer_load_dwordx4 v92, %[rw], s44 offen nt lds\ns_add_u32 m0, %[tb], 6144\ns_add_u32 s44, %[wo], 4472832\nbuffer_load_dwordx4 v92, %[rw], s44 offen nt lds\ns_add_u32 m0, %[tb], 7168\ns_add_u32 s44, %[wo], 4530176\n" \
"buffer_load_dwordx4 v92, %[rw], s44 offen nt lds\nds_read_b64_tr_b16 a[84:85], v93 offset:8192\nds_read_b64_tr_b16 a[86:87], v93 offset:8704\nds_read_b64_tr_b16 a[92:93], v93 offset:8200\nds_read_b64_tr_b16 a[94:95], v93 offset:8712\nds_read_b64_tr_b16 a[100:101], v93 offset:20480\nds_read_b64_tr_b16 a[102:103], v93 offset:20992\nds_read_b64_tr_b16 v[68:69], v93 offset:20488\n" \
"ds_read_b64_tr_b16 v[70:71], v93 offset:21000\ns_waitcnt lgkmcnt(0)\nv_mfma_f32_16x16x16_bf16 v[8:11], a[84:85], v[6:7], v[8:11]\nv_mfma_f32_16x16x16_bf16 v[12:15], a[92:93], v[6:7], v[12:15]\nv_mfma_f32_16x16x16_bf16 v[16:19], a[100:101], v[6:7], v[16:19]\nv_mfma_f32_16x16x16_bf16 v[20:23], v[68:69], v[6:7], v[20:23]\nv_mfma_f32_16x16x16_bf16 v[8:11], a[86:87], v[6:7], v[8:11]\nv_mfma_f32_16x16x16_bf16 v[12:15], a[94:95], v[6:7], v[12:15]\n" \
"v_mfma_f32_16x16x16_bf16 v[16:19], a[102:103], v[6:7], v[16:19]\nv_mfma_f32_16x16x16_bf16 v[20:23], v[70:71], v[6:7], v[20:23]\ns_waitcnt vmcnt(28)\ns_barrier\ns_add_u32 m0, %[tb], 8192\ns_add_u32 s44, %[wo], 4587520\nbuffer_load_dwordx4 v92, %[rw], s44 offen nt lds\ns_add_u32 m0, %[tb], 9216\n" \
"s_add_u32 s44, %[wo], 4644864\nbuffer_load_dwordx4 v92, %[rw], s44 offen nt lds\ns_add_u32 m0, %[tb], 10240\ns_add_u32 s44, %[wo], 4702208\nbuffer_load_dwordx4 v92, %[rw], s44 offen nt lds\ns_add_u32 m0, %[tb], 11264\ns_add_u32 s44, %[wo], 4759552\nbuffer_load_dwordx4 v92, %[rw], s44 offen nt lds\n" \
"ds_read_b64_tr_b16 a[136:137], v94 offset:0\nds_read_b64_tr_b16 a[138:139], v94 offset:512\nds_read_b64_tr_b16 v[64:65], v94 offset:8\nds_read_b64_tr_b16 v[66:67], v94 offset:520\nds_read_b64_tr_b16 a[144:145], v94 offset:24576\nds_read_b64_tr_b16 a[146:147], v94 offset:25088\nds_read_b64_tr_b16 a[152:153], v94 offset:24584\nds_read_b64_tr_b16 a[154:155], v94 offset:25096\n" \
"s_waitcnt lgkmcnt(0)\nv_mfma_f32_16x16x16_bf16 v[8:11], a[136:137], v[6:7], v[8:11]\nv_mfma_f32_16x16x16_bf16 v[12:15], v[64:65], v[6:7], v[12:15]\nv_mfma_f32_16x16x16_bf16 v[16:19], a[144:145], v[6:7], v[16:19]\nv_mfma_f32_16x16x16_bf16 v[20:23], a[152:153], v[6:7], v[20:23]\nv_mfma_f32_16x16x16_bf16 v[8:11], a[138:139], v[6:7], v[8:11]\nv_mfma_f32_16x16x16_bf16 v[12:15], v[66:67], v[6:7], v[12:15]\nv_mfma_f32_16x16x16_bf16 v[16:19], a[146:147], v[6:7], v[16:19]\n" \
"v_mfma_f32_16x16x16_bf16 v[20:23], a[154:155], v[6:7], v[20:23]\ns_waitcnt vmcnt(28)\ns_barrier\ns_add_u32 m0, %[lw], 0\ns_add_u32 s44, %[wo], 4816896\nbuffer_load_dwordx4 v92, %[rw], s44 offen nt lds\ns_add_u32 m0, %[lw], 1024\ns_add_u32 s44, %[wo], 4874240\n" \
"buffer_load_dwordx4 v92, %[rw], s44 offen nt lds\ns_add_u32 m0, %[lw], 2048\ns_add_u32 s44, %[wo], 4931584\nbuffer_load_dwordx4 v92, %[rw], s44 offen nt lds\ns_add_u32 m0, %[lw], 3072\ns_add_u32 s44, %[wo], 4988928\nbuffer_load_dwordx4 v92, %[rw], s44 offen nt lds\nds_read_b64_tr_b16 a[140:141], v94 offset:4096\n" \
"ds_read_b64_tr_b16 a[142:143], v94 offset:4608\nds_read_b64_tr_b16 v[60:61], v94 offset:4104\nds_read_b64_tr_b16 v[62:63], v94 offset:4616\nds_read_b64_tr_b16 a[148:149], v94 offset:28672\nds_read_b64_tr_b16 a[150:151], v94 offset:29184\nds_read_b64_tr_b16 a[156:157], v94 offset:28680\nds_read_b64_tr_b16 a[158:159], v94 offset:29192\ns_waitcnt lgkmcnt(0)\n" \
"v_mfma_f32_16x16x16_bf16 v[8:11], a[140:141], v[6:7], v[8:11]\nv_mfma_f32_16x16x16_bf16 v[12:15], v[60:61], v[6:7], v[12:15]\nv_mfma_f32_16x16x16_bf16 v[16:19], a[148:149], v[6:7], v[16:19]\nv_mfma_f32_16x16x16_bf16 v[20:23], a[156:157], v[6:7], v[20:23]\nv_mfma_f32_16x16x16_bf16 v[8:11], a[142:143], v[6:7], v[8:11]\nv_mfma_f32_16x16x16_bf16 v[12:15], v[62:63], v[6:7], v[12:15]\nv_mfma_f32_16x16x16_bf16 v[16:19], a[150:151], v[6:7], v[16:19]\nv_mfma_f32_16x16x16_bf16 v[20:23], a[158:159], v[6:7], v[20:23]\n" \
"s_waitcnt vmcnt(28)\ns_barrier\ns_add_u32 m0, %[lw], 4096\ns_add_u32 s44, %[wo], 5046272\nbuffer_load_dwordx4 v92, %[rw], s44 offen nt lds\ns_add_u32 m0, %[lw], 5120\ns_add_u32 s44, %[wo], 5103616\nbuffer_load_dwordx4 v92, %[rw], s44 offen nt lds\n" \
"s_add_u32 m0, %[lw], 6144\ns_add_u32 s44, %[wo], 5160960\nbuffer_load_dwordx4 v92, %[rw], s44 offen nt lds\ns_add_u32 m0, %[lw], 7168\ns_add_u32 s44, %[wo], 5218304\nbuffer_load_dwordx4 v92, %[rw], s44 offen nt lds\nds_read_b64_tr_b16 a[160:161], v94 offset:8192\nds_read_b64_tr_b16 a[162:163], v94 offset:8704\n" \
"ds_read_b64_tr_b16 a[168:169], v94 offset:8200\nds_read_b64_tr_b16 a[170:171], v94 offset:8712\nds_read_b64_tr_b16 v[56:57], v94 offset:32768\nds_read_b64_tr_b16 v[58:59], v94 offset:33280\nds_read_b64_tr_b16 a[176:177], v94 offset:32776\nds_read_b64_tr_b16 a[178:179], v94 offset:33288\ns_waitcnt lgkmcnt(0)\nv_mfma_f32_16x16x16_bf16 v[8:11], a[160:161], v[6:7], v[8:11]\n" \
"v_mfma_f32_16x16x16_bf16 v[12:15], a[168:169], v[6:7], v[12:15]\nv_mfma_f32_16x16x16_bf16 v[16:19], v[56:57], v[6:7], v[16:19]\nv_mfma_f32_16x16x16_bf16 v[20:23], a[176:177], v[6:7], v[20:23]\nv_mfma_f32_16x16x16_bf16 v[8:11], a[162:163], v[6:7], v[8:11]\nv_mfma_f32_16x16x16_bf16 v[12:15], a[170:171], v[6:7], v[12:15]\nv_mfma_f32_16x16x16_bf16 v[16:19], v[58:59], v[6:7], v[16:19]\nv_mfma_f32_16x16x16_bf16 v[20:23], a[178:179], v[6:7], v[20:23]\ns_waitcnt vmcnt(28)\n" \
"s_barrier\ns_add_u32 m0, %[lw], 8192\ns_add_u32 s44, %[wo], 5275648\nbuffer_load_dwordx4 v92, %[rw], s44 offen nt lds\ns_add_u32 m0, %[lw], 9216\ns_add_u32 s44, %[wo], 5332992\nbuffer_load_dwordx4 v92, %[rw], s44 offen nt lds\ns_add_u32 m0, %[lw], 10240\n" \
"s_add_u32 s44, %[wo], 5390336\nbuffer_load_dwordx4 v92, %[rw], s44 offen nt lds\ns_add_u32 m0, %[lw], 11264\ns_add_u32 s44, %[wo], 5447680\nbuffer_load_dwordx4 v92, %[rw], s44 offen nt lds\nds_read_b64_tr_b16 a[164:165], v94 offset:12288\nds_read_b64_tr_b16 a[166:167], v94 offset:12800\nds_read_b64_tr_b16 a[172:173], v94 offset:12296\n" \
"ds_read_b64_tr_b16 a[174:175], v94 offset:12808\nds_read_b64_tr_b16 v[52:53], v94 offset:36864\nds_read_b64_tr_b16 v[54:55], v94 offset:37376\nds_read_b64_tr_b16 a[180:181], v94 offset:36872\nds_read_b64_tr_b16 a[182:183], v94 offset:37384\ns_waitcnt lgkmcnt(0)\nv_mfma_f32_16x16x16_bf16 v[8:11], a[164:165], v[6:7], v[8:11]\nv_mfma_f32_16x16x16_bf16 v[12:15], a[172:173], v[6:7], v[12:15]\n" \
"v_mfma_f32_16x16x16_bf16 v[16:19], v[52:53], v[6:7], v[16:19]\nv_mfma_f32_16x16x16_bf16 v[20:23], a[180:181], v[6:7], v[20:23]\nv_mfma_f32_16x16x16_bf16 v[8:11], a[166:167], v[6:7], v[8:11]\nv_mfma_f32_16x16x16_bf16 v[12:15], a[174:175], v[6:7], v[12:15]\nv_mfma_f32_16x16x16_bf16 v[16:19], v[54:55], v[6:7], v[16:19]\nv_mfma_f32_16x16x16_bf16 v[20:23], a[182:183], v[6:7], v[20:23]\ns_waitcnt vmcnt(28)\ns_barrier\n" \
"s_add_u32 m0, %[lw], 12288\ns_add_u32 s44, %[wo], 5963776\nbuffer_load_dwordx4 v92, %[rw], s44 offen nt lds\ns_add_u32 m0, %[lw], 13312\ns_add_u32 s44, %[wo], 6021120\nbuffer_load_dwordx4 v92, %[rw], s44 offen nt lds\ns_add_u32 m0, %[lw], 14336\ns_add_u32 s44, %[wo], 6078464\n" \
"buffer_load_dwordx4 v92, %[rw], s44 offen nt lds\ns_add_u32 m0, %[lw], 15360\ns_add_u32 s44, %[wo], 6135808\nbuffer_load_dwordx4 v92, %[rw], s44 offen nt lds\nds_read_b64_tr_b16 a[184:185], v94 offset:16384\nds_read_b64_tr_b16 a[186:187], v94 offset:16896\nds_read_b64_tr_b16 a[192:193], v94 offset:16392\nds_read_b64_tr_b16 a[194:195], v94 offset:16904\n" \
"ds_read_b64_tr_b16 a[200:201], v94 offset:40960\nds_read_b64_tr_b16 a[202:203], v94 offset:41472\nds_read_b64_tr_b16 v[48:49], v94 offset:40968\nds_read_b64_tr_b16 v[50:51], v94 offset:41480\ns_waitcnt lgkmcnt(0)\nv_mfma_f32_16x16x16_bf16 v[8:11], a[184:185], v[6:7], v[8:11]\nv_mfma_f32_16x16x16_bf16 v[12:15], a[192:193], v[6:7], v[12:15]\nv_mfma_f32_16x16x16_bf16 v[16:19], a[200:201], v[6:7], v[16:19]\n" \
"v_mfma_f32_16x16x16_bf16 v[20:23], v[48:49], v[6:7], v[20:23]\nv_mfma_f32_16x16x16_bf16 v[8:11], a[186:187], v[6:7], v[8:11]\nv_mfma_f32_16x16x16_bf16 v[12:15], a[194:195], v[6:7], v[12:15]\nv_mfma_f32_16x16x16_bf16 v[16:19], a[202:203], v[6:7], v[16:19]\nv_mfma_f32_16x16x16_bf16 v[20:23], v[50:51], v[6:7], v[20:23]\ns_waitcnt vmcnt(28)\ns_barrier\ns_add_u32 m0, %[lw], 16384\n" \
"s_add_u32 s44, %[wo], 6193152\nbuffer_load_dwordx4 v92, %[rw], s44 offen nt lds\ns_add_u32 m0, %[lw], 17408\ns_add_u32 s44, %[wo], 6250496\nbuffer_load_dwordx4 v92, %[rw], s44 offen nt lds\ns_add_u32 m0, %[lw], 18432\ns_add_u32 s44, %[wo], 6307840\nbuffer_load_dwordx4 v92, %[rw], s44 offen nt lds\n" \
"s_add_u32 m0, %[lw], 19456\ns_add_u32 s44, %[wo], 6365184\nbuffer_load_dwordx4 v92, %[rw], s44 offen nt lds\nds_read_b64_tr_b16 a[188:189], v94 offset:20480\nds_read_b64_tr_b16 a[190:191], v94 offset:20992\nds_read_b64_tr_b16 a[196:197], v94 offset:20488\nds_read_b64_tr_b16 a[198:199], v94 offset:21000\nds_read_b64_tr_b16 a[204:205], v94 offset:45056\n" \
"ds_read_b64_tr_b16 a[206:207], v94 offset:45568\nds_read_b64_tr_b16 v[44:45], v94 offset:45064\nds_read_b64_tr_b16 v[46:47], v94 offset:45576\ns_waitcnt lgkmcnt(0)\nv_mfma_f32_16x16x16_bf16 v[8:11], a[188:189], v[6:7], v[8:11]\nv_mfma_f32_16x16x16_bf16 v[12:15], a[196:197], v[6:7], v[12:15]\nv_mfma_f32_16x16x16_bf16 v[16:19], a[204:205], v[6:7], v[16:19]\nv_mfma_f32_16x16x16_bf16 v[20:23], v[44:45], v[6:7], v[20:23]\n" \
"v_mfma_f32_16x16x16_bf16 v[8:11], a[190:191], v[6:7], v[8:11]\nv_mfma_f32_16x16x16_bf16 v[12:15], a[198:199], v[6:7], v[12:15]\nv_mfma_f32_16x16x16_bf16 v[16:19], a[206:207], v[6:7], v[16:19]\nv_mfma_f32_16x16x16_bf16 v[20:23], v[46:47], v[6:7], v[20:23]\ns_waitcnt vmcnt(28)\ns_barrier\ns_add_u32 m0, %[lw], 20480\ns_add_u32 s44, %[wo], 6422528\n" \
"buffer_load_dwordx4 v92, %[rw], s44 offen nt lds\ns_add_u32 m0, %[lw], 21504\ns_add_u32 s44, %[wo], 6479872\nbuffer_load_dwordx4 v92, %[rw], s44 offen nt lds\ns_add_u32 m0, %[lw], 22528\ns_add_u32 s44, %[wo], 6537216\nbuffer_load_dwordx4 v92, %[rw], s44 offen nt lds\ns_add_u32 m0, %[lw], 23552\n" \
"s_add_u32 s44, %[wo], 6594560\nbuffer_load_dwordx4 v92, %[rw], s44 offen nt lds\nds_read_b64_tr_b16 a[240:241], v93 offset:0\nds_read_b64_tr_b16 a[242:243], v93 offset:512\nds_read_b64_tr_b16 v[40:41], v93 offset:8\nds_read_b64_tr_b16 v[42:43], v93 offset:520\nds_read_b64_tr_b16 a[248:249], v93 offset:12288\nds_read_b64_tr_b16 a[250:251], v93 offset:12800\n" \
"ds_read_b64_tr_b16 v[96:97], v93 offset:12296\nds_read_b64_tr_b16 v[98:99], v93 offset:12808\ns_waitcnt lgkmcnt(0)\nv_mfma_f32_16x16x16_bf16 v[8:11], a[240:241], v[6:7], v[8:11]\nv_mfma_f32_16x16x16_bf16 v[12:15], v[40:41], v[6:7], v[12:15]\nv_mfma_f32_16x16x16_bf16 v[16:19], a[248:249], v[6:7], v[16:19]\nv_mfma_f32_16x16x16_bf16 v[20:23], v[96:97], v[6:7], v[20:23]\nv_mfma_f32_16x16x16_bf16 v[8:11], a[242:243], v[6:7], v[8:11]\n" \
"v_mfma_f32_16x16x16_bf16 v[12:15], v[42:43], v[6:7], v[12:15]\nv_mfma_f32_16x16x16_bf16 v[16:19], a[250:251], v[6:7], v[16:19]\nv_mfma_f32_16x16x16_bf16 v[20:23], v[98:99], v[6:7], v[20:23]\ns_waitcnt vmcnt(28)\ns_barrier\ns_add_u32 m0, %[tb], 0\ns_add_u32 s44, %[wo], 6651904\nbuffer_load_dwordx4 v92, %[rw], s44 offen nt lds\n" \
"s_add_u32 m0, %[tb], 1024\ns_add_u32 s44, %[wo], 6709248\nbuffer_load_dwordx4 v92, %[rw], s44 offen nt lds\ns_add_u32 m0, %[tb], 2048\ns_add_u32 s44, %[wo], 6766592\nbuffer_load_dwordx4 v92, %[rw], s44 offen nt lds\ns_add_u32 m0, %[tb], 3072\ns_add_u32 s44, %[wo], 6823936\n" \
"buffer_load_dwordx4 v92, %[rw], s44 offen nt lds\nds_read_b64_tr_b16 a[244:245], v93 offset:4096\nds_read_b64_tr_b16 a[246:247], v93 offset:4608\nds_read_b64_tr_b16 v[36:37], v93 offset:4104\nds_read_b64_tr_b16 v[38:39], v93 offset:4616\nds_read_b64_tr_b16 a[252:253], v93 offset:16384\nds_read_b64_tr_b16 a[254:255], v93 offset:16896\nds_read_b64_tr_b16 v[100:101], v93 offset:16392\n" \
"ds_read_b64_tr_b16 v[102:103], v93 offset:16904\ns_waitcnt lgkmcnt(0)\nv_mfma_f32_16x16x16_bf16 v[8:11], a[244:245], v[6:7], v[8:11]\nv_mfma_f32_16x16x16_bf16 v[12:15], v[36:37], v[6:7], v[12:15]\nv_mfma_f32_16x16x16_bf16 v[16:19], a[252:253], v[6:7], v[16:19]\nv_mfma_f32_16x16x16_bf16 v[20:23], v[100:101], v[6:7], v[20:23]\nv_mfma_f32_16x16x16_bf16 v[8:11], a[246:247], v[6:7], v[8:11]\nv_mfma_f32_16x16x16_bf16 v[12:15], v[38:39], v[6:7], v[12:15]\n" \
"v_mfma_f32_16x16x16_bf16 v[16:19], a[254:255], v[6:7], v[16:19]\nv_mfma_f32_16x16x16_bf16 v[20:23], v[102:103], v[6:7], v[20:23]\ns_waitcnt vmcnt(28)\ns_barrier\ns_add_u32 m0, %[tb], 4096\ns_add_u32 s44, %[wo], 6881280\nbuffer_load_dwordx4 v92, %[rw], s44 offen nt lds\ns_add_u32 m0, %[tb], 5120\n" \
"s_add_u32 s44, %[wo], 6938624\nbuffer_load_dwordx4 v92, %[rw], s44 offen nt lds\ns_add_u32 m0, %[tb], 6144\ns_add_u32 s44, %[wo], 6995968\nbuffer_load_dwordx4 v92, %[rw], s44 offen nt lds\ns_add_u32 m0, %[tb], 7168\ns_add_u32 s44, %[wo], 7053312\nbuffer_load_dwordx4 v92, %[rw], s44 offen nt lds\n" \
"ds_read_b64_tr_b16 v[104:105], v93 offset:8192\nds_read_b64_tr_b16 v[106:107], v93 offset:8704\nds_read_b64_tr_b16 v[112:113], v93 offset:8200\nds_read_b64_tr_b16 v[114:115], v93 offset:8712\nds_read_b64_tr_b16 v[32:33], v93 offset:20480\nds_read_b64_tr_b16 v[34:35], v93 offset:20992\nds_read_b64_tr_b16 v[120:121], v93 offset:20488\nds_read_b64_tr_b16 v[122:123], v93 offset:21000\n" \
"s_waitcnt lgkmcnt(0)\nv_mfma_f32_16x16x16_bf16 v[8:11], v[104:105], v[6:7], v[8:11]\nv_mfma_f32_16x16x16_bf16 v[12:15], v[112:113], v[6:7], v[12:15]\nv_mfma_f32_16x16x16_bf16 v[16:19], v[32:33], v[6:7], v[16:19]\nv_mfma_f32_16x16x16_bf16 v[20:23], v[120:121], v[6:7], v[20:23]\nv_mfma_f32_16x16x16_bf16 v[8:11], v[106:107], v[6:7], v[8:11]\nv_mfma_f32_16x16x16_bf16 v[12:15], v[114:115], v[6:7], v[12:15]\nv_mfma_f32_16x16x16_bf16 v[16:19], v[34:35], v[6:7], v[16:19]\n" \
"v_mfma_f32_16x16x16_bf16 v[20:23], v[122:123], v[6:7], v[20:23]\ns_waitcnt vmcnt(28)\ns_barrier\ns_add_u32 m0, %[tb], 8192\ns_add_u32 s44, %[wo], 7110656\nbuffer_load_dwordx4 v92, %[rw], s44 offen nt lds\ns_add_u32 m0, %[tb], 9216\ns_add_u32 s44, %[wo], 7168000\n" \
"buffer_load_dwordx4 v92, %[rw], s44 offen nt lds\ns_add_u32 m0, %[tb], 10240\ns_add_u32 s44, %[wo], 7225344\nbuffer_load_dwordx4 v92, %[rw], s44 offen nt lds\ns_add_u32 m0, %[tb], 11264\ns_add_u32 s44, %[wo], 7282688\nbuffer_load_dwordx4 v92, %[rw], s44 offen nt lds\nds_read_b64_tr_b16 v[108:109], v94 offset:0\n" \
"ds_read_b64_tr_b16 v[110:111], v94 offset:512\nds_read_b64_tr_b16 v[116:117], v94 offset:8\nds_read_b64_tr_b16 v[118:119], v94 offset:520\nds_read_b64_tr_b16 v[28:29], v94 offset:24576\nds_read_b64_tr_b16 v[30:31], v94 offset:25088\nds_read_b64_tr_b16 v[124:125], v94 offset:24584\nds_read_b64_tr_b16 v[126:127], v94 offset:25096\ns_waitcnt lgkmcnt(0)\n" \
"v_mfma_f32_16x16x16_bf16 v[8:11], v[108:109], v[6:7], v[8:11]\nv_mfma_f32_16x16x16_bf16 v[12:15], v[116:117], v[6:7], v[12:15]\nv_mfma_f32_16x16x16_bf16 v[16:19], v[28:29], v[6:7], v[16:19]\nv_mfma_f32_16x16x16_bf16 v[20:23], v[124:125], v[6:7], v[20:23]\nv_mfma_f32_16x16x16_bf16 v[8:11], v[110:111], v[6:7], v[8:11]\nv_mfma_f32_16x16x16_bf16 v[12:15], v[118:119], v[6:7], v[12:15]\nv_mfma_f32_16x16x16_bf16 v[16:19], v[30:31], v[6:7], v[16:19]\nv_mfma_f32_16x16x16_bf16 v[20:23], v[126:127], v[6:7], v[20:23]\n" \
"s_waitcnt vmcnt(28)\ns_barrier\nds_read_b64_tr_b16 v[128:129], v94 offset:4096\nds_read_b64_tr_b16 v[130:131], v94 offset:4608\nds_read_b64_tr_b16 v[136:137], v94 offset:4104\nds_read_b64_tr_b16 v[138:139], v94 offset:4616\nds_read_b64_tr_b16 v[144:145], v94 offset:28672\nds_read_b64_tr_b16 v[146:147], v94 offset:29184\n" \
"ds_read_b64_tr_b16 v[24:25], v94 offset:28680\nds_read_b64_tr_b16 v[26:27], v94 offset:29192\ns_waitcnt lgkmcnt(0)\nv_mfma_f32_16x16x16_bf16 v[8:11], v[128:129], v[6:7], v[8:11]\nv_mfma_f32_16x16x16_bf16 v[12:15], v[136:137], v[6:7], v[12:15]\nv_mfma_f32_16x16x16_bf16 v[16:19], v[144:145], v[6:7], v[16:19]\nv_mfma_f32_16x16x16_bf16 v[20:23], v[24:25], v[6:7], v[20:23]\nv_mfma_f32_16x16x16_bf16 v[8:11], v[130:131], v[6:7], v[8:11]\n" \
"v_mfma_f32_16x16x16_bf16 v[12:15], v[138:139], v[6:7], v[12:15]\nv_mfma_f32_16x16x16_bf16 v[16:19], v[146:147], v[6:7], v[16:19]\nv_mfma_f32_16x16x16_bf16 v[20:23], v[26:27], v[6:7], v[20:23]\ns_waitcnt vmcnt(24)\nds_write_b128 v1, v[88:91] offset:0\nds_write_b128 v1, v[84:87] offset:1024\nds_write_b128 v1, v[80:83] offset:2048\nds_write_b128 v1, v[76:79] offset:3072\n" \
"s_barrier\nds_read_b64_tr_b16 v[132:133], v94 offset:8192\nds_read_b64_tr_b16 v[134:135], v94 offset:8704\nds_read_b64_tr_b16 v[140:141], v94 offset:8200\nds_read_b64_tr_b16 v[142:143], v94 offset:8712\nds_read_b64_tr_b16 v[148:149], v94 offset:32768\nds_read_b64_tr_b16 v[150:151], v94 offset:33280\nds_read_b64_tr_b16 v[76:77], v94 offset:32776\n" \
"ds_read_b64_tr_b16 v[78:79], v94 offset:33288\ns_waitcnt lgkmcnt(0)\nv_mfma_f32_16x16x16_bf16 v[8:11], v[132:133], v[6:7], v[8:11]\nv_mfma_f32_16x16x16_bf16 v[12:15], v[140:141], v[6:7], v[12:15]\nv_mfma_f32_16x16x16_bf16 v[16:19], v[148:149], v[6:7], v[16:19]\nv_mfma_f32_16x16x16_bf16 v[20:23], v[76:77], v[6:7], v[20:23]\nv_mfma_f32_16x16x16_bf16 v[8:11], v[134:135], v[6:7], v[8:11]\nv_mfma_f32_16x16x16_bf16 v[12:15], v[142:143], v[6:7], v[12:15]\n" \
"v_mfma_f32_16x16x16_bf16 v[16:19], v[150:151], v[6:7], v[16:19]\nv_mfma_f32_16x16x16_bf16 v[20:23], v[78:79], v[6:7], v[20:23]\ns_waitcnt vmcnt(20)\nds_write_b128 v1, v[72:75] offset:4096\nds_write_b128 v1, v[68:71] offset:5120\nds_write_b128 v1, v[64:67] offset:6144\nds_write_b128 v1, v[60:63] offset:7168\ns_barrier\n" \
"ds_read_b64_tr_b16 v[184:185], v94 offset:12288\nds_read_b64_tr_b16 v[186:187], v94 offset:12800\nds_read_b64_tr_b16 v[60:61], v94 offset:12296\nds_read_b64_tr_b16 v[62:63], v94 offset:12808\nds_read_b64_tr_b16 v[192:193], v94 offset:36864\nds_read_b64_tr_b16 v[194:195], v94 offset:37376\nds_read_b64_tr_b16 v[200:201], v94 offset:36872\nds_read_b64_tr_b16 v[202:203], v94 offset:37384\n" \
"s_waitcnt lgkmcnt(0)\nv_mfma_f32_16x16x16_bf16 v[8:11], v[184:185], v[6:7], v[8:11]\nv_mfma_f32_16x16x16_bf16 v[12:15], v[60:61], v[6:7], v[12:15]\nv_mfma_f32_16x16x16_bf16 v[16:19], v[192:193], v[6:7], v[16:19]\nv_mfma_f32_16x16x16_bf16 v[20:23], v[200:201], v[6:7], v[20:23]\nv_mfma_f32_16x16x16_bf16 v[8:11], v[186:187], v[6:7], v[8:11]\nv_mfma_f32_16x16x16_bf16 v[12:15], v[62:63], v[6:7], v[12:15]\nv_mfma_f32_16x16x16_bf16 v[16:19], v[194:195], v[6:7], v[16:19]\n" \
"v_mfma_f32_16x16x16_bf16 v[20:23], v[202:203], v[6:7], v[20:23]\ns_waitcnt vmcnt(16)\nds_write_b128 v1, v[56:59] offset:8192\nds_write_b128 v1, v[52:55] offset:9216\nds_write_b128 v1, v[48:51] offset:10240\nds_write_b128 v1, v[44:47] offset:11264\ns_barrier\nds_read_b64_tr_b16 v[188:189], v94 offset:16384\n" \
"ds_read_b64_tr_b16 v[190:191], v94 offset:16896\nds_read_b64_tr_b16 v[44:45], v94 offset:16392\nds_read_b64_tr_b16 v[46:47], v94 offset:16904\nds_read_b64_tr_b16 v[196:197], v94 offset:40960\nds_read_b64_tr_b16 v[198:199], v94 offset:41472\nds_read_b64_tr_b16 v[204:205], v94 offset:40968\nds_read_b64_tr_b16 v[206:207], v94 offset:41480\ns_waitcnt lgkmcnt(0)\n" \
"v_mfma_f32_16x16x16_bf16 v[8:11], v[188:189], v[6:7], v[8:11]\nv_mfma_f32_16x16x16_bf16 v[12:15], v[44:45], v[6:7], v[12:15]\nv_mfma_f32_16x16x16_bf16 v[16:19], v[196:197], v[6:7], v[16:19]\nv_mfma_f32_16x16x16_bf16 v[20:23], v[204:205], v[6:7], v[20:23]\nv_mfma_f32_16x16x16_bf16 v[8:11], v[190:191], v[6:7], v[8:11]\nv_mfma_f32_16x16x16_bf16 v[12:15], v[46:47], v[6:7], v[12:15]\nv_mfma_f32_16x16x16_bf16 v[16:19], v[198:199], v[6:7], v[16:19]\nv_mfma_f32_16x16x16_bf16 v[20:23], v[206:207], v[6:7], v[20:23]\n" \
"s_waitcnt vmcnt(12)\nds_write_b128 v1, v[40:43] offset:12288\nds_write_b128 v1, v[36:39] offset:13312\nds_write_b128 v1, v[32:35] offset:14336\nds_write_b128 v1, v[28:31] offset:15360\ns_barrier\nds_read_b64_tr_b16 v[208:209], v94 offset:20480\nds_read_b64_tr_b16 v[210:211], v94 offset:20992\n" \
"ds_read_b64_tr_b16 v[216:217], v94 offset:20488\nds_read_b64_tr_b16 v[218:219], v94 offset:21000\nds_read_b64_tr_b16 v[28:29], v94 offset:45056\nds_read_b64_tr_b16 v[30:31], v94 offset:45568\nds_read_b64_tr_b16 v[224:225], v94 offset:45064\nds_read_b64_tr_b16 v[226:227], v94 offset:45576\ns_waitcnt lgkmcnt(0)\nv_mfma_f32_16x16x16_bf16 v[8:11], v[208:209], v[6:7], v[8:11]\n" \
"v_mfma_f32_16x16x16_bf16 v[12:15], v[216:217], v[6:7], v[12:15]\nv_mfma_f32_16x16x16_bf16 v[16:19], v[28:29], v[6:7], v[16:19]\nv_mfma_f32_16x16x16_bf16 v[20:23], v[224:225], v[6:7], v[20:23]\nv_mfma_f32_16x16x16_bf16 v[8:11], v[210:211], v[6:7], v[8:11]\nv_mfma_f32_16x16x16_bf16 v[12:15], v[218:219], v[6:7], v[12:15]\nv_mfma_f32_16x16x16_bf16 v[16:19], v[30:31], v[6:7], v[16:19]\nv_mfma_f32_16x16x16_bf16 v[20:23], v[226:227], v[6:7], v[20:23]\ns_waitcnt vmcnt(8)\n" \
"ds_write_b128 v1, v[24:27] offset:16384\nds_write_b128 v1, v[76:79] offset:17408\nds_write_b128 v1, v[60:63] offset:18432\nds_write_b128 v1, v[44:47] offset:19456\ns_barrier\nds_read_b64_tr_b16 v[212:213], v93 offset:0\nds_read_b64_tr_b16 v[214:215], v93 offset:512\nds_read_b64_tr_b16 v[220:221], v93 offset:8\n" \
"ds_read_b64_tr_b16 v[222:223], v93 offset:520\nds_read_b64_tr_b16 v[44:45], v93 offset:12288\nds_read_b64_tr_b16 v[46:47], v93 offset:12800\nds_read_b64_tr_b16 v[228:229], v93 offset:12296\nds_read_b64_tr_b16 v[230:231], v93 offset:12808\ns_waitcnt lgkmcnt(0)\nv_mfma_f32_16x16x16_bf16 v[8:11], v[212:213], v[6:7], v[8:11]\nv_mfma_f32_16x16x16_bf16 v[12:15], v[220:221], v[6:7], v[12:15]\n" \
"v_mfma_f32_16x16x16_bf16 v[16:19], v[44:45], v[6:7], v[16:19]\nv_mfma_f32_16x16x16_bf16 v[20:23], v[228:229], v[6:7], v[20:23]\nv_mfma_f32_16x16x16_bf16 v[8:11], v[214:215], v[6:7], v[8:11]\nv_mfma_f32_16x16x16_bf16 v[12:15], v[222:223], v[6:7], v[12:15]\nv_mfma_f32_16x16x16_bf16 v[16:19], v[46:47], v[6:7], v[16:19]\nv_mfma_f32_16x16x16_bf16 v[20:23], v[230:231], v[6:7], v[20:23]\ns_waitcnt vmcnt(4)\nds_write_b128 v1, v[28:31] offset:20480\n" \
"ds_write_b128 v1, v[44:47] offset:21504\ns_barrier\nds_read_b64_tr_b16 v[232:233], v93 offset:4096\nds_read_b64_tr_b16 v[234:235], v93 offset:4608\nds_read_b64_tr_b16 v[240:241], v93 offset:4104\nds_read_b64_tr_b16 v[242:243], v93 offset:4616\nds_read_b64_tr_b16 v[248:249], v93 offset:16384\nds_read_b64_tr_b16 v[250:251], v93 offset:16896\n" \
"ds_read_b64_tr_b16 v[44:45], v93 offset:16392\nds_read_b64_tr_b16 v[46:47], v93 offset:16904\ns_waitcnt lgkmcnt(0)\nv_mfma_f32_16x16x16_bf16 v[8:11], v[232:233], v[6:7], v[8:11]\nv_mfma_f32_16x16x16_bf16 v[12:15], v[240:241], v[6:7], v[12:15]\nv_mfma_f32_16x16x16_bf16 v[16:19], v[248:249], v[6:7], v[16:19]\nv_mfma_f32_16x16x16_bf16 v[20:23], v[44:45], v[6:7], v[20:23]\nv_mfma_f32_16x16x16_bf16 v[8:11], v[234:235], v[6:7], v[8:11]\n" \
"v_mfma_f32_16x16x16_bf16 v[12:15], v[242:243], v[6:7], v[12:15]\nv_mfma_f32_16x16x16_bf16 v[16:19], v[250:251], v[6:7], v[16:19]\nv_mfma_f32_16x16x16_bf16 v[20:23], v[46:47], v[6:7], v[20:23]\ns_waitcnt vmcnt(0)\nds_write_b128 v1, v[44:47] offset:22528\ns_barrier\nds_read_b64_tr_b16 v[236:237], v93 offset:8192\nds_read_b64_tr_b16 v[238:239], v93 offset:8704\n" \
"ds_read_b64_tr_b16 v[244:245], v93 offset:8200\nds_read_b64_tr_b16 v[246:247], v93 offset:8712\nds_read_b64_tr_b16 v[252:253], v93 offset:20480\nds_read_b64_tr_b16 v[254:255], v93 offset:20992\nds_read_b64_tr_b16 v[44:45], v93 offset:20488\nds_read_b64_tr_b16 v[46:47], v93 offset:21000\ns_waitcnt lgkmcnt(0)\nv_mfma_f32_16x16x16_bf16 v[8:11], v[236:237], v[6:7], v[8:11]\n" \
"v_mfma_f32_16x16x16_bf16 v[12:15], v[244:245], v[6:7], v[12:15]\nv_mfma_f32_16x16x16_bf16 v[16:19], v[252:253], v[6:7], v[16:19]\nv_mfma_f32_16x16x16_bf16 v[20:23], v[44:45], v[6:7], v[20:23]\nv_mfma_f32_16x16x16_bf16 v[8:11], v[238:239], v[6:7], v[8:11]\nv_mfma_f32_16x16x16_bf16 v[12:15], v[246:247], v[6:7], v[12:15]\nv_mfma_f32_16x16x16_bf16 v[16:19], v[254:255], v[6:7], v[16:19]\nv_mfma_f32_16x16x16_bf16 v[20:23], v[46:47], v[6:7], v[20:23]\nds_write_b128 v1, v[44:47] offset:23552\n" \
"s_waitcnt lgkmcnt(0)\ns_barrier\ns_mov_b32 s40, 0\ns_mov_b32 s43, 0\ns_lshl_b32 s57, %[w], 12\ns_mov_b32 s58, 0xbeeff558\ns_mov_b32 s59, 0x409d30fc\ns_mov_b32 s56, -2147483648\n" \
".Lgg_next%=:\ns_cmp_lg_u32 %[w], 0\ns_cbranch_scc1 .Lgg_w1%=\nbuffer_load_dword v56, v3, %[rs], 0 offen sc1\ns_waitcnt vmcnt(0)\ns_barrier\nbuffer_store_dword v90, off, %[rs], s56 sc1\n.Lgg_poll%=:\n" \
"v_cmp_gt_i32 vcc, s40, v56\ns_cbranch_vccz .Lgg_go%=\ns_cmp_ge_u32 s40, 62\ns_cbranch_scc1 .Lgg_ns1050%=\ns_sleep 12\n.Lgg_ns1050%=:\ns_sleep 1\nbuffer_load_dword v56, v3, %[rs], 0 offen sc1\n" \
"s_waitcnt vmcnt(0)\ns_add_u32 s43, s43, 1\ns_cmp_lt_u32 s43, 262144\ns_cbranch_scc1 .Lgg_poll%=\ns_branch .Lgg_abort%=\n.Lgg_w1%=:\ns_waitcnt vmcnt(0)\ns_barrier\n" \
".Lgg_go%=:\ns_add_u32 s44, s40, 0\ns_lshl_b32 s44, s44, 17\ns_add_u32 s44, s44, %[xn]\ns_lshr_b32 s45, s44, 8\ns_and_b32 s45, s45, 0x2000\ns_xor_b32 s41, s44, s45\ns_add_u32 s61, s41, %[xw]\n" \
"s_barrier\ns_add_u32 m0, s57, 0\ns_add_u32 s60, s61, 0\nbuffer_load_dwordx4 v0, %[rs], s60 offen offset:0 sc1 lds\nbuffer_load_dwordx4 v0, %[rs], s60 offen offset:1024 sc1 lds\nbuffer_load_dwordx4 v0, %[rs], s60 offen offset:2048 sc1 lds\nbuffer_load_dwordx4 v0, %[rs], s60 offen offset:3072 sc1 lds\ns_add_u32 m0, s57, 16384\n" \
"s_add_u32 s60, s61, 32768\nbuffer_load_dwordx4 v0, %[rs], s60 offen offset:0 sc1 lds\nbuffer_load_dwordx4 v0, %[rs], s60 offen offset:1024 sc1 lds\nbuffer_load_dwordx4 v0, %[rs], s60 offen offset:2048 sc1 lds\nbuffer_load_dwordx4 v0, %[rs], s60 offen offset:3072 sc1 lds\nbuffer_load_dwordx4 v[40:43], v5, %[rs], s41 offen offset:0 sc1\nbuffer_load_dwordx4 v[44:47], v5, %[rs], s41 offen offset:256 sc1\ns_lshl_b32 s44, s40, 19\n" \
"s_add_u32 s44, s44, %[ho]\ns_lshr_b32 s45, s44, 8\ns_and_b32 s45, s45, 0x2000\ns_xor_b32 s55, s44, s45\ns_waitcnt vmcnt(6)\ns_barrier\nds_read_b128 v[56:59], v2 offset:0\nds_read_b128 v[60:63], v2 offset:2048\n" \
"ds_read_b128 v[64:67], v2 offset:4096\ns_waitcnt lgkmcnt(2)\nv_mfma_f32_16x16x32_bf16 v[24:27], a[0:3], v[56:59], 0\nds_read_b128 v[68:71], v2 offset:6144\nds_read_b128 v[80:83], v1 offset:0\nv_mfma_f32_16x16x32_bf16 v[28:31], a[8:11], v[56:59], 0\nbuffer_load_dwordx4 v[48:51], v5, %[rs], s41 offen offset:512 sc1\nv_mfma_f32_16x16x32_bf16 v[32:35], a[16:19], v[56:59], 0\n" \
"v_mfma_f32_16x16x32_bf16 v[36:39], a[24:27], v[56:59], 0\ns_waitcnt lgkmcnt(3)\nds_read_b128 v[72:75], v1 offset:1024\nv_mfma_f32_16x16x32_bf16 v[24:27], a[4:7], v[60:63], v[24:27]\nds_read_b128 v[56:59], v2 offset:8192\nv_mfma_f32_16x16x32_bf16 v[28:31], a[12:15], v[60:63], v[28:31]\nbuffer_load_dwordx4 v[52:55], v5, %[rs], s41 offen offset:768 sc1\nv_mfma_f32_16x16x32_bf16 v[32:35], a[20:23], v[60:63], v[32:35]\n" \
"v_mfma_f32_16x16x32_bf16 v[36:39], a[28:31], v[60:63], v[36:39]\ns_waitcnt lgkmcnt(4)\nds_read_b128 v[76:79], v1 offset:2048\nv_mfma_f32_16x16x32_bf16 v[24:27], a[32:35], v[64:67], v[24:27]\ns_waitcnt lgkmcnt(3)\ns_add_u32 m0, s57, 32768\nds_read_b128 v[60:63], v2 offset:10240\nv_mfma_f32_16x16x32_bf16 v[28:31], v[80:83], v[64:67], v[28:31]\n" \
"s_add_u32 s60, s61, 65536\nbuffer_load_dwordx4 v0, %[rs], s60 offen offset:0 sc1 lds\nv_mfma_f32_16x16x32_bf16 v[32:35], a[40:43], v[64:67], v[32:35]\nv_mfma_f32_16x16x32_bf16 v[36:39], a[48:51], v[64:67], v[36:39]\nds_read_b128 v[80:83], v1 offset:3072\nv_mfma_f32_16x16x32_bf16 v[24:27], a[36:39], v[68:71], v[24:27]\ns_waitcnt lgkmcnt(4)\nds_read_b128 v[64:67], v2 offset:12288\n" \
"v_mfma_f32_16x16x32_bf16 v[28:31], v[72:75], v[68:71], v[28:31]\nbuffer_load_dwordx4 v0, %[rs], s60 offen offset:1024 sc1 lds\nv_mfma_f32_16x16x32_bf16 v[32:35], a[44:47], v[68:71], v[32:35]\nv_mfma_f32_16x16x32_bf16 v[36:39], a[52:55], v[68:71], v[36:39]\ns_waitcnt lgkmcnt(4)\nds_read_b128 v[72:75], v1 offset:4096\nv_mfma_f32_16x16x32_bf16 v[24:27], a[56:59], v[56:59], v[24:27]\nds_read_b128 v[68:71], v2 offset:14336\n" \
"v_mfma_f32_16x16x32_bf16 v[28:31], a[64:67], v[56:59], v[28:31]\ns_waitcnt lgkmcnt(5)\nbuffer_load_dwordx4 v0, %[rs], s60 offen offset:2048 sc1 lds\nv_mfma_f32_16x16x32_bf16 v[32:35], v[76:79], v[56:59], v[32:35]\nv_mfma_f32_16x16x32_bf16 v[36:39], a[72:75], v[56:59], v[36:39]\ns_waitcnt lgkmcnt(4)\nds_read_b128 v[76:79], v1 offset:5120\nv_mfma_f32_16x16x32_bf16 v[24:27], a[60:63], v[60:63], v[24:27]\n" \
"s_waitcnt vmcnt(7)\ns_barrier\nds_read_b128 v[56:59], v2 offset:16384\nv_mfma_f32_16x16x32_bf16 v[28:31], a[68:71], v[60:63], v[28:31]\ns_waitcnt lgkmcnt(5)\nbuffer_load_dwordx4 v0, %[rs], s60 offen offset:3072 sc1 lds\nv_mfma_f32_16x16x32_bf16 v[32:35], v[80:83], v[60:63], v[32:35]\nv_mfma_f32_16x16x32_bf16 v[36:39], a[76:79], v[60:63], v[36:39]\n" \
"s_waitcnt lgkmcnt(4)\nv_mfma_f32_16x16x32_bf16 v[24:27], a[80:83], v[64:67], v[24:27]\ns_add_u32 m0, s57, 49152\nds_read_b128 v[60:63], v2 offset:18432\nv_mfma_f32_16x16x32_bf16 v[28:31], a[88:91], v[64:67], v[28:31]\ns_add_u32 s60, s61, 98304\nbuffer_load_dwordx4 v0, %[rs], s60 offen offset:0 sc1 lds\nv_mfma_f32_16x16x32_bf16 v[32:35], a[96:99], v[64:67], v[32:35]\n" \
"s_waitcnt lgkmcnt(4)\nv_mfma_f32_16x16x32_bf16 v[36:39], v[72:75], v[64:67], v[36:39]\ns_waitcnt lgkmcnt(3)\nv_mfma_f32_16x16x32_bf16 v[24:27], a[84:87], v[68:71], v[24:27]\nds_read_b128 v[64:67], v2 offset:20480\nv_mfma_f32_16x16x32_bf16 v[28:31], a[92:95], v[68:71], v[28:31]\nbuffer_load_dwordx4 v0, %[rs], s60 offen offset:1024 sc1 lds\nv_mfma_f32_16x16x32_bf16 v[32:35], a[100:103], v[68:71], v[32:35]\n" \
"s_waitcnt lgkmcnt(3)\nv_mfma_f32_16x16x32_bf16 v[36:39], v[76:79], v[68:71], v[36:39]\ns_waitcnt lgkmcnt(2)\nds_read_b128 v[76:79], v1 offset:6144\nv_mfma_f32_16x16x32_bf16 v[24:27], a[104:107], v[56:59], v[24:27]\nds_read_b128 v[68:71], v2 offset:22528\nv_mfma_f32_16x16x32_bf16 v[28:31], a[112:115], v[56:59], v[28:31]\nbuffer_load_dwordx4 v0, %[rs], s60 offen offset:2048 sc1 lds\n" \
"v_mfma_f32_16x16x32_bf16 v[32:35], a[120:123], v[56:59], v[32:35]\nv_mfma_f32_16x16x32_bf16 v[36:39], a[128:131], v[56:59], v[36:39]\ns_waitcnt lgkmcnt(3)\nds_read_b128 v[80:83], v1 offset:7168\nv_mfma_f32_16x16x32_bf16 v[24:27], a[108:111], v[60:63], v[24:27]\nds_read_b128 v[56:59], v2 offset:24576\nv_mfma_f32_16x16x32_bf16 v[28:31], a[116:119], v[60:63], v[28:31]\nbuffer_load_dwordx4 v0, %[rs], s60 offen offset:3072 sc1 lds\n" \
"v_mfma_f32_16x16x32_bf16 v[32:35], a[124:127], v[60:63], v[32:35]\nv_mfma_f32_16x16x32_bf16 v[36:39], a[132:135], v[60:63], v[36:39]\ns_waitcnt lgkmcnt(4)\nds_read_b128 v[72:75], v1 offset:8192\nv_mfma_f32_16x16x32_bf16 v[24:27], a[136:139], v[64:67], v[24:27]\ns_waitcnt vmcnt(8)\ns_waitcnt lgkmcnt(4)\nds_read_b128 v[60:63], v2 offset:26624\n" \
"v_add_f32 v84, v40, v44\nv_mfma_f32_16x16x32_bf16 v[28:31], v[76:79], v[64:67], v[28:31]\nv_add_f32 v85, v48, v52\nv_add_f32 v84, v84, v85\nv_mfma_f32_16x16x32_bf16 v[32:35], a[144:147], v[64:67], v[32:35]\nv_mov_b32 v85, v84\nv_mfma_f32_16x16x32_bf16 v[36:39], a[152:155], v[64:67], v[36:39]\ns_waitcnt lgkmcnt(4)\n" \
"ds_read_b128 v[76:79], v1 offset:9216\nv_mfma_f32_16x16x32_bf16 v[24:27], a[140:143], v[68:71], v[24:27]\ns_waitcnt lgkmcnt(4)\nds_read_b128 v[64:67], v2 offset:28672\nv_mfma_f32_16x16x32_bf16 v[28:31], v[80:83], v[68:71], v[28:31]\nv_permlane16_swap_b32 v84, v85\nv_mfma_f32_16x16x32_bf16 v[32:35], a[148:151], v[68:71], v[32:35]\nv_add_f32 v84, v84, v85\n" \
"v_mov_b32 v85, v84\nv_mfma_f32_16x16x32_bf16 v[36:39], a[156:159], v[68:71], v[36:39]\ns_waitcnt lgkmcnt(4)\nds_read_b128 v[80:83], v1 offset:10240\nv_mfma_f32_16x16x32_bf16 v[24:27], a[160:163], v[56:59], v[24:27]\nds_read_b128 v[68:71], v2 offset:30720\nv_mfma_f32_16x16x32_bf16 v[28:31], a[168:171], v[56:59], v[28:31]\ns_waitcnt lgkmcnt(5)\n" \
"v_permlane32_swap_b32 v84, v85\nv_mfma_f32_16x16x32_bf16 v[32:35], v[72:75], v[56:59], v[32:35]\nv_add_f32 v84, v84, v85\nv_mul_f32 v84, 0xbd800000, v84\nv_mfma_f32_16x16x32_bf16 v[36:39], a[176:179], v[56:59], v[36:39]\ns_waitcnt lgkmcnt(4)\nds_read_b128 v[72:75], v1 offset:11264\nv_add_f32 v40, v40, v84\n" \
"v_mfma_f32_16x16x32_bf16 v[24:27], a[164:167], v[60:63], v[24:27]\ns_waitcnt vmcnt(4)\ns_barrier\nds_read_b128 v[56:59], v2 offset:32768\nv_mfma_f32_16x16x32_bf16 v[28:31], a[172:175], v[60:63], v[28:31]\ns_waitcnt lgkmcnt(5)\nv_mul_f32 v40, 0x41000000, v40\nv_fma_f32 v41, v40, v40, v41\n" \
"v_mfma_f32_16x16x32_bf16 v[32:35], v[76:79], v[60:63], v[32:35]\nv_add_f32 v44, v44, v84\nv_mul_f32 v44, 0x41000000, v44\nv_mfma_f32_16x16x32_bf16 v[36:39], a[180:183], v[60:63], v[36:39]\ns_waitcnt lgkmcnt(4)\nv_fma_f32 v45, v44, v44, v45\nv_add_f32 v48, v48, v84\nv_mfma_f32_16x16x32_bf16 v[24:27], a[184:187], v[64:67], v[24:27]\n" \
"ds_read_b128 v[60:63], v2 offset:34816\nv_mul_f32 v48, 0x41000000, v48\nv_mfma_f32_16x16x32_bf16 v[28:31], a[192:195], v[64:67], v[28:31]\nv_fma_f32 v49, v48, v48, v49\nv_add_f32 v52, v52, v84\nv_mfma_f32_16x16x32_bf16 v[32:35], a[200:203], v[64:67], v[32:35]\ns_waitcnt lgkmcnt(4)\nv_mul_f32 v52, 0x41000000, v52\n" \
"v_fma_f32 v53, v52, v52, v53\nv_mfma_f32_16x16x32_bf16 v[36:39], v[80:83], v[64:67], v[36:39]\ns_waitcnt lgkmcnt(3)\nv_add_f32 v85, v41, v45\nv_add_f32 v86, v49, v53\nv_mfma_f32_16x16x32_bf16 v[24:27], a[188:191], v[68:71], v[24:27]\nds_read_b128 v[64:67], v2 offset:36864\nv_add_f32 v85, v85, v86\n" \
"v_mfma_f32_16x16x32_bf16 v[28:31], a[196:199], v[68:71], v[28:31]\nv_mov_b32 v86, v85\nv_mfma_f32_16x16x32_bf16 v[32:35], a[204:207], v[68:71], v[32:35]\ns_waitcnt lgkmcnt(3)\nv_permlane16_swap_b32 v85, v86\nv_mfma_f32_16x16x32_bf16 v[36:39], v[72:75], v[68:71], v[36:39]\ns_waitcnt lgkmcnt(2)\nds_read_b128 v[72:75], v1 offset:12288\n" \
"v_add_f32 v85, v85, v86\nv_mfma_f32_16x16x32_bf16 v[24:27], a[208:211], v[56:59], v[24:27]\nds_read_b128 v[68:71], v2 offset:38912\nv_mov_b32 v86, v85\nv_mfma_f32_16x16x32_bf16 v[28:31], a[216:219], v[56:59], v[28:31]\ns_nop 0\nv_permlane32_swap_b32 v85, v86\nv_mfma_f32_16x16x32_bf16 v[32:35], a[224:227], v[56:59], v[32:35]\n" \
"v_add_f32 v85, v85, v86\nv_mul_f32 v85, 0x3a800000, v85\nv_mfma_f32_16x16x32_bf16 v[36:39], a[232:235], v[56:59], v[36:39]\ns_waitcnt lgkmcnt(3)\nds_read_b128 v[76:79], v1 offset:13312\nv_cvt_pk_bf16_f32 v85, v85, v85\nv_mfma_f32_16x16x32_bf16 v[24:27], a[212:215], v[60:63], v[24:27]\nds_read_b128 v[56:59], v2 offset:40960\n" \
"v_and_b32 v85, 0xffff0000, v85\nv_mfma_f32_16x16x32_bf16 v[28:31], a[220:223], v[60:63], v[28:31]\nv_add_f32 v85, 0x3727c5ac, v85\nv_cvt_pk_bf16_f32 v85, v85, v85\nv_mfma_f32_16x16x32_bf16 v[32:35], a[228:231], v[60:63], v[32:35]\nv_and_b32 v85, 0xffff0000, v85\nv_cvt_pk_bf16_f32 v84, v84, v84\nv_mfma_f32_16x16x32_bf16 v[36:39], a[236:239], v[60:63], v[36:39]\n" \
"s_waitcnt lgkmcnt(4)\nds_read_b128 v[80:83], v1 offset:14336\nv_and_b32 v84, 0xffff0000, v84\nv_mfma_f32_16x16x32_bf16 v[24:27], a[240:243], v[64:67], v[24:27]\ns_waitcnt lgkmcnt(4)\nds_read_b128 v[60:63], v2 offset:43008\nv_mfma_f32_16x16x32_bf16 v[28:31], v[72:75], v[64:67], v[28:31]\nv_sqrt_f32 v85, v85\n" \
"v_mfma_f32_16x16x32_bf16 v[32:35], a[248:251], v[64:67], v[32:35]\nv_cvt_pk_bf16_f32 v85, v85, v85\nv_and_b32 v85, 0xffff0000, v85\nv_mfma_f32_16x16x32_bf16 v[36:39], v[96:99], v[64:67], v[36:39]\ns_waitcnt lgkmcnt(4)\nds_read_b128 v[72:75], v1 offset:15360\nv_mul_f32 v86, s58, v85\nv_mfma_f32_16x16x32_bf16 v[24:27], a[244:247], v[68:71], v[24:27]\n" \
"s_waitcnt lgkmcnt(4)\nds_read_b128 v[64:67], v2 offset:45056\nv_add_f32 v87, v42, v46\nv_mfma_f32_16x16x32_bf16 v[28:31], v[76:79], v[68:71], v[28:31]\nv_rcp_f32 v85, v85\nv_mfma_f32_16x16x32_bf16 v[32:35], a[252:255], v[68:71], v[32:35]\nv_mul_f32 v85, s58, v85\nv_add_f32 v88, v50, v54\n" \
"v_mfma_f32_16x16x32_bf16 v[36:39], v[100:103], v[68:71], v[36:39]\ns_waitcnt lgkmcnt(4)\nds_read_b128 v[76:79], v1 offset:16384\nv_add_f32 v87, v87, v88\nv_mfma_f32_16x16x32_bf16 v[24:27], v[104:107], v[56:59], v[24:27]\nds_read_b128 v[68:71], v2 offset:47104\nv_mov_b32 v88, v87\nv_mfma_f32_16x16x32_bf16 v[28:31], v[112:115], v[56:59], v[28:31]\n" \
"s_waitcnt lgkmcnt(5)\nv_permlane16_swap_b32 v87, v88\nv_mfma_f32_16x16x32_bf16 v[32:35], v[80:83], v[56:59], v[32:35]\nv_add_f32 v87, v87, v88\nv_mov_b32 v88, v87\nv_mfma_f32_16x16x32_bf16 v[36:39], v[120:123], v[56:59], v[36:39]\ns_waitcnt lgkmcnt(4)\nds_read_b128 v[80:83], v1 offset:17408\n" \
"v_mfma_f32_16x16x32_bf16 v[24:27], v[108:111], v[60:63], v[24:27]\ns_waitcnt vmcnt(0)\ns_barrier\nds_read_b128 v[56:59], v2 offset:49152\nv_mfma_f32_16x16x32_bf16 v[28:31], v[116:119], v[60:63], v[28:31]\ns_waitcnt lgkmcnt(5)\nv_permlane32_swap_b32 v87, v88\nv_mfma_f32_16x16x32_bf16 v[32:35], v[72:75], v[60:63], v[32:35]\n" \
"v_add_f32 v87, v87, v88\nv_mul_f32 v87, 0xbd800000, v87\nv_mfma_f32_16x16x32_bf16 v[36:39], v[124:127], v[60:63], v[36:39]\ns_waitcnt lgkmcnt(4)\nv_add_f32 v42, v42, v87\nv_mul_f32 v42, 0x41000000, v42\nv_mfma_f32_16x16x32_bf16 v[24:27], v[128:131], v[64:67], v[24:27]\nds_read_b128 v[60:63], v2 offset:51200\n" \
"v_fma_f32 v43, v42, v42, v43\nv_mfma_f32_16x16x32_bf16 v[28:31], v[136:139], v[64:67], v[28:31]\nv_add_f32 v46, v46, v87\nv_mul_f32 v46, 0x41000000, v46\nv_mfma_f32_16x16x32_bf16 v[32:35], v[144:147], v[64:67], v[32:35]\ns_waitcnt lgkmcnt(4)\nv_fma_f32 v47, v46, v46, v47\nv_add_f32 v50, v50, v87\n" \
"v_mfma_f32_16x16x32_bf16 v[36:39], v[76:79], v[64:67], v[36:39]\ns_waitcnt lgkmcnt(3)\nv_mul_f32 v50, 0x41000000, v50\nv_fma_f32 v51, v50, v50, v51\nv_mfma_f32_16x16x32_bf16 v[24:27], v[132:135], v[68:71], v[24:27]\nds_read_b128 v[64:67], v2 offset:53248\nv_add_f32 v54, v54, v87\nv_mfma_f32_16x16x32_bf16 v[28:31], v[140:143], v[68:71], v[28:31]\n" \
"v_mul_f32 v54, 0x41000000, v54\nv_fma_f32 v55, v54, v54, v55\nv_mfma_f32_16x16x32_bf16 v[32:35], v[148:151], v[68:71], v[32:35]\ns_waitcnt lgkmcnt(3)\nv_add_f32 v88, v43, v47\nv_add_f32 v89, v51, v55\nv_mfma_f32_16x16x32_bf16 v[36:39], v[80:83], v[68:71], v[36:39]\ns_waitcnt lgkmcnt(2)\n" \
"ds_read_b128 v[80:83], v1 offset:18432\nv_add_f32 v88, v88, v89\nv_mfma_f32_16x16x32_bf16 v[24:27], v[152:155], v[56:59], v[24:27]\nds_read_b128 v[68:71], v2 offset:55296\nv_mov_b32 v89, v88\nv_mfma_f32_16x16x32_bf16 v[28:31], v[160:163], v[56:59], v[28:31]\ns_nop 0\nv_permlane16_swap_b32 v88, v89\n" \
"v_mfma_f32_16x16x32_bf16 v[32:35], v[168:171], v[56:59], v[32:35]\nv_add_f32 v88, v88, v89\nv_mov_b32 v89, v88\nv_mfma_f32_16x16x32_bf16 v[36:39], v[176:179], v[56:59], v[36:39]\ns_waitcnt lgkmcnt(3)\nds_read_b128 v[72:75], v1 offset:19456\nv_mfma_f32_16x16x32_bf16 v[24:27], v[156:159], v[60:63], v[24:27]\nds_read_b128 v[56:59], v2 offset:57344\n" \
"v_mfma_f32_16x16x32_bf16 v[28:31], v[164:167], v[60:63], v[28:31]\nv_permlane32_swap_b32 v88, v89\nv_mfma_f32_16x16x32_bf16 v[32:35], v[172:175], v[60:63], v[32:35]\nv_add_f32 v88, v88, v89\nv_mul_f32 v88, 0x3a800000, v88\nv_mfma_f32_16x16x32_bf16 v[36:39], v[180:183], v[60:63], v[36:39]\ns_waitcnt lgkmcnt(4)\nds_read_b128 v[76:79], v1 offset:20480\n" \
"v_cvt_pk_bf16_f32 v88, v88, v88\nv_mfma_f32_16x16x32_bf16 v[24:27], v[184:187], v[64:67], v[24:27]\ns_waitcnt lgkmcnt(4)\nds_read_b128 v[60:63], v2 offset:59392\nv_and_b32 v88, 0xffff0000, v88\nv_mfma_f32_16x16x32_bf16 v[28:31], v[80:83], v[64:67], v[28:31]\nv_add_f32 v88, 0x3727c5ac, v88\nv_cvt_pk_bf16_f32 v88, v88, v88\n" \
"v_mfma_f32_16x16x32_bf16 v[32:35], v[192:195], v[64:67], v[32:35]\nv_and_b32 v88, 0xffff0000, v88\nv_cvt_pk_bf16_f32 v87, v87, v87\nv_mfma_f32_16x16x32_bf16 v[36:39], v[200:203], v[64:67], v[36:39]\ns_waitcnt lgkmcnt(4)\nds_read_b128 v[80:83], v1 offset:21504\nv_and_b32 v87, 0xffff0000, v87\nv_mfma_f32_16x16x32_bf16 v[24:27], v[188:191], v[68:71], v[24:27]\n" \
"s_waitcnt lgkmcnt(4)\nds_read_b128 v[64:67], v2 offset:61440\nv_mfma_f32_16x16x32_bf16 v[28:31], v[72:75], v[68:71], v[28:31]\nv_sqrt_f32 v88, v88\nv_mfma_f32_16x16x32_bf16 v[32:35], v[196:199], v[68:71], v[32:35]\nv_cvt_pk_bf16_f32 v88, v88, v88\nv_and_b32 v88, 0xffff0000, v88\nv_mfma_f32_16x16x32_bf16 v[36:39], v[204:207], v[68:71], v[36:39]\n" \
"s_waitcnt lgkmcnt(4)\nds_read_b128 v[72:75], v1 offset:22528\nv_mul_f32 v89, s58, v88\nv_mfma_f32_16x16x32_bf16 v[24:27], v[208:211], v[56:59], v[24:27]\nds_read_b128 v[68:71], v2 offset:63488\nv_mfma_f32_16x16x32_bf16 v[28:31], v[216:219], v[56:59], v[28:31]\ns_waitcnt lgkmcnt(5)\nv_rcp_f32 v88, v88\n" \
"v_mfma_f32_16x16x32_bf16 v[32:35], v[76:79], v[56:59], v[32:35]\nv_mul_f32 v88, s58, v88\nv_mfma_f32_16x16x32_bf16 v[36:39], v[224:227], v[56:59], v[36:39]\ns_waitcnt lgkmcnt(4)\nds_read_b128 v[76:79], v1 offset:23552\nv_mfma_f32_16x16x32_bf16 v[24:27], v[212:215], v[60:63], v[24:27]\nds_read_b128 v[56:59], v2 offset:256\nv_mfma_f32_16x16x32_bf16 v[28:31], v[220:223], v[60:63], v[28:31]\n" \
"s_waitcnt lgkmcnt(5)\nv_mfma_f32_16x16x32_bf16 v[32:35], v[80:83], v[60:63], v[32:35]\nv_mfma_f32_16x16x32_bf16 v[36:39], v[228:231], v[60:63], v[36:39]\ns_waitcnt lgkmcnt(4)\nv_mfma_f32_16x16x32_bf16 v[24:27], v[232:235], v[64:67], v[24:27]\nds_read_b128 v[60:63], v2 offset:2304\nv_mfma_f32_16x16x32_bf16 v[28:31], v[240:243], v[64:67], v[28:31]\nv_mfma_f32_16x16x32_bf16 v[32:35], v[248:251], v[64:67], v[32:35]\n" \
"s_waitcnt lgkmcnt(4)\nv_mfma_f32_16x16x32_bf16 v[36:39], v[72:75], v[64:67], v[36:39]\ns_waitcnt lgkmcnt(3)\nv_mfma_f32_16x16x32_bf16 v[24:27], v[236:239], v[68:71], v[24:27]\nds_read_b128 v[64:67], v2 offset:4352\nv_mfma_f32_16x16x32_bf16 v[28:31], v[244:247], v[68:71], v[28:31]\nv_mfma_f32_16x16x32_bf16 v[32:35], v[252:255], v[68:71], v[32:35]\ns_waitcnt lgkmcnt(3)\n" \
"v_mfma_f32_16x16x32_bf16 v[36:39], v[76:79], v[68:71], v[36:39]\ns_waitcnt lgkmcnt(2)\nds_read_b128 v[76:79], v1 offset:0\nv_mfma_f32_16x16x32_bf16 v[40:43], a[0:3], v[56:59], 0\nds_read_b128 v[68:71], v2 offset:6400\nv_mfma_f32_16x16x32_bf16 v[44:47], a[8:11], v[56:59], 0\ns_lshl_b32 s44, s40, 1\nv_mov_b32 v90, s44\n" \
"v_mfma_f32_16x16x32_bf16 v[48:51], a[16:19], v[56:59], 0\nv_mfma_f32_16x16x32_bf16 v[52:55], a[24:27], v[56:59], 0\ns_waitcnt lgkmcnt(3)\nds_read_b128 v[80:83], v1 offset:1024\nv_fma_f32 v24, v8, v84, v24\nv_mfma_f32_16x16x32_bf16 v[40:43], a[4:7], v[60:63], v[40:43]\nds_read_b128 v[56:59], v2 offset:8448\nv_fma_f32 v25, v9, v84, v25\n" \
"v_mfma_f32_16x16x32_bf16 v[44:47], a[12:15], v[60:63], v[44:47]\nv_fma_f32 v32, v16, v84, v32\nv_fma_f32 v33, v17, v84, v33\nv_mfma_f32_16x16x32_bf16 v[48:51], a[20:23], v[60:63], v[48:51]\nv_mul_f32 v24, v24, v85\nv_mul_f32 v25, v25, v85\nv_mfma_f32_16x16x32_bf16 v[52:55], a[28:31], v[60:63], v[52:55]\ns_waitcnt lgkmcnt(4)\n" \
"ds_read_b128 v[72:75], v1 offset:2048\nv_fma_f32 v91, v24, v24, s59\nv_mfma_f32_16x16x32_bf16 v[40:43], a[32:35], v[64:67], v[40:43]\ns_waitcnt lgkmcnt(4)\nds_read_b128 v[60:63], v2 offset:10496\nv_fma_f32 v92, v25, v25, s59\nv_mfma_f32_16x16x32_bf16 v[44:47], v[76:79], v[64:67], v[44:47]\nv_mul_f32 v91, v91, v24\n" \
"v_mul_f32 v92, v92, v25\nv_mfma_f32_16x16x32_bf16 v[48:51], a[40:43], v[64:67], v[48:51]\nv_exp_f32 v91, v91\nv_mfma_f32_16x16x32_bf16 v[52:55], a[48:51], v[64:67], v[52:55]\ns_waitcnt lgkmcnt(4)\nds_read_b128 v[76:79], v1 offset:3072\nv_mul_f32 v24, v24, v32\nv_mfma_f32_16x16x32_bf16 v[40:43], a[36:39], v[68:71], v[40:43]\n" \
"s_waitcnt lgkmcnt(4)\nds_read_b128 v[64:67], v2 offset:12544\nv_mul_f32 v25, v25, v33\nv_mfma_f32_16x16x32_bf16 v[44:47], v[80:83], v[68:71], v[44:47]\nv_exp_f32 v92, v92\nv_mfma_f32_16x16x32_bf16 v[48:51], a[44:47], v[68:71], v[48:51]\nv_fma_f32 v91, v91, v86, v86\nv_fma_f32 v92, v92, v86, v86\n" \
"v_mfma_f32_16x16x32_bf16 v[52:55], a[52:55], v[68:71], v[52:55]\ns_waitcnt lgkmcnt(4)\nds_read_b128 v[80:83], v1 offset:4096\nv_fma_f32 v26, v10, v84, v26\nv_mfma_f32_16x16x32_bf16 v[40:43], a[56:59], v[56:59], v[40:43]\nds_read_b128 v[68:71], v2 offset:14592\nv_fma_f32 v27, v11, v84, v27\nv_mfma_f32_16x16x32_bf16 v[44:47], a[64:67], v[56:59], v[44:47]\n" \
"s_waitcnt lgkmcnt(5)\nv_rcp_f32 v91, v91\nv_mfma_f32_16x16x32_bf16 v[48:51], v[72:75], v[56:59], v[48:51]\nv_rcp_f32 v92, v92\nv_mfma_f32_16x16x32_bf16 v[52:55], a[72:75], v[56:59], v[52:55]\ns_waitcnt lgkmcnt(4)\nds_read_b128 v[72:75], v1 offset:5120\nv_mul_f32 v24, v24, v91\n" \
"v_mfma_f32_16x16x32_bf16 v[40:43], a[60:63], v[60:63], v[40:43]\nds_read_b128 v[56:59], v2 offset:16640\nv_mul_f32 v25, v25, v92\nv_mfma_f32_16x16x32_bf16 v[44:47], a[68:71], v[60:63], v[44:47]\ns_waitcnt lgkmcnt(5)\nv_cvt_pk_bf16_f32 v32, v24, v25\nv_fma_f32 v34, v18, v84, v34\nv_mfma_f32_16x16x32_bf16 v[48:51], v[76:79], v[60:63], v[48:51]\n" \
"v_fma_f32 v35, v19, v84, v35\nv_mul_f32 v26, v26, v85\nv_mfma_f32_16x16x32_bf16 v[52:55], a[76:79], v[60:63], v[52:55]\ns_waitcnt lgkmcnt(4)\nv_mul_f32 v27, v27, v85\nv_fma_f32 v91, v26, v26, s59\nv_mfma_f32_16x16x32_bf16 v[40:43], a[80:83], v[64:67], v[40:43]\nds_read_b128 v[60:63], v2 offset:18688\n" \
"v_fma_f32 v92, v27, v27, s59\nv_mfma_f32_16x16x32_bf16 v[44:47], a[88:91], v[64:67], v[44:47]\nv_mul_f32 v91, v91, v26\nv_mul_f32 v92, v92, v27\nv_mfma_f32_16x16x32_bf16 v[48:51], a[96:99], v[64:67], v[48:51]\ns_waitcnt lgkmcnt(4)\nv_exp_f32 v91, v91\nv_mfma_f32_16x16x32_bf16 v[52:55], v[80:83], v[64:67], v[52:55]\n" \
"s_waitcnt lgkmcnt(3)\nv_exp_f32 v92, v92\nv_mfma_f32_16x16x32_bf16 v[40:43], a[84:87], v[68:71], v[40:43]\nds_read_b128 v[64:67], v2 offset:20736\nv_mul_f32 v26, v26, v34\nv_mfma_f32_16x16x32_bf16 v[44:47], a[92:95], v[68:71], v[44:47]\nv_mul_f32 v27, v27, v35\nv_fma_f32 v91, v91, v86, v86\n" \
"v_mfma_f32_16x16x32_bf16 v[48:51], a[100:103], v[68:71], v[48:51]\ns_waitcnt lgkmcnt(3)\nv_fma_f32 v92, v92, v86, v86\nv_fma_f32 v28, v12, v84, v28\nv_mfma_f32_16x16x32_bf16 v[52:55], v[72:75], v[68:71], v[52:55]\ns_waitcnt lgkmcnt(2)\nds_read_b128 v[72:75], v1 offset:6144\nv_fma_f32 v29, v13, v84, v29\n" \
"v_mfma_f32_16x16x32_bf16 v[40:43], a[104:107], v[56:59], v[40:43]\nds_read_b128 v[68:71], v2 offset:22784\nv_fma_f32 v36, v20, v84, v36\nv_mfma_f32_16x16x32_bf16 v[44:47], a[112:115], v[56:59], v[44:47]\nv_rcp_f32 v91, v91\nv_mfma_f32_16x16x32_bf16 v[48:51], a[120:123], v[56:59], v[48:51]\nv_rcp_f32 v92, v92\nv_mfma_f32_16x16x32_bf16 v[52:55], a[128:131], v[56:59], v[52:55]\n" \
"s_waitcnt lgkmcnt(3)\nds_read_b128 v[76:79], v1 offset:7168\nv_mul_f32 v26, v26, v91\nv_mfma_f32_16x16x32_bf16 v[40:43], a[108:111], v[60:63], v[40:43]\nds_read_b128 v[56:59], v2 offset:24832\nv_mul_f32 v27, v27, v92\nv_mfma_f32_16x16x32_bf16 v[44:47], a[116:119], v[60:63], v[44:47]\nv_cvt_pk_bf16_f32 v33, v26, v27\n" \
"v_fma_f32 v37, v21, v84, v37\nv_mfma_f32_16x16x32_bf16 v[48:51], a[124:127], v[60:63], v[48:51]\nv_mul_f32 v28, v28, v85\nv_mul_f32 v29, v29, v85\nv_mfma_f32_16x16x32_bf16 v[52:55], a[132:135], v[60:63], v[52:55]\ns_waitcnt lgkmcnt(4)\nds_read_b128 v[80:83], v1 offset:8192\nv_fma_f32 v91, v28, v28, s59\n" \
"v_mfma_f32_16x16x32_bf16 v[40:43], a[136:139], v[64:67], v[40:43]\ns_waitcnt lgkmcnt(4)\nds_read_b128 v[60:63], v2 offset:26880\nv_fma_f32 v92, v29, v29, s59\nv_mfma_f32_16x16x32_bf16 v[44:47], v[72:75], v[64:67], v[44:47]\nv_mul_f32 v91, v91, v28\nv_mul_f32 v92, v92, v29\nv_mfma_f32_16x16x32_bf16 v[48:51], a[144:147], v[64:67], v[48:51]\n" \
"v_exp_f32 v91, v91\nv_mfma_f32_16x16x32_bf16 v[52:55], a[152:155], v[64:67], v[52:55]\ns_waitcnt lgkmcnt(4)\nds_read_b128 v[72:75], v1 offset:9216\nv_mul_f32 v28, v28, v36\nv_mfma_f32_16x16x32_bf16 v[40:43], a[140:143], v[68:71], v[40:43]\ns_waitcnt lgkmcnt(4)\nds_read_b128 v[64:67], v2 offset:28928\n" \
"v_mul_f32 v29, v29, v37\nv_mfma_f32_16x16x32_bf16 v[44:47], v[76:79], v[68:71], v[44:47]\nv_exp_f32 v92, v92\nv_mfma_f32_16x16x32_bf16 v[48:51], a[148:151], v[68:71], v[48:51]\nv_fma_f32 v91, v91, v86, v86\nv_fma_f32 v92, v92, v86, v86\nv_mfma_f32_16x16x32_bf16 v[52:55], a[156:159], v[68:71], v[52:55]\ns_waitcnt lgkmcnt(4)\n" \
"ds_read_b128 v[76:79], v1 offset:10240\nv_fma_f32 v30, v14, v84, v30\nv_mfma_f32_16x16x32_bf16 v[40:43], a[160:163], v[56:59], v[40:43]\nds_read_b128 v[68:71], v2 offset:30976\nv_fma_f32 v31, v15, v84, v31\nv_mfma_f32_16x16x32_bf16 v[44:47], a[168:171], v[56:59], v[44:47]\ns_waitcnt lgkmcnt(5)\nv_rcp_f32 v91, v91\n" \
"v_mfma_f32_16x16x32_bf16 v[48:51], v[80:83], v[56:59], v[48:51]\nv_rcp_f32 v92, v92\nv_mfma_f32_16x16x32_bf16 v[52:55], a[176:179], v[56:59], v[52:55]\ns_waitcnt lgkmcnt(4)\nds_read_b128 v[80:83], v1 offset:11264\nv_mul_f32 v28, v28, v91\nv_mfma_f32_16x16x32_bf16 v[40:43], a[164:167], v[60:63], v[40:43]\nds_read_b128 v[56:59], v2 offset:33024\n" \
"v_mul_f32 v29, v29, v92\nv_mfma_f32_16x16x32_bf16 v[44:47], a[172:175], v[60:63], v[44:47]\ns_waitcnt lgkmcnt(5)\nv_cvt_pk_bf16_f32 v34, v28, v29\nv_fma_f32 v38, v22, v84, v38\nv_mfma_f32_16x16x32_bf16 v[48:51], v[72:75], v[60:63], v[48:51]\nv_fma_f32 v39, v23, v84, v39\nv_mul_f32 v30, v30, v85\n" \
"v_mfma_f32_16x16x32_bf16 v[52:55], a[180:183], v[60:63], v[52:55]\ns_waitcnt lgkmcnt(4)\nv_mul_f32 v31, v31, v85\nv_fma_f32 v91, v30, v30, s59\nv_mfma_f32_16x16x32_bf16 v[40:43], a[184:187], v[64:67], v[40:43]\nds_read_b128 v[60:63], v2 offset:35072\nv_fma_f32 v92, v31, v31, s59\nv_mfma_f32_16x16x32_bf16 v[44:47], a[192:195], v[64:67], v[44:47]\n" \
"v_mul_f32 v91, v91, v30\nv_mul_f32 v92, v92, v31\nv_mfma_f32_16x16x32_bf16 v[48:51], a[200:203], v[64:67], v[48:51]\ns_waitcnt lgkmcnt(4)\nv_exp_f32 v91, v91\nv_mfma_f32_16x16x32_bf16 v[52:55], v[76:79], v[64:67], v[52:55]\ns_waitcnt lgkmcnt(3)\nv_exp_f32 v92, v92\n" \
"v_mfma_f32_16x16x32_bf16 v[40:43], a[188:191], v[68:71], v[40:43]\nds_read_b128 v[64:67], v2 offset:37120\nv_mul_f32 v30, v30, v38\nv_mfma_f32_16x16x32_bf16 v[44:47], a[196:199], v[68:71], v[44:47]\nv_mul_f32 v31, v31, v39\nv_fma_f32 v91, v91, v86, v86\nv_mfma_f32_16x16x32_bf16 v[48:51], a[204:207], v[68:71], v[48:51]\ns_waitcnt lgkmcnt(3)\n" \
"v_fma_f32 v92, v92, v86, v86\nv_mfma_f32_16x16x32_bf16 v[52:55], v[80:83], v[68:71], v[52:55]\ns_waitcnt lgkmcnt(2)\nds_read_b128 v[80:83], v1 offset:12288\nv_mfma_f32_16x16x32_bf16 v[40:43], a[208:211], v[56:59], v[40:43]\nds_read_b128 v[68:71], v2 offset:39168\nv_mfma_f32_16x16x32_bf16 v[44:47], a[216:219], v[56:59], v[44:47]\nv_rcp_f32 v91, v91\n" \
"v_mfma_f32_16x16x32_bf16 v[48:51], a[224:227], v[56:59], v[48:51]\nv_rcp_f32 v92, v92\nv_mfma_f32_16x16x32_bf16 v[52:55], a[232:235], v[56:59], v[52:55]\ns_waitcnt lgkmcnt(3)\nds_read_b128 v[72:75], v1 offset:13312\nv_mul_f32 v30, v30, v91\nv_mfma_f32_16x16x32_bf16 v[40:43], a[212:215], v[60:63], v[40:43]\nds_read_b128 v[56:59], v2 offset:41216\n" \
"v_mul_f32 v31, v31, v92\nv_mfma_f32_16x16x32_bf16 v[44:47], a[220:223], v[60:63], v[44:47]\nv_cvt_pk_bf16_f32 v35, v30, v31\nv_mfma_f32_16x16x32_bf16 v[48:51], a[228:231], v[60:63], v[48:51]\nbuffer_store_dwordx4 v[32:35], v4, %[rs], s55 offen offset:0 sc1\nv_mfma_f32_16x16x32_bf16 v[52:55], a[236:239], v[60:63], v[52:55]\ns_waitcnt lgkmcnt(4)\nds_read_b128 v[76:79], v1 offset:14336\n" \
"v_mfma_f32_16x16x32_bf16 v[40:43], a[240:243], v[64:67], v[40:43]\ns_waitcnt lgkmcnt(4)\nds_read_b128 v[60:63], v2 offset:43264\nv_mfma_f32_16x16x32_bf16 v[44:47], v[80:83], v[64:67], v[44:47]\nv_mfma_f32_16x16x32_bf16 v[48:51], a[248:251], v[64:67], v[48:51]\nv_mfma_f32_16x16x32_bf16 v[52:55], v[96:99], v[64:67], v[52:55]\ns_waitcnt lgkmcnt(4)\nds_read_b128 v[80:83], v1 offset:15360\n" \
"v_mfma_f32_16x16x32_bf16 v[40:43], a[244:247], v[68:71], v[40:43]\ns_waitcnt lgkmcnt(4)\nds_read_b128 v[64:67], v2 offset:45312\nv_mfma_f32_16x16x32_bf16 v[44:47], v[72:75], v[68:71], v[44:47]\nv_mfma_f32_16x16x32_bf16 v[48:51], a[252:255], v[68:71], v[48:51]\nv_mfma_f32_16x16x32_bf16 v[52:55], v[100:103], v[68:71], v[52:55]\ns_waitcnt lgkmcnt(4)\nds_read_b128 v[72:75], v1 offset:16384\n" \
"v_mfma_f32_16x16x32_bf16 v[40:43], v[104:107], v[56:59], v[40:43]\nds_read_b128 v[68:71], v2 offset:47360\nv_mfma_f32_16x16x32_bf16 v[44:47], v[112:115], v[56:59], v[44:47]\ns_waitcnt lgkmcnt(5)\nv_mfma_f32_16x16x32_bf16 v[48:51], v[76:79], v[56:59], v[48:51]\nv_mfma_f32_16x16x32_bf16 v[52:55], v[120:123], v[56:59], v[52:55]\ns_waitcnt lgkmcnt(4)\nds_read_b128 v[76:79], v1 offset:17408\n" \
"v_mfma_f32_16x16x32_bf16 v[40:43], v[108:111], v[60:63], v[40:43]\nds_read_b128 v[56:59], v2 offset:49408\nv_mfma_f32_16x16x32_bf16 v[44:47], v[116:119], v[60:63], v[44:47]\ns_waitcnt lgkmcnt(5)\nv_mfma_f32_16x16x32_bf16 v[48:51], v[80:83], v[60:63], v[48:51]\nv_mfma_f32_16x16x32_bf16 v[52:55], v[124:127], v[60:63], v[52:55]\ns_waitcnt lgkmcnt(4)\nv_mfma_f32_16x16x32_bf16 v[40:43], v[128:131], v[64:67], v[40:43]\n" \
"ds_read_b128 v[60:63], v2 offset:51456\nv_mfma_f32_16x16x32_bf16 v[44:47], v[136:139], v[64:67], v[44:47]\nv_mfma_f32_16x16x32_bf16 v[48:51], v[144:147], v[64:67], v[48:51]\ns_waitcnt lgkmcnt(4)\nv_mfma_f32_16x16x32_bf16 v[52:55], v[72:75], v[64:67], v[52:55]\ns_waitcnt lgkmcnt(3)\nv_mfma_f32_16x16x32_bf16 v[40:43], v[132:135], v[68:71], v[40:43]\nds_read_b128 v[64:67], v2 offset:53504\n" \
"v_mfma_f32_16x16x32_bf16 v[44:47], v[140:143], v[68:71], v[44:47]\nv_mfma_f32_16x16x32_bf16 v[48:51], v[148:151], v[68:71], v[48:51]\ns_waitcnt lgkmcnt(3)\nv_mfma_f32_16x16x32_bf16 v[52:55], v[76:79], v[68:71], v[52:55]\ns_waitcnt lgkmcnt(2)\nds_read_b128 v[76:79], v1 offset:18432\nv_mfma_f32_16x16x32_bf16 v[40:43], v[152:155], v[56:59], v[40:43]\nds_read_b128 v[68:71], v2 offset:55552\n" \
"v_mfma_f32_16x16x32_bf16 v[44:47], v[160:163], v[56:59], v[44:47]\nv_mfma_f32_16x16x32_bf16 v[48:51], v[168:171], v[56:59], v[48:51]\nv_mfma_f32_16x16x32_bf16 v[52:55], v[176:179], v[56:59], v[52:55]\ns_waitcnt lgkmcnt(3)\nds_read_b128 v[80:83], v1 offset:19456\nv_mfma_f32_16x16x32_bf16 v[40:43], v[156:159], v[60:63], v[40:43]\nds_read_b128 v[56:59], v2 offset:57600\nv_mfma_f32_16x16x32_bf16 v[44:47], v[164:167], v[60:63], v[44:47]\n" \
"v_mfma_f32_16x16x32_bf16 v[48:51], v[172:175], v[60:63], v[48:51]\nv_mfma_f32_16x16x32_bf16 v[52:55], v[180:183], v[60:63], v[52:55]\ns_waitcnt lgkmcnt(4)\nds_read_b128 v[72:75], v1 offset:20480\nv_mfma_f32_16x16x32_bf16 v[40:43], v[184:187], v[64:67], v[40:43]\ns_waitcnt lgkmcnt(4)\nds_read_b128 v[60:63], v2 offset:59648\nv_mfma_f32_16x16x32_bf16 v[44:47], v[76:79], v[64:67], v[44:47]\n" \
"v_mfma_f32_16x16x32_bf16 v[48:51], v[192:195], v[64:67], v[48:51]\nv_mfma_f32_16x16x32_bf16 v[52:55], v[200:203], v[64:67], v[52:55]\ns_waitcnt lgkmcnt(4)\nds_read_b128 v[76:79], v1 offset:21504\nv_mfma_f32_16x16x32_bf16 v[40:43], v[188:191], v[68:71], v[40:43]\ns_waitcnt lgkmcnt(4)\nds_read_b128 v[64:67], v2 offset:61696\nv_mfma_f32_16x16x32_bf16 v[44:47], v[80:83], v[68:71], v[44:47]\n" \
"v_mfma_f32_16x16x32_bf16 v[48:51], v[196:199], v[68:71], v[48:51]\nv_mfma_f32_16x16x32_bf16 v[52:55], v[204:207], v[68:71], v[52:55]\ns_waitcnt lgkmcnt(4)\nds_read_b128 v[80:83], v1 offset:22528\nv_mfma_f32_16x16x32_bf16 v[40:43], v[208:211], v[56:59], v[40:43]\nds_read_b128 v[68:71], v2 offset:63744\nv_mfma_f32_16x16x32_bf16 v[44:47], v[216:219], v[56:59], v[44:47]\ns_waitcnt lgkmcnt(5)\n" \
"v_mfma_f32_16x16x32_bf16 v[48:51], v[72:75], v[56:59], v[48:51]\nv_mfma_f32_16x16x32_bf16 v[52:55], v[224:227], v[56:59], v[52:55]\ns_waitcnt lgkmcnt(4)\nds_read_b128 v[72:75], v1 offset:23552\nv_mfma_f32_16x16x32_bf16 v[40:43], v[212:215], v[60:63], v[40:43]\nv_mfma_f32_16x16x32_bf16 v[44:47], v[220:223], v[60:63], v[44:47]\ns_waitcnt lgkmcnt(4)\nv_mfma_f32_16x16x32_bf16 v[48:51], v[76:79], v[60:63], v[48:51]\n" \
"v_mfma_f32_16x16x32_bf16 v[52:55], v[228:231], v[60:63], v[52:55]\ns_waitcnt lgkmcnt(3)\nv_mfma_f32_16x16x32_bf16 v[40:43], v[232:235], v[64:67], v[40:43]\nv_mfma_f32_16x16x32_bf16 v[44:47], v[240:243], v[64:67], v[44:47]\nv_mfma_f32_16x16x32_bf16 v[48:51], v[248:251], v[64:67], v[48:51]\ns_waitcnt lgkmcnt(2)\nv_mfma_f32_16x16x32_bf16 v[52:55], v[80:83], v[64:67], v[52:55]\ns_waitcnt lgkmcnt(1)\n" \
"v_mfma_f32_16x16x32_bf16 v[40:43], v[236:239], v[68:71], v[40:43]\nv_mfma_f32_16x16x32_bf16 v[44:47], v[244:247], v[68:71], v[44:47]\nv_mfma_f32_16x16x32_bf16 v[48:51], v[252:255], v[68:71], v[48:51]\ns_waitcnt lgkmcnt(0)\nv_mfma_f32_16x16x32_bf16 v[52:55], v[72:75], v[68:71], v[52:55]\nv_fma_f32 v40, v8, v87, v40\nv_fma_f32 v41, v9, v87, v41\ns_nop 1\n" \
"v_fma_f32 v48, v16, v87, v48\nv_fma_f32 v49, v17, v87, v49\nv_mul_f32 v40, v40, v88\nv_mul_f32 v41, v41, v88\nv_fma_f32 v91, v40, v40, s59\nv_fma_f32 v92, v41, v41, s59\nv_mul_f32 v91, v91, v40\nv_mul_f32 v92, v92, v41\n" \
"v_exp_f32 v91, v91\nv_exp_f32 v92, v92\nv_mul_f32 v40, v40, v48\nv_mul_f32 v41, v41, v49\nv_fma_f32 v91, v91, v89, v89\nv_fma_f32 v92, v92, v89, v89\nv_rcp_f32 v91, v91\nv_rcp_f32 v92, v92\n" \
"v_mul_f32 v40, v40, v91\nv_mul_f32 v41, v41, v92\nv_cvt_pk_bf16_f32 v48, v40, v41\nv_fma_f32 v42, v10, v87, v42\nv_fma_f32 v43, v11, v87, v43\nv_fma_f32 v50, v18, v87, v50\nv_fma_f32 v51, v19, v87, v51\nv_mul_f32 v42, v42, v88\n" \
"v_mul_f32 v43, v43, v88\nv_fma_f32 v91, v42, v42, s59\nv_fma_f32 v92, v43, v43, s59\nv_mul_f32 v91, v91, v42\nv_mul_f32 v92, v92, v43\nv_exp_f32 v91, v91\nv_exp_f32 v92, v92\nv_mul_f32 v42, v42, v50\n" \
"v_mul_f32 v43, v43, v51\nv_fma_f32 v91, v91, v89, v89\nv_fma_f32 v92, v92, v89, v89\nv_rcp_f32 v91, v91\nv_rcp_f32 v92, v92\nv_mul_f32 v42, v42, v91\nv_mul_f32 v43, v43, v92\nv_cvt_pk_bf16_f32 v49, v42, v43\n" \
"v_fma_f32 v44, v12, v87, v44\nv_fma_f32 v45, v13, v87, v45\nv_fma_f32 v52, v20, v87, v52\nv_fma_f32 v53, v21, v87, v53\nv_mul_f32 v44, v44, v88\nv_mul_f32 v45, v45, v88\nv_fma_f32 v91, v44, v44, s59\nv_fma_f32 v92, v45, v45, s59\n" \
"v_mul_f32 v91, v91, v44\nv_mul_f32 v92, v92, v45\nv_exp_f32 v91, v91\nv_exp_f32 v92, v92\nv_mul_f32 v44, v44, v52\nv_mul_f32 v45, v45, v53\nv_fma_f32 v91, v91, v89, v89\nv_fma_f32 v92, v92, v89, v89\n" \
"v_rcp_f32 v91, v91\nv_rcp_f32 v92, v92\nv_mul_f32 v44, v44, v91\nv_mul_f32 v45, v45, v92\nv_cvt_pk_bf16_f32 v50, v44, v45\nv_fma_f32 v46, v14, v87, v46\nv_fma_f32 v47, v15, v87, v47\nv_fma_f32 v54, v22, v87, v54\n" \
"v_fma_f32 v55, v23, v87, v55\nv_mul_f32 v46, v46, v88\nv_mul_f32 v47, v47, v88\nv_fma_f32 v91, v46, v46, s59\nv_fma_f32 v92, v47, v47, s59\nv_mul_f32 v91, v91, v46\nv_mul_f32 v92, v92, v47\nv_exp_f32 v91, v91\n" \
"v_exp_f32 v92, v92\nv_mul_f32 v46, v46, v54\nv_mul_f32 v47, v47, v55\nv_fma_f32 v91, v91, v89, v89\nv_fma_f32 v92, v92, v89, v89\nv_rcp_f32 v91, v91\nv_rcp_f32 v92, v92\nv_mul_f32 v46, v46, v91\n" \
"v_mul_f32 v47, v47, v92\nv_cvt_pk_bf16_f32 v51, v46, v47\nbuffer_store_dwordx4 v[48:51], v4, %[rs], s55 offen offset:1024 sc1\nv_add_u32 v90, 1, v90\ns_mov_b32 s56, %[fh]\ns_add_u32 s40, s40, 1\ns_cmp_lt_u32 s40, 64\ns_cbranch_scc1 .Lgg_next%=\n" \
"s_waitcnt vmcnt(0)\ns_barrier\nbuffer_store_dword v90, off, %[rs], s56 sc1\ns_branch .Lgg_end%=\n.Lgg_abort%=:\ns_mov_b64 exec, -1\nv_mov_b32 v16, 0\ns_movk_i32 s44, 6400\n" \
"buffer_store_dword v16, off, %[rs], s44 sc1\n.Lgg_end%=:\ns_waitcnt vmcnt(0) lgkmcnt(0)\n" \
""
#define DD_ASM \
"v_mbcnt_lo_u32_b32 v255, -1, 0\nv_mbcnt_hi_u32_b32 v255, -1, v255\nv_lshlrev_b32 v255, 2, v255\nv_add_u32 v255, %[tb], v255\nds_read_b32 v0, v255 offset:0\nds_read_b32 v1, v255 offset:256\nds_read_b32 v2, v255 offset:512\nds_read_b32 v3, v255 offset:768\n" \
"ds_read_b32 v4, v255 offset:1024\nds_read_b32 v5, v255 offset:1280\nds_read_b32 v6, v255 offset:1536\nds_read_b32 v7, v255 offset:1792\nds_read_b32 v125, v255 offset:2048\nds_read_b32 v126, v255 offset:2304\nds_read_b32 v127, v255 offset:2560\ns_waitcnt lgkmcnt(0)\n" \
"s_add_u32 m0, %[tb], 8192\ns_add_u32 s44, %[wo], 0\nbuffer_load_dwordx4 v125, %[rw], s44 offen nt lds\ns_add_u32 m0, %[tb], 9216\ns_add_u32 s44, %[wo], 16384\nbuffer_load_dwordx4 v125, %[rw], s44 offen nt lds\ns_add_u32 m0, %[tb], 10240\ns_add_u32 s44, %[wo], 32768\n" \
"buffer_load_dwordx4 v125, %[rw], s44 offen nt lds\ns_add_u32 m0, %[tb], 11264\ns_add_u32 s44, %[wo], 49152\nbuffer_load_dwordx4 v125, %[rw], s44 offen nt lds\ns_add_u32 m0, %[lw], 0\ns_add_u32 s44, %[wo], 65536\nbuffer_load_dwordx4 v125, %[rw], s44 offen nt lds\ns_add_u32 m0, %[lw], 1024\n" \
"s_add_u32 s44, %[wo], 81920\nbuffer_load_dwordx4 v125, %[rw], s44 offen nt lds\ns_add_u32 m0, %[lw], 2048\ns_add_u32 s44, %[wo], 98304\nbuffer_load_dwordx4 v125, %[rw], s44 offen nt lds\ns_add_u32 m0, %[lw], 3072\ns_add_u32 s44, %[wo], 114688\nbuffer_load_dwordx4 v125, %[rw], s44 offen nt lds\n" \
"s_add_u32 m0, %[lw], 4096\ns_add_u32 s44, %[wo], 131072\nbuffer_load_dwordx4 v125, %[rw], s44 offen nt lds\ns_add_u32 m0, %[lw], 5120\ns_add_u32 s44, %[wo], 147456\nbuffer_load_dwordx4 v125, %[rw], s44 offen nt lds\ns_add_u32 m0, %[lw], 6144\ns_add_u32 s44, %[wo], 163840\n" \
"buffer_load_dwordx4 v125, %[rw], s44 offen nt lds\ns_add_u32 m0, %[lw], 7168\ns_add_u32 s44, %[wo], 180224\nbuffer_load_dwordx4 v125, %[rw], s44 offen nt lds\ns_add_u32 m0, %[lw], 8192\ns_add_u32 s44, %[wo], 196608\nbuffer_load_dwordx4 v125, %[rw], s44 offen nt lds\ns_add_u32 m0, %[lw], 9216\n" \
"s_add_u32 s44, %[wo], 212992\nbuffer_load_dwordx4 v125, %[rw], s44 offen nt lds\ns_add_u32 m0, %[lw], 10240\ns_add_u32 s44, %[wo], 229376\nbuffer_load_dwordx4 v125, %[rw], s44 offen nt lds\ns_add_u32 m0, %[lw], 11264\ns_add_u32 s44, %[wo], 245760\nbuffer_load_dwordx4 v125, %[rw], s44 offen nt lds\n" \
"s_add_u32 m0, %[lw], 12288\ns_add_u32 s44, %[wo], 262144\nbuffer_load_dwordx4 v125, %[rw], s44 offen nt lds\ns_add_u32 m0, %[lw], 13312\ns_add_u32 s44, %[wo], 278528\nbuffer_load_dwordx4 v125, %[rw], s44 offen nt lds\ns_add_u32 m0, %[lw], 14336\ns_add_u32 s44, %[wo], 294912\n" \
"buffer_load_dwordx4 v125, %[rw], s44 offen nt lds\ns_add_u32 m0, %[lw], 15360\ns_add_u32 s44, %[wo], 311296\nbuffer_load_dwordx4 v125, %[rw], s44 offen nt lds\ns_add_u32 m0, %[lw], 16384\ns_add_u32 s44, %[wo], 327680\nbuffer_load_dwordx4 v125, %[rw], s44 offen nt lds\ns_add_u32 m0, %[lw], 17408\n" \
"s_add_u32 s44, %[wo], 344064\nbuffer_load_dwordx4 v125, %[rw], s44 offen nt lds\ns_add_u32 m0, %[lw], 18432\ns_add_u32 s44, %[wo], 360448\nbuffer_load_dwordx4 v125, %[rw], s44 offen nt lds\ns_add_u32 m0, %[lw], 19456\ns_add_u32 s44, %[wo], 376832\nbuffer_load_dwordx4 v125, %[rw], s44 offen nt lds\n" \
"s_add_u32 m0, %[lw], 20480\ns_add_u32 s44, %[wo], 393216\nbuffer_load_dwordx4 v125, %[rw], s44 offen nt lds\ns_add_u32 m0, %[lw], 21504\ns_add_u32 s44, %[wo], 409600\nbuffer_load_dwordx4 v125, %[rw], s44 offen nt lds\ns_add_u32 m0, %[lw], 22528\ns_add_u32 s44, %[wo], 425984\n" \
"buffer_load_dwordx4 v125, %[rw], s44 offen nt lds\ns_add_u32 m0, %[lw], 23552\ns_add_u32 s44, %[wo], 442368\nbuffer_load_dwordx4 v125, %[rw], s44 offen nt lds\ns_add_u32 m0, %[tb], 0\ns_add_u32 s44, %[wo], 458752\nbuffer_load_dwordx4 v125, %[rw], s44 offen nt lds\ns_add_u32 m0, %[tb], 1024\n" \
"s_add_u32 s44, %[wo], 475136\nbuffer_load_dwordx4 v125, %[rw], s44 offen nt lds\ns_add_u32 m0, %[tb], 2048\ns_add_u32 s44, %[wo], 491520\nbuffer_load_dwordx4 v125, %[rw], s44 offen nt lds\ns_add_u32 m0, %[tb], 3072\ns_add_u32 s44, %[wo], 507904\nbuffer_load_dwordx4 v125, %[rw], s44 offen nt lds\n" \
"s_add_u32 m0, %[tb], 4096\ns_add_u32 s44, %[wo], 524288\nbuffer_load_dwordx4 v125, %[rw], s44 offen nt lds\ns_add_u32 m0, %[tb], 5120\ns_add_u32 s44, %[wo], 540672\nbuffer_load_dwordx4 v125, %[rw], s44 offen nt lds\ns_add_u32 m0, %[tb], 6144\ns_add_u32 s44, %[wo], 557056\n" \
"buffer_load_dwordx4 v125, %[rw], s44 offen nt lds\ns_add_u32 m0, %[tb], 7168\ns_add_u32 s44, %[wo], 573440\nbuffer_load_dwordx4 v125, %[rw], s44 offen nt lds\ns_waitcnt vmcnt(32)\nds_read_b64_tr_b16 a[0:1], v126 offset:8192\nds_read_b64_tr_b16 a[2:3], v126 offset:8704\nds_read_b64_tr_b16 a[8:9], v126 offset:8224\n" \
"ds_read_b64_tr_b16 a[10:11], v126 offset:8736\nds_read_b64_tr_b16 a[16:17], v126 offset:8256\nds_read_b64_tr_b16 a[18:19], v126 offset:8768\nds_read_b64_tr_b16 a[24:25], v126 offset:8288\nds_read_b64_tr_b16 a[26:27], v126 offset:8800\ns_waitcnt lgkmcnt(0)\ns_add_u32 m0, %[tb], 8192\ns_add_u32 s44, %[wo], 589824\n" \
"buffer_load_dwordx4 v125, %[rw], s44 offen nt lds\ns_add_u32 m0, %[tb], 9216\ns_add_u32 s44, %[wo], 606208\nbuffer_load_dwordx4 v125, %[rw], s44 offen nt lds\ns_add_u32 m0, %[tb], 10240\ns_add_u32 s44, %[wo], 622592\nbuffer_load_dwordx4 v125, %[rw], s44 offen nt lds\ns_add_u32 m0, %[tb], 11264\n" \
"s_add_u32 s44, %[wo], 638976\nbuffer_load_dwordx4 v125, %[rw], s44 offen nt lds\ns_waitcnt vmcnt(32)\nds_read_b64_tr_b16 a[4:5], v127 offset:0\nds_read_b64_tr_b16 a[6:7], v127 offset:512\nds_read_b64_tr_b16 a[12:13], v127 offset:32\nds_read_b64_tr_b16 a[14:15], v127 offset:544\nds_read_b64_tr_b16 a[20:21], v127 offset:64\n" \
"ds_read_b64_tr_b16 a[22:23], v127 offset:576\nds_read_b64_tr_b16 a[28:29], v127 offset:96\nds_read_b64_tr_b16 a[30:31], v127 offset:608\ns_waitcnt lgkmcnt(0)\ns_add_u32 m0, %[lw], 0\ns_add_u32 s44, %[wo], 655360\nbuffer_load_dwordx4 v125, %[rw], s44 offen nt lds\ns_add_u32 m0, %[lw], 1024\n" \
"s_add_u32 s44, %[wo], 671744\nbuffer_load_dwordx4 v125, %[rw], s44 offen nt lds\ns_add_u32 m0, %[lw], 2048\ns_add_u32 s44, %[wo], 688128\nbuffer_load_dwordx4 v125, %[rw], s44 offen nt lds\ns_add_u32 m0, %[lw], 3072\ns_add_u32 s44, %[wo], 704512\nbuffer_load_dwordx4 v125, %[rw], s44 offen nt lds\n" \
"s_waitcnt vmcnt(32)\nds_read_b64_tr_b16 a[32:33], v127 offset:4096\nds_read_b64_tr_b16 a[34:35], v127 offset:4608\nds_read_b64_tr_b16 v[92:93], v127 offset:4128\nds_read_b64_tr_b16 v[94:95], v127 offset:4640\nds_read_b64_tr_b16 a[40:41], v127 offset:4160\nds_read_b64_tr_b16 a[42:43], v127 offset:4672\nds_read_b64_tr_b16 a[48:49], v127 offset:4192\n" \
"ds_read_b64_tr_b16 a[50:51], v127 offset:4704\ns_waitcnt lgkmcnt(0)\ns_add_u32 m0, %[lw], 4096\ns_add_u32 s44, %[wo], 720896\nbuffer_load_dwordx4 v125, %[rw], s44 offen nt lds\ns_add_u32 m0, %[lw], 5120\ns_add_u32 s44, %[wo], 737280\nbuffer_load_dwordx4 v125, %[rw], s44 offen nt lds\n" \
"s_add_u32 m0, %[lw], 6144\ns_add_u32 s44, %[wo], 753664\nbuffer_load_dwordx4 v125, %[rw], s44 offen nt lds\ns_add_u32 m0, %[lw], 7168\ns_add_u32 s44, %[wo], 770048\nbuffer_load_dwordx4 v125, %[rw], s44 offen nt lds\ns_waitcnt vmcnt(32)\nds_read_b64_tr_b16 a[36:37], v127 offset:8192\n" \
"ds_read_b64_tr_b16 a[38:39], v127 offset:8704\nds_read_b64_tr_b16 v[88:89], v127 offset:8224\nds_read_b64_tr_b16 v[90:91], v127 offset:8736\nds_read_b64_tr_b16 a[44:45], v127 offset:8256\nds_read_b64_tr_b16 a[46:47], v127 offset:8768\nds_read_b64_tr_b16 a[52:53], v127 offset:8288\nds_read_b64_tr_b16 a[54:55], v127 offset:8800\ns_waitcnt lgkmcnt(0)\n" \
"s_add_u32 m0, %[lw], 8192\ns_add_u32 s44, %[wo], 786432\nbuffer_load_dwordx4 v125, %[rw], s44 offen nt lds\ns_add_u32 m0, %[lw], 9216\ns_add_u32 s44, %[wo], 802816\nbuffer_load_dwordx4 v125, %[rw], s44 offen nt lds\ns_add_u32 m0, %[lw], 10240\ns_add_u32 s44, %[wo], 819200\n" \
"buffer_load_dwordx4 v125, %[rw], s44 offen nt lds\ns_add_u32 m0, %[lw], 11264\ns_add_u32 s44, %[wo], 835584\nbuffer_load_dwordx4 v125, %[rw], s44 offen nt lds\ns_waitcnt vmcnt(32)\nds_read_b64_tr_b16 a[56:57], v127 offset:12288\nds_read_b64_tr_b16 a[58:59], v127 offset:12800\nds_read_b64_tr_b16 a[64:65], v127 offset:12320\n" \
"ds_read_b64_tr_b16 a[66:67], v127 offset:12832\nds_read_b64_tr_b16 a[72:73], v127 offset:12352\nds_read_b64_tr_b16 a[74:75], v127 offset:12864\nds_read_b64_tr_b16 a[80:81], v127 offset:12384\nds_read_b64_tr_b16 a[82:83], v127 offset:12896\ns_waitcnt lgkmcnt(0)\ns_add_u32 m0, %[lw], 12288\ns_add_u32 s44, %[wo], 851968\n" \
"buffer_load_dwordx4 v125, %[rw], s44 offen nt lds\ns_add_u32 m0, %[lw], 13312\ns_add_u32 s44, %[wo], 868352\nbuffer_load_dwordx4 v125, %[rw], s44 offen nt lds\ns_add_u32 m0, %[lw], 14336\ns_add_u32 s44, %[wo], 884736\nbuffer_load_dwordx4 v125, %[rw], s44 offen nt lds\ns_add_u32 m0, %[lw], 15360\n" \
"s_add_u32 s44, %[wo], 901120\nbuffer_load_dwordx4 v125, %[rw], s44 offen nt lds\ns_waitcnt vmcnt(32)\nds_read_b64_tr_b16 a[60:61], v127 offset:16384\nds_read_b64_tr_b16 a[62:63], v127 offset:16896\nds_read_b64_tr_b16 a[68:69], v127 offset:16416\nds_read_b64_tr_b16 a[70:71], v127 offset:16928\nds_read_b64_tr_b16 a[76:77], v127 offset:16448\n" \
"ds_read_b64_tr_b16 a[78:79], v127 offset:16960\nds_read_b64_tr_b16 a[84:85], v127 offset:16480\nds_read_b64_tr_b16 a[86:87], v127 offset:16992\ns_waitcnt lgkmcnt(0)\ns_add_u32 m0, %[lw], 16384\ns_add_u32 s44, %[wo], 917504\nbuffer_load_dwordx4 v125, %[rw], s44 offen nt lds\ns_add_u32 m0, %[lw], 17408\n" \
"s_add_u32 s44, %[wo], 933888\nbuffer_load_dwordx4 v125, %[rw], s44 offen nt lds\ns_add_u32 m0, %[lw], 18432\ns_add_u32 s44, %[wo], 950272\nbuffer_load_dwordx4 v125, %[rw], s44 offen nt lds\ns_add_u32 m0, %[lw], 19456\ns_add_u32 s44, %[wo], 966656\nbuffer_load_dwordx4 v125, %[rw], s44 offen nt lds\n" \
"s_waitcnt vmcnt(32)\nds_read_b64_tr_b16 a[88:89], v127 offset:20480\nds_read_b64_tr_b16 a[90:91], v127 offset:20992\nds_read_b64_tr_b16 a[96:97], v127 offset:20512\nds_read_b64_tr_b16 a[98:99], v127 offset:21024\nds_read_b64_tr_b16 a[104:105], v127 offset:20544\nds_read_b64_tr_b16 a[106:107], v127 offset:21056\nds_read_b64_tr_b16 v[84:85], v127 offset:20576\n" \
"ds_read_b64_tr_b16 v[86:87], v127 offset:21088\ns_waitcnt lgkmcnt(0)\ns_add_u32 m0, %[lw], 20480\ns_add_u32 s44, %[wo], 983040\nbuffer_load_dwordx4 v125, %[rw], s44 offen nt lds\ns_add_u32 m0, %[lw], 21504\ns_add_u32 s44, %[wo], 999424\nbuffer_load_dwordx4 v125, %[rw], s44 offen nt lds\n" \
"s_add_u32 m0, %[lw], 22528\ns_add_u32 s44, %[wo], 1015808\nbuffer_load_dwordx4 v125, %[rw], s44 offen nt lds\ns_add_u32 m0, %[lw], 23552\ns_add_u32 s44, %[wo], 1032192\nbuffer_load_dwordx4 v125, %[rw], s44 offen nt lds\ns_waitcnt vmcnt(32)\nds_read_b64_tr_b16 a[92:93], v126 offset:0\n" \
"ds_read_b64_tr_b16 a[94:95], v126 offset:512\nds_read_b64_tr_b16 a[100:101], v126 offset:32\nds_read_b64_tr_b16 a[102:103], v126 offset:544\nds_read_b64_tr_b16 a[108:109], v126 offset:64\nds_read_b64_tr_b16 a[110:111], v126 offset:576\nds_read_b64_tr_b16 v[80:81], v126 offset:96\nds_read_b64_tr_b16 v[82:83], v126 offset:608\ns_waitcnt lgkmcnt(0)\n" \
"s_add_u32 m0, %[tb], 0\ns_add_u32 s44, %[wo], 1048576\nbuffer_load_dwordx4 v125, %[rw], s44 offen nt lds\ns_add_u32 m0, %[tb], 1024\ns_add_u32 s44, %[wo], 1064960\nbuffer_load_dwordx4 v125, %[rw], s44 offen nt lds\ns_add_u32 m0, %[tb], 2048\ns_add_u32 s44, %[wo], 1081344\n" \
"buffer_load_dwordx4 v125, %[rw], s44 offen nt lds\ns_add_u32 m0, %[tb], 3072\ns_add_u32 s44, %[wo], 1097728\nbuffer_load_dwordx4 v125, %[rw], s44 offen nt lds\ns_waitcnt vmcnt(32)\nds_read_b64_tr_b16 a[112:113], v126 offset:4096\nds_read_b64_tr_b16 a[114:115], v126 offset:4608\nds_read_b64_tr_b16 a[120:121], v126 offset:4128\n" \
"ds_read_b64_tr_b16 a[122:123], v126 offset:4640\nds_read_b64_tr_b16 a[128:129], v126 offset:4160\nds_read_b64_tr_b16 a[130:131], v126 offset:4672\nds_read_b64_tr_b16 a[136:137], v126 offset:4192\nds_read_b64_tr_b16 a[138:139], v126 offset:4704\ns_waitcnt lgkmcnt(0)\ns_add_u32 m0, %[tb], 4096\ns_add_u32 s44, %[wo], 1114112\n" \
"buffer_load_dwordx4 v125, %[rw], s44 offen nt lds\ns_add_u32 m0, %[tb], 5120\ns_add_u32 s44, %[wo], 1130496\nbuffer_load_dwordx4 v125, %[rw], s44 offen nt lds\ns_add_u32 m0, %[tb], 6144\ns_add_u32 s44, %[wo], 1146880\nbuffer_load_dwordx4 v125, %[rw], s44 offen nt lds\ns_add_u32 m0, %[tb], 7168\n" \
"s_add_u32 s44, %[wo], 1163264\nbuffer_load_dwordx4 v125, %[rw], s44 offen nt lds\ns_waitcnt vmcnt(32)\nds_read_b64_tr_b16 a[116:117], v126 offset:8192\nds_read_b64_tr_b16 a[118:119], v126 offset:8704\nds_read_b64_tr_b16 a[124:125], v126 offset:8224\nds_read_b64_tr_b16 a[126:127], v126 offset:8736\nds_read_b64_tr_b16 a[132:133], v126 offset:8256\n" \
"ds_read_b64_tr_b16 a[134:135], v126 offset:8768\nds_read_b64_tr_b16 a[140:141], v126 offset:8288\nds_read_b64_tr_b16 a[142:143], v126 offset:8800\ns_waitcnt lgkmcnt(0)\ns_add_u32 m0, %[tb], 8192\ns_add_u32 s44, %[wo], 1179648\nbuffer_load_dwordx4 v125, %[rw], s44 offen nt lds\ns_add_u32 m0, %[tb], 9216\n" \
"s_add_u32 s44, %[wo], 1196032\nbuffer_load_dwordx4 v125, %[rw], s44 offen nt lds\ns_add_u32 m0, %[tb], 10240\ns_add_u32 s44, %[wo], 1212416\nbuffer_load_dwordx4 v125, %[rw], s44 offen nt lds\ns_add_u32 m0, %[tb], 11264\ns_add_u32 s44, %[wo], 1228800\nbuffer_load_dwordx4 v125, %[rw], s44 offen nt lds\n" \
"s_waitcnt vmcnt(32)\nds_read_b64_tr_b16 a[144:145], v127 offset:0\nds_read_b64_tr_b16 a[146:147], v127 offset:512\nds_read_b64_tr_b16 v[76:77], v127 offset:32\nds_read_b64_tr_b16 v[78:79], v127 offset:544\nds_read_b64_tr_b16 a[152:153], v127 offset:64\nds_read_b64_tr_b16 a[154:155], v127 offset:576\nds_read_b64_tr_b16 a[160:161], v127 offset:96\n" \
"ds_read_b64_tr_b16 a[162:163], v127 offset:608\ns_waitcnt lgkmcnt(0)\ns_add_u32 m0, %[lw], 0\ns_add_u32 s44, %[wo], 1245184\nbuffer_load_dwordx4 v125, %[rw], s44 offen nt lds\ns_add_u32 m0, %[lw], 1024\ns_add_u32 s44, %[wo], 1261568\nbuffer_load_dwordx4 v125, %[rw], s44 offen nt lds\n" \
"s_add_u32 m0, %[lw], 2048\ns_add_u32 s44, %[wo], 1277952\nbuffer_load_dwordx4 v125, %[rw], s44 offen nt lds\ns_add_u32 m0, %[lw], 3072\ns_add_u32 s44, %[wo], 1294336\nbuffer_load_dwordx4 v125, %[rw], s44 offen nt lds\ns_waitcnt vmcnt(32)\nds_read_b64_tr_b16 a[148:149], v127 offset:4096\n" \
"ds_read_b64_tr_b16 a[150:151], v127 offset:4608\nds_read_b64_tr_b16 v[72:73], v127 offset:4128\nds_read_b64_tr_b16 v[74:75], v127 offset:4640\nds_read_b64_tr_b16 a[156:157], v127 offset:4160\nds_read_b64_tr_b16 a[158:159], v127 offset:4672\nds_read_b64_tr_b16 a[164:165], v127 offset:4192\nds_read_b64_tr_b16 a[166:167], v127 offset:4704\ns_waitcnt lgkmcnt(0)\n" \
"s_add_u32 m0, %[lw], 4096\ns_add_u32 s44, %[wo], 1310720\nbuffer_load_dwordx4 v125, %[rw], s44 offen nt lds\ns_add_u32 m0, %[lw], 5120\ns_add_u32 s44, %[wo], 1327104\nbuffer_load_dwordx4 v125, %[rw], s44 offen nt lds\ns_add_u32 m0, %[lw], 6144\ns_add_u32 s44, %[wo], 1343488\n" \
"buffer_load_dwordx4 v125, %[rw], s44 offen nt lds\ns_add_u32 m0, %[lw], 7168\ns_add_u32 s44, %[wo], 1359872\nbuffer_load_dwordx4 v125, %[rw], s44 offen nt lds\ns_waitcnt vmcnt(32)\nds_read_b64_tr_b16 a[168:169], v127 offset:8192\nds_read_b64_tr_b16 a[170:171], v127 offset:8704\nds_read_b64_tr_b16 a[176:177], v127 offset:8224\n" \
"ds_read_b64_tr_b16 a[178:179], v127 offset:8736\nds_read_b64_tr_b16 v[68:69], v127 offset:8256\nds_read_b64_tr_b16 v[70:71], v127 offset:8768\nds_read_b64_tr_b16 a[184:185], v127 offset:8288\nds_read_b64_tr_b16 a[186:187], v127 offset:8800\ns_waitcnt lgkmcnt(0)\ns_add_u32 m0, %[lw], 8192\ns_add_u32 s44, %[wo], 1376256\n" \
"buffer_load_dwordx4 v125, %[rw], s44 offen nt lds\ns_add_u32 m0, %[lw], 9216\ns_add_u32 s44, %[wo], 1392640\nbuffer_load_dwordx4 v125, %[rw], s44 offen nt lds\ns_add_u32 m0, %[lw], 10240\ns_add_u32 s44, %[wo], 1409024\nbuffer_load_dwordx4 v125, %[rw], s44 offen nt lds\ns_add_u32 m0, %[lw], 11264\n" \
"s_add_u32 s44, %[wo], 1425408\nbuffer_load_dwordx4 v125, %[rw], s44 offen nt lds\ns_waitcnt vmcnt(32)\nds_read_b64_tr_b16 a[172:173], v127 offset:12288\nds_read_b64_tr_b16 a[174:175], v127 offset:12800\nds_read_b64_tr_b16 a[180:181], v127 offset:12320\nds_read_b64_tr_b16 a[182:183], v127 offset:12832\nds_read_b64_tr_b16 v[64:65], v127 offset:12352\n" \
"ds_read_b64_tr_b16 v[66:67], v127 offset:12864\nds_read_b64_tr_b16 a[188:189], v127 offset:12384\nds_read_b64_tr_b16 a[190:191], v127 offset:12896\ns_waitcnt lgkmcnt(0)\ns_add_u32 m0, %[lw], 12288\ns_add_u32 s44, %[wo], 1441792\nbuffer_load_dwordx4 v125, %[rw], s44 offen nt lds\ns_add_u32 m0, %[lw], 13312\n" \
"s_add_u32 s44, %[wo], 1458176\nbuffer_load_dwordx4 v125, %[rw], s44 offen nt lds\ns_add_u32 m0, %[lw], 14336\ns_add_u32 s44, %[wo], 1474560\nbuffer_load_dwordx4 v125, %[rw], s44 offen nt lds\ns_add_u32 m0, %[lw], 15360\ns_add_u32 s44, %[wo], 1490944\nbuffer_load_dwordx4 v125, %[rw], s44 offen nt lds\n" \
"s_waitcnt vmcnt(32)\nds_read_b64_tr_b16 a[192:193], v127 offset:16384\nds_read_b64_tr_b16 a[194:195], v127 offset:16896\nds_read_b64_tr_b16 a[200:201], v127 offset:16416\nds_read_b64_tr_b16 a[202:203], v127 offset:16928\nds_read_b64_tr_b16 a[208:209], v127 offset:16448\nds_read_b64_tr_b16 a[210:211], v127 offset:16960\nds_read_b64_tr_b16 a[216:217], v127 offset:16480\n" \
"ds_read_b64_tr_b16 a[218:219], v127 offset:16992\ns_waitcnt lgkmcnt(0)\ns_add_u32 m0, %[lw], 16384\ns_add_u32 s44, %[wo], 1507328\nbuffer_load_dwordx4 v125, %[rw], s44 offen nt lds\ns_add_u32 m0, %[lw], 17408\ns_add_u32 s44, %[wo], 1523712\nbuffer_load_dwordx4 v125, %[rw], s44 offen nt lds\n" \
"s_add_u32 m0, %[lw], 18432\ns_add_u32 s44, %[wo], 1540096\nbuffer_load_dwordx4 v125, %[rw], s44 offen nt lds\ns_add_u32 m0, %[lw], 19456\ns_add_u32 s44, %[wo], 1556480\nbuffer_load_dwordx4 v125, %[rw], s44 offen nt lds\ns_waitcnt vmcnt(32)\nds_read_b64_tr_b16 a[196:197], v127 offset:20480\n" \
"ds_read_b64_tr_b16 a[198:199], v127 offset:20992\nds_read_b64_tr_b16 a[204:205], v127 offset:20512\nds_read_b64_tr_b16 a[206:207], v127 offset:21024\nds_read_b64_tr_b16 a[212:213], v127 offset:20544\nds_read_b64_tr_b16 a[214:215], v127 offset:21056\nds_read_b64_tr_b16 a[220:221], v127 offset:20576\nds_read_b64_tr_b16 a[222:223], v127 offset:21088\ns_waitcnt lgkmcnt(0)\n" \
"s_add_u32 m0, %[lw], 20480\ns_add_u32 s44, %[wo], 1572864\nbuffer_load_dwordx4 v125, %[rw], s44 offen nt lds\ns_add_u32 m0, %[lw], 21504\ns_add_u32 s44, %[wo], 1589248\nbuffer_load_dwordx4 v125, %[rw], s44 offen nt lds\ns_add_u32 m0, %[lw], 22528\ns_add_u32 s44, %[wo], 1605632\n" \
"buffer_load_dwordx4 v125, %[rw], s44 offen nt lds\ns_add_u32 m0, %[lw], 23552\ns_add_u32 s44, %[wo], 1622016\nbuffer_load_dwordx4 v125, %[rw], s44 offen nt lds\ns_waitcnt vmcnt(32)\nds_read_b64_tr_b16 v[60:61], v126 offset:0\nds_read_b64_tr_b16 v[62:63], v126 offset:512\nds_read_b64_tr_b16 a[224:225], v126 offset:32\n" \
"ds_read_b64_tr_b16 a[226:227], v126 offset:544\nds_read_b64_tr_b16 a[232:233], v126 offset:64\nds_read_b64_tr_b16 a[234:235], v126 offset:576\nds_read_b64_tr_b16 a[240:241], v126 offset:96\nds_read_b64_tr_b16 a[242:243], v126 offset:608\ns_waitcnt lgkmcnt(0)\ns_add_u32 m0, %[tb], 0\ns_add_u32 s44, %[wo], 1638400\n" \
"buffer_load_dwordx4 v125, %[rw], s44 offen nt lds\ns_add_u32 m0, %[tb], 1024\ns_add_u32 s44, %[wo], 1654784\nbuffer_load_dwordx4 v125, %[rw], s44 offen nt lds\ns_add_u32 m0, %[tb], 2048\ns_add_u32 s44, %[wo], 1671168\nbuffer_load_dwordx4 v125, %[rw], s44 offen nt lds\ns_add_u32 m0, %[tb], 3072\n" \
"s_add_u32 s44, %[wo], 1687552\nbuffer_load_dwordx4 v125, %[rw], s44 offen nt lds\ns_waitcnt vmcnt(32)\nds_read_b64_tr_b16 v[56:57], v126 offset:4096\nds_read_b64_tr_b16 v[58:59], v126 offset:4608\nds_read_b64_tr_b16 a[228:229], v126 offset:4128\nds_read_b64_tr_b16 a[230:231], v126 offset:4640\nds_read_b64_tr_b16 a[236:237], v126 offset:4160\n" \
"ds_read_b64_tr_b16 a[238:239], v126 offset:4672\nds_read_b64_tr_b16 a[244:245], v126 offset:4192\nds_read_b64_tr_b16 a[246:247], v126 offset:4704\ns_waitcnt lgkmcnt(0)\ns_add_u32 m0, %[tb], 4096\ns_add_u32 s44, %[wo], 1703936\nbuffer_load_dwordx4 v125, %[rw], s44 offen nt lds\ns_add_u32 m0, %[tb], 5120\n" \
"s_add_u32 s44, %[wo], 1720320\nbuffer_load_dwordx4 v125, %[rw], s44 offen nt lds\ns_add_u32 m0, %[tb], 6144\ns_add_u32 s44, %[wo], 1736704\nbuffer_load_dwordx4 v125, %[rw], s44 offen nt lds\ns_add_u32 m0, %[tb], 7168\ns_add_u32 s44, %[wo], 1753088\nbuffer_load_dwordx4 v125, %[rw], s44 offen nt lds\n" \
"s_waitcnt vmcnt(32)\nds_read_b64_tr_b16 a[248:249], v126 offset:8192\nds_read_b64_tr_b16 a[250:251], v126 offset:8704\nds_read_b64_tr_b16 v[128:129], v126 offset:8224\nds_read_b64_tr_b16 v[130:131], v126 offset:8736\nds_read_b64_tr_b16 v[136:137], v126 offset:8256\nds_read_b64_tr_b16 v[138:139], v126 offset:8768\nds_read_b64_tr_b16 v[144:145], v126 offset:8288\n" \
"ds_read_b64_tr_b16 v[146:147], v126 offset:8800\ns_waitcnt lgkmcnt(0)\ns_add_u32 m0, %[tb], 8192\ns_add_u32 s44, %[wo], 1769472\nbuffer_load_dwordx4 v125, %[rw], s44 offen nt lds\ns_add_u32 m0, %[tb], 9216\ns_add_u32 s44, %[wo], 1785856\nbuffer_load_dwordx4 v125, %[rw], s44 offen nt lds\n" \
"s_add_u32 m0, %[tb], 10240\ns_add_u32 s44, %[wo], 1802240\nbuffer_load_dwordx4 v125, %[rw], s44 offen nt lds\ns_add_u32 m0, %[tb], 11264\ns_add_u32 s44, %[wo], 1818624\nbuffer_load_dwordx4 v125, %[rw], s44 offen nt lds\ns_waitcnt vmcnt(32)\nds_read_b64_tr_b16 a[252:253], v127 offset:0\n" \
"ds_read_b64_tr_b16 a[254:255], v127 offset:512\nds_read_b64_tr_b16 v[132:133], v127 offset:32\nds_read_b64_tr_b16 v[134:135], v127 offset:544\nds_read_b64_tr_b16 v[140:141], v127 offset:64\nds_read_b64_tr_b16 v[142:143], v127 offset:576\nds_read_b64_tr_b16 v[148:149], v127 offset:96\nds_read_b64_tr_b16 v[150:151], v127 offset:608\ns_waitcnt lgkmcnt(0)\n" \
"ds_write_b128 v1, v[92:95] offset:0\nds_write_b128 v1, v[88:91] offset:1024\nds_write_b128 v1, v[84:87] offset:2048\nds_write_b128 v1, v[80:83] offset:3072\ns_waitcnt vmcnt(28)\nds_read_b64_tr_b16 v[152:153], v127 offset:4096\nds_read_b64_tr_b16 v[154:155], v127 offset:4608\nds_read_b64_tr_b16 v[160:161], v127 offset:4128\n" \
"ds_read_b64_tr_b16 v[162:163], v127 offset:4640\nds_read_b64_tr_b16 v[80:81], v127 offset:4160\nds_read_b64_tr_b16 v[82:83], v127 offset:4672\nds_read_b64_tr_b16 v[168:169], v127 offset:4192\nds_read_b64_tr_b16 v[170:171], v127 offset:4704\ns_waitcnt lgkmcnt(0)\nds_write_b128 v1, v[76:79] offset:4096\nds_write_b128 v1, v[72:75] offset:5120\n" \
"ds_write_b128 v1, v[68:71] offset:6144\nds_write_b128 v1, v[64:67] offset:7168\ns_waitcnt vmcnt(24)\nds_read_b64_tr_b16 v[156:157], v127 offset:8192\nds_read_b64_tr_b16 v[158:159], v127 offset:8704\nds_read_b64_tr_b16 v[164:165], v127 offset:8224\nds_read_b64_tr_b16 v[166:167], v127 offset:8736\nds_read_b64_tr_b16 v[64:65], v127 offset:8256\n" \
"ds_read_b64_tr_b16 v[66:67], v127 offset:8768\nds_read_b64_tr_b16 v[172:173], v127 offset:8288\nds_read_b64_tr_b16 v[174:175], v127 offset:8800\ns_waitcnt lgkmcnt(0)\nds_write_b128 v1, v[60:63] offset:8192\nds_write_b128 v1, v[56:59] offset:9216\nds_write_b128 v1, v[80:83] offset:10240\nds_write_b128 v1, v[64:67] offset:11264\n" \
"s_waitcnt vmcnt(20)\nds_read_b64_tr_b16 v[176:177], v127 offset:12288\nds_read_b64_tr_b16 v[178:179], v127 offset:12800\nds_read_b64_tr_b16 v[184:185], v127 offset:12320\nds_read_b64_tr_b16 v[186:187], v127 offset:12832\nds_read_b64_tr_b16 v[192:193], v127 offset:12352\nds_read_b64_tr_b16 v[194:195], v127 offset:12864\nds_read_b64_tr_b16 v[200:201], v127 offset:12384\n" \
"ds_read_b64_tr_b16 v[202:203], v127 offset:12896\ns_waitcnt lgkmcnt(0)\ns_waitcnt vmcnt(16)\nds_read_b64_tr_b16 v[180:181], v127 offset:16384\nds_read_b64_tr_b16 v[182:183], v127 offset:16896\nds_read_b64_tr_b16 v[188:189], v127 offset:16416\nds_read_b64_tr_b16 v[190:191], v127 offset:16928\nds_read_b64_tr_b16 v[196:197], v127 offset:16448\n" \
"ds_read_b64_tr_b16 v[198:199], v127 offset:16960\nds_read_b64_tr_b16 v[204:205], v127 offset:16480\nds_read_b64_tr_b16 v[206:207], v127 offset:16992\ns_waitcnt lgkmcnt(0)\ns_waitcnt vmcnt(12)\nds_read_b64_tr_b16 v[64:65], v127 offset:20480\nds_read_b64_tr_b16 v[66:67], v127 offset:20992\nds_read_b64_tr_b16 v[208:209], v127 offset:20512\n" \
"ds_read_b64_tr_b16 v[210:211], v127 offset:21024\nds_read_b64_tr_b16 v[216:217], v127 offset:20544\nds_read_b64_tr_b16 v[218:219], v127 offset:21056\nds_read_b64_tr_b16 v[224:225], v127 offset:20576\nds_read_b64_tr_b16 v[226:227], v127 offset:21088\ns_waitcnt lgkmcnt(0)\nds_write_b128 v1, v[64:67] offset:12288\ns_waitcnt vmcnt(8)\n" \
"ds_read_b64_tr_b16 v[64:65], v126 offset:0\nds_read_b64_tr_b16 v[66:67], v126 offset:512\nds_read_b64_tr_b16 v[212:213], v126 offset:32\nds_read_b64_tr_b16 v[214:215], v126 offset:544\nds_read_b64_tr_b16 v[220:221], v126 offset:64\nds_read_b64_tr_b16 v[222:223], v126 offset:576\nds_read_b64_tr_b16 v[228:229], v126 offset:96\nds_read_b64_tr_b16 v[230:231], v126 offset:608\n" \
"s_waitcnt lgkmcnt(0)\nds_write_b128 v1, v[64:67] offset:13312\ns_waitcnt vmcnt(4)\nds_read_b64_tr_b16 v[232:233], v126 offset:4096\nds_read_b64_tr_b16 v[234:235], v126 offset:4608\nds_read_b64_tr_b16 v[64:65], v126 offset:4128\nds_read_b64_tr_b16 v[66:67], v126 offset:4640\nds_read_b64_tr_b16 v[240:241], v126 offset:4160\n" \
"ds_read_b64_tr_b16 v[242:243], v126 offset:4672\nds_read_b64_tr_b16 v[248:249], v126 offset:4192\nds_read_b64_tr_b16 v[250:251], v126 offset:4704\ns_waitcnt lgkmcnt(0)\nds_write_b128 v1, v[64:67] offset:14336\ns_waitcnt vmcnt(0)\nds_read_b64_tr_b16 v[236:237], v126 offset:8192\nds_read_b64_tr_b16 v[238:239], v126 offset:8704\n" \
"ds_read_b64_tr_b16 v[64:65], v126 offset:8224\nds_read_b64_tr_b16 v[66:67], v126 offset:8736\nds_read_b64_tr_b16 v[244:245], v126 offset:8256\nds_read_b64_tr_b16 v[246:247], v126 offset:8768\nds_read_b64_tr_b16 v[252:253], v126 offset:8288\nds_read_b64_tr_b16 v[254:255], v126 offset:8800\ns_waitcnt lgkmcnt(0)\nds_write_b128 v1, v[64:67] offset:15360\n" \
"s_waitcnt lgkmcnt(0)\ns_barrier\ns_mov_b32 s40, 0\ns_mov_b32 s43, 0\ns_mov_b32 s56, -2147483648\n.Ldd_next%=:\nbuffer_load_dword v8, v7, %[rs], 0 offen sc1\ns_lshl_b32 s58, s40, 1\n" \
"s_lshl_b32 s44, s40, 19\ns_add_u32 s44, s44, %[ho]\ns_lshr_b32 s45, s44, 8\ns_and_b32 s45, s45, 0x2000\ns_xor_b32 s55, s44, s45\ns_waitcnt vmcnt(0)\nbuffer_store_dword v9, off, %[rs], s56 sc1\n.Ldd_w0%=:\n" \
"v_cmp_gt_i32 vcc, s58, v8\ns_cbranch_vccz .Ldd_w0go%=\ns_cmp_ge_u32 s40, 62\ns_cbranch_scc1 .Ldd_ns666%=\ns_sleep 16\n.Ldd_ns666%=:\ns_sleep 1\nbuffer_load_dword v8, v7, %[rs], 0 offen sc1\n" \
"s_waitcnt vmcnt(0)\ns_add_u32 s43, s43, 1\ns_cmp_lt_u32 s43, 262144\ns_cbranch_scc1 .Ldd_w0%=\ns_branch .Ldd_abort%=\n.Ldd_w0go%=:\ns_add_u32 s60, s55, 0\nbuffer_load_dwordx4 v[48:51], v0, %[rs], s60 offen offset:0 sc1\n" \
"buffer_load_dwordx4 v[52:55], v0, %[rs], s60 offen offset:2048 sc1\ns_add_u32 s60, s55, 4096\nbuffer_load_dwordx4 v[56:59], v0, %[rs], s60 offen offset:0 sc1\nbuffer_load_dwordx4 v[60:63], v0, %[rs], s60 offen offset:2048 sc1\ns_add_u32 s60, s55, 16384\nbuffer_load_dwordx4 v[64:67], v0, %[rs], s60 offen offset:0 sc1\nbuffer_load_dwordx4 v[68:71], v0, %[rs], s60 offen offset:2048 sc1\ns_add_u32 s60, s55, 20480\n" \
"buffer_load_dwordx4 v[72:75], v0, %[rs], s60 offen offset:0 sc1\nbuffer_load_dwordx4 v[76:79], v0, %[rs], s60 offen offset:2048 sc1\ns_add_u32 s60, s55, 32768\nbuffer_load_dwordx4 v[80:83], v0, %[rs], s60 offen offset:0 sc1\nbuffer_load_dwordx4 v[84:87], v0, %[rs], s60 offen offset:2048 sc1\ns_add_u32 s60, s55, 36864\nbuffer_load_dwordx4 v[88:91], v0, %[rs], s60 offen offset:0 sc1\nbuffer_load_dwordx4 v[92:95], v0, %[rs], s60 offen offset:2048 sc1\n" \
"s_add_u32 s60, s55, 49152\nbuffer_load_dwordx4 v[96:99], v0, %[rs], s60 offen offset:0 sc1\nbuffer_load_dwordx4 v[100:103], v0, %[rs], s60 offen offset:2048 sc1\ns_add_u32 s60, s55, 53248\nbuffer_load_dwordx4 v[104:107], v0, %[rs], s60 offen offset:0 sc1\nbuffer_load_dwordx4 v[108:111], v0, %[rs], s60 offen offset:2048 sc1\ns_add_u32 s44, s40, 0\ns_lshl_b32 s44, s44, 17\n" \
"s_add_u32 s44, s44, %[xn]\ns_lshr_b32 s45, s44, 8\ns_and_b32 s45, s45, 0x2000\ns_xor_b32 s41, s44, s45\ns_lshl_b32 s44, s40, 16\ns_cmp_eq_u32 %[l], 0\ns_cselect_b32 s42, s41, s44\ns_add_u32 s58, s58, 1\n" \
"ds_read_b128 v[120:123], v1 offset:0\ns_waitcnt vmcnt(15)\nv_mfma_f32_16x16x32_bf16 v[16:19], a[0:3], v[48:51], 0\nds_read_b128 v[124:127], v1 offset:1024\nv_mfma_f32_16x16x32_bf16 v[24:27], a[8:11], v[48:51], 0\nbuffer_load_dword v8, v7, %[rs], 0 offen sc1\nv_mfma_f32_16x16x32_bf16 v[32:35], a[16:19], v[48:51], 0\ns_add_u32 s60, s55, 65536\n" \
"v_mfma_f32_16x16x32_bf16 v[40:43], a[24:27], v[48:51], 0\ns_waitcnt vmcnt(15)\nbuffer_load_dwordx4 v[48:51], v0, %[rs], s60 offen offset:0 sc1\nv_mfma_f32_16x16x32_bf16 v[16:19], a[4:7], v[52:55], v[16:19]\nv_mfma_f32_16x16x32_bf16 v[24:27], a[12:15], v[52:55], v[24:27]\nv_mfma_f32_16x16x32_bf16 v[32:35], a[20:23], v[52:55], v[32:35]\nv_mfma_f32_16x16x32_bf16 v[40:43], a[28:31], v[52:55], v[40:43]\ns_waitcnt vmcnt(15)\n" \
"buffer_load_dwordx4 v[52:55], v0, %[rs], s60 offen offset:2048 sc1\nv_mfma_f32_16x16x32_bf16 v[16:19], a[32:35], v[56:59], v[16:19]\ns_waitcnt lgkmcnt(1)\nv_mfma_f32_16x16x32_bf16 v[24:27], v[120:123], v[56:59], v[24:27]\nv_mfma_f32_16x16x32_bf16 v[32:35], a[40:43], v[56:59], v[32:35]\ns_add_u32 s60, s55, 69632\nv_mfma_f32_16x16x32_bf16 v[40:43], a[48:51], v[56:59], v[40:43]\ns_waitcnt vmcnt(15)\n" \
"buffer_load_dwordx4 v[56:59], v0, %[rs], s60 offen offset:0 sc1\nv_mfma_f32_16x16x32_bf16 v[16:19], a[36:39], v[60:63], v[16:19]\ns_waitcnt lgkmcnt(0)\nds_read_b128 v[120:123], v1 offset:2048\nv_mfma_f32_16x16x32_bf16 v[24:27], v[124:127], v[60:63], v[24:27]\nv_mfma_f32_16x16x32_bf16 v[32:35], a[44:47], v[60:63], v[32:35]\nv_mfma_f32_16x16x32_bf16 v[40:43], a[52:55], v[60:63], v[40:43]\ns_waitcnt vmcnt(15)\n" \
"buffer_load_dwordx4 v[60:63], v0, %[rs], s60 offen offset:2048 sc1\nv_mfma_f32_16x16x32_bf16 v[16:19], a[56:59], v[64:67], v[16:19]\nds_read_b128 v[124:127], v1 offset:3072\nv_mfma_f32_16x16x32_bf16 v[24:27], a[64:67], v[64:67], v[24:27]\nv_mfma_f32_16x16x32_bf16 v[32:35], a[72:75], v[64:67], v[32:35]\ns_add_u32 s60, s55, 81920\nv_mfma_f32_16x16x32_bf16 v[40:43], a[80:83], v[64:67], v[40:43]\ns_waitcnt vmcnt(15)\n" \
"buffer_load_dwordx4 v[64:67], v0, %[rs], s60 offen offset:0 sc1\nv_mfma_f32_16x16x32_bf16 v[16:19], a[60:63], v[68:71], v[16:19]\nv_mfma_f32_16x16x32_bf16 v[24:27], a[68:71], v[68:71], v[24:27]\nv_mfma_f32_16x16x32_bf16 v[32:35], a[76:79], v[68:71], v[32:35]\nv_mfma_f32_16x16x32_bf16 v[40:43], a[84:87], v[68:71], v[40:43]\ns_waitcnt vmcnt(15)\nbuffer_load_dwordx4 v[68:71], v0, %[rs], s60 offen offset:2048 sc1\nv_mfma_f32_16x16x32_bf16 v[16:19], a[88:91], v[72:75], v[16:19]\n" \
"v_mfma_f32_16x16x32_bf16 v[24:27], a[96:99], v[72:75], v[24:27]\nv_mfma_f32_16x16x32_bf16 v[32:35], a[104:107], v[72:75], v[32:35]\ns_waitcnt lgkmcnt(1)\ns_add_u32 s60, s55, 86016\nv_mfma_f32_16x16x32_bf16 v[40:43], v[120:123], v[72:75], v[40:43]\ns_waitcnt vmcnt(15)\nbuffer_load_dwordx4 v[72:75], v0, %[rs], s60 offen offset:0 sc1\nv_mfma_f32_16x16x32_bf16 v[16:19], a[92:95], v[76:79], v[16:19]\n" \
"ds_read_b128 v[120:123], v1 offset:4096\nv_mfma_f32_16x16x32_bf16 v[24:27], a[100:103], v[76:79], v[24:27]\nv_mfma_f32_16x16x32_bf16 v[32:35], a[108:111], v[76:79], v[32:35]\ns_waitcnt lgkmcnt(1)\nv_mfma_f32_16x16x32_bf16 v[40:43], v[124:127], v[76:79], v[40:43]\ns_waitcnt vmcnt(15)\nbuffer_load_dwordx4 v[76:79], v0, %[rs], s60 offen offset:2048 sc1\nv_mfma_f32_16x16x32_bf16 v[16:19], a[112:115], v[80:83], v[16:19]\n" \
"ds_read_b128 v[124:127], v1 offset:5120\nv_mfma_f32_16x16x32_bf16 v[24:27], a[120:123], v[80:83], v[24:27]\nv_mfma_f32_16x16x32_bf16 v[32:35], a[128:131], v[80:83], v[32:35]\ns_add_u32 s60, s55, 98304\nv_mfma_f32_16x16x32_bf16 v[40:43], a[136:139], v[80:83], v[40:43]\ns_waitcnt vmcnt(15)\nbuffer_load_dwordx4 v[80:83], v0, %[rs], s60 offen offset:0 sc1\nv_mfma_f32_16x16x32_bf16 v[16:19], a[116:119], v[84:87], v[16:19]\n" \
"ds_read_b128 v[112:115], v1 offset:6144\nv_mfma_f32_16x16x32_bf16 v[24:27], a[124:127], v[84:87], v[24:27]\nv_mfma_f32_16x16x32_bf16 v[32:35], a[132:135], v[84:87], v[32:35]\nv_mfma_f32_16x16x32_bf16 v[40:43], a[140:143], v[84:87], v[40:43]\ns_waitcnt vmcnt(15)\nbuffer_load_dwordx4 v[84:87], v0, %[rs], s60 offen offset:2048 sc1\nv_mfma_f32_16x16x32_bf16 v[16:19], a[144:147], v[88:91], v[16:19]\ns_waitcnt lgkmcnt(2)\n" \
"ds_read_b128 v[116:119], v1 offset:7168\nv_mfma_f32_16x16x32_bf16 v[24:27], v[120:123], v[88:91], v[24:27]\nv_mfma_f32_16x16x32_bf16 v[32:35], a[152:155], v[88:91], v[32:35]\ns_add_u32 s60, s55, 102400\nv_mfma_f32_16x16x32_bf16 v[40:43], a[160:163], v[88:91], v[40:43]\ns_waitcnt vmcnt(15)\nbuffer_load_dwordx4 v[88:91], v0, %[rs], s60 offen offset:0 sc1\nv_mfma_f32_16x16x32_bf16 v[16:19], a[148:151], v[92:95], v[16:19]\n" \
"s_waitcnt lgkmcnt(2)\nv_mfma_f32_16x16x32_bf16 v[24:27], v[124:127], v[92:95], v[24:27]\ns_waitcnt vmcnt(11)\nv_cmp_gt_i32 vcc, s58, v8\nv_mfma_f32_16x16x32_bf16 v[32:35], a[156:159], v[92:95], v[32:35]\ns_cmp_eq_u64 vcc, 0\ns_cselect_b32 s61, s55, -2147483648\nv_mfma_f32_16x16x32_bf16 v[40:43], a[164:167], v[92:95], v[40:43]\n" \
"buffer_load_dwordx4 v[92:95], v0, %[rs], s60 offen offset:2048 sc1\nv_mfma_f32_16x16x32_bf16 v[16:19], a[168:171], v[96:99], v[16:19]\nv_mfma_f32_16x16x32_bf16 v[24:27], a[176:179], v[96:99], v[24:27]\ns_waitcnt lgkmcnt(1)\nv_mfma_f32_16x16x32_bf16 v[32:35], v[112:115], v[96:99], v[32:35]\nv_mfma_f32_16x16x32_bf16 v[40:43], a[184:187], v[96:99], v[40:43]\ns_add_u32 s60, s61, 0\nbuffer_load_dwordx4 v[96:99], v0, %[rs], s60 offen offset:1024 sc1\n" \
"v_mfma_f32_16x16x32_bf16 v[16:19], a[172:175], v[100:103], v[16:19]\nds_read_b128 v[112:115], v1 offset:8192\nv_mfma_f32_16x16x32_bf16 v[24:27], a[180:183], v[100:103], v[24:27]\ns_waitcnt lgkmcnt(1)\nv_mfma_f32_16x16x32_bf16 v[32:35], v[116:119], v[100:103], v[32:35]\nv_mfma_f32_16x16x32_bf16 v[40:43], a[188:191], v[100:103], v[40:43]\nbuffer_load_dwordx4 v[100:103], v0, %[rs], s60 offen offset:3072 sc1\nv_mfma_f32_16x16x32_bf16 v[16:19], a[192:195], v[104:107], v[16:19]\n" \
"ds_read_b128 v[116:119], v1 offset:9216\nv_mfma_f32_16x16x32_bf16 v[24:27], a[200:203], v[104:107], v[24:27]\nv_mfma_f32_16x16x32_bf16 v[32:35], a[208:211], v[104:107], v[32:35]\nv_mfma_f32_16x16x32_bf16 v[40:43], a[216:219], v[104:107], v[40:43]\ns_add_u32 s60, s61, 4096\nbuffer_load_dwordx4 v[104:107], v0, %[rs], s60 offen offset:1024 sc1\nv_mfma_f32_16x16x32_bf16 v[16:19], a[196:199], v[108:111], v[16:19]\nv_mfma_f32_16x16x32_bf16 v[24:27], a[204:207], v[108:111], v[24:27]\n" \
"v_mfma_f32_16x16x32_bf16 v[32:35], a[212:215], v[108:111], v[32:35]\nv_mfma_f32_16x16x32_bf16 v[40:43], a[220:223], v[108:111], v[40:43]\ns_waitcnt vmcnt(14)\ns_waitcnt lgkmcnt(1)\nbuffer_load_dwordx4 v[108:111], v0, %[rs], s60 offen offset:3072 sc1\nv_mfma_f32_16x16x32_bf16 v[16:19], v[112:115], v[48:51], v[16:19]\nv_mfma_f32_16x16x32_bf16 v[24:27], a[224:227], v[48:51], v[24:27]\nv_mfma_f32_16x16x32_bf16 v[32:35], a[232:235], v[48:51], v[32:35]\n" \
"s_add_u32 s60, s61, 16384\nv_mfma_f32_16x16x32_bf16 v[40:43], a[240:243], v[48:51], v[40:43]\ns_waitcnt vmcnt(14)\ns_waitcnt lgkmcnt(0)\nbuffer_load_dwordx4 v[48:51], v0, %[rs], s60 offen offset:1024 sc1\nv_mfma_f32_16x16x32_bf16 v[16:19], v[116:119], v[52:55], v[16:19]\nds_read_b128 v[112:115], v1 offset:10240\nv_mfma_f32_16x16x32_bf16 v[24:27], a[228:231], v[52:55], v[24:27]\n" \
"v_mfma_f32_16x16x32_bf16 v[32:35], a[236:239], v[52:55], v[32:35]\nv_mfma_f32_16x16x32_bf16 v[40:43], a[244:247], v[52:55], v[40:43]\ns_waitcnt vmcnt(14)\nbuffer_load_dwordx4 v[52:55], v0, %[rs], s60 offen offset:3072 sc1\nv_mfma_f32_16x16x32_bf16 v[16:19], a[248:251], v[56:59], v[16:19]\nds_read_b128 v[116:119], v1 offset:11264\nv_mfma_f32_16x16x32_bf16 v[24:27], v[128:131], v[56:59], v[24:27]\nv_mfma_f32_16x16x32_bf16 v[32:35], v[136:139], v[56:59], v[32:35]\n" \
"s_add_u32 s60, s61, 20480\nv_mfma_f32_16x16x32_bf16 v[40:43], v[144:147], v[56:59], v[40:43]\ns_waitcnt vmcnt(14)\nbuffer_load_dwordx4 v[56:59], v0, %[rs], s60 offen offset:1024 sc1\nv_mfma_f32_16x16x32_bf16 v[16:19], a[252:255], v[60:63], v[16:19]\nv_mfma_f32_16x16x32_bf16 v[24:27], v[132:135], v[60:63], v[24:27]\nv_mfma_f32_16x16x32_bf16 v[32:35], v[140:143], v[60:63], v[32:35]\nv_mfma_f32_16x16x32_bf16 v[40:43], v[148:151], v[60:63], v[40:43]\n" \
"s_waitcnt vmcnt(14)\nbuffer_load_dwordx4 v[60:63], v0, %[rs], s60 offen offset:3072 sc1\nv_mfma_f32_16x16x32_bf16 v[16:19], v[152:155], v[64:67], v[16:19]\nv_mfma_f32_16x16x32_bf16 v[24:27], v[160:163], v[64:67], v[24:27]\ns_waitcnt lgkmcnt(1)\nv_mfma_f32_16x16x32_bf16 v[32:35], v[112:115], v[64:67], v[32:35]\ns_add_u32 s60, s61, 32768\nv_mfma_f32_16x16x32_bf16 v[40:43], v[168:171], v[64:67], v[40:43]\n" \
"s_waitcnt vmcnt(14)\nbuffer_load_dwordx4 v[64:67], v0, %[rs], s60 offen offset:1024 sc1\nv_mfma_f32_16x16x32_bf16 v[16:19], v[156:159], v[68:71], v[16:19]\nds_read_b128 v[112:115], v1 offset:12288\nv_mfma_f32_16x16x32_bf16 v[24:27], v[164:167], v[68:71], v[24:27]\ns_waitcnt lgkmcnt(1)\nv_mfma_f32_16x16x32_bf16 v[32:35], v[116:119], v[68:71], v[32:35]\nv_mfma_f32_16x16x32_bf16 v[40:43], v[172:175], v[68:71], v[40:43]\n" \
"s_waitcnt vmcnt(14)\nbuffer_load_dwordx4 v[68:71], v0, %[rs], s60 offen offset:3072 sc1\nv_mfma_f32_16x16x32_bf16 v[16:19], v[176:179], v[72:75], v[16:19]\nds_read_b128 v[116:119], v1 offset:13312\nv_mfma_f32_16x16x32_bf16 v[24:27], v[184:187], v[72:75], v[24:27]\nv_mfma_f32_16x16x32_bf16 v[32:35], v[192:195], v[72:75], v[32:35]\ns_add_u32 s60, s61, 36864\nv_mfma_f32_16x16x32_bf16 v[40:43], v[200:203], v[72:75], v[40:43]\n" \
"s_waitcnt vmcnt(14)\nbuffer_load_dwordx4 v[72:75], v0, %[rs], s60 offen offset:1024 sc1\nv_mfma_f32_16x16x32_bf16 v[16:19], v[180:183], v[76:79], v[16:19]\nds_read_b128 v[120:123], v1 offset:14336\nv_mfma_f32_16x16x32_bf16 v[24:27], v[188:191], v[76:79], v[24:27]\nv_mfma_f32_16x16x32_bf16 v[32:35], v[196:199], v[76:79], v[32:35]\nv_mfma_f32_16x16x32_bf16 v[40:43], v[204:207], v[76:79], v[40:43]\ns_waitcnt vmcnt(14)\n" \
"s_waitcnt lgkmcnt(2)\nbuffer_load_dwordx4 v[76:79], v0, %[rs], s60 offen offset:3072 sc1\nv_mfma_f32_16x16x32_bf16 v[16:19], v[112:115], v[80:83], v[16:19]\nds_read_b128 v[124:127], v1 offset:15360\nv_mfma_f32_16x16x32_bf16 v[24:27], v[208:211], v[80:83], v[24:27]\nv_mfma_f32_16x16x32_bf16 v[32:35], v[216:219], v[80:83], v[32:35]\ns_add_u32 s60, s61, 49152\nv_mfma_f32_16x16x32_bf16 v[40:43], v[224:227], v[80:83], v[40:43]\n" \
"s_waitcnt vmcnt(14)\ns_waitcnt lgkmcnt(2)\nbuffer_load_dwordx4 v[80:83], v0, %[rs], s60 offen offset:1024 sc1\nv_mfma_f32_16x16x32_bf16 v[16:19], v[116:119], v[84:87], v[16:19]\nv_mfma_f32_16x16x32_bf16 v[24:27], v[212:215], v[84:87], v[24:27]\nv_mfma_f32_16x16x32_bf16 v[32:35], v[220:223], v[84:87], v[32:35]\nv_mfma_f32_16x16x32_bf16 v[40:43], v[228:231], v[84:87], v[40:43]\ns_waitcnt vmcnt(14)\n" \
"buffer_load_dwordx4 v[84:87], v0, %[rs], s60 offen offset:3072 sc1\nv_mfma_f32_16x16x32_bf16 v[16:19], v[232:235], v[88:91], v[16:19]\ns_waitcnt lgkmcnt(1)\nv_mfma_f32_16x16x32_bf16 v[24:27], v[120:123], v[88:91], v[24:27]\nv_mfma_f32_16x16x32_bf16 v[32:35], v[240:243], v[88:91], v[32:35]\ns_add_u32 s60, s61, 53248\nv_mfma_f32_16x16x32_bf16 v[40:43], v[248:251], v[88:91], v[40:43]\ns_waitcnt vmcnt(14)\n" \
"buffer_load_dwordx4 v[88:91], v0, %[rs], s60 offen offset:1024 sc1\nv_mfma_f32_16x16x32_bf16 v[16:19], v[236:239], v[92:95], v[16:19]\ns_waitcnt lgkmcnt(0)\nds_read_b128 v[120:123], v1 offset:0\nv_mfma_f32_16x16x32_bf16 v[24:27], v[124:127], v[92:95], v[24:27]\nv_mfma_f32_16x16x32_bf16 v[32:35], v[244:247], v[92:95], v[32:35]\nv_mfma_f32_16x16x32_bf16 v[40:43], v[252:255], v[92:95], v[40:43]\nbuffer_load_dwordx4 v[92:95], v0, %[rs], s60 offen offset:3072 sc1\n" \
"s_cbranch_vccnz .Ldd_slow%=\n.Ldd_slowr%=:\ns_waitcnt vmcnt(15)\nv_mfma_f32_16x16x32_bf16 v[20:23], a[0:3], v[96:99], 0\nds_read_b128 v[124:127], v1 offset:1024\nv_mfma_f32_16x16x32_bf16 v[28:31], a[8:11], v[96:99], 0\nv_mfma_f32_16x16x32_bf16 v[36:39], a[16:19], v[96:99], 0\ns_add_u32 s60, s55, 65536\n" \
"v_mfma_f32_16x16x32_bf16 v[44:47], a[24:27], v[96:99], 0\ns_waitcnt vmcnt(14)\nbuffer_load_dwordx4 v[96:99], v0, %[rs], s60 offen offset:1024 sc1\nv_mfma_f32_16x16x32_bf16 v[20:23], a[4:7], v[100:103], v[20:23]\nv_mfma_f32_16x16x32_bf16 v[28:31], a[12:15], v[100:103], v[28:31]\nv_mfma_f32_16x16x32_bf16 v[36:39], a[20:23], v[100:103], v[36:39]\nv_mfma_f32_16x16x32_bf16 v[44:47], a[28:31], v[100:103], v[44:47]\ns_waitcnt vmcnt(14)\n" \
"buffer_load_dwordx4 v[100:103], v0, %[rs], s60 offen offset:3072 sc1\nv_mfma_f32_16x16x32_bf16 v[20:23], a[32:35], v[104:107], v[20:23]\ns_waitcnt lgkmcnt(1)\nv_mfma_f32_16x16x32_bf16 v[28:31], v[120:123], v[104:107], v[28:31]\nds_write_b128 v2, v[16:19] offset:0\nv_mfma_f32_16x16x32_bf16 v[36:39], a[40:43], v[104:107], v[36:39]\ns_add_u32 s60, s55, 69632\nv_mfma_f32_16x16x32_bf16 v[44:47], a[48:51], v[104:107], v[44:47]\n" \
"s_waitcnt vmcnt(14)\nbuffer_load_dwordx4 v[104:107], v0, %[rs], s60 offen offset:1024 sc1\nv_mfma_f32_16x16x32_bf16 v[20:23], a[36:39], v[108:111], v[20:23]\ns_waitcnt lgkmcnt(1)\nds_read_b128 v[120:123], v1 offset:2048\nv_mfma_f32_16x16x32_bf16 v[28:31], v[124:127], v[108:111], v[28:31]\nv_mfma_f32_16x16x32_bf16 v[36:39], a[44:47], v[108:111], v[36:39]\nv_mfma_f32_16x16x32_bf16 v[44:47], a[52:55], v[108:111], v[44:47]\n" \
"s_waitcnt vmcnt(14)\nbuffer_load_dwordx4 v[108:111], v0, %[rs], s60 offen offset:3072 sc1\nv_mfma_f32_16x16x32_bf16 v[20:23], a[56:59], v[48:51], v[20:23]\nds_read_b128 v[124:127], v1 offset:3072\nv_mfma_f32_16x16x32_bf16 v[28:31], a[64:67], v[48:51], v[28:31]\nds_write_b128 v2, v[24:27] offset:2048\nv_mfma_f32_16x16x32_bf16 v[36:39], a[72:75], v[48:51], v[36:39]\ns_add_u32 s60, s55, 81920\n" \
"v_mfma_f32_16x16x32_bf16 v[44:47], a[80:83], v[48:51], v[44:47]\ns_waitcnt vmcnt(14)\nbuffer_load_dwordx4 v[48:51], v0, %[rs], s60 offen offset:1024 sc1\nv_mfma_f32_16x16x32_bf16 v[20:23], a[60:63], v[52:55], v[20:23]\nv_mfma_f32_16x16x32_bf16 v[28:31], a[68:71], v[52:55], v[28:31]\nv_mfma_f32_16x16x32_bf16 v[36:39], a[76:79], v[52:55], v[36:39]\nv_mfma_f32_16x16x32_bf16 v[44:47], a[84:87], v[52:55], v[44:47]\ns_waitcnt vmcnt(14)\n" \
"buffer_load_dwordx4 v[52:55], v0, %[rs], s60 offen offset:3072 sc1\nv_mfma_f32_16x16x32_bf16 v[20:23], a[88:91], v[56:59], v[20:23]\nv_mfma_f32_16x16x32_bf16 v[28:31], a[96:99], v[56:59], v[28:31]\nds_write_b128 v2, v[32:35] offset:4096\nv_mfma_f32_16x16x32_bf16 v[36:39], a[104:107], v[56:59], v[36:39]\ns_waitcnt lgkmcnt(3)\ns_add_u32 s60, s55, 86016\nv_mfma_f32_16x16x32_bf16 v[44:47], v[120:123], v[56:59], v[44:47]\n" \
"s_waitcnt vmcnt(14)\nbuffer_load_dwordx4 v[56:59], v0, %[rs], s60 offen offset:1024 sc1\nv_mfma_f32_16x16x32_bf16 v[20:23], a[92:95], v[60:63], v[20:23]\nds_read_b128 v[120:123], v1 offset:4096\nv_mfma_f32_16x16x32_bf16 v[28:31], a[100:103], v[60:63], v[28:31]\nv_mfma_f32_16x16x32_bf16 v[36:39], a[108:111], v[60:63], v[36:39]\ns_waitcnt lgkmcnt(3)\nv_mfma_f32_16x16x32_bf16 v[44:47], v[124:127], v[60:63], v[44:47]\n" \
"s_waitcnt vmcnt(14)\nbuffer_load_dwordx4 v[60:63], v0, %[rs], s60 offen offset:3072 sc1\nv_mfma_f32_16x16x32_bf16 v[20:23], a[112:115], v[64:67], v[20:23]\nds_read_b128 v[124:127], v1 offset:5120\nv_mfma_f32_16x16x32_bf16 v[28:31], a[120:123], v[64:67], v[28:31]\nds_write_b128 v2, v[40:43] offset:6144\nv_mfma_f32_16x16x32_bf16 v[36:39], a[128:131], v[64:67], v[36:39]\ns_add_u32 s60, s55, 98304\n" \
"v_mfma_f32_16x16x32_bf16 v[44:47], a[136:139], v[64:67], v[44:47]\ns_waitcnt vmcnt(14)\nbuffer_load_dwordx4 v[64:67], v0, %[rs], s60 offen offset:1024 sc1\nv_mfma_f32_16x16x32_bf16 v[20:23], a[116:119], v[68:71], v[20:23]\nds_read_b128 v[112:115], v1 offset:6144\nv_mfma_f32_16x16x32_bf16 v[28:31], a[124:127], v[68:71], v[28:31]\nv_mfma_f32_16x16x32_bf16 v[36:39], a[132:135], v[68:71], v[36:39]\nv_mfma_f32_16x16x32_bf16 v[44:47], a[140:143], v[68:71], v[44:47]\n" \
"s_waitcnt vmcnt(14)\nbuffer_load_dwordx4 v[68:71], v0, %[rs], s60 offen offset:3072 sc1\nv_mfma_f32_16x16x32_bf16 v[20:23], a[144:147], v[72:75], v[20:23]\ns_waitcnt lgkmcnt(3)\nds_read_b128 v[116:119], v1 offset:7168\nv_mfma_f32_16x16x32_bf16 v[28:31], v[120:123], v[72:75], v[28:31]\nv_mfma_f32_16x16x32_bf16 v[36:39], a[152:155], v[72:75], v[36:39]\ns_add_u32 s60, s55, 102400\n" \
"v_mfma_f32_16x16x32_bf16 v[44:47], a[160:163], v[72:75], v[44:47]\ns_waitcnt vmcnt(14)\nbuffer_load_dwordx4 v[72:75], v0, %[rs], s60 offen offset:1024 sc1\nv_mfma_f32_16x16x32_bf16 v[20:23], a[148:151], v[76:79], v[20:23]\ns_waitcnt lgkmcnt(3)\nv_mfma_f32_16x16x32_bf16 v[28:31], v[124:127], v[76:79], v[28:31]\nv_mfma_f32_16x16x32_bf16 v[36:39], a[156:159], v[76:79], v[36:39]\nv_mfma_f32_16x16x32_bf16 v[44:47], a[164:167], v[76:79], v[44:47]\n" \
"s_waitcnt vmcnt(14)\nbuffer_load_dwordx4 v[76:79], v0, %[rs], s60 offen offset:3072 sc1\nv_mfma_f32_16x16x32_bf16 v[20:23], a[168:171], v[80:83], v[20:23]\nv_mfma_f32_16x16x32_bf16 v[28:31], a[176:179], v[80:83], v[28:31]\ns_waitcnt lgkmcnt(1)\nv_mfma_f32_16x16x32_bf16 v[36:39], v[112:115], v[80:83], v[36:39]\nv_mfma_f32_16x16x32_bf16 v[44:47], a[184:187], v[80:83], v[44:47]\ns_waitcnt vmcnt(14)\n" \
"buffer_load_dwordx4 v[12:15], v4, %[rs], s41 offen sc1\nv_mfma_f32_16x16x32_bf16 v[20:23], a[172:175], v[84:87], v[20:23]\nds_read_b128 v[112:115], v1 offset:8192\nv_mfma_f32_16x16x32_bf16 v[28:31], a[180:183], v[84:87], v[28:31]\ns_waitcnt lgkmcnt(1)\nv_mfma_f32_16x16x32_bf16 v[36:39], v[116:119], v[84:87], v[36:39]\nv_mfma_f32_16x16x32_bf16 v[44:47], a[188:191], v[84:87], v[44:47]\ns_waitcnt vmcnt(14)\n" \
"v_mfma_f32_16x16x32_bf16 v[20:23], a[192:195], v[88:91], v[20:23]\nds_read_b128 v[116:119], v1 offset:9216\nv_mfma_f32_16x16x32_bf16 v[28:31], a[200:203], v[88:91], v[28:31]\nv_mfma_f32_16x16x32_bf16 v[36:39], a[208:211], v[88:91], v[36:39]\nv_mfma_f32_16x16x32_bf16 v[44:47], a[216:219], v[88:91], v[44:47]\ns_waitcnt vmcnt(13)\nv_mfma_f32_16x16x32_bf16 v[20:23], a[196:199], v[92:95], v[20:23]\nv_mfma_f32_16x16x32_bf16 v[28:31], a[204:207], v[92:95], v[28:31]\n" \
"v_mfma_f32_16x16x32_bf16 v[36:39], a[212:215], v[92:95], v[36:39]\nv_mfma_f32_16x16x32_bf16 v[44:47], a[220:223], v[92:95], v[44:47]\ns_waitcnt vmcnt(12)\ns_waitcnt lgkmcnt(1)\nv_mfma_f32_16x16x32_bf16 v[20:23], v[112:115], v[96:99], v[20:23]\nv_mfma_f32_16x16x32_bf16 v[28:31], a[224:227], v[96:99], v[28:31]\nv_mfma_f32_16x16x32_bf16 v[36:39], a[232:235], v[96:99], v[36:39]\nv_mfma_f32_16x16x32_bf16 v[44:47], a[240:243], v[96:99], v[44:47]\n" \
"s_waitcnt vmcnt(11)\ns_waitcnt lgkmcnt(0)\nv_mfma_f32_16x16x32_bf16 v[20:23], v[116:119], v[100:103], v[20:23]\nds_read_b128 v[112:115], v1 offset:10240\nv_mfma_f32_16x16x32_bf16 v[28:31], a[228:231], v[100:103], v[28:31]\nv_mfma_f32_16x16x32_bf16 v[36:39], a[236:239], v[100:103], v[36:39]\nv_mfma_f32_16x16x32_bf16 v[44:47], a[244:247], v[100:103], v[44:47]\ns_waitcnt vmcnt(10)\n" \
"v_mfma_f32_16x16x32_bf16 v[20:23], a[248:251], v[104:107], v[20:23]\nds_read_b128 v[116:119], v1 offset:11264\nv_mfma_f32_16x16x32_bf16 v[28:31], v[128:131], v[104:107], v[28:31]\nv_mfma_f32_16x16x32_bf16 v[36:39], v[136:139], v[104:107], v[36:39]\nv_mfma_f32_16x16x32_bf16 v[44:47], v[144:147], v[104:107], v[44:47]\ns_waitcnt vmcnt(9)\nv_mfma_f32_16x16x32_bf16 v[20:23], a[252:255], v[108:111], v[20:23]\nv_mfma_f32_16x16x32_bf16 v[28:31], v[132:135], v[108:111], v[28:31]\n" \
"v_mfma_f32_16x16x32_bf16 v[36:39], v[140:143], v[108:111], v[36:39]\nv_mfma_f32_16x16x32_bf16 v[44:47], v[148:151], v[108:111], v[44:47]\ns_waitcnt vmcnt(8)\nv_mfma_f32_16x16x32_bf16 v[20:23], v[152:155], v[48:51], v[20:23]\nv_mfma_f32_16x16x32_bf16 v[28:31], v[160:163], v[48:51], v[28:31]\ns_waitcnt lgkmcnt(1)\nv_mfma_f32_16x16x32_bf16 v[36:39], v[112:115], v[48:51], v[36:39]\nv_mfma_f32_16x16x32_bf16 v[44:47], v[168:171], v[48:51], v[44:47]\n" \
"s_waitcnt vmcnt(7)\nv_mfma_f32_16x16x32_bf16 v[20:23], v[156:159], v[52:55], v[20:23]\nds_read_b128 v[112:115], v1 offset:12288\nv_mfma_f32_16x16x32_bf16 v[28:31], v[164:167], v[52:55], v[28:31]\ns_waitcnt lgkmcnt(1)\nv_mfma_f32_16x16x32_bf16 v[36:39], v[116:119], v[52:55], v[36:39]\nv_mfma_f32_16x16x32_bf16 v[44:47], v[172:175], v[52:55], v[44:47]\ns_waitcnt vmcnt(6)\n" \
"v_mfma_f32_16x16x32_bf16 v[20:23], v[176:179], v[56:59], v[20:23]\nds_read_b128 v[116:119], v1 offset:13312\nv_mfma_f32_16x16x32_bf16 v[28:31], v[184:187], v[56:59], v[28:31]\nv_mfma_f32_16x16x32_bf16 v[36:39], v[192:195], v[56:59], v[36:39]\nv_mfma_f32_16x16x32_bf16 v[44:47], v[200:203], v[56:59], v[44:47]\ns_waitcnt vmcnt(5)\nv_mfma_f32_16x16x32_bf16 v[20:23], v[180:183], v[60:63], v[20:23]\nds_read_b128 v[120:123], v1 offset:14336\n" \
"v_mfma_f32_16x16x32_bf16 v[28:31], v[188:191], v[60:63], v[28:31]\nv_mfma_f32_16x16x32_bf16 v[36:39], v[196:199], v[60:63], v[36:39]\nv_mfma_f32_16x16x32_bf16 v[44:47], v[204:207], v[60:63], v[44:47]\ns_waitcnt vmcnt(4)\ns_waitcnt lgkmcnt(2)\nv_mfma_f32_16x16x32_bf16 v[20:23], v[112:115], v[64:67], v[20:23]\nds_read_b128 v[124:127], v1 offset:15360\nv_mfma_f32_16x16x32_bf16 v[28:31], v[208:211], v[64:67], v[28:31]\n" \
"v_mfma_f32_16x16x32_bf16 v[36:39], v[216:219], v[64:67], v[36:39]\nv_mfma_f32_16x16x32_bf16 v[44:47], v[224:227], v[64:67], v[44:47]\ns_waitcnt vmcnt(3)\ns_waitcnt lgkmcnt(2)\nv_mfma_f32_16x16x32_bf16 v[20:23], v[116:119], v[68:71], v[20:23]\nv_mfma_f32_16x16x32_bf16 v[28:31], v[212:215], v[68:71], v[28:31]\nv_mfma_f32_16x16x32_bf16 v[36:39], v[220:223], v[68:71], v[36:39]\nv_mfma_f32_16x16x32_bf16 v[44:47], v[228:231], v[68:71], v[44:47]\n" \
"s_waitcnt vmcnt(2)\nv_mfma_f32_16x16x32_bf16 v[20:23], v[232:235], v[72:75], v[20:23]\ns_waitcnt lgkmcnt(1)\nv_mfma_f32_16x16x32_bf16 v[28:31], v[120:123], v[72:75], v[28:31]\nv_mfma_f32_16x16x32_bf16 v[36:39], v[240:243], v[72:75], v[36:39]\nv_mfma_f32_16x16x32_bf16 v[44:47], v[248:251], v[72:75], v[44:47]\ns_waitcnt vmcnt(1)\nv_mfma_f32_16x16x32_bf16 v[20:23], v[236:239], v[76:79], v[20:23]\n" \
"s_waitcnt vmcnt(0)\ns_waitcnt lgkmcnt(0)\nv_lshlrev_b32 v80, 16, v12\nv_and_b32 v81, 0xffff0000, v12\nv_mfma_f32_16x16x32_bf16 v[28:31], v[124:127], v[76:79], v[28:31]\nv_lshlrev_b32 v82, 16, v13\nv_and_b32 v83, 0xffff0000, v13\nv_mfma_f32_16x16x32_bf16 v[36:39], v[244:247], v[76:79], v[36:39]\n" \
"v_lshlrev_b32 v84, 16, v14\nv_and_b32 v85, 0xffff0000, v14\nv_mfma_f32_16x16x32_bf16 v[44:47], v[252:255], v[76:79], v[44:47]\ns_mul_i32 s44, s40, %[l]\nds_write_b128 v2, v[20:23] offset:1024\nds_write_b128 v2, v[28:31] offset:3072\ns_nop 0\nds_write_b128 v2, v[36:39] offset:5120\n" \
"s_nop 2\nds_write_b128 v2, v[44:47] offset:7168\nv_lshlrev_b32 v86, 16, v15\nv_and_b32 v87, 0xffff0000, v15\nv_mov_b32 v9, s40\ns_cmp_eq_u32 s44, 0\ns_cselect_b32 s56, %[fd], -2147483648\ns_add_u32 s40, s40, 1\n" \
"s_waitcnt lgkmcnt(0)\ns_barrier\nds_read_b128 v[16:19], v3 offset:0\nds_read_b128 v[20:23], v3 offset:256\nds_read_b128 v[24:27], v3 offset:8192\nds_read_b128 v[28:31], v3 offset:8448\nds_read_b128 v[32:35], v3 offset:16384\nds_read_b128 v[36:39], v3 offset:16640\n" \
"ds_read_b128 v[40:43], v3 offset:24576\nds_read_b128 v[44:47], v3 offset:24832\ns_waitcnt lgkmcnt(6)\nv_pk_add_f32 v[16:17], v[16:17], v[80:81]\nv_pk_add_f32 v[18:19], v[18:19], v[82:83]\nv_pk_add_f32 v[20:21], v[20:21], v[84:85]\nv_pk_add_f32 v[22:23], v[22:23], v[86:87]\ns_waitcnt lgkmcnt(4)\n" \
"v_pk_add_f32 v[16:17], v[16:17], v[24:25]\nv_pk_add_f32 v[18:19], v[18:19], v[26:27]\nv_pk_add_f32 v[20:21], v[20:21], v[28:29]\nv_pk_add_f32 v[22:23], v[22:23], v[30:31]\ns_waitcnt lgkmcnt(2)\nv_pk_add_f32 v[16:17], v[16:17], v[32:33]\nv_pk_add_f32 v[18:19], v[18:19], v[34:35]\nv_pk_add_f32 v[20:21], v[20:21], v[36:37]\n" \
"v_pk_add_f32 v[22:23], v[22:23], v[38:39]\ns_waitcnt lgkmcnt(0)\nv_pk_add_f32 v[16:17], v[16:17], v[40:41]\nv_pk_add_f32 v[18:19], v[18:19], v[42:43]\nv_pk_add_f32 v[20:21], v[20:21], v[44:45]\nv_pk_add_f32 v[22:23], v[22:23], v[46:47]\nv_cvt_pk_bf16_f32 v12, v16, v17\nv_cvt_pk_bf16_f32 v13, v18, v19\n" \
"v_cvt_pk_bf16_f32 v14, v20, v21\nv_cvt_pk_bf16_f32 v15, v22, v23\nbuffer_store_dwordx4 v[12:15], v5, %[ro], s42 offen sc1\nv_pk_add_f32 v[24:25], v[16:17], v[18:19]\nv_pk_add_f32 v[26:27], v[20:21], v[22:23]\nv_pk_add_f32 v[24:25], v[24:25], v[26:27]\nv_add_f32 v24, v24, v25\ns_nop 1\n" \
"v_add_f32_dpp v25, v24, v24 row_ror:8 row_mask:0xf bank_mask:0xf\nv_add_f32_dpp v26, v24, v24 row_ror:8 row_mask:0xf bank_mask:0xf\ns_nop 1\nv_permlane16_swap_b32 v25, v26\nv_add_f32 v24, v25, v26\nv_add_f32 v25, v25, v26\ns_nop 1\nv_permlane32_swap_b32 v24, v25\n" \
"v_add_f32 v24, v24, v25\nv_mul_f32 v24, 0x3c800000, v24\nv_pk_add_f32 v[16:17], v[16:17], v[24:25] op_sel_hi:[1,0] neg_lo:[0,1] neg_hi:[0,1]\nv_pk_add_f32 v[18:19], v[18:19], v[24:25] op_sel_hi:[1,0] neg_lo:[0,1] neg_hi:[0,1]\nv_pk_add_f32 v[20:21], v[20:21], v[24:25] op_sel_hi:[1,0] neg_lo:[0,1] neg_hi:[0,1]\nv_pk_add_f32 v[22:23], v[22:23], v[24:25] op_sel_hi:[1,0] neg_lo:[0,1] neg_hi:[0,1]\nv_pk_mul_f32 v[28:29], v[16:17], v[16:17]\nv_pk_mul_f32 v[30:31], v[18:19], v[18:19]\n" \
"v_pk_fma_f32 v[28:29], v[20:21], v[20:21], v[28:29]\nv_pk_fma_f32 v[30:31], v[22:23], v[22:23], v[30:31]\nv_pk_add_f32 v[28:29], v[28:29], v[30:31]\nv_add_f32 v28, v28, v29\ns_nop 1\nv_add_f32_dpp v29, v28, v28 row_ror:8 row_mask:0xf bank_mask:0xf\nv_add_f32_dpp v30, v28, v28 row_ror:8 row_mask:0xf bank_mask:0xf\ns_nop 1\n" \
"v_permlane16_swap_b32 v29, v30\nv_add_f32 v28, v29, v30\nv_add_f32 v29, v29, v30\ns_nop 1\nv_permlane32_swap_b32 v28, v29\nv_add_f32 v28, v28, v29\nv_mov_b32 v25, v28\nbuffer_store_dwordx2 v[24:25], v6, %[rs], s41 offen sc1\n" \
"v_xor_b32 v2, 0x8000, v2\nv_xor_b32 v3, 0x8000, v3\ns_cmp_lt_u32 s40, 64\ns_cbranch_scc1 .Ldd_next%=\ns_waitcnt vmcnt(0)\nbuffer_store_dword v9, off, %[rs], s56 sc1\ns_branch .Ldd_end%=\n.Ldd_slow%=:\n" \
".Ldd_w1%=:\nv_cmp_gt_i32 vcc, s58, v8\ns_cbranch_vccz .Ldd_w1go%=\ns_cmp_ge_u32 s40, 62\ns_cbranch_scc1 .Ldd_ns1219%=\ns_sleep 16\n.Ldd_ns1219%=:\ns_sleep 1\n" \
"buffer_load_dword v8, v7, %[rs], 0 offen sc1\ns_waitcnt vmcnt(0)\ns_add_u32 s43, s43, 1\ns_cmp_lt_u32 s43, 262144\ns_cbranch_scc1 .Ldd_w1%=\ns_branch .Ldd_abort%=\n.Ldd_w1go%=:\ns_add_u32 s60, s55, 0\n" \
"buffer_load_dwordx4 v[96:99], v0, %[rs], s60 offen offset:1024 sc1\nbuffer_load_dwordx4 v[100:103], v0, %[rs], s60 offen offset:3072 sc1\ns_add_u32 s60, s55, 4096\nbuffer_load_dwordx4 v[104:107], v0, %[rs], s60 offen offset:1024 sc1\nbuffer_load_dwordx4 v[108:111], v0, %[rs], s60 offen offset:3072 sc1\ns_add_u32 s60, s55, 16384\nbuffer_load_dwordx4 v[48:51], v0, %[rs], s60 offen offset:1024 sc1\nbuffer_load_dwordx4 v[52:55], v0, %[rs], s60 offen offset:3072 sc1\n" \
"s_add_u32 s60, s55, 20480\nbuffer_load_dwordx4 v[56:59], v0, %[rs], s60 offen offset:1024 sc1\nbuffer_load_dwordx4 v[60:63], v0, %[rs], s60 offen offset:3072 sc1\ns_add_u32 s60, s55, 32768\nbuffer_load_dwordx4 v[64:67], v0, %[rs], s60 offen offset:1024 sc1\nbuffer_load_dwordx4 v[68:71], v0, %[rs], s60 offen offset:3072 sc1\ns_add_u32 s60, s55, 36864\nbuffer_load_dwordx4 v[72:75], v0, %[rs], s60 offen offset:1024 sc1\n" \
"buffer_load_dwordx4 v[76:79], v0, %[rs], s60 offen offset:3072 sc1\ns_add_u32 s60, s55, 49152\nbuffer_load_dwordx4 v[80:83], v0, %[rs], s60 offen offset:1024 sc1\nbuffer_load_dwordx4 v[84:87], v0, %[rs], s60 offen offset:3072 sc1\ns_add_u32 s60, s55, 53248\nbuffer_load_dwordx4 v[88:91], v0, %[rs], s60 offen offset:1024 sc1\nbuffer_load_dwordx4 v[92:95], v0, %[rs], s60 offen offset:3072 sc1\ns_branch .Ldd_slowr%=\n" \
".Ldd_abort%=:\ns_mov_b64 exec, -1\nv_mov_b32 v16, 0\ns_movk_i32 s44, 6400\nbuffer_store_dword v16, off, %[rs], s44 sc1\n.Ldd_end%=:\ns_waitcnt vmcnt(0) lgkmcnt(0)\n" \
""
#define PX4 1
#define QX4 7
#define G1F 1
#define A1F 0

#define V10(p, x) p #x "0", p #x "1", p #x "2", p #x "3", p #x "4", p #x "5", p #x "6", p #x "7", p #x "8", p #x "9"
#define V256(p) V10(p, ), V10(p, 1), V10(p, 2), V10(p, 3), V10(p, 4), V10(p, 5), V10(p, 6), V10(p, 7), V10(p, 8), V10(p, 9), V10(p, 10), V10(p, 11), V10(p, 12), V10(p, 13), V10(p, 14), V10(p, 15), V10(p, 16), \
    V10(p, 17), V10(p, 18), V10(p, 19), V10(p, 20), V10(p, 21), V10(p, 22), V10(p, 23), V10(p, 24), p "250", p "251", p "252", p "253", p "254", p "255"
#define CLOB "memory", "vcc", "scc", V256("v"), V256("a"), V10("s", 4), V10("s", 5), V10("s", 6), V10("s", 7), V10("s", 8)
template <int N> DEVI void table(lds_char* p, const unsigned (&t)[N]) {
#pragma unroll
    for (int i = 0; i < N; ++i) *(l1*)(p + i * 256) = t[i];
    WL0;
}
DEVI unsigned prof_at(int cu, int w) { return O_PROF + (262144 + cu * 2560 + w * 8) * 4; }
DEVI unsigned sp_idx(int lane) { return (lane & 2) ? 0xEEEEu : 0x4444u; }
DEVI int sp_row(int r, int hf) { return 4 * (r >> 1) + 2 * hf + (r & 1); }

DEVI void qk_role(const Args& a, const int w, lds_char* smem, int l, int h) {
    const int lane = lane_id(), g = lane >> 4, n = lane & 15, b = n & 3, p = n >> 2;
    const unsigned l0 = (unsigned)(size_t)smem, tb = l0 + w * 12288, lw = l0 + 55296 + w * 24576, ls = l * SD, IMG = l0 + 32768, MIM = l0 + 49152;
    const unsigned xw = (w >> 1) * 16384 + (w & 1) * 4096, oc = (h >> 3) * 16384 + ls, tr = (8 * g + p) * 128 + b * 16;
    const unsigned cm[4] = {4 * g > n ? 0xF149F2CAu : 0u, 4 * g + 1 > n ? 0xF149F2CAu : 0u, 4 * g + 2 > n ? 0xF149F2CAu : 0u, 4 * g + 3 > n ? 0xF149F2CAu : 0u};
    const unsigned t[19] = {
        l0 + lane * 16, lw + lane * 16, l0 + (g >> 1) * 1024 + (g & 1) * 512 + n * 16, O_E1 + ls + xw + lane * 16, sp_idx(lane),
        IMG + (w >> 1) * 8192 + ((w & 1) * 8 + 2 * g) * 512 + n * 16,
        MIM + (w * 8 + 2 * p) * 144 + g * 32 + b * 4,
        IMG + g * 512 + (w ? n * 16 : 8192 + (16 * (b >> 1) + 8 * (p >> 1) + 2 * (p & 1) + (b & 1)) * 16),
        MIM + g * 144 + (n & 7) * 16, MIM + 2304 + g * 144 + (2 * p + (b & 1)) * 16,
        w ? O_PT + (h >> 3) * 32768 + ls + lane * 16 + (w == 1 ? 16384 + ((h & 7) >> 1) * 1024 + (h & 1) * 8 : (h & 7) * 1024)
          : (QX4 & 1) && n >> 3 ? OOB : O_PM + oc + ((h >> 1) & 3) * 1024 + ((2 * (h & 1) + (g >> 1)) * 16 + sp_row(n & 7, g & 1)) * 16 + (n >> 3) * 8,
        w ? (QX4 & 4) && g ? OOB : O_PT + (h >> 3) * 32768 + ls + 20480 + ((h >> 2) & 1) * 2048 + (w - 1) * 1024 + (h & 3) * 256 + n * 16 + g * 4
          : n >> 3 || ((QX4 & 2) && g >> 1) ? OOB : O_PM + oc + 4096 + ((h & 7) >> 1) * 256 + sp_row(n & 7, g & 1) * 16 + (h & 1) * 8 + (g >> 1) * 4,
        w == 3 ? (l ? F_X + SD + p * 128 + (X1F ? 0 : b * 4) : F_X0 + b * 4) : cm[0], cm[1], cm[2], cm[3],
        (lane >> 3) * 8192u + (lane & 7) * 16, tb + tr, lw + tr};
    table(smem + w * 12288 + lane * 4, t);
    asm volatile(QK_ASM : : [ws] "s"(a.ws), [sz] "n"(WS_RS), [rw] "s"(mkrs(w < 2 ? a.Wq : a.Wk, 8u << 20)), [wo] "s"(h * 256 + (w & 1) * 128), [tb] "s"(tb), [lw] "s"(lw), [l0] "s"(l0), [w] "s"(w),
        [xn] "s"(O_XN + ls), [xw] "s"(xw), [fs] "s"(l ? 0 : 128), [sg] "s"(F_Y0 + 4096 + ls + h * 128 + 16), [pr] "s"(prof_at(l * 80 + h, w)) : CLOB, "s36", "s37", "s38", "s39");
    __builtin_amdgcn_endpgm();
}
DEVI void vu_role(const Args& a, const int w, lds_char* smem, int l, int h) {
    const int lane = lane_id(), g = lane >> 4, n = lane & 15, b = n & 3, p = n >> 2;
    const unsigned l0 = (unsigned)(size_t)smem, tb = l0 + w * 12288, lw = l0 + 61440 + w * 24576, ls = l * SD;
    const unsigned xw = (w >> 1) * 16384 + (w & 1) * 4096, tv = (8 * g + p) * 128 + b * 16 + (w & 1) * 64, to = (4 * g + (p & 1)) * 128 + (p >> 1) * 32 + b * 8;
    const unsigned t[18] = {
        l0 + lane * 16, lw + lane * 16, l0 + (g >> 1) * 1024 + (g & 1) * 512 + n * 16, O_E1 + ls + xw + lane * 16, sp_idx(lane),
        l0 + 57344 + (2 * w + (n >> 3)) * 256 + (n >> 3) * 128 + g * 32 + (n & 7) * 2,
        O_UM + ls + (4 * w + (g & 1)) * 65536 + (h >> 3) * 16384 + (g >> 1) * 2048 + (h & 7) * 256 + n * 16,
        sd(O_Y + h * 131072 + ls) + w * 32768 + (g >> 1) * 1024 + (g & 1) * 256 + n * 16,
        l ? F_X + SD + p * 128 + (X1F ? 0 : b * 4) : F_X0 + b * 4, l0 + 49152 + (w >> 1) * 2048 + (2 * (w & 1) + (g >> 1)) * 256 + (n ^ (g >> 1) * 8) * 16 + (g & 1) * 8, sp_idx(lane >> 2), l0 + g * 256 + (g & 1) * 128 + (n & 7) * 16,
        (lane >> 3) * 8192u + (lane & 7) * 16, (8 * (lane >> 5) + ((lane >> 3) & 3)) * 2048u + (lane & 7) * 16, tb + tv - (w & 1) * 12288, lw + tv - (w & 1) * 24576, tb + to, lw + to};
    table(smem + w * 12288 + lane * 4, t);
    asm volatile(VU_ASM : : [ws] "s"(a.ws), [sz] "n"(WS_RS), [rw] "s"(mkrs(a.Wv, 8u << 20)), [ro] "s"(mkrs(a.Wo, 8u << 20)), [wo] "s"(h * 256 + (w >> 1) * 128 + (w & 1) * 262144), [w2] "s"(h * 262144 + w * 512), [tb] "s"(tb), [lw] "s"(lw), [w] "s"(w),
        [xn] "s"(O_XN + ls), [xw] "s"(xw), [fs] "s"(l ? 0 : 128), [xt] "s"(w * 16384 - xw), [fy] "s"(F_Y0 + ls + h * 128 + w * 4), [sg] "s"(w == 1 ? F_Y0 + ls + h * 128 + 16 : OOB), [pr] "s"(prof_at(l * 80 + 32 + h, w) - 128) : CLOB, "s36", "s37", "s38", "s39");
    __builtin_amdgcn_endpgm();
}
DEVI void rd_role(const Args& a, const int w, lds_char* smem, int l, int c) {
    const int lane = lane_id(), g = lane >> 4, n = lane & 15, r = 2 * (n >> 2) + (n & 1), hf = (n >> 1) & 1;
    const rsrc_t rs = mkrs(a.ws, WS_RS);
    const unsigned l0 = (unsigned)(size_t)smem, tb = l0 + 65536 + w * 4096, ls = l * SD, vp = O_E1 + ls + (c >> 3) * 16384 + (c & 7) * 1024 + w * 256 + n * 16;
    {
        const v4u ff = {SENT, SENT, SENT, SENT};
#pragma unroll
        for (int i = 0; i < 8; ++i) st16(ff, rs, O_UM + ls + c * 65536 + w * 16384 + i * 1024 + lane * 16);
#pragma unroll
        for (int i = 0; i < 2; ++i) st16(ff, rs, O_PT + ls + c * 16384 + w * 2048 + i * 1024 + lane * 16);
        st16(ff, rs, c < 8 ? O_PM + ls + w * 16384 + c * 1024 + lane * 16 : OOB);
        st16(ff, rs, g ? vp + g * 32768 : OOB);
        WV0; BAR;
    }
    st4(g & 1 ? *(const unsigned*)(a.mem0 + r * 1024 + c * 64 + w * 16 + 4 * g + 2 * hf) : nosent(*(const unsigned*)(a.mem0 + r * 1024 + c * 64 + w * 16 + 4 * g + 2 * hf)), rs, g & 1 ? vp + g * 4 : OOB, 0);
    WV0;
    st4(nosent(*(const unsigned*)(a.mem0 + r * 1024 + c * 64 + w * 16 + 4 * g + 2 * hf)), rs, g & 1 ? OOB : vp + g * 4, 0);
    const int gi = lane >> 3, r8 = lane & 7;
    const unsigned t[12] = {
        (unsigned)lane * 16, sp_idx(lane), l0 + w * 4096 + g * 576 + ((n >> 3) * 16 + hf * 4 + 2 * ((n >> 2) & 1) + (n & 1)) * 4,
        l0 + (r >> 1) * 576 + w * 128 + (g >> 1) * 64 + (r & 1) * 32 + hf * 16,
        PX4 && g >> 1 ? OOB : (vp ^ ((g & 1) * 65536)) + (g >> 1) * 8, g & 1 ? SENT : 0u,
        l0 + 16384 + w * 8192 + lane * 16, l0 + 16384 + ((gi >> 1) * 2 + (w >> 1)) * 1024 + ((gi & 1) * 32 + (w & 1) * 8 + r8) * 16,
        (c >> 1) * 16384u + (c & 1) * 4096 + gi * 512 + (w * 8 + r8) * 16,
        gi ? OOB : O_ST - O_XR + c * 256 + ((w & 1) * 8 + r8) * 16 + (w >> 1) * 8,
        F_Y0 + ls + (w * 8 + r8) * 128 + (c >> 2) * 4, F_Y0 + ls + (gi & 1) * 4096 + (w * 8 + r8) * 128 + 16};
    table(smem + 65536 + w * 4096 + lane * 4, t);
    asm volatile(RD_ASM : : [rs] "s"(rs), [tb] "s"(tb), [su] "s"(O_UM + ls + c * 65536 + w * 16384), [sp] "s"(O_PM + ls + w * 16384), [st] "s"(O_PT + ls + w * 32768),
        [sy] "s"(sd(O_Y + w * (1u << 20) + ls) + (c >> 1) * 16384 + (c & 1) * 4096), [at] "s"(O_XR + ls), [du] "s"((c & 2) * 5 / 2), [fa] "s"(A1F && w ? OOB : F_A + ls + c * 128 + w * 4), [pr] "s"(prof_at(l * 80 + 64 + c, w) + 128) : CLOB);
    __builtin_amdgcn_endpgm();
}
DEVI void gg_role(const Args& a, const int w, lds_char* smem, int l, int gi) {
    const int lane = lane_id(), g = lane >> 4, n = lane & 15, b = n & 3, p = n >> 2;
    const unsigned l0 = (unsigned)(size_t)smem, tb = l0 + w * 12288, lw = l0 + 65536 + w * 24576, ls = l * SD, tv = (8 * g + p) * 128 + b * 16 + (w & 1) * 64, fh = (g >> 1) * 1024 + (g & 1) * 512 + n * 16;
    const unsigned t[9] = {l0 + lane * 16, lw + lane * 16, l0 + fh, F_A + ls + (lane >> 2) * 128 + (A1F ? 0 : (lane & 3) * 4), w * 2048u + lane * 16, O_ST - O_XR + g * 1024 + n * 16, (lane >> 3) * 7168u + (lane & 7) * 16, tb + tv - (w & 1) * 12288, lw + tv - (w & 1) * 24576};
    table(smem + w * 12288 + lane * 4, t);
    asm volatile(GG_ASM : : [rs] "s"(mkrs(a.ws, WS_RS)), [rw] "s"(mkrs(w & 1 ? a.Wu : a.Wg, 3584 * 2048)), [wo] "s"(gi * 256 + (w >> 1) * 128), [tb] "s"(tb), [lw] "s"(lw), [w] "s"(w),
        [xn] "s"(O_XR + ls), [xw] "s"((w >> 1) * 16384 + (w & 1) * 4096), [ho] "s"(O_H + ls + gi * 16384), [fh] "s"(F_H + ls + (gi / 7) * 1024 + (gi % 7) * 128 + w * 4), [pr] "s"(prof_at(160 + l * 28 + gi, w) + 128) : CLOB);
    __builtin_amdgcn_endpgm();
}
DEVI void dd_role(const Args& a, const int w, lds_char* smem, int l, int c) {
    const int lane = lane_id(), g = lane >> 4, b = lane & 3, p = (lane >> 2) & 3, gi = lane >> 3, r8 = lane & 7;
    const unsigned l0 = (unsigned)(size_t)smem, tb = l0 + w * 12288, lw = l0 + 65536 + w * 24576, ls = l * SD, tr = (8 * g + p) * 128 + b * 8;
    const unsigned ta = (c >> 1) * 16384u + (c & 1) * 4096 + gi * 512 + (w * 8 + r8) * 16;
    const rsrc_t rs = mkrs(a.ws, WS_RS);
    const unsigned t[11] = {(unsigned)lane * 16, lw + lane * 16, l0 + w * 8192 + lane * 16, l0 + ((gi >> 1) * 2 + (w >> 1)) * 1024 + ((gi & 1) * 32 + (w & 1) * 8 + r8) * 16,
        ta, l ? (w * 8 + r8) * 2048 + c * 128 + gi * 16 : ta, gi || l ? OOB : O_ST - O_XR + 4096 + c * 256 + (w * 8 + r8) * 8,
        F_H + ls + w * 1024 + ((lane >> 2) % 7) * 128 + (G1F ? 0 : b * 4), gi * 2048u + r8 * 16, tb + tr, lw + tr};
    table(smem + w * 12288 + lane * 4, t);
    asm volatile(DD_ASM : : [rs] "s"(rs), [rw] "s"(mkrs(a.Wd, 3584 * 2048)), [ro] "s"(mkrs(l ? (char*)a.out : a.ws, l ? 2048 * 2048 : WS_RS)), [wo] "s"(w * (896 * 2048) + c * 128), [tb] "s"(tb), [lw] "s"(lw), [l] "s"(l),
        [xn] "s"(O_XR + ls), [ho] "s"(O_H + ls + w * 7 * 16384), [fd] "s"(D1F && w ? OOB : F_D + ls + c * 128 + w * 4), [lt] "s"(F_D + ls + c * 128 + w * 4 + 64), [pr] "s"(prof_at(216 + l * 16 + c, w) + 128) : CLOB);
    __builtin_amdgcn_endpgm();
}

#define CPF 74
#define CPJ 0
#define LNW 0
#define DLQ 150
#define DLS 400
#define SPL 0
#define PLAN 3
#define PFA 0
#define PFB 3
#define PFL 1
#define VUX 1
#define LNS 40
#define LN1 700
#define DLG 1100
#define DLD 1500
#define DL1 1500
#define DLG1 2600
#define DLD1 3000
DEVI Args args(unsigned sh) {
    auto p = (const __attribute__((address_space(4))) Args*)__builtin_amdgcn_kernarg_segment_ptr();
    asm volatile("" : "+s"(p));
    Args a;
    a.x = p->x; a.mem0 = p->mem0; a.Wq = p->Wq; a.Wk = p->Wk; a.Wv = p->Wv; a.Wo = p->Wo; a.Wg = p->Wg; a.Wu = p->Wu; a.Wd = p->Wd; a.ws = p->ws + sh; a.out = p->out;
    return a;
}
DEVI unsigned beat_wait(unsigned x0) {
    const rsrc_t r0 = mkrs(args(0).ws, 2u << 20);
    for (unsigned spins = 0; spins < SPIN_CAP; ++spins) {
        CB;
        const v2u t = __builtin_amdgcn_raw_buffer_load_b64(r0, 0, O_HB, AUX_SC1);
        if (RFL(t[0]) != x0) return RFL(t[1]) & (2u << 20);
        __builtin_amdgcn_s_sleep(8);
    }
    __builtin_amdgcn_endpgm();
}
#define MPF 0
DEVI void mpf(rsrc_t rs, unsigned o) {
    o += O_E1 + lane_id() * 128;
    unsigned spins = 0;
    for (int s = 0; s < NSEG && spins < SPIN_CAP; ++spins) {
        CB;
        if (__ballot(ld4(rs, o, (s & 3) * 32768) == SENT) == 0ull) { ++s; for (int i = 0; i < MPF; ++i) __builtin_amdgcn_s_sleep(8); }
    }
}
DEVI void leader(const Args& a, const int w, lds_char* smem, unsigned x0) {
    const int lane = lane_id();
    const rsrc_t r0 = mkrs(a.ws, 4u << 20);
    unsigned t[2] = {~0u, ~0u};
    for (int i = 0; i < 2 * NPRB; ++i) {
        const unsigned t0 = (unsigned)__builtin_amdgcn_s_memtime();
        WL0;
        if (lane == 0) st4(0u, r0, 0, O_PRB + (i & 1) * SD + w * 1024 + (i >> 1) * 128);
        WV0;
        const unsigned d = (unsigned)__builtin_amdgcn_s_memtime() - t0;
        if (i & 1) t[1] = min(t[1], d); else t[0] = min(t[0], d);
    }
    *(l2*)(smem + w * 8) = v2u{t[0], t[1]};
    WL0; BAR;
    const v4u p = *(l4*)smem, q = *(l4*)(smem + 16);
    const unsigned sh = RFL(min(min(p[0], p[2]), min(q[0], q[2])) < min(min(p[1], p[3]), min(q[1], q[3])) ? 2u << 20 : 0u);
    const unsigned o = sh + w * 1024 + lane * 16;
    const v4u ff = {SENT, SENT, SENT, SENT};
#pragma unroll
    for (int i = 0; i < 8; ++i) st16(ff, r0, o + O_E1 + i * 4096);
#pragma unroll
    for (int i = 1; i < 16; ++i) if (i != 2 && i != 11) st16(ff, r0, o + i * 4096);
    WV0; BAR;
    if (w) { if (MPF && w > 1) mpf(r0, sh + SD + (w & 1) * 16384); return; }
    for (unsigned spins = 0; spins < SPIN_CAP; ++spins) {
        if (lane == 0) st8(v2u{++x0, sh}, r0, 0, O_HB);
        if (__ballot((int)ld4(r0, sh + F_D + SD + (lane >> 2) * 128 + (D1F ? 0 : (lane & 3) * 4), 0) < 0) == 0ull) break;
        __builtin_amdgcn_s_sleep(16);
    }
}
__global__ __launch_bounds__(256, 1) void sys_kernel(Args) {
    extern __shared__ __attribute__((aligned(16))) char smem_[];
    lds_char* smem = (lds_char*)smem_;
    const int x = blockIdx.x & 7, j = blockIdx.x >> 3, wv = RFL(threadIdx.x >> 6), l = x >> 2, xx = x & 3;
    const unsigned t0 = (unsigned)__builtin_amdgcn_s_memrealtime();
    auto until = [&](unsigned dl) AI { while ((unsigned)__builtin_amdgcn_s_memrealtime() - t0 < dl) __builtin_amdgcn_s_sleep(8); };
    unsigned x0 = 0;
    if (l || j >= 16) x0 = RFL(ld4(mkrs(args(0).ws, 2u << 20), 0, O_HB));
    if (j < 16) {
        if (l) { until(j ? LN1 + j * LNS : 0); ln0(args(0), wv, j * 4 + xx, [&]() AI { return beat_wait(x0); }); until(DL1); }
        if (DLQ && !l && j < 8) until(DLQ);
        if (j < 8) qk_role(args(0), wv, smem, l, xx * 8 + j); else vu_role(args(0), wv, smem, l, (xx ^ VUX) * 8 + j - 8);
        return;
    }
    if (j >= 16 + CPJ && j < 20 + CPJ) {
        unsigned long long pc; unsigned n;
        asm volatile("s_getpc_b64 %0\n\ts_mov_b32 %1, .-_Z10sys_kernel4Args" : "=s"(pc), "=s"(n));
        const rsrc_t rc = mkrs((const void*)(pc - n), CPF * 1024);
        __builtin_amdgcn_raw_ptr_buffer_load_lds(rc, smem + LDSB - 1024 + wv * 256, 4, lane_id() * 128, ((j - 16 - CPJ) * 4 + wv) * 8192, 0, 0);
    }
    {
        const int lane = lane_id(), q = (j - 16) * 4 + wv, m = SPL && !l ? ((xx & 1) != (SPL & 1)) * 2 + (q >> 5) : q >> 4;
        const Args a = args(0);
        const u16 *W0 = a.Wq, *W1 = a.Wk, *W2 = a.Wv, *W3 = a.Wo;
        asm volatile("" : "+s"(W0), "+s"(W1), "+s"(W2), "+s"(W3));
        const u16* W = m == 0 ? W0 : m == 1 ? W1 : m == 2 ? W2 : W3;
        const rsrc_t rw = mkrs(W, 8u << 20);
        if (l) {
#pragma unroll
            for (int i = 0; i < PFA; ++i) __builtin_amdgcn_raw_ptr_buffer_load_lds(rw, smem + LDSB - 1024 + wv * 256, 4, lane * 256, i * (1u << 20) + (xx * 16 + (q & 15)) * 16384, 0, 0);
        } else {
            const unsigned po = (0x100u - (unsigned)(size_t)W) & 0x300u, hx = SPL ? (xx & 2) + ((q >> 4) & 1) : m < 2 ? xx : xx ^ VUX;
            const unsigned vo = m == 3 ? (lane >> 1) * 1024 + (lane & 1) * 128 : (lane >> 2) * 8192 + ((lane >> 1) & 1) * 1024 + (lane & 1) * 128;
            const unsigned so = po + (m == 3 ? hx * (2u << 20) + (q & 15) * 32768 : hx * 2048 + (q & 15) * 131072);
#pragma unroll
            for (int i = 0; i < PFB; ++i) __builtin_amdgcn_raw_ptr_buffer_load_lds(rw, smem + LDSB - 1024 + wv * 256, 4, vo, so + i * (m == 3 ? 524288 : 2u << 20), 0, 0);
            const unsigned pl = (0x1F00u - (unsigned)(size_t)W) & 0x1F00u;
#pragma unroll
            for (int i = 0; i < PFL; ++i) __builtin_amdgcn_raw_ptr_buffer_load_lds(rw, smem + LDSB - 1024 + wv * 256, 4, (lane >> 1) * 8192 + (lane & 1) * 128, (pl >> 11) == hx && m < 3 ? pl + ((q & 15) + 16 * i) * 262144 : OOB, 0, 0);
        }
    }
    int ro, ix;
    if ((xx & 1) == (PLAN & 1)) { if (j < 24) { ro = 0; ix = (xx >> 1) * 8 + j - 16; } else if (j < 26) { ro = 3; ix = (xx >> 1) * 2 + j - 24; } else { ro = 1; ix = (xx >> 1) * 14 + j - 26; } }
    else if (j < 24) { ro = 1; ix = (xx >> 1) * 14 + j - 10; } else { ro = 2; ix = (xx >> 1) * 8 + j - 24; }
    if (ro == 3 && l) { if (ix == 0) leader(args(0), wv, smem, x0); else if (MPF && wv > 1) mpf(mkrs(args(0).ws, 4u << 20), beat_wait(x0) + SD + (wv & 1) * 16384); return; }
    const unsigned sh = beat_wait(x0);
    if (ro == 0) { rd_role(args(sh), wv, smem, l, ix); return; }
    if (ro == 1) { const Args a = args(sh); until(l ? DLG1 : ((((unsigned)(size_t)a.Wg >> 8) + ix) & 3) == 1 || ((((unsigned)(size_t)a.Wu >> 8) + ix) & 3) == 1 ? DLG - DLS : DLG); WV0; BAR; gg_role(a, wv, smem, l, ix); return; }
    if (ro == 2) { const Args a = args(sh); until((l ? DLD1 : DLD) - (((((unsigned)(size_t)a.Wd >> 8) + (ix >> 1)) & 3) == 1 ? DLS + 200 : 0)); WV0; BAR; dd_role(a, wv, smem, l, ix); return; }
    r_role(args(sh), wv, ix);
}

extern "C" void kernel_launch(void* const* d_in, const int* in_sizes, int n_in, void* d_out, int out_size, void* d_ws, size_t ws_size, hipStream_t stream) {
    (void)in_sizes; (void)n_in; (void)out_size; (void)ws_size;
    Args a;
    a.x = (const u16*)d_in[0]; a.mem0 = (const u16*)d_in[1]; a.Wq = (const u16*)d_in[2]; a.Wk = (const u16*)d_in[3]; a.Wv = (const u16*)d_in[4]; a.Wo = (const u16*)d_in[5];
    a.Wg = (const u16*)d_in[8]; a.Wu = (const u16*)d_in[9]; a.Wd = (const u16*)d_in[10];
    a.ws = (char*)(((uintptr_t)d_ws + (2u << 20) - 1) & ~(uintptr_t)((2u << 20) - 1)); a.out = (u16*)d_out;
    (void)hipFuncSetAttribute((const void*)sys_kernel, hipFuncAttributeMaxDynamicSharedMemorySize, LDSB);
    hipLaunchKernelGGL(sys_kernel, dim3(256), dim3(256), LDSB, stream, a);
}
```

```cpp
#include <hip/hip_runtime.h>
#include <stdint.h>

typedef unsigned short u16;
typedef unsigned long long u64;
typedef __attribute__((ext_vector_type(8))) short bf16x8;
typedef __attribute__((ext_vector_type(4))) short s16x4;
typedef __attribute__((ext_vector_type(4))) float f32x4;
typedef __attribute__((ext_vector_type(16))) float f32x16;
typedef __attribute__((ext_vector_type(2))) float f32x2;
typedef __attribute__((ext_vector_type(4))) unsigned v4u;
typedef __attribute__((ext_vector_type(2))) unsigned v2u;
typedef __amdgpu_buffer_rsrc_t rsrc_t;
typedef __attribute__((address_space(3))) char lds_char;
typedef __attribute__((address_space(3))) bf16x8 l8;
typedef __attribute__((address_space(3))) v4u l4;
typedef __attribute__((address_space(3))) v2u l2;
typedef __attribute__((address_space(3))) unsigned l1;
typedef __attribute__((address_space(3))) f32x4 lf4;
typedef __attribute__((address_space(3))) float lf1;
#define DEVI __device__ __forceinline__
#define AI __attribute__((always_inline))

constexpr int NSEG = 64;
constexpr float ATT_SCALE = 0.08838834764831845f;
constexpr float LOG2E = 1.4426950408889634f;
#define NPRB 4
constexpr unsigned SD = 8192;
DEVI unsigned sd(unsigned o) { return o ^ ((o >> 8) & SD); }
constexpr unsigned F_X0 = 32768;
constexpr unsigned F_X = 4096;
constexpr unsigned F_A = 16384;
constexpr unsigned F_H = F_A + 2048;
constexpr unsigned F_D = F_H + 4096;
constexpr unsigned F_Y0 = 49152;
constexpr unsigned F_ABORT = 6400;
constexpr unsigned O_PM = 65536;
constexpr unsigned O_E1 = 131072;
constexpr unsigned O_PT = 262144;
constexpr unsigned O_UM = 524288;
constexpr unsigned CTRL_END = O_UM + (1u << 20);
constexpr unsigned O_PRB = 0x1F0000, O_HB = 0x1FFF00;
constexpr unsigned O_XN = 4u << 20;
constexpr unsigned O_XR = O_XN + (8u << 20);
constexpr unsigned O_ST = O_XR + (8u << 20);
constexpr unsigned O_H = O_ST + (8u << 20);
constexpr unsigned O_Y = O_H + (36u << 20);
constexpr unsigned WS_NEED = O_Y + (8u << 20);
constexpr unsigned O_PROF = WS_NEED;
constexpr unsigned WS_RS = WS_NEED;
static_assert(CTRL_END <= O_PRB && CTRL_END % 16384 == 0 && O_Y % (8u << 20) == 0, "workspace");
constexpr unsigned OOB = 0x80000000u, OOB2 = 0x90000000u, SENT = 0xFFFFFFFFu;

struct Args { const u16 *x, *mem0, *Wq, *Wk, *Wv, *Wo, *Wg, *Wu, *Wd; char* ws; u16* out; };

typedef __attribute__((ext_vector_type(2))) __bf16 bf16x2_t;
DEVI unsigned pack2(float lo, float hi) { f32x2 v = {lo, hi}; bf16x2_t b = __builtin_convertvector(v, bf16x2_t); return __builtin_bit_cast(unsigned, b); }
DEVI float bflo(unsigned u) { return __uint_as_float(u << 16); }
DEVI float bfhi(unsigned u) { return __uint_as_float(u & 0xffff0000u); }
DEVI float rbf(float x) { return bflo(pack2(x, x)); }
DEVI bf16x8 as8(v4u v) { return __builtin_bit_cast(bf16x8, v); }
DEVI f32x4 mfma16(bf16x8 a, bf16x8 b, f32x4 c) { return __builtin_amdgcn_mfma_f32_16x16x32_bf16(a, b, c, 0, 0, 0); }
DEVI f32x16 mfma32(bf16x8 a, bf16x8 b, f32x16 c) { return __builtin_amdgcn_mfma_f32_32x32x16_bf16(a, b, c, 0, 0, 0); }
constexpr int AUX_SC1 = 16;
DEVI v4u ld16(rsrc_t r, unsigned vo, unsigned so) { return __builtin_amdgcn_raw_buffer_load_b128(r, vo, so, AUX_SC1); }
DEVI unsigned ld4(rsrc_t r, unsigned vo, unsigned so) { return __builtin_amdgcn_raw_buffer_load_b32(r, vo, so, AUX_SC1); }
DEVI void st16(v4u v, rsrc_t r, unsigned vo) { __builtin_amdgcn_raw_buffer_store_b128(v, r, vo, 0, AUX_SC1); }
DEVI void st8(v2u v, rsrc_t r, unsigned vo, unsigned so) { __builtin_amdgcn_raw_buffer_store_b64(v, r, vo, so, AUX_SC1); }
DEVI void st4(unsigned v, rsrc_t r, unsigned vo, unsigned so) { __builtin_amdgcn_raw_buffer_store_b32(v, r, vo, so, AUX_SC1); }
DEVI v2u trd(lds_char* p) { return __builtin_bit_cast(v2u, __builtin_amdgcn_ds_read_tr16_b64_v4i16((__attribute__((address_space(3))) s16x4*)p)); }
template <int O1, int O2> DEVI void tra(v2u& lo, v2u& hi, unsigned a) { asm volatile("ds_read_b64_tr_b16 %0, %2 offset:%3\n\tds_read_b64_tr_b16 %1, %2 offset:%4" : "=&v"(lo), "=&v"(hi) : "v"(a), "n"(O1), "n"(O2) : "memory"); }
DEVI void trw(v2u& a, v2u& b, v2u& c, v2u& d) { asm volatile("s_waitcnt lgkmcnt(0)" : "+v"(a), "+v"(b), "+v"(c), "+v"(d)::"memory"); }
DEVI rsrc_t mkrs(const void* p, unsigned n) { return __builtin_amdgcn_make_buffer_rsrc((void*)p, 0, (int)n, 0x00020000); }
#define CB asm volatile("" ::: "memory")
#define SB __builtin_amdgcn_sched_barrier(0)
#define WL0 asm volatile("s_waitcnt lgkmcnt(0)" ::: "memory")
#define WV0 asm volatile("s_waitcnt vmcnt(0)" ::: "memory")
#define WV(n) asm volatile("s_waitcnt vmcnt(%0)" ::"n"(n) : "memory")
#define BAR do { __builtin_amdgcn_s_barrier(); CB; } while (0)
#define RFL(x) __builtin_amdgcn_readfirstlane(x)
DEVI int lane_id() { int x; asm volatile("v_mbcnt_lo_u32_b32 %0, -1, 0\n\tv_mbcnt_hi_u32_b32 %0, -1, %0" : "=v"(x)); return x; }
template <int I> struct IC { static constexpr int v = I; };
template <int B, int E_, class F> DEVI void sfor(F&& f) { if constexpr (B < E_) { f(IC<B>{}); sfor<B + 1, E_>(f); } }
DEVI float red_x32(float x, bool mx) { unsigned u = __float_as_uint(x); auto r = __builtin_amdgcn_permlane32_swap(u, u, false, false); float a = __uint_as_float(r[0]), b = __uint_as_float(r[1]); return mx ? fmaxf(a, b) : a + b; }
DEVI float red_x16(float x, bool mx) { unsigned u = __float_as_uint(x); auto r = __builtin_amdgcn_permlane16_swap(u, u, false, false); float a = __uint_as_float(r[0]), b = __uint_as_float(r[1]); return mx ? fmaxf(a, b) : a + b; }
DEVI unsigned vmax4(v4u d) { return max(max(d[0], d[1]), max(d[2], d[3])); }
DEVI unsigned nosent(unsigned v) { return min(v, SENT - 1u); }
constexpr unsigned SPIN_CAP = 1u << 18;
DEVI bool spin_bail(unsigned& spins, rsrc_t rs) {
    if (++spins > SPIN_CAP) { st4(0u, rs, 0, F_ABORT); return true; }
    return (spins & 255u) == 0u && RFL(ld4(rs, 0, F_ABORT)) != SENT;
}
#define TSTAMP(idx)
constexpr int LDSB = 160 * 1024;
DEVI void idle(unsigned dl) { const unsigned t0 = (unsigned)__builtin_amdgcn_s_memrealtime(); while ((unsigned)__builtin_amdgcn_s_memrealtime() - t0 < dl) __builtin_amdgcn_s_sleep(8); }
#define RXE 0
#define D1F 0
#define X1F 0

DEVI float red_rows(float s) {
    s += __uint_as_float(__builtin_amdgcn_update_dpp(0, __float_as_uint(s), 0x128, 0xf, 0xf, false));
    s = red_x16(s, false); return red_x32(s, false);
}
DEVI f32x2 up2(unsigned u) { return f32x2{bflo(u), bfhi(u)}; }
DEVI float ln_inv(float var) { return __builtin_amdgcn_rcpf(rbf(__builtin_amdgcn_sqrtf(rbf(rbf(fmaxf(var, 0.f)) + 1e-5f)))); }
template <class F> DEVI void ln0(const Args& a, const int w, int b, F&& base) {
    const int lane = lane_id(), row = w * 8 + (lane & 7);
    const rsrc_t rx = mkrs(a.x, 2048 * 2048);
    v4u u[16];
#pragma unroll
    for (int i = 0; i < 16; ++i) u[i] = __builtin_amdgcn_raw_buffer_load_b128(rx, row * 2048 + (lane >> 3) * 16 + i * 128, b * 65536, 0);
    const float k0 = __shfl(bflo(u[0][0]), lane & 7);
    f32x2 s1 = {0.f, 0.f}, s2 = {0.f, 0.f};
#pragma unroll
    for (int i = 0; i < 16; ++i)
#pragma unroll
        for (int e = 0; e < 4; ++e) { const f32x2 d = up2(u[i][e]) - k0; s1 += d; s2 += d * d; }
    const float s = red_rows(s1[0] + s1[1]) * (1.0f / 1024.0f), q = red_rows(s2[0] + s2[1]) * (1.0f / 1024.0f);
    float inv = ln_inv(q - s * s), c = -rbf(k0 + s) * inv;
    asm volatile("" : "+v"(inv), "+v"(c));
    const rsrc_t rs = mkrs(a.ws + base(), WS_RS);
    const unsigned o = sd(O_XN + b * 131072) + (lane >> 3) * 512 + row * 16;
#pragma unroll
    for (int i = 0; i < 16; ++i) {
        v4u r;
#pragma unroll
        for (int e = 0; e < 4; ++e) { const f32x2 z = up2(u[i][e]) * inv + c; r[e] = pack2(z[0], z[1]); }
        st16(r, rs, o + (i >> 1) * 16384 + (i & 1) * 4096);
    }
#pragma unroll
    for (int i = 0; i < 16; ++i) st16(u[i], rs, o + (O_XR - O_XN) + (i >> 1) * 16384 + (i & 1) * 4096);
    st16(u[0], rs, sd(O_ST + b * 131072) + w * 1024 + lane * 16); st16(u[0], rs, sd(O_ST + b * 131072) + 4096 + w * 1024 + lane * 16); st16(u[0], rs, sd(O_ST + SD + b * 131072) + w * 1024 + lane * 16);
    WV0;
    st4(b, rs, 0, F_X0 + b * 128 + w * 4);
}
#define RSLP 8
DEVI void r_role(const Args& a, const int w, int rq) {
    const int lane = lane_id(), row = (w >> 1) * 16 + (rq & 1) * 8 + (lane & 7), cq = (rq >> 1) * 2 + (w & 1);
    const rsrc_t rs = mkrs(a.ws, WS_RS);
    const unsigned fo = cq * 32768 + (lane >> 3) * 512 + row * 16, so = O_ST - O_XR + 4096 + (lane >> 3) * 512 + row * 8;
    auto io = [](int i) -> unsigned { return (i >> 1) * 16384 + (i & 1) * 4096; };
    const unsigned fd = F_D + (lane & 15) * 128 + (D1F ? 0 : (row >> 3) * 4);
    for (int b = 0; b < NSEG; ++b) {
        unsigned spins = 0;
        for (;;) {
            CB;
            if (__ballot((int)ld4(rs, fd, 0) < b) == 0ull) break;
            if (spin_bail(spins, rs)) return;
            __builtin_amdgcn_s_sleep(RSLP);
        }
        const unsigned sx = sd(O_XR + b * 131072);
        const v2u s0 = __builtin_amdgcn_raw_buffer_load_b64(rs, so, sx, AUX_SC1), s1 = __builtin_amdgcn_raw_buffer_load_b64(rs, so + 256, sx, AUX_SC1);
        v4u u[4];
#pragma unroll
        for (int i = 0; i < 4; ++i) u[i] = ld16(rs, fo + io(i), sx);
        const float m0 = __uint_as_float(s0[0]), m1 = __uint_as_float(s1[0]), mean = red_rows(m0 + m1) * 0.0625f;
        const float var = red_rows(__uint_as_float(s0[1]) + __uint_as_float(s1[1]) + 64.f * ((m0 - mean) * (m0 - mean) + (m1 - mean) * (m1 - mean))) * (1.0f / 1024.0f);
        const float inv = ln_inv(var), c = -rbf(mean) * inv;
#pragma unroll
        for (int i = 0; i < 4; ++i) {
            v4u o;
#pragma unroll
            for (int e = 0; e < 4; ++e) { const f32x2 z = up2(u[i][e]) * inv + c; o[e] = pack2(z[0], z[1]); }
            st16(o, rs, fo + io(i) + ((sx ^ SD) - (O_XR - O_XN)));
        }
        CB;
#pragma unroll
        for (int i = 0; i < 4; ++i) st16(u[i], rs, fo + io(i) + (sx ^ SD));
        WV0;
        if (X1F) BAR;
        st4(b, rs, 0, X1F && w ? OOB : F_X + SD + rq * 128 + w * 4);
        if (rq == 0 && w == 0) TSTAMP(200000 + 64 + b);
    }
}
#define QK_ASM \
"v_mbcnt_lo_u32_b32 v255, -1, 0\nv_mbcnt_hi_u32_b32 v255, -1, v255\nv_lshlrev_b32 v255, 2, v255\nv_add_u32 v255, %[tb], v255\nds_read_b32 v0, v255 offset:0\nds_read_b32 v1, v255 offset:256\nds_read_b32 v2, v255 offset:512\nds_read_b32 v3, v255 offset:768\n" \
"ds_read_b32 v4, v255 offset:1024\nds_read_b32 v5, v255 offset:1280\nds_read_b32 v6, v255 offset:1536\nds_read_b32 v7, v255 offset:1792\nds_read_b32 v8, v255 offset:2048\nds_read_b32 v9, v255 offset:2304\nds_read_b32 v10, v255 offset:2560\nds_read_b32 v11, v255 offset:2816\n" \
"ds_read_b32 v12, v255 offset:3072\nds_read_b32 v13, v255 offset:3328\nds_read_b32 v14, v255 offset:3584\nds_read_b32 v15, v255 offset:3840\nds_read_b32 v92, v255 offset:4096\nds_read_b32 v93, v255 offset:4352\nds_read_b32 v94, v255 offset:4608\ns_waitcnt lgkmcnt(0)\n" \
"s_mov_b64 s[36:37], %[ws]\ns_mov_b32 s38, %[sz]\ns_mov_b32 s39, 0x20000\ns_mov_b32 s45, 2096896\nbuffer_load_dword v95, off, s[36:39], s45 sc1\ns_add_u32 m0, %[lw], 16384\ns_add_u32 s44, %[wo], 0\nbuffer_load_dwordx4 v92, %[rw], s44 offen nt lds\n" \
"s_add_u32 m0, %[lw], 17408\ns_add_u32 s44, %[wo], 65536\nbuffer_load_dwordx4 v92, %[rw], s44 offen nt lds\ns_add_u32 m0, %[lw], 18432\ns_add_u32 s44, %[wo], 131072\nbuffer_load_dwordx4 v92, %[rw], s44 offen nt lds\ns_add_u32 m0, %[lw], 19456\ns_add_u32 s44, %[wo], 196608\n" \
"buffer_load_dwordx4 v92, %[rw], s44 offen nt lds\ns_add_u32 m0, %[lw], 20480\ns_add_u32 s44, %[wo], 262144\nbuffer_load_dwordx4 v92, %[rw], s44 offen nt lds\ns_add_u32 m0, %[lw], 21504\ns_add_u32 s44, %[wo], 327680\nbuffer_load_dwordx4 v92, %[rw], s44 offen nt lds\ns_add_u32 m0, %[lw], 22528\n" \
"s_add_u32 s44, %[wo], 393216\nbuffer_load_dwordx4 v92, %[rw], s44 offen nt lds\ns_add_u32 m0, %[lw], 23552\ns_add_u32 s44, %[wo], 458752\nbuffer_load_dwordx4 v92, %[rw], s44 offen nt lds\ns_add_u32 m0, %[tb], 0\ns_add_u32 s44, %[wo], 524288\nbuffer_load_dwordx4 v92, %[rw], s44 offen nt lds\n" \
"s_add_u32 m0, %[tb], 1024\ns_add_u32 s44, %[wo], 589824\nbuffer_load_dwordx4 v92, %[rw], s44 offen nt lds\ns_add_u32 m0, %[tb], 2048\ns_add_u32 s44, %[wo], 655360\nbuffer_load_dwordx4 v92, %[rw], s44 offen nt lds\ns_add_u32 m0, %[tb], 3072\ns_add_u32 s44, %[wo], 720896\n" \
"buffer_load_dwordx4 v92, %[rw], s44 offen nt lds\ns_add_u32 m0, %[tb], 4096\ns_add_u32 s44, %[wo], 786432\nbuffer_load_dwordx4 v92, %[rw], s44 offen nt lds\ns_add_u32 m0, %[tb], 5120\ns_add_u32 s44, %[wo], 851968\nbuffer_load_dwordx4 v92, %[rw], s44 offen nt lds\ns_add_u32 m0, %[tb], 6144\n" \
"s_add_u32 s44, %[wo], 917504\nbuffer_load_dwordx4 v92, %[rw], s44 offen nt lds\ns_add_u32 m0, %[tb], 7168\ns_add_u32 s44, %[wo], 983040\nbuffer_load_dwordx4 v92, %[rw], s44 offen nt lds\ns_add_u32 m0, %[tb], 8192\ns_add_u32 s44, %[wo], 1048576\nbuffer_load_dwordx4 v92, %[rw], s44 offen nt lds\n" \
"s_add_u32 m0, %[tb], 9216\ns_add_u32 s44, %[wo], 1114112\nbuffer_load_dwordx4 v92, %[rw], s44 offen nt lds\ns_add_u32 m0, %[tb], 10240\ns_add_u32 s44, %[wo], 1179648\nbuffer_load_dwordx4 v92, %[rw], s44 offen nt lds\ns_add_u32 m0, %[tb], 11264\ns_add_u32 s44, %[wo], 1245184\n" \
"buffer_load_dwordx4 v92, %[rw], s44 offen nt lds\ns_add_u32 m0, %[lw], 0\ns_add_u32 s44, %[wo], 1310720\nbuffer_load_dwordx4 v92, %[rw], s44 offen nt lds\ns_add_u32 m0, %[lw], 1024\ns_add_u32 s44, %[wo], 1376256\nbuffer_load_dwordx4 v92, %[rw], s44 offen nt lds\ns_add_u32 m0, %[lw], 2048\n" \
"s_add_u32 s44, %[wo], 1441792\nbuffer_load_dwordx4 v92, %[rw], s44 offen nt lds\ns_add_u32 m0, %[lw], 3072\ns_add_u32 s44, %[wo], 1507328\nbuffer_load_dwordx4 v92, %[rw], s44 offen nt lds\ns_add_u32 m0, %[lw], 4096\ns_add_u32 s44, %[wo], 1572864\nbuffer_load_dwordx4 v92, %[rw], s44 offen nt lds\n" \
"s_add_u32 m0, %[lw], 5120\ns_add_u32 s44, %[wo], 1638400\nbuffer_load_dwordx4 v92, %[rw], s44 offen nt lds\ns_add_u32 m0, %[lw], 6144\ns_add_u32 s44, %[wo], 1703936\nbuffer_load_dwordx4 v92, %[rw], s44 offen nt lds\ns_add_u32 m0, %[lw], 7168\ns_add_u32 s44, %[wo], 1769472\n" \
"buffer_load_dwordx4 v92, %[rw], s44 offen nt lds\ns_add_u32 m0, %[lw], 8192\ns_add_u32 s44, %[wo], 1835008\nbuffer_load_dwordx4 v92, %[rw], s44 offen nt lds\ns_add_u32 m0, %[lw], 9216\ns_add_u32 s44, %[wo], 1900544\nbuffer_load_dwordx4 v92, %[rw], s44 offen nt lds\ns_add_u32 m0, %[lw], 10240\n" \
"s_add_u32 s44, %[wo], 1966080\nbuffer_load_dwordx4 v92, %[rw], s44 offen nt lds\ns_add_u32 m0, %[lw], 11264\ns_add_u32 s44, %[wo], 2031616\nbuffer_load_dwordx4 v92, %[rw], s44 offen nt lds\ns_add_u32 m0, %[lw], 12288\ns_add_u32 s44, %[wo], 2097152\nbuffer_load_dwordx4 v92, %[rw], s44 offen nt lds\n" \
"s_add_u32 m0, %[lw], 13312\ns_add_u32 s44, %[wo], 2162688\nbuffer_load_dwordx4 v92, %[rw], s44 offen nt lds\ns_add_u32 m0, %[lw], 14336\ns_add_u32 s44, %[wo], 2228224\nbuffer_load_dwordx4 v92, %[rw], s44 offen nt lds\ns_add_u32 m0, %[lw], 15360\ns_add_u32 s44, %[wo], 2293760\n" \
"buffer_load_dwordx4 v92, %[rw], s44 offen nt lds\ns_waitcnt vmcnt(32)\nds_read_b64_tr_b16 a[0:1], v94 offset:16384\nds_read_b64_tr_b16 a[2:3], v94 offset:16896\nds_read_b64_tr_b16 a[8:9], v94 offset:16392\nds_read_b64_tr_b16 a[10:11], v94 offset:16904\nds_read_b64_tr_b16 a[16:17], v94 offset:16448\nds_read_b64_tr_b16 a[18:19], v94 offset:16960\n" \
"ds_read_b64_tr_b16 a[24:25], v94 offset:16456\nds_read_b64_tr_b16 a[26:27], v94 offset:16968\ns_waitcnt lgkmcnt(0)\ns_add_u32 m0, %[lw], 16384\ns_add_u32 s44, %[wo], 2359296\nbuffer_load_dwordx4 v92, %[rw], s44 offen nt lds\ns_add_u32 m0, %[lw], 17408\ns_add_u32 s44, %[wo], 2424832\n" \
"buffer_load_dwordx4 v92, %[rw], s44 offen nt lds\ns_add_u32 m0, %[lw], 18432\ns_add_u32 s44, %[wo], 2490368\nbuffer_load_dwordx4 v92, %[rw], s44 offen nt lds\ns_add_u32 m0, %[lw], 19456\ns_add_u32 s44, %[wo], 2555904\nbuffer_load_dwordx4 v92, %[rw], s44 offen nt lds\ns_waitcnt vmcnt(32)\n" \
"ds_read_b64_tr_b16 a[4:5], v94 offset:20480\nds_read_b64_tr_b16 a[6:7], v94 offset:20992\nds_read_b64_tr_b16 a[12:13], v94 offset:20488\nds_read_b64_tr_b16 a[14:15], v94 offset:21000\nds_read_b64_tr_b16 a[20:21], v94 offset:20544\nds_read_b64_tr_b16 a[22:23], v94 offset:21056\nds_read_b64_tr_b16 a[28:29], v94 offset:20552\nds_read_b64_tr_b16 a[30:31], v94 offset:21064\n" \
"s_waitcnt lgkmcnt(0)\ns_add_u32 m0, %[lw], 20480\ns_add_u32 s44, %[wo], 2621440\nbuffer_load_dwordx4 v92, %[rw], s44 offen nt lds\ns_add_u32 m0, %[lw], 21504\ns_add_u32 s44, %[wo], 2686976\nbuffer_load_dwordx4 v92, %[rw], s44 offen nt lds\ns_add_u32 m0, %[lw], 22528\n" \
"s_add_u32 s44, %[wo], 2752512\nbuffer_load_dwordx4 v92, %[rw], s44 offen nt lds\ns_add_u32 m0, %[lw], 23552\ns_add_u32 s44, %[wo], 2818048\nbuffer_load_dwordx4 v92, %[rw], s44 offen nt lds\ns_waitcnt vmcnt(32)\nds_read_b64_tr_b16 a[32:33], v93 offset:0\nds_read_b64_tr_b16 a[34:35], v93 offset:512\n" \
"ds_read_b64_tr_b16 v[84:85], v93 offset:8\nds_read_b64_tr_b16 v[86:87], v93 offset:520\nds_read_b64_tr_b16 a[40:41], v93 offset:64\nds_read_b64_tr_b16 a[42:43], v93 offset:576\nds_read_b64_tr_b16 a[48:49], v93 offset:72\nds_read_b64_tr_b16 a[50:51], v93 offset:584\ns_waitcnt lgkmcnt(0)\ns_add_u32 m0, %[tb], 0\n" \
"s_add_u32 s44, %[wo], 2883584\nbuffer_load_dwordx4 v92, %[rw], s44 offen nt lds\ns_add_u32 m0, %[tb], 1024\ns_add_u32 s44, %[wo], 2949120\nbuffer_load_dwordx4 v92, %[rw], s44 offen nt lds\ns_add_u32 m0, %[tb], 2048\ns_add_u32 s44, %[wo], 3014656\nbuffer_load_dwordx4 v92, %[rw], s44 offen nt lds\n" \
"s_add_u32 m0, %[tb], 3072\ns_add_u32 s44, %[wo], 3080192\nbuffer_load_dwordx4 v92, %[rw], s44 offen nt lds\ns_waitcnt vmcnt(32)\nds_read_b64_tr_b16 a[36:37], v93 offset:4096\nds_read_b64_tr_b16 a[38:39], v93 offset:4608\nds_read_b64_tr_b16 v[80:81], v93 offset:4104\nds_read_b64_tr_b16 v[82:83], v93 offset:4616\n" \
"ds_read_b64_tr_b16 a[44:45], v93 offset:4160\nds_read_b64_tr_b16 a[46:47], v93 offset:4672\nds_read_b64_tr_b16 a[52:53], v93 offset:4168\nds_read_b64_tr_b16 a[54:55], v93 offset:4680\ns_waitcnt lgkmcnt(0)\ns_add_u32 m0, %[tb], 4096\ns_add_u32 s44, %[wo], 3145728\nbuffer_load_dwordx4 v92, %[rw], s44 offen nt lds\n" \
"s_add_u32 m0, %[tb], 5120\ns_add_u32 s44, %[wo], 3211264\nbuffer_load_dwordx4 v92, %[rw], s44 offen nt lds\ns_add_u32 m0, %[tb], 6144\ns_add_u32 s44, %[wo], 3276800\nbuffer_load_dwordx4 v92, %[rw], s44 offen nt lds\ns_add_u32 m0, %[tb], 7168\ns_add_u32 s44, %[wo], 3342336\n" \
"buffer_load_dwordx4 v92, %[rw], s44 offen nt lds\ns_waitcnt vmcnt(32)\nds_read_b64_tr_b16 a[56:57], v93 offset:8192\nds_read_b64_tr_b16 a[58:59], v93 offset:8704\nds_read_b64_tr_b16 a[64:65], v93 offset:8200\nds_read_b64_tr_b16 a[66:67], v93 offset:8712\nds_read_b64_tr_b16 v[76:77], v93 offset:8256\nds_read_b64_tr_b16 v[78:79], v93 offset:8768\n" \
"ds_read_b64_tr_b16 a[72:73], v93 offset:8264\nds_read_b64_tr_b16 a[74:75], v93 offset:8776\ns_waitcnt lgkmcnt(0)\ns_add_u32 m0, %[tb], 8192\ns_add_u32 s44, %[wo], 3407872\nbuffer_load_dwordx4 v92, %[rw], s44 offen nt lds\ns_add_u32 m0, %[tb], 9216\ns_add_u32 s44, %[wo], 3473408\n" \
"buffer_load_dwordx4 v92, %[rw], s44 offen nt lds\ns_add_u32 m0, %[tb], 10240\ns_add_u32 s44, %[wo], 3538944\nbuffer_load_dwordx4 v92, %[rw], s44 offen nt lds\ns_add_u32 m0, %[tb], 11264\ns_add_u32 s44, %[wo], 3604480\nbuffer_load_dwordx4 v92, %[rw], s44 offen nt lds\ns_waitcnt vmcnt(32)\n" \
"ds_read_b64_tr_b16 a[60:61], v94 offset:0\nds_read_b64_tr_b16 a[62:63], v94 offset:512\nds_read_b64_tr_b16 a[68:69], v94 offset:8\nds_read_b64_tr_b16 a[70:71], v94 offset:520\nds_read_b64_tr_b16 v[72:73], v94 offset:64\nds_read_b64_tr_b16 v[74:75], v94 offset:576\nds_read_b64_tr_b16 a[76:77], v94 offset:72\nds_read_b64_tr_b16 a[78:79], v94 offset:584\n" \
"s_waitcnt lgkmcnt(0)\ns_add_u32 m0, %[lw], 0\ns_add_u32 s44, %[wo], 3670016\nbuffer_load_dwordx4 v92, %[rw], s44 offen nt lds\ns_add_u32 m0, %[lw], 1024\ns_add_u32 s44, %[wo], 3735552\nbuffer_load_dwordx4 v92, %[rw], s44 offen nt lds\ns_add_u32 m0, %[lw], 2048\n" \
"s_add_u32 s44, %[wo], 3801088\nbuffer_load_dwordx4 v92, %[rw], s44 offen nt lds\ns_add_u32 m0, %[lw], 3072\ns_add_u32 s44, %[wo], 3866624\nbuffer_load_dwordx4 v92, %[rw], s44 offen nt lds\ns_waitcnt vmcnt(32)\nds_read_b64_tr_b16 a[80:81], v94 offset:4096\nds_read_b64_tr_b16 a[82:83], v94 offset:4608\n" \
"ds_read_b64_tr_b16 a[88:89], v94 offset:4104\nds_read_b64_tr_b16 a[90:91], v94 offset:4616\nds_read_b64_tr_b16 a[96:97], v94 offset:4160\nds_read_b64_tr_b16 a[98:99], v94 offset:4672\nds_read_b64_tr_b16 v[68:69], v94 offset:4168\nds_read_b64_tr_b16 v[70:71], v94 offset:4680\ns_waitcnt lgkmcnt(0)\ns_add_u32 m0, %[lw], 4096\n" \
"s_add_u32 s44, %[wo], 3932160\nbuffer_load_dwordx4 v92, %[rw], s44 offen nt lds\ns_add_u32 m0, %[lw], 5120\ns_add_u32 s44, %[wo], 3997696\nbuffer_load_dwordx4 v92, %[rw], s44 offen nt lds\ns_add_u32 m0, %[lw], 6144\ns_add_u32 s44, %[wo], 4063232\nbuffer_load_dwordx4 v92, %[rw], s44 offen nt lds\n" \
"s_add_u32 m0, %[lw], 7168\ns_add_u32 s44, %[wo], 4128768\nbuffer_load_dwordx4 v92, %[rw], s44 offen nt lds\ns_waitcnt vmcnt(32)\nds_read_b64_tr_b16 a[84:85], v94 offset:8192\nds_read_b64_tr_b16 a[86:87], v94 offset:8704\nds_read_b64_tr_b16 a[92:93], v94 offset:8200\nds_read_b64_tr_b16 a[94:95], v94 offset:8712\n" \
"ds_read_b64_tr_b16 a[100:101], v94 offset:8256\nds_read_b64_tr_b16 a[102:103], v94 offset:8768\nds_read_b64_tr_b16 v[64:65], v94 offset:8264\nds_read_b64_tr_b16 v[66:67], v94 offset:8776\ns_waitcnt lgkmcnt(0)\ns_add_u32 m0, %[lw], 8192\ns_add_u32 s44, %[wo], 4194304\nbuffer_load_dwordx4 v92, %[rw], s44 offen nt lds\n" \
"s_add_u32 m0, %[lw], 9216\ns_add_u32 s44, %[wo], 4259840\nbuffer_load_dwordx4 v92, %[rw], s44 offen nt lds\ns_add_u32 m0, %[lw], 10240\ns_add_u32 s44, %[wo], 4325376\nbuffer_load_dwordx4 v92, %[rw], s44 offen nt lds\ns_add_u32 m0, %[lw], 11264\ns_add_u32 s44, %[wo], 4390912\n" \
"buffer_load_dwordx4 v92, %[rw], s44 offen nt lds\ns_waitcnt vmcnt(32)\nds_read_b64_tr_b16 a[104:105], v94 offset:12288\nds_read_b64_tr_b16 a[106:107], v94 offset:12800\nds_read_b64_tr_b16 a[112:113], v94 offset:12296\nds_read_b64_tr_b16 a[114:115], v94 offset:12808\nds_read_b64_tr_b16 a[120:121], v94 offset:12352\nds_read_b64_tr_b16 a[122:123], v94 offset:12864\n" \
"ds_read_b64_tr_b16 a[128:129], v94 offset:12360\nds_read_b64_tr_b16 a[130:131], v94 offset:12872\ns_waitcnt lgkmcnt(0)\ns_add_u32 m0, %[lw], 12288\ns_add_u32 s44, %[wo], 4456448\nbuffer_load_dwordx4 v92, %[rw], s44 offen nt lds\ns_add_u32 m0, %[lw], 13312\ns_add_u32 s44, %[wo], 4521984\n" \
"buffer_load_dwordx4 v92, %[rw], s44 offen nt lds\ns_add_u32 m0, %[lw], 14336\ns_add_u32 s44, %[wo], 4587520\nbuffer_load_dwordx4 v92, %[rw], s44 offen nt lds\ns_add_u32 m0, %[lw], 15360\ns_add_u32 s44, %[wo], 4653056\nbuffer_load_dwordx4 v92, %[rw], s44 offen nt lds\ns_waitcnt vmcnt(32)\n" \
"ds_read_b64_tr_b16 a[108:109], v94 offset:16384\nds_read_b64_tr_b16 a[110:111], v94 offset:16896\nds_read_b64_tr_b16 a[116:117], v94 offset:16392\nds_read_b64_tr_b16 a[118:119], v94 offset:16904\nds_read_b64_tr_b16 a[124:125], v94 offset:16448\nds_read_b64_tr_b16 a[126:127], v94 offset:16960\nds_read_b64_tr_b16 a[132:133], v94 offset:16456\nds_read_b64_tr_b16 a[134:135], v94 offset:16968\n" \
"s_waitcnt lgkmcnt(0)\ns_add_u32 m0, %[lw], 16384\ns_add_u32 s44, %[wo], 4718592\nbuffer_load_dwordx4 v92, %[rw], s44 offen nt lds\ns_add_u32 m0, %[lw], 17408\ns_add_u32 s44, %[wo], 4784128\nbuffer_load_dwordx4 v92, %[rw], s44 offen nt lds\ns_add_u32 m0, %[lw], 18432\n" \
"s_add_u32 s44, %[wo], 4849664\nbuffer_load_dwordx4 v92, %[rw], s44 offen nt lds\ns_add_u32 m0, %[lw], 19456\ns_add_u32 s44, %[wo], 4915200\nbuffer_load_dwordx4 v92, %[rw], s44 offen nt lds\ns_waitcnt vmcnt(32)\nds_read_b64_tr_b16 a[136:137], v94 offset:20480\nds_read_b64_tr_b16 a[138:139], v94 offset:20992\n" \
"ds_read_b64_tr_b16 v[60:61], v94 offset:20488\nds_read_b64_tr_b16 v[62:63], v94 offset:21000\nds_read_b64_tr_b16 a[144:145], v94 offset:20544\nds_read_b64_tr_b16 a[146:147], v94 offset:21056\nds_read_b64_tr_b16 a[152:153], v94 offset:20552\nds_read_b64_tr_b16 a[154:155], v94 offset:21064\ns_waitcnt lgkmcnt(0)\ns_add_u32 m0, %[lw], 20480\n" \
"s_add_u32 s44, %[wo], 4980736\nbuffer_load_dwordx4 v92, %[rw], s44 offen nt lds\ns_add_u32 m0, %[lw], 21504\ns_add_u32 s44, %[wo], 5046272\nbuffer_load_dwordx4 v92, %[rw], s44 offen nt lds\ns_add_u32 m0, %[lw], 22528\ns_add_u32 s44, %[wo], 5111808\nbuffer_load_dwordx4 v92, %[rw], s44 offen nt lds\n" \
"s_add_u32 m0, %[lw], 23552\ns_add_u32 s44, %[wo], 5177344\nbuffer_load_dwordx4 v92, %[rw], s44 offen nt lds\ns_waitcnt vmcnt(32)\nds_read_b64_tr_b16 a[140:141], v93 offset:0\nds_read_b64_tr_b16 a[142:143], v93 offset:512\nds_read_b64_tr_b16 v[56:57], v93 offset:8\nds_read_b64_tr_b16 v[58:59], v93 offset:520\n" \
"ds_read_b64_tr_b16 a[148:149], v93 offset:64\nds_read_b64_tr_b16 a[150:151], v93 offset:576\nds_read_b64_tr_b16 a[156:157], v93 offset:72\nds_read_b64_tr_b16 a[158:159], v93 offset:584\ns_waitcnt lgkmcnt(0)\ns_add_u32 m0, %[tb], 0\ns_add_u32 s44, %[wo], 5242880\nbuffer_load_dwordx4 v92, %[rw], s44 offen nt lds\n" \
"s_add_u32 m0, %[tb], 1024\ns_add_u32 s44, %[wo], 5308416\nbuffer_load_dwordx4 v92, %[rw], s44 offen nt lds\ns_add_u32 m0, %[tb], 2048\ns_add_u32 s44, %[wo], 5373952\nbuffer_load_dwordx4 v92, %[rw], s44 offen nt lds\ns_add_u32 m0, %[tb], 3072\ns_add_u32 s44, %[wo], 5439488\n" \
"buffer_load_dwordx4 v92, %[rw], s44 offen nt lds\ns_waitcnt vmcnt(32)\nds_read_b64_tr_b16 a[160:161], v93 offset:4096\nds_read_b64_tr_b16 a[162:163], v93 offset:4608\nds_read_b64_tr_b16 a[168:169], v93 offset:4104\nds_read_b64_tr_b16 a[170:171], v93 offset:4616\nds_read_b64_tr_b16 v[52:53], v93 offset:4160\nds_read_b64_tr_b16 v[54:55], v93 offset:4672\n" \
"ds_read_b64_tr_b16 a[176:177], v93 offset:4168\nds_read_b64_tr_b16 a[178:179], v93 offset:4680\ns_waitcnt lgkmcnt(0)\ns_add_u32 m0, %[tb], 4096\ns_add_u32 s44, %[wo], 5505024\nbuffer_load_dwordx4 v92, %[rw], s44 offen nt lds\ns_add_u32 m0, %[tb], 5120\ns_add_u32 s44, %[wo], 5570560\n" \
"buffer_load_dwordx4 v92, %[rw], s44 offen nt lds\ns_add_u32 m0, %[tb], 6144\ns_add_u32 s44, %[wo], 5636096\nbuffer_load_dwordx4 v92, %[rw], s44 offen nt lds\ns_add_u32 m0, %[tb], 7168\ns_add_u32 s44, %[wo], 5701632\nbuffer_load_dwordx4 v92, %[rw], s44 offen nt lds\ns_waitcnt vmcnt(32)\n" \
"ds_read_b64_tr_b16 a[164:165], v93 offset:8192\nds_read_b64_tr_b16 a[166:167], v93 offset:8704\nds_read_b64_tr_b16 a[172:173], v93 offset:8200\nds_read_b64_tr_b16 a[174:175], v93 offset:8712\nds_read_b64_tr_b16 v[48:49], v93 offset:8256\nds_read_b64_tr_b16 v[50:51], v93 offset:8768\nds_read_b64_tr_b16 a[180:181], v93 offset:8264\nds_read_b64_tr_b16 a[182:183], v93 offset:8776\n" \
"s_waitcnt lgkmcnt(0)\ns_add_u32 m0, %[tb], 8192\ns_add_u32 s44, %[wo], 5767168\nbuffer_load_dwordx4 v92, %[rw], s44 offen nt lds\ns_add_u32 m0, %[tb], 9216\ns_add_u32 s44, %[wo], 5832704\nbuffer_load_dwordx4 v92, %[rw], s44 offen nt lds\ns_add_u32 m0, %[tb], 10240\n" \
"s_add_u32 s44, %[wo], 5898240\nbuffer_load_dwordx4 v92, %[rw], s44 offen nt lds\ns_add_u32 m0, %[tb], 11264\ns_add_u32 s44, %[wo], 5963776\nbuffer_load_dwordx4 v92, %[rw], s44 offen nt lds\ns_waitcnt vmcnt(32)\nds_read_b64_tr_b16 a[184:185], v94 offset:0\nds_read_b64_tr_b16 a[186:187], v94 offset:512\n" \
"ds_read_b64_tr_b16 a[192:193], v94 offset:8\nds_read_b64_tr_b16 a[194:195], v94 offset:520\nds_read_b64_tr_b16 a[200:201], v94 offset:64\nds_read_b64_tr_b16 a[202:203], v94 offset:576\nds_read_b64_tr_b16 v[44:45], v94 offset:72\nds_read_b64_tr_b16 v[46:47], v94 offset:584\ns_waitcnt lgkmcnt(0)\ns_add_u32 m0, %[lw], 0\n" \
"s_add_u32 s44, %[wo], 6029312\nbuffer_load_dwordx4 v92, %[rw], s44 offen nt lds\ns_add_u32 m0, %[lw], 1024\ns_add_u32 s44, %[wo], 6094848\nbuffer_load_dwordx4 v92, %[rw], s44 offen nt lds\ns_add_u32 m0, %[lw], 2048\ns_add_u32 s44, %[wo], 6160384\nbuffer_load_dwordx4 v92, %[rw], s44 offen nt lds\n" \
"s_add_u32 m0, %[lw], 3072\ns_add_u32 s44, %[wo], 6225920\nbuffer_load_dwordx4 v92, %[rw], s44 offen nt lds\ns_waitcnt vmcnt(32)\nds_read_b64_tr_b16 a[188:189], v94 offset:4096\nds_read_b64_tr_b16 a[190:191], v94 offset:4608\nds_read_b64_tr_b16 a[196:197], v94 offset:4104\nds_read_b64_tr_b16 a[198:199], v94 offset:4616\n" \
"ds_read_b64_tr_b16 a[204:205], v94 offset:4160\nds_read_b64_tr_b16 a[206:207], v94 offset:4672\nds_read_b64_tr_b16 v[40:41], v94 offset:4168\nds_read_b64_tr_b16 v[42:43], v94 offset:4680\ns_waitcnt lgkmcnt(0)\ns_add_u32 m0, %[lw], 4096\ns_add_u32 s44, %[wo], 6291456\nbuffer_load_dwordx4 v92, %[rw], s44 offen nt lds\n" \
"s_add_u32 m0, %[lw], 5120\ns_add_u32 s44, %[wo], 6356992\nbuffer_load_dwordx4 v92, %[rw], s44 offen nt lds\ns_add_u32 m0, %[lw], 6144\ns_add_u32 s44, %[wo], 6422528\nbuffer_load_dwordx4 v92, %[rw], s44 offen nt lds\ns_add_u32 m0, %[lw], 7168\ns_add_u32 s44, %[wo], 6488064\n" \
"buffer_load_dwordx4 v92, %[rw], s44 offen nt lds\ns_waitcnt vmcnt(32)\nds_read_b64_tr_b16 a[208:209], v94 offset:8192\nds_read_b64_tr_b16 a[210:211], v94 offset:8704\nds_read_b64_tr_b16 a[216:217], v94 offset:8200\nds_read_b64_tr_b16 a[218:219], v94 offset:8712\nds_read_b64_tr_b16 a[224:225], v94 offset:8256\nds_read_b64_tr_b16 a[226:227], v94 offset:8768\n" \
"ds_read_b64_tr_b16 a[232:233], v94 offset:8264\nds_read_b64_tr_b16 a[234:235], v94 offset:8776\ns_waitcnt lgkmcnt(0)\ns_add_u32 m0, %[lw], 8192\ns_add_u32 s44, %[wo], 6553600\nbuffer_load_dwordx4 v92, %[rw], s44 offen nt lds\ns_add_u32 m0, %[lw], 9216\ns_add_u32 s44, %[wo], 6619136\n" \
"buffer_load_dwordx4 v92, %[rw], s44 offen nt lds\ns_add_u32 m0, %[lw], 10240\ns_add_u32 s44, %[wo], 6684672\nbuffer_load_dwordx4 v92, %[rw], s44 offen nt lds\ns_add_u32 m0, %[lw], 11264\ns_add_u32 s44, %[wo], 6750208\nbuffer_load_dwordx4 v92, %[rw], s44 offen nt lds\ns_waitcnt vmcnt(32)\n" \
"ds_read_b64_tr_b16 a[212:213], v94 offset:12288\nds_read_b64_tr_b16 a[214:215], v94 offset:12800\nds_read_b64_tr_b16 a[220:221], v94 offset:12296\nds_read_b64_tr_b16 a[222:223], v94 offset:12808\nds_read_b64_tr_b16 a[228:229], v94 offset:12352\nds_read_b64_tr_b16 a[230:231], v94 offset:12864\nds_read_b64_tr_b16 a[236:237], v94 offset:12360\nds_read_b64_tr_b16 a[238:239], v94 offset:12872\n" \
"s_waitcnt lgkmcnt(0)\ns_add_u32 m0, %[lw], 12288\ns_add_u32 s44, %[wo], 6815744\nbuffer_load_dwordx4 v92, %[rw], s44 offen nt lds\ns_add_u32 m0, %[lw], 13312\ns_add_u32 s44, %[wo], 6881280\nbuffer_load_dwordx4 v92, %[rw], s44 offen nt lds\ns_add_u32 m0, %[lw], 14336\n" \
"s_add_u32 s44, %[wo], 6946816\nbuffer_load_dwordx4 v92, %[rw], s44 offen nt lds\ns_add_u32 m0, %[lw], 15360\ns_add_u32 s44, %[wo], 7012352\nbuffer_load_dwordx4 v92, %[rw], s44 offen nt lds\ns_waitcnt vmcnt(32)\nds_read_b64_tr_b16 a[240:241], v94 offset:16384\nds_read_b64_tr_b16 a[242:243], v94 offset:16896\n" \
"ds_read_b64_tr_b16 v[36:37], v94 offset:16392\nds_read_b64_tr_b16 v[38:39], v94 offset:16904\nds_read_b64_tr_b16 a[248:249], v94 offset:16448\nds_read_b64_tr_b16 a[250:251], v94 offset:16960\nds_read_b64_tr_b16 v[96:97], v94 offset:16456\nds_read_b64_tr_b16 v[98:99], v94 offset:16968\ns_waitcnt lgkmcnt(0)\ns_add_u32 m0, %[lw], 16384\n" \
"s_add_u32 s44, %[wo], 7077888\nbuffer_load_dwordx4 v92, %[rw], s44 offen nt lds\ns_add_u32 m0, %[lw], 17408\ns_add_u32 s44, %[wo], 7143424\nbuffer_load_dwordx4 v92, %[rw], s44 offen nt lds\ns_add_u32 m0, %[lw], 18432\ns_add_u32 s44, %[wo], 7208960\nbuffer_load_dwordx4 v92, %[rw], s44 offen nt lds\n" \
"s_add_u32 m0, %[lw], 19456\ns_add_u32 s44, %[wo], 7274496\nbuffer_load_dwordx4 v92, %[rw], s44 offen nt lds\ns_waitcnt vmcnt(32)\nds_read_b64_tr_b16 a[244:245], v94 offset:20480\nds_read_b64_tr_b16 a[246:247], v94 offset:20992\nds_read_b64_tr_b16 v[32:33], v94 offset:20488\nds_read_b64_tr_b16 v[34:35], v94 offset:21000\n" \
"ds_read_b64_tr_b16 a[252:253], v94 offset:20544\nds_read_b64_tr_b16 a[254:255], v94 offset:21056\nds_read_b64_tr_b16 v[100:101], v94 offset:20552\nds_read_b64_tr_b16 v[102:103], v94 offset:21064\ns_waitcnt lgkmcnt(0)\ns_add_u32 m0, %[lw], 20480\ns_add_u32 s44, %[wo], 7340032\nbuffer_load_dwordx4 v92, %[rw], s44 offen nt lds\n" \
"s_add_u32 m0, %[lw], 21504\ns_add_u32 s44, %[wo], 7405568\nbuffer_load_dwordx4 v92, %[rw], s44 offen nt lds\ns_add_u32 m0, %[lw], 22528\ns_add_u32 s44, %[wo], 7471104\nbuffer_load_dwordx4 v92, %[rw], s44 offen nt lds\ns_add_u32 m0, %[lw], 23552\ns_add_u32 s44, %[wo], 7536640\n" \
"buffer_load_dwordx4 v92, %[rw], s44 offen nt lds\ns_waitcnt vmcnt(32)\nds_read_b64_tr_b16 v[104:105], v93 offset:0\nds_read_b64_tr_b16 v[106:107], v93 offset:512\nds_read_b64_tr_b16 v[112:113], v93 offset:8\nds_read_b64_tr_b16 v[114:115], v93 offset:520\nds_read_b64_tr_b16 v[28:29], v93 offset:64\nds_read_b64_tr_b16 v[30:31], v93 offset:576\n" \
"ds_read_b64_tr_b16 v[120:121], v93 offset:72\nds_read_b64_tr_b16 v[122:123], v93 offset:584\ns_waitcnt lgkmcnt(0)\ns_add_u32 m0, %[tb], 0\ns_add_u32 s44, %[wo], 7602176\nbuffer_load_dwordx4 v92, %[rw], s44 offen nt lds\ns_add_u32 m0, %[tb], 1024\ns_add_u32 s44, %[wo], 7667712\n" \
"buffer_load_dwordx4 v92, %[rw], s44 offen nt lds\ns_add_u32 m0, %[tb], 2048\ns_add_u32 s44, %[wo], 7733248\nbuffer_load_dwordx4 v92, %[rw], s44 offen nt lds\ns_add_u32 m0, %[tb], 3072\ns_add_u32 s44, %[wo], 7798784\nbuffer_load_dwordx4 v92, %[rw], s44 offen nt lds\ns_waitcnt vmcnt(32)\n" \
"ds_read_b64_tr_b16 v[108:109], v93 offset:4096\nds_read_b64_tr_b16 v[110:111], v93 offset:4608\nds_read_b64_tr_b16 v[116:117], v93 offset:4104\nds_read_b64_tr_b16 v[118:119], v93 offset:4616\nds_read_b64_tr_b16 v[24:25], v93 offset:4160\nds_read_b64_tr_b16 v[26:27], v93 offset:4672\nds_read_b64_tr_b16 v[124:125], v93 offset:4168\nds_read_b64_tr_b16 v[126:127], v93 offset:4680\n" \
"s_waitcnt lgkmcnt(0)\nbuffer_load_dwordx2 v[88:89], off, s[36:39], s45 sc1\ns_add_u32 m0, %[tb], 4096\ns_add_u32 s44, %[wo], 7864320\nbuffer_load_dwordx4 v92, %[rw], s44 offen nt lds\ns_add_u32 m0, %[tb], 5120\ns_add_u32 s44, %[wo], 7929856\nbuffer_load_dwordx4 v92, %[rw], s44 offen nt lds\n" \
"s_add_u32 m0, %[tb], 6144\ns_add_u32 s44, %[wo], 7995392\nbuffer_load_dwordx4 v92, %[rw], s44 offen nt lds\ns_add_u32 m0, %[tb], 7168\ns_add_u32 s44, %[wo], 8060928\nbuffer_load_dwordx4 v92, %[rw], s44 offen nt lds\ns_waitcnt vmcnt(33)\nds_read_b64_tr_b16 v[128:129], v93 offset:8192\n" \
"ds_read_b64_tr_b16 v[130:131], v93 offset:8704\nds_read_b64_tr_b16 v[136:137], v93 offset:8200\nds_read_b64_tr_b16 v[138:139], v93 offset:8712\nds_read_b64_tr_b16 v[144:145], v93 offset:8256\nds_read_b64_tr_b16 v[146:147], v93 offset:8768\nds_read_b64_tr_b16 v[20:21], v93 offset:8264\nds_read_b64_tr_b16 v[22:23], v93 offset:8776\ns_waitcnt lgkmcnt(0)\n" \
"s_add_u32 m0, %[tb], 8192\ns_add_u32 s44, %[wo], 8126464\nbuffer_load_dwordx4 v92, %[rw], s44 offen nt lds\ns_add_u32 m0, %[tb], 9216\ns_add_u32 s44, %[wo], 8192000\nbuffer_load_dwordx4 v92, %[rw], s44 offen nt lds\ns_add_u32 m0, %[tb], 10240\ns_add_u32 s44, %[wo], 8257536\n" \
"buffer_load_dwordx4 v92, %[rw], s44 offen nt lds\ns_add_u32 m0, %[tb], 11264\ns_add_u32 s44, %[wo], 8323072\nbuffer_load_dwordx4 v92, %[rw], s44 offen nt lds\ns_waitcnt vmcnt(33)\nds_read_b64_tr_b16 v[132:133], v94 offset:0\nds_read_b64_tr_b16 v[134:135], v94 offset:512\nds_read_b64_tr_b16 v[140:141], v94 offset:8\n" \
"ds_read_b64_tr_b16 v[142:143], v94 offset:520\nds_read_b64_tr_b16 v[148:149], v94 offset:64\nds_read_b64_tr_b16 v[150:151], v94 offset:576\nds_read_b64_tr_b16 v[16:17], v94 offset:72\nds_read_b64_tr_b16 v[18:19], v94 offset:584\ns_waitcnt lgkmcnt(0)\nds_write_b128 v1, v[84:87] offset:0\nds_write_b128 v1, v[80:83] offset:1024\n" \
"ds_write_b128 v1, v[76:79] offset:2048\nds_write_b128 v1, v[72:75] offset:3072\ns_waitcnt vmcnt(29)\nds_read_b64_tr_b16 v[152:153], v94 offset:4096\nds_read_b64_tr_b16 v[154:155], v94 offset:4608\nds_read_b64_tr_b16 v[160:161], v94 offset:4104\nds_read_b64_tr_b16 v[162:163], v94 offset:4616\nds_read_b64_tr_b16 v[168:169], v94 offset:4160\n" \
"ds_read_b64_tr_b16 v[170:171], v94 offset:4672\nds_read_b64_tr_b16 v[176:177], v94 offset:4168\nds_read_b64_tr_b16 v[178:179], v94 offset:4680\ns_waitcnt lgkmcnt(0)\nds_write_b128 v1, v[68:71] offset:4096\nds_write_b128 v1, v[64:67] offset:5120\nds_write_b128 v1, v[60:63] offset:6144\nds_write_b128 v1, v[56:59] offset:7168\n" \
"s_waitcnt vmcnt(25)\nds_read_b64_tr_b16 v[156:157], v94 offset:8192\nds_read_b64_tr_b16 v[158:159], v94 offset:8704\nds_read_b64_tr_b16 v[164:165], v94 offset:8200\nds_read_b64_tr_b16 v[166:167], v94 offset:8712\nds_read_b64_tr_b16 v[172:173], v94 offset:8256\nds_read_b64_tr_b16 v[174:175], v94 offset:8768\nds_read_b64_tr_b16 v[180:181], v94 offset:8264\n" \
"ds_read_b64_tr_b16 v[182:183], v94 offset:8776\ns_waitcnt lgkmcnt(0)\nds_write_b128 v1, v[52:55] offset:8192\nds_write_b128 v1, v[48:51] offset:9216\nds_write_b128 v1, v[44:47] offset:10240\nds_write_b128 v1, v[40:43] offset:11264\ns_waitcnt vmcnt(21)\nds_read_b64_tr_b16 v[184:185], v94 offset:12288\n" \
"ds_read_b64_tr_b16 v[186:187], v94 offset:12800\nds_read_b64_tr_b16 v[40:41], v94 offset:12296\nds_read_b64_tr_b16 v[42:43], v94 offset:12808\nds_read_b64_tr_b16 v[192:193], v94 offset:12352\nds_read_b64_tr_b16 v[194:195], v94 offset:12864\nds_read_b64_tr_b16 v[200:201], v94 offset:12360\nds_read_b64_tr_b16 v[202:203], v94 offset:12872\ns_waitcnt lgkmcnt(0)\n" \
"ds_write_b128 v1, v[36:39] offset:12288\nds_write_b128 v1, v[32:35] offset:13312\nds_write_b128 v1, v[28:31] offset:14336\nds_write_b128 v1, v[24:27] offset:15360\ns_waitcnt vmcnt(17)\nds_read_b64_tr_b16 v[188:189], v94 offset:16384\nds_read_b64_tr_b16 v[190:191], v94 offset:16896\nds_read_b64_tr_b16 v[24:25], v94 offset:16392\n" \
"ds_read_b64_tr_b16 v[26:27], v94 offset:16904\nds_read_b64_tr_b16 v[196:197], v94 offset:16448\nds_read_b64_tr_b16 v[198:199], v94 offset:16960\nds_read_b64_tr_b16 v[204:205], v94 offset:16456\nds_read_b64_tr_b16 v[206:207], v94 offset:16968\ns_waitcnt lgkmcnt(0)\nds_write_b128 v1, v[20:23] offset:16384\nds_write_b128 v1, v[16:19] offset:17408\n" \
"ds_write_b128 v1, v[40:43] offset:18432\nds_write_b128 v1, v[24:27] offset:19456\ns_waitcnt vmcnt(13)\nds_read_b64_tr_b16 v[208:209], v94 offset:20480\nds_read_b64_tr_b16 v[210:211], v94 offset:20992\nds_read_b64_tr_b16 v[216:217], v94 offset:20488\nds_read_b64_tr_b16 v[218:219], v94 offset:21000\nds_read_b64_tr_b16 v[24:25], v94 offset:20544\n" \
"ds_read_b64_tr_b16 v[26:27], v94 offset:21056\nds_read_b64_tr_b16 v[224:225], v94 offset:20552\nds_read_b64_tr_b16 v[226:227], v94 offset:21064\ns_waitcnt lgkmcnt(0)\nds_write_b128 v1, v[24:27] offset:20480\ns_waitcnt vmcnt(9)\nds_read_b64_tr_b16 v[212:213], v93 offset:0\nds_read_b64_tr_b16 v[214:215], v93 offset:512\n" \
"ds_read_b64_tr_b16 v[220:221], v93 offset:8\nds_read_b64_tr_b16 v[222:223], v93 offset:520\nds_read_b64_tr_b16 v[24:25], v93 offset:64\nds_read_b64_tr_b16 v[26:27], v93 offset:576\nds_read_b64_tr_b16 v[228:229], v93 offset:72\nds_read_b64_tr_b16 v[230:231], v93 offset:584\ns_waitcnt lgkmcnt(0)\nds_write_b128 v1, v[24:27] offset:21504\n" \
"s_waitcnt vmcnt(4)\nds_read_b64_tr_b16 v[232:233], v93 offset:4096\nds_read_b64_tr_b16 v[234:235], v93 offset:4608\nds_read_b64_tr_b16 v[240:241], v93 offset:4104\nds_read_b64_tr_b16 v[242:243], v93 offset:4616\nds_read_b64_tr_b16 v[248:249], v93 offset:4160\nds_read_b64_tr_b16 v[250:251], v93 offset:4672\nds_read_b64_tr_b16 v[24:25], v93 offset:4168\n" \
"ds_read_b64_tr_b16 v[26:27], v93 offset:4680\ns_waitcnt lgkmcnt(0)\nds_write_b128 v1, v[24:27] offset:22528\ns_waitcnt vmcnt(0)\nds_read_b64_tr_b16 v[236:237], v93 offset:8192\nds_read_b64_tr_b16 v[238:239], v93 offset:8704\nds_read_b64_tr_b16 v[244:245], v93 offset:8200\nds_read_b64_tr_b16 v[246:247], v93 offset:8712\n" \
"ds_read_b64_tr_b16 v[252:253], v93 offset:8256\nds_read_b64_tr_b16 v[254:255], v93 offset:8768\nds_read_b64_tr_b16 v[24:25], v93 offset:8264\nds_read_b64_tr_b16 v[26:27], v93 offset:8776\ns_waitcnt lgkmcnt(0)\ns_mov_b32 s43, 0\ns_mov_b32 s86, 110\ns_mov_b32 s89, 0\n" \
".Lqk_hb%=:\nv_cmp_ne_u32 vcc, v95, v88\ns_cbranch_vccnz .Lqk_hb1%=\ns_sleep 4\nbuffer_load_dwordx2 v[88:89], off, s[36:39], s45 sc1\ns_waitcnt vmcnt(0)\ns_add_u32 s43, s43, 1\ns_cmp_lt_u32 s43, 262144\n" \
"s_cbranch_scc1 .Lqk_hb%=\ns_branch .Lqk_abort%=\n.Lqk_hb1%=:\nv_readfirstlane_b32 s44, v89\ns_and_b32 s44, s44, 0x200000\ns_add_u32 s36, s36, s44\ns_addc_u32 s37, s37, 0\nds_write_b128 v1, v[24:27] offset:23552\n" \
"s_waitcnt lgkmcnt(0)\ns_barrier\ns_mov_b32 s40, -1\ns_mov_b32 s43, 0\ns_mov_b32 s61, 0x7fff7fff\ns_mov_b32 s58, 0x3e0293ee\ns_mov_b32 s59, 0xbe0293ee\ns_lshl_b32 s57, %[w], 12\n" \
"s_mov_b32 s62, 0xff00ff00\ns_mov_b32 s63, 0xff00ff00\ns_mov_b32 s56, 0\ns_cmp_eq_u32 %[w], 3\ns_cbranch_scc1 .Lqk_fetch0%=\n.Lqk_pwait%=:\ns_barrier\nds_read_b128 v[80:83], v2 offset:0\n" \
"ds_read_b128 v[84:87], v2 offset:256\nds_read_b128 v[88:91], v2 offset:2048\n.Lqk_proj%=:\ns_waitcnt lgkmcnt(2)\nv_mfma_f32_16x16x32_bf16 v[16:19], a[0:3], v[80:83], 0\nds_read_b128 v[92:95], v2 offset:2304\nv_mfma_f32_16x16x32_bf16 v[20:23], a[8:11], v[80:83], 0\nds_read_b128 v[72:75], v1 offset:0\n" \
"v_mfma_f32_16x16x32_bf16 v[24:27], a[16:19], v[80:83], 0\nv_mfma_f32_16x16x32_bf16 v[28:31], a[24:27], v[80:83], 0\ns_waitcnt lgkmcnt(3)\nv_mfma_f32_16x16x32_bf16 v[32:35], a[0:3], v[84:87], 0\nds_read_b128 v[80:83], v2 offset:4096\nv_mfma_f32_16x16x32_bf16 v[36:39], a[8:11], v[84:87], 0\nv_mfma_f32_16x16x32_bf16 v[40:43], a[16:19], v[84:87], 0\nv_mfma_f32_16x16x32_bf16 v[44:47], a[24:27], v[84:87], 0\n" \
"s_waitcnt lgkmcnt(3)\nv_mfma_f32_16x16x32_bf16 v[16:19], a[4:7], v[88:91], v[16:19]\nds_read_b128 v[84:87], v2 offset:4352\nv_mfma_f32_16x16x32_bf16 v[20:23], a[12:15], v[88:91], v[20:23]\nds_read_b128 v[76:79], v1 offset:1024\nv_mfma_f32_16x16x32_bf16 v[24:27], a[20:23], v[88:91], v[24:27]\nv_mfma_f32_16x16x32_bf16 v[28:31], a[28:31], v[88:91], v[28:31]\ns_waitcnt lgkmcnt(4)\n" \
"v_mfma_f32_16x16x32_bf16 v[32:35], a[4:7], v[92:95], v[32:35]\nds_read_b128 v[88:91], v2 offset:6144\nv_mfma_f32_16x16x32_bf16 v[36:39], a[12:15], v[92:95], v[36:39]\nv_mfma_f32_16x16x32_bf16 v[40:43], a[20:23], v[92:95], v[40:43]\nv_mfma_f32_16x16x32_bf16 v[44:47], a[28:31], v[92:95], v[44:47]\ns_waitcnt lgkmcnt(3)\nv_mfma_f32_16x16x32_bf16 v[16:19], a[32:35], v[80:83], v[16:19]\nds_read_b128 v[92:95], v2 offset:6400\n" \
"v_mfma_f32_16x16x32_bf16 v[20:23], v[72:75], v[80:83], v[20:23]\nds_read_b128 v[64:67], v1 offset:2048\nv_mfma_f32_16x16x32_bf16 v[24:27], a[40:43], v[80:83], v[24:27]\nv_mfma_f32_16x16x32_bf16 v[28:31], a[48:51], v[80:83], v[28:31]\ns_waitcnt lgkmcnt(4)\nv_mfma_f32_16x16x32_bf16 v[32:35], a[32:35], v[84:87], v[32:35]\nds_read_b128 v[80:83], v2 offset:8192\nv_mfma_f32_16x16x32_bf16 v[36:39], v[72:75], v[84:87], v[36:39]\n" \
"v_mfma_f32_16x16x32_bf16 v[40:43], a[40:43], v[84:87], v[40:43]\nv_mfma_f32_16x16x32_bf16 v[44:47], a[48:51], v[84:87], v[44:47]\ns_waitcnt lgkmcnt(3)\nv_mfma_f32_16x16x32_bf16 v[16:19], a[36:39], v[88:91], v[16:19]\nds_read_b128 v[84:87], v2 offset:8448\nv_mfma_f32_16x16x32_bf16 v[20:23], v[76:79], v[88:91], v[20:23]\nds_read_b128 v[68:71], v1 offset:3072\nv_mfma_f32_16x16x32_bf16 v[24:27], a[44:47], v[88:91], v[24:27]\n" \
"v_mfma_f32_16x16x32_bf16 v[28:31], a[52:55], v[88:91], v[28:31]\ns_waitcnt lgkmcnt(4)\nv_mfma_f32_16x16x32_bf16 v[32:35], a[36:39], v[92:95], v[32:35]\nds_read_b128 v[88:91], v2 offset:10240\nv_mfma_f32_16x16x32_bf16 v[36:39], v[76:79], v[92:95], v[36:39]\nv_mfma_f32_16x16x32_bf16 v[40:43], a[44:47], v[92:95], v[40:43]\nv_mfma_f32_16x16x32_bf16 v[44:47], a[52:55], v[92:95], v[44:47]\ns_waitcnt lgkmcnt(3)\n" \
"v_mfma_f32_16x16x32_bf16 v[16:19], a[56:59], v[80:83], v[16:19]\nds_read_b128 v[92:95], v2 offset:10496\nv_mfma_f32_16x16x32_bf16 v[20:23], a[64:67], v[80:83], v[20:23]\nds_read_b128 v[72:75], v1 offset:4096\nv_mfma_f32_16x16x32_bf16 v[24:27], v[64:67], v[80:83], v[24:27]\nv_mfma_f32_16x16x32_bf16 v[28:31], a[72:75], v[80:83], v[28:31]\ns_waitcnt lgkmcnt(4)\nv_mfma_f32_16x16x32_bf16 v[32:35], a[56:59], v[84:87], v[32:35]\n" \
"ds_read_b128 v[80:83], v2 offset:12288\nv_mfma_f32_16x16x32_bf16 v[36:39], a[64:67], v[84:87], v[36:39]\nv_mfma_f32_16x16x32_bf16 v[40:43], v[64:67], v[84:87], v[40:43]\nv_mfma_f32_16x16x32_bf16 v[44:47], a[72:75], v[84:87], v[44:47]\ns_waitcnt lgkmcnt(3)\nv_mfma_f32_16x16x32_bf16 v[16:19], a[60:63], v[88:91], v[16:19]\nds_read_b128 v[84:87], v2 offset:12544\nv_mfma_f32_16x16x32_bf16 v[20:23], a[68:71], v[88:91], v[20:23]\n" \
"ds_read_b128 v[76:79], v1 offset:5120\nv_mfma_f32_16x16x32_bf16 v[24:27], v[68:71], v[88:91], v[24:27]\nv_mfma_f32_16x16x32_bf16 v[28:31], a[76:79], v[88:91], v[28:31]\ns_waitcnt lgkmcnt(4)\nv_mfma_f32_16x16x32_bf16 v[32:35], a[60:63], v[92:95], v[32:35]\nds_read_b128 v[88:91], v2 offset:14336\nv_mfma_f32_16x16x32_bf16 v[36:39], a[68:71], v[92:95], v[36:39]\nv_mfma_f32_16x16x32_bf16 v[40:43], v[68:71], v[92:95], v[40:43]\n" \
"v_mfma_f32_16x16x32_bf16 v[44:47], a[76:79], v[92:95], v[44:47]\n.Lqk_pj%=:\ns_waitcnt lgkmcnt(3)\nv_mfma_f32_16x16x32_bf16 v[16:19], a[80:83], v[80:83], v[16:19]\nds_read_b128 v[92:95], v2 offset:14592\nv_mfma_f32_16x16x32_bf16 v[20:23], a[88:91], v[80:83], v[20:23]\ns_add_u32 s40, s40, 1\nv_mfma_f32_16x16x32_bf16 v[24:27], a[96:99], v[80:83], v[24:27]\n" \
"s_lshl_b32 s44, s40, 17\nv_mfma_f32_16x16x32_bf16 v[28:31], v[72:75], v[80:83], v[28:31]\ns_add_u32 s44, s44, %[xn]\ns_waitcnt lgkmcnt(3)\nv_mfma_f32_16x16x32_bf16 v[32:35], a[80:83], v[84:87], v[32:35]\ns_barrier\nds_read_b128 v[80:83], v2 offset:16384\nv_mfma_f32_16x16x32_bf16 v[36:39], a[88:91], v[84:87], v[36:39]\n" \
"s_lshr_b32 s45, s44, 8\nv_mfma_f32_16x16x32_bf16 v[40:43], a[96:99], v[84:87], v[40:43]\ns_and_b32 s45, s45, 0x2000\nv_mfma_f32_16x16x32_bf16 v[44:47], v[72:75], v[84:87], v[44:47]\ns_xor_b32 s41, s44, s45\ns_waitcnt lgkmcnt(2)\nv_mfma_f32_16x16x32_bf16 v[16:19], a[84:87], v[88:91], v[16:19]\nds_read_b128 v[84:87], v2 offset:16640\n" \
"v_mfma_f32_16x16x32_bf16 v[20:23], a[92:95], v[88:91], v[20:23]\ns_add_u32 s41, s41, %[xw]\ns_and_b32 s42, s40, 3\ns_add_u32 m0, s57, 32768\nv_mfma_f32_16x16x32_bf16 v[24:27], a[100:103], v[88:91], v[24:27]\ns_add_u32 s60, s41, 65536\nbuffer_load_dwordx4 v0, s[36:39], s60 offen offset:0 sc1 lds\nv_mfma_f32_16x16x32_bf16 v[28:31], v[76:79], v[88:91], v[28:31]\n" \
"s_lshl_b32 s42, s42, 15\ns_waitcnt lgkmcnt(2)\nv_mfma_f32_16x16x32_bf16 v[32:35], a[84:87], v[92:95], v[32:35]\nds_read_b128 v[88:91], v2 offset:18432\nv_mfma_f32_16x16x32_bf16 v[36:39], a[92:95], v[92:95], v[36:39]\ns_and_b32 s44, s40, 1\nv_mfma_f32_16x16x32_bf16 v[40:43], a[100:103], v[92:95], v[40:43]\ns_lshl_b32 s55, s44, 17\n" \
"buffer_load_dwordx4 v0, s[36:39], s60 offen offset:1024 sc1 lds\nv_mfma_f32_16x16x32_bf16 v[44:47], v[76:79], v[92:95], v[44:47]\ns_add_u32 s56, s56, %[fs]\ns_waitcnt lgkmcnt(2)\nv_mfma_f32_16x16x32_bf16 v[16:19], a[104:107], v[80:83], v[16:19]\nds_read_b128 v[92:95], v2 offset:18688\nv_mfma_f32_16x16x32_bf16 v[20:23], a[112:115], v[80:83], v[20:23]\nds_read_b128 v[72:75], v1 offset:6144\n" \
"v_mfma_f32_16x16x32_bf16 v[24:27], a[120:123], v[80:83], v[24:27]\nbuffer_load_dwordx4 v0, s[36:39], s60 offen offset:2048 sc1 lds\nv_mfma_f32_16x16x32_bf16 v[28:31], a[128:131], v[80:83], v[28:31]\ns_waitcnt lgkmcnt(3)\nv_mfma_f32_16x16x32_bf16 v[32:35], a[104:107], v[84:87], v[32:35]\nds_read_b128 v[80:83], v2 offset:20480\nv_mfma_f32_16x16x32_bf16 v[36:39], a[112:115], v[84:87], v[36:39]\nv_mfma_f32_16x16x32_bf16 v[40:43], a[120:123], v[84:87], v[40:43]\n" \
"buffer_load_dwordx4 v0, s[36:39], s60 offen offset:3072 sc1 lds\nv_mfma_f32_16x16x32_bf16 v[44:47], a[128:131], v[84:87], v[44:47]\ns_waitcnt lgkmcnt(3)\nv_mfma_f32_16x16x32_bf16 v[16:19], a[108:111], v[88:91], v[16:19]\nds_read_b128 v[84:87], v2 offset:20736\nv_mfma_f32_16x16x32_bf16 v[20:23], a[116:119], v[88:91], v[20:23]\nds_read_b128 v[76:79], v1 offset:7168\nv_mfma_f32_16x16x32_bf16 v[24:27], a[124:127], v[88:91], v[24:27]\n" \
"v_mfma_f32_16x16x32_bf16 v[28:31], a[132:135], v[88:91], v[28:31]\ns_waitcnt lgkmcnt(4)\nv_mfma_f32_16x16x32_bf16 v[32:35], a[108:111], v[92:95], v[32:35]\nds_read_b128 v[88:91], v2 offset:22528\nv_mfma_f32_16x16x32_bf16 v[36:39], a[116:119], v[92:95], v[36:39]\nv_mfma_f32_16x16x32_bf16 v[40:43], a[124:127], v[92:95], v[40:43]\nv_mfma_f32_16x16x32_bf16 v[44:47], a[132:135], v[92:95], v[44:47]\ns_waitcnt lgkmcnt(3)\n" \
"v_mfma_f32_16x16x32_bf16 v[16:19], a[136:139], v[80:83], v[16:19]\nds_read_b128 v[92:95], v2 offset:22784\nv_mfma_f32_16x16x32_bf16 v[20:23], v[72:75], v[80:83], v[20:23]\nds_read_b128 v[64:67], v1 offset:8192\nv_mfma_f32_16x16x32_bf16 v[24:27], a[144:147], v[80:83], v[24:27]\nv_mfma_f32_16x16x32_bf16 v[28:31], a[152:155], v[80:83], v[28:31]\ns_waitcnt lgkmcnt(4)\nv_mfma_f32_16x16x32_bf16 v[32:35], a[136:139], v[84:87], v[32:35]\n" \
"ds_read_b128 v[80:83], v2 offset:24576\nv_mfma_f32_16x16x32_bf16 v[36:39], v[72:75], v[84:87], v[36:39]\nv_mfma_f32_16x16x32_bf16 v[40:43], a[144:147], v[84:87], v[40:43]\nv_mfma_f32_16x16x32_bf16 v[44:47], a[152:155], v[84:87], v[44:47]\ns_waitcnt lgkmcnt(3)\nv_mfma_f32_16x16x32_bf16 v[16:19], a[140:143], v[88:91], v[16:19]\nds_read_b128 v[84:87], v2 offset:24832\nv_mfma_f32_16x16x32_bf16 v[20:23], v[76:79], v[88:91], v[20:23]\n" \
"ds_read_b128 v[68:71], v1 offset:9216\nv_mfma_f32_16x16x32_bf16 v[24:27], a[148:151], v[88:91], v[24:27]\nv_mfma_f32_16x16x32_bf16 v[28:31], a[156:159], v[88:91], v[28:31]\ns_waitcnt lgkmcnt(4)\nv_mfma_f32_16x16x32_bf16 v[32:35], a[140:143], v[92:95], v[32:35]\nds_read_b128 v[88:91], v2 offset:26624\nv_mfma_f32_16x16x32_bf16 v[36:39], v[76:79], v[92:95], v[36:39]\nv_mfma_f32_16x16x32_bf16 v[40:43], a[148:151], v[92:95], v[40:43]\n" \
"v_mfma_f32_16x16x32_bf16 v[44:47], a[156:159], v[92:95], v[44:47]\ns_waitcnt lgkmcnt(3)\nv_mfma_f32_16x16x32_bf16 v[16:19], a[160:163], v[80:83], v[16:19]\nds_read_b128 v[92:95], v2 offset:26880\nv_mfma_f32_16x16x32_bf16 v[20:23], a[168:171], v[80:83], v[20:23]\nds_read_b128 v[72:75], v1 offset:10240\nv_mfma_f32_16x16x32_bf16 v[24:27], v[64:67], v[80:83], v[24:27]\nv_mfma_f32_16x16x32_bf16 v[28:31], a[176:179], v[80:83], v[28:31]\n" \
"s_waitcnt lgkmcnt(4)\nv_mfma_f32_16x16x32_bf16 v[32:35], a[160:163], v[84:87], v[32:35]\nds_read_b128 v[80:83], v2 offset:28672\nv_mfma_f32_16x16x32_bf16 v[36:39], a[168:171], v[84:87], v[36:39]\nv_mfma_f32_16x16x32_bf16 v[40:43], v[64:67], v[84:87], v[40:43]\nv_mfma_f32_16x16x32_bf16 v[44:47], a[176:179], v[84:87], v[44:47]\ns_waitcnt lgkmcnt(3)\nv_mfma_f32_16x16x32_bf16 v[16:19], a[164:167], v[88:91], v[16:19]\n" \
"ds_read_b128 v[84:87], v2 offset:28928\nv_mfma_f32_16x16x32_bf16 v[20:23], a[172:175], v[88:91], v[20:23]\nds_read_b128 v[76:79], v1 offset:11264\nv_mfma_f32_16x16x32_bf16 v[24:27], v[68:71], v[88:91], v[24:27]\nv_mfma_f32_16x16x32_bf16 v[28:31], a[180:183], v[88:91], v[28:31]\ns_waitcnt lgkmcnt(4)\nv_mfma_f32_16x16x32_bf16 v[32:35], a[164:167], v[92:95], v[32:35]\nds_read_b128 v[88:91], v2 offset:30720\n" \
"v_mfma_f32_16x16x32_bf16 v[36:39], a[172:175], v[92:95], v[36:39]\nv_mfma_f32_16x16x32_bf16 v[40:43], v[68:71], v[92:95], v[40:43]\nv_mfma_f32_16x16x32_bf16 v[44:47], a[180:183], v[92:95], v[44:47]\ns_waitcnt lgkmcnt(3)\nv_mfma_f32_16x16x32_bf16 v[16:19], a[184:187], v[80:83], v[16:19]\nds_read_b128 v[92:95], v2 offset:30976\nv_mfma_f32_16x16x32_bf16 v[20:23], a[192:195], v[80:83], v[20:23]\nv_mfma_f32_16x16x32_bf16 v[24:27], a[200:203], v[80:83], v[24:27]\n" \
"v_mfma_f32_16x16x32_bf16 v[28:31], v[72:75], v[80:83], v[28:31]\ns_waitcnt lgkmcnt(3)\nv_mfma_f32_16x16x32_bf16 v[32:35], a[184:187], v[84:87], v[32:35]\ns_waitcnt vmcnt(0)\ns_barrier\nds_read_b128 v[48:51], v2 offset:32768\nv_mfma_f32_16x16x32_bf16 v[36:39], a[192:195], v[84:87], v[36:39]\ns_add_u32 m0, s57, 0\n" \
"v_mfma_f32_16x16x32_bf16 v[40:43], a[200:203], v[84:87], v[40:43]\ns_add_u32 s60, s41, 98304\nbuffer_load_dwordx4 v0, s[36:39], s60 offen offset:0 sc1 lds\nv_mfma_f32_16x16x32_bf16 v[44:47], v[72:75], v[84:87], v[44:47]\ns_waitcnt lgkmcnt(2)\nv_mfma_f32_16x16x32_bf16 v[16:19], a[188:191], v[88:91], v[16:19]\nds_read_b128 v[52:55], v2 offset:33024\nv_mfma_f32_16x16x32_bf16 v[20:23], a[196:199], v[88:91], v[20:23]\n" \
"v_mfma_f32_16x16x32_bf16 v[24:27], a[204:207], v[88:91], v[24:27]\nbuffer_load_dwordx4 v0, s[36:39], s60 offen offset:1024 sc1 lds\nv_mfma_f32_16x16x32_bf16 v[28:31], v[76:79], v[88:91], v[28:31]\ns_waitcnt lgkmcnt(2)\nv_mfma_f32_16x16x32_bf16 v[32:35], a[188:191], v[92:95], v[32:35]\nds_read_b128 v[56:59], v2 offset:34816\nv_mfma_f32_16x16x32_bf16 v[36:39], a[196:199], v[92:95], v[36:39]\nv_mfma_f32_16x16x32_bf16 v[40:43], a[204:207], v[92:95], v[40:43]\n" \
"buffer_load_dwordx4 v0, s[36:39], s60 offen offset:2048 sc1 lds\nv_mfma_f32_16x16x32_bf16 v[44:47], v[76:79], v[92:95], v[44:47]\ns_waitcnt lgkmcnt(2)\nv_mfma_f32_16x16x32_bf16 v[16:19], a[208:211], v[48:51], v[16:19]\nds_read_b128 v[60:63], v2 offset:35072\nv_mfma_f32_16x16x32_bf16 v[20:23], a[216:219], v[48:51], v[20:23]\nds_read_b128 v[72:75], v1 offset:12288\nv_mfma_f32_16x16x32_bf16 v[24:27], a[224:227], v[48:51], v[24:27]\n" \
"buffer_load_dwordx4 v0, s[36:39], s60 offen offset:3072 sc1 lds\nv_mfma_f32_16x16x32_bf16 v[28:31], a[232:235], v[48:51], v[28:31]\ns_waitcnt lgkmcnt(3)\nv_mfma_f32_16x16x32_bf16 v[32:35], a[208:211], v[52:55], v[32:35]\nds_read_b128 v[48:51], v2 offset:36864\nv_mfma_f32_16x16x32_bf16 v[36:39], a[216:219], v[52:55], v[36:39]\nv_mfma_f32_16x16x32_bf16 v[40:43], a[224:227], v[52:55], v[40:43]\nv_mfma_f32_16x16x32_bf16 v[44:47], a[232:235], v[52:55], v[44:47]\n" \
"s_waitcnt lgkmcnt(3)\nv_mfma_f32_16x16x32_bf16 v[16:19], a[212:215], v[56:59], v[16:19]\nds_read_b128 v[52:55], v2 offset:37120\nv_mfma_f32_16x16x32_bf16 v[20:23], a[220:223], v[56:59], v[20:23]\nds_read_b128 v[76:79], v1 offset:13312\nv_mfma_f32_16x16x32_bf16 v[24:27], a[228:231], v[56:59], v[24:27]\nv_mfma_f32_16x16x32_bf16 v[28:31], a[236:239], v[56:59], v[28:31]\ns_waitcnt lgkmcnt(4)\n" \
"v_mfma_f32_16x16x32_bf16 v[32:35], a[212:215], v[60:63], v[32:35]\nds_read_b128 v[56:59], v2 offset:38912\nv_mfma_f32_16x16x32_bf16 v[36:39], a[220:223], v[60:63], v[36:39]\nv_mfma_f32_16x16x32_bf16 v[40:43], a[228:231], v[60:63], v[40:43]\nv_mfma_f32_16x16x32_bf16 v[44:47], a[236:239], v[60:63], v[44:47]\ns_waitcnt lgkmcnt(3)\nv_mfma_f32_16x16x32_bf16 v[16:19], a[240:243], v[48:51], v[16:19]\nds_read_b128 v[60:63], v2 offset:39168\n" \
"v_mfma_f32_16x16x32_bf16 v[20:23], v[72:75], v[48:51], v[20:23]\nds_read_b128 v[64:67], v1 offset:14336\nv_mfma_f32_16x16x32_bf16 v[24:27], a[248:251], v[48:51], v[24:27]\nv_mfma_f32_16x16x32_bf16 v[28:31], v[96:99], v[48:51], v[28:31]\ns_waitcnt lgkmcnt(4)\nv_mfma_f32_16x16x32_bf16 v[32:35], a[240:243], v[52:55], v[32:35]\nds_read_b128 v[48:51], v2 offset:40960\nv_mfma_f32_16x16x32_bf16 v[36:39], v[72:75], v[52:55], v[36:39]\n" \
"v_mfma_f32_16x16x32_bf16 v[40:43], a[248:251], v[52:55], v[40:43]\nv_mfma_f32_16x16x32_bf16 v[44:47], v[96:99], v[52:55], v[44:47]\ns_waitcnt lgkmcnt(3)\nv_mfma_f32_16x16x32_bf16 v[16:19], a[244:247], v[56:59], v[16:19]\nds_read_b128 v[52:55], v2 offset:41216\nv_mfma_f32_16x16x32_bf16 v[20:23], v[76:79], v[56:59], v[20:23]\nds_read_b128 v[68:71], v1 offset:15360\nv_mfma_f32_16x16x32_bf16 v[24:27], a[252:255], v[56:59], v[24:27]\n" \
"v_mfma_f32_16x16x32_bf16 v[28:31], v[100:103], v[56:59], v[28:31]\ns_waitcnt lgkmcnt(4)\nv_mfma_f32_16x16x32_bf16 v[32:35], a[244:247], v[60:63], v[32:35]\nds_read_b128 v[56:59], v2 offset:43008\nv_mfma_f32_16x16x32_bf16 v[36:39], v[76:79], v[60:63], v[36:39]\nv_mfma_f32_16x16x32_bf16 v[40:43], a[252:255], v[60:63], v[40:43]\nv_mfma_f32_16x16x32_bf16 v[44:47], v[100:103], v[60:63], v[44:47]\ns_waitcnt lgkmcnt(3)\n" \
"v_mfma_f32_16x16x32_bf16 v[16:19], v[104:107], v[48:51], v[16:19]\nds_read_b128 v[60:63], v2 offset:43264\nv_mfma_f32_16x16x32_bf16 v[20:23], v[112:115], v[48:51], v[20:23]\nds_read_b128 v[72:75], v1 offset:16384\nv_mfma_f32_16x16x32_bf16 v[24:27], v[64:67], v[48:51], v[24:27]\nv_mfma_f32_16x16x32_bf16 v[28:31], v[120:123], v[48:51], v[28:31]\ns_waitcnt lgkmcnt(4)\nv_mfma_f32_16x16x32_bf16 v[32:35], v[104:107], v[52:55], v[32:35]\n" \
"ds_read_b128 v[48:51], v2 offset:45056\nv_mfma_f32_16x16x32_bf16 v[36:39], v[112:115], v[52:55], v[36:39]\nv_mfma_f32_16x16x32_bf16 v[40:43], v[64:67], v[52:55], v[40:43]\nv_mfma_f32_16x16x32_bf16 v[44:47], v[120:123], v[52:55], v[44:47]\ns_waitcnt lgkmcnt(3)\nv_mfma_f32_16x16x32_bf16 v[16:19], v[108:111], v[56:59], v[16:19]\nds_read_b128 v[52:55], v2 offset:45312\nv_mfma_f32_16x16x32_bf16 v[20:23], v[116:119], v[56:59], v[20:23]\n" \
"ds_read_b128 v[76:79], v1 offset:17408\nv_mfma_f32_16x16x32_bf16 v[24:27], v[68:71], v[56:59], v[24:27]\nv_mfma_f32_16x16x32_bf16 v[28:31], v[124:127], v[56:59], v[28:31]\ns_waitcnt lgkmcnt(4)\nv_mfma_f32_16x16x32_bf16 v[32:35], v[108:111], v[60:63], v[32:35]\nds_read_b128 v[56:59], v2 offset:47104\nv_mfma_f32_16x16x32_bf16 v[36:39], v[116:119], v[60:63], v[36:39]\nv_mfma_f32_16x16x32_bf16 v[40:43], v[68:71], v[60:63], v[40:43]\n" \
"v_mfma_f32_16x16x32_bf16 v[44:47], v[124:127], v[60:63], v[44:47]\ns_waitcnt lgkmcnt(3)\nv_mfma_f32_16x16x32_bf16 v[16:19], v[128:131], v[48:51], v[16:19]\nds_read_b128 v[60:63], v2 offset:47360\nv_mfma_f32_16x16x32_bf16 v[20:23], v[136:139], v[48:51], v[20:23]\nv_mfma_f32_16x16x32_bf16 v[24:27], v[144:147], v[48:51], v[24:27]\nv_mfma_f32_16x16x32_bf16 v[28:31], v[72:75], v[48:51], v[28:31]\ns_waitcnt lgkmcnt(3)\n" \
"v_mfma_f32_16x16x32_bf16 v[32:35], v[128:131], v[52:55], v[32:35]\ns_waitcnt lgkmcnt(0)\ns_waitcnt vmcnt(0)\ns_barrier\nds_read_b128 v[48:51], v2 offset:0\nv_mfma_f32_16x16x32_bf16 v[36:39], v[136:139], v[52:55], v[36:39]\nv_mfma_f32_16x16x32_bf16 v[40:43], v[144:147], v[52:55], v[40:43]\nv_mfma_f32_16x16x32_bf16 v[44:47], v[72:75], v[52:55], v[44:47]\n" \
"v_mfma_f32_16x16x32_bf16 v[16:19], v[132:135], v[56:59], v[16:19]\nds_read_b128 v[52:55], v2 offset:256\nv_mfma_f32_16x16x32_bf16 v[20:23], v[140:143], v[56:59], v[20:23]\nv_mfma_f32_16x16x32_bf16 v[24:27], v[148:151], v[56:59], v[24:27]\nv_mfma_f32_16x16x32_bf16 v[28:31], v[76:79], v[56:59], v[28:31]\nv_mfma_f32_16x16x32_bf16 v[32:35], v[132:135], v[60:63], v[32:35]\nds_read_b128 v[56:59], v2 offset:2048\nv_mfma_f32_16x16x32_bf16 v[36:39], v[140:143], v[60:63], v[36:39]\n" \
"v_mfma_f32_16x16x32_bf16 v[40:43], v[148:151], v[60:63], v[40:43]\nv_mfma_f32_16x16x32_bf16 v[44:47], v[76:79], v[60:63], v[44:47]\ns_waitcnt lgkmcnt(2)\nv_mfma_f32_16x16x32_bf16 v[16:19], v[152:155], v[48:51], v[16:19]\nds_read_b128 v[60:63], v2 offset:2304\nv_mfma_f32_16x16x32_bf16 v[20:23], v[160:163], v[48:51], v[20:23]\nds_read_b128 v[72:75], v1 offset:18432\nv_mfma_f32_16x16x32_bf16 v[24:27], v[168:171], v[48:51], v[24:27]\n" \
"v_mfma_f32_16x16x32_bf16 v[28:31], v[176:179], v[48:51], v[28:31]\ns_waitcnt lgkmcnt(3)\nv_mfma_f32_16x16x32_bf16 v[32:35], v[152:155], v[52:55], v[32:35]\nds_read_b128 v[48:51], v2 offset:4096\nv_mfma_f32_16x16x32_bf16 v[36:39], v[160:163], v[52:55], v[36:39]\nv_mfma_f32_16x16x32_bf16 v[40:43], v[168:171], v[52:55], v[40:43]\nv_mfma_f32_16x16x32_bf16 v[44:47], v[176:179], v[52:55], v[44:47]\ns_waitcnt lgkmcnt(3)\n" \
"v_mfma_f32_16x16x32_bf16 v[16:19], v[156:159], v[56:59], v[16:19]\nds_read_b128 v[52:55], v2 offset:4352\nv_mfma_f32_16x16x32_bf16 v[20:23], v[164:167], v[56:59], v[20:23]\nds_read_b128 v[76:79], v1 offset:19456\nv_mfma_f32_16x16x32_bf16 v[24:27], v[172:175], v[56:59], v[24:27]\nv_mfma_f32_16x16x32_bf16 v[28:31], v[180:183], v[56:59], v[28:31]\ns_waitcnt lgkmcnt(4)\nv_mfma_f32_16x16x32_bf16 v[32:35], v[156:159], v[60:63], v[32:35]\n" \
"ds_read_b128 v[56:59], v2 offset:6144\nv_mfma_f32_16x16x32_bf16 v[36:39], v[164:167], v[60:63], v[36:39]\nv_mfma_f32_16x16x32_bf16 v[40:43], v[172:175], v[60:63], v[40:43]\nv_mfma_f32_16x16x32_bf16 v[44:47], v[180:183], v[60:63], v[44:47]\ns_waitcnt lgkmcnt(3)\nv_mfma_f32_16x16x32_bf16 v[16:19], v[184:187], v[48:51], v[16:19]\nds_read_b128 v[60:63], v2 offset:6400\nv_mfma_f32_16x16x32_bf16 v[20:23], v[72:75], v[48:51], v[20:23]\n" \
"ds_read_b128 v[64:67], v1 offset:20480\nv_mfma_f32_16x16x32_bf16 v[24:27], v[192:195], v[48:51], v[24:27]\nv_mfma_f32_16x16x32_bf16 v[28:31], v[200:203], v[48:51], v[28:31]\ns_waitcnt lgkmcnt(4)\nv_mfma_f32_16x16x32_bf16 v[32:35], v[184:187], v[52:55], v[32:35]\nds_read_b128 v[48:51], v2 offset:8192\nv_mfma_f32_16x16x32_bf16 v[36:39], v[72:75], v[52:55], v[36:39]\nv_mfma_f32_16x16x32_bf16 v[40:43], v[192:195], v[52:55], v[40:43]\n" \
"v_mfma_f32_16x16x32_bf16 v[44:47], v[200:203], v[52:55], v[44:47]\ns_waitcnt lgkmcnt(3)\nv_mfma_f32_16x16x32_bf16 v[16:19], v[188:191], v[56:59], v[16:19]\nds_read_b128 v[52:55], v2 offset:10240\nv_mfma_f32_16x16x32_bf16 v[20:23], v[76:79], v[56:59], v[20:23]\nds_read_b128 v[68:71], v1 offset:21504\nv_mfma_f32_16x16x32_bf16 v[24:27], v[196:199], v[56:59], v[24:27]\nbuffer_load_dwordx4 v[80:83], v3, s[36:39], s42 offen offset:0 sc1\n" \
"v_mfma_f32_16x16x32_bf16 v[28:31], v[204:207], v[56:59], v[28:31]\ns_waitcnt lgkmcnt(4)\nv_mfma_f32_16x16x32_bf16 v[32:35], v[188:191], v[60:63], v[32:35]\nds_read_b128 v[56:59], v2 offset:12288\nv_mfma_f32_16x16x32_bf16 v[36:39], v[76:79], v[60:63], v[36:39]\nds_read_b128 v[72:75], v1 offset:22528\nv_mfma_f32_16x16x32_bf16 v[40:43], v[196:199], v[60:63], v[40:43]\nbuffer_load_dwordx4 v[84:87], v3, s[36:39], s42 offen offset:1024 sc1\n" \
"v_mfma_f32_16x16x32_bf16 v[44:47], v[204:207], v[60:63], v[44:47]\ns_waitcnt lgkmcnt(4)\nv_mfma_f32_16x16x32_bf16 v[16:19], v[208:211], v[48:51], v[16:19]\nds_read_b128 v[60:63], v2 offset:14336\nv_mfma_f32_16x16x32_bf16 v[20:23], v[216:219], v[48:51], v[20:23]\nds_read_b128 v[76:79], v1 offset:23552\nv_mfma_f32_16x16x32_bf16 v[24:27], v[64:67], v[48:51], v[24:27]\nbuffer_load_dwordx4 v[88:91], v3, s[36:39], s42 offen offset:2048 sc1\n" \
"v_mfma_f32_16x16x32_bf16 v[28:31], v[224:227], v[48:51], v[28:31]\ns_waitcnt lgkmcnt(5)\nv_mfma_f32_16x16x32_bf16 v[16:19], v[212:215], v[52:55], v[16:19]\nds_read_b128 v[48:51], v2 offset:8448\nv_mfma_f32_16x16x32_bf16 v[20:23], v[220:223], v[52:55], v[20:23]\ns_waitcnt lgkmcnt(5)\nv_mfma_f32_16x16x32_bf16 v[24:27], v[68:71], v[52:55], v[24:27]\nbuffer_load_dwordx4 v[92:95], v3, s[36:39], s42 offen offset:3072 sc1\n" \
"v_mfma_f32_16x16x32_bf16 v[28:31], v[228:231], v[52:55], v[28:31]\ns_waitcnt lgkmcnt(4)\nv_mfma_f32_16x16x32_bf16 v[16:19], v[232:235], v[56:59], v[16:19]\nds_read_b128 v[52:55], v2 offset:10496\nv_mfma_f32_16x16x32_bf16 v[20:23], v[240:243], v[56:59], v[20:23]\nv_mfma_f32_16x16x32_bf16 v[24:27], v[248:251], v[56:59], v[24:27]\ns_waitcnt lgkmcnt(4)\nv_mfma_f32_16x16x32_bf16 v[28:31], v[72:75], v[56:59], v[28:31]\n" \
"s_waitcnt lgkmcnt(3)\nv_mfma_f32_16x16x32_bf16 v[16:19], v[236:239], v[60:63], v[16:19]\nds_read_b128 v[56:59], v2 offset:12544\nv_mfma_f32_16x16x32_bf16 v[20:23], v[244:247], v[60:63], v[20:23]\nv_mfma_f32_16x16x32_bf16 v[24:27], v[252:255], v[60:63], v[24:27]\ns_waitcnt lgkmcnt(3)\nv_mfma_f32_16x16x32_bf16 v[28:31], v[76:79], v[60:63], v[28:31]\ns_waitcnt lgkmcnt(2)\n" \
"v_mfma_f32_16x16x32_bf16 v[32:35], v[208:211], v[48:51], v[32:35]\nds_read_b128 v[60:63], v2 offset:14592\nv_mfma_f32_16x16x32_bf16 v[36:39], v[216:219], v[48:51], v[36:39]\nv_cvt_pk_bf16_f32 v20, v16, v20\nv_cvt_pk_bf16_f32 v21, v17, v21\nv_mfma_f32_16x16x32_bf16 v[40:43], v[64:67], v[48:51], v[40:43]\nv_cvt_pk_bf16_f32 v22, v18, v22\nv_cvt_pk_bf16_f32 v23, v19, v23\n" \
"v_mfma_f32_16x16x32_bf16 v[44:47], v[224:227], v[48:51], v[44:47]\ns_waitcnt lgkmcnt(2)\nds_write_b128 v5, v[20:23] offset:0\nv_mfma_f32_16x16x32_bf16 v[32:35], v[212:215], v[52:55], v[32:35]\nv_cvt_pk_bf16_f32 v28, v24, v28\nv_cvt_pk_bf16_f32 v29, v25, v29\nv_mfma_f32_16x16x32_bf16 v[36:39], v[220:223], v[52:55], v[36:39]\nv_cvt_pk_bf16_f32 v30, v26, v30\n" \
"v_cvt_pk_bf16_f32 v31, v27, v31\nv_mfma_f32_16x16x32_bf16 v[40:43], v[68:71], v[52:55], v[40:43]\nv_mov_b64 v[48:49], 0\nv_mov_b64 v[50:51], 0\nv_mfma_f32_16x16x32_bf16 v[44:47], v[228:231], v[52:55], v[44:47]\ns_cmp_eq_u32 %[w], 1\nds_write_b128 v5, v[28:31] offset:512\ns_cbranch_scc1 .Lqk_c1%=\n" \
"s_cmp_eq_u32 %[w], 2\ns_cbranch_scc1 .Lqk_c2%=\ns_cmp_eq_u32 %[w], 3\ns_cbranch_scc1 .Lqk_c3%=\n.Lqk_c0%=:\ns_waitcnt lgkmcnt(3)\nv_mfma_f32_16x16x32_bf16 v[32:35], v[232:235], v[56:59], v[32:35]\nv_mov_b64 v[52:53], 0\n" \
"v_mov_b64 v[54:55], 0\nv_mfma_f32_16x16x32_bf16 v[36:39], v[240:243], v[56:59], v[36:39]\ns_waitcnt lgkmcnt(2)\nds_read_b128 v[64:67], v1 offset:0\nv_mfma_f32_16x16x32_bf16 v[32:35], v[236:239], v[60:63], v[32:35]\nds_read_b128 v[68:71], v1 offset:1024\nv_mfma_f32_16x16x32_bf16 v[36:39], v[244:247], v[60:63], v[36:39]\ns_bfe_i32 s54, s40, 0x10000\n" \
"v_mfma_f32_16x16x32_bf16 v[40:43], v[248:251], v[56:59], v[40:43]\nv_mfma_f32_16x16x32_bf16 v[44:47], v[72:75], v[56:59], v[44:47]\nv_cvt_pk_bf16_f32 v36, v32, v36\nv_cvt_pk_bf16_f32 v37, v33, v37\nv_mfma_f32_16x16x32_bf16 v[40:43], v[252:255], v[60:63], v[40:43]\nv_cvt_pk_bf16_f32 v38, v34, v38\nv_cvt_pk_bf16_f32 v39, v35, v39\nv_mfma_f32_16x16x32_bf16 v[44:47], v[76:79], v[60:63], v[44:47]\n" \
"ds_read_b128 v[72:75], v1 offset:2048\nds_read_b128 v[76:79], v1 offset:3072\nv_mov_b64 v[56:57], 0\nv_mov_b64 v[58:59], 0\nv_mov_b64 v[60:61], 0\nv_mov_b64 v[62:63], 0\n.Lqk_poll0%=:\ns_waitcnt vmcnt(0)\n" \
"v_smfmac_f32_16x16x64_bf16 v[48:51], v[80:83], a[0:7], v4\nds_write_b128 v0, v[80:83] offset:16384\nv_max3_u32 v16, v80, v82, v84\nv_smfmac_f32_16x16x64_bf16 v[52:55], v[80:83], a[8:15], v4\nv_max3_u32 v16, v16, v86, v88\nv_smfmac_f32_16x16x64_bf16 v[56:59], v[80:83], a[16:23], v4\nds_write_b128 v0, v[84:87] offset:17408\nv_max3_u32 v16, v16, v90, v92\n" \
"v_max_u32 v16, v16, v94\nv_smfmac_f32_16x16x64_bf16 v[60:63], v[80:83], a[24:31], v4\nv_cmp_eq_u32 vcc, -1, v16\nv_smfmac_f32_16x16x64_bf16 v[48:51], v[84:87], a[32:39], v4\nds_write_b128 v0, v[88:91] offset:18432\ns_cbranch_vccnz .Lqk_re0%=\ns_waitcnt lgkmcnt(5)\nv_smfmac_f32_16x16x64_bf16 v[52:55], v[84:87], v[64:71], v4\n" \
"v_smfmac_f32_16x16x64_bf16 v[56:59], v[84:87], a[40:47], v4\nds_write_b128 v0, v[92:95] offset:19456\nv_smfmac_f32_16x16x64_bf16 v[60:63], v[84:87], a[48:55], v4\nv_smfmac_f32_16x16x64_bf16 v[48:51], v[88:91], a[56:63], v4\nv_smfmac_f32_16x16x64_bf16 v[52:55], v[88:91], a[64:71], v4\ns_waitcnt lgkmcnt(0)\n.Lqk_jn0%=:\ns_barrier\n" \
"s_memrealtime s[68:69]\nds_read_b128 v[80:83], v0 offset:20480\nds_read_b128 v[64:67], v1 offset:4096\nds_read_b128 v[68:71], v1 offset:5120\nv_mov_b32 v24, s40\nv_smfmac_f32_16x16x64_bf16 v[56:59], v[88:91], v[72:79], v4\nds_read_b128 v[72:75], v1 offset:6144\nds_read_b128 v[76:79], v1 offset:7168\n" \
"v_smfmac_f32_16x16x64_bf16 v[60:63], v[88:91], a[72:79], v4\nds_read_b128 v[84:87], v0 offset:21504\nds_read_b128 v[88:91], v0 offset:22528\nv_smfmac_f32_16x16x64_bf16 v[48:51], v[92:95], a[80:87], v4\nds_write_b128 v5, v[36:39] offset:256\nv_cvt_pk_bf16_f32 v44, v40, v44\nv_cvt_pk_bf16_f32 v45, v41, v45\nv_cvt_pk_bf16_f32 v46, v42, v46\n" \
"v_cvt_pk_bf16_f32 v47, v43, v47\nv_smfmac_f32_16x16x64_bf16 v[52:55], v[92:95], a[88:95], v4\nv_smfmac_f32_16x16x64_bf16 v[56:59], v[92:95], a[96:103], v4\ns_waitcnt lgkmcnt(5)\nv_smfmac_f32_16x16x64_bf16 v[60:63], v[92:95], v[64:71], v4\ns_sub_u32 s88, s86, s89\nds_read_b128 v[92:95], v0 offset:23552\nds_read_b128 v[64:67], v1 offset:8192\n" \
"ds_read_b128 v[68:71], v1 offset:9216\nv_smfmac_f32_16x16x64_bf16 v[48:51], v[80:83], a[104:111], v4\ns_min_u32 s88, s88, 150\nds_write_b128 v5, v[44:47] offset:768\nv_smfmac_f32_16x16x64_bf16 v[52:55], v[80:83], a[112:119], v4\ns_add_u32 s88, s88, s68\ns_mov_b32 s89, s68\nv_mov_b32 v25, s88\n" \
"v_smfmac_f32_16x16x64_bf16 v[56:59], v[80:83], a[120:127], v4\nbuffer_store_dwordx2 v[24:25], off, s[36:39], %[sg] sc1\nv_smfmac_f32_16x16x64_bf16 v[60:63], v[80:83], a[128:135], v4\ns_waitcnt lgkmcnt(6)\nds_read_b128 v[80:83], v0 offset:24576\nv_smfmac_f32_16x16x64_bf16 v[48:51], v[84:87], a[136:143], v4\nv_smfmac_f32_16x16x64_bf16 v[52:55], v[84:87], v[72:79], v4\nds_read_b128 v[72:75], v1 offset:10240\n" \
"ds_read_b128 v[76:79], v1 offset:11264\nv_smfmac_f32_16x16x64_bf16 v[56:59], v[84:87], a[144:151], v4\nv_smfmac_f32_16x16x64_bf16 v[60:63], v[84:87], a[152:159], v4\ns_waitcnt lgkmcnt(8)\nds_read_b128 v[84:87], v0 offset:25600\nv_smfmac_f32_16x16x64_bf16 v[48:51], v[88:91], a[160:167], v4\ns_waitcnt lgkmcnt(4)\ns_barrier\n" \
"v_smfmac_f32_16x16x64_bf16 v[52:55], v[88:91], a[168:175], v4\nv_smfmac_f32_16x16x64_bf16 v[56:59], v[88:91], v[64:71], v4\nds_read_b128 v[64:67], v1 offset:12288\nds_read_b128 v[68:71], v1 offset:13312\nv_smfmac_f32_16x16x64_bf16 v[60:63], v[88:91], a[176:183], v4\nds_read_b128 v[88:91], v0 offset:26624\nv_smfmac_f32_16x16x64_bf16 v[48:51], v[92:95], a[184:191], v4\nds_read_b128 v[16:19], v7 offset:0\n" \
"v_smfmac_f32_16x16x64_bf16 v[52:55], v[92:95], a[192:199], v4\nv_smfmac_f32_16x16x64_bf16 v[56:59], v[92:95], a[200:207], v4\ns_waitcnt lgkmcnt(5)\nds_read_b128 v[20:23], v7 offset:64\nv_smfmac_f32_16x16x64_bf16 v[60:63], v[92:95], v[72:79], v4\nds_read_b128 v[92:95], v0 offset:27648\nds_read_b128 v[72:75], v1 offset:14336\nds_read_b128 v[76:79], v1 offset:15360\n" \
"v_smfmac_f32_16x16x64_bf16 v[48:51], v[80:83], a[208:215], v4\nds_read_b128 v[24:27], v7 offset:2048\nv_smfmac_f32_16x16x64_bf16 v[52:55], v[80:83], a[216:223], v4\nv_smfmac_f32_16x16x64_bf16 v[56:59], v[80:83], a[224:231], v4\nds_read_b128 v[28:31], v7 offset:2112\nv_smfmac_f32_16x16x64_bf16 v[60:63], v[80:83], a[232:239], v4\ns_waitcnt lgkmcnt(10)\nds_read_b128 v[80:83], v0 offset:28672\n" \
"v_smfmac_f32_16x16x64_bf16 v[48:51], v[84:87], a[240:247], v4\ns_waitcnt lgkmcnt(9)\nds_read_b128 v[32:35], v7 offset:4096\nv_smfmac_f32_16x16x64_bf16 v[52:55], v[84:87], v[64:71], v4\nds_read_b128 v[64:67], v1 offset:16384\nds_read_b128 v[68:71], v1 offset:17408\nv_smfmac_f32_16x16x64_bf16 v[56:59], v[84:87], a[248:255], v4\nds_read_b128 v[36:39], v7 offset:4160\n" \
"v_smfmac_f32_16x16x64_bf16 v[60:63], v[84:87], v[96:103], v4\ns_waitcnt lgkmcnt(12)\nds_read_b128 v[84:87], v0 offset:29696\nv_smfmac_f32_16x16x64_bf16 v[48:51], v[88:91], v[104:111], v4\nds_read_b128 v[40:43], v7 offset:6144\nv_smfmac_f32_16x16x64_bf16 v[52:55], v[88:91], v[112:119], v4\ns_waitcnt lgkmcnt(9)\nv_smfmac_f32_16x16x64_bf16 v[56:59], v[88:91], v[72:79], v4\n" \
"ds_read_b128 v[72:75], v1 offset:18432\nds_read_b128 v[76:79], v1 offset:19456\nv_smfmac_f32_16x16x64_bf16 v[60:63], v[88:91], v[120:127], v4\nds_read_b128 v[88:91], v0 offset:30720\nv_smfmac_f32_16x16x64_bf16 v[48:51], v[92:95], v[128:135], v4\nds_read_b128 v[44:47], v7 offset:6208\nv_smfmac_f32_16x16x64_bf16 v[52:55], v[92:95], v[136:143], v4\nv_smfmac_f32_16x16x64_bf16 v[56:59], v[92:95], v[144:151], v4\n" \
"s_waitcnt lgkmcnt(7)\nv_smfmac_f32_16x16x64_bf16 v[60:63], v[92:95], v[64:71], v4\nds_read_b128 v[92:95], v0 offset:31744\nds_read_b128 v[64:67], v1 offset:20480\nds_read_b128 v[68:71], v1 offset:21504\nv_smfmac_f32_16x16x64_bf16 v[48:51], v[80:83], v[152:159], v4\nv_smfmac_f32_16x16x64_bf16 v[52:55], v[80:83], v[160:167], v4\nv_smfmac_f32_16x16x64_bf16 v[56:59], v[80:83], v[168:175], v4\n" \
"v_smfmac_f32_16x16x64_bf16 v[60:63], v[80:83], v[176:183], v4\ns_waitcnt lgkmcnt(8)\nv_smfmac_f32_16x16x64_bf16 v[48:51], v[84:87], v[184:191], v4\ns_waitcnt lgkmcnt(5)\nv_smfmac_f32_16x16x64_bf16 v[52:55], v[84:87], v[72:79], v4\nds_read_b128 v[72:75], v1 offset:22528\nds_read_b128 v[76:79], v1 offset:23552\nv_smfmac_f32_16x16x64_bf16 v[56:59], v[84:87], v[192:199], v4\n" \
"v_smfmac_f32_16x16x64_bf16 v[60:63], v[84:87], v[200:207], v4\ns_waitcnt lgkmcnt(6)\nv_smfmac_f32_16x16x64_bf16 v[48:51], v[88:91], v[208:215], v4\nv_smfmac_f32_16x16x64_bf16 v[52:55], v[88:91], v[216:223], v4\ns_waitcnt lgkmcnt(4)\nv_smfmac_f32_16x16x64_bf16 v[48:51], v[92:95], v[232:239], v4\nv_smfmac_f32_16x16x64_bf16 v[52:55], v[92:95], v[240:247], v4\ns_waitcnt lgkmcnt(2)\n" \
"v_smfmac_f32_16x16x64_bf16 v[56:59], v[88:91], v[64:71], v4\nv_add_f32 v48, v48, v50\nv_smfmac_f32_16x16x64_bf16 v[60:63], v[88:91], v[224:231], v4\nv_add_f32 v52, v52, v54\nv_add_f32 v49, v49, v51\nv_smfmac_f32_16x16x64_bf16 v[56:59], v[92:95], v[248:255], v4\ns_waitcnt lgkmcnt(0)\nv_add_f32 v53, v53, v55\n" \
"v_cvt_pk_bf16_f32 v50, v48, v52\nv_smfmac_f32_16x16x64_bf16 v[60:63], v[92:95], v[72:79], v4\nv_cvt_pk_bf16_f32 v51, v49, v53\nds_write2_b32 v6, v50, v51 offset0:0 offset1:4\ns_nop 1\nv_add_f32 v56, v56, v58\nv_add_f32 v57, v57, v59\ns_nop 1\n" \
"v_add_f32 v60, v60, v62\nv_add_f32 v61, v61, v63\nv_cvt_pk_bf16_f32 v62, v56, v60\nv_cvt_pk_bf16_f32 v63, v57, v61\nds_write2_b32 v6, v62, v63 offset0:36 offset1:40\nv_mov_b32 v61, s54\ns_waitcnt lgkmcnt(0)\ns_barrier\n" \
"ds_read_b128 v[80:83], v8 offset:0\nds_read_b128 v[84:87], v8 offset:576\nds_read_b128 v[88:91], v8 offset:1152\nds_read_b128 v[92:95], v8 offset:1728\nds_read_b128 v[64:67], v9 offset:0\nds_read_b128 v[68:71], v9 offset:576\nds_read_b128 v[72:75], v9 offset:1152\nds_read_b128 v[76:79], v9 offset:1728\n" \
"ds_read_b32 v60, v0 offset:53760\ns_waitcnt lgkmcnt(8)\nv_mfma_f32_16x16x32_bf16 v[52:55], v[16:19], v[80:83], 0\nv_mfma_f32_16x16x32_bf16 v[56:59], v[20:23], v[80:83], 0\ns_waitcnt lgkmcnt(7)\nv_mfma_f32_16x16x32_bf16 v[52:55], v[24:27], v[84:87], v[52:55]\nv_mfma_f32_16x16x32_bf16 v[56:59], v[28:31], v[84:87], v[56:59]\ns_waitcnt lgkmcnt(6)\n" \
"v_mfma_f32_16x16x32_bf16 v[52:55], v[32:35], v[88:91], v[52:55]\nv_mfma_f32_16x16x32_bf16 v[56:59], v[36:39], v[88:91], v[56:59]\ns_waitcnt lgkmcnt(5)\nv_mfma_f32_16x16x32_bf16 v[52:55], v[40:43], v[92:95], v[52:55]\nv_mfma_f32_16x16x32_bf16 v[56:59], v[44:47], v[92:95], v[56:59]\ns_waitcnt lgkmcnt(4)\nv_mfma_f32_16x16x32_bf16 v[48:51], v[64:67], v[80:83], 0\ns_waitcnt lgkmcnt(3)\n" \
"v_mfma_f32_16x16x32_bf16 v[48:51], v[68:71], v[84:87], v[48:51]\ns_waitcnt lgkmcnt(2)\nds_read_b128 v[80:83], v2 offset:0\nv_mfma_f32_16x16x32_bf16 v[48:51], v[72:75], v[88:91], v[48:51]\ns_waitcnt lgkmcnt(2)\nds_read_b128 v[84:87], v2 offset:256\nv_mfma_f32_16x16x32_bf16 v[48:51], v[76:79], v[92:95], v[48:51]\nds_read_b128 v[88:91], v2 offset:2048\n" \
"v_max3_f32 v62, v52, v53, v54\nv_max3_f32 v63, v55, v56, v57\nv_max3_f32 v62, v62, v58, v59\nv_cndmask_b32 v52, v52, v56, s[62:63]\nv_cndmask_b32 v53, v53, v57, s[62:63]\nv_cndmask_b32 v54, v54, v58, s[62:63]\nv_cndmask_b32 v55, v55, v59, s[62:63]\nv_max_f32 v62, v62, v63\n" \
"v_max3_f32 v63, v62, v48, v49\nv_max3_f32 v62, v62, v48, v49\ns_waitcnt lgkmcnt(2)\nv_mfma_f32_16x16x32_bf16 v[16:19], a[0:3], v[80:83], 0\nds_read_b128 v[92:95], v2 offset:2304\nv_permlane16_swap_b32 v62, v63\nv_mfma_f32_16x16x32_bf16 v[20:23], a[8:11], v[80:83], 0\nds_read_b128 v[72:75], v1 offset:0\n" \
"v_max_f32 v50, v62, v63\nv_max_f32 v62, v62, v63\nv_mfma_f32_16x16x32_bf16 v[24:27], a[16:19], v[80:83], 0\ns_nop 0\nv_permlane32_swap_b32 v62, v50\nv_max_f32 v62, v62, v50\nv_mfma_f32_16x16x32_bf16 v[28:31], a[24:27], v[80:83], 0\ns_waitcnt lgkmcnt(3)\n" \
"v_mul_f32 v62, s59, v62\nv_fma_f32 v52, v52, s58, v62\nv_fma_f32 v53, v53, s58, v62\nv_mfma_f32_16x16x32_bf16 v[32:35], a[0:3], v[84:87], 0\nds_read_b128 v[80:83], v2 offset:4096\nv_exp_f32 v52, v52\nv_mfma_f32_16x16x32_bf16 v[36:39], a[8:11], v[84:87], 0\nv_fma_f32 v54, v54, s58, v62\n" \
"v_exp_f32 v53, v53\nv_mfma_f32_16x16x32_bf16 v[40:43], a[16:19], v[84:87], 0\nv_fma_f32 v55, v55, s58, v62\nv_exp_f32 v54, v54\nv_mfma_f32_16x16x32_bf16 v[44:47], a[24:27], v[84:87], 0\ns_waitcnt lgkmcnt(3)\nv_add_f32 v63, v52, v53\nv_fma_f32 v48, v48, s58, v62\n" \
"v_add_f32 v63, v63, v54\nv_mfma_f32_16x16x32_bf16 v[16:19], a[4:7], v[88:91], v[16:19]\nds_read_b128 v[84:87], v2 offset:4352\nv_exp_f32 v55, v55\nv_mfma_f32_16x16x32_bf16 v[20:23], a[12:15], v[88:91], v[20:23]\nds_read_b128 v[76:79], v1 offset:1024\nv_fma_f32 v49, v49, s58, v62\nv_add_f32 v63, v63, v55\n" \
"v_mfma_f32_16x16x32_bf16 v[24:27], a[20:23], v[88:91], v[24:27]\nv_exp_f32 v48, v48\nv_mfma_f32_16x16x32_bf16 v[28:31], a[28:31], v[88:91], v[28:31]\ns_waitcnt lgkmcnt(4)\nv_exp_f32 v49, v49\nv_add_f32 v50, v48, v63\nv_mfma_f32_16x16x32_bf16 v[32:35], a[4:7], v[92:95], v[32:35]\nds_read_b128 v[88:91], v2 offset:6144\n" \
"v_add_f32 v50, v50, v49\nv_mfma_f32_16x16x32_bf16 v[36:39], a[12:15], v[92:95], v[36:39]\ns_nop 0\nv_add_f32_dpp v62, v63, v50 row_ror:8 row_mask:0xf bank_mask:0xf\nv_add_f32_dpp v50, v63, v50 row_ror:8 row_mask:0xf bank_mask:0xf\nv_mfma_f32_16x16x32_bf16 v[40:43], a[20:23], v[92:95], v[40:43]\ns_nop 0\nv_permlane16_swap_b32 v62, v50\n" \
"v_add_f32 v63, v62, v50\nv_mfma_f32_16x16x32_bf16 v[44:47], a[28:31], v[92:95], v[44:47]\ns_waitcnt lgkmcnt(3)\nv_add_f32 v50, v62, v50\nv_mfma_f32_16x16x32_bf16 v[16:19], a[32:35], v[80:83], v[16:19]\nds_read_b128 v[92:95], v2 offset:6400\nv_permlane32_swap_b32 v63, v50\nv_mfma_f32_16x16x32_bf16 v[20:23], v[72:75], v[80:83], v[20:23]\n" \
"ds_read_b128 v[64:67], v1 offset:2048\nv_add_f32 v63, v63, v50\nv_mfma_f32_16x16x32_bf16 v[24:27], a[40:43], v[80:83], v[24:27]\nv_rcp_f32 v63, v63\nv_mfma_f32_16x16x32_bf16 v[28:31], a[48:51], v[80:83], v[28:31]\ns_waitcnt lgkmcnt(4)\nv_mul_f32 v52, v52, v63\nv_mul_f32 v53, v53, v63\n" \
"v_mul_f32 v54, v54, v63\nv_mfma_f32_16x16x32_bf16 v[32:35], a[32:35], v[84:87], v[32:35]\nds_read_b128 v[80:83], v2 offset:8192\nv_mul_f32 v55, v55, v63\nv_mul_f32 v48, v48, v63\nv_mfma_f32_16x16x32_bf16 v[36:39], v[72:75], v[84:87], v[36:39]\nv_mul_f32 v49, v49, v63\nv_cvt_pk_bf16_f32 v52, v52, v53\n" \
"v_cvt_pk_bf16_f32 v53, v54, v55\nv_mfma_f32_16x16x32_bf16 v[40:43], a[40:43], v[84:87], v[40:43]\nv_cvt_pk_bf16_f32 v48, v48, v49\nv_bfi_b32 v52, s61, v52, v61\nv_bfi_b32 v53, s61, v53, v61\nv_mfma_f32_16x16x32_bf16 v[44:47], a[48:51], v[84:87], v[44:47]\ns_waitcnt lgkmcnt(3)\nv_bfi_b32 v48, s61, v48, v61\n" \
"v_mov_b32_dpp v54, v52 row_ror:8 row_mask:0xf bank_mask:0xf\nv_mov_b32_dpp v55, v53 row_ror:8 row_mask:0xf bank_mask:0xf\nv_mfma_f32_16x16x32_bf16 v[16:19], a[36:39], v[88:91], v[16:19]\nds_read_b128 v[84:87], v2 offset:8448\nv_mfma_f32_16x16x32_bf16 v[20:23], v[76:79], v[88:91], v[20:23]\nds_read_b128 v[68:71], v1 offset:3072\nv_mfma_f32_16x16x32_bf16 v[24:27], a[44:47], v[88:91], v[24:27]\nbuffer_store_dwordx4 v[52:55], v10, s[36:39], 0 offen sc1\n" \
"v_mov_b32 v49, v48\nv_mfma_f32_16x16x32_bf16 v[28:31], a[52:55], v[88:91], v[28:31]\ns_waitcnt lgkmcnt(4)\nv_permlane32_swap_b32 v48, v49\nv_mfma_f32_16x16x32_bf16 v[32:35], a[36:39], v[92:95], v[32:35]\nds_read_b128 v[88:91], v2 offset:10240\nv_mfma_f32_16x16x32_bf16 v[36:39], v[76:79], v[92:95], v[36:39]\ns_memrealtime s[86:87]\n" \
"buffer_store_dwordx2 v[48:49], v11, s[36:39], 0 offen sc1\nv_cmp_ne_u32 vcc, 0, v60\nv_mfma_f32_16x16x32_bf16 v[40:43], a[44:47], v[92:95], v[40:43]\nv_mfma_f32_16x16x32_bf16 v[44:47], a[52:55], v[92:95], v[44:47]\ns_waitcnt lgkmcnt(3)\nv_mfma_f32_16x16x32_bf16 v[16:19], a[56:59], v[80:83], v[16:19]\nds_read_b128 v[92:95], v2 offset:10496\nv_mfma_f32_16x16x32_bf16 v[20:23], a[64:67], v[80:83], v[20:23]\n" \
"ds_read_b128 v[72:75], v1 offset:4096\nv_mfma_f32_16x16x32_bf16 v[24:27], v[64:67], v[80:83], v[24:27]\nv_mfma_f32_16x16x32_bf16 v[28:31], a[72:75], v[80:83], v[28:31]\ns_waitcnt lgkmcnt(4)\nv_mfma_f32_16x16x32_bf16 v[32:35], a[56:59], v[84:87], v[32:35]\nds_read_b128 v[80:83], v2 offset:12288\nv_mfma_f32_16x16x32_bf16 v[36:39], a[64:67], v[84:87], v[36:39]\nv_mfma_f32_16x16x32_bf16 v[40:43], v[64:67], v[84:87], v[40:43]\n" \
"v_mfma_f32_16x16x32_bf16 v[44:47], a[72:75], v[84:87], v[44:47]\ns_waitcnt lgkmcnt(3)\nv_mfma_f32_16x16x32_bf16 v[16:19], a[60:63], v[88:91], v[16:19]\nds_read_b128 v[84:87], v2 offset:12544\nv_mfma_f32_16x16x32_bf16 v[20:23], a[68:71], v[88:91], v[20:23]\nds_read_b128 v[76:79], v1 offset:5120\nv_mfma_f32_16x16x32_bf16 v[24:27], v[68:71], v[88:91], v[24:27]\nv_mfma_f32_16x16x32_bf16 v[28:31], a[76:79], v[88:91], v[28:31]\n" \
"s_waitcnt lgkmcnt(4)\nv_mfma_f32_16x16x32_bf16 v[32:35], a[60:63], v[92:95], v[32:35]\nds_read_b128 v[88:91], v2 offset:14336\nv_mfma_f32_16x16x32_bf16 v[36:39], a[68:71], v[92:95], v[36:39]\nv_mfma_f32_16x16x32_bf16 v[40:43], v[68:71], v[92:95], v[40:43]\nv_mfma_f32_16x16x32_bf16 v[44:47], a[76:79], v[92:95], v[44:47]\ns_cbranch_vccnz .Lqk_pj%=\ns_cmp_lt_i32 s40, 63\n" \
"s_waitcnt lgkmcnt(0)\ns_cbranch_scc1 .Lqk_pwait%=\ns_branch .Lqk_end%=\n.Lqk_re0%=:\nv_max_u32 v17, v80, v82\nv_cmp_eq_u32 vcc, -1, v17\ns_cbranch_vccz .Lqk_sk1617%=\ns_mov_b64 exec, vcc\n" \
"buffer_load_dwordx4 v[80:83], v3, s[36:39], s42 offen offset:0 sc1\ns_mov_b64 exec, -1\n.Lqk_sk1617%=:\nv_max_u32 v17, v84, v86\nv_cmp_eq_u32 vcc, -1, v17\ns_cbranch_vccz .Lqk_sk1624%=\ns_mov_b64 exec, vcc\nbuffer_load_dwordx4 v[84:87], v3, s[36:39], s42 offen offset:1024 sc1\n" \
"s_mov_b64 exec, -1\n.Lqk_sk1624%=:\nv_max_u32 v17, v88, v90\nv_cmp_eq_u32 vcc, -1, v17\ns_cbranch_vccz .Lqk_sk1631%=\ns_mov_b64 exec, vcc\nbuffer_load_dwordx4 v[88:91], v3, s[36:39], s42 offen offset:2048 sc1\ns_mov_b64 exec, -1\n" \
".Lqk_sk1631%=:\nv_max_u32 v17, v92, v94\nv_cmp_eq_u32 vcc, -1, v17\ns_cbranch_vccz .Lqk_sk1638%=\ns_mov_b64 exec, vcc\nbuffer_load_dwordx4 v[92:95], v3, s[36:39], s42 offen offset:3072 sc1\ns_mov_b64 exec, -1\n.Lqk_sk1638%=:\n" \
"v_mov_b64 v[48:49], 0\nv_mov_b64 v[50:51], 0\nv_mov_b64 v[52:53], 0\nv_mov_b64 v[54:55], 0\nv_mov_b64 v[56:57], 0\nv_mov_b64 v[58:59], 0\nv_mov_b64 v[60:61], 0\nv_mov_b64 v[62:63], 0\n" \
"s_add_u32 s43, s43, 1\ns_cmp_lt_u32 s43, 262144\ns_cbranch_scc1 .Lqk_poll0%=\ns_branch .Lqk_abort%=\n.Lqk_c1%=:\ns_waitcnt lgkmcnt(3)\nv_mfma_f32_16x16x32_bf16 v[32:35], v[232:235], v[56:59], v[32:35]\nv_mov_b64 v[52:53], 0\n" \
"v_mov_b64 v[54:55], 0\nv_mfma_f32_16x16x32_bf16 v[36:39], v[240:243], v[56:59], v[36:39]\ns_waitcnt lgkmcnt(2)\nds_read_b128 v[64:67], v1 offset:6144\nv_mfma_f32_16x16x32_bf16 v[32:35], v[236:239], v[60:63], v[32:35]\nds_read_b128 v[68:71], v1 offset:7168\nv_mfma_f32_16x16x32_bf16 v[36:39], v[244:247], v[60:63], v[36:39]\ns_bfe_i32 s54, s40, 0x10001\n" \
"v_mfma_f32_16x16x32_bf16 v[40:43], v[248:251], v[56:59], v[40:43]\nv_mfma_f32_16x16x32_bf16 v[44:47], v[72:75], v[56:59], v[44:47]\nv_cvt_pk_bf16_f32 v36, v32, v36\nv_cvt_pk_bf16_f32 v37, v33, v37\nv_mfma_f32_16x16x32_bf16 v[40:43], v[252:255], v[60:63], v[40:43]\nv_cvt_pk_bf16_f32 v38, v34, v38\nv_cvt_pk_bf16_f32 v39, v35, v39\nv_mfma_f32_16x16x32_bf16 v[44:47], v[76:79], v[60:63], v[44:47]\n" \
"ds_read_b128 v[72:75], v1 offset:8192\nds_read_b128 v[76:79], v1 offset:9216\nv_mov_b64 v[56:57], 0\nv_mov_b64 v[58:59], 0\nv_mov_b64 v[60:61], 0\nv_mov_b64 v[62:63], 0\n.Lqk_poll1%=:\ns_waitcnt vmcnt(0)\n" \
"v_smfmac_f32_16x16x64_bf16 v[48:51], v[80:83], a[104:111], v4\nds_write_b128 v0, v[80:83] offset:20480\nv_max3_u32 v16, v80, v82, v84\nv_smfmac_f32_16x16x64_bf16 v[52:55], v[80:83], a[112:119], v4\nv_max3_u32 v16, v16, v86, v88\nv_smfmac_f32_16x16x64_bf16 v[56:59], v[80:83], a[120:127], v4\nds_write_b128 v0, v[84:87] offset:21504\nv_max3_u32 v16, v16, v90, v92\n" \
"v_max_u32 v16, v16, v94\nv_smfmac_f32_16x16x64_bf16 v[60:63], v[80:83], a[128:135], v4\nv_cmp_eq_u32 vcc, -1, v16\nv_smfmac_f32_16x16x64_bf16 v[48:51], v[84:87], a[136:143], v4\nds_write_b128 v0, v[88:91] offset:22528\ns_cbranch_vccnz .Lqk_re1%=\ns_waitcnt lgkmcnt(5)\nv_smfmac_f32_16x16x64_bf16 v[52:55], v[84:87], v[64:71], v4\n" \
"v_smfmac_f32_16x16x64_bf16 v[56:59], v[84:87], a[144:151], v4\nds_write_b128 v0, v[92:95] offset:23552\nv_smfmac_f32_16x16x64_bf16 v[60:63], v[84:87], a[152:159], v4\nv_smfmac_f32_16x16x64_bf16 v[48:51], v[88:91], a[160:167], v4\nv_smfmac_f32_16x16x64_bf16 v[52:55], v[88:91], a[168:175], v4\ns_waitcnt lgkmcnt(0)\n.Lqk_jn1%=:\ns_barrier\n" \
"ds_read_b128 v[80:83], v0 offset:24576\nds_read_b128 v[64:67], v1 offset:10240\nds_read_b128 v[68:71], v1 offset:11264\nv_smfmac_f32_16x16x64_bf16 v[56:59], v[88:91], v[72:79], v4\nds_read_b128 v[72:75], v1 offset:12288\nds_read_b128 v[76:79], v1 offset:13312\nv_smfmac_f32_16x16x64_bf16 v[60:63], v[88:91], a[176:183], v4\nds_read_b128 v[84:87], v0 offset:25600\n" \
"ds_read_b128 v[88:91], v0 offset:26624\nv_smfmac_f32_16x16x64_bf16 v[48:51], v[92:95], a[184:191], v4\nds_write_b128 v5, v[36:39] offset:256\nv_cvt_pk_bf16_f32 v44, v40, v44\nv_cvt_pk_bf16_f32 v45, v41, v45\nv_cvt_pk_bf16_f32 v46, v42, v46\nv_cvt_pk_bf16_f32 v47, v43, v47\nv_smfmac_f32_16x16x64_bf16 v[52:55], v[92:95], a[192:199], v4\n" \
"v_smfmac_f32_16x16x64_bf16 v[56:59], v[92:95], a[200:207], v4\ns_waitcnt lgkmcnt(5)\nv_smfmac_f32_16x16x64_bf16 v[60:63], v[92:95], v[64:71], v4\nds_read_b128 v[92:95], v0 offset:27648\nds_read_b128 v[64:67], v1 offset:14336\nds_read_b128 v[68:71], v1 offset:15360\nv_smfmac_f32_16x16x64_bf16 v[48:51], v[80:83], a[208:215], v4\nds_write_b128 v5, v[44:47] offset:768\n" \
"v_smfmac_f32_16x16x64_bf16 v[52:55], v[80:83], a[216:223], v4\nv_smfmac_f32_16x16x64_bf16 v[56:59], v[80:83], a[224:231], v4\nv_smfmac_f32_16x16x64_bf16 v[60:63], v[80:83], a[232:239], v4\ns_waitcnt lgkmcnt(6)\nds_read_b128 v[80:83], v0 offset:28672\nv_smfmac_f32_16x16x64_bf16 v[48:51], v[84:87], a[240:247], v4\nv_smfmac_f32_16x16x64_bf16 v[52:55], v[84:87], v[72:79], v4\nds_read_b128 v[72:75], v1 offset:16384\n" \
"ds_read_b128 v[76:79], v1 offset:17408\nv_smfmac_f32_16x16x64_bf16 v[56:59], v[84:87], a[248:255], v4\nv_smfmac_f32_16x16x64_bf16 v[60:63], v[84:87], v[96:103], v4\ns_waitcnt lgkmcnt(8)\nds_read_b128 v[84:87], v0 offset:29696\nv_smfmac_f32_16x16x64_bf16 v[48:51], v[88:91], v[104:111], v4\ns_waitcnt lgkmcnt(4)\ns_barrier\n" \
"v_smfmac_f32_16x16x64_bf16 v[52:55], v[88:91], v[112:119], v4\nv_smfmac_f32_16x16x64_bf16 v[56:59], v[88:91], v[64:71], v4\nds_read_b128 v[64:67], v1 offset:18432\nds_read_b128 v[68:71], v1 offset:19456\nv_smfmac_f32_16x16x64_bf16 v[60:63], v[88:91], v[120:127], v4\nds_read_b128 v[88:91], v0 offset:30720\nv_smfmac_f32_16x16x64_bf16 v[48:51], v[92:95], v[128:135], v4\nds_read_b128 v[16:19], v7 offset:0\n" \
"v_smfmac_f32_16x16x64_bf16 v[52:55], v[92:95], v[136:143], v4\nv_smfmac_f32_16x16x64_bf16 v[56:59], v[92:95], v[144:151], v4\ns_waitcnt lgkmcnt(5)\nds_read_b128 v[20:23], v7 offset:2048\nv_smfmac_f32_16x16x64_bf16 v[60:63], v[92:95], v[72:79], v4\nds_read_b128 v[92:95], v0 offset:31744\nds_read_b128 v[72:75], v1 offset:20480\nds_read_b128 v[76:79], v1 offset:21504\n" \
"v_smfmac_f32_16x16x64_bf16 v[48:51], v[80:83], v[152:159], v4\nds_read_b128 v[24:27], v7 offset:4096\nv_smfmac_f32_16x16x64_bf16 v[52:55], v[80:83], v[160:167], v4\nv_smfmac_f32_16x16x64_bf16 v[56:59], v[80:83], v[168:175], v4\nds_read_b128 v[28:31], v7 offset:6144\nv_smfmac_f32_16x16x64_bf16 v[60:63], v[80:83], v[176:183], v4\ns_waitcnt lgkmcnt(10)\nds_read_b128 v[80:83], v0 offset:16384\n" \
"v_smfmac_f32_16x16x64_bf16 v[48:51], v[84:87], v[184:191], v4\ns_waitcnt lgkmcnt(9)\nds_read_b128 v[32:35], v7 offset:8192\nv_smfmac_f32_16x16x64_bf16 v[52:55], v[84:87], v[64:71], v4\nds_read_b128 v[64:67], v1 offset:22528\nds_read_b128 v[68:71], v1 offset:23552\nv_smfmac_f32_16x16x64_bf16 v[56:59], v[84:87], v[192:199], v4\nds_read_b128 v[36:39], v7 offset:10240\n" \
"v_smfmac_f32_16x16x64_bf16 v[60:63], v[84:87], v[200:207], v4\ns_waitcnt lgkmcnt(12)\nds_read_b128 v[84:87], v0 offset:17408\nv_smfmac_f32_16x16x64_bf16 v[48:51], v[88:91], v[208:215], v4\nds_read_b128 v[40:43], v7 offset:12288\nv_smfmac_f32_16x16x64_bf16 v[52:55], v[88:91], v[216:223], v4\ns_waitcnt lgkmcnt(9)\nv_smfmac_f32_16x16x64_bf16 v[56:59], v[88:91], v[72:79], v4\n" \
"ds_read_b128 v[72:75], v1 offset:0\nds_read_b128 v[76:79], v1 offset:1024\nv_smfmac_f32_16x16x64_bf16 v[60:63], v[88:91], v[224:231], v4\nds_read_b128 v[88:91], v0 offset:18432\nv_smfmac_f32_16x16x64_bf16 v[48:51], v[92:95], v[232:239], v4\nds_read_b128 v[44:47], v7 offset:14336\nv_smfmac_f32_16x16x64_bf16 v[52:55], v[92:95], v[240:247], v4\nv_smfmac_f32_16x16x64_bf16 v[56:59], v[92:95], v[248:255], v4\n" \
"s_waitcnt lgkmcnt(7)\nv_smfmac_f32_16x16x64_bf16 v[60:63], v[92:95], v[64:71], v4\nds_read_b128 v[92:95], v0 offset:19456\nds_read_b128 v[64:67], v1 offset:2048\nds_read_b128 v[68:71], v1 offset:3072\nv_smfmac_f32_16x16x64_bf16 v[48:51], v[80:83], a[0:7], v4\nv_smfmac_f32_16x16x64_bf16 v[52:55], v[80:83], a[8:15], v4\nv_smfmac_f32_16x16x64_bf16 v[56:59], v[80:83], a[16:23], v4\n" \
"v_smfmac_f32_16x16x64_bf16 v[60:63], v[80:83], a[24:31], v4\ns_waitcnt lgkmcnt(8)\nv_smfmac_f32_16x16x64_bf16 v[48:51], v[84:87], a[32:39], v4\ns_waitcnt lgkmcnt(5)\nv_smfmac_f32_16x16x64_bf16 v[52:55], v[84:87], v[72:79], v4\nds_read_b128 v[72:75], v1 offset:4096\nds_read_b128 v[76:79], v1 offset:5120\nv_smfmac_f32_16x16x64_bf16 v[56:59], v[84:87], a[40:47], v4\n" \
"v_smfmac_f32_16x16x64_bf16 v[60:63], v[84:87], a[48:55], v4\ns_waitcnt lgkmcnt(6)\nv_smfmac_f32_16x16x64_bf16 v[48:51], v[88:91], a[56:63], v4\nv_smfmac_f32_16x16x64_bf16 v[52:55], v[88:91], a[64:71], v4\ns_waitcnt lgkmcnt(4)\nv_smfmac_f32_16x16x64_bf16 v[48:51], v[92:95], a[80:87], v4\nv_smfmac_f32_16x16x64_bf16 v[52:55], v[92:95], a[88:95], v4\ns_waitcnt lgkmcnt(2)\n" \
"v_smfmac_f32_16x16x64_bf16 v[56:59], v[88:91], v[64:71], v4\nv_add_f32 v48, v48, v50\nv_smfmac_f32_16x16x64_bf16 v[60:63], v[88:91], a[72:79], v4\nv_add_f32 v52, v52, v54\nv_add_f32 v49, v49, v51\nv_smfmac_f32_16x16x64_bf16 v[56:59], v[92:95], a[96:103], v4\ns_waitcnt lgkmcnt(0)\nv_add_f32 v53, v53, v55\n" \
"v_cvt_pk_bf16_f32 v50, v48, v52\nv_smfmac_f32_16x16x64_bf16 v[60:63], v[92:95], v[72:79], v4\nv_cvt_pk_bf16_f32 v51, v49, v53\nds_write2_b32 v6, v50, v51 offset0:0 offset1:4\ns_nop 1\nv_add_f32 v56, v56, v58\nv_add_f32 v57, v57, v59\ns_nop 1\n" \
"v_add_f32 v60, v60, v62\nv_add_f32 v61, v61, v63\nv_cvt_pk_bf16_f32 v62, v56, v60\nv_cvt_pk_bf16_f32 v63, v57, v61\nds_write2_b32 v6, v62, v63 offset0:36 offset1:40\nv_mov_b32 v61, s54\nv_mfma_f32_16x16x32_bf16 v[52:55], v[32:35], v[16:19], v[12:15]\nv_mfma_f32_16x16x32_bf16 v[52:55], v[36:39], v[20:23], v[52:55]\n" \
"v_mfma_f32_16x16x32_bf16 v[52:55], v[40:43], v[24:27], v[52:55]\nv_mfma_f32_16x16x32_bf16 v[52:55], v[44:47], v[28:31], v[52:55]\ns_waitcnt lgkmcnt(0)\ns_barrier\nds_read_b128 v[32:35], v9 offset:0\nds_read_b128 v[36:39], v9 offset:576\nds_read_b128 v[40:43], v9 offset:1152\nds_read_b128 v[44:47], v9 offset:1728\n" \
"ds_read_b32 v60, v0 offset:53760\nds_read_b128 v[80:83], v2 offset:0\nds_read_b128 v[84:87], v2 offset:256\nds_read_b128 v[88:91], v2 offset:2048\ns_waitcnt lgkmcnt(7)\nv_mfma_f32_16x16x32_bf16 v[48:51], v[32:35], v[16:19], 0\ns_waitcnt lgkmcnt(6)\nv_mfma_f32_16x16x32_bf16 v[48:51], v[36:39], v[20:23], v[48:51]\n" \
"s_waitcnt lgkmcnt(5)\nv_mfma_f32_16x16x32_bf16 v[48:51], v[40:43], v[24:27], v[48:51]\ns_waitcnt lgkmcnt(4)\nv_mfma_f32_16x16x32_bf16 v[48:51], v[44:47], v[28:31], v[48:51]\ns_waitcnt lgkmcnt(2)\nv_mfma_f32_16x16x32_bf16 v[16:19], a[0:3], v[80:83], 0\nds_read_b128 v[92:95], v2 offset:2304\nv_max3_f32 v62, v52, v53, v54\n" \
"v_max_f32 v62, v62, v55\nv_mfma_f32_16x16x32_bf16 v[20:23], a[8:11], v[80:83], 0\nds_read_b128 v[72:75], v1 offset:0\nv_max3_f32 v63, v62, v48, v49\nv_max3_f32 v62, v62, v48, v49\nv_mfma_f32_16x16x32_bf16 v[24:27], a[16:19], v[80:83], 0\ns_nop 0\nv_permlane16_swap_b32 v62, v63\n" \
"v_max_f32 v50, v62, v63\nv_mfma_f32_16x16x32_bf16 v[28:31], a[24:27], v[80:83], 0\ns_waitcnt lgkmcnt(3)\nv_max_f32 v62, v62, v63\nv_mfma_f32_16x16x32_bf16 v[32:35], a[0:3], v[84:87], 0\nds_read_b128 v[80:83], v2 offset:4096\nv_permlane32_swap_b32 v62, v50\nv_mfma_f32_16x16x32_bf16 v[36:39], a[8:11], v[84:87], 0\n" \
"v_max_f32 v62, v62, v50\nv_mul_f32 v62, s59, v62\nv_fma_f32 v52, v52, s58, v62\nv_mfma_f32_16x16x32_bf16 v[40:43], a[16:19], v[84:87], 0\nv_fma_f32 v53, v53, s58, v62\nv_exp_f32 v52, v52\nv_mfma_f32_16x16x32_bf16 v[44:47], a[24:27], v[84:87], 0\ns_waitcnt lgkmcnt(3)\n" \
"v_fma_f32 v54, v54, s58, v62\nv_exp_f32 v53, v53\nv_mfma_f32_16x16x32_bf16 v[16:19], a[4:7], v[88:91], v[16:19]\nds_read_b128 v[84:87], v2 offset:4352\nv_fma_f32 v55, v55, s58, v62\nv_add_f32 v63, v52, v53\nv_mfma_f32_16x16x32_bf16 v[20:23], a[12:15], v[88:91], v[20:23]\nds_read_b128 v[76:79], v1 offset:1024\n" \
"v_exp_f32 v54, v54\nv_mfma_f32_16x16x32_bf16 v[24:27], a[20:23], v[88:91], v[24:27]\nv_fma_f32 v48, v48, s58, v62\nv_exp_f32 v55, v55\nv_mfma_f32_16x16x32_bf16 v[28:31], a[28:31], v[88:91], v[28:31]\ns_waitcnt lgkmcnt(4)\nv_add_f32 v63, v63, v54\nv_fma_f32 v49, v49, s58, v62\n" \
"v_add_f32 v63, v63, v55\nv_mfma_f32_16x16x32_bf16 v[32:35], a[4:7], v[92:95], v[32:35]\nds_read_b128 v[88:91], v2 offset:6144\nv_exp_f32 v48, v48\nv_mfma_f32_16x16x32_bf16 v[36:39], a[12:15], v[92:95], v[36:39]\nv_exp_f32 v49, v49\nv_add_f32 v63, v63, v48\nv_mfma_f32_16x16x32_bf16 v[40:43], a[20:23], v[92:95], v[40:43]\n" \
"v_add_f32 v62, v63, v49\nv_add_f32 v50, v63, v49\nv_mfma_f32_16x16x32_bf16 v[44:47], a[28:31], v[92:95], v[44:47]\ns_waitcnt lgkmcnt(3)\nv_permlane16_swap_b32 v62, v50\nv_add_f32 v63, v62, v50\nv_mfma_f32_16x16x32_bf16 v[16:19], a[32:35], v[80:83], v[16:19]\nds_read_b128 v[92:95], v2 offset:6400\n" \
"v_add_f32 v50, v62, v50\nv_mfma_f32_16x16x32_bf16 v[20:23], v[72:75], v[80:83], v[20:23]\nds_read_b128 v[64:67], v1 offset:2048\nv_permlane32_swap_b32 v63, v50\nv_mfma_f32_16x16x32_bf16 v[24:27], a[40:43], v[80:83], v[24:27]\nv_add_f32 v63, v63, v50\nv_rcp_f32 v63, v63\nv_mfma_f32_16x16x32_bf16 v[28:31], a[48:51], v[80:83], v[28:31]\n" \
"s_waitcnt lgkmcnt(4)\nv_mul_f32 v52, v52, v63\nv_mul_f32 v53, v53, v63\nv_mul_f32 v54, v54, v63\nv_mfma_f32_16x16x32_bf16 v[32:35], a[32:35], v[84:87], v[32:35]\nds_read_b128 v[80:83], v2 offset:8192\nv_mul_f32 v55, v55, v63\nv_mul_f32 v48, v48, v63\n" \
"v_mfma_f32_16x16x32_bf16 v[36:39], v[72:75], v[84:87], v[36:39]\nv_mul_f32 v49, v49, v63\nv_cvt_pk_bf16_f32 v52, v52, v53\nv_cvt_pk_bf16_f32 v53, v54, v55\nv_mfma_f32_16x16x32_bf16 v[40:43], a[40:43], v[84:87], v[40:43]\nv_cvt_pk_bf16_f32 v48, v48, v49\nv_bfi_b32 v52, s61, v52, v61\nv_bfi_b32 v53, s61, v53, v61\n" \
"v_mfma_f32_16x16x32_bf16 v[44:47], a[48:51], v[84:87], v[44:47]\ns_waitcnt lgkmcnt(3)\nv_bfi_b32 v48, s61, v48, v61\nv_mfma_f32_16x16x32_bf16 v[16:19], a[36:39], v[88:91], v[16:19]\nds_read_b128 v[84:87], v2 offset:8448\nv_mfma_f32_16x16x32_bf16 v[20:23], v[76:79], v[88:91], v[20:23]\nds_read_b128 v[68:71], v1 offset:3072\nv_mfma_f32_16x16x32_bf16 v[24:27], a[44:47], v[88:91], v[24:27]\n" \
"buffer_store_dwordx2 v[52:53], v10, s[36:39], s55 offen sc1\nv_mov_b32 v49, v48\nv_mfma_f32_16x16x32_bf16 v[28:31], a[52:55], v[88:91], v[28:31]\ns_waitcnt lgkmcnt(4)\nv_permlane16_swap_b32 v48, v49\nv_mov_b32 v50, v48\nv_mfma_f32_16x16x32_bf16 v[32:35], a[36:39], v[92:95], v[32:35]\nds_read_b128 v[88:91], v2 offset:10240\n" \
"v_mov_b32 v51, v49\nv_mfma_f32_16x16x32_bf16 v[36:39], v[76:79], v[92:95], v[36:39]\nv_permlane32_swap_b32 v48, v50\nv_mfma_f32_16x16x32_bf16 v[40:43], a[44:47], v[92:95], v[40:43]\nv_permlane32_swap_b32 v49, v51\nv_mfma_f32_16x16x32_bf16 v[44:47], a[52:55], v[92:95], v[44:47]\ns_waitcnt lgkmcnt(3)\nbuffer_store_dwordx4 v[48:51], v11, s[36:39], s55 offen sc1\n" \
"v_cmp_ne_u32 vcc, 0, v60\nv_mfma_f32_16x16x32_bf16 v[16:19], a[56:59], v[80:83], v[16:19]\nds_read_b128 v[92:95], v2 offset:10496\nv_mfma_f32_16x16x32_bf16 v[20:23], a[64:67], v[80:83], v[20:23]\nds_read_b128 v[72:75], v1 offset:4096\nv_mfma_f32_16x16x32_bf16 v[24:27], v[64:67], v[80:83], v[24:27]\nv_mfma_f32_16x16x32_bf16 v[28:31], a[72:75], v[80:83], v[28:31]\ns_waitcnt lgkmcnt(4)\n" \
"v_mfma_f32_16x16x32_bf16 v[32:35], a[56:59], v[84:87], v[32:35]\nds_read_b128 v[80:83], v2 offset:12288\nv_mfma_f32_16x16x32_bf16 v[36:39], a[64:67], v[84:87], v[36:39]\nv_mfma_f32_16x16x32_bf16 v[40:43], v[64:67], v[84:87], v[40:43]\nv_mfma_f32_16x16x32_bf16 v[44:47], a[72:75], v[84:87], v[44:47]\ns_waitcnt lgkmcnt(3)\nv_mfma_f32_16x16x32_bf16 v[16:19], a[60:63], v[88:91], v[16:19]\nds_read_b128 v[84:87], v2 offset:12544\n" \
"v_mfma_f32_16x16x32_bf16 v[20:23], a[68:71], v[88:91], v[20:23]\nds_read_b128 v[76:79], v1 offset:5120\nv_mfma_f32_16x16x32_bf16 v[24:27], v[68:71], v[88:91], v[24:27]\nv_mfma_f32_16x16x32_bf16 v[28:31], a[76:79], v[88:91], v[28:31]\ns_waitcnt lgkmcnt(4)\nv_mfma_f32_16x16x32_bf16 v[32:35], a[60:63], v[92:95], v[32:35]\nds_read_b128 v[88:91], v2 offset:14336\nv_mfma_f32_16x16x32_bf16 v[36:39], a[68:71], v[92:95], v[36:39]\n" \
"v_mfma_f32_16x16x32_bf16 v[40:43], v[68:71], v[92:95], v[40:43]\nv_mfma_f32_16x16x32_bf16 v[44:47], a[76:79], v[92:95], v[44:47]\ns_cbranch_vccnz .Lqk_pj%=\ns_cmp_lt_i32 s40, 63\ns_waitcnt lgkmcnt(0)\ns_cbranch_scc1 .Lqk_pwait%=\ns_branch .Lqk_end%=\n.Lqk_re1%=:\n" \
"v_max_u32 v17, v80, v82\nv_cmp_eq_u32 vcc, -1, v17\ns_cbranch_vccz .Lqk_sk2002%=\ns_mov_b64 exec, vcc\nbuffer_load_dwordx4 v[80:83], v3, s[36:39], s42 offen offset:0 sc1\ns_mov_b64 exec, -1\n.Lqk_sk2002%=:\nv_max_u32 v17, v84, v86\n" \
"v_cmp_eq_u32 vcc, -1, v17\ns_cbranch_vccz .Lqk_sk2009%=\ns_mov_b64 exec, vcc\nbuffer_load_dwordx4 v[84:87], v3, s[36:39], s42 offen offset:1024 sc1\ns_mov_b64 exec, -1\n.Lqk_sk2009%=:\nv_max_u32 v17, v88, v90\nv_cmp_eq_u32 vcc, -1, v17\n" \
"s_cbranch_vccz .Lqk_sk2016%=\ns_mov_b64 exec, vcc\nbuffer_load_dwordx4 v[88:91], v3, s[36:39], s42 offen offset:2048 sc1\ns_mov_b64 exec, -1\n.Lqk_sk2016%=:\nv_max_u32 v17, v92, v94\nv_cmp_eq_u32 vcc, -1, v17\ns_cbranch_vccz .Lqk_sk2023%=\n" \
"s_mov_b64 exec, vcc\nbuffer_load_dwordx4 v[92:95], v3, s[36:39], s42 offen offset:3072 sc1\ns_mov_b64 exec, -1\n.Lqk_sk2023%=:\nv_mov_b64 v[48:49], 0\nv_mov_b64 v[50:51], 0\nv_mov_b64 v[52:53], 0\nv_mov_b64 v[54:55], 0\n" \
"v_mov_b64 v[56:57], 0\nv_mov_b64 v[58:59], 0\nv_mov_b64 v[60:61], 0\nv_mov_b64 v[62:63], 0\ns_add_u32 s43, s43, 1\ns_cmp_lt_u32 s43, 262144\ns_cbranch_scc1 .Lqk_poll1%=\ns_branch .Lqk_abort%=\n" \
".Lqk_c2%=:\ns_waitcnt lgkmcnt(3)\nv_mfma_f32_16x16x32_bf16 v[32:35], v[232:235], v[56:59], v[32:35]\nv_mov_b64 v[52:53], 0\nv_mov_b64 v[54:55], 0\nv_mfma_f32_16x16x32_bf16 v[36:39], v[240:243], v[56:59], v[36:39]\ns_waitcnt lgkmcnt(2)\nds_read_b128 v[64:67], v1 offset:12288\n" \
"v_mfma_f32_16x16x32_bf16 v[32:35], v[236:239], v[60:63], v[32:35]\nds_read_b128 v[68:71], v1 offset:13312\nv_mfma_f32_16x16x32_bf16 v[36:39], v[244:247], v[60:63], v[36:39]\ns_bfe_i32 s54, s40, 0x10001\nv_mfma_f32_16x16x32_bf16 v[40:43], v[248:251], v[56:59], v[40:43]\nv_mfma_f32_16x16x32_bf16 v[44:47], v[72:75], v[56:59], v[44:47]\nv_cvt_pk_bf16_f32 v36, v32, v36\nv_cvt_pk_bf16_f32 v37, v33, v37\n" \
"v_mfma_f32_16x16x32_bf16 v[40:43], v[252:255], v[60:63], v[40:43]\nv_cvt_pk_bf16_f32 v38, v34, v38\nv_cvt_pk_bf16_f32 v39, v35, v39\nv_mfma_f32_16x16x32_bf16 v[44:47], v[76:79], v[60:63], v[44:47]\nds_read_b128 v[72:75], v1 offset:14336\nds_read_b128 v[76:79], v1 offset:15360\nv_mov_b64 v[56:57], 0\nv_mov_b64 v[58:59], 0\n" \
"v_mov_b64 v[60:61], 0\nv_mov_b64 v[62:63], 0\n.Lqk_poll2%=:\ns_waitcnt vmcnt(0)\nv_smfmac_f32_16x16x64_bf16 v[48:51], v[80:83], a[208:215], v4\nds_write_b128 v0, v[80:83] offset:24576\nv_max3_u32 v16, v80, v82, v84\nv_smfmac_f32_16x16x64_bf16 v[52:55], v[80:83], a[216:223], v4\n" \
"v_max3_u32 v16, v16, v86, v88\nv_smfmac_f32_16x16x64_bf16 v[56:59], v[80:83], a[224:231], v4\nds_write_b128 v0, v[84:87] offset:25600\nv_max3_u32 v16, v16, v90, v92\nv_max_u32 v16, v16, v94\nv_smfmac_f32_16x16x64_bf16 v[60:63], v[80:83], a[232:239], v4\nv_cmp_eq_u32 vcc, -1, v16\nv_smfmac_f32_16x16x64_bf16 v[48:51], v[84:87], a[240:247], v4\n" \
"ds_write_b128 v0, v[88:91] offset:26624\ns_cbranch_vccnz .Lqk_re2%=\ns_waitcnt lgkmcnt(5)\nv_smfmac_f32_16x16x64_bf16 v[52:55], v[84:87], v[64:71], v4\nv_smfmac_f32_16x16x64_bf16 v[56:59], v[84:87], a[248:255], v4\nds_write_b128 v0, v[92:95] offset:27648\nv_smfmac_f32_16x16x64_bf16 v[60:63], v[84:87], v[96:103], v4\nv_smfmac_f32_16x16x64_bf16 v[48:51], v[88:91], v[104:111], v4\n" \
"v_smfmac_f32_16x16x64_bf16 v[52:55], v[88:91], v[112:119], v4\ns_waitcnt lgkmcnt(0)\n.Lqk_jn2%=:\ns_barrier\nds_read_b128 v[80:83], v0 offset:28672\nds_read_b128 v[64:67], v1 offset:16384\nds_read_b128 v[68:71], v1 offset:17408\nv_smfmac_f32_16x16x64_bf16 v[56:59], v[88:91], v[72:79], v4\n" \
"ds_read_b128 v[72:75], v1 offset:18432\nds_read_b128 v[76:79], v1 offset:19456\nv_smfmac_f32_16x16x64_bf16 v[60:63], v[88:91], v[120:127], v4\nds_read_b128 v[84:87], v0 offset:29696\nds_read_b128 v[88:91], v0 offset:30720\nv_smfmac_f32_16x16x64_bf16 v[48:51], v[92:95], v[128:135], v4\nds_write_b128 v5, v[36:39] offset:256\nv_cvt_pk_bf16_f32 v44, v40, v44\n" \
"v_cvt_pk_bf16_f32 v45, v41, v45\nv_cvt_pk_bf16_f32 v46, v42, v46\nv_cvt_pk_bf16_f32 v47, v43, v47\nv_smfmac_f32_16x16x64_bf16 v[52:55], v[92:95], v[136:143], v4\nv_smfmac_f32_16x16x64_bf16 v[56:59], v[92:95], v[144:151], v4\ns_waitcnt lgkmcnt(5)\nv_smfmac_f32_16x16x64_bf16 v[60:63], v[92:95], v[64:71], v4\nds_read_b128 v[92:95], v0 offset:31744\n" \
"ds_read_b128 v[64:67], v1 offset:20480\nds_read_b128 v[68:71], v1 offset:21504\nv_smfmac_f32_16x16x64_bf16 v[48:51], v[80:83], v[152:159], v4\nds_write_b128 v5, v[44:47] offset:768\nv_smfmac_f32_16x16x64_bf16 v[52:55], v[80:83], v[160:167], v4\nv_smfmac_f32_16x16x64_bf16 v[56:59], v[80:83], v[168:175], v4\nv_smfmac_f32_16x16x64_bf16 v[60:63], v[80:83], v[176:183], v4\ns_waitcnt lgkmcnt(6)\n" \
"ds_read_b128 v[80:83], v0 offset:16384\nv_smfmac_f32_16x16x64_bf16 v[48:51], v[84:87], v[184:191], v4\nv_smfmac_f32_16x16x64_bf16 v[52:55], v[84:87], v[72:79], v4\nds_read_b128 v[72:75], v1 offset:22528\nds_read_b128 v[76:79], v1 offset:23552\nv_smfmac_f32_16x16x64_bf16 v[56:59], v[84:87], v[192:199], v4\nv_smfmac_f32_16x16x64_bf16 v[60:63], v[84:87], v[200:207], v4\ns_waitcnt lgkmcnt(8)\n" \
"ds_read_b128 v[84:87], v0 offset:17408\nv_smfmac_f32_16x16x64_bf16 v[48:51], v[88:91], v[208:215], v4\ns_waitcnt lgkmcnt(4)\ns_barrier\nv_smfmac_f32_16x16x64_bf16 v[52:55], v[88:91], v[216:223], v4\nv_smfmac_f32_16x16x64_bf16 v[56:59], v[88:91], v[64:71], v4\nds_read_b128 v[64:67], v1 offset:0\nds_read_b128 v[68:71], v1 offset:1024\n" \
"v_smfmac_f32_16x16x64_bf16 v[60:63], v[88:91], v[224:231], v4\nds_read_b128 v[88:91], v0 offset:18432\nv_smfmac_f32_16x16x64_bf16 v[48:51], v[92:95], v[232:239], v4\nds_read_b128 v[16:19], v7 offset:256\nv_smfmac_f32_16x16x64_bf16 v[52:55], v[92:95], v[240:247], v4\nv_smfmac_f32_16x16x64_bf16 v[56:59], v[92:95], v[248:255], v4\ns_waitcnt lgkmcnt(5)\nds_read_b128 v[20:23], v7 offset:2304\n" \
"v_smfmac_f32_16x16x64_bf16 v[60:63], v[92:95], v[72:79], v4\nds_read_b128 v[92:95], v0 offset:19456\nds_read_b128 v[72:75], v1 offset:2048\nds_read_b128 v[76:79], v1 offset:3072\nv_smfmac_f32_16x16x64_bf16 v[48:51], v[80:83], a[0:7], v4\nds_read_b128 v[24:27], v7 offset:4352\nv_smfmac_f32_16x16x64_bf16 v[52:55], v[80:83], a[8:15], v4\nv_smfmac_f32_16x16x64_bf16 v[56:59], v[80:83], a[16:23], v4\n" \
"ds_read_b128 v[28:31], v7 offset:6400\nv_smfmac_f32_16x16x64_bf16 v[60:63], v[80:83], a[24:31], v4\ns_waitcnt lgkmcnt(10)\nds_read_b128 v[80:83], v0 offset:20480\nv_smfmac_f32_16x16x64_bf16 v[48:51], v[84:87], a[32:39], v4\ns_waitcnt lgkmcnt(9)\nds_read_b128 v[32:35], v7 offset:8192\nv_smfmac_f32_16x16x64_bf16 v[52:55], v[84:87], v[64:71], v4\n" \
"ds_read_b128 v[64:67], v1 offset:4096\nds_read_b128 v[68:71], v1 offset:5120\nv_smfmac_f32_16x16x64_bf16 v[56:59], v[84:87], a[40:47], v4\nds_read_b128 v[36:39], v7 offset:10240\nv_smfmac_f32_16x16x64_bf16 v[60:63], v[84:87], a[48:55], v4\ns_waitcnt lgkmcnt(12)\nds_read_b128 v[84:87], v0 offset:21504\nv_smfmac_f32_16x16x64_bf16 v[48:51], v[88:91], a[56:63], v4\n" \
"ds_read_b128 v[40:43], v7 offset:12288\nv_smfmac_f32_16x16x64_bf16 v[52:55], v[88:91], a[64:71], v4\ns_waitcnt lgkmcnt(9)\nv_smfmac_f32_16x16x64_bf16 v[56:59], v[88:91], v[72:79], v4\nds_read_b128 v[72:75], v1 offset:6144\nds_read_b128 v[76:79], v1 offset:7168\nv_smfmac_f32_16x16x64_bf16 v[60:63], v[88:91], a[72:79], v4\nds_read_b128 v[88:91], v0 offset:22528\n" \
"v_smfmac_f32_16x16x64_bf16 v[48:51], v[92:95], a[80:87], v4\nds_read_b128 v[44:47], v7 offset:14336\nv_smfmac_f32_16x16x64_bf16 v[52:55], v[92:95], a[88:95], v4\nv_smfmac_f32_16x16x64_bf16 v[56:59], v[92:95], a[96:103], v4\ns_waitcnt lgkmcnt(7)\nv_smfmac_f32_16x16x64_bf16 v[60:63], v[92:95], v[64:71], v4\nds_read_b128 v[92:95], v0 offset:23552\nds_read_b128 v[64:67], v1 offset:8192\n" \
"ds_read_b128 v[68:71], v1 offset:9216\nv_smfmac_f32_16x16x64_bf16 v[48:51], v[80:83], a[104:111], v4\nv_smfmac_f32_16x16x64_bf16 v[52:55], v[80:83], a[112:119], v4\nv_smfmac_f32_16x16x64_bf16 v[56:59], v[80:83], a[120:127], v4\nv_smfmac_f32_16x16x64_bf16 v[60:63], v[80:83], a[128:135], v4\ns_waitcnt lgkmcnt(8)\nv_smfmac_f32_16x16x64_bf16 v[48:51], v[84:87], a[136:143], v4\ns_waitcnt lgkmcnt(5)\n" \
"ds_read_b128 v[80:83], v7 offset:8448\nv_smfmac_f32_16x16x64_bf16 v[52:55], v[84:87], v[72:79], v4\nds_read_b128 v[72:75], v1 offset:10240\nds_read_b128 v[76:79], v1 offset:11264\nv_smfmac_f32_16x16x64_bf16 v[56:59], v[84:87], a[144:151], v4\nv_smfmac_f32_16x16x64_bf16 v[60:63], v[84:87], a[152:159], v4\ns_waitcnt lgkmcnt(7)\nv_smfmac_f32_16x16x64_bf16 v[48:51], v[88:91], a[160:167], v4\n" \
"ds_read_b128 v[84:87], v7 offset:10496\nv_smfmac_f32_16x16x64_bf16 v[52:55], v[88:91], a[168:175], v4\ns_waitcnt lgkmcnt(6)\nv_smfmac_f32_16x16x64_bf16 v[48:51], v[92:95], a[184:191], v4\nv_smfmac_f32_16x16x64_bf16 v[52:55], v[92:95], a[192:199], v4\ns_waitcnt lgkmcnt(4)\nv_smfmac_f32_16x16x64_bf16 v[56:59], v[88:91], v[64:71], v4\nds_read_b128 v[64:67], v7 offset:12544\n" \
"v_add_f32 v48, v48, v50\nv_smfmac_f32_16x16x64_bf16 v[60:63], v[88:91], a[176:183], v4\nds_read_b128 v[68:71], v7 offset:14592\nv_add_f32 v52, v52, v54\nv_add_f32 v49, v49, v51\nv_smfmac_f32_16x16x64_bf16 v[56:59], v[92:95], a[200:207], v4\ns_waitcnt lgkmcnt(3)\nv_add_f32 v53, v53, v55\n" \
"v_cvt_pk_bf16_f32 v50, v48, v52\nv_smfmac_f32_16x16x64_bf16 v[60:63], v[92:95], v[72:79], v4\nv_cvt_pk_bf16_f32 v51, v49, v53\nds_write2_b32 v6, v50, v51 offset0:0 offset1:4\ns_nop 1\nv_add_f32 v56, v56, v58\nv_add_f32 v57, v57, v59\ns_nop 1\n" \
"v_add_f32 v60, v60, v62\nv_add_f32 v61, v61, v63\nv_cvt_pk_bf16_f32 v62, v56, v60\nv_cvt_pk_bf16_f32 v63, v57, v61\nds_write2_b32 v6, v62, v63 offset0:36 offset1:40\nv_mov_b32 v61, s54\nv_mfma_f32_16x16x32_bf16 v[52:55], v[32:35], v[16:19], 0\nv_mfma_f32_16x16x32_bf16 v[52:55], v[36:39], v[20:23], v[52:55]\n" \
"v_mfma_f32_16x16x32_bf16 v[52:55], v[40:43], v[24:27], v[52:55]\nv_mfma_f32_16x16x32_bf16 v[52:55], v[44:47], v[28:31], v[52:55]\ns_waitcnt lgkmcnt(0)\ns_barrier\nds_read_b128 v[32:35], v9 offset:0\nds_read_b128 v[36:39], v9 offset:576\nds_read_b128 v[40:43], v9 offset:1152\nds_read_b128 v[44:47], v9 offset:1728\n" \
"ds_read_b32 v60, v0 offset:53760\nv_mfma_f32_16x16x32_bf16 v[56:59], v[80:83], v[16:19], v[12:15]\nv_mfma_f32_16x16x32_bf16 v[56:59], v[84:87], v[20:23], v[56:59]\nv_mfma_f32_16x16x32_bf16 v[56:59], v[64:67], v[24:27], v[56:59]\nv_mfma_f32_16x16x32_bf16 v[56:59], v[68:71], v[28:31], v[56:59]\ns_waitcnt lgkmcnt(4)\nds_read_b128 v[80:83], v2 offset:0\nds_read_b128 v[84:87], v2 offset:256\n" \
"ds_read_b128 v[88:91], v2 offset:2048\nv_mfma_f32_16x16x32_bf16 v[48:51], v[32:35], v[16:19], 0\ns_waitcnt lgkmcnt(6)\nv_mfma_f32_16x16x32_bf16 v[48:51], v[36:39], v[20:23], v[48:51]\ns_waitcnt lgkmcnt(5)\nv_mfma_f32_16x16x32_bf16 v[48:51], v[40:43], v[24:27], v[48:51]\ns_waitcnt lgkmcnt(4)\nv_mfma_f32_16x16x32_bf16 v[48:51], v[44:47], v[28:31], v[48:51]\n" \
"s_waitcnt lgkmcnt(2)\nv_mfma_f32_16x16x32_bf16 v[16:19], a[0:3], v[80:83], 0\nds_read_b128 v[92:95], v2 offset:2304\nv_max3_f32 v62, v52, v53, v54\nv_max3_f32 v62, v62, v56, v57\nv_mfma_f32_16x16x32_bf16 v[20:23], a[8:11], v[80:83], 0\nds_read_b128 v[72:75], v1 offset:0\nv_max3_f32 v62, v62, v58, v59\n" \
"v_max_f32 v62, v62, v55\nv_mfma_f32_16x16x32_bf16 v[24:27], a[16:19], v[80:83], 0\nv_max3_f32 v63, v62, v48, v49\nv_max3_f32 v62, v62, v48, v49\nv_mfma_f32_16x16x32_bf16 v[28:31], a[24:27], v[80:83], 0\ns_waitcnt lgkmcnt(3)\nv_permlane16_swap_b32 v62, v63\nv_max_f32 v50, v62, v63\n" \
"v_mfma_f32_16x16x32_bf16 v[32:35], a[0:3], v[84:87], 0\nds_read_b128 v[80:83], v2 offset:4096\nv_max_f32 v62, v62, v63\nv_mfma_f32_16x16x32_bf16 v[36:39], a[8:11], v[84:87], 0\ns_nop 0\nv_permlane32_swap_b32 v62, v50\nv_max_f32 v62, v62, v50\nv_mfma_f32_16x16x32_bf16 v[40:43], a[16:19], v[84:87], 0\n" \
"v_mul_f32 v62, s59, v62\nv_fma_f32 v52, v52, s58, v62\nv_fma_f32 v53, v53, s58, v62\nv_mfma_f32_16x16x32_bf16 v[44:47], a[24:27], v[84:87], 0\ns_waitcnt lgkmcnt(3)\nv_exp_f32 v52, v52\nv_fma_f32 v54, v54, s58, v62\nv_mfma_f32_16x16x32_bf16 v[16:19], a[4:7], v[88:91], v[16:19]\n" \
"ds_read_b128 v[84:87], v2 offset:4352\nv_exp_f32 v53, v53\nv_mfma_f32_16x16x32_bf16 v[20:23], a[12:15], v[88:91], v[20:23]\nds_read_b128 v[76:79], v1 offset:1024\nv_fma_f32 v55, v55, s58, v62\nv_add_f32 v63, v52, v53\nv_mfma_f32_16x16x32_bf16 v[24:27], a[20:23], v[88:91], v[24:27]\nv_exp_f32 v54, v54\n" \
"v_fma_f32 v56, v56, s58, v62\nv_mfma_f32_16x16x32_bf16 v[28:31], a[28:31], v[88:91], v[28:31]\ns_waitcnt lgkmcnt(4)\nv_exp_f32 v55, v55\nv_add_f32 v63, v63, v54\nv_mfma_f32_16x16x32_bf16 v[32:35], a[4:7], v[92:95], v[32:35]\nds_read_b128 v[88:91], v2 offset:6144\nv_fma_f32 v57, v57, s58, v62\n" \
"v_add_f32 v63, v63, v55\nv_mfma_f32_16x16x32_bf16 v[36:39], a[12:15], v[92:95], v[36:39]\nv_exp_f32 v56, v56\nv_fma_f32 v58, v58, s58, v62\nv_mfma_f32_16x16x32_bf16 v[40:43], a[20:23], v[92:95], v[40:43]\nv_exp_f32 v57, v57\nv_add_f32 v63, v63, v56\nv_mfma_f32_16x16x32_bf16 v[44:47], a[28:31], v[92:95], v[44:47]\n" \
"s_waitcnt lgkmcnt(3)\nv_fma_f32 v59, v59, s58, v62\nv_exp_f32 v58, v58\nv_mfma_f32_16x16x32_bf16 v[16:19], a[32:35], v[80:83], v[16:19]\nds_read_b128 v[92:95], v2 offset:6400\nv_add_f32 v63, v63, v57\nv_fma_f32 v48, v48, s58, v62\nv_mfma_f32_16x16x32_bf16 v[20:23], v[72:75], v[80:83], v[20:23]\n" \
"ds_read_b128 v[64:67], v1 offset:2048\nv_exp_f32 v59, v59\nv_mfma_f32_16x16x32_bf16 v[24:27], a[40:43], v[80:83], v[24:27]\nv_add_f32 v63, v63, v58\nv_fma_f32 v49, v49, s58, v62\nv_add_f32 v63, v63, v59\nv_mfma_f32_16x16x32_bf16 v[28:31], a[48:51], v[80:83], v[28:31]\ns_waitcnt lgkmcnt(4)\n" \
"v_exp_f32 v48, v48\nv_mfma_f32_16x16x32_bf16 v[32:35], a[32:35], v[84:87], v[32:35]\nds_read_b128 v[80:83], v2 offset:8192\nv_exp_f32 v49, v49\nv_mfma_f32_16x16x32_bf16 v[36:39], v[72:75], v[84:87], v[36:39]\nv_add_f32 v63, v63, v48\nv_add_f32 v62, v63, v49\nv_add_f32 v50, v63, v49\n" \
"v_mfma_f32_16x16x32_bf16 v[40:43], a[40:43], v[84:87], v[40:43]\ns_nop 0\nv_permlane16_swap_b32 v62, v50\nv_add_f32 v63, v62, v50\nv_mfma_f32_16x16x32_bf16 v[44:47], a[48:51], v[84:87], v[44:47]\ns_waitcnt lgkmcnt(3)\nv_add_f32 v50, v62, v50\nv_mfma_f32_16x16x32_bf16 v[16:19], a[36:39], v[88:91], v[16:19]\n" \
"ds_read_b128 v[84:87], v2 offset:8448\nv_permlane32_swap_b32 v63, v50\nv_mfma_f32_16x16x32_bf16 v[20:23], v[76:79], v[88:91], v[20:23]\nds_read_b128 v[68:71], v1 offset:3072\nv_add_f32 v63, v63, v50\nv_mfma_f32_16x16x32_bf16 v[24:27], a[44:47], v[88:91], v[24:27]\nv_rcp_f32 v63, v63\nv_mfma_f32_16x16x32_bf16 v[28:31], a[52:55], v[88:91], v[28:31]\n" \
"s_waitcnt lgkmcnt(4)\nv_mul_f32 v52, v52, v63\nv_mul_f32 v53, v53, v63\nv_mul_f32 v54, v54, v63\nv_mfma_f32_16x16x32_bf16 v[32:35], a[36:39], v[92:95], v[32:35]\nds_read_b128 v[88:91], v2 offset:10240\nv_mul_f32 v55, v55, v63\nv_mul_f32 v56, v56, v63\n" \
"v_mfma_f32_16x16x32_bf16 v[36:39], v[76:79], v[92:95], v[36:39]\nv_mul_f32 v57, v57, v63\nv_mul_f32 v58, v58, v63\nv_mul_f32 v59, v59, v63\nv_mfma_f32_16x16x32_bf16 v[40:43], a[44:47], v[92:95], v[40:43]\nv_mul_f32 v48, v48, v63\nv_mul_f32 v49, v49, v63\nv_cvt_pk_bf16_f32 v52, v52, v53\n" \
"v_mfma_f32_16x16x32_bf16 v[44:47], a[52:55], v[92:95], v[44:47]\ns_waitcnt lgkmcnt(3)\nv_cvt_pk_bf16_f32 v53, v54, v55\nv_cvt_pk_bf16_f32 v48, v48, v49\nv_cvt_pk_bf16_f32 v54, v56, v57\nv_mfma_f32_16x16x32_bf16 v[16:19], a[56:59], v[80:83], v[16:19]\nds_read_b128 v[92:95], v2 offset:10496\nv_cvt_pk_bf16_f32 v55, v58, v59\n" \
"v_bfi_b32 v52, s61, v52, v61\nv_mfma_f32_16x16x32_bf16 v[20:23], a[64:67], v[80:83], v[20:23]\nds_read_b128 v[72:75], v1 offset:4096\nv_bfi_b32 v53, s61, v53, v61\nv_bfi_b32 v54, s61, v54, v61\nv_mfma_f32_16x16x32_bf16 v[24:27], v[64:67], v[80:83], v[24:27]\nv_bfi_b32 v55, s61, v55, v61\nv_bfi_b32 v48, s61, v48, v61\n" \
"v_mfma_f32_16x16x32_bf16 v[28:31], a[72:75], v[80:83], v[28:31]\ns_waitcnt lgkmcnt(4)\nbuffer_store_dwordx4 v[52:55], v10, s[36:39], s55 offen sc1\nv_mov_b32 v49, v48\nv_mfma_f32_16x16x32_bf16 v[32:35], a[56:59], v[84:87], v[32:35]\nds_read_b128 v[80:83], v2 offset:12288\nv_permlane16_swap_b32 v48, v49\nv_mfma_f32_16x16x32_bf16 v[36:39], a[64:67], v[84:87], v[36:39]\n" \
"v_mov_b32 v50, v48\nv_mov_b32 v51, v49\nv_mfma_f32_16x16x32_bf16 v[40:43], v[64:67], v[84:87], v[40:43]\nv_permlane32_swap_b32 v48, v50\nv_mfma_f32_16x16x32_bf16 v[44:47], a[72:75], v[84:87], v[44:47]\ns_waitcnt lgkmcnt(3)\nv_permlane32_swap_b32 v49, v51\nv_mfma_f32_16x16x32_bf16 v[16:19], a[60:63], v[88:91], v[16:19]\n" \
"ds_read_b128 v[84:87], v2 offset:12544\nv_mfma_f32_16x16x32_bf16 v[20:23], a[68:71], v[88:91], v[20:23]\nds_read_b128 v[76:79], v1 offset:5120\nv_mfma_f32_16x16x32_bf16 v[24:27], v[68:71], v[88:91], v[24:27]\nbuffer_store_dwordx4 v[48:51], v11, s[36:39], s55 offen sc1\nv_cmp_ne_u32 vcc, 0, v60\nv_mfma_f32_16x16x32_bf16 v[28:31], a[76:79], v[88:91], v[28:31]\ns_waitcnt lgkmcnt(4)\n" \
"v_mfma_f32_16x16x32_bf16 v[32:35], a[60:63], v[92:95], v[32:35]\nds_read_b128 v[88:91], v2 offset:14336\nv_mfma_f32_16x16x32_bf16 v[36:39], a[68:71], v[92:95], v[36:39]\nv_mfma_f32_16x16x32_bf16 v[40:43], v[68:71], v[92:95], v[40:43]\nv_mfma_f32_16x16x32_bf16 v[44:47], a[76:79], v[92:95], v[44:47]\ns_cbranch_vccnz .Lqk_pj%=\ns_cmp_lt_i32 s40, 63\ns_waitcnt lgkmcnt(0)\n" \
"s_cbranch_scc1 .Lqk_pwait%=\ns_branch .Lqk_end%=\n.Lqk_re2%=:\nv_max_u32 v17, v80, v82\nv_cmp_eq_u32 vcc, -1, v17\ns_cbranch_vccz .Lqk_sk2417%=\ns_mov_b64 exec, vcc\nbuffer_load_dwordx4 v[80:83], v3, s[36:39], s42 offen offset:0 sc1\n" \
"s_mov_b64 exec, -1\n.Lqk_sk2417%=:\nv_max_u32 v17, v84, v86\nv_cmp_eq_u32 vcc, -1, v17\ns_cbranch_vccz .Lqk_sk2424%=\ns_mov_b64 exec, vcc\nbuffer_load_dwordx4 v[84:87], v3, s[36:39], s42 offen offset:1024 sc1\ns_mov_b64 exec, -1\n" \
".Lqk_sk2424%=:\nv_max_u32 v17, v88, v90\nv_cmp_eq_u32 vcc, -1, v17\ns_cbranch_vccz .Lqk_sk2431%=\ns_mov_b64 exec, vcc\nbuffer_load_dwordx4 v[88:91], v3, s[36:39], s42 offen offset:2048 sc1\ns_mov_b64 exec, -1\n.Lqk_sk2431%=:\n" \
"v_max_u32 v17, v92, v94\nv_cmp_eq_u32 vcc, -1, v17\ns_cbranch_vccz .Lqk_sk2438%=\ns_mov_b64 exec, vcc\nbuffer_load_dwordx4 v[92:95], v3, s[36:39], s42 offen offset:3072 sc1\ns_mov_b64 exec, -1\n.Lqk_sk2438%=:\nv_mov_b64 v[48:49], 0\n" \
"v_mov_b64 v[50:51], 0\nv_mov_b64 v[52:53], 0\nv_mov_b64 v[54:55], 0\nv_mov_b64 v[56:57], 0\nv_mov_b64 v[58:59], 0\nv_mov_b64 v[60:61], 0\nv_mov_b64 v[62:63], 0\ns_add_u32 s43, s43, 1\n" \
"s_cmp_lt_u32 s43, 262144\ns_cbranch_scc1 .Lqk_poll2%=\ns_branch .Lqk_abort%=\n.Lqk_c3%=:\ns_waitcnt lgkmcnt(3)\nv_mfma_f32_16x16x32_bf16 v[32:35], v[232:235], v[56:59], v[32:35]\ns_add_u32 s44, s40, 1\nv_mov_b64 v[52:53], 0\n" \
"v_mov_b64 v[54:55], 0\nv_mfma_f32_16x16x32_bf16 v[36:39], v[240:243], v[56:59], v[36:39]\ns_lshl_b32 s44, s44, 17\ns_waitcnt lgkmcnt(2)\nds_read_b128 v[64:67], v1 offset:18432\nv_mfma_f32_16x16x32_bf16 v[32:35], v[236:239], v[60:63], v[32:35]\ns_add_u32 s44, s44, %[xn]\ns_lshr_b32 s45, s44, 8\n" \
"ds_read_b128 v[68:71], v1 offset:19456\nv_mfma_f32_16x16x32_bf16 v[36:39], v[244:247], v[60:63], v[36:39]\ns_and_b32 s45, s45, 0x2000\ns_xor_b32 s41, s44, s45\nv_mfma_f32_16x16x32_bf16 v[40:43], v[248:251], v[56:59], v[40:43]\nbuffer_load_dword v16, v12, s[36:39], s56 offen sc1\nv_mfma_f32_16x16x32_bf16 v[44:47], v[72:75], v[56:59], v[44:47]\nv_cvt_pk_bf16_f32 v36, v32, v36\n" \
"v_cvt_pk_bf16_f32 v37, v33, v37\nv_mfma_f32_16x16x32_bf16 v[40:43], v[252:255], v[60:63], v[40:43]\nv_cvt_pk_bf16_f32 v38, v34, v38\nv_cvt_pk_bf16_f32 v39, v35, v39\nv_mfma_f32_16x16x32_bf16 v[44:47], v[76:79], v[60:63], v[44:47]\nds_read_b128 v[72:75], v1 offset:20480\nds_read_b128 v[76:79], v1 offset:21504\nv_mov_b64 v[56:57], 0\n" \
"v_mov_b64 v[58:59], 0\nv_mov_b64 v[60:61], 0\nv_mov_b64 v[62:63], 0\n.Lqk_poll3%=:\ns_waitcnt vmcnt(1)\nv_smfmac_f32_16x16x64_bf16 v[48:51], v[80:83], v[152:159], v4\nds_write_b128 v0, v[80:83] offset:28672\nv_max3_u32 v17, v80, v82, v84\n" \
"v_smfmac_f32_16x16x64_bf16 v[52:55], v[80:83], v[160:167], v4\nv_max3_u32 v17, v17, v86, v88\nv_smfmac_f32_16x16x64_bf16 v[56:59], v[80:83], v[168:175], v4\nds_write_b128 v0, v[84:87] offset:29696\nv_max3_u32 v17, v17, v90, v92\nv_max_u32 v17, v17, v94\nv_smfmac_f32_16x16x64_bf16 v[60:63], v[80:83], v[176:183], v4\nv_cmp_eq_u32 vcc, -1, v17\n" \
"v_smfmac_f32_16x16x64_bf16 v[48:51], v[84:87], v[184:191], v4\nds_write_b128 v0, v[88:91] offset:30720\ns_cbranch_vccnz .Lqk_re3%=\ns_waitcnt lgkmcnt(5)\nv_smfmac_f32_16x16x64_bf16 v[52:55], v[84:87], v[64:71], v4\nv_smfmac_f32_16x16x64_bf16 v[56:59], v[84:87], v[192:199], v4\nds_write_b128 v0, v[92:95] offset:31744\nv_smfmac_f32_16x16x64_bf16 v[60:63], v[84:87], v[200:207], v4\n" \
"v_smfmac_f32_16x16x64_bf16 v[48:51], v[88:91], v[208:215], v4\nv_smfmac_f32_16x16x64_bf16 v[52:55], v[88:91], v[216:223], v4\ns_waitcnt lgkmcnt(0)\n.Lqk_jn3%=:\ns_barrier\nds_read_b128 v[80:83], v0 offset:16384\nds_read_b128 v[64:67], v1 offset:22528\nds_read_b128 v[68:71], v1 offset:23552\n" \
"v_smfmac_f32_16x16x64_bf16 v[56:59], v[88:91], v[72:79], v4\ns_waitcnt vmcnt(0)\nds_read_b128 v[72:75], v1 offset:0\nds_read_b128 v[76:79], v1 offset:1024\nv_cmp_ge_i32 vcc, s40, v16\nv_smfmac_f32_16x16x64_bf16 v[60:63], v[88:91], v[224:231], v4\ns_cmp_eq_u64 vcc, 0\ns_cselect_b32 s61, s41, -2147483648\n" \
"s_cselect_b32 s44, 1, 0\nds_read_b128 v[84:87], v0 offset:17408\nds_read_b128 v[88:91], v0 offset:18432\nv_smfmac_f32_16x16x64_bf16 v[48:51], v[92:95], v[232:239], v4\ns_add_u32 m0, %[l0], 0\ns_add_u32 s60, s61, 0\nds_write_b128 v5, v[36:39] offset:256\nbuffer_load_dwordx4 v0, s[36:39], s60 offen offset:0 sc1 lds\n" \
"v_cvt_pk_bf16_f32 v44, v40, v44\nv_cvt_pk_bf16_f32 v45, v41, v45\nv_cvt_pk_bf16_f32 v46, v42, v46\nv_cvt_pk_bf16_f32 v47, v43, v47\nv_smfmac_f32_16x16x64_bf16 v[52:55], v[92:95], v[240:247], v4\nv_mov_b32 v16, s44\nv_smfmac_f32_16x16x64_bf16 v[56:59], v[92:95], v[248:255], v4\ns_waitcnt lgkmcnt(5)\n" \
"buffer_load_dwordx4 v0, s[36:39], s60 offen offset:1024 sc1 lds\nv_smfmac_f32_16x16x64_bf16 v[60:63], v[92:95], v[64:71], v4\nds_read_b128 v[92:95], v0 offset:19456\nds_read_b128 v[64:67], v1 offset:2048\nds_read_b128 v[68:71], v1 offset:3072\nds_write_b32 v0, v16 offset:53760\nv_smfmac_f32_16x16x64_bf16 v[48:51], v[80:83], a[0:7], v4\nds_write_b128 v5, v[44:47] offset:768\n" \
"buffer_load_dwordx4 v0, s[36:39], s60 offen offset:2048 sc1 lds\nv_smfmac_f32_16x16x64_bf16 v[52:55], v[80:83], a[8:15], v4\nv_smfmac_f32_16x16x64_bf16 v[56:59], v[80:83], a[16:23], v4\nbuffer_load_dwordx4 v0, s[36:39], s60 offen offset:3072 sc1 lds\nv_smfmac_f32_16x16x64_bf16 v[60:63], v[80:83], a[24:31], v4\ns_waitcnt lgkmcnt(7)\ns_add_u32 m0, %[l0], 4096\nds_read_b128 v[80:83], v0 offset:20480\n" \
"v_smfmac_f32_16x16x64_bf16 v[48:51], v[84:87], a[32:39], v4\ns_add_u32 s60, s61, 4096\nbuffer_load_dwordx4 v0, s[36:39], s60 offen offset:0 sc1 lds\nv_smfmac_f32_16x16x64_bf16 v[52:55], v[84:87], v[72:79], v4\nds_read_b128 v[72:75], v1 offset:4096\nds_read_b128 v[76:79], v1 offset:5120\nv_smfmac_f32_16x16x64_bf16 v[56:59], v[84:87], a[40:47], v4\nbuffer_load_dwordx4 v0, s[36:39], s60 offen offset:1024 sc1 lds\n" \
"v_smfmac_f32_16x16x64_bf16 v[60:63], v[84:87], a[48:55], v4\ns_waitcnt lgkmcnt(9)\nds_read_b128 v[84:87], v0 offset:21504\nv_smfmac_f32_16x16x64_bf16 v[48:51], v[88:91], a[56:63], v4\ns_waitcnt lgkmcnt(4)\ns_barrier\nbuffer_load_dwordx4 v0, s[36:39], s60 offen offset:2048 sc1 lds\nv_smfmac_f32_16x16x64_bf16 v[52:55], v[88:91], a[64:71], v4\n" \
"v_smfmac_f32_16x16x64_bf16 v[56:59], v[88:91], v[64:71], v4\nds_read_b128 v[64:67], v1 offset:6144\nds_read_b128 v[68:71], v1 offset:7168\nbuffer_load_dwordx4 v0, s[36:39], s60 offen offset:3072 sc1 lds\nv_smfmac_f32_16x16x64_bf16 v[60:63], v[88:91], a[72:79], v4\ns_add_u32 m0, %[l0], 8192\nds_read_b128 v[88:91], v0 offset:22528\nv_smfmac_f32_16x16x64_bf16 v[48:51], v[92:95], a[80:87], v4\n" \
"s_add_u32 s60, s61, 16384\nbuffer_load_dwordx4 v0, s[36:39], s60 offen offset:0 sc1 lds\nv_smfmac_f32_16x16x64_bf16 v[52:55], v[92:95], a[88:95], v4\nv_smfmac_f32_16x16x64_bf16 v[56:59], v[92:95], a[96:103], v4\ns_waitcnt lgkmcnt(4)\nbuffer_load_dwordx4 v0, s[36:39], s60 offen offset:1024 sc1 lds\nv_smfmac_f32_16x16x64_bf16 v[60:63], v[92:95], v[72:79], v4\nds_read_b128 v[92:95], v0 offset:23552\n" \
"ds_read_b128 v[72:75], v1 offset:8192\nds_read_b128 v[76:79], v1 offset:9216\nv_smfmac_f32_16x16x64_bf16 v[48:51], v[80:83], a[104:111], v4\nbuffer_load_dwordx4 v0, s[36:39], s60 offen offset:2048 sc1 lds\nv_smfmac_f32_16x16x64_bf16 v[52:55], v[80:83], a[112:119], v4\nv_smfmac_f32_16x16x64_bf16 v[56:59], v[80:83], a[120:127], v4\nbuffer_load_dwordx4 v0, s[36:39], s60 offen offset:3072 sc1 lds\nv_smfmac_f32_16x16x64_bf16 v[60:63], v[80:83], a[128:135], v4\n" \
"s_waitcnt lgkmcnt(6)\ns_add_u32 m0, %[l0], 12288\nds_read_b128 v[80:83], v0 offset:24576\nv_smfmac_f32_16x16x64_bf16 v[48:51], v[84:87], a[136:143], v4\ns_add_u32 s60, s61, 20480\ns_waitcnt lgkmcnt(5)\nbuffer_load_dwordx4 v0, s[36:39], s60 offen offset:0 sc1 lds\nv_smfmac_f32_16x16x64_bf16 v[52:55], v[84:87], v[64:71], v4\n" \
"ds_read_b128 v[64:67], v1 offset:10240\nds_read_b128 v[68:71], v1 offset:11264\nv_smfmac_f32_16x16x64_bf16 v[56:59], v[84:87], a[144:151], v4\nbuffer_load_dwordx4 v0, s[36:39], s60 offen offset:1024 sc1 lds\nv_smfmac_f32_16x16x64_bf16 v[60:63], v[84:87], a[152:159], v4\ns_waitcnt lgkmcnt(6)\nds_read_b128 v[84:87], v0 offset:25600\nv_smfmac_f32_16x16x64_bf16 v[48:51], v[88:91], a[160:167], v4\n" \
"buffer_load_dwordx4 v0, s[36:39], s60 offen offset:2048 sc1 lds\nv_smfmac_f32_16x16x64_bf16 v[52:55], v[88:91], a[168:175], v4\ns_waitcnt lgkmcnt(4)\nv_smfmac_f32_16x16x64_bf16 v[56:59], v[88:91], v[72:79], v4\nds_read_b128 v[72:75], v1 offset:12288\nds_read_b128 v[76:79], v1 offset:13312\nbuffer_load_dwordx4 v0, s[36:39], s60 offen offset:3072 sc1 lds\nv_smfmac_f32_16x16x64_bf16 v[60:63], v[88:91], a[176:183], v4\n" \
"ds_read_b128 v[88:91], v0 offset:26624\nv_smfmac_f32_16x16x64_bf16 v[48:51], v[92:95], a[184:191], v4\nv_smfmac_f32_16x16x64_bf16 v[52:55], v[92:95], a[192:199], v4\nv_smfmac_f32_16x16x64_bf16 v[56:59], v[92:95], a[200:207], v4\ns_waitcnt lgkmcnt(4)\nv_smfmac_f32_16x16x64_bf16 v[60:63], v[92:95], v[64:71], v4\nds_read_b128 v[92:95], v0 offset:27648\nds_read_b128 v[64:67], v1 offset:14336\n" \
"ds_read_b128 v[68:71], v1 offset:15360\nv_smfmac_f32_16x16x64_bf16 v[48:51], v[80:83], a[208:215], v4\nv_smfmac_f32_16x16x64_bf16 v[52:55], v[80:83], a[216:223], v4\nv_smfmac_f32_16x16x64_bf16 v[56:59], v[80:83], a[224:231], v4\nv_smfmac_f32_16x16x64_bf16 v[60:63], v[80:83], a[232:239], v4\ns_waitcnt lgkmcnt(6)\nv_smfmac_f32_16x16x64_bf16 v[48:51], v[84:87], a[240:247], v4\ns_waitcnt lgkmcnt(4)\n" \
"v_smfmac_f32_16x16x64_bf16 v[52:55], v[84:87], v[72:79], v4\nds_read_b128 v[72:75], v1 offset:16384\nds_read_b128 v[76:79], v1 offset:17408\nv_smfmac_f32_16x16x64_bf16 v[56:59], v[84:87], a[248:255], v4\nv_smfmac_f32_16x16x64_bf16 v[60:63], v[84:87], v[96:103], v4\ns_waitcnt lgkmcnt(5)\nv_smfmac_f32_16x16x64_bf16 v[48:51], v[88:91], v[104:111], v4\nv_smfmac_f32_16x16x64_bf16 v[52:55], v[88:91], v[112:119], v4\n" \
"s_waitcnt lgkmcnt(4)\nv_smfmac_f32_16x16x64_bf16 v[48:51], v[92:95], v[128:135], v4\nv_smfmac_f32_16x16x64_bf16 v[52:55], v[92:95], v[136:143], v4\ns_waitcnt lgkmcnt(2)\nv_smfmac_f32_16x16x64_bf16 v[56:59], v[88:91], v[64:71], v4\nv_add_f32 v48, v48, v50\nv_smfmac_f32_16x16x64_bf16 v[60:63], v[88:91], v[120:127], v4\nv_add_f32 v52, v52, v54\n" \
"v_add_f32 v49, v49, v51\nv_smfmac_f32_16x16x64_bf16 v[56:59], v[92:95], v[144:151], v4\ns_waitcnt lgkmcnt(0)\nv_add_f32 v53, v53, v55\nv_cvt_pk_bf16_f32 v50, v48, v52\nv_smfmac_f32_16x16x64_bf16 v[60:63], v[92:95], v[72:79], v4\nv_cvt_pk_bf16_f32 v51, v49, v53\nds_write2_b32 v6, v50, v51 offset0:0 offset1:4\n" \
"s_nop 1\nv_add_f32 v56, v56, v58\nv_add_f32 v57, v57, v59\ns_nop 1\nv_add_f32 v60, v60, v62\nv_add_f32 v61, v61, v63\nv_cvt_pk_bf16_f32 v62, v56, v60\nv_cvt_pk_bf16_f32 v63, v57, v61\n" \
"ds_write2_b32 v6, v62, v63 offset0:36 offset1:40\ns_waitcnt vmcnt(0)\ns_waitcnt lgkmcnt(0)\ns_barrier\ns_cbranch_vccnz .Lqk_slow%=\nds_read_b128 v[80:83], v2 offset:0\nds_read_b128 v[84:87], v2 offset:256\nds_read_b128 v[88:91], v2 offset:2048\n" \
"s_add_u32 m0, %[l0], 16384\ns_add_u32 s60, s41, 32768\nbuffer_load_dwordx4 v0, s[36:39], s60 offen offset:0 sc1 lds\nbuffer_load_dwordx4 v0, s[36:39], s60 offen offset:1024 sc1 lds\nbuffer_load_dwordx4 v0, s[36:39], s60 offen offset:2048 sc1 lds\nbuffer_load_dwordx4 v0, s[36:39], s60 offen offset:3072 sc1 lds\ns_add_u32 m0, %[l0], 20480\ns_add_u32 s60, s41, 36864\n" \
"buffer_load_dwordx4 v0, s[36:39], s60 offen offset:0 sc1 lds\nbuffer_load_dwordx4 v0, s[36:39], s60 offen offset:1024 sc1 lds\nbuffer_load_dwordx4 v0, s[36:39], s60 offen offset:2048 sc1 lds\nbuffer_load_dwordx4 v0, s[36:39], s60 offen offset:3072 sc1 lds\ns_add_u32 m0, %[l0], 24576\ns_add_u32 s60, s41, 49152\nbuffer_load_dwordx4 v0, s[36:39], s60 offen offset:0 sc1 lds\nbuffer_load_dwordx4 v0, s[36:39], s60 offen offset:1024 sc1 lds\n" \
"buffer_load_dwordx4 v0, s[36:39], s60 offen offset:2048 sc1 lds\nbuffer_load_dwordx4 v0, s[36:39], s60 offen offset:3072 sc1 lds\ns_add_u32 m0, %[l0], 28672\ns_add_u32 s60, s41, 53248\nbuffer_load_dwordx4 v0, s[36:39], s60 offen offset:0 sc1 lds\nbuffer_load_dwordx4 v0, s[36:39], s60 offen offset:1024 sc1 lds\nbuffer_load_dwordx4 v0, s[36:39], s60 offen offset:2048 sc1 lds\nbuffer_load_dwordx4 v0, s[36:39], s60 offen offset:3072 sc1 lds\n" \
"s_waitcnt lgkmcnt(2)\nv_mfma_f32_16x16x32_bf16 v[16:19], a[0:3], v[80:83], 0\nds_read_b128 v[92:95], v2 offset:2304\nv_mfma_f32_16x16x32_bf16 v[20:23], a[8:11], v[80:83], 0\nds_read_b128 v[72:75], v1 offset:0\nv_mfma_f32_16x16x32_bf16 v[24:27], a[16:19], v[80:83], 0\nv_mfma_f32_16x16x32_bf16 v[28:31], a[24:27], v[80:83], 0\ns_waitcnt lgkmcnt(3)\n" \
"v_mfma_f32_16x16x32_bf16 v[32:35], a[0:3], v[84:87], 0\nds_read_b128 v[80:83], v2 offset:4096\nv_mfma_f32_16x16x32_bf16 v[36:39], a[8:11], v[84:87], 0\nv_mfma_f32_16x16x32_bf16 v[40:43], a[16:19], v[84:87], 0\nv_mfma_f32_16x16x32_bf16 v[44:47], a[24:27], v[84:87], 0\ns_waitcnt lgkmcnt(3)\nv_mfma_f32_16x16x32_bf16 v[16:19], a[4:7], v[88:91], v[16:19]\nds_read_b128 v[84:87], v2 offset:4352\n" \
"v_mfma_f32_16x16x32_bf16 v[20:23], a[12:15], v[88:91], v[20:23]\nds_read_b128 v[76:79], v1 offset:1024\nv_mfma_f32_16x16x32_bf16 v[24:27], a[20:23], v[88:91], v[24:27]\nv_mfma_f32_16x16x32_bf16 v[28:31], a[28:31], v[88:91], v[28:31]\ns_waitcnt lgkmcnt(4)\nv_mfma_f32_16x16x32_bf16 v[32:35], a[4:7], v[92:95], v[32:35]\nds_read_b128 v[88:91], v2 offset:6144\nv_mfma_f32_16x16x32_bf16 v[36:39], a[12:15], v[92:95], v[36:39]\n" \
"v_mfma_f32_16x16x32_bf16 v[40:43], a[20:23], v[92:95], v[40:43]\nv_mfma_f32_16x16x32_bf16 v[44:47], a[28:31], v[92:95], v[44:47]\ns_waitcnt lgkmcnt(3)\nv_mfma_f32_16x16x32_bf16 v[16:19], a[32:35], v[80:83], v[16:19]\nds_read_b128 v[92:95], v2 offset:6400\nv_mfma_f32_16x16x32_bf16 v[20:23], v[72:75], v[80:83], v[20:23]\nds_read_b128 v[64:67], v1 offset:2048\nv_mfma_f32_16x16x32_bf16 v[24:27], a[40:43], v[80:83], v[24:27]\n" \
"v_mfma_f32_16x16x32_bf16 v[28:31], a[48:51], v[80:83], v[28:31]\ns_waitcnt lgkmcnt(4)\nv_mfma_f32_16x16x32_bf16 v[32:35], a[32:35], v[84:87], v[32:35]\nds_read_b128 v[80:83], v2 offset:8192\nv_mfma_f32_16x16x32_bf16 v[36:39], v[72:75], v[84:87], v[36:39]\nv_mfma_f32_16x16x32_bf16 v[40:43], a[40:43], v[84:87], v[40:43]\nv_mfma_f32_16x16x32_bf16 v[44:47], a[48:51], v[84:87], v[44:47]\ns_waitcnt lgkmcnt(3)\n" \
"v_mfma_f32_16x16x32_bf16 v[16:19], a[36:39], v[88:91], v[16:19]\nds_read_b128 v[84:87], v2 offset:8448\nv_mfma_f32_16x16x32_bf16 v[20:23], v[76:79], v[88:91], v[20:23]\nds_read_b128 v[68:71], v1 offset:3072\nv_mfma_f32_16x16x32_bf16 v[24:27], a[44:47], v[88:91], v[24:27]\nv_mfma_f32_16x16x32_bf16 v[28:31], a[52:55], v[88:91], v[28:31]\ns_waitcnt lgkmcnt(4)\nv_mfma_f32_16x16x32_bf16 v[32:35], a[36:39], v[92:95], v[32:35]\n" \
"ds_read_b128 v[88:91], v2 offset:10240\nv_mfma_f32_16x16x32_bf16 v[36:39], v[76:79], v[92:95], v[36:39]\nv_mfma_f32_16x16x32_bf16 v[40:43], a[44:47], v[92:95], v[40:43]\nv_mfma_f32_16x16x32_bf16 v[44:47], a[52:55], v[92:95], v[44:47]\ns_waitcnt lgkmcnt(3)\nv_mfma_f32_16x16x32_bf16 v[16:19], a[56:59], v[80:83], v[16:19]\nds_read_b128 v[92:95], v2 offset:10496\nv_mfma_f32_16x16x32_bf16 v[20:23], a[64:67], v[80:83], v[20:23]\n" \
"ds_read_b128 v[72:75], v1 offset:4096\nv_mfma_f32_16x16x32_bf16 v[24:27], v[64:67], v[80:83], v[24:27]\nv_mfma_f32_16x16x32_bf16 v[28:31], a[72:75], v[80:83], v[28:31]\ns_waitcnt lgkmcnt(4)\nv_mfma_f32_16x16x32_bf16 v[32:35], a[56:59], v[84:87], v[32:35]\nds_read_b128 v[80:83], v2 offset:12288\nv_mfma_f32_16x16x32_bf16 v[36:39], a[64:67], v[84:87], v[36:39]\nv_mfma_f32_16x16x32_bf16 v[40:43], v[64:67], v[84:87], v[40:43]\n" \
"v_mfma_f32_16x16x32_bf16 v[44:47], a[72:75], v[84:87], v[44:47]\ns_waitcnt lgkmcnt(3)\nv_mfma_f32_16x16x32_bf16 v[16:19], a[60:63], v[88:91], v[16:19]\nds_read_b128 v[84:87], v2 offset:12544\nv_mfma_f32_16x16x32_bf16 v[20:23], a[68:71], v[88:91], v[20:23]\nds_read_b128 v[76:79], v1 offset:5120\nv_mfma_f32_16x16x32_bf16 v[24:27], v[68:71], v[88:91], v[24:27]\nv_mfma_f32_16x16x32_bf16 v[28:31], a[76:79], v[88:91], v[28:31]\n" \
"s_waitcnt lgkmcnt(4)\nv_mfma_f32_16x16x32_bf16 v[32:35], a[60:63], v[92:95], v[32:35]\nds_read_b128 v[88:91], v2 offset:14336\nv_mfma_f32_16x16x32_bf16 v[36:39], a[68:71], v[92:95], v[36:39]\nv_mfma_f32_16x16x32_bf16 v[40:43], v[68:71], v[92:95], v[40:43]\nv_mfma_f32_16x16x32_bf16 v[44:47], a[76:79], v[92:95], v[44:47]\ns_waitcnt vmcnt(0)\ns_branch .Lqk_pj%=\n" \
".Lqk_slow%=:\ns_cmp_ge_i32 s40, 63\ns_cbranch_scc1 .Lqk_end%=\n.Lqk_fetch0%=:\nbuffer_load_dword v16, v12, s[36:39], s56 offen sc1\ns_waitcnt vmcnt(0)\nv_cmp_ge_i32 vcc, s40, v16\ns_cbranch_vccz .Lqk_fok%=\n" \
"s_add_u32 s43, s43, 1\ns_cmp_lt_u32 s43, 262144\ns_cbranch_scc1 .Lqk_fetch0%=\ns_branch .Lqk_abort%=\n.Lqk_fok%=:\ns_add_u32 s44, s40, 1\ns_lshl_b32 s44, s44, 17\ns_add_u32 s44, s44, %[xn]\n" \
"s_lshr_b32 s45, s44, 8\ns_and_b32 s45, s45, 0x2000\ns_xor_b32 s41, s44, s45\ns_add_u32 m0, %[l0], 0\ns_add_u32 s60, s41, 0\nbuffer_load_dwordx4 v0, s[36:39], s60 offen offset:0 sc1 lds\nbuffer_load_dwordx4 v0, s[36:39], s60 offen offset:1024 sc1 lds\nbuffer_load_dwordx4 v0, s[36:39], s60 offen offset:2048 sc1 lds\n" \
"buffer_load_dwordx4 v0, s[36:39], s60 offen offset:3072 sc1 lds\ns_add_u32 m0, %[l0], 4096\ns_add_u32 s60, s41, 4096\nbuffer_load_dwordx4 v0, s[36:39], s60 offen offset:0 sc1 lds\nbuffer_load_dwordx4 v0, s[36:39], s60 offen offset:1024 sc1 lds\nbuffer_load_dwordx4 v0, s[36:39], s60 offen offset:2048 sc1 lds\nbuffer_load_dwordx4 v0, s[36:39], s60 offen offset:3072 sc1 lds\ns_add_u32 m0, %[l0], 8192\n" \
"s_add_u32 s60, s41, 16384\nbuffer_load_dwordx4 v0, s[36:39], s60 offen offset:0 sc1 lds\nbuffer_load_dwordx4 v0, s[36:39], s60 offen offset:1024 sc1 lds\nbuffer_load_dwordx4 v0, s[36:39], s60 offen offset:2048 sc1 lds\nbuffer_load_dwordx4 v0, s[36:39], s60 offen offset:3072 sc1 lds\ns_add_u32 m0, %[l0], 12288\ns_add_u32 s60, s41, 20480\nbuffer_load_dwordx4 v0, s[36:39], s60 offen offset:0 sc1 lds\n" \
"buffer_load_dwordx4 v0, s[36:39], s60 offen offset:1024 sc1 lds\nbuffer_load_dwordx4 v0, s[36:39], s60 offen offset:2048 sc1 lds\nbuffer_load_dwordx4 v0, s[36:39], s60 offen offset:3072 sc1 lds\ns_add_u32 m0, %[l0], 16384\ns_add_u32 s60, s41, 32768\nbuffer_load_dwordx4 v0, s[36:39], s60 offen offset:0 sc1 lds\nbuffer_load_dwordx4 v0, s[36:39], s60 offen offset:1024 sc1 lds\nbuffer_load_dwordx4 v0, s[36:39], s60 offen offset:2048 sc1 lds\n" \
"buffer_load_dwordx4 v0, s[36:39], s60 offen offset:3072 sc1 lds\ns_add_u32 m0, %[l0], 20480\ns_add_u32 s60, s41, 36864\nbuffer_load_dwordx4 v0, s[36:39], s60 offen offset:0 sc1 lds\nbuffer_load_dwordx4 v0, s[36:39], s60 offen offset:1024 sc1 lds\nbuffer_load_dwordx4 v0, s[36:39], s60 offen offset:2048 sc1 lds\nbuffer_load_dwordx4 v0, s[36:39], s60 offen offset:3072 sc1 lds\ns_add_u32 m0, %[l0], 24576\n" \
"s_add_u32 s60, s41, 49152\nbuffer_load_dwordx4 v0, s[36:39], s60 offen offset:0 sc1 lds\nbuffer_load_dwordx4 v0, s[36:39], s60 offen offset:1024 sc1 lds\nbuffer_load_dwordx4 v0, s[36:39], s60 offen offset:2048 sc1 lds\nbuffer_load_dwordx4 v0, s[36:39], s60 offen offset:3072 sc1 lds\ns_add_u32 m0, %[l0], 28672\ns_add_u32 s60, s41, 53248\nbuffer_load_dwordx4 v0, s[36:39], s60 offen offset:0 sc1 lds\n" \
"buffer_load_dwordx4 v0, s[36:39], s60 offen offset:1024 sc1 lds\nbuffer_load_dwordx4 v0, s[36:39], s60 offen offset:2048 sc1 lds\nbuffer_load_dwordx4 v0, s[36:39], s60 offen offset:3072 sc1 lds\ns_waitcnt vmcnt(0)\ns_branch .Lqk_pwait%=\n.Lqk_re3%=:\nv_max_u32 v17, v80, v82\nv_cmp_eq_u32 vcc, -1, v17\n" \
"s_cbranch_vccz .Lqk_sk2850%=\ns_mov_b64 exec, vcc\nbuffer_load_dwordx4 v[80:83], v3, s[36:39], s42 offen offset:0 sc1\ns_mov_b64 exec, -1\n.Lqk_sk2850%=:\nv_max_u32 v17, v84, v86\nv_cmp_eq_u32 vcc, -1, v17\ns_cbranch_vccz .Lqk_sk2857%=\n" \
"s_mov_b64 exec, vcc\nbuffer_load_dwordx4 v[84:87], v3, s[36:39], s42 offen offset:1024 sc1\ns_mov_b64 exec, -1\n.Lqk_sk2857%=:\nv_max_u32 v17, v88, v90\nv_cmp_eq_u32 vcc, -1, v17\ns_cbranch_vccz .Lqk_sk2864%=\ns_mov_b64 exec, vcc\n" \
"buffer_load_dwordx4 v[88:91], v3, s[36:39], s42 offen offset:2048 sc1\ns_mov_b64 exec, -1\n.Lqk_sk2864%=:\nv_max_u32 v17, v92, v94\nv_cmp_eq_u32 vcc, -1, v17\ns_cbranch_vccz .Lqk_sk2871%=\ns_mov_b64 exec, vcc\nbuffer_load_dwordx4 v[92:95], v3, s[36:39], s42 offen offset:3072 sc1\n" \
"s_mov_b64 exec, -1\n.Lqk_sk2871%=:\nbuffer_load_dword v16, v12, s[36:39], s56 offen sc1\nv_mov_b64 v[48:49], 0\nv_mov_b64 v[50:51], 0\nv_mov_b64 v[52:53], 0\nv_mov_b64 v[54:55], 0\nv_mov_b64 v[56:57], 0\n" \
"v_mov_b64 v[58:59], 0\nv_mov_b64 v[60:61], 0\nv_mov_b64 v[62:63], 0\ns_add_u32 s43, s43, 1\ns_cmp_lt_u32 s43, 262144\ns_cbranch_scc1 .Lqk_poll3%=\ns_branch .Lqk_abort%=\n.Lqk_abort%=:\n" \
"s_mov_b64 exec, -1\nv_mov_b32 v16, 0\ns_movk_i32 s44, 6400\nbuffer_store_dword v16, off, s[36:39], s44 sc1\n.Lqk_end%=:\ns_waitcnt vmcnt(0) lgkmcnt(0)\n" \
""
#define VU_ASM \
"v_mbcnt_lo_u32_b32 v255, -1, 0\nv_mbcnt_hi_u32_b32 v255, -1, v255\nv_lshlrev_b32 v255, 2, v255\nv_add_u32 v255, %[tb], v255\nds_read_b32 v0, v255 offset:0\nds_read_b32 v1, v255 offset:256\nds_read_b32 v2, v255 offset:512\nds_read_b32 v3, v255 offset:768\n" \
"ds_read_b32 v4, v255 offset:1024\nds_read_b32 v5, v255 offset:1280\nds_read_b32 v6, v255 offset:1536\nds_read_b32 v7, v255 offset:1792\nds_read_b32 v8, v255 offset:2048\nds_read_b32 v9, v255 offset:2304\nds_read_b32 v10, v255 offset:2560\nds_read_b32 v11, v255 offset:2816\n" \
"ds_read_b32 v90, v255 offset:3072\nds_read_b32 v91, v255 offset:3328\nds_read_b32 v92, v255 offset:3584\nds_read_b32 v93, v255 offset:3840\nds_read_b32 v94, v255 offset:4096\nds_read_b32 v95, v255 offset:4352\ns_waitcnt lgkmcnt(0)\ns_mov_b64 s[36:37], %[ws]\n" \
"s_mov_b32 s38, %[sz]\ns_mov_b32 s39, 0x20000\ns_mov_b32 s45, 2096896\nbuffer_load_dword v15, off, s[36:39], s45 sc1\ns_add_u32 m0, %[lw], 16384\ns_add_u32 s44, %[wo], 0\nbuffer_load_dwordx4 v90, %[rw], s44 offen nt lds\ns_add_u32 m0, %[lw], 17408\n" \
"s_add_u32 s44, %[wo], 65536\nbuffer_load_dwordx4 v90, %[rw], s44 offen nt lds\ns_add_u32 m0, %[lw], 18432\ns_add_u32 s44, %[wo], 131072\nbuffer_load_dwordx4 v90, %[rw], s44 offen nt lds\ns_add_u32 m0, %[lw], 19456\ns_add_u32 s44, %[wo], 196608\nbuffer_load_dwordx4 v90, %[rw], s44 offen nt lds\n" \
"s_add_u32 m0, %[lw], 20480\ns_add_u32 s44, %[wo], 524288\nbuffer_load_dwordx4 v90, %[rw], s44 offen nt lds\ns_add_u32 m0, %[lw], 21504\ns_add_u32 s44, %[wo], 589824\nbuffer_load_dwordx4 v90, %[rw], s44 offen nt lds\ns_add_u32 m0, %[lw], 22528\ns_add_u32 s44, %[wo], 655360\n" \
"buffer_load_dwordx4 v90, %[rw], s44 offen nt lds\ns_add_u32 m0, %[lw], 23552\ns_add_u32 s44, %[wo], 720896\nbuffer_load_dwordx4 v90, %[rw], s44 offen nt lds\ns_add_u32 m0, %[tb], 0\ns_add_u32 s44, %[wo], 1048576\nbuffer_load_dwordx4 v90, %[rw], s44 offen nt lds\ns_add_u32 m0, %[tb], 1024\n" \
"s_add_u32 s44, %[wo], 1114112\nbuffer_load_dwordx4 v90, %[rw], s44 offen nt lds\ns_add_u32 m0, %[tb], 2048\ns_add_u32 s44, %[wo], 1179648\nbuffer_load_dwordx4 v90, %[rw], s44 offen nt lds\ns_add_u32 m0, %[tb], 3072\ns_add_u32 s44, %[wo], 1245184\nbuffer_load_dwordx4 v90, %[rw], s44 offen nt lds\n" \
"s_add_u32 m0, %[tb], 4096\ns_add_u32 s44, %[wo], 1572864\nbuffer_load_dwordx4 v90, %[rw], s44 offen nt lds\ns_add_u32 m0, %[tb], 5120\ns_add_u32 s44, %[wo], 1638400\nbuffer_load_dwordx4 v90, %[rw], s44 offen nt lds\ns_add_u32 m0, %[tb], 6144\ns_add_u32 s44, %[wo], 1703936\n" \
"buffer_load_dwordx4 v90, %[rw], s44 offen nt lds\ns_add_u32 m0, %[tb], 7168\ns_add_u32 s44, %[wo], 1769472\nbuffer_load_dwordx4 v90, %[rw], s44 offen nt lds\ns_add_u32 m0, %[tb], 8192\ns_add_u32 s44, %[wo], 2097152\nbuffer_load_dwordx4 v90, %[rw], s44 offen nt lds\ns_add_u32 m0, %[tb], 9216\n" \
"s_add_u32 s44, %[wo], 2162688\nbuffer_load_dwordx4 v90, %[rw], s44 offen nt lds\ns_add_u32 m0, %[tb], 10240\ns_add_u32 s44, %[wo], 2228224\nbuffer_load_dwordx4 v90, %[rw], s44 offen nt lds\ns_add_u32 m0, %[tb], 11264\ns_add_u32 s44, %[wo], 2293760\nbuffer_load_dwordx4 v90, %[rw], s44 offen nt lds\n" \
"s_add_u32 m0, %[lw], 0\ns_add_u32 s44, %[wo], 2621440\nbuffer_load_dwordx4 v90, %[rw], s44 offen nt lds\ns_add_u32 m0, %[lw], 1024\ns_add_u32 s44, %[wo], 2686976\nbuffer_load_dwordx4 v90, %[rw], s44 offen nt lds\ns_add_u32 m0, %[lw], 2048\ns_add_u32 s44, %[wo], 2752512\n" \
"buffer_load_dwordx4 v90, %[rw], s44 offen nt lds\ns_add_u32 m0, %[lw], 3072\ns_add_u32 s44, %[wo], 2818048\nbuffer_load_dwordx4 v90, %[rw], s44 offen nt lds\ns_add_u32 m0, %[lw], 4096\ns_add_u32 s44, %[wo], 3145728\nbuffer_load_dwordx4 v90, %[rw], s44 offen nt lds\ns_add_u32 m0, %[lw], 5120\n" \
"s_add_u32 s44, %[wo], 3211264\nbuffer_load_dwordx4 v90, %[rw], s44 offen nt lds\ns_add_u32 m0, %[lw], 6144\ns_add_u32 s44, %[wo], 3276800\nbuffer_load_dwordx4 v90, %[rw], s44 offen nt lds\ns_add_u32 m0, %[lw], 7168\ns_add_u32 s44, %[wo], 3342336\nbuffer_load_dwordx4 v90, %[rw], s44 offen nt lds\n" \
"s_add_u32 m0, %[lw], 8192\ns_add_u32 s44, %[wo], 3670016\nbuffer_load_dwordx4 v90, %[rw], s44 offen nt lds\ns_add_u32 m0, %[lw], 9216\ns_add_u32 s44, %[wo], 3735552\nbuffer_load_dwordx4 v90, %[rw], s44 offen nt lds\ns_add_u32 m0, %[lw], 10240\ns_add_u32 s44, %[wo], 3801088\n" \
"buffer_load_dwordx4 v90, %[rw], s44 offen nt lds\ns_add_u32 m0, %[lw], 11264\ns_add_u32 s44, %[wo], 3866624\nbuffer_load_dwordx4 v90, %[rw], s44 offen nt lds\ns_waitcnt vmcnt(28)\ns_barrier\ns_add_u32 m0, %[lw], 12288\ns_add_u32 s44, %[wo], 4194304\n" \
"buffer_load_dwordx4 v90, %[rw], s44 offen nt lds\ns_add_u32 m0, %[lw], 13312\ns_add_u32 s44, %[wo], 4259840\nbuffer_load_dwordx4 v90, %[rw], s44 offen nt lds\ns_add_u32 m0, %[lw], 14336\ns_add_u32 s44, %[wo], 4325376\nbuffer_load_dwordx4 v90, %[rw], s44 offen nt lds\ns_add_u32 m0, %[lw], 15360\n" \
"s_add_u32 s44, %[wo], 4390912\nbuffer_load_dwordx4 v90, %[rw], s44 offen nt lds\nds_read_b64_tr_b16 a[0:1], v93 offset:16384\nds_read_b64_tr_b16 a[2:3], v93 offset:16896\nds_read_b64_tr_b16 a[16:17], v93 offset:16392\nds_read_b64_tr_b16 a[18:19], v93 offset:16904\nds_read_b64_tr_b16 a[4:5], v93 offset:40960\nds_read_b64_tr_b16 a[6:7], v93 offset:41472\n" \
"ds_read_b64_tr_b16 a[20:21], v93 offset:40968\nds_read_b64_tr_b16 a[22:23], v93 offset:41480\ns_waitcnt lgkmcnt(0)\ns_waitcnt vmcnt(28)\ns_barrier\ns_add_u32 m0, %[lw], 16384\ns_add_u32 s44, %[wo], 4718592\nbuffer_load_dwordx4 v90, %[rw], s44 offen nt lds\n" \
"s_add_u32 m0, %[lw], 17408\ns_add_u32 s44, %[wo], 4784128\nbuffer_load_dwordx4 v90, %[rw], s44 offen nt lds\ns_add_u32 m0, %[lw], 18432\ns_add_u32 s44, %[wo], 4849664\nbuffer_load_dwordx4 v90, %[rw], s44 offen nt lds\ns_add_u32 m0, %[lw], 19456\ns_add_u32 s44, %[wo], 4915200\n" \
"buffer_load_dwordx4 v90, %[rw], s44 offen nt lds\nds_read_b64_tr_b16 a[24:25], v93 offset:20480\nds_read_b64_tr_b16 a[26:27], v93 offset:20992\nds_read_b64_tr_b16 a[40:41], v93 offset:20488\nds_read_b64_tr_b16 a[42:43], v93 offset:21000\nds_read_b64_tr_b16 a[28:29], v93 offset:45056\nds_read_b64_tr_b16 a[30:31], v93 offset:45568\nds_read_b64_tr_b16 a[44:45], v93 offset:45064\n" \
"ds_read_b64_tr_b16 a[46:47], v93 offset:45576\ns_waitcnt lgkmcnt(0)\ns_waitcnt vmcnt(28)\ns_barrier\ns_add_u32 m0, %[lw], 20480\ns_add_u32 s44, %[wo], 5242880\nbuffer_load_dwordx4 v90, %[rw], s44 offen nt lds\ns_add_u32 m0, %[lw], 21504\n" \
"s_add_u32 s44, %[wo], 5308416\nbuffer_load_dwordx4 v90, %[rw], s44 offen nt lds\ns_add_u32 m0, %[lw], 22528\ns_add_u32 s44, %[wo], 5373952\nbuffer_load_dwordx4 v90, %[rw], s44 offen nt lds\ns_add_u32 m0, %[lw], 23552\ns_add_u32 s44, %[wo], 5439488\nbuffer_load_dwordx4 v90, %[rw], s44 offen nt lds\n" \
"ds_read_b64_tr_b16 v[84:85], v92 offset:0\nds_read_b64_tr_b16 v[86:87], v92 offset:512\nds_read_b64_tr_b16 a[64:65], v92 offset:8\nds_read_b64_tr_b16 a[66:67], v92 offset:520\nds_read_b64_tr_b16 v[80:81], v92 offset:12288\nds_read_b64_tr_b16 v[82:83], v92 offset:12800\nds_read_b64_tr_b16 a[68:69], v92 offset:12296\nds_read_b64_tr_b16 a[70:71], v92 offset:12808\n" \
"s_waitcnt lgkmcnt(0)\ns_waitcnt vmcnt(28)\ns_barrier\ns_add_u32 m0, %[tb], 0\ns_add_u32 s44, %[wo], 5767168\nbuffer_load_dwordx4 v90, %[rw], s44 offen nt lds\ns_add_u32 m0, %[tb], 1024\ns_add_u32 s44, %[wo], 5832704\n" \
"buffer_load_dwordx4 v90, %[rw], s44 offen nt lds\ns_add_u32 m0, %[tb], 2048\ns_add_u32 s44, %[wo], 5898240\nbuffer_load_dwordx4 v90, %[rw], s44 offen nt lds\ns_add_u32 m0, %[tb], 3072\ns_add_u32 s44, %[wo], 5963776\nbuffer_load_dwordx4 v90, %[rw], s44 offen nt lds\nds_read_b64_tr_b16 a[80:81], v92 offset:4096\n" \
"ds_read_b64_tr_b16 a[82:83], v92 offset:4608\nds_read_b64_tr_b16 a[88:89], v92 offset:4104\nds_read_b64_tr_b16 a[90:91], v92 offset:4616\nds_read_b64_tr_b16 a[84:85], v92 offset:16384\nds_read_b64_tr_b16 a[86:87], v92 offset:16896\nds_read_b64_tr_b16 a[92:93], v92 offset:16392\nds_read_b64_tr_b16 a[94:95], v92 offset:16904\ns_waitcnt lgkmcnt(0)\n" \
"s_waitcnt vmcnt(28)\ns_barrier\ns_add_u32 m0, %[tb], 4096\ns_add_u32 s44, %[wo], 6291456\nbuffer_load_dwordx4 v90, %[rw], s44 offen nt lds\ns_add_u32 m0, %[tb], 5120\ns_add_u32 s44, %[wo], 6356992\nbuffer_load_dwordx4 v90, %[rw], s44 offen nt lds\n" \
"s_add_u32 m0, %[tb], 6144\ns_add_u32 s44, %[wo], 6422528\nbuffer_load_dwordx4 v90, %[rw], s44 offen nt lds\ns_add_u32 m0, %[tb], 7168\ns_add_u32 s44, %[wo], 6488064\nbuffer_load_dwordx4 v90, %[rw], s44 offen nt lds\nds_read_b64_tr_b16 a[104:105], v92 offset:8192\nds_read_b64_tr_b16 a[106:107], v92 offset:8704\n" \
"ds_read_b64_tr_b16 a[120:121], v92 offset:8200\nds_read_b64_tr_b16 a[122:123], v92 offset:8712\nds_read_b64_tr_b16 a[108:109], v92 offset:20480\nds_read_b64_tr_b16 a[110:111], v92 offset:20992\nds_read_b64_tr_b16 a[124:125], v92 offset:20488\nds_read_b64_tr_b16 a[126:127], v92 offset:21000\ns_waitcnt lgkmcnt(0)\ns_waitcnt vmcnt(28)\n" \
"s_barrier\ns_add_u32 m0, %[tb], 8192\ns_add_u32 s44, %[wo], 6815744\nbuffer_load_dwordx4 v90, %[rw], s44 offen nt lds\ns_add_u32 m0, %[tb], 9216\ns_add_u32 s44, %[wo], 6881280\nbuffer_load_dwordx4 v90, %[rw], s44 offen nt lds\ns_add_u32 m0, %[tb], 10240\n" \
"s_add_u32 s44, %[wo], 6946816\nbuffer_load_dwordx4 v90, %[rw], s44 offen nt lds\ns_add_u32 m0, %[tb], 11264\ns_add_u32 s44, %[wo], 7012352\nbuffer_load_dwordx4 v90, %[rw], s44 offen nt lds\nds_read_b64_tr_b16 a[128:129], v93 offset:0\nds_read_b64_tr_b16 a[130:131], v93 offset:512\nds_read_b64_tr_b16 v[76:77], v93 offset:8\n" \
"ds_read_b64_tr_b16 v[78:79], v93 offset:520\nds_read_b64_tr_b16 a[132:133], v93 offset:24576\nds_read_b64_tr_b16 a[134:135], v93 offset:25088\nds_read_b64_tr_b16 v[72:73], v93 offset:24584\nds_read_b64_tr_b16 v[74:75], v93 offset:25096\ns_waitcnt lgkmcnt(0)\ns_waitcnt vmcnt(28)\ns_barrier\n" \
"s_add_u32 m0, %[lw], 0\ns_add_u32 s44, %[wo], 7340032\nbuffer_load_dwordx4 v90, %[rw], s44 offen nt lds\ns_add_u32 m0, %[lw], 1024\ns_add_u32 s44, %[wo], 7405568\nbuffer_load_dwordx4 v90, %[rw], s44 offen nt lds\ns_add_u32 m0, %[lw], 2048\ns_add_u32 s44, %[wo], 7471104\n" \
"buffer_load_dwordx4 v90, %[rw], s44 offen nt lds\ns_add_u32 m0, %[lw], 3072\ns_add_u32 s44, %[wo], 7536640\nbuffer_load_dwordx4 v90, %[rw], s44 offen nt lds\nds_read_b64_tr_b16 a[152:153], v93 offset:4096\nds_read_b64_tr_b16 a[154:155], v93 offset:4608\nds_read_b64_tr_b16 a[168:169], v93 offset:4104\nds_read_b64_tr_b16 a[170:171], v93 offset:4616\n" \
"ds_read_b64_tr_b16 a[156:157], v93 offset:28672\nds_read_b64_tr_b16 a[158:159], v93 offset:29184\nds_read_b64_tr_b16 a[172:173], v93 offset:28680\nds_read_b64_tr_b16 a[174:175], v93 offset:29192\ns_waitcnt lgkmcnt(0)\ns_waitcnt vmcnt(28)\ns_barrier\ns_add_u32 m0, %[lw], 4096\n" \
"s_add_u32 s44, %[wo], 7864320\nbuffer_load_dwordx4 v90, %[rw], s44 offen nt lds\ns_add_u32 m0, %[lw], 5120\ns_add_u32 s44, %[wo], 7929856\nbuffer_load_dwordx4 v90, %[rw], s44 offen nt lds\ns_add_u32 m0, %[lw], 6144\ns_add_u32 s44, %[wo], 7995392\nbuffer_load_dwordx4 v90, %[rw], s44 offen nt lds\n" \
"s_add_u32 m0, %[lw], 7168\ns_add_u32 s44, %[wo], 8060928\nbuffer_load_dwordx4 v90, %[rw], s44 offen nt lds\nds_read_b64_tr_b16 a[184:185], v93 offset:8192\nds_read_b64_tr_b16 a[186:187], v93 offset:8704\nds_read_b64_tr_b16 a[192:193], v93 offset:8200\nds_read_b64_tr_b16 a[194:195], v93 offset:8712\nds_read_b64_tr_b16 a[188:189], v93 offset:32768\n" \
"ds_read_b64_tr_b16 a[190:191], v93 offset:33280\nds_read_b64_tr_b16 a[196:197], v93 offset:32776\nds_read_b64_tr_b16 a[198:199], v93 offset:33288\ns_waitcnt lgkmcnt(0)\ns_waitcnt vmcnt(28)\ns_barrier\ns_add_u32 m0, %[lw], 8192\ns_add_u32 s44, %[w2], 0\n" \
"buffer_load_dwordx4 v91, %[ro], s44 offen nt lds\ns_add_u32 m0, %[lw], 9216\ns_add_u32 s44, %[w2], 32768\nbuffer_load_dwordx4 v91, %[ro], s44 offen nt lds\ns_add_u32 m0, %[lw], 10240\ns_add_u32 s44, %[w2], 65536\nbuffer_load_dwordx4 v91, %[ro], s44 offen nt lds\ns_add_u32 m0, %[lw], 11264\n" \
"s_add_u32 s44, %[w2], 98304\nbuffer_load_dwordx4 v91, %[ro], s44 offen nt lds\nds_read_b64_tr_b16 v[68:69], v93 offset:12288\nds_read_b64_tr_b16 v[70:71], v93 offset:12800\nds_read_b64_tr_b16 a[216:217], v93 offset:12296\nds_read_b64_tr_b16 a[218:219], v93 offset:12808\nds_read_b64_tr_b16 v[64:65], v93 offset:36864\nds_read_b64_tr_b16 v[66:67], v93 offset:37376\n" \
"ds_read_b64_tr_b16 a[220:221], v93 offset:36872\nds_read_b64_tr_b16 a[222:223], v93 offset:37384\ns_waitcnt lgkmcnt(0)\ns_waitcnt vmcnt(28)\ns_barrier\ns_add_u32 m0, %[lw], 12288\ns_add_u32 s44, %[w2], 128\nbuffer_load_dwordx4 v91, %[ro], s44 offen nt lds\n" \
"s_add_u32 m0, %[lw], 13312\ns_add_u32 s44, %[w2], 32896\nbuffer_load_dwordx4 v91, %[ro], s44 offen nt lds\ns_add_u32 m0, %[lw], 14336\ns_add_u32 s44, %[w2], 65664\nbuffer_load_dwordx4 v91, %[ro], s44 offen nt lds\ns_add_u32 m0, %[lw], 15360\ns_add_u32 s44, %[w2], 98432\n" \
"buffer_load_dwordx4 v91, %[ro], s44 offen nt lds\nds_read_b64_tr_b16 a[224:225], v93 offset:16384\nds_read_b64_tr_b16 a[226:227], v93 offset:16896\nds_read_b64_tr_b16 a[240:241], v93 offset:16392\nds_read_b64_tr_b16 a[242:243], v93 offset:16904\nds_read_b64_tr_b16 a[228:229], v93 offset:40960\nds_read_b64_tr_b16 a[230:231], v93 offset:41472\nds_read_b64_tr_b16 a[244:245], v93 offset:40968\n" \
"ds_read_b64_tr_b16 a[246:247], v93 offset:41480\ns_waitcnt lgkmcnt(0)\ns_waitcnt vmcnt(28)\ns_barrier\ns_add_u32 m0, %[lw], 16384\ns_add_u32 s44, %[w2], 256\nbuffer_load_dwordx4 v91, %[ro], s44 offen nt lds\ns_add_u32 m0, %[lw], 17408\n" \
"s_add_u32 s44, %[w2], 33024\nbuffer_load_dwordx4 v91, %[ro], s44 offen nt lds\ns_add_u32 m0, %[lw], 18432\ns_add_u32 s44, %[w2], 65792\nbuffer_load_dwordx4 v91, %[ro], s44 offen nt lds\ns_add_u32 m0, %[lw], 19456\ns_add_u32 s44, %[w2], 98560\nbuffer_load_dwordx4 v91, %[ro], s44 offen nt lds\n" \
"ds_read_b64_tr_b16 v[96:97], v93 offset:20480\nds_read_b64_tr_b16 v[98:99], v93 offset:20992\nds_read_b64_tr_b16 v[112:113], v93 offset:20488\nds_read_b64_tr_b16 v[114:115], v93 offset:21000\nds_read_b64_tr_b16 v[100:101], v93 offset:45056\nds_read_b64_tr_b16 v[102:103], v93 offset:45568\nds_read_b64_tr_b16 v[116:117], v93 offset:45064\nds_read_b64_tr_b16 v[118:119], v93 offset:45576\n" \
"s_waitcnt lgkmcnt(0)\ns_waitcnt vmcnt(28)\ns_barrier\ns_add_u32 m0, %[lw], 20480\ns_add_u32 s44, %[w2], 384\nbuffer_load_dwordx4 v91, %[ro], s44 offen nt lds\ns_add_u32 m0, %[lw], 21504\ns_add_u32 s44, %[w2], 33152\n" \
"buffer_load_dwordx4 v91, %[ro], s44 offen nt lds\ns_add_u32 m0, %[lw], 22528\ns_add_u32 s44, %[w2], 65920\nbuffer_load_dwordx4 v91, %[ro], s44 offen nt lds\ns_add_u32 m0, %[lw], 23552\ns_add_u32 s44, %[w2], 98688\nbuffer_load_dwordx4 v91, %[ro], s44 offen nt lds\nds_read_b64_tr_b16 v[128:129], v92 offset:0\n" \
"ds_read_b64_tr_b16 v[130:131], v92 offset:512\nds_read_b64_tr_b16 v[60:61], v92 offset:8\nds_read_b64_tr_b16 v[62:63], v92 offset:520\nds_read_b64_tr_b16 v[132:133], v92 offset:12288\nds_read_b64_tr_b16 v[134:135], v92 offset:12800\nds_read_b64_tr_b16 v[56:57], v92 offset:12296\nds_read_b64_tr_b16 v[58:59], v92 offset:12808\ns_waitcnt lgkmcnt(0)\n" \
"s_waitcnt vmcnt(28)\ns_barrier\ns_add_u32 m0, %[tb], 0\ns_add_u32 s44, %[w2], 139264\nbuffer_load_dwordx4 v91, %[ro], s44 offen nt lds\ns_add_u32 m0, %[tb], 1024\ns_add_u32 s44, %[w2], 172032\nbuffer_load_dwordx4 v91, %[ro], s44 offen nt lds\n" \
"s_add_u32 m0, %[tb], 2048\ns_add_u32 s44, %[w2], 204800\nbuffer_load_dwordx4 v91, %[ro], s44 offen nt lds\ns_add_u32 m0, %[tb], 3072\ns_add_u32 s44, %[w2], 237568\nbuffer_load_dwordx4 v91, %[ro], s44 offen nt lds\nds_read_b64_tr_b16 v[144:145], v92 offset:4096\nds_read_b64_tr_b16 v[146:147], v92 offset:4608\n" \
"ds_read_b64_tr_b16 v[160:161], v92 offset:4104\nds_read_b64_tr_b16 v[162:163], v92 offset:4616\nds_read_b64_tr_b16 v[148:149], v92 offset:16384\nds_read_b64_tr_b16 v[150:151], v92 offset:16896\nds_read_b64_tr_b16 v[164:165], v92 offset:16392\nds_read_b64_tr_b16 v[166:167], v92 offset:16904\ns_waitcnt lgkmcnt(0)\ns_waitcnt vmcnt(28)\n" \
"s_barrier\ns_add_u32 m0, %[tb], 4096\ns_add_u32 s44, %[w2], 139392\nbuffer_load_dwordx4 v91, %[ro], s44 offen nt lds\ns_add_u32 m0, %[tb], 5120\ns_add_u32 s44, %[w2], 172160\nbuffer_load_dwordx4 v91, %[ro], s44 offen nt lds\ns_add_u32 m0, %[tb], 6144\n" \
"s_add_u32 s44, %[w2], 204928\nbuffer_load_dwordx4 v91, %[ro], s44 offen nt lds\ns_add_u32 m0, %[tb], 7168\ns_add_u32 s44, %[w2], 237696\nbuffer_load_dwordx4 v91, %[ro], s44 offen nt lds\nds_read_b64_tr_b16 v[168:169], v92 offset:8192\nds_read_b64_tr_b16 v[170:171], v92 offset:8704\nds_read_b64_tr_b16 v[184:185], v92 offset:8200\n" \
"ds_read_b64_tr_b16 v[186:187], v92 offset:8712\nds_read_b64_tr_b16 v[172:173], v92 offset:20480\nds_read_b64_tr_b16 v[174:175], v92 offset:20992\nds_read_b64_tr_b16 v[188:189], v92 offset:20488\nds_read_b64_tr_b16 v[190:191], v92 offset:21000\ns_waitcnt lgkmcnt(0)\ns_waitcnt vmcnt(28)\ns_barrier\n" \
"s_add_u32 m0, %[tb], 8192\ns_add_u32 s44, %[w2], 139520\nbuffer_load_dwordx4 v91, %[ro], s44 offen nt lds\ns_add_u32 m0, %[tb], 9216\ns_add_u32 s44, %[w2], 172288\nbuffer_load_dwordx4 v91, %[ro], s44 offen nt lds\ns_add_u32 m0, %[tb], 10240\ns_add_u32 s44, %[w2], 205056\n" \
"buffer_load_dwordx4 v91, %[ro], s44 offen nt lds\ns_add_u32 m0, %[tb], 11264\ns_add_u32 s44, %[w2], 237824\nbuffer_load_dwordx4 v91, %[ro], s44 offen nt lds\nds_read_b64_tr_b16 v[200:201], v93 offset:0\nds_read_b64_tr_b16 v[202:203], v93 offset:512\nds_read_b64_tr_b16 v[216:217], v93 offset:8\nds_read_b64_tr_b16 v[218:219], v93 offset:520\n" \
"ds_read_b64_tr_b16 v[204:205], v93 offset:24576\nds_read_b64_tr_b16 v[206:207], v93 offset:25088\nds_read_b64_tr_b16 v[220:221], v93 offset:24584\nds_read_b64_tr_b16 v[222:223], v93 offset:25096\ns_waitcnt lgkmcnt(0)\ns_waitcnt vmcnt(28)\ns_barrier\ns_add_u32 m0, %[lw], 0\n" \
"s_add_u32 s44, %[w2], 139648\nbuffer_load_dwordx4 v91, %[ro], s44 offen nt lds\ns_add_u32 m0, %[lw], 1024\ns_add_u32 s44, %[w2], 172416\nbuffer_load_dwordx4 v91, %[ro], s44 offen nt lds\ns_add_u32 m0, %[lw], 2048\ns_add_u32 s44, %[w2], 205184\nbuffer_load_dwordx4 v91, %[ro], s44 offen nt lds\n" \
"s_add_u32 m0, %[lw], 3072\ns_add_u32 s44, %[w2], 237952\nbuffer_load_dwordx4 v91, %[ro], s44 offen nt lds\nds_read_b64_tr_b16 v[232:233], v93 offset:4096\nds_read_b64_tr_b16 v[234:235], v93 offset:4608\nds_read_b64_tr_b16 v[240:241], v93 offset:4104\nds_read_b64_tr_b16 v[242:243], v93 offset:4616\nds_read_b64_tr_b16 v[236:237], v93 offset:28672\n" \
"ds_read_b64_tr_b16 v[238:239], v93 offset:29184\nds_read_b64_tr_b16 v[244:245], v93 offset:28680\nds_read_b64_tr_b16 v[246:247], v93 offset:29192\ns_waitcnt lgkmcnt(0)\ns_waitcnt vmcnt(28)\ns_barrier\ns_add_u32 m0, %[lw], 4096\ns_add_u32 s44, %[w2], 131072\n" \
"buffer_load_dwordx4 v91, %[ro], s44 offen nt lds\ns_add_u32 m0, %[lw], 5120\ns_add_u32 s44, %[w2], 163840\nbuffer_load_dwordx4 v91, %[ro], s44 offen nt lds\ns_add_u32 m0, %[lw], 6144\ns_add_u32 s44, %[w2], 196608\nbuffer_load_dwordx4 v91, %[ro], s44 offen nt lds\ns_add_u32 m0, %[lw], 7168\n" \
"s_add_u32 s44, %[w2], 229376\nbuffer_load_dwordx4 v91, %[ro], s44 offen nt lds\nds_read_b64_tr_b16 a[8:9], v95 offset:8192\nds_read_b64_tr_b16 a[10:11], v95 offset:8448\nds_read_b64_tr_b16 a[12:13], v95 offset:10240\nds_read_b64_tr_b16 a[14:15], v95 offset:10496\nds_read_b64_tr_b16 a[208:209], v95 offset:8256\nds_read_b64_tr_b16 a[210:211], v95 offset:8512\n" \
"ds_read_b64_tr_b16 a[212:213], v95 offset:10304\nds_read_b64_tr_b16 a[214:215], v95 offset:10560\ns_waitcnt lgkmcnt(0)\ns_waitcnt vmcnt(28)\ns_barrier\ns_add_u32 m0, %[lw], 8192\ns_add_u32 s44, %[w2], 131200\nbuffer_load_dwordx4 v91, %[ro], s44 offen nt lds\n" \
"s_add_u32 m0, %[lw], 9216\ns_add_u32 s44, %[w2], 163968\nbuffer_load_dwordx4 v91, %[ro], s44 offen nt lds\ns_add_u32 m0, %[lw], 10240\ns_add_u32 s44, %[w2], 196736\nbuffer_load_dwordx4 v91, %[ro], s44 offen nt lds\ns_add_u32 m0, %[lw], 11264\ns_add_u32 s44, %[w2], 229504\n" \
"buffer_load_dwordx4 v91, %[ro], s44 offen nt lds\nds_read_b64_tr_b16 a[56:57], v95 offset:12288\nds_read_b64_tr_b16 a[58:59], v95 offset:12544\nds_read_b64_tr_b16 a[60:61], v95 offset:14336\nds_read_b64_tr_b16 a[62:63], v95 offset:14592\nds_read_b64_tr_b16 v[104:105], v95 offset:12352\nds_read_b64_tr_b16 v[106:107], v95 offset:12608\nds_read_b64_tr_b16 v[108:109], v95 offset:14400\n" \
"ds_read_b64_tr_b16 v[110:111], v95 offset:14656\ns_waitcnt lgkmcnt(0)\ns_waitcnt vmcnt(28)\ns_barrier\ns_add_u32 m0, %[lw], 12288\ns_add_u32 s44, %[w2], 131328\nbuffer_load_dwordx4 v91, %[ro], s44 offen nt lds\ns_add_u32 m0, %[lw], 13312\n" \
"s_add_u32 s44, %[w2], 164096\nbuffer_load_dwordx4 v91, %[ro], s44 offen nt lds\ns_add_u32 m0, %[lw], 14336\ns_add_u32 s44, %[w2], 196864\nbuffer_load_dwordx4 v91, %[ro], s44 offen nt lds\ns_add_u32 m0, %[lw], 15360\ns_add_u32 s44, %[w2], 229632\nbuffer_load_dwordx4 v91, %[ro], s44 offen nt lds\n" \
"ds_read_b64_tr_b16 a[112:113], v95 offset:16384\nds_read_b64_tr_b16 a[114:115], v95 offset:16640\nds_read_b64_tr_b16 a[116:117], v95 offset:18432\nds_read_b64_tr_b16 a[118:119], v95 offset:18688\nds_read_b64_tr_b16 v[152:153], v95 offset:16448\nds_read_b64_tr_b16 v[154:155], v95 offset:16704\nds_read_b64_tr_b16 v[156:157], v95 offset:18496\nds_read_b64_tr_b16 v[158:159], v95 offset:18752\n" \
"s_waitcnt lgkmcnt(0)\ns_waitcnt vmcnt(28)\ns_barrier\ns_add_u32 m0, %[lw], 16384\ns_add_u32 s44, %[w2], 131456\nbuffer_load_dwordx4 v91, %[ro], s44 offen nt lds\ns_add_u32 m0, %[lw], 17408\ns_add_u32 s44, %[w2], 164224\n" \
"buffer_load_dwordx4 v91, %[ro], s44 offen nt lds\ns_add_u32 m0, %[lw], 18432\ns_add_u32 s44, %[w2], 196992\nbuffer_load_dwordx4 v91, %[ro], s44 offen nt lds\ns_add_u32 m0, %[lw], 19456\ns_add_u32 s44, %[w2], 229760\nbuffer_load_dwordx4 v91, %[ro], s44 offen nt lds\nds_read_b64_tr_b16 a[160:161], v95 offset:20480\n" \
"ds_read_b64_tr_b16 a[162:163], v95 offset:20736\nds_read_b64_tr_b16 a[164:165], v95 offset:22528\nds_read_b64_tr_b16 a[166:167], v95 offset:22784\nds_read_b64_tr_b16 v[208:209], v95 offset:20544\nds_read_b64_tr_b16 v[210:211], v95 offset:20800\nds_read_b64_tr_b16 v[212:213], v95 offset:22592\nds_read_b64_tr_b16 v[214:215], v95 offset:22848\ns_waitcnt lgkmcnt(0)\n" \
"s_waitcnt vmcnt(28)\ns_barrier\ns_add_u32 m0, %[lw], 20480\ns_add_u32 s44, %[w2], 8192\nbuffer_load_dwordx4 v91, %[ro], s44 offen nt lds\ns_add_u32 m0, %[lw], 21504\ns_add_u32 s44, %[w2], 40960\nbuffer_load_dwordx4 v91, %[ro], s44 offen nt lds\n" \
"s_add_u32 m0, %[lw], 22528\ns_add_u32 s44, %[w2], 73728\nbuffer_load_dwordx4 v91, %[ro], s44 offen nt lds\ns_add_u32 m0, %[lw], 23552\ns_add_u32 s44, %[w2], 106496\nbuffer_load_dwordx4 v91, %[ro], s44 offen nt lds\nds_read_b64_tr_b16 a[48:49], v94 offset:0\nds_read_b64_tr_b16 a[50:51], v94 offset:256\n" \
"ds_read_b64_tr_b16 a[52:53], v94 offset:2048\nds_read_b64_tr_b16 a[54:55], v94 offset:2304\nds_read_b64_tr_b16 a[248:249], v94 offset:64\nds_read_b64_tr_b16 a[250:251], v94 offset:320\nds_read_b64_tr_b16 a[252:253], v94 offset:2112\nds_read_b64_tr_b16 a[254:255], v94 offset:2368\ns_waitcnt lgkmcnt(0)\ns_waitcnt vmcnt(28)\n" \
"s_barrier\ns_add_u32 m0, %[tb], 0\ns_add_u32 s44, %[w2], 8320\nbuffer_load_dwordx4 v91, %[ro], s44 offen nt lds\ns_add_u32 m0, %[tb], 1024\ns_add_u32 s44, %[w2], 41088\nbuffer_load_dwordx4 v91, %[ro], s44 offen nt lds\ns_add_u32 m0, %[tb], 2048\n" \
"s_add_u32 s44, %[w2], 73856\nbuffer_load_dwordx4 v91, %[ro], s44 offen nt lds\ns_add_u32 m0, %[tb], 3072\ns_add_u32 s44, %[w2], 106624\nbuffer_load_dwordx4 v91, %[ro], s44 offen nt lds\nds_read_b64_tr_b16 a[96:97], v94 offset:4096\nds_read_b64_tr_b16 a[98:99], v94 offset:4352\nds_read_b64_tr_b16 a[100:101], v94 offset:6144\n" \
"ds_read_b64_tr_b16 a[102:103], v94 offset:6400\nds_read_b64_tr_b16 v[136:137], v94 offset:4160\nds_read_b64_tr_b16 v[138:139], v94 offset:4416\nds_read_b64_tr_b16 v[140:141], v94 offset:6208\nds_read_b64_tr_b16 v[142:143], v94 offset:6464\ns_waitcnt lgkmcnt(0)\nbuffer_load_dwordx2 v[12:13], off, s[36:39], s45 sc1\ns_waitcnt vmcnt(29)\n" \
"s_barrier\ns_add_u32 m0, %[tb], 4096\ns_add_u32 s44, %[w2], 8448\nbuffer_load_dwordx4 v91, %[ro], s44 offen nt lds\ns_add_u32 m0, %[tb], 5120\ns_add_u32 s44, %[w2], 41216\nbuffer_load_dwordx4 v91, %[ro], s44 offen nt lds\ns_add_u32 m0, %[tb], 6144\n" \
"s_add_u32 s44, %[w2], 73984\nbuffer_load_dwordx4 v91, %[ro], s44 offen nt lds\ns_add_u32 m0, %[tb], 7168\ns_add_u32 s44, %[w2], 106752\nbuffer_load_dwordx4 v91, %[ro], s44 offen nt lds\nds_read_b64_tr_b16 a[144:145], v94 offset:8192\nds_read_b64_tr_b16 a[146:147], v94 offset:8448\nds_read_b64_tr_b16 a[148:149], v94 offset:10240\n" \
"ds_read_b64_tr_b16 a[150:151], v94 offset:10496\nds_read_b64_tr_b16 v[192:193], v94 offset:8256\nds_read_b64_tr_b16 v[194:195], v94 offset:8512\nds_read_b64_tr_b16 v[196:197], v94 offset:10304\nds_read_b64_tr_b16 v[198:199], v94 offset:10560\ns_waitcnt lgkmcnt(0)\ns_waitcnt vmcnt(29)\ns_barrier\n" \
"s_add_u32 m0, %[tb], 8192\ns_add_u32 s44, %[w2], 8576\nbuffer_load_dwordx4 v91, %[ro], s44 offen nt lds\ns_add_u32 m0, %[tb], 9216\ns_add_u32 s44, %[w2], 41344\nbuffer_load_dwordx4 v91, %[ro], s44 offen nt lds\ns_add_u32 m0, %[tb], 10240\ns_add_u32 s44, %[w2], 74112\n" \
"buffer_load_dwordx4 v91, %[ro], s44 offen nt lds\ns_add_u32 m0, %[tb], 11264\ns_add_u32 s44, %[w2], 106880\nbuffer_load_dwordx4 v91, %[ro], s44 offen nt lds\nds_read_b64_tr_b16 a[200:201], v95 offset:0\nds_read_b64_tr_b16 a[202:203], v95 offset:256\nds_read_b64_tr_b16 a[204:205], v95 offset:2048\nds_read_b64_tr_b16 a[206:207], v95 offset:2304\n" \
"ds_read_b64_tr_b16 v[248:249], v95 offset:64\nds_read_b64_tr_b16 v[250:251], v95 offset:320\nds_read_b64_tr_b16 v[252:253], v95 offset:2112\nds_read_b64_tr_b16 v[254:255], v95 offset:2368\ns_waitcnt lgkmcnt(0)\ns_waitcnt vmcnt(29)\ns_barrier\nds_read_b64_tr_b16 v[52:53], v95 offset:4096\n" \
"ds_read_b64_tr_b16 v[54:55], v95 offset:4352\nds_read_b64_tr_b16 v[48:49], v95 offset:6144\nds_read_b64_tr_b16 v[50:51], v95 offset:6400\nds_read_b64_tr_b16 v[44:45], v95 offset:4160\nds_read_b64_tr_b16 v[46:47], v95 offset:4416\nds_read_b64_tr_b16 v[40:41], v95 offset:6208\nds_read_b64_tr_b16 v[42:43], v95 offset:6464\ns_waitcnt lgkmcnt(0)\n" \
"ds_write_b128 v1, v[84:87] offset:0\nds_write_b128 v1, v[80:83] offset:1024\nds_write_b128 v1, v[76:79] offset:2048\nds_write_b128 v1, v[72:75] offset:3072\ns_waitcnt vmcnt(25)\ns_barrier\nds_read_b64_tr_b16 a[72:73], v95 offset:8192\nds_read_b64_tr_b16 a[74:75], v95 offset:8448\n" \
"ds_read_b64_tr_b16 a[76:77], v95 offset:10240\nds_read_b64_tr_b16 a[78:79], v95 offset:10496\nds_read_b64_tr_b16 v[120:121], v95 offset:8256\nds_read_b64_tr_b16 v[122:123], v95 offset:8512\nds_read_b64_tr_b16 v[124:125], v95 offset:10304\nds_read_b64_tr_b16 v[126:127], v95 offset:10560\ns_waitcnt lgkmcnt(0)\nds_write_b128 v1, v[68:71] offset:4096\n" \
"ds_write_b128 v1, v[64:67] offset:5120\nds_write_b128 v1, v[60:63] offset:6144\nds_write_b128 v1, v[56:59] offset:7168\ns_waitcnt vmcnt(21)\ns_barrier\nds_read_b64_tr_b16 v[56:57], v95 offset:12288\nds_read_b64_tr_b16 v[58:59], v95 offset:12544\nds_read_b64_tr_b16 v[60:61], v95 offset:14336\n" \
"ds_read_b64_tr_b16 v[62:63], v95 offset:14592\nds_read_b64_tr_b16 v[64:65], v95 offset:12352\nds_read_b64_tr_b16 v[66:67], v95 offset:12608\nds_read_b64_tr_b16 v[68:69], v95 offset:14400\nds_read_b64_tr_b16 v[70:71], v95 offset:14656\ns_waitcnt lgkmcnt(0)\nds_write_b128 v1, v[52:55] offset:8192\nds_write_b128 v1, v[48:51] offset:9216\n" \
"ds_write_b128 v1, v[44:47] offset:10240\nds_write_b128 v1, v[40:43] offset:11264\ns_waitcnt vmcnt(17)\ns_barrier\nds_read_b64_tr_b16 a[176:177], v95 offset:16384\nds_read_b64_tr_b16 a[178:179], v95 offset:16640\nds_read_b64_tr_b16 a[180:181], v95 offset:18432\nds_read_b64_tr_b16 a[182:183], v95 offset:18688\n" \
"ds_read_b64_tr_b16 v[224:225], v95 offset:16448\nds_read_b64_tr_b16 v[226:227], v95 offset:16704\nds_read_b64_tr_b16 v[228:229], v95 offset:18496\nds_read_b64_tr_b16 v[230:231], v95 offset:18752\ns_waitcnt lgkmcnt(0)\nds_write_b128 v1, v[56:59] offset:12288\nds_write_b128 v1, v[60:63] offset:13312\nds_write_b128 v1, v[64:67] offset:14336\n" \
"ds_write_b128 v1, v[68:71] offset:15360\ns_waitcnt vmcnt(13)\ns_barrier\nds_read_b64_tr_b16 a[32:33], v95 offset:20480\nds_read_b64_tr_b16 a[34:35], v95 offset:20736\nds_read_b64_tr_b16 a[36:37], v95 offset:22528\nds_read_b64_tr_b16 a[38:39], v95 offset:22784\nds_read_b64_tr_b16 a[232:233], v95 offset:20544\n" \
"ds_read_b64_tr_b16 a[234:235], v95 offset:20800\nds_read_b64_tr_b16 a[236:237], v95 offset:22592\nds_read_b64_tr_b16 a[238:239], v95 offset:22848\ns_waitcnt lgkmcnt(0)\ns_waitcnt vmcnt(9)\ns_barrier\nds_read_b64_tr_b16 v[68:69], v94 offset:0\nds_read_b64_tr_b16 v[70:71], v94 offset:256\n" \
"ds_read_b64_tr_b16 v[64:65], v94 offset:2048\nds_read_b64_tr_b16 v[66:67], v94 offset:2304\nds_read_b64_tr_b16 v[60:61], v94 offset:64\nds_read_b64_tr_b16 v[62:63], v94 offset:320\nds_read_b64_tr_b16 v[56:57], v94 offset:2112\nds_read_b64_tr_b16 v[58:59], v94 offset:2368\ns_waitcnt lgkmcnt(0)\nds_write_b128 v1, v[68:71] offset:16384\n" \
"ds_write_b128 v1, v[64:67] offset:17408\nds_write_b128 v1, v[60:63] offset:18432\nds_write_b128 v1, v[56:59] offset:19456\ns_waitcnt vmcnt(4)\ns_barrier\nds_read_b64_tr_b16 a[136:137], v94 offset:4096\nds_read_b64_tr_b16 a[138:139], v94 offset:4352\nds_read_b64_tr_b16 a[140:141], v94 offset:6144\n" \
"ds_read_b64_tr_b16 a[142:143], v94 offset:6400\nds_read_b64_tr_b16 v[176:177], v94 offset:4160\nds_read_b64_tr_b16 v[178:179], v94 offset:4416\nds_read_b64_tr_b16 v[180:181], v94 offset:6208\nds_read_b64_tr_b16 v[182:183], v94 offset:6464\ns_waitcnt lgkmcnt(0)\ns_waitcnt vmcnt(0)\ns_barrier\n" \
"ds_read_b64_tr_b16 v[56:57], v94 offset:8192\nds_read_b64_tr_b16 v[58:59], v94 offset:8448\nds_read_b64_tr_b16 v[60:61], v94 offset:10240\nds_read_b64_tr_b16 v[62:63], v94 offset:10496\nds_read_b64_tr_b16 v[64:65], v94 offset:8256\nds_read_b64_tr_b16 v[66:67], v94 offset:8512\nds_read_b64_tr_b16 v[68:69], v94 offset:10304\nds_read_b64_tr_b16 v[70:71], v94 offset:10560\n" \
"s_waitcnt lgkmcnt(0)\ns_mov_b32 s43, 0\ns_mov_b32 s86, 110\ns_mov_b32 s89, 0\n.Lvu_hb%=:\nv_cmp_ne_u32 vcc, v15, v12\ns_cbranch_vccnz .Lvu_hb1%=\ns_sleep 4\n" \
"buffer_load_dwordx2 v[12:13], off, s[36:39], s45 sc1\ns_waitcnt vmcnt(0)\ns_add_u32 s43, s43, 1\ns_cmp_lt_u32 s43, 262144\ns_cbranch_scc1 .Lvu_hb%=\ns_branch .Lvu_abort%=\n.Lvu_hb1%=:\nv_readfirstlane_b32 s44, v13\n" \
"s_and_b32 s44, s44, 0x200000\ns_add_u32 s36, s36, s44\ns_addc_u32 s37, s37, 0\nds_write_b128 v1, v[56:59] offset:20480\nds_write_b128 v1, v[60:63] offset:21504\nds_write_b128 v1, v[64:67] offset:22528\nds_write_b128 v1, v[68:71] offset:23552\ns_waitcnt lgkmcnt(0)\n" \
"s_barrier\ns_mov_b32 s40, -1\ns_mov_b32 s43, 0\ns_lshl_b32 s57, %[w], 12\ns_mov_b32 s56, 0\ns_mov_b32 s42, 0\ns_mov_b32 s60, -2147483648\ns_mov_b32 s54, -2147483648\n" \
"s_add_u32 s44, s40, 1\ns_lshl_b32 s44, s44, 17\ns_add_u32 s44, s44, %[xn]\ns_lshr_b32 s45, s44, 8\ns_and_b32 s45, s45, 0x2000\ns_xor_b32 s41, s44, s45\ns_add_u32 s41, s41, %[xw]\n.Lvu_f0%=:\n" \
"buffer_load_dword v14, v8, s[36:39], s56 offen sc1\ns_waitcnt vmcnt(0)\nv_cmp_ge_i32 vcc, s40, v14\ns_cbranch_vccz .Lvu_f0fok%=\ns_add_u32 s43, s43, 1\ns_cmp_lt_u32 s43, 262144\ns_cbranch_scc1 .Lvu_f0%=\ns_branch .Lvu_abort%=\n" \
".Lvu_f0fok%=:\ns_add_u32 m0, s57, 0\ns_add_u32 s58, s41, 0\nbuffer_load_dwordx4 v0, s[36:39], s58 offen offset:0 sc1 lds\nbuffer_load_dwordx4 v0, s[36:39], s58 offen offset:1024 sc1 lds\nbuffer_load_dwordx4 v0, s[36:39], s58 offen offset:2048 sc1 lds\nbuffer_load_dwordx4 v0, s[36:39], s58 offen offset:3072 sc1 lds\ns_add_u32 m0, s57, 32768\n" \
"s_add_u32 s58, s41, 32768\nbuffer_load_dwordx4 v0, s[36:39], s58 offen offset:0 sc1 lds\nbuffer_load_dwordx4 v0, s[36:39], s58 offen offset:1024 sc1 lds\nbuffer_load_dwordx4 v0, s[36:39], s58 offen offset:2048 sc1 lds\nbuffer_load_dwordx4 v0, s[36:39], s58 offen offset:3072 sc1 lds\ns_waitcnt vmcnt(0)\ns_barrier\nds_read_b128 v[32:35], v2 offset:0\n" \
"ds_read_b128 v[36:39], v2 offset:256\nds_read_b128 v[40:43], v2 offset:2048\nds_read_b128 v[44:47], v2 offset:2304\n.Lvu_tok%=:\nds_read_b128 v[64:67], v2 offset:4096\nds_read_b128 v[68:71], v2 offset:4352\ns_waitcnt lgkmcnt(5)\nv_mfma_f32_16x16x32_bf16 v[16:19], a[0:3], v[32:35], 0\n" \
"v_cvt_pk_bf16_f32 v92, v80, v81\nv_cvt_pk_bf16_f32 v93, v82, v83\nv_mfma_f32_16x16x32_bf16 v[20:23], a[16:19], v[32:35], 0\ns_waitcnt lgkmcnt(4)\nds_read_b128 v[32:35], v2 offset:6144\nv_cvt_pk_bf16_f32 v94, v84, v85\nv_cvt_pk_bf16_f32 v95, v86, v87\nv_mfma_f32_16x16x32_bf16 v[24:27], a[0:3], v[36:39], 0\n" \
"v_and_or_b32 v92, v92, -2, v13\nv_and_or_b32 v94, v94, -2, v13\nv_mfma_f32_16x16x32_bf16 v[28:31], a[16:19], v[36:39], 0\ns_waitcnt lgkmcnt(4)\nbuffer_store_dwordx4 v[92:95], v7, s[36:39], s60 offen offset:2560 sc1\nds_read_b128 v[36:39], v2 offset:6400\nv_mfma_f32_16x16x32_bf16 v[16:19], a[4:7], v[40:43], v[16:19]\nds_read_b128 v[56:59], v1 offset:0\n" \
"ds_read_b128 v[60:63], v1 offset:1024\nv_mfma_f32_16x16x32_bf16 v[20:23], a[20:23], v[40:43], v[20:23]\ns_waitcnt lgkmcnt(6)\ns_add_u32 m0, s57, 16384\nds_read_b128 v[40:43], v2 offset:8192\nv_mfma_f32_16x16x32_bf16 v[24:27], a[4:7], v[44:47], v[24:27]\ns_add_u32 s60, s41, 65536\nbuffer_load_dwordx4 v0, s[36:39], s60 offen offset:0 sc1 lds\n" \
"v_mfma_f32_16x16x32_bf16 v[28:31], a[20:23], v[44:47], v[28:31]\ns_waitcnt lgkmcnt(6)\nds_read_b128 v[44:47], v2 offset:8448\nv_mfma_f32_16x16x32_bf16 v[16:19], a[24:27], v[64:67], v[16:19]\nv_mfma_f32_16x16x32_bf16 v[20:23], a[40:43], v[64:67], v[20:23]\ns_waitcnt lgkmcnt(6)\nds_read_b128 v[64:67], v2 offset:10240\nv_mfma_f32_16x16x32_bf16 v[24:27], a[24:27], v[68:71], v[24:27]\n" \
"buffer_load_dwordx4 v0, s[36:39], s60 offen offset:1024 sc1 lds\nv_mfma_f32_16x16x32_bf16 v[28:31], a[40:43], v[68:71], v[28:31]\ns_waitcnt lgkmcnt(6)\nds_read_b128 v[68:71], v2 offset:10496\nv_mfma_f32_16x16x32_bf16 v[16:19], a[28:31], v[32:35], v[16:19]\nv_mfma_f32_16x16x32_bf16 v[20:23], a[44:47], v[32:35], v[20:23]\ns_waitcnt lgkmcnt(6)\nds_read_b128 v[32:35], v2 offset:12288\n" \
"v_mfma_f32_16x16x32_bf16 v[24:27], a[28:31], v[36:39], v[24:27]\nbuffer_load_dwordx4 v0, s[36:39], s60 offen offset:2048 sc1 lds\nv_mfma_f32_16x16x32_bf16 v[28:31], a[44:47], v[36:39], v[28:31]\ns_waitcnt lgkmcnt(4)\nds_read_b128 v[36:39], v2 offset:12544\nv_mfma_f32_16x16x32_bf16 v[16:19], v[56:59], v[40:43], v[16:19]\nv_mfma_f32_16x16x32_bf16 v[20:23], a[64:67], v[40:43], v[20:23]\ns_waitcnt lgkmcnt(4)\n" \
"ds_read_b128 v[40:43], v2 offset:14336\nv_mfma_f32_16x16x32_bf16 v[24:27], v[56:59], v[44:47], v[24:27]\nbuffer_load_dwordx4 v0, s[36:39], s60 offen offset:3072 sc1 lds\nv_mfma_f32_16x16x32_bf16 v[28:31], a[64:67], v[44:47], v[28:31]\ns_waitcnt lgkmcnt(4)\nds_read_b128 v[44:47], v2 offset:14592\nv_mfma_f32_16x16x32_bf16 v[16:19], v[60:63], v[64:67], v[16:19]\ns_add_u32 s56, s56, %[fs]\n" \
"s_add_u32 s62, s40, 1\ns_lshr_b32 m0, s57, 4\nv_mfma_f32_16x16x32_bf16 v[20:23], a[68:71], v[64:67], v[20:23]\ns_add_u32 m0, m0, 159744\ns_waitcnt lgkmcnt(4)\nds_read_b128 v[64:67], v2 offset:32768\nv_mfma_f32_16x16x32_bf16 v[24:27], v[60:63], v[68:71], v[24:27]\nbuffer_load_dword v8, s[36:39], s56 offen sc1 lds\n" \
"v_lshrrev_b32 v14, 2, v0\nv_mfma_f32_16x16x32_bf16 v[28:31], a[68:71], v[68:71], v[28:31]\ns_waitcnt lgkmcnt(4)\nds_read_b128 v[68:71], v2 offset:33024\nv_mfma_f32_16x16x32_bf16 v[16:19], a[80:83], v[32:35], v[16:19]\nv_add_u32 v14, 159744, v14\nv_mfma_f32_16x16x32_bf16 v[20:23], a[88:91], v[32:35], v[20:23]\ns_waitcnt lgkmcnt(4)\n" \
"ds_read_b128 v[32:35], v2 offset:34816\nv_mfma_f32_16x16x32_bf16 v[24:27], a[80:83], v[36:39], v[24:27]\nds_read_b128 v[56:59], v1 offset:2048\nds_read_b128 v[60:63], v1 offset:3072\nv_mfma_f32_16x16x32_bf16 v[28:31], a[88:91], v[36:39], v[28:31]\ns_waitcnt lgkmcnt(6)\nds_read_b128 v[36:39], v2 offset:35072\nv_mfma_f32_16x16x32_bf16 v[16:19], a[84:87], v[40:43], v[16:19]\n" \
"v_mfma_f32_16x16x32_bf16 v[20:23], a[92:95], v[40:43], v[20:23]\ns_waitcnt lgkmcnt(6)\nds_read_b128 v[40:43], v2 offset:36864\nv_mfma_f32_16x16x32_bf16 v[24:27], a[84:87], v[44:47], v[24:27]\nv_mfma_f32_16x16x32_bf16 v[28:31], a[92:95], v[44:47], v[28:31]\ns_waitcnt lgkmcnt(6)\nds_read_b128 v[44:47], v2 offset:37120\nv_mfma_f32_16x16x32_bf16 v[16:19], a[104:107], v[64:67], v[16:19]\n" \
"v_mfma_f32_16x16x32_bf16 v[20:23], a[120:123], v[64:67], v[20:23]\ns_waitcnt lgkmcnt(6)\nds_read_b128 v[64:67], v2 offset:38912\nv_mfma_f32_16x16x32_bf16 v[24:27], a[104:107], v[68:71], v[24:27]\ns_barrier\nv_mfma_f32_16x16x32_bf16 v[28:31], a[120:123], v[68:71], v[28:31]\ns_waitcnt lgkmcnt(6)\ns_add_u32 m0, s57, 0\n" \
"ds_read_b128 v[68:71], v2 offset:39168\nv_mfma_f32_16x16x32_bf16 v[16:19], a[108:111], v[32:35], v[16:19]\ns_add_u32 s60, s41, 98304\nbuffer_load_dwordx4 v0, s[36:39], s60 offen offset:0 sc1 lds\nv_mfma_f32_16x16x32_bf16 v[20:23], a[124:127], v[32:35], v[20:23]\ns_waitcnt lgkmcnt(4)\nds_read_b128 v[32:35], v2 offset:40960\nv_mfma_f32_16x16x32_bf16 v[24:27], a[108:111], v[36:39], v[24:27]\n" \
"v_mfma_f32_16x16x32_bf16 v[28:31], a[124:127], v[36:39], v[28:31]\ns_waitcnt lgkmcnt(4)\nds_read_b128 v[36:39], v2 offset:41216\nv_mfma_f32_16x16x32_bf16 v[16:19], a[128:131], v[40:43], v[16:19]\nbuffer_load_dwordx4 v0, s[36:39], s60 offen offset:1024 sc1 lds\nv_mfma_f32_16x16x32_bf16 v[20:23], v[56:59], v[40:43], v[20:23]\ns_waitcnt lgkmcnt(4)\nds_read_b128 v[40:43], v2 offset:43008\n" \
"v_mfma_f32_16x16x32_bf16 v[24:27], a[128:131], v[44:47], v[24:27]\nv_mfma_f32_16x16x32_bf16 v[28:31], v[56:59], v[44:47], v[28:31]\ns_waitcnt lgkmcnt(4)\nds_read_b128 v[44:47], v2 offset:43264\nv_mfma_f32_16x16x32_bf16 v[16:19], a[132:135], v[64:67], v[16:19]\nbuffer_load_dwordx4 v0, s[36:39], s60 offen offset:2048 sc1 lds\nv_mfma_f32_16x16x32_bf16 v[20:23], v[60:63], v[64:67], v[20:23]\ns_waitcnt lgkmcnt(4)\n" \
"ds_read_b128 v[64:67], v2 offset:45056\nv_mfma_f32_16x16x32_bf16 v[24:27], a[132:135], v[68:71], v[24:27]\nv_mfma_f32_16x16x32_bf16 v[28:31], v[60:63], v[68:71], v[28:31]\ns_waitcnt lgkmcnt(4)\nds_read_b128 v[68:71], v2 offset:45312\nv_mfma_f32_16x16x32_bf16 v[16:19], a[152:155], v[32:35], v[16:19]\nbuffer_load_dwordx4 v0, s[36:39], s60 offen offset:3072 sc1 lds\nv_mfma_f32_16x16x32_bf16 v[20:23], a[168:171], v[32:35], v[20:23]\n" \
"s_waitcnt lgkmcnt(4)\ns_add_u32 s44, s40, 2\nds_read_b128 v[32:35], v2 offset:47104\nv_mfma_f32_16x16x32_bf16 v[24:27], a[152:155], v[36:39], v[24:27]\ns_lshl_b32 s44, s44, 17\nds_read_b128 v[56:59], v1 offset:4096\nds_read_b128 v[60:63], v1 offset:5120\nv_mfma_f32_16x16x32_bf16 v[28:31], a[168:171], v[36:39], v[28:31]\n" \
"s_waitcnt lgkmcnt(6)\nds_read_b128 v[36:39], v2 offset:47360\nv_mfma_f32_16x16x32_bf16 v[16:19], a[156:159], v[40:43], v[16:19]\ns_add_u32 s44, s44, %[xn]\ns_lshr_b32 s45, s44, 8\nv_mfma_f32_16x16x32_bf16 v[20:23], a[172:175], v[40:43], v[20:23]\ns_waitcnt lgkmcnt(6)\nv_mfma_f32_16x16x32_bf16 v[24:27], a[156:159], v[44:47], v[24:27]\n" \
"s_and_b32 s45, s45, 0x2000\ns_xor_b32 s41, s44, s45\nv_mfma_f32_16x16x32_bf16 v[28:31], a[172:175], v[44:47], v[28:31]\ns_waitcnt lgkmcnt(0)\ns_waitcnt vmcnt(4)\ns_barrier\nds_read_b128 v[40:43], v2 offset:16384\nds_read_b128 v[44:47], v2 offset:16640\n" \
"v_mfma_f32_16x16x32_bf16 v[16:19], a[184:187], v[64:67], v[16:19]\nds_read_b32 v14, v14\nv_mfma_f32_16x16x32_bf16 v[20:23], a[192:195], v[64:67], v[20:23]\nds_read_b128 v[64:67], v2 offset:18432\nv_mfma_f32_16x16x32_bf16 v[24:27], a[184:187], v[68:71], v[24:27]\ns_add_u32 s41, s41, %[xw]\nv_mfma_f32_16x16x32_bf16 v[28:31], a[192:195], v[68:71], v[28:31]\nds_read_b128 v[68:71], v2 offset:18688\n" \
"v_mfma_f32_16x16x32_bf16 v[16:19], a[188:191], v[32:35], v[16:19]\nv_mfma_f32_16x16x32_bf16 v[20:23], a[196:199], v[32:35], v[20:23]\nds_read_b128 v[32:35], v2 offset:20480\nv_mfma_f32_16x16x32_bf16 v[24:27], a[188:191], v[36:39], v[24:27]\ns_waitcnt lgkmcnt(3)\nv_cmp_ge_i32 s[64:65], s62, v14\nv_mfma_f32_16x16x32_bf16 v[28:31], a[196:199], v[36:39], v[28:31]\ns_cmp_eq_u64 s[64:65], 0\n" \
"s_cselect_b32 s61, s41, -2147483648\nds_read_b128 v[36:39], v2 offset:20736\nv_mfma_f32_16x16x32_bf16 v[16:19], v[56:59], v[40:43], v[16:19]\ns_waitcnt vmcnt(4)\nbuffer_store_dword v15, off, s[36:39], s54 sc1\nv_mfma_f32_16x16x32_bf16 v[20:23], a[216:219], v[40:43], v[20:23]\ns_add_u32 m0, s57, 32768\nds_read_b128 v[40:43], v2 offset:22528\n" \
"v_mfma_f32_16x16x32_bf16 v[24:27], v[56:59], v[44:47], v[24:27]\ns_add_u32 s58, s61, 32768\nbuffer_load_dwordx4 v0, s[36:39], s58 offen offset:0 sc1 lds\nv_mfma_f32_16x16x32_bf16 v[28:31], a[216:219], v[44:47], v[28:31]\ns_waitcnt lgkmcnt(4)\nds_read_b128 v[44:47], v2 offset:22784\nv_mfma_f32_16x16x32_bf16 v[16:19], v[60:63], v[64:67], v[16:19]\nv_mfma_f32_16x16x32_bf16 v[20:23], a[220:223], v[64:67], v[20:23]\n" \
"s_waitcnt lgkmcnt(4)\nds_read_b128 v[64:67], v2 offset:24576\nv_mfma_f32_16x16x32_bf16 v[24:27], v[60:63], v[68:71], v[24:27]\nbuffer_load_dwordx4 v0, s[36:39], s58 offen offset:1024 sc1 lds\nv_mfma_f32_16x16x32_bf16 v[28:31], a[220:223], v[68:71], v[28:31]\ns_waitcnt lgkmcnt(4)\nds_read_b128 v[68:71], v2 offset:24832\nv_mfma_f32_16x16x32_bf16 v[16:19], a[224:227], v[32:35], v[16:19]\n" \
"ds_read_b128 v[56:59], v1 offset:6144\nds_read_b128 v[60:63], v1 offset:7168\nv_mfma_f32_16x16x32_bf16 v[20:23], a[240:243], v[32:35], v[20:23]\ns_waitcnt lgkmcnt(6)\nds_read_b128 v[32:35], v2 offset:26624\nv_mfma_f32_16x16x32_bf16 v[24:27], a[224:227], v[36:39], v[24:27]\nbuffer_load_dwordx4 v0, s[36:39], s58 offen offset:2048 sc1 lds\nv_mfma_f32_16x16x32_bf16 v[28:31], a[240:243], v[36:39], v[28:31]\n" \
"s_waitcnt lgkmcnt(6)\nds_read_b128 v[36:39], v2 offset:26880\nv_mfma_f32_16x16x32_bf16 v[16:19], a[228:231], v[40:43], v[16:19]\nv_mfma_f32_16x16x32_bf16 v[20:23], a[244:247], v[40:43], v[20:23]\ns_waitcnt lgkmcnt(6)\nds_read_b128 v[40:43], v2 offset:28672\nv_mfma_f32_16x16x32_bf16 v[24:27], a[228:231], v[44:47], v[24:27]\nbuffer_load_dwordx4 v0, s[36:39], s58 offen offset:3072 sc1 lds\n" \
"v_mfma_f32_16x16x32_bf16 v[28:31], a[244:247], v[44:47], v[28:31]\ns_waitcnt lgkmcnt(6)\nds_read_b128 v[44:47], v2 offset:28928\nv_mfma_f32_16x16x32_bf16 v[16:19], v[96:99], v[64:67], v[16:19]\nv_mfma_f32_16x16x32_bf16 v[20:23], v[112:115], v[64:67], v[20:23]\ns_waitcnt lgkmcnt(6)\nds_read_b128 v[64:67], v2 offset:30720\nv_mfma_f32_16x16x32_bf16 v[24:27], v[96:99], v[68:71], v[24:27]\n" \
"v_mfma_f32_16x16x32_bf16 v[28:31], v[112:115], v[68:71], v[28:31]\ns_waitcnt lgkmcnt(4)\nds_read_b128 v[68:71], v2 offset:30976\nv_mfma_f32_16x16x32_bf16 v[16:19], v[100:103], v[32:35], v[16:19]\nv_mfma_f32_16x16x32_bf16 v[20:23], v[116:119], v[32:35], v[20:23]\ns_waitcnt lgkmcnt(4)\nv_mfma_f32_16x16x32_bf16 v[24:27], v[100:103], v[36:39], v[24:27]\nv_mfma_f32_16x16x32_bf16 v[28:31], v[116:119], v[36:39], v[28:31]\n" \
"s_waitcnt lgkmcnt(0)\ns_waitcnt vmcnt(5)\ns_barrier\nds_read_b128 v[32:35], v2 offset:0\nds_read_b128 v[36:39], v2 offset:2048\nv_mfma_f32_16x16x32_bf16 v[16:19], v[128:131], v[40:43], v[16:19]\ns_add_u32 m0, s57, 16384\ns_nop 0\n" \
"buffer_load_dwordx4 v3, s[36:39], s42 offen offset:0 sc1 lds\nv_mfma_f32_16x16x32_bf16 v[20:23], v[56:59], v[40:43], v[20:23]\nds_read_b128 v[40:43], v2 offset:4096\nv_mfma_f32_16x16x32_bf16 v[24:27], v[128:131], v[44:47], v[24:27]\nv_mfma_f32_16x16x32_bf16 v[28:31], v[56:59], v[44:47], v[28:31]\nds_read_b128 v[44:47], v2 offset:6144\nv_mfma_f32_16x16x32_bf16 v[16:19], v[132:135], v[64:67], v[16:19]\nbuffer_load_dwordx4 v3, s[36:39], s42 offen offset:1024 sc1 lds\n" \
"v_mfma_f32_16x16x32_bf16 v[20:23], v[60:63], v[64:67], v[20:23]\nds_read_b128 v[64:67], v2 offset:8192\nv_mfma_f32_16x16x32_bf16 v[24:27], v[132:135], v[68:71], v[24:27]\nv_mfma_f32_16x16x32_bf16 v[28:31], v[60:63], v[68:71], v[28:31]\ns_waitcnt lgkmcnt(4)\nds_read_b128 v[68:71], v2 offset:10240\nv_mfma_f32_16x16x32_bf16 v[16:19], v[144:147], v[32:35], v[16:19]\nbuffer_load_dwordx4 v3, s[36:39], s42 offen offset:2048 sc1 lds\n" \
"v_mfma_f32_16x16x32_bf16 v[20:23], v[160:163], v[32:35], v[20:23]\ns_waitcnt lgkmcnt(4)\nds_read_b128 v[32:35], v2 offset:12288\nv_mfma_f32_16x16x32_bf16 v[16:19], v[148:151], v[36:39], v[16:19]\nv_mfma_f32_16x16x32_bf16 v[20:23], v[164:167], v[36:39], v[20:23]\ns_waitcnt lgkmcnt(4)\nds_read_b128 v[36:39], v2 offset:14336\nv_mfma_f32_16x16x32_bf16 v[16:19], v[168:171], v[40:43], v[16:19]\n" \
"buffer_load_dwordx4 v3, s[36:39], s42 offen offset:3072 sc1 lds\nv_add_u32 v14, s57, v0\nv_mfma_f32_16x16x32_bf16 v[20:23], v[184:187], v[40:43], v[20:23]\ns_waitcnt lgkmcnt(4)\nds_read_b128 v[40:43], v2 offset:256\nv_mfma_f32_16x16x32_bf16 v[16:19], v[172:175], v[44:47], v[16:19]\nv_mov_b64 v[48:49], 0\nv_mfma_f32_16x16x32_bf16 v[20:23], v[188:191], v[44:47], v[20:23]\n" \
"s_waitcnt lgkmcnt(4)\nds_read_b128 v[44:47], v2 offset:2304\nv_mov_b64 v[50:51], 0\nv_mfma_f32_16x16x32_bf16 v[16:19], v[200:203], v[64:67], v[16:19]\nv_mov_b64 v[52:53], 0\nv_mfma_f32_16x16x32_bf16 v[20:23], v[216:219], v[64:67], v[20:23]\ns_waitcnt lgkmcnt(4)\nds_read_b128 v[64:67], v2 offset:4352\n" \
"v_mov_b64 v[54:55], 0\nv_mfma_f32_16x16x32_bf16 v[16:19], v[204:207], v[68:71], v[16:19]\nds_read_b128 v[56:59], v1 offset:0\nds_read_b128 v[60:63], v1 offset:1024\nv_mfma_f32_16x16x32_bf16 v[20:23], v[220:223], v[68:71], v[20:23]\ns_waitcnt lgkmcnt(6)\nds_read_b128 v[68:71], v2 offset:6400\nv_mfma_f32_16x16x32_bf16 v[16:19], v[232:235], v[32:35], v[16:19]\n" \
"s_waitcnt vmcnt(0)\nds_read_b128 v[80:83], v14 offset:16384\nds_read_b128 v[84:87], v14 offset:17408\nv_mfma_f32_16x16x32_bf16 v[20:23], v[240:243], v[32:35], v[20:23]\ns_waitcnt lgkmcnt(8)\nds_read_b128 v[32:35], v2 offset:8448\nv_mfma_f32_16x16x32_bf16 v[16:19], v[236:239], v[36:39], v[16:19]\nds_read_b128 v[88:91], v14 offset:18432\n" \
"ds_read_b128 v[92:95], v14 offset:19456\nv_mfma_f32_16x16x32_bf16 v[20:23], v[244:247], v[36:39], v[20:23]\ns_waitcnt lgkmcnt(10)\nds_read_b128 v[36:39], v2 offset:10496\nv_mfma_f32_16x16x32_bf16 v[24:27], v[144:147], v[40:43], v[24:27]\nv_mfma_f32_16x16x32_bf16 v[28:31], v[160:163], v[40:43], v[28:31]\ns_waitcnt lgkmcnt(10)\nds_read_b128 v[40:43], v2 offset:12544\n" \
"v_cvt_pk_bf16_f32 v72, v16, v17\nv_mfma_f32_16x16x32_bf16 v[24:27], v[148:151], v[44:47], v[24:27]\nv_cvt_pk_bf16_f32 v73, v18, v19\nv_mfma_f32_16x16x32_bf16 v[28:31], v[164:167], v[44:47], v[28:31]\ns_waitcnt lgkmcnt(10)\nds_read_b128 v[44:47], v2 offset:14592\nv_cvt_pk_bf16_f32 v74, v20, v21\nv_mfma_f32_16x16x32_bf16 v[24:27], v[168:171], v[64:67], v[24:27]\n" \
"s_waitcnt lgkmcnt(6)\nv_max3_u32 v76, v80, v82, v84\nv_cvt_pk_bf16_f32 v75, v22, v23\nv_mfma_f32_16x16x32_bf16 v[28:31], v[184:187], v[64:67], v[28:31]\ns_waitcnt lgkmcnt(4)\nds_write2st64_b64 v9, v[72:73], v[74:75] offset0:0 offset1:8\nv_max3_u32 v76, v76, v86, v88\nv_mfma_f32_16x16x32_bf16 v[24:27], v[172:175], v[68:71], v[24:27]\n" \
"s_waitcnt lgkmcnt(4)\nv_max3_u32 v76, v76, v90, v92\nv_max_u32 v76, v76, v94\nv_mfma_f32_16x16x32_bf16 v[28:31], v[188:191], v[68:71], v[28:31]\nv_cmp_eq_u32 vcc, -1, v76\nv_mfma_f32_16x16x32_bf16 v[24:27], v[200:203], v[32:35], v[24:27]\ns_cbranch_vccnz .Lvu_re%=\n.Lvu_rer%=:\n" \
"s_barrier\ns_memrealtime s[68:69]\nds_read_b128 v[80:83], v0 offset:16384\nv_mfma_f32_16x16x32_bf16 v[28:31], v[216:219], v[32:35], v[28:31]\ns_waitcnt lgkmcnt(4)\nds_read_b128 v[84:87], v0 offset:17408\nv_mfma_f32_16x16x32_bf16 v[24:27], v[204:207], v[36:39], v[24:27]\nds_read_b128 v[88:91], v0 offset:18432\n" \
"v_mfma_f32_16x16x32_bf16 v[28:31], v[220:223], v[36:39], v[28:31]\ns_waitcnt lgkmcnt(5)\nds_read_b128 v[92:95], v0 offset:19456\nv_mfma_f32_16x16x32_bf16 v[24:27], v[232:235], v[40:43], v[24:27]\nds_read_b128 v[16:19], v0 offset:20480\nv_mfma_f32_16x16x32_bf16 v[28:31], v[240:243], v[40:43], v[28:31]\ns_waitcnt lgkmcnt(6)\nds_read_b128 v[20:23], v0 offset:21504\n" \
"v_mfma_f32_16x16x32_bf16 v[24:27], v[236:239], v[44:47], v[24:27]\nds_read_b128 v[32:35], v0 offset:22528\nv_mfma_f32_16x16x32_bf16 v[28:31], v[244:247], v[44:47], v[28:31]\ns_cmp_lt_i32 s40, 0\nds_read_b128 v[36:39], v0 offset:23552\ns_cbranch_scc1 .Lvu_first%=\n.Lvu_crit%=:\ns_waitcnt lgkmcnt(7)\n" \
"v_smfmac_f32_16x16x64_bf16 v[48:51], v[80:83], a[0:7], v4\ns_waitcnt lgkmcnt(6)\nds_read_b128 v[64:67], v1 offset:4096\nds_read_b128 v[40:43], v0 offset:24576\nv_mov_b32 v14, s40\nv_smfmac_f32_16x16x64_bf16 v[48:51], v[84:87], a[24:31], v4\ns_waitcnt lgkmcnt(7)\nds_read_b128 v[68:71], v1 offset:5120\n" \
"ds_read_b128 v[44:47], v0 offset:25600\nv_cvt_pk_bf16_f32 v76, v24, v25\nv_smfmac_f32_16x16x64_bf16 v[48:51], v[88:91], v[56:63], v4\ns_sub_u32 s88, s86, s89\ns_min_u32 s88, s88, 150\ns_waitcnt lgkmcnt(8)\nds_read_b128 v[80:83], v0 offset:28672\nv_cvt_pk_bf16_f32 v77, v26, v27\n" \
"v_smfmac_f32_16x16x64_bf16 v[48:51], v[92:95], a[80:87], v4\ns_waitcnt lgkmcnt(8)\nds_read_b128 v[84:87], v0 offset:29696\nv_cvt_pk_bf16_f32 v78, v28, v29\nv_smfmac_f32_16x16x64_bf16 v[48:51], v[16:19], a[104:111], v4\ns_add_u32 s88, s88, s68\ns_mov_b32 s89, s68\ns_waitcnt lgkmcnt(8)\n" \
"ds_read_b128 v[88:91], v0 offset:30720\nv_cvt_pk_bf16_f32 v79, v30, v31\nv_smfmac_f32_16x16x64_bf16 v[48:51], v[20:23], a[128:135], v4\ns_waitcnt lgkmcnt(8)\nds_read_b128 v[92:95], v0 offset:31744\nv_mov_b32 v15, s88\nv_smfmac_f32_16x16x64_bf16 v[48:51], v[32:35], a[152:159], v4\ns_waitcnt lgkmcnt(8)\n" \
"ds_read_b128 v[24:27], v0 offset:26624\nbuffer_store_dwordx2 v[14:15], off, s[36:39], %[sg] sc1\nv_smfmac_f32_16x16x64_bf16 v[48:51], v[36:39], a[184:191], v4\ns_waitcnt lgkmcnt(7)\ns_waitcnt lgkmcnt(6)\nds_read_b128 v[28:31], v0 offset:27648\nv_smfmac_f32_16x16x64_bf16 v[48:51], v[40:43], v[64:71], v4\ns_waitcnt lgkmcnt(6)\n" \
"ds_read_b128 v[56:59], v1 offset:2048\nv_smfmac_f32_16x16x64_bf16 v[48:51], v[44:47], a[224:231], v4\ns_waitcnt lgkmcnt(6)\nds_read_b128 v[60:63], v1 offset:3072\nv_smfmac_f32_16x16x64_bf16 v[48:51], v[80:83], v[144:151], v4\ns_waitcnt lgkmcnt(6)\nds_write2st64_b64 v9, v[76:77], v[78:79] offset0:2 offset1:10\nv_smfmac_f32_16x16x64_bf16 v[48:51], v[84:87], v[168:175], v4\n" \
"s_waitcnt lgkmcnt(6)\nv_smfmac_f32_16x16x64_bf16 v[48:51], v[88:91], v[200:207], v4\ns_waitcnt lgkmcnt(5)\nv_smfmac_f32_16x16x64_bf16 v[48:51], v[92:95], v[232:239], v4\ns_waitcnt lgkmcnt(4)\nv_smfmac_f32_16x16x64_bf16 v[48:51], v[24:27], v[96:103], v4\ns_waitcnt lgkmcnt(3)\nds_read_b128 v[64:67], v1 offset:6144\n" \
"v_smfmac_f32_16x16x64_bf16 v[48:51], v[28:31], v[128:135], v4\nds_read_b128 v[68:71], v1 offset:7168\nv_smfmac_f32_16x16x64_bf16 v[52:55], v[80:83], v[160:167], v4\nv_smfmac_f32_16x16x64_bf16 v[52:55], v[84:87], v[184:191], v4\nv_add_f32 v48, v48, v50\nv_smfmac_f32_16x16x64_bf16 v[52:55], v[88:91], v[216:223], v4\nds_read_b128 v[80:83], v0 offset:16384\nv_add_f32 v49, v49, v51\n" \
"v_smfmac_f32_16x16x64_bf16 v[52:55], v[92:95], v[240:247], v4\nds_read_b128 v[84:87], v0 offset:17408\nv_cvt_pk_bf16_f32 v14, v48, v49\nv_smfmac_f32_16x16x64_bf16 v[52:55], v[16:19], a[120:127], v4\ns_waitcnt lgkmcnt(5)\nds_write_b16 v5, v14 offset:0\nds_write_b16_d16_hi v5, v14 offset:16\nv_smfmac_f32_16x16x64_bf16 v[52:55], v[20:23], v[56:63], v4\n" \
"ds_read_b128 v[88:91], v0 offset:18432\nv_mov_b64 v[16:17], 0\nv_smfmac_f32_16x16x64_bf16 v[52:55], v[24:27], v[112:119], v4\ns_waitcnt lgkmcnt(5)\nds_read_b128 v[92:95], v0 offset:19456\nv_mov_b64 v[18:19], 0\nv_smfmac_f32_16x16x64_bf16 v[52:55], v[28:31], v[64:71], v4\nds_read_b128 v[56:59], v1 offset:8192\n" \
"v_mov_b64 v[20:21], 0\nv_smfmac_f32_16x16x64_bf16 v[52:55], v[32:35], a[168:175], v4\ns_waitcnt lgkmcnt(3)\nds_read_b128 v[60:63], v1 offset:9216\ns_barrier\nds_read_b128 v[72:75], v11 offset:57344\nv_smfmac_f32_16x16x64_bf16 v[52:55], v[36:39], a[192:199], v4\nds_read_b128 v[76:79], v11 offset:58368\n" \
"v_mov_b64 v[22:23], 0\nv_smfmac_f32_16x16x64_bf16 v[52:55], v[40:43], a[216:223], v4\nv_mov_b64 v[24:25], 0\nv_smfmac_f32_16x16x64_bf16 v[52:55], v[44:47], a[240:247], v4\nds_read_b128 v[64:67], v1 offset:12288\nv_mov_b64 v[26:27], 0\nv_smfmac_f32_16x16x64_bf16 v[52:55], v[80:83], a[16:23], v4\nds_read_b128 v[68:71], v1 offset:13312\n" \
"v_mov_b64 v[28:29], 0\nv_smfmac_f32_16x16x64_bf16 v[52:55], v[84:87], a[40:47], v4\ns_waitcnt lgkmcnt(7)\nv_mov_b64 v[30:31], 0\nv_smfmac_f32_16x16x64_bf16 v[52:55], v[88:91], a[64:71], v4\ns_waitcnt lgkmcnt(6)\nv_mov_b64 v[32:33], 0\nv_smfmac_f32_16x16x64_bf16 v[52:55], v[92:95], a[88:95], v4\n" \
"s_waitcnt lgkmcnt(3)\nv_mov_b64 v[34:35], 0\nv_smfmac_f32_16x16x64_bf16 v[16:19], v[72:75], a[8:15], v10\ns_waitcnt lgkmcnt(2)\nv_mov_b64 v[36:37], 0\nv_smfmac_f32_16x16x64_bf16 v[16:19], v[76:79], v[56:63], v10\nds_read_b128 v[56:59], v1 offset:10240\nv_add_f32 v52, v52, v54\n" \
"v_smfmac_f32_16x16x64_bf16 v[20:23], v[72:75], a[56:63], v10\nds_read_b128 v[60:63], v1 offset:11264\nv_add_f32 v53, v53, v55\nv_smfmac_f32_16x16x64_bf16 v[20:23], v[76:79], a[72:79], v10\nv_cvt_pk_bf16_f32 v14, v52, v53\nv_smfmac_f32_16x16x64_bf16 v[24:27], v[72:75], a[112:119], v10\ns_waitcnt lgkmcnt(2)\nds_write_b16 v5, v14 offset:2048\n" \
"ds_write_b16_d16_hi v5, v14 offset:2064\nv_smfmac_f32_16x16x64_bf16 v[24:27], v[76:79], v[64:71], v10\nds_read_b128 v[64:67], v1 offset:14336\nv_mov_b64 v[38:39], 0\nv_smfmac_f32_16x16x64_bf16 v[28:31], v[72:75], a[160:167], v10\nds_read_b128 v[68:71], v1 offset:15360\nv_mov_b64 v[40:41], 0\nv_smfmac_f32_16x16x64_bf16 v[28:31], v[76:79], a[176:183], v10\n" \
"v_mov_b64 v[42:43], 0\nv_smfmac_f32_16x16x64_bf16 v[32:35], v[72:75], a[208:215], v10\ns_waitcnt lgkmcnt(2)\ns_barrier\ns_mov_b32 s59, 0\nds_read_b128 v[80:83], v11 offset:59392\nv_smfmac_f32_16x16x64_bf16 v[32:35], v[76:79], v[56:63], v10\ns_add_u32 m0, s57, 0\n" \
"s_add_u32 s58, s61, 0\nds_read_b128 v[56:59], v1 offset:16384\nbuffer_load_dwordx4 v0, s[36:39], s58 offen offset:0 sc1 lds\nds_read_b128 v[84:87], v11 offset:60416\nv_smfmac_f32_16x16x64_bf16 v[36:39], v[72:75], v[104:111], v10\nds_read_b128 v[60:63], v1 offset:17408\nv_mov_b64 v[44:45], 0\nv_smfmac_f32_16x16x64_bf16 v[36:39], v[76:79], v[120:127], v10\n" \
"v_mov_b64 v[46:47], 0\nv_smfmac_f32_16x16x64_bf16 v[40:43], v[72:75], v[152:159], v10\ns_waitcnt lgkmcnt(4)\nbuffer_load_dwordx4 v0, s[36:39], s58 offen offset:1024 sc1 lds\nv_smfmac_f32_16x16x64_bf16 v[40:43], v[76:79], v[64:71], v10\nds_read_b128 v[64:67], v1 offset:20480\nv_mov_b64 v[48:49], 0\nv_smfmac_f32_16x16x64_bf16 v[44:47], v[72:75], v[208:215], v10\n" \
"ds_read_b128 v[68:71], v1 offset:21504\nv_mov_b64 v[50:51], 0\nv_smfmac_f32_16x16x64_bf16 v[44:47], v[76:79], v[224:231], v10\ns_waitcnt lgkmcnt(5)\nbuffer_load_dwordx4 v0, s[36:39], s58 offen offset:2048 sc1 lds\nv_smfmac_f32_16x16x64_bf16 v[16:19], v[80:83], a[32:39], v10\ns_waitcnt lgkmcnt(3)\nds_read_b128 v[72:75], v11 offset:49152\n" \
"v_mov_b64 v[52:53], 0\nv_smfmac_f32_16x16x64_bf16 v[16:19], v[84:87], a[48:55], v10\ns_waitcnt lgkmcnt(3)\nds_read_b128 v[76:79], v11 offset:50176\nv_mov_b64 v[54:55], 0\nv_smfmac_f32_16x16x64_bf16 v[20:23], v[80:83], v[56:63], v10\nds_read_b128 v[56:59], v1 offset:18432\nbuffer_load_dwordx4 v0, s[36:39], s58 offen offset:3072 sc1 lds\n" \
"v_smfmac_f32_16x16x64_bf16 v[20:23], v[84:87], a[96:103], v10\nds_read_b128 v[60:63], v1 offset:19456\nv_cvt_pk_bf16_f32 v88, v16, v17\nv_smfmac_f32_16x16x64_bf16 v[24:27], v[80:83], a[136:143], v10\nv_and_or_b32 v88, v88, -2, v12\nv_cvt_pk_bf16_f32 v89, v18, v19\nv_smfmac_f32_16x16x64_bf16 v[24:27], v[84:87], a[144:151], v10\ns_waitcnt lgkmcnt(4)\n" \
"v_cvt_pk_bf16_f32 v90, v20, v21\nv_cvt_pk_bf16_f32 v91, v22, v23\nv_and_or_b32 v90, v90, -2, v12\nv_smfmac_f32_16x16x64_bf16 v[28:31], v[80:83], v[64:71], v10\nds_read_b128 v[64:67], v1 offset:22528\nv_permlane16_swap_b32 v89, v91\nv_smfmac_f32_16x16x64_bf16 v[28:31], v[84:87], a[200:207], v10\nds_read_b128 v[68:71], v1 offset:23552\n" \
"v_permlane16_swap_b32 v88, v90\nv_smfmac_f32_16x16x64_bf16 v[32:35], v[80:83], a[232:239], v10\ns_add_u32 s63, s59, 0\nds_read_b128 v[16:19], v11 offset:51200\nv_cvt_pk_bf16_f32 v92, v24, v25\nv_and_or_b32 v92, v92, -2, v12\nv_smfmac_f32_16x16x64_bf16 v[32:35], v[84:87], a[248:255], v10\ns_waitcnt lgkmcnt(3)\n" \
"ds_read_b128 v[20:23], v11 offset:52224\nbuffer_store_dwordx4 v[88:91], v6, s[36:39], s63 offen sc1\nv_cvt_pk_bf16_f32 v93, v26, v27\nv_cvt_pk_bf16_f32 v94, v28, v29\nv_cvt_pk_bf16_f32 v95, v30, v31\nv_and_or_b32 v94, v94, -2, v12\nv_smfmac_f32_16x16x64_bf16 v[36:39], v[80:83], v[56:63], v10\nds_read_b128 v[56:59], v1 offset:8192\n" \
"v_permlane16_swap_b32 v93, v95\nv_smfmac_f32_16x16x64_bf16 v[36:39], v[84:87], v[136:143], v10\nds_read_b128 v[60:63], v1 offset:9216\nv_permlane16_swap_b32 v92, v94\nv_smfmac_f32_16x16x64_bf16 v[40:43], v[80:83], v[176:183], v10\ns_add_u32 s63, s59, 131072\nds_read_b128 v[24:27], v11 offset:53248\nv_cvt_pk_bf16_f32 v88, v32, v33\n" \
"v_and_or_b32 v88, v88, -2, v12\nv_smfmac_f32_16x16x64_bf16 v[40:43], v[84:87], v[192:199], v10\ns_waitcnt lgkmcnt(5)\nds_read_b128 v[28:31], v11 offset:54272\nbuffer_store_dwordx4 v[92:95], v6, s[36:39], s63 offen sc1\nv_cvt_pk_bf16_f32 v89, v34, v35\nv_cvt_pk_bf16_f32 v90, v36, v37\nv_cvt_pk_bf16_f32 v91, v38, v39\n" \
"v_and_or_b32 v90, v90, -2, v12\nv_smfmac_f32_16x16x64_bf16 v[44:47], v[80:83], v[64:71], v10\nds_read_b128 v[64:67], v1 offset:16384\nv_permlane16_swap_b32 v89, v91\nv_smfmac_f32_16x16x64_bf16 v[44:47], v[84:87], v[248:255], v10\ns_cmp_ge_i32 s40, 63\nds_read_b128 v[68:71], v1 offset:17408\nv_permlane16_swap_b32 v88, v90\n" \
"s_cbranch_scc1 .Lvu_last%=\n.Lvu_y%=:\nv_smfmac_f32_16x16x64_bf16 v[48:51], v[72:75], a[8:15], v10\ns_add_u32 s63, s59, 4096\nds_read_b128 v[32:35], v11 offset:55296\nds_read_b128 v[36:39], v11 offset:56320\nv_cvt_pk_bf16_f32 v92, v40, v41\nv_and_or_b32 v92, v92, -2, v12\n" \
"v_smfmac_f32_16x16x64_bf16 v[52:55], v[76:79], a[8:15], v10\ns_waitcnt lgkmcnt(9)\ns_waitcnt lgkmcnt(6)\nbuffer_store_dwordx4 v[88:91], v6, s[36:39], s63 offen sc1\nv_cvt_pk_bf16_f32 v93, v42, v43\nv_cvt_pk_bf16_f32 v94, v44, v45\nv_cvt_pk_bf16_f32 v95, v46, v47\nv_and_or_b32 v94, v94, -2, v12\n" \
"v_smfmac_f32_16x16x64_bf16 v[48:51], v[16:19], v[56:63], v10\ns_add_u32 s40, s40, 1\ns_and_b32 s44, s40, 1\nv_permlane16_swap_b32 v93, v95\nv_smfmac_f32_16x16x64_bf16 v[52:55], v[20:23], v[56:63], v10\ns_lshl_b32 s55, s44, 22\ns_waitcnt lgkmcnt(5)\nv_permlane16_swap_b32 v92, v94\n" \
"v_smfmac_f32_16x16x64_bf16 v[48:51], v[24:27], a[32:39], v10\ns_waitcnt lgkmcnt(4)\ns_add_u32 s63, s59, 135168\nds_read_b128 v[56:59], v1 offset:12288\nv_smfmac_f32_16x16x64_bf16 v[52:55], v[28:31], a[32:39], v10\ns_waitcnt lgkmcnt(2)\nds_read_b128 v[60:63], v1 offset:13312\nbuffer_store_dwordx4 v[92:95], v6, s[36:39], s63 offen sc1\n" \
"v_mov_b64 v[80:81], 0\nv_mov_b64 v[82:83], 0\nv_mov_b64 v[84:85], 0\nv_mov_b64 v[86:87], 0\nv_smfmac_f32_16x16x64_bf16 v[48:51], v[32:35], a[48:55], v10\ns_memrealtime s[86:87]\ns_bfe_u32 s45, s40, 0x10001\ns_waitcnt lgkmcnt(2)\n" \
"v_smfmac_f32_16x16x64_bf16 v[52:55], v[36:39], a[48:55], v10\nv_mov_b32 v13, s45\nv_smfmac_f32_16x16x64_bf16 v[80:83], v[72:75], a[56:63], v10\nv_cvt_pk_bf16_f32 v88, v48, v49\nv_smfmac_f32_16x16x64_bf16 v[84:87], v[76:79], a[56:63], v10\nv_cvt_pk_bf16_f32 v89, v50, v51\nv_smfmac_f32_16x16x64_bf16 v[80:83], v[16:19], a[72:79], v10\nv_cvt_pk_bf16_f32 v90, v52, v53\n" \
"v_smfmac_f32_16x16x64_bf16 v[84:87], v[20:23], a[72:79], v10\ns_add_u32 s60, s55, 0\nv_cvt_pk_bf16_f32 v91, v54, v55\nv_smfmac_f32_16x16x64_bf16 v[80:83], v[24:27], v[64:71], v10\nv_and_or_b32 v88, v88, -2, v13\nv_smfmac_f32_16x16x64_bf16 v[84:87], v[28:31], v[64:71], v10\ns_and_b32 s42, s40, 3\ns_lshl_b32 s42, s42, 15\n" \
"v_and_or_b32 v90, v90, -2, v13\nv_smfmac_f32_16x16x64_bf16 v[80:83], v[32:35], a[96:103], v10\nbuffer_store_dwordx4 v[88:91], v7, s[36:39], s60 offen offset:0 sc1\nv_mov_b64 v[48:49], 0\nv_mov_b64 v[50:51], 0\nv_mov_b64 v[52:53], 0\nv_mov_b32 v12, s44\nv_smfmac_f32_16x16x64_bf16 v[84:87], v[36:39], a[96:103], v10\n" \
"ds_read_b128 v[64:67], v1 offset:20480\nv_mov_b64 v[54:55], 0\nv_smfmac_f32_16x16x64_bf16 v[48:51], v[72:75], a[112:119], v10\nds_read_b128 v[68:71], v1 offset:21504\nv_cvt_pk_bf16_f32 v92, v80, v81\nv_smfmac_f32_16x16x64_bf16 v[52:55], v[76:79], a[112:119], v10\ns_waitcnt lgkmcnt(2)\nv_cvt_pk_bf16_f32 v93, v82, v83\n" \
"v_smfmac_f32_16x16x64_bf16 v[48:51], v[16:19], v[56:63], v10\nv_cvt_pk_bf16_f32 v94, v84, v85\nv_smfmac_f32_16x16x64_bf16 v[52:55], v[20:23], v[56:63], v10\ns_add_u32 s60, s55, 4096\nv_cvt_pk_bf16_f32 v95, v86, v87\nv_smfmac_f32_16x16x64_bf16 v[48:51], v[24:27], a[136:143], v10\nds_read_b128 v[56:59], v1 offset:10240\nv_and_or_b32 v92, v92, -2, v13\n" \
"v_smfmac_f32_16x16x64_bf16 v[52:55], v[28:31], a[136:143], v10\nds_read_b128 v[60:63], v1 offset:11264\nv_and_or_b32 v94, v94, -2, v13\nv_smfmac_f32_16x16x64_bf16 v[48:51], v[32:35], a[144:151], v10\ns_waitcnt vmcnt(5)\nbuffer_store_dwordx4 v[92:95], v7, s[36:39], s60 offen offset:0 sc1\nv_mov_b64 v[80:81], 0\nv_mov_b64 v[82:83], 0\n" \
"v_mov_b64 v[84:85], 0\ns_barrier\nv_smfmac_f32_16x16x64_bf16 v[52:55], v[36:39], a[144:151], v10\nv_mov_b64 v[86:87], 0\nv_smfmac_f32_16x16x64_bf16 v[80:83], v[72:75], a[160:167], v10\nv_cvt_pk_bf16_f32 v88, v48, v49\nv_smfmac_f32_16x16x64_bf16 v[84:87], v[76:79], a[160:167], v10\nv_cvt_pk_bf16_f32 v89, v50, v51\n" \
"v_smfmac_f32_16x16x64_bf16 v[80:83], v[16:19], a[176:183], v10\ns_cmp_lt_u32 s40, 2\ns_cselect_b32 s54, %[fy], -2147483648\nv_cvt_pk_bf16_f32 v90, v52, v53\nv_smfmac_f32_16x16x64_bf16 v[84:87], v[20:23], a[176:183], v10\ns_add_u32 s60, s55, 16384\ns_waitcnt lgkmcnt(2)\nv_cvt_pk_bf16_f32 v91, v54, v55\n" \
"v_smfmac_f32_16x16x64_bf16 v[80:83], v[24:27], v[64:71], v10\nv_and_or_b32 v88, v88, -2, v13\nv_smfmac_f32_16x16x64_bf16 v[84:87], v[28:31], v[64:71], v10\nv_and_or_b32 v90, v90, -2, v13\nv_smfmac_f32_16x16x64_bf16 v[80:83], v[32:35], a[200:207], v10\nbuffer_store_dwordx4 v[88:91], v7, s[36:39], s60 offen offset:0 sc1\nv_mov_b64 v[48:49], 0\nv_mov_b64 v[50:51], 0\n" \
"v_mov_b64 v[52:53], 0\nv_smfmac_f32_16x16x64_bf16 v[84:87], v[36:39], a[200:207], v10\nds_read_b128 v[64:67], v1 offset:18432\nv_mov_b64 v[54:55], 0\nv_smfmac_f32_16x16x64_bf16 v[48:51], v[72:75], a[208:215], v10\nds_read_b128 v[68:71], v1 offset:19456\nv_cvt_pk_bf16_f32 v92, v80, v81\nv_smfmac_f32_16x16x64_bf16 v[52:55], v[76:79], a[208:215], v10\n" \
"s_waitcnt lgkmcnt(2)\nv_cvt_pk_bf16_f32 v93, v82, v83\nv_smfmac_f32_16x16x64_bf16 v[48:51], v[16:19], v[56:63], v10\nv_cvt_pk_bf16_f32 v94, v84, v85\nv_smfmac_f32_16x16x64_bf16 v[52:55], v[20:23], v[56:63], v10\ns_add_u32 s60, s55, 20480\nv_cvt_pk_bf16_f32 v95, v86, v87\nv_smfmac_f32_16x16x64_bf16 v[48:51], v[24:27], a[232:239], v10\n" \
"ds_read_b128 v[56:59], v1 offset:14336\nv_and_or_b32 v92, v92, -2, v13\nv_smfmac_f32_16x16x64_bf16 v[52:55], v[28:31], a[232:239], v10\nds_read_b128 v[60:63], v1 offset:15360\nv_and_or_b32 v94, v94, -2, v13\nv_smfmac_f32_16x16x64_bf16 v[48:51], v[32:35], a[248:255], v10\nbuffer_store_dwordx4 v[92:95], v7, s[36:39], s60 offen offset:0 sc1\nv_mov_b64 v[80:81], 0\n" \
"v_mov_b64 v[82:83], 0\nv_mov_b64 v[84:85], 0\nv_smfmac_f32_16x16x64_bf16 v[52:55], v[36:39], a[248:255], v10\nv_mov_b64 v[86:87], 0\nv_smfmac_f32_16x16x64_bf16 v[80:83], v[72:75], v[104:111], v10\nv_cvt_pk_bf16_f32 v88, v48, v49\nv_smfmac_f32_16x16x64_bf16 v[84:87], v[76:79], v[104:111], v10\nv_cvt_pk_bf16_f32 v89, v50, v51\n" \
"v_smfmac_f32_16x16x64_bf16 v[80:83], v[16:19], v[120:127], v10\nv_cvt_pk_bf16_f32 v90, v52, v53\nv_smfmac_f32_16x16x64_bf16 v[84:87], v[20:23], v[120:127], v10\ns_add_u32 s60, s55, 0\ns_waitcnt lgkmcnt(2)\nv_cvt_pk_bf16_f32 v91, v54, v55\nv_smfmac_f32_16x16x64_bf16 v[80:83], v[24:27], v[64:71], v10\nv_and_or_b32 v88, v88, -2, v13\n" \
"v_smfmac_f32_16x16x64_bf16 v[84:87], v[28:31], v[64:71], v10\nv_and_or_b32 v90, v90, -2, v13\nv_smfmac_f32_16x16x64_bf16 v[80:83], v[32:35], v[136:143], v10\nbuffer_store_dwordx4 v[88:91], v7, s[36:39], s60 offen offset:2048 sc1\nv_xor_b32 v11, 128, v11\nv_mov_b64 v[48:49], 0\nv_mov_b64 v[50:51], 0\nv_mov_b64 v[52:53], 0\n" \
"v_smfmac_f32_16x16x64_bf16 v[84:87], v[36:39], v[136:143], v10\nds_read_b128 v[64:67], v1 offset:22528\nv_mov_b64 v[54:55], 0\nv_smfmac_f32_16x16x64_bf16 v[48:51], v[72:75], v[152:159], v10\nds_read_b128 v[68:71], v1 offset:23552\nv_cvt_pk_bf16_f32 v92, v80, v81\nv_smfmac_f32_16x16x64_bf16 v[52:55], v[76:79], v[152:159], v10\ns_waitcnt lgkmcnt(2)\n" \
"ds_read_b128 v[40:43], v11 offset:49152\nv_cvt_pk_bf16_f32 v93, v82, v83\nv_smfmac_f32_16x16x64_bf16 v[48:51], v[16:19], v[56:63], v10\nds_read_b128 v[44:47], v11 offset:50176\nv_cvt_pk_bf16_f32 v94, v84, v85\nv_smfmac_f32_16x16x64_bf16 v[52:55], v[20:23], v[56:63], v10\ns_add_u32 s60, s55, 4096\nv_cvt_pk_bf16_f32 v95, v86, v87\n" \
"v_smfmac_f32_16x16x64_bf16 v[48:51], v[24:27], v[176:183], v10\nds_read_b128 v[56:59], v1 offset:8192\nv_and_or_b32 v92, v92, -2, v13\nv_smfmac_f32_16x16x64_bf16 v[52:55], v[28:31], v[176:183], v10\nds_read_b128 v[60:63], v1 offset:9216\nv_and_or_b32 v94, v94, -2, v13\nv_smfmac_f32_16x16x64_bf16 v[48:51], v[32:35], v[192:199], v10\nbuffer_store_dwordx4 v[92:95], v7, s[36:39], s60 offen offset:2048 sc1\n" \
"v_mov_b64 v[80:81], 0\nv_mov_b64 v[82:83], 0\nv_mov_b64 v[84:85], 0\nv_smfmac_f32_16x16x64_bf16 v[52:55], v[36:39], v[192:199], v10\nv_mov_b64 v[86:87], 0\nv_smfmac_f32_16x16x64_bf16 v[80:83], v[72:75], v[208:215], v10\nds_read_b128 v[72:75], v11 offset:51200\nv_cvt_pk_bf16_f32 v88, v48, v49\n" \
"v_smfmac_f32_16x16x64_bf16 v[84:87], v[76:79], v[208:215], v10\nds_read_b128 v[76:79], v11 offset:52224\nv_cvt_pk_bf16_f32 v89, v50, v51\nv_smfmac_f32_16x16x64_bf16 v[80:83], v[16:19], v[224:231], v10\ns_add_u32 s60, s55, 16384\nds_read_b128 v[16:19], v11 offset:53248\nv_cvt_pk_bf16_f32 v90, v52, v53\nv_smfmac_f32_16x16x64_bf16 v[84:87], v[20:23], v[224:231], v10\n" \
"s_waitcnt lgkmcnt(7)\nds_read_b128 v[20:23], v11 offset:54272\nv_cvt_pk_bf16_f32 v91, v54, v55\nv_smfmac_f32_16x16x64_bf16 v[80:83], v[24:27], v[64:71], v10\nds_read_b128 v[24:27], v11 offset:55296\nv_and_or_b32 v88, v88, -2, v13\nv_smfmac_f32_16x16x64_bf16 v[84:87], v[28:31], v[64:71], v10\nds_read_b128 v[28:31], v11 offset:56320\n" \
"v_and_or_b32 v90, v90, -2, v13\nv_smfmac_f32_16x16x64_bf16 v[80:83], v[32:35], v[248:255], v10\nbuffer_store_dwordx4 v[88:91], v7, s[36:39], s60 offen offset:2048 sc1\nv_mov_b64 v[48:49], 0\nv_mov_b64 v[50:51], 0\nv_mov_b64 v[52:53], 0\nv_smfmac_f32_16x16x64_bf16 v[84:87], v[36:39], v[248:255], v10\ns_waitcnt lgkmcnt(9)\n" \
"ds_read_b128 v[64:67], v1 offset:16384\nv_mov_b64 v[54:55], 0\nv_smfmac_f32_16x16x64_bf16 v[48:51], v[40:43], a[8:15], v10\ns_waitcnt lgkmcnt(9)\nds_read_b128 v[68:71], v1 offset:17408\nv_cvt_pk_bf16_f32 v92, v80, v81\nv_smfmac_f32_16x16x64_bf16 v[52:55], v[44:47], a[8:15], v10\ns_waitcnt lgkmcnt(7)\n" \
"v_cvt_pk_bf16_f32 v93, v82, v83\nv_smfmac_f32_16x16x64_bf16 v[48:51], v[72:75], v[56:63], v10\ns_waitcnt lgkmcnt(6)\nv_cvt_pk_bf16_f32 v94, v84, v85\nv_smfmac_f32_16x16x64_bf16 v[52:55], v[76:79], v[56:63], v10\ns_add_u32 s60, s55, 20480\ns_waitcnt lgkmcnt(5)\nv_cvt_pk_bf16_f32 v95, v86, v87\n" \
"v_smfmac_f32_16x16x64_bf16 v[48:51], v[16:19], a[32:39], v10\ns_waitcnt lgkmcnt(4)\nds_read_b128 v[56:59], v1 offset:12288\nv_and_or_b32 v92, v92, -2, v13\nv_smfmac_f32_16x16x64_bf16 v[52:55], v[20:23], a[32:39], v10\ns_waitcnt lgkmcnt(4)\nds_read_b128 v[60:63], v1 offset:13312\nv_and_or_b32 v94, v94, -2, v13\n" \
"v_smfmac_f32_16x16x64_bf16 v[48:51], v[24:27], a[48:55], v10\ns_waitcnt lgkmcnt(4)\nbuffer_store_dwordx4 v[92:95], v7, s[36:39], s60 offen offset:2048 sc1\nv_xor_b32 v11, 128, v11\nv_mov_b64 v[80:81], 0\nv_mov_b64 v[82:83], 0\nv_mov_b64 v[84:85], 0\nv_smfmac_f32_16x16x64_bf16 v[52:55], v[28:31], a[48:55], v10\n" \
"v_mov_b64 v[86:87], 0\nv_smfmac_f32_16x16x64_bf16 v[80:83], v[40:43], a[56:63], v10\nv_cvt_pk_bf16_f32 v88, v48, v49\nv_smfmac_f32_16x16x64_bf16 v[84:87], v[44:47], a[56:63], v10\nv_cvt_pk_bf16_f32 v89, v50, v51\nv_smfmac_f32_16x16x64_bf16 v[80:83], v[72:75], a[72:79], v10\nv_cvt_pk_bf16_f32 v90, v52, v53\nv_smfmac_f32_16x16x64_bf16 v[84:87], v[76:79], a[72:79], v10\n" \
"s_add_u32 s60, s55, 0\ns_waitcnt lgkmcnt(2)\nv_cvt_pk_bf16_f32 v91, v54, v55\nv_smfmac_f32_16x16x64_bf16 v[80:83], v[16:19], v[64:71], v10\nv_and_or_b32 v88, v88, -2, v13\nv_smfmac_f32_16x16x64_bf16 v[84:87], v[20:23], v[64:71], v10\nv_and_or_b32 v90, v90, -2, v13\nv_smfmac_f32_16x16x64_bf16 v[80:83], v[24:27], a[96:103], v10\n" \
"buffer_store_dwordx4 v[88:91], v7, s[36:39], s60 offen offset:512 sc1\nv_mov_b64 v[48:49], 0\nv_mov_b64 v[50:51], 0\nv_mov_b64 v[52:53], 0\nv_smfmac_f32_16x16x64_bf16 v[84:87], v[28:31], a[96:103], v10\nds_read_b128 v[64:67], v1 offset:20480\nv_mov_b64 v[54:55], 0\nv_smfmac_f32_16x16x64_bf16 v[48:51], v[40:43], a[112:119], v10\n" \
"ds_read_b128 v[68:71], v1 offset:21504\nv_cvt_pk_bf16_f32 v92, v80, v81\nv_smfmac_f32_16x16x64_bf16 v[52:55], v[44:47], a[112:119], v10\ns_waitcnt lgkmcnt(2)\nv_cvt_pk_bf16_f32 v93, v82, v83\nv_smfmac_f32_16x16x64_bf16 v[48:51], v[72:75], v[56:63], v10\nv_cvt_pk_bf16_f32 v94, v84, v85\nv_smfmac_f32_16x16x64_bf16 v[52:55], v[76:79], v[56:63], v10\n" \
"s_add_u32 s60, s55, 4096\nv_cvt_pk_bf16_f32 v95, v86, v87\nv_smfmac_f32_16x16x64_bf16 v[48:51], v[16:19], a[136:143], v10\nds_read_b128 v[56:59], v1 offset:10240\nv_and_or_b32 v92, v92, -2, v13\nv_smfmac_f32_16x16x64_bf16 v[52:55], v[20:23], a[136:143], v10\nds_read_b128 v[60:63], v1 offset:11264\nv_and_or_b32 v94, v94, -2, v13\n" \
"v_smfmac_f32_16x16x64_bf16 v[48:51], v[24:27], a[144:151], v10\nbuffer_store_dwordx4 v[92:95], v7, s[36:39], s60 offen offset:512 sc1\nv_mov_b64 v[80:81], 0\nv_mov_b64 v[82:83], 0\nv_mov_b64 v[84:85], 0\nv_smfmac_f32_16x16x64_bf16 v[52:55], v[28:31], a[144:151], v10\nv_mov_b64 v[86:87], 0\nv_smfmac_f32_16x16x64_bf16 v[80:83], v[40:43], a[160:167], v10\n" \
"v_cvt_pk_bf16_f32 v88, v48, v49\nv_smfmac_f32_16x16x64_bf16 v[84:87], v[44:47], a[160:167], v10\nv_cvt_pk_bf16_f32 v89, v50, v51\nv_smfmac_f32_16x16x64_bf16 v[80:83], v[72:75], a[176:183], v10\nv_cvt_pk_bf16_f32 v90, v52, v53\nv_smfmac_f32_16x16x64_bf16 v[84:87], v[76:79], a[176:183], v10\ns_add_u32 s60, s55, 16384\ns_waitcnt lgkmcnt(2)\n" \
"v_cvt_pk_bf16_f32 v91, v54, v55\nv_smfmac_f32_16x16x64_bf16 v[80:83], v[16:19], v[64:71], v10\nv_and_or_b32 v88, v88, -2, v13\nv_smfmac_f32_16x16x64_bf16 v[84:87], v[20:23], v[64:71], v10\nv_and_or_b32 v90, v90, -2, v13\nv_smfmac_f32_16x16x64_bf16 v[80:83], v[24:27], a[200:207], v10\nbuffer_store_dwordx4 v[88:91], v7, s[36:39], s60 offen offset:512 sc1\nv_mov_b64 v[48:49], 0\n" \
"v_mov_b64 v[50:51], 0\nv_mov_b64 v[52:53], 0\nv_smfmac_f32_16x16x64_bf16 v[84:87], v[28:31], a[200:207], v10\nds_read_b128 v[64:67], v1 offset:18432\nv_mov_b64 v[54:55], 0\nv_smfmac_f32_16x16x64_bf16 v[48:51], v[40:43], a[208:215], v10\nds_read_b128 v[68:71], v1 offset:19456\nv_cvt_pk_bf16_f32 v92, v80, v81\n" \
"v_smfmac_f32_16x16x64_bf16 v[52:55], v[44:47], a[208:215], v10\ns_waitcnt lgkmcnt(2)\nv_cvt_pk_bf16_f32 v93, v82, v83\nv_smfmac_f32_16x16x64_bf16 v[48:51], v[72:75], v[56:63], v10\nv_cvt_pk_bf16_f32 v94, v84, v85\nv_smfmac_f32_16x16x64_bf16 v[52:55], v[76:79], v[56:63], v10\ns_add_u32 s60, s55, 20480\nv_cvt_pk_bf16_f32 v95, v86, v87\n" \
"v_smfmac_f32_16x16x64_bf16 v[48:51], v[16:19], a[232:239], v10\nds_read_b128 v[56:59], v1 offset:14336\nv_and_or_b32 v92, v92, -2, v13\nv_smfmac_f32_16x16x64_bf16 v[52:55], v[20:23], a[232:239], v10\nds_read_b128 v[60:63], v1 offset:15360\nv_and_or_b32 v94, v94, -2, v13\nv_smfmac_f32_16x16x64_bf16 v[48:51], v[24:27], a[248:255], v10\nbuffer_store_dwordx4 v[92:95], v7, s[36:39], s60 offen offset:512 sc1\n" \
"v_mov_b64 v[80:81], 0\nv_mov_b64 v[82:83], 0\nv_mov_b64 v[84:85], 0\nv_smfmac_f32_16x16x64_bf16 v[52:55], v[28:31], a[248:255], v10\nv_mov_b64 v[86:87], 0\nv_smfmac_f32_16x16x64_bf16 v[80:83], v[40:43], v[104:111], v10\nv_cvt_pk_bf16_f32 v88, v48, v49\nv_smfmac_f32_16x16x64_bf16 v[84:87], v[44:47], v[104:111], v10\n" \
"v_cvt_pk_bf16_f32 v89, v50, v51\nv_smfmac_f32_16x16x64_bf16 v[80:83], v[72:75], v[120:127], v10\nv_cvt_pk_bf16_f32 v90, v52, v53\nv_smfmac_f32_16x16x64_bf16 v[84:87], v[76:79], v[120:127], v10\ns_add_u32 s60, s55, 0\ns_waitcnt lgkmcnt(2)\nv_cvt_pk_bf16_f32 v91, v54, v55\nv_smfmac_f32_16x16x64_bf16 v[80:83], v[16:19], v[64:71], v10\n" \
"v_and_or_b32 v88, v88, -2, v13\nv_smfmac_f32_16x16x64_bf16 v[84:87], v[20:23], v[64:71], v10\nv_and_or_b32 v90, v90, -2, v13\nv_smfmac_f32_16x16x64_bf16 v[80:83], v[24:27], v[136:143], v10\nbuffer_store_dwordx4 v[88:91], v7, s[36:39], s60 offen offset:2560 sc1\nv_mov_b64 v[48:49], 0\nv_mov_b64 v[50:51], 0\nv_mov_b64 v[52:53], 0\n" \
"v_smfmac_f32_16x16x64_bf16 v[84:87], v[28:31], v[136:143], v10\nds_read_b128 v[64:67], v1 offset:22528\nv_mov_b64 v[54:55], 0\nv_smfmac_f32_16x16x64_bf16 v[48:51], v[40:43], v[152:159], v10\nds_read_b128 v[68:71], v1 offset:23552\nv_cvt_pk_bf16_f32 v92, v80, v81\nv_smfmac_f32_16x16x64_bf16 v[52:55], v[44:47], v[152:159], v10\ns_waitcnt lgkmcnt(2)\n" \
"v_cvt_pk_bf16_f32 v93, v82, v83\nv_smfmac_f32_16x16x64_bf16 v[48:51], v[72:75], v[56:63], v10\nv_cvt_pk_bf16_f32 v94, v84, v85\nv_smfmac_f32_16x16x64_bf16 v[52:55], v[76:79], v[56:63], v10\ns_add_u32 s60, s55, 4096\nv_cvt_pk_bf16_f32 v95, v86, v87\nv_smfmac_f32_16x16x64_bf16 v[48:51], v[16:19], v[176:183], v10\nv_and_or_b32 v92, v92, -2, v13\n" \
"v_smfmac_f32_16x16x64_bf16 v[52:55], v[20:23], v[176:183], v10\nv_and_or_b32 v94, v94, -2, v13\nv_smfmac_f32_16x16x64_bf16 v[48:51], v[24:27], v[192:199], v10\nbuffer_store_dwordx4 v[92:95], v7, s[36:39], s60 offen offset:2560 sc1\nv_mov_b64 v[80:81], 0\nv_mov_b64 v[82:83], 0\nv_mov_b64 v[84:85], 0\nv_smfmac_f32_16x16x64_bf16 v[52:55], v[28:31], v[192:199], v10\n" \
"ds_read_b128 v[32:35], v2 offset:0\nv_mov_b64 v[86:87], 0\nv_smfmac_f32_16x16x64_bf16 v[80:83], v[40:43], v[208:215], v10\nds_read_b128 v[36:39], v2 offset:256\nv_cvt_pk_bf16_f32 v88, v48, v49\nv_smfmac_f32_16x16x64_bf16 v[84:87], v[44:47], v[208:215], v10\nv_cvt_pk_bf16_f32 v89, v50, v51\nv_smfmac_f32_16x16x64_bf16 v[80:83], v[72:75], v[224:231], v10\n" \
"s_add_u32 s60, s55, 16384\nds_read_b128 v[40:43], v2 offset:2048\nv_cvt_pk_bf16_f32 v90, v52, v53\nv_smfmac_f32_16x16x64_bf16 v[84:87], v[76:79], v[224:231], v10\ns_waitcnt lgkmcnt(3)\nds_read_b128 v[44:47], v2 offset:2304\nv_cvt_pk_bf16_f32 v91, v54, v55\nv_smfmac_f32_16x16x64_bf16 v[80:83], v[16:19], v[64:71], v10\n" \
"v_and_or_b32 v88, v88, -2, v13\nv_smfmac_f32_16x16x64_bf16 v[84:87], v[20:23], v[64:71], v10\nv_and_or_b32 v90, v90, -2, v13\nv_smfmac_f32_16x16x64_bf16 v[80:83], v[24:27], v[248:255], v10\nbuffer_store_dwordx4 v[88:91], v7, s[36:39], s60 offen offset:2560 sc1\nv_mov_b64 v[48:49], 0\nv_mov_b64 v[50:51], 0\nv_mov_b64 v[52:53], 0\n" \
"v_mov_b32 v15, s40\nv_smfmac_f32_16x16x64_bf16 v[84:87], v[28:31], v[248:255], v10\ns_add_u32 s60, s55, 20480\ns_cmp_eq_u64 s[64:65], 0\nv_mov_b64 v[54:55], 0\ns_cbranch_scc1 .Lvu_tok%=\nv_cvt_pk_bf16_f32 v92, v80, v81\nv_cvt_pk_bf16_f32 v93, v82, v83\n" \
"s_nop 1\nv_cvt_pk_bf16_f32 v94, v84, v85\nv_cvt_pk_bf16_f32 v95, v86, v87\nv_and_or_b32 v92, v92, -2, v13\nv_and_or_b32 v94, v94, -2, v13\nbuffer_store_dwordx4 v[92:95], v7, s[36:39], s60 offen offset:2560 sc1\ns_mov_b32 s60, -2147483648\ns_cmp_lt_i32 s40, 63\n" \
"s_cbranch_scc1 .Lvu_f0%=\ns_mov_b32 s61, -2147483648\nv_add_u32 v14, s57, v0\ns_waitcnt lgkmcnt(0)\n.Lvu_nov%=:\ns_add_u32 m0, s57, 16384\ns_nop 0\nbuffer_load_dwordx4 v3, s[36:39], s42 offen offset:0 sc1 lds\n" \
"buffer_load_dwordx4 v3, s[36:39], s42 offen offset:1024 sc1 lds\nbuffer_load_dwordx4 v3, s[36:39], s42 offen offset:2048 sc1 lds\nbuffer_load_dwordx4 v3, s[36:39], s42 offen offset:3072 sc1 lds\ns_waitcnt vmcnt(0)\nds_read_b128 v[80:83], v14 offset:16384\nds_read_b128 v[84:87], v14 offset:17408\nds_read_b128 v[88:91], v14 offset:18432\nds_read_b128 v[92:95], v14 offset:19456\n" \
"s_waitcnt lgkmcnt(2)\nv_max3_u32 v76, v80, v82, v84\ns_waitcnt lgkmcnt(1)\nv_max3_u32 v76, v76, v86, v88\ns_waitcnt lgkmcnt(0)\nv_max3_u32 v76, v76, v90, v92\nv_max_u32 v76, v76, v94\nv_cmp_eq_u32 vcc, -1, v76\n" \
"s_cbranch_vccz .Lvu_novfok%=\ns_add_u32 s43, s43, 1\ns_cmp_lt_u32 s43, 262144\ns_cbranch_scc1 .Lvu_nov%=\ns_branch .Lvu_abort%=\n.Lvu_novfok%=:\ns_waitcnt lgkmcnt(0)\ns_barrier\n" \
"s_memrealtime s[68:69]\nds_read_b128 v[56:59], v1 offset:0\nds_read_b128 v[60:63], v1 offset:1024\nds_read_b128 v[80:83], v0 offset:16384\nds_read_b128 v[84:87], v0 offset:17408\nds_read_b128 v[88:91], v0 offset:18432\nds_read_b128 v[92:95], v0 offset:19456\nds_read_b128 v[16:19], v0 offset:20480\n" \
"ds_read_b128 v[20:23], v0 offset:21504\nds_read_b128 v[32:35], v0 offset:22528\nds_read_b128 v[36:39], v0 offset:23552\ns_waitcnt lgkmcnt(0)\ns_branch .Lvu_crit%=\n.Lvu_re%=:\n.Lvu_re2%=:\ns_add_u32 m0, s57, 16384\n" \
"v_max_u32 v76, v80, v82\nv_cmp_eq_u32 vcc, -1, v76\ns_cbranch_vccz .Lvu_sk1893%=\ns_mov_b64 exec, vcc\nbuffer_load_dwordx4 v3, s[36:39], s42 offen offset:0 sc1 lds\ns_mov_b64 exec, -1\n.Lvu_sk1893%=:\nv_max_u32 v76, v84, v86\n" \
"v_cmp_eq_u32 vcc, -1, v76\ns_cbranch_vccz .Lvu_sk1900%=\ns_mov_b64 exec, vcc\nbuffer_load_dwordx4 v3, s[36:39], s42 offen offset:1024 sc1 lds\ns_mov_b64 exec, -1\n.Lvu_sk1900%=:\nv_max_u32 v76, v88, v90\nv_cmp_eq_u32 vcc, -1, v76\n" \
"s_cbranch_vccz .Lvu_sk1907%=\ns_mov_b64 exec, vcc\nbuffer_load_dwordx4 v3, s[36:39], s42 offen offset:2048 sc1 lds\ns_mov_b64 exec, -1\n.Lvu_sk1907%=:\nv_max_u32 v76, v92, v94\nv_cmp_eq_u32 vcc, -1, v76\ns_cbranch_vccz .Lvu_sk1914%=\n" \
"s_mov_b64 exec, vcc\nbuffer_load_dwordx4 v3, s[36:39], s42 offen offset:3072 sc1 lds\ns_mov_b64 exec, -1\n.Lvu_sk1914%=:\n.Lvu_skf%=:\ns_waitcnt vmcnt(0)\nds_read_b128 v[80:83], v14 offset:16384\nds_read_b128 v[84:87], v14 offset:17408\n" \
"ds_read_b128 v[88:91], v14 offset:18432\nds_read_b128 v[92:95], v14 offset:19456\ns_waitcnt lgkmcnt(2)\nv_max3_u32 v76, v80, v82, v84\ns_waitcnt lgkmcnt(1)\nv_max3_u32 v76, v76, v86, v88\ns_waitcnt lgkmcnt(0)\nv_max3_u32 v76, v76, v90, v92\n" \
"v_max_u32 v76, v76, v94\nv_cmp_eq_u32 vcc, -1, v76\ns_cbranch_vccz .Lvu_rer%=\ns_add_u32 s43, s43, 1\ns_cmp_lt_u32 s43, 262144\ns_cbranch_scc1 .Lvu_re2%=\ns_branch .Lvu_abort%=\n.Lvu_rew%=:\n" \
"ds_read_b128 v[80:83], v14 offset:16384\nds_read_b128 v[84:87], v14 offset:17408\nds_read_b128 v[88:91], v14 offset:18432\nds_read_b128 v[92:95], v14 offset:19456\ns_waitcnt lgkmcnt(0)\ns_branch .Lvu_rer%=\n.Lvu_first%=:\ns_nop 7\n" \
"v_cvt_pk_bf16_f32 v76, v24, v25\nv_cvt_pk_bf16_f32 v77, v26, v27\nv_cvt_pk_bf16_f32 v78, v28, v29\nv_cvt_pk_bf16_f32 v79, v30, v31\nds_write2st64_b64 v9, v[76:77], v[78:79] offset0:2 offset1:10\ns_waitcnt vmcnt(0) lgkmcnt(0)\ns_barrier\nds_read_b128 v[56:59], v1 offset:8192\n" \
"ds_read_b128 v[60:63], v1 offset:9216\nds_read_b128 v[64:67], v1 offset:16384\nds_read_b128 v[68:71], v1 offset:17408\nds_read_b128 v[72:75], v11 offset:49152\nds_read_b128 v[76:79], v11 offset:50176\nds_read_b128 v[16:19], v11 offset:51200\nds_read_b128 v[20:23], v11 offset:52224\nds_read_b128 v[24:27], v11 offset:53248\n" \
"ds_read_b128 v[28:31], v11 offset:54272\nds_read_b128 v[32:35], v11 offset:55296\nds_read_b128 v[36:39], v11 offset:56320\ns_waitcnt lgkmcnt(0)\ns_add_u32 m0, s57, 0\ns_add_u32 s58, s61, 0\nbuffer_load_dwordx4 v0, s[36:39], s58 offen offset:0 sc1 lds\nbuffer_load_dwordx4 v0, s[36:39], s58 offen offset:1024 sc1 lds\n" \
"buffer_load_dwordx4 v0, s[36:39], s58 offen offset:2048 sc1 lds\nbuffer_load_dwordx4 v0, s[36:39], s58 offen offset:3072 sc1 lds\ns_mov_b32 s59, -2147483648\nbuffer_store_dword v14, off, s[36:39], s59\nbuffer_store_dword v14, off, s[36:39], s59\nbuffer_store_dword v14, off, s[36:39], s59\ns_branch .Lvu_y%=\n.Lvu_last%=:\n" \
"s_nop 7\nv_cvt_pk_bf16_f32 v92, v40, v41\nv_and_or_b32 v92, v92, -2, v12\ns_add_u32 s63, s59, 4096\nbuffer_store_dwordx4 v[88:91], v6, s[36:39], s63 offen sc1\nv_cvt_pk_bf16_f32 v93, v42, v43\nv_cvt_pk_bf16_f32 v94, v44, v45\nv_cvt_pk_bf16_f32 v95, v46, v47\n" \
"v_and_or_b32 v94, v94, -2, v12\ns_nop 0\nv_permlane16_swap_b32 v93, v95\nv_permlane16_swap_b32 v92, v94\ns_add_u32 s63, s59, 135168\nbuffer_store_dwordx4 v[92:95], v6, s[36:39], s63 offen sc1\ns_branch .Lvu_end%=\n.Lvu_abort%=:\n" \
"s_mov_b64 exec, -1\nv_mov_b32 v16, 0\ns_movk_i32 s44, 6400\nbuffer_store_dword v16, off, s[36:39], s44 sc1\n.Lvu_end%=:\ns_waitcnt vmcnt(0) lgkmcnt(0)\n" \
""
#define RD_ASM \
"v_mbcnt_lo_u32_b32 v255, -1, 0\nv_mbcnt_hi_u32_b32 v255, -1, v255\nv_lshlrev_b32 v255, 2, v255\nv_add_u32 v255, %[tb], v255\nds_read_b32 v196, v255 offset:0\nds_read_b32 v197, v255 offset:256\nds_read_b32 v198, v255 offset:512\nds_read_b32 v199, v255 offset:768\n" \
"ds_read_b32 v200, v255 offset:1024\nds_read_b32 v201, v255 offset:1280\nds_read_b32 v202, v255 offset:1536\nds_read_b32 v203, v255 offset:1792\nds_read_b32 v204, v255 offset:2048\nds_read_b32 v205, v255 offset:2304\nds_read_b32 v206, v255 offset:2560\nds_read_b32 v207, v255 offset:2816\n" \
"s_waitcnt lgkmcnt(0)\nv_accvgpr_write_b32 a32, v206\nv_accvgpr_write_b32 a33, v207\ns_mov_b32 s40, 0\ns_mov_b32 s43, 0\ns_add_u32 s57, %[su], 4096\ns_add_u32 s56, %[sp], 4096\ns_mov_b32 s41, %[sy]\n" \
"s_mov_b32 s59, -2147483648\n.Lrd_yflag%=:\nv_accvgpr_read_b32 v206, a32\nbuffer_load_dword v208, v206, %[rs], 0 offen sc1\ns_waitcnt vmcnt(0)\nv_cmp_gt_i32 vcc, s40, v208\ns_cbranch_vccz .Lrd_yld%=\ns_add_u32 s43, s43, 1\n" \
"s_cmp_lt_u32 s43, 262144\ns_cbranch_scc1 .Lrd_yflag%=\ns_branch .Lrd_abort%=\n.Lrd_yld%=:\ns_add_u32 s60, s41, 0\nbuffer_load_dwordx4 v[0:3], v196, %[rs], s60 offen offset:0 sc1\ns_add_u32 s60, s41, 0\nbuffer_load_dwordx4 v[32:35], v196, %[rs], s60 offen offset:1024 sc1\n" \
"s_add_u32 s60, s41, 0\nbuffer_load_dwordx4 v[64:67], v196, %[rs], s60 offen offset:2048 sc1\ns_add_u32 s60, s41, 131072\nbuffer_load_dwordx4 v[4:7], v196, %[rs], s60 offen offset:0 sc1\ns_add_u32 s60, s41, 131072\nbuffer_load_dwordx4 v[36:39], v196, %[rs], s60 offen offset:1024 sc1\ns_add_u32 s60, s41, 131072\nbuffer_load_dwordx4 v[68:71], v196, %[rs], s60 offen offset:2048 sc1\n" \
"s_add_u32 s60, s41, 262144\nbuffer_load_dwordx4 v[8:11], v196, %[rs], s60 offen offset:0 sc1\ns_add_u32 s60, s41, 262144\nbuffer_load_dwordx4 v[40:43], v196, %[rs], s60 offen offset:1024 sc1\ns_add_u32 s60, s41, 262144\nbuffer_load_dwordx4 v[72:75], v196, %[rs], s60 offen offset:2048 sc1\ns_add_u32 s60, s41, 393216\nbuffer_load_dwordx4 v[12:15], v196, %[rs], s60 offen offset:0 sc1\n" \
"s_add_u32 s60, s41, 393216\nbuffer_load_dwordx4 v[44:47], v196, %[rs], s60 offen offset:1024 sc1\ns_add_u32 s60, s41, 393216\nbuffer_load_dwordx4 v[76:79], v196, %[rs], s60 offen offset:2048 sc1\ns_add_u32 s60, s41, 524288\nbuffer_load_dwordx4 v[16:19], v196, %[rs], s60 offen offset:0 sc1\ns_add_u32 s60, s41, 524288\nbuffer_load_dwordx4 v[48:51], v196, %[rs], s60 offen offset:1024 sc1\n" \
"s_add_u32 s60, s41, 524288\nbuffer_load_dwordx4 v[80:83], v196, %[rs], s60 offen offset:2048 sc1\ns_add_u32 s60, s41, 655360\nbuffer_load_dwordx4 v[20:23], v196, %[rs], s60 offen offset:0 sc1\ns_add_u32 s60, s41, 655360\nbuffer_load_dwordx4 v[52:55], v196, %[rs], s60 offen offset:1024 sc1\ns_add_u32 s60, s41, 655360\nbuffer_load_dwordx4 v[84:87], v196, %[rs], s60 offen offset:2048 sc1\n" \
"s_add_u32 s60, s41, 786432\nbuffer_load_dwordx4 v[24:27], v196, %[rs], s60 offen offset:0 sc1\ns_add_u32 s60, s41, 786432\nbuffer_load_dwordx4 v[56:59], v196, %[rs], s60 offen offset:1024 sc1\ns_add_u32 s60, s41, 786432\nbuffer_load_dwordx4 v[88:91], v196, %[rs], s60 offen offset:2048 sc1\ns_add_u32 s60, s41, 917504\nbuffer_load_dwordx4 v[28:31], v196, %[rs], s60 offen offset:0 sc1\n" \
"s_add_u32 s60, s41, 917504\nbuffer_load_dwordx4 v[60:63], v196, %[rs], s60 offen offset:1024 sc1\ns_add_u32 s60, s41, 917504\nbuffer_load_dwordx4 v[92:95], v196, %[rs], s60 offen offset:2048 sc1\ns_add_u32 s60, s41, 0\nbuffer_load_dwordx4 v[96:99], v196, %[rs], s60 offen offset:3072 sc1\ns_add_u32 s60, s41, 131072\nbuffer_load_dwordx4 v[100:103], v196, %[rs], s60 offen offset:3072 sc1\n" \
"s_add_u32 s60, s41, 262144\nbuffer_load_dwordx4 v[104:107], v196, %[rs], s60 offen offset:3072 sc1\ns_add_u32 s60, s41, 393216\nbuffer_load_dwordx4 v[108:111], v196, %[rs], s60 offen offset:3072 sc1\ns_add_u32 s60, s41, 524288\nbuffer_load_dwordx4 v[112:115], v196, %[rs], s60 offen offset:3072 sc1\ns_add_u32 s60, s41, 655360\nbuffer_load_dwordx4 v[116:119], v196, %[rs], s60 offen offset:3072 sc1\n" \
"s_add_u32 s60, s41, 786432\nbuffer_load_dwordx4 v[120:123], v196, %[rs], s60 offen offset:3072 sc1\ns_add_u32 s60, s41, 917504\nbuffer_load_dwordx4 v[124:127], v196, %[rs], s60 offen offset:3072 sc1\ns_mov_b64 exec, -1\nv_accvgpr_read_b32 v212, a33\nbuffer_load_dwordx2 v[214:215], v212, %[rs], 0 offen sc1\ns_waitcnt vmcnt(0)\n" \
"s_branch .Lrd_ychk%=\n.Lrd_u2%=:\nbuffer_load_dwordx4 v[136:139], v196, %[rs], %[su] offen offset:2048 sc1\nbuffer_load_dwordx4 v[140:143], v196, %[rs], %[su] offen offset:3072 sc1\ns_add_u32 s43, s43, 1\ns_cmp_ge_u32 s43, 262144\ns_cbranch_scc1 .Lrd_abort%=\ns_waitcnt vmcnt(0)\n" \
"v_bitop3_b32 v208, v136, v138, s54 bitop3:0x7e\nv_bitop3_b32 v209, v140, v142, s54 bitop3:0x7e\nv_bitop3_b32 v208, v208, 1, v209 bitop3:0xc8\nv_cmp_ne_u32 vcc, 0, v208\ns_cbranch_vccnz .Lrd_u2%=\n.Lrd_u3%=:\nbuffer_load_dwordx4 v[128:131], v196, %[rs], %[su] offen offset:0 sc1\nbuffer_load_dwordx4 v[132:135], v196, %[rs], %[su] offen offset:1024 sc1\n" \
"buffer_load_dwordx4 v[144:147], v196, %[rs], s57 offen offset:0 sc1\nbuffer_load_dwordx4 v[148:151], v196, %[rs], s57 offen offset:1024 sc1\nbuffer_load_dwordx4 v[152:155], v196, %[rs], s57 offen offset:2048 sc1\nbuffer_load_dwordx4 v[156:159], v196, %[rs], s57 offen offset:3072 sc1\nbuffer_load_dwordx4 v[160:163], v196, %[rs], %[sp] offen offset:0 sc1\nbuffer_load_dwordx4 v[164:167], v196, %[rs], %[sp] offen offset:1024 sc1\nbuffer_load_dwordx4 v[168:171], v196, %[rs], %[sp] offen offset:2048 sc1\nbuffer_load_dwordx4 v[172:175], v196, %[rs], %[sp] offen offset:3072 sc1\n" \
"buffer_load_dwordx4 v[176:179], v196, %[rs], s56 offen offset:0 sc1\n.Lrd_ra%=:\ns_waitcnt vmcnt(5)\nv_bitop3_b32 v208, v128, v130, s54 bitop3:0x7e\nv_bitop3_b32 v209, v136, v138, s54 bitop3:0x7e\nv_bitop3_b32 v208, v208, 1, v209 bitop3:0xc8\nv_cmp_ne_u32 s[86:87], 0, v208\nv_bitop3_b32 v208, v144, v146, s54 bitop3:0x7e\n" \
"v_bitop3_b32 v209, v152, v154, s54 bitop3:0x7e\nv_bitop3_b32 v208, v208, 1, v209 bitop3:0xc8\nv_cmp_ne_u32 s[88:89], 0, v208\nv_bitop3_b32 v208, v132, v134, s54 bitop3:0x7e\nv_bitop3_b32 v209, v140, v142, s54 bitop3:0x7e\nv_bitop3_b32 v208, v208, 1, v209 bitop3:0xc8\nv_cmp_ne_u32 s[68:69], 0, v208\nv_bitop3_b32 v208, v148, v150, s54 bitop3:0x7e\n" \
"v_bitop3_b32 v209, v156, v158, s54 bitop3:0x7e\nv_bitop3_b32 v208, v208, 1, v209 bitop3:0xc8\nv_cmp_ne_u32 s[62:63], 0, v208\ns_or_b64 vcc, s[86:87], s[88:89]\ns_or_b64 vcc, vcc, s[68:69]\ns_or_b64 vcc, vcc, s[62:63]\ns_waitcnt vmcnt(4)\nv_smfmac_f32_16x16x64_bf16 v[180:183], v[160:163], v[0:7], v197\n" \
"v_bitop3_b32 v208, v160, v162, s54 bitop3:0x7e\nv_smfmac_f32_16x16x64_bf16 v[184:187], v[160:163], v[32:39], v197\nv_cmp_gt_i32 s[46:47], 0, v208\nv_smfmac_f32_16x16x64_bf16 v[188:191], v[160:163], v[64:71], v197\nv_smfmac_f32_16x16x64_bf16 v[192:195], v[160:163], v[96:103], v197\ns_or_b64 vcc, vcc, s[46:47]\ns_waitcnt vmcnt(3)\nv_smfmac_f32_16x16x64_bf16 v[180:183], v[164:167], v[8:15], v197\n" \
"v_bitop3_b32 v208, v164, v166, s54 bitop3:0x7e\nv_smfmac_f32_16x16x64_bf16 v[184:187], v[164:167], v[40:47], v197\nv_cmp_gt_i32 s[48:49], 0, v208\nv_smfmac_f32_16x16x64_bf16 v[188:191], v[164:167], v[72:79], v197\nv_smfmac_f32_16x16x64_bf16 v[192:195], v[164:167], v[104:111], v197\ns_or_b64 vcc, vcc, s[48:49]\ns_waitcnt vmcnt(2)\nv_smfmac_f32_16x16x64_bf16 v[180:183], v[168:171], v[16:23], v197\n" \
"v_bitop3_b32 v208, v168, v170, s54 bitop3:0x7e\nv_smfmac_f32_16x16x64_bf16 v[184:187], v[168:171], v[48:55], v197\nv_cmp_gt_i32 s[50:51], 0, v208\nv_smfmac_f32_16x16x64_bf16 v[188:191], v[168:171], v[80:87], v197\nv_smfmac_f32_16x16x64_bf16 v[192:195], v[168:171], v[112:119], v197\ns_or_b64 vcc, vcc, s[50:51]\ns_waitcnt vmcnt(1)\nv_smfmac_f32_16x16x64_bf16 v[180:183], v[172:175], v[24:31], v197\n" \
"s_waitcnt vmcnt(0)\nv_bitop3_b32 v208, v172, v174, s54 bitop3:0x7e\nv_smfmac_f32_16x16x64_bf16 v[180:183], v[176:179], v[128:135], v197\nv_bitop3_b32 v209, v176, v178, s54 bitop3:0x7e\nv_smfmac_f32_16x16x64_bf16 v[184:187], v[172:175], v[56:63], v197\nv_bitop3_b32 v210, v177, v179, s54 bitop3:0x7e\nv_smfmac_f32_16x16x64_bf16 v[184:187], v[176:179], v[136:143], v197\nv_cmp_gt_i32 s[52:53], 0, v208\n" \
"v_smfmac_f32_16x16x64_bf16 v[188:191], v[172:175], v[88:95], v197\nv_or_b32 v209, v209, v210\nv_smfmac_f32_16x16x64_bf16 v[188:191], v[176:179], v[144:151], v197\ns_or_b64 vcc, vcc, s[52:53]\nv_cmp_gt_i32 s[64:65], 0, v209\nv_smfmac_f32_16x16x64_bf16 v[192:195], v[172:175], v[120:127], v197\nv_smfmac_f32_16x16x64_bf16 v[192:195], v[176:179], v[152:159], v197\ns_or_b64 vcc, vcc, s[64:65]\n" \
"s_cbranch_scc1 .Lrd_rb%=\ns_add_u32 s60, s58, 16384\nv_add_f32 v180, v180, v182\nv_add_f32 v181, v181, v183\nds_write2_b32 v198, v180, v181 offset0:0 offset1:8\nbuffer_load_dwordx4 v[212:215], v196, %[rs], s60 offen offset:0 sc1\nv_add_f32 v184, v184, v186\nv_add_f32 v185, v185, v187\n" \
"ds_write2_b32 v198, v184, v185 offset0:32 offset1:40\ns_add_u32 s60, s58, 0\nbuffer_load_dwordx4 v[216:219], v196, %[rs], s60 offen offset:0 sc1\nv_add_f32 v188, v188, v190\nv_add_f32 v189, v189, v191\nds_write2_b32 v198, v188, v189 offset0:64 offset1:72\ns_add_u32 s60, s58, 0\nbuffer_load_dwordx4 v[220:223], v196, %[rs], s60 offen offset:1024 sc1\n" \
"v_add_f32 v192, v192, v194\nv_add_f32 v193, v193, v195\nds_write2_b32 v198, v192, v193 offset0:96 offset1:104\ns_add_u32 s60, s58, 16384\nbuffer_load_dwordx4 v[224:227], v196, %[rs], s60 offen offset:1024 sc1\ns_waitcnt lgkmcnt(0)\ns_barrier\nds_read_b128 v[180:183], v199 offset:0\n" \
"ds_read_b128 v[184:187], v199 offset:4096\nds_read_b128 v[188:191], v199 offset:8192\nds_read_b128 v[192:195], v199 offset:12288\ns_and_b32 s44, s54, 0x80008000\ns_add_u32 s60, s58, 0\nbuffer_load_dwordx4 v[228:231], v196, %[rs], s60 offen offset:2048 sc1\ns_waitcnt lgkmcnt(2)\nv_pk_add_f32 v[182:183], v[182:183], v[186:187]\n" \
"v_pk_add_f32 v[180:181], v[180:181], v[184:185]\ns_waitcnt lgkmcnt(0)\nv_pk_add_f32 v[186:187], v[188:189], v[192:193]\nv_pk_add_f32 v[184:185], v[190:191], v[194:195]\nv_pk_add_f32 v[180:181], v[180:181], v[186:187]\nv_pk_add_f32 v[182:183], v[182:183], v[184:185]\nv_cvt_pk_bf16_f32 v180, v180, v181\nv_xor_b32 v180, s44, v180\n" \
"v_cvt_pk_bf16_f32 v181, v182, v183\nv_min_u32 v180, -2, v180\nv_xor_b32 v209, s44, v181\nv_or_b32 v208, v180, v201\nv_mov_b32 v211, v209\nv_or_b32 v210, v180, v201\ns_nop 0\nv_permlane32_swap_b32 v209, v211\n" \
"v_permlane32_swap_b32 v208, v210\nbuffer_store_dwordx4 v[208:211], v206, %[rs], 0 offen sc1\ns_add_u32 s60, s58, 0\nbuffer_load_dwordx4 v[232:235], v196, %[rs], s60 offen offset:3072 sc1\ns_add_u32 s60, s58, 16384\nbuffer_load_dwordx4 v[236:239], v196, %[rs], s60 offen offset:2048 sc1\ns_add_u32 s60, s58, 4096\nbuffer_load_dwordx4 v[240:243], v196, %[rs], s60 offen offset:0 sc1\n" \
"s_add_u32 s60, s58, 4096\nbuffer_load_dwordx4 v[244:247], v196, %[rs], s60 offen offset:1024 sc1\ns_add_u32 s60, s58, 16384\nbuffer_load_dwordx4 v[248:251], v196, %[rs], s60 offen offset:3072 sc1\ns_xor_b32 s41, s41, 4194304\nv_mov_b32 v207, s40\ns_waitcnt vmcnt(10)\nv_bitop3_b32 v209, v212, v214, s61 bitop3:0x7e\n" \
"v_cmp_gt_i32 vcc, 0, v209\ns_cbranch_vccnz .Lrd_tre0%=\n.Lrd_tl0%=:\nv_mfma_f32_16x16x16_bf16 a[0:3], v[0:1], v[212:213], 0\ns_add_u32 s60, s58, 20480\nbuffer_load_dwordx4 v[252:255], v196, %[rs], s60 offen offset:0 sc1\nv_mfma_f32_16x16x16_bf16 a[8:11], v[32:33], v[212:213], 0\nv_mfma_f32_16x16x16_bf16 a[16:19], v[64:65], v[212:213], 0\n" \
"v_mfma_f32_16x16x16_bf16 a[24:27], v[96:97], v[212:213], 0\ns_waitcnt vmcnt(10)\nv_mfma_f32_16x16x16_bf16 a[0:3], v[4:5], v[214:215], a[0:3]\ns_add_u32 s60, s58, 20480\nbuffer_load_dwordx4 v[160:163], v196, %[rs], s60 offen offset:2048 sc1\nv_bitop3_b32 v209, v216, v218, s61 bitop3:0x7e\nv_mfma_f32_16x16x16_bf16 a[8:11], v[36:37], v[214:215], a[8:11]\ns_lshl_b32 s42, s40, 17\n" \
"s_add_u32 s42, s42, %[at]\nv_mfma_f32_16x16x16_bf16 a[16:19], v[68:69], v[214:215], a[16:19]\nv_cmp_gt_i32 vcc, 0, v209\nv_mfma_f32_16x16x16_bf16 a[24:27], v[100:101], v[214:215], a[24:27]\ns_cbranch_vccnz .Lrd_tre1%=\n.Lrd_tl1%=:\ns_waitcnt vmcnt(10)\nv_mfma_f32_16x16x32_bf16 a[4:7], v[0:3], v[216:219], 0\n" \
"s_add_u32 s60, s58, 4096\nbuffer_load_dwordx4 v[164:167], v196, %[rs], s60 offen offset:2048 sc1\nv_bitop3_b32 v209, v220, v222, s61 bitop3:0x7e\nv_mfma_f32_16x16x32_bf16 a[12:15], v[32:35], v[216:219], 0\ns_lshr_b32 s45, s42, 8\ns_and_b32 s45, s45, 0x2000\nv_mfma_f32_16x16x32_bf16 a[20:23], v[64:67], v[216:219], 0\nv_cmp_gt_i32 vcc, 0, v209\n" \
"v_mfma_f32_16x16x32_bf16 a[28:31], v[96:99], v[216:219], 0\ns_cbranch_vccnz .Lrd_tre2%=\n.Lrd_tl2%=:\ns_waitcnt vmcnt(10)\nv_mfma_f32_16x16x32_bf16 a[4:7], v[4:7], v[220:223], a[4:7]\ns_add_u32 s60, s58, 4096\nbuffer_load_dwordx4 v[168:171], v196, %[rs], s60 offen offset:3072 sc1\nv_bitop3_b32 v209, v224, v226, s61 bitop3:0x7e\n" \
"v_mfma_f32_16x16x32_bf16 a[12:15], v[36:39], v[220:223], a[12:15]\ns_xor_b32 s42, s42, s45\ns_add_u32 s40, s40, 1\nv_mfma_f32_16x16x32_bf16 a[20:23], v[68:71], v[220:223], a[20:23]\nv_cmp_gt_i32 vcc, 0, v209\nv_mfma_f32_16x16x32_bf16 a[28:31], v[100:103], v[220:223], a[28:31]\ns_cbranch_vccnz .Lrd_tre3%=\n.Lrd_tl3%=:\n" \
"v_mfma_f32_16x16x16_bf16 a[0:3], v[8:9], v[224:225], a[0:3]\ns_add_u32 s60, s58, 20480\nbuffer_load_dwordx4 v[172:175], v196, %[rs], s60 offen offset:1024 sc1\nv_mfma_f32_16x16x16_bf16 a[8:11], v[40:41], v[224:225], a[8:11]\nv_mfma_f32_16x16x16_bf16 a[16:19], v[72:73], v[224:225], a[16:19]\nbuffer_load_dwordx4 v[212:215], v204, %[rs], s42 offen sc1\nv_mfma_f32_16x16x16_bf16 a[24:27], v[104:105], v[224:225], a[24:27]\ns_waitcnt vmcnt(12)\n" \
"v_mfma_f32_16x16x16_bf16 a[0:3], v[12:13], v[226:227], a[0:3]\ns_add_u32 s60, s58, 20480\nbuffer_load_dwordx4 v[176:179], v196, %[rs], s60 offen offset:3072 sc1\nv_bitop3_b32 v209, v228, v230, s61 bitop3:0x7e\nv_mfma_f32_16x16x16_bf16 a[8:11], v[44:45], v[226:227], a[8:11]\ns_lshl_b32 s62, s55, 31\ns_or_b32 s62, s62, 1.0\ns_mov_b32 s63, s62\n" \
"v_mfma_f32_16x16x16_bf16 a[16:19], v[76:77], v[226:227], a[16:19]\nv_cmp_gt_i32 vcc, 0, v209\nv_mfma_f32_16x16x16_bf16 a[24:27], v[108:109], v[226:227], a[24:27]\ns_cbranch_vccnz .Lrd_tre4%=\n.Lrd_tl4%=:\ns_waitcnt vmcnt(11)\nv_mfma_f32_16x16x32_bf16 a[4:7], v[8:11], v[228:231], a[4:7]\ns_cmp_eq_u32 s40, 1\n" \
"s_cbranch_scc1 .Lrd_y1%=\n.Lrd_y1r%=:\ns_add_u32 s60, s41, 0\nbuffer_load_dwordx4 v[0:3], v196, %[rs], s60 offen offset:0 sc1\nv_bitop3_b32 v209, v232, v234, s61 bitop3:0x7e\nv_mfma_f32_16x16x32_bf16 a[12:15], v[40:43], v[228:231], a[12:15]\nv_mfma_f32_16x16x32_bf16 a[20:23], v[72:75], v[228:231], a[20:23]\nv_cmp_gt_i32 vcc, 0, v209\n" \
"v_mfma_f32_16x16x32_bf16 a[28:31], v[104:107], v[228:231], a[28:31]\ns_cbranch_vccnz .Lrd_tre5%=\n.Lrd_tl5%=:\ns_waitcnt vmcnt(11)\nv_mfma_f32_16x16x32_bf16 a[4:7], v[12:15], v[232:235], a[4:7]\ns_add_u32 s60, s41, 0\nbuffer_load_dwordx4 v[32:35], v196, %[rs], s60 offen offset:1024 sc1\nv_bitop3_b32 v209, v236, v238, s61 bitop3:0x7e\n" \
"v_mfma_f32_16x16x32_bf16 a[12:15], v[44:47], v[232:235], a[12:15]\nv_mfma_f32_16x16x32_bf16 a[20:23], v[76:79], v[232:235], a[20:23]\nv_cmp_gt_i32 vcc, 0, v209\nv_mfma_f32_16x16x32_bf16 a[28:31], v[108:111], v[232:235], a[28:31]\ns_cbranch_vccnz .Lrd_tre6%=\n.Lrd_tl6%=:\nv_mfma_f32_16x16x16_bf16 a[0:3], v[16:17], v[236:237], a[0:3]\ns_add_u32 s60, s41, 0\n" \
"buffer_load_dwordx4 v[64:67], v196, %[rs], s60 offen offset:2048 sc1\nv_mfma_f32_16x16x16_bf16 a[8:11], v[48:49], v[236:237], a[8:11]\nv_mfma_f32_16x16x16_bf16 a[16:19], v[80:81], v[236:237], a[16:19]\nv_mfma_f32_16x16x16_bf16 a[24:27], v[112:113], v[236:237], a[24:27]\ns_waitcnt vmcnt(12)\nv_mfma_f32_16x16x16_bf16 a[0:3], v[20:21], v[238:239], a[0:3]\ns_add_u32 s60, s41, 131072\nbuffer_load_dwordx4 v[4:7], v196, %[rs], s60 offen offset:0 sc1\n" \
"v_bitop3_b32 v209, v240, v242, s61 bitop3:0x7e\nv_mfma_f32_16x16x16_bf16 a[8:11], v[52:53], v[238:239], a[8:11]\nv_mfma_f32_16x16x16_bf16 a[16:19], v[84:85], v[238:239], a[16:19]\nv_cmp_gt_i32 vcc, 0, v209\nv_mfma_f32_16x16x16_bf16 a[24:27], v[116:117], v[238:239], a[24:27]\ns_cbranch_vccnz .Lrd_tre7%=\n.Lrd_tl7%=:\ns_waitcnt vmcnt(12)\n" \
"v_mfma_f32_16x16x32_bf16 a[4:7], v[16:19], v[240:243], a[4:7]\ns_add_u32 s60, s41, 131072\nbuffer_load_dwordx4 v[36:39], v196, %[rs], s60 offen offset:1024 sc1\nv_bitop3_b32 v209, v244, v246, s61 bitop3:0x7e\nv_mfma_f32_16x16x32_bf16 a[12:15], v[48:51], v[240:243], a[12:15]\nv_mfma_f32_16x16x32_bf16 a[20:23], v[80:83], v[240:243], a[20:23]\nv_cmp_gt_i32 vcc, 0, v209\nv_mfma_f32_16x16x32_bf16 a[28:31], v[112:115], v[240:243], a[28:31]\n" \
"s_cbranch_vccnz .Lrd_tre8%=\n.Lrd_tl8%=:\ns_waitcnt vmcnt(12)\nv_mfma_f32_16x16x32_bf16 a[4:7], v[20:23], v[244:247], a[4:7]\ns_add_u32 s60, s41, 131072\nbuffer_load_dwordx4 v[68:71], v196, %[rs], s60 offen offset:2048 sc1\nv_bitop3_b32 v209, v248, v250, s61 bitop3:0x7e\nv_mfma_f32_16x16x32_bf16 a[12:15], v[52:55], v[244:247], a[12:15]\n" \
"v_mfma_f32_16x16x32_bf16 a[20:23], v[84:87], v[244:247], a[20:23]\nv_cmp_gt_i32 vcc, 0, v209\nv_mfma_f32_16x16x32_bf16 a[28:31], v[116:119], v[244:247], a[28:31]\ns_cbranch_vccnz .Lrd_tre9%=\n.Lrd_tl9%=:\nv_mfma_f32_16x16x16_bf16 a[0:3], v[24:25], v[248:249], a[0:3]\ns_add_u32 s60, s41, 262144\nbuffer_load_dwordx4 v[8:11], v196, %[rs], s60 offen offset:0 sc1\n" \
"v_mfma_f32_16x16x16_bf16 a[8:11], v[56:57], v[248:249], a[8:11]\nv_mfma_f32_16x16x16_bf16 a[16:19], v[88:89], v[248:249], a[16:19]\nv_mfma_f32_16x16x16_bf16 a[24:27], v[120:121], v[248:249], a[24:27]\ns_waitcnt vmcnt(13)\nv_mfma_f32_16x16x16_bf16 a[0:3], v[28:29], v[250:251], a[0:3]\ns_add_u32 s60, s41, 262144\nbuffer_load_dwordx4 v[40:43], v196, %[rs], s60 offen offset:1024 sc1\nv_bitop3_b32 v209, v252, v254, s61 bitop3:0x7e\n" \
"v_mfma_f32_16x16x16_bf16 a[8:11], v[60:61], v[250:251], a[8:11]\nv_bitop3_b32 v210, v253, v255, s61 bitop3:0x7e\nv_or_b32 v209, v209, v210\nv_mfma_f32_16x16x16_bf16 a[16:19], v[92:93], v[250:251], a[16:19]\nv_cmp_gt_i32 vcc, 0, v209\nv_mfma_f32_16x16x16_bf16 a[24:27], v[124:125], v[250:251], a[24:27]\ns_cbranch_vccnz .Lrd_tre10%=\n.Lrd_tl10%=:\n" \
"s_waitcnt vmcnt(13)\nv_mfma_f32_16x16x32_bf16 a[0:3], v[128:131], v[252:255], a[0:3]\ns_add_u32 s60, s41, 262144\nbuffer_load_dwordx4 v[72:75], v196, %[rs], s60 offen offset:2048 sc1\nv_bitop3_b32 v209, v160, v162, s61 bitop3:0x7e\nv_mfma_f32_16x16x32_bf16 a[8:11], v[136:139], v[252:255], a[8:11]\nv_bitop3_b32 v210, v161, v163, s61 bitop3:0x7e\nv_or_b32 v209, v209, v210\n" \
"v_mfma_f32_16x16x32_bf16 a[16:19], v[144:147], v[252:255], a[16:19]\nv_cmp_gt_i32 vcc, 0, v209\nv_mfma_f32_16x16x32_bf16 a[24:27], v[152:155], v[252:255], a[24:27]\ns_cbranch_vccnz .Lrd_tre11%=\n.Lrd_tl11%=:\ns_waitcnt vmcnt(13)\nv_mfma_f32_16x16x32_bf16 a[0:3], v[132:135], v[160:163], a[0:3]\ns_add_u32 s60, s41, 393216\n" \
"buffer_load_dwordx4 v[12:15], v196, %[rs], s60 offen offset:0 sc1\nv_bitop3_b32 v209, v164, v166, s61 bitop3:0x7e\nv_mfma_f32_16x16x32_bf16 a[8:11], v[140:143], v[160:163], a[8:11]\nv_mfma_f32_16x16x32_bf16 a[16:19], v[148:151], v[160:163], a[16:19]\nv_cmp_gt_i32 vcc, 0, v209\nv_mfma_f32_16x16x32_bf16 a[24:27], v[156:159], v[160:163], a[24:27]\ns_cbranch_vccnz .Lrd_tre12%=\n.Lrd_tl12%=:\n" \
"s_waitcnt vmcnt(13)\nv_mfma_f32_16x16x32_bf16 a[4:7], v[24:27], v[164:167], a[4:7]\ns_add_u32 s60, s41, 393216\nbuffer_load_dwordx4 v[44:47], v196, %[rs], s60 offen offset:1024 sc1\nv_bitop3_b32 v209, v168, v170, s61 bitop3:0x7e\nv_mfma_f32_16x16x32_bf16 a[12:15], v[56:59], v[164:167], a[12:15]\nv_mfma_f32_16x16x32_bf16 a[20:23], v[88:91], v[164:167], a[20:23]\nds_write_b128 v202, a[0:3] offset:0\n" \
"v_cmp_gt_i32 vcc, 0, v209\nv_mfma_f32_16x16x32_bf16 a[28:31], v[120:123], v[164:167], a[28:31]\ns_cbranch_vccnz .Lrd_tre13%=\n.Lrd_tl13%=:\ns_waitcnt vmcnt(13)\nv_mfma_f32_16x16x32_bf16 a[4:7], v[28:31], v[168:171], a[4:7]\ns_add_u32 s60, s41, 393216\nbuffer_load_dwordx4 v[76:79], v196, %[rs], s60 offen offset:2048 sc1\n" \
"v_bitop3_b32 v209, v172, v174, s61 bitop3:0x7e\nv_mfma_f32_16x16x32_bf16 a[12:15], v[60:63], v[168:171], a[12:15]\nv_bitop3_b32 v210, v173, v175, s61 bitop3:0x7e\nv_or_b32 v209, v209, v210\nv_mfma_f32_16x16x32_bf16 a[20:23], v[92:95], v[168:171], a[20:23]\nds_write_b128 v202, a[8:11] offset:2048\nv_cmp_gt_i32 vcc, 0, v209\nv_mfma_f32_16x16x32_bf16 a[28:31], v[124:127], v[168:171], a[28:31]\n" \
"s_cbranch_vccnz .Lrd_tre14%=\n.Lrd_tl14%=:\ns_waitcnt vmcnt(12)\nv_mfma_f32_16x16x32_bf16 a[4:7], v[128:131], v[172:175], a[4:7]\ns_add_u32 s60, s41, 524288\nbuffer_load_dwordx4 v[16:19], v196, %[rs], s60 offen offset:0 sc1\nv_bitop3_b32 v209, v176, v178, s61 bitop3:0x7e\nv_mfma_f32_16x16x32_bf16 a[12:15], v[136:139], v[172:175], a[12:15]\n" \
"v_bitop3_b32 v210, v177, v179, s61 bitop3:0x7e\nv_or_b32 v209, v209, v210\nv_mfma_f32_16x16x32_bf16 a[20:23], v[144:147], v[172:175], a[20:23]\nds_write_b128 v202, a[16:19] offset:4096\nv_cmp_gt_i32 vcc, 0, v209\nv_mfma_f32_16x16x32_bf16 a[28:31], v[152:155], v[172:175], a[28:31]\ns_cbranch_vccnz .Lrd_tre15%=\n.Lrd_tl15%=:\n" \
"v_mfma_f32_16x16x32_bf16 a[4:7], v[132:135], v[176:179], a[4:7]\ns_add_u32 s60, s41, 524288\nbuffer_load_dwordx4 v[48:51], v196, %[rs], s60 offen offset:1024 sc1\nv_lshlrev_b32 v216, 16, v212\nv_and_b32 v217, 0xffff0000, v212\nv_mfma_f32_16x16x32_bf16 a[12:15], v[140:143], v[176:179], a[12:15]\nv_lshlrev_b32 v218, 16, v213\nv_and_b32 v219, 0xffff0000, v213\n" \
"v_mfma_f32_16x16x32_bf16 a[20:23], v[148:151], v[176:179], a[20:23]\nds_write_b128 v202, a[24:27] offset:6144\nv_lshlrev_b32 v220, 16, v214\nv_and_b32 v221, 0xffff0000, v214\nv_mfma_f32_16x16x32_bf16 a[28:31], v[156:159], v[176:179], a[28:31]\ns_add_u32 s60, s41, 524288\nbuffer_load_dwordx4 v[80:83], v196, %[rs], s60 offen offset:2048 sc1\ns_add_u32 s60, s41, 655360\n" \
"ds_write_b128 v202, a[4:7] offset:1024\nbuffer_load_dwordx4 v[20:23], v196, %[rs], s60 offen offset:0 sc1\nds_write_b128 v202, a[12:15] offset:3072\nv_lshlrev_b32 v222, 16, v215\nv_and_b32 v223, 0xffff0000, v215\ns_add_u32 s60, s41, 655360\nbuffer_load_dwordx4 v[52:55], v196, %[rs], s60 offen offset:1024 sc1\nds_write_b128 v202, a[20:23] offset:5120\n" \
"s_add_u32 s60, s41, 655360\nbuffer_load_dwordx4 v[84:87], v196, %[rs], s60 offen offset:2048 sc1\nds_write_b128 v202, a[28:31] offset:7168\ns_waitcnt lgkmcnt(0)\ns_barrier\nds_read_b128 v[224:227], v203 offset:0\nds_read_b128 v[228:231], v203 offset:256\nds_read_b128 v[232:235], v203 offset:8192\n" \
"ds_read_b128 v[236:239], v203 offset:8448\nds_read_b128 v[240:243], v203 offset:16384\nds_read_b128 v[244:247], v203 offset:16640\nds_read_b128 v[248:251], v203 offset:24576\nds_read_b128 v[252:255], v203 offset:24832\ns_add_u32 s60, s41, 786432\nbuffer_load_dwordx4 v[24:27], v196, %[rs], s60 offen offset:0 sc1\ns_waitcnt lgkmcnt(4)\n" \
"v_pk_add_f32 v[224:225], v[224:225], v[232:233]\nv_pk_add_f32 v[226:227], v[226:227], v[234:235]\nv_pk_add_f32 v[228:229], v[228:229], v[236:237]\nv_pk_add_f32 v[230:231], v[230:231], v[238:239]\ns_add_u32 s60, s41, 786432\nbuffer_load_dwordx4 v[56:59], v196, %[rs], s60 offen offset:1024 sc1\ns_waitcnt lgkmcnt(2)\nv_pk_add_f32 v[224:225], v[224:225], v[240:241]\n" \
"v_pk_add_f32 v[226:227], v[226:227], v[242:243]\nv_pk_add_f32 v[228:229], v[228:229], v[244:245]\nv_pk_add_f32 v[230:231], v[230:231], v[246:247]\ns_add_u32 s60, s41, 786432\nbuffer_load_dwordx4 v[88:91], v196, %[rs], s60 offen offset:2048 sc1\ns_waitcnt lgkmcnt(0)\nv_pk_add_f32 v[224:225], v[224:225], v[248:249]\nv_pk_add_f32 v[226:227], v[226:227], v[250:251]\n" \
"v_pk_add_f32 v[228:229], v[228:229], v[252:253]\nv_pk_add_f32 v[230:231], v[230:231], v[254:255]\nv_pk_fma_f32 v[224:225], v[224:225], s[62:63], v[216:217]\nv_pk_fma_f32 v[226:227], v[226:227], s[62:63], v[218:219]\nv_pk_fma_f32 v[228:229], v[228:229], s[62:63], v[220:221]\nv_pk_fma_f32 v[230:231], v[230:231], s[62:63], v[222:223]\nv_cvt_pk_bf16_f32 v208, v224, v225\nv_cvt_pk_bf16_f32 v209, v226, v227\n" \
"v_cvt_pk_bf16_f32 v210, v228, v229\nv_cvt_pk_bf16_f32 v211, v230, v231\nbuffer_store_dwordx4 v[208:211], v204, %[rs], s42 offen sc1\nv_pk_add_f32 v[232:233], v[224:225], v[226:227]\nv_pk_add_f32 v[234:235], v[228:229], v[230:231]\nv_pk_add_f32 v[232:233], v[232:233], v[234:235]\ns_add_u32 s60, s41, 917504\nbuffer_load_dwordx4 v[28:31], v196, %[rs], s60 offen offset:0 sc1\n" \
"s_add_u32 s60, s41, 917504\nbuffer_load_dwordx4 v[60:63], v196, %[rs], s60 offen offset:1024 sc1\nv_add_f32 v232, v232, v233\ns_nop 1\nv_add_f32_dpp v233, v232, v232 row_ror:8 row_mask:0xf bank_mask:0xf\nv_add_f32_dpp v234, v232, v232 row_ror:8 row_mask:0xf bank_mask:0xf\ns_nop 1\nv_permlane16_swap_b32 v233, v234\n" \
"v_add_f32 v232, v233, v234\nv_add_f32 v233, v233, v234\ns_nop 1\nv_permlane32_swap_b32 v232, v233\nv_add_f32 v232, v232, v233\nv_mul_f32 v232, 0x3c800000, v232\nv_pk_add_f32 v[224:225], v[224:225], v[232:233] op_sel_hi:[1,0] neg_lo:[0,1] neg_hi:[0,1]\nv_pk_add_f32 v[226:227], v[226:227], v[232:233] op_sel_hi:[1,0] neg_lo:[0,1] neg_hi:[0,1]\n" \
"v_pk_add_f32 v[228:229], v[228:229], v[232:233] op_sel_hi:[1,0] neg_lo:[0,1] neg_hi:[0,1]\nv_pk_add_f32 v[230:231], v[230:231], v[232:233] op_sel_hi:[1,0] neg_lo:[0,1] neg_hi:[0,1]\ns_add_u32 s60, s41, 917504\nbuffer_load_dwordx4 v[92:95], v196, %[rs], s60 offen offset:2048 sc1\ns_add_u32 s60, s41, 0\nbuffer_load_dwordx4 v[96:99], v196, %[rs], s60 offen offset:3072 sc1\nv_pk_mul_f32 v[236:237], v[224:225], v[224:225]\nv_pk_mul_f32 v[238:239], v[226:227], v[226:227]\n" \
"v_pk_fma_f32 v[236:237], v[228:229], v[228:229], v[236:237]\nv_pk_fma_f32 v[238:239], v[230:231], v[230:231], v[238:239]\nv_pk_add_f32 v[236:237], v[236:237], v[238:239]\nv_add_f32 v236, v236, v237\ns_nop 1\nv_add_f32_dpp v237, v236, v236 row_ror:8 row_mask:0xf bank_mask:0xf\nv_add_f32_dpp v238, v236, v236 row_ror:8 row_mask:0xf bank_mask:0xf\ns_nop 1\n" \
"v_permlane16_swap_b32 v237, v238\nv_add_f32 v236, v237, v238\nv_add_f32 v237, v237, v238\ns_nop 1\nv_permlane32_swap_b32 v236, v237\nv_add_f32 v236, v236, v237\nv_mov_b32 v233, v236\nbuffer_store_dwordx2 v[232:233], v205, %[rs], s42 offen sc1\n" \
"s_mov_b32 s59, %[fa]\ns_add_u32 s60, s41, 131072\nbuffer_load_dwordx4 v[100:103], v196, %[rs], s60 offen offset:3072 sc1\nv_accvgpr_read_b32 v212, a33\nbuffer_load_dwordx2 v[214:215], v212, %[rs], 0 offen sc1\ns_add_u32 s60, s41, 262144\nbuffer_load_dwordx4 v[104:107], v196, %[rs], s60 offen offset:3072 sc1\ns_add_u32 s60, s41, 393216\n" \
"buffer_load_dwordx4 v[108:111], v196, %[rs], s60 offen offset:3072 sc1\ns_add_u32 s60, s41, 524288\nbuffer_load_dwordx4 v[112:115], v196, %[rs], s60 offen offset:3072 sc1\ns_add_u32 s60, s41, 655360\nbuffer_load_dwordx4 v[116:119], v196, %[rs], s60 offen offset:3072 sc1\ns_add_u32 s60, s41, 786432\nbuffer_load_dwordx4 v[120:123], v196, %[rs], s60 offen offset:3072 sc1\ns_add_u32 s60, s41, 917504\n" \
"buffer_load_dwordx4 v[124:127], v196, %[rs], s60 offen offset:3072 sc1\ns_cmp_ge_u32 s40, 64\ns_cbranch_scc1 .Lrd_fin%=\n.Lrd_ychk%=:\ns_and_b32 s44, s40, 1\ns_bfe_u32 s55, s40, 0x10001\ns_mul_i32 s54, s44, 0x80008001\ns_mul_i32 s61, s55, 0x80008000\n" \
"s_lshl_b32 s58, s44, 17\ns_add_u32 s58, s58, %[st]\ns_add_u32 s44, s40, 1\ns_and_b32 s44, s44, 3\ns_lshl_b32 s44, s44, 15\nv_xor_b32 v206, s44, v200\nv_mov_b64 v[180:181], 0\nv_mov_b64 v[182:183], 0\n" \
"v_mov_b64 v[184:185], 0\nv_mov_b64 v[186:187], 0\nv_mov_b64 v[188:189], 0\nv_mov_b64 v[190:191], 0\nv_mov_b64 v[192:193], 0\nv_mov_b64 v[194:195], 0\ns_waitcnt vmcnt(6)\n.Lrd_sg0%=:\n" \
"s_memrealtime s[68:69]\nv_cmp_ne_u32 vcc, s40, v214\ns_cbranch_vccnz .Lrd_sg1%=\nv_max_u32_dpp v215, v215, v215 quad_perm:[1,0,3,2] row_mask:0xf bank_mask:0xf\ns_nop 1\nv_max_u32_dpp v215, v215, v215 quad_perm:[2,3,0,1] row_mask:0xf bank_mask:0xf\ns_nop 1\nv_max_u32_dpp v215, v215, v215 row_half_mirror row_mask:0xf bank_mask:0xf\n" \
"s_nop 0\nv_readlane_b32 s86, v215, 0\nv_readlane_b32 s87, v215, 8\ns_add_u32 s86, s86, %[du]\n.Lrd_tw626%=:\ns_waitcnt lgkmcnt(0)\ns_sub_u32 s44, s86, s68\ns_add_u32 s44, s44, 11\n" \
"s_cmp_lt_u32 s44, 250\ns_cbranch_scc1 .Lrd_tw626x%=\nbuffer_load_dwordx4 v[128:131], v196, %[rs], %[su] offen offset:0 sc1\nbuffer_load_dwordx4 v[132:135], v196, %[rs], %[su] offen offset:1024 sc1\ns_memrealtime s[68:69]\nbuffer_load_dwordx4 v[136:139], v196, %[rs], %[su] offen offset:2048 sc1\nbuffer_load_dwordx4 v[140:143], v196, %[rs], %[su] offen offset:3072 sc1\n.Lrd_tw637%=:\n" \
"s_waitcnt lgkmcnt(0)\ns_sub_u32 s44, s86, s68\ns_add_u32 s44, s44, 21\ns_cmp_lt_u32 s44, 250\ns_cbranch_scc1 .Lrd_tw637x%=\nbuffer_load_dwordx4 v[144:147], v196, %[rs], s57 offen offset:0 sc1\nbuffer_load_dwordx4 v[148:151], v196, %[rs], s57 offen offset:1024 sc1\ns_memrealtime s[68:69]\n" \
"buffer_load_dwordx4 v[152:155], v196, %[rs], s57 offen offset:2048 sc1\nbuffer_load_dwordx4 v[156:159], v196, %[rs], s57 offen offset:3072 sc1\nbuffer_store_dword v207, off, %[rs], s59 sc1\ns_mov_b32 s59, -2147483648\n.Lrd_tw650%=:\ns_waitcnt lgkmcnt(0)\ns_sub_u32 s44, s87, s68\ns_add_u32 s44, s44, 24\n" \
"s_cmp_lt_u32 s44, 250\ns_cbranch_scc1 .Lrd_tw650x%=\nbuffer_load_dwordx4 v[160:163], v196, %[rs], %[sp] offen offset:0 sc1\nbuffer_load_dwordx4 v[164:167], v196, %[rs], %[sp] offen offset:1024 sc1\nbuffer_load_dwordx4 v[168:171], v196, %[rs], %[sp] offen offset:2048 sc1\nbuffer_load_dwordx4 v[172:175], v196, %[rs], %[sp] offen offset:3072 sc1\nbuffer_load_dwordx4 v[176:179], v196, %[rs], s56 offen offset:0 sc1\nv_bitop3_b32 v209, v0, v2, s55 bitop3:0x7e\n" \
"v_bitop3_b32 v210, v32, v34, s55 bitop3:0x7e\nv_or3_b32 v208, v209, v210, 0\nv_bitop3_b32 v209, v64, v66, s55 bitop3:0x7e\nv_bitop3_b32 v210, v4, v6, s55 bitop3:0x7e\nv_or3_b32 v208, v209, v210, v208\nv_bitop3_b32 v209, v36, v38, s55 bitop3:0x7e\nv_bitop3_b32 v210, v68, v70, s55 bitop3:0x7e\nv_or3_b32 v208, v209, v210, v208\n" \
"v_bitop3_b32 v209, v8, v10, s55 bitop3:0x7e\nv_bitop3_b32 v210, v40, v42, s55 bitop3:0x7e\nv_or3_b32 v208, v209, v210, v208\nv_bitop3_b32 v209, v72, v74, s55 bitop3:0x7e\nv_bitop3_b32 v210, v12, v14, s55 bitop3:0x7e\nv_or3_b32 v208, v209, v210, v208\nv_bitop3_b32 v209, v44, v46, s55 bitop3:0x7e\nv_bitop3_b32 v210, v76, v78, s55 bitop3:0x7e\n" \
"v_or3_b32 v208, v209, v210, v208\nv_bitop3_b32 v209, v16, v18, s55 bitop3:0x7e\nv_bitop3_b32 v210, v48, v50, s55 bitop3:0x7e\nv_or3_b32 v208, v209, v210, v208\nv_bitop3_b32 v209, v80, v82, s55 bitop3:0x7e\nv_bitop3_b32 v210, v20, v22, s55 bitop3:0x7e\nv_or3_b32 v208, v209, v210, v208\nv_bitop3_b32 v209, v52, v54, s55 bitop3:0x7e\n" \
"v_bitop3_b32 v210, v84, v86, s55 bitop3:0x7e\nv_or3_b32 v208, v209, v210, v208\nv_bitop3_b32 v209, v24, v26, s55 bitop3:0x7e\nv_bitop3_b32 v210, v56, v58, s55 bitop3:0x7e\nv_or3_b32 v208, v209, v210, v208\nv_bitop3_b32 v209, v88, v90, s55 bitop3:0x7e\nv_bitop3_b32 v210, v28, v30, s55 bitop3:0x7e\nv_or3_b32 v208, v209, v210, v208\n" \
"v_bitop3_b32 v209, v60, v62, s55 bitop3:0x7e\nv_bitop3_b32 v210, v92, v94, s55 bitop3:0x7e\nv_or3_b32 v208, v209, v210, v208\ns_waitcnt vmcnt(18)\nv_bitop3_b32 v209, v96, v98, s55 bitop3:0x7e\nv_bitop3_b32 v210, v100, v102, s55 bitop3:0x7e\nv_or3_b32 v208, v209, v210, v208\nv_bitop3_b32 v209, v104, v106, s55 bitop3:0x7e\n" \
"v_bitop3_b32 v210, v108, v110, s55 bitop3:0x7e\nv_or3_b32 v208, v209, v210, v208\ns_waitcnt vmcnt(14)\nv_bitop3_b32 v209, v112, v114, s55 bitop3:0x7e\nv_bitop3_b32 v210, v116, v118, s55 bitop3:0x7e\nv_or3_b32 v208, v209, v210, v208\nv_bitop3_b32 v209, v120, v122, s55 bitop3:0x7e\nv_bitop3_b32 v210, v124, v126, s55 bitop3:0x7e\n" \
"v_or3_b32 v208, v209, v210, v208\nv_and_b32 v208, 1, v208\nv_cmp_ne_u32 vcc, 0, v208\ns_cbranch_vccnz .Lrd_yre%=\ns_branch .Lrd_ra%=\n.Lrd_sg1%=:\nbuffer_load_dwordx2 v[214:215], v212, %[rs], 0 offen sc1\ns_waitcnt vmcnt(0)\n" \
"buffer_store_dword v207, off, %[rs], s59 sc1\ns_mov_b32 s59, -2147483648\ns_add_u32 s43, s43, 1\ns_cmp_lt_u32 s43, 262144\ns_cbranch_scc1 .Lrd_sg0%=\ns_branch .Lrd_abort%=\n.Lrd_tw626x%=:\ns_memrealtime s[68:69]\n" \
"s_branch .Lrd_tw626%=\n.Lrd_tw637x%=:\ns_memrealtime s[68:69]\ns_branch .Lrd_tw637%=\n.Lrd_tw650x%=:\ns_memrealtime s[68:69]\ns_branch .Lrd_tw650%=\n.Lrd_fin%=:\n" \
"s_waitcnt vmcnt(0)\nbuffer_store_dword v207, off, %[rs], s59 sc1\ns_mov_b32 s59, -2147483648\ns_branch .Lrd_end%=\n.Lrd_yre%=:\ns_mov_b64 exec, vcc\ns_waitcnt vmcnt(0)\ns_add_u32 s43, s43, 1\n" \
"s_cmp_lt_u32 s43, 262144\ns_cbranch_scc1 .Lrd_yld%=\ns_branch .Lrd_abort%=\n.Lrd_rb%=:\ns_mov_b64 exec, s[46:47]\ns_cbranch_execz .Lrd_rs0%=\nbuffer_load_dwordx4 v[160:163], v196, %[rs], %[sp] offen offset:0 sc1\n.Lrd_rs0%=:\n" \
"s_mov_b64 exec, s[48:49]\ns_cbranch_execz .Lrd_rs1%=\nbuffer_load_dwordx4 v[164:167], v196, %[rs], %[sp] offen offset:1024 sc1\n.Lrd_rs1%=:\ns_mov_b64 exec, s[50:51]\ns_cbranch_execz .Lrd_rs2%=\nbuffer_load_dwordx4 v[168:171], v196, %[rs], %[sp] offen offset:2048 sc1\n.Lrd_rs2%=:\n" \
"s_mov_b64 exec, s[52:53]\ns_cbranch_execz .Lrd_rs3%=\nbuffer_load_dwordx4 v[172:175], v196, %[rs], %[sp] offen offset:3072 sc1\n.Lrd_rs3%=:\ns_mov_b64 exec, s[64:65]\ns_cbranch_execz .Lrd_rs4%=\nbuffer_load_dwordx4 v[176:179], v196, %[rs], s56 offen offset:0 sc1\n.Lrd_rs4%=:\n" \
"s_mov_b64 exec, s[86:87]\ns_cbranch_execz .Lrd_rs5%=\nbuffer_load_dwordx4 v[128:131], v196, %[rs], %[su] offen offset:0 sc1\nbuffer_load_dwordx4 v[136:139], v196, %[rs], %[su] offen offset:2048 sc1\n.Lrd_rs5%=:\ns_mov_b64 exec, s[88:89]\ns_cbranch_execz .Lrd_rs6%=\nbuffer_load_dwordx4 v[144:147], v196, %[rs], s57 offen offset:0 sc1\n" \
"buffer_load_dwordx4 v[152:155], v196, %[rs], s57 offen offset:2048 sc1\n.Lrd_rs6%=:\ns_mov_b64 exec, s[68:69]\ns_cbranch_execz .Lrd_rs7%=\nbuffer_load_dwordx4 v[132:135], v196, %[rs], %[su] offen offset:1024 sc1\nbuffer_load_dwordx4 v[140:143], v196, %[rs], %[su] offen offset:3072 sc1\n.Lrd_rs7%=:\ns_mov_b64 exec, s[62:63]\n" \
"s_cbranch_execz .Lrd_rs8%=\nbuffer_load_dwordx4 v[148:151], v196, %[rs], s57 offen offset:1024 sc1\nbuffer_load_dwordx4 v[156:159], v196, %[rs], s57 offen offset:3072 sc1\n.Lrd_rs8%=:\ns_mov_b64 exec, -1\nv_mov_b64 v[180:181], 0\nv_mov_b64 v[182:183], 0\nv_mov_b64 v[184:185], 0\n" \
"v_mov_b64 v[186:187], 0\nv_mov_b64 v[188:189], 0\nv_mov_b64 v[190:191], 0\nv_mov_b64 v[192:193], 0\nv_mov_b64 v[194:195], 0\ns_waitcnt vmcnt(0)\ns_add_u32 s43, s43, 1\ns_cmp_lt_u32 s43, 262144\n" \
"s_cbranch_scc1 .Lrd_ra%=\ns_branch .Lrd_abort%=\n.Lrd_y1%=:\nv_accvgpr_read_b32 v206, a32\nbuffer_load_dword v208, v206, %[rs], 0 offen sc1\ns_waitcnt vmcnt(0)\nv_cmp_gt_i32 vcc, 1, v208\ns_cbranch_vccz .Lrd_y1r%=\n" \
"s_add_u32 s43, s43, 1\ns_cmp_lt_u32 s43, 262144\ns_cbranch_scc1 .Lrd_y1%=\ns_branch .Lrd_abort%=\n.Lrd_tre0%=:\ns_add_u32 s60, s58, 16384\nbuffer_load_dwordx4 v[212:215], v196, %[rs], s60 offen offset:0 sc1\ns_waitcnt vmcnt(0)\n" \
"v_bitop3_b32 v209, v212, v214, s61 bitop3:0x7e\nv_cmp_gt_i32 vcc, 0, v209\ns_cbranch_vccz .Lrd_tl0%=\ns_add_u32 s43, s43, 1\ns_cmp_lt_u32 s43, 262144\ns_cbranch_scc1 .Lrd_tre0%=\ns_branch .Lrd_abort%=\n.Lrd_tre1%=:\n" \
"s_add_u32 s60, s58, 0\nbuffer_load_dwordx4 v[216:219], v196, %[rs], s60 offen offset:0 sc1\ns_waitcnt vmcnt(0)\nv_bitop3_b32 v209, v216, v218, s61 bitop3:0x7e\nv_cmp_gt_i32 vcc, 0, v209\ns_cbranch_vccz .Lrd_tl1%=\ns_add_u32 s43, s43, 1\ns_cmp_lt_u32 s43, 262144\n" \
"s_cbranch_scc1 .Lrd_tre1%=\ns_branch .Lrd_abort%=\n.Lrd_tre2%=:\ns_add_u32 s60, s58, 0\nbuffer_load_dwordx4 v[220:223], v196, %[rs], s60 offen offset:1024 sc1\ns_waitcnt vmcnt(0)\nv_bitop3_b32 v209, v220, v222, s61 bitop3:0x7e\nv_cmp_gt_i32 vcc, 0, v209\n" \
"s_cbranch_vccz .Lrd_tl2%=\ns_add_u32 s43, s43, 1\ns_cmp_lt_u32 s43, 262144\ns_cbranch_scc1 .Lrd_tre2%=\ns_branch .Lrd_abort%=\n.Lrd_tre3%=:\ns_add_u32 s60, s58, 16384\nbuffer_load_dwordx4 v[224:227], v196, %[rs], s60 offen offset:1024 sc1\n" \
"s_waitcnt vmcnt(0)\nv_bitop3_b32 v209, v224, v226, s61 bitop3:0x7e\nv_cmp_gt_i32 vcc, 0, v209\ns_cbranch_vccz .Lrd_tl3%=\ns_add_u32 s43, s43, 1\ns_cmp_lt_u32 s43, 262144\ns_cbranch_scc1 .Lrd_tre3%=\ns_branch .Lrd_abort%=\n" \
".Lrd_tre4%=:\ns_add_u32 s60, s58, 0\nbuffer_load_dwordx4 v[228:231], v196, %[rs], s60 offen offset:2048 sc1\ns_waitcnt vmcnt(0)\nv_bitop3_b32 v209, v228, v230, s61 bitop3:0x7e\nv_cmp_gt_i32 vcc, 0, v209\ns_cbranch_vccz .Lrd_tl4%=\ns_add_u32 s43, s43, 1\n" \
"s_cmp_lt_u32 s43, 262144\ns_cbranch_scc1 .Lrd_tre4%=\ns_branch .Lrd_abort%=\n.Lrd_tre5%=:\ns_add_u32 s60, s58, 0\nbuffer_load_dwordx4 v[232:235], v196, %[rs], s60 offen offset:3072 sc1\ns_waitcnt vmcnt(0)\nv_bitop3_b32 v209, v232, v234, s61 bitop3:0x7e\n" \
"v_cmp_gt_i32 vcc, 0, v209\ns_cbranch_vccz .Lrd_tl5%=\ns_add_u32 s43, s43, 1\ns_cmp_lt_u32 s43, 262144\ns_cbranch_scc1 .Lrd_tre5%=\ns_branch .Lrd_abort%=\n.Lrd_tre6%=:\ns_add_u32 s60, s58, 16384\n" \
"buffer_load_dwordx4 v[236:239], v196, %[rs], s60 offen offset:2048 sc1\ns_waitcnt vmcnt(0)\nv_bitop3_b32 v209, v236, v238, s61 bitop3:0x7e\nv_cmp_gt_i32 vcc, 0, v209\ns_cbranch_vccz .Lrd_tl6%=\ns_add_u32 s43, s43, 1\ns_cmp_lt_u32 s43, 262144\ns_cbranch_scc1 .Lrd_tre6%=\n" \
"s_branch .Lrd_abort%=\n.Lrd_tre7%=:\ns_add_u32 s60, s58, 4096\nbuffer_load_dwordx4 v[240:243], v196, %[rs], s60 offen offset:0 sc1\ns_waitcnt vmcnt(0)\nv_bitop3_b32 v209, v240, v242, s61 bitop3:0x7e\nv_cmp_gt_i32 vcc, 0, v209\ns_cbranch_vccz .Lrd_tl7%=\n" \
"s_add_u32 s43, s43, 1\ns_cmp_lt_u32 s43, 262144\ns_cbranch_scc1 .Lrd_tre7%=\ns_branch .Lrd_abort%=\n.Lrd_tre8%=:\ns_add_u32 s60, s58, 4096\nbuffer_load_dwordx4 v[244:247], v196, %[rs], s60 offen offset:1024 sc1\ns_waitcnt vmcnt(0)\n" \
"v_bitop3_b32 v209, v244, v246, s61 bitop3:0x7e\nv_cmp_gt_i32 vcc, 0, v209\ns_cbranch_vccz .Lrd_tl8%=\ns_add_u32 s43, s43, 1\ns_cmp_lt_u32 s43, 262144\ns_cbranch_scc1 .Lrd_tre8%=\ns_branch .Lrd_abort%=\n.Lrd_tre9%=:\n" \
"s_add_u32 s60, s58, 16384\nbuffer_load_dwordx4 v[248:251], v196, %[rs], s60 offen offset:3072 sc1\ns_waitcnt vmcnt(0)\nv_bitop3_b32 v209, v248, v250, s61 bitop3:0x7e\nv_cmp_gt_i32 vcc, 0, v209\ns_cbranch_vccz .Lrd_tl9%=\ns_add_u32 s43, s43, 1\ns_cmp_lt_u32 s43, 262144\n" \
"s_cbranch_scc1 .Lrd_tre9%=\ns_branch .Lrd_abort%=\n.Lrd_tre10%=:\ns_add_u32 s60, s58, 20480\nbuffer_load_dwordx4 v[252:255], v196, %[rs], s60 offen offset:0 sc1\ns_waitcnt vmcnt(0)\nv_bitop3_b32 v209, v252, v254, s61 bitop3:0x7e\nv_bitop3_b32 v210, v253, v255, s61 bitop3:0x7e\n" \
"v_or_b32 v209, v209, v210\nv_cmp_gt_i32 vcc, 0, v209\ns_cbranch_vccz .Lrd_tl10%=\ns_add_u32 s43, s43, 1\ns_cmp_lt_u32 s43, 262144\ns_cbranch_scc1 .Lrd_tre10%=\ns_branch .Lrd_abort%=\n.Lrd_tre11%=:\n" \
"s_add_u32 s60, s58, 20480\nbuffer_load_dwordx4 v[160:163], v196, %[rs], s60 offen offset:2048 sc1\ns_waitcnt vmcnt(0)\nv_bitop3_b32 v209, v160, v162, s61 bitop3:0x7e\nv_bitop3_b32 v210, v161, v163, s61 bitop3:0x7e\nv_or_b32 v209, v209, v210\nv_cmp_gt_i32 vcc, 0, v209\ns_cbranch_vccz .Lrd_tl11%=\n" \
"s_add_u32 s43, s43, 1\ns_cmp_lt_u32 s43, 262144\ns_cbranch_scc1 .Lrd_tre11%=\ns_branch .Lrd_abort%=\n.Lrd_tre12%=:\ns_add_u32 s60, s58, 4096\nbuffer_load_dwordx4 v[164:167], v196, %[rs], s60 offen offset:2048 sc1\ns_waitcnt vmcnt(0)\n" \
"v_bitop3_b32 v209, v164, v166, s61 bitop3:0x7e\nv_cmp_gt_i32 vcc, 0, v209\ns_cbranch_vccz .Lrd_tl12%=\ns_add_u32 s43, s43, 1\ns_cmp_lt_u32 s43, 262144\ns_cbranch_scc1 .Lrd_tre12%=\ns_branch .Lrd_abort%=\n.Lrd_tre13%=:\n" \
"s_add_u32 s60, s58, 4096\nbuffer_load_dwordx4 v[168:171], v196, %[rs], s60 offen offset:3072 sc1\ns_waitcnt vmcnt(0)\nv_bitop3_b32 v209, v168, v170, s61 bitop3:0x7e\nv_cmp_gt_i32 vcc, 0, v209\ns_cbranch_vccz .Lrd_tl13%=\ns_add_u32 s43, s43, 1\ns_cmp_lt_u32 s43, 262144\n" \
"s_cbranch_scc1 .Lrd_tre13%=\ns_branch .Lrd_abort%=\n.Lrd_tre14%=:\ns_add_u32 s60, s58, 20480\nbuffer_load_dwordx4 v[172:175], v196, %[rs], s60 offen offset:1024 sc1\ns_waitcnt vmcnt(0)\nv_bitop3_b32 v209, v172, v174, s61 bitop3:0x7e\nv_bitop3_b32 v210, v173, v175, s61 bitop3:0x7e\n" \
"v_or_b32 v209, v209, v210\nv_cmp_gt_i32 vcc, 0, v209\ns_cbranch_vccz .Lrd_tl14%=\ns_add_u32 s43, s43, 1\ns_cmp_lt_u32 s43, 262144\ns_cbranch_scc1 .Lrd_tre14%=\ns_branch .Lrd_abort%=\n.Lrd_tre15%=:\n" \
"s_add_u32 s60, s58, 20480\nbuffer_load_dwordx4 v[176:179], v196, %[rs], s60 offen offset:3072 sc1\ns_waitcnt vmcnt(0)\nv_bitop3_b32 v209, v176, v178, s61 bitop3:0x7e\nv_bitop3_b32 v210, v177, v179, s61 bitop3:0x7e\nv_or_b32 v209, v209, v210\nv_cmp_gt_i32 vcc, 0, v209\ns_cbranch_vccz .Lrd_tl15%=\n" \
"s_add_u32 s43, s43, 1\ns_cmp_lt_u32 s43, 262144\ns_cbranch_scc1 .Lrd_tre15%=\ns_branch .Lrd_abort%=\n.Lrd_abort%=:\ns_mov_b64 exec, -1\nv_mov_b32 v16, 0\ns_movk_i32 s44, 6400\n" \
"buffer_store_dword v16, off, %[rs], s44 sc1\n.Lrd_end%=:\ns_waitcnt vmcnt(0) lgkmcnt(0)\n" \
""
#define GG_ASM \
"v_mbcnt_lo_u32_b32 v255, -1, 0\nv_mbcnt_hi_u32_b32 v255, -1, v255\nv_lshlrev_b32 v255, 2, v255\nv_add_u32 v255, %[tb], v255\nds_read_b32 v0, v255 offset:0\nds_read_b32 v1, v255 offset:256\nds_read_b32 v2, v255 offset:512\nds_read_b32 v3, v255 offset:768\n" \
"ds_read_b32 v4, v255 offset:1024\nds_read_b32 v5, v255 offset:1280\nds_read_b32 v92, v255 offset:1536\nds_read_b32 v93, v255 offset:1792\nds_read_b32 v94, v255 offset:2048\ns_waitcnt lgkmcnt(0)\nv_mov_b32 v6, 0x3f803f80\nv_mov_b32 v7, 0x3f803f80\n" \
"s_add_u32 m0, %[lw], 16384\ns_add_u32 s44, %[wo], 0\nbuffer_load_dwordx4 v92, %[rw], s44 offen nt lds\ns_add_u32 m0, %[lw], 17408\ns_add_u32 s44, %[wo], 57344\nbuffer_load_dwordx4 v92, %[rw], s44 offen nt lds\ns_add_u32 m0, %[lw], 18432\ns_add_u32 s44, %[wo], 114688\n" \
"buffer_load_dwordx4 v92, %[rw], s44 offen nt lds\ns_add_u32 m0, %[lw], 19456\ns_add_u32 s44, %[wo], 172032\nbuffer_load_dwordx4 v92, %[rw], s44 offen nt lds\ns_add_u32 m0, %[lw], 20480\ns_add_u32 s44, %[wo], 229376\nbuffer_load_dwordx4 v92, %[rw], s44 offen nt lds\ns_add_u32 m0, %[lw], 21504\n" \
"s_add_u32 s44, %[wo], 286720\nbuffer_load_dwordx4 v92, %[rw], s44 offen nt lds\ns_add_u32 m0, %[lw], 22528\ns_add_u32 s44, %[wo], 344064\nbuffer_load_dwordx4 v92, %[rw], s44 offen nt lds\ns_add_u32 m0, %[lw], 23552\ns_add_u32 s44, %[wo], 401408\nbuffer_load_dwordx4 v92, %[rw], s44 offen nt lds\n" \
"s_add_u32 m0, %[tb], 0\ns_add_u32 s44, %[wo], 1835008\nbuffer_load_dwordx4 v92, %[rw], s44 offen nt lds\ns_add_u32 m0, %[tb], 1024\ns_add_u32 s44, %[wo], 1892352\nbuffer_load_dwordx4 v92, %[rw], s44 offen nt lds\ns_add_u32 m0, %[tb], 2048\ns_add_u32 s44, %[wo], 1949696\n" \
"buffer_load_dwordx4 v92, %[rw], s44 offen nt lds\ns_add_u32 m0, %[tb], 3072\ns_add_u32 s44, %[wo], 2007040\nbuffer_load_dwordx4 v92, %[rw], s44 offen nt lds\ns_add_u32 m0, %[tb], 4096\ns_add_u32 s44, %[wo], 2064384\nbuffer_load_dwordx4 v92, %[rw], s44 offen nt lds\ns_add_u32 m0, %[tb], 5120\n" \
"s_add_u32 s44, %[wo], 2121728\nbuffer_load_dwordx4 v92, %[rw], s44 offen nt lds\ns_add_u32 m0, %[tb], 6144\ns_add_u32 s44, %[wo], 2179072\nbuffer_load_dwordx4 v92, %[rw], s44 offen nt lds\ns_add_u32 m0, %[tb], 7168\ns_add_u32 s44, %[wo], 2236416\nbuffer_load_dwordx4 v92, %[rw], s44 offen nt lds\n" \
"s_add_u32 m0, %[tb], 8192\ns_add_u32 s44, %[wo], 3670016\nbuffer_load_dwordx4 v92, %[rw], s44 offen nt lds\ns_add_u32 m0, %[tb], 9216\ns_add_u32 s44, %[wo], 3727360\nbuffer_load_dwordx4 v92, %[rw], s44 offen nt lds\ns_add_u32 m0, %[tb], 10240\ns_add_u32 s44, %[wo], 3784704\n" \
"buffer_load_dwordx4 v92, %[rw], s44 offen nt lds\ns_add_u32 m0, %[tb], 11264\ns_add_u32 s44, %[wo], 3842048\nbuffer_load_dwordx4 v92, %[rw], s44 offen nt lds\ns_add_u32 m0, %[lw], 0\ns_add_u32 s44, %[wo], 3899392\nbuffer_load_dwordx4 v92, %[rw], s44 offen nt lds\ns_add_u32 m0, %[lw], 1024\n" \
"s_add_u32 s44, %[wo], 3956736\nbuffer_load_dwordx4 v92, %[rw], s44 offen nt lds\ns_add_u32 m0, %[lw], 2048\ns_add_u32 s44, %[wo], 4014080\nbuffer_load_dwordx4 v92, %[rw], s44 offen nt lds\ns_add_u32 m0, %[lw], 3072\ns_add_u32 s44, %[wo], 4071424\nbuffer_load_dwordx4 v92, %[rw], s44 offen nt lds\n" \
"s_add_u32 m0, %[lw], 4096\ns_add_u32 s44, %[wo], 5505024\nbuffer_load_dwordx4 v92, %[rw], s44 offen nt lds\ns_add_u32 m0, %[lw], 5120\ns_add_u32 s44, %[wo], 5562368\nbuffer_load_dwordx4 v92, %[rw], s44 offen nt lds\ns_add_u32 m0, %[lw], 6144\ns_add_u32 s44, %[wo], 5619712\n" \
"buffer_load_dwordx4 v92, %[rw], s44 offen nt lds\ns_add_u32 m0, %[lw], 7168\ns_add_u32 s44, %[wo], 5677056\nbuffer_load_dwordx4 v92, %[rw], s44 offen nt lds\ns_add_u32 m0, %[lw], 8192\ns_add_u32 s44, %[wo], 5734400\nbuffer_load_dwordx4 v92, %[rw], s44 offen nt lds\ns_add_u32 m0, %[lw], 9216\n" \
"s_add_u32 s44, %[wo], 5791744\nbuffer_load_dwordx4 v92, %[rw], s44 offen nt lds\ns_add_u32 m0, %[lw], 10240\ns_add_u32 s44, %[wo], 5849088\nbuffer_load_dwordx4 v92, %[rw], s44 offen nt lds\ns_add_u32 m0, %[lw], 11264\ns_add_u32 s44, %[wo], 5906432\nbuffer_load_dwordx4 v92, %[rw], s44 offen nt lds\n" \
"s_waitcnt vmcnt(28)\ns_barrier\ns_add_u32 m0, %[lw], 12288\ns_add_u32 s44, %[wo], 458752\nbuffer_load_dwordx4 v92, %[rw], s44 offen nt lds\ns_add_u32 m0, %[lw], 13312\ns_add_u32 s44, %[wo], 516096\nbuffer_load_dwordx4 v92, %[rw], s44 offen nt lds\n" \
"s_add_u32 m0, %[lw], 14336\ns_add_u32 s44, %[wo], 573440\nbuffer_load_dwordx4 v92, %[rw], s44 offen nt lds\ns_add_u32 m0, %[lw], 15360\ns_add_u32 s44, %[wo], 630784\nbuffer_load_dwordx4 v92, %[rw], s44 offen nt lds\nds_read_b64_tr_b16 a[0:1], v94 offset:16384\nds_read_b64_tr_b16 a[2:3], v94 offset:16896\n" \
"ds_read_b64_tr_b16 a[8:9], v94 offset:16392\nds_read_b64_tr_b16 a[10:11], v94 offset:16904\nds_read_b64_tr_b16 a[16:17], v94 offset:40960\nds_read_b64_tr_b16 a[18:19], v94 offset:41472\nds_read_b64_tr_b16 a[24:25], v94 offset:40968\nds_read_b64_tr_b16 a[26:27], v94 offset:41480\ns_waitcnt lgkmcnt(0)\nv_mfma_f32_16x16x16_bf16 v[8:11], a[0:1], v[6:7], 0\n" \
"v_mfma_f32_16x16x16_bf16 v[12:15], a[8:9], v[6:7], 0\nv_mfma_f32_16x16x16_bf16 v[16:19], a[16:17], v[6:7], 0\nv_mfma_f32_16x16x16_bf16 v[20:23], a[24:25], v[6:7], 0\nv_mfma_f32_16x16x16_bf16 v[8:11], a[2:3], v[6:7], v[8:11]\nv_mfma_f32_16x16x16_bf16 v[12:15], a[10:11], v[6:7], v[12:15]\nv_mfma_f32_16x16x16_bf16 v[16:19], a[18:19], v[6:7], v[16:19]\nv_mfma_f32_16x16x16_bf16 v[20:23], a[26:27], v[6:7], v[20:23]\ns_waitcnt vmcnt(28)\n" \
"s_barrier\ns_add_u32 m0, %[lw], 16384\ns_add_u32 s44, %[wo], 688128\nbuffer_load_dwordx4 v92, %[rw], s44 offen nt lds\ns_add_u32 m0, %[lw], 17408\ns_add_u32 s44, %[wo], 745472\nbuffer_load_dwordx4 v92, %[rw], s44 offen nt lds\ns_add_u32 m0, %[lw], 18432\n" \
"s_add_u32 s44, %[wo], 802816\nbuffer_load_dwordx4 v92, %[rw], s44 offen nt lds\ns_add_u32 m0, %[lw], 19456\ns_add_u32 s44, %[wo], 860160\nbuffer_load_dwordx4 v92, %[rw], s44 offen nt lds\nds_read_b64_tr_b16 a[4:5], v94 offset:20480\nds_read_b64_tr_b16 a[6:7], v94 offset:20992\nds_read_b64_tr_b16 a[12:13], v94 offset:20488\n" \
"ds_read_b64_tr_b16 a[14:15], v94 offset:21000\nds_read_b64_tr_b16 a[20:21], v94 offset:45056\nds_read_b64_tr_b16 a[22:23], v94 offset:45568\nds_read_b64_tr_b16 a[28:29], v94 offset:45064\nds_read_b64_tr_b16 a[30:31], v94 offset:45576\ns_waitcnt lgkmcnt(0)\nv_mfma_f32_16x16x16_bf16 v[8:11], a[4:5], v[6:7], v[8:11]\nv_mfma_f32_16x16x16_bf16 v[12:15], a[12:13], v[6:7], v[12:15]\n" \
"v_mfma_f32_16x16x16_bf16 v[16:19], a[20:21], v[6:7], v[16:19]\nv_mfma_f32_16x16x16_bf16 v[20:23], a[28:29], v[6:7], v[20:23]\nv_mfma_f32_16x16x16_bf16 v[8:11], a[6:7], v[6:7], v[8:11]\nv_mfma_f32_16x16x16_bf16 v[12:15], a[14:15], v[6:7], v[12:15]\nv_mfma_f32_16x16x16_bf16 v[16:19], a[22:23], v[6:7], v[16:19]\nv_mfma_f32_16x16x16_bf16 v[20:23], a[30:31], v[6:7], v[20:23]\ns_waitcnt vmcnt(28)\ns_barrier\n" \
"s_add_u32 m0, %[lw], 20480\ns_add_u32 s44, %[wo], 917504\nbuffer_load_dwordx4 v92, %[rw], s44 offen nt lds\ns_add_u32 m0, %[lw], 21504\ns_add_u32 s44, %[wo], 974848\nbuffer_load_dwordx4 v92, %[rw], s44 offen nt lds\ns_add_u32 m0, %[lw], 22528\ns_add_u32 s44, %[wo], 1032192\n" \
"buffer_load_dwordx4 v92, %[rw], s44 offen nt lds\ns_add_u32 m0, %[lw], 23552\ns_add_u32 s44, %[wo], 1089536\nbuffer_load_dwordx4 v92, %[rw], s44 offen nt lds\nds_read_b64_tr_b16 a[104:105], v93 offset:0\nds_read_b64_tr_b16 a[106:107], v93 offset:512\nds_read_b64_tr_b16 a[112:113], v93 offset:8\nds_read_b64_tr_b16 a[114:115], v93 offset:520\n" \
"ds_read_b64_tr_b16 a[120:121], v93 offset:12288\nds_read_b64_tr_b16 a[122:123], v93 offset:12800\nds_read_b64_tr_b16 a[128:129], v93 offset:12296\nds_read_b64_tr_b16 a[130:131], v93 offset:12808\ns_waitcnt lgkmcnt(0)\nv_mfma_f32_16x16x16_bf16 v[8:11], a[104:105], v[6:7], v[8:11]\nv_mfma_f32_16x16x16_bf16 v[12:15], a[112:113], v[6:7], v[12:15]\nv_mfma_f32_16x16x16_bf16 v[16:19], a[120:121], v[6:7], v[16:19]\n" \
"v_mfma_f32_16x16x16_bf16 v[20:23], a[128:129], v[6:7], v[20:23]\nv_mfma_f32_16x16x16_bf16 v[8:11], a[106:107], v[6:7], v[8:11]\nv_mfma_f32_16x16x16_bf16 v[12:15], a[114:115], v[6:7], v[12:15]\nv_mfma_f32_16x16x16_bf16 v[16:19], a[122:123], v[6:7], v[16:19]\nv_mfma_f32_16x16x16_bf16 v[20:23], a[130:131], v[6:7], v[20:23]\ns_waitcnt vmcnt(28)\ns_barrier\ns_add_u32 m0, %[tb], 0\n" \
"s_add_u32 s44, %[wo], 1146880\nbuffer_load_dwordx4 v92, %[rw], s44 offen nt lds\ns_add_u32 m0, %[tb], 1024\ns_add_u32 s44, %[wo], 1204224\nbuffer_load_dwordx4 v92, %[rw], s44 offen nt lds\ns_add_u32 m0, %[tb], 2048\ns_add_u32 s44, %[wo], 1261568\nbuffer_load_dwordx4 v92, %[rw], s44 offen nt lds\n" \
"s_add_u32 m0, %[tb], 3072\ns_add_u32 s44, %[wo], 1318912\nbuffer_load_dwordx4 v92, %[rw], s44 offen nt lds\nds_read_b64_tr_b16 a[108:109], v93 offset:4096\nds_read_b64_tr_b16 a[110:111], v93 offset:4608\nds_read_b64_tr_b16 a[116:117], v93 offset:4104\nds_read_b64_tr_b16 a[118:119], v93 offset:4616\nds_read_b64_tr_b16 a[124:125], v93 offset:16384\n" \
"ds_read_b64_tr_b16 a[126:127], v93 offset:16896\nds_read_b64_tr_b16 a[132:133], v93 offset:16392\nds_read_b64_tr_b16 a[134:135], v93 offset:16904\ns_waitcnt lgkmcnt(0)\nv_mfma_f32_16x16x16_bf16 v[8:11], a[108:109], v[6:7], v[8:11]\nv_mfma_f32_16x16x16_bf16 v[12:15], a[116:117], v[6:7], v[12:15]\nv_mfma_f32_16x16x16_bf16 v[16:19], a[124:125], v[6:7], v[16:19]\nv_mfma_f32_16x16x16_bf16 v[20:23], a[132:133], v[6:7], v[20:23]\n" \
"v_mfma_f32_16x16x16_bf16 v[8:11], a[110:111], v[6:7], v[8:11]\nv_mfma_f32_16x16x16_bf16 v[12:15], a[118:119], v[6:7], v[12:15]\nv_mfma_f32_16x16x16_bf16 v[16:19], a[126:127], v[6:7], v[16:19]\nv_mfma_f32_16x16x16_bf16 v[20:23], a[134:135], v[6:7], v[20:23]\ns_waitcnt vmcnt(28)\ns_barrier\ns_add_u32 m0, %[tb], 4096\ns_add_u32 s44, %[wo], 1376256\n" \
"buffer_load_dwordx4 v92, %[rw], s44 offen nt lds\ns_add_u32 m0, %[tb], 5120\ns_add_u32 s44, %[wo], 1433600\nbuffer_load_dwordx4 v92, %[rw], s44 offen nt lds\ns_add_u32 m0, %[tb], 6144\ns_add_u32 s44, %[wo], 1490944\nbuffer_load_dwordx4 v92, %[rw], s44 offen nt lds\ns_add_u32 m0, %[tb], 7168\n" \
"s_add_u32 s44, %[wo], 1548288\nbuffer_load_dwordx4 v92, %[rw], s44 offen nt lds\nds_read_b64_tr_b16 a[208:209], v93 offset:8192\nds_read_b64_tr_b16 a[210:211], v93 offset:8704\nds_read_b64_tr_b16 a[216:217], v93 offset:8200\nds_read_b64_tr_b16 a[218:219], v93 offset:8712\nds_read_b64_tr_b16 a[224:225], v93 offset:20480\nds_read_b64_tr_b16 a[226:227], v93 offset:20992\n" \
"ds_read_b64_tr_b16 a[232:233], v93 offset:20488\nds_read_b64_tr_b16 a[234:235], v93 offset:21000\ns_waitcnt lgkmcnt(0)\nv_mfma_f32_16x16x16_bf16 v[8:11], a[208:209], v[6:7], v[8:11]\nv_mfma_f32_16x16x16_bf16 v[12:15], a[216:217], v[6:7], v[12:15]\nv_mfma_f32_16x16x16_bf16 v[16:19], a[224:225], v[6:7], v[16:19]\nv_mfma_f32_16x16x16_bf16 v[20:23], a[232:233], v[6:7], v[20:23]\nv_mfma_f32_16x16x16_bf16 v[8:11], a[210:211], v[6:7], v[8:11]\n" \
"v_mfma_f32_16x16x16_bf16 v[12:15], a[218:219], v[6:7], v[12:15]\nv_mfma_f32_16x16x16_bf16 v[16:19], a[226:227], v[6:7], v[16:19]\nv_mfma_f32_16x16x16_bf16 v[20:23], a[234:235], v[6:7], v[20:23]\ns_waitcnt vmcnt(28)\ns_barrier\ns_add_u32 m0, %[tb], 8192\ns_add_u32 s44, %[wo], 1605632\nbuffer_load_dwordx4 v92, %[rw], s44 offen nt lds\n" \
"s_add_u32 m0, %[tb], 9216\ns_add_u32 s44, %[wo], 1662976\nbuffer_load_dwordx4 v92, %[rw], s44 offen nt lds\ns_add_u32 m0, %[tb], 10240\ns_add_u32 s44, %[wo], 1720320\nbuffer_load_dwordx4 v92, %[rw], s44 offen nt lds\ns_add_u32 m0, %[tb], 11264\ns_add_u32 s44, %[wo], 1777664\n" \
"buffer_load_dwordx4 v92, %[rw], s44 offen nt lds\nds_read_b64_tr_b16 a[212:213], v94 offset:0\nds_read_b64_tr_b16 a[214:215], v94 offset:512\nds_read_b64_tr_b16 a[220:221], v94 offset:8\nds_read_b64_tr_b16 a[222:223], v94 offset:520\nds_read_b64_tr_b16 a[228:229], v94 offset:24576\nds_read_b64_tr_b16 a[230:231], v94 offset:25088\nds_read_b64_tr_b16 a[236:237], v94 offset:24584\n" \
"ds_read_b64_tr_b16 a[238:239], v94 offset:25096\ns_waitcnt lgkmcnt(0)\nv_mfma_f32_16x16x16_bf16 v[8:11], a[212:213], v[6:7], v[8:11]\nv_mfma_f32_16x16x16_bf16 v[12:15], a[220:221], v[6:7], v[12:15]\nv_mfma_f32_16x16x16_bf16 v[16:19], a[228:229], v[6:7], v[16:19]\nv_mfma_f32_16x16x16_bf16 v[20:23], a[236:237], v[6:7], v[20:23]\nv_mfma_f32_16x16x16_bf16 v[8:11], a[214:215], v[6:7], v[8:11]\nv_mfma_f32_16x16x16_bf16 v[12:15], a[222:223], v[6:7], v[12:15]\n" \
"v_mfma_f32_16x16x16_bf16 v[16:19], a[230:231], v[6:7], v[16:19]\nv_mfma_f32_16x16x16_bf16 v[20:23], a[238:239], v[6:7], v[20:23]\ns_waitcnt vmcnt(28)\ns_barrier\ns_add_u32 m0, %[lw], 0\ns_add_u32 s44, %[wo], 2293760\nbuffer_load_dwordx4 v92, %[rw], s44 offen nt lds\ns_add_u32 m0, %[lw], 1024\n" \
"s_add_u32 s44, %[wo], 2351104\nbuffer_load_dwordx4 v92, %[rw], s44 offen nt lds\ns_add_u32 m0, %[lw], 2048\ns_add_u32 s44, %[wo], 2408448\nbuffer_load_dwordx4 v92, %[rw], s44 offen nt lds\ns_add_u32 m0, %[lw], 3072\ns_add_u32 s44, %[wo], 2465792\nbuffer_load_dwordx4 v92, %[rw], s44 offen nt lds\n" \
"ds_read_b64_tr_b16 v[152:153], v94 offset:4096\nds_read_b64_tr_b16 v[154:155], v94 offset:4608\nds_read_b64_tr_b16 v[160:161], v94 offset:4104\nds_read_b64_tr_b16 v[162:163], v94 offset:4616\nds_read_b64_tr_b16 v[168:169], v94 offset:28672\nds_read_b64_tr_b16 v[170:171], v94 offset:29184\nds_read_b64_tr_b16 v[176:177], v94 offset:28680\nds_read_b64_tr_b16 v[178:179], v94 offset:29192\n" \
"s_waitcnt lgkmcnt(0)\nv_mfma_f32_16x16x16_bf16 v[8:11], v[152:153], v[6:7], v[8:11]\nv_mfma_f32_16x16x16_bf16 v[12:15], v[160:161], v[6:7], v[12:15]\nv_mfma_f32_16x16x16_bf16 v[16:19], v[168:169], v[6:7], v[16:19]\nv_mfma_f32_16x16x16_bf16 v[20:23], v[176:177], v[6:7], v[20:23]\nv_mfma_f32_16x16x16_bf16 v[8:11], v[154:155], v[6:7], v[8:11]\nv_mfma_f32_16x16x16_bf16 v[12:15], v[162:163], v[6:7], v[12:15]\nv_mfma_f32_16x16x16_bf16 v[16:19], v[170:171], v[6:7], v[16:19]\n" \
"v_mfma_f32_16x16x16_bf16 v[20:23], v[178:179], v[6:7], v[20:23]\ns_waitcnt vmcnt(28)\ns_barrier\ns_add_u32 m0, %[lw], 4096\ns_add_u32 s44, %[wo], 2523136\nbuffer_load_dwordx4 v92, %[rw], s44 offen nt lds\ns_add_u32 m0, %[lw], 5120\ns_add_u32 s44, %[wo], 2580480\n" \
"buffer_load_dwordx4 v92, %[rw], s44 offen nt lds\ns_add_u32 m0, %[lw], 6144\ns_add_u32 s44, %[wo], 2637824\nbuffer_load_dwordx4 v92, %[rw], s44 offen nt lds\ns_add_u32 m0, %[lw], 7168\ns_add_u32 s44, %[wo], 2695168\nbuffer_load_dwordx4 v92, %[rw], s44 offen nt lds\nds_read_b64_tr_b16 v[156:157], v94 offset:8192\n" \
"ds_read_b64_tr_b16 v[158:159], v94 offset:8704\nds_read_b64_tr_b16 v[164:165], v94 offset:8200\nds_read_b64_tr_b16 v[166:167], v94 offset:8712\nds_read_b64_tr_b16 v[172:173], v94 offset:32768\nds_read_b64_tr_b16 v[174:175], v94 offset:33280\nds_read_b64_tr_b16 v[180:181], v94 offset:32776\nds_read_b64_tr_b16 v[182:183], v94 offset:33288\ns_waitcnt lgkmcnt(0)\n" \
"v_mfma_f32_16x16x16_bf16 v[8:11], v[156:157], v[6:7], v[8:11]\nv_mfma_f32_16x16x16_bf16 v[12:15], v[164:165], v[6:7], v[12:15]\nv_mfma_f32_16x16x16_bf16 v[16:19], v[172:173], v[6:7], v[16:19]\nv_mfma_f32_16x16x16_bf16 v[20:23], v[180:181], v[6:7], v[20:23]\nv_mfma_f32_16x16x16_bf16 v[8:11], v[158:159], v[6:7], v[8:11]\nv_mfma_f32_16x16x16_bf16 v[12:15], v[166:167], v[6:7], v[12:15]\nv_mfma_f32_16x16x16_bf16 v[16:19], v[174:175], v[6:7], v[16:19]\nv_mfma_f32_16x16x16_bf16 v[20:23], v[182:183], v[6:7], v[20:23]\n" \
"s_waitcnt vmcnt(28)\ns_barrier\ns_add_u32 m0, %[lw], 8192\ns_add_u32 s44, %[wo], 2752512\nbuffer_load_dwordx4 v92, %[rw], s44 offen nt lds\ns_add_u32 m0, %[lw], 9216\ns_add_u32 s44, %[wo], 2809856\nbuffer_load_dwordx4 v92, %[rw], s44 offen nt lds\n" \
"s_add_u32 m0, %[lw], 10240\ns_add_u32 s44, %[wo], 2867200\nbuffer_load_dwordx4 v92, %[rw], s44 offen nt lds\ns_add_u32 m0, %[lw], 11264\ns_add_u32 s44, %[wo], 2924544\nbuffer_load_dwordx4 v92, %[rw], s44 offen nt lds\nds_read_b64_tr_b16 a[32:33], v94 offset:12288\nds_read_b64_tr_b16 a[34:35], v94 offset:12800\n" \
"ds_read_b64_tr_b16 v[88:89], v94 offset:12296\nds_read_b64_tr_b16 v[90:91], v94 offset:12808\nds_read_b64_tr_b16 a[40:41], v94 offset:36864\nds_read_b64_tr_b16 a[42:43], v94 offset:37376\nds_read_b64_tr_b16 a[48:49], v94 offset:36872\nds_read_b64_tr_b16 a[50:51], v94 offset:37384\ns_waitcnt lgkmcnt(0)\nv_mfma_f32_16x16x16_bf16 v[8:11], a[32:33], v[6:7], v[8:11]\n" \
"v_mfma_f32_16x16x16_bf16 v[12:15], v[88:89], v[6:7], v[12:15]\nv_mfma_f32_16x16x16_bf16 v[16:19], a[40:41], v[6:7], v[16:19]\nv_mfma_f32_16x16x16_bf16 v[20:23], a[48:49], v[6:7], v[20:23]\nv_mfma_f32_16x16x16_bf16 v[8:11], a[34:35], v[6:7], v[8:11]\nv_mfma_f32_16x16x16_bf16 v[12:15], v[90:91], v[6:7], v[12:15]\nv_mfma_f32_16x16x16_bf16 v[16:19], a[42:43], v[6:7], v[16:19]\nv_mfma_f32_16x16x16_bf16 v[20:23], a[50:51], v[6:7], v[20:23]\ns_waitcnt vmcnt(28)\n" \
"s_barrier\ns_add_u32 m0, %[lw], 12288\ns_add_u32 s44, %[wo], 2981888\nbuffer_load_dwordx4 v92, %[rw], s44 offen nt lds\ns_add_u32 m0, %[lw], 13312\ns_add_u32 s44, %[wo], 3039232\nbuffer_load_dwordx4 v92, %[rw], s44 offen nt lds\ns_add_u32 m0, %[lw], 14336\n" \
"s_add_u32 s44, %[wo], 3096576\nbuffer_load_dwordx4 v92, %[rw], s44 offen nt lds\ns_add_u32 m0, %[lw], 15360\ns_add_u32 s44, %[wo], 3153920\nbuffer_load_dwordx4 v92, %[rw], s44 offen nt lds\nds_read_b64_tr_b16 a[36:37], v94 offset:16384\nds_read_b64_tr_b16 a[38:39], v94 offset:16896\nds_read_b64_tr_b16 v[84:85], v94 offset:16392\n" \
"ds_read_b64_tr_b16 v[86:87], v94 offset:16904\nds_read_b64_tr_b16 a[44:45], v94 offset:40960\nds_read_b64_tr_b16 a[46:47], v94 offset:41472\nds_read_b64_tr_b16 a[52:53], v94 offset:40968\nds_read_b64_tr_b16 a[54:55], v94 offset:41480\ns_waitcnt lgkmcnt(0)\nv_mfma_f32_16x16x16_bf16 v[8:11], a[36:37], v[6:7], v[8:11]\nv_mfma_f32_16x16x16_bf16 v[12:15], v[84:85], v[6:7], v[12:15]\n" \
"v_mfma_f32_16x16x16_bf16 v[16:19], a[44:45], v[6:7], v[16:19]\nv_mfma_f32_16x16x16_bf16 v[20:23], a[52:53], v[6:7], v[20:23]\nv_mfma_f32_16x16x16_bf16 v[8:11], a[38:39], v[6:7], v[8:11]\nv_mfma_f32_16x16x16_bf16 v[12:15], v[86:87], v[6:7], v[12:15]\nv_mfma_f32_16x16x16_bf16 v[16:19], a[46:47], v[6:7], v[16:19]\nv_mfma_f32_16x16x16_bf16 v[20:23], a[54:55], v[6:7], v[20:23]\ns_waitcnt vmcnt(28)\ns_barrier\n" \
"s_add_u32 m0, %[lw], 16384\ns_add_u32 s44, %[wo], 3211264\nbuffer_load_dwordx4 v92, %[rw], s44 offen nt lds\ns_add_u32 m0, %[lw], 17408\ns_add_u32 s44, %[wo], 3268608\nbuffer_load_dwordx4 v92, %[rw], s44 offen nt lds\ns_add_u32 m0, %[lw], 18432\ns_add_u32 s44, %[wo], 3325952\n" \
"buffer_load_dwordx4 v92, %[rw], s44 offen nt lds\ns_add_u32 m0, %[lw], 19456\ns_add_u32 s44, %[wo], 3383296\nbuffer_load_dwordx4 v92, %[rw], s44 offen nt lds\nds_read_b64_tr_b16 a[56:57], v94 offset:20480\nds_read_b64_tr_b16 a[58:59], v94 offset:20992\nds_read_b64_tr_b16 a[64:65], v94 offset:20488\nds_read_b64_tr_b16 a[66:67], v94 offset:21000\n" \
"ds_read_b64_tr_b16 v[80:81], v94 offset:45056\nds_read_b64_tr_b16 v[82:83], v94 offset:45568\nds_read_b64_tr_b16 a[72:73], v94 offset:45064\nds_read_b64_tr_b16 a[74:75], v94 offset:45576\ns_waitcnt lgkmcnt(0)\nv_mfma_f32_16x16x16_bf16 v[8:11], a[56:57], v[6:7], v[8:11]\nv_mfma_f32_16x16x16_bf16 v[12:15], a[64:65], v[6:7], v[12:15]\nv_mfma_f32_16x16x16_bf16 v[16:19], v[80:81], v[6:7], v[16:19]\n" \
"v_mfma_f32_16x16x16_bf16 v[20:23], a[72:73], v[6:7], v[20:23]\nv_mfma_f32_16x16x16_bf16 v[8:11], a[58:59], v[6:7], v[8:11]\nv_mfma_f32_16x16x16_bf16 v[12:15], a[66:67], v[6:7], v[12:15]\nv_mfma_f32_16x16x16_bf16 v[16:19], v[82:83], v[6:7], v[16:19]\nv_mfma_f32_16x16x16_bf16 v[20:23], a[74:75], v[6:7], v[20:23]\ns_waitcnt vmcnt(28)\ns_barrier\ns_add_u32 m0, %[lw], 20480\n" \
"s_add_u32 s44, %[wo], 3440640\nbuffer_load_dwordx4 v92, %[rw], s44 offen nt lds\ns_add_u32 m0, %[lw], 21504\ns_add_u32 s44, %[wo], 3497984\nbuffer_load_dwordx4 v92, %[rw], s44 offen nt lds\ns_add_u32 m0, %[lw], 22528\ns_add_u32 s44, %[wo], 3555328\nbuffer_load_dwordx4 v92, %[rw], s44 offen nt lds\n" \
"s_add_u32 m0, %[lw], 23552\ns_add_u32 s44, %[wo], 3612672\nbuffer_load_dwordx4 v92, %[rw], s44 offen nt lds\nds_read_b64_tr_b16 a[60:61], v93 offset:0\nds_read_b64_tr_b16 a[62:63], v93 offset:512\nds_read_b64_tr_b16 a[68:69], v93 offset:8\nds_read_b64_tr_b16 a[70:71], v93 offset:520\nds_read_b64_tr_b16 v[76:77], v93 offset:12288\n" \
"ds_read_b64_tr_b16 v[78:79], v93 offset:12800\nds_read_b64_tr_b16 a[76:77], v93 offset:12296\nds_read_b64_tr_b16 a[78:79], v93 offset:12808\ns_waitcnt lgkmcnt(0)\nv_mfma_f32_16x16x16_bf16 v[8:11], a[60:61], v[6:7], v[8:11]\nv_mfma_f32_16x16x16_bf16 v[12:15], a[68:69], v[6:7], v[12:15]\nv_mfma_f32_16x16x16_bf16 v[16:19], v[76:77], v[6:7], v[16:19]\nv_mfma_f32_16x16x16_bf16 v[20:23], a[76:77], v[6:7], v[20:23]\n" \
"v_mfma_f32_16x16x16_bf16 v[8:11], a[62:63], v[6:7], v[8:11]\nv_mfma_f32_16x16x16_bf16 v[12:15], a[70:71], v[6:7], v[12:15]\nv_mfma_f32_16x16x16_bf16 v[16:19], v[78:79], v[6:7], v[16:19]\nv_mfma_f32_16x16x16_bf16 v[20:23], a[78:79], v[6:7], v[20:23]\ns_waitcnt vmcnt(28)\ns_barrier\ns_add_u32 m0, %[tb], 0\ns_add_u32 s44, %[wo], 4128768\n" \
"buffer_load_dwordx4 v92, %[rw], s44 offen nt lds\ns_add_u32 m0, %[tb], 1024\ns_add_u32 s44, %[wo], 4186112\nbuffer_load_dwordx4 v92, %[rw], s44 offen nt lds\ns_add_u32 m0, %[tb], 2048\ns_add_u32 s44, %[wo], 4243456\nbuffer_load_dwordx4 v92, %[rw], s44 offen nt lds\ns_add_u32 m0, %[tb], 3072\n" \
"s_add_u32 s44, %[wo], 4300800\nbuffer_load_dwordx4 v92, %[rw], s44 offen nt lds\nds_read_b64_tr_b16 a[80:81], v93 offset:4096\nds_read_b64_tr_b16 a[82:83], v93 offset:4608\nds_read_b64_tr_b16 a[88:89], v93 offset:4104\nds_read_b64_tr_b16 a[90:91], v93 offset:4616\nds_read_b64_tr_b16 a[96:97], v93 offset:16384\nds_read_b64_tr_b16 a[98:99], v93 offset:16896\n" \
"ds_read_b64_tr_b16 v[72:73], v93 offset:16392\nds_read_b64_tr_b16 v[74:75], v93 offset:16904\ns_waitcnt lgkmcnt(0)\nv_mfma_f32_16x16x16_bf16 v[8:11], a[80:81], v[6:7], v[8:11]\nv_mfma_f32_16x16x16_bf16 v[12:15], a[88:89], v[6:7], v[12:15]\nv_mfma_f32_16x16x16_bf16 v[16:19], a[96:97], v[6:7], v[16:19]\nv_mfma_f32_16x16x16_bf16 v[20:23], v[72:73], v[6:7], v[20:23]\nv_mfma_f32_16x16x16_bf16 v[8:11], a[82:83], v[6:7], v[8:11]\n" \
"v_mfma_f32_16x16x16_bf16 v[12:15], a[90:91], v[6:7], v[12:15]\nv_mfma_f32_16x16x16_bf16 v[16:19], a[98:99], v[6:7], v[16:19]\nv_mfma_f32_16x16x16_bf16 v[20:23], v[74:75], v[6:7], v[20:23]\ns_waitcnt vmcnt(28)\ns_barrier\ns_add_u32 m0, %[tb], 4096\ns_add_u32 s44, %[wo], 4358144\nbuffer_load_dwordx4 v92, %[rw], s44 offen nt lds\n" \
"s_add_u32 m0, %[tb], 5120\ns_add_u32 s44, %[wo], 4415488\nbuffer_load_dwordx4 v92, %[rw], s44 offen nt lds\ns_add_u32 m0, %[tb], 6144\ns_add_u32 s44, %[wo], 4472832\nbuffer_load_dwordx4 v92, %[rw], s44 offen nt lds\ns_add_u32 m0, %[tb], 7168\ns_add_u32 s44, %[wo], 4530176\n" \
"buffer_load_dwordx4 v92, %[rw], s44 offen nt lds\nds_read_b64_tr_b16 a[84:85], v93 offset:8192\nds_read_b64_tr_b16 a[86:87], v93 offset:8704\nds_read_b64_tr_b16 a[92:93], v93 offset:8200\nds_read_b64_tr_b16 a[94:95], v93 offset:8712\nds_read_b64_tr_b16 a[100:101], v93 offset:20480\nds_read_b64_tr_b16 a[102:103], v93 offset:20992\nds_read_b64_tr_b16 v[68:69], v93 offset:20488\n" \
"ds_read_b64_tr_b16 v[70:71], v93 offset:21000\ns_waitcnt lgkmcnt(0)\nv_mfma_f32_16x16x16_bf16 v[8:11], a[84:85], v[6:7], v[8:11]\nv_mfma_f32_16x16x16_bf16 v[12:15], a[92:93], v[6:7], v[12:15]\nv_mfma_f32_16x16x16_bf16 v[16:19], a[100:101], v[6:7], v[16:19]\nv_mfma_f32_16x16x16_bf16 v[20:23], v[68:69], v[6:7], v[20:23]\nv_mfma_f32_16x16x16_bf16 v[8:11], a[86:87], v[6:7], v[8:11]\nv_mfma_f32_16x16x16_bf16 v[12:15], a[94:95], v[6:7], v[12:15]\n" \
"v_mfma_f32_16x16x16_bf16 v[16:19], a[102:103], v[6:7], v[16:19]\nv_mfma_f32_16x16x16_bf16 v[20:23], v[70:71], v[6:7], v[20:23]\ns_waitcnt vmcnt(28)\ns_barrier\ns_add_u32 m0, %[tb], 8192\ns_add_u32 s44, %[wo], 4587520\nbuffer_load_dwordx4 v92, %[rw], s44 offen nt lds\ns_add_u32 m0, %[tb], 9216\n" \
"s_add_u32 s44, %[wo], 4644864\nbuffer_load_dwordx4 v92, %[rw], s44 offen nt lds\ns_add_u32 m0, %[tb], 10240\ns_add_u32 s44, %[wo], 4702208\nbuffer_load_dwordx4 v92, %[rw], s44 offen nt lds\ns_add_u32 m0, %[tb], 11264\ns_add_u32 s44, %[wo], 4759552\nbuffer_load_dwordx4 v92, %[rw], s44 offen nt lds\n" \
"ds_read_b64_tr_b16 a[136:137], v94 offset:0\nds_read_b64_tr_b16 a[138:139], v94 offset:512\nds_read_b64_tr_b16 v[64:65], v94 offset:8\nds_read_b64_tr_b16 v[66:67], v94 offset:520\nds_read_b64_tr_b16 a[144:145], v94 offset:24576\nds_read_b64_tr_b16 a[146:147], v94 offset:25088\nds_read_b64_tr_b16 a[152:153], v94 offset:24584\nds_read_b64_tr_b16 a[154:155], v94 offset:25096\n" \
"s_waitcnt lgkmcnt(0)\nv_mfma_f32_16x16x16_bf16 v[8:11], a[136:137], v[6:7], v[8:11]\nv_mfma_f32_16x16x16_bf16 v[12:15], v[64:65], v[6:7], v[12:15]\nv_mfma_f32_16x16x16_bf16 v[16:19], a[144:145], v[6:7], v[16:19]\nv_mfma_f32_16x16x16_bf16 v[20:23], a[152:153], v[6:7], v[20:23]\nv_mfma_f32_16x16x16_bf16 v[8:11], a[138:139], v[6:7], v[8:11]\nv_mfma_f32_16x16x16_bf16 v[12:15], v[66:67], v[6:7], v[12:15]\nv_mfma_f32_16x16x16_bf16 v[16:19], a[146:147], v[6:7], v[16:19]\n" \
"v_mfma_f32_16x16x16_bf16 v[20:23], a[154:155], v[6:7], v[20:23]\ns_waitcnt vmcnt(28)\ns_barrier\ns_add_u32 m0, %[lw], 0\ns_add_u32 s44, %[wo], 4816896\nbuffer_load_dwordx4 v92, %[rw], s44 offen nt lds\ns_add_u32 m0, %[lw], 1024\ns_add_u32 s44, %[wo], 4874240\n" \
"buffer_load_dwordx4 v92, %[rw], s44 offen nt lds\ns_add_u32 m0, %[lw], 2048\ns_add_u32 s44, %[wo], 4931584\nbuffer_load_dwordx4 v92, %[rw], s44 offen nt lds\ns_add_u32 m0, %[lw], 3072\ns_add_u32 s44, %[wo], 4988928\nbuffer_load_dwordx4 v92, %[rw], s44 offen nt lds\nds_read_b64_tr_b16 a[140:141], v94 offset:4096\n" \
"ds_read_b64_tr_b16 a[142:143], v94 offset:4608\nds_read_b64_tr_b16 v[60:61], v94 offset:4104\nds_read_b64_tr_b16 v[62:63], v94 offset:4616\nds_read_b64_tr_b16 a[148:149], v94 offset:28672\nds_read_b64_tr_b16 a[150:151], v94 offset:29184\nds_read_b64_tr_b16 a[156:157], v94 offset:28680\nds_read_b64_tr_b16 a[158:159], v94 offset:29192\ns_waitcnt lgkmcnt(0)\n" \
"v_mfma_f32_16x16x16_bf16 v[8:11], a[140:141], v[6:7], v[8:11]\nv_mfma_f32_16x16x16_bf16 v[12:15], v[60:61], v[6:7], v[12:15]\nv_mfma_f32_16x16x16_bf16 v[16:19], a[148:149], v[6:7], v[16:19]\nv_mfma_f32_16x16x16_bf16 v[20:23], a[156:157], v[6:7], v[20:23]\nv_mfma_f32_16x16x16_bf16 v[8:11], a[142:143], v[6:7], v[8:11]\nv_mfma_f32_16x16x16_bf16 v[12:15], v[62:63], v[6:7], v[12:15]\nv_mfma_f32_16x16x16_bf16 v[16:19], a[150:151], v[6:7], v[16:19]\nv_mfma_f32_16x16x16_bf16 v[20:23], a[158:159], v[6:7], v[20:23]\n" \
"s_waitcnt vmcnt(28)\ns_barrier\ns_add_u32 m0, %[lw], 4096\ns_add_u32 s44, %[wo], 5046272\nbuffer_load_dwordx4 v92, %[rw], s44 offen nt lds\ns_add_u32 m0, %[lw], 5120\ns_add_u32 s44, %[wo], 5103616\nbuffer_load_dwordx4 v92, %[rw], s44 offen nt lds\n" \
"s_add_u32 m0, %[lw], 6144\ns_add_u32 s44, %[wo], 5160960\nbuffer_load_dwordx4 v92, %[rw], s44 offen nt lds\ns_add_u32 m0, %[lw], 7168\ns_add_u32 s44, %[wo], 5218304\nbuffer_load_dwordx4 v92, %[rw], s44 offen nt lds\nds_read_b64_tr_b16 a[160:161], v94 offset:8192\nds_read_b64_tr_b16 a[162:163], v94 offset:8704\n" \
"ds_read_b64_tr_b16 a[168:169], v94 offset:8200\nds_read_b64_tr_b16 a[170:171], v94 offset:8712\nds_read_b64_tr_b16 v[56:57], v94 offset:32768\nds_read_b64_tr_b16 v[58:59], v94 offset:33280\nds_read_b64_tr_b16 a[176:177], v94 offset:32776\nds_read_b64_tr_b16 a[178:179], v94 offset:33288\ns_waitcnt lgkmcnt(0)\nv_mfma_f32_16x16x16_bf16 v[8:11], a[160:161], v[6:7], v[8:11]\n" \
"v_mfma_f32_16x16x16_bf16 v[12:15], a[168:169], v[6:7], v[12:15]\nv_mfma_f32_16x16x16_bf16 v[16:19], v[56:57], v[6:7], v[16:19]\nv_mfma_f32_16x16x16_bf16 v[20:23], a[176:177], v[6:7], v[20:23]\nv_mfma_f32_16x16x16_bf16 v[8:11], a[162:163], v[6:7], v[8:11]\nv_mfma_f32_16x16x16_bf16 v[12:15], a[170:171], v[6:7], v[12:15]\nv_mfma_f32_16x16x16_bf16 v[16:19], v[58:59], v[6:7], v[16:19]\nv_mfma_f32_16x16x16_bf16 v[20:23], a[178:179], v[6:7], v[20:23]\ns_waitcnt vmcnt(28)\n" \
"s_barrier\ns_add_u32 m0, %[lw], 8192\ns_add_u32 s44, %[wo], 5275648\nbuffer_load_dwordx4 v92, %[rw], s44 offen nt lds\ns_add_u32 m0, %[lw], 9216\ns_add_u32 s44, %[wo], 5332992\nbuffer_load_dwordx4 v92, %[rw], s44 offen nt lds\ns_add_u32 m0, %[lw], 10240\n" \
"s_add_u32 s44, %[wo], 5390336\nbuffer_load_dwordx4 v92, %[rw], s44 offen nt lds\ns_add_u32 m0, %[lw], 11264\ns_add_u32 s44, %[wo], 5447680\nbuffer_load_dwordx4 v92, %[rw], s44 offen nt lds\nds_read_b64_tr_b16 a[164:165], v94 offset:12288\nds_read_b64_tr_b16 a[166:167], v94 offset:12800\nds_read_b64_tr_b16 a[172:173], v94 offset:12296\n" \
"ds_read_b64_tr_b16 a[174:175], v94 offset:12808\nds_read_b64_tr_b16 v[52:53], v94 offset:36864\nds_read_b64_tr_b16 v[54:55], v94 offset:37376\nds_read_b64_tr_b16 a[180:181], v94 offset:36872\nds_read_b64_tr_b16 a[182:183], v94 offset:37384\ns_waitcnt lgkmcnt(0)\nv_mfma_f32_16x16x16_bf16 v[8:11], a[164:165], v[6:7], v[8:11]\nv_mfma_f32_16x16x16_bf16 v[12:15], a[172:173], v[6:7], v[12:15]\n" \
"v_mfma_f32_16x16x16_bf16 v[16:19], v[52:53], v[6:7], v[16:19]\nv_mfma_f32_16x16x16_bf16 v[20:23], a[180:181], v[6:7], v[20:23]\nv_mfma_f32_16x16x16_bf16 v[8:11], a[166:167], v[6:7], v[8:11]\nv_mfma_f32_16x16x16_bf16 v[12:15], a[174:175], v[6:7], v[12:15]\nv_mfma_f32_16x16x16_bf16 v[16:19], v[54:55], v[6:7], v[16:19]\nv_mfma_f32_16x16x16_bf16 v[20:23], a[182:183], v[6:7], v[20:23]\ns_waitcnt vmcnt(28)\ns_barrier\n" \
"s_add_u32 m0, %[lw], 12288\ns_add_u32 s44, %[wo], 5963776\nbuffer_load_dwordx4 v92, %[rw], s44 offen nt lds\ns_add_u32 m0, %[lw], 13312\ns_add_u32 s44, %[wo], 6021120\nbuffer_load_dwordx4 v92, %[rw], s44 offen nt lds\ns_add_u32 m0, %[lw], 14336\ns_add_u32 s44, %[wo], 6078464\n" \
"buffer_load_dwordx4 v92, %[rw], s44 offen nt lds\ns_add_u32 m0, %[lw], 15360\ns_add_u32 s44, %[wo], 6135808\nbuffer_load_dwordx4 v92, %[rw], s44 offen nt lds\nds_read_b64_tr_b16 a[184:185], v94 offset:16384\nds_read_b64_tr_b16 a[186:187], v94 offset:16896\nds_read_b64_tr_b16 a[192:193], v94 offset:16392\nds_read_b64_tr_b16 a[194:195], v94 offset:16904\n" \
"ds_read_b64_tr_b16 a[200:201], v94 offset:40960\nds_read_b64_tr_b16 a[202:203], v94 offset:41472\nds_read_b64_tr_b16 v[48:49], v94 offset:40968\nds_read_b64_tr_b16 v[50:51], v94 offset:41480\ns_waitcnt lgkmcnt(0)\nv_mfma_f32_16x16x16_bf16 v[8:11], a[184:185], v[6:7], v[8:11]\nv_mfma_f32_16x16x16_bf16 v[12:15], a[192:193], v[6:7], v[12:15]\nv_mfma_f32_16x16x16_bf16 v[16:19], a[200:201], v[6:7], v[16:19]\n" \
"v_mfma_f32_16x16x16_bf16 v[20:23], v[48:49], v[6:7], v[20:23]\nv_mfma_f32_16x16x16_bf16 v[8:11], a[186:187], v[6:7], v[8:11]\nv_mfma_f32_16x16x16_bf16 v[12:15], a[194:195], v[6:7], v[12:15]\nv_mfma_f32_16x16x16_bf16 v[16:19], a[202:203], v[6:7], v[16:19]\nv_mfma_f32_16x16x16_bf16 v[20:23], v[50:51], v[6:7], v[20:23]\ns_waitcnt vmcnt(28)\ns_barrier\ns_add_u32 m0, %[lw], 16384\n" \
"s_add_u32 s44, %[wo], 6193152\nbuffer_load_dwordx4 v92, %[rw], s44 offen nt lds\ns_add_u32 m0, %[lw], 17408\ns_add_u32 s44, %[wo], 6250496\nbuffer_load_dwordx4 v92, %[rw], s44 offen nt lds\ns_add_u32 m0, %[lw], 18432\ns_add_u32 s44, %[wo], 6307840\nbuffer_load_dwordx4 v92, %[rw], s44 offen nt lds\n" \
"s_add_u32 m0, %[lw], 19456\ns_add_u32 s44, %[wo], 6365184\nbuffer_load_dwordx4 v92, %[rw], s44 offen nt lds\nds_read_b64_tr_b16 a[188:189], v94 offset:20480\nds_read_b64_tr_b16 a[190:191], v94 offset:20992\nds_read_b64_tr_b16 a[196:197], v94 offset:20488\nds_read_b64_tr_b16 a[198:199], v94 offset:21000\nds_read_b64_tr_b16 a[204:205], v94 offset:45056\n" \
"ds_read_b64_tr_b16 a[206:207], v94 offset:45568\nds_read_b64_tr_b16 v[44:45], v94 offset:45064\nds_read_b64_tr_b16 v[46:47], v94 offset:45576\ns_waitcnt lgkmcnt(0)\nv_mfma_f32_16x16x16_bf16 v[8:11], a[188:189], v[6:7], v[8:11]\nv_mfma_f32_16x16x16_bf16 v[12:15], a[196:197], v[6:7], v[12:15]\nv_mfma_f32_16x16x16_bf16 v[16:19], a[204:205], v[6:7], v[16:19]\nv_mfma_f32_16x16x16_bf16 v[20:23], v[44:45], v[6:7], v[20:23]\n" \
"v_mfma_f32_16x16x16_bf16 v[8:11], a[190:191], v[6:7], v[8:11]\nv_mfma_f32_16x16x16_bf16 v[12:15], a[198:199], v[6:7], v[12:15]\nv_mfma_f32_16x16x16_bf16 v[16:19], a[206:207], v[6:7], v[16:19]\nv_mfma_f32_16x16x16_bf16 v[20:23], v[46:47], v[6:7], v[20:23]\ns_waitcnt vmcnt(28)\ns_barrier\ns_add_u32 m0, %[lw], 20480\ns_add_u32 s44, %[wo], 6422528\n" \
"buffer_load_dwordx4 v92, %[rw], s44 offen nt lds\ns_add_u32 m0, %[lw], 21504\ns_add_u32 s44, %[wo], 6479872\nbuffer_load_dwordx4 v92, %[rw], s44 offen nt lds\ns_add_u32 m0, %[lw], 22528\ns_add_u32 s44, %[wo], 6537216\nbuffer_load_dwordx4 v92, %[rw], s44 offen nt lds\ns_add_u32 m0, %[lw], 23552\n" \
"s_add_u32 s44, %[wo], 6594560\nbuffer_load_dwordx4 v92, %[rw], s44 offen nt lds\nds_read_b64_tr_b16 a[240:241], v93 offset:0\nds_read_b64_tr_b16 a[242:243], v93 offset:512\nds_read_b64_tr_b16 v[40:41], v93 offset:8\nds_read_b64_tr_b16 v[42:43], v93 offset:520\nds_read_b64_tr_b16 a[248:249], v93 offset:12288\nds_read_b64_tr_b16 a[250:251], v93 offset:12800\n" \
"ds_read_b64_tr_b16 v[96:97], v93 offset:12296\nds_read_b64_tr_b16 v[98:99], v93 offset:12808\ns_waitcnt lgkmcnt(0)\nv_mfma_f32_16x16x16_bf16 v[8:11], a[240:241], v[6:7], v[8:11]\nv_mfma_f32_16x16x16_bf16 v[12:15], v[40:41], v[6:7], v[12:15]\nv_mfma_f32_16x16x16_bf16 v[16:19], a[248:249], v[6:7], v[16:19]\nv_mfma_f32_16x16x16_bf16 v[20:23], v[96:97], v[6:7], v[20:23]\nv_mfma_f32_16x16x16_bf16 v[8:11], a[242:243], v[6:7], v[8:11]\n" \
"v_mfma_f32_16x16x16_bf16 v[12:15], v[42:43], v[6:7], v[12:15]\nv_mfma_f32_16x16x16_bf16 v[16:19], a[250:251], v[6:7], v[16:19]\nv_mfma_f32_16x16x16_bf16 v[20:23], v[98:99], v[6:7], v[20:23]\ns_waitcnt vmcnt(28)\ns_barrier\ns_add_u32 m0, %[tb], 0\ns_add_u32 s44, %[wo], 6651904\nbuffer_load_dwordx4 v92, %[rw], s44 offen nt lds\n" \
"s_add_u32 m0, %[tb], 1024\ns_add_u32 s44, %[wo], 6709248\nbuffer_load_dwordx4 v92, %[rw], s44 offen nt lds\ns_add_u32 m0, %[tb], 2048\ns_add_u32 s44, %[wo], 6766592\nbuffer_load_dwordx4 v92, %[rw], s44 offen nt lds\ns_add_u32 m0, %[tb], 3072\ns_add_u32 s44, %[wo], 6823936\n" \
"buffer_load_dwordx4 v92, %[rw], s44 offen nt lds\nds_read_b64_tr_b16 a[244:245], v93 offset:4096\nds_read_b64_tr_b16 a[246:247], v93 offset:4608\nds_read_b64_tr_b16 v[36:37], v93 offset:4104\nds_read_b64_tr_b16 v[38:39], v93 offset:4616\nds_read_b64_tr_b16 a[252:253], v93 offset:16384\nds_read_b64_tr_b16 a[254:255], v93 offset:16896\nds_read_b64_tr_b16 v[100:101], v93 offset:16392\n" \
"ds_read_b64_tr_b16 v[102:103], v93 offset:16904\ns_waitcnt lgkmcnt(0)\nv_mfma_f32_16x16x16_bf16 v[8:11], a[244:245], v[6:7], v[8:11]\nv_mfma_f32_16x16x16_bf16 v[12:15], v[36:37], v[6:7], v[12:15]\nv_mfma_f32_16x16x16_bf16 v[16:19], a[252:253], v[6:7], v[16:19]\nv_mfma_f32_16x16x16_bf16 v[20:23], v[100:101], v[6:7], v[20:23]\nv_mfma_f32_16x16x16_bf16 v[8:11], a[246:247], v[6:7], v[8:11]\nv_mfma_f32_16x16x16_bf16 v[12:15], v[38:39], v[6:7], v[12:15]\n" \
"v_mfma_f32_16x16x16_bf16 v[16:19], a[254:255], v[6:7], v[16:19]\nv_mfma_f32_16x16x16_bf16 v[20:23], v[102:103], v[6:7], v[20:23]\ns_waitcnt vmcnt(28)\ns_barrier\ns_add_u32 m0, %[tb], 4096\ns_add_u32 s44, %[wo], 6881280\nbuffer_load_dwordx4 v92, %[rw], s44 offen nt lds\ns_add_u32 m0, %[tb], 5120\n" \
"s_add_u32 s44, %[wo], 6938624\nbuffer_load_dwordx4 v92, %[rw], s44 offen nt lds\ns_add_u32 m0, %[tb], 6144\ns_add_u32 s44, %[wo], 6995968\nbuffer_load_dwordx4 v92, %[rw], s44 offen nt lds\ns_add_u32 m0, %[tb], 7168\ns_add_u32 s44, %[wo], 7053312\nbuffer_load_dwordx4 v92, %[rw], s44 offen nt lds\n" \
"ds_read_b64_tr_b16 v[104:105], v93 offset:8192\nds_read_b64_tr_b16 v[106:107], v93 offset:8704\nds_read_b64_tr_b16 v[112:113], v93 offset:8200\nds_read_b64_tr_b16 v[114:115], v93 offset:8712\nds_read_b64_tr_b16 v[32:33], v93 offset:20480\nds_read_b64_tr_b16 v[34:35], v93 offset:20992\nds_read_b64_tr_b16 v[120:121], v93 offset:20488\nds_read_b64_tr_b16 v[122:123], v93 offset:21000\n" \
"s_waitcnt lgkmcnt(0)\nv_mfma_f32_16x16x16_bf16 v[8:11], v[104:105], v[6:7], v[8:11]\nv_mfma_f32_16x16x16_bf16 v[12:15], v[112:113], v[6:7], v[12:15]\nv_mfma_f32_16x16x16_bf16 v[16:19], v[32:33], v[6:7], v[16:19]\nv_mfma_f32_16x16x16_bf16 v[20:23], v[120:121], v[6:7], v[20:23]\nv_mfma_f32_16x16x16_bf16 v[8:11], v[106:107], v[6:7], v[8:11]\nv_mfma_f32_16x16x16_bf16 v[12:15], v[114:115], v[6:7], v[12:15]\nv_mfma_f32_16x16x16_bf16 v[16:19], v[34:35], v[6:7], v[16:19]\n" \
"v_mfma_f32_16x16x16_bf16 v[20:23], v[122:123], v[6:7], v[20:23]\ns_waitcnt vmcnt(28)\ns_barrier\ns_add_u32 m0, %[tb], 8192\ns_add_u32 s44, %[wo], 7110656\nbuffer_load_dwordx4 v92, %[rw], s44 offen nt lds\ns_add_u32 m0, %[tb], 9216\ns_add_u32 s44, %[wo], 7168000\n" \
"buffer_load_dwordx4 v92, %[rw], s44 offen nt lds\ns_add_u32 m0, %[tb], 10240\ns_add_u32 s44, %[wo], 7225344\nbuffer_load_dwordx4 v92, %[rw], s44 offen nt lds\ns_add_u32 m0, %[tb], 11264\ns_add_u32 s44, %[wo], 7282688\nbuffer_load_dwordx4 v92, %[rw], s44 offen nt lds\nds_read_b64_tr_b16 v[108:109], v94 offset:0\n" \
"ds_read_b64_tr_b16 v[110:111], v94 offset:512\nds_read_b64_tr_b16 v[116:117], v94 offset:8\nds_read_b64_tr_b16 v[118:119], v94 offset:520\nds_read_b64_tr_b16 v[28:29], v94 offset:24576\nds_read_b64_tr_b16 v[30:31], v94 offset:25088\nds_read_b64_tr_b16 v[124:125], v94 offset:24584\nds_read_b64_tr_b16 v[126:127], v94 offset:25096\ns_waitcnt lgkmcnt(0)\n" \
"v_mfma_f32_16x16x16_bf16 v[8:11], v[108:109], v[6:7], v[8:11]\nv_mfma_f32_16x16x16_bf16 v[12:15], v[116:117], v[6:7], v[12:15]\nv_mfma_f32_16x16x16_bf16 v[16:19], v[28:29], v[6:7], v[16:19]\nv_mfma_f32_16x16x16_bf16 v[20:23], v[124:125], v[6:7], v[20:23]\nv_mfma_f32_16x16x16_bf16 v[8:11], v[110:111], v[6:7], v[8:11]\nv_mfma_f32_16x16x16_bf16 v[12:15], v[118:119], v[6:7], v[12:15]\nv_mfma_f32_16x16x16_bf16 v[16:19], v[30:31], v[6:7], v[16:19]\nv_mfma_f32_16x16x16_bf16 v[20:23], v[126:127], v[6:7], v[20:23]\n" \
"s_waitcnt vmcnt(28)\ns_barrier\nds_read_b64_tr_b16 v[128:129], v94 offset:4096\nds_read_b64_tr_b16 v[130:131], v94 offset:4608\nds_read_b64_tr_b16 v[136:137], v94 offset:4104\nds_read_b64_tr_b16 v[138:139], v94 offset:4616\nds_read_b64_tr_b16 v[144:145], v94 offset:28672\nds_read_b64_tr_b16 v[146:147], v94 offset:29184\n" \
"ds_read_b64_tr_b16 v[24:25], v94 offset:28680\nds_read_b64_tr_b16 v[26:27], v94 offset:29192\ns_waitcnt lgkmcnt(0)\nv_mfma_f32_16x16x16_bf16 v[8:11], v[128:129], v[6:7], v[8:11]\nv_mfma_f32_16x16x16_bf16 v[12:15], v[136:137], v[6:7], v[12:15]\nv_mfma_f32_16x16x16_bf16 v[16:19], v[144:145], v[6:7], v[16:19]\nv_mfma_f32_16x16x16_bf16 v[20:23], v[24:25], v[6:7], v[20:23]\nv_mfma_f32_16x16x16_bf16 v[8:11], v[130:131], v[6:7], v[8:11]\n" \
"v_mfma_f32_16x16x16_bf16 v[12:15], v[138:139], v[6:7], v[12:15]\nv_mfma_f32_16x16x16_bf16 v[16:19], v[146:147], v[6:7], v[16:19]\nv_mfma_f32_16x16x16_bf16 v[20:23], v[26:27], v[6:7], v[20:23]\ns_waitcnt vmcnt(24)\nds_write_b128 v1, v[88:91] offset:0\nds_write_b128 v1, v[84:87] offset:1024\nds_write_b128 v1, v[80:83] offset:2048\nds_write_b128 v1, v[76:79] offset:3072\n" \
"s_barrier\nds_read_b64_tr_b16 v[132:133], v94 offset:8192\nds_read_b64_tr_b16 v[134:135], v94 offset:8704\nds_read_b64_tr_b16 v[140:141], v94 offset:8200\nds_read_b64_tr_b16 v[142:143], v94 offset:8712\nds_read_b64_tr_b16 v[148:149], v94 offset:32768\nds_read_b64_tr_b16 v[150:151], v94 offset:33280\nds_read_b64_tr_b16 v[76:77], v94 offset:32776\n" \
"ds_read_b64_tr_b16 v[78:79], v94 offset:33288\ns_waitcnt lgkmcnt(0)\nv_mfma_f32_16x16x16_bf16 v[8:11], v[132:133], v[6:7], v[8:11]\nv_mfma_f32_16x16x16_bf16 v[12:15], v[140:141], v[6:7], v[12:15]\nv_mfma_f32_16x16x16_bf16 v[16:19], v[148:149], v[6:7], v[16:19]\nv_mfma_f32_16x16x16_bf16 v[20:23], v[76:77], v[6:7], v[20:23]\nv_mfma_f32_16x16x16_bf16 v[8:11], v[134:135], v[6:7], v[8:11]\nv_mfma_f32_16x16x16_bf16 v[12:15], v[142:143], v[6:7], v[12:15]\n" \
"v_mfma_f32_16x16x16_bf16 v[16:19], v[150:151], v[6:7], v[16:19]\nv_mfma_f32_16x16x16_bf16 v[20:23], v[78:79], v[6:7], v[20:23]\ns_waitcnt vmcnt(20)\nds_write_b128 v1, v[72:75] offset:4096\nds_write_b128 v1, v[68:71] offset:5120\nds_write_b128 v1, v[64:67] offset:6144\nds_write_b128 v1, v[60:63] offset:7168\ns_barrier\n" \
"ds_read_b64_tr_b16 v[184:185], v94 offset:12288\nds_read_b64_tr_b16 v[186:187], v94 offset:12800\nds_read_b64_tr_b16 v[60:61], v94 offset:12296\nds_read_b64_tr_b16 v[62:63], v94 offset:12808\nds_read_b64_tr_b16 v[192:193], v94 offset:36864\nds_read_b64_tr_b16 v[194:195], v94 offset:37376\nds_read_b64_tr_b16 v[200:201], v94 offset:36872\nds_read_b64_tr_b16 v[202:203], v94 offset:37384\n" \
"s_waitcnt lgkmcnt(0)\nv_mfma_f32_16x16x16_bf16 v[8:11], v[184:185], v[6:7], v[8:11]\nv_mfma_f32_16x16x16_bf16 v[12:15], v[60:61], v[6:7], v[12:15]\nv_mfma_f32_16x16x16_bf16 v[16:19], v[192:193], v[6:7], v[16:19]\nv_mfma_f32_16x16x16_bf16 v[20:23], v[200:201], v[6:7], v[20:23]\nv_mfma_f32_16x16x16_bf16 v[8:11], v[186:187], v[6:7], v[8:11]\nv_mfma_f32_16x16x16_bf16 v[12:15], v[62:63], v[6:7], v[12:15]\nv_mfma_f32_16x16x16_bf16 v[16:19], v[194:195], v[6:7], v[16:19]\n" \
"v_mfma_f32_16x16x16_bf16 v[20:23], v[202:203], v[6:7], v[20:23]\ns_waitcnt vmcnt(16)\nds_write_b128 v1, v[56:59] offset:8192\nds_write_b128 v1, v[52:55] offset:9216\nds_write_b128 v1, v[48:51] offset:10240\nds_write_b128 v1, v[44:47] offset:11264\ns_barrier\nds_read_b64_tr_b16 v[188:189], v94 offset:16384\n" \
"ds_read_b64_tr_b16 v[190:191], v94 offset:16896\nds_read_b64_tr_b16 v[44:45], v94 offset:16392\nds_read_b64_tr_b16 v[46:47], v94 offset:16904\nds_read_b64_tr_b16 v[196:197], v94 offset:40960\nds_read_b64_tr_b16 v[198:199], v94 offset:41472\nds_read_b64_tr_b16 v[204:205], v94 offset:40968\nds_read_b64_tr_b16 v[206:207], v94 offset:41480\ns_waitcnt lgkmcnt(0)\n" \
"v_mfma_f32_16x16x16_bf16 v[8:11], v[188:189], v[6:7], v[8:11]\nv_mfma_f32_16x16x16_bf16 v[12:15], v[44:45], v[6:7], v[12:15]\nv_mfma_f32_16x16x16_bf16 v[16:19], v[196:197], v[6:7], v[16:19]\nv_mfma_f32_16x16x16_bf16 v[20:23], v[204:205], v[6:7], v[20:23]\nv_mfma_f32_16x16x16_bf16 v[8:11], v[190:191], v[6:7], v[8:11]\nv_mfma_f32_16x16x16_bf16 v[12:15], v[46:47], v[6:7], v[12:15]\nv_mfma_f32_16x16x16_bf16 v[16:19], v[198:199], v[6:7], v[16:19]\nv_mfma_f32_16x16x16_bf16 v[20:23], v[206:207], v[6:7], v[20:23]\n" \
"s_waitcnt vmcnt(12)\nds_write_b128 v1, v[40:43] offset:12288\nds_write_b128 v1, v[36:39] offset:13312\nds_write_b128 v1, v[32:35] offset:14336\nds_write_b128 v1, v[28:31] offset:15360\ns_barrier\nds_read_b64_tr_b16 v[208:209], v94 offset:20480\nds_read_b64_tr_b16 v[210:211], v94 offset:20992\n" \
"ds_read_b64_tr_b16 v[216:217], v94 offset:20488\nds_read_b64_tr_b16 v[218:219], v94 offset:21000\nds_read_b64_tr_b16 v[28:29], v94 offset:45056\nds_read_b64_tr_b16 v[30:31], v94 offset:45568\nds_read_b64_tr_b16 v[224:225], v94 offset:45064\nds_read_b64_tr_b16 v[226:227], v94 offset:45576\ns_waitcnt lgkmcnt(0)\nv_mfma_f32_16x16x16_bf16 v[8:11], v[208:209], v[6:7], v[8:11]\n" \
"v_mfma_f32_16x16x16_bf16 v[12:15], v[216:217], v[6:7], v[12:15]\nv_mfma_f32_16x16x16_bf16 v[16:19], v[28:29], v[6:7], v[16:19]\nv_mfma_f32_16x16x16_bf16 v[20:23], v[224:225], v[6:7], v[20:23]\nv_mfma_f32_16x16x16_bf16 v[8:11], v[210:211], v[6:7], v[8:11]\nv_mfma_f32_16x16x16_bf16 v[12:15], v[218:219], v[6:7], v[12:15]\nv_mfma_f32_16x16x16_bf16 v[16:19], v[30:31], v[6:7], v[16:19]\nv_mfma_f32_16x16x16_bf16 v[20:23], v[226:227], v[6:7], v[20:23]\ns_waitcnt vmcnt(8)\n" \
"ds_write_b128 v1, v[24:27] offset:16384\nds_write_b128 v1, v[76:79] offset:17408\nds_write_b128 v1, v[60:63] offset:18432\nds_write_b128 v1, v[44:47] offset:19456\ns_barrier\nds_read_b64_tr_b16 v[212:213], v93 offset:0\nds_read_b64_tr_b16 v[214:215], v93 offset:512\nds_read_b64_tr_b16 v[220:221], v93 offset:8\n" \
"ds_read_b64_tr_b16 v[222:223], v93 offset:520\nds_read_b64_tr_b16 v[44:45], v93 offset:12288\nds_read_b64_tr_b16 v[46:47], v93 offset:12800\nds_read_b64_tr_b16 v[228:229], v93 offset:12296\nds_read_b64_tr_b16 v[230:231], v93 offset:12808\ns_waitcnt lgkmcnt(0)\nv_mfma_f32_16x16x16_bf16 v[8:11], v[212:213], v[6:7], v[8:11]\nv_mfma_f32_16x16x16_bf16 v[12:15], v[220:221], v[6:7], v[12:15]\n" \
"v_mfma_f32_16x16x16_bf16 v[16:19], v[44:45], v[6:7], v[16:19]\nv_mfma_f32_16x16x16_bf16 v[20:23], v[228:229], v[6:7], v[20:23]\nv_mfma_f32_16x16x16_bf16 v[8:11], v[214:215], v[6:7], v[8:11]\nv_mfma_f32_16x16x16_bf16 v[12:15], v[222:223], v[6:7], v[12:15]\nv_mfma_f32_16x16x16_bf16 v[16:19], v[46:47], v[6:7], v[16:19]\nv_mfma_f32_16x16x16_bf16 v[20:23], v[230:231], v[6:7], v[20:23]\ns_waitcnt vmcnt(4)\nds_write_b128 v1, v[28:31] offset:20480\n" \
"ds_write_b128 v1, v[44:47] offset:21504\ns_barrier\nds_read_b64_tr_b16 v[232:233], v93 offset:4096\nds_read_b64_tr_b16 v[234:235], v93 offset:4608\nds_read_b64_tr_b16 v[240:241], v93 offset:4104\nds_read_b64_tr_b16 v[242:243], v93 offset:4616\nds_read_b64_tr_b16 v[248:249], v93 offset:16384\nds_read_b64_tr_b16 v[250:251], v93 offset:16896\n" \
"ds_read_b64_tr_b16 v[44:45], v93 offset:16392\nds_read_b64_tr_b16 v[46:47], v93 offset:16904\ns_waitcnt lgkmcnt(0)\nv_mfma_f32_16x16x16_bf16 v[8:11], v[232:233], v[6:7], v[8:11]\nv_mfma_f32_16x16x16_bf16 v[12:15], v[240:241], v[6:7], v[12:15]\nv_mfma_f32_16x16x16_bf16 v[16:19], v[248:249], v[6:7], v[16:19]\nv_mfma_f32_16x16x16_bf16 v[20:23], v[44:45], v[6:7], v[20:23]\nv_mfma_f32_16x16x16_bf16 v[8:11], v[234:235], v[6:7], v[8:11]\n" \
"v_mfma_f32_16x16x16_bf16 v[12:15], v[242:243], v[6:7], v[12:15]\nv_mfma_f32_16x16x16_bf16 v[16:19], v[250:251], v[6:7], v[16:19]\nv_mfma_f32_16x16x16_bf16 v[20:23], v[46:47], v[6:7], v[20:23]\ns_waitcnt vmcnt(0)\nds_write_b128 v1, v[44:47] offset:22528\ns_barrier\nds_read_b64_tr_b16 v[236:237], v93 offset:8192\nds_read_b64_tr_b16 v[238:239], v93 offset:8704\n" \
"ds_read_b64_tr_b16 v[244:245], v93 offset:8200\nds_read_b64_tr_b16 v[246:247], v93 offset:8712\nds_read_b64_tr_b16 v[252:253], v93 offset:20480\nds_read_b64_tr_b16 v[254:255], v93 offset:20992\nds_read_b64_tr_b16 v[44:45], v93 offset:20488\nds_read_b64_tr_b16 v[46:47], v93 offset:21000\ns_waitcnt lgkmcnt(0)\nv_mfma_f32_16x16x16_bf16 v[8:11], v[236:237], v[6:7], v[8:11]\n" \
"v_mfma_f32_16x16x16_bf16 v[12:15], v[244:245], v[6:7], v[12:15]\nv_mfma_f32_16x16x16_bf16 v[16:19], v[252:253], v[6:7], v[16:19]\nv_mfma_f32_16x16x16_bf16 v[20:23], v[44:45], v[6:7], v[20:23]\nv_mfma_f32_16x16x16_bf16 v[8:11], v[238:239], v[6:7], v[8:11]\nv_mfma_f32_16x16x16_bf16 v[12:15], v[246:247], v[6:7], v[12:15]\nv_mfma_f32_16x16x16_bf16 v[16:19], v[254:255], v[6:7], v[16:19]\nv_mfma_f32_16x16x16_bf16 v[20:23], v[46:47], v[6:7], v[20:23]\nds_write_b128 v1, v[44:47] offset:23552\n" \
"s_waitcnt lgkmcnt(0)\ns_barrier\ns_mov_b32 s40, 0\ns_mov_b32 s43, 0\ns_lshl_b32 s57, %[w], 12\ns_mov_b32 s58, 0xbeeff558\ns_mov_b32 s59, 0x409d30fc\ns_mov_b32 s56, -2147483648\n" \
".Lgg_next%=:\ns_cmp_lg_u32 %[w], 0\ns_cbranch_scc1 .Lgg_w1%=\nbuffer_load_dword v56, v3, %[rs], 0 offen sc1\ns_waitcnt vmcnt(0)\ns_barrier\nbuffer_store_dword v90, off, %[rs], s56 sc1\ns_mov_b32 s56, -2147483648\n" \
".Lgg_poll%=:\nv_cmp_gt_i32 vcc, s40, v56\ns_cbranch_vccz .Lgg_go%=\ns_cmp_ge_u32 s40, 62\ns_cbranch_scc1 .Lgg_ns1051%=\ns_sleep 6\n.Lgg_ns1051%=:\ns_sleep 1\n" \
"buffer_load_dword v56, v3, %[rs], 0 offen sc1\ns_waitcnt vmcnt(0)\ns_add_u32 s43, s43, 1\ns_cmp_lt_u32 s43, 262144\ns_cbranch_scc1 .Lgg_poll%=\ns_branch .Lgg_abort%=\n.Lgg_w1%=:\ns_waitcnt vmcnt(0)\n" \
"s_barrier\n.Lgg_go%=:\ns_add_u32 s44, s40, 0\ns_lshl_b32 s44, s44, 17\ns_add_u32 s44, s44, %[xn]\ns_lshr_b32 s45, s44, 8\ns_and_b32 s45, s45, 0x2000\ns_xor_b32 s41, s44, s45\n" \
"s_add_u32 s61, s41, %[xw]\ns_barrier\ns_add_u32 m0, s57, 0\ns_add_u32 s60, s61, 0\nbuffer_load_dwordx4 v0, %[rs], s60 offen offset:0 sc1 lds\nbuffer_load_dwordx4 v0, %[rs], s60 offen offset:1024 sc1 lds\nbuffer_load_dwordx4 v0, %[rs], s60 offen offset:2048 sc1 lds\nbuffer_load_dwordx4 v0, %[rs], s60 offen offset:3072 sc1 lds\n" \
"s_add_u32 m0, s57, 16384\ns_add_u32 s60, s61, 32768\nbuffer_load_dwordx4 v0, %[rs], s60 offen offset:0 sc1 lds\nbuffer_load_dwordx4 v0, %[rs], s60 offen offset:1024 sc1 lds\nbuffer_load_dwordx4 v0, %[rs], s60 offen offset:2048 sc1 lds\nbuffer_load_dwordx4 v0, %[rs], s60 offen offset:3072 sc1 lds\nbuffer_load_dwordx4 v[40:43], v5, %[rs], s41 offen offset:0 sc1\nbuffer_load_dwordx4 v[44:47], v5, %[rs], s41 offen offset:256 sc1\n" \
"s_lshl_b32 s44, s40, 19\ns_add_u32 s44, s44, %[ho]\ns_lshr_b32 s45, s44, 8\ns_and_b32 s45, s45, 0x2000\ns_xor_b32 s55, s44, s45\ns_waitcnt vmcnt(6)\ns_barrier\nds_read_b128 v[56:59], v2 offset:0\n" \
"ds_read_b128 v[60:63], v2 offset:2048\nds_read_b128 v[64:67], v2 offset:4096\ns_waitcnt lgkmcnt(2)\nv_mfma_f32_16x16x32_bf16 v[24:27], a[0:3], v[56:59], 0\nds_read_b128 v[68:71], v2 offset:6144\nv_mfma_f32_16x16x32_bf16 v[28:31], a[8:11], v[56:59], 0\nds_read_b128 v[72:75], v1 offset:0\nbuffer_load_dwordx4 v[48:51], v5, %[rs], s41 offen offset:512 sc1\n" \
"v_mfma_f32_16x16x32_bf16 v[32:35], a[16:19], v[56:59], 0\nds_read_b128 v[76:79], v1 offset:1024\nv_mfma_f32_16x16x32_bf16 v[36:39], a[24:27], v[56:59], 0\ns_waitcnt lgkmcnt(4)\nv_mfma_f32_16x16x32_bf16 v[24:27], a[4:7], v[60:63], v[24:27]\nds_read_b128 v[56:59], v2 offset:8192\nv_mfma_f32_16x16x32_bf16 v[28:31], a[12:15], v[60:63], v[28:31]\nds_read_b128 v[80:83], v1 offset:2048\n" \
"buffer_load_dwordx4 v[52:55], v5, %[rs], s41 offen offset:768 sc1\nv_mfma_f32_16x16x32_bf16 v[32:35], a[20:23], v[60:63], v[32:35]\nv_mfma_f32_16x16x32_bf16 v[36:39], a[28:31], v[60:63], v[36:39]\ns_waitcnt lgkmcnt(5)\nv_mfma_f32_16x16x32_bf16 v[24:27], a[32:35], v[64:67], v[24:27]\ns_waitcnt lgkmcnt(3)\ns_add_u32 m0, s57, 32768\nds_read_b128 v[60:63], v2 offset:10240\n" \
"v_mfma_f32_16x16x32_bf16 v[28:31], v[72:75], v[64:67], v[28:31]\ns_add_u32 s60, s61, 65536\nds_read_b128 v[72:75], v1 offset:3072\nbuffer_load_dwordx4 v0, %[rs], s60 offen offset:0 sc1 lds\nv_mfma_f32_16x16x32_bf16 v[32:35], a[40:43], v[64:67], v[32:35]\nv_mfma_f32_16x16x32_bf16 v[36:39], a[48:51], v[64:67], v[36:39]\nv_mfma_f32_16x16x32_bf16 v[24:27], a[36:39], v[68:71], v[24:27]\ns_waitcnt lgkmcnt(4)\n" \
"ds_read_b128 v[64:67], v2 offset:12288\nv_mfma_f32_16x16x32_bf16 v[28:31], v[76:79], v[68:71], v[28:31]\nbuffer_load_dwordx4 v0, %[rs], s60 offen offset:1024 sc1 lds\nv_mfma_f32_16x16x32_bf16 v[32:35], a[44:47], v[68:71], v[32:35]\nv_mfma_f32_16x16x32_bf16 v[36:39], a[52:55], v[68:71], v[36:39]\ns_waitcnt lgkmcnt(4)\nds_read_b128 v[76:79], v1 offset:4096\nv_mfma_f32_16x16x32_bf16 v[24:27], a[56:59], v[56:59], v[24:27]\n" \
"ds_read_b128 v[68:71], v2 offset:14336\nv_mfma_f32_16x16x32_bf16 v[28:31], a[64:67], v[56:59], v[28:31]\ns_waitcnt lgkmcnt(5)\nbuffer_load_dwordx4 v0, %[rs], s60 offen offset:2048 sc1 lds\nv_mfma_f32_16x16x32_bf16 v[32:35], v[80:83], v[56:59], v[32:35]\nv_mfma_f32_16x16x32_bf16 v[36:39], a[72:75], v[56:59], v[36:39]\ns_waitcnt lgkmcnt(4)\nds_read_b128 v[80:83], v1 offset:5120\n" \
"v_mfma_f32_16x16x32_bf16 v[24:27], a[60:63], v[60:63], v[24:27]\ns_waitcnt vmcnt(7)\ns_barrier\nds_read_b128 v[56:59], v2 offset:16384\nv_mfma_f32_16x16x32_bf16 v[28:31], a[68:71], v[60:63], v[28:31]\ns_waitcnt lgkmcnt(5)\nbuffer_load_dwordx4 v0, %[rs], s60 offen offset:3072 sc1 lds\nv_mfma_f32_16x16x32_bf16 v[32:35], v[72:75], v[60:63], v[32:35]\n" \
"v_mfma_f32_16x16x32_bf16 v[36:39], a[76:79], v[60:63], v[36:39]\ns_waitcnt lgkmcnt(4)\nv_mfma_f32_16x16x32_bf16 v[24:27], a[80:83], v[64:67], v[24:27]\ns_add_u32 m0, s57, 49152\nds_read_b128 v[60:63], v2 offset:18432\nv_mfma_f32_16x16x32_bf16 v[28:31], a[88:91], v[64:67], v[28:31]\ns_add_u32 s60, s61, 98304\nbuffer_load_dwordx4 v0, %[rs], s60 offen offset:0 sc1 lds\n" \
"v_mfma_f32_16x16x32_bf16 v[32:35], a[96:99], v[64:67], v[32:35]\ns_waitcnt lgkmcnt(4)\nv_mfma_f32_16x16x32_bf16 v[36:39], v[76:79], v[64:67], v[36:39]\ns_waitcnt lgkmcnt(3)\nv_mfma_f32_16x16x32_bf16 v[24:27], a[84:87], v[68:71], v[24:27]\nds_read_b128 v[64:67], v2 offset:20480\nv_mfma_f32_16x16x32_bf16 v[28:31], a[92:95], v[68:71], v[28:31]\nds_read_b128 v[72:75], v1 offset:6144\n" \
"buffer_load_dwordx4 v0, %[rs], s60 offen offset:1024 sc1 lds\nv_mfma_f32_16x16x32_bf16 v[32:35], a[100:103], v[68:71], v[32:35]\ns_waitcnt lgkmcnt(4)\nv_mfma_f32_16x16x32_bf16 v[36:39], v[80:83], v[68:71], v[36:39]\ns_waitcnt lgkmcnt(3)\nv_mfma_f32_16x16x32_bf16 v[24:27], a[104:107], v[56:59], v[24:27]\nds_read_b128 v[68:71], v2 offset:22528\nv_mfma_f32_16x16x32_bf16 v[28:31], a[112:115], v[56:59], v[28:31]\n" \
"ds_read_b128 v[76:79], v1 offset:7168\nbuffer_load_dwordx4 v0, %[rs], s60 offen offset:2048 sc1 lds\nv_mfma_f32_16x16x32_bf16 v[32:35], a[120:123], v[56:59], v[32:35]\nv_mfma_f32_16x16x32_bf16 v[36:39], a[128:131], v[56:59], v[36:39]\ns_waitcnt lgkmcnt(4)\nv_mfma_f32_16x16x32_bf16 v[24:27], a[108:111], v[60:63], v[24:27]\nds_read_b128 v[56:59], v2 offset:24576\nv_mfma_f32_16x16x32_bf16 v[28:31], a[116:119], v[60:63], v[28:31]\n" \
"ds_read_b128 v[80:83], v1 offset:8192\nbuffer_load_dwordx4 v0, %[rs], s60 offen offset:3072 sc1 lds\nv_mfma_f32_16x16x32_bf16 v[32:35], a[124:127], v[60:63], v[32:35]\nv_mfma_f32_16x16x32_bf16 v[36:39], a[132:135], v[60:63], v[36:39]\ns_waitcnt lgkmcnt(5)\nv_mfma_f32_16x16x32_bf16 v[24:27], a[136:139], v[64:67], v[24:27]\ns_waitcnt vmcnt(8)\ns_waitcnt lgkmcnt(4)\n" \
"ds_read_b128 v[60:63], v2 offset:26624\nv_add_f32 v84, v40, v44\nv_mfma_f32_16x16x32_bf16 v[28:31], v[72:75], v[64:67], v[28:31]\nds_read_b128 v[72:75], v1 offset:9216\nv_add_f32 v85, v48, v52\nv_mfma_f32_16x16x32_bf16 v[32:35], a[144:147], v[64:67], v[32:35]\nv_add_f32 v84, v84, v85\nv_mov_b32 v85, v84\n" \
"v_mfma_f32_16x16x32_bf16 v[36:39], a[152:155], v[64:67], v[36:39]\ns_waitcnt lgkmcnt(5)\nv_permlane16_swap_b32 v84, v85\nv_mfma_f32_16x16x32_bf16 v[24:27], a[140:143], v[68:71], v[24:27]\ns_waitcnt lgkmcnt(4)\nds_read_b128 v[64:67], v2 offset:28672\nv_add_f32 v84, v84, v85\nv_mfma_f32_16x16x32_bf16 v[28:31], v[76:79], v[68:71], v[28:31]\n" \
"v_mov_b32 v85, v84\nv_mfma_f32_16x16x32_bf16 v[32:35], a[148:151], v[68:71], v[32:35]\ns_nop 0\nv_permlane32_swap_b32 v84, v85\nv_mfma_f32_16x16x32_bf16 v[36:39], a[156:159], v[68:71], v[36:39]\ns_waitcnt lgkmcnt(4)\nds_read_b128 v[76:79], v1 offset:10240\nv_add_f32 v84, v84, v85\n" \
"v_mfma_f32_16x16x32_bf16 v[24:27], a[160:163], v[56:59], v[24:27]\nds_read_b128 v[68:71], v2 offset:30720\nv_mul_f32 v84, 0xbd800000, v84\nv_mfma_f32_16x16x32_bf16 v[28:31], a[168:171], v[56:59], v[28:31]\ns_waitcnt lgkmcnt(5)\nv_add_f32 v40, v40, v84\nv_mul_f32 v40, 0x41000000, v40\nv_mfma_f32_16x16x32_bf16 v[32:35], v[80:83], v[56:59], v[32:35]\n" \
"v_fma_f32 v41, v40, v40, v41\nv_add_f32 v44, v44, v84\nv_mfma_f32_16x16x32_bf16 v[36:39], a[176:179], v[56:59], v[36:39]\ns_waitcnt lgkmcnt(4)\nds_read_b128 v[80:83], v1 offset:11264\nv_mul_f32 v44, 0x41000000, v44\nv_mfma_f32_16x16x32_bf16 v[24:27], a[164:167], v[60:63], v[24:27]\ns_waitcnt vmcnt(4)\n" \
"s_barrier\nds_read_b128 v[56:59], v2 offset:32768\nv_mfma_f32_16x16x32_bf16 v[28:31], a[172:175], v[60:63], v[28:31]\ns_waitcnt lgkmcnt(5)\nv_fma_f32 v45, v44, v44, v45\nv_add_f32 v48, v48, v84\nv_mfma_f32_16x16x32_bf16 v[32:35], v[72:75], v[60:63], v[32:35]\nv_mul_f32 v48, 0x41000000, v48\n" \
"v_fma_f32 v49, v48, v48, v49\nv_mfma_f32_16x16x32_bf16 v[36:39], a[180:183], v[60:63], v[36:39]\ns_waitcnt lgkmcnt(4)\nv_add_f32 v52, v52, v84\nv_mul_f32 v52, 0x41000000, v52\nv_mfma_f32_16x16x32_bf16 v[24:27], a[184:187], v[64:67], v[24:27]\nds_read_b128 v[60:63], v2 offset:34816\nv_fma_f32 v53, v52, v52, v53\n" \
"v_mfma_f32_16x16x32_bf16 v[28:31], a[192:195], v[64:67], v[28:31]\nv_add_f32 v85, v41, v45\nv_add_f32 v86, v49, v53\nv_mfma_f32_16x16x32_bf16 v[32:35], a[200:203], v[64:67], v[32:35]\ns_waitcnt lgkmcnt(4)\nv_add_f32 v85, v85, v86\nv_mov_b32 v86, v85\nv_mfma_f32_16x16x32_bf16 v[36:39], v[76:79], v[64:67], v[36:39]\n" \
"s_waitcnt lgkmcnt(3)\nv_permlane16_swap_b32 v85, v86\nv_mfma_f32_16x16x32_bf16 v[24:27], a[188:191], v[68:71], v[24:27]\nds_read_b128 v[64:67], v2 offset:36864\nv_add_f32 v85, v85, v86\nv_mfma_f32_16x16x32_bf16 v[28:31], a[196:199], v[68:71], v[28:31]\nds_read_b128 v[72:75], v1 offset:12288\nv_mov_b32 v86, v85\n" \
"v_mfma_f32_16x16x32_bf16 v[32:35], a[204:207], v[68:71], v[32:35]\ns_waitcnt lgkmcnt(4)\nv_permlane32_swap_b32 v85, v86\nv_mfma_f32_16x16x32_bf16 v[36:39], v[80:83], v[68:71], v[36:39]\ns_waitcnt lgkmcnt(3)\nv_add_f32 v85, v85, v86\nv_mul_f32 v85, 0x3a800000, v85\nv_mfma_f32_16x16x32_bf16 v[24:27], a[208:211], v[56:59], v[24:27]\n" \
"ds_read_b128 v[68:71], v2 offset:38912\nv_cvt_pk_bf16_f32 v85, v85, v85\nv_mfma_f32_16x16x32_bf16 v[28:31], a[216:219], v[56:59], v[28:31]\nds_read_b128 v[76:79], v1 offset:13312\nv_and_b32 v85, 0xffff0000, v85\nv_mfma_f32_16x16x32_bf16 v[32:35], a[224:227], v[56:59], v[32:35]\nv_add_f32 v85, 0x3727c5ac, v85\nv_cvt_pk_bf16_f32 v85, v85, v85\n" \
"v_mfma_f32_16x16x32_bf16 v[36:39], a[232:235], v[56:59], v[36:39]\ns_waitcnt lgkmcnt(4)\nv_and_b32 v85, 0xffff0000, v85\nv_cvt_pk_bf16_f32 v84, v84, v84\nv_mfma_f32_16x16x32_bf16 v[24:27], a[212:215], v[60:63], v[24:27]\nds_read_b128 v[56:59], v2 offset:40960\nv_and_b32 v84, 0xffff0000, v84\nv_mfma_f32_16x16x32_bf16 v[28:31], a[220:223], v[60:63], v[28:31]\n" \
"ds_read_b128 v[80:83], v1 offset:14336\nv_mfma_f32_16x16x32_bf16 v[32:35], a[228:231], v[60:63], v[32:35]\nv_sqrt_f32 v85, v85\nv_mfma_f32_16x16x32_bf16 v[36:39], a[236:239], v[60:63], v[36:39]\ns_waitcnt lgkmcnt(5)\nv_cvt_pk_bf16_f32 v85, v85, v85\nv_and_b32 v85, 0xffff0000, v85\nv_mfma_f32_16x16x32_bf16 v[24:27], a[240:243], v[64:67], v[24:27]\n" \
"s_waitcnt lgkmcnt(4)\nds_read_b128 v[60:63], v2 offset:43008\nv_mul_f32 v86, s58, v85\nv_mfma_f32_16x16x32_bf16 v[28:31], v[72:75], v[64:67], v[28:31]\nds_read_b128 v[72:75], v1 offset:15360\nv_add_f32 v87, v42, v46\nv_mfma_f32_16x16x32_bf16 v[32:35], a[248:251], v[64:67], v[32:35]\nv_rcp_f32 v85, v85\n" \
"v_mfma_f32_16x16x32_bf16 v[36:39], v[96:99], v[64:67], v[36:39]\ns_waitcnt lgkmcnt(5)\nv_mul_f32 v85, s58, v85\nv_add_f32 v88, v50, v54\nv_mfma_f32_16x16x32_bf16 v[24:27], a[244:247], v[68:71], v[24:27]\ns_waitcnt lgkmcnt(4)\nds_read_b128 v[64:67], v2 offset:45056\nv_add_f32 v87, v87, v88\n" \
"v_mfma_f32_16x16x32_bf16 v[28:31], v[76:79], v[68:71], v[28:31]\nv_mov_b32 v88, v87\nv_mfma_f32_16x16x32_bf16 v[32:35], a[252:255], v[68:71], v[32:35]\ns_nop 0\nv_permlane16_swap_b32 v87, v88\nv_mfma_f32_16x16x32_bf16 v[36:39], v[100:103], v[68:71], v[36:39]\ns_waitcnt lgkmcnt(4)\nds_read_b128 v[76:79], v1 offset:16384\n" \
"v_add_f32 v87, v87, v88\nv_mfma_f32_16x16x32_bf16 v[24:27], v[104:107], v[56:59], v[24:27]\nds_read_b128 v[68:71], v2 offset:47104\nv_mov_b32 v88, v87\nv_mfma_f32_16x16x32_bf16 v[28:31], v[112:115], v[56:59], v[28:31]\ns_waitcnt lgkmcnt(5)\nv_permlane32_swap_b32 v87, v88\nv_mfma_f32_16x16x32_bf16 v[32:35], v[80:83], v[56:59], v[32:35]\n" \
"v_add_f32 v87, v87, v88\nv_mul_f32 v87, 0xbd800000, v87\nv_mfma_f32_16x16x32_bf16 v[36:39], v[120:123], v[56:59], v[36:39]\ns_waitcnt lgkmcnt(4)\nds_read_b128 v[80:83], v1 offset:17408\nv_add_f32 v42, v42, v87\nv_mfma_f32_16x16x32_bf16 v[24:27], v[108:111], v[60:63], v[24:27]\ns_waitcnt vmcnt(0)\n" \
"s_barrier\nds_read_b128 v[56:59], v2 offset:49152\nv_mfma_f32_16x16x32_bf16 v[28:31], v[116:119], v[60:63], v[28:31]\ns_waitcnt lgkmcnt(5)\nv_mul_f32 v42, 0x41000000, v42\nv_fma_f32 v43, v42, v42, v43\nv_mfma_f32_16x16x32_bf16 v[32:35], v[72:75], v[60:63], v[32:35]\nv_add_f32 v46, v46, v87\n" \
"v_mul_f32 v46, 0x41000000, v46\nv_mfma_f32_16x16x32_bf16 v[36:39], v[124:127], v[60:63], v[36:39]\ns_waitcnt lgkmcnt(4)\nv_fma_f32 v47, v46, v46, v47\nv_add_f32 v50, v50, v87\nv_mfma_f32_16x16x32_bf16 v[24:27], v[128:131], v[64:67], v[24:27]\nds_read_b128 v[60:63], v2 offset:51200\nv_mul_f32 v50, 0x41000000, v50\n" \
"v_mfma_f32_16x16x32_bf16 v[28:31], v[136:139], v[64:67], v[28:31]\nv_fma_f32 v51, v50, v50, v51\nv_add_f32 v54, v54, v87\nv_mfma_f32_16x16x32_bf16 v[32:35], v[144:147], v[64:67], v[32:35]\ns_waitcnt lgkmcnt(4)\nv_mul_f32 v54, 0x41000000, v54\nv_fma_f32 v55, v54, v54, v55\nv_mfma_f32_16x16x32_bf16 v[36:39], v[76:79], v[64:67], v[36:39]\n" \
"s_waitcnt lgkmcnt(3)\nv_add_f32 v88, v43, v47\nv_add_f32 v89, v51, v55\nv_mfma_f32_16x16x32_bf16 v[24:27], v[132:135], v[68:71], v[24:27]\nds_read_b128 v[64:67], v2 offset:53248\nv_add_f32 v88, v88, v89\nv_mfma_f32_16x16x32_bf16 v[28:31], v[140:143], v[68:71], v[28:31]\nds_read_b128 v[72:75], v1 offset:18432\n" \
"v_mov_b32 v89, v88\nv_mfma_f32_16x16x32_bf16 v[32:35], v[148:151], v[68:71], v[32:35]\ns_waitcnt lgkmcnt(4)\nv_permlane16_swap_b32 v88, v89\nv_mfma_f32_16x16x32_bf16 v[36:39], v[80:83], v[68:71], v[36:39]\ns_waitcnt lgkmcnt(3)\nv_add_f32 v88, v88, v89\nv_mov_b32 v89, v88\n" \
"v_mfma_f32_16x16x32_bf16 v[24:27], v[152:155], v[56:59], v[24:27]\nds_read_b128 v[68:71], v2 offset:55296\nv_mfma_f32_16x16x32_bf16 v[28:31], v[160:163], v[56:59], v[28:31]\nds_read_b128 v[76:79], v1 offset:19456\nv_mfma_f32_16x16x32_bf16 v[32:35], v[168:171], v[56:59], v[32:35]\nv_permlane32_swap_b32 v88, v89\nv_mfma_f32_16x16x32_bf16 v[36:39], v[176:179], v[56:59], v[36:39]\ns_waitcnt lgkmcnt(4)\n" \
"v_add_f32 v88, v88, v89\nv_mul_f32 v88, 0x3a800000, v88\nv_mfma_f32_16x16x32_bf16 v[24:27], v[156:159], v[60:63], v[24:27]\nds_read_b128 v[56:59], v2 offset:57344\nv_cvt_pk_bf16_f32 v88, v88, v88\nv_mfma_f32_16x16x32_bf16 v[28:31], v[164:167], v[60:63], v[28:31]\nds_read_b128 v[80:83], v1 offset:20480\nv_and_b32 v88, 0xffff0000, v88\n" \
"v_mfma_f32_16x16x32_bf16 v[32:35], v[172:175], v[60:63], v[32:35]\nv_add_f32 v88, 0x3727c5ac, v88\nv_cvt_pk_bf16_f32 v88, v88, v88\nv_mfma_f32_16x16x32_bf16 v[36:39], v[180:183], v[60:63], v[36:39]\ns_waitcnt lgkmcnt(5)\nv_and_b32 v88, 0xffff0000, v88\nv_cvt_pk_bf16_f32 v87, v87, v87\nv_mfma_f32_16x16x32_bf16 v[24:27], v[184:187], v[64:67], v[24:27]\n" \
"s_waitcnt lgkmcnt(4)\nds_read_b128 v[60:63], v2 offset:59392\nv_and_b32 v87, 0xffff0000, v87\nv_mfma_f32_16x16x32_bf16 v[28:31], v[72:75], v[64:67], v[28:31]\nds_read_b128 v[72:75], v1 offset:21504\nv_mfma_f32_16x16x32_bf16 v[32:35], v[192:195], v[64:67], v[32:35]\nv_sqrt_f32 v88, v88\nv_mfma_f32_16x16x32_bf16 v[36:39], v[200:203], v[64:67], v[36:39]\n" \
"s_waitcnt lgkmcnt(5)\nv_cvt_pk_bf16_f32 v88, v88, v88\nv_and_b32 v88, 0xffff0000, v88\nv_mfma_f32_16x16x32_bf16 v[24:27], v[188:191], v[68:71], v[24:27]\ns_waitcnt lgkmcnt(4)\nds_read_b128 v[64:67], v2 offset:61440\nv_mul_f32 v89, s58, v88\nv_mfma_f32_16x16x32_bf16 v[28:31], v[76:79], v[68:71], v[28:31]\n" \
"v_rcp_f32 v88, v88\nv_mfma_f32_16x16x32_bf16 v[32:35], v[196:199], v[68:71], v[32:35]\nv_mul_f32 v88, s58, v88\nv_mfma_f32_16x16x32_bf16 v[36:39], v[204:207], v[68:71], v[36:39]\ns_waitcnt lgkmcnt(4)\nds_read_b128 v[76:79], v1 offset:22528\nv_mfma_f32_16x16x32_bf16 v[24:27], v[208:211], v[56:59], v[24:27]\nds_read_b128 v[68:71], v2 offset:63488\n" \
"v_mfma_f32_16x16x32_bf16 v[28:31], v[216:219], v[56:59], v[28:31]\ns_waitcnt lgkmcnt(5)\nv_mfma_f32_16x16x32_bf16 v[32:35], v[80:83], v[56:59], v[32:35]\nv_mfma_f32_16x16x32_bf16 v[36:39], v[224:227], v[56:59], v[36:39]\ns_waitcnt lgkmcnt(4)\nds_read_b128 v[80:83], v1 offset:23552\nv_mfma_f32_16x16x32_bf16 v[24:27], v[212:215], v[60:63], v[24:27]\nds_read_b128 v[56:59], v2 offset:256\n" \
"v_mfma_f32_16x16x32_bf16 v[28:31], v[220:223], v[60:63], v[28:31]\ns_waitcnt lgkmcnt(5)\nv_mfma_f32_16x16x32_bf16 v[32:35], v[72:75], v[60:63], v[32:35]\nv_mfma_f32_16x16x32_bf16 v[36:39], v[228:231], v[60:63], v[36:39]\ns_waitcnt lgkmcnt(4)\nv_mfma_f32_16x16x32_bf16 v[24:27], v[232:235], v[64:67], v[24:27]\nds_read_b128 v[60:63], v2 offset:2304\nv_mfma_f32_16x16x32_bf16 v[28:31], v[240:243], v[64:67], v[28:31]\n" \
"v_mfma_f32_16x16x32_bf16 v[32:35], v[248:251], v[64:67], v[32:35]\ns_waitcnt lgkmcnt(4)\nv_mfma_f32_16x16x32_bf16 v[36:39], v[76:79], v[64:67], v[36:39]\ns_waitcnt lgkmcnt(3)\nv_mfma_f32_16x16x32_bf16 v[24:27], v[236:239], v[68:71], v[24:27]\nds_read_b128 v[64:67], v2 offset:4352\nv_mfma_f32_16x16x32_bf16 v[28:31], v[244:247], v[68:71], v[28:31]\nds_read_b128 v[72:75], v1 offset:0\n" \
"v_mfma_f32_16x16x32_bf16 v[32:35], v[252:255], v[68:71], v[32:35]\ns_waitcnt lgkmcnt(4)\nv_mfma_f32_16x16x32_bf16 v[36:39], v[80:83], v[68:71], v[36:39]\ns_waitcnt lgkmcnt(3)\nv_mfma_f32_16x16x32_bf16 v[40:43], a[0:3], v[56:59], 0\nds_read_b128 v[68:71], v2 offset:6400\nv_mfma_f32_16x16x32_bf16 v[44:47], a[8:11], v[56:59], 0\ns_lshl_b32 s44, s40, 1\n" \
"ds_read_b128 v[76:79], v1 offset:1024\nv_mov_b32 v90, s44\nv_mfma_f32_16x16x32_bf16 v[48:51], a[16:19], v[56:59], 0\nv_mfma_f32_16x16x32_bf16 v[52:55], a[24:27], v[56:59], 0\ns_waitcnt lgkmcnt(4)\nv_fma_f32 v24, v8, v84, v24\nv_fma_f32 v25, v9, v84, v25\nv_mfma_f32_16x16x32_bf16 v[40:43], a[4:7], v[60:63], v[40:43]\n" \
"ds_read_b128 v[56:59], v2 offset:8448\nv_fma_f32 v32, v16, v84, v32\nv_mfma_f32_16x16x32_bf16 v[44:47], a[12:15], v[60:63], v[44:47]\nds_read_b128 v[80:83], v1 offset:2048\nv_fma_f32 v33, v17, v84, v33\nv_mfma_f32_16x16x32_bf16 v[48:51], a[20:23], v[60:63], v[48:51]\nv_mul_f32 v24, v24, v85\nv_mul_f32 v25, v25, v85\n" \
"v_mfma_f32_16x16x32_bf16 v[52:55], a[28:31], v[60:63], v[52:55]\ns_waitcnt lgkmcnt(5)\nv_fma_f32 v91, v24, v24, s59\nv_fma_f32 v92, v25, v25, s59\nv_mfma_f32_16x16x32_bf16 v[40:43], a[32:35], v[64:67], v[40:43]\ns_waitcnt lgkmcnt(4)\nds_read_b128 v[60:63], v2 offset:10496\nv_mul_f32 v91, v91, v24\n" \
"v_mfma_f32_16x16x32_bf16 v[44:47], v[72:75], v[64:67], v[44:47]\nds_read_b128 v[72:75], v1 offset:3072\nv_mul_f32 v92, v92, v25\nv_mfma_f32_16x16x32_bf16 v[48:51], a[40:43], v[64:67], v[48:51]\nv_exp_f32 v91, v91\nv_mfma_f32_16x16x32_bf16 v[52:55], a[48:51], v[64:67], v[52:55]\ns_waitcnt lgkmcnt(5)\nv_exp_f32 v92, v92\n" \
"v_mfma_f32_16x16x32_bf16 v[40:43], a[36:39], v[68:71], v[40:43]\ns_waitcnt lgkmcnt(4)\nds_read_b128 v[64:67], v2 offset:12544\nv_mul_f32 v24, v24, v32\nv_mfma_f32_16x16x32_bf16 v[44:47], v[76:79], v[68:71], v[44:47]\nv_mul_f32 v25, v25, v33\nv_fma_f32 v91, v91, v86, v86\nv_mfma_f32_16x16x32_bf16 v[48:51], a[44:47], v[68:71], v[48:51]\n" \
"v_fma_f32 v92, v92, v86, v86\nv_fma_f32 v26, v10, v84, v26\nv_mfma_f32_16x16x32_bf16 v[52:55], a[52:55], v[68:71], v[52:55]\ns_waitcnt lgkmcnt(4)\nds_read_b128 v[76:79], v1 offset:4096\nv_fma_f32 v27, v11, v84, v27\nv_mfma_f32_16x16x32_bf16 v[40:43], a[56:59], v[56:59], v[40:43]\nds_read_b128 v[68:71], v2 offset:14592\n" \
"v_fma_f32 v34, v18, v84, v34\nv_mfma_f32_16x16x32_bf16 v[44:47], a[64:67], v[56:59], v[44:47]\ns_waitcnt lgkmcnt(5)\nv_rcp_f32 v91, v91\nv_mfma_f32_16x16x32_bf16 v[48:51], v[80:83], v[56:59], v[48:51]\nv_rcp_f32 v92, v92\nv_mfma_f32_16x16x32_bf16 v[52:55], a[72:75], v[56:59], v[52:55]\ns_waitcnt lgkmcnt(4)\n" \
"ds_read_b128 v[80:83], v1 offset:5120\nv_mul_f32 v24, v24, v91\nv_mfma_f32_16x16x32_bf16 v[40:43], a[60:63], v[60:63], v[40:43]\nds_read_b128 v[56:59], v2 offset:16640\nv_mul_f32 v25, v25, v92\nv_mfma_f32_16x16x32_bf16 v[44:47], a[68:71], v[60:63], v[44:47]\ns_waitcnt lgkmcnt(5)\nv_cvt_pk_bf16_f32 v32, v24, v25\n" \
"v_fma_f32 v35, v19, v84, v35\nv_mfma_f32_16x16x32_bf16 v[48:51], v[72:75], v[60:63], v[48:51]\nv_mul_f32 v26, v26, v85\nv_mul_f32 v27, v27, v85\nv_mfma_f32_16x16x32_bf16 v[52:55], a[76:79], v[60:63], v[52:55]\ns_waitcnt lgkmcnt(4)\nv_fma_f32 v91, v26, v26, s59\nv_fma_f32 v92, v27, v27, s59\n" \
"v_mfma_f32_16x16x32_bf16 v[40:43], a[80:83], v[64:67], v[40:43]\nds_read_b128 v[60:63], v2 offset:18688\nv_mul_f32 v91, v91, v26\nv_mfma_f32_16x16x32_bf16 v[44:47], a[88:91], v[64:67], v[44:47]\nv_mul_f32 v92, v92, v27\nv_mul_f32 v26, v26, v34\nv_mfma_f32_16x16x32_bf16 v[48:51], a[96:99], v[64:67], v[48:51]\ns_waitcnt lgkmcnt(4)\n" \
"v_exp_f32 v91, v91\nv_mfma_f32_16x16x32_bf16 v[52:55], v[76:79], v[64:67], v[52:55]\ns_waitcnt lgkmcnt(3)\nv_exp_f32 v92, v92\nv_mfma_f32_16x16x32_bf16 v[40:43], a[84:87], v[68:71], v[40:43]\nds_read_b128 v[64:67], v2 offset:20736\nv_mul_f32 v27, v27, v35\nv_mfma_f32_16x16x32_bf16 v[44:47], a[92:95], v[68:71], v[44:47]\n" \
"ds_read_b128 v[72:75], v1 offset:6144\nv_fma_f32 v91, v91, v86, v86\nv_mfma_f32_16x16x32_bf16 v[48:51], a[100:103], v[68:71], v[48:51]\ns_waitcnt lgkmcnt(4)\nv_fma_f32 v92, v92, v86, v86\nv_fma_f32 v28, v12, v84, v28\nv_mfma_f32_16x16x32_bf16 v[52:55], v[80:83], v[68:71], v[52:55]\ns_waitcnt lgkmcnt(3)\n" \
"v_rcp_f32 v91, v91\nv_mfma_f32_16x16x32_bf16 v[40:43], a[104:107], v[56:59], v[40:43]\nds_read_b128 v[68:71], v2 offset:22784\nv_mul_f32 v26, v26, v91\nv_mfma_f32_16x16x32_bf16 v[44:47], a[112:115], v[56:59], v[44:47]\nds_read_b128 v[76:79], v1 offset:7168\nv_fma_f32 v29, v13, v84, v29\nv_mfma_f32_16x16x32_bf16 v[48:51], a[120:123], v[56:59], v[48:51]\n" \
"v_rcp_f32 v92, v92\nv_mfma_f32_16x16x32_bf16 v[52:55], a[128:131], v[56:59], v[52:55]\ns_waitcnt lgkmcnt(4)\nv_mul_f32 v27, v27, v92\nv_cvt_pk_bf16_f32 v33, v26, v27\nv_mfma_f32_16x16x32_bf16 v[40:43], a[108:111], v[60:63], v[40:43]\nds_read_b128 v[56:59], v2 offset:24832\nv_fma_f32 v36, v20, v84, v36\n" \
"v_mfma_f32_16x16x32_bf16 v[44:47], a[116:119], v[60:63], v[44:47]\nds_read_b128 v[80:83], v1 offset:8192\nv_fma_f32 v37, v21, v84, v37\nv_mfma_f32_16x16x32_bf16 v[48:51], a[124:127], v[60:63], v[48:51]\nv_mul_f32 v28, v28, v85\nv_mul_f32 v29, v29, v85\nv_mfma_f32_16x16x32_bf16 v[52:55], a[132:135], v[60:63], v[52:55]\ns_waitcnt lgkmcnt(5)\n" \
"v_fma_f32 v91, v28, v28, s59\nv_fma_f32 v92, v29, v29, s59\nv_mfma_f32_16x16x32_bf16 v[40:43], a[136:139], v[64:67], v[40:43]\ns_waitcnt lgkmcnt(4)\nds_read_b128 v[60:63], v2 offset:26880\nv_mul_f32 v91, v91, v28\nv_mfma_f32_16x16x32_bf16 v[44:47], v[72:75], v[64:67], v[44:47]\nds_read_b128 v[72:75], v1 offset:9216\n" \
"v_mul_f32 v92, v92, v29\nv_mfma_f32_16x16x32_bf16 v[48:51], a[144:147], v[64:67], v[48:51]\nv_exp_f32 v91, v91\nv_mfma_f32_16x16x32_bf16 v[52:55], a[152:155], v[64:67], v[52:55]\ns_waitcnt lgkmcnt(5)\nv_exp_f32 v92, v92\nv_mfma_f32_16x16x32_bf16 v[40:43], a[140:143], v[68:71], v[40:43]\ns_waitcnt lgkmcnt(4)\n" \
"ds_read_b128 v[64:67], v2 offset:28928\nv_mul_f32 v28, v28, v36\nv_mfma_f32_16x16x32_bf16 v[44:47], v[76:79], v[68:71], v[44:47]\nv_mul_f32 v29, v29, v37\nv_fma_f32 v91, v91, v86, v86\nv_mfma_f32_16x16x32_bf16 v[48:51], a[148:151], v[68:71], v[48:51]\nv_fma_f32 v92, v92, v86, v86\nv_fma_f32 v30, v14, v84, v30\n" \
"v_mfma_f32_16x16x32_bf16 v[52:55], a[156:159], v[68:71], v[52:55]\ns_waitcnt lgkmcnt(4)\nds_read_b128 v[76:79], v1 offset:10240\nv_fma_f32 v31, v15, v84, v31\nv_mfma_f32_16x16x32_bf16 v[40:43], a[160:163], v[56:59], v[40:43]\nds_read_b128 v[68:71], v2 offset:30976\nv_fma_f32 v38, v22, v84, v38\nv_mfma_f32_16x16x32_bf16 v[44:47], a[168:171], v[56:59], v[44:47]\n" \
"s_waitcnt lgkmcnt(5)\nv_rcp_f32 v91, v91\nv_mfma_f32_16x16x32_bf16 v[48:51], v[80:83], v[56:59], v[48:51]\nv_rcp_f32 v92, v92\nv_mfma_f32_16x16x32_bf16 v[52:55], a[176:179], v[56:59], v[52:55]\ns_waitcnt lgkmcnt(4)\nds_read_b128 v[80:83], v1 offset:11264\nv_mul_f32 v28, v28, v91\n" \
"v_mfma_f32_16x16x32_bf16 v[40:43], a[164:167], v[60:63], v[40:43]\nds_read_b128 v[56:59], v2 offset:33024\nv_mul_f32 v29, v29, v92\nv_mfma_f32_16x16x32_bf16 v[44:47], a[172:175], v[60:63], v[44:47]\ns_waitcnt lgkmcnt(5)\nv_cvt_pk_bf16_f32 v34, v28, v29\nv_fma_f32 v39, v23, v84, v39\nv_mfma_f32_16x16x32_bf16 v[48:51], v[72:75], v[60:63], v[48:51]\n" \
"v_mul_f32 v30, v30, v85\nv_mul_f32 v31, v31, v85\nv_mfma_f32_16x16x32_bf16 v[52:55], a[180:183], v[60:63], v[52:55]\ns_waitcnt lgkmcnt(4)\nv_fma_f32 v91, v30, v30, s59\nv_fma_f32 v92, v31, v31, s59\nv_mfma_f32_16x16x32_bf16 v[40:43], a[184:187], v[64:67], v[40:43]\nds_read_b128 v[60:63], v2 offset:35072\n" \
"v_mul_f32 v91, v91, v30\nv_mfma_f32_16x16x32_bf16 v[44:47], a[192:195], v[64:67], v[44:47]\nv_mul_f32 v92, v92, v31\nv_mul_f32 v30, v30, v38\nv_mfma_f32_16x16x32_bf16 v[48:51], a[200:203], v[64:67], v[48:51]\ns_waitcnt lgkmcnt(4)\nv_exp_f32 v91, v91\nv_mfma_f32_16x16x32_bf16 v[52:55], v[76:79], v[64:67], v[52:55]\n" \
"s_waitcnt lgkmcnt(3)\nv_exp_f32 v92, v92\nv_mfma_f32_16x16x32_bf16 v[40:43], a[188:191], v[68:71], v[40:43]\nds_read_b128 v[64:67], v2 offset:37120\nv_mul_f32 v31, v31, v39\nv_mfma_f32_16x16x32_bf16 v[44:47], a[196:199], v[68:71], v[44:47]\nds_read_b128 v[72:75], v1 offset:12288\nv_fma_f32 v91, v91, v86, v86\n" \
"v_mfma_f32_16x16x32_bf16 v[48:51], a[204:207], v[68:71], v[48:51]\ns_waitcnt lgkmcnt(4)\nv_fma_f32 v92, v92, v86, v86\nv_mfma_f32_16x16x32_bf16 v[52:55], v[80:83], v[68:71], v[52:55]\ns_waitcnt lgkmcnt(3)\nv_rcp_f32 v91, v91\nv_mfma_f32_16x16x32_bf16 v[40:43], a[208:211], v[56:59], v[40:43]\nds_read_b128 v[68:71], v2 offset:39168\n" \
"v_mul_f32 v30, v30, v91\nv_mfma_f32_16x16x32_bf16 v[44:47], a[216:219], v[56:59], v[44:47]\nds_read_b128 v[76:79], v1 offset:13312\nv_mfma_f32_16x16x32_bf16 v[48:51], a[224:227], v[56:59], v[48:51]\nv_rcp_f32 v92, v92\nv_mfma_f32_16x16x32_bf16 v[52:55], a[232:235], v[56:59], v[52:55]\ns_waitcnt lgkmcnt(4)\nv_mul_f32 v31, v31, v92\n" \
"v_cvt_pk_bf16_f32 v35, v30, v31\nv_mfma_f32_16x16x32_bf16 v[40:43], a[212:215], v[60:63], v[40:43]\nds_read_b128 v[56:59], v2 offset:41216\nv_mfma_f32_16x16x32_bf16 v[44:47], a[220:223], v[60:63], v[44:47]\nds_read_b128 v[80:83], v1 offset:14336\nv_mfma_f32_16x16x32_bf16 v[48:51], a[228:231], v[60:63], v[48:51]\nbuffer_store_dwordx4 v[32:35], v4, %[rs], s55 offen offset:0 sc1\nv_mfma_f32_16x16x32_bf16 v[52:55], a[236:239], v[60:63], v[52:55]\n" \
"s_waitcnt lgkmcnt(5)\nv_mfma_f32_16x16x32_bf16 v[40:43], a[240:243], v[64:67], v[40:43]\ns_waitcnt lgkmcnt(4)\nds_read_b128 v[60:63], v2 offset:43264\nv_mfma_f32_16x16x32_bf16 v[44:47], v[72:75], v[64:67], v[44:47]\nds_read_b128 v[72:75], v1 offset:15360\nv_mfma_f32_16x16x32_bf16 v[48:51], a[248:251], v[64:67], v[48:51]\nv_mfma_f32_16x16x32_bf16 v[52:55], v[96:99], v[64:67], v[52:55]\n" \
"s_waitcnt lgkmcnt(5)\nv_mfma_f32_16x16x32_bf16 v[40:43], a[244:247], v[68:71], v[40:43]\ns_waitcnt lgkmcnt(4)\nds_read_b128 v[64:67], v2 offset:45312\nv_mfma_f32_16x16x32_bf16 v[44:47], v[76:79], v[68:71], v[44:47]\nds_read_b128 v[24:27], v2 offset:47360\nv_mfma_f32_16x16x32_bf16 v[48:51], a[252:255], v[68:71], v[48:51]\nds_read_b128 v[28:31], v2 offset:49408\n" \
"v_mfma_f32_16x16x32_bf16 v[52:55], v[100:103], v[68:71], v[52:55]\ns_waitcnt lgkmcnt(6)\nv_mfma_f32_16x16x32_bf16 v[40:43], v[104:107], v[56:59], v[40:43]\ns_waitcnt lgkmcnt(5)\nds_read_b128 v[68:71], v2 offset:51456\nv_mfma_f32_16x16x32_bf16 v[48:51], v[80:83], v[56:59], v[48:51]\ns_waitcnt lgkmcnt(5)\nv_mfma_f32_16x16x32_bf16 v[40:43], v[108:111], v[60:63], v[40:43]\n" \
"s_waitcnt lgkmcnt(4)\nds_read_b128 v[56:59], v2 offset:53504\nv_mfma_f32_16x16x32_bf16 v[48:51], v[72:75], v[60:63], v[48:51]\ns_waitcnt lgkmcnt(4)\nv_mfma_f32_16x16x32_bf16 v[40:43], v[128:131], v[64:67], v[40:43]\nds_read_b128 v[60:63], v2 offset:55552\nv_mfma_f32_16x16x32_bf16 v[48:51], v[144:147], v[64:67], v[48:51]\ns_waitcnt lgkmcnt(4)\n" \
"ds_read_b128 v[76:79], v1 offset:20480\nv_mfma_f32_16x16x32_bf16 v[40:43], v[132:135], v[24:27], v[40:43]\nds_read_b128 v[64:67], v2 offset:57600\nv_mfma_f32_16x16x32_bf16 v[48:51], v[148:151], v[24:27], v[48:51]\ns_waitcnt lgkmcnt(5)\nds_read_b128 v[80:83], v1 offset:21504\nv_mfma_f32_16x16x32_bf16 v[40:43], v[152:155], v[28:31], v[40:43]\nds_read_b128 v[24:27], v2 offset:59648\n" \
"v_mfma_f32_16x16x32_bf16 v[48:51], v[168:171], v[28:31], v[48:51]\ns_waitcnt lgkmcnt(6)\nv_mfma_f32_16x16x32_bf16 v[40:43], v[156:159], v[68:71], v[40:43]\nds_read_b128 v[28:31], v2 offset:61696\nv_mfma_f32_16x16x32_bf16 v[48:51], v[172:175], v[68:71], v[48:51]\ns_waitcnt lgkmcnt(6)\nv_mfma_f32_16x16x32_bf16 v[40:43], v[184:187], v[56:59], v[40:43]\nds_read_b128 v[68:71], v2 offset:63744\n" \
"v_mfma_f32_16x16x32_bf16 v[48:51], v[192:195], v[56:59], v[48:51]\ns_waitcnt lgkmcnt(6)\nv_mfma_f32_16x16x32_bf16 v[40:43], v[188:191], v[60:63], v[40:43]\nds_read_b128 v[56:59], v2 offset:41216\nv_mfma_f32_16x16x32_bf16 v[48:51], v[196:199], v[60:63], v[48:51]\ns_waitcnt lgkmcnt(5)\nv_mfma_f32_16x16x32_bf16 v[40:43], v[208:211], v[64:67], v[40:43]\nds_read_b128 v[60:63], v2 offset:43264\n" \
"v_mfma_f32_16x16x32_bf16 v[48:51], v[76:79], v[64:67], v[48:51]\ns_waitcnt lgkmcnt(4)\nds_read_b128 v[72:75], v1 offset:16384\nv_mfma_f32_16x16x32_bf16 v[40:43], v[212:215], v[24:27], v[40:43]\nds_read_b128 v[64:67], v2 offset:45312\nv_mfma_f32_16x16x32_bf16 v[48:51], v[80:83], v[24:27], v[48:51]\ns_waitcnt lgkmcnt(5)\nds_read_b128 v[76:79], v1 offset:17408\n" \
"v_mfma_f32_16x16x32_bf16 v[40:43], v[232:235], v[28:31], v[40:43]\nds_read_b128 v[24:27], v2 offset:47360\nv_mfma_f32_16x16x32_bf16 v[48:51], v[248:251], v[28:31], v[48:51]\ns_waitcnt vmcnt(0)\ns_barrier\ns_waitcnt lgkmcnt(6)\nbuffer_store_dword v90, off, %[rs], %[fh] sc1\nv_mfma_f32_16x16x32_bf16 v[40:43], v[236:239], v[68:71], v[40:43]\n" \
"ds_read_b128 v[28:31], v2 offset:49408\nv_mfma_f32_16x16x32_bf16 v[48:51], v[252:255], v[68:71], v[48:51]\ns_waitcnt lgkmcnt(6)\nds_read_b128 v[80:83], v1 offset:18432\nv_mfma_f32_16x16x32_bf16 v[44:47], v[112:115], v[56:59], v[44:47]\nds_read_b128 v[68:71], v2 offset:51456\nv_mfma_f32_16x16x32_bf16 v[52:55], v[120:123], v[56:59], v[52:55]\ns_waitcnt lgkmcnt(7)\n" \
"v_fma_f32 v40, v8, v87, v40\nv_fma_f32 v41, v9, v87, v41\nv_mfma_f32_16x16x32_bf16 v[44:47], v[116:119], v[60:63], v[44:47]\nds_read_b128 v[56:59], v2 offset:53504\nv_fma_f32 v48, v16, v87, v48\nv_mfma_f32_16x16x32_bf16 v[52:55], v[124:127], v[60:63], v[52:55]\ns_waitcnt lgkmcnt(6)\nv_fma_f32 v49, v17, v87, v49\n" \
"v_mul_f32 v40, v40, v88\nv_mfma_f32_16x16x32_bf16 v[44:47], v[136:139], v[64:67], v[44:47]\nds_read_b128 v[60:63], v2 offset:55552\nv_mul_f32 v41, v41, v88\nv_mfma_f32_16x16x32_bf16 v[52:55], v[72:75], v[64:67], v[52:55]\ns_waitcnt lgkmcnt(5)\nds_read_b128 v[72:75], v1 offset:19456\nv_fma_f32 v91, v40, v40, s59\n" \
"v_mfma_f32_16x16x32_bf16 v[44:47], v[140:143], v[24:27], v[44:47]\nds_read_b128 v[64:67], v2 offset:57600\nv_fma_f32 v92, v41, v41, s59\nv_mfma_f32_16x16x32_bf16 v[52:55], v[76:79], v[24:27], v[52:55]\ns_waitcnt lgkmcnt(6)\nv_mul_f32 v91, v91, v40\nv_mul_f32 v92, v92, v41\nv_mfma_f32_16x16x32_bf16 v[44:47], v[160:163], v[28:31], v[44:47]\n" \
"ds_read_b128 v[24:27], v2 offset:59648\nv_mul_f32 v40, v40, v48\nv_mfma_f32_16x16x32_bf16 v[52:55], v[176:179], v[28:31], v[52:55]\ns_waitcnt lgkmcnt(5)\nds_read_b128 v[76:79], v1 offset:22528\nv_mul_f32 v41, v41, v49\nv_mfma_f32_16x16x32_bf16 v[44:47], v[164:167], v[68:71], v[44:47]\nds_read_b128 v[28:31], v2 offset:61696\n" \
"v_mfma_f32_16x16x32_bf16 v[52:55], v[180:183], v[68:71], v[52:55]\ns_waitcnt lgkmcnt(6)\nv_exp_f32 v91, v91\nv_mfma_f32_16x16x32_bf16 v[44:47], v[80:83], v[56:59], v[44:47]\nds_read_b128 v[68:71], v2 offset:63744\nds_read_b128 v[80:83], v1 offset:23552\nv_mfma_f32_16x16x32_bf16 v[52:55], v[200:203], v[56:59], v[52:55]\ns_waitcnt lgkmcnt(6)\n" \
"v_exp_f32 v92, v92\nv_mfma_f32_16x16x32_bf16 v[44:47], v[72:75], v[60:63], v[44:47]\nv_fma_f32 v91, v91, v89, v89\nv_fma_f32 v92, v92, v89, v89\nv_mfma_f32_16x16x32_bf16 v[52:55], v[204:207], v[60:63], v[52:55]\ns_waitcnt lgkmcnt(5)\nv_rcp_f32 v91, v91\nv_mfma_f32_16x16x32_bf16 v[44:47], v[216:219], v[64:67], v[44:47]\n" \
"v_rcp_f32 v92, v92\nv_mfma_f32_16x16x32_bf16 v[52:55], v[224:227], v[64:67], v[52:55]\ns_waitcnt lgkmcnt(4)\nv_mul_f32 v40, v40, v91\nv_mul_f32 v41, v41, v92\nv_mfma_f32_16x16x32_bf16 v[44:47], v[220:223], v[24:27], v[44:47]\nv_cvt_pk_bf16_f32 v48, v40, v41\nv_fma_f32 v42, v10, v87, v42\n" \
"v_mfma_f32_16x16x32_bf16 v[52:55], v[228:231], v[24:27], v[52:55]\ns_waitcnt lgkmcnt(2)\nv_fma_f32 v43, v11, v87, v43\nv_fma_f32 v50, v18, v87, v50\nv_mfma_f32_16x16x32_bf16 v[44:47], v[240:243], v[28:31], v[44:47]\nv_fma_f32 v51, v19, v87, v51\nv_mul_f32 v42, v42, v88\nv_mfma_f32_16x16x32_bf16 v[52:55], v[76:79], v[28:31], v[52:55]\n" \
"s_waitcnt lgkmcnt(1)\nv_mul_f32 v43, v43, v88\nv_fma_f32 v91, v42, v42, s59\nv_mfma_f32_16x16x32_bf16 v[44:47], v[244:247], v[68:71], v[44:47]\ns_waitcnt lgkmcnt(0)\nv_fma_f32 v92, v43, v43, s59\nv_mul_f32 v91, v91, v42\nv_mfma_f32_16x16x32_bf16 v[52:55], v[80:83], v[68:71], v[52:55]\n" \
"v_mul_f32 v92, v92, v43\nv_mul_f32 v42, v42, v50\nv_exp_f32 v91, v91\nv_exp_f32 v92, v92\nv_mul_f32 v43, v43, v51\nv_fma_f32 v91, v91, v89, v89\nv_fma_f32 v92, v92, v89, v89\nv_rcp_f32 v91, v91\n" \
"v_rcp_f32 v92, v92\nv_mul_f32 v42, v42, v91\nv_mul_f32 v43, v43, v92\nv_cvt_pk_bf16_f32 v49, v42, v43\nv_fma_f32 v44, v12, v87, v44\nv_fma_f32 v45, v13, v87, v45\nv_fma_f32 v52, v20, v87, v52\nv_fma_f32 v53, v21, v87, v53\n" \
"v_mul_f32 v44, v44, v88\nv_mul_f32 v45, v45, v88\nv_fma_f32 v91, v44, v44, s59\nv_fma_f32 v92, v45, v45, s59\nv_mul_f32 v91, v91, v44\nv_mul_f32 v92, v92, v45\nv_exp_f32 v91, v91\nv_exp_f32 v92, v92\n" \
"v_mul_f32 v44, v44, v52\nv_mul_f32 v45, v45, v53\nv_fma_f32 v91, v91, v89, v89\nv_fma_f32 v92, v92, v89, v89\nv_rcp_f32 v91, v91\nv_rcp_f32 v92, v92\nv_mul_f32 v44, v44, v91\nv_mul_f32 v45, v45, v92\n" \
"v_cvt_pk_bf16_f32 v50, v44, v45\nv_fma_f32 v46, v14, v87, v46\nv_fma_f32 v47, v15, v87, v47\nv_fma_f32 v54, v22, v87, v54\nv_fma_f32 v55, v23, v87, v55\nv_mul_f32 v46, v46, v88\nv_mul_f32 v47, v47, v88\nv_fma_f32 v91, v46, v46, s59\n" \
"v_fma_f32 v92, v47, v47, s59\nv_mul_f32 v91, v91, v46\nv_mul_f32 v92, v92, v47\nv_exp_f32 v91, v91\nv_exp_f32 v92, v92\nv_mul_f32 v46, v46, v54\nv_mul_f32 v47, v47, v55\nv_fma_f32 v91, v91, v89, v89\n" \
"v_fma_f32 v92, v92, v89, v89\nv_rcp_f32 v91, v91\nv_rcp_f32 v92, v92\nv_mul_f32 v46, v46, v91\nv_mul_f32 v47, v47, v92\nv_cvt_pk_bf16_f32 v51, v46, v47\nbuffer_store_dwordx4 v[48:51], v4, %[rs], s55 offen offset:1024 sc1\nv_add_u32 v90, 1, v90\n" \
"s_mov_b32 s56, %[fh]\ns_add_u32 s40, s40, 1\ns_cmp_lt_u32 s40, 64\ns_cbranch_scc1 .Lgg_next%=\ns_waitcnt vmcnt(0)\ns_barrier\nbuffer_store_dword v90, off, %[rs], s56 sc1\ns_branch .Lgg_end%=\n" \
".Lgg_abort%=:\ns_mov_b64 exec, -1\nv_mov_b32 v16, 0\ns_movk_i32 s44, 6400\nbuffer_store_dword v16, off, %[rs], s44 sc1\n.Lgg_end%=:\ns_waitcnt vmcnt(0) lgkmcnt(0)\n" \
""
#define DD_ASM \
"v_mbcnt_lo_u32_b32 v255, -1, 0\nv_mbcnt_hi_u32_b32 v255, -1, v255\nv_lshlrev_b32 v255, 2, v255\nv_add_u32 v255, %[tb], v255\nds_read_b32 v0, v255 offset:0\nds_read_b32 v1, v255 offset:256\nds_read_b32 v2, v255 offset:512\nds_read_b32 v3, v255 offset:768\n" \
"ds_read_b32 v4, v255 offset:1024\nds_read_b32 v5, v255 offset:1280\nds_read_b32 v6, v255 offset:1536\nds_read_b32 v7, v255 offset:1792\nds_read_b32 v125, v255 offset:2048\nds_read_b32 v126, v255 offset:2304\nds_read_b32 v127, v255 offset:2560\ns_waitcnt lgkmcnt(0)\n" \
"s_add_u32 m0, %[tb], 8192\ns_add_u32 s44, %[wo], 0\nbuffer_load_dwordx4 v125, %[rw], s44 offen nt lds\ns_add_u32 m0, %[tb], 9216\ns_add_u32 s44, %[wo], 16384\nbuffer_load_dwordx4 v125, %[rw], s44 offen nt lds\ns_add_u32 m0, %[tb], 10240\ns_add_u32 s44, %[wo], 32768\n" \
"buffer_load_dwordx4 v125, %[rw], s44 offen nt lds\ns_add_u32 m0, %[tb], 11264\ns_add_u32 s44, %[wo], 49152\nbuffer_load_dwordx4 v125, %[rw], s44 offen nt lds\ns_add_u32 m0, %[lw], 0\ns_add_u32 s44, %[wo], 65536\nbuffer_load_dwordx4 v125, %[rw], s44 offen nt lds\ns_add_u32 m0, %[lw], 1024\n" \
"s_add_u32 s44, %[wo], 81920\nbuffer_load_dwordx4 v125, %[rw], s44 offen nt lds\ns_add_u32 m0, %[lw], 2048\ns_add_u32 s44, %[wo], 98304\nbuffer_load_dwordx4 v125, %[rw], s44 offen nt lds\ns_add_u32 m0, %[lw], 3072\ns_add_u32 s44, %[wo], 114688\nbuffer_load_dwordx4 v125, %[rw], s44 offen nt lds\n" \
"s_add_u32 m0, %[lw], 4096\ns_add_u32 s44, %[wo], 131072\nbuffer_load_dwordx4 v125, %[rw], s44 offen nt lds\ns_add_u32 m0, %[lw], 5120\ns_add_u32 s44, %[wo], 147456\nbuffer_load_dwordx4 v125, %[rw], s44 offen nt lds\ns_add_u32 m0, %[lw], 6144\ns_add_u32 s44, %[wo], 163840\n" \
"buffer_load_dwordx4 v125, %[rw], s44 offen nt lds\ns_add_u32 m0, %[lw], 7168\ns_add_u32 s44, %[wo], 180224\nbuffer_load_dwordx4 v125, %[rw], s44 offen nt lds\ns_add_u32 m0, %[lw], 8192\ns_add_u32 s44, %[wo], 196608\nbuffer_load_dwordx4 v125, %[rw], s44 offen nt lds\ns_add_u32 m0, %[lw], 9216\n" \
"s_add_u32 s44, %[wo], 212992\nbuffer_load_dwordx4 v125, %[rw], s44 offen nt lds\ns_add_u32 m0, %[lw], 10240\ns_add_u32 s44, %[wo], 229376\nbuffer_load_dwordx4 v125, %[rw], s44 offen nt lds\ns_add_u32 m0, %[lw], 11264\ns_add_u32 s44, %[wo], 245760\nbuffer_load_dwordx4 v125, %[rw], s44 offen nt lds\n" \
"s_add_u32 m0, %[lw], 12288\ns_add_u32 s44, %[wo], 262144\nbuffer_load_dwordx4 v125, %[rw], s44 offen nt lds\ns_add_u32 m0, %[lw], 13312\ns_add_u32 s44, %[wo], 278528\nbuffer_load_dwordx4 v125, %[rw], s44 offen nt lds\ns_add_u32 m0, %[lw], 14336\ns_add_u32 s44, %[wo], 294912\n" \
"buffer_load_dwordx4 v125, %[rw], s44 offen nt lds\ns_add_u32 m0, %[lw], 15360\ns_add_u32 s44, %[wo], 311296\nbuffer_load_dwordx4 v125, %[rw], s44 offen nt lds\ns_add_u32 m0, %[lw], 16384\ns_add_u32 s44, %[wo], 327680\nbuffer_load_dwordx4 v125, %[rw], s44 offen nt lds\ns_add_u32 m0, %[lw], 17408\n" \
"s_add_u32 s44, %[wo], 344064\nbuffer_load_dwordx4 v125, %[rw], s44 offen nt lds\ns_add_u32 m0, %[lw], 18432\ns_add_u32 s44, %[wo], 360448\nbuffer_load_dwordx4 v125, %[rw], s44 offen nt lds\ns_add_u32 m0, %[lw], 19456\ns_add_u32 s44, %[wo], 376832\nbuffer_load_dwordx4 v125, %[rw], s44 offen nt lds\n" \
"s_add_u32 m0, %[lw], 20480\ns_add_u32 s44, %[wo], 393216\nbuffer_load_dwordx4 v125, %[rw], s44 offen nt lds\ns_add_u32 m0, %[lw], 21504\ns_add_u32 s44, %[wo], 409600\nbuffer_load_dwordx4 v125, %[rw], s44 offen nt lds\ns_add_u32 m0, %[lw], 22528\ns_add_u32 s44, %[wo], 425984\n" \
"buffer_load_dwordx4 v125, %[rw], s44 offen nt lds\ns_add_u32 m0, %[lw], 23552\ns_add_u32 s44, %[wo], 442368\nbuffer_load_dwordx4 v125, %[rw], s44 offen nt lds\ns_add_u32 m0, %[tb], 0\ns_add_u32 s44, %[wo], 458752\nbuffer_load_dwordx4 v125, %[rw], s44 offen nt lds\ns_add_u32 m0, %[tb], 1024\n" \
"s_add_u32 s44, %[wo], 475136\nbuffer_load_dwordx4 v125, %[rw], s44 offen nt lds\ns_add_u32 m0, %[tb], 2048\ns_add_u32 s44, %[wo], 491520\nbuffer_load_dwordx4 v125, %[rw], s44 offen nt lds\ns_add_u32 m0, %[tb], 3072\ns_add_u32 s44, %[wo], 507904\nbuffer_load_dwordx4 v125, %[rw], s44 offen nt lds\n" \
"s_add_u32 m0, %[tb], 4096\ns_add_u32 s44, %[wo], 524288\nbuffer_load_dwordx4 v125, %[rw], s44 offen nt lds\ns_add_u32 m0, %[tb], 5120\ns_add_u32 s44, %[wo], 540672\nbuffer_load_dwordx4 v125, %[rw], s44 offen nt lds\ns_add_u32 m0, %[tb], 6144\ns_add_u32 s44, %[wo], 557056\n" \
"buffer_load_dwordx4 v125, %[rw], s44 offen nt lds\ns_add_u32 m0, %[tb], 7168\ns_add_u32 s44, %[wo], 573440\nbuffer_load_dwordx4 v125, %[rw], s44 offen nt lds\ns_waitcnt vmcnt(32)\nds_read_b64_tr_b16 a[0:1], v126 offset:8192\nds_read_b64_tr_b16 a[2:3], v126 offset:8704\nds_read_b64_tr_b16 a[8:9], v126 offset:8224\n" \
"ds_read_b64_tr_b16 a[10:11], v126 offset:8736\nds_read_b64_tr_b16 a[16:17], v126 offset:8256\nds_read_b64_tr_b16 a[18:19], v126 offset:8768\nds_read_b64_tr_b16 a[24:25], v126 offset:8288\nds_read_b64_tr_b16 a[26:27], v126 offset:8800\ns_waitcnt lgkmcnt(0)\ns_add_u32 m0, %[tb], 8192\ns_add_u32 s44, %[wo], 589824\n" \
"buffer_load_dwordx4 v125, %[rw], s44 offen nt lds\ns_add_u32 m0, %[tb], 9216\ns_add_u32 s44, %[wo], 606208\nbuffer_load_dwordx4 v125, %[rw], s44 offen nt lds\ns_add_u32 m0, %[tb], 10240\ns_add_u32 s44, %[wo], 622592\nbuffer_load_dwordx4 v125, %[rw], s44 offen nt lds\ns_add_u32 m0, %[tb], 11264\n" \
"s_add_u32 s44, %[wo], 638976\nbuffer_load_dwordx4 v125, %[rw], s44 offen nt lds\ns_waitcnt vmcnt(32)\nds_read_b64_tr_b16 a[4:5], v127 offset:0\nds_read_b64_tr_b16 a[6:7], v127 offset:512\nds_read_b64_tr_b16 a[12:13], v127 offset:32\nds_read_b64_tr_b16 a[14:15], v127 offset:544\nds_read_b64_tr_b16 a[20:21], v127 offset:64\n" \
"ds_read_b64_tr_b16 a[22:23], v127 offset:576\nds_read_b64_tr_b16 a[28:29], v127 offset:96\nds_read_b64_tr_b16 a[30:31], v127 offset:608\ns_waitcnt lgkmcnt(0)\ns_add_u32 m0, %[lw], 0\ns_add_u32 s44, %[wo], 655360\nbuffer_load_dwordx4 v125, %[rw], s44 offen nt lds\ns_add_u32 m0, %[lw], 1024\n" \
"s_add_u32 s44, %[wo], 671744\nbuffer_load_dwordx4 v125, %[rw], s44 offen nt lds\ns_add_u32 m0, %[lw], 2048\ns_add_u32 s44, %[wo], 688128\nbuffer_load_dwordx4 v125, %[rw], s44 offen nt lds\ns_add_u32 m0, %[lw], 3072\ns_add_u32 s44, %[wo], 704512\nbuffer_load_dwordx4 v125, %[rw], s44 offen nt lds\n" \
"s_waitcnt vmcnt(32)\nds_read_b64_tr_b16 a[32:33], v127 offset:4096\nds_read_b64_tr_b16 a[34:35], v127 offset:4608\nds_read_b64_tr_b16 v[92:93], v127 offset:4128\nds_read_b64_tr_b16 v[94:95], v127 offset:4640\nds_read_b64_tr_b16 a[40:41], v127 offset:4160\nds_read_b64_tr_b16 a[42:43], v127 offset:4672\nds_read_b64_tr_b16 a[48:49], v127 offset:4192\n" \
"ds_read_b64_tr_b16 a[50:51], v127 offset:4704\ns_waitcnt lgkmcnt(0)\ns_add_u32 m0, %[lw], 4096\ns_add_u32 s44, %[wo], 720896\nbuffer_load_dwordx4 v125, %[rw], s44 offen nt lds\ns_add_u32 m0, %[lw], 5120\ns_add_u32 s44, %[wo], 737280\nbuffer_load_dwordx4 v125, %[rw], s44 offen nt lds\n" \
"s_add_u32 m0, %[lw], 6144\ns_add_u32 s44, %[wo], 753664\nbuffer_load_dwordx4 v125, %[rw], s44 offen nt lds\ns_add_u32 m0, %[lw], 7168\ns_add_u32 s44, %[wo], 770048\nbuffer_load_dwordx4 v125, %[rw], s44 offen nt lds\ns_waitcnt vmcnt(32)\nds_read_b64_tr_b16 a[36:37], v127 offset:8192\n" \
"ds_read_b64_tr_b16 a[38:39], v127 offset:8704\nds_read_b64_tr_b16 v[88:89], v127 offset:8224\nds_read_b64_tr_b16 v[90:91], v127 offset:8736\nds_read_b64_tr_b16 a[44:45], v127 offset:8256\nds_read_b64_tr_b16 a[46:47], v127 offset:8768\nds_read_b64_tr_b16 a[52:53], v127 offset:8288\nds_read_b64_tr_b16 a[54:55], v127 offset:8800\ns_waitcnt lgkmcnt(0)\n" \
"s_add_u32 m0, %[lw], 8192\ns_add_u32 s44, %[wo], 786432\nbuffer_load_dwordx4 v125, %[rw], s44 offen nt lds\ns_add_u32 m0, %[lw], 9216\ns_add_u32 s44, %[wo], 802816\nbuffer_load_dwordx4 v125, %[rw], s44 offen nt lds\ns_add_u32 m0, %[lw], 10240\ns_add_u32 s44, %[wo], 819200\n" \
"buffer_load_dwordx4 v125, %[rw], s44 offen nt lds\ns_add_u32 m0, %[lw], 11264\ns_add_u32 s44, %[wo], 835584\nbuffer_load_dwordx4 v125, %[rw], s44 offen nt lds\ns_waitcnt vmcnt(32)\nds_read_b64_tr_b16 a[56:57], v127 offset:12288\nds_read_b64_tr_b16 a[58:59], v127 offset:12800\nds_read_b64_tr_b16 a[64:65], v127 offset:12320\n" \
"ds_read_b64_tr_b16 a[66:67], v127 offset:12832\nds_read_b64_tr_b16 a[72:73], v127 offset:12352\nds_read_b64_tr_b16 a[74:75], v127 offset:12864\nds_read_b64_tr_b16 a[80:81], v127 offset:12384\nds_read_b64_tr_b16 a[82:83], v127 offset:12896\ns_waitcnt lgkmcnt(0)\ns_add_u32 m0, %[lw], 12288\ns_add_u32 s44, %[wo], 851968\n" \
"buffer_load_dwordx4 v125, %[rw], s44 offen nt lds\ns_add_u32 m0, %[lw], 13312\ns_add_u32 s44, %[wo], 868352\nbuffer_load_dwordx4 v125, %[rw], s44 offen nt lds\ns_add_u32 m0, %[lw], 14336\ns_add_u32 s44, %[wo], 884736\nbuffer_load_dwordx4 v125, %[rw], s44 offen nt lds\ns_add_u32 m0, %[lw], 15360\n" \
"s_add_u32 s44, %[wo], 901120\nbuffer_load_dwordx4 v125, %[rw], s44 offen nt lds\ns_waitcnt vmcnt(32)\nds_read_b64_tr_b16 a[60:61], v127 offset:16384\nds_read_b64_tr_b16 a[62:63], v127 offset:16896\nds_read_b64_tr_b16 a[68:69], v127 offset:16416\nds_read_b64_tr_b16 a[70:71], v127 offset:16928\nds_read_b64_tr_b16 a[76:77], v127 offset:16448\n" \
"ds_read_b64_tr_b16 a[78:79], v127 offset:16960\nds_read_b64_tr_b16 a[84:85], v127 offset:16480\nds_read_b64_tr_b16 a[86:87], v127 offset:16992\ns_waitcnt lgkmcnt(0)\ns_add_u32 m0, %[lw], 16384\ns_add_u32 s44, %[wo], 917504\nbuffer_load_dwordx4 v125, %[rw], s44 offen nt lds\ns_add_u32 m0, %[lw], 17408\n" \
"s_add_u32 s44, %[wo], 933888\nbuffer_load_dwordx4 v125, %[rw], s44 offen nt lds\ns_add_u32 m0, %[lw], 18432\ns_add_u32 s44, %[wo], 950272\nbuffer_load_dwordx4 v125, %[rw], s44 offen nt lds\ns_add_u32 m0, %[lw], 19456\ns_add_u32 s44, %[wo], 966656\nbuffer_load_dwordx4 v125, %[rw], s44 offen nt lds\n" \
"s_waitcnt vmcnt(32)\nds_read_b64_tr_b16 a[88:89], v127 offset:20480\nds_read_b64_tr_b16 a[90:91], v127 offset:20992\nds_read_b64_tr_b16 a[96:97], v127 offset:20512\nds_read_b64_tr_b16 a[98:99], v127 offset:21024\nds_read_b64_tr_b16 a[104:105], v127 offset:20544\nds_read_b64_tr_b16 a[106:107], v127 offset:21056\nds_read_b64_tr_b16 v[84:85], v127 offset:20576\n" \
"ds_read_b64_tr_b16 v[86:87], v127 offset:21088\ns_waitcnt lgkmcnt(0)\ns_add_u32 m0, %[lw], 20480\ns_add_u32 s44, %[wo], 983040\nbuffer_load_dwordx4 v125, %[rw], s44 offen nt lds\ns_add_u32 m0, %[lw], 21504\ns_add_u32 s44, %[wo], 999424\nbuffer_load_dwordx4 v125, %[rw], s44 offen nt lds\n" \
"s_add_u32 m0, %[lw], 22528\ns_add_u32 s44, %[wo], 1015808\nbuffer_load_dwordx4 v125, %[rw], s44 offen nt lds\ns_add_u32 m0, %[lw], 23552\ns_add_u32 s44, %[wo], 1032192\nbuffer_load_dwordx4 v125, %[rw], s44 offen nt lds\ns_waitcnt vmcnt(32)\nds_read_b64_tr_b16 a[92:93], v126 offset:0\n" \
"ds_read_b64_tr_b16 a[94:95], v126 offset:512\nds_read_b64_tr_b16 a[100:101], v126 offset:32\nds_read_b64_tr_b16 a[102:103], v126 offset:544\nds_read_b64_tr_b16 a[108:109], v126 offset:64\nds_read_b64_tr_b16 a[110:111], v126 offset:576\nds_read_b64_tr_b16 v[80:81], v126 offset:96\nds_read_b64_tr_b16 v[82:83], v126 offset:608\ns_waitcnt lgkmcnt(0)\n" \
"s_add_u32 m0, %[tb], 0\ns_add_u32 s44, %[wo], 1048576\nbuffer_load_dwordx4 v125, %[rw], s44 offen nt lds\ns_add_u32 m0, %[tb], 1024\ns_add_u32 s44, %[wo], 1064960\nbuffer_load_dwordx4 v125, %[rw], s44 offen nt lds\ns_add_u32 m0, %[tb], 2048\ns_add_u32 s44, %[wo], 1081344\n" \
"buffer_load_dwordx4 v125, %[rw], s44 offen nt lds\ns_add_u32 m0, %[tb], 3072\ns_add_u32 s44, %[wo], 1097728\nbuffer_load_dwordx4 v125, %[rw], s44 offen nt lds\ns_waitcnt vmcnt(32)\nds_read_b64_tr_b16 a[112:113], v126 offset:4096\nds_read_b64_tr_b16 a[114:115], v126 offset:4608\nds_read_b64_tr_b16 a[120:121], v126 offset:4128\n" \
"ds_read_b64_tr_b16 a[122:123], v126 offset:4640\nds_read_b64_tr_b16 a[128:129], v126 offset:4160\nds_read_b64_tr_b16 a[130:131], v126 offset:4672\nds_read_b64_tr_b16 a[136:137], v126 offset:4192\nds_read_b64_tr_b16 a[138:139], v126 offset:4704\ns_waitcnt lgkmcnt(0)\ns_add_u32 m0, %[tb], 4096\ns_add_u32 s44, %[wo], 1114112\n" \
"buffer_load_dwordx4 v125, %[rw], s44 offen nt lds\ns_add_u32 m0, %[tb], 5120\ns_add_u32 s44, %[wo], 1130496\nbuffer_load_dwordx4 v125, %[rw], s44 offen nt lds\ns_add_u32 m0, %[tb], 6144\ns_add_u32 s44, %[wo], 1146880\nbuffer_load_dwordx4 v125, %[rw], s44 offen nt lds\ns_add_u32 m0, %[tb], 7168\n" \
"s_add_u32 s44, %[wo], 1163264\nbuffer_load_dwordx4 v125, %[rw], s44 offen nt lds\ns_waitcnt vmcnt(32)\nds_read_b64_tr_b16 a[116:117], v126 offset:8192\nds_read_b64_tr_b16 a[118:119], v126 offset:8704\nds_read_b64_tr_b16 a[124:125], v126 offset:8224\nds_read_b64_tr_b16 a[126:127], v126 offset:8736\nds_read_b64_tr_b16 a[132:133], v126 offset:8256\n" \
"ds_read_b64_tr_b16 a[134:135], v126 offset:8768\nds_read_b64_tr_b16 a[140:141], v126 offset:8288\nds_read_b64_tr_b16 a[142:143], v126 offset:8800\ns_waitcnt lgkmcnt(0)\ns_add_u32 m0, %[tb], 8192\ns_add_u32 s44, %[wo], 1179648\nbuffer_load_dwordx4 v125, %[rw], s44 offen nt lds\ns_add_u32 m0, %[tb], 9216\n" \
"s_add_u32 s44, %[wo], 1196032\nbuffer_load_dwordx4 v125, %[rw], s44 offen nt lds\ns_add_u32 m0, %[tb], 10240\ns_add_u32 s44, %[wo], 1212416\nbuffer_load_dwordx4 v125, %[rw], s44 offen nt lds\ns_add_u32 m0, %[tb], 11264\ns_add_u32 s44, %[wo], 1228800\nbuffer_load_dwordx4 v125, %[rw], s44 offen nt lds\n" \
"s_waitcnt vmcnt(32)\nds_read_b64_tr_b16 a[144:145], v127 offset:0\nds_read_b64_tr_b16 a[146:147], v127 offset:512\nds_read_b64_tr_b16 v[76:77], v127 offset:32\nds_read_b64_tr_b16 v[78:79], v127 offset:544\nds_read_b64_tr_b16 a[152:153], v127 offset:64\nds_read_b64_tr_b16 a[154:155], v127 offset:576\nds_read_b64_tr_b16 a[160:161], v127 offset:96\n" \
"ds_read_b64_tr_b16 a[162:163], v127 offset:608\ns_waitcnt lgkmcnt(0)\ns_add_u32 m0, %[lw], 0\ns_add_u32 s44, %[wo], 1245184\nbuffer_load_dwordx4 v125, %[rw], s44 offen nt lds\ns_add_u32 m0, %[lw], 1024\ns_add_u32 s44, %[wo], 1261568\nbuffer_load_dwordx4 v125, %[rw], s44 offen nt lds\n" \
"s_add_u32 m0, %[lw], 2048\ns_add_u32 s44, %[wo], 1277952\nbuffer_load_dwordx4 v125, %[rw], s44 offen nt lds\ns_add_u32 m0, %[lw], 3072\ns_add_u32 s44, %[wo], 1294336\nbuffer_load_dwordx4 v125, %[rw], s44 offen nt lds\ns_waitcnt vmcnt(32)\nds_read_b64_tr_b16 a[148:149], v127 offset:4096\n" \
"ds_read_b64_tr_b16 a[150:151], v127 offset:4608\nds_read_b64_tr_b16 v[72:73], v127 offset:4128\nds_read_b64_tr_b16 v[74:75], v127 offset:4640\nds_read_b64_tr_b16 a[156:157], v127 offset:4160\nds_read_b64_tr_b16 a[158:159], v127 offset:4672\nds_read_b64_tr_b16 a[164:165], v127 offset:4192\nds_read_b64_tr_b16 a[166:167], v127 offset:4704\ns_waitcnt lgkmcnt(0)\n" \
"s_add_u32 m0, %[lw], 4096\ns_add_u32 s44, %[wo], 1310720\nbuffer_load_dwordx4 v125, %[rw], s44 offen nt lds\ns_add_u32 m0, %[lw], 5120\ns_add_u32 s44, %[wo], 1327104\nbuffer_load_dwordx4 v125, %[rw], s44 offen nt lds\ns_add_u32 m0, %[lw], 6144\ns_add_u32 s44, %[wo], 1343488\n" \
"buffer_load_dwordx4 v125, %[rw], s44 offen nt lds\ns_add_u32 m0, %[lw], 7168\ns_add_u32 s44, %[wo], 1359872\nbuffer_load_dwordx4 v125, %[rw], s44 offen nt lds\ns_waitcnt vmcnt(32)\nds_read_b64_tr_b16 a[168:169], v127 offset:8192\nds_read_b64_tr_b16 a[170:171], v127 offset:8704\nds_read_b64_tr_b16 a[176:177], v127 offset:8224\n" \
"ds_read_b64_tr_b16 a[178:179], v127 offset:8736\nds_read_b64_tr_b16 v[68:69], v127 offset:8256\nds_read_b64_tr_b16 v[70:71], v127 offset:8768\nds_read_b64_tr_b16 a[184:185], v127 offset:8288\nds_read_b64_tr_b16 a[186:187], v127 offset:8800\ns_waitcnt lgkmcnt(0)\ns_add_u32 m0, %[lw], 8192\ns_add_u32 s44, %[wo], 1376256\n" \
"buffer_load_dwordx4 v125, %[rw], s44 offen nt lds\ns_add_u32 m0, %[lw], 9216\ns_add_u32 s44, %[wo], 1392640\nbuffer_load_dwordx4 v125, %[rw], s44 offen nt lds\ns_add_u32 m0, %[lw], 10240\ns_add_u32 s44, %[wo], 1409024\nbuffer_load_dwordx4 v125, %[rw], s44 offen nt lds\ns_add_u32 m0, %[lw], 11264\n" \
"s_add_u32 s44, %[wo], 1425408\nbuffer_load_dwordx4 v125, %[rw], s44 offen nt lds\ns_waitcnt vmcnt(32)\nds_read_b64_tr_b16 a[172:173], v127 offset:12288\nds_read_b64_tr_b16 a[174:175], v127 offset:12800\nds_read_b64_tr_b16 a[180:181], v127 offset:12320\nds_read_b64_tr_b16 a[182:183], v127 offset:12832\nds_read_b64_tr_b16 v[64:65], v127 offset:12352\n" \
"ds_read_b64_tr_b16 v[66:67], v127 offset:12864\nds_read_b64_tr_b16 a[188:189], v127 offset:12384\nds_read_b64_tr_b16 a[190:191], v127 offset:12896\ns_waitcnt lgkmcnt(0)\ns_add_u32 m0, %[lw], 12288\ns_add_u32 s44, %[wo], 1441792\nbuffer_load_dwordx4 v125, %[rw], s44 offen nt lds\ns_add_u32 m0, %[lw], 13312\n" \
"s_add_u32 s44, %[wo], 1458176\nbuffer_load_dwordx4 v125, %[rw], s44 offen nt lds\ns_add_u32 m0, %[lw], 14336\ns_add_u32 s44, %[wo], 1474560\nbuffer_load_dwordx4 v125, %[rw], s44 offen nt lds\ns_add_u32 m0, %[lw], 15360\ns_add_u32 s44, %[wo], 1490944\nbuffer_load_dwordx4 v125, %[rw], s44 offen nt lds\n" \
"s_waitcnt vmcnt(32)\nds_read_b64_tr_b16 a[192:193], v127 offset:16384\nds_read_b64_tr_b16 a[194:195], v127 offset:16896\nds_read_b64_tr_b16 a[200:201], v127 offset:16416\nds_read_b64_tr_b16 a[202:203], v127 offset:16928\nds_read_b64_tr_b16 a[208:209], v127 offset:16448\nds_read_b64_tr_b16 a[210:211], v127 offset:16960\nds_read_b64_tr_b16 a[216:217], v127 offset:16480\n" \
"ds_read_b64_tr_b16 a[218:219], v127 offset:16992\ns_waitcnt lgkmcnt(0)\ns_add_u32 m0, %[lw], 16384\ns_add_u32 s44, %[wo], 1507328\nbuffer_load_dwordx4 v125, %[rw], s44 offen nt lds\ns_add_u32 m0, %[lw], 17408\ns_add_u32 s44, %[wo], 1523712\nbuffer_load_dwordx4 v125, %[rw], s44 offen nt lds\n" \
"s_add_u32 m0, %[lw], 18432\ns_add_u32 s44, %[wo], 1540096\nbuffer_load_dwordx4 v125, %[rw], s44 offen nt lds\ns_add_u32 m0, %[lw], 19456\ns_add_u32 s44, %[wo], 1556480\nbuffer_load_dwordx4 v125, %[rw], s44 offen nt lds\ns_waitcnt vmcnt(32)\nds_read_b64_tr_b16 a[196:197], v127 offset:20480\n" \
"ds_read_b64_tr_b16 a[198:199], v127 offset:20992\nds_read_b64_tr_b16 a[204:205], v127 offset:20512\nds_read_b64_tr_b16 a[206:207], v127 offset:21024\nds_read_b64_tr_b16 a[212:213], v127 offset:20544\nds_read_b64_tr_b16 a[214:215], v127 offset:21056\nds_read_b64_tr_b16 a[220:221], v127 offset:20576\nds_read_b64_tr_b16 a[222:223], v127 offset:21088\ns_waitcnt lgkmcnt(0)\n" \
"s_add_u32 m0, %[lw], 20480\ns_add_u32 s44, %[wo], 1572864\nbuffer_load_dwordx4 v125, %[rw], s44 offen nt lds\ns_add_u32 m0, %[lw], 21504\ns_add_u32 s44, %[wo], 1589248\nbuffer_load_dwordx4 v125, %[rw], s44 offen nt lds\ns_add_u32 m0, %[lw], 22528\ns_add_u32 s44, %[wo], 1605632\n" \
"buffer_load_dwordx4 v125, %[rw], s44 offen nt lds\ns_add_u32 m0, %[lw], 23552\ns_add_u32 s44, %[wo], 1622016\nbuffer_load_dwordx4 v125, %[rw], s44 offen nt lds\ns_waitcnt vmcnt(32)\nds_read_b64_tr_b16 v[60:61], v126 offset:0\nds_read_b64_tr_b16 v[62:63], v126 offset:512\nds_read_b64_tr_b16 a[224:225], v126 offset:32\n" \
"ds_read_b64_tr_b16 a[226:227], v126 offset:544\nds_read_b64_tr_b16 a[232:233], v126 offset:64\nds_read_b64_tr_b16 a[234:235], v126 offset:576\nds_read_b64_tr_b16 a[240:241], v126 offset:96\nds_read_b64_tr_b16 a[242:243], v126 offset:608\ns_waitcnt lgkmcnt(0)\ns_add_u32 m0, %[tb], 0\ns_add_u32 s44, %[wo], 1638400\n" \
"buffer_load_dwordx4 v125, %[rw], s44 offen nt lds\ns_add_u32 m0, %[tb], 1024\ns_add_u32 s44, %[wo], 1654784\nbuffer_load_dwordx4 v125, %[rw], s44 offen nt lds\ns_add_u32 m0, %[tb], 2048\ns_add_u32 s44, %[wo], 1671168\nbuffer_load_dwordx4 v125, %[rw], s44 offen nt lds\ns_add_u32 m0, %[tb], 3072\n" \
"s_add_u32 s44, %[wo], 1687552\nbuffer_load_dwordx4 v125, %[rw], s44 offen nt lds\ns_waitcnt vmcnt(32)\nds_read_b64_tr_b16 v[56:57], v126 offset:4096\nds_read_b64_tr_b16 v[58:59], v126 offset:4608\nds_read_b64_tr_b16 a[228:229], v126 offset:4128\nds_read_b64_tr_b16 a[230:231], v126 offset:4640\nds_read_b64_tr_b16 a[236:237], v126 offset:4160\n" \
"ds_read_b64_tr_b16 a[238:239], v126 offset:4672\nds_read_b64_tr_b16 a[244:245], v126 offset:4192\nds_read_b64_tr_b16 a[246:247], v126 offset:4704\ns_waitcnt lgkmcnt(0)\ns_add_u32 m0, %[tb], 4096\ns_add_u32 s44, %[wo], 1703936\nbuffer_load_dwordx4 v125, %[rw], s44 offen nt lds\ns_add_u32 m0, %[tb], 5120\n" \
"s_add_u32 s44, %[wo], 1720320\nbuffer_load_dwordx4 v125, %[rw], s44 offen nt lds\ns_add_u32 m0, %[tb], 6144\ns_add_u32 s44, %[wo], 1736704\nbuffer_load_dwordx4 v125, %[rw], s44 offen nt lds\ns_add_u32 m0, %[tb], 7168\ns_add_u32 s44, %[wo], 1753088\nbuffer_load_dwordx4 v125, %[rw], s44 offen nt lds\n" \
"s_waitcnt vmcnt(32)\nds_read_b64_tr_b16 a[248:249], v126 offset:8192\nds_read_b64_tr_b16 a[250:251], v126 offset:8704\nds_read_b64_tr_b16 v[128:129], v126 offset:8224\nds_read_b64_tr_b16 v[130:131], v126 offset:8736\nds_read_b64_tr_b16 v[136:137], v126 offset:8256\nds_read_b64_tr_b16 v[138:139], v126 offset:8768\nds_read_b64_tr_b16 v[144:145], v126 offset:8288\n" \
"ds_read_b64_tr_b16 v[146:147], v126 offset:8800\ns_waitcnt lgkmcnt(0)\ns_add_u32 m0, %[tb], 8192\ns_add_u32 s44, %[wo], 1769472\nbuffer_load_dwordx4 v125, %[rw], s44 offen nt lds\ns_add_u32 m0, %[tb], 9216\ns_add_u32 s44, %[wo], 1785856\nbuffer_load_dwordx4 v125, %[rw], s44 offen nt lds\n" \
"s_add_u32 m0, %[tb], 10240\ns_add_u32 s44, %[wo], 1802240\nbuffer_load_dwordx4 v125, %[rw], s44 offen nt lds\ns_add_u32 m0, %[tb], 11264\ns_add_u32 s44, %[wo], 1818624\nbuffer_load_dwordx4 v125, %[rw], s44 offen nt lds\ns_waitcnt vmcnt(32)\nds_read_b64_tr_b16 a[252:253], v127 offset:0\n" \
"ds_read_b64_tr_b16 a[254:255], v127 offset:512\nds_read_b64_tr_b16 v[132:133], v127 offset:32\nds_read_b64_tr_b16 v[134:135], v127 offset:544\nds_read_b64_tr_b16 v[140:141], v127 offset:64\nds_read_b64_tr_b16 v[142:143], v127 offset:576\nds_read_b64_tr_b16 v[148:149], v127 offset:96\nds_read_b64_tr_b16 v[150:151], v127 offset:608\ns_waitcnt lgkmcnt(0)\n" \
"ds_write_b128 v1, v[92:95] offset:0\nds_write_b128 v1, v[88:91] offset:1024\nds_write_b128 v1, v[84:87] offset:2048\nds_write_b128 v1, v[80:83] offset:3072\ns_waitcnt vmcnt(28)\nds_read_b64_tr_b16 v[152:153], v127 offset:4096\nds_read_b64_tr_b16 v[154:155], v127 offset:4608\nds_read_b64_tr_b16 v[160:161], v127 offset:4128\n" \
"ds_read_b64_tr_b16 v[162:163], v127 offset:4640\nds_read_b64_tr_b16 v[80:81], v127 offset:4160\nds_read_b64_tr_b16 v[82:83], v127 offset:4672\nds_read_b64_tr_b16 v[168:169], v127 offset:4192\nds_read_b64_tr_b16 v[170:171], v127 offset:4704\ns_waitcnt lgkmcnt(0)\nds_write_b128 v1, v[76:79] offset:4096\nds_write_b128 v1, v[72:75] offset:5120\n" \
"ds_write_b128 v1, v[68:71] offset:6144\nds_write_b128 v1, v[64:67] offset:7168\ns_waitcnt vmcnt(24)\nds_read_b64_tr_b16 v[156:157], v127 offset:8192\nds_read_b64_tr_b16 v[158:159], v127 offset:8704\nds_read_b64_tr_b16 v[164:165], v127 offset:8224\nds_read_b64_tr_b16 v[166:167], v127 offset:8736\nds_read_b64_tr_b16 v[64:65], v127 offset:8256\n" \
"ds_read_b64_tr_b16 v[66:67], v127 offset:8768\nds_read_b64_tr_b16 v[172:173], v127 offset:8288\nds_read_b64_tr_b16 v[174:175], v127 offset:8800\ns_waitcnt lgkmcnt(0)\nds_write_b128 v1, v[60:63] offset:8192\nds_write_b128 v1, v[56:59] offset:9216\nds_write_b128 v1, v[80:83] offset:10240\nds_write_b128 v1, v[64:67] offset:11264\n" \
"s_waitcnt vmcnt(20)\nds_read_b64_tr_b16 v[176:177], v127 offset:12288\nds_read_b64_tr_b16 v[178:179], v127 offset:12800\nds_read_b64_tr_b16 v[184:185], v127 offset:12320\nds_read_b64_tr_b16 v[186:187], v127 offset:12832\nds_read_b64_tr_b16 v[192:193], v127 offset:12352\nds_read_b64_tr_b16 v[194:195], v127 offset:12864\nds_read_b64_tr_b16 v[200:201], v127 offset:12384\n" \
"ds_read_b64_tr_b16 v[202:203], v127 offset:12896\ns_waitcnt lgkmcnt(0)\ns_waitcnt vmcnt(16)\nds_read_b64_tr_b16 v[180:181], v127 offset:16384\nds_read_b64_tr_b16 v[182:183], v127 offset:16896\nds_read_b64_tr_b16 v[188:189], v127 offset:16416\nds_read_b64_tr_b16 v[190:191], v127 offset:16928\nds_read_b64_tr_b16 v[196:197], v127 offset:16448\n" \
"ds_read_b64_tr_b16 v[198:199], v127 offset:16960\nds_read_b64_tr_b16 v[204:205], v127 offset:16480\nds_read_b64_tr_b16 v[206:207], v127 offset:16992\ns_waitcnt lgkmcnt(0)\ns_waitcnt vmcnt(12)\nds_read_b64_tr_b16 v[64:65], v127 offset:20480\nds_read_b64_tr_b16 v[66:67], v127 offset:20992\nds_read_b64_tr_b16 v[208:209], v127 offset:20512\n" \
"ds_read_b64_tr_b16 v[210:211], v127 offset:21024\nds_read_b64_tr_b16 v[216:217], v127 offset:20544\nds_read_b64_tr_b16 v[218:219], v127 offset:21056\nds_read_b64_tr_b16 v[224:225], v127 offset:20576\nds_read_b64_tr_b16 v[226:227], v127 offset:21088\ns_waitcnt lgkmcnt(0)\nds_write_b128 v1, v[64:67] offset:12288\ns_waitcnt vmcnt(8)\n" \
"ds_read_b64_tr_b16 v[64:65], v126 offset:0\nds_read_b64_tr_b16 v[66:67], v126 offset:512\nds_read_b64_tr_b16 v[212:213], v126 offset:32\nds_read_b64_tr_b16 v[214:215], v126 offset:544\nds_read_b64_tr_b16 v[220:221], v126 offset:64\nds_read_b64_tr_b16 v[222:223], v126 offset:576\nds_read_b64_tr_b16 v[228:229], v126 offset:96\nds_read_b64_tr_b16 v[230:231], v126 offset:608\n" \
"s_waitcnt lgkmcnt(0)\nds_write_b128 v1, v[64:67] offset:13312\ns_waitcnt vmcnt(4)\nds_read_b64_tr_b16 v[232:233], v126 offset:4096\nds_read_b64_tr_b16 v[234:235], v126 offset:4608\nds_read_b64_tr_b16 v[64:65], v126 offset:4128\nds_read_b64_tr_b16 v[66:67], v126 offset:4640\nds_read_b64_tr_b16 v[240:241], v126 offset:4160\n" \
"ds_read_b64_tr_b16 v[242:243], v126 offset:4672\nds_read_b64_tr_b16 v[248:249], v126 offset:4192\nds_read_b64_tr_b16 v[250:251], v126 offset:4704\ns_waitcnt lgkmcnt(0)\nds_write_b128 v1, v[64:67] offset:14336\ns_waitcnt vmcnt(0)\nds_read_b64_tr_b16 v[236:237], v126 offset:8192\nds_read_b64_tr_b16 v[238:239], v126 offset:8704\n" \
"ds_read_b64_tr_b16 v[64:65], v126 offset:8224\nds_read_b64_tr_b16 v[66:67], v126 offset:8736\nds_read_b64_tr_b16 v[244:245], v126 offset:8256\nds_read_b64_tr_b16 v[246:247], v126 offset:8768\nds_read_b64_tr_b16 v[252:253], v126 offset:8288\nds_read_b64_tr_b16 v[254:255], v126 offset:8800\ns_waitcnt lgkmcnt(0)\nds_write_b128 v1, v[64:67] offset:15360\n" \
"s_waitcnt lgkmcnt(0)\ns_barrier\ns_mov_b32 s40, 0\ns_mov_b32 s43, 0\ns_mov_b32 s56, -2147483648\n.Ldd_next%=:\nbuffer_load_dword v8, v7, %[rs], 0 offen sc1\ns_lshl_b32 s58, s40, 1\n" \
"s_add_u32 s44, s40, 0\ns_lshl_b32 s44, s44, 19\ns_add_u32 s44, s44, %[ho]\ns_lshr_b32 s45, s44, 8\ns_and_b32 s45, s45, 0x2000\ns_xor_b32 s55, s44, s45\ns_waitcnt vmcnt(0)\nbuffer_store_dword v9, off, %[rs], s56 sc1\n" \
"s_mov_b32 s56, -2147483648\n.Ldd_w0%=:\nv_cmp_gt_i32 vcc, s58, v8\ns_cbranch_vccz .Ldd_w0go%=\ns_cmp_ge_u32 s40, 62\ns_cbranch_scc1 .Ldd_ns668%=\ns_sleep 8\n.Ldd_ns668%=:\n" \
"s_sleep 1\nbuffer_load_dword v8, v7, %[rs], 0 offen sc1\ns_waitcnt vmcnt(0)\ns_add_u32 s43, s43, 1\ns_cmp_lt_u32 s43, 262144\ns_cbranch_scc1 .Ldd_w0%=\ns_branch .Ldd_abort%=\n.Ldd_w0go%=:\n" \
"s_add_u32 s60, s55, 0\nbuffer_load_dwordx4 v[48:51], v0, %[rs], s60 offen offset:0 sc1\nbuffer_load_dwordx4 v[52:55], v0, %[rs], s60 offen offset:2048 sc1\ns_add_u32 s60, s55, 4096\nbuffer_load_dwordx4 v[56:59], v0, %[rs], s60 offen offset:0 sc1\nbuffer_load_dwordx4 v[60:63], v0, %[rs], s60 offen offset:2048 sc1\ns_add_u32 s60, s55, 16384\nbuffer_load_dwordx4 v[64:67], v0, %[rs], s60 offen offset:0 sc1\n" \
"buffer_load_dwordx4 v[68:71], v0, %[rs], s60 offen offset:2048 sc1\ns_add_u32 s60, s55, 20480\nbuffer_load_dwordx4 v[72:75], v0, %[rs], s60 offen offset:0 sc1\nbuffer_load_dwordx4 v[76:79], v0, %[rs], s60 offen offset:2048 sc1\ns_add_u32 s60, s55, 32768\nbuffer_load_dwordx4 v[80:83], v0, %[rs], s60 offen offset:0 sc1\nbuffer_load_dwordx4 v[84:87], v0, %[rs], s60 offen offset:2048 sc1\ns_add_u32 s60, s55, 36864\n" \
"buffer_load_dwordx4 v[88:91], v0, %[rs], s60 offen offset:0 sc1\nbuffer_load_dwordx4 v[92:95], v0, %[rs], s60 offen offset:2048 sc1\ns_add_u32 s60, s55, 49152\nbuffer_load_dwordx4 v[96:99], v0, %[rs], s60 offen offset:0 sc1\nbuffer_load_dwordx4 v[100:103], v0, %[rs], s60 offen offset:2048 sc1\n.Ldd_run%=:\ns_add_u32 s44, s40, 0\ns_lshl_b32 s44, s44, 17\n" \
"s_add_u32 s44, s44, %[xn]\ns_lshr_b32 s45, s44, 8\ns_and_b32 s45, s45, 0x2000\ns_xor_b32 s41, s44, s45\ns_lshl_b32 s44, s40, 16\ns_cmp_eq_u32 %[l], 0\ns_cselect_b32 s42, s41, s44\ns_add_u32 s58, s58, 1\n" \
"ds_read_b128 v[120:123], v1 offset:0\ns_waitcnt vmcnt(13)\nv_mfma_f32_16x16x32_bf16 v[16:19], a[0:3], v[48:51], 0\nds_read_b128 v[124:127], v1 offset:1024\nv_mfma_f32_16x16x32_bf16 v[24:27], a[8:11], v[48:51], 0\nbuffer_load_dword v8, v7, %[rs], 0 offen sc1\nv_mfma_f32_16x16x32_bf16 v[32:35], a[16:19], v[48:51], 0\ns_add_u32 s60, s55, 53248\n" \
"v_mfma_f32_16x16x32_bf16 v[40:43], a[24:27], v[48:51], 0\ns_waitcnt vmcnt(13)\nbuffer_load_dwordx4 v[48:51], v0, %[rs], s60 offen offset:0 sc1\nv_mfma_f32_16x16x32_bf16 v[16:19], a[4:7], v[52:55], v[16:19]\nv_mfma_f32_16x16x32_bf16 v[24:27], a[12:15], v[52:55], v[24:27]\nv_mfma_f32_16x16x32_bf16 v[32:35], a[20:23], v[52:55], v[32:35]\nv_mfma_f32_16x16x32_bf16 v[40:43], a[28:31], v[52:55], v[40:43]\ns_waitcnt vmcnt(13)\n" \
"buffer_load_dwordx4 v[52:55], v0, %[rs], s60 offen offset:2048 sc1\nv_mfma_f32_16x16x32_bf16 v[16:19], a[32:35], v[56:59], v[16:19]\ns_waitcnt lgkmcnt(1)\nv_mfma_f32_16x16x32_bf16 v[24:27], v[120:123], v[56:59], v[24:27]\nv_mfma_f32_16x16x32_bf16 v[32:35], a[40:43], v[56:59], v[32:35]\ns_add_u32 s60, s55, 65536\nv_mfma_f32_16x16x32_bf16 v[40:43], a[48:51], v[56:59], v[40:43]\ns_waitcnt vmcnt(13)\n" \
"buffer_load_dwordx4 v[56:59], v0, %[rs], s60 offen offset:0 sc1\nv_mfma_f32_16x16x32_bf16 v[16:19], a[36:39], v[60:63], v[16:19]\ns_waitcnt lgkmcnt(0)\nds_read_b128 v[120:123], v1 offset:2048\nv_mfma_f32_16x16x32_bf16 v[24:27], v[124:127], v[60:63], v[24:27]\nv_mfma_f32_16x16x32_bf16 v[32:35], a[44:47], v[60:63], v[32:35]\nv_mfma_f32_16x16x32_bf16 v[40:43], a[52:55], v[60:63], v[40:43]\ns_waitcnt vmcnt(13)\n" \
"buffer_load_dwordx4 v[60:63], v0, %[rs], s60 offen offset:2048 sc1\nv_mfma_f32_16x16x32_bf16 v[16:19], a[56:59], v[64:67], v[16:19]\nds_read_b128 v[124:127], v1 offset:3072\nv_mfma_f32_16x16x32_bf16 v[24:27], a[64:67], v[64:67], v[24:27]\nv_mfma_f32_16x16x32_bf16 v[32:35], a[72:75], v[64:67], v[32:35]\ns_add_u32 s60, s55, 69632\nv_mfma_f32_16x16x32_bf16 v[40:43], a[80:83], v[64:67], v[40:43]\ns_waitcnt vmcnt(13)\n" \
"buffer_load_dwordx4 v[64:67], v0, %[rs], s60 offen offset:0 sc1\nv_mfma_f32_16x16x32_bf16 v[16:19], a[60:63], v[68:71], v[16:19]\nv_mfma_f32_16x16x32_bf16 v[24:27], a[68:71], v[68:71], v[24:27]\nv_mfma_f32_16x16x32_bf16 v[32:35], a[76:79], v[68:71], v[32:35]\nv_mfma_f32_16x16x32_bf16 v[40:43], a[84:87], v[68:71], v[40:43]\ns_waitcnt vmcnt(13)\nbuffer_load_dwordx4 v[68:71], v0, %[rs], s60 offen offset:2048 sc1\nv_mfma_f32_16x16x32_bf16 v[16:19], a[88:91], v[72:75], v[16:19]\n" \
"v_mfma_f32_16x16x32_bf16 v[24:27], a[96:99], v[72:75], v[24:27]\nv_mfma_f32_16x16x32_bf16 v[32:35], a[104:107], v[72:75], v[32:35]\ns_waitcnt lgkmcnt(1)\ns_add_u32 s60, s55, 81920\nv_mfma_f32_16x16x32_bf16 v[40:43], v[120:123], v[72:75], v[40:43]\ns_waitcnt vmcnt(13)\nbuffer_load_dwordx4 v[72:75], v0, %[rs], s60 offen offset:0 sc1\nv_mfma_f32_16x16x32_bf16 v[16:19], a[92:95], v[76:79], v[16:19]\n" \
"ds_read_b128 v[120:123], v1 offset:4096\nv_mfma_f32_16x16x32_bf16 v[24:27], a[100:103], v[76:79], v[24:27]\nv_mfma_f32_16x16x32_bf16 v[32:35], a[108:111], v[76:79], v[32:35]\ns_waitcnt lgkmcnt(1)\nv_mfma_f32_16x16x32_bf16 v[40:43], v[124:127], v[76:79], v[40:43]\ns_waitcnt vmcnt(13)\nbuffer_load_dwordx4 v[76:79], v0, %[rs], s60 offen offset:2048 sc1\nv_mfma_f32_16x16x32_bf16 v[16:19], a[112:115], v[80:83], v[16:19]\n" \
"ds_read_b128 v[124:127], v1 offset:5120\nv_mfma_f32_16x16x32_bf16 v[24:27], a[120:123], v[80:83], v[24:27]\nv_mfma_f32_16x16x32_bf16 v[32:35], a[128:131], v[80:83], v[32:35]\ns_add_u32 s60, s55, 86016\nv_mfma_f32_16x16x32_bf16 v[40:43], a[136:139], v[80:83], v[40:43]\ns_waitcnt vmcnt(13)\nbuffer_load_dwordx4 v[80:83], v0, %[rs], s60 offen offset:0 sc1\nv_mfma_f32_16x16x32_bf16 v[16:19], a[116:119], v[84:87], v[16:19]\n" \
"ds_read_b128 v[112:115], v1 offset:6144\nv_mfma_f32_16x16x32_bf16 v[24:27], a[124:127], v[84:87], v[24:27]\nv_mfma_f32_16x16x32_bf16 v[32:35], a[132:135], v[84:87], v[32:35]\nv_mfma_f32_16x16x32_bf16 v[40:43], a[140:143], v[84:87], v[40:43]\ns_waitcnt vmcnt(13)\nbuffer_load_dwordx4 v[84:87], v0, %[rs], s60 offen offset:2048 sc1\nv_mfma_f32_16x16x32_bf16 v[16:19], a[144:147], v[88:91], v[16:19]\ns_waitcnt lgkmcnt(2)\n" \
"ds_read_b128 v[116:119], v1 offset:7168\nv_mfma_f32_16x16x32_bf16 v[24:27], v[120:123], v[88:91], v[24:27]\nv_mfma_f32_16x16x32_bf16 v[32:35], a[152:155], v[88:91], v[32:35]\ns_add_u32 s60, s55, 98304\nv_mfma_f32_16x16x32_bf16 v[40:43], a[160:163], v[88:91], v[40:43]\ns_waitcnt vmcnt(13)\nbuffer_load_dwordx4 v[88:91], v0, %[rs], s60 offen offset:0 sc1\nv_mfma_f32_16x16x32_bf16 v[16:19], a[148:151], v[92:95], v[16:19]\n" \
"s_waitcnt lgkmcnt(2)\nv_mfma_f32_16x16x32_bf16 v[24:27], v[124:127], v[92:95], v[24:27]\nv_mfma_f32_16x16x32_bf16 v[32:35], a[156:159], v[92:95], v[32:35]\nv_mfma_f32_16x16x32_bf16 v[40:43], a[164:167], v[92:95], v[40:43]\ns_waitcnt vmcnt(13)\nbuffer_load_dwordx4 v[92:95], v0, %[rs], s60 offen offset:2048 sc1\nv_mfma_f32_16x16x32_bf16 v[16:19], a[168:171], v[96:99], v[16:19]\nv_mfma_f32_16x16x32_bf16 v[24:27], a[176:179], v[96:99], v[24:27]\n" \
"s_waitcnt lgkmcnt(1)\nv_mfma_f32_16x16x32_bf16 v[32:35], v[112:115], v[96:99], v[32:35]\ns_add_u32 s60, s55, 102400\nv_mfma_f32_16x16x32_bf16 v[40:43], a[184:187], v[96:99], v[40:43]\ns_waitcnt vmcnt(13)\nbuffer_load_dwordx4 v[96:99], v0, %[rs], s60 offen offset:0 sc1\nv_mfma_f32_16x16x32_bf16 v[16:19], a[172:175], v[100:103], v[16:19]\nds_read_b128 v[112:115], v1 offset:8192\n" \
"v_mfma_f32_16x16x32_bf16 v[24:27], a[180:183], v[100:103], v[24:27]\ns_waitcnt vmcnt(13)\ns_waitcnt lgkmcnt(1)\nv_cmp_gt_i32 vcc, s58, v8\nv_mfma_f32_16x16x32_bf16 v[32:35], v[116:119], v[100:103], v[32:35]\ns_cmp_eq_u64 vcc, 0\ns_cselect_b32 s61, s55, -2147483648\nv_mfma_f32_16x16x32_bf16 v[40:43], a[188:191], v[100:103], v[40:43]\n" \
"s_waitcnt vmcnt(12)\nbuffer_load_dwordx4 v[100:103], v0, %[rs], s60 offen offset:2048 sc1\nv_mfma_f32_16x16x32_bf16 v[16:19], a[192:195], v[48:51], v[16:19]\nds_read_b128 v[116:119], v1 offset:9216\nv_mfma_f32_16x16x32_bf16 v[24:27], a[200:203], v[48:51], v[24:27]\nv_mfma_f32_16x16x32_bf16 v[32:35], a[208:211], v[48:51], v[32:35]\ns_add_u32 s60, s61, 0\nv_mfma_f32_16x16x32_bf16 v[40:43], a[216:219], v[48:51], v[40:43]\n" \
"s_waitcnt vmcnt(12)\nbuffer_load_dwordx4 v[48:51], v0, %[rs], s60 offen offset:1024 sc1\nv_mfma_f32_16x16x32_bf16 v[16:19], a[196:199], v[52:55], v[16:19]\nv_mfma_f32_16x16x32_bf16 v[24:27], a[204:207], v[52:55], v[24:27]\nv_mfma_f32_16x16x32_bf16 v[32:35], a[212:215], v[52:55], v[32:35]\nv_mfma_f32_16x16x32_bf16 v[40:43], a[220:223], v[52:55], v[40:43]\ns_waitcnt vmcnt(12)\ns_waitcnt lgkmcnt(1)\n" \
"buffer_load_dwordx4 v[52:55], v0, %[rs], s60 offen offset:3072 sc1\nv_mfma_f32_16x16x32_bf16 v[16:19], v[112:115], v[56:59], v[16:19]\nv_mfma_f32_16x16x32_bf16 v[24:27], a[224:227], v[56:59], v[24:27]\nv_mfma_f32_16x16x32_bf16 v[32:35], a[232:235], v[56:59], v[32:35]\ns_add_u32 s60, s61, 4096\nv_mfma_f32_16x16x32_bf16 v[40:43], a[240:243], v[56:59], v[40:43]\ns_waitcnt vmcnt(12)\ns_waitcnt lgkmcnt(0)\n" \
"buffer_load_dwordx4 v[56:59], v0, %[rs], s60 offen offset:1024 sc1\nv_mfma_f32_16x16x32_bf16 v[16:19], v[116:119], v[60:63], v[16:19]\nds_read_b128 v[112:115], v1 offset:10240\nv_mfma_f32_16x16x32_bf16 v[24:27], a[228:231], v[60:63], v[24:27]\nv_mfma_f32_16x16x32_bf16 v[32:35], a[236:239], v[60:63], v[32:35]\nv_mfma_f32_16x16x32_bf16 v[40:43], a[244:247], v[60:63], v[40:43]\ns_waitcnt vmcnt(12)\nbuffer_load_dwordx4 v[60:63], v0, %[rs], s60 offen offset:3072 sc1\n" \
"v_mfma_f32_16x16x32_bf16 v[16:19], a[248:251], v[64:67], v[16:19]\nds_read_b128 v[116:119], v1 offset:11264\nv_mfma_f32_16x16x32_bf16 v[24:27], v[128:131], v[64:67], v[24:27]\nv_mfma_f32_16x16x32_bf16 v[32:35], v[136:139], v[64:67], v[32:35]\ns_add_u32 s60, s61, 16384\nv_mfma_f32_16x16x32_bf16 v[40:43], v[144:147], v[64:67], v[40:43]\ns_waitcnt vmcnt(12)\nbuffer_load_dwordx4 v[64:67], v0, %[rs], s60 offen offset:1024 sc1\n" \
"v_mfma_f32_16x16x32_bf16 v[16:19], a[252:255], v[68:71], v[16:19]\nv_mfma_f32_16x16x32_bf16 v[24:27], v[132:135], v[68:71], v[24:27]\nv_mfma_f32_16x16x32_bf16 v[32:35], v[140:143], v[68:71], v[32:35]\nv_mfma_f32_16x16x32_bf16 v[40:43], v[148:151], v[68:71], v[40:43]\ns_waitcnt vmcnt(12)\nbuffer_load_dwordx4 v[68:71], v0, %[rs], s60 offen offset:3072 sc1\nv_mfma_f32_16x16x32_bf16 v[16:19], v[152:155], v[72:75], v[16:19]\nv_mfma_f32_16x16x32_bf16 v[24:27], v[160:163], v[72:75], v[24:27]\n" \
"s_waitcnt lgkmcnt(1)\nv_mfma_f32_16x16x32_bf16 v[32:35], v[112:115], v[72:75], v[32:35]\ns_add_u32 s60, s61, 20480\nv_mfma_f32_16x16x32_bf16 v[40:43], v[168:171], v[72:75], v[40:43]\ns_waitcnt vmcnt(12)\nbuffer_load_dwordx4 v[72:75], v0, %[rs], s60 offen offset:1024 sc1\nv_mfma_f32_16x16x32_bf16 v[16:19], v[156:159], v[76:79], v[16:19]\nds_read_b128 v[112:115], v1 offset:12288\n" \
"v_mfma_f32_16x16x32_bf16 v[24:27], v[164:167], v[76:79], v[24:27]\ns_waitcnt lgkmcnt(1)\nv_mfma_f32_16x16x32_bf16 v[32:35], v[116:119], v[76:79], v[32:35]\nv_mfma_f32_16x16x32_bf16 v[40:43], v[172:175], v[76:79], v[40:43]\ns_waitcnt vmcnt(12)\nbuffer_load_dwordx4 v[76:79], v0, %[rs], s60 offen offset:3072 sc1\nv_mfma_f32_16x16x32_bf16 v[16:19], v[176:179], v[80:83], v[16:19]\nds_read_b128 v[116:119], v1 offset:13312\n" \
"v_mfma_f32_16x16x32_bf16 v[24:27], v[184:187], v[80:83], v[24:27]\nv_mfma_f32_16x16x32_bf16 v[32:35], v[192:195], v[80:83], v[32:35]\ns_add_u32 s60, s61, 32768\nv_mfma_f32_16x16x32_bf16 v[40:43], v[200:203], v[80:83], v[40:43]\ns_waitcnt vmcnt(12)\nbuffer_load_dwordx4 v[80:83], v0, %[rs], s60 offen offset:1024 sc1\nv_mfma_f32_16x16x32_bf16 v[16:19], v[180:183], v[84:87], v[16:19]\nds_read_b128 v[120:123], v1 offset:14336\n" \
"v_mfma_f32_16x16x32_bf16 v[24:27], v[188:191], v[84:87], v[24:27]\nv_mfma_f32_16x16x32_bf16 v[32:35], v[196:199], v[84:87], v[32:35]\nv_mfma_f32_16x16x32_bf16 v[40:43], v[204:207], v[84:87], v[40:43]\ns_waitcnt vmcnt(12)\ns_waitcnt lgkmcnt(2)\nbuffer_load_dwordx4 v[84:87], v0, %[rs], s60 offen offset:3072 sc1\nv_mfma_f32_16x16x32_bf16 v[16:19], v[112:115], v[88:91], v[16:19]\nds_read_b128 v[124:127], v1 offset:15360\n" \
"v_mfma_f32_16x16x32_bf16 v[24:27], v[208:211], v[88:91], v[24:27]\nv_mfma_f32_16x16x32_bf16 v[32:35], v[216:219], v[88:91], v[32:35]\ns_add_u32 s60, s61, 36864\nv_mfma_f32_16x16x32_bf16 v[40:43], v[224:227], v[88:91], v[40:43]\ns_waitcnt vmcnt(12)\ns_waitcnt lgkmcnt(2)\nbuffer_load_dwordx4 v[88:91], v0, %[rs], s60 offen offset:1024 sc1\nv_mfma_f32_16x16x32_bf16 v[16:19], v[116:119], v[92:95], v[16:19]\n" \
"v_mfma_f32_16x16x32_bf16 v[24:27], v[212:215], v[92:95], v[24:27]\nv_mfma_f32_16x16x32_bf16 v[32:35], v[220:223], v[92:95], v[32:35]\nv_mfma_f32_16x16x32_bf16 v[40:43], v[228:231], v[92:95], v[40:43]\ns_waitcnt vmcnt(12)\nbuffer_load_dwordx4 v[92:95], v0, %[rs], s60 offen offset:3072 sc1\nv_mfma_f32_16x16x32_bf16 v[16:19], v[232:235], v[96:99], v[16:19]\ns_waitcnt lgkmcnt(1)\nv_mfma_f32_16x16x32_bf16 v[24:27], v[120:123], v[96:99], v[24:27]\n" \
"v_mfma_f32_16x16x32_bf16 v[32:35], v[240:243], v[96:99], v[32:35]\ns_add_u32 s60, s61, 49152\nv_mfma_f32_16x16x32_bf16 v[40:43], v[248:251], v[96:99], v[40:43]\ns_waitcnt vmcnt(12)\nbuffer_load_dwordx4 v[96:99], v0, %[rs], s60 offen offset:1024 sc1\nv_mfma_f32_16x16x32_bf16 v[16:19], v[236:239], v[100:103], v[16:19]\ns_waitcnt lgkmcnt(0)\nds_read_b128 v[120:123], v1 offset:0\n" \
"v_mfma_f32_16x16x32_bf16 v[24:27], v[124:127], v[100:103], v[24:27]\nv_mfma_f32_16x16x32_bf16 v[32:35], v[244:247], v[100:103], v[32:35]\nv_mfma_f32_16x16x32_bf16 v[40:43], v[252:255], v[100:103], v[40:43]\nbuffer_load_dwordx4 v[100:103], v0, %[rs], s60 offen offset:3072 sc1\ns_cbranch_vccnz .Ldd_slow%=\n.Ldd_slowr%=:\ns_waitcnt vmcnt(13)\nv_mfma_f32_16x16x32_bf16 v[20:23], a[0:3], v[48:51], 0\n" \
"ds_read_b128 v[124:127], v1 offset:1024\nv_mfma_f32_16x16x32_bf16 v[28:31], a[8:11], v[48:51], 0\nv_mfma_f32_16x16x32_bf16 v[36:39], a[16:19], v[48:51], 0\ns_add_u32 s60, s55, 53248\nv_mfma_f32_16x16x32_bf16 v[44:47], a[24:27], v[48:51], 0\ns_waitcnt vmcnt(12)\nbuffer_load_dwordx4 v[48:51], v0, %[rs], s60 offen offset:1024 sc1\nv_mfma_f32_16x16x32_bf16 v[20:23], a[4:7], v[52:55], v[20:23]\n" \
"v_mfma_f32_16x16x32_bf16 v[28:31], a[12:15], v[52:55], v[28:31]\nv_mfma_f32_16x16x32_bf16 v[36:39], a[20:23], v[52:55], v[36:39]\nv_mfma_f32_16x16x32_bf16 v[44:47], a[28:31], v[52:55], v[44:47]\ns_waitcnt vmcnt(12)\nbuffer_load_dwordx4 v[52:55], v0, %[rs], s60 offen offset:3072 sc1\nv_mfma_f32_16x16x32_bf16 v[20:23], a[32:35], v[56:59], v[20:23]\ns_waitcnt lgkmcnt(1)\nv_mfma_f32_16x16x32_bf16 v[28:31], v[120:123], v[56:59], v[28:31]\n" \
"ds_write_b128 v2, v[16:19] offset:0\nv_mfma_f32_16x16x32_bf16 v[36:39], a[40:43], v[56:59], v[36:39]\ns_add_u32 s60, s55, 65536\nv_mfma_f32_16x16x32_bf16 v[44:47], a[48:51], v[56:59], v[44:47]\ns_waitcnt vmcnt(12)\nbuffer_load_dwordx4 v[56:59], v0, %[rs], s60 offen offset:1024 sc1\nv_mfma_f32_16x16x32_bf16 v[20:23], a[36:39], v[60:63], v[20:23]\ns_waitcnt lgkmcnt(1)\n" \
"ds_read_b128 v[120:123], v1 offset:2048\nv_mfma_f32_16x16x32_bf16 v[28:31], v[124:127], v[60:63], v[28:31]\nv_mfma_f32_16x16x32_bf16 v[36:39], a[44:47], v[60:63], v[36:39]\nv_mfma_f32_16x16x32_bf16 v[44:47], a[52:55], v[60:63], v[44:47]\ns_waitcnt vmcnt(12)\nbuffer_load_dwordx4 v[60:63], v0, %[rs], s60 offen offset:3072 sc1\nv_mfma_f32_16x16x32_bf16 v[20:23], a[56:59], v[64:67], v[20:23]\nds_read_b128 v[124:127], v1 offset:3072\n" \
"v_mfma_f32_16x16x32_bf16 v[28:31], a[64:67], v[64:67], v[28:31]\nds_write_b128 v2, v[24:27] offset:2048\nv_mfma_f32_16x16x32_bf16 v[36:39], a[72:75], v[64:67], v[36:39]\ns_add_u32 s60, s55, 69632\nv_mfma_f32_16x16x32_bf16 v[44:47], a[80:83], v[64:67], v[44:47]\ns_waitcnt vmcnt(12)\nbuffer_load_dwordx4 v[64:67], v0, %[rs], s60 offen offset:1024 sc1\nv_mfma_f32_16x16x32_bf16 v[20:23], a[60:63], v[68:71], v[20:23]\n" \
"v_mfma_f32_16x16x32_bf16 v[28:31], a[68:71], v[68:71], v[28:31]\nv_mfma_f32_16x16x32_bf16 v[36:39], a[76:79], v[68:71], v[36:39]\nv_mfma_f32_16x16x32_bf16 v[44:47], a[84:87], v[68:71], v[44:47]\ns_waitcnt vmcnt(12)\nbuffer_load_dwordx4 v[68:71], v0, %[rs], s60 offen offset:3072 sc1\nv_mfma_f32_16x16x32_bf16 v[20:23], a[88:91], v[72:75], v[20:23]\nv_mfma_f32_16x16x32_bf16 v[28:31], a[96:99], v[72:75], v[28:31]\nds_write_b128 v2, v[32:35] offset:4096\n" \
"v_mfma_f32_16x16x32_bf16 v[36:39], a[104:107], v[72:75], v[36:39]\ns_waitcnt lgkmcnt(3)\ns_add_u32 s60, s55, 81920\nv_mfma_f32_16x16x32_bf16 v[44:47], v[120:123], v[72:75], v[44:47]\ns_waitcnt vmcnt(12)\nbuffer_load_dwordx4 v[72:75], v0, %[rs], s60 offen offset:1024 sc1\nv_mfma_f32_16x16x32_bf16 v[20:23], a[92:95], v[76:79], v[20:23]\nds_read_b128 v[120:123], v1 offset:4096\n" \
"v_mfma_f32_16x16x32_bf16 v[28:31], a[100:103], v[76:79], v[28:31]\nv_mfma_f32_16x16x32_bf16 v[36:39], a[108:111], v[76:79], v[36:39]\ns_waitcnt lgkmcnt(3)\nv_mfma_f32_16x16x32_bf16 v[44:47], v[124:127], v[76:79], v[44:47]\ns_waitcnt vmcnt(12)\nbuffer_load_dwordx4 v[76:79], v0, %[rs], s60 offen offset:3072 sc1\nv_mfma_f32_16x16x32_bf16 v[20:23], a[112:115], v[80:83], v[20:23]\nds_read_b128 v[124:127], v1 offset:5120\n" \
"v_mfma_f32_16x16x32_bf16 v[28:31], a[120:123], v[80:83], v[28:31]\nds_write_b128 v2, v[40:43] offset:6144\nv_mfma_f32_16x16x32_bf16 v[36:39], a[128:131], v[80:83], v[36:39]\ns_add_u32 s60, s55, 86016\nv_mfma_f32_16x16x32_bf16 v[44:47], a[136:139], v[80:83], v[44:47]\ns_waitcnt vmcnt(12)\nbuffer_load_dwordx4 v[80:83], v0, %[rs], s60 offen offset:1024 sc1\nv_mfma_f32_16x16x32_bf16 v[20:23], a[116:119], v[84:87], v[20:23]\n" \
"ds_read_b128 v[112:115], v1 offset:6144\nv_mfma_f32_16x16x32_bf16 v[28:31], a[124:127], v[84:87], v[28:31]\nv_mfma_f32_16x16x32_bf16 v[36:39], a[132:135], v[84:87], v[36:39]\nv_mfma_f32_16x16x32_bf16 v[44:47], a[140:143], v[84:87], v[44:47]\ns_waitcnt vmcnt(12)\nbuffer_load_dwordx4 v[84:87], v0, %[rs], s60 offen offset:3072 sc1\nv_mfma_f32_16x16x32_bf16 v[20:23], a[144:147], v[88:91], v[20:23]\ns_waitcnt lgkmcnt(3)\n" \
"ds_read_b128 v[116:119], v1 offset:7168\nv_mfma_f32_16x16x32_bf16 v[28:31], v[120:123], v[88:91], v[28:31]\nv_mfma_f32_16x16x32_bf16 v[36:39], a[152:155], v[88:91], v[36:39]\ns_add_u32 s60, s55, 98304\nv_mfma_f32_16x16x32_bf16 v[44:47], a[160:163], v[88:91], v[44:47]\ns_waitcnt vmcnt(12)\nbuffer_load_dwordx4 v[88:91], v0, %[rs], s60 offen offset:1024 sc1\nv_mfma_f32_16x16x32_bf16 v[20:23], a[148:151], v[92:95], v[20:23]\n" \
"s_waitcnt lgkmcnt(3)\nv_mfma_f32_16x16x32_bf16 v[28:31], v[124:127], v[92:95], v[28:31]\nbuffer_load_dwordx4 v[12:15], v4, %[rs], s41 offen sc1\nv_mfma_f32_16x16x32_bf16 v[36:39], a[156:159], v[92:95], v[36:39]\nv_mfma_f32_16x16x32_bf16 v[44:47], a[164:167], v[92:95], v[44:47]\ns_waitcnt vmcnt(13)\nbuffer_load_dwordx4 v[92:95], v0, %[rs], s60 offen offset:3072 sc1\nv_mfma_f32_16x16x32_bf16 v[20:23], a[168:171], v[96:99], v[20:23]\n" \
"v_mfma_f32_16x16x32_bf16 v[28:31], a[176:179], v[96:99], v[28:31]\ns_waitcnt lgkmcnt(1)\nv_mfma_f32_16x16x32_bf16 v[36:39], v[112:115], v[96:99], v[36:39]\ns_add_u32 s60, s55, 102400\nv_mfma_f32_16x16x32_bf16 v[44:47], a[184:187], v[96:99], v[44:47]\ns_waitcnt vmcnt(13)\nbuffer_load_dwordx4 v[96:99], v0, %[rs], s60 offen offset:1024 sc1\nv_mfma_f32_16x16x32_bf16 v[20:23], a[172:175], v[100:103], v[20:23]\n" \
"ds_read_b128 v[112:115], v1 offset:8192\nv_mfma_f32_16x16x32_bf16 v[28:31], a[180:183], v[100:103], v[28:31]\ns_waitcnt lgkmcnt(1)\nv_mfma_f32_16x16x32_bf16 v[36:39], v[116:119], v[100:103], v[36:39]\nv_mfma_f32_16x16x32_bf16 v[44:47], a[188:191], v[100:103], v[44:47]\ns_waitcnt vmcnt(13)\nbuffer_load_dwordx4 v[100:103], v0, %[rs], s60 offen offset:3072 sc1\nv_mfma_f32_16x16x32_bf16 v[20:23], a[192:195], v[48:51], v[20:23]\n" \
"ds_read_b128 v[116:119], v1 offset:9216\nv_mfma_f32_16x16x32_bf16 v[28:31], a[200:203], v[48:51], v[28:31]\nv_mfma_f32_16x16x32_bf16 v[36:39], a[208:211], v[48:51], v[36:39]\nv_mfma_f32_16x16x32_bf16 v[44:47], a[216:219], v[48:51], v[44:47]\ns_waitcnt vmcnt(13)\nv_mfma_f32_16x16x32_bf16 v[20:23], a[196:199], v[52:55], v[20:23]\nv_mfma_f32_16x16x32_bf16 v[28:31], a[204:207], v[52:55], v[28:31]\nv_mfma_f32_16x16x32_bf16 v[36:39], a[212:215], v[52:55], v[36:39]\n" \
"v_mfma_f32_16x16x32_bf16 v[44:47], a[220:223], v[52:55], v[44:47]\ns_waitcnt vmcnt(12)\ns_waitcnt lgkmcnt(1)\nv_mfma_f32_16x16x32_bf16 v[20:23], v[112:115], v[56:59], v[20:23]\nv_mfma_f32_16x16x32_bf16 v[28:31], a[224:227], v[56:59], v[28:31]\nv_mfma_f32_16x16x32_bf16 v[36:39], a[232:235], v[56:59], v[36:39]\nv_mfma_f32_16x16x32_bf16 v[44:47], a[240:243], v[56:59], v[44:47]\ns_waitcnt vmcnt(11)\n" \
"s_waitcnt lgkmcnt(0)\nv_mfma_f32_16x16x32_bf16 v[20:23], v[116:119], v[60:63], v[20:23]\nds_read_b128 v[112:115], v1 offset:10240\nv_mfma_f32_16x16x32_bf16 v[28:31], a[228:231], v[60:63], v[28:31]\nv_mfma_f32_16x16x32_bf16 v[36:39], a[236:239], v[60:63], v[36:39]\nv_mfma_f32_16x16x32_bf16 v[44:47], a[244:247], v[60:63], v[44:47]\ns_waitcnt vmcnt(10)\nv_mfma_f32_16x16x32_bf16 v[20:23], a[248:251], v[64:67], v[20:23]\n" \
"ds_read_b128 v[116:119], v1 offset:11264\nv_mfma_f32_16x16x32_bf16 v[28:31], v[128:131], v[64:67], v[28:31]\nv_mfma_f32_16x16x32_bf16 v[36:39], v[136:139], v[64:67], v[36:39]\nv_mfma_f32_16x16x32_bf16 v[44:47], v[144:147], v[64:67], v[44:47]\ns_waitcnt vmcnt(9)\nv_mfma_f32_16x16x32_bf16 v[20:23], a[252:255], v[68:71], v[20:23]\nv_mfma_f32_16x16x32_bf16 v[28:31], v[132:135], v[68:71], v[28:31]\nv_mfma_f32_16x16x32_bf16 v[36:39], v[140:143], v[68:71], v[36:39]\n" \
"v_mfma_f32_16x16x32_bf16 v[44:47], v[148:151], v[68:71], v[44:47]\ns_waitcnt vmcnt(8)\nv_mfma_f32_16x16x32_bf16 v[20:23], v[152:155], v[72:75], v[20:23]\nv_mfma_f32_16x16x32_bf16 v[28:31], v[160:163], v[72:75], v[28:31]\ns_waitcnt lgkmcnt(1)\nv_mfma_f32_16x16x32_bf16 v[36:39], v[112:115], v[72:75], v[36:39]\nv_mfma_f32_16x16x32_bf16 v[44:47], v[168:171], v[72:75], v[44:47]\ns_waitcnt vmcnt(7)\n" \
"v_mfma_f32_16x16x32_bf16 v[20:23], v[156:159], v[76:79], v[20:23]\nds_read_b128 v[112:115], v1 offset:12288\nv_mfma_f32_16x16x32_bf16 v[28:31], v[164:167], v[76:79], v[28:31]\ns_waitcnt lgkmcnt(1)\nv_mfma_f32_16x16x32_bf16 v[36:39], v[116:119], v[76:79], v[36:39]\nv_mfma_f32_16x16x32_bf16 v[44:47], v[172:175], v[76:79], v[44:47]\ns_waitcnt vmcnt(6)\nv_mfma_f32_16x16x32_bf16 v[20:23], v[176:179], v[80:83], v[20:23]\n" \
"ds_read_b128 v[116:119], v1 offset:13312\nv_mfma_f32_16x16x32_bf16 v[28:31], v[184:187], v[80:83], v[28:31]\nv_mfma_f32_16x16x32_bf16 v[36:39], v[192:195], v[80:83], v[36:39]\nv_mfma_f32_16x16x32_bf16 v[44:47], v[200:203], v[80:83], v[44:47]\ns_waitcnt vmcnt(5)\nv_mfma_f32_16x16x32_bf16 v[20:23], v[180:183], v[84:87], v[20:23]\nds_read_b128 v[120:123], v1 offset:14336\nv_mfma_f32_16x16x32_bf16 v[28:31], v[188:191], v[84:87], v[28:31]\n" \
"v_mfma_f32_16x16x32_bf16 v[36:39], v[196:199], v[84:87], v[36:39]\nv_mfma_f32_16x16x32_bf16 v[44:47], v[204:207], v[84:87], v[44:47]\ns_waitcnt vmcnt(4)\ns_waitcnt lgkmcnt(2)\nv_mfma_f32_16x16x32_bf16 v[20:23], v[112:115], v[88:91], v[20:23]\nds_read_b128 v[124:127], v1 offset:15360\nv_mfma_f32_16x16x32_bf16 v[28:31], v[208:211], v[88:91], v[28:31]\nv_mfma_f32_16x16x32_bf16 v[36:39], v[216:219], v[88:91], v[36:39]\n" \
"v_mfma_f32_16x16x32_bf16 v[44:47], v[224:227], v[88:91], v[44:47]\ns_waitcnt vmcnt(2)\ns_waitcnt lgkmcnt(2)\nv_mfma_f32_16x16x32_bf16 v[20:23], v[116:119], v[92:95], v[20:23]\nv_mfma_f32_16x16x32_bf16 v[28:31], v[212:215], v[92:95], v[28:31]\nv_mfma_f32_16x16x32_bf16 v[36:39], v[220:223], v[92:95], v[36:39]\nv_mfma_f32_16x16x32_bf16 v[44:47], v[228:231], v[92:95], v[44:47]\ns_waitcnt vmcnt(1)\n" \
"v_mfma_f32_16x16x32_bf16 v[20:23], v[232:235], v[96:99], v[20:23]\ns_waitcnt lgkmcnt(1)\nv_mfma_f32_16x16x32_bf16 v[28:31], v[120:123], v[96:99], v[28:31]\nv_mfma_f32_16x16x32_bf16 v[36:39], v[240:243], v[96:99], v[36:39]\nv_mfma_f32_16x16x32_bf16 v[44:47], v[248:251], v[96:99], v[44:47]\ns_waitcnt vmcnt(0)\nv_mfma_f32_16x16x32_bf16 v[20:23], v[236:239], v[100:103], v[20:23]\ns_waitcnt lgkmcnt(0)\n" \
"v_lshlrev_b32 v104, 16, v12\nv_and_b32 v105, 0xffff0000, v12\nv_mfma_f32_16x16x32_bf16 v[28:31], v[124:127], v[100:103], v[28:31]\nv_lshlrev_b32 v106, 16, v13\nv_and_b32 v107, 0xffff0000, v13\nv_mfma_f32_16x16x32_bf16 v[36:39], v[244:247], v[100:103], v[36:39]\nv_lshlrev_b32 v108, 16, v14\nv_and_b32 v109, 0xffff0000, v14\n" \
"v_mfma_f32_16x16x32_bf16 v[44:47], v[252:255], v[100:103], v[44:47]\ns_mul_i32 s44, s40, %[l]\nds_write_b128 v2, v[20:23] offset:1024\nds_write_b128 v2, v[28:31] offset:3072\ns_nop 0\nds_write_b128 v2, v[36:39] offset:5120\ns_nop 2\nds_write_b128 v2, v[44:47] offset:7168\n" \
"v_lshlrev_b32 v110, 16, v15\nv_and_b32 v111, 0xffff0000, v15\nv_mov_b32 v9, s40\ns_cmp_eq_u32 s44, 0\ns_cselect_b32 s56, %[fd], -2147483648\ns_add_u32 s40, s40, 1\ns_waitcnt lgkmcnt(0)\ns_barrier\n" \
"ds_read_b128 v[16:19], v3 offset:0\nds_read_b128 v[20:23], v3 offset:256\nds_read_b128 v[24:27], v3 offset:8192\nds_read_b128 v[28:31], v3 offset:8448\nds_read_b128 v[32:35], v3 offset:16384\nds_read_b128 v[36:39], v3 offset:16640\nds_read_b128 v[40:43], v3 offset:24576\nds_read_b128 v[44:47], v3 offset:24832\n" \
"s_waitcnt lgkmcnt(6)\nv_pk_add_f32 v[16:17], v[16:17], v[104:105]\nv_pk_add_f32 v[18:19], v[18:19], v[106:107]\nv_pk_add_f32 v[20:21], v[20:21], v[108:109]\nv_pk_add_f32 v[22:23], v[22:23], v[110:111]\ns_waitcnt lgkmcnt(4)\nv_pk_add_f32 v[16:17], v[16:17], v[24:25]\nv_pk_add_f32 v[18:19], v[18:19], v[26:27]\n" \
"v_pk_add_f32 v[20:21], v[20:21], v[28:29]\nv_pk_add_f32 v[22:23], v[22:23], v[30:31]\ns_waitcnt lgkmcnt(2)\nv_pk_add_f32 v[16:17], v[16:17], v[32:33]\nv_pk_add_f32 v[18:19], v[18:19], v[34:35]\nv_pk_add_f32 v[20:21], v[20:21], v[36:37]\nv_pk_add_f32 v[22:23], v[22:23], v[38:39]\ns_waitcnt lgkmcnt(0)\n" \
"v_pk_add_f32 v[16:17], v[16:17], v[40:41]\nv_pk_add_f32 v[18:19], v[18:19], v[42:43]\nv_pk_add_f32 v[20:21], v[20:21], v[44:45]\nv_pk_add_f32 v[22:23], v[22:23], v[46:47]\nv_cvt_pk_bf16_f32 v12, v16, v17\nv_cvt_pk_bf16_f32 v13, v18, v19\nv_cvt_pk_bf16_f32 v14, v20, v21\nv_cvt_pk_bf16_f32 v15, v22, v23\n" \
"buffer_store_dwordx4 v[12:15], v5, %[ro], s42 offen sc1\nv_pk_add_f32 v[24:25], v[16:17], v[18:19]\nv_pk_add_f32 v[26:27], v[20:21], v[22:23]\nv_pk_add_f32 v[24:25], v[24:25], v[26:27]\nv_add_f32 v24, v24, v25\ns_nop 1\nv_add_f32_dpp v25, v24, v24 row_ror:8 row_mask:0xf bank_mask:0xf\nv_add_f32_dpp v26, v24, v24 row_ror:8 row_mask:0xf bank_mask:0xf\n" \
"s_nop 1\nv_permlane16_swap_b32 v25, v26\nv_add_f32 v24, v25, v26\nv_add_f32 v25, v25, v26\ns_nop 1\nv_permlane32_swap_b32 v24, v25\nv_add_f32 v24, v24, v25\nv_mul_f32 v24, 0x3c800000, v24\n" \
"v_pk_add_f32 v[16:17], v[16:17], v[24:25] op_sel_hi:[1,0] neg_lo:[0,1] neg_hi:[0,1]\nv_pk_add_f32 v[18:19], v[18:19], v[24:25] op_sel_hi:[1,0] neg_lo:[0,1] neg_hi:[0,1]\nv_pk_add_f32 v[20:21], v[20:21], v[24:25] op_sel_hi:[1,0] neg_lo:[0,1] neg_hi:[0,1]\nv_pk_add_f32 v[22:23], v[22:23], v[24:25] op_sel_hi:[1,0] neg_lo:[0,1] neg_hi:[0,1]\nv_pk_mul_f32 v[28:29], v[16:17], v[16:17]\nv_pk_mul_f32 v[30:31], v[18:19], v[18:19]\nv_pk_fma_f32 v[28:29], v[20:21], v[20:21], v[28:29]\nv_pk_fma_f32 v[30:31], v[22:23], v[22:23], v[30:31]\n" \
"v_pk_add_f32 v[28:29], v[28:29], v[30:31]\nv_add_f32 v28, v28, v29\ns_nop 1\nv_add_f32_dpp v29, v28, v28 row_ror:8 row_mask:0xf bank_mask:0xf\nv_add_f32_dpp v30, v28, v28 row_ror:8 row_mask:0xf bank_mask:0xf\ns_nop 1\nv_permlane16_swap_b32 v29, v30\nv_add_f32 v28, v29, v30\n" \
"v_add_f32 v29, v29, v30\ns_nop 1\nv_permlane32_swap_b32 v28, v29\nv_add_f32 v28, v28, v29\nv_mov_b32 v25, v28\nbuffer_store_dwordx2 v[24:25], v6, %[rs], s41 offen sc1\nv_xor_b32 v2, 0x8000, v2\nv_xor_b32 v3, 0x8000, v3\n" \
"s_cmp_lt_u32 s40, 64\ns_cbranch_scc1 .Ldd_next%=\ns_waitcnt vmcnt(0)\nbuffer_store_dword v9, off, %[rs], s56 sc1\ns_branch .Ldd_end%=\n.Ldd_slow%=:\n.Ldd_w1%=:\nv_cmp_gt_i32 vcc, s58, v8\n" \
"s_cbranch_vccz .Ldd_w1go%=\ns_cmp_ge_u32 s40, 62\ns_cbranch_scc1 .Ldd_ns1225%=\ns_sleep 8\n.Ldd_ns1225%=:\ns_sleep 1\nbuffer_load_dword v8, v7, %[rs], 0 offen sc1\ns_waitcnt vmcnt(0)\n" \
"s_add_u32 s43, s43, 1\ns_cmp_lt_u32 s43, 262144\ns_cbranch_scc1 .Ldd_w1%=\ns_branch .Ldd_abort%=\n.Ldd_w1go%=:\ns_add_u32 s60, s55, 0\nbuffer_load_dwordx4 v[48:51], v0, %[rs], s60 offen offset:1024 sc1\nbuffer_load_dwordx4 v[52:55], v0, %[rs], s60 offen offset:3072 sc1\n" \
"s_add_u32 s60, s55, 4096\nbuffer_load_dwordx4 v[56:59], v0, %[rs], s60 offen offset:1024 sc1\nbuffer_load_dwordx4 v[60:63], v0, %[rs], s60 offen offset:3072 sc1\ns_add_u32 s60, s55, 16384\nbuffer_load_dwordx4 v[64:67], v0, %[rs], s60 offen offset:1024 sc1\nbuffer_load_dwordx4 v[68:71], v0, %[rs], s60 offen offset:3072 sc1\ns_add_u32 s60, s55, 20480\nbuffer_load_dwordx4 v[72:75], v0, %[rs], s60 offen offset:1024 sc1\n" \
"buffer_load_dwordx4 v[76:79], v0, %[rs], s60 offen offset:3072 sc1\ns_add_u32 s60, s55, 32768\nbuffer_load_dwordx4 v[80:83], v0, %[rs], s60 offen offset:1024 sc1\nbuffer_load_dwordx4 v[84:87], v0, %[rs], s60 offen offset:3072 sc1\ns_add_u32 s60, s55, 36864\nbuffer_load_dwordx4 v[88:91], v0, %[rs], s60 offen offset:1024 sc1\nbuffer_load_dwordx4 v[92:95], v0, %[rs], s60 offen offset:3072 sc1\ns_add_u32 s60, s55, 49152\n" \
"buffer_load_dwordx4 v[96:99], v0, %[rs], s60 offen offset:1024 sc1\nbuffer_load_dwordx4 v[100:103], v0, %[rs], s60 offen offset:3072 sc1\ns_branch .Ldd_slowr%=\n.Ldd_abort%=:\ns_mov_b64 exec, -1\nv_mov_b32 v16, 0\ns_movk_i32 s44, 6400\nbuffer_store_dword v16, off, %[rs], s44 sc1\n" \
".Ldd_end%=:\ns_waitcnt vmcnt(0) lgkmcnt(0)\n" \
""
#define PX4 1
#define QX4 7
#define G1F 1
#define A1F 0

#define V10(p, x) p #x "0", p #x "1", p #x "2", p #x "3", p #x "4", p #x "5", p #x "6", p #x "7", p #x "8", p #x "9"
#define V256(p) V10(p, ), V10(p, 1), V10(p, 2), V10(p, 3), V10(p, 4), V10(p, 5), V10(p, 6), V10(p, 7), V10(p, 8), V10(p, 9), V10(p, 10), V10(p, 11), V10(p, 12), V10(p, 13), V10(p, 14), V10(p, 15), V10(p, 16), \
    V10(p, 17), V10(p, 18), V10(p, 19), V10(p, 20), V10(p, 21), V10(p, 22), V10(p, 23), V10(p, 24), p "250", p "251", p "252", p "253", p "254", p "255"
#define CLOB "memory", "vcc", "scc", V256("v"), V256("a"), V10("s", 4), V10("s", 5), V10("s", 6), V10("s", 7), V10("s", 8)
template <int N> DEVI void table(lds_char* p, const unsigned (&t)[N]) {
#pragma unroll
    for (int i = 0; i < N; ++i) *(l1*)(p + i * 256) = t[i];
    WL0;
}
DEVI unsigned prof_at(int cu, int w) { return O_PROF + (262144 + cu * 2560 + w * 8) * 4; }
DEVI unsigned sp_idx(int lane) { return (lane & 2) ? 0xEEEEu : 0x4444u; }
DEVI int sp_row(int r, int hf) { return 4 * (r >> 1) + 2 * hf + (r & 1); }

DEVI void qk_role(const Args& a, const int w, lds_char* smem, int l, int h) {
    const int lane = lane_id(), g = lane >> 4, n = lane & 15, b = n & 3, p = n >> 2;
    const unsigned l0 = (unsigned)(size_t)smem, tb = l0 + w * 12288, lw = l0 + 55296 + w * 24576, ls = l * SD, IMG = l0 + 32768, MIM = l0 + 49152;
    const unsigned xw = (w >> 1) * 16384 + (w & 1) * 4096, oc = (h >> 3) * 16384 + ls, tr = (8 * g + p) * 128 + b * 16;
    const unsigned cm[4] = {4 * g > n ? 0xF149F2CAu : 0u, 4 * g + 1 > n ? 0xF149F2CAu : 0u, 4 * g + 2 > n ? 0xF149F2CAu : 0u, 4 * g + 3 > n ? 0xF149F2CAu : 0u};
    const unsigned t[19] = {
        l0 + lane * 16, lw + lane * 16, l0 + (g >> 1) * 1024 + (g & 1) * 512 + n * 16, O_E1 + ls + xw + lane * 16, sp_idx(lane),
        IMG + (w >> 1) * 8192 + ((w & 1) * 8 + 2 * g) * 512 + n * 16,
        MIM + (w * 8 + 2 * p) * 144 + g * 32 + b * 4,
        IMG + g * 512 + (w ? n * 16 : 8192 + (16 * (b >> 1) + 8 * (p >> 1) + 2 * (p & 1) + (b & 1)) * 16),
        MIM + g * 144 + (n & 7) * 16, MIM + 2304 + g * 144 + (2 * p + (b & 1)) * 16,
        w ? O_PT + (h >> 3) * 32768 + ls + lane * 16 + (w == 1 ? 16384 + ((h & 7) >> 1) * 1024 + (h & 1) * 8 : (h & 7) * 1024)
          : (QX4 & 1) && n >> 3 ? OOB : O_PM + oc + ((h >> 1) & 3) * 1024 + ((2 * (h & 1) + (g >> 1)) * 16 + sp_row(n & 7, g & 1)) * 16 + (n >> 3) * 8,
        w ? (QX4 & 4) && g ? OOB : O_PT + (h >> 3) * 32768 + ls + 20480 + ((h >> 2) & 1) * 2048 + (w - 1) * 1024 + (h & 3) * 256 + n * 16 + g * 4
          : n >> 3 || ((QX4 & 2) && g >> 1) ? OOB : O_PM + oc + 4096 + ((h & 7) >> 1) * 256 + sp_row(n & 7, g & 1) * 16 + (h & 1) * 8 + (g >> 1) * 4,
        w == 3 ? (l ? F_X + SD + p * 128 + (X1F ? 0 : b * 4) : F_X0 + b * 4) : cm[0], cm[1], cm[2], cm[3],
        (lane >> 3) * 8192u + (lane & 7) * 16, tb + tr, lw + tr};
    table(smem + w * 12288 + lane * 4, t);
    asm volatile(QK_ASM : : [ws] "s"(a.ws), [sz] "n"(WS_RS), [rw] "s"(mkrs(w < 2 ? a.Wq : a.Wk, 8u << 20)), [wo] "s"(h * 256 + (w & 1) * 128), [tb] "s"(tb), [lw] "s"(lw), [l0] "s"(l0), [w] "s"(w),
        [xn] "s"(O_XN + ls), [xw] "s"(xw), [fs] "s"(l ? 0 : 128), [sg] "s"(F_Y0 + 4096 + ls + h * 128 + 16), [pr] "s"(prof_at(l * 80 + h, w)) : CLOB, "s36", "s37", "s38", "s39");
    __builtin_amdgcn_endpgm();
}
DEVI void vu_role(const Args& a, const int w, lds_char* smem, int l, int h) {
    const int lane = lane_id(), g = lane >> 4, n = lane & 15, b = n & 3, p = n >> 2;
    const unsigned l0 = (unsigned)(size_t)smem, tb = l0 + w * 12288, lw = l0 + 61440 + w * 24576, ls = l * SD;
    const unsigned xw = (w >> 1) * 16384 + (w & 1) * 4096, tv = (8 * g + p) * 128 + b * 16 + (w & 1) * 64, to = (4 * g + (p & 1)) * 128 + (p >> 1) * 32 + b * 8;
    const unsigned t[18] = {
        l0 + lane * 16, lw + lane * 16, l0 + (g >> 1) * 1024 + (g & 1) * 512 + n * 16, O_E1 + ls + xw + lane * 16, sp_idx(lane),
        l0 + 57344 + (2 * w + (n >> 3)) * 256 + (n >> 3) * 128 + g * 32 + (n & 7) * 2,
        O_UM + ls + (4 * w + (g & 1)) * 65536 + (h >> 3) * 16384 + (g >> 1) * 2048 + (h & 7) * 256 + n * 16,
        sd(O_Y + h * 131072 + ls) + w * 32768 + (g >> 1) * 1024 + (g & 1) * 256 + n * 16,
        l ? F_X + SD + p * 128 + (X1F ? 0 : b * 4) : F_X0 + b * 4, l0 + 49152 + (w >> 1) * 2048 + (2 * (w & 1) + (g >> 1)) * 256 + (n ^ (g >> 1) * 8) * 16 + (g & 1) * 8, sp_idx(lane >> 2), l0 + g * 256 + (g & 1) * 128 + (n & 7) * 16,
        (lane >> 3) * 8192u + (lane & 7) * 16, (8 * (lane >> 5) + ((lane >> 3) & 3)) * 2048u + (lane & 7) * 16, tb + tv - (w & 1) * 12288, lw + tv - (w & 1) * 24576, tb + to, lw + to};
    table(smem + w * 12288 + lane * 4, t);
    asm volatile(VU_ASM : : [ws] "s"(a.ws), [sz] "n"(WS_RS), [rw] "s"(mkrs(a.Wv, 8u << 20)), [ro] "s"(mkrs(a.Wo, 8u << 20)), [wo] "s"(h * 256 + (w >> 1) * 128 + (w & 1) * 262144), [w2] "s"(h * 262144 + w * 512), [tb] "s"(tb), [lw] "s"(lw), [w] "s"(w),
        [xn] "s"(O_XN + ls), [xw] "s"(xw), [fs] "s"(l ? 0 : 128), [xt] "s"(w * 16384 - xw), [fy] "s"(F_Y0 + ls + h * 128 + w * 4), [sg] "s"(w == 1 ? F_Y0 + ls + h * 128 + 16 : OOB), [pr] "s"(prof_at(l * 80 + 32 + h, w) - 128) : CLOB, "s36", "s37", "s38", "s39");
    __builtin_amdgcn_endpgm();
}
DEVI void rd_role(const Args& a, const int w, lds_char* smem, int l, int c) {
    const int lane = lane_id(), g = lane >> 4, n = lane & 15, r = 2 * (n >> 2) + (n & 1), hf = (n >> 1) & 1;
    const rsrc_t rs = mkrs(a.ws, WS_RS);
    const unsigned l0 = (unsigned)(size_t)smem, tb = l0 + 65536 + w * 4096, ls = l * SD, vp = O_E1 + ls + (c >> 3) * 16384 + (c & 7) * 1024 + w * 256 + n * 16;
    {
        const v4u ff = {SENT, SENT, SENT, SENT};
#pragma unroll
        for (int i = 0; i < 8; ++i) st16(ff, rs, O_UM + ls + c * 65536 + w * 16384 + i * 1024 + lane * 16);
#pragma unroll
        for (int i = 0; i < 2; ++i) st16(ff, rs, O_PT + ls + c * 16384 + w * 2048 + i * 1024 + lane * 16);
        st16(ff, rs, c < 8 ? O_PM + ls + w * 16384 + c * 1024 + lane * 16 : OOB);
        st16(ff, rs, g ? vp + g * 32768 : OOB);
        WV0; BAR;
    }
    st4(g & 1 ? *(const unsigned*)(a.mem0 + r * 1024 + c * 64 + w * 16 + 4 * g + 2 * hf) : nosent(*(const unsigned*)(a.mem0 + r * 1024 + c * 64 + w * 16 + 4 * g + 2 * hf)), rs, g & 1 ? vp + g * 4 : OOB, 0);
    WV0;
    st4(nosent(*(const unsigned*)(a.mem0 + r * 1024 + c * 64 + w * 16 + 4 * g + 2 * hf)), rs, g & 1 ? OOB : vp + g * 4, 0);
    const int gi = lane >> 3, r8 = lane & 7;
    const unsigned t[12] = {
        (unsigned)lane * 16, sp_idx(lane), l0 + w * 4096 + g * 576 + ((n >> 3) * 16 + hf * 4 + 2 * ((n >> 2) & 1) + (n & 1)) * 4,
        l0 + (r >> 1) * 576 + w * 128 + (g >> 1) * 64 + (r & 1) * 32 + hf * 16,
        PX4 && g >> 1 ? OOB : (vp ^ ((g & 1) * 65536)) + (g >> 1) * 8, g & 1 ? SENT : 0u,
        l0 + 16384 + w * 8192 + lane * 16, l0 + 16384 + ((gi >> 1) * 2 + (w >> 1)) * 1024 + ((gi & 1) * 32 + (w & 1) * 8 + r8) * 16,
        (c >> 1) * 16384u + (c & 1) * 4096 + gi * 512 + (w * 8 + r8) * 16,
        gi ? OOB : O_ST - O_XR + c * 256 + ((w & 1) * 8 + r8) * 16 + (w >> 1) * 8,
        F_Y0 + ls + (w * 8 + r8) * 128 + (c >> 2) * 4, F_Y0 + ls + (gi & 1) * 4096 + (w * 8 + r8) * 128 + 16};
    table(smem + 65536 + w * 4096 + lane * 4, t);
    asm volatile(RD_ASM : : [rs] "s"(rs), [tb] "s"(tb), [su] "s"(O_UM + ls + c * 65536 + w * 16384), [sp] "s"(O_PM + ls + w * 16384), [st] "s"(O_PT + ls + w * 32768),
        [sy] "s"(sd(O_Y + w * (1u << 20) + ls) + (c >> 1) * 16384 + (c & 1) * 4096), [at] "s"(O_XR + ls), [du] "s"((c & 2) * 5 / 2), [fa] "s"(A1F && w ? OOB : F_A + ls + c * 128 + w * 4), [pr] "s"(prof_at(l * 80 + 64 + c, w) + 128) : CLOB);
    __builtin_amdgcn_endpgm();
}
DEVI void gg_role(const Args& a, const int w, lds_char* smem, int l, int gi) {
    const int lane = lane_id(), g = lane >> 4, n = lane & 15, b = n & 3, p = n >> 2;
    const unsigned l0 = (unsigned)(size_t)smem, tb = l0 + w * 12288, lw = l0 + 65536 + w * 24576, ls = l * SD, tv = (8 * g + p) * 128 + b * 16 + (w & 1) * 64, fh = (g >> 1) * 1024 + (g & 1) * 512 + n * 16;
    const unsigned t[9] = {l0 + lane * 16, lw + lane * 16, l0 + fh, w ? OOB : F_A + ls + (lane >> 2) * 128 + (A1F ? 0 : (lane & 3) * 4), w * 2048u + lane * 16, O_ST - O_XR + g * 1024 + n * 16, (lane >> 3) * 7168u + (lane & 7) * 16, tb + tv - (w & 1) * 12288, lw + tv - (w & 1) * 24576};
    table(smem + w * 12288 + lane * 4, t);
    asm volatile(GG_ASM : : [rs] "s"(mkrs(a.ws, WS_RS)), [rw] "s"(mkrs(w & 1 ? a.Wu : a.Wg, 3584 * 2048)), [wo] "s"(gi * 256 + (w >> 1) * 128), [tb] "s"(tb), [lw] "s"(lw), [w] "s"(w),
        [xn] "s"(O_XR + ls), [xw] "s"((w >> 1) * 16384 + (w & 1) * 4096), [ho] "s"(O_H + ls + gi * 16384), [fh] "s"(G1F && w ? OOB : F_H + ls + (gi / 7) * 1024 + (gi % 7) * 128 + w * 4), [pr] "s"(prof_at(160 + l * 28 + gi, w) + 128) : CLOB);
    __builtin_amdgcn_endpgm();
}
DEVI void dd_role(const Args& a, const int w, lds_char* smem, int l, int c) {
    const int lane = lane_id(), g = lane >> 4, b = lane & 3, p = (lane >> 2) & 3, gi = lane >> 3, r8 = lane & 7;
    const unsigned l0 = (unsigned)(size_t)smem, tb = l0 + w * 12288, lw = l0 + 65536 + w * 24576, ls = l * SD, tr = (8 * g + p) * 128 + b * 8;
    const unsigned ta = (c >> 1) * 16384u + (c & 1) * 4096 + gi * 512 + (w * 8 + r8) * 16;
    const rsrc_t rs = mkrs(a.ws, WS_RS);
    const unsigned t[11] = {(unsigned)lane * 16, lw + lane * 16, l0 + w * 8192 + lane * 16, l0 + ((gi >> 1) * 2 + (w >> 1)) * 1024 + ((gi & 1) * 32 + (w & 1) * 8 + r8) * 16,
        ta, l ? (w * 8 + r8) * 2048 + c * 128 + gi * 16 : ta, gi || l ? OOB : O_ST - O_XR + 4096 + c * 256 + (w * 8 + r8) * 8,
        F_H + ls + w * 1024 + ((lane >> 2) % 7) * 128 + (G1F ? 0 : b * 4), gi * 2048u + r8 * 16, tb + tr, lw + tr};
    table(smem + w * 12288 + lane * 4, t);
    asm volatile(DD_ASM : : [rs] "s"(rs), [rw] "s"(mkrs(a.Wd, 3584 * 2048)), [ro] "s"(mkrs(l ? (char*)a.out : a.ws, l ? 2048 * 2048 : WS_RS)), [wo] "s"(w * (896 * 2048) + c * 128), [tb] "s"(tb), [lw] "s"(lw), [l] "s"(l),
        [xn] "s"(O_XR + ls), [ho] "s"(O_H + ls + w * 7 * 16384), [fd] "s"(D1F && w ? OOB : F_D + ls + c * 128 + w * 4), [lt] "s"(F_D + ls + c * 128 + w * 4 + 64), [pr] "s"(prof_at(216 + l * 16 + c, w) + 128) : CLOB);
    __builtin_amdgcn_endpgm();
}

#define CPF 74
#define CPJ 0
#define LNW 0
#define DLQ 150
#define DLS 400
#define SPL 0
#define PLAN 3
#define PFA 0
#define PFB 3
#define PFL 1
#define VUX 1
#define LNS 40
#define LN1 700
#define DLG 1100
#define DLD 1500
#define DL1 1500
#define DLG1 2600
#define DLD1 3000
DEVI Args args(unsigned sh) {
    auto p = (const __attribute__((address_space(4))) Args*)__builtin_amdgcn_kernarg_segment_ptr();
    asm volatile("" : "+s"(p));
    Args a;
    a.x = p->x; a.mem0 = p->mem0; a.Wq = p->Wq; a.Wk = p->Wk; a.Wv = p->Wv; a.Wo = p->Wo; a.Wg = p->Wg; a.Wu = p->Wu; a.Wd = p->Wd; a.ws = p->ws + sh; a.out = p->out;
    return a;
}
DEVI unsigned beat_wait(unsigned x0) {
    const rsrc_t r0 = mkrs(args(0).ws, 2u << 20);
    for (unsigned spins = 0; spins < SPIN_CAP; ++spins) {
        CB;
        const v2u t = __builtin_amdgcn_raw_buffer_load_b64(r0, 0, O_HB, AUX_SC1);
        if (RFL(t[0]) != x0) return RFL(t[1]) & (2u << 20);
        __builtin_amdgcn_s_sleep(8);
    }
    __builtin_amdgcn_endpgm();
}
#define MPF 0
DEVI void mpf(rsrc_t rs, unsigned o) {
    o += O_E1 + lane_id() * 128;
    unsigned spins = 0;
    for (int s = 0; s < NSEG && spins < SPIN_CAP; ++spins) {
        CB;
        if (__ballot(ld4(rs, o, (s & 3) * 32768) == SENT) == 0ull) { ++s; for (int i = 0; i < MPF; ++i) __builtin_amdgcn_s_sleep(8); }
    }
}
DEVI void leader(const Args& a, const int w, lds_char* smem, unsigned x0) {
    const int lane = lane_id();
    const rsrc_t r0 = mkrs(a.ws, 4u << 20);
    unsigned t[2] = {~0u, ~0u};
    for (int i = 0; i < 2 * NPRB; ++i) {
        const unsigned t0 = (unsigned)__builtin_amdgcn_s_memtime();
        WL0;
        if (lane == 0) st4(0u, r0, 0, O_PRB + (i & 1) * SD + w * 1024 + (i >> 1) * 128);
        WV0;
        const unsigned d = (unsigned)__builtin_amdgcn_s_memtime() - t0;
        if (i & 1) t[1] = min(t[1], d); else t[0] = min(t[0], d);
    }
    *(l2*)(smem + w * 8) = v2u{t[0], t[1]};
    WL0; BAR;
    const v4u p = *(l4*)smem, q = *(l4*)(smem + 16);
    const unsigned sh = RFL(min(min(p[0], p[2]), min(q[0], q[2])) < min(min(p[1], p[3]), min(q[1], q[3])) ? 2u << 20 : 0u);
    const unsigned o = sh + w * 1024 + lane * 16;
    const v4u ff = {SENT, SENT, SENT, SENT};
#pragma unroll
    for (int i = 0; i < 8; ++i) st16(ff, r0, o + O_E1 + i * 4096);
#pragma unroll
    for (int i = 1; i < 16; ++i) if (i != 2 && i != 11) st16(ff, r0, o + i * 4096);
    WV0; BAR;
    if (w) { if (MPF && w > 1) mpf(r0, sh + SD + (w & 1) * 16384); return; }
    for (unsigned spins = 0; spins < SPIN_CAP; ++spins) {
        if (lane == 0) st8(v2u{++x0, sh}, r0, 0, O_HB);
        if (__ballot((int)ld4(r0, sh + F_D + SD + (lane >> 2) * 128 + (D1F ? 0 : (lane & 3) * 4), 0) < 0) == 0ull) break;
        __builtin_amdgcn_s_sleep(16);
    }
}
__global__ __launch_bounds__(256, 1) void sys_kernel(Args) {
    extern __shared__ __attribute__((aligned(16))) char smem_[];
    lds_char* smem = (lds_char*)smem_;
    const int x = blockIdx.x & 7, j = blockIdx.x >> 3, wv = RFL(threadIdx.x >> 6), l = x >> 2, xx = x & 3;
    const unsigned t0 = (unsigned)__builtin_amdgcn_s_memrealtime();
    auto until = [&](unsigned dl) AI { while ((unsigned)__builtin_amdgcn_s_memrealtime() - t0 < dl) __builtin_amdgcn_s_sleep(8); };
    unsigned x0 = 0;
    if (l || j >= 16) x0 = RFL(ld4(mkrs(args(0).ws, 2u << 20), 0, O_HB));
    if (j < 16) {
        if (l) { until(j ? LN1 + j * LNS : 0); ln0(args(0), wv, j * 4 + xx, [&]() AI { return beat_wait(x0); }); until(DL1); }
        if (DLQ && !l && j < 8) until(DLQ);
        if (j < 8) qk_role(args(0), wv, smem, l, xx * 8 + j); else vu_role(args(0), wv, smem, l, (xx ^ VUX) * 8 + j - 8);
        return;
    }
    if (j >= 16 + CPJ && j < 20 + CPJ) {
        unsigned long long pc; unsigned n;
        asm volatile("s_getpc_b64 %0\n\ts_mov_b32 %1, .-_Z10sys_kernel4Args" : "=s"(pc), "=s"(n));
        const rsrc_t rc = mkrs((const void*)(pc - n), CPF * 1024);
        __builtin_amdgcn_raw_ptr_buffer_load_lds(rc, smem + LDSB - 1024 + wv * 256, 4, lane_id() * 128, ((j - 16 - CPJ) * 4 + wv) * 8192, 0, 0);
    }
    {
        const int lane = lane_id(), q = (j - 16) * 4 + wv, m = SPL && !l ? ((xx & 1) != (SPL & 1)) * 2 + (q >> 5) : q >> 4;
        const Args a = args(0);
        const u16 *W0 = a.Wq, *W1 = a.Wk, *W2 = a.Wv, *W3 = a.Wo;
        asm volatile("" : "+s"(W0), "+s"(W1), "+s"(W2), "+s"(W3));
        const u16* W = m == 0 ? W0 : m == 1 ? W1 : m == 2 ? W2 : W3;
        const rsrc_t rw = mkrs(W, 8u << 20);
        if (l) {
#pragma unroll
            for (int i = 0; i < PFA; ++i) __builtin_amdgcn_raw_ptr_buffer_load_lds(rw, smem + LDSB - 1024 + wv * 256, 4, lane * 256, i * (1u << 20) + (xx * 16 + (q & 15)) * 16384, 0, 0);
        } else {
            const unsigned po = (0x100u - (unsigned)(size_t)W) & 0x300u, hx = SPL ? (xx & 2) + ((q >> 4) & 1) : m < 2 ? xx : xx ^ VUX;
            const unsigned vo = m == 3 ? (lane >> 1) * 1024 + (lane & 1) * 128 : (lane >> 2) * 8192 + ((lane >> 1) & 1) * 1024 + (lane & 1) * 128;
            const unsigned so = po + (m == 3 ? hx * (2u << 20) + (q & 15) * 32768 : hx * 2048 + (q & 15) * 131072);
#pragma unroll
            for (int i = 0; i < PFB; ++i) __builtin_amdgcn_raw_ptr_buffer_load_lds(rw, smem + LDSB - 1024 + wv * 256, 4, vo, so + i * (m == 3 ? 524288 : 2u << 20), 0, 0);
            const unsigned pl = (0x1F00u - (unsigned)(size_t)W) & 0x1F00u;
#pragma unroll
            for (int i = 0; i < PFL; ++i) __builtin_amdgcn_raw_ptr_buffer_load_lds(rw, smem + LDSB - 1024 + wv * 256, 4, (lane >> 1) * 8192 + (lane & 1) * 128, (pl >> 11) == hx && m < 3 ? pl + ((q & 15) + 16 * i) * 262144 : OOB, 0, 0);
        }
    }
    int ro, ix;
    if ((xx & 1) == (PLAN & 1)) { if (j < 24) { ro = 0; ix = (xx >> 1) * 8 + j - 16; } else if (j < 26) { ro = 3; ix = (xx >> 1) * 2 + j - 24; } else { ro = 1; ix = (xx >> 1) * 14 + j - 26; } }
    else if (j < 24) { ro = 1; ix = (xx >> 1) * 14 + j - 10; } else { ro = 2; ix = (xx >> 1) * 8 + j - 24; }
    if (ro == 3 && l) { if (ix == 0) leader(args(0), wv, smem, x0); else if (MPF && wv > 1) mpf(mkrs(args(0).ws, 4u << 20), beat_wait(x0) + SD + (wv & 1) * 16384); return; }
    const unsigned sh = beat_wait(x0);
    if (ro == 0) { rd_role(args(sh), wv, smem, l, ix); return; }
    if (ro == 1) { const Args a = args(sh); until(l ? DLG1 : ((((unsigned)(size_t)a.Wg >> 8) + ix) & 3) == 1 || ((((unsigned)(size_t)a.Wu >> 8) + ix) & 3) == 1 ? DLG - DLS : DLG); WV0; BAR; gg_role(a, wv, smem, l, ix); return; }
    if (ro == 2) { const Args a = args(sh); until((l ? DLD1 : DLD) - (((((unsigned)(size_t)a.Wd >> 8) + (ix >> 1)) & 3) == 1 ? DLS + 200 : 0)); WV0; BAR; dd_role(a, wv, smem, l, ix); return; }
    r_role(args(sh), wv, ix);
}

extern "C" void kernel_launch(void* const* d_in, const int* in_sizes, int n_in, void* d_out, int out_size, void* d_ws, size_t ws_size, hipStream_t stream) {
    (void)in_sizes; (void)n_in; (void)out_size; (void)ws_size;
    Args a;
    a.x = (const u16*)d_in[0]; a.mem0 = (const u16*)d_in[1]; a.Wq = (const u16*)d_in[2]; a.Wk = (const u16*)d_in[3]; a.Wv = (const u16*)d_in[4]; a.Wo = (const u16*)d_in[5];
    a.Wg = (const u16*)d_in[8]; a.Wu = (const u16*)d_in[9]; a.Wd = (const u16*)d_in[10];
    a.ws = (char*)(((uintptr_t)d_ws + (2u << 20) - 1) & ~(uintptr_t)((2u << 20) - 1)); a.out = (u16*)d_out;
    (void)hipFuncSetAttribute((const void*)sys_kernel, hipFuncAttributeMaxDynamicSharedMemorySize, LDSB);
    hipLaunchKernelGGL(sys_kernel, dim3(256), dim3(256), LDSB, stream, a);
}
```

```cpp
#include <hip/hip_runtime.h>
#include <stdint.h>

typedef unsigned short u16;
typedef unsigned long long u64;
typedef __attribute__((ext_vector_type(8))) short bf16x8;
typedef __attribute__((ext_vector_type(4))) short s16x4;
typedef __attribute__((ext_vector_type(4))) float f32x4;
typedef __attribute__((ext_vector_type(16))) float f32x16;
typedef __attribute__((ext_vector_type(2))) float f32x2;
typedef __attribute__((ext_vector_type(4))) unsigned v4u;
typedef __attribute__((ext_vector_type(2))) unsigned v2u;
typedef __amdgpu_buffer_rsrc_t rsrc_t;
typedef __attribute__((address_space(3))) char lds_char;
typedef __attribute__((address_space(3))) bf16x8 l8;
typedef __attribute__((address_space(3))) v4u l4;
typedef __attribute__((address_space(3))) v2u l2;
typedef __attribute__((address_space(3))) unsigned l1;
typedef __attribute__((address_space(3))) f32x4 lf4;
typedef __attribute__((address_space(3))) float lf1;
#define DEVI __device__ __forceinline__
#define AI __attribute__((always_inline))

constexpr int NSEG = 64;
constexpr float ATT_SCALE = 0.08838834764831845f;
constexpr float LOG2E = 1.4426950408889634f;
#define NPRB 4
constexpr unsigned SD = 8192;
DEVI unsigned sd(unsigned o) { return o ^ ((o >> 8) & SD); }
constexpr unsigned F_X0 = 32768;
constexpr unsigned F_X = 4096;
constexpr unsigned F_A = 16384;
constexpr unsigned F_H = F_A + 2048;
constexpr unsigned F_D = F_H + 4096;
constexpr unsigned F_Y0 = 49152;
constexpr unsigned F_ABORT = 6400;
constexpr unsigned O_PM = 65536;
constexpr unsigned O_E1 = 131072;
constexpr unsigned O_PT = 262144;
constexpr unsigned O_UM = 524288;
constexpr unsigned CTRL_END = O_UM + (1u << 20);
constexpr unsigned O_PRB = 0x1F0000, O_HB = 0x1FFF00;
constexpr unsigned O_XN = 4u << 20;
constexpr unsigned O_XR = O_XN + (8u << 20);
constexpr unsigned O_ST = O_XR + (8u << 20);
constexpr unsigned O_H = O_ST + (8u << 20);
constexpr unsigned O_Y = O_H + (36u << 20);
constexpr unsigned WS_NEED = O_Y + (8u << 20);
constexpr unsigned O_PROF = WS_NEED;
constexpr unsigned WS_RS = WS_NEED;
static_assert(CTRL_END <= O_PRB && CTRL_END % 16384 == 0 && O_Y % (8u << 20) == 0, "workspace");
constexpr unsigned OOB = 0x80000000u, OOB2 = 0x90000000u, SENT = 0xFFFFFFFFu;

struct Args { const u16 *x, *mem0, *Wq, *Wk, *Wv, *Wo, *Wg, *Wu, *Wd; char* ws; u16* out; };

typedef __attribute__((ext_vector_type(2))) __bf16 bf16x2_t;
DEVI unsigned pack2(float lo, float hi) { f32x2 v = {lo, hi}; bf16x2_t b = __builtin_convertvector(v, bf16x2_t); return __builtin_bit_cast(unsigned, b); }
DEVI float bflo(unsigned u) { return __uint_as_float(u << 16); }
DEVI float bfhi(unsigned u) { return __uint_as_float(u & 0xffff0000u); }
DEVI float rbf(float x) { return bflo(pack2(x, x)); }
DEVI bf16x8 as8(v4u v) { return __builtin_bit_cast(bf16x8, v); }
DEVI f32x4 mfma16(bf16x8 a, bf16x8 b, f32x4 c) { return __builtin_amdgcn_mfma_f32_16x16x32_bf16(a, b, c, 0, 0, 0); }
DEVI f32x16 mfma32(bf16x8 a, bf16x8 b, f32x16 c) { return __builtin_amdgcn_mfma_f32_32x32x16_bf16(a, b, c, 0, 0, 0); }
constexpr int AUX_SC1 = 16;
DEVI v4u ld16(rsrc_t r, unsigned vo, unsigned so) { return __builtin_amdgcn_raw_buffer_load_b128(r, vo, so, AUX_SC1); }
DEVI unsigned ld4(rsrc_t r, unsigned vo, unsigned so) { return __builtin_amdgcn_raw_buffer_load_b32(r, vo, so, AUX_SC1); }
DEVI void st16(v4u v, rsrc_t r, unsigned vo) { __builtin_amdgcn_raw_buffer_store_b128(v, r, vo, 0, AUX_SC1); }
DEVI void st8(v2u v, rsrc_t r, unsigned vo, unsigned so) { __builtin_amdgcn_raw_buffer_store_b64(v, r, vo, so, AUX_SC1); }
DEVI void st4(unsigned v, rsrc_t r, unsigned vo, unsigned so) { __builtin_amdgcn_raw_buffer_store_b32(v, r, vo, so, AUX_SC1); }
DEVI v2u trd(lds_char* p) { return __builtin_bit_cast(v2u, __builtin_amdgcn_ds_read_tr16_b64_v4i16((__attribute__((address_space(3))) s16x4*)p)); }
template <int O1, int O2> DEVI void tra(v2u& lo, v2u& hi, unsigned a) { asm volatile("ds_read_b64_tr_b16 %0, %2 offset:%3\n\tds_read_b64_tr_b16 %1, %2 offset:%4" : "=&v"(lo), "=&v"(hi) : "v"(a), "n"(O1), "n"(O2) : "memory"); }
DEVI void trw(v2u& a, v2u& b, v2u& c, v2u& d) { asm volatile("s_waitcnt lgkmcnt(0)" : "+v"(a), "+v"(b), "+v"(c), "+v"(d)::"memory"); }
DEVI rsrc_t mkrs(const void* p, unsigned n) { return __builtin_amdgcn_make_buffer_rsrc((void*)p, 0, (int)n, 0x00020000); }
#define CB asm volatile("" ::: "memory")
#define SB __builtin_amdgcn_sched_barrier(0)
#define WL0 asm volatile("s_waitcnt lgkmcnt(0)" ::: "memory")
#define WV0 asm volatile("s_waitcnt vmcnt(0)" ::: "memory")
#define WV(n) asm volatile("s_waitcnt vmcnt(%0)" ::"n"(n) : "memory")
#define BAR do { __builtin_amdgcn_s_barrier(); CB; } while (0)
#define RFL(x) __builtin_amdgcn_readfirstlane(x)
DEVI int lane_id() { int x; asm volatile("v_mbcnt_lo_u32_b32 %0, -1, 0\n\tv_mbcnt_hi_u32_b32 %0, -1, %0" : "=v"(x)); return x; }
template <int I> struct IC { static constexpr int v = I; };
template <int B, int E_, class F> DEVI void sfor(F&& f) { if constexpr (B < E_) { f(IC<B>{}); sfor<B + 1, E_>(f); } }
DEVI float red_x32(float x, bool mx) { unsigned u = __float_as_uint(x); auto r = __builtin_amdgcn_permlane32_swap(u, u, false, false); float a = __uint_as_float(r[0]), b = __uint_as_float(r[1]); return mx ? fmaxf(a, b) : a + b; }
DEVI float red_x16(float x, bool mx) { unsigned u = __float_as_uint(x); auto r = __builtin_amdgcn_permlane16_swap(u, u, false, false); float a = __uint_as_float(r[0]), b = __uint_as_float(r[1]); return mx ? fmaxf(a, b) : a + b; }
DEVI unsigned vmax4(v4u d) { return max(max(d[0], d[1]), max(d[2], d[3])); }
DEVI unsigned nosent(unsigned v) { return min(v, SENT - 1u); }
constexpr unsigned SPIN_CAP = 1u << 18;
DEVI bool spin_bail(unsigned& spins, rsrc_t rs) {
    if (++spins > SPIN_CAP) { st4(0u, rs, 0, F_ABORT); return true; }
    return (spins & 255u) == 0u && RFL(ld4(rs, 0, F_ABORT)) != SENT;
}
#define TSTAMP(idx)
constexpr int LDSB = 160 * 1024;
DEVI void idle(unsigned dl) { const unsigned t0 = (unsigned)__builtin_amdgcn_s_memrealtime(); while ((unsigned)__builtin_amdgcn_s_memrealtime() - t0 < dl) __builtin_amdgcn_s_sleep(8); }
#define RXE 0
#define D1F 0
#define X1F 0

DEVI float red_rows(float s) {
    s += __uint_as_float(__builtin_amdgcn_update_dpp(0, __float_as_uint(s), 0x128, 0xf, 0xf, false));
    s = red_x16(s, false); return red_x32(s, false);
}
DEVI f32x2 up2(unsigned u) { return f32x2{bflo(u), bfhi(u)}; }
DEVI float ln_inv(float var) { return __builtin_amdgcn_rcpf(rbf(__builtin_amdgcn_sqrtf(rbf(rbf(fmaxf(var, 0.f)) + 1e-5f)))); }
template <class F> DEVI void ln0(const Args& a, const int w, int b, F&& base) {
    const int lane = lane_id(), row = w * 8 + (lane & 7);
    const rsrc_t rx = mkrs(a.x, 2048 * 2048);
    v4u u[16];
#pragma unroll
    for (int i = 0; i < 16; ++i) u[i] = __builtin_amdgcn_raw_buffer_load_b128(rx, row * 2048 + (lane >> 3) * 16 + i * 128, b * 65536, 0);
    const float k0 = __shfl(bflo(u[0][0]), lane & 7);
    f32x2 s1 = {0.f, 0.f}, s2 = {0.f, 0.f};
#pragma unroll
    for (int i = 0; i < 16; ++i)
#pragma unroll
        for (int e = 0; e < 4; ++e) { const f32x2 d = up2(u[i][e]) - k0; s1 += d; s2 += d * d; }
    const float s = red_rows(s1[0] + s1[1]) * (1.0f / 1024.0f), q = red_rows(s2[0] + s2[1]) * (1.0f / 1024.0f);
    float inv = ln_inv(q - s * s), c = -rbf(k0 + s) * inv;
    asm volatile("" : "+v"(inv), "+v"(c));
    const rsrc_t rs = mkrs(a.ws + base(), WS_RS);
    const unsigned o = sd(O_XN + b * 131072) + (lane >> 3) * 512 + row * 16;
#pragma unroll
    for (int i = 0; i < 16; ++i) {
        v4u r;
#pragma unroll
        for (int e = 0; e < 4; ++e) { const f32x2 z = up2(u[i][e]) * inv + c; r[e] = pack2(z[0], z[1]); }
        st16(r, rs, o + (i >> 1) * 16384 + (i & 1) * 4096);
    }
#pragma unroll
    for (int i = 0; i < 16; ++i) st16(u[i], rs, o + (O_XR - O_XN) + (i >> 1) * 16384 + (i & 1) * 4096);
    st16(u[0], rs, sd(O_ST + b * 131072) + w * 1024 + lane * 16); st16(u[0], rs, sd(O_ST + b * 131072) + 4096 + w * 1024 + lane * 16); st16(u[0], rs, sd(O_ST + SD + b * 131072) + w * 1024 + lane * 16);
    WV0;
    st4(b, rs, 0, F_X0 + b * 128 + w * 4);
}
#define RSLP 8
DEVI void r_role(const Args& a, const int w, int rq) {
    const int lane = lane_id(), row = (w >> 1) * 16 + (rq & 1) * 8 + (lane & 7), cq = (rq >> 1) * 2 + (w & 1);
    const rsrc_t rs = mkrs(a.ws, WS_RS);
    const unsigned fo = cq * 32768 + (lane >> 3) * 512 + row * 16, so = O_ST - O_XR + 4096 + (lane >> 3) * 512 + row * 8;
    auto io = [](int i) -> unsigned { return (i >> 1) * 16384 + (i & 1) * 4096; };
    const unsigned fd = F_D + (lane & 15) * 128 + (D1F ? 0 : (row >> 3) * 4);
    for (int b = 0; b < NSEG; ++b) {
        unsigned spins = 0;
        for (;;) {
            CB;
            if (__ballot((int)ld4(rs, fd, 0) < b) == 0ull) break;
            if (spin_bail(spins, rs)) return;
            __builtin_amdgcn_s_sleep(RSLP);
        }
        const unsigned sx = sd(O_XR + b * 131072);
        const v2u s0 = __builtin_amdgcn_raw_buffer_load_b64(rs, so, sx, AUX_SC1), s1 = __builtin_amdgcn_raw_buffer_load_b64(rs, so + 256, sx, AUX_SC1);
        v4u u[4];
#pragma unroll
        for (int i = 0; i < 4; ++i) u[i] = ld16(rs, fo + io(i), sx);
        const float m0 = __uint_as_float(s0[0]), m1 = __uint_as_float(s1[0]), mean = red_rows(m0 + m1) * 0.0625f;
        const float var = red_rows(__uint_as_float(s0[1]) + __uint_as_float(s1[1]) + 64.f * ((m0 - mean) * (m0 - mean) + (m1 - mean) * (m1 - mean))) * (1.0f / 1024.0f);
        const float inv = ln_inv(var), c = -rbf(mean) * inv;
#pragma unroll
        for (int i = 0; i < 4; ++i) {
            v4u o;
#pragma unroll
            for (int e = 0; e < 4; ++e) { const f32x2 z = up2(u[i][e]) * inv + c; o[e] = pack2(z[0], z[1]); }
            st16(o, rs, fo + io(i) + ((sx ^ SD) - (O_XR - O_XN)));
        }
        CB;
#pragma unroll
        for (int i = 0; i < 4; ++i) st16(u[i], rs, fo + io(i) + (sx ^ SD));
        WV0;
        if (X1F) BAR;
        st4(b, rs, 0, X1F && w ? OOB : F_X + SD + rq * 128 + w * 4);
        if (rq == 0 && w == 0) TSTAMP(200000 + 64 + b);
    }
}
#define QK_ASM \
"v_mbcnt_lo_u32_b32 v255, -1, 0\nv_mbcnt_hi_u32_b32 v255, -1, v255\nv_lshlrev_b32 v255, 2, v255\nv_add_u32 v255, %[tb], v255\nds_read_b32 v0, v255 offset:0\nds_read_b32 v1, v255 offset:256\nds_read_b32 v2, v255 offset:512\nds_read_b32 v3, v255 offset:768\n" \
"ds_read_b32 v4, v255 offset:1024\nds_read_b32 v5, v255 offset:1280\nds_read_b32 v6, v255 offset:1536\nds_read_b32 v7, v255 offset:1792\nds_read_b32 v8, v255 offset:2048\nds_read_b32 v9, v255 offset:2304\nds_read_b32 v10, v255 offset:2560\nds_read_b32 v11, v255 offset:2816\n" \
"ds_read_b32 v12, v255 offset:3072\nds_read_b32 v13, v255 offset:3328\nds_read_b32 v14, v255 offset:3584\nds_read_b32 v15, v255 offset:3840\nds_read_b32 v92, v255 offset:4096\nds_read_b32 v93, v255 offset:4352\nds_read_b32 v94, v255 offset:4608\ns_waitcnt lgkmcnt(0)\n" \
"s_mov_b64 s[36:37], %[ws]\ns_mov_b32 s38, %[sz]\ns_mov_b32 s39, 0x20000\ns_mov_b32 s45, 2096896\nbuffer_load_dword v95, off, s[36:39], s45 sc1\ns_add_u32 m0, %[lw], 16384\ns_add_u32 s44, %[wo], 0\nbuffer_load_dwordx4 v92, %[rw], s44 offen nt lds\n" \
"s_add_u32 m0, %[lw], 17408\ns_add_u32 s44, %[wo], 65536\nbuffer_load_dwordx4 v92, %[rw], s44 offen nt lds\ns_add_u32 m0, %[lw], 18432\ns_add_u32 s44, %[wo], 131072\nbuffer_load_dwordx4 v92, %[rw], s44 offen nt lds\ns_add_u32 m0, %[lw], 19456\ns_add_u32 s44, %[wo], 196608\n" \
"buffer_load_dwordx4 v92, %[rw], s44 offen nt lds\ns_add_u32 m0, %[lw], 20480\ns_add_u32 s44, %[wo], 262144\nbuffer_load_dwordx4 v92, %[rw], s44 offen nt lds\ns_add_u32 m0, %[lw], 21504\ns_add_u32 s44, %[wo], 327680\nbuffer_load_dwordx4 v92, %[rw], s44 offen nt lds\ns_add_u32 m0, %[lw], 22528\n" \
"s_add_u32 s44, %[wo], 393216\nbuffer_load_dwordx4 v92, %[rw], s44 offen nt lds\ns_add_u32 m0, %[lw], 23552\ns_add_u32 s44, %[wo], 458752\nbuffer_load_dwordx4 v92, %[rw], s44 offen nt lds\ns_add_u32 m0, %[tb], 0\ns_add_u32 s44, %[wo], 524288\nbuffer_load_dwordx4 v92, %[rw], s44 offen nt lds\n" \
"s_add_u32 m0, %[tb], 1024\ns_add_u32 s44, %[wo], 589824\nbuffer_load_dwordx4 v92, %[rw], s44 offen nt lds\ns_add_u32 m0, %[tb], 2048\ns_add_u32 s44, %[wo], 655360\nbuffer_load_dwordx4 v92, %[rw], s44 offen nt lds\ns_add_u32 m0, %[tb], 3072\ns_add_u32 s44, %[wo], 720896\n" \
"buffer_load_dwordx4 v92, %[rw], s44 offen nt lds\ns_add_u32 m0, %[tb], 4096\ns_add_u32 s44, %[wo], 786432\nbuffer_load_dwordx4 v92, %[rw], s44 offen nt lds\ns_add_u32 m0, %[tb], 5120\ns_add_u32 s44, %[wo], 851968\nbuffer_load_dwordx4 v92, %[rw], s44 offen nt lds\ns_add_u32 m0, %[tb], 6144\n" \
"s_add_u32 s44, %[wo], 917504\nbuffer_load_dwordx4 v92, %[rw], s44 offen nt lds\ns_add_u32 m0, %[tb], 7168\ns_add_u32 s44, %[wo], 983040\nbuffer_load_dwordx4 v92, %[rw], s44 offen nt lds\ns_add_u32 m0, %[tb], 8192\ns_add_u32 s44, %[wo], 1048576\nbuffer_load_dwordx4 v92, %[rw], s44 offen nt lds\n" \
"s_add_u32 m0, %[tb], 9216\ns_add_u32 s44, %[wo], 1114112\nbuffer_load_dwordx4 v92, %[rw], s44 offen nt lds\ns_add_u32 m0, %[tb], 10240\ns_add_u32 s44, %[wo], 1179648\nbuffer_load_dwordx4 v92, %[rw], s44 offen nt lds\ns_add_u32 m0, %[tb], 11264\ns_add_u32 s44, %[wo], 1245184\n" \
"buffer_load_dwordx4 v92, %[rw], s44 offen nt lds\ns_add_u32 m0, %[lw], 0\ns_add_u32 s44, %[wo], 1310720\nbuffer_load_dwordx4 v92, %[rw], s44 offen nt lds\ns_add_u32 m0, %[lw], 1024\ns_add_u32 s44, %[wo], 1376256\nbuffer_load_dwordx4 v92, %[rw], s44 offen nt lds\ns_add_u32 m0, %[lw], 2048\n" \
"s_add_u32 s44, %[wo], 1441792\nbuffer_load_dwordx4 v92, %[rw], s44 offen nt lds\ns_add_u32 m0, %[lw], 3072\ns_add_u32 s44, %[wo], 1507328\nbuffer_load_dwordx4 v92, %[rw], s44 offen nt lds\ns_add_u32 m0, %[lw], 4096\ns_add_u32 s44, %[wo], 1572864\nbuffer_load_dwordx4 v92, %[rw], s44 offen nt lds\n" \
"s_add_u32 m0, %[lw], 5120\ns_add_u32 s44, %[wo], 1638400\nbuffer_load_dwordx4 v92, %[rw], s44 offen nt lds\ns_add_u32 m0, %[lw], 6144\ns_add_u32 s44, %[wo], 1703936\nbuffer_load_dwordx4 v92, %[rw], s44 offen nt lds\ns_add_u32 m0, %[lw], 7168\ns_add_u32 s44, %[wo], 1769472\n" \
"buffer_load_dwordx4 v92, %[rw], s44 offen nt lds\ns_add_u32 m0, %[lw], 8192\ns_add_u32 s44, %[wo], 1835008\nbuffer_load_dwordx4 v92, %[rw], s44 offen nt lds\ns_add_u32 m0, %[lw], 9216\ns_add_u32 s44, %[wo], 1900544\nbuffer_load_dwordx4 v92, %[rw], s44 offen nt lds\ns_add_u32 m0, %[lw], 10240\n" \
"s_add_u32 s44, %[wo], 1966080\nbuffer_load_dwordx4 v92, %[rw], s44 offen nt lds\ns_add_u32 m0, %[lw], 11264\ns_add_u32 s44, %[wo], 2031616\nbuffer_load_dwordx4 v92, %[rw], s44 offen nt lds\ns_add_u32 m0, %[lw], 12288\ns_add_u32 s44, %[wo], 2097152\nbuffer_load_dwordx4 v92, %[rw], s44 offen nt lds\n" \
"s_add_u32 m0, %[lw], 13312\ns_add_u32 s44, %[wo], 2162688\nbuffer_load_dwordx4 v92, %[rw], s44 offen nt lds\ns_add_u32 m0, %[lw], 14336\ns_add_u32 s44, %[wo], 2228224\nbuffer_load_dwordx4 v92, %[rw], s44 offen nt lds\ns_add_u32 m0, %[lw], 15360\ns_add_u32 s44, %[wo], 2293760\n" \
"buffer_load_dwordx4 v92, %[rw], s44 offen nt lds\ns_waitcnt vmcnt(32)\nds_read_b64_tr_b16 a[0:1], v94 offset:16384\nds_read_b64_tr_b16 a[2:3], v94 offset:16896\nds_read_b64_tr_b16 a[8:9], v94 offset:16392\nds_read_b64_tr_b16 a[10:11], v94 offset:16904\nds_read_b64_tr_b16 a[16:17], v94 offset:16448\nds_read_b64_tr_b16 a[18:19], v94 offset:16960\n" \
"ds_read_b64_tr_b16 a[24:25], v94 offset:16456\nds_read_b64_tr_b16 a[26:27], v94 offset:16968\ns_waitcnt lgkmcnt(0)\ns_add_u32 m0, %[lw], 16384\ns_add_u32 s44, %[wo], 2359296\nbuffer_load_dwordx4 v92, %[rw], s44 offen nt lds\ns_add_u32 m0, %[lw], 17408\ns_add_u32 s44, %[wo], 2424832\n" \
"buffer_load_dwordx4 v92, %[rw], s44 offen nt lds\ns_add_u32 m0, %[lw], 18432\ns_add_u32 s44, %[wo], 2490368\nbuffer_load_dwordx4 v92, %[rw], s44 offen nt lds\ns_add_u32 m0, %[lw], 19456\ns_add_u32 s44, %[wo], 2555904\nbuffer_load_dwordx4 v92, %[rw], s44 offen nt lds\ns_waitcnt vmcnt(32)\n" \
"ds_read_b64_tr_b16 a[4:5], v94 offset:20480\nds_read_b64_tr_b16 a[6:7], v94 offset:20992\nds_read_b64_tr_b16 a[12:13], v94 offset:20488\nds_read_b64_tr_b16 a[14:15], v94 offset:21000\nds_read_b64_tr_b16 a[20:21], v94 offset:20544\nds_read_b64_tr_b16 a[22:23], v94 offset:21056\nds_read_b64_tr_b16 a[28:29], v94 offset:20552\nds_read_b64_tr_b16 a[30:31], v94 offset:21064\n" \
"s_waitcnt lgkmcnt(0)\ns_add_u32 m0, %[lw], 20480\ns_add_u32 s44, %[wo], 2621440\nbuffer_load_dwordx4 v92, %[rw], s44 offen nt lds\ns_add_u32 m0, %[lw], 21504\ns_add_u32 s44, %[wo], 2686976\nbuffer_load_dwordx4 v92, %[rw], s44 offen nt lds\ns_add_u32 m0, %[lw], 22528\n" \
"s_add_u32 s44, %[wo], 2752512\nbuffer_load_dwordx4 v92, %[rw], s44 offen nt lds\ns_add_u32 m0, %[lw], 23552\ns_add_u32 s44, %[wo], 2818048\nbuffer_load_dwordx4 v92, %[rw], s44 offen nt lds\ns_waitcnt vmcnt(32)\nds_read_b64_tr_b16 a[32:33], v93 offset:0\nds_read_b64_tr_b16 a[34:35], v93 offset:512\n" \
"ds_read_b64_tr_b16 v[84:85], v93 offset:8\nds_read_b64_tr_b16 v[86:87], v93 offset:520\nds_read_b64_tr_b16 a[40:41], v93 offset:64\nds_read_b64_tr_b16 a[42:43], v93 offset:576\nds_read_b64_tr_b16 a[48:49], v93 offset:72\nds_read_b64_tr_b16 a[50:51], v93 offset:584\ns_waitcnt lgkmcnt(0)\ns_add_u32 m0, %[tb], 0\n" \
"s_add_u32 s44, %[wo], 2883584\nbuffer_load_dwordx4 v92, %[rw], s44 offen nt lds\ns_add_u32 m0, %[tb], 1024\ns_add_u32 s44, %[wo], 2949120\nbuffer_load_dwordx4 v92, %[rw], s44 offen nt lds\ns_add_u32 m0, %[tb], 2048\ns_add_u32 s44, %[wo], 3014656\nbuffer_load_dwordx4 v92, %[rw], s44 offen nt lds\n" \
"s_add_u32 m0, %[tb], 3072\ns_add_u32 s44, %[wo], 3080192\nbuffer_load_dwordx4 v92, %[rw], s44 offen nt lds\ns_waitcnt vmcnt(32)\nds_read_b64_tr_b16 a[36:37], v93 offset:4096\nds_read_b64_tr_b16 a[38:39], v93 offset:4608\nds_read_b64_tr_b16 v[80:81], v93 offset:4104\nds_read_b64_tr_b16 v[82:83], v93 offset:4616\n" \
"ds_read_b64_tr_b16 a[44:45], v93 offset:4160\nds_read_b64_tr_b16 a[46:47], v93 offset:4672\nds_read_b64_tr_b16 a[52:53], v93 offset:4168\nds_read_b64_tr_b16 a[54:55], v93 offset:4680\ns_waitcnt lgkmcnt(0)\ns_add_u32 m0, %[tb], 4096\ns_add_u32 s44, %[wo], 3145728\nbuffer_load_dwordx4 v92, %[rw], s44 offen nt lds\n" \
"s_add_u32 m0, %[tb], 5120\ns_add_u32 s44, %[wo], 3211264\nbuffer_load_dwordx4 v92, %[rw], s44 offen nt lds\ns_add_u32 m0, %[tb], 6144\ns_add_u32 s44, %[wo], 3276800\nbuffer_load_dwordx4 v92, %[rw], s44 offen nt lds\ns_add_u32 m0, %[tb], 7168\ns_add_u32 s44, %[wo], 3342336\n" \
"buffer_load_dwordx4 v92, %[rw], s44 offen nt lds\ns_waitcnt vmcnt(32)\nds_read_b64_tr_b16 a[56:57], v93 offset:8192\nds_read_b64_tr_b16 a[58:59], v93 offset:8704\nds_read_b64_tr_b16 a[64:65], v93 offset:8200\nds_read_b64_tr_b16 a[66:67], v93 offset:8712\nds_read_b64_tr_b16 v[76:77], v93 offset:8256\nds_read_b64_tr_b16 v[78:79], v93 offset:8768\n" \
"ds_read_b64_tr_b16 a[72:73], v93 offset:8264\nds_read_b64_tr_b16 a[74:75], v93 offset:8776\ns_waitcnt lgkmcnt(0)\ns_add_u32 m0, %[tb], 8192\ns_add_u32 s44, %[wo], 3407872\nbuffer_load_dwordx4 v92, %[rw], s44 offen nt lds\ns_add_u32 m0, %[tb], 9216\ns_add_u32 s44, %[wo], 3473408\n" \
"buffer_load_dwordx4 v92, %[rw], s44 offen nt lds\ns_add_u32 m0, %[tb], 10240\ns_add_u32 s44, %[wo], 3538944\nbuffer_load_dwordx4 v92, %[rw], s44 offen nt lds\ns_add_u32 m0, %[tb], 11264\ns_add_u32 s44, %[wo], 3604480\nbuffer_load_dwordx4 v92, %[rw], s44 offen nt lds\ns_waitcnt vmcnt(32)\n" \
"ds_read_b64_tr_b16 a[60:61], v94 offset:0\nds_read_b64_tr_b16 a[62:63], v94 offset:512\nds_read_b64_tr_b16 a[68:69], v94 offset:8\nds_read_b64_tr_b16 a[70:71], v94 offset:520\nds_read_b64_tr_b16 v[72:73], v94 offset:64\nds_read_b64_tr_b16 v[74:75], v94 offset:576\nds_read_b64_tr_b16 a[76:77], v94 offset:72\nds_read_b64_tr_b16 a[78:79], v94 offset:584\n" \
"s_waitcnt lgkmcnt(0)\ns_add_u32 m0, %[lw], 0\ns_add_u32 s44, %[wo], 3670016\nbuffer_load_dwordx4 v92, %[rw], s44 offen nt lds\ns_add_u32 m0, %[lw], 1024\ns_add_u32 s44, %[wo], 3735552\nbuffer_load_dwordx4 v92, %[rw], s44 offen nt lds\ns_add_u32 m0, %[lw], 2048\n" \
"s_add_u32 s44, %[wo], 3801088\nbuffer_load_dwordx4 v92, %[rw], s44 offen nt lds\ns_add_u32 m0, %[lw], 3072\ns_add_u32 s44, %[wo], 3866624\nbuffer_load_dwordx4 v92, %[rw], s44 offen nt lds\ns_waitcnt vmcnt(32)\nds_read_b64_tr_b16 a[80:81], v94 offset:4096\nds_read_b64_tr_b16 a[82:83], v94 offset:4608\n" \
"ds_read_b64_tr_b16 a[88:89], v94 offset:4104\nds_read_b64_tr_b16 a[90:91], v94 offset:4616\nds_read_b64_tr_b16 a[96:97], v94 offset:4160\nds_read_b64_tr_b16 a[98:99], v94 offset:4672\nds_read_b64_tr_b16 v[68:69], v94 offset:4168\nds_read_b64_tr_b16 v[70:71], v94 offset:4680\ns_waitcnt lgkmcnt(0)\ns_add_u32 m0, %[lw], 4096\n" \
"s_add_u32 s44, %[wo], 3932160\nbuffer_load_dwordx4 v92, %[rw], s44 offen nt lds\ns_add_u32 m0, %[lw], 5120\ns_add_u32 s44, %[wo], 3997696\nbuffer_load_dwordx4 v92, %[rw], s44 offen nt lds\ns_add_u32 m0, %[lw], 6144\ns_add_u32 s44, %[wo], 4063232\nbuffer_load_dwordx4 v92, %[rw], s44 offen nt lds\n" \
"s_add_u32 m0, %[lw], 7168\ns_add_u32 s44, %[wo], 4128768\nbuffer_load_dwordx4 v92, %[rw], s44 offen nt lds\ns_waitcnt vmcnt(32)\nds_read_b64_tr_b16 a[84:85], v94 offset:8192\nds_read_b64_tr_b16 a[86:87], v94 offset:8704\nds_read_b64_tr_b16 a[92:93], v94 offset:8200\nds_read_b64_tr_b16 a[94:95], v94 offset:8712\n" \
"ds_read_b64_tr_b16 a[100:101], v94 offset:8256\nds_read_b64_tr_b16 a[102:103], v94 offset:8768\nds_read_b64_tr_b16 v[64:65], v94 offset:8264\nds_read_b64_tr_b16 v[66:67], v94 offset:8776\ns_waitcnt lgkmcnt(0)\ns_add_u32 m0, %[lw], 8192\ns_add_u32 s44, %[wo], 4194304\nbuffer_load_dwordx4 v92, %[rw], s44 offen nt lds\n" \
"s_add_u32 m0, %[lw], 9216\ns_add_u32 s44, %[wo], 4259840\nbuffer_load_dwordx4 v92, %[rw], s44 offen nt lds\ns_add_u32 m0, %[lw], 10240\ns_add_u32 s44, %[wo], 4325376\nbuffer_load_dwordx4 v92, %[rw], s44 offen nt lds\ns_add_u32 m0, %[lw], 11264\ns_add_u32 s44, %[wo], 4390912\n" \
"buffer_load_dwordx4 v92, %[rw], s44 offen nt lds\ns_waitcnt vmcnt(32)\nds_read_b64_tr_b16 a[104:105], v94 offset:12288\nds_read_b64_tr_b16 a[106:107], v94 offset:12800\nds_read_b64_tr_b16 a[112:113], v94 offset:12296\nds_read_b64_tr_b16 a[114:115], v94 offset:12808\nds_read_b64_tr_b16 a[120:121], v94 offset:12352\nds_read_b64_tr_b16 a[122:123], v94 offset:12864\n" \
"ds_read_b64_tr_b16 a[128:129], v94 offset:12360\nds_read_b64_tr_b16 a[130:131], v94 offset:12872\ns_waitcnt lgkmcnt(0)\ns_add_u32 m0, %[lw], 12288\ns_add_u32 s44, %[wo], 4456448\nbuffer_load_dwordx4 v92, %[rw], s44 offen nt lds\ns_add_u32 m0, %[lw], 13312\ns_add_u32 s44, %[wo], 4521984\n" \
"buffer_load_dwordx4 v92, %[rw], s44 offen nt lds\ns_add_u32 m0, %[lw], 14336\ns_add_u32 s44, %[wo], 4587520\nbuffer_load_dwordx4 v92, %[rw], s44 offen nt lds\ns_add_u32 m0, %[lw], 15360\ns_add_u32 s44, %[wo], 4653056\nbuffer_load_dwordx4 v92, %[rw], s44 offen nt lds\ns_waitcnt vmcnt(32)\n" \
"ds_read_b64_tr_b16 a[108:109], v94 offset:16384\nds_read_b64_tr_b16 a[110:111], v94 offset:16896\nds_read_b64_tr_b16 a[116:117], v94 offset:16392\nds_read_b64_tr_b16 a[118:119], v94 offset:16904\nds_read_b64_tr_b16 a[124:125], v94 offset:16448\nds_read_b64_tr_b16 a[126:127], v94 offset:16960\nds_read_b64_tr_b16 a[132:133], v94 offset:16456\nds_read_b64_tr_b16 a[134:135], v94 offset:16968\n" \
"s_waitcnt lgkmcnt(0)\ns_add_u32 m0, %[lw], 16384\ns_add_u32 s44, %[wo], 4718592\nbuffer_load_dwordx4 v92, %[rw], s44 offen nt lds\ns_add_u32 m0, %[lw], 17408\ns_add_u32 s44, %[wo], 4784128\nbuffer_load_dwordx4 v92, %[rw], s44 offen nt lds\ns_add_u32 m0, %[lw], 18432\n" \
"s_add_u32 s44, %[wo], 4849664\nbuffer_load_dwordx4 v92, %[rw], s44 offen nt lds\ns_add_u32 m0, %[lw], 19456\ns_add_u32 s44, %[wo], 4915200\nbuffer_load_dwordx4 v92, %[rw], s44 offen nt lds\ns_waitcnt vmcnt(32)\nds_read_b64_tr_b16 a[136:137], v94 offset:20480\nds_read_b64_tr_b16 a[138:139], v94 offset:20992\n" \
"ds_read_b64_tr_b16 v[60:61], v94 offset:20488\nds_read_b64_tr_b16 v[62:63], v94 offset:21000\nds_read_b64_tr_b16 a[144:145], v94 offset:20544\nds_read_b64_tr_b16 a[146:147], v94 offset:21056\nds_read_b64_tr_b16 a[152:153], v94 offset:20552\nds_read_b64_tr_b16 a[154:155], v94 offset:21064\ns_waitcnt lgkmcnt(0)\ns_add_u32 m0, %[lw], 20480\n" \
"s_add_u32 s44, %[wo], 4980736\nbuffer_load_dwordx4 v92, %[rw], s44 offen nt lds\ns_add_u32 m0, %[lw], 21504\ns_add_u32 s44, %[wo], 5046272\nbuffer_load_dwordx4 v92, %[rw], s44 offen nt lds\ns_add_u32 m0, %[lw], 22528\ns_add_u32 s44, %[wo], 5111808\nbuffer_load_dwordx4 v92, %[rw], s44 offen nt lds\n" \
"s_add_u32 m0, %[lw], 23552\ns_add_u32 s44, %[wo], 5177344\nbuffer_load_dwordx4 v92, %[rw], s44 offen nt lds\ns_waitcnt vmcnt(32)\nds_read_b64_tr_b16 a[140:141], v93 offset:0\nds_read_b64_tr_b16 a[142:143], v93 offset:512\nds_read_b64_tr_b16 v[56:57], v93 offset:8\nds_read_b64_tr_b16 v[58:59], v93 offset:520\n" \
"ds_read_b64_tr_b16 a[148:149], v93 offset:64\nds_read_b64_tr_b16 a[150:151], v93 offset:576\nds_read_b64_tr_b16 a[156:157], v93 offset:72\nds_read_b64_tr_b16 a[158:159], v93 offset:584\ns_waitcnt lgkmcnt(0)\ns_add_u32 m0, %[tb], 0\ns_add_u32 s44, %[wo], 5242880\nbuffer_load_dwordx4 v92, %[rw], s44 offen nt lds\n" \
"s_add_u32 m0, %[tb], 1024\ns_add_u32 s44, %[wo], 5308416\nbuffer_load_dwordx4 v92, %[rw], s44 offen nt lds\ns_add_u32 m0, %[tb], 2048\ns_add_u32 s44, %[wo], 5373952\nbuffer_load_dwordx4 v92, %[rw], s44 offen nt lds\ns_add_u32 m0, %[tb], 3072\ns_add_u32 s44, %[wo], 5439488\n" \
"buffer_load_dwordx4 v92, %[rw], s44 offen nt lds\ns_waitcnt vmcnt(32)\nds_read_b64_tr_b16 a[160:161], v93 offset:4096\nds_read_b64_tr_b16 a[162:163], v93 offset:4608\nds_read_b64_tr_b16 a[168:169], v93 offset:4104\nds_read_b64_tr_b16 a[170:171], v93 offset:4616\nds_read_b64_tr_b16 v[52:53], v93 offset:4160\nds_read_b64_tr_b16 v[54:55], v93 offset:4672\n" \
"ds_read_b64_tr_b16 a[176:177], v93 offset:4168\nds_read_b64_tr_b16 a[178:179], v93 offset:4680\ns_waitcnt lgkmcnt(0)\ns_add_u32 m0, %[tb], 4096\ns_add_u32 s44, %[wo], 5505024\nbuffer_load_dwordx4 v92, %[rw], s44 offen nt lds\ns_add_u32 m0, %[tb], 5120\ns_add_u32 s44, %[wo], 5570560\n" \
"buffer_load_dwordx4 v92, %[rw], s44 offen nt lds\ns_add_u32 m0, %[tb], 6144\ns_add_u32 s44, %[wo], 5636096\nbuffer_load_dwordx4 v92, %[rw], s44 offen nt lds\ns_add_u32 m0, %[tb], 7168\ns_add_u32 s44, %[wo], 5701632\nbuffer_load_dwordx4 v92, %[rw], s44 offen nt lds\ns_waitcnt vmcnt(32)\n" \
"ds_read_b64_tr_b16 a[164:165], v93 offset:8192\nds_read_b64_tr_b16 a[166:167], v93 offset:8704\nds_read_b64_tr_b16 a[172:173], v93 offset:8200\nds_read_b64_tr_b16 a[174:175], v93 offset:8712\nds_read_b64_tr_b16 v[48:49], v93 offset:8256\nds_read_b64_tr_b16 v[50:51], v93 offset:8768\nds_read_b64_tr_b16 a[180:181], v93 offset:8264\nds_read_b64_tr_b16 a[182:183], v93 offset:8776\n" \
"s_waitcnt lgkmcnt(0)\ns_add_u32 m0, %[tb], 8192\ns_add_u32 s44, %[wo], 5767168\nbuffer_load_dwordx4 v92, %[rw], s44 offen nt lds\ns_add_u32 m0, %[tb], 9216\ns_add_u32 s44, %[wo], 5832704\nbuffer_load_dwordx4 v92, %[rw], s44 offen nt lds\ns_add_u32 m0, %[tb], 10240\n" \
"s_add_u32 s44, %[wo], 5898240\nbuffer_load_dwordx4 v92, %[rw], s44 offen nt lds\ns_add_u32 m0, %[tb], 11264\ns_add_u32 s44, %[wo], 5963776\nbuffer_load_dwordx4 v92, %[rw], s44 offen nt lds\ns_waitcnt vmcnt(32)\nds_read_b64_tr_b16 a[184:185], v94 offset:0\nds_read_b64_tr_b16 a[186:187], v94 offset:512\n" \
"ds_read_b64_tr_b16 a[192:193], v94 offset:8\nds_read_b64_tr_b16 a[194:195], v94 offset:520\nds_read_b64_tr_b16 a[200:201], v94 offset:64\nds_read_b64_tr_b16 a[202:203], v94 offset:576\nds_read_b64_tr_b16 v[44:45], v94 offset:72\nds_read_b64_tr_b16 v[46:47], v94 offset:584\ns_waitcnt lgkmcnt(0)\ns_add_u32 m0, %[lw], 0\n" \
"s_add_u32 s44, %[wo], 6029312\nbuffer_load_dwordx4 v92, %[rw], s44 offen nt lds\ns_add_u32 m0, %[lw], 1024\ns_add_u32 s44, %[wo], 6094848\nbuffer_load_dwordx4 v92, %[rw], s44 offen nt lds\ns_add_u32 m0, %[lw], 2048\ns_add_u32 s44, %[wo], 6160384\nbuffer_load_dwordx4 v92, %[rw], s44 offen nt lds\n" \
"s_add_u32 m0, %[lw], 3072\ns_add_u32 s44, %[wo], 6225920\nbuffer_load_dwordx4 v92, %[rw], s44 offen nt lds\ns_waitcnt vmcnt(32)\nds_read_b64_tr_b16 a[188:189], v94 offset:4096\nds_read_b64_tr_b16 a[190:191], v94 offset:4608\nds_read_b64_tr_b16 a[196:197], v94 offset:4104\nds_read_b64_tr_b16 a[198:199], v94 offset:4616\n" \
"ds_read_b64_tr_b16 a[204:205], v94 offset:4160\nds_read_b64_tr_b16 a[206:207], v94 offset:4672\nds_read_b64_tr_b16 v[40:41], v94 offset:4168\nds_read_b64_tr_b16 v[42:43], v94 offset:4680\ns_waitcnt lgkmcnt(0)\ns_add_u32 m0, %[lw], 4096\ns_add_u32 s44, %[wo], 6291456\nbuffer_load_dwordx4 v92, %[rw], s44 offen nt lds\n" \
"s_add_u32 m0, %[lw], 5120\ns_add_u32 s44, %[wo], 6356992\nbuffer_load_dwordx4 v92, %[rw], s44 offen nt lds\ns_add_u32 m0, %[lw], 6144\ns_add_u32 s44, %[wo], 6422528\nbuffer_load_dwordx4 v92, %[rw], s44 offen nt lds\ns_add_u32 m0, %[lw], 7168\ns_add_u32 s44, %[wo], 6488064\n" \
"buffer_load_dwordx4 v92, %[rw], s44 offen nt lds\ns_waitcnt vmcnt(32)\nds_read_b64_tr_b16 a[208:209], v94 offset:8192\nds_read_b64_tr_b16 a[210:211], v94 offset:8704\nds_read_b64_tr_b16 a[216:217], v94 offset:8200\nds_read_b64_tr_b16 a[218:219], v94 offset:8712\nds_read_b64_tr_b16 a[224:225], v94 offset:8256\nds_read_b64_tr_b16 a[226:227], v94 offset:8768\n" \
"ds_read_b64_tr_b16 a[232:233], v94 offset:8264\nds_read_b64_tr_b16 a[234:235], v94 offset:8776\ns_waitcnt lgkmcnt(0)\ns_add_u32 m0, %[lw], 8192\ns_add_u32 s44, %[wo], 6553600\nbuffer_load_dwordx4 v92, %[rw], s44 offen nt lds\ns_add_u32 m0, %[lw], 9216\ns_add_u32 s44, %[wo], 6619136\n" \
"buffer_load_dwordx4 v92, %[rw], s44 offen nt lds\ns_add_u32 m0, %[lw], 10240\ns_add_u32 s44, %[wo], 6684672\nbuffer_load_dwordx4 v92, %[rw], s44 offen nt lds\ns_add_u32 m0, %[lw], 11264\ns_add_u32 s44, %[wo], 6750208\nbuffer_load_dwordx4 v92, %[rw], s44 offen nt lds\ns_waitcnt vmcnt(32)\n" \
"ds_read_b64_tr_b16 a[212:213], v94 offset:12288\nds_read_b64_tr_b16 a[214:215], v94 offset:12800\nds_read_b64_tr_b16 a[220:221], v94 offset:12296\nds_read_b64_tr_b16 a[222:223], v94 offset:12808\nds_read_b64_tr_b16 a[228:229], v94 offset:12352\nds_read_b64_tr_b16 a[230:231], v94 offset:12864\nds_read_b64_tr_b16 a[236:237], v94 offset:12360\nds_read_b64_tr_b16 a[238:239], v94 offset:12872\n" \
"s_waitcnt lgkmcnt(0)\ns_add_u32 m0, %[lw], 12288\ns_add_u32 s44, %[wo], 6815744\nbuffer_load_dwordx4 v92, %[rw], s44 offen nt lds\ns_add_u32 m0, %[lw], 13312\ns_add_u32 s44, %[wo], 6881280\nbuffer_load_dwordx4 v92, %[rw], s44 offen nt lds\ns_add_u32 m0, %[lw], 14336\n" \
"s_add_u32 s44, %[wo], 6946816\nbuffer_load_dwordx4 v92, %[rw], s44 offen nt lds\ns_add_u32 m0, %[lw], 15360\ns_add_u32 s44, %[wo], 7012352\nbuffer_load_dwordx4 v92, %[rw], s44 offen nt lds\ns_waitcnt vmcnt(32)\nds_read_b64_tr_b16 a[240:241], v94 offset:16384\nds_read_b64_tr_b16 a[242:243], v94 offset:16896\n" \
"ds_read_b64_tr_b16 v[36:37], v94 offset:16392\nds_read_b64_tr_b16 v[38:39], v94 offset:16904\nds_read_b64_tr_b16 a[248:249], v94 offset:16448\nds_read_b64_tr_b16 a[250:251], v94 offset:16960\nds_read_b64_tr_b16 v[96:97], v94 offset:16456\nds_read_b64_tr_b16 v[98:99], v94 offset:16968\ns_waitcnt lgkmcnt(0)\ns_add_u32 m0, %[lw], 16384\n" \
"s_add_u32 s44, %[wo], 7077888\nbuffer_load_dwordx4 v92, %[rw], s44 offen nt lds\ns_add_u32 m0, %[lw], 17408\ns_add_u32 s44, %[wo], 7143424\nbuffer_load_dwordx4 v92, %[rw], s44 offen nt lds\ns_add_u32 m0, %[lw], 18432\ns_add_u32 s44, %[wo], 7208960\nbuffer_load_dwordx4 v92, %[rw], s44 offen nt lds\n" \
"s_add_u32 m0, %[lw], 19456\ns_add_u32 s44, %[wo], 7274496\nbuffer_load_dwordx4 v92, %[rw], s44 offen nt lds\ns_waitcnt vmcnt(32)\nds_read_b64_tr_b16 a[244:245], v94 offset:20480\nds_read_b64_tr_b16 a[246:247], v94 offset:20992\nds_read_b64_tr_b16 v[32:33], v94 offset:20488\nds_read_b64_tr_b16 v[34:35], v94 offset:21000\n" \
"ds_read_b64_tr_b16 a[252:253], v94 offset:20544\nds_read_b64_tr_b16 a[254:255], v94 offset:21056\nds_read_b64_tr_b16 v[100:101], v94 offset:20552\nds_read_b64_tr_b16 v[102:103], v94 offset:21064\ns_waitcnt lgkmcnt(0)\ns_add_u32 m0, %[lw], 20480\ns_add_u32 s44, %[wo], 7340032\nbuffer_load_dwordx4 v92, %[rw], s44 offen nt lds\n" \
"s_add_u32 m0, %[lw], 21504\ns_add_u32 s44, %[wo], 7405568\nbuffer_load_dwordx4 v92, %[rw], s44 offen nt lds\ns_add_u32 m0, %[lw], 22528\ns_add_u32 s44, %[wo], 7471104\nbuffer_load_dwordx4 v92, %[rw], s44 offen nt lds\ns_add_u32 m0, %[lw], 23552\ns_add_u32 s44, %[wo], 7536640\n" \
"buffer_load_dwordx4 v92, %[rw], s44 offen nt lds\ns_waitcnt vmcnt(32)\nds_read_b64_tr_b16 v[104:105], v93 offset:0\nds_read_b64_tr_b16 v[106:107], v93 offset:512\nds_read_b64_tr_b16 v[112:113], v93 offset:8\nds_read_b64_tr_b16 v[114:115], v93 offset:520\nds_read_b64_tr_b16 v[28:29], v93 offset:64\nds_read_b64_tr_b16 v[30:31], v93 offset:576\n" \
"ds_read_b64_tr_b16 v[120:121], v93 offset:72\nds_read_b64_tr_b16 v[122:123], v93 offset:584\ns_waitcnt lgkmcnt(0)\ns_add_u32 m0, %[tb], 0\ns_add_u32 s44, %[wo], 7602176\nbuffer_load_dwordx4 v92, %[rw], s44 offen nt lds\ns_add_u32 m0, %[tb], 1024\ns_add_u32 s44, %[wo], 7667712\n" \
"buffer_load_dwordx4 v92, %[rw], s44 offen nt lds\ns_add_u32 m0, %[tb], 2048\ns_add_u32 s44, %[wo], 7733248\nbuffer_load_dwordx4 v92, %[rw], s44 offen nt lds\ns_add_u32 m0, %[tb], 3072\ns_add_u32 s44, %[wo], 7798784\nbuffer_load_dwordx4 v92, %[rw], s44 offen nt lds\ns_waitcnt vmcnt(32)\n" \
"ds_read_b64_tr_b16 v[108:109], v93 offset:4096\nds_read_b64_tr_b16 v[110:111], v93 offset:4608\nds_read_b64_tr_b16 v[116:117], v93 offset:4104\nds_read_b64_tr_b16 v[118:119], v93 offset:4616\nds_read_b64_tr_b16 v[24:25], v93 offset:4160\nds_read_b64_tr_b16 v[26:27], v93 offset:4672\nds_read_b64_tr_b16 v[124:125], v93 offset:4168\nds_read_b64_tr_b16 v[126:127], v93 offset:4680\n" \
"s_waitcnt lgkmcnt(0)\nbuffer_load_dwordx2 v[88:89], off, s[36:39], s45 sc1\ns_add_u32 m0, %[tb], 4096\ns_add_u32 s44, %[wo], 7864320\nbuffer_load_dwordx4 v92, %[rw], s44 offen nt lds\ns_add_u32 m0, %[tb], 5120\ns_add_u32 s44, %[wo], 7929856\nbuffer_load_dwordx4 v92, %[rw], s44 offen nt lds\n" \
"s_add_u32 m0, %[tb], 6144\ns_add_u32 s44, %[wo], 7995392\nbuffer_load_dwordx4 v92, %[rw], s44 offen nt lds\ns_add_u32 m0, %[tb], 7168\ns_add_u32 s44, %[wo], 8060928\nbuffer_load_dwordx4 v92, %[rw], s44 offen nt lds\ns_waitcnt vmcnt(33)\nds_read_b64_tr_b16 v[128:129], v93 offset:8192\n" \
"ds_read_b64_tr_b16 v[130:131], v93 offset:8704\nds_read_b64_tr_b16 v[136:137], v93 offset:8200\nds_read_b64_tr_b16 v[138:139], v93 offset:8712\nds_read_b64_tr_b16 v[144:145], v93 offset:8256\nds_read_b64_tr_b16 v[146:147], v93 offset:8768\nds_read_b64_tr_b16 v[20:21], v93 offset:8264\nds_read_b64_tr_b16 v[22:23], v93 offset:8776\ns_waitcnt lgkmcnt(0)\n" \
"s_add_u32 m0, %[tb], 8192\ns_add_u32 s44, %[wo], 8126464\nbuffer_load_dwordx4 v92, %[rw], s44 offen nt lds\ns_add_u32 m0, %[tb], 9216\ns_add_u32 s44, %[wo], 8192000\nbuffer_load_dwordx4 v92, %[rw], s44 offen nt lds\ns_add_u32 m0, %[tb], 10240\ns_add_u32 s44, %[wo], 8257536\n" \
"buffer_load_dwordx4 v92, %[rw], s44 offen nt lds\ns_add_u32 m0, %[tb], 11264\ns_add_u32 s44, %[wo], 8323072\nbuffer_load_dwordx4 v92, %[rw], s44 offen nt lds\ns_waitcnt vmcnt(33)\nds_read_b64_tr_b16 v[132:133], v94 offset:0\nds_read_b64_tr_b16 v[134:135], v94 offset:512\nds_read_b64_tr_b16 v[140:141], v94 offset:8\n" \
"ds_read_b64_tr_b16 v[142:143], v94 offset:520\nds_read_b64_tr_b16 v[148:149], v94 offset:64\nds_read_b64_tr_b16 v[150:151], v94 offset:576\nds_read_b64_tr_b16 v[16:17], v94 offset:72\nds_read_b64_tr_b16 v[18:19], v94 offset:584\ns_waitcnt lgkmcnt(0)\nds_write_b128 v1, v[84:87] offset:0\nds_write_b128 v1, v[80:83] offset:1024\n" \
"ds_write_b128 v1, v[76:79] offset:2048\nds_write_b128 v1, v[72:75] offset:3072\ns_waitcnt vmcnt(29)\nds_read_b64_tr_b16 v[152:153], v94 offset:4096\nds_read_b64_tr_b16 v[154:155], v94 offset:4608\nds_read_b64_tr_b16 v[160:161], v94 offset:4104\nds_read_b64_tr_b16 v[162:163], v94 offset:4616\nds_read_b64_tr_b16 v[168:169], v94 offset:4160\n" \
"ds_read_b64_tr_b16 v[170:171], v94 offset:4672\nds_read_b64_tr_b16 v[176:177], v94 offset:4168\nds_read_b64_tr_b16 v[178:179], v94 offset:4680\ns_waitcnt lgkmcnt(0)\nds_write_b128 v1, v[68:71] offset:4096\nds_write_b128 v1, v[64:67] offset:5120\nds_write_b128 v1, v[60:63] offset:6144\nds_write_b128 v1, v[56:59] offset:7168\n" \
"s_waitcnt vmcnt(25)\nds_read_b64_tr_b16 v[156:157], v94 offset:8192\nds_read_b64_tr_b16 v[158:159], v94 offset:8704\nds_read_b64_tr_b16 v[164:165], v94 offset:8200\nds_read_b64_tr_b16 v[166:167], v94 offset:8712\nds_read_b64_tr_b16 v[172:173], v94 offset:8256\nds_read_b64_tr_b16 v[174:175], v94 offset:8768\nds_read_b64_tr_b16 v[180:181], v94 offset:8264\n" \
"ds_read_b64_tr_b16 v[182:183], v94 offset:8776\ns_waitcnt lgkmcnt(0)\nds_write_b128 v1, v[52:55] offset:8192\nds_write_b128 v1, v[48:51] offset:9216\nds_write_b128 v1, v[44:47] offset:10240\nds_write_b128 v1, v[40:43] offset:11264\ns_waitcnt vmcnt(21)\nds_read_b64_tr_b16 v[184:185], v94 offset:12288\n" \
"ds_read_b64_tr_b16 v[186:187], v94 offset:12800\nds_read_b64_tr_b16 v[40:41], v94 offset:12296\nds_read_b64_tr_b16 v[42:43], v94 offset:12808\nds_read_b64_tr_b16 v[192:193], v94 offset:12352\nds_read_b64_tr_b16 v[194:195], v94 offset:12864\nds_read_b64_tr_b16 v[200:201], v94 offset:12360\nds_read_b64_tr_b16 v[202:203], v94 offset:12872\ns_waitcnt lgkmcnt(0)\n" \
"ds_write_b128 v1, v[36:39] offset:12288\nds_write_b128 v1, v[32:35] offset:13312\nds_write_b128 v1, v[28:31] offset:14336\nds_write_b128 v1, v[24:27] offset:15360\ns_waitcnt vmcnt(17)\nds_read_b64_tr_b16 v[188:189], v94 offset:16384\nds_read_b64_tr_b16 v[190:191], v94 offset:16896\nds_read_b64_tr_b16 v[24:25], v94 offset:16392\n" \
"ds_read_b64_tr_b16 v[26:27], v94 offset:16904\nds_read_b64_tr_b16 v[196:197], v94 offset:16448\nds_read_b64_tr_b16 v[198:199], v94 offset:16960\nds_read_b64_tr_b16 v[204:205], v94 offset:16456\nds_read_b64_tr_b16 v[206:207], v94 offset:16968\ns_waitcnt lgkmcnt(0)\nds_write_b128 v1, v[20:23] offset:16384\nds_write_b128 v1, v[16:19] offset:17408\n" \
"ds_write_b128 v1, v[40:43] offset:18432\nds_write_b128 v1, v[24:27] offset:19456\ns_waitcnt vmcnt(13)\nds_read_b64_tr_b16 v[208:209], v94 offset:20480\nds_read_b64_tr_b16 v[210:211], v94 offset:20992\nds_read_b64_tr_b16 v[216:217], v94 offset:20488\nds_read_b64_tr_b16 v[218:219], v94 offset:21000\nds_read_b64_tr_b16 v[24:25], v94 offset:20544\n" \
"ds_read_b64_tr_b16 v[26:27], v94 offset:21056\nds_read_b64_tr_b16 v[224:225], v94 offset:20552\nds_read_b64_tr_b16 v[226:227], v94 offset:21064\ns_waitcnt lgkmcnt(0)\nds_write_b128 v1, v[24:27] offset:20480\ns_waitcnt vmcnt(9)\nds_read_b64_tr_b16 v[212:213], v93 offset:0\nds_read_b64_tr_b16 v[214:215], v93 offset:512\n" \
"ds_read_b64_tr_b16 v[220:221], v93 offset:8\nds_read_b64_tr_b16 v[222:223], v93 offset:520\nds_read_b64_tr_b16 v[24:25], v93 offset:64\nds_read_b64_tr_b16 v[26:27], v93 offset:576\nds_read_b64_tr_b16 v[228:229], v93 offset:72\nds_read_b64_tr_b16 v[230:231], v93 offset:584\ns_waitcnt lgkmcnt(0)\nds_write_b128 v1, v[24:27] offset:21504\n" \
"s_waitcnt vmcnt(4)\nds_read_b64_tr_b16 v[232:233], v93 offset:4096\nds_read_b64_tr_b16 v[234:235], v93 offset:4608\nds_read_b64_tr_b16 v[240:241], v93 offset:4104\nds_read_b64_tr_b16 v[242:243], v93 offset:4616\nds_read_b64_tr_b16 v[248:249], v93 offset:4160\nds_read_b64_tr_b16 v[250:251], v93 offset:4672\nds_read_b64_tr_b16 v[24:25], v93 offset:4168\n" \
"ds_read_b64_tr_b16 v[26:27], v93 offset:4680\ns_waitcnt lgkmcnt(0)\nds_write_b128 v1, v[24:27] offset:22528\ns_waitcnt vmcnt(0)\nds_read_b64_tr_b16 v[236:237], v93 offset:8192\nds_read_b64_tr_b16 v[238:239], v93 offset:8704\nds_read_b64_tr_b16 v[244:245], v93 offset:8200\nds_read_b64_tr_b16 v[246:247], v93 offset:8712\n" \
"ds_read_b64_tr_b16 v[252:253], v93 offset:8256\nds_read_b64_tr_b16 v[254:255], v93 offset:8768\nds_read_b64_tr_b16 v[24:25], v93 offset:8264\nds_read_b64_tr_b16 v[26:27], v93 offset:8776\ns_waitcnt lgkmcnt(0)\ns_mov_b32 s43, 0\ns_mov_b32 s86, 110\ns_mov_b32 s89, 0\n" \
".Lqk_hb%=:\nv_cmp_ne_u32 vcc, v95, v88\ns_cbranch_vccnz .Lqk_hb1%=\ns_sleep 4\nbuffer_load_dwordx2 v[88:89], off, s[36:39], s45 sc1\ns_waitcnt vmcnt(0)\ns_add_u32 s43, s43, 1\ns_cmp_lt_u32 s43, 262144\n" \
"s_cbranch_scc1 .Lqk_hb%=\ns_branch .Lqk_abort%=\n.Lqk_hb1%=:\nv_readfirstlane_b32 s44, v89\ns_and_b32 s44, s44, 0x200000\ns_add_u32 s36, s36, s44\ns_addc_u32 s37, s37, 0\nds_write_b128 v1, v[24:27] offset:23552\n" \
"s_waitcnt lgkmcnt(0)\ns_barrier\ns_mov_b32 s40, -1\ns_mov_b32 s43, 0\ns_mov_b32 s61, 0x7fff7fff\ns_mov_b32 s58, 0x3e0293ee\ns_mov_b32 s59, 0xbe0293ee\ns_lshl_b32 s57, %[w], 12\n" \
"s_mov_b32 s62, 0xff00ff00\ns_mov_b32 s63, 0xff00ff00\ns_mov_b32 s56, 0\ns_cmp_eq_u32 %[w], 3\ns_cbranch_scc1 .Lqk_fetch0%=\n.Lqk_pwait%=:\ns_barrier\nds_read_b128 v[80:83], v2 offset:0\n" \
"ds_read_b128 v[84:87], v2 offset:256\nds_read_b128 v[88:91], v2 offset:2048\n.Lqk_proj%=:\ns_waitcnt lgkmcnt(2)\nv_mfma_f32_16x16x32_bf16 v[16:19], a[0:3], v[80:83], 0\nds_read_b128 v[92:95], v2 offset:2304\nv_mfma_f32_16x16x32_bf16 v[20:23], a[8:11], v[80:83], 0\nds_read_b128 v[72:75], v1 offset:0\n" \
"v_mfma_f32_16x16x32_bf16 v[24:27], a[16:19], v[80:83], 0\nv_mfma_f32_16x16x32_bf16 v[28:31], a[24:27], v[80:83], 0\ns_waitcnt lgkmcnt(3)\nv_mfma_f32_16x16x32_bf16 v[32:35], a[0:3], v[84:87], 0\nds_read_b128 v[80:83], v2 offset:4096\nv_mfma_f32_16x16x32_bf16 v[36:39], a[8:11], v[84:87], 0\nv_mfma_f32_16x16x32_bf16 v[40:43], a[16:19], v[84:87], 0\nv_mfma_f32_16x16x32_bf16 v[44:47], a[24:27], v[84:87], 0\n" \
"s_waitcnt lgkmcnt(3)\nv_mfma_f32_16x16x32_bf16 v[16:19], a[4:7], v[88:91], v[16:19]\nds_read_b128 v[84:87], v2 offset:4352\nv_mfma_f32_16x16x32_bf16 v[20:23], a[12:15], v[88:91], v[20:23]\nds_read_b128 v[76:79], v1 offset:1024\nv_mfma_f32_16x16x32_bf16 v[24:27], a[20:23], v[88:91], v[24:27]\nv_mfma_f32_16x16x32_bf16 v[28:31], a[28:31], v[88:91], v[28:31]\ns_waitcnt lgkmcnt(4)\n" \
"v_mfma_f32_16x16x32_bf16 v[32:35], a[4:7], v[92:95], v[32:35]\nds_read_b128 v[88:91], v2 offset:6144\nv_mfma_f32_16x16x32_bf16 v[36:39], a[12:15], v[92:95], v[36:39]\nv_mfma_f32_16x16x32_bf16 v[40:43], a[20:23], v[92:95], v[40:43]\nv_mfma_f32_16x16x32_bf16 v[44:47], a[28:31], v[92:95], v[44:47]\ns_waitcnt lgkmcnt(3)\nv_mfma_f32_16x16x32_bf16 v[16:19], a[32:35], v[80:83], v[16:19]\nds_read_b128 v[92:95], v2 offset:6400\n" \
"v_mfma_f32_16x16x32_bf16 v[20:23], v[72:75], v[80:83], v[20:23]\nds_read_b128 v[64:67], v1 offset:2048\nv_mfma_f32_16x16x32_bf16 v[24:27], a[40:43], v[80:83], v[24:27]\nv_mfma_f32_16x16x32_bf16 v[28:31], a[48:51], v[80:83], v[28:31]\ns_waitcnt lgkmcnt(4)\nv_mfma_f32_16x16x32_bf16 v[32:35], a[32:35], v[84:87], v[32:35]\nds_read_b128 v[80:83], v2 offset:8192\nv_mfma_f32_16x16x32_bf16 v[36:39], v[72:75], v[84:87], v[36:39]\n" \
"v_mfma_f32_16x16x32_bf16 v[40:43], a[40:43], v[84:87], v[40:43]\nv_mfma_f32_16x16x32_bf16 v[44:47], a[48:51], v[84:87], v[44:47]\ns_waitcnt lgkmcnt(3)\nv_mfma_f32_16x16x32_bf16 v[16:19], a[36:39], v[88:91], v[16:19]\nds_read_b128 v[84:87], v2 offset:8448\nv_mfma_f32_16x16x32_bf16 v[20:23], v[76:79], v[88:91], v[20:23]\nds_read_b128 v[68:71], v1 offset:3072\nv_mfma_f32_16x16x32_bf16 v[24:27], a[44:47], v[88:91], v[24:27]\n" \
"v_mfma_f32_16x16x32_bf16 v[28:31], a[52:55], v[88:91], v[28:31]\ns_waitcnt lgkmcnt(4)\nv_mfma_f32_16x16x32_bf16 v[32:35], a[36:39], v[92:95], v[32:35]\nds_read_b128 v[88:91], v2 offset:10240\nv_mfma_f32_16x16x32_bf16 v[36:39], v[76:79], v[92:95], v[36:39]\nv_mfma_f32_16x16x32_bf16 v[40:43], a[44:47], v[92:95], v[40:43]\nv_mfma_f32_16x16x32_bf16 v[44:47], a[52:55], v[92:95], v[44:47]\ns_waitcnt lgkmcnt(3)\n" \
"v_mfma_f32_16x16x32_bf16 v[16:19], a[56:59], v[80:83], v[16:19]\nds_read_b128 v[92:95], v2 offset:10496\nv_mfma_f32_16x16x32_bf16 v[20:23], a[64:67], v[80:83], v[20:23]\nds_read_b128 v[72:75], v1 offset:4096\nv_mfma_f32_16x16x32_bf16 v[24:27], v[64:67], v[80:83], v[24:27]\nv_mfma_f32_16x16x32_bf16 v[28:31], a[72:75], v[80:83], v[28:31]\ns_waitcnt lgkmcnt(4)\nv_mfma_f32_16x16x32_bf16 v[32:35], a[56:59], v[84:87], v[32:35]\n" \
"ds_read_b128 v[80:83], v2 offset:12288\nv_mfma_f32_16x16x32_bf16 v[36:39], a[64:67], v[84:87], v[36:39]\nv_mfma_f32_16x16x32_bf16 v[40:43], v[64:67], v[84:87], v[40:43]\nv_mfma_f32_16x16x32_bf16 v[44:47], a[72:75], v[84:87], v[44:47]\ns_waitcnt lgkmcnt(3)\nv_mfma_f32_16x16x32_bf16 v[16:19], a[60:63], v[88:91], v[16:19]\nds_read_b128 v[84:87], v2 offset:12544\nv_mfma_f32_16x16x32_bf16 v[20:23], a[68:71], v[88:91], v[20:23]\n" \
"ds_read_b128 v[76:79], v1 offset:5120\nv_mfma_f32_16x16x32_bf16 v[24:27], v[68:71], v[88:91], v[24:27]\nv_mfma_f32_16x16x32_bf16 v[28:31], a[76:79], v[88:91], v[28:31]\ns_waitcnt lgkmcnt(4)\nv_mfma_f32_16x16x32_bf16 v[32:35], a[60:63], v[92:95], v[32:35]\nds_read_b128 v[88:91], v2 offset:14336\nv_mfma_f32_16x16x32_bf16 v[36:39], a[68:71], v[92:95], v[36:39]\nv_mfma_f32_16x16x32_bf16 v[40:43], v[68:71], v[92:95], v[40:43]\n" \
"v_mfma_f32_16x16x32_bf16 v[44:47], a[76:79], v[92:95], v[44:47]\n.Lqk_pj%=:\ns_waitcnt lgkmcnt(3)\nv_mfma_f32_16x16x32_bf16 v[16:19], a[80:83], v[80:83], v[16:19]\nds_read_b128 v[92:95], v2 offset:14592\nv_mfma_f32_16x16x32_bf16 v[20:23], a[88:91], v[80:83], v[20:23]\ns_add_u32 s40, s40, 1\nv_mfma_f32_16x16x32_bf16 v[24:27], a[96:99], v[80:83], v[24:27]\n" \
"s_lshl_b32 s44, s40, 17\nv_mfma_f32_16x16x32_bf16 v[28:31], v[72:75], v[80:83], v[28:31]\ns_add_u32 s44, s44, %[xn]\ns_waitcnt lgkmcnt(3)\nv_mfma_f32_16x16x32_bf16 v[32:35], a[80:83], v[84:87], v[32:35]\ns_barrier\nds_read_b128 v[80:83], v2 offset:16384\nv_mfma_f32_16x16x32_bf16 v[36:39], a[88:91], v[84:87], v[36:39]\n" \
"s_lshr_b32 s45, s44, 8\nv_mfma_f32_16x16x32_bf16 v[40:43], a[96:99], v[84:87], v[40:43]\ns_and_b32 s45, s45, 0x2000\nv_mfma_f32_16x16x32_bf16 v[44:47], v[72:75], v[84:87], v[44:47]\ns_xor_b32 s41, s44, s45\ns_waitcnt lgkmcnt(2)\nv_mfma_f32_16x16x32_bf16 v[16:19], a[84:87], v[88:91], v[16:19]\nds_read_b128 v[84:87], v2 offset:16640\n" \
"v_mfma_f32_16x16x32_bf16 v[20:23], a[92:95], v[88:91], v[20:23]\ns_add_u32 s41, s41, %[xw]\ns_and_b32 s42, s40, 3\ns_add_u32 m0, s57, 32768\nv_mfma_f32_16x16x32_bf16 v[24:27], a[100:103], v[88:91], v[24:27]\ns_add_u32 s60, s41, 65536\nbuffer_load_dwordx4 v0, s[36:39], s60 offen offset:0 sc1 lds\nv_mfma_f32_16x16x32_bf16 v[28:31], v[76:79], v[88:91], v[28:31]\n" \
"s_lshl_b32 s42, s42, 15\ns_waitcnt lgkmcnt(2)\nv_mfma_f32_16x16x32_bf16 v[32:35], a[84:87], v[92:95], v[32:35]\nds_read_b128 v[88:91], v2 offset:18432\nv_mfma_f32_16x16x32_bf16 v[36:39], a[92:95], v[92:95], v[36:39]\ns_and_b32 s44, s40, 1\nv_mfma_f32_16x16x32_bf16 v[40:43], a[100:103], v[92:95], v[40:43]\ns_lshl_b32 s55, s44, 17\n" \
"buffer_load_dwordx4 v0, s[36:39], s60 offen offset:1024 sc1 lds\nv_mfma_f32_16x16x32_bf16 v[44:47], v[76:79], v[92:95], v[44:47]\ns_add_u32 s56, s56, %[fs]\ns_waitcnt lgkmcnt(2)\nv_mfma_f32_16x16x32_bf16 v[16:19], a[104:107], v[80:83], v[16:19]\nds_read_b128 v[92:95], v2 offset:18688\nv_mfma_f32_16x16x32_bf16 v[20:23], a[112:115], v[80:83], v[20:23]\nds_read_b128 v[72:75], v1 offset:6144\n" \
"v_mfma_f32_16x16x32_bf16 v[24:27], a[120:123], v[80:83], v[24:27]\nbuffer_load_dwordx4 v0, s[36:39], s60 offen offset:2048 sc1 lds\nv_mfma_f32_16x16x32_bf16 v[28:31], a[128:131], v[80:83], v[28:31]\ns_waitcnt lgkmcnt(3)\nv_mfma_f32_16x16x32_bf16 v[32:35], a[104:107], v[84:87], v[32:35]\nds_read_b128 v[80:83], v2 offset:20480\nv_mfma_f32_16x16x32_bf16 v[36:39], a[112:115], v[84:87], v[36:39]\nv_mfma_f32_16x16x32_bf16 v[40:43], a[120:123], v[84:87], v[40:43]\n" \
"buffer_load_dwordx4 v0, s[36:39], s60 offen offset:3072 sc1 lds\nv_mfma_f32_16x16x32_bf16 v[44:47], a[128:131], v[84:87], v[44:47]\ns_waitcnt lgkmcnt(3)\nv_mfma_f32_16x16x32_bf16 v[16:19], a[108:111], v[88:91], v[16:19]\nds_read_b128 v[84:87], v2 offset:20736\nv_mfma_f32_16x16x32_bf16 v[20:23], a[116:119], v[88:91], v[20:23]\nds_read_b128 v[76:79], v1 offset:7168\nv_mfma_f32_16x16x32_bf16 v[24:27], a[124:127], v[88:91], v[24:27]\n" \
"v_mfma_f32_16x16x32_bf16 v[28:31], a[132:135], v[88:91], v[28:31]\ns_waitcnt lgkmcnt(4)\nv_mfma_f32_16x16x32_bf16 v[32:35], a[108:111], v[92:95], v[32:35]\nds_read_b128 v[88:91], v2 offset:22528\nv_mfma_f32_16x16x32_bf16 v[36:39], a[116:119], v[92:95], v[36:39]\nv_mfma_f32_16x16x32_bf16 v[40:43], a[124:127], v[92:95], v[40:43]\nv_mfma_f32_16x16x32_bf16 v[44:47], a[132:135], v[92:95], v[44:47]\ns_waitcnt lgkmcnt(3)\n" \
"v_mfma_f32_16x16x32_bf16 v[16:19], a[136:139], v[80:83], v[16:19]\nds_read_b128 v[92:95], v2 offset:22784\nv_mfma_f32_16x16x32_bf16 v[20:23], v[72:75], v[80:83], v[20:23]\nds_read_b128 v[64:67], v1 offset:8192\nv_mfma_f32_16x16x32_bf16 v[24:27], a[144:147], v[80:83], v[24:27]\nv_mfma_f32_16x16x32_bf16 v[28:31], a[152:155], v[80:83], v[28:31]\ns_waitcnt lgkmcnt(4)\nv_mfma_f32_16x16x32_bf16 v[32:35], a[136:139], v[84:87], v[32:35]\n" \
"ds_read_b128 v[80:83], v2 offset:24576\nv_mfma_f32_16x16x32_bf16 v[36:39], v[72:75], v[84:87], v[36:39]\nv_mfma_f32_16x16x32_bf16 v[40:43], a[144:147], v[84:87], v[40:43]\nv_mfma_f32_16x16x32_bf16 v[44:47], a[152:155], v[84:87], v[44:47]\ns_waitcnt lgkmcnt(3)\nv_mfma_f32_16x16x32_bf16 v[16:19], a[140:143], v[88:91], v[16:19]\nds_read_b128 v[84:87], v2 offset:24832\nv_mfma_f32_16x16x32_bf16 v[20:23], v[76:79], v[88:91], v[20:23]\n" \
"ds_read_b128 v[68:71], v1 offset:9216\nv_mfma_f32_16x16x32_bf16 v[24:27], a[148:151], v[88:91], v[24:27]\nv_mfma_f32_16x16x32_bf16 v[28:31], a[156:159], v[88:91], v[28:31]\ns_waitcnt lgkmcnt(4)\nv_mfma_f32_16x16x32_bf16 v[32:35], a[140:143], v[92:95], v[32:35]\nds_read_b128 v[88:91], v2 offset:26624\nv_mfma_f32_16x16x32_bf16 v[36:39], v[76:79], v[92:95], v[36:39]\nv_mfma_f32_16x16x32_bf16 v[40:43], a[148:151], v[92:95], v[40:43]\n" \
"v_mfma_f32_16x16x32_bf16 v[44:47], a[156:159], v[92:95], v[44:47]\ns_waitcnt lgkmcnt(3)\nv_mfma_f32_16x16x32_bf16 v[16:19], a[160:163], v[80:83], v[16:19]\nds_read_b128 v[92:95], v2 offset:26880\nv_mfma_f32_16x16x32_bf16 v[20:23], a[168:171], v[80:83], v[20:23]\nds_read_b128 v[72:75], v1 offset:10240\nv_mfma_f32_16x16x32_bf16 v[24:27], v[64:67], v[80:83], v[24:27]\nv_mfma_f32_16x16x32_bf16 v[28:31], a[176:179], v[80:83], v[28:31]\n" \
"s_waitcnt lgkmcnt(4)\nv_mfma_f32_16x16x32_bf16 v[32:35], a[160:163], v[84:87], v[32:35]\nds_read_b128 v[80:83], v2 offset:28672\nv_mfma_f32_16x16x32_bf16 v[36:39], a[168:171], v[84:87], v[36:39]\nv_mfma_f32_16x16x32_bf16 v[40:43], v[64:67], v[84:87], v[40:43]\nv_mfma_f32_16x16x32_bf16 v[44:47], a[176:179], v[84:87], v[44:47]\ns_waitcnt lgkmcnt(3)\nv_mfma_f32_16x16x32_bf16 v[16:19], a[164:167], v[88:91], v[16:19]\n" \
"ds_read_b128 v[84:87], v2 offset:28928\nv_mfma_f32_16x16x32_bf16 v[20:23], a[172:175], v[88:91], v[20:23]\nds_read_b128 v[76:79], v1 offset:11264\nv_mfma_f32_16x16x32_bf16 v[24:27], v[68:71], v[88:91], v[24:27]\nv_mfma_f32_16x16x32_bf16 v[28:31], a[180:183], v[88:91], v[28:31]\ns_waitcnt lgkmcnt(4)\nv_mfma_f32_16x16x32_bf16 v[32:35], a[164:167], v[92:95], v[32:35]\nds_read_b128 v[88:91], v2 offset:30720\n" \
"v_mfma_f32_16x16x32_bf16 v[36:39], a[172:175], v[92:95], v[36:39]\nv_mfma_f32_16x16x32_bf16 v[40:43], v[68:71], v[92:95], v[40:43]\nv_mfma_f32_16x16x32_bf16 v[44:47], a[180:183], v[92:95], v[44:47]\ns_waitcnt lgkmcnt(3)\nv_mfma_f32_16x16x32_bf16 v[16:19], a[184:187], v[80:83], v[16:19]\nds_read_b128 v[92:95], v2 offset:30976\nv_mfma_f32_16x16x32_bf16 v[20:23], a[192:195], v[80:83], v[20:23]\nv_mfma_f32_16x16x32_bf16 v[24:27], a[200:203], v[80:83], v[24:27]\n" \
"v_mfma_f32_16x16x32_bf16 v[28:31], v[72:75], v[80:83], v[28:31]\ns_waitcnt lgkmcnt(3)\nv_mfma_f32_16x16x32_bf16 v[32:35], a[184:187], v[84:87], v[32:35]\ns_waitcnt vmcnt(0)\ns_barrier\nds_read_b128 v[48:51], v2 offset:32768\nv_mfma_f32_16x16x32_bf16 v[36:39], a[192:195], v[84:87], v[36:39]\ns_add_u32 m0, s57, 0\n" \
"v_mfma_f32_16x16x32_bf16 v[40:43], a[200:203], v[84:87], v[40:43]\ns_add_u32 s60, s41, 98304\nbuffer_load_dwordx4 v0, s[36:39], s60 offen offset:0 sc1 lds\nv_mfma_f32_16x16x32_bf16 v[44:47], v[72:75], v[84:87], v[44:47]\ns_waitcnt lgkmcnt(2)\nv_mfma_f32_16x16x32_bf16 v[16:19], a[188:191], v[88:91], v[16:19]\nds_read_b128 v[52:55], v2 offset:33024\nv_mfma_f32_16x16x32_bf16 v[20:23], a[196:199], v[88:91], v[20:23]\n" \
"v_mfma_f32_16x16x32_bf16 v[24:27], a[204:207], v[88:91], v[24:27]\nbuffer_load_dwordx4 v0, s[36:39], s60 offen offset:1024 sc1 lds\nv_mfma_f32_16x16x32_bf16 v[28:31], v[76:79], v[88:91], v[28:31]\ns_waitcnt lgkmcnt(2)\nv_mfma_f32_16x16x32_bf16 v[32:35], a[188:191], v[92:95], v[32:35]\nds_read_b128 v[56:59], v2 offset:34816\nv_mfma_f32_16x16x32_bf16 v[36:39], a[196:199], v[92:95], v[36:39]\nv_mfma_f32_16x16x32_bf16 v[40:43], a[204:207], v[92:95], v[40:43]\n" \
"buffer_load_dwordx4 v0, s[36:39], s60 offen offset:2048 sc1 lds\nv_mfma_f32_16x16x32_bf16 v[44:47], v[76:79], v[92:95], v[44:47]\ns_waitcnt lgkmcnt(2)\nv_mfma_f32_16x16x32_bf16 v[16:19], a[208:211], v[48:51], v[16:19]\nds_read_b128 v[60:63], v2 offset:35072\nv_mfma_f32_16x16x32_bf16 v[20:23], a[216:219], v[48:51], v[20:23]\nds_read_b128 v[72:75], v1 offset:12288\nv_mfma_f32_16x16x32_bf16 v[24:27], a[224:227], v[48:51], v[24:27]\n" \
"buffer_load_dwordx4 v0, s[36:39], s60 offen offset:3072 sc1 lds\nv_mfma_f32_16x16x32_bf16 v[28:31], a[232:235], v[48:51], v[28:31]\ns_waitcnt lgkmcnt(3)\nv_mfma_f32_16x16x32_bf16 v[32:35], a[208:211], v[52:55], v[32:35]\nds_read_b128 v[48:51], v2 offset:36864\nv_mfma_f32_16x16x32_bf16 v[36:39], a[216:219], v[52:55], v[36:39]\nv_mfma_f32_16x16x32_bf16 v[40:43], a[224:227], v[52:55], v[40:43]\nv_mfma_f32_16x16x32_bf16 v[44:47], a[232:235], v[52:55], v[44:47]\n" \
"s_waitcnt lgkmcnt(3)\nv_mfma_f32_16x16x32_bf16 v[16:19], a[212:215], v[56:59], v[16:19]\nds_read_b128 v[52:55], v2 offset:37120\nv_mfma_f32_16x16x32_bf16 v[20:23], a[220:223], v[56:59], v[20:23]\nds_read_b128 v[76:79], v1 offset:13312\nv_mfma_f32_16x16x32_bf16 v[24:27], a[228:231], v[56:59], v[24:27]\nv_mfma_f32_16x16x32_bf16 v[28:31], a[236:239], v[56:59], v[28:31]\ns_waitcnt lgkmcnt(4)\n" \
"v_mfma_f32_16x16x32_bf16 v[32:35], a[212:215], v[60:63], v[32:35]\nds_read_b128 v[56:59], v2 offset:38912\nv_mfma_f32_16x16x32_bf16 v[36:39], a[220:223], v[60:63], v[36:39]\nv_mfma_f32_16x16x32_bf16 v[40:43], a[228:231], v[60:63], v[40:43]\nv_mfma_f32_16x16x32_bf16 v[44:47], a[236:239], v[60:63], v[44:47]\ns_waitcnt lgkmcnt(3)\nv_mfma_f32_16x16x32_bf16 v[16:19], a[240:243], v[48:51], v[16:19]\nds_read_b128 v[60:63], v2 offset:39168\n" \
"v_mfma_f32_16x16x32_bf16 v[20:23], v[72:75], v[48:51], v[20:23]\nds_read_b128 v[64:67], v1 offset:14336\nv_mfma_f32_16x16x32_bf16 v[24:27], a[248:251], v[48:51], v[24:27]\nv_mfma_f32_16x16x32_bf16 v[28:31], v[96:99], v[48:51], v[28:31]\ns_waitcnt lgkmcnt(4)\nv_mfma_f32_16x16x32_bf16 v[32:35], a[240:243], v[52:55], v[32:35]\nds_read_b128 v[48:51], v2 offset:40960\nv_mfma_f32_16x16x32_bf16 v[36:39], v[72:75], v[52:55], v[36:39]\n" \
"v_mfma_f32_16x16x32_bf16 v[40:43], a[248:251], v[52:55], v[40:43]\nv_mfma_f32_16x16x32_bf16 v[44:47], v[96:99], v[52:55], v[44:47]\ns_waitcnt lgkmcnt(3)\nv_mfma_f32_16x16x32_bf16 v[16:19], a[244:247], v[56:59], v[16:19]\nds_read_b128 v[52:55], v2 offset:41216\nv_mfma_f32_16x16x32_bf16 v[20:23], v[76:79], v[56:59], v[20:23]\nds_read_b128 v[68:71], v1 offset:15360\nv_mfma_f32_16x16x32_bf16 v[24:27], a[252:255], v[56:59], v[24:27]\n" \
"v_mfma_f32_16x16x32_bf16 v[28:31], v[100:103], v[56:59], v[28:31]\ns_waitcnt lgkmcnt(4)\nv_mfma_f32_16x16x32_bf16 v[32:35], a[244:247], v[60:63], v[32:35]\nds_read_b128 v[56:59], v2 offset:43008\nv_mfma_f32_16x16x32_bf16 v[36:39], v[76:79], v[60:63], v[36:39]\nv_mfma_f32_16x16x32_bf16 v[40:43], a[252:255], v[60:63], v[40:43]\nv_mfma_f32_16x16x32_bf16 v[44:47], v[100:103], v[60:63], v[44:47]\ns_waitcnt lgkmcnt(3)\n" \
"v_mfma_f32_16x16x32_bf16 v[16:19], v[104:107], v[48:51], v[16:19]\nds_read_b128 v[60:63], v2 offset:43264\nv_mfma_f32_16x16x32_bf16 v[20:23], v[112:115], v[48:51], v[20:23]\nds_read_b128 v[72:75], v1 offset:16384\nv_mfma_f32_16x16x32_bf16 v[24:27], v[64:67], v[48:51], v[24:27]\nv_mfma_f32_16x16x32_bf16 v[28:31], v[120:123], v[48:51], v[28:31]\ns_waitcnt lgkmcnt(4)\nv_mfma_f32_16x16x32_bf16 v[32:35], v[104:107], v[52:55], v[32:35]\n" \
"ds_read_b128 v[48:51], v2 offset:45056\nv_mfma_f32_16x16x32_bf16 v[36:39], v[112:115], v[52:55], v[36:39]\nv_mfma_f32_16x16x32_bf16 v[40:43], v[64:67], v[52:55], v[40:43]\nv_mfma_f32_16x16x32_bf16 v[44:47], v[120:123], v[52:55], v[44:47]\ns_waitcnt lgkmcnt(3)\nv_mfma_f32_16x16x32_bf16 v[16:19], v[108:111], v[56:59], v[16:19]\nds_read_b128 v[52:55], v2 offset:45312\nv_mfma_f32_16x16x32_bf16 v[20:23], v[116:119], v[56:59], v[20:23]\n" \
"ds_read_b128 v[76:79], v1 offset:17408\nv_mfma_f32_16x16x32_bf16 v[24:27], v[68:71], v[56:59], v[24:27]\nv_mfma_f32_16x16x32_bf16 v[28:31], v[124:127], v[56:59], v[28:31]\ns_waitcnt lgkmcnt(4)\nv_mfma_f32_16x16x32_bf16 v[32:35], v[108:111], v[60:63], v[32:35]\nds_read_b128 v[56:59], v2 offset:47104\nv_mfma_f32_16x16x32_bf16 v[36:39], v[116:119], v[60:63], v[36:39]\nv_mfma_f32_16x16x32_bf16 v[40:43], v[68:71], v[60:63], v[40:43]\n" \
"v_mfma_f32_16x16x32_bf16 v[44:47], v[124:127], v[60:63], v[44:47]\ns_waitcnt lgkmcnt(3)\nv_mfma_f32_16x16x32_bf16 v[16:19], v[128:131], v[48:51], v[16:19]\nds_read_b128 v[60:63], v2 offset:47360\nv_mfma_f32_16x16x32_bf16 v[20:23], v[136:139], v[48:51], v[20:23]\nv_mfma_f32_16x16x32_bf16 v[24:27], v[144:147], v[48:51], v[24:27]\nv_mfma_f32_16x16x32_bf16 v[28:31], v[72:75], v[48:51], v[28:31]\ns_waitcnt lgkmcnt(3)\n" \
"v_mfma_f32_16x16x32_bf16 v[32:35], v[128:131], v[52:55], v[32:35]\ns_waitcnt lgkmcnt(0)\ns_waitcnt vmcnt(0)\ns_barrier\nds_read_b128 v[48:51], v2 offset:0\nv_mfma_f32_16x16x32_bf16 v[36:39], v[136:139], v[52:55], v[36:39]\nv_mfma_f32_16x16x32_bf16 v[40:43], v[144:147], v[52:55], v[40:43]\nv_mfma_f32_16x16x32_bf16 v[44:47], v[72:75], v[52:55], v[44:47]\n" \
"v_mfma_f32_16x16x32_bf16 v[16:19], v[132:135], v[56:59], v[16:19]\nds_read_b128 v[52:55], v2 offset:256\nv_mfma_f32_16x16x32_bf16 v[20:23], v[140:143], v[56:59], v[20:23]\nv_mfma_f32_16x16x32_bf16 v[24:27], v[148:151], v[56:59], v[24:27]\nv_mfma_f32_16x16x32_bf16 v[28:31], v[76:79], v[56:59], v[28:31]\nv_mfma_f32_16x16x32_bf16 v[32:35], v[132:135], v[60:63], v[32:35]\nds_read_b128 v[56:59], v2 offset:2048\nv_mfma_f32_16x16x32_bf16 v[36:39], v[140:143], v[60:63], v[36:39]\n" \
"v_mfma_f32_16x16x32_bf16 v[40:43], v[148:151], v[60:63], v[40:43]\nv_mfma_f32_16x16x32_bf16 v[44:47], v[76:79], v[60:63], v[44:47]\ns_waitcnt lgkmcnt(2)\nv_mfma_f32_16x16x32_bf16 v[16:19], v[152:155], v[48:51], v[16:19]\nds_read_b128 v[60:63], v2 offset:2304\nv_mfma_f32_16x16x32_bf16 v[20:23], v[160:163], v[48:51], v[20:23]\nds_read_b128 v[72:75], v1 offset:18432\nv_mfma_f32_16x16x32_bf16 v[24:27], v[168:171], v[48:51], v[24:27]\n" \
"v_mfma_f32_16x16x32_bf16 v[28:31], v[176:179], v[48:51], v[28:31]\ns_waitcnt lgkmcnt(3)\nv_mfma_f32_16x16x32_bf16 v[32:35], v[152:155], v[52:55], v[32:35]\nds_read_b128 v[48:51], v2 offset:4096\nv_mfma_f32_16x16x32_bf16 v[36:39], v[160:163], v[52:55], v[36:39]\nv_mfma_f32_16x16x32_bf16 v[40:43], v[168:171], v[52:55], v[40:43]\nv_mfma_f32_16x16x32_bf16 v[44:47], v[176:179], v[52:55], v[44:47]\ns_waitcnt lgkmcnt(3)\n" \
"v_mfma_f32_16x16x32_bf16 v[16:19], v[156:159], v[56:59], v[16:19]\nds_read_b128 v[52:55], v2 offset:4352\nv_mfma_f32_16x16x32_bf16 v[20:23], v[164:167], v[56:59], v[20:23]\nds_read_b128 v[76:79], v1 offset:19456\nv_mfma_f32_16x16x32_bf16 v[24:27], v[172:175], v[56:59], v[24:27]\nv_mfma_f32_16x16x32_bf16 v[28:31], v[180:183], v[56:59], v[28:31]\ns_waitcnt lgkmcnt(4)\nv_mfma_f32_16x16x32_bf16 v[32:35], v[156:159], v[60:63], v[32:35]\n" \
"ds_read_b128 v[56:59], v2 offset:6144\nv_mfma_f32_16x16x32_bf16 v[36:39], v[164:167], v[60:63], v[36:39]\nv_mfma_f32_16x16x32_bf16 v[40:43], v[172:175], v[60:63], v[40:43]\nv_mfma_f32_16x16x32_bf16 v[44:47], v[180:183], v[60:63], v[44:47]\ns_waitcnt lgkmcnt(3)\nv_mfma_f32_16x16x32_bf16 v[16:19], v[184:187], v[48:51], v[16:19]\nds_read_b128 v[60:63], v2 offset:6400\nv_mfma_f32_16x16x32_bf16 v[20:23], v[72:75], v[48:51], v[20:23]\n" \
"ds_read_b128 v[64:67], v1 offset:20480\nv_mfma_f32_16x16x32_bf16 v[24:27], v[192:195], v[48:51], v[24:27]\nv_mfma_f32_16x16x32_bf16 v[28:31], v[200:203], v[48:51], v[28:31]\ns_waitcnt lgkmcnt(4)\nv_mfma_f32_16x16x32_bf16 v[32:35], v[184:187], v[52:55], v[32:35]\nds_read_b128 v[48:51], v2 offset:8192\nv_mfma_f32_16x16x32_bf16 v[36:39], v[72:75], v[52:55], v[36:39]\nv_mfma_f32_16x16x32_bf16 v[40:43], v[192:195], v[52:55], v[40:43]\n" \
"v_mfma_f32_16x16x32_bf16 v[44:47], v[200:203], v[52:55], v[44:47]\ns_waitcnt lgkmcnt(3)\nv_mfma_f32_16x16x32_bf16 v[16:19], v[188:191], v[56:59], v[16:19]\nds_read_b128 v[52:55], v2 offset:10240\nv_mfma_f32_16x16x32_bf16 v[20:23], v[76:79], v[56:59], v[20:23]\nds_read_b128 v[68:71], v1 offset:21504\nv_mfma_f32_16x16x32_bf16 v[24:27], v[196:199], v[56:59], v[24:27]\nbuffer_load_dwordx4 v[80:83], v3, s[36:39], s42 offen offset:0 sc1\n" \
"v_mfma_f32_16x16x32_bf16 v[28:31], v[204:207], v[56:59], v[28:31]\ns_waitcnt lgkmcnt(4)\nv_mfma_f32_16x16x32_bf16 v[32:35], v[188:191], v[60:63], v[32:35]\nds_read_b128 v[56:59], v2 offset:12288\nv_mfma_f32_16x16x32_bf16 v[36:39], v[76:79], v[60:63], v[36:39]\nds_read_b128 v[72:75], v1 offset:22528\nv_mfma_f32_16x16x32_bf16 v[40:43], v[196:199], v[60:63], v[40:43]\nbuffer_load_dwordx4 v[84:87], v3, s[36:39], s42 offen offset:1024 sc1\n" \
"v_mfma_f32_16x16x32_bf16 v[44:47], v[204:207], v[60:63], v[44:47]\ns_waitcnt lgkmcnt(4)\nv_mfma_f32_16x16x32_bf16 v[16:19], v[208:211], v[48:51], v[16:19]\nds_read_b128 v[60:63], v2 offset:14336\nv_mfma_f32_16x16x32_bf16 v[20:23], v[216:219], v[48:51], v[20:23]\nds_read_b128 v[76:79], v1 offset:23552\nv_mfma_f32_16x16x32_bf16 v[24:27], v[64:67], v[48:51], v[24:27]\nbuffer_load_dwordx4 v[88:91], v3, s[36:39], s42 offen offset:2048 sc1\n" \
"v_mfma_f32_16x16x32_bf16 v[28:31], v[224:227], v[48:51], v[28:31]\ns_waitcnt lgkmcnt(5)\nv_mfma_f32_16x16x32_bf16 v[16:19], v[212:215], v[52:55], v[16:19]\nds_read_b128 v[48:51], v2 offset:8448\nv_mfma_f32_16x16x32_bf16 v[20:23], v[220:223], v[52:55], v[20:23]\ns_waitcnt lgkmcnt(5)\nv_mfma_f32_16x16x32_bf16 v[24:27], v[68:71], v[52:55], v[24:27]\nbuffer_load_dwordx4 v[92:95], v3, s[36:39], s42 offen offset:3072 sc1\n" \
"v_mfma_f32_16x16x32_bf16 v[28:31], v[228:231], v[52:55], v[28:31]\ns_waitcnt lgkmcnt(4)\nv_mfma_f32_16x16x32_bf16 v[16:19], v[232:235], v[56:59], v[16:19]\nds_read_b128 v[52:55], v2 offset:10496\nv_mfma_f32_16x16x32_bf16 v[20:23], v[240:243], v[56:59], v[20:23]\nv_mfma_f32_16x16x32_bf16 v[24:27], v[248:251], v[56:59], v[24:27]\ns_waitcnt lgkmcnt(4)\nv_mfma_f32_16x16x32_bf16 v[28:31], v[72:75], v[56:59], v[28:31]\n" \
"s_waitcnt lgkmcnt(3)\nv_mfma_f32_16x16x32_bf16 v[16:19], v[236:239], v[60:63], v[16:19]\nds_read_b128 v[56:59], v2 offset:12544\nv_mfma_f32_16x16x32_bf16 v[20:23], v[244:247], v[60:63], v[20:23]\nv_mfma_f32_16x16x32_bf16 v[24:27], v[252:255], v[60:63], v[24:27]\ns_waitcnt lgkmcnt(3)\nv_mfma_f32_16x16x32_bf16 v[28:31], v[76:79], v[60:63], v[28:31]\ns_waitcnt lgkmcnt(2)\n" \
"v_mfma_f32_16x16x32_bf16 v[32:35], v[208:211], v[48:51], v[32:35]\nds_read_b128 v[60:63], v2 offset:14592\nv_mfma_f32_16x16x32_bf16 v[36:39], v[216:219], v[48:51], v[36:39]\nv_cvt_pk_bf16_f32 v20, v16, v20\nv_cvt_pk_bf16_f32 v21, v17, v21\nv_mfma_f32_16x16x32_bf16 v[40:43], v[64:67], v[48:51], v[40:43]\nv_cvt_pk_bf16_f32 v22, v18, v22\nv_cvt_pk_bf16_f32 v23, v19, v23\n" \
"v_mfma_f32_16x16x32_bf16 v[44:47], v[224:227], v[48:51], v[44:47]\ns_waitcnt lgkmcnt(2)\nds_write_b128 v5, v[20:23] offset:0\nv_mfma_f32_16x16x32_bf16 v[32:35], v[212:215], v[52:55], v[32:35]\nv_cvt_pk_bf16_f32 v28, v24, v28\nv_cvt_pk_bf16_f32 v29, v25, v29\nv_mfma_f32_16x16x32_bf16 v[36:39], v[220:223], v[52:55], v[36:39]\nv_cvt_pk_bf16_f32 v30, v26, v30\n" \
"v_cvt_pk_bf16_f32 v31, v27, v31\nv_mfma_f32_16x16x32_bf16 v[40:43], v[68:71], v[52:55], v[40:43]\nv_mov_b64 v[48:49], 0\nv_mov_b64 v[50:51], 0\nv_mfma_f32_16x16x32_bf16 v[44:47], v[228:231], v[52:55], v[44:47]\ns_cmp_eq_u32 %[w], 1\nds_write_b128 v5, v[28:31] offset:512\ns_cbranch_scc1 .Lqk_c1%=\n" \
"s_cmp_eq_u32 %[w], 2\ns_cbranch_scc1 .Lqk_c2%=\ns_cmp_eq_u32 %[w], 3\ns_cbranch_scc1 .Lqk_c3%=\n.Lqk_c0%=:\ns_waitcnt lgkmcnt(3)\nv_mfma_f32_16x16x32_bf16 v[32:35], v[232:235], v[56:59], v[32:35]\nv_mov_b64 v[52:53], 0\n" \
"v_mov_b64 v[54:55], 0\nv_mfma_f32_16x16x32_bf16 v[36:39], v[240:243], v[56:59], v[36:39]\ns_waitcnt lgkmcnt(2)\nds_read_b128 v[64:67], v1 offset:0\nv_mfma_f32_16x16x32_bf16 v[32:35], v[236:239], v[60:63], v[32:35]\nds_read_b128 v[68:71], v1 offset:1024\nv_mfma_f32_16x16x32_bf16 v[36:39], v[244:247], v[60:63], v[36:39]\ns_bfe_i32 s54, s40, 0x10000\n" \
"v_mfma_f32_16x16x32_bf16 v[40:43], v[248:251], v[56:59], v[40:43]\nv_mfma_f32_16x16x32_bf16 v[44:47], v[72:75], v[56:59], v[44:47]\nv_cvt_pk_bf16_f32 v36, v32, v36\nv_cvt_pk_bf16_f32 v37, v33, v37\nv_mfma_f32_16x16x32_bf16 v[40:43], v[252:255], v[60:63], v[40:43]\nv_cvt_pk_bf16_f32 v38, v34, v38\nv_cvt_pk_bf16_f32 v39, v35, v39\nv_mfma_f32_16x16x32_bf16 v[44:47], v[76:79], v[60:63], v[44:47]\n" \
"ds_read_b128 v[72:75], v1 offset:2048\nds_read_b128 v[76:79], v1 offset:3072\nv_mov_b64 v[56:57], 0\nv_mov_b64 v[58:59], 0\nv_mov_b64 v[60:61], 0\nv_mov_b64 v[62:63], 0\n.Lqk_poll0%=:\ns_waitcnt vmcnt(0)\n" \
"v_smfmac_f32_16x16x64_bf16 v[48:51], v[80:83], a[0:7], v4\nds_write_b128 v0, v[80:83] offset:16384\nv_max3_u32 v16, v80, v82, v84\nv_smfmac_f32_16x16x64_bf16 v[52:55], v[80:83], a[8:15], v4\nv_max3_u32 v16, v16, v86, v88\nv_smfmac_f32_16x16x64_bf16 v[56:59], v[80:83], a[16:23], v4\nds_write_b128 v0, v[84:87] offset:17408\nv_max3_u32 v16, v16, v90, v92\n" \
"v_max_u32 v16, v16, v94\nv_smfmac_f32_16x16x64_bf16 v[60:63], v[80:83], a[24:31], v4\nv_cmp_eq_u32 vcc, -1, v16\nv_smfmac_f32_16x16x64_bf16 v[48:51], v[84:87], a[32:39], v4\nds_write_b128 v0, v[88:91] offset:18432\ns_cbranch_vccnz .Lqk_re0%=\ns_waitcnt lgkmcnt(5)\nv_smfmac_f32_16x16x64_bf16 v[52:55], v[84:87], v[64:71], v4\n" \
"v_smfmac_f32_16x16x64_bf16 v[56:59], v[84:87], a[40:47], v4\nds_write_b128 v0, v[92:95] offset:19456\nv_smfmac_f32_16x16x64_bf16 v[60:63], v[84:87], a[48:55], v4\nv_smfmac_f32_16x16x64_bf16 v[48:51], v[88:91], a[56:63], v4\nv_smfmac_f32_16x16x64_bf16 v[52:55], v[88:91], a[64:71], v4\ns_waitcnt lgkmcnt(0)\n.Lqk_jn0%=:\ns_barrier\n" \
"s_memrealtime s[68:69]\nds_read_b128 v[80:83], v0 offset:20480\nds_read_b128 v[64:67], v1 offset:4096\nds_read_b128 v[68:71], v1 offset:5120\nv_mov_b32 v24, s40\nv_smfmac_f32_16x16x64_bf16 v[56:59], v[88:91], v[72:79], v4\nds_read_b128 v[72:75], v1 offset:6144\nds_read_b128 v[76:79], v1 offset:7168\n" \
"v_smfmac_f32_16x16x64_bf16 v[60:63], v[88:91], a[72:79], v4\nds_read_b128 v[84:87], v0 offset:21504\nds_read_b128 v[88:91], v0 offset:22528\nv_smfmac_f32_16x16x64_bf16 v[48:51], v[92:95], a[80:87], v4\nds_write_b128 v5, v[36:39] offset:256\nv_cvt_pk_bf16_f32 v44, v40, v44\nv_cvt_pk_bf16_f32 v45, v41, v45\nv_cvt_pk_bf16_f32 v46, v42, v46\n" \
"v_cvt_pk_bf16_f32 v47, v43, v47\nv_smfmac_f32_16x16x64_bf16 v[52:55], v[92:95], a[88:95], v4\nv_smfmac_f32_16x16x64_bf16 v[56:59], v[92:95], a[96:103], v4\ns_waitcnt lgkmcnt(5)\nv_smfmac_f32_16x16x64_bf16 v[60:63], v[92:95], v[64:71], v4\ns_sub_u32 s88, s86, s89\nds_read_b128 v[92:95], v0 offset:23552\nds_read_b128 v[64:67], v1 offset:8192\n" \
"ds_read_b128 v[68:71], v1 offset:9216\nv_smfmac_f32_16x16x64_bf16 v[48:51], v[80:83], a[104:111], v4\ns_min_u32 s88, s88, 150\nds_write_b128 v5, v[44:47] offset:768\nv_smfmac_f32_16x16x64_bf16 v[52:55], v[80:83], a[112:119], v4\ns_add_u32 s88, s88, s68\ns_mov_b32 s89, s68\nv_mov_b32 v25, s88\n" \
"v_smfmac_f32_16x16x64_bf16 v[56:59], v[80:83], a[120:127], v4\nbuffer_store_dwordx2 v[24:25], off, s[36:39], %[sg] sc1\nv_smfmac_f32_16x16x64_bf16 v[60:63], v[80:83], a[128:135], v4\ns_waitcnt lgkmcnt(6)\nds_read_b128 v[80:83], v0 offset:24576\nv_smfmac_f32_16x16x64_bf16 v[48:51], v[84:87], a[136:143], v4\nv_smfmac_f32_16x16x64_bf16 v[52:55], v[84:87], v[72:79], v4\nds_read_b128 v[72:75], v1 offset:10240\n" \
"ds_read_b128 v[76:79], v1 offset:11264\nv_smfmac_f32_16x16x64_bf16 v[56:59], v[84:87], a[144:151], v4\nv_smfmac_f32_16x16x64_bf16 v[60:63], v[84:87], a[152:159], v4\ns_waitcnt lgkmcnt(8)\nds_read_b128 v[84:87], v0 offset:25600\nv_smfmac_f32_16x16x64_bf16 v[48:51], v[88:91], a[160:167], v4\ns_waitcnt lgkmcnt(4)\ns_barrier\n" \
"v_smfmac_f32_16x16x64_bf16 v[52:55], v[88:91], a[168:175], v4\nv_smfmac_f32_16x16x64_bf16 v[56:59], v[88:91], v[64:71], v4\nds_read_b128 v[64:67], v1 offset:12288\nds_read_b128 v[68:71], v1 offset:13312\nv_smfmac_f32_16x16x64_bf16 v[60:63], v[88:91], a[176:183], v4\nds_read_b128 v[88:91], v0 offset:26624\nv_smfmac_f32_16x16x64_bf16 v[48:51], v[92:95], a[184:191], v4\nds_read_b128 v[16:19], v7 offset:0\n" \
"v_smfmac_f32_16x16x64_bf16 v[52:55], v[92:95], a[192:199], v4\nv_smfmac_f32_16x16x64_bf16 v[56:59], v[92:95], a[200:207], v4\ns_waitcnt lgkmcnt(5)\nds_read_b128 v[20:23], v7 offset:64\nv_smfmac_f32_16x16x64_bf16 v[60:63], v[92:95], v[72:79], v4\nds_read_b128 v[92:95], v0 offset:27648\nds_read_b128 v[72:75], v1 offset:14336\nds_read_b128 v[76:79], v1 offset:15360\n" \
"v_smfmac_f32_16x16x64_bf16 v[48:51], v[80:83], a[208:215], v4\nds_read_b128 v[24:27], v7 offset:2048\nv_smfmac_f32_16x16x64_bf16 v[52:55], v[80:83], a[216:223], v4\nv_smfmac_f32_16x16x64_bf16 v[56:59], v[80:83], a[224:231], v4\nds_read_b128 v[28:31], v7 offset:2112\nv_smfmac_f32_16x16x64_bf16 v[60:63], v[80:83], a[232:239], v4\ns_waitcnt lgkmcnt(10)\nds_read_b128 v[80:83], v0 offset:28672\n" \
"v_smfmac_f32_16x16x64_bf16 v[48:51], v[84:87], a[240:247], v4\ns_waitcnt lgkmcnt(9)\nds_read_b128 v[32:35], v7 offset:4096\nv_smfmac_f32_16x16x64_bf16 v[52:55], v[84:87], v[64:71], v4\nds_read_b128 v[64:67], v1 offset:16384\nds_read_b128 v[68:71], v1 offset:17408\nv_smfmac_f32_16x16x64_bf16 v[56:59], v[84:87], a[248:255], v4\nds_read_b128 v[36:39], v7 offset:4160\n" \
"v_smfmac_f32_16x16x64_bf16 v[60:63], v[84:87], v[96:103], v4\ns_waitcnt lgkmcnt(12)\nds_read_b128 v[84:87], v0 offset:29696\nv_smfmac_f32_16x16x64_bf16 v[48:51], v[88:91], v[104:111], v4\nds_read_b128 v[40:43], v7 offset:6144\nv_smfmac_f32_16x16x64_bf16 v[52:55], v[88:91], v[112:119], v4\ns_waitcnt lgkmcnt(9)\nv_smfmac_f32_16x16x64_bf16 v[56:59], v[88:91], v[72:79], v4\n" \
"ds_read_b128 v[72:75], v1 offset:18432\nds_read_b128 v[76:79], v1 offset:19456\nv_smfmac_f32_16x16x64_bf16 v[60:63], v[88:91], v[120:127], v4\nds_read_b128 v[88:91], v0 offset:30720\nv_smfmac_f32_16x16x64_bf16 v[48:51], v[92:95], v[128:135], v4\nds_read_b128 v[44:47], v7 offset:6208\nv_smfmac_f32_16x16x64_bf16 v[52:55], v[92:95], v[136:143], v4\nv_smfmac_f32_16x16x64_bf16 v[56:59], v[92:95], v[144:151], v4\n" \
"s_waitcnt lgkmcnt(7)\nv_smfmac_f32_16x16x64_bf16 v[60:63], v[92:95], v[64:71], v4\nds_read_b128 v[92:95], v0 offset:31744\nds_read_b128 v[64:67], v1 offset:20480\nds_read_b128 v[68:71], v1 offset:21504\nv_smfmac_f32_16x16x64_bf16 v[48:51], v[80:83], v[152:159], v4\nv_smfmac_f32_16x16x64_bf16 v[52:55], v[80:83], v[160:167], v4\nv_smfmac_f32_16x16x64_bf16 v[56:59], v[80:83], v[168:175], v4\n" \
"v_smfmac_f32_16x16x64_bf16 v[60:63], v[80:83], v[176:183], v4\ns_waitcnt lgkmcnt(8)\nv_smfmac_f32_16x16x64_bf16 v[48:51], v[84:87], v[184:191], v4\ns_waitcnt lgkmcnt(5)\nv_smfmac_f32_16x16x64_bf16 v[52:55], v[84:87], v[72:79], v4\nds_read_b128 v[72:75], v1 offset:22528\nds_read_b128 v[76:79], v1 offset:23552\nv_smfmac_f32_16x16x64_bf16 v[56:59], v[84:87], v[192:199], v4\n" \
"v_smfmac_f32_16x16x64_bf16 v[60:63], v[84:87], v[200:207], v4\ns_waitcnt lgkmcnt(6)\nv_smfmac_f32_16x16x64_bf16 v[48:51], v[88:91], v[208:215], v4\nv_smfmac_f32_16x16x64_bf16 v[52:55], v[88:91], v[216:223], v4\ns_waitcnt lgkmcnt(4)\nv_smfmac_f32_16x16x64_bf16 v[48:51], v[92:95], v[232:239], v4\nv_smfmac_f32_16x16x64_bf16 v[52:55], v[92:95], v[240:247], v4\ns_waitcnt lgkmcnt(2)\n" \
"v_smfmac_f32_16x16x64_bf16 v[56:59], v[88:91], v[64:71], v4\nv_add_f32 v48, v48, v50\nv_smfmac_f32_16x16x64_bf16 v[60:63], v[88:91], v[224:231], v4\nv_add_f32 v52, v52, v54\nv_add_f32 v49, v49, v51\nv_smfmac_f32_16x16x64_bf16 v[56:59], v[92:95], v[248:255], v4\ns_waitcnt lgkmcnt(0)\nv_add_f32 v53, v53, v55\n" \
"v_cvt_pk_bf16_f32 v50, v48, v52\nv_smfmac_f32_16x16x64_bf16 v[60:63], v[92:95], v[72:79], v4\nv_cvt_pk_bf16_f32 v51, v49, v53\nds_write2_b32 v6, v50, v51 offset0:0 offset1:4\ns_nop 1\nv_add_f32 v56, v56, v58\nv_add_f32 v57, v57, v59\ns_nop 1\n" \
"v_add_f32 v60, v60, v62\nv_add_f32 v61, v61, v63\nv_cvt_pk_bf16_f32 v62, v56, v60\nv_cvt_pk_bf16_f32 v63, v57, v61\nds_write2_b32 v6, v62, v63 offset0:36 offset1:40\nv_mov_b32 v61, s54\ns_waitcnt lgkmcnt(0)\ns_barrier\n" \
"ds_read_b128 v[80:83], v8 offset:0\nds_read_b128 v[84:87], v8 offset:576\nds_read_b128 v[88:91], v8 offset:1152\nds_read_b128 v[92:95], v8 offset:1728\nds_read_b128 v[64:67], v9 offset:0\nds_read_b128 v[68:71], v9 offset:576\nds_read_b128 v[72:75], v9 offset:1152\nds_read_b128 v[76:79], v9 offset:1728\n" \
"ds_read_b32 v60, v0 offset:53760\ns_waitcnt lgkmcnt(8)\nv_mfma_f32_16x16x32_bf16 v[52:55], v[16:19], v[80:83], 0\nv_mfma_f32_16x16x32_bf16 v[56:59], v[20:23], v[80:83], 0\ns_waitcnt lgkmcnt(7)\nv_mfma_f32_16x16x32_bf16 v[52:55], v[24:27], v[84:87], v[52:55]\nv_mfma_f32_16x16x32_bf16 v[56:59], v[28:31], v[84:87], v[56:59]\ns_waitcnt lgkmcnt(6)\n" \
"v_mfma_f32_16x16x32_bf16 v[52:55], v[32:35], v[88:91], v[52:55]\nv_mfma_f32_16x16x32_bf16 v[56:59], v[36:39], v[88:91], v[56:59]\ns_waitcnt lgkmcnt(5)\nv_mfma_f32_16x16x32_bf16 v[52:55], v[40:43], v[92:95], v[52:55]\nv_mfma_f32_16x16x32_bf16 v[56:59], v[44:47], v[92:95], v[56:59]\ns_waitcnt lgkmcnt(4)\nv_mfma_f32_16x16x32_bf16 v[48:51], v[64:67], v[80:83], 0\ns_waitcnt lgkmcnt(3)\n" \
"v_mfma_f32_16x16x32_bf16 v[48:51], v[68:71], v[84:87], v[48:51]\ns_waitcnt lgkmcnt(2)\nds_read_b128 v[80:83], v2 offset:0\nv_mfma_f32_16x16x32_bf16 v[48:51], v[72:75], v[88:91], v[48:51]\ns_waitcnt lgkmcnt(2)\nds_read_b128 v[84:87], v2 offset:256\nv_mfma_f32_16x16x32_bf16 v[48:51], v[76:79], v[92:95], v[48:51]\nds_read_b128 v[88:91], v2 offset:2048\n" \
"v_max3_f32 v62, v52, v53, v54\nv_max3_f32 v63, v55, v56, v57\nv_max3_f32 v62, v62, v58, v59\nv_cndmask_b32 v52, v52, v56, s[62:63]\nv_cndmask_b32 v53, v53, v57, s[62:63]\nv_cndmask_b32 v54, v54, v58, s[62:63]\nv_cndmask_b32 v55, v55, v59, s[62:63]\nv_max_f32 v62, v62, v63\n" \
"v_max3_f32 v63, v62, v48, v49\nv_max3_f32 v62, v62, v48, v49\ns_waitcnt lgkmcnt(2)\nv_mfma_f32_16x16x32_bf16 v[16:19], a[0:3], v[80:83], 0\nds_read_b128 v[92:95], v2 offset:2304\nv_permlane16_swap_b32 v62, v63\nv_mfma_f32_16x16x32_bf16 v[20:23], a[8:11], v[80:83], 0\nds_read_b128 v[72:75], v1 offset:0\n" \
"v_max_f32 v50, v62, v63\nv_max_f32 v62, v62, v63\nv_mfma_f32_16x16x32_bf16 v[24:27], a[16:19], v[80:83], 0\ns_nop 0\nv_permlane32_swap_b32 v62, v50\nv_max_f32 v62, v62, v50\nv_mfma_f32_16x16x32_bf16 v[28:31], a[24:27], v[80:83], 0\ns_waitcnt lgkmcnt(3)\n" \
"v_mul_f32 v62, s59, v62\nv_fma_f32 v52, v52, s58, v62\nv_fma_f32 v53, v53, s58, v62\nv_mfma_f32_16x16x32_bf16 v[32:35], a[0:3], v[84:87], 0\nds_read_b128 v[80:83], v2 offset:4096\nv_exp_f32 v52, v52\nv_mfma_f32_16x16x32_bf16 v[36:39], a[8:11], v[84:87], 0\nv_fma_f32 v54, v54, s58, v62\n" \
"v_exp_f32 v53, v53\nv_mfma_f32_16x16x32_bf16 v[40:43], a[16:19], v[84:87], 0\nv_fma_f32 v55, v55, s58, v62\nv_exp_f32 v54, v54\nv_mfma_f32_16x16x32_bf16 v[44:47], a[24:27], v[84:87], 0\ns_waitcnt lgkmcnt(3)\nv_add_f32 v63, v52, v53\nv_fma_f32 v48, v48, s58, v62\n" \
"v_add_f32 v63, v63, v54\nv_mfma_f32_16x16x32_bf16 v[16:19], a[4:7], v[88:91], v[16:19]\nds_read_b128 v[84:87], v2 offset:4352\nv_exp_f32 v55, v55\nv_mfma_f32_16x16x32_bf16 v[20:23], a[12:15], v[88:91], v[20:23]\nds_read_b128 v[76:79], v1 offset:1024\nv_fma_f32 v49, v49, s58, v62\nv_add_f32 v63, v63, v55\n" \
"v_mfma_f32_16x16x32_bf16 v[24:27], a[20:23], v[88:91], v[24:27]\nv_exp_f32 v48, v48\nv_mfma_f32_16x16x32_bf16 v[28:31], a[28:31], v[88:91], v[28:31]\ns_waitcnt lgkmcnt(4)\nv_exp_f32 v49, v49\nv_add_f32 v50, v48, v63\nv_mfma_f32_16x16x32_bf16 v[32:35], a[4:7], v[92:95], v[32:35]\nds_read_b128 v[88:91], v2 offset:6144\n" \
"v_add_f32 v50, v50, v49\nv_mfma_f32_16x16x32_bf16 v[36:39], a[12:15], v[92:95], v[36:39]\ns_nop 0\nv_add_f32_dpp v62, v63, v50 row_ror:8 row_mask:0xf bank_mask:0xf\nv_add_f32_dpp v50, v63, v50 row_ror:8 row_mask:0xf bank_mask:0xf\nv_mfma_f32_16x16x32_bf16 v[40:43], a[20:23], v[92:95], v[40:43]\ns_nop 0\nv_permlane16_swap_b32 v62, v50\n" \
"v_add_f32 v63, v62, v50\nv_mfma_f32_16x16x32_bf16 v[44:47], a[28:31], v[92:95], v[44:47]\ns_waitcnt lgkmcnt(3)\nv_add_f32 v50, v62, v50\nv_mfma_f32_16x16x32_bf16 v[16:19], a[32:35], v[80:83], v[16:19]\nds_read_b128 v[92:95], v2 offset:6400\nv_permlane32_swap_b32 v63, v50\nv_mfma_f32_16x16x32_bf16 v[20:23], v[72:75], v[80:83], v[20:23]\n" \
"ds_read_b128 v[64:67], v1 offset:2048\nv_add_f32 v63, v63, v50\nv_mfma_f32_16x16x32_bf16 v[24:27], a[40:43], v[80:83], v[24:27]\nv_rcp_f32 v63, v63\nv_mfma_f32_16x16x32_bf16 v[28:31], a[48:51], v[80:83], v[28:31]\ns_waitcnt lgkmcnt(4)\nv_mul_f32 v52, v52, v63\nv_mul_f32 v53, v53, v63\n" \
"v_mul_f32 v54, v54, v63\nv_mfma_f32_16x16x32_bf16 v[32:35], a[32:35], v[84:87], v[32:35]\nds_read_b128 v[80:83], v2 offset:8192\nv_mul_f32 v55, v55, v63\nv_mul_f32 v48, v48, v63\nv_mfma_f32_16x16x32_bf16 v[36:39], v[72:75], v[84:87], v[36:39]\nv_mul_f32 v49, v49, v63\nv_cvt_pk_bf16_f32 v52, v52, v53\n" \
"v_cvt_pk_bf16_f32 v53, v54, v55\nv_mfma_f32_16x16x32_bf16 v[40:43], a[40:43], v[84:87], v[40:43]\nv_cvt_pk_bf16_f32 v48, v48, v49\nv_bfi_b32 v52, s61, v52, v61\nv_bfi_b32 v53, s61, v53, v61\nv_mfma_f32_16x16x32_bf16 v[44:47], a[48:51], v[84:87], v[44:47]\ns_waitcnt lgkmcnt(3)\nv_bfi_b32 v48, s61, v48, v61\n" \
"v_mov_b32_dpp v54, v52 row_ror:8 row_mask:0xf bank_mask:0xf\nv_mov_b32_dpp v55, v53 row_ror:8 row_mask:0xf bank_mask:0xf\nv_mfma_f32_16x16x32_bf16 v[16:19], a[36:39], v[88:91], v[16:19]\nds_read_b128 v[84:87], v2 offset:8448\nv_mfma_f32_16x16x32_bf16 v[20:23], v[76:79], v[88:91], v[20:23]\nds_read_b128 v[68:71], v1 offset:3072\nv_mfma_f32_16x16x32_bf16 v[24:27], a[44:47], v[88:91], v[24:27]\nbuffer_store_dwordx4 v[52:55], v10, s[36:39], 0 offen sc1\n" \
"v_mov_b32 v49, v48\nv_mfma_f32_16x16x32_bf16 v[28:31], a[52:55], v[88:91], v[28:31]\ns_waitcnt lgkmcnt(4)\nv_permlane32_swap_b32 v48, v49\nv_mfma_f32_16x16x32_bf16 v[32:35], a[36:39], v[92:95], v[32:35]\nds_read_b128 v[88:91], v2 offset:10240\nv_mfma_f32_16x16x32_bf16 v[36:39], v[76:79], v[92:95], v[36:39]\ns_memrealtime s[86:87]\n" \
"buffer_store_dwordx2 v[48:49], v11, s[36:39], 0 offen sc1\nv_cmp_ne_u32 vcc, 0, v60\nv_mfma_f32_16x16x32_bf16 v[40:43], a[44:47], v[92:95], v[40:43]\nv_mfma_f32_16x16x32_bf16 v[44:47], a[52:55], v[92:95], v[44:47]\ns_waitcnt lgkmcnt(3)\nv_mfma_f32_16x16x32_bf16 v[16:19], a[56:59], v[80:83], v[16:19]\nds_read_b128 v[92:95], v2 offset:10496\nv_mfma_f32_16x16x32_bf16 v[20:23], a[64:67], v[80:83], v[20:23]\n" \
"ds_read_b128 v[72:75], v1 offset:4096\nv_mfma_f32_16x16x32_bf16 v[24:27], v[64:67], v[80:83], v[24:27]\nv_mfma_f32_16x16x32_bf16 v[28:31], a[72:75], v[80:83], v[28:31]\ns_waitcnt lgkmcnt(4)\nv_mfma_f32_16x16x32_bf16 v[32:35], a[56:59], v[84:87], v[32:35]\nds_read_b128 v[80:83], v2 offset:12288\nv_mfma_f32_16x16x32_bf16 v[36:39], a[64:67], v[84:87], v[36:39]\nv_mfma_f32_16x16x32_bf16 v[40:43], v[64:67], v[84:87], v[40:43]\n" \
"v_mfma_f32_16x16x32_bf16 v[44:47], a[72:75], v[84:87], v[44:47]\ns_waitcnt lgkmcnt(3)\nv_mfma_f32_16x16x32_bf16 v[16:19], a[60:63], v[88:91], v[16:19]\nds_read_b128 v[84:87], v2 offset:12544\nv_mfma_f32_16x16x32_bf16 v[20:23], a[68:71], v[88:91], v[20:23]\nds_read_b128 v[76:79], v1 offset:5120\nv_mfma_f32_16x16x32_bf16 v[24:27], v[68:71], v[88:91], v[24:27]\nv_mfma_f32_16x16x32_bf16 v[28:31], a[76:79], v[88:91], v[28:31]\n" \
"s_waitcnt lgkmcnt(4)\nv_mfma_f32_16x16x32_bf16 v[32:35], a[60:63], v[92:95], v[32:35]\nds_read_b128 v[88:91], v2 offset:14336\nv_mfma_f32_16x16x32_bf16 v[36:39], a[68:71], v[92:95], v[36:39]\nv_mfma_f32_16x16x32_bf16 v[40:43], v[68:71], v[92:95], v[40:43]\nv_mfma_f32_16x16x32_bf16 v[44:47], a[76:79], v[92:95], v[44:47]\ns_cbranch_vccnz .Lqk_pj%=\ns_cmp_lt_i32 s40, 63\n" \
"s_waitcnt lgkmcnt(0)\ns_cbranch_scc1 .Lqk_pwait%=\ns_branch .Lqk_end%=\n.Lqk_re0%=:\nv_max_u32 v17, v80, v82\nv_cmp_eq_u32 vcc, -1, v17\ns_cbranch_vccz .Lqk_sk1617%=\ns_mov_b64 exec, vcc\n" \
"buffer_load_dwordx4 v[80:83], v3, s[36:39], s42 offen offset:0 sc1\ns_mov_b64 exec, -1\n.Lqk_sk1617%=:\nv_max_u32 v17, v84, v86\nv_cmp_eq_u32 vcc, -1, v17\ns_cbranch_vccz .Lqk_sk1624%=\ns_mov_b64 exec, vcc\nbuffer_load_dwordx4 v[84:87], v3, s[36:39], s42 offen offset:1024 sc1\n" \
"s_mov_b64 exec, -1\n.Lqk_sk1624%=:\nv_max_u32 v17, v88, v90\nv_cmp_eq_u32 vcc, -1, v17\ns_cbranch_vccz .Lqk_sk1631%=\ns_mov_b64 exec, vcc\nbuffer_load_dwordx4 v[88:91], v3, s[36:39], s42 offen offset:2048 sc1\ns_mov_b64 exec, -1\n" \
".Lqk_sk1631%=:\nv_max_u32 v17, v92, v94\nv_cmp_eq_u32 vcc, -1, v17\ns_cbranch_vccz .Lqk_sk1638%=\ns_mov_b64 exec, vcc\nbuffer_load_dwordx4 v[92:95], v3, s[36:39], s42 offen offset:3072 sc1\ns_mov_b64 exec, -1\n.Lqk_sk1638%=:\n" \
"v_mov_b64 v[48:49], 0\nv_mov_b64 v[50:51], 0\nv_mov_b64 v[52:53], 0\nv_mov_b64 v[54:55], 0\nv_mov_b64 v[56:57], 0\nv_mov_b64 v[58:59], 0\nv_mov_b64 v[60:61], 0\nv_mov_b64 v[62:63], 0\n" \
"s_add_u32 s43, s43, 1\ns_cmp_lt_u32 s43, 262144\ns_cbranch_scc1 .Lqk_poll0%=\ns_branch .Lqk_abort%=\n.Lqk_c1%=:\ns_waitcnt lgkmcnt(3)\nv_mfma_f32_16x16x32_bf16 v[32:35], v[232:235], v[56:59], v[32:35]\nv_mov_b64 v[52:53], 0\n" \
"v_mov_b64 v[54:55], 0\nv_mfma_f32_16x16x32_bf16 v[36:39], v[240:243], v[56:59], v[36:39]\ns_waitcnt lgkmcnt(2)\nds_read_b128 v[64:67], v1 offset:6144\nv_mfma_f32_16x16x32_bf16 v[32:35], v[236:239], v[60:63], v[32:35]\nds_read_b128 v[68:71], v1 offset:7168\nv_mfma_f32_16x16x32_bf16 v[36:39], v[244:247], v[60:63], v[36:39]\ns_bfe_i32 s54, s40, 0x10001\n" \
"v_mfma_f32_16x16x32_bf16 v[40:43], v[248:251], v[56:59], v[40:43]\nv_mfma_f32_16x16x32_bf16 v[44:47], v[72:75], v[56:59], v[44:47]\nv_cvt_pk_bf16_f32 v36, v32, v36\nv_cvt_pk_bf16_f32 v37, v33, v37\nv_mfma_f32_16x16x32_bf16 v[40:43], v[252:255], v[60:63], v[40:43]\nv_cvt_pk_bf16_f32 v38, v34, v38\nv_cvt_pk_bf16_f32 v39, v35, v39\nv_mfma_f32_16x16x32_bf16 v[44:47], v[76:79], v[60:63], v[44:47]\n" \
"ds_read_b128 v[72:75], v1 offset:8192\nds_read_b128 v[76:79], v1 offset:9216\nv_mov_b64 v[56:57], 0\nv_mov_b64 v[58:59], 0\nv_mov_b64 v[60:61], 0\nv_mov_b64 v[62:63], 0\n.Lqk_poll1%=:\ns_waitcnt vmcnt(0)\n" \
"v_smfmac_f32_16x16x64_bf16 v[48:51], v[80:83], a[104:111], v4\nds_write_b128 v0, v[80:83] offset:20480\nv_max3_u32 v16, v80, v82, v84\nv_smfmac_f32_16x16x64_bf16 v[52:55], v[80:83], a[112:119], v4\nv_max3_u32 v16, v16, v86, v88\nv_smfmac_f32_16x16x64_bf16 v[56:59], v[80:83], a[120:127], v4\nds_write_b128 v0, v[84:87] offset:21504\nv_max3_u32 v16, v16, v90, v92\n" \
"v_max_u32 v16, v16, v94\nv_smfmac_f32_16x16x64_bf16 v[60:63], v[80:83], a[128:135], v4\nv_cmp_eq_u32 vcc, -1, v16\nv_smfmac_f32_16x16x64_bf16 v[48:51], v[84:87], a[136:143], v4\nds_write_b128 v0, v[88:91] offset:22528\ns_cbranch_vccnz .Lqk_re1%=\ns_waitcnt lgkmcnt(5)\nv_smfmac_f32_16x16x64_bf16 v[52:55], v[84:87], v[64:71], v4\n" \
"v_smfmac_f32_16x16x64_bf16 v[56:59], v[84:87], a[144:151], v4\nds_write_b128 v0, v[92:95] offset:23552\nv_smfmac_f32_16x16x64_bf16 v[60:63], v[84:87], a[152:159], v4\nv_smfmac_f32_16x16x64_bf16 v[48:51], v[88:91], a[160:167], v4\nv_smfmac_f32_16x16x64_bf16 v[52:55], v[88:91], a[168:175], v4\ns_waitcnt lgkmcnt(0)\n.Lqk_jn1%=:\ns_barrier\n" \
"ds_read_b128 v[80:83], v0 offset:24576\nds_read_b128 v[64:67], v1 offset:10240\nds_read_b128 v[68:71], v1 offset:11264\nv_smfmac_f32_16x16x64_bf16 v[56:59], v[88:91], v[72:79], v4\nds_read_b128 v[72:75], v1 offset:12288\nds_read_b128 v[76:79], v1 offset:13312\nv_smfmac_f32_16x16x64_bf16 v[60:63], v[88:91], a[176:183], v4\nds_read_b128 v[84:87], v0 offset:25600\n" \
"ds_read_b128 v[88:91], v0 offset:26624\nv_smfmac_f32_16x16x64_bf16 v[48:51], v[92:95], a[184:191], v4\nds_write_b128 v5, v[36:39] offset:256\nv_cvt_pk_bf16_f32 v44, v40, v44\nv_cvt_pk_bf16_f32 v45, v41, v45\nv_cvt_pk_bf16_f32 v46, v42, v46\nv_cvt_pk_bf16_f32 v47, v43, v47\nv_smfmac_f32_16x16x64_bf16 v[52:55], v[92:95], a[192:199], v4\n" \
"v_smfmac_f32_16x16x64_bf16 v[56:59], v[92:95], a[200:207], v4\ns_waitcnt lgkmcnt(5)\nv_smfmac_f32_16x16x64_bf16 v[60:63], v[92:95], v[64:71], v4\nds_read_b128 v[92:95], v0 offset:27648\nds_read_b128 v[64:67], v1 offset:14336\nds_read_b128 v[68:71], v1 offset:15360\nv_smfmac_f32_16x16x64_bf16 v[48:51], v[80:83], a[208:215], v4\nds_write_b128 v5, v[44:47] offset:768\n" \
"v_smfmac_f32_16x16x64_bf16 v[52:55], v[80:83], a[216:223], v4\nv_smfmac_f32_16x16x64_bf16 v[56:59], v[80:83], a[224:231], v4\nv_smfmac_f32_16x16x64_bf16 v[60:63], v[80:83], a[232:239], v4\ns_waitcnt lgkmcnt(6)\nds_read_b128 v[80:83], v0 offset:28672\nv_smfmac_f32_16x16x64_bf16 v[48:51], v[84:87], a[240:247], v4\nv_smfmac_f32_16x16x64_bf16 v[52:55], v[84:87], v[72:79], v4\nds_read_b128 v[72:75], v1 offset:16384\n" \
"ds_read_b128 v[76:79], v1 offset:17408\nv_smfmac_f32_16x16x64_bf16 v[56:59], v[84:87], a[248:255], v4\nv_smfmac_f32_16x16x64_bf16 v[60:63], v[84:87], v[96:103], v4\ns_waitcnt lgkmcnt(8)\nds_read_b128 v[84:87], v0 offset:29696\nv_smfmac_f32_16x16x64_bf16 v[48:51], v[88:91], v[104:111], v4\ns_waitcnt lgkmcnt(4)\ns_barrier\n" \
"v_smfmac_f32_16x16x64_bf16 v[52:55], v[88:91], v[112:119], v4\nv_smfmac_f32_16x16x64_bf16 v[56:59], v[88:91], v[64:71], v4\nds_read_b128 v[64:67], v1 offset:18432\nds_read_b128 v[68:71], v1 offset:19456\nv_smfmac_f32_16x16x64_bf16 v[60:63], v[88:91], v[120:127], v4\nds_read_b128 v[88:91], v0 offset:30720\nv_smfmac_f32_16x16x64_bf16 v[48:51], v[92:95], v[128:135], v4\nds_read_b128 v[16:19], v7 offset:0\n" \
"v_smfmac_f32_16x16x64_bf16 v[52:55], v[92:95], v[136:143], v4\nv_smfmac_f32_16x16x64_bf16 v[56:59], v[92:95], v[144:151], v4\ns_waitcnt lgkmcnt(5)\nds_read_b128 v[20:23], v7 offset:2048\nv_smfmac_f32_16x16x64_bf16 v[60:63], v[92:95], v[72:79], v4\nds_read_b128 v[92:95], v0 offset:31744\nds_read_b128 v[72:75], v1 offset:20480\nds_read_b128 v[76:79], v1 offset:21504\n" \
"v_smfmac_f32_16x16x64_bf16 v[48:51], v[80:83], v[152:159], v4\nds_read_b128 v[24:27], v7 offset:4096\nv_smfmac_f32_16x16x64_bf16 v[52:55], v[80:83], v[160:167], v4\nv_smfmac_f32_16x16x64_bf16 v[56:59], v[80:83], v[168:175], v4\nds_read_b128 v[28:31], v7 offset:6144\nv_smfmac_f32_16x16x64_bf16 v[60:63], v[80:83], v[176:183], v4\ns_waitcnt lgkmcnt(10)\nds_read_b128 v[80:83], v0 offset:16384\n" \
"v_smfmac_f32_16x16x64_bf16 v[48:51], v[84:87], v[184:191], v4\ns_waitcnt lgkmcnt(9)\nds_read_b128 v[32:35], v7 offset:8192\nv_smfmac_f32_16x16x64_bf16 v[52:55], v[84:87], v[64:71], v4\nds_read_b128 v[64:67], v1 offset:22528\nds_read_b128 v[68:71], v1 offset:23552\nv_smfmac_f32_16x16x64_bf16 v[56:59], v[84:87], v[192:199], v4\nds_read_b128 v[36:39], v7 offset:10240\n" \
"v_smfmac_f32_16x16x64_bf16 v[60:63], v[84:87], v[200:207], v4\ns_waitcnt lgkmcnt(12)\nds_read_b128 v[84:87], v0 offset:17408\nv_smfmac_f32_16x16x64_bf16 v[48:51], v[88:91], v[208:215], v4\nds_read_b128 v[40:43], v7 offset:12288\nv_smfmac_f32_16x16x64_bf16 v[52:55], v[88:91], v[216:223], v4\ns_waitcnt lgkmcnt(9)\nv_smfmac_f32_16x16x64_bf16 v[56:59], v[88:91], v[72:79], v4\n" \
"ds_read_b128 v[72:75], v1 offset:0\nds_read_b128 v[76:79], v1 offset:1024\nv_smfmac_f32_16x16x64_bf16 v[60:63], v[88:91], v[224:231], v4\nds_read_b128 v[88:91], v0 offset:18432\nv_smfmac_f32_16x16x64_bf16 v[48:51], v[92:95], v[232:239], v4\nds_read_b128 v[44:47], v7 offset:14336\nv_smfmac_f32_16x16x64_bf16 v[52:55], v[92:95], v[240:247], v4\nv_smfmac_f32_16x16x64_bf16 v[56:59], v[92:95], v[248:255], v4\n" \
"s_waitcnt lgkmcnt(7)\nv_smfmac_f32_16x16x64_bf16 v[60:63], v[92:95], v[64:71], v4\nds_read_b128 v[92:95], v0 offset:19456\nds_read_b128 v[64:67], v1 offset:2048\nds_read_b128 v[68:71], v1 offset:3072\nv_smfmac_f32_16x16x64_bf16 v[48:51], v[80:83], a[0:7], v4\nv_smfmac_f32_16x16x64_bf16 v[52:55], v[80:83], a[8:15], v4\nv_smfmac_f32_16x16x64_bf16 v[56:59], v[80:83], a[16:23], v4\n" \
"v_smfmac_f32_16x16x64_bf16 v[60:63], v[80:83], a[24:31], v4\ns_waitcnt lgkmcnt(8)\nv_smfmac_f32_16x16x64_bf16 v[48:51], v[84:87], a[32:39], v4\ns_waitcnt lgkmcnt(5)\nv_smfmac_f32_16x16x64_bf16 v[52:55], v[84:87], v[72:79], v4\nds_read_b128 v[72:75], v1 offset:4096\nds_read_b128 v[76:79], v1 offset:5120\nv_smfmac_f32_16x16x64_bf16 v[56:59], v[84:87], a[40:47], v4\n" \
"v_smfmac_f32_16x16x64_bf16 v[60:63], v[84:87], a[48:55], v4\ns_waitcnt lgkmcnt(6)\nv_smfmac_f32_16x16x64_bf16 v[48:51], v[88:91], a[56:63], v4\nv_smfmac_f32_16x16x64_bf16 v[52:55], v[88:91], a[64:71], v4\ns_waitcnt lgkmcnt(4)\nv_smfmac_f32_16x16x64_bf16 v[48:51], v[92:95], a[80:87], v4\nv_smfmac_f32_16x16x64_bf16 v[52:55], v[92:95], a[88:95], v4\ns_waitcnt lgkmcnt(2)\n" \
"v_smfmac_f32_16x16x64_bf16 v[56:59], v[88:91], v[64:71], v4\nv_add_f32 v48, v48, v50\nv_smfmac_f32_16x16x64_bf16 v[60:63], v[88:91], a[72:79], v4\nv_add_f32 v52, v52, v54\nv_add_f32 v49, v49, v51\nv_smfmac_f32_16x16x64_bf16 v[56:59], v[92:95], a[96:103], v4\ns_waitcnt lgkmcnt(0)\nv_add_f32 v53, v53, v55\n" \
"v_cvt_pk_bf16_f32 v50, v48, v52\nv_smfmac_f32_16x16x64_bf16 v[60:63], v[92:95], v[72:79], v4\nv_cvt_pk_bf16_f32 v51, v49, v53\nds_write2_b32 v6, v50, v51 offset0:0 offset1:4\ns_nop 1\nv_add_f32 v56, v56, v58\nv_add_f32 v57, v57, v59\ns_nop 1\n" \
"v_add_f32 v60, v60, v62\nv_add_f32 v61, v61, v63\nv_cvt_pk_bf16_f32 v62, v56, v60\nv_cvt_pk_bf16_f32 v63, v57, v61\nds_write2_b32 v6, v62, v63 offset0:36 offset1:40\nv_mov_b32 v61, s54\nv_mfma_f32_16x16x32_bf16 v[52:55], v[32:35], v[16:19], v[12:15]\nv_mfma_f32_16x16x32_bf16 v[52:55], v[36:39], v[20:23], v[52:55]\n" \
"v_mfma_f32_16x16x32_bf16 v[52:55], v[40:43], v[24:27], v[52:55]\nv_mfma_f32_16x16x32_bf16 v[52:55], v[44:47], v[28:31], v[52:55]\ns_waitcnt lgkmcnt(0)\ns_barrier\ns_nop 10\nds_read_b128 v[32:35], v9 offset:0\nds_read_b128 v[36:39], v9 offset:576\nds_read_b128 v[40:43], v9 offset:1152\n" \
"ds_read_b128 v[44:47], v9 offset:1728\nds_read_b32 v60, v0 offset:53760\nds_read_b128 v[80:83], v2 offset:0\nds_read_b128 v[84:87], v2 offset:256\nds_read_b128 v[88:91], v2 offset:2048\ns_waitcnt lgkmcnt(7)\nv_mfma_f32_16x16x32_bf16 v[48:51], v[32:35], v[16:19], 0\ns_waitcnt lgkmcnt(6)\n" \
"v_mfma_f32_16x16x32_bf16 v[48:51], v[36:39], v[20:23], v[48:51]\ns_waitcnt lgkmcnt(5)\nv_mfma_f32_16x16x32_bf16 v[48:51], v[40:43], v[24:27], v[48:51]\ns_waitcnt lgkmcnt(4)\nv_mfma_f32_16x16x32_bf16 v[48:51], v[44:47], v[28:31], v[48:51]\ns_waitcnt lgkmcnt(2)\nv_mfma_f32_16x16x32_bf16 v[16:19], a[0:3], v[80:83], 0\nds_read_b128 v[92:95], v2 offset:2304\n" \
"v_max3_f32 v62, v52, v53, v54\nv_max_f32 v62, v62, v55\nv_mfma_f32_16x16x32_bf16 v[20:23], a[8:11], v[80:83], 0\nds_read_b128 v[72:75], v1 offset:0\nv_max3_f32 v63, v62, v48, v49\nv_max3_f32 v62, v62, v48, v49\nv_mfma_f32_16x16x32_bf16 v[24:27], a[16:19], v[80:83], 0\ns_nop 0\n" \
"v_permlane16_swap_b32 v62, v63\nv_max_f32 v50, v62, v63\nv_mfma_f32_16x16x32_bf16 v[28:31], a[24:27], v[80:83], 0\ns_waitcnt lgkmcnt(3)\nv_max_f32 v62, v62, v63\nv_mfma_f32_16x16x32_bf16 v[32:35], a[0:3], v[84:87], 0\nds_read_b128 v[80:83], v2 offset:4096\nv_permlane32_swap_b32 v62, v50\n" \
"v_mfma_f32_16x16x32_bf16 v[36:39], a[8:11], v[84:87], 0\nv_max_f32 v62, v62, v50\nv_mul_f32 v62, s59, v62\nv_fma_f32 v52, v52, s58, v62\nv_mfma_f32_16x16x32_bf16 v[40:43], a[16:19], v[84:87], 0\nv_fma_f32 v53, v53, s58, v62\nv_exp_f32 v52, v52\nv_mfma_f32_16x16x32_bf16 v[44:47], a[24:27], v[84:87], 0\n" \
"s_waitcnt lgkmcnt(3)\nv_fma_f32 v54, v54, s58, v62\nv_exp_f32 v53, v53\nv_mfma_f32_16x16x32_bf16 v[16:19], a[4:7], v[88:91], v[16:19]\nds_read_b128 v[84:87], v2 offset:4352\nv_fma_f32 v55, v55, s58, v62\nv_add_f32 v63, v52, v53\nv_mfma_f32_16x16x32_bf16 v[20:23], a[12:15], v[88:91], v[20:23]\n" \
"ds_read_b128 v[76:79], v1 offset:1024\nv_exp_f32 v54, v54\nv_mfma_f32_16x16x32_bf16 v[24:27], a[20:23], v[88:91], v[24:27]\nv_fma_f32 v48, v48, s58, v62\nv_exp_f32 v55, v55\nv_mfma_f32_16x16x32_bf16 v[28:31], a[28:31], v[88:91], v[28:31]\ns_waitcnt lgkmcnt(4)\nv_add_f32 v63, v63, v54\n" \
"v_fma_f32 v49, v49, s58, v62\nv_add_f32 v63, v63, v55\nv_mfma_f32_16x16x32_bf16 v[32:35], a[4:7], v[92:95], v[32:35]\nds_read_b128 v[88:91], v2 offset:6144\nv_exp_f32 v48, v48\nv_mfma_f32_16x16x32_bf16 v[36:39], a[12:15], v[92:95], v[36:39]\nv_exp_f32 v49, v49\nv_add_f32 v63, v63, v48\n" \
"v_mfma_f32_16x16x32_bf16 v[40:43], a[20:23], v[92:95], v[40:43]\nv_add_f32 v62, v63, v49\nv_add_f32 v50, v63, v49\nv_mfma_f32_16x16x32_bf16 v[44:47], a[28:31], v[92:95], v[44:47]\ns_waitcnt lgkmcnt(3)\nv_permlane16_swap_b32 v62, v50\nv_add_f32 v63, v62, v50\nv_mfma_f32_16x16x32_bf16 v[16:19], a[32:35], v[80:83], v[16:19]\n" \
"ds_read_b128 v[92:95], v2 offset:6400\nv_add_f32 v50, v62, v50\nv_mfma_f32_16x16x32_bf16 v[20:23], v[72:75], v[80:83], v[20:23]\nds_read_b128 v[64:67], v1 offset:2048\nv_permlane32_swap_b32 v63, v50\nv_mfma_f32_16x16x32_bf16 v[24:27], a[40:43], v[80:83], v[24:27]\nv_add_f32 v63, v63, v50\nv_rcp_f32 v63, v63\n" \
"v_mfma_f32_16x16x32_bf16 v[28:31], a[48:51], v[80:83], v[28:31]\ns_waitcnt lgkmcnt(4)\nv_mul_f32 v52, v52, v63\nv_mul_f32 v53, v53, v63\nv_mul_f32 v54, v54, v63\nv_mfma_f32_16x16x32_bf16 v[32:35], a[32:35], v[84:87], v[32:35]\nds_read_b128 v[80:83], v2 offset:8192\nv_mul_f32 v55, v55, v63\n" \
"v_mul_f32 v48, v48, v63\nv_mfma_f32_16x16x32_bf16 v[36:39], v[72:75], v[84:87], v[36:39]\nv_mul_f32 v49, v49, v63\nv_cvt_pk_bf16_f32 v52, v52, v53\nv_cvt_pk_bf16_f32 v53, v54, v55\nv_mfma_f32_16x16x32_bf16 v[40:43], a[40:43], v[84:87], v[40:43]\nv_cvt_pk_bf16_f32 v48, v48, v49\nv_bfi_b32 v52, s61, v52, v61\n" \
"v_bfi_b32 v53, s61, v53, v61\nv_mfma_f32_16x16x32_bf16 v[44:47], a[48:51], v[84:87], v[44:47]\ns_waitcnt lgkmcnt(3)\nv_bfi_b32 v48, s61, v48, v61\nv_mfma_f32_16x16x32_bf16 v[16:19], a[36:39], v[88:91], v[16:19]\nds_read_b128 v[84:87], v2 offset:8448\nv_mfma_f32_16x16x32_bf16 v[20:23], v[76:79], v[88:91], v[20:23]\nds_read_b128 v[68:71], v1 offset:3072\n" \
"v_mfma_f32_16x16x32_bf16 v[24:27], a[44:47], v[88:91], v[24:27]\nbuffer_store_dwordx2 v[52:53], v10, s[36:39], s55 offen sc1\nv_mov_b32 v49, v48\nv_mfma_f32_16x16x32_bf16 v[28:31], a[52:55], v[88:91], v[28:31]\ns_waitcnt lgkmcnt(4)\nv_permlane16_swap_b32 v48, v49\nv_mov_b32 v50, v48\nv_mfma_f32_16x16x32_bf16 v[32:35], a[36:39], v[92:95], v[32:35]\n" \
"ds_read_b128 v[88:91], v2 offset:10240\nv_mov_b32 v51, v49\nv_mfma_f32_16x16x32_bf16 v[36:39], v[76:79], v[92:95], v[36:39]\nv_permlane32_swap_b32 v48, v50\nv_mfma_f32_16x16x32_bf16 v[40:43], a[44:47], v[92:95], v[40:43]\nv_permlane32_swap_b32 v49, v51\nv_mfma_f32_16x16x32_bf16 v[44:47], a[52:55], v[92:95], v[44:47]\ns_waitcnt lgkmcnt(3)\n" \
"buffer_store_dwordx4 v[48:51], v11, s[36:39], s55 offen sc1\nv_cmp_ne_u32 vcc, 0, v60\nv_mfma_f32_16x16x32_bf16 v[16:19], a[56:59], v[80:83], v[16:19]\nds_read_b128 v[92:95], v2 offset:10496\nv_mfma_f32_16x16x32_bf16 v[20:23], a[64:67], v[80:83], v[20:23]\nds_read_b128 v[72:75], v1 offset:4096\nv_mfma_f32_16x16x32_bf16 v[24:27], v[64:67], v[80:83], v[24:27]\nv_mfma_f32_16x16x32_bf16 v[28:31], a[72:75], v[80:83], v[28:31]\n" \
"s_waitcnt lgkmcnt(4)\nv_mfma_f32_16x16x32_bf16 v[32:35], a[56:59], v[84:87], v[32:35]\nds_read_b128 v[80:83], v2 offset:12288\nv_mfma_f32_16x16x32_bf16 v[36:39], a[64:67], v[84:87], v[36:39]\nv_mfma_f32_16x16x32_bf16 v[40:43], v[64:67], v[84:87], v[40:43]\nv_mfma_f32_16x16x32_bf16 v[44:47], a[72:75], v[84:87], v[44:47]\ns_waitcnt lgkmcnt(3)\nv_mfma_f32_16x16x32_bf16 v[16:19], a[60:63], v[88:91], v[16:19]\n" \
"ds_read_b128 v[84:87], v2 offset:12544\nv_mfma_f32_16x16x32_bf16 v[20:23], a[68:71], v[88:91], v[20:23]\nds_read_b128 v[76:79], v1 offset:5120\nv_mfma_f32_16x16x32_bf16 v[24:27], v[68:71], v[88:91], v[24:27]\nv_mfma_f32_16x16x32_bf16 v[28:31], a[76:79], v[88:91], v[28:31]\ns_waitcnt lgkmcnt(4)\nv_mfma_f32_16x16x32_bf16 v[32:35], a[60:63], v[92:95], v[32:35]\nds_read_b128 v[88:91], v2 offset:14336\n" \
"v_mfma_f32_16x16x32_bf16 v[36:39], a[68:71], v[92:95], v[36:39]\nv_mfma_f32_16x16x32_bf16 v[40:43], v[68:71], v[92:95], v[40:43]\nv_mfma_f32_16x16x32_bf16 v[44:47], a[76:79], v[92:95], v[44:47]\ns_cbranch_vccnz .Lqk_pj%=\ns_cmp_lt_i32 s40, 63\ns_waitcnt lgkmcnt(0)\ns_cbranch_scc1 .Lqk_pwait%=\ns_branch .Lqk_end%=\n" \
".Lqk_re1%=:\nv_max_u32 v17, v80, v82\nv_cmp_eq_u32 vcc, -1, v17\ns_cbranch_vccz .Lqk_sk2003%=\ns_mov_b64 exec, vcc\nbuffer_load_dwordx4 v[80:83], v3, s[36:39], s42 offen offset:0 sc1\ns_mov_b64 exec, -1\n.Lqk_sk2003%=:\n" \
"v_max_u32 v17, v84, v86\nv_cmp_eq_u32 vcc, -1, v17\ns_cbranch_vccz .Lqk_sk2010%=\ns_mov_b64 exec, vcc\nbuffer_load_dwordx4 v[84:87], v3, s[36:39], s42 offen offset:1024 sc1\ns_mov_b64 exec, -1\n.Lqk_sk2010%=:\nv_max_u32 v17, v88, v90\n" \
"v_cmp_eq_u32 vcc, -1, v17\ns_cbranch_vccz .Lqk_sk2017%=\ns_mov_b64 exec, vcc\nbuffer_load_dwordx4 v[88:91], v3, s[36:39], s42 offen offset:2048 sc1\ns_mov_b64 exec, -1\n.Lqk_sk2017%=:\nv_max_u32 v17, v92, v94\nv_cmp_eq_u32 vcc, -1, v17\n" \
"s_cbranch_vccz .Lqk_sk2024%=\ns_mov_b64 exec, vcc\nbuffer_load_dwordx4 v[92:95], v3, s[36:39], s42 offen offset:3072 sc1\ns_mov_b64 exec, -1\n.Lqk_sk2024%=:\nv_mov_b64 v[48:49], 0\nv_mov_b64 v[50:51], 0\nv_mov_b64 v[52:53], 0\n" \
"v_mov_b64 v[54:55], 0\nv_mov_b64 v[56:57], 0\nv_mov_b64 v[58:59], 0\nv_mov_b64 v[60:61], 0\nv_mov_b64 v[62:63], 0\ns_add_u32 s43, s43, 1\ns_cmp_lt_u32 s43, 262144\ns_cbranch_scc1 .Lqk_poll1%=\n" \
"s_branch .Lqk_abort%=\n.Lqk_c2%=:\ns_waitcnt lgkmcnt(3)\nv_mfma_f32_16x16x32_bf16 v[32:35], v[232:235], v[56:59], v[32:35]\nv_mov_b64 v[52:53], 0\nv_mov_b64 v[54:55], 0\nv_mfma_f32_16x16x32_bf16 v[36:39], v[240:243], v[56:59], v[36:39]\ns_waitcnt lgkmcnt(2)\n" \
"ds_read_b128 v[64:67], v1 offset:12288\nv_mfma_f32_16x16x32_bf16 v[32:35], v[236:239], v[60:63], v[32:35]\nds_read_b128 v[68:71], v1 offset:13312\nv_mfma_f32_16x16x32_bf16 v[36:39], v[244:247], v[60:63], v[36:39]\ns_bfe_i32 s54, s40, 0x10001\nv_mfma_f32_16x16x32_bf16 v[40:43], v[248:251], v[56:59], v[40:43]\nv_mfma_f32_16x16x32_bf16 v[44:47], v[72:75], v[56:59], v[44:47]\nv_cvt_pk_bf16_f32 v36, v32, v36\n" \
"v_cvt_pk_bf16_f32 v37, v33, v37\nv_mfma_f32_16x16x32_bf16 v[40:43], v[252:255], v[60:63], v[40:43]\nv_cvt_pk_bf16_f32 v38, v34, v38\nv_cvt_pk_bf16_f32 v39, v35, v39\nv_mfma_f32_16x16x32_bf16 v[44:47], v[76:79], v[60:63], v[44:47]\nds_read_b128 v[72:75], v1 offset:14336\nds_read_b128 v[76:79], v1 offset:15360\nv_mov_b64 v[56:57], 0\n" \
"v_mov_b64 v[58:59], 0\nv_mov_b64 v[60:61], 0\nv_mov_b64 v[62:63], 0\n.Lqk_poll2%=:\ns_waitcnt vmcnt(0)\nv_smfmac_f32_16x16x64_bf16 v[48:51], v[80:83], a[208:215], v4\nds_write_b128 v0, v[80:83] offset:24576\nv_max3_u32 v16, v80, v82, v84\n" \
"v_smfmac_f32_16x16x64_bf16 v[52:55], v[80:83], a[216:223], v4\nv_max3_u32 v16, v16, v86, v88\nv_smfmac_f32_16x16x64_bf16 v[56:59], v[80:83], a[224:231], v4\nds_write_b128 v0, v[84:87] offset:25600\nv_max3_u32 v16, v16, v90, v92\nv_max_u32 v16, v16, v94\nv_smfmac_f32_16x16x64_bf16 v[60:63], v[80:83], a[232:239], v4\nv_cmp_eq_u32 vcc, -1, v16\n" \
"v_smfmac_f32_16x16x64_bf16 v[48:51], v[84:87], a[240:247], v4\nds_write_b128 v0, v[88:91] offset:26624\ns_cbranch_vccnz .Lqk_re2%=\ns_waitcnt lgkmcnt(5)\nv_smfmac_f32_16x16x64_bf16 v[52:55], v[84:87], v[64:71], v4\nv_smfmac_f32_16x16x64_bf16 v[56:59], v[84:87], a[248:255], v4\nds_write_b128 v0, v[92:95] offset:27648\nv_smfmac_f32_16x16x64_bf16 v[60:63], v[84:87], v[96:103], v4\n" \
"v_smfmac_f32_16x16x64_bf16 v[48:51], v[88:91], v[104:111], v4\nv_smfmac_f32_16x16x64_bf16 v[52:55], v[88:91], v[112:119], v4\ns_waitcnt lgkmcnt(0)\n.Lqk_jn2%=:\ns_barrier\nds_read_b128 v[80:83], v0 offset:28672\nds_read_b128 v[64:67], v1 offset:16384\nds_read_b128 v[68:71], v1 offset:17408\n" \
"v_smfmac_f32_16x16x64_bf16 v[56:59], v[88:91], v[72:79], v4\nds_read_b128 v[72:75], v1 offset:18432\nds_read_b128 v[76:79], v1 offset:19456\nv_smfmac_f32_16x16x64_bf16 v[60:63], v[88:91], v[120:127], v4\nds_read_b128 v[84:87], v0 offset:29696\nds_read_b128 v[88:91], v0 offset:30720\nv_smfmac_f32_16x16x64_bf16 v[48:51], v[92:95], v[128:135], v4\nds_write_b128 v5, v[36:39] offset:256\n" \
"v_cvt_pk_bf16_f32 v44, v40, v44\nv_cvt_pk_bf16_f32 v45, v41, v45\nv_cvt_pk_bf16_f32 v46, v42, v46\nv_cvt_pk_bf16_f32 v47, v43, v47\nv_smfmac_f32_16x16x64_bf16 v[52:55], v[92:95], v[136:143], v4\nv_smfmac_f32_16x16x64_bf16 v[56:59], v[92:95], v[144:151], v4\ns_waitcnt lgkmcnt(5)\nv_smfmac_f32_16x16x64_bf16 v[60:63], v[92:95], v[64:71], v4\n" \
"ds_read_b128 v[92:95], v0 offset:31744\nds_read_b128 v[64:67], v1 offset:20480\nds_read_b128 v[68:71], v1 offset:21504\nv_smfmac_f32_16x16x64_bf16 v[48:51], v[80:83], v[152:159], v4\nds_write_b128 v5, v[44:47] offset:768\nv_smfmac_f32_16x16x64_bf16 v[52:55], v[80:83], v[160:167], v4\nv_smfmac_f32_16x16x64_bf16 v[56:59], v[80:83], v[168:175], v4\nv_smfmac_f32_16x16x64_bf16 v[60:63], v[80:83], v[176:183], v4\n" \
"s_waitcnt lgkmcnt(6)\nds_read_b128 v[80:83], v0 offset:16384\nv_smfmac_f32_16x16x64_bf16 v[48:51], v[84:87], v[184:191], v4\nv_smfmac_f32_16x16x64_bf16 v[52:55], v[84:87], v[72:79], v4\nds_read_b128 v[72:75], v1 offset:22528\nds_read_b128 v[76:79], v1 offset:23552\nv_smfmac_f32_16x16x64_bf16 v[56:59], v[84:87], v[192:199], v4\nv_smfmac_f32_16x16x64_bf16 v[60:63], v[84:87], v[200:207], v4\n" \
"s_waitcnt lgkmcnt(8)\nds_read_b128 v[84:87], v0 offset:17408\nv_smfmac_f32_16x16x64_bf16 v[48:51], v[88:91], v[208:215], v4\ns_waitcnt lgkmcnt(4)\ns_barrier\nv_smfmac_f32_16x16x64_bf16 v[52:55], v[88:91], v[216:223], v4\nv_smfmac_f32_16x16x64_bf16 v[56:59], v[88:91], v[64:71], v4\nds_read_b128 v[64:67], v1 offset:0\n" \
"ds_read_b128 v[68:71], v1 offset:1024\nv_smfmac_f32_16x16x64_bf16 v[60:63], v[88:91], v[224:231], v4\nds_read_b128 v[88:91], v0 offset:18432\nv_smfmac_f32_16x16x64_bf16 v[48:51], v[92:95], v[232:239], v4\nds_read_b128 v[16:19], v7 offset:256\nv_smfmac_f32_16x16x64_bf16 v[52:55], v[92:95], v[240:247], v4\nv_smfmac_f32_16x16x64_bf16 v[56:59], v[92:95], v[248:255], v4\ns_waitcnt lgkmcnt(5)\n" \
"ds_read_b128 v[20:23], v7 offset:2304\nv_smfmac_f32_16x16x64_bf16 v[60:63], v[92:95], v[72:79], v4\nds_read_b128 v[92:95], v0 offset:19456\nds_read_b128 v[72:75], v1 offset:2048\nds_read_b128 v[76:79], v1 offset:3072\nv_smfmac_f32_16x16x64_bf16 v[48:51], v[80:83], a[0:7], v4\nds_read_b128 v[24:27], v7 offset:4352\nv_smfmac_f32_16x16x64_bf16 v[52:55], v[80:83], a[8:15], v4\n" \
"v_smfmac_f32_16x16x64_bf16 v[56:59], v[80:83], a[16:23], v4\nds_read_b128 v[28:31], v7 offset:6400\nv_smfmac_f32_16x16x64_bf16 v[60:63], v[80:83], a[24:31], v4\ns_waitcnt lgkmcnt(10)\nds_read_b128 v[80:83], v0 offset:20480\nv_smfmac_f32_16x16x64_bf16 v[48:51], v[84:87], a[32:39], v4\ns_waitcnt lgkmcnt(9)\nds_read_b128 v[32:35], v7 offset:8192\n" \
"v_smfmac_f32_16x16x64_bf16 v[52:55], v[84:87], v[64:71], v4\nds_read_b128 v[64:67], v1 offset:4096\nds_read_b128 v[68:71], v1 offset:5120\nv_smfmac_f32_16x16x64_bf16 v[56:59], v[84:87], a[40:47], v4\nds_read_b128 v[36:39], v7 offset:10240\nv_smfmac_f32_16x16x64_bf16 v[60:63], v[84:87], a[48:55], v4\ns_waitcnt lgkmcnt(12)\nds_read_b128 v[84:87], v0 offset:21504\n" \
"v_smfmac_f32_16x16x64_bf16 v[48:51], v[88:91], a[56:63], v4\nds_read_b128 v[40:43], v7 offset:12288\nv_smfmac_f32_16x16x64_bf16 v[52:55], v[88:91], a[64:71], v4\ns_waitcnt lgkmcnt(9)\nv_smfmac_f32_16x16x64_bf16 v[56:59], v[88:91], v[72:79], v4\nds_read_b128 v[72:75], v1 offset:6144\nds_read_b128 v[76:79], v1 offset:7168\nv_smfmac_f32_16x16x64_bf16 v[60:63], v[88:91], a[72:79], v4\n" \
"ds_read_b128 v[88:91], v0 offset:22528\nv_smfmac_f32_16x16x64_bf16 v[48:51], v[92:95], a[80:87], v4\nds_read_b128 v[44:47], v7 offset:14336\nv_smfmac_f32_16x16x64_bf16 v[52:55], v[92:95], a[88:95], v4\nv_smfmac_f32_16x16x64_bf16 v[56:59], v[92:95], a[96:103], v4\ns_waitcnt lgkmcnt(7)\nv_smfmac_f32_16x16x64_bf16 v[60:63], v[92:95], v[64:71], v4\nds_read_b128 v[92:95], v0 offset:23552\n" \
"ds_read_b128 v[64:67], v1 offset:8192\nds_read_b128 v[68:71], v1 offset:9216\nv_smfmac_f32_16x16x64_bf16 v[48:51], v[80:83], a[104:111], v4\nv_smfmac_f32_16x16x64_bf16 v[52:55], v[80:83], a[112:119], v4\nv_smfmac_f32_16x16x64_bf16 v[56:59], v[80:83], a[120:127], v4\nv_smfmac_f32_16x16x64_bf16 v[60:63], v[80:83], a[128:135], v4\ns_waitcnt lgkmcnt(8)\nv_smfmac_f32_16x16x64_bf16 v[48:51], v[84:87], a[136:143], v4\n" \
"s_waitcnt lgkmcnt(5)\nds_read_b128 v[80:83], v7 offset:8448\nv_smfmac_f32_16x16x64_bf16 v[52:55], v[84:87], v[72:79], v4\nds_read_b128 v[72:75], v1 offset:10240\nds_read_b128 v[76:79], v1 offset:11264\nv_smfmac_f32_16x16x64_bf16 v[56:59], v[84:87], a[144:151], v4\nv_smfmac_f32_16x16x64_bf16 v[60:63], v[84:87], a[152:159], v4\ns_waitcnt lgkmcnt(7)\n" \
"v_smfmac_f32_16x16x64_bf16 v[48:51], v[88:91], a[160:167], v4\nds_read_b128 v[84:87], v7 offset:10496\nv_smfmac_f32_16x16x64_bf16 v[52:55], v[88:91], a[168:175], v4\ns_waitcnt lgkmcnt(6)\nv_smfmac_f32_16x16x64_bf16 v[48:51], v[92:95], a[184:191], v4\nv_smfmac_f32_16x16x64_bf16 v[52:55], v[92:95], a[192:199], v4\ns_waitcnt lgkmcnt(4)\nv_smfmac_f32_16x16x64_bf16 v[56:59], v[88:91], v[64:71], v4\n" \
"ds_read_b128 v[64:67], v7 offset:12544\nv_add_f32 v48, v48, v50\nv_smfmac_f32_16x16x64_bf16 v[60:63], v[88:91], a[176:183], v4\nds_read_b128 v[68:71], v7 offset:14592\nv_add_f32 v52, v52, v54\nv_add_f32 v49, v49, v51\nv_smfmac_f32_16x16x64_bf16 v[56:59], v[92:95], a[200:207], v4\ns_waitcnt lgkmcnt(3)\n" \
"v_add_f32 v53, v53, v55\nv_cvt_pk_bf16_f32 v50, v48, v52\nv_smfmac_f32_16x16x64_bf16 v[60:63], v[92:95], v[72:79], v4\nv_cvt_pk_bf16_f32 v51, v49, v53\nds_write2_b32 v6, v50, v51 offset0:0 offset1:4\ns_nop 1\nv_add_f32 v56, v56, v58\nv_add_f32 v57, v57, v59\n" \
"s_nop 1\nv_add_f32 v60, v60, v62\nv_add_f32 v61, v61, v63\nv_cvt_pk_bf16_f32 v62, v56, v60\nv_cvt_pk_bf16_f32 v63, v57, v61\nds_write2_b32 v6, v62, v63 offset0:36 offset1:40\nv_mov_b32 v61, s54\nv_mfma_f32_16x16x32_bf16 v[52:55], v[32:35], v[16:19], 0\n" \
"v_mfma_f32_16x16x32_bf16 v[52:55], v[36:39], v[20:23], v[52:55]\nv_mfma_f32_16x16x32_bf16 v[52:55], v[40:43], v[24:27], v[52:55]\nv_mfma_f32_16x16x32_bf16 v[52:55], v[44:47], v[28:31], v[52:55]\ns_waitcnt lgkmcnt(0)\ns_barrier\nv_mfma_f32_16x16x32_bf16 v[56:59], v[80:83], v[16:19], v[12:15]\nv_mfma_f32_16x16x32_bf16 v[56:59], v[84:87], v[20:23], v[56:59]\nds_read_b128 v[32:35], v9 offset:0\n" \
"ds_read_b128 v[36:39], v9 offset:576\nds_read_b128 v[40:43], v9 offset:1152\nds_read_b128 v[44:47], v9 offset:1728\nds_read_b32 v60, v0 offset:53760\nv_mfma_f32_16x16x32_bf16 v[56:59], v[64:67], v[24:27], v[56:59]\nv_mfma_f32_16x16x32_bf16 v[56:59], v[68:71], v[28:31], v[56:59]\ns_waitcnt lgkmcnt(4)\nds_read_b128 v[80:83], v2 offset:0\n" \
"ds_read_b128 v[84:87], v2 offset:256\nds_read_b128 v[88:91], v2 offset:2048\nv_mfma_f32_16x16x32_bf16 v[48:51], v[32:35], v[16:19], 0\ns_waitcnt lgkmcnt(6)\nv_mfma_f32_16x16x32_bf16 v[48:51], v[36:39], v[20:23], v[48:51]\ns_waitcnt lgkmcnt(5)\nv_mfma_f32_16x16x32_bf16 v[48:51], v[40:43], v[24:27], v[48:51]\ns_waitcnt lgkmcnt(4)\n" \
"v_mfma_f32_16x16x32_bf16 v[48:51], v[44:47], v[28:31], v[48:51]\ns_waitcnt lgkmcnt(2)\nv_mfma_f32_16x16x32_bf16 v[16:19], a[0:3], v[80:83], 0\nds_read_b128 v[92:95], v2 offset:2304\nv_max3_f32 v62, v52, v53, v54\nv_max3_f32 v62, v62, v56, v57\nv_mfma_f32_16x16x32_bf16 v[20:23], a[8:11], v[80:83], 0\nds_read_b128 v[72:75], v1 offset:0\n" \
"v_max3_f32 v62, v62, v58, v59\nv_max_f32 v62, v62, v55\nv_mfma_f32_16x16x32_bf16 v[24:27], a[16:19], v[80:83], 0\nv_max3_f32 v63, v62, v48, v49\nv_max3_f32 v62, v62, v48, v49\nv_mfma_f32_16x16x32_bf16 v[28:31], a[24:27], v[80:83], 0\ns_waitcnt lgkmcnt(3)\nv_permlane16_swap_b32 v62, v63\n" \
"v_max_f32 v50, v62, v63\nv_mfma_f32_16x16x32_bf16 v[32:35], a[0:3], v[84:87], 0\nds_read_b128 v[80:83], v2 offset:4096\nv_max_f32 v62, v62, v63\nv_mfma_f32_16x16x32_bf16 v[36:39], a[8:11], v[84:87], 0\ns_nop 0\nv_permlane32_swap_b32 v62, v50\nv_max_f32 v62, v62, v50\n" \
"v_mfma_f32_16x16x32_bf16 v[40:43], a[16:19], v[84:87], 0\nv_mul_f32 v62, s59, v62\nv_fma_f32 v52, v52, s58, v62\nv_fma_f32 v53, v53, s58, v62\nv_mfma_f32_16x16x32_bf16 v[44:47], a[24:27], v[84:87], 0\ns_waitcnt lgkmcnt(3)\nv_exp_f32 v52, v52\nv_fma_f32 v54, v54, s58, v62\n" \
"v_mfma_f32_16x16x32_bf16 v[16:19], a[4:7], v[88:91], v[16:19]\nds_read_b128 v[84:87], v2 offset:4352\nv_exp_f32 v53, v53\nv_mfma_f32_16x16x32_bf16 v[20:23], a[12:15], v[88:91], v[20:23]\nds_read_b128 v[76:79], v1 offset:1024\nv_fma_f32 v55, v55, s58, v62\nv_add_f32 v63, v52, v53\nv_mfma_f32_16x16x32_bf16 v[24:27], a[20:23], v[88:91], v[24:27]\n" \
"v_exp_f32 v54, v54\nv_fma_f32 v56, v56, s58, v62\nv_mfma_f32_16x16x32_bf16 v[28:31], a[28:31], v[88:91], v[28:31]\ns_waitcnt lgkmcnt(4)\nv_exp_f32 v55, v55\nv_add_f32 v63, v63, v54\nv_mfma_f32_16x16x32_bf16 v[32:35], a[4:7], v[92:95], v[32:35]\nds_read_b128 v[88:91], v2 offset:6144\n" \
"v_fma_f32 v57, v57, s58, v62\nv_add_f32 v63, v63, v55\nv_mfma_f32_16x16x32_bf16 v[36:39], a[12:15], v[92:95], v[36:39]\nv_exp_f32 v56, v56\nv_fma_f32 v58, v58, s58, v62\nv_mfma_f32_16x16x32_bf16 v[40:43], a[20:23], v[92:95], v[40:43]\nv_exp_f32 v57, v57\nv_add_f32 v63, v63, v56\n" \
"v_mfma_f32_16x16x32_bf16 v[44:47], a[28:31], v[92:95], v[44:47]\ns_waitcnt lgkmcnt(3)\nv_fma_f32 v59, v59, s58, v62\nv_exp_f32 v58, v58\nv_mfma_f32_16x16x32_bf16 v[16:19], a[32:35], v[80:83], v[16:19]\nds_read_b128 v[92:95], v2 offset:6400\nv_add_f32 v63, v63, v57\nv_fma_f32 v48, v48, s58, v62\n" \
"v_mfma_f32_16x16x32_bf16 v[20:23], v[72:75], v[80:83], v[20:23]\nds_read_b128 v[64:67], v1 offset:2048\nv_exp_f32 v59, v59\nv_mfma_f32_16x16x32_bf16 v[24:27], a[40:43], v[80:83], v[24:27]\nv_add_f32 v63, v63, v58\nv_fma_f32 v49, v49, s58, v62\nv_add_f32 v63, v63, v59\nv_mfma_f32_16x16x32_bf16 v[28:31], a[48:51], v[80:83], v[28:31]\n" \
"s_waitcnt lgkmcnt(4)\nv_exp_f32 v48, v48\nv_mfma_f32_16x16x32_bf16 v[32:35], a[32:35], v[84:87], v[32:35]\nds_read_b128 v[80:83], v2 offset:8192\nv_exp_f32 v49, v49\nv_mfma_f32_16x16x32_bf16 v[36:39], v[72:75], v[84:87], v[36:39]\nv_add_f32 v63, v63, v48\nv_add_f32 v62, v63, v49\n" \
"v_add_f32 v50, v63, v49\nv_mfma_f32_16x16x32_bf16 v[40:43], a[40:43], v[84:87], v[40:43]\ns_nop 0\nv_permlane16_swap_b32 v62, v50\nv_add_f32 v63, v62, v50\nv_mfma_f32_16x16x32_bf16 v[44:47], a[48:51], v[84:87], v[44:47]\ns_waitcnt lgkmcnt(3)\nv_add_f32 v50, v62, v50\n" \
"v_mfma_f32_16x16x32_bf16 v[16:19], a[36:39], v[88:91], v[16:19]\nds_read_b128 v[84:87], v2 offset:8448\nv_permlane32_swap_b32 v63, v50\nv_mfma_f32_16x16x32_bf16 v[20:23], v[76:79], v[88:91], v[20:23]\nds_read_b128 v[68:71], v1 offset:3072\nv_add_f32 v63, v63, v50\nv_mfma_f32_16x16x32_bf16 v[24:27], a[44:47], v[88:91], v[24:27]\nv_rcp_f32 v63, v63\n" \
"v_mfma_f32_16x16x32_bf16 v[28:31], a[52:55], v[88:91], v[28:31]\ns_waitcnt lgkmcnt(4)\nv_mul_f32 v52, v52, v63\nv_mul_f32 v53, v53, v63\nv_mul_f32 v54, v54, v63\nv_mfma_f32_16x16x32_bf16 v[32:35], a[36:39], v[92:95], v[32:35]\nds_read_b128 v[88:91], v2 offset:10240\nv_mul_f32 v55, v55, v63\n" \
"v_mul_f32 v56, v56, v63\nv_mfma_f32_16x16x32_bf16 v[36:39], v[76:79], v[92:95], v[36:39]\nv_mul_f32 v57, v57, v63\nv_mul_f32 v58, v58, v63\nv_mul_f32 v59, v59, v63\nv_mfma_f32_16x16x32_bf16 v[40:43], a[44:47], v[92:95], v[40:43]\nv_mul_f32 v48, v48, v63\nv_mul_f32 v49, v49, v63\n" \
"v_cvt_pk_bf16_f32 v52, v52, v53\nv_mfma_f32_16x16x32_bf16 v[44:47], a[52:55], v[92:95], v[44:47]\ns_waitcnt lgkmcnt(3)\nv_cvt_pk_bf16_f32 v53, v54, v55\nv_cvt_pk_bf16_f32 v48, v48, v49\nv_cvt_pk_bf16_f32 v54, v56, v57\nv_mfma_f32_16x16x32_bf16 v[16:19], a[56:59], v[80:83], v[16:19]\nds_read_b128 v[92:95], v2 offset:10496\n" \
"v_cvt_pk_bf16_f32 v55, v58, v59\nv_bfi_b32 v52, s61, v52, v61\nv_mfma_f32_16x16x32_bf16 v[20:23], a[64:67], v[80:83], v[20:23]\nds_read_b128 v[72:75], v1 offset:4096\nv_bfi_b32 v53, s61, v53, v61\nv_bfi_b32 v54, s61, v54, v61\nv_mfma_f32_16x16x32_bf16 v[24:27], v[64:67], v[80:83], v[24:27]\nv_bfi_b32 v55, s61, v55, v61\n" \
"v_bfi_b32 v48, s61, v48, v61\nv_mfma_f32_16x16x32_bf16 v[28:31], a[72:75], v[80:83], v[28:31]\ns_waitcnt lgkmcnt(4)\nbuffer_store_dwordx4 v[52:55], v10, s[36:39], s55 offen sc1\nv_mov_b32 v49, v48\nv_mfma_f32_16x16x32_bf16 v[32:35], a[56:59], v[84:87], v[32:35]\nds_read_b128 v[80:83], v2 offset:12288\nv_permlane16_swap_b32 v48, v49\n" \
"v_mfma_f32_16x16x32_bf16 v[36:39], a[64:67], v[84:87], v[36:39]\nv_mov_b32 v50, v48\nv_mov_b32 v51, v49\nv_mfma_f32_16x16x32_bf16 v[40:43], v[64:67], v[84:87], v[40:43]\nv_permlane32_swap_b32 v48, v50\nv_mfma_f32_16x16x32_bf16 v[44:47], a[72:75], v[84:87], v[44:47]\ns_waitcnt lgkmcnt(3)\nv_permlane32_swap_b32 v49, v51\n" \
"v_mfma_f32_16x16x32_bf16 v[16:19], a[60:63], v[88:91], v[16:19]\nds_read_b128 v[84:87], v2 offset:12544\nv_mfma_f32_16x16x32_bf16 v[20:23], a[68:71], v[88:91], v[20:23]\nds_read_b128 v[76:79], v1 offset:5120\nv_mfma_f32_16x16x32_bf16 v[24:27], v[68:71], v[88:91], v[24:27]\nbuffer_store_dwordx4 v[48:51], v11, s[36:39], s55 offen sc1\nv_cmp_ne_u32 vcc, 0, v60\nv_mfma_f32_16x16x32_bf16 v[28:31], a[76:79], v[88:91], v[28:31]\n" \
"s_waitcnt lgkmcnt(4)\nv_mfma_f32_16x16x32_bf16 v[32:35], a[60:63], v[92:95], v[32:35]\nds_read_b128 v[88:91], v2 offset:14336\nv_mfma_f32_16x16x32_bf16 v[36:39], a[68:71], v[92:95], v[36:39]\nv_mfma_f32_16x16x32_bf16 v[40:43], v[68:71], v[92:95], v[40:43]\nv_mfma_f32_16x16x32_bf16 v[44:47], a[76:79], v[92:95], v[44:47]\ns_cbranch_vccnz .Lqk_pj%=\ns_cmp_lt_i32 s40, 63\n" \
"s_waitcnt lgkmcnt(0)\ns_cbranch_scc1 .Lqk_pwait%=\ns_branch .Lqk_end%=\n.Lqk_re2%=:\nv_max_u32 v17, v80, v82\nv_cmp_eq_u32 vcc, -1, v17\ns_cbranch_vccz .Lqk_sk2418%=\ns_mov_b64 exec, vcc\n" \
"buffer_load_dwordx4 v[80:83], v3, s[36:39], s42 offen offset:0 sc1\ns_mov_b64 exec, -1\n.Lqk_sk2418%=:\nv_max_u32 v17, v84, v86\nv_cmp_eq_u32 vcc, -1, v17\ns_cbranch_vccz .Lqk_sk2425%=\ns_mov_b64 exec, vcc\nbuffer_load_dwordx4 v[84:87], v3, s[36:39], s42 offen offset:1024 sc1\n" \
"s_mov_b64 exec, -1\n.Lqk_sk2425%=:\nv_max_u32 v17, v88, v90\nv_cmp_eq_u32 vcc, -1, v17\ns_cbranch_vccz .Lqk_sk2432%=\ns_mov_b64 exec, vcc\nbuffer_load_dwordx4 v[88:91], v3, s[36:39], s42 offen offset:2048 sc1\ns_mov_b64 exec, -1\n" \
".Lqk_sk2432%=:\nv_max_u32 v17, v92, v94\nv_cmp_eq_u32 vcc, -1, v17\ns_cbranch_vccz .Lqk_sk2439%=\ns_mov_b64 exec, vcc\nbuffer_load_dwordx4 v[92:95], v3, s[36:39], s42 offen offset:3072 sc1\ns_mov_b64 exec, -1\n.Lqk_sk2439%=:\n" \
"v_mov_b64 v[48:49], 0\nv_mov_b64 v[50:51], 0\nv_mov_b64 v[52:53], 0\nv_mov_b64 v[54:55], 0\nv_mov_b64 v[56:57], 0\nv_mov_b64 v[58:59], 0\nv_mov_b64 v[60:61], 0\nv_mov_b64 v[62:63], 0\n" \
"s_add_u32 s43, s43, 1\ns_cmp_lt_u32 s43, 262144\ns_cbranch_scc1 .Lqk_poll2%=\ns_branch .Lqk_abort%=\n.Lqk_c3%=:\ns_waitcnt lgkmcnt(3)\nv_mfma_f32_16x16x32_bf16 v[32:35], v[232:235], v[56:59], v[32:35]\ns_add_u32 s44, s40, 1\n" \
"v_mov_b64 v[52:53], 0\nv_mov_b64 v[54:55], 0\nv_mfma_f32_16x16x32_bf16 v[36:39], v[240:243], v[56:59], v[36:39]\ns_lshl_b32 s44, s44, 17\ns_waitcnt lgkmcnt(2)\nds_read_b128 v[64:67], v1 offset:18432\nv_mfma_f32_16x16x32_bf16 v[32:35], v[236:239], v[60:63], v[32:35]\ns_add_u32 s44, s44, %[xn]\n" \
"s_lshr_b32 s45, s44, 8\nds_read_b128 v[68:71], v1 offset:19456\nv_mfma_f32_16x16x32_bf16 v[36:39], v[244:247], v[60:63], v[36:39]\ns_and_b32 s45, s45, 0x2000\ns_xor_b32 s41, s44, s45\nv_mfma_f32_16x16x32_bf16 v[40:43], v[248:251], v[56:59], v[40:43]\nbuffer_load_dword v16, v12, s[36:39], s56 offen sc1\nv_mfma_f32_16x16x32_bf16 v[44:47], v[72:75], v[56:59], v[44:47]\n" \
"v_cvt_pk_bf16_f32 v36, v32, v36\nv_cvt_pk_bf16_f32 v37, v33, v37\nv_mfma_f32_16x16x32_bf16 v[40:43], v[252:255], v[60:63], v[40:43]\nv_cvt_pk_bf16_f32 v38, v34, v38\nv_cvt_pk_bf16_f32 v39, v35, v39\nv_mfma_f32_16x16x32_bf16 v[44:47], v[76:79], v[60:63], v[44:47]\nds_read_b128 v[72:75], v1 offset:20480\nds_read_b128 v[76:79], v1 offset:21504\n" \
"v_mov_b64 v[56:57], 0\nv_mov_b64 v[58:59], 0\nv_mov_b64 v[60:61], 0\nv_mov_b64 v[62:63], 0\n.Lqk_poll3%=:\ns_waitcnt vmcnt(1)\nv_smfmac_f32_16x16x64_bf16 v[48:51], v[80:83], v[152:159], v4\nds_write_b128 v0, v[80:83] offset:28672\n" \
"v_max3_u32 v17, v80, v82, v84\nv_smfmac_f32_16x16x64_bf16 v[52:55], v[80:83], v[160:167], v4\nv_max3_u32 v17, v17, v86, v88\nv_smfmac_f32_16x16x64_bf16 v[56:59], v[80:83], v[168:175], v4\nds_write_b128 v0, v[84:87] offset:29696\nv_max3_u32 v17, v17, v90, v92\nv_max_u32 v17, v17, v94\nv_smfmac_f32_16x16x64_bf16 v[60:63], v[80:83], v[176:183], v4\n" \
"v_cmp_eq_u32 vcc, -1, v17\nv_smfmac_f32_16x16x64_bf16 v[48:51], v[84:87], v[184:191], v4\nds_write_b128 v0, v[88:91] offset:30720\ns_cbranch_vccnz .Lqk_re3%=\ns_waitcnt lgkmcnt(5)\nv_smfmac_f32_16x16x64_bf16 v[52:55], v[84:87], v[64:71], v4\nv_smfmac_f32_16x16x64_bf16 v[56:59], v[84:87], v[192:199], v4\nds_write_b128 v0, v[92:95] offset:31744\n" \
"v_smfmac_f32_16x16x64_bf16 v[60:63], v[84:87], v[200:207], v4\nv_smfmac_f32_16x16x64_bf16 v[48:51], v[88:91], v[208:215], v4\nv_smfmac_f32_16x16x64_bf16 v[52:55], v[88:91], v[216:223], v4\ns_waitcnt lgkmcnt(0)\n.Lqk_jn3%=:\ns_barrier\nds_read_b128 v[80:83], v0 offset:16384\nds_read_b128 v[64:67], v1 offset:22528\n" \
"ds_read_b128 v[68:71], v1 offset:23552\nv_smfmac_f32_16x16x64_bf16 v[56:59], v[88:91], v[72:79], v4\ns_waitcnt vmcnt(0)\nds_read_b128 v[72:75], v1 offset:0\nds_read_b128 v[76:79], v1 offset:1024\nv_cmp_ge_i32 vcc, s40, v16\nv_smfmac_f32_16x16x64_bf16 v[60:63], v[88:91], v[224:231], v4\ns_cmp_eq_u64 vcc, 0\n" \
"s_cselect_b32 s61, s41, -2147483648\ns_cselect_b32 s44, 1, 0\nds_read_b128 v[84:87], v0 offset:17408\nds_read_b128 v[88:91], v0 offset:18432\nv_smfmac_f32_16x16x64_bf16 v[48:51], v[92:95], v[232:239], v4\ns_add_u32 m0, %[l0], 0\ns_add_u32 s60, s61, 0\nds_write_b128 v5, v[36:39] offset:256\n" \
"buffer_load_dwordx4 v0, s[36:39], s60 offen offset:0 sc1 lds\nv_cvt_pk_bf16_f32 v44, v40, v44\nv_cvt_pk_bf16_f32 v45, v41, v45\nv_cvt_pk_bf16_f32 v46, v42, v46\nv_cvt_pk_bf16_f32 v47, v43, v47\nv_smfmac_f32_16x16x64_bf16 v[52:55], v[92:95], v[240:247], v4\nv_mov_b32 v16, s44\nv_smfmac_f32_16x16x64_bf16 v[56:59], v[92:95], v[248:255], v4\n" \
"s_waitcnt lgkmcnt(5)\nbuffer_load_dwordx4 v0, s[36:39], s60 offen offset:1024 sc1 lds\nv_smfmac_f32_16x16x64_bf16 v[60:63], v[92:95], v[64:71], v4\nds_read_b128 v[92:95], v0 offset:19456\nds_read_b128 v[64:67], v1 offset:2048\nds_read_b128 v[68:71], v1 offset:3072\nds_write_b32 v0, v16 offset:53760\nv_smfmac_f32_16x16x64_bf16 v[48:51], v[80:83], a[0:7], v4\n" \
"ds_write_b128 v5, v[44:47] offset:768\nbuffer_load_dwordx4 v0, s[36:39], s60 offen offset:2048 sc1 lds\nv_smfmac_f32_16x16x64_bf16 v[52:55], v[80:83], a[8:15], v4\nv_smfmac_f32_16x16x64_bf16 v[56:59], v[80:83], a[16:23], v4\nbuffer_load_dwordx4 v0, s[36:39], s60 offen offset:3072 sc1 lds\nv_smfmac_f32_16x16x64_bf16 v[60:63], v[80:83], a[24:31], v4\ns_waitcnt lgkmcnt(7)\ns_add_u32 m0, %[l0], 4096\n" \
"ds_read_b128 v[80:83], v0 offset:20480\nv_smfmac_f32_16x16x64_bf16 v[48:51], v[84:87], a[32:39], v4\ns_add_u32 s60, s61, 4096\nbuffer_load_dwordx4 v0, s[36:39], s60 offen offset:0 sc1 lds\nv_smfmac_f32_16x16x64_bf16 v[52:55], v[84:87], v[72:79], v4\nds_read_b128 v[72:75], v1 offset:4096\nds_read_b128 v[76:79], v1 offset:5120\nv_smfmac_f32_16x16x64_bf16 v[56:59], v[84:87], a[40:47], v4\n" \
"buffer_load_dwordx4 v0, s[36:39], s60 offen offset:1024 sc1 lds\nv_smfmac_f32_16x16x64_bf16 v[60:63], v[84:87], a[48:55], v4\ns_waitcnt lgkmcnt(9)\nds_read_b128 v[84:87], v0 offset:21504\nv_smfmac_f32_16x16x64_bf16 v[48:51], v[88:91], a[56:63], v4\ns_waitcnt lgkmcnt(4)\ns_barrier\nbuffer_load_dwordx4 v0, s[36:39], s60 offen offset:2048 sc1 lds\n" \
"v_smfmac_f32_16x16x64_bf16 v[52:55], v[88:91], a[64:71], v4\nv_smfmac_f32_16x16x64_bf16 v[56:59], v[88:91], v[64:71], v4\nds_read_b128 v[64:67], v1 offset:6144\nds_read_b128 v[68:71], v1 offset:7168\nbuffer_load_dwordx4 v0, s[36:39], s60 offen offset:3072 sc1 lds\nv_smfmac_f32_16x16x64_bf16 v[60:63], v[88:91], a[72:79], v4\ns_add_u32 m0, %[l0], 8192\nds_read_b128 v[88:91], v0 offset:22528\n" \
"v_smfmac_f32_16x16x64_bf16 v[48:51], v[92:95], a[80:87], v4\ns_add_u32 s60, s61, 16384\nbuffer_load_dwordx4 v0, s[36:39], s60 offen offset:0 sc1 lds\nv_smfmac_f32_16x16x64_bf16 v[52:55], v[92:95], a[88:95], v4\nv_smfmac_f32_16x16x64_bf16 v[56:59], v[92:95], a[96:103], v4\ns_waitcnt lgkmcnt(4)\nbuffer_load_dwordx4 v0, s[36:39], s60 offen offset:1024 sc1 lds\nv_smfmac_f32_16x16x64_bf16 v[60:63], v[92:95], v[72:79], v4\n" \
"ds_read_b128 v[92:95], v0 offset:23552\nds_read_b128 v[72:75], v1 offset:8192\nds_read_b128 v[76:79], v1 offset:9216\nv_smfmac_f32_16x16x64_bf16 v[48:51], v[80:83], a[104:111], v4\nbuffer_load_dwordx4 v0, s[36:39], s60 offen offset:2048 sc1 lds\nv_smfmac_f32_16x16x64_bf16 v[52:55], v[80:83], a[112:119], v4\nv_smfmac_f32_16x16x64_bf16 v[56:59], v[80:83], a[120:127], v4\nbuffer_load_dwordx4 v0, s[36:39], s60 offen offset:3072 sc1 lds\n" \
"v_smfmac_f32_16x16x64_bf16 v[60:63], v[80:83], a[128:135], v4\ns_waitcnt lgkmcnt(6)\ns_add_u32 m0, %[l0], 12288\nds_read_b128 v[80:83], v0 offset:24576\nv_smfmac_f32_16x16x64_bf16 v[48:51], v[84:87], a[136:143], v4\ns_add_u32 s60, s61, 20480\ns_waitcnt lgkmcnt(5)\nbuffer_load_dwordx4 v0, s[36:39], s60 offen offset:0 sc1 lds\n" \
"v_smfmac_f32_16x16x64_bf16 v[52:55], v[84:87], v[64:71], v4\nds_read_b128 v[64:67], v1 offset:10240\nds_read_b128 v[68:71], v1 offset:11264\nv_smfmac_f32_16x16x64_bf16 v[56:59], v[84:87], a[144:151], v4\nbuffer_load_dwordx4 v0, s[36:39], s60 offen offset:1024 sc1 lds\nv_smfmac_f32_16x16x64_bf16 v[60:63], v[84:87], a[152:159], v4\ns_waitcnt lgkmcnt(6)\nds_read_b128 v[84:87], v0 offset:25600\n" \
"v_smfmac_f32_16x16x64_bf16 v[48:51], v[88:91], a[160:167], v4\nbuffer_load_dwordx4 v0, s[36:39], s60 offen offset:2048 sc1 lds\nv_smfmac_f32_16x16x64_bf16 v[52:55], v[88:91], a[168:175], v4\ns_waitcnt lgkmcnt(4)\nv_smfmac_f32_16x16x64_bf16 v[56:59], v[88:91], v[72:79], v4\nds_read_b128 v[72:75], v1 offset:12288\nds_read_b128 v[76:79], v1 offset:13312\nbuffer_load_dwordx4 v0, s[36:39], s60 offen offset:3072 sc1 lds\n" \
"v_smfmac_f32_16x16x64_bf16 v[60:63], v[88:91], a[176:183], v4\nds_read_b128 v[88:91], v0 offset:26624\nv_smfmac_f32_16x16x64_bf16 v[48:51], v[92:95], a[184:191], v4\nv_smfmac_f32_16x16x64_bf16 v[52:55], v[92:95], a[192:199], v4\nv_smfmac_f32_16x16x64_bf16 v[56:59], v[92:95], a[200:207], v4\ns_waitcnt lgkmcnt(4)\nv_smfmac_f32_16x16x64_bf16 v[60:63], v[92:95], v[64:71], v4\nds_read_b128 v[92:95], v0 offset:27648\n" \
"ds_read_b128 v[64:67], v1 offset:14336\nds_read_b128 v[68:71], v1 offset:15360\nv_smfmac_f32_16x16x64_bf16 v[48:51], v[80:83], a[208:215], v4\nv_smfmac_f32_16x16x64_bf16 v[52:55], v[80:83], a[216:223], v4\nv_smfmac_f32_16x16x64_bf16 v[56:59], v[80:83], a[224:231], v4\nv_smfmac_f32_16x16x64_bf16 v[60:63], v[80:83], a[232:239], v4\ns_waitcnt lgkmcnt(6)\nv_smfmac_f32_16x16x64_bf16 v[48:51], v[84:87], a[240:247], v4\n" \
"s_waitcnt lgkmcnt(4)\nv_smfmac_f32_16x16x64_bf16 v[52:55], v[84:87], v[72:79], v4\nds_read_b128 v[72:75], v1 offset:16384\nds_read_b128 v[76:79], v1 offset:17408\nv_smfmac_f32_16x16x64_bf16 v[56:59], v[84:87], a[248:255], v4\nv_smfmac_f32_16x16x64_bf16 v[60:63], v[84:87], v[96:103], v4\ns_waitcnt lgkmcnt(5)\nv_smfmac_f32_16x16x64_bf16 v[48:51], v[88:91], v[104:111], v4\n" \
"v_smfmac_f32_16x16x64_bf16 v[52:55], v[88:91], v[112:119], v4\ns_waitcnt lgkmcnt(4)\nv_smfmac_f32_16x16x64_bf16 v[48:51], v[92:95], v[128:135], v4\nv_smfmac_f32_16x16x64_bf16 v[52:55], v[92:95], v[136:143], v4\ns_waitcnt lgkmcnt(2)\nv_smfmac_f32_16x16x64_bf16 v[56:59], v[88:91], v[64:71], v4\nv_add_f32 v48, v48, v50\nv_smfmac_f32_16x16x64_bf16 v[60:63], v[88:91], v[120:127], v4\n" \
"v_add_f32 v52, v52, v54\nv_add_f32 v49, v49, v51\nv_smfmac_f32_16x16x64_bf16 v[56:59], v[92:95], v[144:151], v4\ns_waitcnt lgkmcnt(0)\nv_add_f32 v53, v53, v55\nv_cvt_pk_bf16_f32 v50, v48, v52\nv_smfmac_f32_16x16x64_bf16 v[60:63], v[92:95], v[72:79], v4\nv_cvt_pk_bf16_f32 v51, v49, v53\n" \
"ds_write2_b32 v6, v50, v51 offset0:0 offset1:4\ns_nop 1\nv_add_f32 v56, v56, v58\nv_add_f32 v57, v57, v59\ns_nop 1\nv_add_f32 v60, v60, v62\nv_add_f32 v61, v61, v63\nv_cvt_pk_bf16_f32 v62, v56, v60\n" \
"v_cvt_pk_bf16_f32 v63, v57, v61\nds_write2_b32 v6, v62, v63 offset0:36 offset1:40\ns_waitcnt vmcnt(0)\ns_waitcnt lgkmcnt(0)\ns_barrier\ns_cbranch_vccnz .Lqk_slow%=\ns_add_u32 m0, %[l0], 16384\ns_add_u32 s60, s41, 32768\n" \
"buffer_load_dwordx4 v0, s[36:39], s60 offen offset:0 sc1 lds\nbuffer_load_dwordx4 v0, s[36:39], s60 offen offset:1024 sc1 lds\nbuffer_load_dwordx4 v0, s[36:39], s60 offen offset:2048 sc1 lds\nbuffer_load_dwordx4 v0, s[36:39], s60 offen offset:3072 sc1 lds\ns_add_u32 m0, %[l0], 20480\ns_add_u32 s60, s41, 36864\nbuffer_load_dwordx4 v0, s[36:39], s60 offen offset:0 sc1 lds\nbuffer_load_dwordx4 v0, s[36:39], s60 offen offset:1024 sc1 lds\n" \
"ds_read_b128 v[80:83], v2 offset:0\nds_read_b128 v[84:87], v2 offset:256\nds_read_b128 v[88:91], v2 offset:2048\nbuffer_load_dwordx4 v0, s[36:39], s60 offen offset:2048 sc1 lds\nbuffer_load_dwordx4 v0, s[36:39], s60 offen offset:3072 sc1 lds\ns_add_u32 m0, %[l0], 24576\ns_add_u32 s60, s41, 49152\nbuffer_load_dwordx4 v0, s[36:39], s60 offen offset:0 sc1 lds\n" \
"buffer_load_dwordx4 v0, s[36:39], s60 offen offset:1024 sc1 lds\nbuffer_load_dwordx4 v0, s[36:39], s60 offen offset:2048 sc1 lds\nbuffer_load_dwordx4 v0, s[36:39], s60 offen offset:3072 sc1 lds\ns_add_u32 m0, %[l0], 28672\ns_add_u32 s60, s41, 53248\nbuffer_load_dwordx4 v0, s[36:39], s60 offen offset:0 sc1 lds\nbuffer_load_dwordx4 v0, s[36:39], s60 offen offset:1024 sc1 lds\nbuffer_load_dwordx4 v0, s[36:39], s60 offen offset:2048 sc1 lds\n" \
"buffer_load_dwordx4 v0, s[36:39], s60 offen offset:3072 sc1 lds\ns_waitcnt lgkmcnt(2)\nv_mfma_f32_16x16x32_bf16 v[16:19], a[0:3], v[80:83], 0\nds_read_b128 v[92:95], v2 offset:2304\nv_mfma_f32_16x16x32_bf16 v[20:23], a[8:11], v[80:83], 0\nds_read_b128 v[72:75], v1 offset:0\nv_mfma_f32_16x16x32_bf16 v[24:27], a[16:19], v[80:83], 0\nv_mfma_f32_16x16x32_bf16 v[28:31], a[24:27], v[80:83], 0\n" \
"s_waitcnt lgkmcnt(3)\nv_mfma_f32_16x16x32_bf16 v[32:35], a[0:3], v[84:87], 0\nds_read_b128 v[80:83], v2 offset:4096\nv_mfma_f32_16x16x32_bf16 v[36:39], a[8:11], v[84:87], 0\nv_mfma_f32_16x16x32_bf16 v[40:43], a[16:19], v[84:87], 0\nv_mfma_f32_16x16x32_bf16 v[44:47], a[24:27], v[84:87], 0\ns_waitcnt lgkmcnt(3)\nv_mfma_f32_16x16x32_bf16 v[16:19], a[4:7], v[88:91], v[16:19]\n" \
"ds_read_b128 v[84:87], v2 offset:4352\nv_mfma_f32_16x16x32_bf16 v[20:23], a[12:15], v[88:91], v[20:23]\nds_read_b128 v[76:79], v1 offset:1024\nv_mfma_f32_16x16x32_bf16 v[24:27], a[20:23], v[88:91], v[24:27]\nv_mfma_f32_16x16x32_bf16 v[28:31], a[28:31], v[88:91], v[28:31]\ns_waitcnt lgkmcnt(4)\nv_mfma_f32_16x16x32_bf16 v[32:35], a[4:7], v[92:95], v[32:35]\nds_read_b128 v[88:91], v2 offset:6144\n" \
"v_mfma_f32_16x16x32_bf16 v[36:39], a[12:15], v[92:95], v[36:39]\nv_mfma_f32_16x16x32_bf16 v[40:43], a[20:23], v[92:95], v[40:43]\nv_mfma_f32_16x16x32_bf16 v[44:47], a[28:31], v[92:95], v[44:47]\ns_waitcnt lgkmcnt(3)\nv_mfma_f32_16x16x32_bf16 v[16:19], a[32:35], v[80:83], v[16:19]\nds_read_b128 v[92:95], v2 offset:6400\nv_mfma_f32_16x16x32_bf16 v[20:23], v[72:75], v[80:83], v[20:23]\nds_read_b128 v[64:67], v1 offset:2048\n" \
"v_mfma_f32_16x16x32_bf16 v[24:27], a[40:43], v[80:83], v[24:27]\nv_mfma_f32_16x16x32_bf16 v[28:31], a[48:51], v[80:83], v[28:31]\ns_waitcnt lgkmcnt(4)\nv_mfma_f32_16x16x32_bf16 v[32:35], a[32:35], v[84:87], v[32:35]\nds_read_b128 v[80:83], v2 offset:8192\nv_mfma_f32_16x16x32_bf16 v[36:39], v[72:75], v[84:87], v[36:39]\nv_mfma_f32_16x16x32_bf16 v[40:43], a[40:43], v[84:87], v[40:43]\nv_mfma_f32_16x16x32_bf16 v[44:47], a[48:51], v[84:87], v[44:47]\n" \
"s_waitcnt lgkmcnt(3)\nv_mfma_f32_16x16x32_bf16 v[16:19], a[36:39], v[88:91], v[16:19]\nds_read_b128 v[84:87], v2 offset:8448\nv_mfma_f32_16x16x32_bf16 v[20:23], v[76:79], v[88:91], v[20:23]\nds_read_b128 v[68:71], v1 offset:3072\nv_mfma_f32_16x16x32_bf16 v[24:27], a[44:47], v[88:91], v[24:27]\nv_mfma_f32_16x16x32_bf16 v[28:31], a[52:55], v[88:91], v[28:31]\ns_waitcnt lgkmcnt(4)\n" \
"v_mfma_f32_16x16x32_bf16 v[32:35], a[36:39], v[92:95], v[32:35]\nds_read_b128 v[88:91], v2 offset:10240\nv_mfma_f32_16x16x32_bf16 v[36:39], v[76:79], v[92:95], v[36:39]\nv_mfma_f32_16x16x32_bf16 v[40:43], a[44:47], v[92:95], v[40:43]\nv_mfma_f32_16x16x32_bf16 v[44:47], a[52:55], v[92:95], v[44:47]\ns_waitcnt lgkmcnt(3)\nv_mfma_f32_16x16x32_bf16 v[16:19], a[56:59], v[80:83], v[16:19]\nds_read_b128 v[92:95], v2 offset:10496\n" \
"v_mfma_f32_16x16x32_bf16 v[20:23], a[64:67], v[80:83], v[20:23]\nds_read_b128 v[72:75], v1 offset:4096\nv_mfma_f32_16x16x32_bf16 v[24:27], v[64:67], v[80:83], v[24:27]\nv_mfma_f32_16x16x32_bf16 v[28:31], a[72:75], v[80:83], v[28:31]\ns_waitcnt lgkmcnt(4)\nv_mfma_f32_16x16x32_bf16 v[32:35], a[56:59], v[84:87], v[32:35]\nds_read_b128 v[80:83], v2 offset:12288\nv_mfma_f32_16x16x32_bf16 v[36:39], a[64:67], v[84:87], v[36:39]\n" \
"v_mfma_f32_16x16x32_bf16 v[40:43], v[64:67], v[84:87], v[40:43]\nv_mfma_f32_16x16x32_bf16 v[44:47], a[72:75], v[84:87], v[44:47]\ns_waitcnt lgkmcnt(3)\nv_mfma_f32_16x16x32_bf16 v[16:19], a[60:63], v[88:91], v[16:19]\nds_read_b128 v[84:87], v2 offset:12544\nv_mfma_f32_16x16x32_bf16 v[20:23], a[68:71], v[88:91], v[20:23]\nds_read_b128 v[76:79], v1 offset:5120\nv_mfma_f32_16x16x32_bf16 v[24:27], v[68:71], v[88:91], v[24:27]\n" \
"v_mfma_f32_16x16x32_bf16 v[28:31], a[76:79], v[88:91], v[28:31]\ns_waitcnt lgkmcnt(4)\nv_mfma_f32_16x16x32_bf16 v[32:35], a[60:63], v[92:95], v[32:35]\nds_read_b128 v[88:91], v2 offset:14336\nv_mfma_f32_16x16x32_bf16 v[36:39], a[68:71], v[92:95], v[36:39]\nv_mfma_f32_16x16x32_bf16 v[40:43], v[68:71], v[92:95], v[40:43]\nv_mfma_f32_16x16x32_bf16 v[44:47], a[76:79], v[92:95], v[44:47]\ns_waitcnt vmcnt(0)\n" \
"s_branch .Lqk_pj%=\n.Lqk_slow%=:\ns_cmp_ge_i32 s40, 63\ns_cbranch_scc1 .Lqk_end%=\n.Lqk_fetch0%=:\nbuffer_load_dword v16, v12, s[36:39], s56 offen sc1\ns_waitcnt vmcnt(0)\nv_cmp_ge_i32 vcc, s40, v16\n" \
"s_cbranch_vccz .Lqk_fok%=\ns_add_u32 s43, s43, 1\ns_cmp_lt_u32 s43, 262144\ns_cbranch_scc1 .Lqk_fetch0%=\ns_branch .Lqk_abort%=\n.Lqk_fok%=:\ns_add_u32 s44, s40, 1\ns_lshl_b32 s44, s44, 17\n" \
"s_add_u32 s44, s44, %[xn]\ns_lshr_b32 s45, s44, 8\ns_and_b32 s45, s45, 0x2000\ns_xor_b32 s41, s44, s45\ns_add_u32 m0, %[l0], 0\ns_add_u32 s60, s41, 0\nbuffer_load_dwordx4 v0, s[36:39], s60 offen offset:0 sc1 lds\nbuffer_load_dwordx4 v0, s[36:39], s60 offen offset:1024 sc1 lds\n" \
"buffer_load_dwordx4 v0, s[36:39], s60 offen offset:2048 sc1 lds\nbuffer_load_dwordx4 v0, s[36:39], s60 offen offset:3072 sc1 lds\ns_add_u32 m0, %[l0], 4096\ns_add_u32 s60, s41, 4096\nbuffer_load_dwordx4 v0, s[36:39], s60 offen offset:0 sc1 lds\nbuffer_load_dwordx4 v0, s[36:39], s60 offen offset:1024 sc1 lds\nbuffer_load_dwordx4 v0, s[36:39], s60 offen offset:2048 sc1 lds\nbuffer_load_dwordx4 v0, s[36:39], s60 offen offset:3072 sc1 lds\n" \
"s_add_u32 m0, %[l0], 8192\ns_add_u32 s60, s41, 16384\nbuffer_load_dwordx4 v0, s[36:39], s60 offen offset:0 sc1 lds\nbuffer_load_dwordx4 v0, s[36:39], s60 offen offset:1024 sc1 lds\nbuffer_load_dwordx4 v0, s[36:39], s60 offen offset:2048 sc1 lds\nbuffer_load_dwordx4 v0, s[36:39], s60 offen offset:3072 sc1 lds\ns_add_u32 m0, %[l0], 12288\ns_add_u32 s60, s41, 20480\n" \
"buffer_load_dwordx4 v0, s[36:39], s60 offen offset:0 sc1 lds\nbuffer_load_dwordx4 v0, s[36:39], s60 offen offset:1024 sc1 lds\nbuffer_load_dwordx4 v0, s[36:39], s60 offen offset:2048 sc1 lds\nbuffer_load_dwordx4 v0, s[36:39], s60 offen offset:3072 sc1 lds\ns_add_u32 m0, %[l0], 16384\ns_add_u32 s60, s41, 32768\nbuffer_load_dwordx4 v0, s[36:39], s60 offen offset:0 sc1 lds\nbuffer_load_dwordx4 v0, s[36:39], s60 offen offset:1024 sc1 lds\n" \
"buffer_load_dwordx4 v0, s[36:39], s60 offen offset:2048 sc1 lds\nbuffer_load_dwordx4 v0, s[36:39], s60 offen offset:3072 sc1 lds\ns_add_u32 m0, %[l0], 20480\ns_add_u32 s60, s41, 36864\nbuffer_load_dwordx4 v0, s[36:39], s60 offen offset:0 sc1 lds\nbuffer_load_dwordx4 v0, s[36:39], s60 offen offset:1024 sc1 lds\nbuffer_load_dwordx4 v0, s[36:39], s60 offen offset:2048 sc1 lds\nbuffer_load_dwordx4 v0, s[36:39], s60 offen offset:3072 sc1 lds\n" \
"s_add_u32 m0, %[l0], 24576\ns_add_u32 s60, s41, 49152\nbuffer_load_dwordx4 v0, s[36:39], s60 offen offset:0 sc1 lds\nbuffer_load_dwordx4 v0, s[36:39], s60 offen offset:1024 sc1 lds\nbuffer_load_dwordx4 v0, s[36:39], s60 offen offset:2048 sc1 lds\nbuffer_load_dwordx4 v0, s[36:39], s60 offen offset:3072 sc1 lds\ns_add_u32 m0, %[l0], 28672\ns_add_u32 s60, s41, 53248\n" \
"buffer_load_dwordx4 v0, s[36:39], s60 offen offset:0 sc1 lds\nbuffer_load_dwordx4 v0, s[36:39], s60 offen offset:1024 sc1 lds\nbuffer_load_dwordx4 v0, s[36:39], s60 offen offset:2048 sc1 lds\nbuffer_load_dwordx4 v0, s[36:39], s60 offen offset:3072 sc1 lds\ns_waitcnt vmcnt(0)\ns_branch .Lqk_pwait%=\n.Lqk_re3%=:\nv_max_u32 v17, v80, v82\n" \
"v_cmp_eq_u32 vcc, -1, v17\ns_cbranch_vccz .Lqk_sk2851%=\ns_mov_b64 exec, vcc\nbuffer_load_dwordx4 v[80:83], v3, s[36:39], s42 offen offset:0 sc1\ns_mov_b64 exec, -1\n.Lqk_sk2851%=:\nv_max_u32 v17, v84, v86\nv_cmp_eq_u32 vcc, -1, v17\n" \
"s_cbranch_vccz .Lqk_sk2858%=\ns_mov_b64 exec, vcc\nbuffer_load_dwordx4 v[84:87], v3, s[36:39], s42 offen offset:1024 sc1\ns_mov_b64 exec, -1\n.Lqk_sk2858%=:\nv_max_u32 v17, v88, v90\nv_cmp_eq_u32 vcc, -1, v17\ns_cbranch_vccz .Lqk_sk2865%=\n" \
"s_mov_b64 exec, vcc\nbuffer_load_dwordx4 v[88:91], v3, s[36:39], s42 offen offset:2048 sc1\ns_mov_b64 exec, -1\n.Lqk_sk2865%=:\nv_max_u32 v17, v92, v94\nv_cmp_eq_u32 vcc, -1, v17\ns_cbranch_vccz .Lqk_sk2872%=\ns_mov_b64 exec, vcc\n" \
"buffer_load_dwordx4 v[92:95], v3, s[36:39], s42 offen offset:3072 sc1\ns_mov_b64 exec, -1\n.Lqk_sk2872%=:\nbuffer_load_dword v16, v12, s[36:39], s56 offen sc1\nv_mov_b64 v[48:49], 0\nv_mov_b64 v[50:51], 0\nv_mov_b64 v[52:53], 0\nv_mov_b64 v[54:55], 0\n" \
"v_mov_b64 v[56:57], 0\nv_mov_b64 v[58:59], 0\nv_mov_b64 v[60:61], 0\nv_mov_b64 v[62:63], 0\ns_add_u32 s43, s43, 1\ns_cmp_lt_u32 s43, 262144\ns_cbranch_scc1 .Lqk_poll3%=\ns_branch .Lqk_abort%=\n" \
".Lqk_abort%=:\ns_mov_b64 exec, -1\nv_mov_b32 v16, 0\ns_movk_i32 s44, 6400\nbuffer_store_dword v16, off, s[36:39], s44 sc1\n.Lqk_end%=:\ns_waitcnt vmcnt(0) lgkmcnt(0)\n" \
""
#define VU_ASM \
"v_mbcnt_lo_u32_b32 v255, -1, 0\nv_mbcnt_hi_u32_b32 v255, -1, v255\nv_lshlrev_b32 v255, 2, v255\nv_add_u32 v255, %[tb], v255\nds_read_b32 v0, v255 offset:0\nds_read_b32 v1, v255 offset:256\nds_read_b32 v2, v255 offset:512\nds_read_b32 v3, v255 offset:768\n" \
"ds_read_b32 v4, v255 offset:1024\nds_read_b32 v5, v255 offset:1280\nds_read_b32 v6, v255 offset:1536\nds_read_b32 v7, v255 offset:1792\nds_read_b32 v8, v255 offset:2048\nds_read_b32 v9, v255 offset:2304\nds_read_b32 v10, v255 offset:2560\nds_read_b32 v11, v255 offset:2816\n" \
"ds_read_b32 v90, v255 offset:3072\nds_read_b32 v91, v255 offset:3328\nds_read_b32 v92, v255 offset:3584\nds_read_b32 v93, v255 offset:3840\nds_read_b32 v94, v255 offset:4096\nds_read_b32 v95, v255 offset:4352\ns_waitcnt lgkmcnt(0)\ns_mov_b64 s[36:37], %[ws]\n" \
"s_mov_b32 s38, %[sz]\ns_mov_b32 s39, 0x20000\ns_mov_b32 s45, 2096896\nbuffer_load_dword v15, off, s[36:39], s45 sc1\ns_add_u32 m0, %[lw], 16384\ns_add_u32 s44, %[wo], 0\nbuffer_load_dwordx4 v90, %[rw], s44 offen nt lds\ns_add_u32 m0, %[lw], 17408\n" \
"s_add_u32 s44, %[wo], 65536\nbuffer_load_dwordx4 v90, %[rw], s44 offen nt lds\ns_add_u32 m0, %[lw], 18432\ns_add_u32 s44, %[wo], 131072\nbuffer_load_dwordx4 v90, %[rw], s44 offen nt lds\ns_add_u32 m0, %[lw], 19456\ns_add_u32 s44, %[wo], 196608\nbuffer_load_dwordx4 v90, %[rw], s44 offen nt lds\n" \
"s_add_u32 m0, %[lw], 20480\ns_add_u32 s44, %[wo], 524288\nbuffer_load_dwordx4 v90, %[rw], s44 offen nt lds\ns_add_u32 m0, %[lw], 21504\ns_add_u32 s44, %[wo], 589824\nbuffer_load_dwordx4 v90, %[rw], s44 offen nt lds\ns_add_u32 m0, %[lw], 22528\ns_add_u32 s44, %[wo], 655360\n" \
"buffer_load_dwordx4 v90, %[rw], s44 offen nt lds\ns_add_u32 m0, %[lw], 23552\ns_add_u32 s44, %[wo], 720896\nbuffer_load_dwordx4 v90, %[rw], s44 offen nt lds\ns_add_u32 m0, %[tb], 0\ns_add_u32 s44, %[wo], 1048576\nbuffer_load_dwordx4 v90, %[rw], s44 offen nt lds\ns_add_u32 m0, %[tb], 1024\n" \
"s_add_u32 s44, %[wo], 1114112\nbuffer_load_dwordx4 v90, %[rw], s44 offen nt lds\ns_add_u32 m0, %[tb], 2048\ns_add_u32 s44, %[wo], 1179648\nbuffer_load_dwordx4 v90, %[rw], s44 offen nt lds\ns_add_u32 m0, %[tb], 3072\ns_add_u32 s44, %[wo], 1245184\nbuffer_load_dwordx4 v90, %[rw], s44 offen nt lds\n" \
"s_add_u32 m0, %[tb], 4096\ns_add_u32 s44, %[wo], 1572864\nbuffer_load_dwordx4 v90, %[rw], s44 offen nt lds\ns_add_u32 m0, %[tb], 5120\ns_add_u32 s44, %[wo], 1638400\nbuffer_load_dwordx4 v90, %[rw], s44 offen nt lds\ns_add_u32 m0, %[tb], 6144\ns_add_u32 s44, %[wo], 1703936\n" \
"buffer_load_dwordx4 v90, %[rw], s44 offen nt lds\ns_add_u32 m0, %[tb], 7168\ns_add_u32 s44, %[wo], 1769472\nbuffer_load_dwordx4 v90, %[rw], s44 offen nt lds\ns_add_u32 m0, %[tb], 8192\ns_add_u32 s44, %[wo], 2097152\nbuffer_load_dwordx4 v90, %[rw], s44 offen nt lds\ns_add_u32 m0, %[tb], 9216\n" \
"s_add_u32 s44, %[wo], 2162688\nbuffer_load_dwordx4 v90, %[rw], s44 offen nt lds\ns_add_u32 m0, %[tb], 10240\ns_add_u32 s44, %[wo], 2228224\nbuffer_load_dwordx4 v90, %[rw], s44 offen nt lds\ns_add_u32 m0, %[tb], 11264\ns_add_u32 s44, %[wo], 2293760\nbuffer_load_dwordx4 v90, %[rw], s44 offen nt lds\n" \
"s_add_u32 m0, %[lw], 0\ns_add_u32 s44, %[wo], 2621440\nbuffer_load_dwordx4 v90, %[rw], s44 offen nt lds\ns_add_u32 m0, %[lw], 1024\ns_add_u32 s44, %[wo], 2686976\nbuffer_load_dwordx4 v90, %[rw], s44 offen nt lds\ns_add_u32 m0, %[lw], 2048\ns_add_u32 s44, %[wo], 2752512\n" \
"buffer_load_dwordx4 v90, %[rw], s44 offen nt lds\ns_add_u32 m0, %[lw], 3072\ns_add_u32 s44, %[wo], 2818048\nbuffer_load_dwordx4 v90, %[rw], s44 offen nt lds\ns_add_u32 m0, %[lw], 4096\ns_add_u32 s44, %[wo], 3145728\nbuffer_load_dwordx4 v90, %[rw], s44 offen nt lds\ns_add_u32 m0, %[lw], 5120\n" \
"s_add_u32 s44, %[wo], 3211264\nbuffer_load_dwordx4 v90, %[rw], s44 offen nt lds\ns_add_u32 m0, %[lw], 6144\ns_add_u32 s44, %[wo], 3276800\nbuffer_load_dwordx4 v90, %[rw], s44 offen nt lds\ns_add_u32 m0, %[lw], 7168\ns_add_u32 s44, %[wo], 3342336\nbuffer_load_dwordx4 v90, %[rw], s44 offen nt lds\n" \
"s_add_u32 m0, %[lw], 8192\ns_add_u32 s44, %[wo], 3670016\nbuffer_load_dwordx4 v90, %[rw], s44 offen nt lds\ns_add_u32 m0, %[lw], 9216\ns_add_u32 s44, %[wo], 3735552\nbuffer_load_dwordx4 v90, %[rw], s44 offen nt lds\ns_add_u32 m0, %[lw], 10240\ns_add_u32 s44, %[wo], 3801088\n" \
"buffer_load_dwordx4 v90, %[rw], s44 offen nt lds\ns_add_u32 m0, %[lw], 11264\ns_add_u32 s44, %[wo], 3866624\nbuffer_load_dwordx4 v90, %[rw], s44 offen nt lds\ns_waitcnt vmcnt(28)\ns_barrier\ns_add_u32 m0, %[lw], 12288\ns_add_u32 s44, %[wo], 4194304\n" \
"buffer_load_dwordx4 v90, %[rw], s44 offen nt lds\ns_add_u32 m0, %[lw], 13312\ns_add_u32 s44, %[wo], 4259840\nbuffer_load_dwordx4 v90, %[rw], s44 offen nt lds\ns_add_u32 m0, %[lw], 14336\ns_add_u32 s44, %[wo], 4325376\nbuffer_load_dwordx4 v90, %[rw], s44 offen nt lds\ns_add_u32 m0, %[lw], 15360\n" \
"s_add_u32 s44, %[wo], 4390912\nbuffer_load_dwordx4 v90, %[rw], s44 offen nt lds\nds_read_b64_tr_b16 a[0:1], v93 offset:16384\nds_read_b64_tr_b16 a[2:3], v93 offset:16896\nds_read_b64_tr_b16 a[16:17], v93 offset:16392\nds_read_b64_tr_b16 a[18:19], v93 offset:16904\nds_read_b64_tr_b16 a[4:5], v93 offset:40960\nds_read_b64_tr_b16 a[6:7], v93 offset:41472\n" \
"ds_read_b64_tr_b16 a[20:21], v93 offset:40968\nds_read_b64_tr_b16 a[22:23], v93 offset:41480\ns_waitcnt lgkmcnt(0)\ns_waitcnt vmcnt(28)\ns_barrier\ns_add_u32 m0, %[lw], 16384\ns_add_u32 s44, %[wo], 4718592\nbuffer_load_dwordx4 v90, %[rw], s44 offen nt lds\n" \
"s_add_u32 m0, %[lw], 17408\ns_add_u32 s44, %[wo], 4784128\nbuffer_load_dwordx4 v90, %[rw], s44 offen nt lds\ns_add_u32 m0, %[lw], 18432\ns_add_u32 s44, %[wo], 4849664\nbuffer_load_dwordx4 v90, %[rw], s44 offen nt lds\ns_add_u32 m0, %[lw], 19456\ns_add_u32 s44, %[wo], 4915200\n" \
"buffer_load_dwordx4 v90, %[rw], s44 offen nt lds\nds_read_b64_tr_b16 a[24:25], v93 offset:20480\nds_read_b64_tr_b16 a[26:27], v93 offset:20992\nds_read_b64_tr_b16 a[40:41], v93 offset:20488\nds_read_b64_tr_b16 a[42:43], v93 offset:21000\nds_read_b64_tr_b16 a[28:29], v93 offset:45056\nds_read_b64_tr_b16 a[30:31], v93 offset:45568\nds_read_b64_tr_b16 a[44:45], v93 offset:45064\n" \
"ds_read_b64_tr_b16 a[46:47], v93 offset:45576\ns_waitcnt lgkmcnt(0)\ns_waitcnt vmcnt(28)\ns_barrier\ns_add_u32 m0, %[lw], 20480\ns_add_u32 s44, %[wo], 5242880\nbuffer_load_dwordx4 v90, %[rw], s44 offen nt lds\ns_add_u32 m0, %[lw], 21504\n" \
"s_add_u32 s44, %[wo], 5308416\nbuffer_load_dwordx4 v90, %[rw], s44 offen nt lds\ns_add_u32 m0, %[lw], 22528\ns_add_u32 s44, %[wo], 5373952\nbuffer_load_dwordx4 v90, %[rw], s44 offen nt lds\ns_add_u32 m0, %[lw], 23552\ns_add_u32 s44, %[wo], 5439488\nbuffer_load_dwordx4 v90, %[rw], s44 offen nt lds\n" \
"ds_read_b64_tr_b16 v[84:85], v92 offset:0\nds_read_b64_tr_b16 v[86:87], v92 offset:512\nds_read_b64_tr_b16 a[64:65], v92 offset:8\nds_read_b64_tr_b16 a[66:67], v92 offset:520\nds_read_b64_tr_b16 v[80:81], v92 offset:12288\nds_read_b64_tr_b16 v[82:83], v92 offset:12800\nds_read_b64_tr_b16 a[68:69], v92 offset:12296\nds_read_b64_tr_b16 a[70:71], v92 offset:12808\n" \
"s_waitcnt lgkmcnt(0)\ns_waitcnt vmcnt(28)\ns_barrier\ns_add_u32 m0, %[tb], 0\ns_add_u32 s44, %[wo], 5767168\nbuffer_load_dwordx4 v90, %[rw], s44 offen nt lds\ns_add_u32 m0, %[tb], 1024\ns_add_u32 s44, %[wo], 5832704\n" \
"buffer_load_dwordx4 v90, %[rw], s44 offen nt lds\ns_add_u32 m0, %[tb], 2048\ns_add_u32 s44, %[wo], 5898240\nbuffer_load_dwordx4 v90, %[rw], s44 offen nt lds\ns_add_u32 m0, %[tb], 3072\ns_add_u32 s44, %[wo], 5963776\nbuffer_load_dwordx4 v90, %[rw], s44 offen nt lds\nds_read_b64_tr_b16 a[80:81], v92 offset:4096\n" \
"ds_read_b64_tr_b16 a[82:83], v92 offset:4608\nds_read_b64_tr_b16 a[88:89], v92 offset:4104\nds_read_b64_tr_b16 a[90:91], v92 offset:4616\nds_read_b64_tr_b16 a[84:85], v92 offset:16384\nds_read_b64_tr_b16 a[86:87], v92 offset:16896\nds_read_b64_tr_b16 a[92:93], v92 offset:16392\nds_read_b64_tr_b16 a[94:95], v92 offset:16904\ns_waitcnt lgkmcnt(0)\n" \
"s_waitcnt vmcnt(28)\ns_barrier\ns_add_u32 m0, %[tb], 4096\ns_add_u32 s44, %[wo], 6291456\nbuffer_load_dwordx4 v90, %[rw], s44 offen nt lds\ns_add_u32 m0, %[tb], 5120\ns_add_u32 s44, %[wo], 6356992\nbuffer_load_dwordx4 v90, %[rw], s44 offen nt lds\n" \
"s_add_u32 m0, %[tb], 6144\ns_add_u32 s44, %[wo], 6422528\nbuffer_load_dwordx4 v90, %[rw], s44 offen nt lds\ns_add_u32 m0, %[tb], 7168\ns_add_u32 s44, %[wo], 6488064\nbuffer_load_dwordx4 v90, %[rw], s44 offen nt lds\nds_read_b64_tr_b16 a[104:105], v92 offset:8192\nds_read_b64_tr_b16 a[106:107], v92 offset:8704\n" \
"ds_read_b64_tr_b16 a[120:121], v92 offset:8200\nds_read_b64_tr_b16 a[122:123], v92 offset:8712\nds_read_b64_tr_b16 a[108:109], v92 offset:20480\nds_read_b64_tr_b16 a[110:111], v92 offset:20992\nds_read_b64_tr_b16 a[124:125], v92 offset:20488\nds_read_b64_tr_b16 a[126:127], v92 offset:21000\ns_waitcnt lgkmcnt(0)\ns_waitcnt vmcnt(28)\n" \
"s_barrier\ns_add_u32 m0, %[tb], 8192\ns_add_u32 s44, %[wo], 6815744\nbuffer_load_dwordx4 v90, %[rw], s44 offen nt lds\ns_add_u32 m0, %[tb], 9216\ns_add_u32 s44, %[wo], 6881280\nbuffer_load_dwordx4 v90, %[rw], s44 offen nt lds\ns_add_u32 m0, %[tb], 10240\n" \
"s_add_u32 s44, %[wo], 6946816\nbuffer_load_dwordx4 v90, %[rw], s44 offen nt lds\ns_add_u32 m0, %[tb], 11264\ns_add_u32 s44, %[wo], 7012352\nbuffer_load_dwordx4 v90, %[rw], s44 offen nt lds\nds_read_b64_tr_b16 a[128:129], v93 offset:0\nds_read_b64_tr_b16 a[130:131], v93 offset:512\nds_read_b64_tr_b16 v[76:77], v93 offset:8\n" \
"ds_read_b64_tr_b16 v[78:79], v93 offset:520\nds_read_b64_tr_b16 a[132:133], v93 offset:24576\nds_read_b64_tr_b16 a[134:135], v93 offset:25088\nds_read_b64_tr_b16 v[72:73], v93 offset:24584\nds_read_b64_tr_b16 v[74:75], v93 offset:25096\ns_waitcnt lgkmcnt(0)\ns_waitcnt vmcnt(28)\ns_barrier\n" \
"s_add_u32 m0, %[lw], 0\ns_add_u32 s44, %[wo], 7340032\nbuffer_load_dwordx4 v90, %[rw], s44 offen nt lds\ns_add_u32 m0, %[lw], 1024\ns_add_u32 s44, %[wo], 7405568\nbuffer_load_dwordx4 v90, %[rw], s44 offen nt lds\ns_add_u32 m0, %[lw], 2048\ns_add_u32 s44, %[wo], 7471104\n" \
"buffer_load_dwordx4 v90, %[rw], s44 offen nt lds\ns_add_u32 m0, %[lw], 3072\ns_add_u32 s44, %[wo], 7536640\nbuffer_load_dwordx4 v90, %[rw], s44 offen nt lds\nds_read_b64_tr_b16 a[152:153], v93 offset:4096\nds_read_b64_tr_b16 a[154:155], v93 offset:4608\nds_read_b64_tr_b16 a[168:169], v93 offset:4104\nds_read_b64_tr_b16 a[170:171], v93 offset:4616\n" \
"ds_read_b64_tr_b16 a[156:157], v93 offset:28672\nds_read_b64_tr_b16 a[158:159], v93 offset:29184\nds_read_b64_tr_b16 a[172:173], v93 offset:28680\nds_read_b64_tr_b16 a[174:175], v93 offset:29192\ns_waitcnt lgkmcnt(0)\ns_waitcnt vmcnt(28)\ns_barrier\ns_add_u32 m0, %[lw], 4096\n" \
"s_add_u32 s44, %[wo], 7864320\nbuffer_load_dwordx4 v90, %[rw], s44 offen nt lds\ns_add_u32 m0, %[lw], 5120\ns_add_u32 s44, %[wo], 7929856\nbuffer_load_dwordx4 v90, %[rw], s44 offen nt lds\ns_add_u32 m0, %[lw], 6144\ns_add_u32 s44, %[wo], 7995392\nbuffer_load_dwordx4 v90, %[rw], s44 offen nt lds\n" \
"s_add_u32 m0, %[lw], 7168\ns_add_u32 s44, %[wo], 8060928\nbuffer_load_dwordx4 v90, %[rw], s44 offen nt lds\nds_read_b64_tr_b16 a[184:185], v93 offset:8192\nds_read_b64_tr_b16 a[186:187], v93 offset:8704\nds_read_b64_tr_b16 a[192:193], v93 offset:8200\nds_read_b64_tr_b16 a[194:195], v93 offset:8712\nds_read_b64_tr_b16 a[188:189], v93 offset:32768\n" \
"ds_read_b64_tr_b16 a[190:191], v93 offset:33280\nds_read_b64_tr_b16 a[196:197], v93 offset:32776\nds_read_b64_tr_b16 a[198:199], v93 offset:33288\ns_waitcnt lgkmcnt(0)\ns_waitcnt vmcnt(28)\ns_barrier\ns_add_u32 m0, %[lw], 8192\ns_add_u32 s44, %[w2], 0\n" \
"buffer_load_dwordx4 v91, %[ro], s44 offen nt lds\ns_add_u32 m0, %[lw], 9216\ns_add_u32 s44, %[w2], 32768\nbuffer_load_dwordx4 v91, %[ro], s44 offen nt lds\ns_add_u32 m0, %[lw], 10240\ns_add_u32 s44, %[w2], 65536\nbuffer_load_dwordx4 v91, %[ro], s44 offen nt lds\ns_add_u32 m0, %[lw], 11264\n" \
"s_add_u32 s44, %[w2], 98304\nbuffer_load_dwordx4 v91, %[ro], s44 offen nt lds\nds_read_b64_tr_b16 v[68:69], v93 offset:12288\nds_read_b64_tr_b16 v[70:71], v93 offset:12800\nds_read_b64_tr_b16 a[216:217], v93 offset:12296\nds_read_b64_tr_b16 a[218:219], v93 offset:12808\nds_read_b64_tr_b16 v[64:65], v93 offset:36864\nds_read_b64_tr_b16 v[66:67], v93 offset:37376\n" \
"ds_read_b64_tr_b16 a[220:221], v93 offset:36872\nds_read_b64_tr_b16 a[222:223], v93 offset:37384\ns_waitcnt lgkmcnt(0)\ns_waitcnt vmcnt(28)\ns_barrier\ns_add_u32 m0, %[lw], 12288\ns_add_u32 s44, %[w2], 128\nbuffer_load_dwordx4 v91, %[ro], s44 offen nt lds\n" \
"s_add_u32 m0, %[lw], 13312\ns_add_u32 s44, %[w2], 32896\nbuffer_load_dwordx4 v91, %[ro], s44 offen nt lds\ns_add_u32 m0, %[lw], 14336\ns_add_u32 s44, %[w2], 65664\nbuffer_load_dwordx4 v91, %[ro], s44 offen nt lds\ns_add_u32 m0, %[lw], 15360\ns_add_u32 s44, %[w2], 98432\n" \
"buffer_load_dwordx4 v91, %[ro], s44 offen nt lds\nds_read_b64_tr_b16 a[224:225], v93 offset:16384\nds_read_b64_tr_b16 a[226:227], v93 offset:16896\nds_read_b64_tr_b16 a[240:241], v93 offset:16392\nds_read_b64_tr_b16 a[242:243], v93 offset:16904\nds_read_b64_tr_b16 a[228:229], v93 offset:40960\nds_read_b64_tr_b16 a[230:231], v93 offset:41472\nds_read_b64_tr_b16 a[244:245], v93 offset:40968\n" \
"ds_read_b64_tr_b16 a[246:247], v93 offset:41480\ns_waitcnt lgkmcnt(0)\ns_waitcnt vmcnt(28)\ns_barrier\ns_add_u32 m0, %[lw], 16384\ns_add_u32 s44, %[w2], 256\nbuffer_load_dwordx4 v91, %[ro], s44 offen nt lds\ns_add_u32 m0, %[lw], 17408\n" \
"s_add_u32 s44, %[w2], 33024\nbuffer_load_dwordx4 v91, %[ro], s44 offen nt lds\ns_add_u32 m0, %[lw], 18432\ns_add_u32 s44, %[w2], 65792\nbuffer_load_dwordx4 v91, %[ro], s44 offen nt lds\ns_add_u32 m0, %[lw], 19456\ns_add_u32 s44, %[w2], 98560\nbuffer_load_dwordx4 v91, %[ro], s44 offen nt lds\n" \
"ds_read_b64_tr_b16 v[96:97], v93 offset:20480\nds_read_b64_tr_b16 v[98:99], v93 offset:20992\nds_read_b64_tr_b16 v[112:113], v93 offset:20488\nds_read_b64_tr_b16 v[114:115], v93 offset:21000\nds_read_b64_tr_b16 v[100:101], v93 offset:45056\nds_read_b64_tr_b16 v[102:103], v93 offset:45568\nds_read_b64_tr_b16 v[116:117], v93 offset:45064\nds_read_b64_tr_b16 v[118:119], v93 offset:45576\n" \
"s_waitcnt lgkmcnt(0)\ns_waitcnt vmcnt(28)\ns_barrier\ns_add_u32 m0, %[lw], 20480\ns_add_u32 s44, %[w2], 384\nbuffer_load_dwordx4 v91, %[ro], s44 offen nt lds\ns_add_u32 m0, %[lw], 21504\ns_add_u32 s44, %[w2], 33152\n" \
"buffer_load_dwordx4 v91, %[ro], s44 offen nt lds\ns_add_u32 m0, %[lw], 22528\ns_add_u32 s44, %[w2], 65920\nbuffer_load_dwordx4 v91, %[ro], s44 offen nt lds\ns_add_u32 m0, %[lw], 23552\ns_add_u32 s44, %[w2], 98688\nbuffer_load_dwordx4 v91, %[ro], s44 offen nt lds\nds_read_b64_tr_b16 v[128:129], v92 offset:0\n" \
"ds_read_b64_tr_b16 v[130:131], v92 offset:512\nds_read_b64_tr_b16 v[60:61], v92 offset:8\nds_read_b64_tr_b16 v[62:63], v92 offset:520\nds_read_b64_tr_b16 v[132:133], v92 offset:12288\nds_read_b64_tr_b16 v[134:135], v92 offset:12800\nds_read_b64_tr_b16 v[56:57], v92 offset:12296\nds_read_b64_tr_b16 v[58:59], v92 offset:12808\ns_waitcnt lgkmcnt(0)\n" \
"s_waitcnt vmcnt(28)\ns_barrier\ns_add_u32 m0, %[tb], 0\ns_add_u32 s44, %[w2], 139264\nbuffer_load_dwordx4 v91, %[ro], s44 offen nt lds\ns_add_u32 m0, %[tb], 1024\ns_add_u32 s44, %[w2], 172032\nbuffer_load_dwordx4 v91, %[ro], s44 offen nt lds\n" \
"s_add_u32 m0, %[tb], 2048\ns_add_u32 s44, %[w2], 204800\nbuffer_load_dwordx4 v91, %[ro], s44 offen nt lds\ns_add_u32 m0, %[tb], 3072\ns_add_u32 s44, %[w2], 237568\nbuffer_load_dwordx4 v91, %[ro], s44 offen nt lds\nds_read_b64_tr_b16 v[144:145], v92 offset:4096\nds_read_b64_tr_b16 v[146:147], v92 offset:4608\n" \
"ds_read_b64_tr_b16 v[160:161], v92 offset:4104\nds_read_b64_tr_b16 v[162:163], v92 offset:4616\nds_read_b64_tr_b16 v[148:149], v92 offset:16384\nds_read_b64_tr_b16 v[150:151], v92 offset:16896\nds_read_b64_tr_b16 v[164:165], v92 offset:16392\nds_read_b64_tr_b16 v[166:167], v92 offset:16904\ns_waitcnt lgkmcnt(0)\ns_waitcnt vmcnt(28)\n" \
"s_barrier\ns_add_u32 m0, %[tb], 4096\ns_add_u32 s44, %[w2], 139392\nbuffer_load_dwordx4 v91, %[ro], s44 offen nt lds\ns_add_u32 m0, %[tb], 5120\ns_add_u32 s44, %[w2], 172160\nbuffer_load_dwordx4 v91, %[ro], s44 offen nt lds\ns_add_u32 m0, %[tb], 6144\n" \
"s_add_u32 s44, %[w2], 204928\nbuffer_load_dwordx4 v91, %[ro], s44 offen nt lds\ns_add_u32 m0, %[tb], 7168\ns_add_u32 s44, %[w2], 237696\nbuffer_load_dwordx4 v91, %[ro], s44 offen nt lds\nds_read_b64_tr_b16 v[168:169], v92 offset:8192\nds_read_b64_tr_b16 v[170:171], v92 offset:8704\nds_read_b64_tr_b16 v[184:185], v92 offset:8200\n" \
"ds_read_b64_tr_b16 v[186:187], v92 offset:8712\nds_read_b64_tr_b16 v[172:173], v92 offset:20480\nds_read_b64_tr_b16 v[174:175], v92 offset:20992\nds_read_b64_tr_b16 v[188:189], v92 offset:20488\nds_read_b64_tr_b16 v[190:191], v92 offset:21000\ns_waitcnt lgkmcnt(0)\ns_waitcnt vmcnt(28)\ns_barrier\n" \
"s_add_u32 m0, %[tb], 8192\ns_add_u32 s44, %[w2], 139520\nbuffer_load_dwordx4 v91, %[ro], s44 offen nt lds\ns_add_u32 m0, %[tb], 9216\ns_add_u32 s44, %[w2], 172288\nbuffer_load_dwordx4 v91, %[ro], s44 offen nt lds\ns_add_u32 m0, %[tb], 10240\ns_add_u32 s44, %[w2], 205056\n" \
"buffer_load_dwordx4 v91, %[ro], s44 offen nt lds\ns_add_u32 m0, %[tb], 11264\ns_add_u32 s44, %[w2], 237824\nbuffer_load_dwordx4 v91, %[ro], s44 offen nt lds\nds_read_b64_tr_b16 v[200:201], v93 offset:0\nds_read_b64_tr_b16 v[202:203], v93 offset:512\nds_read_b64_tr_b16 v[216:217], v93 offset:8\nds_read_b64_tr_b16 v[218:219], v93 offset:520\n" \
"ds_read_b64_tr_b16 v[204:205], v93 offset:24576\nds_read_b64_tr_b16 v[206:207], v93 offset:25088\nds_read_b64_tr_b16 v[220:221], v93 offset:24584\nds_read_b64_tr_b16 v[222:223], v93 offset:25096\ns_waitcnt lgkmcnt(0)\ns_waitcnt vmcnt(28)\ns_barrier\ns_add_u32 m0, %[lw], 0\n" \
"s_add_u32 s44, %[w2], 139648\nbuffer_load_dwordx4 v91, %[ro], s44 offen nt lds\ns_add_u32 m0, %[lw], 1024\ns_add_u32 s44, %[w2], 172416\nbuffer_load_dwordx4 v91, %[ro], s44 offen nt lds\ns_add_u32 m0, %[lw], 2048\ns_add_u32 s44, %[w2], 205184\nbuffer_load_dwordx4 v91, %[ro], s44 offen nt lds\n" \
"s_add_u32 m0, %[lw], 3072\ns_add_u32 s44, %[w2], 237952\nbuffer_load_dwordx4 v91, %[ro], s44 offen nt lds\nds_read_b64_tr_b16 v[232:233], v93 offset:4096\nds_read_b64_tr_b16 v[234:235], v93 offset:4608\nds_read_b64_tr_b16 v[240:241], v93 offset:4104\nds_read_b64_tr_b16 v[242:243], v93 offset:4616\nds_read_b64_tr_b16 v[236:237], v93 offset:28672\n" \
"ds_read_b64_tr_b16 v[238:239], v93 offset:29184\nds_read_b64_tr_b16 v[244:245], v93 offset:28680\nds_read_b64_tr_b16 v[246:247], v93 offset:29192\ns_waitcnt lgkmcnt(0)\ns_waitcnt vmcnt(28)\ns_barrier\ns_add_u32 m0, %[lw], 4096\ns_add_u32 s44, %[w2], 131072\n" \
"buffer_load_dwordx4 v91, %[ro], s44 offen nt lds\ns_add_u32 m0, %[lw], 5120\ns_add_u32 s44, %[w2], 163840\nbuffer_load_dwordx4 v91, %[ro], s44 offen nt lds\ns_add_u32 m0, %[lw], 6144\ns_add_u32 s44, %[w2], 196608\nbuffer_load_dwordx4 v91, %[ro], s44 offen nt lds\ns_add_u32 m0, %[lw], 7168\n" \
"s_add_u32 s44, %[w2], 229376\nbuffer_load_dwordx4 v91, %[ro], s44 offen nt lds\nds_read_b64_tr_b16 a[8:9], v95 offset:8192\nds_read_b64_tr_b16 a[10:11], v95 offset:8448\nds_read_b64_tr_b16 a[12:13], v95 offset:10240\nds_read_b64_tr_b16 a[14:15], v95 offset:10496\nds_read_b64_tr_b16 a[208:209], v95 offset:8256\nds_read_b64_tr_b16 a[210:211], v95 offset:8512\n" \
"ds_read_b64_tr_b16 a[212:213], v95 offset:10304\nds_read_b64_tr_b16 a[214:215], v95 offset:10560\ns_waitcnt lgkmcnt(0)\ns_waitcnt vmcnt(28)\ns_barrier\ns_add_u32 m0, %[lw], 8192\ns_add_u32 s44, %[w2], 131200\nbuffer_load_dwordx4 v91, %[ro], s44 offen nt lds\n" \
"s_add_u32 m0, %[lw], 9216\ns_add_u32 s44, %[w2], 163968\nbuffer_load_dwordx4 v91, %[ro], s44 offen nt lds\ns_add_u32 m0, %[lw], 10240\ns_add_u32 s44, %[w2], 196736\nbuffer_load_dwordx4 v91, %[ro], s44 offen nt lds\ns_add_u32 m0, %[lw], 11264\ns_add_u32 s44, %[w2], 229504\n" \
"buffer_load_dwordx4 v91, %[ro], s44 offen nt lds\nds_read_b64_tr_b16 a[56:57], v95 offset:12288\nds_read_b64_tr_b16 a[58:59], v95 offset:12544\nds_read_b64_tr_b16 a[60:61], v95 offset:14336\nds_read_b64_tr_b16 a[62:63], v95 offset:14592\nds_read_b64_tr_b16 v[104:105], v95 offset:12352\nds_read_b64_tr_b16 v[106:107], v95 offset:12608\nds_read_b64_tr_b16 v[108:109], v95 offset:14400\n" \
"ds_read_b64_tr_b16 v[110:111], v95 offset:14656\ns_waitcnt lgkmcnt(0)\ns_waitcnt vmcnt(28)\ns_barrier\ns_add_u32 m0, %[lw], 12288\ns_add_u32 s44, %[w2], 131328\nbuffer_load_dwordx4 v91, %[ro], s44 offen nt lds\ns_add_u32 m0, %[lw], 13312\n" \
"s_add_u32 s44, %[w2], 164096\nbuffer_load_dwordx4 v91, %[ro], s44 offen nt lds\ns_add_u32 m0, %[lw], 14336\ns_add_u32 s44, %[w2], 196864\nbuffer_load_dwordx4 v91, %[ro], s44 offen nt lds\ns_add_u32 m0, %[lw], 15360\ns_add_u32 s44, %[w2], 229632\nbuffer_load_dwordx4 v91, %[ro], s44 offen nt lds\n" \
"ds_read_b64_tr_b16 a[112:113], v95 offset:16384\nds_read_b64_tr_b16 a[114:115], v95 offset:16640\nds_read_b64_tr_b16 a[116:117], v95 offset:18432\nds_read_b64_tr_b16 a[118:119], v95 offset:18688\nds_read_b64_tr_b16 v[152:153], v95 offset:16448\nds_read_b64_tr_b16 v[154:155], v95 offset:16704\nds_read_b64_tr_b16 v[156:157], v95 offset:18496\nds_read_b64_tr_b16 v[158:159], v95 offset:18752\n" \
"s_waitcnt lgkmcnt(0)\ns_waitcnt vmcnt(28)\ns_barrier\ns_add_u32 m0, %[lw], 16384\ns_add_u32 s44, %[w2], 131456\nbuffer_load_dwordx4 v91, %[ro], s44 offen nt lds\ns_add_u32 m0, %[lw], 17408\ns_add_u32 s44, %[w2], 164224\n" \
"buffer_load_dwordx4 v91, %[ro], s44 offen nt lds\ns_add_u32 m0, %[lw], 18432\ns_add_u32 s44, %[w2], 196992\nbuffer_load_dwordx4 v91, %[ro], s44 offen nt lds\ns_add_u32 m0, %[lw], 19456\ns_add_u32 s44, %[w2], 229760\nbuffer_load_dwordx4 v91, %[ro], s44 offen nt lds\nds_read_b64_tr_b16 a[160:161], v95 offset:20480\n" \
"ds_read_b64_tr_b16 a[162:163], v95 offset:20736\nds_read_b64_tr_b16 a[164:165], v95 offset:22528\nds_read_b64_tr_b16 a[166:167], v95 offset:22784\nds_read_b64_tr_b16 v[208:209], v95 offset:20544\nds_read_b64_tr_b16 v[210:211], v95 offset:20800\nds_read_b64_tr_b16 v[212:213], v95 offset:22592\nds_read_b64_tr_b16 v[214:215], v95 offset:22848\ns_waitcnt lgkmcnt(0)\n" \
"s_waitcnt vmcnt(28)\ns_barrier\ns_add_u32 m0, %[lw], 20480\ns_add_u32 s44, %[w2], 8192\nbuffer_load_dwordx4 v91, %[ro], s44 offen nt lds\ns_add_u32 m0, %[lw], 21504\ns_add_u32 s44, %[w2], 40960\nbuffer_load_dwordx4 v91, %[ro], s44 offen nt lds\n" \
"s_add_u32 m0, %[lw], 22528\ns_add_u32 s44, %[w2], 73728\nbuffer_load_dwordx4 v91, %[ro], s44 offen nt lds\ns_add_u32 m0, %[lw], 23552\ns_add_u32 s44, %[w2], 106496\nbuffer_load_dwordx4 v91, %[ro], s44 offen nt lds\nds_read_b64_tr_b16 a[48:49], v94 offset:0\nds_read_b64_tr_b16 a[50:51], v94 offset:256\n" \
"ds_read_b64_tr_b16 a[52:53], v94 offset:2048\nds_read_b64_tr_b16 a[54:55], v94 offset:2304\nds_read_b64_tr_b16 a[248:249], v94 offset:64\nds_read_b64_tr_b16 a[250:251], v94 offset:320\nds_read_b64_tr_b16 a[252:253], v94 offset:2112\nds_read_b64_tr_b16 a[254:255], v94 offset:2368\ns_waitcnt lgkmcnt(0)\ns_waitcnt vmcnt(28)\n" \
"s_barrier\ns_add_u32 m0, %[tb], 0\ns_add_u32 s44, %[w2], 8320\nbuffer_load_dwordx4 v91, %[ro], s44 offen nt lds\ns_add_u32 m0, %[tb], 1024\ns_add_u32 s44, %[w2], 41088\nbuffer_load_dwordx4 v91, %[ro], s44 offen nt lds\ns_add_u32 m0, %[tb], 2048\n" \
"s_add_u32 s44, %[w2], 73856\nbuffer_load_dwordx4 v91, %[ro], s44 offen nt lds\ns_add_u32 m0, %[tb], 3072\ns_add_u32 s44, %[w2], 106624\nbuffer_load_dwordx4 v91, %[ro], s44 offen nt lds\nds_read_b64_tr_b16 a[96:97], v94 offset:4096\nds_read_b64_tr_b16 a[98:99], v94 offset:4352\nds_read_b64_tr_b16 a[100:101], v94 offset:6144\n" \
"ds_read_b64_tr_b16 a[102:103], v94 offset:6400\nds_read_b64_tr_b16 v[136:137], v94 offset:4160\nds_read_b64_tr_b16 v[138:139], v94 offset:4416\nds_read_b64_tr_b16 v[140:141], v94 offset:6208\nds_read_b64_tr_b16 v[142:143], v94 offset:6464\ns_waitcnt lgkmcnt(0)\nbuffer_load_dwordx2 v[12:13], off, s[36:39], s45 sc1\ns_waitcnt vmcnt(29)\n" \
"s_barrier\ns_add_u32 m0, %[tb], 4096\ns_add_u32 s44, %[w2], 8448\nbuffer_load_dwordx4 v91, %[ro], s44 offen nt lds\ns_add_u32 m0, %[tb], 5120\ns_add_u32 s44, %[w2], 41216\nbuffer_load_dwordx4 v91, %[ro], s44 offen nt lds\ns_add_u32 m0, %[tb], 6144\n" \
"s_add_u32 s44, %[w2], 73984\nbuffer_load_dwordx4 v91, %[ro], s44 offen nt lds\ns_add_u32 m0, %[tb], 7168\ns_add_u32 s44, %[w2], 106752\nbuffer_load_dwordx4 v91, %[ro], s44 offen nt lds\nds_read_b64_tr_b16 a[144:145], v94 offset:8192\nds_read_b64_tr_b16 a[146:147], v94 offset:8448\nds_read_b64_tr_b16 a[148:149], v94 offset:10240\n" \
"ds_read_b64_tr_b16 a[150:151], v94 offset:10496\nds_read_b64_tr_b16 v[192:193], v94 offset:8256\nds_read_b64_tr_b16 v[194:195], v94 offset:8512\nds_read_b64_tr_b16 v[196:197], v94 offset:10304\nds_read_b64_tr_b16 v[198:199], v94 offset:10560\ns_waitcnt lgkmcnt(0)\ns_waitcnt vmcnt(29)\ns_barrier\n" \
"s_add_u32 m0, %[tb], 8192\ns_add_u32 s44, %[w2], 8576\nbuffer_load_dwordx4 v91, %[ro], s44 offen nt lds\ns_add_u32 m0, %[tb], 9216\ns_add_u32 s44, %[w2], 41344\nbuffer_load_dwordx4 v91, %[ro], s44 offen nt lds\ns_add_u32 m0, %[tb], 10240\ns_add_u32 s44, %[w2], 74112\n" \
"buffer_load_dwordx4 v91, %[ro], s44 offen nt lds\ns_add_u32 m0, %[tb], 11264\ns_add_u32 s44, %[w2], 106880\nbuffer_load_dwordx4 v91, %[ro], s44 offen nt lds\nds_read_b64_tr_b16 a[200:201], v95 offset:0\nds_read_b64_tr_b16 a[202:203], v95 offset:256\nds_read_b64_tr_b16 a[204:205], v95 offset:2048\nds_read_b64_tr_b16 a[206:207], v95 offset:2304\n" \
"ds_read_b64_tr_b16 v[248:249], v95 offset:64\nds_read_b64_tr_b16 v[250:251], v95 offset:320\nds_read_b64_tr_b16 v[252:253], v95 offset:2112\nds_read_b64_tr_b16 v[254:255], v95 offset:2368\ns_waitcnt lgkmcnt(0)\ns_waitcnt vmcnt(29)\ns_barrier\nds_read_b64_tr_b16 v[52:53], v95 offset:4096\n" \
"ds_read_b64_tr_b16 v[54:55], v95 offset:4352\nds_read_b64_tr_b16 v[48:49], v95 offset:6144\nds_read_b64_tr_b16 v[50:51], v95 offset:6400\nds_read_b64_tr_b16 v[44:45], v95 offset:4160\nds_read_b64_tr_b16 v[46:47], v95 offset:4416\nds_read_b64_tr_b16 v[40:41], v95 offset:6208\nds_read_b64_tr_b16 v[42:43], v95 offset:6464\ns_waitcnt lgkmcnt(0)\n" \
"ds_write_b128 v1, v[84:87] offset:0\nds_write_b128 v1, v[80:83] offset:1024\nds_write_b128 v1, v[76:79] offset:2048\nds_write_b128 v1, v[72:75] offset:3072\ns_waitcnt vmcnt(25)\ns_barrier\nds_read_b64_tr_b16 a[72:73], v95 offset:8192\nds_read_b64_tr_b16 a[74:75], v95 offset:8448\n" \
"ds_read_b64_tr_b16 a[76:77], v95 offset:10240\nds_read_b64_tr_b16 a[78:79], v95 offset:10496\nds_read_b64_tr_b16 v[120:121], v95 offset:8256\nds_read_b64_tr_b16 v[122:123], v95 offset:8512\nds_read_b64_tr_b16 v[124:125], v95 offset:10304\nds_read_b64_tr_b16 v[126:127], v95 offset:10560\ns_waitcnt lgkmcnt(0)\nds_write_b128 v1, v[68:71] offset:4096\n" \
"ds_write_b128 v1, v[64:67] offset:5120\nds_write_b128 v1, v[60:63] offset:6144\nds_write_b128 v1, v[56:59] offset:7168\ns_waitcnt vmcnt(21)\ns_barrier\nds_read_b64_tr_b16 v[56:57], v95 offset:12288\nds_read_b64_tr_b16 v[58:59], v95 offset:12544\nds_read_b64_tr_b16 v[60:61], v95 offset:14336\n" \
"ds_read_b64_tr_b16 v[62:63], v95 offset:14592\nds_read_b64_tr_b16 v[64:65], v95 offset:12352\nds_read_b64_tr_b16 v[66:67], v95 offset:12608\nds_read_b64_tr_b16 v[68:69], v95 offset:14400\nds_read_b64_tr_b16 v[70:71], v95 offset:14656\ns_waitcnt lgkmcnt(0)\nds_write_b128 v1, v[52:55] offset:8192\nds_write_b128 v1, v[48:51] offset:9216\n" \
"ds_write_b128 v1, v[44:47] offset:10240\nds_write_b128 v1, v[40:43] offset:11264\ns_waitcnt vmcnt(17)\ns_barrier\nds_read_b64_tr_b16 a[176:177], v95 offset:16384\nds_read_b64_tr_b16 a[178:179], v95 offset:16640\nds_read_b64_tr_b16 a[180:181], v95 offset:18432\nds_read_b64_tr_b16 a[182:183], v95 offset:18688\n" \
"ds_read_b64_tr_b16 v[224:225], v95 offset:16448\nds_read_b64_tr_b16 v[226:227], v95 offset:16704\nds_read_b64_tr_b16 v[228:229], v95 offset:18496\nds_read_b64_tr_b16 v[230:231], v95 offset:18752\ns_waitcnt lgkmcnt(0)\nds_write_b128 v1, v[56:59] offset:12288\nds_write_b128 v1, v[60:63] offset:13312\nds_write_b128 v1, v[64:67] offset:14336\n" \
"ds_write_b128 v1, v[68:71] offset:15360\ns_waitcnt vmcnt(13)\ns_barrier\nds_read_b64_tr_b16 a[32:33], v95 offset:20480\nds_read_b64_tr_b16 a[34:35], v95 offset:20736\nds_read_b64_tr_b16 a[36:37], v95 offset:22528\nds_read_b64_tr_b16 a[38:39], v95 offset:22784\nds_read_b64_tr_b16 a[232:233], v95 offset:20544\n" \
"ds_read_b64_tr_b16 a[234:235], v95 offset:20800\nds_read_b64_tr_b16 a[236:237], v95 offset:22592\nds_read_b64_tr_b16 a[238:239], v95 offset:22848\ns_waitcnt lgkmcnt(0)\ns_waitcnt vmcnt(9)\ns_barrier\nds_read_b64_tr_b16 v[68:69], v94 offset:0\nds_read_b64_tr_b16 v[70:71], v94 offset:256\n" \
"ds_read_b64_tr_b16 v[64:65], v94 offset:2048\nds_read_b64_tr_b16 v[66:67], v94 offset:2304\nds_read_b64_tr_b16 v[60:61], v94 offset:64\nds_read_b64_tr_b16 v[62:63], v94 offset:320\nds_read_b64_tr_b16 v[56:57], v94 offset:2112\nds_read_b64_tr_b16 v[58:59], v94 offset:2368\ns_waitcnt lgkmcnt(0)\nds_write_b128 v1, v[68:71] offset:16384\n" \
"ds_write_b128 v1, v[64:67] offset:17408\nds_write_b128 v1, v[60:63] offset:18432\nds_write_b128 v1, v[56:59] offset:19456\ns_waitcnt vmcnt(4)\ns_barrier\nds_read_b64_tr_b16 a[136:137], v94 offset:4096\nds_read_b64_tr_b16 a[138:139], v94 offset:4352\nds_read_b64_tr_b16 a[140:141], v94 offset:6144\n" \
"ds_read_b64_tr_b16 a[142:143], v94 offset:6400\nds_read_b64_tr_b16 v[176:177], v94 offset:4160\nds_read_b64_tr_b16 v[178:179], v94 offset:4416\nds_read_b64_tr_b16 v[180:181], v94 offset:6208\nds_read_b64_tr_b16 v[182:183], v94 offset:6464\ns_waitcnt lgkmcnt(0)\ns_waitcnt vmcnt(0)\ns_barrier\n" \
"ds_read_b64_tr_b16 v[56:57], v94 offset:8192\nds_read_b64_tr_b16 v[58:59], v94 offset:8448\nds_read_b64_tr_b16 v[60:61], v94 offset:10240\nds_read_b64_tr_b16 v[62:63], v94 offset:10496\nds_read_b64_tr_b16 v[64:65], v94 offset:8256\nds_read_b64_tr_b16 v[66:67], v94 offset:8512\nds_read_b64_tr_b16 v[68:69], v94 offset:10304\nds_read_b64_tr_b16 v[70:71], v94 offset:10560\n" \
"s_waitcnt lgkmcnt(0)\ns_mov_b32 s43, 0\ns_mov_b32 s86, 110\ns_mov_b32 s89, 0\n.Lvu_hb%=:\nv_cmp_ne_u32 vcc, v15, v12\ns_cbranch_vccnz .Lvu_hb1%=\ns_sleep 4\n" \
"buffer_load_dwordx2 v[12:13], off, s[36:39], s45 sc1\ns_waitcnt vmcnt(0)\ns_add_u32 s43, s43, 1\ns_cmp_lt_u32 s43, 262144\ns_cbranch_scc1 .Lvu_hb%=\ns_branch .Lvu_abort%=\n.Lvu_hb1%=:\nv_readfirstlane_b32 s44, v13\n" \
"s_and_b32 s44, s44, 0x200000\ns_add_u32 s36, s36, s44\ns_addc_u32 s37, s37, 0\nds_write_b128 v1, v[56:59] offset:20480\nds_write_b128 v1, v[60:63] offset:21504\nds_write_b128 v1, v[64:67] offset:22528\nds_write_b128 v1, v[68:71] offset:23552\ns_waitcnt lgkmcnt(0)\n" \
"s_barrier\ns_mov_b32 s40, -1\ns_mov_b32 s43, 0\ns_lshl_b32 s57, %[w], 12\ns_mov_b32 s56, 0\ns_mov_b32 s42, 0\ns_mov_b32 s60, -2147483648\ns_mov_b32 s54, -2147483648\n" \
"s_add_u32 s44, s40, 1\ns_lshl_b32 s44, s44, 17\ns_add_u32 s44, s44, %[xn]\ns_lshr_b32 s45, s44, 8\ns_and_b32 s45, s45, 0x2000\ns_xor_b32 s41, s44, s45\ns_add_u32 s41, s41, %[xw]\n.Lvu_f0%=:\n" \
"buffer_load_dword v14, v8, s[36:39], s56 offen sc1\ns_waitcnt vmcnt(0)\nv_cmp_ge_i32 vcc, s40, v14\ns_cbranch_vccz .Lvu_f0fok%=\ns_add_u32 s43, s43, 1\ns_cmp_lt_u32 s43, 262144\ns_cbranch_scc1 .Lvu_f0%=\ns_branch .Lvu_abort%=\n" \
".Lvu_f0fok%=:\ns_add_u32 m0, s57, 0\ns_add_u32 s58, s41, 0\nbuffer_load_dwordx4 v0, s[36:39], s58 offen offset:0 sc1 lds\nbuffer_load_dwordx4 v0, s[36:39], s58 offen offset:1024 sc1 lds\nbuffer_load_dwordx4 v0, s[36:39], s58 offen offset:2048 sc1 lds\nbuffer_load_dwordx4 v0, s[36:39], s58 offen offset:3072 sc1 lds\ns_add_u32 m0, s57, 32768\n" \
"s_add_u32 s58, s41, 32768\nbuffer_load_dwordx4 v0, s[36:39], s58 offen offset:0 sc1 lds\nbuffer_load_dwordx4 v0, s[36:39], s58 offen offset:1024 sc1 lds\nbuffer_load_dwordx4 v0, s[36:39], s58 offen offset:2048 sc1 lds\nbuffer_load_dwordx4 v0, s[36:39], s58 offen offset:3072 sc1 lds\ns_waitcnt vmcnt(0)\ns_barrier\nds_read_b128 v[32:35], v2 offset:0\n" \
"ds_read_b128 v[36:39], v2 offset:256\nds_read_b128 v[40:43], v2 offset:2048\nds_read_b128 v[44:47], v2 offset:2304\n.Lvu_tok%=:\nds_read_b128 v[64:67], v2 offset:4096\nds_read_b128 v[68:71], v2 offset:4352\ns_waitcnt lgkmcnt(5)\nv_mfma_f32_16x16x32_bf16 v[16:19], a[0:3], v[32:35], 0\n" \
"v_cvt_pk_bf16_f32 v92, v80, v81\nv_cvt_pk_bf16_f32 v93, v82, v83\nv_mfma_f32_16x16x32_bf16 v[20:23], a[16:19], v[32:35], 0\ns_waitcnt lgkmcnt(4)\nds_read_b128 v[32:35], v2 offset:6144\nv_cvt_pk_bf16_f32 v94, v84, v85\nv_cvt_pk_bf16_f32 v95, v86, v87\nv_mfma_f32_16x16x32_bf16 v[24:27], a[0:3], v[36:39], 0\n" \
"v_and_or_b32 v92, v92, -2, v13\nv_and_or_b32 v94, v94, -2, v13\nv_mfma_f32_16x16x32_bf16 v[28:31], a[16:19], v[36:39], 0\ns_waitcnt lgkmcnt(4)\nbuffer_store_dwordx4 v[92:95], v7, s[36:39], s60 offen offset:2560 sc1\nds_read_b128 v[36:39], v2 offset:6400\nv_mfma_f32_16x16x32_bf16 v[16:19], a[4:7], v[40:43], v[16:19]\nds_read_b128 v[56:59], v1 offset:0\n" \
"ds_read_b128 v[60:63], v1 offset:1024\nv_mfma_f32_16x16x32_bf16 v[20:23], a[20:23], v[40:43], v[20:23]\ns_waitcnt lgkmcnt(6)\ns_add_u32 m0, s57, 16384\nds_read_b128 v[40:43], v2 offset:8192\nv_mfma_f32_16x16x32_bf16 v[24:27], a[4:7], v[44:47], v[24:27]\ns_add_u32 s60, s41, 65536\nbuffer_load_dwordx4 v0, s[36:39], s60 offen offset:0 sc1 lds\n" \
"v_mfma_f32_16x16x32_bf16 v[28:31], a[20:23], v[44:47], v[28:31]\ns_waitcnt lgkmcnt(6)\nds_read_b128 v[44:47], v2 offset:8448\nv_mfma_f32_16x16x32_bf16 v[16:19], a[24:27], v[64:67], v[16:19]\nv_mfma_f32_16x16x32_bf16 v[20:23], a[40:43], v[64:67], v[20:23]\ns_waitcnt lgkmcnt(6)\nds_read_b128 v[64:67], v2 offset:10240\nv_mfma_f32_16x16x32_bf16 v[24:27], a[24:27], v[68:71], v[24:27]\n" \
"buffer_load_dwordx4 v0, s[36:39], s60 offen offset:1024 sc1 lds\nv_mfma_f32_16x16x32_bf16 v[28:31], a[40:43], v[68:71], v[28:31]\ns_waitcnt lgkmcnt(6)\nds_read_b128 v[68:71], v2 offset:10496\nv_mfma_f32_16x16x32_bf16 v[16:19], a[28:31], v[32:35], v[16:19]\nv_mfma_f32_16x16x32_bf16 v[20:23], a[44:47], v[32:35], v[20:23]\ns_waitcnt lgkmcnt(6)\nds_read_b128 v[32:35], v2 offset:12288\n" \
"v_mfma_f32_16x16x32_bf16 v[24:27], a[28:31], v[36:39], v[24:27]\nbuffer_load_dwordx4 v0, s[36:39], s60 offen offset:2048 sc1 lds\nv_mfma_f32_16x16x32_bf16 v[28:31], a[44:47], v[36:39], v[28:31]\ns_waitcnt lgkmcnt(4)\nds_read_b128 v[36:39], v2 offset:12544\nv_mfma_f32_16x16x32_bf16 v[16:19], v[56:59], v[40:43], v[16:19]\nv_mfma_f32_16x16x32_bf16 v[20:23], a[64:67], v[40:43], v[20:23]\ns_waitcnt lgkmcnt(4)\n" \
"ds_read_b128 v[40:43], v2 offset:14336\nv_mfma_f32_16x16x32_bf16 v[24:27], v[56:59], v[44:47], v[24:27]\nbuffer_load_dwordx4 v0, s[36:39], s60 offen offset:3072 sc1 lds\nv_mfma_f32_16x16x32_bf16 v[28:31], a[64:67], v[44:47], v[28:31]\ns_waitcnt lgkmcnt(4)\nds_read_b128 v[44:47], v2 offset:14592\nv_mfma_f32_16x16x32_bf16 v[16:19], v[60:63], v[64:67], v[16:19]\ns_add_u32 s56, s56, %[fs]\n" \
"s_add_u32 s62, s40, 1\ns_lshr_b32 m0, s57, 4\nv_mfma_f32_16x16x32_bf16 v[20:23], a[68:71], v[64:67], v[20:23]\ns_add_u32 m0, m0, 159744\ns_waitcnt lgkmcnt(4)\nds_read_b128 v[64:67], v2 offset:32768\nv_mfma_f32_16x16x32_bf16 v[24:27], v[60:63], v[68:71], v[24:27]\nbuffer_load_dword v8, s[36:39], s56 offen sc1 lds\n" \
"v_lshrrev_b32 v14, 2, v0\nv_mfma_f32_16x16x32_bf16 v[28:31], a[68:71], v[68:71], v[28:31]\ns_waitcnt lgkmcnt(4)\nds_read_b128 v[68:71], v2 offset:33024\nv_mfma_f32_16x16x32_bf16 v[16:19], a[80:83], v[32:35], v[16:19]\nv_add_u32 v14, 159744, v14\nv_mfma_f32_16x16x32_bf16 v[20:23], a[88:91], v[32:35], v[20:23]\ns_waitcnt lgkmcnt(4)\n" \
"ds_read_b128 v[32:35], v2 offset:34816\nv_mfma_f32_16x16x32_bf16 v[24:27], a[80:83], v[36:39], v[24:27]\nds_read_b128 v[56:59], v1 offset:2048\nds_read_b128 v[60:63], v1 offset:3072\nv_mfma_f32_16x16x32_bf16 v[28:31], a[88:91], v[36:39], v[28:31]\ns_waitcnt lgkmcnt(6)\nds_read_b128 v[36:39], v2 offset:35072\nv_mfma_f32_16x16x32_bf16 v[16:19], a[84:87], v[40:43], v[16:19]\n" \
"v_mfma_f32_16x16x32_bf16 v[20:23], a[92:95], v[40:43], v[20:23]\ns_waitcnt lgkmcnt(6)\nds_read_b128 v[40:43], v2 offset:36864\nv_mfma_f32_16x16x32_bf16 v[24:27], a[84:87], v[44:47], v[24:27]\nv_mfma_f32_16x16x32_bf16 v[28:31], a[92:95], v[44:47], v[28:31]\ns_waitcnt lgkmcnt(6)\nds_read_b128 v[44:47], v2 offset:37120\nv_mfma_f32_16x16x32_bf16 v[16:19], a[104:107], v[64:67], v[16:19]\n" \
"v_mfma_f32_16x16x32_bf16 v[20:23], a[120:123], v[64:67], v[20:23]\ns_waitcnt lgkmcnt(6)\nds_read_b128 v[64:67], v2 offset:38912\nv_mfma_f32_16x16x32_bf16 v[24:27], a[104:107], v[68:71], v[24:27]\ns_barrier\nv_mfma_f32_16x16x32_bf16 v[28:31], a[120:123], v[68:71], v[28:31]\ns_waitcnt lgkmcnt(6)\ns_add_u32 m0, s57, 0\n" \
"ds_read_b128 v[68:71], v2 offset:39168\nv_mfma_f32_16x16x32_bf16 v[16:19], a[108:111], v[32:35], v[16:19]\ns_add_u32 s60, s41, 98304\nbuffer_load_dwordx4 v0, s[36:39], s60 offen offset:0 sc1 lds\nv_mfma_f32_16x16x32_bf16 v[20:23], a[124:127], v[32:35], v[20:23]\ns_waitcnt lgkmcnt(4)\nds_read_b128 v[32:35], v2 offset:40960\nv_mfma_f32_16x16x32_bf16 v[24:27], a[108:111], v[36:39], v[24:27]\n" \
"v_mfma_f32_16x16x32_bf16 v[28:31], a[124:127], v[36:39], v[28:31]\ns_waitcnt lgkmcnt(4)\nds_read_b128 v[36:39], v2 offset:41216\nv_mfma_f32_16x16x32_bf16 v[16:19], a[128:131], v[40:43], v[16:19]\nbuffer_load_dwordx4 v0, s[36:39], s60 offen offset:1024 sc1 lds\nv_mfma_f32_16x16x32_bf16 v[20:23], v[56:59], v[40:43], v[20:23]\ns_waitcnt lgkmcnt(4)\nds_read_b128 v[40:43], v2 offset:43008\n" \
"v_mfma_f32_16x16x32_bf16 v[24:27], a[128:131], v[44:47], v[24:27]\nv_mfma_f32_16x16x32_bf16 v[28:31], v[56:59], v[44:47], v[28:31]\ns_waitcnt lgkmcnt(4)\nds_read_b128 v[44:47], v2 offset:43264\nv_mfma_f32_16x16x32_bf16 v[16:19], a[132:135], v[64:67], v[16:19]\nbuffer_load_dwordx4 v0, s[36:39], s60 offen offset:2048 sc1 lds\nv_mfma_f32_16x16x32_bf16 v[20:23], v[60:63], v[64:67], v[20:23]\ns_waitcnt lgkmcnt(4)\n" \
"ds_read_b128 v[64:67], v2 offset:45056\nv_mfma_f32_16x16x32_bf16 v[24:27], a[132:135], v[68:71], v[24:27]\nv_mfma_f32_16x16x32_bf16 v[28:31], v[60:63], v[68:71], v[28:31]\ns_waitcnt lgkmcnt(4)\nds_read_b128 v[68:71], v2 offset:45312\nv_mfma_f32_16x16x32_bf16 v[16:19], a[152:155], v[32:35], v[16:19]\nbuffer_load_dwordx4 v0, s[36:39], s60 offen offset:3072 sc1 lds\nv_mfma_f32_16x16x32_bf16 v[20:23], a[168:171], v[32:35], v[20:23]\n" \
"s_waitcnt lgkmcnt(4)\ns_add_u32 s44, s40, 2\nds_read_b128 v[32:35], v2 offset:47104\nv_mfma_f32_16x16x32_bf16 v[24:27], a[152:155], v[36:39], v[24:27]\ns_lshl_b32 s44, s44, 17\nds_read_b128 v[56:59], v1 offset:4096\nds_read_b128 v[60:63], v1 offset:5120\nv_mfma_f32_16x16x32_bf16 v[28:31], a[168:171], v[36:39], v[28:31]\n" \
"s_waitcnt lgkmcnt(6)\nds_read_b128 v[36:39], v2 offset:47360\nv_mfma_f32_16x16x32_bf16 v[16:19], a[156:159], v[40:43], v[16:19]\ns_add_u32 s44, s44, %[xn]\ns_lshr_b32 s45, s44, 8\nv_mfma_f32_16x16x32_bf16 v[20:23], a[172:175], v[40:43], v[20:23]\ns_waitcnt lgkmcnt(6)\nv_mfma_f32_16x16x32_bf16 v[24:27], a[156:159], v[44:47], v[24:27]\n" \
"s_and_b32 s45, s45, 0x2000\ns_xor_b32 s41, s44, s45\nv_mfma_f32_16x16x32_bf16 v[28:31], a[172:175], v[44:47], v[28:31]\ns_waitcnt lgkmcnt(0)\ns_waitcnt vmcnt(4)\ns_barrier\nds_read_b128 v[40:43], v2 offset:16384\nds_read_b128 v[44:47], v2 offset:16640\n" \
"v_mfma_f32_16x16x32_bf16 v[16:19], a[184:187], v[64:67], v[16:19]\nds_read_b32 v14, v14\nv_mfma_f32_16x16x32_bf16 v[20:23], a[192:195], v[64:67], v[20:23]\nds_read_b128 v[64:67], v2 offset:18432\nv_mfma_f32_16x16x32_bf16 v[24:27], a[184:187], v[68:71], v[24:27]\ns_add_u32 s41, s41, %[xw]\nv_mfma_f32_16x16x32_bf16 v[28:31], a[192:195], v[68:71], v[28:31]\nds_read_b128 v[68:71], v2 offset:18688\n" \
"v_mfma_f32_16x16x32_bf16 v[16:19], a[188:191], v[32:35], v[16:19]\nv_mfma_f32_16x16x32_bf16 v[20:23], a[196:199], v[32:35], v[20:23]\nds_read_b128 v[32:35], v2 offset:20480\nv_mfma_f32_16x16x32_bf16 v[24:27], a[188:191], v[36:39], v[24:27]\ns_waitcnt lgkmcnt(3)\nv_cmp_ge_i32 s[64:65], s62, v14\nv_mfma_f32_16x16x32_bf16 v[28:31], a[196:199], v[36:39], v[28:31]\ns_cmp_eq_u64 s[64:65], 0\n" \
"s_cselect_b32 s61, s41, -2147483648\nds_read_b128 v[36:39], v2 offset:20736\nv_mfma_f32_16x16x32_bf16 v[16:19], v[56:59], v[40:43], v[16:19]\ns_waitcnt vmcnt(4)\nbuffer_store_dword v15, off, s[36:39], s54 sc1\nv_mfma_f32_16x16x32_bf16 v[20:23], a[216:219], v[40:43], v[20:23]\ns_add_u32 m0, s57, 32768\nds_read_b128 v[40:43], v2 offset:22528\n" \
"v_mfma_f32_16x16x32_bf16 v[24:27], v[56:59], v[44:47], v[24:27]\ns_add_u32 s58, s61, 32768\nbuffer_load_dwordx4 v0, s[36:39], s58 offen offset:0 sc1 lds\nv_mfma_f32_16x16x32_bf16 v[28:31], a[216:219], v[44:47], v[28:31]\ns_waitcnt lgkmcnt(4)\nds_read_b128 v[44:47], v2 offset:22784\nv_mfma_f32_16x16x32_bf16 v[16:19], v[60:63], v[64:67], v[16:19]\nv_mfma_f32_16x16x32_bf16 v[20:23], a[220:223], v[64:67], v[20:23]\n" \
"s_waitcnt lgkmcnt(4)\nds_read_b128 v[64:67], v2 offset:24576\nv_mfma_f32_16x16x32_bf16 v[24:27], v[60:63], v[68:71], v[24:27]\nbuffer_load_dwordx4 v0, s[36:39], s58 offen offset:1024 sc1 lds\nv_mfma_f32_16x16x32_bf16 v[28:31], a[220:223], v[68:71], v[28:31]\ns_waitcnt lgkmcnt(4)\nds_read_b128 v[68:71], v2 offset:24832\nv_mfma_f32_16x16x32_bf16 v[16:19], a[224:227], v[32:35], v[16:19]\n" \
"ds_read_b128 v[56:59], v1 offset:6144\nds_read_b128 v[60:63], v1 offset:7168\nv_mfma_f32_16x16x32_bf16 v[20:23], a[240:243], v[32:35], v[20:23]\ns_waitcnt lgkmcnt(6)\nds_read_b128 v[32:35], v2 offset:26624\nv_mfma_f32_16x16x32_bf16 v[24:27], a[224:227], v[36:39], v[24:27]\nbuffer_load_dwordx4 v0, s[36:39], s58 offen offset:2048 sc1 lds\nv_mfma_f32_16x16x32_bf16 v[28:31], a[240:243], v[36:39], v[28:31]\n" \
"s_waitcnt lgkmcnt(6)\nds_read_b128 v[36:39], v2 offset:26880\nv_mfma_f32_16x16x32_bf16 v[16:19], a[228:231], v[40:43], v[16:19]\nv_mfma_f32_16x16x32_bf16 v[20:23], a[244:247], v[40:43], v[20:23]\ns_waitcnt lgkmcnt(6)\nds_read_b128 v[40:43], v2 offset:28672\nv_mfma_f32_16x16x32_bf16 v[24:27], a[228:231], v[44:47], v[24:27]\nbuffer_load_dwordx4 v0, s[36:39], s58 offen offset:3072 sc1 lds\n" \
"v_mfma_f32_16x16x32_bf16 v[28:31], a[244:247], v[44:47], v[28:31]\ns_waitcnt lgkmcnt(6)\nds_read_b128 v[44:47], v2 offset:28928\nv_mfma_f32_16x16x32_bf16 v[16:19], v[96:99], v[64:67], v[16:19]\nv_mfma_f32_16x16x32_bf16 v[20:23], v[112:115], v[64:67], v[20:23]\ns_waitcnt lgkmcnt(6)\nds_read_b128 v[64:67], v2 offset:30720\nv_mfma_f32_16x16x32_bf16 v[24:27], v[96:99], v[68:71], v[24:27]\n" \
"v_mfma_f32_16x16x32_bf16 v[28:31], v[112:115], v[68:71], v[28:31]\ns_waitcnt lgkmcnt(4)\nds_read_b128 v[68:71], v2 offset:30976\nv_mfma_f32_16x16x32_bf16 v[16:19], v[100:103], v[32:35], v[16:19]\nv_mfma_f32_16x16x32_bf16 v[20:23], v[116:119], v[32:35], v[20:23]\ns_waitcnt lgkmcnt(4)\nv_mfma_f32_16x16x32_bf16 v[24:27], v[100:103], v[36:39], v[24:27]\nv_mfma_f32_16x16x32_bf16 v[28:31], v[116:119], v[36:39], v[28:31]\n" \
"s_waitcnt lgkmcnt(0)\ns_waitcnt vmcnt(5)\ns_barrier\nds_read_b128 v[32:35], v2 offset:0\nds_read_b128 v[36:39], v2 offset:2048\nv_mfma_f32_16x16x32_bf16 v[16:19], v[128:131], v[40:43], v[16:19]\ns_add_u32 m0, s57, 16384\ns_nop 0\n" \
"buffer_load_dwordx4 v3, s[36:39], s42 offen offset:0 sc1 lds\nv_mfma_f32_16x16x32_bf16 v[20:23], v[56:59], v[40:43], v[20:23]\nds_read_b128 v[40:43], v2 offset:4096\nv_mfma_f32_16x16x32_bf16 v[24:27], v[128:131], v[44:47], v[24:27]\nv_mfma_f32_16x16x32_bf16 v[28:31], v[56:59], v[44:47], v[28:31]\nds_read_b128 v[44:47], v2 offset:6144\nv_mfma_f32_16x16x32_bf16 v[16:19], v[132:135], v[64:67], v[16:19]\nbuffer_load_dwordx4 v3, s[36:39], s42 offen offset:1024 sc1 lds\n" \
"v_mfma_f32_16x16x32_bf16 v[20:23], v[60:63], v[64:67], v[20:23]\nds_read_b128 v[64:67], v2 offset:8192\nv_mfma_f32_16x16x32_bf16 v[24:27], v[132:135], v[68:71], v[24:27]\nv_mfma_f32_16x16x32_bf16 v[28:31], v[60:63], v[68:71], v[28:31]\ns_waitcnt lgkmcnt(4)\nds_read_b128 v[68:71], v2 offset:10240\nv_mfma_f32_16x16x32_bf16 v[16:19], v[144:147], v[32:35], v[16:19]\nbuffer_load_dwordx4 v3, s[36:39], s42 offen offset:2048 sc1 lds\n" \
"v_mfma_f32_16x16x32_bf16 v[20:23], v[160:163], v[32:35], v[20:23]\ns_waitcnt lgkmcnt(4)\nds_read_b128 v[32:35], v2 offset:12288\nv_mfma_f32_16x16x32_bf16 v[16:19], v[148:151], v[36:39], v[16:19]\nv_mfma_f32_16x16x32_bf16 v[20:23], v[164:167], v[36:39], v[20:23]\ns_waitcnt lgkmcnt(4)\nds_read_b128 v[36:39], v2 offset:14336\nv_mfma_f32_16x16x32_bf16 v[16:19], v[168:171], v[40:43], v[16:19]\n" \
"buffer_load_dwordx4 v3, s[36:39], s42 offen offset:3072 sc1 lds\nv_add_u32 v14, s57, v0\nv_mfma_f32_16x16x32_bf16 v[20:23], v[184:187], v[40:43], v[20:23]\ns_waitcnt lgkmcnt(4)\nds_read_b128 v[40:43], v2 offset:256\nv_mfma_f32_16x16x32_bf16 v[16:19], v[172:175], v[44:47], v[16:19]\nv_mov_b64 v[48:49], 0\nv_mfma_f32_16x16x32_bf16 v[20:23], v[188:191], v[44:47], v[20:23]\n" \
"s_waitcnt lgkmcnt(4)\nds_read_b128 v[44:47], v2 offset:2304\nv_mov_b64 v[50:51], 0\nv_mfma_f32_16x16x32_bf16 v[16:19], v[200:203], v[64:67], v[16:19]\nv_mov_b64 v[52:53], 0\nv_mfma_f32_16x16x32_bf16 v[20:23], v[216:219], v[64:67], v[20:23]\ns_waitcnt lgkmcnt(4)\nds_read_b128 v[64:67], v2 offset:4352\n" \
"v_mov_b64 v[54:55], 0\nv_mfma_f32_16x16x32_bf16 v[16:19], v[204:207], v[68:71], v[16:19]\nds_read_b128 v[56:59], v1 offset:0\nds_read_b128 v[60:63], v1 offset:1024\nv_mfma_f32_16x16x32_bf16 v[20:23], v[220:223], v[68:71], v[20:23]\ns_waitcnt lgkmcnt(6)\nds_read_b128 v[68:71], v2 offset:6400\nv_mfma_f32_16x16x32_bf16 v[16:19], v[232:235], v[32:35], v[16:19]\n" \
"s_waitcnt vmcnt(0)\nds_read_b128 v[80:83], v14 offset:16384\nds_read_b128 v[84:87], v14 offset:17408\nv_mfma_f32_16x16x32_bf16 v[20:23], v[240:243], v[32:35], v[20:23]\ns_waitcnt lgkmcnt(8)\nds_read_b128 v[32:35], v2 offset:8448\nv_mfma_f32_16x16x32_bf16 v[16:19], v[236:239], v[36:39], v[16:19]\nds_read_b128 v[88:91], v14 offset:18432\n" \
"ds_read_b128 v[92:95], v14 offset:19456\nv_mfma_f32_16x16x32_bf16 v[20:23], v[244:247], v[36:39], v[20:23]\ns_waitcnt lgkmcnt(10)\nds_read_b128 v[36:39], v2 offset:10496\nv_mfma_f32_16x16x32_bf16 v[24:27], v[144:147], v[40:43], v[24:27]\nv_mfma_f32_16x16x32_bf16 v[28:31], v[160:163], v[40:43], v[28:31]\ns_waitcnt lgkmcnt(10)\nds_read_b128 v[40:43], v2 offset:12544\n" \
"v_cvt_pk_bf16_f32 v72, v16, v17\nv_mfma_f32_16x16x32_bf16 v[24:27], v[148:151], v[44:47], v[24:27]\nv_cvt_pk_bf16_f32 v73, v18, v19\nv_mfma_f32_16x16x32_bf16 v[28:31], v[164:167], v[44:47], v[28:31]\ns_waitcnt lgkmcnt(10)\nds_read_b128 v[44:47], v2 offset:14592\nv_cvt_pk_bf16_f32 v74, v20, v21\nv_mfma_f32_16x16x32_bf16 v[24:27], v[168:171], v[64:67], v[24:27]\n" \
"s_waitcnt lgkmcnt(6)\nv_max3_u32 v76, v80, v82, v84\nv_cvt_pk_bf16_f32 v75, v22, v23\nv_mfma_f32_16x16x32_bf16 v[28:31], v[184:187], v[64:67], v[28:31]\ns_waitcnt lgkmcnt(4)\nds_write2st64_b64 v9, v[72:73], v[74:75] offset0:0 offset1:8\nv_max3_u32 v76, v76, v86, v88\nv_mfma_f32_16x16x32_bf16 v[24:27], v[172:175], v[68:71], v[24:27]\n" \
"s_waitcnt lgkmcnt(4)\nv_max3_u32 v76, v76, v90, v92\nv_max_u32 v76, v76, v94\nv_mfma_f32_16x16x32_bf16 v[28:31], v[188:191], v[68:71], v[28:31]\nv_cmp_eq_u32 vcc, -1, v76\nv_mfma_f32_16x16x32_bf16 v[24:27], v[200:203], v[32:35], v[24:27]\ns_cbranch_vccnz .Lvu_re%=\n.Lvu_rer%=:\n" \
"s_barrier\ns_memrealtime s[68:69]\nds_read_b128 v[80:83], v0 offset:16384\nv_mfma_f32_16x16x32_bf16 v[28:31], v[216:219], v[32:35], v[28:31]\ns_waitcnt lgkmcnt(4)\nds_read_b128 v[84:87], v0 offset:17408\nv_mfma_f32_16x16x32_bf16 v[24:27], v[204:207], v[36:39], v[24:27]\nds_read_b128 v[88:91], v0 offset:18432\n" \
"v_mfma_f32_16x16x32_bf16 v[28:31], v[220:223], v[36:39], v[28:31]\ns_waitcnt lgkmcnt(5)\nds_read_b128 v[92:95], v0 offset:19456\nv_mfma_f32_16x16x32_bf16 v[24:27], v[232:235], v[40:43], v[24:27]\nds_read_b128 v[16:19], v0 offset:20480\nv_mfma_f32_16x16x32_bf16 v[28:31], v[240:243], v[40:43], v[28:31]\ns_waitcnt lgkmcnt(6)\nds_read_b128 v[20:23], v0 offset:21504\n" \
"v_mfma_f32_16x16x32_bf16 v[24:27], v[236:239], v[44:47], v[24:27]\nds_read_b128 v[32:35], v0 offset:22528\nv_mfma_f32_16x16x32_bf16 v[28:31], v[244:247], v[44:47], v[28:31]\ns_cmp_lt_i32 s40, 0\nds_read_b128 v[36:39], v0 offset:23552\ns_cbranch_scc1 .Lvu_first%=\n.Lvu_crit%=:\ns_waitcnt lgkmcnt(7)\n" \
"v_smfmac_f32_16x16x64_bf16 v[48:51], v[80:83], a[0:7], v4\ns_waitcnt lgkmcnt(6)\nds_read_b128 v[64:67], v1 offset:4096\nds_read_b128 v[40:43], v0 offset:24576\nv_mov_b32 v14, s40\nv_smfmac_f32_16x16x64_bf16 v[48:51], v[84:87], a[24:31], v4\ns_waitcnt lgkmcnt(7)\nds_read_b128 v[68:71], v1 offset:5120\n" \
"ds_read_b128 v[44:47], v0 offset:25600\nv_cvt_pk_bf16_f32 v76, v24, v25\nv_smfmac_f32_16x16x64_bf16 v[48:51], v[88:91], v[56:63], v4\ns_sub_u32 s88, s86, s89\ns_min_u32 s88, s88, 150\ns_waitcnt lgkmcnt(8)\nds_read_b128 v[80:83], v0 offset:28672\nv_cvt_pk_bf16_f32 v77, v26, v27\n" \
"v_smfmac_f32_16x16x64_bf16 v[48:51], v[92:95], a[80:87], v4\ns_waitcnt lgkmcnt(8)\nds_read_b128 v[84:87], v0 offset:29696\nv_cvt_pk_bf16_f32 v78, v28, v29\nv_smfmac_f32_16x16x64_bf16 v[48:51], v[16:19], a[104:111], v4\ns_add_u32 s88, s88, s68\ns_mov_b32 s89, s68\ns_waitcnt lgkmcnt(8)\n" \
"ds_read_b128 v[88:91], v0 offset:30720\nv_cvt_pk_bf16_f32 v79, v30, v31\nv_smfmac_f32_16x16x64_bf16 v[48:51], v[20:23], a[128:135], v4\ns_waitcnt lgkmcnt(8)\nds_read_b128 v[92:95], v0 offset:31744\nv_mov_b32 v15, s88\nv_smfmac_f32_16x16x64_bf16 v[48:51], v[32:35], a[152:159], v4\ns_waitcnt lgkmcnt(8)\n" \
"ds_read_b128 v[24:27], v0 offset:26624\nbuffer_store_dwordx2 v[14:15], off, s[36:39], %[sg] sc1\nv_smfmac_f32_16x16x64_bf16 v[48:51], v[36:39], a[184:191], v4\ns_waitcnt lgkmcnt(7)\ns_waitcnt lgkmcnt(6)\nds_read_b128 v[28:31], v0 offset:27648\nv_smfmac_f32_16x16x64_bf16 v[48:51], v[40:43], v[64:71], v4\ns_waitcnt lgkmcnt(6)\n" \
"ds_read_b128 v[56:59], v1 offset:2048\nv_smfmac_f32_16x16x64_bf16 v[48:51], v[44:47], a[224:231], v4\ns_waitcnt lgkmcnt(6)\nds_read_b128 v[60:63], v1 offset:3072\nv_smfmac_f32_16x16x64_bf16 v[48:51], v[80:83], v[144:151], v4\ns_waitcnt lgkmcnt(6)\nds_write2st64_b64 v9, v[76:77], v[78:79] offset0:2 offset1:10\nv_smfmac_f32_16x16x64_bf16 v[48:51], v[84:87], v[168:175], v4\n" \
"s_waitcnt lgkmcnt(6)\nv_smfmac_f32_16x16x64_bf16 v[48:51], v[88:91], v[200:207], v4\ns_waitcnt lgkmcnt(5)\nv_smfmac_f32_16x16x64_bf16 v[48:51], v[92:95], v[232:239], v4\ns_waitcnt lgkmcnt(4)\nv_smfmac_f32_16x16x64_bf16 v[48:51], v[24:27], v[96:103], v4\ns_waitcnt lgkmcnt(3)\nds_read_b128 v[64:67], v1 offset:6144\n" \
"v_smfmac_f32_16x16x64_bf16 v[48:51], v[28:31], v[128:135], v4\nds_read_b128 v[68:71], v1 offset:7168\nv_smfmac_f32_16x16x64_bf16 v[52:55], v[80:83], v[160:167], v4\nv_smfmac_f32_16x16x64_bf16 v[52:55], v[84:87], v[184:191], v4\nv_add_f32 v48, v48, v50\nv_smfmac_f32_16x16x64_bf16 v[52:55], v[88:91], v[216:223], v4\nds_read_b128 v[80:83], v0 offset:16384\nv_add_f32 v49, v49, v51\n" \
"v_smfmac_f32_16x16x64_bf16 v[52:55], v[92:95], v[240:247], v4\nds_read_b128 v[84:87], v0 offset:17408\nv_cvt_pk_bf16_f32 v14, v48, v49\nv_smfmac_f32_16x16x64_bf16 v[52:55], v[16:19], a[120:127], v4\ns_waitcnt lgkmcnt(5)\nds_write_b16 v5, v14 offset:0\nds_write_b16_d16_hi v5, v14 offset:16\nv_smfmac_f32_16x16x64_bf16 v[52:55], v[20:23], v[56:63], v4\n" \
"ds_read_b128 v[88:91], v0 offset:18432\nv_mov_b64 v[16:17], 0\nv_smfmac_f32_16x16x64_bf16 v[52:55], v[24:27], v[112:119], v4\ns_waitcnt lgkmcnt(5)\nds_read_b128 v[92:95], v0 offset:19456\nv_mov_b64 v[18:19], 0\nv_smfmac_f32_16x16x64_bf16 v[52:55], v[28:31], v[64:71], v4\nds_read_b128 v[56:59], v1 offset:8192\n" \
"v_mov_b64 v[20:21], 0\nv_smfmac_f32_16x16x64_bf16 v[52:55], v[32:35], a[168:175], v4\ns_waitcnt lgkmcnt(3)\nds_read_b128 v[60:63], v1 offset:9216\ns_barrier\nds_read_b128 v[72:75], v11 offset:57344\nv_smfmac_f32_16x16x64_bf16 v[52:55], v[36:39], a[192:199], v4\nds_read_b128 v[76:79], v11 offset:58368\n" \
"v_mov_b64 v[22:23], 0\nv_smfmac_f32_16x16x64_bf16 v[52:55], v[40:43], a[216:223], v4\nv_mov_b64 v[24:25], 0\nv_smfmac_f32_16x16x64_bf16 v[52:55], v[44:47], a[240:247], v4\nds_read_b128 v[64:67], v1 offset:12288\nv_mov_b64 v[26:27], 0\nv_smfmac_f32_16x16x64_bf16 v[52:55], v[80:83], a[16:23], v4\nds_read_b128 v[68:71], v1 offset:13312\n" \
"v_mov_b64 v[28:29], 0\nv_smfmac_f32_16x16x64_bf16 v[52:55], v[84:87], a[40:47], v4\ns_waitcnt lgkmcnt(7)\nv_mov_b64 v[30:31], 0\nv_smfmac_f32_16x16x64_bf16 v[52:55], v[88:91], a[64:71], v4\ns_waitcnt lgkmcnt(6)\nv_mov_b64 v[32:33], 0\nv_smfmac_f32_16x16x64_bf16 v[52:55], v[92:95], a[88:95], v4\n" \
"s_waitcnt lgkmcnt(3)\nv_mov_b64 v[34:35], 0\nv_smfmac_f32_16x16x64_bf16 v[16:19], v[72:75], a[8:15], v10\ns_waitcnt lgkmcnt(2)\nv_mov_b64 v[36:37], 0\nv_smfmac_f32_16x16x64_bf16 v[16:19], v[76:79], v[56:63], v10\nds_read_b128 v[56:59], v1 offset:10240\nv_add_f32 v52, v52, v54\n" \
"v_smfmac_f32_16x16x64_bf16 v[20:23], v[72:75], a[56:63], v10\nds_read_b128 v[60:63], v1 offset:11264\nv_add_f32 v53, v53, v55\nv_smfmac_f32_16x16x64_bf16 v[20:23], v[76:79], a[72:79], v10\nv_cvt_pk_bf16_f32 v14, v52, v53\nv_smfmac_f32_16x16x64_bf16 v[24:27], v[72:75], a[112:119], v10\ns_waitcnt lgkmcnt(2)\nds_write_b16 v5, v14 offset:2048\n" \
"ds_write_b16_d16_hi v5, v14 offset:2064\nv_smfmac_f32_16x16x64_bf16 v[24:27], v[76:79], v[64:71], v10\nds_read_b128 v[64:67], v1 offset:14336\nv_mov_b64 v[38:39], 0\nv_smfmac_f32_16x16x64_bf16 v[28:31], v[72:75], a[160:167], v10\nds_read_b128 v[68:71], v1 offset:15360\nv_mov_b64 v[40:41], 0\nv_smfmac_f32_16x16x64_bf16 v[28:31], v[76:79], a[176:183], v10\n" \
"v_mov_b64 v[42:43], 0\nv_smfmac_f32_16x16x64_bf16 v[32:35], v[72:75], a[208:215], v10\ns_waitcnt lgkmcnt(2)\ns_barrier\ns_mov_b32 s59, 0\nds_read_b128 v[80:83], v11 offset:59392\nv_smfmac_f32_16x16x64_bf16 v[32:35], v[76:79], v[56:63], v10\ns_add_u32 m0, s57, 0\n" \
"s_add_u32 s58, s61, 0\nds_read_b128 v[56:59], v1 offset:16384\nbuffer_load_dwordx4 v0, s[36:39], s58 offen offset:0 sc1 lds\nds_read_b128 v[84:87], v11 offset:60416\nv_smfmac_f32_16x16x64_bf16 v[36:39], v[72:75], v[104:111], v10\nds_read_b128 v[60:63], v1 offset:17408\nv_mov_b64 v[44:45], 0\nv_smfmac_f32_16x16x64_bf16 v[36:39], v[76:79], v[120:127], v10\n" \
"v_mov_b64 v[46:47], 0\nv_smfmac_f32_16x16x64_bf16 v[40:43], v[72:75], v[152:159], v10\ns_waitcnt lgkmcnt(4)\nbuffer_load_dwordx4 v0, s[36:39], s58 offen offset:1024 sc1 lds\nv_smfmac_f32_16x16x64_bf16 v[40:43], v[76:79], v[64:71], v10\nds_read_b128 v[64:67], v1 offset:20480\nv_mov_b64 v[48:49], 0\nv_smfmac_f32_16x16x64_bf16 v[44:47], v[72:75], v[208:215], v10\n" \
"ds_read_b128 v[68:71], v1 offset:21504\nv_mov_b64 v[50:51], 0\nv_smfmac_f32_16x16x64_bf16 v[44:47], v[76:79], v[224:231], v10\ns_waitcnt lgkmcnt(5)\nbuffer_load_dwordx4 v0, s[36:39], s58 offen offset:2048 sc1 lds\nv_smfmac_f32_16x16x64_bf16 v[16:19], v[80:83], a[32:39], v10\ns_waitcnt lgkmcnt(3)\nds_read_b128 v[72:75], v11 offset:49152\n" \
"v_mov_b64 v[52:53], 0\nv_smfmac_f32_16x16x64_bf16 v[16:19], v[84:87], a[48:55], v10\ns_waitcnt lgkmcnt(3)\nds_read_b128 v[76:79], v11 offset:50176\nv_mov_b64 v[54:55], 0\nv_smfmac_f32_16x16x64_bf16 v[20:23], v[80:83], v[56:63], v10\nds_read_b128 v[56:59], v1 offset:18432\nbuffer_load_dwordx4 v0, s[36:39], s58 offen offset:3072 sc1 lds\n" \
"v_smfmac_f32_16x16x64_bf16 v[20:23], v[84:87], a[96:103], v10\nds_read_b128 v[60:63], v1 offset:19456\nv_cvt_pk_bf16_f32 v88, v16, v17\nv_smfmac_f32_16x16x64_bf16 v[24:27], v[80:83], a[136:143], v10\nv_and_or_b32 v88, v88, -2, v12\nv_cvt_pk_bf16_f32 v89, v18, v19\nv_smfmac_f32_16x16x64_bf16 v[24:27], v[84:87], a[144:151], v10\ns_waitcnt lgkmcnt(4)\n" \
"v_cvt_pk_bf16_f32 v90, v20, v21\nv_cvt_pk_bf16_f32 v91, v22, v23\nv_and_or_b32 v90, v90, -2, v12\nv_smfmac_f32_16x16x64_bf16 v[28:31], v[80:83], v[64:71], v10\nds_read_b128 v[64:67], v1 offset:22528\nv_permlane16_swap_b32 v89, v91\nv_smfmac_f32_16x16x64_bf16 v[28:31], v[84:87], a[200:207], v10\nds_read_b128 v[68:71], v1 offset:23552\n" \
"v_permlane16_swap_b32 v88, v90\nv_smfmac_f32_16x16x64_bf16 v[32:35], v[80:83], a[232:239], v10\ns_add_u32 s63, s59, 0\nds_read_b128 v[16:19], v11 offset:51200\nv_cvt_pk_bf16_f32 v92, v24, v25\nv_and_or_b32 v92, v92, -2, v12\nv_smfmac_f32_16x16x64_bf16 v[32:35], v[84:87], a[248:255], v10\ns_waitcnt lgkmcnt(3)\n" \
"ds_read_b128 v[20:23], v11 offset:52224\nbuffer_store_dwordx4 v[88:91], v6, s[36:39], s63 offen sc1\nv_cvt_pk_bf16_f32 v93, v26, v27\nv_cvt_pk_bf16_f32 v94, v28, v29\nv_cvt_pk_bf16_f32 v95, v30, v31\nv_and_or_b32 v94, v94, -2, v12\nv_smfmac_f32_16x16x64_bf16 v[36:39], v[80:83], v[56:63], v10\nds_read_b128 v[56:59], v1 offset:8192\n" \
"v_permlane16_swap_b32 v93, v95\nv_smfmac_f32_16x16x64_bf16 v[36:39], v[84:87], v[136:143], v10\nds_read_b128 v[60:63], v1 offset:9216\nv_permlane16_swap_b32 v92, v94\nv_smfmac_f32_16x16x64_bf16 v[40:43], v[80:83], v[176:183], v10\ns_add_u32 s63, s59, 131072\nds_read_b128 v[24:27], v11 offset:53248\nv_cvt_pk_bf16_f32 v88, v32, v33\n" \
"v_and_or_b32 v88, v88, -2, v12\nv_smfmac_f32_16x16x64_bf16 v[40:43], v[84:87], v[192:199], v10\ns_waitcnt lgkmcnt(5)\nds_read_b128 v[28:31], v11 offset:54272\nbuffer_store_dwordx4 v[92:95], v6, s[36:39], s63 offen sc1\nv_cvt_pk_bf16_f32 v89, v34, v35\nv_cvt_pk_bf16_f32 v90, v36, v37\nv_cvt_pk_bf16_f32 v91, v38, v39\n" \
"v_and_or_b32 v90, v90, -2, v12\nv_smfmac_f32_16x16x64_bf16 v[44:47], v[80:83], v[64:71], v10\nds_read_b128 v[64:67], v1 offset:16384\nv_permlane16_swap_b32 v89, v91\nv_smfmac_f32_16x16x64_bf16 v[44:47], v[84:87], v[248:255], v10\ns_cmp_ge_i32 s40, 63\nds_read_b128 v[68:71], v1 offset:17408\nv_permlane16_swap_b32 v88, v90\n" \
"s_cbranch_scc1 .Lvu_last%=\n.Lvu_y%=:\nv_smfmac_f32_16x16x64_bf16 v[48:51], v[72:75], a[8:15], v10\ns_add_u32 s63, s59, 4096\nds_read_b128 v[32:35], v11 offset:55296\nds_read_b128 v[36:39], v11 offset:56320\nv_cvt_pk_bf16_f32 v92, v40, v41\nv_and_or_b32 v92, v92, -2, v12\n" \
"v_smfmac_f32_16x16x64_bf16 v[52:55], v[76:79], a[8:15], v10\ns_waitcnt lgkmcnt(9)\ns_waitcnt lgkmcnt(6)\nbuffer_store_dwordx4 v[88:91], v6, s[36:39], s63 offen sc1\nv_cvt_pk_bf16_f32 v93, v42, v43\nv_cvt_pk_bf16_f32 v94, v44, v45\nv_cvt_pk_bf16_f32 v95, v46, v47\nv_and_or_b32 v94, v94, -2, v12\n" \
"v_smfmac_f32_16x16x64_bf16 v[48:51], v[16:19], v[56:63], v10\ns_add_u32 s40, s40, 1\ns_and_b32 s44, s40, 1\nv_permlane16_swap_b32 v93, v95\nv_smfmac_f32_16x16x64_bf16 v[52:55], v[20:23], v[56:63], v10\ns_lshl_b32 s55, s44, 22\ns_waitcnt lgkmcnt(5)\nv_permlane16_swap_b32 v92, v94\n" \
"v_smfmac_f32_16x16x64_bf16 v[48:51], v[24:27], a[32:39], v10\ns_waitcnt lgkmcnt(4)\ns_add_u32 s63, s59, 135168\nds_read_b128 v[56:59], v1 offset:12288\nv_smfmac_f32_16x16x64_bf16 v[52:55], v[28:31], a[32:39], v10\ns_waitcnt lgkmcnt(2)\nds_read_b128 v[60:63], v1 offset:13312\nbuffer_store_dwordx4 v[92:95], v6, s[36:39], s63 offen sc1\n" \
"v_mov_b64 v[80:81], 0\nv_mov_b64 v[82:83], 0\nv_mov_b64 v[84:85], 0\nv_mov_b64 v[86:87], 0\nv_smfmac_f32_16x16x64_bf16 v[48:51], v[32:35], a[48:55], v10\ns_memrealtime s[86:87]\ns_bfe_u32 s45, s40, 0x10001\ns_waitcnt lgkmcnt(2)\n" \
"v_smfmac_f32_16x16x64_bf16 v[52:55], v[36:39], a[48:55], v10\nv_mov_b32 v13, s45\nv_smfmac_f32_16x16x64_bf16 v[80:83], v[72:75], a[56:63], v10\nv_cvt_pk_bf16_f32 v88, v48, v49\nv_smfmac_f32_16x16x64_bf16 v[84:87], v[76:79], a[56:63], v10\nv_cvt_pk_bf16_f32 v89, v50, v51\nv_smfmac_f32_16x16x64_bf16 v[80:83], v[16:19], a[72:79], v10\nv_cvt_pk_bf16_f32 v90, v52, v53\n" \
"v_smfmac_f32_16x16x64_bf16 v[84:87], v[20:23], a[72:79], v10\ns_add_u32 s60, s55, 0\nv_cvt_pk_bf16_f32 v91, v54, v55\nv_smfmac_f32_16x16x64_bf16 v[80:83], v[24:27], v[64:71], v10\nv_and_or_b32 v88, v88, -2, v13\nv_smfmac_f32_16x16x64_bf16 v[84:87], v[28:31], v[64:71], v10\ns_and_b32 s42, s40, 3\ns_lshl_b32 s42, s42, 15\n" \
"v_and_or_b32 v90, v90, -2, v13\nv_smfmac_f32_16x16x64_bf16 v[80:83], v[32:35], a[96:103], v10\nbuffer_store_dwordx4 v[88:91], v7, s[36:39], s60 offen offset:0 sc1\nv_mov_b64 v[48:49], 0\nv_mov_b64 v[50:51], 0\nv_mov_b64 v[52:53], 0\nv_mov_b32 v12, s44\nv_smfmac_f32_16x16x64_bf16 v[84:87], v[36:39], a[96:103], v10\n" \
"ds_read_b128 v[64:67], v1 offset:20480\nv_mov_b64 v[54:55], 0\nv_smfmac_f32_16x16x64_bf16 v[48:51], v[72:75], a[112:119], v10\nds_read_b128 v[68:71], v1 offset:21504\nv_cvt_pk_bf16_f32 v92, v80, v81\nv_smfmac_f32_16x16x64_bf16 v[52:55], v[76:79], a[112:119], v10\ns_waitcnt lgkmcnt(2)\nv_cvt_pk_bf16_f32 v93, v82, v83\n" \
"v_smfmac_f32_16x16x64_bf16 v[48:51], v[16:19], v[56:63], v10\nv_cvt_pk_bf16_f32 v94, v84, v85\nv_smfmac_f32_16x16x64_bf16 v[52:55], v[20:23], v[56:63], v10\ns_add_u32 s60, s55, 4096\nv_cvt_pk_bf16_f32 v95, v86, v87\nv_smfmac_f32_16x16x64_bf16 v[48:51], v[24:27], a[136:143], v10\nds_read_b128 v[56:59], v1 offset:10240\nv_and_or_b32 v92, v92, -2, v13\n" \
"v_smfmac_f32_16x16x64_bf16 v[52:55], v[28:31], a[136:143], v10\nds_read_b128 v[60:63], v1 offset:11264\nv_and_or_b32 v94, v94, -2, v13\nv_smfmac_f32_16x16x64_bf16 v[48:51], v[32:35], a[144:151], v10\ns_waitcnt vmcnt(5)\nbuffer_store_dwordx4 v[92:95], v7, s[36:39], s60 offen offset:0 sc1\nv_mov_b64 v[80:81], 0\nv_mov_b64 v[82:83], 0\n" \
"v_mov_b64 v[84:85], 0\ns_barrier\nv_smfmac_f32_16x16x64_bf16 v[52:55], v[36:39], a[144:151], v10\nv_mov_b64 v[86:87], 0\nv_smfmac_f32_16x16x64_bf16 v[80:83], v[72:75], a[160:167], v10\nv_cvt_pk_bf16_f32 v88, v48, v49\nv_smfmac_f32_16x16x64_bf16 v[84:87], v[76:79], a[160:167], v10\nv_cvt_pk_bf16_f32 v89, v50, v51\n" \
"v_smfmac_f32_16x16x64_bf16 v[80:83], v[16:19], a[176:183], v10\ns_cmp_lt_u32 s40, 2\ns_cselect_b32 s54, %[fy], -2147483648\nv_cvt_pk_bf16_f32 v90, v52, v53\nv_smfmac_f32_16x16x64_bf16 v[84:87], v[20:23], a[176:183], v10\ns_add_u32 s60, s55, 16384\ns_waitcnt lgkmcnt(2)\nv_cvt_pk_bf16_f32 v91, v54, v55\n" \
"v_smfmac_f32_16x16x64_bf16 v[80:83], v[24:27], v[64:71], v10\nv_and_or_b32 v88, v88, -2, v13\nv_smfmac_f32_16x16x64_bf16 v[84:87], v[28:31], v[64:71], v10\nv_and_or_b32 v90, v90, -2, v13\nv_smfmac_f32_16x16x64_bf16 v[80:83], v[32:35], a[200:207], v10\nbuffer_store_dwordx4 v[88:91], v7, s[36:39], s60 offen offset:0 sc1\nv_mov_b64 v[48:49], 0\nv_mov_b64 v[50:51], 0\n" \
"v_mov_b64 v[52:53], 0\nv_smfmac_f32_16x16x64_bf16 v[84:87], v[36:39], a[200:207], v10\nds_read_b128 v[64:67], v1 offset:18432\nv_mov_b64 v[54:55], 0\nv_smfmac_f32_16x16x64_bf16 v[48:51], v[72:75], a[208:215], v10\nds_read_b128 v[68:71], v1 offset:19456\nv_cvt_pk_bf16_f32 v92, v80, v81\nv_smfmac_f32_16x16x64_bf16 v[52:55], v[76:79], a[208:215], v10\n" \
"s_waitcnt lgkmcnt(2)\nv_cvt_pk_bf16_f32 v93, v82, v83\nv_smfmac_f32_16x16x64_bf16 v[48:51], v[16:19], v[56:63], v10\nv_cvt_pk_bf16_f32 v94, v84, v85\nv_smfmac_f32_16x16x64_bf16 v[52:55], v[20:23], v[56:63], v10\ns_add_u32 s60, s55, 20480\nv_cvt_pk_bf16_f32 v95, v86, v87\nv_smfmac_f32_16x16x64_bf16 v[48:51], v[24:27], a[232:239], v10\n" \
"ds_read_b128 v[56:59], v1 offset:14336\nv_and_or_b32 v92, v92, -2, v13\nv_smfmac_f32_16x16x64_bf16 v[52:55], v[28:31], a[232:239], v10\nds_read_b128 v[60:63], v1 offset:15360\nv_and_or_b32 v94, v94, -2, v13\nv_smfmac_f32_16x16x64_bf16 v[48:51], v[32:35], a[248:255], v10\nbuffer_store_dwordx4 v[92:95], v7, s[36:39], s60 offen offset:0 sc1\nv_mov_b64 v[80:81], 0\n" \
"v_mov_b64 v[82:83], 0\nv_mov_b64 v[84:85], 0\nv_smfmac_f32_16x16x64_bf16 v[52:55], v[36:39], a[248:255], v10\nv_mov_b64 v[86:87], 0\nv_smfmac_f32_16x16x64_bf16 v[80:83], v[72:75], v[104:111], v10\nv_cvt_pk_bf16_f32 v88, v48, v49\nv_smfmac_f32_16x16x64_bf16 v[84:87], v[76:79], v[104:111], v10\nv_cvt_pk_bf16_f32 v89, v50, v51\n" \
"v_smfmac_f32_16x16x64_bf16 v[80:83], v[16:19], v[120:127], v10\nv_cvt_pk_bf16_f32 v90, v52, v53\nv_smfmac_f32_16x16x64_bf16 v[84:87], v[20:23], v[120:127], v10\ns_add_u32 s60, s55, 0\ns_waitcnt lgkmcnt(2)\nv_cvt_pk_bf16_f32 v91, v54, v55\nv_smfmac_f32_16x16x64_bf16 v[80:83], v[24:27], v[64:71], v10\nv_and_or_b32 v88, v88, -2, v13\n" \
"v_smfmac_f32_16x16x64_bf16 v[84:87], v[28:31], v[64:71], v10\nv_and_or_b32 v90, v90, -2, v13\nv_smfmac_f32_16x16x64_bf16 v[80:83], v[32:35], v[136:143], v10\nbuffer_store_dwordx4 v[88:91], v7, s[36:39], s60 offen offset:2048 sc1\nv_xor_b32 v11, 128, v11\nv_mov_b64 v[48:49], 0\nv_mov_b64 v[50:51], 0\nv_mov_b64 v[52:53], 0\n" \
"v_smfmac_f32_16x16x64_bf16 v[84:87], v[36:39], v[136:143], v10\nds_read_b128 v[64:67], v1 offset:22528\nv_mov_b64 v[54:55], 0\nv_smfmac_f32_16x16x64_bf16 v[48:51], v[72:75], v[152:159], v10\nds_read_b128 v[68:71], v1 offset:23552\nv_cvt_pk_bf16_f32 v92, v80, v81\nv_smfmac_f32_16x16x64_bf16 v[52:55], v[76:79], v[152:159], v10\ns_waitcnt lgkmcnt(2)\n" \
"ds_read_b128 v[40:43], v11 offset:49152\nv_cvt_pk_bf16_f32 v93, v82, v83\nv_smfmac_f32_16x16x64_bf16 v[48:51], v[16:19], v[56:63], v10\nds_read_b128 v[44:47], v11 offset:50176\nv_cvt_pk_bf16_f32 v94, v84, v85\nv_smfmac_f32_16x16x64_bf16 v[52:55], v[20:23], v[56:63], v10\ns_add_u32 s60, s55, 4096\nv_cvt_pk_bf16_f32 v95, v86, v87\n" \
"v_smfmac_f32_16x16x64_bf16 v[48:51], v[24:27], v[176:183], v10\nds_read_b128 v[56:59], v1 offset:8192\nv_and_or_b32 v92, v92, -2, v13\nv_smfmac_f32_16x16x64_bf16 v[52:55], v[28:31], v[176:183], v10\nds_read_b128 v[60:63], v1 offset:9216\nv_and_or_b32 v94, v94, -2, v13\nv_smfmac_f32_16x16x64_bf16 v[48:51], v[32:35], v[192:199], v10\nbuffer_store_dwordx4 v[92:95], v7, s[36:39], s60 offen offset:2048 sc1\n" \
"v_mov_b64 v[80:81], 0\nv_mov_b64 v[82:83], 0\nv_mov_b64 v[84:85], 0\nv_smfmac_f32_16x16x64_bf16 v[52:55], v[36:39], v[192:199], v10\nv_mov_b64 v[86:87], 0\nv_smfmac_f32_16x16x64_bf16 v[80:83], v[72:75], v[208:215], v10\nds_read_b128 v[72:75], v11 offset:51200\nv_cvt_pk_bf16_f32 v88, v48, v49\n" \
"v_smfmac_f32_16x16x64_bf16 v[84:87], v[76:79], v[208:215], v10\nds_read_b128 v[76:79], v11 offset:52224\nv_cvt_pk_bf16_f32 v89, v50, v51\nv_smfmac_f32_16x16x64_bf16 v[80:83], v[16:19], v[224:231], v10\ns_add_u32 s60, s55, 16384\nds_read_b128 v[16:19], v11 offset:53248\nv_cvt_pk_bf16_f32 v90, v52, v53\nv_smfmac_f32_16x16x64_bf16 v[84:87], v[20:23], v[224:231], v10\n" \
"s_waitcnt lgkmcnt(7)\nds_read_b128 v[20:23], v11 offset:54272\nv_cvt_pk_bf16_f32 v91, v54, v55\nv_smfmac_f32_16x16x64_bf16 v[80:83], v[24:27], v[64:71], v10\nds_read_b128 v[24:27], v11 offset:55296\nv_and_or_b32 v88, v88, -2, v13\nv_smfmac_f32_16x16x64_bf16 v[84:87], v[28:31], v[64:71], v10\nds_read_b128 v[28:31], v11 offset:56320\n" \
"v_and_or_b32 v90, v90, -2, v13\nv_smfmac_f32_16x16x64_bf16 v[80:83], v[32:35], v[248:255], v10\nbuffer_store_dwordx4 v[88:91], v7, s[36:39], s60 offen offset:2048 sc1\nv_mov_b64 v[48:49], 0\nv_mov_b64 v[50:51], 0\nv_mov_b64 v[52:53], 0\nv_smfmac_f32_16x16x64_bf16 v[84:87], v[36:39], v[248:255], v10\ns_waitcnt lgkmcnt(9)\n" \
"ds_read_b128 v[64:67], v1 offset:16384\nv_mov_b64 v[54:55], 0\nv_smfmac_f32_16x16x64_bf16 v[48:51], v[40:43], a[8:15], v10\ns_waitcnt lgkmcnt(9)\nds_read_b128 v[68:71], v1 offset:17408\nv_cvt_pk_bf16_f32 v92, v80, v81\nv_smfmac_f32_16x16x64_bf16 v[52:55], v[44:47], a[8:15], v10\ns_waitcnt lgkmcnt(7)\n" \
"v_cvt_pk_bf16_f32 v93, v82, v83\nv_smfmac_f32_16x16x64_bf16 v[48:51], v[72:75], v[56:63], v10\ns_waitcnt lgkmcnt(6)\nv_cvt_pk_bf16_f32 v94, v84, v85\nv_smfmac_f32_16x16x64_bf16 v[52:55], v[76:79], v[56:63], v10\ns_add_u32 s60, s55, 20480\ns_waitcnt lgkmcnt(5)\nv_cvt_pk_bf16_f32 v95, v86, v87\n" \
"v_smfmac_f32_16x16x64_bf16 v[48:51], v[16:19], a[32:39], v10\ns_waitcnt lgkmcnt(4)\nds_read_b128 v[56:59], v1 offset:12288\nv_and_or_b32 v92, v92, -2, v13\nv_smfmac_f32_16x16x64_bf16 v[52:55], v[20:23], a[32:39], v10\ns_waitcnt lgkmcnt(4)\nds_read_b128 v[60:63], v1 offset:13312\nv_and_or_b32 v94, v94, -2, v13\n" \
"v_smfmac_f32_16x16x64_bf16 v[48:51], v[24:27], a[48:55], v10\ns_waitcnt lgkmcnt(4)\nbuffer_store_dwordx4 v[92:95], v7, s[36:39], s60 offen offset:2048 sc1\nv_xor_b32 v11, 128, v11\nv_mov_b64 v[80:81], 0\nv_mov_b64 v[82:83], 0\nv_mov_b64 v[84:85], 0\nv_smfmac_f32_16x16x64_bf16 v[52:55], v[28:31], a[48:55], v10\n" \
"v_mov_b64 v[86:87], 0\nv_smfmac_f32_16x16x64_bf16 v[80:83], v[40:43], a[56:63], v10\nv_cvt_pk_bf16_f32 v88, v48, v49\nv_smfmac_f32_16x16x64_bf16 v[84:87], v[44:47], a[56:63], v10\nv_cvt_pk_bf16_f32 v89, v50, v51\nv_smfmac_f32_16x16x64_bf16 v[80:83], v[72:75], a[72:79], v10\nv_cvt_pk_bf16_f32 v90, v52, v53\nv_smfmac_f32_16x16x64_bf16 v[84:87], v[76:79], a[72:79], v10\n" \
"s_add_u32 s60, s55, 0\ns_waitcnt lgkmcnt(2)\nv_cvt_pk_bf16_f32 v91, v54, v55\nv_smfmac_f32_16x16x64_bf16 v[80:83], v[16:19], v[64:71], v10\nv_and_or_b32 v88, v88, -2, v13\nv_smfmac_f32_16x16x64_bf16 v[84:87], v[20:23], v[64:71], v10\nv_and_or_b32 v90, v90, -2, v13\nv_smfmac_f32_16x16x64_bf16 v[80:83], v[24:27], a[96:103], v10\n" \
"buffer_store_dwordx4 v[88:91], v7, s[36:39], s60 offen offset:512 sc1\nv_mov_b64 v[48:49], 0\nv_mov_b64 v[50:51], 0\nv_mov_b64 v[52:53], 0\nv_smfmac_f32_16x16x64_bf16 v[84:87], v[28:31], a[96:103], v10\nds_read_b128 v[64:67], v1 offset:20480\nv_mov_b64 v[54:55], 0\nv_smfmac_f32_16x16x64_bf16 v[48:51], v[40:43], a[112:119], v10\n" \
"ds_read_b128 v[68:71], v1 offset:21504\nv_cvt_pk_bf16_f32 v92, v80, v81\nv_smfmac_f32_16x16x64_bf16 v[52:55], v[44:47], a[112:119], v10\ns_waitcnt lgkmcnt(2)\nv_cvt_pk_bf16_f32 v93, v82, v83\nv_smfmac_f32_16x16x64_bf16 v[48:51], v[72:75], v[56:63], v10\nv_cvt_pk_bf16_f32 v94, v84, v85\nv_smfmac_f32_16x16x64_bf16 v[52:55], v[76:79], v[56:63], v10\n" \
"s_add_u32 s60, s55, 4096\nv_cvt_pk_bf16_f32 v95, v86, v87\nv_smfmac_f32_16x16x64_bf16 v[48:51], v[16:19], a[136:143], v10\nds_read_b128 v[56:59], v1 offset:10240\nv_and_or_b32 v92, v92, -2, v13\nv_smfmac_f32_16x16x64_bf16 v[52:55], v[20:23], a[136:143], v10\nds_read_b128 v[60:63], v1 offset:11264\nv_and_or_b32 v94, v94, -2, v13\n" \
"v_smfmac_f32_16x16x64_bf16 v[48:51], v[24:27], a[144:151], v10\nbuffer_store_dwordx4 v[92:95], v7, s[36:39], s60 offen offset:512 sc1\nv_mov_b64 v[80:81], 0\nv_mov_b64 v[82:83], 0\nv_mov_b64 v[84:85], 0\nv_smfmac_f32_16x16x64_bf16 v[52:55], v[28:31], a[144:151], v10\nv_mov_b64 v[86:87], 0\nv_smfmac_f32_16x16x64_bf16 v[80:83], v[40:43], a[160:167], v10\n" \
"v_cvt_pk_bf16_f32 v88, v48, v49\nv_smfmac_f32_16x16x64_bf16 v[84:87], v[44:47], a[160:167], v10\nv_cvt_pk_bf16_f32 v89, v50, v51\nv_smfmac_f32_16x16x64_bf16 v[80:83], v[72:75], a[176:183], v10\nv_cvt_pk_bf16_f32 v90, v52, v53\nv_smfmac_f32_16x16x64_bf16 v[84:87], v[76:79], a[176:183], v10\ns_add_u32 s60, s55, 16384\ns_waitcnt lgkmcnt(2)\n" \
"v_cvt_pk_bf16_f32 v91, v54, v55\nv_smfmac_f32_16x16x64_bf16 v[80:83], v[16:19], v[64:71], v10\nv_and_or_b32 v88, v88, -2, v13\nv_smfmac_f32_16x16x64_bf16 v[84:87], v[20:23], v[64:71], v10\nv_and_or_b32 v90, v90, -2, v13\nv_smfmac_f32_16x16x64_bf16 v[80:83], v[24:27], a[200:207], v10\nbuffer_store_dwordx4 v[88:91], v7, s[36:39], s60 offen offset:512 sc1\nv_mov_b64 v[48:49], 0\n" \
"v_mov_b64 v[50:51], 0\nv_mov_b64 v[52:53], 0\nv_smfmac_f32_16x16x64_bf16 v[84:87], v[28:31], a[200:207], v10\nds_read_b128 v[64:67], v1 offset:18432\nv_mov_b64 v[54:55], 0\nv_smfmac_f32_16x16x64_bf16 v[48:51], v[40:43], a[208:215], v10\nds_read_b128 v[68:71], v1 offset:19456\nv_cvt_pk_bf16_f32 v92, v80, v81\n" \
"v_smfmac_f32_16x16x64_bf16 v[52:55], v[44:47], a[208:215], v10\ns_waitcnt lgkmcnt(2)\nv_cvt_pk_bf16_f32 v93, v82, v83\nv_smfmac_f32_16x16x64_bf16 v[48:51], v[72:75], v[56:63], v10\nv_cvt_pk_bf16_f32 v94, v84, v85\nv_smfmac_f32_16x16x64_bf16 v[52:55], v[76:79], v[56:63], v10\ns_add_u32 s60, s55, 20480\nv_cvt_pk_bf16_f32 v95, v86, v87\n" \
"v_smfmac_f32_16x16x64_bf16 v[48:51], v[16:19], a[232:239], v10\nds_read_b128 v[56:59], v1 offset:14336\nv_and_or_b32 v92, v92, -2, v13\nv_smfmac_f32_16x16x64_bf16 v[52:55], v[20:23], a[232:239], v10\nds_read_b128 v[60:63], v1 offset:15360\nv_and_or_b32 v94, v94, -2, v13\nv_smfmac_f32_16x16x64_bf16 v[48:51], v[24:27], a[248:255], v10\nbuffer_store_dwordx4 v[92:95], v7, s[36:39], s60 offen offset:512 sc1\n" \
"v_mov_b64 v[80:81], 0\nv_mov_b64 v[82:83], 0\nv_mov_b64 v[84:85], 0\nv_smfmac_f32_16x16x64_bf16 v[52:55], v[28:31], a[248:255], v10\nv_mov_b64 v[86:87], 0\nv_smfmac_f32_16x16x64_bf16 v[80:83], v[40:43], v[104:111], v10\nv_cvt_pk_bf16_f32 v88, v48, v49\nv_smfmac_f32_16x16x64_bf16 v[84:87], v[44:47], v[104:111], v10\n" \
"v_cvt_pk_bf16_f32 v89, v50, v51\nv_smfmac_f32_16x16x64_bf16 v[80:83], v[72:75], v[120:127], v10\nv_cvt_pk_bf16_f32 v90, v52, v53\nv_smfmac_f32_16x16x64_bf16 v[84:87], v[76:79], v[120:127], v10\ns_add_u32 s60, s55, 0\ns_waitcnt lgkmcnt(2)\nv_cvt_pk_bf16_f32 v91, v54, v55\nv_smfmac_f32_16x16x64_bf16 v[80:83], v[16:19], v[64:71], v10\n" \
"v_and_or_b32 v88, v88, -2, v13\nv_smfmac_f32_16x16x64_bf16 v[84:87], v[20:23], v[64:71], v10\nv_and_or_b32 v90, v90, -2, v13\nv_smfmac_f32_16x16x64_bf16 v[80:83], v[24:27], v[136:143], v10\nbuffer_store_dwordx4 v[88:91], v7, s[36:39], s60 offen offset:2560 sc1\nv_mov_b64 v[48:49], 0\nv_mov_b64 v[50:51], 0\nv_mov_b64 v[52:53], 0\n" \
"v_smfmac_f32_16x16x64_bf16 v[84:87], v[28:31], v[136:143], v10\nds_read_b128 v[64:67], v1 offset:22528\nv_mov_b64 v[54:55], 0\nv_smfmac_f32_16x16x64_bf16 v[48:51], v[40:43], v[152:159], v10\nds_read_b128 v[68:71], v1 offset:23552\nv_cvt_pk_bf16_f32 v92, v80, v81\nv_smfmac_f32_16x16x64_bf16 v[52:55], v[44:47], v[152:159], v10\ns_waitcnt lgkmcnt(2)\n" \
"v_cvt_pk_bf16_f32 v93, v82, v83\nv_smfmac_f32_16x16x64_bf16 v[48:51], v[72:75], v[56:63], v10\nv_cvt_pk_bf16_f32 v94, v84, v85\nv_smfmac_f32_16x16x64_bf16 v[52:55], v[76:79], v[56:63], v10\ns_add_u32 s60, s55, 4096\nv_cvt_pk_bf16_f32 v95, v86, v87\nv_smfmac_f32_16x16x64_bf16 v[48:51], v[16:19], v[176:183], v10\nv_and_or_b32 v92, v92, -2, v13\n" \
"v_smfmac_f32_16x16x64_bf16 v[52:55], v[20:23], v[176:183], v10\nv_and_or_b32 v94, v94, -2, v13\nv_smfmac_f32_16x16x64_bf16 v[48:51], v[24:27], v[192:199], v10\nbuffer_store_dwordx4 v[92:95], v7, s[36:39], s60 offen offset:2560 sc1\nv_mov_b64 v[80:81], 0\nv_mov_b64 v[82:83], 0\nv_mov_b64 v[84:85], 0\nv_smfmac_f32_16x16x64_bf16 v[52:55], v[28:31], v[192:199], v10\n" \
"ds_read_b128 v[32:35], v2 offset:0\nv_mov_b64 v[86:87], 0\nv_smfmac_f32_16x16x64_bf16 v[80:83], v[40:43], v[208:215], v10\nds_read_b128 v[36:39], v2 offset:256\nv_cvt_pk_bf16_f32 v88, v48, v49\nv_smfmac_f32_16x16x64_bf16 v[84:87], v[44:47], v[208:215], v10\nv_cvt_pk_bf16_f32 v89, v50, v51\nv_smfmac_f32_16x16x64_bf16 v[80:83], v[72:75], v[224:231], v10\n" \
"s_add_u32 s60, s55, 16384\nds_read_b128 v[40:43], v2 offset:2048\nv_cvt_pk_bf16_f32 v90, v52, v53\nv_smfmac_f32_16x16x64_bf16 v[84:87], v[76:79], v[224:231], v10\ns_waitcnt lgkmcnt(3)\nds_read_b128 v[44:47], v2 offset:2304\nv_cvt_pk_bf16_f32 v91, v54, v55\nv_smfmac_f32_16x16x64_bf16 v[80:83], v[16:19], v[64:71], v10\n" \
"v_and_or_b32 v88, v88, -2, v13\nv_smfmac_f32_16x16x64_bf16 v[84:87], v[20:23], v[64:71], v10\nv_and_or_b32 v90, v90, -2, v13\nv_smfmac_f32_16x16x64_bf16 v[80:83], v[24:27], v[248:255], v10\nbuffer_store_dwordx4 v[88:91], v7, s[36:39], s60 offen offset:2560 sc1\nv_mov_b64 v[48:49], 0\nv_mov_b64 v[50:51], 0\nv_mov_b64 v[52:53], 0\n" \
"v_mov_b32 v15, s40\nv_smfmac_f32_16x16x64_bf16 v[84:87], v[28:31], v[248:255], v10\ns_add_u32 s60, s55, 20480\ns_cmp_eq_u64 s[64:65], 0\nv_mov_b64 v[54:55], 0\ns_cbranch_scc1 .Lvu_tok%=\nv_cvt_pk_bf16_f32 v92, v80, v81\nv_cvt_pk_bf16_f32 v93, v82, v83\n" \
"s_nop 1\nv_cvt_pk_bf16_f32 v94, v84, v85\nv_cvt_pk_bf16_f32 v95, v86, v87\nv_and_or_b32 v92, v92, -2, v13\nv_and_or_b32 v94, v94, -2, v13\nbuffer_store_dwordx4 v[92:95], v7, s[36:39], s60 offen offset:2560 sc1\ns_mov_b32 s60, -2147483648\ns_cmp_lt_i32 s40, 63\n" \
"s_cbranch_scc1 .Lvu_f0%=\ns_mov_b32 s61, -2147483648\nv_add_u32 v14, s57, v0\ns_waitcnt lgkmcnt(0)\n.Lvu_nov%=:\ns_add_u32 m0, s57, 16384\ns_nop 0\nbuffer_load_dwordx4 v3, s[36:39], s42 offen offset:0 sc1 lds\n" \
"buffer_load_dwordx4 v3, s[36:39], s42 offen offset:1024 sc1 lds\nbuffer_load_dwordx4 v3, s[36:39], s42 offen offset:2048 sc1 lds\nbuffer_load_dwordx4 v3, s[36:39], s42 offen offset:3072 sc1 lds\ns_waitcnt vmcnt(0)\nds_read_b128 v[80:83], v14 offset:16384\nds_read_b128 v[84:87], v14 offset:17408\nds_read_b128 v[88:91], v14 offset:18432\nds_read_b128 v[92:95], v14 offset:19456\n" \
"s_waitcnt lgkmcnt(2)\nv_max3_u32 v76, v80, v82, v84\ns_waitcnt lgkmcnt(1)\nv_max3_u32 v76, v76, v86, v88\ns_waitcnt lgkmcnt(0)\nv_max3_u32 v76, v76, v90, v92\nv_max_u32 v76, v76, v94\nv_cmp_eq_u32 vcc, -1, v76\n" \
"s_cbranch_vccz .Lvu_novfok%=\ns_add_u32 s43, s43, 1\ns_cmp_lt_u32 s43, 262144\ns_cbranch_scc1 .Lvu_nov%=\ns_branch .Lvu_abort%=\n.Lvu_novfok%=:\ns_waitcnt lgkmcnt(0)\ns_barrier\n" \
"s_memrealtime s[68:69]\nds_read_b128 v[56:59], v1 offset:0\nds_read_b128 v[60:63], v1 offset:1024\nds_read_b128 v[80:83], v0 offset:16384\nds_read_b128 v[84:87], v0 offset:17408\nds_read_b128 v[88:91], v0 offset:18432\nds_read_b128 v[92:95], v0 offset:19456\nds_read_b128 v[16:19], v0 offset:20480\n" \
"ds_read_b128 v[20:23], v0 offset:21504\nds_read_b128 v[32:35], v0 offset:22528\nds_read_b128 v[36:39], v0 offset:23552\ns_waitcnt lgkmcnt(0)\ns_branch .Lvu_crit%=\n.Lvu_re%=:\n.Lvu_re2%=:\ns_add_u32 m0, s57, 16384\n" \
"v_max_u32 v76, v80, v82\nv_cmp_eq_u32 vcc, -1, v76\ns_cbranch_vccz .Lvu_sk1893%=\ns_mov_b64 exec, vcc\nbuffer_load_dwordx4 v3, s[36:39], s42 offen offset:0 sc1 lds\ns_mov_b64 exec, -1\n.Lvu_sk1893%=:\nv_max_u32 v76, v84, v86\n" \
"v_cmp_eq_u32 vcc, -1, v76\ns_cbranch_vccz .Lvu_sk1900%=\ns_mov_b64 exec, vcc\nbuffer_load_dwordx4 v3, s[36:39], s42 offen offset:1024 sc1 lds\ns_mov_b64 exec, -1\n.Lvu_sk1900%=:\nv_max_u32 v76, v88, v90\nv_cmp_eq_u32 vcc, -1, v76\n" \
"s_cbranch_vccz .Lvu_sk1907%=\ns_mov_b64 exec, vcc\nbuffer_load_dwordx4 v3, s[36:39], s42 offen offset:2048 sc1 lds\ns_mov_b64 exec, -1\n.Lvu_sk1907%=:\nv_max_u32 v76, v92, v94\nv_cmp_eq_u32 vcc, -1, v76\ns_cbranch_vccz .Lvu_sk1914%=\n" \
"s_mov_b64 exec, vcc\nbuffer_load_dwordx4 v3, s[36:39], s42 offen offset:3072 sc1 lds\ns_mov_b64 exec, -1\n.Lvu_sk1914%=:\n.Lvu_skf%=:\ns_waitcnt vmcnt(0)\nds_read_b128 v[80:83], v14 offset:16384\nds_read_b128 v[84:87], v14 offset:17408\n" \
"ds_read_b128 v[88:91], v14 offset:18432\nds_read_b128 v[92:95], v14 offset:19456\ns_waitcnt lgkmcnt(2)\nv_max3_u32 v76, v80, v82, v84\ns_waitcnt lgkmcnt(1)\nv_max3_u32 v76, v76, v86, v88\ns_waitcnt lgkmcnt(0)\nv_max3_u32 v76, v76, v90, v92\n" \
"v_max_u32 v76, v76, v94\nv_cmp_eq_u32 vcc, -1, v76\ns_cbranch_vccz .Lvu_rer%=\ns_add_u32 s43, s43, 1\ns_cmp_lt_u32 s43, 262144\ns_cbranch_scc1 .Lvu_re2%=\ns_branch .Lvu_abort%=\n.Lvu_rew%=:\n" \
"ds_read_b128 v[80:83], v14 offset:16384\nds_read_b128 v[84:87], v14 offset:17408\nds_read_b128 v[88:91], v14 offset:18432\nds_read_b128 v[92:95], v14 offset:19456\ns_waitcnt lgkmcnt(0)\ns_branch .Lvu_rer%=\n.Lvu_first%=:\ns_nop 7\n" \
"v_cvt_pk_bf16_f32 v76, v24, v25\nv_cvt_pk_bf16_f32 v77, v26, v27\nv_cvt_pk_bf16_f32 v78, v28, v29\nv_cvt_pk_bf16_f32 v79, v30, v31\nds_write2st64_b64 v9, v[76:77], v[78:79] offset0:2 offset1:10\ns_waitcnt vmcnt(0) lgkmcnt(0)\ns_barrier\nds_read_b128 v[56:59], v1 offset:8192\n" \
"ds_read_b128 v[60:63], v1 offset:9216\nds_read_b128 v[64:67], v1 offset:16384\nds_read_b128 v[68:71], v1 offset:17408\nds_read_b128 v[72:75], v11 offset:49152\nds_read_b128 v[76:79], v11 offset:50176\nds_read_b128 v[16:19], v11 offset:51200\nds_read_b128 v[20:23], v11 offset:52224\nds_read_b128 v[24:27], v11 offset:53248\n" \
"ds_read_b128 v[28:31], v11 offset:54272\nds_read_b128 v[32:35], v11 offset:55296\nds_read_b128 v[36:39], v11 offset:56320\ns_waitcnt lgkmcnt(0)\ns_add_u32 m0, s57, 0\ns_add_u32 s58, s61, 0\nbuffer_load_dwordx4 v0, s[36:39], s58 offen offset:0 sc1 lds\nbuffer_load_dwordx4 v0, s[36:39], s58 offen offset:1024 sc1 lds\n" \
"buffer_load_dwordx4 v0, s[36:39], s58 offen offset:2048 sc1 lds\nbuffer_load_dwordx4 v0, s[36:39], s58 offen offset:3072 sc1 lds\ns_mov_b32 s59, -2147483648\nbuffer_store_dword v14, off, s[36:39], s59\nbuffer_store_dword v14, off, s[36:39], s59\nbuffer_store_dword v14, off, s[36:39], s59\ns_branch .Lvu_y%=\n.Lvu_last%=:\n" \
"s_nop 7\nv_cvt_pk_bf16_f32 v92, v40, v41\nv_and_or_b32 v92, v92, -2, v12\ns_add_u32 s63, s59, 4096\nbuffer_store_dwordx4 v[88:91], v6, s[36:39], s63 offen sc1\nv_cvt_pk_bf16_f32 v93, v42, v43\nv_cvt_pk_bf16_f32 v94, v44, v45\nv_cvt_pk_bf16_f32 v95, v46, v47\n" \
"v_and_or_b32 v94, v94, -2, v12\ns_nop 0\nv_permlane16_swap_b32 v93, v95\nv_permlane16_swap_b32 v92, v94\ns_add_u32 s63, s59, 135168\nbuffer_store_dwordx4 v[92:95], v6, s[36:39], s63 offen sc1\ns_branch .Lvu_end%=\n.Lvu_abort%=:\n" \
"s_mov_b64 exec, -1\nv_mov_b32 v16, 0\ns_movk_i32 s44, 6400\nbuffer_store_dword v16, off, s[36:39], s44 sc1\n.Lvu_end%=:\ns_waitcnt vmcnt(0) lgkmcnt(0)\n" \
""
#define RD_ASM \
"v_mbcnt_lo_u32_b32 v255, -1, 0\nv_mbcnt_hi_u32_b32 v255, -1, v255\nv_lshlrev_b32 v255, 2, v255\nv_add_u32 v255, %[tb], v255\nds_read_b32 v196, v255 offset:0\nds_read_b32 v197, v255 offset:256\nds_read_b32 v198, v255 offset:512\nds_read_b32 v199, v255 offset:768\n" \
"ds_read_b32 v200, v255 offset:1024\nds_read_b32 v201, v255 offset:1280\nds_read_b32 v202, v255 offset:1536\nds_read_b32 v203, v255 offset:1792\nds_read_b32 v204, v255 offset:2048\nds_read_b32 v205, v255 offset:2304\nds_read_b32 v206, v255 offset:2560\nds_read_b32 v207, v255 offset:2816\n" \
"s_waitcnt lgkmcnt(0)\nv_accvgpr_write_b32 a32, v206\nv_accvgpr_write_b32 a33, v207\ns_mov_b32 s40, 0\ns_mov_b32 s43, 0\ns_add_u32 s57, %[su], 4096\ns_add_u32 s56, %[sp], 4096\ns_mov_b32 s41, %[sy]\n" \
"s_mov_b32 s59, -2147483648\n.Lrd_yflag%=:\nv_accvgpr_read_b32 v206, a32\nbuffer_load_dword v208, v206, %[rs], 0 offen sc1\ns_waitcnt vmcnt(0)\nv_cmp_gt_i32 vcc, s40, v208\ns_cbranch_vccz .Lrd_yld%=\ns_add_u32 s43, s43, 1\n" \
"s_cmp_lt_u32 s43, 262144\ns_cbranch_scc1 .Lrd_yflag%=\ns_branch .Lrd_abort%=\n.Lrd_yld%=:\ns_add_u32 s60, s41, 0\nbuffer_load_dwordx4 v[0:3], v196, %[rs], s60 offen offset:0 sc1\ns_add_u32 s60, s41, 0\nbuffer_load_dwordx4 v[32:35], v196, %[rs], s60 offen offset:1024 sc1\n" \
"s_add_u32 s60, s41, 0\nbuffer_load_dwordx4 v[64:67], v196, %[rs], s60 offen offset:2048 sc1\ns_add_u32 s60, s41, 131072\nbuffer_load_dwordx4 v[4:7], v196, %[rs], s60 offen offset:0 sc1\ns_add_u32 s60, s41, 131072\nbuffer_load_dwordx4 v[36:39], v196, %[rs], s60 offen offset:1024 sc1\ns_add_u32 s60, s41, 131072\nbuffer_load_dwordx4 v[68:71], v196, %[rs], s60 offen offset:2048 sc1\n" \
"s_add_u32 s60, s41, 262144\nbuffer_load_dwordx4 v[8:11], v196, %[rs], s60 offen offset:0 sc1\ns_add_u32 s60, s41, 262144\nbuffer_load_dwordx4 v[40:43], v196, %[rs], s60 offen offset:1024 sc1\ns_add_u32 s60, s41, 262144\nbuffer_load_dwordx4 v[72:75], v196, %[rs], s60 offen offset:2048 sc1\ns_add_u32 s60, s41, 393216\nbuffer_load_dwordx4 v[12:15], v196, %[rs], s60 offen offset:0 sc1\n" \
"s_add_u32 s60, s41, 393216\nbuffer_load_dwordx4 v[44:47], v196, %[rs], s60 offen offset:1024 sc1\ns_add_u32 s60, s41, 393216\nbuffer_load_dwordx4 v[76:79], v196, %[rs], s60 offen offset:2048 sc1\ns_add_u32 s60, s41, 524288\nbuffer_load_dwordx4 v[16:19], v196, %[rs], s60 offen offset:0 sc1\ns_add_u32 s60, s41, 524288\nbuffer_load_dwordx4 v[48:51], v196, %[rs], s60 offen offset:1024 sc1\n" \
"s_add_u32 s60, s41, 524288\nbuffer_load_dwordx4 v[80:83], v196, %[rs], s60 offen offset:2048 sc1\ns_add_u32 s60, s41, 655360\nbuffer_load_dwordx4 v[20:23], v196, %[rs], s60 offen offset:0 sc1\ns_add_u32 s60, s41, 655360\nbuffer_load_dwordx4 v[52:55], v196, %[rs], s60 offen offset:1024 sc1\ns_add_u32 s60, s41, 655360\nbuffer_load_dwordx4 v[84:87], v196, %[rs], s60 offen offset:2048 sc1\n" \
"s_add_u32 s60, s41, 786432\nbuffer_load_dwordx4 v[24:27], v196, %[rs], s60 offen offset:0 sc1\ns_add_u32 s60, s41, 786432\nbuffer_load_dwordx4 v[56:59], v196, %[rs], s60 offen offset:1024 sc1\ns_add_u32 s60, s41, 786432\nbuffer_load_dwordx4 v[88:91], v196, %[rs], s60 offen offset:2048 sc1\ns_add_u32 s60, s41, 917504\nbuffer_load_dwordx4 v[28:31], v196, %[rs], s60 offen offset:0 sc1\n" \
"s_add_u32 s60, s41, 917504\nbuffer_load_dwordx4 v[60:63], v196, %[rs], s60 offen offset:1024 sc1\ns_add_u32 s60, s41, 917504\nbuffer_load_dwordx4 v[92:95], v196, %[rs], s60 offen offset:2048 sc1\ns_add_u32 s60, s41, 0\nbuffer_load_dwordx4 v[96:99], v196, %[rs], s60 offen offset:3072 sc1\ns_add_u32 s60, s41, 131072\nbuffer_load_dwordx4 v[100:103], v196, %[rs], s60 offen offset:3072 sc1\n" \
"s_add_u32 s60, s41, 262144\nbuffer_load_dwordx4 v[104:107], v196, %[rs], s60 offen offset:3072 sc1\ns_add_u32 s60, s41, 393216\nbuffer_load_dwordx4 v[108:111], v196, %[rs], s60 offen offset:3072 sc1\ns_add_u32 s60, s41, 524288\nbuffer_load_dwordx4 v[112:115], v196, %[rs], s60 offen offset:3072 sc1\ns_add_u32 s60, s41, 655360\nbuffer_load_dwordx4 v[116:119], v196, %[rs], s60 offen offset:3072 sc1\n" \
"s_add_u32 s60, s41, 786432\nbuffer_load_dwordx4 v[120:123], v196, %[rs], s60 offen offset:3072 sc1\ns_add_u32 s60, s41, 917504\nbuffer_load_dwordx4 v[124:127], v196, %[rs], s60 offen offset:3072 sc1\ns_mov_b64 exec, -1\nv_accvgpr_read_b32 v212, a33\nbuffer_load_dwordx2 v[214:215], v212, %[rs], 0 offen sc1\ns_waitcnt vmcnt(0)\n" \
"s_branch .Lrd_ychk%=\n.Lrd_u2%=:\nbuffer_load_dwordx4 v[136:139], v196, %[rs], %[su] offen offset:2048 sc1\nbuffer_load_dwordx4 v[140:143], v196, %[rs], %[su] offen offset:3072 sc1\ns_add_u32 s43, s43, 1\ns_cmp_ge_u32 s43, 262144\ns_cbranch_scc1 .Lrd_abort%=\ns_waitcnt vmcnt(0)\n" \
"v_bitop3_b32 v208, v136, v138, s54 bitop3:0x7e\nv_bitop3_b32 v209, v140, v142, s54 bitop3:0x7e\nv_bitop3_b32 v208, v208, 1, v209 bitop3:0xc8\nv_cmp_ne_u32 vcc, 0, v208\ns_cbranch_vccnz .Lrd_u2%=\n.Lrd_u3%=:\nbuffer_load_dwordx4 v[128:131], v196, %[rs], %[su] offen offset:0 sc1\nbuffer_load_dwordx4 v[132:135], v196, %[rs], %[su] offen offset:1024 sc1\n" \
"buffer_load_dwordx4 v[144:147], v196, %[rs], s57 offen offset:0 sc1\nbuffer_load_dwordx4 v[148:151], v196, %[rs], s57 offen offset:1024 sc1\nbuffer_load_dwordx4 v[152:155], v196, %[rs], s57 offen offset:2048 sc1\nbuffer_load_dwordx4 v[156:159], v196, %[rs], s57 offen offset:3072 sc1\nbuffer_load_dwordx4 v[160:163], v196, %[rs], %[sp] offen offset:0 sc1\nbuffer_load_dwordx4 v[164:167], v196, %[rs], %[sp] offen offset:1024 sc1\nbuffer_load_dwordx4 v[168:171], v196, %[rs], %[sp] offen offset:2048 sc1\nbuffer_load_dwordx4 v[172:175], v196, %[rs], %[sp] offen offset:3072 sc1\n" \
"buffer_load_dwordx4 v[176:179], v196, %[rs], s56 offen offset:0 sc1\n.Lrd_ra%=:\ns_waitcnt vmcnt(10)\nv_bitop3_b32 v208, v128, v130, s54 bitop3:0x7e\nv_bitop3_b32 v209, v136, v138, s54 bitop3:0x7e\nv_bitop3_b32 v208, v208, 1, v209 bitop3:0xc8\nv_cmp_ne_u32 s[86:87], 0, v208\nv_bitop3_b32 v208, v144, v146, s54 bitop3:0x7e\n" \
"v_bitop3_b32 v209, v152, v154, s54 bitop3:0x7e\nv_bitop3_b32 v208, v208, 1, v209 bitop3:0xc8\nv_cmp_ne_u32 s[88:89], 0, v208\nv_bitop3_b32 v208, v132, v134, s54 bitop3:0x7e\nv_bitop3_b32 v209, v140, v142, s54 bitop3:0x7e\nv_bitop3_b32 v208, v208, 1, v209 bitop3:0xc8\nv_cmp_ne_u32 s[68:69], 0, v208\nv_bitop3_b32 v208, v148, v150, s54 bitop3:0x7e\n" \
"v_bitop3_b32 v209, v156, v158, s54 bitop3:0x7e\nv_bitop3_b32 v208, v208, 1, v209 bitop3:0xc8\nv_cmp_ne_u32 s[62:63], 0, v208\ns_or_b64 vcc, s[86:87], s[88:89]\ns_or_b64 vcc, vcc, s[68:69]\ns_or_b64 vcc, vcc, s[62:63]\ns_waitcnt vmcnt(9)\nv_smfmac_f32_16x16x64_bf16 v[180:183], v[160:163], v[0:7], v197\n" \
"v_bitop3_b32 v208, v160, v162, s54 bitop3:0x7e\nv_smfmac_f32_16x16x64_bf16 v[184:187], v[160:163], v[32:39], v197\nv_cmp_gt_i32 s[46:47], 0, v208\nv_smfmac_f32_16x16x64_bf16 v[188:191], v[160:163], v[64:71], v197\nv_smfmac_f32_16x16x64_bf16 v[192:195], v[160:163], v[96:103], v197\ns_or_b64 vcc, vcc, s[46:47]\ns_waitcnt vmcnt(8)\nv_smfmac_f32_16x16x64_bf16 v[180:183], v[164:167], v[8:15], v197\n" \
"v_bitop3_b32 v208, v164, v166, s54 bitop3:0x7e\nv_smfmac_f32_16x16x64_bf16 v[184:187], v[164:167], v[40:47], v197\nv_cmp_gt_i32 s[48:49], 0, v208\nv_smfmac_f32_16x16x64_bf16 v[188:191], v[164:167], v[72:79], v197\nv_smfmac_f32_16x16x64_bf16 v[192:195], v[164:167], v[104:111], v197\ns_or_b64 vcc, vcc, s[48:49]\ns_waitcnt vmcnt(7)\nv_smfmac_f32_16x16x64_bf16 v[180:183], v[168:171], v[16:23], v197\n" \
"v_bitop3_b32 v208, v168, v170, s54 bitop3:0x7e\nv_smfmac_f32_16x16x64_bf16 v[184:187], v[168:171], v[48:55], v197\nv_cmp_gt_i32 s[50:51], 0, v208\nv_smfmac_f32_16x16x64_bf16 v[188:191], v[168:171], v[80:87], v197\nv_smfmac_f32_16x16x64_bf16 v[192:195], v[168:171], v[112:119], v197\ns_or_b64 vcc, vcc, s[50:51]\ns_waitcnt vmcnt(6)\nv_smfmac_f32_16x16x64_bf16 v[180:183], v[172:175], v[24:31], v197\n" \
"v_bitop3_b32 v208, v172, v174, s54 bitop3:0x7e\nv_smfmac_f32_16x16x64_bf16 v[184:187], v[172:175], v[56:63], v197\nv_cmp_gt_i32 s[52:53], 0, v208\nv_smfmac_f32_16x16x64_bf16 v[188:191], v[172:175], v[88:95], v197\nv_smfmac_f32_16x16x64_bf16 v[192:195], v[172:175], v[120:127], v197\ns_or_b64 vcc, vcc, s[52:53]\ns_waitcnt vmcnt(5)\nv_smfmac_f32_16x16x64_bf16 v[180:183], v[176:179], v[128:135], v197\n" \
"v_bitop3_b32 v208, v176, v178, s54 bitop3:0x7e\nv_smfmac_f32_16x16x64_bf16 v[184:187], v[176:179], v[136:143], v197\nv_cmp_gt_i32 s[64:65], 0, v208\nv_smfmac_f32_16x16x64_bf16 v[188:191], v[176:179], v[144:151], v197\ns_or_b64 vcc, vcc, s[64:65]\ns_cbranch_scc1 .Lrd_rb%=\nv_smfmac_f32_16x16x64_bf16 v[192:195], v[176:179], v[152:159], v197\n.Lrd_rd0%=:\n" \
"v_add_f32 v180, v180, v182\nv_add_f32 v181, v181, v183\nds_write2_b32 v198, v180, v181 offset0:0 offset1:8\nv_add_f32 v184, v184, v186\nv_add_f32 v185, v185, v187\nv_add_f32 v188, v188, v190\nv_add_f32 v189, v189, v191\nds_write2_b32 v198, v184, v185 offset0:32 offset1:40\n" \
"v_add_f32 v192, v192, v194\nv_add_f32 v193, v193, v195\nds_write2_b32 v198, v188, v189 offset0:64 offset1:72\nds_write2_b32 v198, v192, v193 offset0:96 offset1:104\ns_add_u32 s60, s58, 0\nbuffer_load_dwordx4 v[232:235], v196, %[rs], s60 offen offset:3072 sc1\ns_add_u32 s60, s58, 16384\nbuffer_load_dwordx4 v[236:239], v196, %[rs], s60 offen offset:2048 sc1\n" \
"s_waitcnt lgkmcnt(0)\ns_barrier\nds_read_b128 v[180:183], v199 offset:0\nds_read_b128 v[184:187], v199 offset:4096\nds_read_b128 v[188:191], v199 offset:8192\nds_read_b128 v[192:195], v199 offset:12288\ns_and_b32 s44, s54, 0x80008000\ns_add_u32 s60, s58, 4096\n" \
"buffer_load_dwordx4 v[240:243], v196, %[rs], s60 offen offset:0 sc1\ns_add_u32 s60, s58, 4096\nbuffer_load_dwordx4 v[244:247], v196, %[rs], s60 offen offset:1024 sc1\ns_waitcnt lgkmcnt(2)\nv_pk_add_f32 v[182:183], v[182:183], v[186:187]\nv_pk_add_f32 v[180:181], v[180:181], v[184:185]\ns_waitcnt lgkmcnt(0)\nv_pk_add_f32 v[186:187], v[188:189], v[192:193]\n" \
"v_pk_add_f32 v[184:185], v[190:191], v[194:195]\nv_pk_add_f32 v[180:181], v[180:181], v[186:187]\nv_pk_add_f32 v[182:183], v[182:183], v[184:185]\nv_cvt_pk_bf16_f32 v180, v180, v181\nv_xor_b32 v180, s44, v180\nv_cvt_pk_bf16_f32 v181, v182, v183\nv_min_u32 v180, -2, v180\nv_xor_b32 v209, s44, v181\n" \
"v_or_b32 v208, v180, v201\nv_mov_b32 v211, v209\nv_or_b32 v210, v180, v201\ns_nop 0\nv_permlane32_swap_b32 v209, v211\nv_permlane32_swap_b32 v208, v210\nbuffer_store_dwordx4 v[208:211], v206, %[rs], 0 offen sc1\ns_add_u32 s60, s58, 16384\n" \
"buffer_load_dwordx4 v[248:251], v196, %[rs], s60 offen offset:3072 sc1\ns_add_u32 s60, s58, 20480\nbuffer_load_dwordx4 v[252:255], v196, %[rs], s60 offen offset:0 sc1\ns_add_u32 s60, s58, 20480\nbuffer_load_dwordx4 v[160:163], v196, %[rs], s60 offen offset:2048 sc1\ns_xor_b32 s41, s41, 4194304\nv_mov_b32 v207, s40\ns_waitcnt vmcnt(12)\n" \
"v_bitop3_b32 v209, v212, v214, s61 bitop3:0x7e\nv_cmp_gt_i32 vcc, 0, v209\ns_cbranch_vccnz .Lrd_tre0%=\n.Lrd_tl0%=:\nv_mfma_f32_16x16x16_bf16 a[0:3], v[0:1], v[212:213], 0\ns_add_u32 s60, s58, 4096\nbuffer_load_dwordx4 v[164:167], v196, %[rs], s60 offen offset:2048 sc1\nv_mfma_f32_16x16x16_bf16 a[8:11], v[32:33], v[212:213], 0\n" \
"v_mfma_f32_16x16x16_bf16 a[16:19], v[64:65], v[212:213], 0\nv_mfma_f32_16x16x16_bf16 a[24:27], v[96:97], v[212:213], 0\ns_waitcnt vmcnt(12)\nv_mfma_f32_16x16x16_bf16 a[0:3], v[4:5], v[214:215], a[0:3]\ns_add_u32 s60, s58, 4096\nbuffer_load_dwordx4 v[168:171], v196, %[rs], s60 offen offset:3072 sc1\nv_bitop3_b32 v209, v216, v218, s61 bitop3:0x7e\nv_mfma_f32_16x16x16_bf16 a[8:11], v[36:37], v[214:215], a[8:11]\n" \
"s_lshl_b32 s42, s40, 17\ns_add_u32 s42, s42, %[at]\nv_mfma_f32_16x16x16_bf16 a[16:19], v[68:69], v[214:215], a[16:19]\nv_cmp_gt_i32 vcc, 0, v209\nv_mfma_f32_16x16x16_bf16 a[24:27], v[100:101], v[214:215], a[24:27]\ns_cbranch_vccnz .Lrd_tre1%=\n.Lrd_tl1%=:\ns_waitcnt vmcnt(12)\n" \
"v_mfma_f32_16x16x32_bf16 a[4:7], v[0:3], v[216:219], 0\ns_add_u32 s60, s58, 20480\nbuffer_load_dwordx4 v[172:175], v196, %[rs], s60 offen offset:1024 sc1\nv_bitop3_b32 v209, v220, v222, s61 bitop3:0x7e\nv_mfma_f32_16x16x32_bf16 a[12:15], v[32:35], v[216:219], 0\ns_lshr_b32 s45, s42, 8\ns_and_b32 s45, s45, 0x2000\nv_mfma_f32_16x16x32_bf16 a[20:23], v[64:67], v[216:219], 0\n" \
"v_cmp_gt_i32 vcc, 0, v209\nv_mfma_f32_16x16x32_bf16 a[28:31], v[96:99], v[216:219], 0\ns_cbranch_vccnz .Lrd_tre2%=\n.Lrd_tl2%=:\ns_waitcnt vmcnt(12)\nv_mfma_f32_16x16x32_bf16 a[4:7], v[4:7], v[220:223], a[4:7]\ns_add_u32 s60, s58, 20480\nbuffer_load_dwordx4 v[176:179], v196, %[rs], s60 offen offset:3072 sc1\n" \
"v_bitop3_b32 v209, v224, v226, s61 bitop3:0x7e\nv_mfma_f32_16x16x32_bf16 a[12:15], v[36:39], v[220:223], a[12:15]\ns_xor_b32 s42, s42, s45\ns_add_u32 s40, s40, 1\nv_mfma_f32_16x16x32_bf16 a[20:23], v[68:71], v[220:223], a[20:23]\nv_cmp_gt_i32 vcc, 0, v209\nv_mfma_f32_16x16x32_bf16 a[28:31], v[100:103], v[220:223], a[28:31]\ns_cbranch_vccnz .Lrd_tre3%=\n" \
".Lrd_tl3%=:\nv_mfma_f32_16x16x16_bf16 a[0:3], v[8:9], v[224:225], a[0:3]\ns_cmp_eq_u32 s40, 1\ns_cbranch_scc1 .Lrd_y1%=\n.Lrd_y1r%=:\ns_add_u32 s60, s41, 0\nbuffer_load_dwordx4 v[0:3], v196, %[rs], s60 offen offset:0 sc1\nv_mfma_f32_16x16x16_bf16 a[8:11], v[40:41], v[224:225], a[8:11]\n" \
"v_mfma_f32_16x16x16_bf16 a[16:19], v[72:73], v[224:225], a[16:19]\nbuffer_load_dwordx4 v[212:215], v204, %[rs], s42 offen sc1\nv_mfma_f32_16x16x16_bf16 a[24:27], v[104:105], v[224:225], a[24:27]\ns_waitcnt vmcnt(14)\nv_mfma_f32_16x16x16_bf16 a[0:3], v[12:13], v[226:227], a[0:3]\ns_add_u32 s60, s41, 0\nbuffer_load_dwordx4 v[32:35], v196, %[rs], s60 offen offset:1024 sc1\nv_bitop3_b32 v209, v228, v230, s61 bitop3:0x7e\n" \
"v_mfma_f32_16x16x16_bf16 a[8:11], v[44:45], v[226:227], a[8:11]\ns_lshl_b32 s62, s55, 31\ns_or_b32 s62, s62, 1.0\ns_mov_b32 s63, s62\nv_mfma_f32_16x16x16_bf16 a[16:19], v[76:77], v[226:227], a[16:19]\nv_cmp_gt_i32 vcc, 0, v209\nv_mfma_f32_16x16x16_bf16 a[24:27], v[108:109], v[226:227], a[24:27]\ns_cbranch_vccnz .Lrd_tre4%=\n" \
".Lrd_tl4%=:\ns_waitcnt vmcnt(14)\nv_mfma_f32_16x16x32_bf16 a[4:7], v[8:11], v[228:231], a[4:7]\ns_add_u32 s60, s41, 0\nbuffer_load_dwordx4 v[64:67], v196, %[rs], s60 offen offset:2048 sc1\nv_bitop3_b32 v209, v232, v234, s61 bitop3:0x7e\nv_mfma_f32_16x16x32_bf16 a[12:15], v[40:43], v[228:231], a[12:15]\nv_mfma_f32_16x16x32_bf16 a[20:23], v[72:75], v[228:231], a[20:23]\n" \
"v_cmp_gt_i32 vcc, 0, v209\nv_mfma_f32_16x16x32_bf16 a[28:31], v[104:107], v[228:231], a[28:31]\ns_cbranch_vccnz .Lrd_tre5%=\n.Lrd_tl5%=:\ns_waitcnt vmcnt(14)\nv_mfma_f32_16x16x32_bf16 a[4:7], v[12:15], v[232:235], a[4:7]\ns_add_u32 s60, s41, 131072\nbuffer_load_dwordx4 v[4:7], v196, %[rs], s60 offen offset:0 sc1\n" \
"v_bitop3_b32 v209, v236, v238, s61 bitop3:0x7e\nv_mfma_f32_16x16x32_bf16 a[12:15], v[44:47], v[232:235], a[12:15]\nv_mfma_f32_16x16x32_bf16 a[20:23], v[76:79], v[232:235], a[20:23]\nv_cmp_gt_i32 vcc, 0, v209\nv_mfma_f32_16x16x32_bf16 a[28:31], v[108:111], v[232:235], a[28:31]\ns_cbranch_vccnz .Lrd_tre6%=\n.Lrd_tl6%=:\nv_mfma_f32_16x16x16_bf16 a[0:3], v[16:17], v[236:237], a[0:3]\n" \
"s_add_u32 s60, s41, 131072\nbuffer_load_dwordx4 v[36:39], v196, %[rs], s60 offen offset:1024 sc1\nv_mfma_f32_16x16x16_bf16 a[8:11], v[48:49], v[236:237], a[8:11]\nv_mfma_f32_16x16x16_bf16 a[16:19], v[80:81], v[236:237], a[16:19]\nv_mfma_f32_16x16x16_bf16 a[24:27], v[112:113], v[236:237], a[24:27]\ns_waitcnt vmcnt(15)\nv_mfma_f32_16x16x16_bf16 a[0:3], v[20:21], v[238:239], a[0:3]\ns_add_u32 s60, s41, 131072\n" \
"buffer_load_dwordx4 v[68:71], v196, %[rs], s60 offen offset:2048 sc1\nv_bitop3_b32 v209, v240, v242, s61 bitop3:0x7e\nv_mfma_f32_16x16x16_bf16 a[8:11], v[52:53], v[238:239], a[8:11]\nv_mfma_f32_16x16x16_bf16 a[16:19], v[84:85], v[238:239], a[16:19]\nv_cmp_gt_i32 vcc, 0, v209\nv_mfma_f32_16x16x16_bf16 a[24:27], v[116:117], v[238:239], a[24:27]\ns_cbranch_vccnz .Lrd_tre7%=\n.Lrd_tl7%=:\n" \
"s_waitcnt vmcnt(15)\nv_mfma_f32_16x16x32_bf16 a[4:7], v[16:19], v[240:243], a[4:7]\ns_add_u32 s60, s41, 262144\nbuffer_load_dwordx4 v[8:11], v196, %[rs], s60 offen offset:0 sc1\nv_bitop3_b32 v209, v244, v246, s61 bitop3:0x7e\nv_mfma_f32_16x16x32_bf16 a[12:15], v[48:51], v[240:243], a[12:15]\nv_mfma_f32_16x16x32_bf16 a[20:23], v[80:83], v[240:243], a[20:23]\nv_cmp_gt_i32 vcc, 0, v209\n" \
"v_mfma_f32_16x16x32_bf16 a[28:31], v[112:115], v[240:243], a[28:31]\ns_cbranch_vccnz .Lrd_tre8%=\n.Lrd_tl8%=:\ns_waitcnt vmcnt(14)\nv_mfma_f32_16x16x32_bf16 a[4:7], v[20:23], v[244:247], a[4:7]\ns_add_u32 s60, s41, 262144\nbuffer_load_dwordx4 v[40:43], v196, %[rs], s60 offen offset:1024 sc1\nv_bitop3_b32 v209, v248, v250, s61 bitop3:0x7e\n" \
"v_mfma_f32_16x16x32_bf16 a[12:15], v[52:55], v[244:247], a[12:15]\nv_mfma_f32_16x16x32_bf16 a[20:23], v[84:87], v[244:247], a[20:23]\nv_cmp_gt_i32 vcc, 0, v209\nv_mfma_f32_16x16x32_bf16 a[28:31], v[116:119], v[244:247], a[28:31]\ns_cbranch_vccnz .Lrd_tre9%=\n.Lrd_tl9%=:\nv_mfma_f32_16x16x16_bf16 a[0:3], v[24:25], v[248:249], a[0:3]\ns_add_u32 s60, s41, 262144\n" \
"buffer_load_dwordx4 v[72:75], v196, %[rs], s60 offen offset:2048 sc1\nv_mfma_f32_16x16x16_bf16 a[8:11], v[56:57], v[248:249], a[8:11]\nv_mfma_f32_16x16x16_bf16 a[16:19], v[88:89], v[248:249], a[16:19]\nv_mfma_f32_16x16x16_bf16 a[24:27], v[120:121], v[248:249], a[24:27]\ns_waitcnt vmcnt(15)\nv_mfma_f32_16x16x16_bf16 a[0:3], v[28:29], v[250:251], a[0:3]\ns_add_u32 s60, s41, 393216\nbuffer_load_dwordx4 v[12:15], v196, %[rs], s60 offen offset:0 sc1\n" \
"v_bitop3_b32 v209, v252, v254, s61 bitop3:0x7e\nv_mfma_f32_16x16x16_bf16 a[8:11], v[60:61], v[250:251], a[8:11]\nv_mfma_f32_16x16x16_bf16 a[16:19], v[92:93], v[250:251], a[16:19]\nv_cmp_gt_i32 vcc, 0, v209\nv_mfma_f32_16x16x16_bf16 a[24:27], v[124:125], v[250:251], a[24:27]\ns_cbranch_vccnz .Lrd_tre10%=\n.Lrd_tl10%=:\ns_waitcnt vmcnt(15)\n" \
"v_mfma_f32_16x16x32_bf16 a[0:3], v[128:131], v[252:255], a[0:3]\ns_add_u32 s60, s41, 393216\nbuffer_load_dwordx4 v[44:47], v196, %[rs], s60 offen offset:1024 sc1\nv_bitop3_b32 v209, v160, v162, s61 bitop3:0x7e\nv_mfma_f32_16x16x32_bf16 a[8:11], v[136:139], v[252:255], a[8:11]\nv_mfma_f32_16x16x32_bf16 a[16:19], v[144:147], v[252:255], a[16:19]\nv_cmp_gt_i32 vcc, 0, v209\nv_mfma_f32_16x16x32_bf16 a[24:27], v[152:155], v[252:255], a[24:27]\n" \
"s_cbranch_vccnz .Lrd_tre11%=\n.Lrd_tl11%=:\ns_waitcnt vmcnt(15)\nv_mfma_f32_16x16x32_bf16 a[0:3], v[132:135], v[160:163], a[0:3]\ns_add_u32 s60, s41, 393216\nbuffer_load_dwordx4 v[76:79], v196, %[rs], s60 offen offset:2048 sc1\nv_bitop3_b32 v209, v164, v166, s61 bitop3:0x7e\nv_mfma_f32_16x16x32_bf16 a[8:11], v[140:143], v[160:163], a[8:11]\n" \
"v_mfma_f32_16x16x32_bf16 a[16:19], v[148:151], v[160:163], a[16:19]\nv_cmp_gt_i32 vcc, 0, v209\nv_mfma_f32_16x16x32_bf16 a[24:27], v[156:159], v[160:163], a[24:27]\ns_cbranch_vccnz .Lrd_tre12%=\n.Lrd_tl12%=:\ns_waitcnt vmcnt(15)\nv_mfma_f32_16x16x32_bf16 a[4:7], v[24:27], v[164:167], a[4:7]\ns_add_u32 s60, s41, 524288\n" \
"buffer_load_dwordx4 v[16:19], v196, %[rs], s60 offen offset:0 sc1\nv_bitop3_b32 v209, v168, v170, s61 bitop3:0x7e\nv_mfma_f32_16x16x32_bf16 a[12:15], v[56:59], v[164:167], a[12:15]\nv_mfma_f32_16x16x32_bf16 a[20:23], v[88:91], v[164:167], a[20:23]\nds_write_b128 v202, a[0:3] offset:0\nv_cmp_gt_i32 vcc, 0, v209\nv_mfma_f32_16x16x32_bf16 a[28:31], v[120:123], v[164:167], a[28:31]\ns_cbranch_vccnz .Lrd_tre13%=\n" \
".Lrd_tl13%=:\ns_waitcnt vmcnt(15)\nv_mfma_f32_16x16x32_bf16 a[4:7], v[28:31], v[168:171], a[4:7]\ns_add_u32 s60, s41, 524288\nbuffer_load_dwordx4 v[48:51], v196, %[rs], s60 offen offset:1024 sc1\nv_bitop3_b32 v209, v172, v174, s61 bitop3:0x7e\nv_mfma_f32_16x16x32_bf16 a[12:15], v[60:63], v[168:171], a[12:15]\nv_mfma_f32_16x16x32_bf16 a[20:23], v[92:95], v[168:171], a[20:23]\n" \
"ds_write_b128 v202, a[8:11] offset:2048\nv_cmp_gt_i32 vcc, 0, v209\nv_mfma_f32_16x16x32_bf16 a[28:31], v[124:127], v[168:171], a[28:31]\ns_cbranch_vccnz .Lrd_tre14%=\n.Lrd_tl14%=:\ns_waitcnt vmcnt(15)\nv_mfma_f32_16x16x32_bf16 a[4:7], v[128:131], v[172:175], a[4:7]\ns_add_u32 s60, s41, 524288\n" \
"buffer_load_dwordx4 v[80:83], v196, %[rs], s60 offen offset:2048 sc1\nv_bitop3_b32 v209, v176, v178, s61 bitop3:0x7e\nv_mfma_f32_16x16x32_bf16 a[12:15], v[136:139], v[172:175], a[12:15]\nv_mfma_f32_16x16x32_bf16 a[20:23], v[144:147], v[172:175], a[20:23]\nds_write_b128 v202, a[16:19] offset:4096\nv_cmp_gt_i32 vcc, 0, v209\nv_mfma_f32_16x16x32_bf16 a[28:31], v[152:155], v[172:175], a[28:31]\ns_cbranch_vccnz .Lrd_tre15%=\n" \
".Lrd_tl15%=:\nv_mfma_f32_16x16x32_bf16 a[4:7], v[132:135], v[176:179], a[4:7]\ns_add_u32 s60, s41, 655360\ns_waitcnt vmcnt(14)\nbuffer_load_dwordx4 v[20:23], v196, %[rs], s60 offen offset:0 sc1\nv_lshlrev_b32 v216, 16, v212\nv_and_b32 v217, 0xffff0000, v212\nv_mfma_f32_16x16x32_bf16 a[12:15], v[140:143], v[176:179], a[12:15]\n" \
"v_lshlrev_b32 v218, 16, v213\nv_and_b32 v219, 0xffff0000, v213\nv_mfma_f32_16x16x32_bf16 a[20:23], v[148:151], v[176:179], a[20:23]\nds_write_b128 v202, a[24:27] offset:6144\nv_lshlrev_b32 v220, 16, v214\nv_and_b32 v221, 0xffff0000, v214\nv_mfma_f32_16x16x32_bf16 a[28:31], v[156:159], v[176:179], a[28:31]\ns_add_u32 s60, s41, 655360\n" \
"buffer_load_dwordx4 v[52:55], v196, %[rs], s60 offen offset:1024 sc1\ns_add_u32 s60, s41, 655360\nds_write_b128 v202, a[4:7] offset:1024\nbuffer_load_dwordx4 v[84:87], v196, %[rs], s60 offen offset:2048 sc1\nds_write_b128 v202, a[12:15] offset:3072\nv_lshlrev_b32 v222, 16, v215\nv_and_b32 v223, 0xffff0000, v215\ns_add_u32 s60, s41, 786432\n" \
"buffer_load_dwordx4 v[24:27], v196, %[rs], s60 offen offset:0 sc1\nds_write_b128 v202, a[20:23] offset:5120\ns_add_u32 s60, s41, 786432\nbuffer_load_dwordx4 v[56:59], v196, %[rs], s60 offen offset:1024 sc1\nds_write_b128 v202, a[28:31] offset:7168\ns_waitcnt lgkmcnt(0)\ns_barrier\nds_read_b128 v[224:227], v203 offset:0\n" \
"ds_read_b128 v[228:231], v203 offset:256\nds_read_b128 v[232:235], v203 offset:8192\nds_read_b128 v[236:239], v203 offset:8448\nds_read_b128 v[240:243], v203 offset:16384\nds_read_b128 v[244:247], v203 offset:16640\nds_read_b128 v[248:251], v203 offset:24576\nds_read_b128 v[252:255], v203 offset:24832\ns_add_u32 s60, s41, 786432\n" \
"buffer_load_dwordx4 v[88:91], v196, %[rs], s60 offen offset:2048 sc1\ns_waitcnt lgkmcnt(4)\nv_pk_add_f32 v[224:225], v[224:225], v[232:233]\nv_pk_add_f32 v[226:227], v[226:227], v[234:235]\nv_pk_add_f32 v[228:229], v[228:229], v[236:237]\nv_pk_add_f32 v[230:231], v[230:231], v[238:239]\ns_add_u32 s60, s41, 917504\nbuffer_load_dwordx4 v[28:31], v196, %[rs], s60 offen offset:0 sc1\n" \
"s_waitcnt lgkmcnt(2)\nv_pk_add_f32 v[224:225], v[224:225], v[240:241]\nv_pk_add_f32 v[226:227], v[226:227], v[242:243]\nv_pk_add_f32 v[228:229], v[228:229], v[244:245]\nv_pk_add_f32 v[230:231], v[230:231], v[246:247]\ns_add_u32 s60, s41, 917504\nbuffer_load_dwordx4 v[60:63], v196, %[rs], s60 offen offset:1024 sc1\ns_waitcnt lgkmcnt(0)\n" \
"v_pk_add_f32 v[224:225], v[224:225], v[248:249]\nv_pk_add_f32 v[226:227], v[226:227], v[250:251]\nv_pk_add_f32 v[228:229], v[228:229], v[252:253]\nv_pk_add_f32 v[230:231], v[230:231], v[254:255]\nv_pk_fma_f32 v[224:225], v[224:225], s[62:63], v[216:217]\nv_pk_fma_f32 v[226:227], v[226:227], s[62:63], v[218:219]\nv_pk_fma_f32 v[228:229], v[228:229], s[62:63], v[220:221]\nv_pk_fma_f32 v[230:231], v[230:231], s[62:63], v[222:223]\n" \
"v_cvt_pk_bf16_f32 v208, v224, v225\nv_cvt_pk_bf16_f32 v209, v226, v227\nv_cvt_pk_bf16_f32 v210, v228, v229\nv_cvt_pk_bf16_f32 v211, v230, v231\nbuffer_store_dwordx4 v[208:211], v204, %[rs], s42 offen sc1\nv_pk_add_f32 v[232:233], v[224:225], v[226:227]\nv_pk_add_f32 v[234:235], v[228:229], v[230:231]\nv_pk_add_f32 v[232:233], v[232:233], v[234:235]\n" \
"s_add_u32 s60, s41, 917504\nbuffer_load_dwordx4 v[92:95], v196, %[rs], s60 offen offset:2048 sc1\ns_add_u32 s60, s41, 0\nbuffer_load_dwordx4 v[96:99], v196, %[rs], s60 offen offset:3072 sc1\nv_add_f32 v232, v232, v233\ns_nop 1\nv_add_f32_dpp v233, v232, v232 row_ror:8 row_mask:0xf bank_mask:0xf\nv_add_f32_dpp v234, v232, v232 row_ror:8 row_mask:0xf bank_mask:0xf\n" \
"s_nop 1\nv_permlane16_swap_b32 v233, v234\nv_add_f32 v232, v233, v234\nv_add_f32 v233, v233, v234\ns_nop 1\nv_permlane32_swap_b32 v232, v233\nv_add_f32 v232, v232, v233\nv_mul_f32 v232, 0x3c800000, v232\n" \
"v_pk_add_f32 v[224:225], v[224:225], v[232:233] op_sel_hi:[1,0] neg_lo:[0,1] neg_hi:[0,1]\nv_pk_add_f32 v[226:227], v[226:227], v[232:233] op_sel_hi:[1,0] neg_lo:[0,1] neg_hi:[0,1]\nv_pk_add_f32 v[228:229], v[228:229], v[232:233] op_sel_hi:[1,0] neg_lo:[0,1] neg_hi:[0,1]\nv_pk_add_f32 v[230:231], v[230:231], v[232:233] op_sel_hi:[1,0] neg_lo:[0,1] neg_hi:[0,1]\ns_add_u32 s60, s41, 131072\nbuffer_load_dwordx4 v[100:103], v196, %[rs], s60 offen offset:3072 sc1\nv_accvgpr_read_b32 v212, a33\nbuffer_load_dwordx2 v[214:215], v212, %[rs], 0 offen sc1\n" \
"v_pk_mul_f32 v[236:237], v[224:225], v[224:225]\nv_pk_mul_f32 v[238:239], v[226:227], v[226:227]\nv_pk_fma_f32 v[236:237], v[228:229], v[228:229], v[236:237]\nv_pk_fma_f32 v[238:239], v[230:231], v[230:231], v[238:239]\nv_pk_add_f32 v[236:237], v[236:237], v[238:239]\nv_add_f32 v236, v236, v237\ns_nop 1\nv_add_f32_dpp v237, v236, v236 row_ror:8 row_mask:0xf bank_mask:0xf\n" \
"v_add_f32_dpp v238, v236, v236 row_ror:8 row_mask:0xf bank_mask:0xf\ns_nop 1\nv_permlane16_swap_b32 v237, v238\nv_add_f32 v236, v237, v238\nv_add_f32 v237, v237, v238\ns_nop 1\nv_permlane32_swap_b32 v236, v237\nv_add_f32 v236, v236, v237\n" \
"v_mov_b32 v233, v236\nbuffer_store_dwordx2 v[232:233], v205, %[rs], s42 offen sc1\ns_mov_b32 s59, %[fa]\ns_add_u32 s60, s41, 262144\nbuffer_load_dwordx4 v[104:107], v196, %[rs], s60 offen offset:3072 sc1\ns_add_u32 s60, s41, 393216\nbuffer_load_dwordx4 v[108:111], v196, %[rs], s60 offen offset:3072 sc1\ns_add_u32 s60, s41, 524288\n" \
"buffer_load_dwordx4 v[112:115], v196, %[rs], s60 offen offset:3072 sc1\ns_add_u32 s60, s41, 655360\nbuffer_load_dwordx4 v[116:119], v196, %[rs], s60 offen offset:3072 sc1\ns_add_u32 s60, s41, 786432\nbuffer_load_dwordx4 v[120:123], v196, %[rs], s60 offen offset:3072 sc1\ns_add_u32 s60, s41, 917504\nbuffer_load_dwordx4 v[124:127], v196, %[rs], s60 offen offset:3072 sc1\ns_cmp_ge_u32 s40, 64\n" \
"s_cbranch_scc1 .Lrd_fin%=\n.Lrd_ychk%=:\ns_and_b32 s44, s40, 1\ns_bfe_u32 s55, s40, 0x10001\ns_mul_i32 s54, s44, 0x80008001\ns_mul_i32 s61, s55, 0x80008000\ns_lshl_b32 s58, s44, 17\ns_add_u32 s58, s58, %[st]\n" \
"s_add_u32 s44, s40, 1\ns_and_b32 s44, s44, 3\ns_lshl_b32 s44, s44, 15\nv_xor_b32 v206, s44, v200\nv_mov_b64 v[180:181], 0\nv_mov_b64 v[182:183], 0\nv_mov_b64 v[184:185], 0\nv_mov_b64 v[186:187], 0\n" \
"v_mov_b64 v[188:189], 0\nv_mov_b64 v[190:191], 0\nv_mov_b64 v[192:193], 0\nv_mov_b64 v[194:195], 0\ns_waitcnt vmcnt(7)\n.Lrd_sg0%=:\ns_memrealtime s[68:69]\nv_cmp_ne_u32 vcc, s40, v214\n" \
"s_cbranch_vccnz .Lrd_sg1%=\nv_max_u32_dpp v215, v215, v215 quad_perm:[1,0,3,2] row_mask:0xf bank_mask:0xf\ns_nop 1\nv_max_u32_dpp v215, v215, v215 quad_perm:[2,3,0,1] row_mask:0xf bank_mask:0xf\ns_nop 1\nv_max_u32_dpp v215, v215, v215 row_half_mirror row_mask:0xf bank_mask:0xf\ns_nop 0\nv_readlane_b32 s86, v215, 0\n" \
"v_readlane_b32 s87, v215, 8\ns_add_u32 s86, s86, %[du]\n.Lrd_tw608%=:\ns_waitcnt lgkmcnt(0)\ns_sub_u32 s44, s86, s68\ns_add_u32 s44, s44, 11\ns_cmp_lt_u32 s44, 250\ns_cbranch_scc1 .Lrd_tw608x%=\n" \
"buffer_load_dwordx4 v[128:131], v196, %[rs], %[su] offen offset:0 sc1\nbuffer_load_dwordx4 v[132:135], v196, %[rs], %[su] offen offset:1024 sc1\ns_memrealtime s[68:69]\nbuffer_load_dwordx4 v[136:139], v196, %[rs], %[su] offen offset:2048 sc1\nbuffer_load_dwordx4 v[140:143], v196, %[rs], %[su] offen offset:3072 sc1\n.Lrd_tw619%=:\ns_waitcnt lgkmcnt(0)\ns_sub_u32 s44, s86, s68\n" \
"s_add_u32 s44, s44, 21\ns_cmp_lt_u32 s44, 250\ns_cbranch_scc1 .Lrd_tw619x%=\nbuffer_load_dwordx4 v[144:147], v196, %[rs], s57 offen offset:0 sc1\nbuffer_load_dwordx4 v[148:151], v196, %[rs], s57 offen offset:1024 sc1\ns_memrealtime s[68:69]\nbuffer_load_dwordx4 v[152:155], v196, %[rs], s57 offen offset:2048 sc1\nbuffer_load_dwordx4 v[156:159], v196, %[rs], s57 offen offset:3072 sc1\n" \
"s_waitcnt vmcnt(14)\nbuffer_store_dword v207, off, %[rs], s59 sc1\ns_mov_b32 s59, -2147483648\n.Lrd_tw633%=:\ns_waitcnt lgkmcnt(0)\ns_sub_u32 s44, s87, s68\ns_add_u32 s44, s44, 24\ns_cmp_lt_u32 s44, 250\n" \
"s_cbranch_scc1 .Lrd_tw633x%=\nbuffer_load_dwordx4 v[160:163], v196, %[rs], %[sp] offen offset:0 sc1\nbuffer_load_dwordx4 v[164:167], v196, %[rs], %[sp] offen offset:1024 sc1\nbuffer_load_dwordx4 v[168:171], v196, %[rs], %[sp] offen offset:2048 sc1\nbuffer_load_dwordx4 v[172:175], v196, %[rs], %[sp] offen offset:3072 sc1\nbuffer_load_dwordx4 v[176:179], v196, %[rs], s56 offen offset:0 sc1\ns_add_u32 s60, s58, 16384\nbuffer_load_dwordx4 v[212:215], v196, %[rs], s60 offen offset:0 sc1\n" \
"v_bitop3_b32 v209, v0, v2, s55 bitop3:0x7e\nv_bitop3_b32 v210, v32, v34, s55 bitop3:0x7e\nv_or3_b32 v208, v209, v210, 0\nv_bitop3_b32 v209, v64, v66, s55 bitop3:0x7e\nv_bitop3_b32 v210, v4, v6, s55 bitop3:0x7e\nv_or3_b32 v208, v209, v210, v208\ns_add_u32 s60, s58, 0\nbuffer_load_dwordx4 v[216:219], v196, %[rs], s60 offen offset:0 sc1\n" \
"v_bitop3_b32 v209, v36, v38, s55 bitop3:0x7e\nv_bitop3_b32 v210, v68, v70, s55 bitop3:0x7e\nv_or3_b32 v208, v209, v210, v208\nv_bitop3_b32 v209, v8, v10, s55 bitop3:0x7e\nv_bitop3_b32 v210, v40, v42, s55 bitop3:0x7e\nv_or3_b32 v208, v209, v210, v208\ns_add_u32 s60, s58, 0\nbuffer_load_dwordx4 v[220:223], v196, %[rs], s60 offen offset:1024 sc1\n" \
"v_bitop3_b32 v209, v72, v74, s55 bitop3:0x7e\nv_bitop3_b32 v210, v12, v14, s55 bitop3:0x7e\nv_or3_b32 v208, v209, v210, v208\nv_bitop3_b32 v209, v44, v46, s55 bitop3:0x7e\nv_bitop3_b32 v210, v76, v78, s55 bitop3:0x7e\nv_or3_b32 v208, v209, v210, v208\ns_add_u32 s60, s58, 16384\nbuffer_load_dwordx4 v[224:227], v196, %[rs], s60 offen offset:1024 sc1\n" \
"v_bitop3_b32 v209, v16, v18, s55 bitop3:0x7e\nv_bitop3_b32 v210, v48, v50, s55 bitop3:0x7e\nv_or3_b32 v208, v209, v210, v208\nv_bitop3_b32 v209, v80, v82, s55 bitop3:0x7e\nv_bitop3_b32 v210, v20, v22, s55 bitop3:0x7e\nv_or3_b32 v208, v209, v210, v208\ns_add_u32 s60, s58, 0\nbuffer_load_dwordx4 v[228:231], v196, %[rs], s60 offen offset:2048 sc1\n" \
"v_bitop3_b32 v209, v52, v54, s55 bitop3:0x7e\nv_bitop3_b32 v210, v84, v86, s55 bitop3:0x7e\nv_or3_b32 v208, v209, v210, v208\nv_bitop3_b32 v209, v24, v26, s55 bitop3:0x7e\nv_bitop3_b32 v210, v56, v58, s55 bitop3:0x7e\nv_or3_b32 v208, v209, v210, v208\nv_bitop3_b32 v209, v88, v90, s55 bitop3:0x7e\nv_bitop3_b32 v210, v28, v30, s55 bitop3:0x7e\n" \
"v_or3_b32 v208, v209, v210, v208\nv_bitop3_b32 v209, v60, v62, s55 bitop3:0x7e\nv_bitop3_b32 v210, v92, v94, s55 bitop3:0x7e\nv_or3_b32 v208, v209, v210, v208\ns_waitcnt vmcnt(23)\nv_bitop3_b32 v209, v96, v98, s55 bitop3:0x7e\nv_bitop3_b32 v210, v100, v102, s55 bitop3:0x7e\nv_or3_b32 v208, v209, v210, v208\n" \
"v_bitop3_b32 v209, v104, v106, s55 bitop3:0x7e\nv_bitop3_b32 v210, v108, v110, s55 bitop3:0x7e\nv_or3_b32 v208, v209, v210, v208\ns_waitcnt vmcnt(19)\nv_bitop3_b32 v209, v112, v114, s55 bitop3:0x7e\nv_bitop3_b32 v210, v116, v118, s55 bitop3:0x7e\nv_or3_b32 v208, v209, v210, v208\nv_bitop3_b32 v209, v120, v122, s55 bitop3:0x7e\n" \
"v_bitop3_b32 v210, v124, v126, s55 bitop3:0x7e\nv_or3_b32 v208, v209, v210, v208\nv_and_b32 v208, 1, v208\nv_cmp_ne_u32 vcc, 0, v208\ns_cbranch_vccnz .Lrd_yre%=\ns_branch .Lrd_ra%=\n.Lrd_sg1%=:\nbuffer_load_dwordx2 v[214:215], v212, %[rs], 0 offen sc1\n" \
"s_waitcnt vmcnt(0)\nbuffer_store_dword v207, off, %[rs], s59 sc1\ns_mov_b32 s59, -2147483648\ns_add_u32 s43, s43, 1\ns_cmp_lt_u32 s43, 262144\ns_cbranch_scc1 .Lrd_sg0%=\ns_branch .Lrd_abort%=\n.Lrd_tw608x%=:\n" \
"s_memrealtime s[68:69]\ns_branch .Lrd_tw608%=\n.Lrd_tw619x%=:\ns_memrealtime s[68:69]\ns_branch .Lrd_tw619%=\n.Lrd_tw633x%=:\ns_memrealtime s[68:69]\ns_branch .Lrd_tw633%=\n" \
".Lrd_fin%=:\ns_waitcnt vmcnt(0)\nbuffer_store_dword v207, off, %[rs], s59 sc1\ns_mov_b32 s59, -2147483648\ns_branch .Lrd_end%=\n.Lrd_yre%=:\ns_mov_b64 exec, vcc\ns_waitcnt vmcnt(0)\n" \
"s_add_u32 s43, s43, 1\ns_cmp_lt_u32 s43, 262144\ns_cbranch_scc1 .Lrd_yld%=\ns_branch .Lrd_abort%=\n.Lrd_rb%=:\ns_mov_b64 exec, s[46:47]\ns_cbranch_execz .Lrd_rs0%=\nbuffer_load_dwordx4 v[160:163], v196, %[rs], %[sp] offen offset:0 sc1\n" \
".Lrd_rs0%=:\ns_mov_b64 exec, s[48:49]\ns_cbranch_execz .Lrd_rs1%=\nbuffer_load_dwordx4 v[164:167], v196, %[rs], %[sp] offen offset:1024 sc1\n.Lrd_rs1%=:\ns_mov_b64 exec, s[50:51]\ns_cbranch_execz .Lrd_rs2%=\nbuffer_load_dwordx4 v[168:171], v196, %[rs], %[sp] offen offset:2048 sc1\n" \
".Lrd_rs2%=:\ns_mov_b64 exec, s[52:53]\ns_cbranch_execz .Lrd_rs3%=\nbuffer_load_dwordx4 v[172:175], v196, %[rs], %[sp] offen offset:3072 sc1\n.Lrd_rs3%=:\ns_mov_b64 exec, s[64:65]\ns_cbranch_execz .Lrd_rs4%=\nbuffer_load_dwordx4 v[176:179], v196, %[rs], s56 offen offset:0 sc1\n" \
".Lrd_rs4%=:\ns_mov_b64 exec, s[86:87]\ns_cbranch_execz .Lrd_rs5%=\nbuffer_load_dwordx4 v[128:131], v196, %[rs], %[su] offen offset:0 sc1\nbuffer_load_dwordx4 v[136:139], v196, %[rs], %[su] offen offset:2048 sc1\n.Lrd_rs5%=:\ns_mov_b64 exec, s[88:89]\ns_cbranch_execz .Lrd_rs6%=\n" \
"buffer_load_dwordx4 v[144:147], v196, %[rs], s57 offen offset:0 sc1\nbuffer_load_dwordx4 v[152:155], v196, %[rs], s57 offen offset:2048 sc1\n.Lrd_rs6%=:\ns_mov_b64 exec, s[68:69]\ns_cbranch_execz .Lrd_rs7%=\nbuffer_load_dwordx4 v[132:135], v196, %[rs], %[su] offen offset:1024 sc1\nbuffer_load_dwordx4 v[140:143], v196, %[rs], %[su] offen offset:3072 sc1\n.Lrd_rs7%=:\n" \
"s_mov_b64 exec, s[62:63]\ns_cbranch_execz .Lrd_rs8%=\nbuffer_load_dwordx4 v[148:151], v196, %[rs], s57 offen offset:1024 sc1\nbuffer_load_dwordx4 v[156:159], v196, %[rs], s57 offen offset:3072 sc1\n.Lrd_rs8%=:\ns_mov_b64 exec, -1\nv_mov_b64 v[180:181], 0\nv_mov_b64 v[182:183], 0\n" \
"v_mov_b64 v[184:185], 0\nv_mov_b64 v[186:187], 0\nv_mov_b64 v[188:189], 0\nv_mov_b64 v[190:191], 0\nv_mov_b64 v[192:193], 0\nv_mov_b64 v[194:195], 0\ns_waitcnt vmcnt(0)\ns_add_u32 s43, s43, 1\n" \
"s_cmp_lt_u32 s43, 262144\ns_cbranch_scc1 .Lrd_ra%=\ns_branch .Lrd_abort%=\n.Lrd_y1%=:\nv_accvgpr_read_b32 v206, a32\nbuffer_load_dword v208, v206, %[rs], 0 offen sc1\ns_waitcnt vmcnt(0)\nv_cmp_gt_i32 vcc, 1, v208\n" \
"s_cbranch_vccz .Lrd_y1r%=\ns_add_u32 s43, s43, 1\ns_cmp_lt_u32 s43, 262144\ns_cbranch_scc1 .Lrd_y1%=\ns_branch .Lrd_abort%=\n.Lrd_tre0%=:\ns_add_u32 s60, s58, 16384\nbuffer_load_dwordx4 v[212:215], v196, %[rs], s60 offen offset:0 sc1\n" \
"s_waitcnt vmcnt(0)\nv_bitop3_b32 v209, v212, v214, s61 bitop3:0x7e\nv_cmp_gt_i32 vcc, 0, v209\ns_cbranch_vccz .Lrd_tl0%=\ns_add_u32 s43, s43, 1\ns_cmp_lt_u32 s43, 262144\ns_cbranch_scc1 .Lrd_tre0%=\ns_branch .Lrd_abort%=\n" \
".Lrd_tre1%=:\ns_add_u32 s60, s58, 0\nbuffer_load_dwordx4 v[216:219], v196, %[rs], s60 offen offset:0 sc1\ns_waitcnt vmcnt(0)\nv_bitop3_b32 v209, v216, v218, s61 bitop3:0x7e\nv_cmp_gt_i32 vcc, 0, v209\ns_cbranch_vccz .Lrd_tl1%=\ns_add_u32 s43, s43, 1\n" \
"s_cmp_lt_u32 s43, 262144\ns_cbranch_scc1 .Lrd_tre1%=\ns_branch .Lrd_abort%=\n.Lrd_tre2%=:\ns_add_u32 s60, s58, 0\nbuffer_load_dwordx4 v[220:223], v196, %[rs], s60 offen offset:1024 sc1\ns_waitcnt vmcnt(0)\nv_bitop3_b32 v209, v220, v222, s61 bitop3:0x7e\n" \
"v_cmp_gt_i32 vcc, 0, v209\ns_cbranch_vccz .Lrd_tl2%=\ns_add_u32 s43, s43, 1\ns_cmp_lt_u32 s43, 262144\ns_cbranch_scc1 .Lrd_tre2%=\ns_branch .Lrd_abort%=\n.Lrd_tre3%=:\ns_add_u32 s60, s58, 16384\n" \
"buffer_load_dwordx4 v[224:227], v196, %[rs], s60 offen offset:1024 sc1\ns_waitcnt vmcnt(0)\nv_bitop3_b32 v209, v224, v226, s61 bitop3:0x7e\nv_cmp_gt_i32 vcc, 0, v209\ns_cbranch_vccz .Lrd_tl3%=\ns_add_u32 s43, s43, 1\ns_cmp_lt_u32 s43, 262144\ns_cbranch_scc1 .Lrd_tre3%=\n" \
"s_branch .Lrd_abort%=\n.Lrd_tre4%=:\ns_add_u32 s60, s58, 0\nbuffer_load_dwordx4 v[228:231], v196, %[rs], s60 offen offset:2048 sc1\ns_waitcnt vmcnt(0)\nv_bitop3_b32 v209, v228, v230, s61 bitop3:0x7e\nv_cmp_gt_i32 vcc, 0, v209\ns_cbranch_vccz .Lrd_tl4%=\n" \
"s_add_u32 s43, s43, 1\ns_cmp_lt_u32 s43, 262144\ns_cbranch_scc1 .Lrd_tre4%=\ns_branch .Lrd_abort%=\n.Lrd_tre5%=:\ns_add_u32 s60, s58, 0\nbuffer_load_dwordx4 v[232:235], v196, %[rs], s60 offen offset:3072 sc1\ns_waitcnt vmcnt(0)\n" \
"v_bitop3_b32 v209, v232, v234, s61 bitop3:0x7e\nv_cmp_gt_i32 vcc, 0, v209\ns_cbranch_vccz .Lrd_tl5%=\ns_add_u32 s43, s43, 1\ns_cmp_lt_u32 s43, 262144\ns_cbranch_scc1 .Lrd_tre5%=\ns_branch .Lrd_abort%=\n.Lrd_tre6%=:\n" \
"s_add_u32 s60, s58, 16384\nbuffer_load_dwordx4 v[236:239], v196, %[rs], s60 offen offset:2048 sc1\ns_waitcnt vmcnt(0)\nv_bitop3_b32 v209, v236, v238, s61 bitop3:0x7e\nv_cmp_gt_i32 vcc, 0, v209\ns_cbranch_vccz .Lrd_tl6%=\ns_add_u32 s43, s43, 1\ns_cmp_lt_u32 s43, 262144\n" \
"s_cbranch_scc1 .Lrd_tre6%=\ns_branch .Lrd_abort%=\n.Lrd_tre7%=:\ns_add_u32 s60, s58, 4096\nbuffer_load_dwordx4 v[240:243], v196, %[rs], s60 offen offset:0 sc1\ns_waitcnt vmcnt(0)\nv_bitop3_b32 v209, v240, v242, s61 bitop3:0x7e\nv_cmp_gt_i32 vcc, 0, v209\n" \
"s_cbranch_vccz .Lrd_tl7%=\ns_add_u32 s43, s43, 1\ns_cmp_lt_u32 s43, 262144\ns_cbranch_scc1 .Lrd_tre7%=\ns_branch .Lrd_abort%=\n.Lrd_tre8%=:\ns_add_u32 s60, s58, 4096\nbuffer_load_dwordx4 v[244:247], v196, %[rs], s60 offen offset:1024 sc1\n" \
"s_waitcnt vmcnt(0)\nv_bitop3_b32 v209, v244, v246, s61 bitop3:0x7e\nv_cmp_gt_i32 vcc, 0, v209\ns_cbranch_vccz .Lrd_tl8%=\ns_add_u32 s43, s43, 1\ns_cmp_lt_u32 s43, 262144\ns_cbranch_scc1 .Lrd_tre8%=\ns_branch .Lrd_abort%=\n" \
".Lrd_tre9%=:\ns_add_u32 s60, s58, 16384\nbuffer_load_dwordx4 v[248:251], v196, %[rs], s60 offen offset:3072 sc1\ns_waitcnt vmcnt(0)\nv_bitop3_b32 v209, v248, v250, s61 bitop3:0x7e\nv_cmp_gt_i32 vcc, 0, v209\ns_cbranch_vccz .Lrd_tl9%=\ns_add_u32 s43, s43, 1\n" \
"s_cmp_lt_u32 s43, 262144\ns_cbranch_scc1 .Lrd_tre9%=\ns_branch .Lrd_abort%=\n.Lrd_tre10%=:\ns_add_u32 s60, s58, 20480\nbuffer_load_dwordx4 v[252:255], v196, %[rs], s60 offen offset:0 sc1\ns_waitcnt vmcnt(0)\nv_bitop3_b32 v209, v252, v254, s61 bitop3:0x7e\n" \
"v_cmp_gt_i32 vcc, 0, v209\ns_cbranch_vccz .Lrd_tl10%=\ns_add_u32 s43, s43, 1\ns_cmp_lt_u32 s43, 262144\ns_cbranch_scc1 .Lrd_tre10%=\ns_branch .Lrd_abort%=\n.Lrd_tre11%=:\ns_add_u32 s60, s58, 20480\n" \
"buffer_load_dwordx4 v[160:163], v196, %[rs], s60 offen offset:2048 sc1\ns_waitcnt vmcnt(0)\nv_bitop3_b32 v209, v160, v162, s61 bitop3:0x7e\nv_cmp_gt_i32 vcc, 0, v209\ns_cbranch_vccz .Lrd_tl11%=\ns_add_u32 s43, s43, 1\ns_cmp_lt_u32 s43, 262144\ns_cbranch_scc1 .Lrd_tre11%=\n" \
"s_branch .Lrd_abort%=\n.Lrd_tre12%=:\ns_add_u32 s60, s58, 4096\nbuffer_load_dwordx4 v[164:167], v196, %[rs], s60 offen offset:2048 sc1\ns_waitcnt vmcnt(0)\nv_bitop3_b32 v209, v164, v166, s61 bitop3:0x7e\nv_cmp_gt_i32 vcc, 0, v209\ns_cbranch_vccz .Lrd_tl12%=\n" \
"s_add_u32 s43, s43, 1\ns_cmp_lt_u32 s43, 262144\ns_cbranch_scc1 .Lrd_tre12%=\ns_branch .Lrd_abort%=\n.Lrd_tre13%=:\ns_add_u32 s60, s58, 4096\nbuffer_load_dwordx4 v[168:171], v196, %[rs], s60 offen offset:3072 sc1\ns_waitcnt vmcnt(0)\n" \
"v_bitop3_b32 v209, v168, v170, s61 bitop3:0x7e\nv_cmp_gt_i32 vcc, 0, v209\ns_cbranch_vccz .Lrd_tl13%=\ns_add_u32 s43, s43, 1\ns_cmp_lt_u32 s43, 262144\ns_cbranch_scc1 .Lrd_tre13%=\ns_branch .Lrd_abort%=\n.Lrd_tre14%=:\n" \
"s_add_u32 s60, s58, 20480\nbuffer_load_dwordx4 v[172:175], v196, %[rs], s60 offen offset:1024 sc1\ns_waitcnt vmcnt(0)\nv_bitop3_b32 v209, v172, v174, s61 bitop3:0x7e\nv_cmp_gt_i32 vcc, 0, v209\ns_cbranch_vccz .Lrd_tl14%=\ns_add_u32 s43, s43, 1\ns_cmp_lt_u32 s43, 262144\n" \
"s_cbranch_scc1 .Lrd_tre14%=\ns_branch .Lrd_abort%=\n.Lrd_tre15%=:\ns_add_u32 s60, s58, 20480\nbuffer_load_dwordx4 v[176:179], v196, %[rs], s60 offen offset:3072 sc1\ns_waitcnt vmcnt(0)\nv_bitop3_b32 v209, v176, v178, s61 bitop3:0x7e\nv_cmp_gt_i32 vcc, 0, v209\n" \
"s_cbranch_vccz .Lrd_tl15%=\ns_add_u32 s43, s43, 1\ns_cmp_lt_u32 s43, 262144\ns_cbranch_scc1 .Lrd_tre15%=\ns_branch .Lrd_abort%=\n.Lrd_abort%=:\ns_mov_b64 exec, -1\nv_mov_b32 v16, 0\n" \
"s_movk_i32 s44, 6400\nbuffer_store_dword v16, off, %[rs], s44 sc1\n.Lrd_end%=:\ns_waitcnt vmcnt(0) lgkmcnt(0)\n" \
""
#define GG_ASM \
"v_mbcnt_lo_u32_b32 v255, -1, 0\nv_mbcnt_hi_u32_b32 v255, -1, v255\nv_lshlrev_b32 v255, 2, v255\nv_add_u32 v255, %[tb], v255\nds_read_b32 v0, v255 offset:0\nds_read_b32 v1, v255 offset:256\nds_read_b32 v2, v255 offset:512\nds_read_b32 v3, v255 offset:768\n" \
"ds_read_b32 v4, v255 offset:1024\nds_read_b32 v5, v255 offset:1280\nds_read_b32 v92, v255 offset:1536\nds_read_b32 v93, v255 offset:1792\nds_read_b32 v94, v255 offset:2048\ns_waitcnt lgkmcnt(0)\nv_mov_b32 v6, 0x3f803f80\nv_mov_b32 v7, 0x3f803f80\n" \
"s_add_u32 m0, %[lw], 16384\ns_add_u32 s44, %[wo], 0\nbuffer_load_dwordx4 v92, %[rw], s44 offen nt lds\ns_add_u32 m0, %[lw], 17408\ns_add_u32 s44, %[wo], 57344\nbuffer_load_dwordx4 v92, %[rw], s44 offen nt lds\ns_add_u32 m0, %[lw], 18432\ns_add_u32 s44, %[wo], 114688\n" \
"buffer_load_dwordx4 v92, %[rw], s44 offen nt lds\ns_add_u32 m0, %[lw], 19456\ns_add_u32 s44, %[wo], 172032\nbuffer_load_dwordx4 v92, %[rw], s44 offen nt lds\ns_add_u32 m0, %[lw], 20480\ns_add_u32 s44, %[wo], 229376\nbuffer_load_dwordx4 v92, %[rw], s44 offen nt lds\ns_add_u32 m0, %[lw], 21504\n" \
"s_add_u32 s44, %[wo], 286720\nbuffer_load_dwordx4 v92, %[rw], s44 offen nt lds\ns_add_u32 m0, %[lw], 22528\ns_add_u32 s44, %[wo], 344064\nbuffer_load_dwordx4 v92, %[rw], s44 offen nt lds\ns_add_u32 m0, %[lw], 23552\ns_add_u32 s44, %[wo], 401408\nbuffer_load_dwordx4 v92, %[rw], s44 offen nt lds\n" \
"s_add_u32 m0, %[tb], 0\ns_add_u32 s44, %[wo], 1835008\nbuffer_load_dwordx4 v92, %[rw], s44 offen nt lds\ns_add_u32 m0, %[tb], 1024\ns_add_u32 s44, %[wo], 1892352\nbuffer_load_dwordx4 v92, %[rw], s44 offen nt lds\ns_add_u32 m0, %[tb], 2048\ns_add_u32 s44, %[wo], 1949696\n" \
"buffer_load_dwordx4 v92, %[rw], s44 offen nt lds\ns_add_u32 m0, %[tb], 3072\ns_add_u32 s44, %[wo], 2007040\nbuffer_load_dwordx4 v92, %[rw], s44 offen nt lds\ns_add_u32 m0, %[tb], 4096\ns_add_u32 s44, %[wo], 2064384\nbuffer_load_dwordx4 v92, %[rw], s44 offen nt lds\ns_add_u32 m0, %[tb], 5120\n" \
"s_add_u32 s44, %[wo], 2121728\nbuffer_load_dwordx4 v92, %[rw], s44 offen nt lds\ns_add_u32 m0, %[tb], 6144\ns_add_u32 s44, %[wo], 2179072\nbuffer_load_dwordx4 v92, %[rw], s44 offen nt lds\ns_add_u32 m0, %[tb], 7168\ns_add_u32 s44, %[wo], 2236416\nbuffer_load_dwordx4 v92, %[rw], s44 offen nt lds\n" \
"s_add_u32 m0, %[tb], 8192\ns_add_u32 s44, %[wo], 3670016\nbuffer_load_dwordx4 v92, %[rw], s44 offen nt lds\ns_add_u32 m0, %[tb], 9216\ns_add_u32 s44, %[wo], 3727360\nbuffer_load_dwordx4 v92, %[rw], s44 offen nt lds\ns_add_u32 m0, %[tb], 10240\ns_add_u32 s44, %[wo], 3784704\n" \
"buffer_load_dwordx4 v92, %[rw], s44 offen nt lds\ns_add_u32 m0, %[tb], 11264\ns_add_u32 s44, %[wo], 3842048\nbuffer_load_dwordx4 v92, %[rw], s44 offen nt lds\ns_add_u32 m0, %[lw], 0\ns_add_u32 s44, %[wo], 3899392\nbuffer_load_dwordx4 v92, %[rw], s44 offen nt lds\ns_add_u32 m0, %[lw], 1024\n" \
"s_add_u32 s44, %[wo], 3956736\nbuffer_load_dwordx4 v92, %[rw], s44 offen nt lds\ns_add_u32 m0, %[lw], 2048\ns_add_u32 s44, %[wo], 4014080\nbuffer_load_dwordx4 v92, %[rw], s44 offen nt lds\ns_add_u32 m0, %[lw], 3072\ns_add_u32 s44, %[wo], 4071424\nbuffer_load_dwordx4 v92, %[rw], s44 offen nt lds\n" \
"s_add_u32 m0, %[lw], 4096\ns_add_u32 s44, %[wo], 5505024\nbuffer_load_dwordx4 v92, %[rw], s44 offen nt lds\ns_add_u32 m0, %[lw], 5120\ns_add_u32 s44, %[wo], 5562368\nbuffer_load_dwordx4 v92, %[rw], s44 offen nt lds\ns_add_u32 m0, %[lw], 6144\ns_add_u32 s44, %[wo], 5619712\n" \
"buffer_load_dwordx4 v92, %[rw], s44 offen nt lds\ns_add_u32 m0, %[lw], 7168\ns_add_u32 s44, %[wo], 5677056\nbuffer_load_dwordx4 v92, %[rw], s44 offen nt lds\ns_add_u32 m0, %[lw], 8192\ns_add_u32 s44, %[wo], 5734400\nbuffer_load_dwordx4 v92, %[rw], s44 offen nt lds\ns_add_u32 m0, %[lw], 9216\n" \
"s_add_u32 s44, %[wo], 5791744\nbuffer_load_dwordx4 v92, %[rw], s44 offen nt lds\ns_add_u32 m0, %[lw], 10240\ns_add_u32 s44, %[wo], 5849088\nbuffer_load_dwordx4 v92, %[rw], s44 offen nt lds\ns_add_u32 m0, %[lw], 11264\ns_add_u32 s44, %[wo], 5906432\nbuffer_load_dwordx4 v92, %[rw], s44 offen nt lds\n" \
"s_waitcnt vmcnt(28)\ns_barrier\ns_add_u32 m0, %[lw], 12288\ns_add_u32 s44, %[wo], 458752\nbuffer_load_dwordx4 v92, %[rw], s44 offen nt lds\ns_add_u32 m0, %[lw], 13312\ns_add_u32 s44, %[wo], 516096\nbuffer_load_dwordx4 v92, %[rw], s44 offen nt lds\n" \
"s_add_u32 m0, %[lw], 14336\ns_add_u32 s44, %[wo], 573440\nbuffer_load_dwordx4 v92, %[rw], s44 offen nt lds\ns_add_u32 m0, %[lw], 15360\ns_add_u32 s44, %[wo], 630784\nbuffer_load_dwordx4 v92, %[rw], s44 offen nt lds\nds_read_b64_tr_b16 a[0:1], v94 offset:16384\nds_read_b64_tr_b16 a[2:3], v94 offset:16896\n" \
"ds_read_b64_tr_b16 a[8:9], v94 offset:16392\nds_read_b64_tr_b16 a[10:11], v94 offset:16904\nds_read_b64_tr_b16 a[16:17], v94 offset:40960\nds_read_b64_tr_b16 a[18:19], v94 offset:41472\nds_read_b64_tr_b16 a[24:25], v94 offset:40968\nds_read_b64_tr_b16 a[26:27], v94 offset:41480\ns_waitcnt lgkmcnt(0)\nv_mfma_f32_16x16x16_bf16 v[8:11], a[0:1], v[6:7], 0\n" \
"v_mfma_f32_16x16x16_bf16 v[12:15], a[8:9], v[6:7], 0\nv_mfma_f32_16x16x16_bf16 v[16:19], a[16:17], v[6:7], 0\nv_mfma_f32_16x16x16_bf16 v[20:23], a[24:25], v[6:7], 0\nv_mfma_f32_16x16x16_bf16 v[8:11], a[2:3], v[6:7], v[8:11]\nv_mfma_f32_16x16x16_bf16 v[12:15], a[10:11], v[6:7], v[12:15]\nv_mfma_f32_16x16x16_bf16 v[16:19], a[18:19], v[6:7], v[16:19]\nv_mfma_f32_16x16x16_bf16 v[20:23], a[26:27], v[6:7], v[20:23]\ns_waitcnt vmcnt(28)\n" \
"s_barrier\ns_add_u32 m0, %[lw], 16384\ns_add_u32 s44, %[wo], 688128\nbuffer_load_dwordx4 v92, %[rw], s44 offen nt lds\ns_add_u32 m0, %[lw], 17408\ns_add_u32 s44, %[wo], 745472\nbuffer_load_dwordx4 v92, %[rw], s44 offen nt lds\ns_add_u32 m0, %[lw], 18432\n" \
"s_add_u32 s44, %[wo], 802816\nbuffer_load_dwordx4 v92, %[rw], s44 offen nt lds\ns_add_u32 m0, %[lw], 19456\ns_add_u32 s44, %[wo], 860160\nbuffer_load_dwordx4 v92, %[rw], s44 offen nt lds\nds_read_b64_tr_b16 a[4:5], v94 offset:20480\nds_read_b64_tr_b16 a[6:7], v94 offset:20992\nds_read_b64_tr_b16 a[12:13], v94 offset:20488\n" \
"ds_read_b64_tr_b16 a[14:15], v94 offset:21000\nds_read_b64_tr_b16 a[20:21], v94 offset:45056\nds_read_b64_tr_b16 a[22:23], v94 offset:45568\nds_read_b64_tr_b16 a[28:29], v94 offset:45064\nds_read_b64_tr_b16 a[30:31], v94 offset:45576\ns_waitcnt lgkmcnt(0)\nv_mfma_f32_16x16x16_bf16 v[8:11], a[4:5], v[6:7], v[8:11]\nv_mfma_f32_16x16x16_bf16 v[12:15], a[12:13], v[6:7], v[12:15]\n" \
"v_mfma_f32_16x16x16_bf16 v[16:19], a[20:21], v[6:7], v[16:19]\nv_mfma_f32_16x16x16_bf16 v[20:23], a[28:29], v[6:7], v[20:23]\nv_mfma_f32_16x16x16_bf16 v[8:11], a[6:7], v[6:7], v[8:11]\nv_mfma_f32_16x16x16_bf16 v[12:15], a[14:15], v[6:7], v[12:15]\nv_mfma_f32_16x16x16_bf16 v[16:19], a[22:23], v[6:7], v[16:19]\nv_mfma_f32_16x16x16_bf16 v[20:23], a[30:31], v[6:7], v[20:23]\ns_waitcnt vmcnt(28)\ns_barrier\n" \
"s_add_u32 m0, %[lw], 20480\ns_add_u32 s44, %[wo], 917504\nbuffer_load_dwordx4 v92, %[rw], s44 offen nt lds\ns_add_u32 m0, %[lw], 21504\ns_add_u32 s44, %[wo], 974848\nbuffer_load_dwordx4 v92, %[rw], s44 offen nt lds\ns_add_u32 m0, %[lw], 22528\ns_add_u32 s44, %[wo], 1032192\n" \
"buffer_load_dwordx4 v92, %[rw], s44 offen nt lds\ns_add_u32 m0, %[lw], 23552\ns_add_u32 s44, %[wo], 1089536\nbuffer_load_dwordx4 v92, %[rw], s44 offen nt lds\nds_read_b64_tr_b16 a[104:105], v93 offset:0\nds_read_b64_tr_b16 a[106:107], v93 offset:512\nds_read_b64_tr_b16 a[112:113], v93 offset:8\nds_read_b64_tr_b16 a[114:115], v93 offset:520\n" \
"ds_read_b64_tr_b16 a[120:121], v93 offset:12288\nds_read_b64_tr_b16 a[122:123], v93 offset:12800\nds_read_b64_tr_b16 a[128:129], v93 offset:12296\nds_read_b64_tr_b16 a[130:131], v93 offset:12808\ns_waitcnt lgkmcnt(0)\nv_mfma_f32_16x16x16_bf16 v[8:11], a[104:105], v[6:7], v[8:11]\nv_mfma_f32_16x16x16_bf16 v[12:15], a[112:113], v[6:7], v[12:15]\nv_mfma_f32_16x16x16_bf16 v[16:19], a[120:121], v[6:7], v[16:19]\n" \
"v_mfma_f32_16x16x16_bf16 v[20:23], a[128:129], v[6:7], v[20:23]\nv_mfma_f32_16x16x16_bf16 v[8:11], a[106:107], v[6:7], v[8:11]\nv_mfma_f32_16x16x16_bf16 v[12:15], a[114:115], v[6:7], v[12:15]\nv_mfma_f32_16x16x16_bf16 v[16:19], a[122:123], v[6:7], v[16:19]\nv_mfma_f32_16x16x16_bf16 v[20:23], a[130:131], v[6:7], v[20:23]\ns_waitcnt vmcnt(28)\ns_barrier\ns_add_u32 m0, %[tb], 0\n" \
"s_add_u32 s44, %[wo], 1146880\nbuffer_load_dwordx4 v92, %[rw], s44 offen nt lds\ns_add_u32 m0, %[tb], 1024\ns_add_u32 s44, %[wo], 1204224\nbuffer_load_dwordx4 v92, %[rw], s44 offen nt lds\ns_add_u32 m0, %[tb], 2048\ns_add_u32 s44, %[wo], 1261568\nbuffer_load_dwordx4 v92, %[rw], s44 offen nt lds\n" \
"s_add_u32 m0, %[tb], 3072\ns_add_u32 s44, %[wo], 1318912\nbuffer_load_dwordx4 v92, %[rw], s44 offen nt lds\nds_read_b64_tr_b16 a[108:109], v93 offset:4096\nds_read_b64_tr_b16 a[110:111], v93 offset:4608\nds_read_b64_tr_b16 a[116:117], v93 offset:4104\nds_read_b64_tr_b16 a[118:119], v93 offset:4616\nds_read_b64_tr_b16 a[124:125], v93 offset:16384\n" \
"ds_read_b64_tr_b16 a[126:127], v93 offset:16896\nds_read_b64_tr_b16 a[132:133], v93 offset:16392\nds_read_b64_tr_b16 a[134:135], v93 offset:16904\ns_waitcnt lgkmcnt(0)\nv_mfma_f32_16x16x16_bf16 v[8:11], a[108:109], v[6:7], v[8:11]\nv_mfma_f32_16x16x16_bf16 v[12:15], a[116:117], v[6:7], v[12:15]\nv_mfma_f32_16x16x16_bf16 v[16:19], a[124:125], v[6:7], v[16:19]\nv_mfma_f32_16x16x16_bf16 v[20:23], a[132:133], v[6:7], v[20:23]\n" \
"v_mfma_f32_16x16x16_bf16 v[8:11], a[110:111], v[6:7], v[8:11]\nv_mfma_f32_16x16x16_bf16 v[12:15], a[118:119], v[6:7], v[12:15]\nv_mfma_f32_16x16x16_bf16 v[16:19], a[126:127], v[6:7], v[16:19]\nv_mfma_f32_16x16x16_bf16 v[20:23], a[134:135], v[6:7], v[20:23]\ns_waitcnt vmcnt(28)\ns_barrier\ns_add_u32 m0, %[tb], 4096\ns_add_u32 s44, %[wo], 1376256\n" \
"buffer_load_dwordx4 v92, %[rw], s44 offen nt lds\ns_add_u32 m0, %[tb], 5120\ns_add_u32 s44, %[wo], 1433600\nbuffer_load_dwordx4 v92, %[rw], s44 offen nt lds\ns_add_u32 m0, %[tb], 6144\ns_add_u32 s44, %[wo], 1490944\nbuffer_load_dwordx4 v92, %[rw], s44 offen nt lds\ns_add_u32 m0, %[tb], 7168\n" \
"s_add_u32 s44, %[wo], 1548288\nbuffer_load_dwordx4 v92, %[rw], s44 offen nt lds\nds_read_b64_tr_b16 a[208:209], v93 offset:8192\nds_read_b64_tr_b16 a[210:211], v93 offset:8704\nds_read_b64_tr_b16 a[216:217], v93 offset:8200\nds_read_b64_tr_b16 a[218:219], v93 offset:8712\nds_read_b64_tr_b16 a[224:225], v93 offset:20480\nds_read_b64_tr_b16 a[226:227], v93 offset:20992\n" \
"ds_read_b64_tr_b16 a[232:233], v93 offset:20488\nds_read_b64_tr_b16 a[234:235], v93 offset:21000\ns_waitcnt lgkmcnt(0)\nv_mfma_f32_16x16x16_bf16 v[8:11], a[208:209], v[6:7], v[8:11]\nv_mfma_f32_16x16x16_bf16 v[12:15], a[216:217], v[6:7], v[12:15]\nv_mfma_f32_16x16x16_bf16 v[16:19], a[224:225], v[6:7], v[16:19]\nv_mfma_f32_16x16x16_bf16 v[20:23], a[232:233], v[6:7], v[20:23]\nv_mfma_f32_16x16x16_bf16 v[8:11], a[210:211], v[6:7], v[8:11]\n" \
"v_mfma_f32_16x16x16_bf16 v[12:15], a[218:219], v[6:7], v[12:15]\nv_mfma_f32_16x16x16_bf16 v[16:19], a[226:227], v[6:7], v[16:19]\nv_mfma_f32_16x16x16_bf16 v[20:23], a[234:235], v[6:7], v[20:23]\ns_waitcnt vmcnt(28)\ns_barrier\ns_add_u32 m0, %[tb], 8192\ns_add_u32 s44, %[wo], 1605632\nbuffer_load_dwordx4 v92, %[rw], s44 offen nt lds\n" \
"s_add_u32 m0, %[tb], 9216\ns_add_u32 s44, %[wo], 1662976\nbuffer_load_dwordx4 v92, %[rw], s44 offen nt lds\ns_add_u32 m0, %[tb], 10240\ns_add_u32 s44, %[wo], 1720320\nbuffer_load_dwordx4 v92, %[rw], s44 offen nt lds\ns_add_u32 m0, %[tb], 11264\ns_add_u32 s44, %[wo], 1777664\n" \
"buffer_load_dwordx4 v92, %[rw], s44 offen nt lds\nds_read_b64_tr_b16 a[212:213], v94 offset:0\nds_read_b64_tr_b16 a[214:215], v94 offset:512\nds_read_b64_tr_b16 a[220:221], v94 offset:8\nds_read_b64_tr_b16 a[222:223], v94 offset:520\nds_read_b64_tr_b16 a[228:229], v94 offset:24576\nds_read_b64_tr_b16 a[230:231], v94 offset:25088\nds_read_b64_tr_b16 a[236:237], v94 offset:24584\n" \
"ds_read_b64_tr_b16 a[238:239], v94 offset:25096\ns_waitcnt lgkmcnt(0)\nv_mfma_f32_16x16x16_bf16 v[8:11], a[212:213], v[6:7], v[8:11]\nv_mfma_f32_16x16x16_bf16 v[12:15], a[220:221], v[6:7], v[12:15]\nv_mfma_f32_16x16x16_bf16 v[16:19], a[228:229], v[6:7], v[16:19]\nv_mfma_f32_16x16x16_bf16 v[20:23], a[236:237], v[6:7], v[20:23]\nv_mfma_f32_16x16x16_bf16 v[8:11], a[214:215], v[6:7], v[8:11]\nv_mfma_f32_16x16x16_bf16 v[12:15], a[222:223], v[6:7], v[12:15]\n" \
"v_mfma_f32_16x16x16_bf16 v[16:19], a[230:231], v[6:7], v[16:19]\nv_mfma_f32_16x16x16_bf16 v[20:23], a[238:239], v[6:7], v[20:23]\ns_waitcnt vmcnt(28)\ns_barrier\ns_add_u32 m0, %[lw], 0\ns_add_u32 s44, %[wo], 2293760\nbuffer_load_dwordx4 v92, %[rw], s44 offen nt lds\ns_add_u32 m0, %[lw], 1024\n" \
"s_add_u32 s44, %[wo], 2351104\nbuffer_load_dwordx4 v92, %[rw], s44 offen nt lds\ns_add_u32 m0, %[lw], 2048\ns_add_u32 s44, %[wo], 2408448\nbuffer_load_dwordx4 v92, %[rw], s44 offen nt lds\ns_add_u32 m0, %[lw], 3072\ns_add_u32 s44, %[wo], 2465792\nbuffer_load_dwordx4 v92, %[rw], s44 offen nt lds\n" \
"ds_read_b64_tr_b16 v[152:153], v94 offset:4096\nds_read_b64_tr_b16 v[154:155], v94 offset:4608\nds_read_b64_tr_b16 v[160:161], v94 offset:4104\nds_read_b64_tr_b16 v[162:163], v94 offset:4616\nds_read_b64_tr_b16 v[168:169], v94 offset:28672\nds_read_b64_tr_b16 v[170:171], v94 offset:29184\nds_read_b64_tr_b16 v[176:177], v94 offset:28680\nds_read_b64_tr_b16 v[178:179], v94 offset:29192\n" \
"s_waitcnt lgkmcnt(0)\nv_mfma_f32_16x16x16_bf16 v[8:11], v[152:153], v[6:7], v[8:11]\nv_mfma_f32_16x16x16_bf16 v[12:15], v[160:161], v[6:7], v[12:15]\nv_mfma_f32_16x16x16_bf16 v[16:19], v[168:169], v[6:7], v[16:19]\nv_mfma_f32_16x16x16_bf16 v[20:23], v[176:177], v[6:7], v[20:23]\nv_mfma_f32_16x16x16_bf16 v[8:11], v[154:155], v[6:7], v[8:11]\nv_mfma_f32_16x16x16_bf16 v[12:15], v[162:163], v[6:7], v[12:15]\nv_mfma_f32_16x16x16_bf16 v[16:19], v[170:171], v[6:7], v[16:19]\n" \
"v_mfma_f32_16x16x16_bf16 v[20:23], v[178:179], v[6:7], v[20:23]\ns_waitcnt vmcnt(28)\ns_barrier\ns_add_u32 m0, %[lw], 4096\ns_add_u32 s44, %[wo], 2523136\nbuffer_load_dwordx4 v92, %[rw], s44 offen nt lds\ns_add_u32 m0, %[lw], 5120\ns_add_u32 s44, %[wo], 2580480\n" \
"buffer_load_dwordx4 v92, %[rw], s44 offen nt lds\ns_add_u32 m0, %[lw], 6144\ns_add_u32 s44, %[wo], 2637824\nbuffer_load_dwordx4 v92, %[rw], s44 offen nt lds\ns_add_u32 m0, %[lw], 7168\ns_add_u32 s44, %[wo], 2695168\nbuffer_load_dwordx4 v92, %[rw], s44 offen nt lds\nds_read_b64_tr_b16 v[156:157], v94 offset:8192\n" \
"ds_read_b64_tr_b16 v[158:159], v94 offset:8704\nds_read_b64_tr_b16 v[164:165], v94 offset:8200\nds_read_b64_tr_b16 v[166:167], v94 offset:8712\nds_read_b64_tr_b16 v[172:173], v94 offset:32768\nds_read_b64_tr_b16 v[174:175], v94 offset:33280\nds_read_b64_tr_b16 v[180:181], v94 offset:32776\nds_read_b64_tr_b16 v[182:183], v94 offset:33288\ns_waitcnt lgkmcnt(0)\n" \
"v_mfma_f32_16x16x16_bf16 v[8:11], v[156:157], v[6:7], v[8:11]\nv_mfma_f32_16x16x16_bf16 v[12:15], v[164:165], v[6:7], v[12:15]\nv_mfma_f32_16x16x16_bf16 v[16:19], v[172:173], v[6:7], v[16:19]\nv_mfma_f32_16x16x16_bf16 v[20:23], v[180:181], v[6:7], v[20:23]\nv_mfma_f32_16x16x16_bf16 v[8:11], v[158:159], v[6:7], v[8:11]\nv_mfma_f32_16x16x16_bf16 v[12:15], v[166:167], v[6:7], v[12:15]\nv_mfma_f32_16x16x16_bf16 v[16:19], v[174:175], v[6:7], v[16:19]\nv_mfma_f32_16x16x16_bf16 v[20:23], v[182:183], v[6:7], v[20:23]\n" \
"s_waitcnt vmcnt(28)\ns_barrier\ns_add_u32 m0, %[lw], 8192\ns_add_u32 s44, %[wo], 2752512\nbuffer_load_dwordx4 v92, %[rw], s44 offen nt lds\ns_add_u32 m0, %[lw], 9216\ns_add_u32 s44, %[wo], 2809856\nbuffer_load_dwordx4 v92, %[rw], s44 offen nt lds\n" \
"s_add_u32 m0, %[lw], 10240\ns_add_u32 s44, %[wo], 2867200\nbuffer_load_dwordx4 v92, %[rw], s44 offen nt lds\ns_add_u32 m0, %[lw], 11264\ns_add_u32 s44, %[wo], 2924544\nbuffer_load_dwordx4 v92, %[rw], s44 offen nt lds\nds_read_b64_tr_b16 a[32:33], v94 offset:12288\nds_read_b64_tr_b16 a[34:35], v94 offset:12800\n" \
"ds_read_b64_tr_b16 v[88:89], v94 offset:12296\nds_read_b64_tr_b16 v[90:91], v94 offset:12808\nds_read_b64_tr_b16 a[40:41], v94 offset:36864\nds_read_b64_tr_b16 a[42:43], v94 offset:37376\nds_read_b64_tr_b16 a[48:49], v94 offset:36872\nds_read_b64_tr_b16 a[50:51], v94 offset:37384\ns_waitcnt lgkmcnt(0)\nv_mfma_f32_16x16x16_bf16 v[8:11], a[32:33], v[6:7], v[8:11]\n" \
"v_mfma_f32_16x16x16_bf16 v[12:15], v[88:89], v[6:7], v[12:15]\nv_mfma_f32_16x16x16_bf16 v[16:19], a[40:41], v[6:7], v[16:19]\nv_mfma_f32_16x16x16_bf16 v[20:23], a[48:49], v[6:7], v[20:23]\nv_mfma_f32_16x16x16_bf16 v[8:11], a[34:35], v[6:7], v[8:11]\nv_mfma_f32_16x16x16_bf16 v[12:15], v[90:91], v[6:7], v[12:15]\nv_mfma_f32_16x16x16_bf16 v[16:19], a[42:43], v[6:7], v[16:19]\nv_mfma_f32_16x16x16_bf16 v[20:23], a[50:51], v[6:7], v[20:23]\ns_waitcnt vmcnt(28)\n" \
"s_barrier\ns_add_u32 m0, %[lw], 12288\ns_add_u32 s44, %[wo], 2981888\nbuffer_load_dwordx4 v92, %[rw], s44 offen nt lds\ns_add_u32 m0, %[lw], 13312\ns_add_u32 s44, %[wo], 3039232\nbuffer_load_dwordx4 v92, %[rw], s44 offen nt lds\ns_add_u32 m0, %[lw], 14336\n" \
"s_add_u32 s44, %[wo], 3096576\nbuffer_load_dwordx4 v92, %[rw], s44 offen nt lds\ns_add_u32 m0, %[lw], 15360\ns_add_u32 s44, %[wo], 3153920\nbuffer_load_dwordx4 v92, %[rw], s44 offen nt lds\nds_read_b64_tr_b16 a[36:37], v94 offset:16384\nds_read_b64_tr_b16 a[38:39], v94 offset:16896\nds_read_b64_tr_b16 v[84:85], v94 offset:16392\n" \
"ds_read_b64_tr_b16 v[86:87], v94 offset:16904\nds_read_b64_tr_b16 a[44:45], v94 offset:40960\nds_read_b64_tr_b16 a[46:47], v94 offset:41472\nds_read_b64_tr_b16 a[52:53], v94 offset:40968\nds_read_b64_tr_b16 a[54:55], v94 offset:41480\ns_waitcnt lgkmcnt(0)\nv_mfma_f32_16x16x16_bf16 v[8:11], a[36:37], v[6:7], v[8:11]\nv_mfma_f32_16x16x16_bf16 v[12:15], v[84:85], v[6:7], v[12:15]\n" \
"v_mfma_f32_16x16x16_bf16 v[16:19], a[44:45], v[6:7], v[16:19]\nv_mfma_f32_16x16x16_bf16 v[20:23], a[52:53], v[6:7], v[20:23]\nv_mfma_f32_16x16x16_bf16 v[8:11], a[38:39], v[6:7], v[8:11]\nv_mfma_f32_16x16x16_bf16 v[12:15], v[86:87], v[6:7], v[12:15]\nv_mfma_f32_16x16x16_bf16 v[16:19], a[46:47], v[6:7], v[16:19]\nv_mfma_f32_16x16x16_bf16 v[20:23], a[54:55], v[6:7], v[20:23]\ns_waitcnt vmcnt(28)\ns_barrier\n" \
"s_add_u32 m0, %[lw], 16384\ns_add_u32 s44, %[wo], 3211264\nbuffer_load_dwordx4 v92, %[rw], s44 offen nt lds\ns_add_u32 m0, %[lw], 17408\ns_add_u32 s44, %[wo], 3268608\nbuffer_load_dwordx4 v92, %[rw], s44 offen nt lds\ns_add_u32 m0, %[lw], 18432\ns_add_u32 s44, %[wo], 3325952\n" \
"buffer_load_dwordx4 v92, %[rw], s44 offen nt lds\ns_add_u32 m0, %[lw], 19456\ns_add_u32 s44, %[wo], 3383296\nbuffer_load_dwordx4 v92, %[rw], s44 offen nt lds\nds_read_b64_tr_b16 a[56:57], v94 offset:20480\nds_read_b64_tr_b16 a[58:59], v94 offset:20992\nds_read_b64_tr_b16 a[64:65], v94 offset:20488\nds_read_b64_tr_b16 a[66:67], v94 offset:21000\n" \
"ds_read_b64_tr_b16 v[80:81], v94 offset:45056\nds_read_b64_tr_b16 v[82:83], v94 offset:45568\nds_read_b64_tr_b16 a[72:73], v94 offset:45064\nds_read_b64_tr_b16 a[74:75], v94 offset:45576\ns_waitcnt lgkmcnt(0)\nv_mfma_f32_16x16x16_bf16 v[8:11], a[56:57], v[6:7], v[8:11]\nv_mfma_f32_16x16x16_bf16 v[12:15], a[64:65], v[6:7], v[12:15]\nv_mfma_f32_16x16x16_bf16 v[16:19], v[80:81], v[6:7], v[16:19]\n" \
"v_mfma_f32_16x16x16_bf16 v[20:23], a[72:73], v[6:7], v[20:23]\nv_mfma_f32_16x16x16_bf16 v[8:11], a[58:59], v[6:7], v[8:11]\nv_mfma_f32_16x16x16_bf16 v[12:15], a[66:67], v[6:7], v[12:15]\nv_mfma_f32_16x16x16_bf16 v[16:19], v[82:83], v[6:7], v[16:19]\nv_mfma_f32_16x16x16_bf16 v[20:23], a[74:75], v[6:7], v[20:23]\ns_waitcnt vmcnt(28)\ns_barrier\ns_add_u32 m0, %[lw], 20480\n" \
"s_add_u32 s44, %[wo], 3440640\nbuffer_load_dwordx4 v92, %[rw], s44 offen nt lds\ns_add_u32 m0, %[lw], 21504\ns_add_u32 s44, %[wo], 3497984\nbuffer_load_dwordx4 v92, %[rw], s44 offen nt lds\ns_add_u32 m0, %[lw], 22528\ns_add_u32 s44, %[wo], 3555328\nbuffer_load_dwordx4 v92, %[rw], s44 offen nt lds\n" \
"s_add_u32 m0, %[lw], 23552\ns_add_u32 s44, %[wo], 3612672\nbuffer_load_dwordx4 v92, %[rw], s44 offen nt lds\nds_read_b64_tr_b16 a[60:61], v93 offset:0\nds_read_b64_tr_b16 a[62:63], v93 offset:512\nds_read_b64_tr_b16 a[68:69], v93 offset:8\nds_read_b64_tr_b16 a[70:71], v93 offset:520\nds_read_b64_tr_b16 v[76:77], v93 offset:12288\n" \
"ds_read_b64_tr_b16 v[78:79], v93 offset:12800\nds_read_b64_tr_b16 a[76:77], v93 offset:12296\nds_read_b64_tr_b16 a[78:79], v93 offset:12808\ns_waitcnt lgkmcnt(0)\nv_mfma_f32_16x16x16_bf16 v[8:11], a[60:61], v[6:7], v[8:11]\nv_mfma_f32_16x16x16_bf16 v[12:15], a[68:69], v[6:7], v[12:15]\nv_mfma_f32_16x16x16_bf16 v[16:19], v[76:77], v[6:7], v[16:19]\nv_mfma_f32_16x16x16_bf16 v[20:23], a[76:77], v[6:7], v[20:23]\n" \
"v_mfma_f32_16x16x16_bf16 v[8:11], a[62:63], v[6:7], v[8:11]\nv_mfma_f32_16x16x16_bf16 v[12:15], a[70:71], v[6:7], v[12:15]\nv_mfma_f32_16x16x16_bf16 v[16:19], v[78:79], v[6:7], v[16:19]\nv_mfma_f32_16x16x16_bf16 v[20:23], a[78:79], v[6:7], v[20:23]\ns_waitcnt vmcnt(28)\ns_barrier\ns_add_u32 m0, %[tb], 0\ns_add_u32 s44, %[wo], 4128768\n" \
"buffer_load_dwordx4 v92, %[rw], s44 offen nt lds\ns_add_u32 m0, %[tb], 1024\ns_add_u32 s44, %[wo], 4186112\nbuffer_load_dwordx4 v92, %[rw], s44 offen nt lds\ns_add_u32 m0, %[tb], 2048\ns_add_u32 s44, %[wo], 4243456\nbuffer_load_dwordx4 v92, %[rw], s44 offen nt lds\ns_add_u32 m0, %[tb], 3072\n" \
"s_add_u32 s44, %[wo], 4300800\nbuffer_load_dwordx4 v92, %[rw], s44 offen nt lds\nds_read_b64_tr_b16 a[80:81], v93 offset:4096\nds_read_b64_tr_b16 a[82:83], v93 offset:4608\nds_read_b64_tr_b16 a[88:89], v93 offset:4104\nds_read_b64_tr_b16 a[90:91], v93 offset:4616\nds_read_b64_tr_b16 a[96:97], v93 offset:16384\nds_read_b64_tr_b16 a[98:99], v93 offset:16896\n" \
"ds_read_b64_tr_b16 v[72:73], v93 offset:16392\nds_read_b64_tr_b16 v[74:75], v93 offset:16904\ns_waitcnt lgkmcnt(0)\nv_mfma_f32_16x16x16_bf16 v[8:11], a[80:81], v[6:7], v[8:11]\nv_mfma_f32_16x16x16_bf16 v[12:15], a[88:89], v[6:7], v[12:15]\nv_mfma_f32_16x16x16_bf16 v[16:19], a[96:97], v[6:7], v[16:19]\nv_mfma_f32_16x16x16_bf16 v[20:23], v[72:73], v[6:7], v[20:23]\nv_mfma_f32_16x16x16_bf16 v[8:11], a[82:83], v[6:7], v[8:11]\n" \
"v_mfma_f32_16x16x16_bf16 v[12:15], a[90:91], v[6:7], v[12:15]\nv_mfma_f32_16x16x16_bf16 v[16:19], a[98:99], v[6:7], v[16:19]\nv_mfma_f32_16x16x16_bf16 v[20:23], v[74:75], v[6:7], v[20:23]\ns_waitcnt vmcnt(28)\ns_barrier\ns_add_u32 m0, %[tb], 4096\ns_add_u32 s44, %[wo], 4358144\nbuffer_load_dwordx4 v92, %[rw], s44 offen nt lds\n" \
"s_add_u32 m0, %[tb], 5120\ns_add_u32 s44, %[wo], 4415488\nbuffer_load_dwordx4 v92, %[rw], s44 offen nt lds\ns_add_u32 m0, %[tb], 6144\ns_add_u32 s44, %[wo], 4472832\nbuffer_load_dwordx4 v92, %[rw], s44 offen nt lds\ns_add_u32 m0, %[tb], 7168\ns_add_u32 s44, %[wo], 4530176\n" \
"buffer_load_dwordx4 v92, %[rw], s44 offen nt lds\nds_read_b64_tr_b16 a[84:85], v93 offset:8192\nds_read_b64_tr_b16 a[86:87], v93 offset:8704\nds_read_b64_tr_b16 a[92:93], v93 offset:8200\nds_read_b64_tr_b16 a[94:95], v93 offset:8712\nds_read_b64_tr_b16 a[100:101], v93 offset:20480\nds_read_b64_tr_b16 a[102:103], v93 offset:20992\nds_read_b64_tr_b16 v[68:69], v93 offset:20488\n" \
"ds_read_b64_tr_b16 v[70:71], v93 offset:21000\ns_waitcnt lgkmcnt(0)\nv_mfma_f32_16x16x16_bf16 v[8:11], a[84:85], v[6:7], v[8:11]\nv_mfma_f32_16x16x16_bf16 v[12:15], a[92:93], v[6:7], v[12:15]\nv_mfma_f32_16x16x16_bf16 v[16:19], a[100:101], v[6:7], v[16:19]\nv_mfma_f32_16x16x16_bf16 v[20:23], v[68:69], v[6:7], v[20:23]\nv_mfma_f32_16x16x16_bf16 v[8:11], a[86:87], v[6:7], v[8:11]\nv_mfma_f32_16x16x16_bf16 v[12:15], a[94:95], v[6:7], v[12:15]\n" \
"v_mfma_f32_16x16x16_bf16 v[16:19], a[102:103], v[6:7], v[16:19]\nv_mfma_f32_16x16x16_bf16 v[20:23], v[70:71], v[6:7], v[20:23]\ns_waitcnt vmcnt(28)\ns_barrier\ns_add_u32 m0, %[tb], 8192\ns_add_u32 s44, %[wo], 4587520\nbuffer_load_dwordx4 v92, %[rw], s44 offen nt lds\ns_add_u32 m0, %[tb], 9216\n" \
"s_add_u32 s44, %[wo], 4644864\nbuffer_load_dwordx4 v92, %[rw], s44 offen nt lds\ns_add_u32 m0, %[tb], 10240\ns_add_u32 s44, %[wo], 4702208\nbuffer_load_dwordx4 v92, %[rw], s44 offen nt lds\ns_add_u32 m0, %[tb], 11264\ns_add_u32 s44, %[wo], 4759552\nbuffer_load_dwordx4 v92, %[rw], s44 offen nt lds\n" \
"ds_read_b64_tr_b16 a[136:137], v94 offset:0\nds_read_b64_tr_b16 a[138:139], v94 offset:512\nds_read_b64_tr_b16 v[64:65], v94 offset:8\nds_read_b64_tr_b16 v[66:67], v94 offset:520\nds_read_b64_tr_b16 a[144:145], v94 offset:24576\nds_read_b64_tr_b16 a[146:147], v94 offset:25088\nds_read_b64_tr_b16 a[152:153], v94 offset:24584\nds_read_b64_tr_b16 a[154:155], v94 offset:25096\n" \
"s_waitcnt lgkmcnt(0)\nv_mfma_f32_16x16x16_bf16 v[8:11], a[136:137], v[6:7], v[8:11]\nv_mfma_f32_16x16x16_bf16 v[12:15], v[64:65], v[6:7], v[12:15]\nv_mfma_f32_16x16x16_bf16 v[16:19], a[144:145], v[6:7], v[16:19]\nv_mfma_f32_16x16x16_bf16 v[20:23], a[152:153], v[6:7], v[20:23]\nv_mfma_f32_16x16x16_bf16 v[8:11], a[138:139], v[6:7], v[8:11]\nv_mfma_f32_16x16x16_bf16 v[12:15], v[66:67], v[6:7], v[12:15]\nv_mfma_f32_16x16x16_bf16 v[16:19], a[146:147], v[6:7], v[16:19]\n" \
"v_mfma_f32_16x16x16_bf16 v[20:23], a[154:155], v[6:7], v[20:23]\ns_waitcnt vmcnt(28)\ns_barrier\ns_add_u32 m0, %[lw], 0\ns_add_u32 s44, %[wo], 4816896\nbuffer_load_dwordx4 v92, %[rw], s44 offen nt lds\ns_add_u32 m0, %[lw], 1024\ns_add_u32 s44, %[wo], 4874240\n" \
"buffer_load_dwordx4 v92, %[rw], s44 offen nt lds\ns_add_u32 m0, %[lw], 2048\ns_add_u32 s44, %[wo], 4931584\nbuffer_load_dwordx4 v92, %[rw], s44 offen nt lds\ns_add_u32 m0, %[lw], 3072\ns_add_u32 s44, %[wo], 4988928\nbuffer_load_dwordx4 v92, %[rw], s44 offen nt lds\nds_read_b64_tr_b16 a[140:141], v94 offset:4096\n" \
"ds_read_b64_tr_b16 a[142:143], v94 offset:4608\nds_read_b64_tr_b16 v[60:61], v94 offset:4104\nds_read_b64_tr_b16 v[62:63], v94 offset:4616\nds_read_b64_tr_b16 a[148:149], v94 offset:28672\nds_read_b64_tr_b16 a[150:151], v94 offset:29184\nds_read_b64_tr_b16 a[156:157], v94 offset:28680\nds_read_b64_tr_b16 a[158:159], v94 offset:29192\ns_waitcnt lgkmcnt(0)\n" \
"v_mfma_f32_16x16x16_bf16 v[8:11], a[140:141], v[6:7], v[8:11]\nv_mfma_f32_16x16x16_bf16 v[12:15], v[60:61], v[6:7], v[12:15]\nv_mfma_f32_16x16x16_bf16 v[16:19], a[148:149], v[6:7], v[16:19]\nv_mfma_f32_16x16x16_bf16 v[20:23], a[156:157], v[6:7], v[20:23]\nv_mfma_f32_16x16x16_bf16 v[8:11], a[142:143], v[6:7], v[8:11]\nv_mfma_f32_16x16x16_bf16 v[12:15], v[62:63], v[6:7], v[12:15]\nv_mfma_f32_16x16x16_bf16 v[16:19], a[150:151], v[6:7], v[16:19]\nv_mfma_f32_16x16x16_bf16 v[20:23], a[158:159], v[6:7], v[20:23]\n" \
"s_waitcnt vmcnt(28)\ns_barrier\ns_add_u32 m0, %[lw], 4096\ns_add_u32 s44, %[wo], 5046272\nbuffer_load_dwordx4 v92, %[rw], s44 offen nt lds\ns_add_u32 m0, %[lw], 5120\ns_add_u32 s44, %[wo], 5103616\nbuffer_load_dwordx4 v92, %[rw], s44 offen nt lds\n" \
"s_add_u32 m0, %[lw], 6144\ns_add_u32 s44, %[wo], 5160960\nbuffer_load_dwordx4 v92, %[rw], s44 offen nt lds\ns_add_u32 m0, %[lw], 7168\ns_add_u32 s44, %[wo], 5218304\nbuffer_load_dwordx4 v92, %[rw], s44 offen nt lds\nds_read_b64_tr_b16 a[160:161], v94 offset:8192\nds_read_b64_tr_b16 a[162:163], v94 offset:8704\n" \
"ds_read_b64_tr_b16 a[168:169], v94 offset:8200\nds_read_b64_tr_b16 a[170:171], v94 offset:8712\nds_read_b64_tr_b16 v[56:57], v94 offset:32768\nds_read_b64_tr_b16 v[58:59], v94 offset:33280\nds_read_b64_tr_b16 a[176:177], v94 offset:32776\nds_read_b64_tr_b16 a[178:179], v94 offset:33288\ns_waitcnt lgkmcnt(0)\nv_mfma_f32_16x16x16_bf16 v[8:11], a[160:161], v[6:7], v[8:11]\n" \
"v_mfma_f32_16x16x16_bf16 v[12:15], a[168:169], v[6:7], v[12:15]\nv_mfma_f32_16x16x16_bf16 v[16:19], v[56:57], v[6:7], v[16:19]\nv_mfma_f32_16x16x16_bf16 v[20:23], a[176:177], v[6:7], v[20:23]\nv_mfma_f32_16x16x16_bf16 v[8:11], a[162:163], v[6:7], v[8:11]\nv_mfma_f32_16x16x16_bf16 v[12:15], a[170:171], v[6:7], v[12:15]\nv_mfma_f32_16x16x16_bf16 v[16:19], v[58:59], v[6:7], v[16:19]\nv_mfma_f32_16x16x16_bf16 v[20:23], a[178:179], v[6:7], v[20:23]\ns_waitcnt vmcnt(28)\n" \
"s_barrier\ns_add_u32 m0, %[lw], 8192\ns_add_u32 s44, %[wo], 5275648\nbuffer_load_dwordx4 v92, %[rw], s44 offen nt lds\ns_add_u32 m0, %[lw], 9216\ns_add_u32 s44, %[wo], 5332992\nbuffer_load_dwordx4 v92, %[rw], s44 offen nt lds\ns_add_u32 m0, %[lw], 10240\n" \
"s_add_u32 s44, %[wo], 5390336\nbuffer_load_dwordx4 v92, %[rw], s44 offen nt lds\ns_add_u32 m0, %[lw], 11264\ns_add_u32 s44, %[wo], 5447680\nbuffer_load_dwordx4 v92, %[rw], s44 offen nt lds\nds_read_b64_tr_b16 a[164:165], v94 offset:12288\nds_read_b64_tr_b16 a[166:167], v94 offset:12800\nds_read_b64_tr_b16 a[172:173], v94 offset:12296\n" \
"ds_read_b64_tr_b16 a[174:175], v94 offset:12808\nds_read_b64_tr_b16 v[52:53], v94 offset:36864\nds_read_b64_tr_b16 v[54:55], v94 offset:37376\nds_read_b64_tr_b16 a[180:181], v94 offset:36872\nds_read_b64_tr_b16 a[182:183], v94 offset:37384\ns_waitcnt lgkmcnt(0)\nv_mfma_f32_16x16x16_bf16 v[8:11], a[164:165], v[6:7], v[8:11]\nv_mfma_f32_16x16x16_bf16 v[12:15], a[172:173], v[6:7], v[12:15]\n" \
"v_mfma_f32_16x16x16_bf16 v[16:19], v[52:53], v[6:7], v[16:19]\nv_mfma_f32_16x16x16_bf16 v[20:23], a[180:181], v[6:7], v[20:23]\nv_mfma_f32_16x16x16_bf16 v[8:11], a[166:167], v[6:7], v[8:11]\nv_mfma_f32_16x16x16_bf16 v[12:15], a[174:175], v[6:7], v[12:15]\nv_mfma_f32_16x16x16_bf16 v[16:19], v[54:55], v[6:7], v[16:19]\nv_mfma_f32_16x16x16_bf16 v[20:23], a[182:183], v[6:7], v[20:23]\ns_waitcnt vmcnt(28)\ns_barrier\n" \
"s_add_u32 m0, %[lw], 12288\ns_add_u32 s44, %[wo], 5963776\nbuffer_load_dwordx4 v92, %[rw], s44 offen nt lds\ns_add_u32 m0, %[lw], 13312\ns_add_u32 s44, %[wo], 6021120\nbuffer_load_dwordx4 v92, %[rw], s44 offen nt lds\ns_add_u32 m0, %[lw], 14336\ns_add_u32 s44, %[wo], 6078464\n" \
"buffer_load_dwordx4 v92, %[rw], s44 offen nt lds\ns_add_u32 m0, %[lw], 15360\ns_add_u32 s44, %[wo], 6135808\nbuffer_load_dwordx4 v92, %[rw], s44 offen nt lds\nds_read_b64_tr_b16 a[184:185], v94 offset:16384\nds_read_b64_tr_b16 a[186:187], v94 offset:16896\nds_read_b64_tr_b16 a[192:193], v94 offset:16392\nds_read_b64_tr_b16 a[194:195], v94 offset:16904\n" \
"ds_read_b64_tr_b16 a[200:201], v94 offset:40960\nds_read_b64_tr_b16 a[202:203], v94 offset:41472\nds_read_b64_tr_b16 v[48:49], v94 offset:40968\nds_read_b64_tr_b16 v[50:51], v94 offset:41480\ns_waitcnt lgkmcnt(0)\nv_mfma_f32_16x16x16_bf16 v[8:11], a[184:185], v[6:7], v[8:11]\nv_mfma_f32_16x16x16_bf16 v[12:15], a[192:193], v[6:7], v[12:15]\nv_mfma_f32_16x16x16_bf16 v[16:19], a[200:201], v[6:7], v[16:19]\n" \
"v_mfma_f32_16x16x16_bf16 v[20:23], v[48:49], v[6:7], v[20:23]\nv_mfma_f32_16x16x16_bf16 v[8:11], a[186:187], v[6:7], v[8:11]\nv_mfma_f32_16x16x16_bf16 v[12:15], a[194:195], v[6:7], v[12:15]\nv_mfma_f32_16x16x16_bf16 v[16:19], a[202:203], v[6:7], v[16:19]\nv_mfma_f32_16x16x16_bf16 v[20:23], v[50:51], v[6:7], v[20:23]\ns_waitcnt vmcnt(28)\ns_barrier\ns_add_u32 m0, %[lw], 16384\n" \
"s_add_u32 s44, %[wo], 6193152\nbuffer_load_dwordx4 v92, %[rw], s44 offen nt lds\ns_add_u32 m0, %[lw], 17408\ns_add_u32 s44, %[wo], 6250496\nbuffer_load_dwordx4 v92, %[rw], s44 offen nt lds\ns_add_u32 m0, %[lw], 18432\ns_add_u32 s44, %[wo], 6307840\nbuffer_load_dwordx4 v92, %[rw], s44 offen nt lds\n" \
"s_add_u32 m0, %[lw], 19456\ns_add_u32 s44, %[wo], 6365184\nbuffer_load_dwordx4 v92, %[rw], s44 offen nt lds\nds_read_b64_tr_b16 a[188:189], v94 offset:20480\nds_read_b64_tr_b16 a[190:191], v94 offset:20992\nds_read_b64_tr_b16 a[196:197], v94 offset:20488\nds_read_b64_tr_b16 a[198:199], v94 offset:21000\nds_read_b64_tr_b16 a[204:205], v94 offset:45056\n" \
"ds_read_b64_tr_b16 a[206:207], v94 offset:45568\nds_read_b64_tr_b16 v[44:45], v94 offset:45064\nds_read_b64_tr_b16 v[46:47], v94 offset:45576\ns_waitcnt lgkmcnt(0)\nv_mfma_f32_16x16x16_bf16 v[8:11], a[188:189], v[6:7], v[8:11]\nv_mfma_f32_16x16x16_bf16 v[12:15], a[196:197], v[6:7], v[12:15]\nv_mfma_f32_16x16x16_bf16 v[16:19], a[204:205], v[6:7], v[16:19]\nv_mfma_f32_16x16x16_bf16 v[20:23], v[44:45], v[6:7], v[20:23]\n" \
"v_mfma_f32_16x16x16_bf16 v[8:11], a[190:191], v[6:7], v[8:11]\nv_mfma_f32_16x16x16_bf16 v[12:15], a[198:199], v[6:7], v[12:15]\nv_mfma_f32_16x16x16_bf16 v[16:19], a[206:207], v[6:7], v[16:19]\nv_mfma_f32_16x16x16_bf16 v[20:23], v[46:47], v[6:7], v[20:23]\ns_waitcnt vmcnt(28)\ns_barrier\ns_add_u32 m0, %[lw], 20480\ns_add_u32 s44, %[wo], 6422528\n" \
"buffer_load_dwordx4 v92, %[rw], s44 offen nt lds\ns_add_u32 m0, %[lw], 21504\ns_add_u32 s44, %[wo], 6479872\nbuffer_load_dwordx4 v92, %[rw], s44 offen nt lds\ns_add_u32 m0, %[lw], 22528\ns_add_u32 s44, %[wo], 6537216\nbuffer_load_dwordx4 v92, %[rw], s44 offen nt lds\ns_add_u32 m0, %[lw], 23552\n" \
"s_add_u32 s44, %[wo], 6594560\nbuffer_load_dwordx4 v92, %[rw], s44 offen nt lds\nds_read_b64_tr_b16 a[240:241], v93 offset:0\nds_read_b64_tr_b16 a[242:243], v93 offset:512\nds_read_b64_tr_b16 v[40:41], v93 offset:8\nds_read_b64_tr_b16 v[42:43], v93 offset:520\nds_read_b64_tr_b16 a[248:249], v93 offset:12288\nds_read_b64_tr_b16 a[250:251], v93 offset:12800\n" \
"ds_read_b64_tr_b16 v[96:97], v93 offset:12296\nds_read_b64_tr_b16 v[98:99], v93 offset:12808\ns_waitcnt lgkmcnt(0)\nv_mfma_f32_16x16x16_bf16 v[8:11], a[240:241], v[6:7], v[8:11]\nv_mfma_f32_16x16x16_bf16 v[12:15], v[40:41], v[6:7], v[12:15]\nv_mfma_f32_16x16x16_bf16 v[16:19], a[248:249], v[6:7], v[16:19]\nv_mfma_f32_16x16x16_bf16 v[20:23], v[96:97], v[6:7], v[20:23]\nv_mfma_f32_16x16x16_bf16 v[8:11], a[242:243], v[6:7], v[8:11]\n" \
"v_mfma_f32_16x16x16_bf16 v[12:15], v[42:43], v[6:7], v[12:15]\nv_mfma_f32_16x16x16_bf16 v[16:19], a[250:251], v[6:7], v[16:19]\nv_mfma_f32_16x16x16_bf16 v[20:23], v[98:99], v[6:7], v[20:23]\ns_waitcnt vmcnt(28)\ns_barrier\ns_add_u32 m0, %[tb], 0\ns_add_u32 s44, %[wo], 6651904\nbuffer_load_dwordx4 v92, %[rw], s44 offen nt lds\n" \
"s_add_u32 m0, %[tb], 1024\ns_add_u32 s44, %[wo], 6709248\nbuffer_load_dwordx4 v92, %[rw], s44 offen nt lds\ns_add_u32 m0, %[tb], 2048\ns_add_u32 s44, %[wo], 6766592\nbuffer_load_dwordx4 v92, %[rw], s44 offen nt lds\ns_add_u32 m0, %[tb], 3072\ns_add_u32 s44, %[wo], 6823936\n" \
"buffer_load_dwordx4 v92, %[rw], s44 offen nt lds\nds_read_b64_tr_b16 a[244:245], v93 offset:4096\nds_read_b64_tr_b16 a[246:247], v93 offset:4608\nds_read_b64_tr_b16 v[36:37], v93 offset:4104\nds_read_b64_tr_b16 v[38:39], v93 offset:4616\nds_read_b64_tr_b16 a[252:253], v93 offset:16384\nds_read_b64_tr_b16 a[254:255], v93 offset:16896\nds_read_b64_tr_b16 v[100:101], v93 offset:16392\n" \
"ds_read_b64_tr_b16 v[102:103], v93 offset:16904\ns_waitcnt lgkmcnt(0)\nv_mfma_f32_16x16x16_bf16 v[8:11], a[244:245], v[6:7], v[8:11]\nv_mfma_f32_16x16x16_bf16 v[12:15], v[36:37], v[6:7], v[12:15]\nv_mfma_f32_16x16x16_bf16 v[16:19], a[252:253], v[6:7], v[16:19]\nv_mfma_f32_16x16x16_bf16 v[20:23], v[100:101], v[6:7], v[20:23]\nv_mfma_f32_16x16x16_bf16 v[8:11], a[246:247], v[6:7], v[8:11]\nv_mfma_f32_16x16x16_bf16 v[12:15], v[38:39], v[6:7], v[12:15]\n" \
"v_mfma_f32_16x16x16_bf16 v[16:19], a[254:255], v[6:7], v[16:19]\nv_mfma_f32_16x16x16_bf16 v[20:23], v[102:103], v[6:7], v[20:23]\ns_waitcnt vmcnt(28)\ns_barrier\ns_add_u32 m0, %[tb], 4096\ns_add_u32 s44, %[wo], 6881280\nbuffer_load_dwordx4 v92, %[rw], s44 offen nt lds\ns_add_u32 m0, %[tb], 5120\n" \
"s_add_u32 s44, %[wo], 6938624\nbuffer_load_dwordx4 v92, %[rw], s44 offen nt lds\ns_add_u32 m0, %[tb], 6144\ns_add_u32 s44, %[wo], 6995968\nbuffer_load_dwordx4 v92, %[rw], s44 offen nt lds\ns_add_u32 m0, %[tb], 7168\ns_add_u32 s44, %[wo], 7053312\nbuffer_load_dwordx4 v92, %[rw], s44 offen nt lds\n" \
"ds_read_b64_tr_b16 v[104:105], v93 offset:8192\nds_read_b64_tr_b16 v[106:107], v93 offset:8704\nds_read_b64_tr_b16 v[112:113], v93 offset:8200\nds_read_b64_tr_b16 v[114:115], v93 offset:8712\nds_read_b64_tr_b16 v[32:33], v93 offset:20480\nds_read_b64_tr_b16 v[34:35], v93 offset:20992\nds_read_b64_tr_b16 v[120:121], v93 offset:20488\nds_read_b64_tr_b16 v[122:123], v93 offset:21000\n" \
"s_waitcnt lgkmcnt(0)\nv_mfma_f32_16x16x16_bf16 v[8:11], v[104:105], v[6:7], v[8:11]\nv_mfma_f32_16x16x16_bf16 v[12:15], v[112:113], v[6:7], v[12:15]\nv_mfma_f32_16x16x16_bf16 v[16:19], v[32:33], v[6:7], v[16:19]\nv_mfma_f32_16x16x16_bf16 v[20:23], v[120:121], v[6:7], v[20:23]\nv_mfma_f32_16x16x16_bf16 v[8:11], v[106:107], v[6:7], v[8:11]\nv_mfma_f32_16x16x16_bf16 v[12:15], v[114:115], v[6:7], v[12:15]\nv_mfma_f32_16x16x16_bf16 v[16:19], v[34:35], v[6:7], v[16:19]\n" \
"v_mfma_f32_16x16x16_bf16 v[20:23], v[122:123], v[6:7], v[20:23]\ns_waitcnt vmcnt(28)\ns_barrier\ns_add_u32 m0, %[tb], 8192\ns_add_u32 s44, %[wo], 7110656\nbuffer_load_dwordx4 v92, %[rw], s44 offen nt lds\ns_add_u32 m0, %[tb], 9216\ns_add_u32 s44, %[wo], 7168000\n" \
"buffer_load_dwordx4 v92, %[rw], s44 offen nt lds\ns_add_u32 m0, %[tb], 10240\ns_add_u32 s44, %[wo], 7225344\nbuffer_load_dwordx4 v92, %[rw], s44 offen nt lds\ns_add_u32 m0, %[tb], 11264\ns_add_u32 s44, %[wo], 7282688\nbuffer_load_dwordx4 v92, %[rw], s44 offen nt lds\nds_read_b64_tr_b16 v[108:109], v94 offset:0\n" \
"ds_read_b64_tr_b16 v[110:111], v94 offset:512\nds_read_b64_tr_b16 v[116:117], v94 offset:8\nds_read_b64_tr_b16 v[118:119], v94 offset:520\nds_read_b64_tr_b16 v[28:29], v94 offset:24576\nds_read_b64_tr_b16 v[30:31], v94 offset:25088\nds_read_b64_tr_b16 v[124:125], v94 offset:24584\nds_read_b64_tr_b16 v[126:127], v94 offset:25096\ns_waitcnt lgkmcnt(0)\n" \
"v_mfma_f32_16x16x16_bf16 v[8:11], v[108:109], v[6:7], v[8:11]\nv_mfma_f32_16x16x16_bf16 v[12:15], v[116:117], v[6:7], v[12:15]\nv_mfma_f32_16x16x16_bf16 v[16:19], v[28:29], v[6:7], v[16:19]\nv_mfma_f32_16x16x16_bf16 v[20:23], v[124:125], v[6:7], v[20:23]\nv_mfma_f32_16x16x16_bf16 v[8:11], v[110:111], v[6:7], v[8:11]\nv_mfma_f32_16x16x16_bf16 v[12:15], v[118:119], v[6:7], v[12:15]\nv_mfma_f32_16x16x16_bf16 v[16:19], v[30:31], v[6:7], v[16:19]\nv_mfma_f32_16x16x16_bf16 v[20:23], v[126:127], v[6:7], v[20:23]\n" \
"s_waitcnt vmcnt(28)\ns_barrier\nds_read_b64_tr_b16 v[128:129], v94 offset:4096\nds_read_b64_tr_b16 v[130:131], v94 offset:4608\nds_read_b64_tr_b16 v[136:137], v94 offset:4104\nds_read_b64_tr_b16 v[138:139], v94 offset:4616\nds_read_b64_tr_b16 v[144:145], v94 offset:28672\nds_read_b64_tr_b16 v[146:147], v94 offset:29184\n" \
"ds_read_b64_tr_b16 v[24:25], v94 offset:28680\nds_read_b64_tr_b16 v[26:27], v94 offset:29192\ns_waitcnt lgkmcnt(0)\nv_mfma_f32_16x16x16_bf16 v[8:11], v[128:129], v[6:7], v[8:11]\nv_mfma_f32_16x16x16_bf16 v[12:15], v[136:137], v[6:7], v[12:15]\nv_mfma_f32_16x16x16_bf16 v[16:19], v[144:145], v[6:7], v[16:19]\nv_mfma_f32_16x16x16_bf16 v[20:23], v[24:25], v[6:7], v[20:23]\nv_mfma_f32_16x16x16_bf16 v[8:11], v[130:131], v[6:7], v[8:11]\n" \
"v_mfma_f32_16x16x16_bf16 v[12:15], v[138:139], v[6:7], v[12:15]\nv_mfma_f32_16x16x16_bf16 v[16:19], v[146:147], v[6:7], v[16:19]\nv_mfma_f32_16x16x16_bf16 v[20:23], v[26:27], v[6:7], v[20:23]\ns_waitcnt vmcnt(24)\nds_write_b128 v1, v[88:91] offset:0\nds_write_b128 v1, v[84:87] offset:1024\nds_write_b128 v1, v[80:83] offset:2048\nds_write_b128 v1, v[76:79] offset:3072\n" \
"s_barrier\nds_read_b64_tr_b16 v[132:133], v94 offset:8192\nds_read_b64_tr_b16 v[134:135], v94 offset:8704\nds_read_b64_tr_b16 v[140:141], v94 offset:8200\nds_read_b64_tr_b16 v[142:143], v94 offset:8712\nds_read_b64_tr_b16 v[148:149], v94 offset:32768\nds_read_b64_tr_b16 v[150:151], v94 offset:33280\nds_read_b64_tr_b16 v[76:77], v94 offset:32776\n" \
"ds_read_b64_tr_b16 v[78:79], v94 offset:33288\ns_waitcnt lgkmcnt(0)\nv_mfma_f32_16x16x16_bf16 v[8:11], v[132:133], v[6:7], v[8:11]\nv_mfma_f32_16x16x16_bf16 v[12:15], v[140:141], v[6:7], v[12:15]\nv_mfma_f32_16x16x16_bf16 v[16:19], v[148:149], v[6:7], v[16:19]\nv_mfma_f32_16x16x16_bf16 v[20:23], v[76:77], v[6:7], v[20:23]\nv_mfma_f32_16x16x16_bf16 v[8:11], v[134:135], v[6:7], v[8:11]\nv_mfma_f32_16x16x16_bf16 v[12:15], v[142:143], v[6:7], v[12:15]\n" \
"v_mfma_f32_16x16x16_bf16 v[16:19], v[150:151], v[6:7], v[16:19]\nv_mfma_f32_16x16x16_bf16 v[20:23], v[78:79], v[6:7], v[20:23]\ns_waitcnt vmcnt(20)\nds_write_b128 v1, v[72:75] offset:4096\nds_write_b128 v1, v[68:71] offset:5120\nds_write_b128 v1, v[64:67] offset:6144\nds_write_b128 v1, v[60:63] offset:7168\ns_barrier\n" \
"ds_read_b64_tr_b16 v[184:185], v94 offset:12288\nds_read_b64_tr_b16 v[186:187], v94 offset:12800\nds_read_b64_tr_b16 v[60:61], v94 offset:12296\nds_read_b64_tr_b16 v[62:63], v94 offset:12808\nds_read_b64_tr_b16 v[192:193], v94 offset:36864\nds_read_b64_tr_b16 v[194:195], v94 offset:37376\nds_read_b64_tr_b16 v[200:201], v94 offset:36872\nds_read_b64_tr_b16 v[202:203], v94 offset:37384\n" \
"s_waitcnt lgkmcnt(0)\nv_mfma_f32_16x16x16_bf16 v[8:11], v[184:185], v[6:7], v[8:11]\nv_mfma_f32_16x16x16_bf16 v[12:15], v[60:61], v[6:7], v[12:15]\nv_mfma_f32_16x16x16_bf16 v[16:19], v[192:193], v[6:7], v[16:19]\nv_mfma_f32_16x16x16_bf16 v[20:23], v[200:201], v[6:7], v[20:23]\nv_mfma_f32_16x16x16_bf16 v[8:11], v[186:187], v[6:7], v[8:11]\nv_mfma_f32_16x16x16_bf16 v[12:15], v[62:63], v[6:7], v[12:15]\nv_mfma_f32_16x16x16_bf16 v[16:19], v[194:195], v[6:7], v[16:19]\n" \
"v_mfma_f32_16x16x16_bf16 v[20:23], v[202:203], v[6:7], v[20:23]\ns_waitcnt vmcnt(16)\nds_write_b128 v1, v[56:59] offset:8192\nds_write_b128 v1, v[52:55] offset:9216\nds_write_b128 v1, v[48:51] offset:10240\nds_write_b128 v1, v[44:47] offset:11264\ns_barrier\nds_read_b64_tr_b16 v[188:189], v94 offset:16384\n" \
"ds_read_b64_tr_b16 v[190:191], v94 offset:16896\nds_read_b64_tr_b16 v[44:45], v94 offset:16392\nds_read_b64_tr_b16 v[46:47], v94 offset:16904\nds_read_b64_tr_b16 v[196:197], v94 offset:40960\nds_read_b64_tr_b16 v[198:199], v94 offset:41472\nds_read_b64_tr_b16 v[204:205], v94 offset:40968\nds_read_b64_tr_b16 v[206:207], v94 offset:41480\ns_waitcnt lgkmcnt(0)\n" \
"v_mfma_f32_16x16x16_bf16 v[8:11], v[188:189], v[6:7], v[8:11]\nv_mfma_f32_16x16x16_bf16 v[12:15], v[44:45], v[6:7], v[12:15]\nv_mfma_f32_16x16x16_bf16 v[16:19], v[196:197], v[6:7], v[16:19]\nv_mfma_f32_16x16x16_bf16 v[20:23], v[204:205], v[6:7], v[20:23]\nv_mfma_f32_16x16x16_bf16 v[8:11], v[190:191], v[6:7], v[8:11]\nv_mfma_f32_16x16x16_bf16 v[12:15], v[46:47], v[6:7], v[12:15]\nv_mfma_f32_16x16x16_bf16 v[16:19], v[198:199], v[6:7], v[16:19]\nv_mfma_f32_16x16x16_bf16 v[20:23], v[206:207], v[6:7], v[20:23]\n" \
"s_waitcnt vmcnt(12)\nds_write_b128 v1, v[40:43] offset:12288\nds_write_b128 v1, v[36:39] offset:13312\nds_write_b128 v1, v[32:35] offset:14336\nds_write_b128 v1, v[28:31] offset:15360\ns_barrier\nds_read_b64_tr_b16 v[208:209], v94 offset:20480\nds_read_b64_tr_b16 v[210:211], v94 offset:20992\n" \
"ds_read_b64_tr_b16 v[216:217], v94 offset:20488\nds_read_b64_tr_b16 v[218:219], v94 offset:21000\nds_read_b64_tr_b16 v[28:29], v94 offset:45056\nds_read_b64_tr_b16 v[30:31], v94 offset:45568\nds_read_b64_tr_b16 v[224:225], v94 offset:45064\nds_read_b64_tr_b16 v[226:227], v94 offset:45576\ns_waitcnt lgkmcnt(0)\nv_mfma_f32_16x16x16_bf16 v[8:11], v[208:209], v[6:7], v[8:11]\n" \
"v_mfma_f32_16x16x16_bf16 v[12:15], v[216:217], v[6:7], v[12:15]\nv_mfma_f32_16x16x16_bf16 v[16:19], v[28:29], v[6:7], v[16:19]\nv_mfma_f32_16x16x16_bf16 v[20:23], v[224:225], v[6:7], v[20:23]\nv_mfma_f32_16x16x16_bf16 v[8:11], v[210:211], v[6:7], v[8:11]\nv_mfma_f32_16x16x16_bf16 v[12:15], v[218:219], v[6:7], v[12:15]\nv_mfma_f32_16x16x16_bf16 v[16:19], v[30:31], v[6:7], v[16:19]\nv_mfma_f32_16x16x16_bf16 v[20:23], v[226:227], v[6:7], v[20:23]\ns_waitcnt vmcnt(8)\n" \
"ds_write_b128 v1, v[24:27] offset:16384\nds_write_b128 v1, v[76:79] offset:17408\nds_write_b128 v1, v[60:63] offset:18432\nds_write_b128 v1, v[44:47] offset:19456\ns_barrier\nds_read_b64_tr_b16 v[212:213], v93 offset:0\nds_read_b64_tr_b16 v[214:215], v93 offset:512\nds_read_b64_tr_b16 v[220:221], v93 offset:8\n" \
"ds_read_b64_tr_b16 v[222:223], v93 offset:520\nds_read_b64_tr_b16 v[44:45], v93 offset:12288\nds_read_b64_tr_b16 v[46:47], v93 offset:12800\nds_read_b64_tr_b16 v[228:229], v93 offset:12296\nds_read_b64_tr_b16 v[230:231], v93 offset:12808\ns_waitcnt lgkmcnt(0)\nv_mfma_f32_16x16x16_bf16 v[8:11], v[212:213], v[6:7], v[8:11]\nv_mfma_f32_16x16x16_bf16 v[12:15], v[220:221], v[6:7], v[12:15]\n" \
"v_mfma_f32_16x16x16_bf16 v[16:19], v[44:45], v[6:7], v[16:19]\nv_mfma_f32_16x16x16_bf16 v[20:23], v[228:229], v[6:7], v[20:23]\nv_mfma_f32_16x16x16_bf16 v[8:11], v[214:215], v[6:7], v[8:11]\nv_mfma_f32_16x16x16_bf16 v[12:15], v[222:223], v[6:7], v[12:15]\nv_mfma_f32_16x16x16_bf16 v[16:19], v[46:47], v[6:7], v[16:19]\nv_mfma_f32_16x16x16_bf16 v[20:23], v[230:231], v[6:7], v[20:23]\ns_waitcnt vmcnt(4)\nds_write_b128 v1, v[28:31] offset:20480\n" \
"ds_write_b128 v1, v[44:47] offset:21504\ns_barrier\nds_read_b64_tr_b16 v[232:233], v93 offset:4096\nds_read_b64_tr_b16 v[234:235], v93 offset:4608\nds_read_b64_tr_b16 v[240:241], v93 offset:4104\nds_read_b64_tr_b16 v[242:243], v93 offset:4616\nds_read_b64_tr_b16 v[248:249], v93 offset:16384\nds_read_b64_tr_b16 v[250:251], v93 offset:16896\n" \
"ds_read_b64_tr_b16 v[44:45], v93 offset:16392\nds_read_b64_tr_b16 v[46:47], v93 offset:16904\ns_waitcnt lgkmcnt(0)\nv_mfma_f32_16x16x16_bf16 v[8:11], v[232:233], v[6:7], v[8:11]\nv_mfma_f32_16x16x16_bf16 v[12:15], v[240:241], v[6:7], v[12:15]\nv_mfma_f32_16x16x16_bf16 v[16:19], v[248:249], v[6:7], v[16:19]\nv_mfma_f32_16x16x16_bf16 v[20:23], v[44:45], v[6:7], v[20:23]\nv_mfma_f32_16x16x16_bf16 v[8:11], v[234:235], v[6:7], v[8:11]\n" \
"v_mfma_f32_16x16x16_bf16 v[12:15], v[242:243], v[6:7], v[12:15]\nv_mfma_f32_16x16x16_bf16 v[16:19], v[250:251], v[6:7], v[16:19]\nv_mfma_f32_16x16x16_bf16 v[20:23], v[46:47], v[6:7], v[20:23]\ns_waitcnt vmcnt(0)\nds_write_b128 v1, v[44:47] offset:22528\ns_barrier\nds_read_b64_tr_b16 v[236:237], v93 offset:8192\nds_read_b64_tr_b16 v[238:239], v93 offset:8704\n" \
"ds_read_b64_tr_b16 v[244:245], v93 offset:8200\nds_read_b64_tr_b16 v[246:247], v93 offset:8712\nds_read_b64_tr_b16 v[252:253], v93 offset:20480\nds_read_b64_tr_b16 v[254:255], v93 offset:20992\nds_read_b64_tr_b16 v[44:45], v93 offset:20488\nds_read_b64_tr_b16 v[46:47], v93 offset:21000\ns_waitcnt lgkmcnt(0)\nv_mfma_f32_16x16x16_bf16 v[8:11], v[236:237], v[6:7], v[8:11]\n" \
"v_mfma_f32_16x16x16_bf16 v[12:15], v[244:245], v[6:7], v[12:15]\nv_mfma_f32_16x16x16_bf16 v[16:19], v[252:253], v[6:7], v[16:19]\nv_mfma_f32_16x16x16_bf16 v[20:23], v[44:45], v[6:7], v[20:23]\nv_mfma_f32_16x16x16_bf16 v[8:11], v[238:239], v[6:7], v[8:11]\nv_mfma_f32_16x16x16_bf16 v[12:15], v[246:247], v[6:7], v[12:15]\nv_mfma_f32_16x16x16_bf16 v[16:19], v[254:255], v[6:7], v[16:19]\nv_mfma_f32_16x16x16_bf16 v[20:23], v[46:47], v[6:7], v[20:23]\nds_write_b128 v1, v[44:47] offset:23552\n" \
"s_waitcnt lgkmcnt(0)\ns_barrier\ns_mov_b32 s86, %[xn0]\ns_mov_b32 s87, %[ho0]\ns_mov_b32 s88, %[fh0]\ns_mov_b32 s89, %[b1]\ns_mov_b32 s40, %[b0]\ns_mov_b32 s43, 0\n" \
"s_lshl_b32 s57, %[w], 12\ns_mov_b32 s58, 0xbeeff558\ns_mov_b32 s59, 0x409d30fc\ns_mov_b32 s56, -2147483648\n.Lgg_next%=:\ns_cmp_lg_u32 %[w], 0\ns_cbranch_scc1 .Lgg_w1%=\nbuffer_load_dword v56, v3, %[rs], 0 offen sc1\n" \
"s_waitcnt vmcnt(0)\ns_barrier\nbuffer_store_dword v90, off, %[rs], s56 sc1\ns_mov_b32 s56, -2147483648\n.Lgg_poll%=:\nv_cmp_gt_i32 vcc, s40, v56\ns_cbranch_vccz .Lgg_go%=\ns_cmp_ge_u32 s40, 62\n" \
"s_cbranch_scc1 .Lgg_ns1055%=\ns_sleep 6\n.Lgg_ns1055%=:\ns_sleep 1\nbuffer_load_dword v56, v3, %[rs], 0 offen sc1\ns_waitcnt vmcnt(0)\ns_add_u32 s43, s43, 1\ns_cmp_lt_u32 s43, 262144\n" \
"s_cbranch_scc1 .Lgg_poll%=\ns_branch .Lgg_abort%=\n.Lgg_w1%=:\ns_waitcnt vmcnt(0)\ns_barrier\n.Lgg_go%=:\ns_add_u32 s44, s40, 0\ns_lshl_b32 s44, s44, 17\n" \
"s_add_u32 s44, s44, s86\ns_lshr_b32 s45, s44, 8\ns_and_b32 s45, s45, 0x2000\ns_xor_b32 s41, s44, s45\ns_add_u32 s61, s41, %[xw]\ns_barrier\ns_add_u32 m0, s57, 0\ns_add_u32 s60, s61, 0\n" \
"buffer_load_dwordx4 v0, %[rs], s60 offen offset:0 sc1 lds\nbuffer_load_dwordx4 v0, %[rs], s60 offen offset:1024 sc1 lds\nbuffer_load_dwordx4 v0, %[rs], s60 offen offset:2048 sc1 lds\nbuffer_load_dwordx4 v0, %[rs], s60 offen offset:3072 sc1 lds\ns_add_u32 m0, s57, 16384\ns_add_u32 s60, s61, 32768\nbuffer_load_dwordx4 v0, %[rs], s60 offen offset:0 sc1 lds\nbuffer_load_dwordx4 v0, %[rs], s60 offen offset:1024 sc1 lds\n" \
"buffer_load_dwordx4 v0, %[rs], s60 offen offset:2048 sc1 lds\nbuffer_load_dwordx4 v0, %[rs], s60 offen offset:3072 sc1 lds\nbuffer_load_dwordx4 v[40:43], v5, %[rs], s41 offen offset:0 sc1\nbuffer_load_dwordx4 v[44:47], v5, %[rs], s41 offen offset:256 sc1\ns_lshl_b32 s44, s40, 19\ns_add_u32 s44, s44, s87\ns_lshr_b32 s45, s44, 8\ns_and_b32 s45, s45, 0x2000\n" \
"s_xor_b32 s55, s44, s45\ns_waitcnt vmcnt(6)\ns_barrier\nds_read_b128 v[56:59], v2 offset:0\nds_read_b128 v[60:63], v2 offset:2048\nds_read_b128 v[64:67], v2 offset:4096\ns_waitcnt lgkmcnt(2)\nv_mfma_f32_16x16x32_bf16 v[24:27], a[0:3], v[56:59], 0\n" \
"ds_read_b128 v[68:71], v2 offset:6144\nv_mfma_f32_16x16x32_bf16 v[28:31], a[8:11], v[56:59], 0\nds_read_b128 v[72:75], v1 offset:0\nbuffer_load_dwordx4 v[48:51], v5, %[rs], s41 offen offset:512 sc1\nv_mfma_f32_16x16x32_bf16 v[32:35], a[16:19], v[56:59], 0\nds_read_b128 v[76:79], v1 offset:1024\nv_mfma_f32_16x16x32_bf16 v[36:39], a[24:27], v[56:59], 0\ns_waitcnt lgkmcnt(4)\n" \
"v_mfma_f32_16x16x32_bf16 v[24:27], a[4:7], v[60:63], v[24:27]\nds_read_b128 v[56:59], v2 offset:8192\nv_mfma_f32_16x16x32_bf16 v[28:31], a[12:15], v[60:63], v[28:31]\nds_read_b128 v[80:83], v1 offset:2048\nbuffer_load_dwordx4 v[52:55], v5, %[rs], s41 offen offset:768 sc1\nv_mfma_f32_16x16x32_bf16 v[32:35], a[20:23], v[60:63], v[32:35]\nv_mfma_f32_16x16x32_bf16 v[36:39], a[28:31], v[60:63], v[36:39]\ns_waitcnt lgkmcnt(5)\n" \
"v_mfma_f32_16x16x32_bf16 v[24:27], a[32:35], v[64:67], v[24:27]\ns_waitcnt lgkmcnt(3)\ns_add_u32 m0, s57, 32768\nds_read_b128 v[60:63], v2 offset:10240\nv_mfma_f32_16x16x32_bf16 v[28:31], v[72:75], v[64:67], v[28:31]\ns_add_u32 s60, s61, 65536\nds_read_b128 v[72:75], v1 offset:3072\nbuffer_load_dwordx4 v0, %[rs], s60 offen offset:0 sc1 lds\n" \
"v_mfma_f32_16x16x32_bf16 v[32:35], a[40:43], v[64:67], v[32:35]\nv_mfma_f32_16x16x32_bf16 v[36:39], a[48:51], v[64:67], v[36:39]\nv_mfma_f32_16x16x32_bf16 v[24:27], a[36:39], v[68:71], v[24:27]\ns_waitcnt lgkmcnt(4)\nds_read_b128 v[64:67], v2 offset:12288\nv_mfma_f32_16x16x32_bf16 v[28:31], v[76:79], v[68:71], v[28:31]\nbuffer_load_dwordx4 v0, %[rs], s60 offen offset:1024 sc1 lds\nv_mfma_f32_16x16x32_bf16 v[32:35], a[44:47], v[68:71], v[32:35]\n" \
"v_mfma_f32_16x16x32_bf16 v[36:39], a[52:55], v[68:71], v[36:39]\ns_waitcnt lgkmcnt(4)\nds_read_b128 v[76:79], v1 offset:4096\nv_mfma_f32_16x16x32_bf16 v[24:27], a[56:59], v[56:59], v[24:27]\nds_read_b128 v[68:71], v2 offset:14336\nv_mfma_f32_16x16x32_bf16 v[28:31], a[64:67], v[56:59], v[28:31]\ns_waitcnt lgkmcnt(5)\nbuffer_load_dwordx4 v0, %[rs], s60 offen offset:2048 sc1 lds\n" \
"v_mfma_f32_16x16x32_bf16 v[32:35], v[80:83], v[56:59], v[32:35]\nv_mfma_f32_16x16x32_bf16 v[36:39], a[72:75], v[56:59], v[36:39]\ns_waitcnt lgkmcnt(4)\nds_read_b128 v[80:83], v1 offset:5120\nv_mfma_f32_16x16x32_bf16 v[24:27], a[60:63], v[60:63], v[24:27]\ns_waitcnt vmcnt(7)\ns_barrier\nds_read_b128 v[56:59], v2 offset:16384\n" \
"v_mfma_f32_16x16x32_bf16 v[28:31], a[68:71], v[60:63], v[28:31]\ns_waitcnt lgkmcnt(5)\nbuffer_load_dwordx4 v0, %[rs], s60 offen offset:3072 sc1 lds\nv_mfma_f32_16x16x32_bf16 v[32:35], v[72:75], v[60:63], v[32:35]\nv_mfma_f32_16x16x32_bf16 v[36:39], a[76:79], v[60:63], v[36:39]\ns_waitcnt lgkmcnt(4)\nv_mfma_f32_16x16x32_bf16 v[24:27], a[80:83], v[64:67], v[24:27]\ns_add_u32 m0, s57, 49152\n" \
"ds_read_b128 v[60:63], v2 offset:18432\nv_mfma_f32_16x16x32_bf16 v[28:31], a[88:91], v[64:67], v[28:31]\ns_add_u32 s60, s61, 98304\nbuffer_load_dwordx4 v0, %[rs], s60 offen offset:0 sc1 lds\nv_mfma_f32_16x16x32_bf16 v[32:35], a[96:99], v[64:67], v[32:35]\ns_waitcnt lgkmcnt(4)\nv_mfma_f32_16x16x32_bf16 v[36:39], v[76:79], v[64:67], v[36:39]\ns_waitcnt lgkmcnt(3)\n" \
"v_mfma_f32_16x16x32_bf16 v[24:27], a[84:87], v[68:71], v[24:27]\nds_read_b128 v[64:67], v2 offset:20480\nv_mfma_f32_16x16x32_bf16 v[28:31], a[92:95], v[68:71], v[28:31]\nds_read_b128 v[72:75], v1 offset:6144\nbuffer_load_dwordx4 v0, %[rs], s60 offen offset:1024 sc1 lds\nv_mfma_f32_16x16x32_bf16 v[32:35], a[100:103], v[68:71], v[32:35]\ns_waitcnt lgkmcnt(4)\nv_mfma_f32_16x16x32_bf16 v[36:39], v[80:83], v[68:71], v[36:39]\n" \
"s_waitcnt lgkmcnt(3)\nv_mfma_f32_16x16x32_bf16 v[24:27], a[104:107], v[56:59], v[24:27]\nds_read_b128 v[68:71], v2 offset:22528\nv_mfma_f32_16x16x32_bf16 v[28:31], a[112:115], v[56:59], v[28:31]\nds_read_b128 v[76:79], v1 offset:7168\nbuffer_load_dwordx4 v0, %[rs], s60 offen offset:2048 sc1 lds\nv_mfma_f32_16x16x32_bf16 v[32:35], a[120:123], v[56:59], v[32:35]\nv_mfma_f32_16x16x32_bf16 v[36:39], a[128:131], v[56:59], v[36:39]\n" \
"s_waitcnt lgkmcnt(4)\nv_mfma_f32_16x16x32_bf16 v[24:27], a[108:111], v[60:63], v[24:27]\nds_read_b128 v[56:59], v2 offset:24576\nv_mfma_f32_16x16x32_bf16 v[28:31], a[116:119], v[60:63], v[28:31]\nds_read_b128 v[80:83], v1 offset:8192\nbuffer_load_dwordx4 v0, %[rs], s60 offen offset:3072 sc1 lds\nv_mfma_f32_16x16x32_bf16 v[32:35], a[124:127], v[60:63], v[32:35]\nv_mfma_f32_16x16x32_bf16 v[36:39], a[132:135], v[60:63], v[36:39]\n" \
"s_waitcnt lgkmcnt(5)\nv_mfma_f32_16x16x32_bf16 v[24:27], a[136:139], v[64:67], v[24:27]\ns_waitcnt vmcnt(8)\ns_waitcnt lgkmcnt(4)\nds_read_b128 v[60:63], v2 offset:26624\nv_add_f32 v84, v40, v44\nv_mfma_f32_16x16x32_bf16 v[28:31], v[72:75], v[64:67], v[28:31]\nds_read_b128 v[72:75], v1 offset:9216\n" \
"v_add_f32 v85, v48, v52\nv_mfma_f32_16x16x32_bf16 v[32:35], a[144:147], v[64:67], v[32:35]\nv_add_f32 v84, v84, v85\nv_mov_b32 v85, v84\nv_mfma_f32_16x16x32_bf16 v[36:39], a[152:155], v[64:67], v[36:39]\ns_waitcnt lgkmcnt(5)\nv_permlane16_swap_b32 v84, v85\nv_mfma_f32_16x16x32_bf16 v[24:27], a[140:143], v[68:71], v[24:27]\n" \
"s_waitcnt lgkmcnt(4)\nds_read_b128 v[64:67], v2 offset:28672\nv_add_f32 v84, v84, v85\nv_mfma_f32_16x16x32_bf16 v[28:31], v[76:79], v[68:71], v[28:31]\nv_mov_b32 v85, v84\nv_mfma_f32_16x16x32_bf16 v[32:35], a[148:151], v[68:71], v[32:35]\ns_nop 0\nv_permlane32_swap_b32 v84, v85\n" \
"v_mfma_f32_16x16x32_bf16 v[36:39], a[156:159], v[68:71], v[36:39]\ns_waitcnt lgkmcnt(4)\nds_read_b128 v[76:79], v1 offset:10240\nv_add_f32 v84, v84, v85\nv_mfma_f32_16x16x32_bf16 v[24:27], a[160:163], v[56:59], v[24:27]\nds_read_b128 v[68:71], v2 offset:30720\nv_mul_f32 v84, 0xbd800000, v84\nv_mfma_f32_16x16x32_bf16 v[28:31], a[168:171], v[56:59], v[28:31]\n" \
"s_waitcnt lgkmcnt(5)\nv_add_f32 v40, v40, v84\nv_mul_f32 v40, 0x41000000, v40\nv_mfma_f32_16x16x32_bf16 v[32:35], v[80:83], v[56:59], v[32:35]\nv_fma_f32 v41, v40, v40, v41\nv_add_f32 v44, v44, v84\nv_mfma_f32_16x16x32_bf16 v[36:39], a[176:179], v[56:59], v[36:39]\ns_waitcnt lgkmcnt(4)\n" \
"ds_read_b128 v[80:83], v1 offset:11264\nv_mul_f32 v44, 0x41000000, v44\nv_mfma_f32_16x16x32_bf16 v[24:27], a[164:167], v[60:63], v[24:27]\ns_waitcnt vmcnt(4)\ns_barrier\nds_read_b128 v[56:59], v2 offset:32768\nv_mfma_f32_16x16x32_bf16 v[28:31], a[172:175], v[60:63], v[28:31]\ns_waitcnt lgkmcnt(5)\n" \
"v_fma_f32 v45, v44, v44, v45\nv_add_f32 v48, v48, v84\nv_mfma_f32_16x16x32_bf16 v[32:35], v[72:75], v[60:63], v[32:35]\nv_mul_f32 v48, 0x41000000, v48\nv_fma_f32 v49, v48, v48, v49\nv_mfma_f32_16x16x32_bf16 v[36:39], a[180:183], v[60:63], v[36:39]\ns_waitcnt lgkmcnt(4)\nv_add_f32 v52, v52, v84\n" \
"v_mul_f32 v52, 0x41000000, v52\nv_mfma_f32_16x16x32_bf16 v[24:27], a[184:187], v[64:67], v[24:27]\nds_read_b128 v[60:63], v2 offset:34816\nv_fma_f32 v53, v52, v52, v53\nv_mfma_f32_16x16x32_bf16 v[28:31], a[192:195], v[64:67], v[28:31]\nv_add_f32 v85, v41, v45\nv_add_f32 v86, v49, v53\nv_mfma_f32_16x16x32_bf16 v[32:35], a[200:203], v[64:67], v[32:35]\n" \
"s_waitcnt lgkmcnt(4)\nv_add_f32 v85, v85, v86\nv_mov_b32 v86, v85\nv_mfma_f32_16x16x32_bf16 v[36:39], v[76:79], v[64:67], v[36:39]\ns_waitcnt lgkmcnt(3)\nv_permlane16_swap_b32 v85, v86\nv_mfma_f32_16x16x32_bf16 v[24:27], a[188:191], v[68:71], v[24:27]\nds_read_b128 v[64:67], v2 offset:36864\n" \
"v_add_f32 v85, v85, v86\nv_mfma_f32_16x16x32_bf16 v[28:31], a[196:199], v[68:71], v[28:31]\nds_read_b128 v[72:75], v1 offset:12288\nv_mov_b32 v86, v85\nv_mfma_f32_16x16x32_bf16 v[32:35], a[204:207], v[68:71], v[32:35]\ns_waitcnt lgkmcnt(4)\nv_permlane32_swap_b32 v85, v86\nv_mfma_f32_16x16x32_bf16 v[36:39], v[80:83], v[68:71], v[36:39]\n" \
"s_waitcnt lgkmcnt(3)\nv_add_f32 v85, v85, v86\nv_mul_f32 v85, 0x3a800000, v85\nv_mfma_f32_16x16x32_bf16 v[24:27], a[208:211], v[56:59], v[24:27]\nds_read_b128 v[68:71], v2 offset:38912\nv_cvt_pk_bf16_f32 v85, v85, v85\nv_mfma_f32_16x16x32_bf16 v[28:31], a[216:219], v[56:59], v[28:31]\nds_read_b128 v[76:79], v1 offset:13312\n" \
"v_and_b32 v85, 0xffff0000, v85\nv_mfma_f32_16x16x32_bf16 v[32:35], a[224:227], v[56:59], v[32:35]\nv_add_f32 v85, 0x3727c5ac, v85\nv_cvt_pk_bf16_f32 v85, v85, v85\nv_mfma_f32_16x16x32_bf16 v[36:39], a[232:235], v[56:59], v[36:39]\ns_waitcnt lgkmcnt(4)\nv_and_b32 v85, 0xffff0000, v85\nv_cvt_pk_bf16_f32 v84, v84, v84\n" \
"v_mfma_f32_16x16x32_bf16 v[24:27], a[212:215], v[60:63], v[24:27]\nds_read_b128 v[56:59], v2 offset:40960\nv_and_b32 v84, 0xffff0000, v84\nv_mfma_f32_16x16x32_bf16 v[28:31], a[220:223], v[60:63], v[28:31]\nds_read_b128 v[80:83], v1 offset:14336\nv_mfma_f32_16x16x32_bf16 v[32:35], a[228:231], v[60:63], v[32:35]\nv_sqrt_f32 v85, v85\nv_mfma_f32_16x16x32_bf16 v[36:39], a[236:239], v[60:63], v[36:39]\n" \
"s_waitcnt lgkmcnt(5)\nv_cvt_pk_bf16_f32 v85, v85, v85\nv_and_b32 v85, 0xffff0000, v85\nv_mfma_f32_16x16x32_bf16 v[24:27], a[240:243], v[64:67], v[24:27]\ns_waitcnt lgkmcnt(4)\nds_read_b128 v[60:63], v2 offset:43008\nv_mul_f32 v86, s58, v85\nv_mfma_f32_16x16x32_bf16 v[28:31], v[72:75], v[64:67], v[28:31]\n" \
"ds_read_b128 v[72:75], v1 offset:15360\nv_add_f32 v87, v42, v46\nv_mfma_f32_16x16x32_bf16 v[32:35], a[248:251], v[64:67], v[32:35]\nv_rcp_f32 v85, v85\nv_mfma_f32_16x16x32_bf16 v[36:39], v[96:99], v[64:67], v[36:39]\ns_waitcnt lgkmcnt(5)\nv_mul_f32 v85, s58, v85\nv_add_f32 v88, v50, v54\n" \
"v_mfma_f32_16x16x32_bf16 v[24:27], a[244:247], v[68:71], v[24:27]\ns_waitcnt lgkmcnt(4)\nds_read_b128 v[64:67], v2 offset:45056\nv_add_f32 v87, v87, v88\nv_mfma_f32_16x16x32_bf16 v[28:31], v[76:79], v[68:71], v[28:31]\nv_mov_b32 v88, v87\nv_mfma_f32_16x16x32_bf16 v[32:35], a[252:255], v[68:71], v[32:35]\ns_nop 0\n" \
"v_permlane16_swap_b32 v87, v88\nv_mfma_f32_16x16x32_bf16 v[36:39], v[100:103], v[68:71], v[36:39]\ns_waitcnt lgkmcnt(4)\nds_read_b128 v[76:79], v1 offset:16384\nv_add_f32 v87, v87, v88\nv_mfma_f32_16x16x32_bf16 v[24:27], v[104:107], v[56:59], v[24:27]\nds_read_b128 v[68:71], v2 offset:47104\nv_mov_b32 v88, v87\n" \
"v_mfma_f32_16x16x32_bf16 v[28:31], v[112:115], v[56:59], v[28:31]\ns_waitcnt lgkmcnt(5)\nv_permlane32_swap_b32 v87, v88\nv_mfma_f32_16x16x32_bf16 v[32:35], v[80:83], v[56:59], v[32:35]\nv_add_f32 v87, v87, v88\nv_mul_f32 v87, 0xbd800000, v87\nv_mfma_f32_16x16x32_bf16 v[36:39], v[120:123], v[56:59], v[36:39]\ns_waitcnt lgkmcnt(4)\n" \
"ds_read_b128 v[80:83], v1 offset:17408\nv_add_f32 v42, v42, v87\nv_mfma_f32_16x16x32_bf16 v[24:27], v[108:111], v[60:63], v[24:27]\ns_waitcnt vmcnt(0)\ns_barrier\nds_read_b128 v[56:59], v2 offset:49152\nv_mfma_f32_16x16x32_bf16 v[28:31], v[116:119], v[60:63], v[28:31]\ns_waitcnt lgkmcnt(5)\n" \
"v_mul_f32 v42, 0x41000000, v42\nv_fma_f32 v43, v42, v42, v43\nv_mfma_f32_16x16x32_bf16 v[32:35], v[72:75], v[60:63], v[32:35]\nv_add_f32 v46, v46, v87\nv_mul_f32 v46, 0x41000000, v46\nv_mfma_f32_16x16x32_bf16 v[36:39], v[124:127], v[60:63], v[36:39]\ns_waitcnt lgkmcnt(4)\nv_fma_f32 v47, v46, v46, v47\n" \
"v_add_f32 v50, v50, v87\nv_mfma_f32_16x16x32_bf16 v[24:27], v[128:131], v[64:67], v[24:27]\nds_read_b128 v[60:63], v2 offset:51200\nv_mul_f32 v50, 0x41000000, v50\nv_mfma_f32_16x16x32_bf16 v[28:31], v[136:139], v[64:67], v[28:31]\nv_fma_f32 v51, v50, v50, v51\nv_add_f32 v54, v54, v87\nv_mfma_f32_16x16x32_bf16 v[32:35], v[144:147], v[64:67], v[32:35]\n" \
"s_waitcnt lgkmcnt(4)\nv_mul_f32 v54, 0x41000000, v54\nv_fma_f32 v55, v54, v54, v55\nv_mfma_f32_16x16x32_bf16 v[36:39], v[76:79], v[64:67], v[36:39]\ns_waitcnt lgkmcnt(3)\nv_add_f32 v88, v43, v47\nv_add_f32 v89, v51, v55\nv_mfma_f32_16x16x32_bf16 v[24:27], v[132:135], v[68:71], v[24:27]\n" \
"ds_read_b128 v[64:67], v2 offset:53248\nv_add_f32 v88, v88, v89\nv_mfma_f32_16x16x32_bf16 v[28:31], v[140:143], v[68:71], v[28:31]\nds_read_b128 v[72:75], v1 offset:18432\nv_mov_b32 v89, v88\nv_mfma_f32_16x16x32_bf16 v[32:35], v[148:151], v[68:71], v[32:35]\ns_waitcnt lgkmcnt(4)\nv_permlane16_swap_b32 v88, v89\n" \
"v_mfma_f32_16x16x32_bf16 v[36:39], v[80:83], v[68:71], v[36:39]\ns_waitcnt lgkmcnt(3)\nv_add_f32 v88, v88, v89\nv_mov_b32 v89, v88\nv_mfma_f32_16x16x32_bf16 v[24:27], v[152:155], v[56:59], v[24:27]\nds_read_b128 v[68:71], v2 offset:55296\nv_mfma_f32_16x16x32_bf16 v[28:31], v[160:163], v[56:59], v[28:31]\nds_read_b128 v[76:79], v1 offset:19456\n" \
"v_mfma_f32_16x16x32_bf16 v[32:35], v[168:171], v[56:59], v[32:35]\nv_permlane32_swap_b32 v88, v89\nv_mfma_f32_16x16x32_bf16 v[36:39], v[176:179], v[56:59], v[36:39]\ns_waitcnt lgkmcnt(4)\nv_add_f32 v88, v88, v89\nv_mul_f32 v88, 0x3a800000, v88\nv_mfma_f32_16x16x32_bf16 v[24:27], v[156:159], v[60:63], v[24:27]\nds_read_b128 v[56:59], v2 offset:57344\n" \
"v_cvt_pk_bf16_f32 v88, v88, v88\nv_mfma_f32_16x16x32_bf16 v[28:31], v[164:167], v[60:63], v[28:31]\nds_read_b128 v[80:83], v1 offset:20480\nv_and_b32 v88, 0xffff0000, v88\nv_mfma_f32_16x16x32_bf16 v[32:35], v[172:175], v[60:63], v[32:35]\nv_add_f32 v88, 0x3727c5ac, v88\nv_cvt_pk_bf16_f32 v88, v88, v88\nv_mfma_f32_16x16x32_bf16 v[36:39], v[180:183], v[60:63], v[36:39]\n" \
"s_waitcnt lgkmcnt(5)\nv_and_b32 v88, 0xffff0000, v88\nv_cvt_pk_bf16_f32 v87, v87, v87\nv_mfma_f32_16x16x32_bf16 v[24:27], v[184:187], v[64:67], v[24:27]\ns_waitcnt lgkmcnt(4)\nds_read_b128 v[60:63], v2 offset:59392\nv_and_b32 v87, 0xffff0000, v87\nv_mfma_f32_16x16x32_bf16 v[28:31], v[72:75], v[64:67], v[28:31]\n" \
"ds_read_b128 v[72:75], v1 offset:21504\nv_mfma_f32_16x16x32_bf16 v[32:35], v[192:195], v[64:67], v[32:35]\nv_sqrt_f32 v88, v88\nv_mfma_f32_16x16x32_bf16 v[36:39], v[200:203], v[64:67], v[36:39]\ns_waitcnt lgkmcnt(5)\nv_cvt_pk_bf16_f32 v88, v88, v88\nv_and_b32 v88, 0xffff0000, v88\nv_mfma_f32_16x16x32_bf16 v[24:27], v[188:191], v[68:71], v[24:27]\n" \
"s_waitcnt lgkmcnt(4)\nds_read_b128 v[64:67], v2 offset:61440\nv_mul_f32 v89, s58, v88\nv_mfma_f32_16x16x32_bf16 v[28:31], v[76:79], v[68:71], v[28:31]\nv_rcp_f32 v88, v88\nv_mfma_f32_16x16x32_bf16 v[32:35], v[196:199], v[68:71], v[32:35]\nv_mul_f32 v88, s58, v88\nv_mfma_f32_16x16x32_bf16 v[36:39], v[204:207], v[68:71], v[36:39]\n" \
"s_waitcnt lgkmcnt(4)\nds_read_b128 v[76:79], v1 offset:22528\nv_mfma_f32_16x16x32_bf16 v[24:27], v[208:211], v[56:59], v[24:27]\nds_read_b128 v[68:71], v2 offset:63488\nv_mfma_f32_16x16x32_bf16 v[28:31], v[216:219], v[56:59], v[28:31]\ns_waitcnt lgkmcnt(5)\nv_mfma_f32_16x16x32_bf16 v[32:35], v[80:83], v[56:59], v[32:35]\nv_mfma_f32_16x16x32_bf16 v[36:39], v[224:227], v[56:59], v[36:39]\n" \
"s_waitcnt lgkmcnt(4)\nds_read_b128 v[80:83], v1 offset:23552\nv_mfma_f32_16x16x32_bf16 v[24:27], v[212:215], v[60:63], v[24:27]\nds_read_b128 v[56:59], v2 offset:256\nv_mfma_f32_16x16x32_bf16 v[28:31], v[220:223], v[60:63], v[28:31]\ns_waitcnt lgkmcnt(5)\nv_mfma_f32_16x16x32_bf16 v[32:35], v[72:75], v[60:63], v[32:35]\nv_mfma_f32_16x16x32_bf16 v[36:39], v[228:231], v[60:63], v[36:39]\n" \
"s_waitcnt lgkmcnt(4)\nv_mfma_f32_16x16x32_bf16 v[24:27], v[232:235], v[64:67], v[24:27]\nds_read_b128 v[60:63], v2 offset:2304\nv_mfma_f32_16x16x32_bf16 v[28:31], v[240:243], v[64:67], v[28:31]\nv_mfma_f32_16x16x32_bf16 v[32:35], v[248:251], v[64:67], v[32:35]\ns_waitcnt lgkmcnt(4)\nv_mfma_f32_16x16x32_bf16 v[36:39], v[76:79], v[64:67], v[36:39]\ns_waitcnt lgkmcnt(3)\n" \
"v_mfma_f32_16x16x32_bf16 v[24:27], v[236:239], v[68:71], v[24:27]\nds_read_b128 v[64:67], v2 offset:4352\nv_mfma_f32_16x16x32_bf16 v[28:31], v[244:247], v[68:71], v[28:31]\nds_read_b128 v[72:75], v1 offset:0\nv_mfma_f32_16x16x32_bf16 v[32:35], v[252:255], v[68:71], v[32:35]\ns_waitcnt lgkmcnt(4)\nv_mfma_f32_16x16x32_bf16 v[36:39], v[80:83], v[68:71], v[36:39]\ns_waitcnt lgkmcnt(3)\n" \
"v_mfma_f32_16x16x32_bf16 v[40:43], a[0:3], v[56:59], 0\nds_read_b128 v[68:71], v2 offset:6400\nv_mfma_f32_16x16x32_bf16 v[44:47], a[8:11], v[56:59], 0\ns_lshl_b32 s44, s40, 1\nds_read_b128 v[76:79], v1 offset:1024\nv_mov_b32 v90, s44\nv_mfma_f32_16x16x32_bf16 v[48:51], a[16:19], v[56:59], 0\nv_mfma_f32_16x16x32_bf16 v[52:55], a[24:27], v[56:59], 0\n" \
"s_waitcnt lgkmcnt(4)\nv_fma_f32 v24, v8, v84, v24\nv_fma_f32 v25, v9, v84, v25\nv_mfma_f32_16x16x32_bf16 v[40:43], a[4:7], v[60:63], v[40:43]\nds_read_b128 v[56:59], v2 offset:8448\nv_fma_f32 v32, v16, v84, v32\nv_mfma_f32_16x16x32_bf16 v[44:47], a[12:15], v[60:63], v[44:47]\nds_read_b128 v[80:83], v1 offset:2048\n" \
"v_fma_f32 v33, v17, v84, v33\nv_mfma_f32_16x16x32_bf16 v[48:51], a[20:23], v[60:63], v[48:51]\nv_mul_f32 v24, v24, v85\nv_mul_f32 v25, v25, v85\nv_mfma_f32_16x16x32_bf16 v[52:55], a[28:31], v[60:63], v[52:55]\ns_waitcnt lgkmcnt(5)\nv_fma_f32 v91, v24, v24, s59\nv_fma_f32 v92, v25, v25, s59\n" \
"v_mfma_f32_16x16x32_bf16 v[40:43], a[32:35], v[64:67], v[40:43]\ns_waitcnt lgkmcnt(4)\nds_read_b128 v[60:63], v2 offset:10496\nv_mul_f32 v91, v91, v24\nv_mfma_f32_16x16x32_bf16 v[44:47], v[72:75], v[64:67], v[44:47]\nds_read_b128 v[72:75], v1 offset:3072\nv_mul_f32 v92, v92, v25\nv_mfma_f32_16x16x32_bf16 v[48:51], a[40:43], v[64:67], v[48:51]\n" \
"v_exp_f32 v91, v91\nv_mfma_f32_16x16x32_bf16 v[52:55], a[48:51], v[64:67], v[52:55]\ns_waitcnt lgkmcnt(5)\nv_exp_f32 v92, v92\nv_mfma_f32_16x16x32_bf16 v[40:43], a[36:39], v[68:71], v[40:43]\ns_waitcnt lgkmcnt(4)\nds_read_b128 v[64:67], v2 offset:12544\nv_mul_f32 v24, v24, v32\n" \
"v_mfma_f32_16x16x32_bf16 v[44:47], v[76:79], v[68:71], v[44:47]\nv_mul_f32 v25, v25, v33\nv_fma_f32 v91, v91, v86, v86\nv_mfma_f32_16x16x32_bf16 v[48:51], a[44:47], v[68:71], v[48:51]\nv_fma_f32 v92, v92, v86, v86\nv_fma_f32 v26, v10, v84, v26\nv_mfma_f32_16x16x32_bf16 v[52:55], a[52:55], v[68:71], v[52:55]\ns_waitcnt lgkmcnt(4)\n" \
"ds_read_b128 v[76:79], v1 offset:4096\nv_fma_f32 v27, v11, v84, v27\nv_mfma_f32_16x16x32_bf16 v[40:43], a[56:59], v[56:59], v[40:43]\nds_read_b128 v[68:71], v2 offset:14592\nv_fma_f32 v34, v18, v84, v34\nv_mfma_f32_16x16x32_bf16 v[44:47], a[64:67], v[56:59], v[44:47]\ns_waitcnt lgkmcnt(5)\nv_rcp_f32 v91, v91\n" \
"v_mfma_f32_16x16x32_bf16 v[48:51], v[80:83], v[56:59], v[48:51]\nv_rcp_f32 v92, v92\nv_mfma_f32_16x16x32_bf16 v[52:55], a[72:75], v[56:59], v[52:55]\ns_waitcnt lgkmcnt(4)\nds_read_b128 v[80:83], v1 offset:5120\nv_mul_f32 v24, v24, v91\nv_mfma_f32_16x16x32_bf16 v[40:43], a[60:63], v[60:63], v[40:43]\nds_read_b128 v[56:59], v2 offset:16640\n" \
"v_mul_f32 v25, v25, v92\nv_mfma_f32_16x16x32_bf16 v[44:47], a[68:71], v[60:63], v[44:47]\ns_waitcnt lgkmcnt(5)\nv_cvt_pk_bf16_f32 v32, v24, v25\nv_fma_f32 v35, v19, v84, v35\nv_mfma_f32_16x16x32_bf16 v[48:51], v[72:75], v[60:63], v[48:51]\nv_mul_f32 v26, v26, v85\nv_mul_f32 v27, v27, v85\n" \
"v_mfma_f32_16x16x32_bf16 v[52:55], a[76:79], v[60:63], v[52:55]\ns_waitcnt lgkmcnt(4)\nv_fma_f32 v91, v26, v26, s59\nv_fma_f32 v92, v27, v27, s59\nv_mfma_f32_16x16x32_bf16 v[40:43], a[80:83], v[64:67], v[40:43]\nds_read_b128 v[60:63], v2 offset:18688\nv_mul_f32 v91, v91, v26\nv_mfma_f32_16x16x32_bf16 v[44:47], a[88:91], v[64:67], v[44:47]\n" \
"v_mul_f32 v92, v92, v27\nv_mul_f32 v26, v26, v34\nv_mfma_f32_16x16x32_bf16 v[48:51], a[96:99], v[64:67], v[48:51]\ns_waitcnt lgkmcnt(4)\nv_exp_f32 v91, v91\nv_mfma_f32_16x16x32_bf16 v[52:55], v[76:79], v[64:67], v[52:55]\ns_waitcnt lgkmcnt(3)\nv_exp_f32 v92, v92\n" \
"v_mfma_f32_16x16x32_bf16 v[40:43], a[84:87], v[68:71], v[40:43]\nds_read_b128 v[64:67], v2 offset:20736\nv_mul_f32 v27, v27, v35\nv_mfma_f32_16x16x32_bf16 v[44:47], a[92:95], v[68:71], v[44:47]\nds_read_b128 v[72:75], v1 offset:6144\nv_fma_f32 v91, v91, v86, v86\nv_mfma_f32_16x16x32_bf16 v[48:51], a[100:103], v[68:71], v[48:51]\ns_waitcnt lgkmcnt(4)\n" \
"v_fma_f32 v92, v92, v86, v86\nv_fma_f32 v28, v12, v84, v28\nv_mfma_f32_16x16x32_bf16 v[52:55], v[80:83], v[68:71], v[52:55]\ns_waitcnt lgkmcnt(3)\nv_rcp_f32 v91, v91\nv_mfma_f32_16x16x32_bf16 v[40:43], a[104:107], v[56:59], v[40:43]\nds_read_b128 v[68:71], v2 offset:22784\nv_mul_f32 v26, v26, v91\n" \
"v_mfma_f32_16x16x32_bf16 v[44:47], a[112:115], v[56:59], v[44:47]\nds_read_b128 v[76:79], v1 offset:7168\nv_fma_f32 v29, v13, v84, v29\nv_mfma_f32_16x16x32_bf16 v[48:51], a[120:123], v[56:59], v[48:51]\nv_rcp_f32 v92, v92\nv_mfma_f32_16x16x32_bf16 v[52:55], a[128:131], v[56:59], v[52:55]\ns_waitcnt lgkmcnt(4)\nv_mul_f32 v27, v27, v92\n" \
"v_cvt_pk_bf16_f32 v33, v26, v27\nv_mfma_f32_16x16x32_bf16 v[40:43], a[108:111], v[60:63], v[40:43]\nds_read_b128 v[56:59], v2 offset:24832\nv_fma_f32 v36, v20, v84, v36\nv_mfma_f32_16x16x32_bf16 v[44:47], a[116:119], v[60:63], v[44:47]\nds_read_b128 v[80:83], v1 offset:8192\nv_fma_f32 v37, v21, v84, v37\nv_mfma_f32_16x16x32_bf16 v[48:51], a[124:127], v[60:63], v[48:51]\n" \
"v_mul_f32 v28, v28, v85\nv_mul_f32 v29, v29, v85\nv_mfma_f32_16x16x32_bf16 v[52:55], a[132:135], v[60:63], v[52:55]\ns_waitcnt lgkmcnt(5)\nv_fma_f32 v91, v28, v28, s59\nv_fma_f32 v92, v29, v29, s59\nv_mfma_f32_16x16x32_bf16 v[40:43], a[136:139], v[64:67], v[40:43]\ns_waitcnt lgkmcnt(4)\n" \
"ds_read_b128 v[60:63], v2 offset:26880\nv_mul_f32 v91, v91, v28\nv_mfma_f32_16x16x32_bf16 v[44:47], v[72:75], v[64:67], v[44:47]\nds_read_b128 v[72:75], v1 offset:9216\nv_mul_f32 v92, v92, v29\nv_mfma_f32_16x16x32_bf16 v[48:51], a[144:147], v[64:67], v[48:51]\nv_exp_f32 v91, v91\nv_mfma_f32_16x16x32_bf16 v[52:55], a[152:155], v[64:67], v[52:55]\n" \
"s_waitcnt lgkmcnt(5)\nv_exp_f32 v92, v92\nv_mfma_f32_16x16x32_bf16 v[40:43], a[140:143], v[68:71], v[40:43]\ns_waitcnt lgkmcnt(4)\nds_read_b128 v[64:67], v2 offset:28928\nv_mul_f32 v28, v28, v36\nv_mfma_f32_16x16x32_bf16 v[44:47], v[76:79], v[68:71], v[44:47]\nv_mul_f32 v29, v29, v37\n" \
"v_fma_f32 v91, v91, v86, v86\nv_mfma_f32_16x16x32_bf16 v[48:51], a[148:151], v[68:71], v[48:51]\nv_fma_f32 v92, v92, v86, v86\nv_fma_f32 v30, v14, v84, v30\nv_mfma_f32_16x16x32_bf16 v[52:55], a[156:159], v[68:71], v[52:55]\ns_waitcnt lgkmcnt(4)\nds_read_b128 v[76:79], v1 offset:10240\nv_fma_f32 v31, v15, v84, v31\n" \
"v_mfma_f32_16x16x32_bf16 v[40:43], a[160:163], v[56:59], v[40:43]\nds_read_b128 v[68:71], v2 offset:30976\nv_fma_f32 v38, v22, v84, v38\nv_mfma_f32_16x16x32_bf16 v[44:47], a[168:171], v[56:59], v[44:47]\ns_waitcnt lgkmcnt(5)\nv_rcp_f32 v91, v91\nv_mfma_f32_16x16x32_bf16 v[48:51], v[80:83], v[56:59], v[48:51]\nv_rcp_f32 v92, v92\n" \
"v_mfma_f32_16x16x32_bf16 v[52:55], a[176:179], v[56:59], v[52:55]\ns_waitcnt lgkmcnt(4)\nds_read_b128 v[80:83], v1 offset:11264\nv_mul_f32 v28, v28, v91\nv_mfma_f32_16x16x32_bf16 v[40:43], a[164:167], v[60:63], v[40:43]\nds_read_b128 v[56:59], v2 offset:33024\nv_mul_f32 v29, v29, v92\nv_mfma_f32_16x16x32_bf16 v[44:47], a[172:175], v[60:63], v[44:47]\n" \
"s_waitcnt lgkmcnt(5)\nv_cvt_pk_bf16_f32 v34, v28, v29\nv_fma_f32 v39, v23, v84, v39\nv_mfma_f32_16x16x32_bf16 v[48:51], v[72:75], v[60:63], v[48:51]\nv_mul_f32 v30, v30, v85\nv_mul_f32 v31, v31, v85\nv_mfma_f32_16x16x32_bf16 v[52:55], a[180:183], v[60:63], v[52:55]\ns_waitcnt lgkmcnt(4)\n" \
"v_fma_f32 v91, v30, v30, s59\nv_fma_f32 v92, v31, v31, s59\nv_mfma_f32_16x16x32_bf16 v[40:43], a[184:187], v[64:67], v[40:43]\nds_read_b128 v[60:63], v2 offset:35072\nv_mul_f32 v91, v91, v30\nv_mfma_f32_16x16x32_bf16 v[44:47], a[192:195], v[64:67], v[44:47]\nv_mul_f32 v92, v92, v31\nv_mul_f32 v30, v30, v38\n" \
"v_mfma_f32_16x16x32_bf16 v[48:51], a[200:203], v[64:67], v[48:51]\ns_waitcnt lgkmcnt(4)\nv_exp_f32 v91, v91\nv_mfma_f32_16x16x32_bf16 v[52:55], v[76:79], v[64:67], v[52:55]\ns_waitcnt lgkmcnt(3)\nv_exp_f32 v92, v92\nv_mfma_f32_16x16x32_bf16 v[40:43], a[188:191], v[68:71], v[40:43]\nds_read_b128 v[64:67], v2 offset:37120\n" \
"v_mul_f32 v31, v31, v39\nv_mfma_f32_16x16x32_bf16 v[44:47], a[196:199], v[68:71], v[44:47]\nds_read_b128 v[72:75], v1 offset:12288\nv_fma_f32 v91, v91, v86, v86\nv_mfma_f32_16x16x32_bf16 v[48:51], a[204:207], v[68:71], v[48:51]\ns_waitcnt lgkmcnt(4)\nv_fma_f32 v92, v92, v86, v86\nv_mfma_f32_16x16x32_bf16 v[52:55], v[80:83], v[68:71], v[52:55]\n" \
"s_waitcnt lgkmcnt(3)\nv_rcp_f32 v91, v91\nv_mfma_f32_16x16x32_bf16 v[40:43], a[208:211], v[56:59], v[40:43]\nds_read_b128 v[68:71], v2 offset:39168\nv_mul_f32 v30, v30, v91\nv_mfma_f32_16x16x32_bf16 v[44:47], a[216:219], v[56:59], v[44:47]\nds_read_b128 v[76:79], v1 offset:13312\nv_mfma_f32_16x16x32_bf16 v[48:51], a[224:227], v[56:59], v[48:51]\n" \
"v_rcp_f32 v92, v92\nv_mfma_f32_16x16x32_bf16 v[52:55], a[232:235], v[56:59], v[52:55]\ns_waitcnt lgkmcnt(4)\nv_mul_f32 v31, v31, v92\nv_cvt_pk_bf16_f32 v35, v30, v31\nv_mfma_f32_16x16x32_bf16 v[40:43], a[212:215], v[60:63], v[40:43]\nds_read_b128 v[56:59], v2 offset:41216\nv_mfma_f32_16x16x32_bf16 v[44:47], a[220:223], v[60:63], v[44:47]\n" \
"ds_read_b128 v[80:83], v1 offset:14336\nv_mfma_f32_16x16x32_bf16 v[48:51], a[228:231], v[60:63], v[48:51]\nbuffer_store_dwordx4 v[32:35], v4, %[rs], s55 offen offset:0 sc1\nv_mfma_f32_16x16x32_bf16 v[52:55], a[236:239], v[60:63], v[52:55]\ns_waitcnt lgkmcnt(5)\nv_mfma_f32_16x16x32_bf16 v[40:43], a[240:243], v[64:67], v[40:43]\ns_waitcnt lgkmcnt(4)\nds_read_b128 v[60:63], v2 offset:43264\n" \
"v_mfma_f32_16x16x32_bf16 v[44:47], v[72:75], v[64:67], v[44:47]\nds_read_b128 v[72:75], v1 offset:15360\nv_mfma_f32_16x16x32_bf16 v[48:51], a[248:251], v[64:67], v[48:51]\nv_mfma_f32_16x16x32_bf16 v[52:55], v[96:99], v[64:67], v[52:55]\ns_waitcnt lgkmcnt(5)\nv_mfma_f32_16x16x32_bf16 v[40:43], a[244:247], v[68:71], v[40:43]\ns_waitcnt lgkmcnt(4)\nds_read_b128 v[64:67], v2 offset:45312\n" \
"v_mfma_f32_16x16x32_bf16 v[44:47], v[76:79], v[68:71], v[44:47]\nds_read_b128 v[24:27], v2 offset:47360\nv_mfma_f32_16x16x32_bf16 v[48:51], a[252:255], v[68:71], v[48:51]\nds_read_b128 v[28:31], v2 offset:49408\nv_mfma_f32_16x16x32_bf16 v[52:55], v[100:103], v[68:71], v[52:55]\ns_waitcnt lgkmcnt(6)\nv_mfma_f32_16x16x32_bf16 v[40:43], v[104:107], v[56:59], v[40:43]\ns_waitcnt lgkmcnt(5)\n" \
"ds_read_b128 v[68:71], v2 offset:51456\nv_mfma_f32_16x16x32_bf16 v[48:51], v[80:83], v[56:59], v[48:51]\ns_waitcnt lgkmcnt(5)\nv_mfma_f32_16x16x32_bf16 v[40:43], v[108:111], v[60:63], v[40:43]\ns_waitcnt lgkmcnt(4)\nds_read_b128 v[56:59], v2 offset:53504\nv_mfma_f32_16x16x32_bf16 v[48:51], v[72:75], v[60:63], v[48:51]\ns_waitcnt lgkmcnt(4)\n" \
"v_mfma_f32_16x16x32_bf16 v[40:43], v[128:131], v[64:67], v[40:43]\nds_read_b128 v[60:63], v2 offset:55552\nv_mfma_f32_16x16x32_bf16 v[48:51], v[144:147], v[64:67], v[48:51]\ns_waitcnt lgkmcnt(4)\nds_read_b128 v[76:79], v1 offset:20480\nv_mfma_f32_16x16x32_bf16 v[40:43], v[132:135], v[24:27], v[40:43]\nds_read_b128 v[64:67], v2 offset:57600\nv_mfma_f32_16x16x32_bf16 v[48:51], v[148:151], v[24:27], v[48:51]\n" \
"s_waitcnt lgkmcnt(5)\nds_read_b128 v[80:83], v1 offset:21504\nv_mfma_f32_16x16x32_bf16 v[40:43], v[152:155], v[28:31], v[40:43]\nds_read_b128 v[24:27], v2 offset:59648\nv_mfma_f32_16x16x32_bf16 v[48:51], v[168:171], v[28:31], v[48:51]\ns_waitcnt lgkmcnt(6)\nv_mfma_f32_16x16x32_bf16 v[40:43], v[156:159], v[68:71], v[40:43]\nds_read_b128 v[28:31], v2 offset:61696\n" \
"v_mfma_f32_16x16x32_bf16 v[48:51], v[172:175], v[68:71], v[48:51]\ns_waitcnt lgkmcnt(6)\nv_mfma_f32_16x16x32_bf16 v[40:43], v[184:187], v[56:59], v[40:43]\nds_read_b128 v[68:71], v2 offset:63744\nv_mfma_f32_16x16x32_bf16 v[48:51], v[192:195], v[56:59], v[48:51]\ns_waitcnt lgkmcnt(6)\nv_mfma_f32_16x16x32_bf16 v[40:43], v[188:191], v[60:63], v[40:43]\nds_read_b128 v[56:59], v2 offset:41216\n" \
"v_mfma_f32_16x16x32_bf16 v[48:51], v[196:199], v[60:63], v[48:51]\ns_waitcnt lgkmcnt(5)\nv_mfma_f32_16x16x32_bf16 v[40:43], v[208:211], v[64:67], v[40:43]\nds_read_b128 v[60:63], v2 offset:43264\nv_mfma_f32_16x16x32_bf16 v[48:51], v[76:79], v[64:67], v[48:51]\ns_waitcnt lgkmcnt(4)\nds_read_b128 v[72:75], v1 offset:16384\nv_mfma_f32_16x16x32_bf16 v[40:43], v[212:215], v[24:27], v[40:43]\n" \
"ds_read_b128 v[64:67], v2 offset:45312\nv_mfma_f32_16x16x32_bf16 v[48:51], v[80:83], v[24:27], v[48:51]\ns_waitcnt lgkmcnt(5)\nds_read_b128 v[76:79], v1 offset:17408\nv_mfma_f32_16x16x32_bf16 v[40:43], v[232:235], v[28:31], v[40:43]\nds_read_b128 v[24:27], v2 offset:47360\nv_mfma_f32_16x16x32_bf16 v[48:51], v[248:251], v[28:31], v[48:51]\ns_waitcnt vmcnt(0)\n" \
"s_barrier\ns_waitcnt lgkmcnt(6)\nbuffer_store_dword v90, off, %[rs], s88 sc1\nv_mfma_f32_16x16x32_bf16 v[40:43], v[236:239], v[68:71], v[40:43]\nds_read_b128 v[28:31], v2 offset:49408\nv_mfma_f32_16x16x32_bf16 v[48:51], v[252:255], v[68:71], v[48:51]\ns_waitcnt lgkmcnt(6)\nds_read_b128 v[80:83], v1 offset:18432\n" \
"v_mfma_f32_16x16x32_bf16 v[44:47], v[112:115], v[56:59], v[44:47]\nds_read_b128 v[68:71], v2 offset:51456\nv_mfma_f32_16x16x32_bf16 v[52:55], v[120:123], v[56:59], v[52:55]\ns_waitcnt lgkmcnt(7)\nv_fma_f32 v40, v8, v87, v40\nv_fma_f32 v41, v9, v87, v41\nv_mfma_f32_16x16x32_bf16 v[44:47], v[116:119], v[60:63], v[44:47]\nds_read_b128 v[56:59], v2 offset:53504\n" \
"v_fma_f32 v48, v16, v87, v48\nv_mfma_f32_16x16x32_bf16 v[52:55], v[124:127], v[60:63], v[52:55]\ns_waitcnt lgkmcnt(6)\nv_fma_f32 v49, v17, v87, v49\nv_mul_f32 v40, v40, v88\nv_mfma_f32_16x16x32_bf16 v[44:47], v[136:139], v[64:67], v[44:47]\nds_read_b128 v[60:63], v2 offset:55552\nv_mul_f32 v41, v41, v88\n" \
"v_mfma_f32_16x16x32_bf16 v[52:55], v[72:75], v[64:67], v[52:55]\ns_waitcnt lgkmcnt(5)\nds_read_b128 v[72:75], v1 offset:19456\nv_fma_f32 v91, v40, v40, s59\nv_mfma_f32_16x16x32_bf16 v[44:47], v[140:143], v[24:27], v[44:47]\nds_read_b128 v[64:67], v2 offset:57600\nv_fma_f32 v92, v41, v41, s59\nv_mfma_f32_16x16x32_bf16 v[52:55], v[76:79], v[24:27], v[52:55]\n" \
"s_waitcnt lgkmcnt(6)\nv_mul_f32 v91, v91, v40\nv_mul_f32 v92, v92, v41\nv_mfma_f32_16x16x32_bf16 v[44:47], v[160:163], v[28:31], v[44:47]\nds_read_b128 v[24:27], v2 offset:59648\nv_mul_f32 v40, v40, v48\nv_mfma_f32_16x16x32_bf16 v[52:55], v[176:179], v[28:31], v[52:55]\ns_waitcnt lgkmcnt(5)\n" \
"ds_read_b128 v[76:79], v1 offset:22528\nv_mul_f32 v41, v41, v49\nv_mfma_f32_16x16x32_bf16 v[44:47], v[164:167], v[68:71], v[44:47]\nds_read_b128 v[28:31], v2 offset:61696\nv_mfma_f32_16x16x32_bf16 v[52:55], v[180:183], v[68:71], v[52:55]\ns_waitcnt lgkmcnt(6)\nv_exp_f32 v91, v91\nv_mfma_f32_16x16x32_bf16 v[44:47], v[80:83], v[56:59], v[44:47]\n" \
"ds_read_b128 v[68:71], v2 offset:63744\nds_read_b128 v[80:83], v1 offset:23552\nv_mfma_f32_16x16x32_bf16 v[52:55], v[200:203], v[56:59], v[52:55]\ns_waitcnt lgkmcnt(6)\nv_exp_f32 v92, v92\nv_mfma_f32_16x16x32_bf16 v[44:47], v[72:75], v[60:63], v[44:47]\nv_fma_f32 v91, v91, v89, v89\nv_fma_f32 v92, v92, v89, v89\n" \
"v_mfma_f32_16x16x32_bf16 v[52:55], v[204:207], v[60:63], v[52:55]\ns_waitcnt lgkmcnt(5)\nv_rcp_f32 v91, v91\nv_mfma_f32_16x16x32_bf16 v[44:47], v[216:219], v[64:67], v[44:47]\nv_rcp_f32 v92, v92\nv_mfma_f32_16x16x32_bf16 v[52:55], v[224:227], v[64:67], v[52:55]\ns_waitcnt lgkmcnt(4)\nv_mul_f32 v40, v40, v91\n" \
"v_mul_f32 v41, v41, v92\nv_mfma_f32_16x16x32_bf16 v[44:47], v[220:223], v[24:27], v[44:47]\nv_cvt_pk_bf16_f32 v48, v40, v41\nv_fma_f32 v42, v10, v87, v42\nv_mfma_f32_16x16x32_bf16 v[52:55], v[228:231], v[24:27], v[52:55]\ns_waitcnt lgkmcnt(2)\nv_fma_f32 v43, v11, v87, v43\nv_fma_f32 v50, v18, v87, v50\n" \
"v_mfma_f32_16x16x32_bf16 v[44:47], v[240:243], v[28:31], v[44:47]\nv_fma_f32 v51, v19, v87, v51\nv_mul_f32 v42, v42, v88\nv_mfma_f32_16x16x32_bf16 v[52:55], v[76:79], v[28:31], v[52:55]\ns_waitcnt lgkmcnt(1)\nv_mul_f32 v43, v43, v88\nv_fma_f32 v91, v42, v42, s59\nv_mfma_f32_16x16x32_bf16 v[44:47], v[244:247], v[68:71], v[44:47]\n" \
"s_waitcnt lgkmcnt(0)\nv_fma_f32 v92, v43, v43, s59\nv_mul_f32 v91, v91, v42\nv_mfma_f32_16x16x32_bf16 v[52:55], v[80:83], v[68:71], v[52:55]\nv_mul_f32 v92, v92, v43\nv_mul_f32 v42, v42, v50\nv_exp_f32 v91, v91\nv_exp_f32 v92, v92\n" \
"v_mul_f32 v43, v43, v51\nv_fma_f32 v91, v91, v89, v89\nv_fma_f32 v92, v92, v89, v89\nv_rcp_f32 v91, v91\nv_rcp_f32 v92, v92\nv_mul_f32 v42, v42, v91\nv_mul_f32 v43, v43, v92\nv_cvt_pk_bf16_f32 v49, v42, v43\n" \
"v_fma_f32 v44, v12, v87, v44\nv_fma_f32 v45, v13, v87, v45\nv_fma_f32 v52, v20, v87, v52\nv_fma_f32 v53, v21, v87, v53\nv_mul_f32 v44, v44, v88\nv_mul_f32 v45, v45, v88\nv_fma_f32 v91, v44, v44, s59\nv_fma_f32 v92, v45, v45, s59\n" \
"v_mul_f32 v91, v91, v44\nv_mul_f32 v92, v92, v45\nv_exp_f32 v91, v91\nv_exp_f32 v92, v92\nv_mul_f32 v44, v44, v52\nv_mul_f32 v45, v45, v53\nv_fma_f32 v91, v91, v89, v89\nv_fma_f32 v92, v92, v89, v89\n" \
"v_rcp_f32 v91, v91\nv_rcp_f32 v92, v92\nv_mul_f32 v44, v44, v91\nv_mul_f32 v45, v45, v92\nv_cvt_pk_bf16_f32 v50, v44, v45\nv_fma_f32 v46, v14, v87, v46\nv_fma_f32 v47, v15, v87, v47\nv_fma_f32 v54, v22, v87, v54\n" \
"v_fma_f32 v55, v23, v87, v55\nv_mul_f32 v46, v46, v88\nv_mul_f32 v47, v47, v88\nv_fma_f32 v91, v46, v46, s59\nv_fma_f32 v92, v47, v47, s59\nv_mul_f32 v91, v91, v46\nv_mul_f32 v92, v92, v47\nv_exp_f32 v91, v91\n" \
"v_exp_f32 v92, v92\nv_mul_f32 v46, v46, v54\nv_mul_f32 v47, v47, v55\nv_fma_f32 v91, v91, v89, v89\nv_fma_f32 v92, v92, v89, v89\nv_rcp_f32 v91, v91\nv_rcp_f32 v92, v92\nv_mul_f32 v46, v46, v91\n" \
"v_mul_f32 v47, v47, v92\nv_cvt_pk_bf16_f32 v51, v46, v47\nbuffer_store_dwordx4 v[48:51], v4, %[rs], s55 offen offset:1024 sc1\nv_add_u32 v90, 1, v90\ns_mov_b32 s56, s88\ns_add_u32 s40, s40, 1\ns_cmp_lt_u32 s40, s89\ns_cbranch_scc1 .Lgg_next%=\n" \
"s_cmp_eq_u32 s89, 64\ns_cbranch_scc1 .Lgg_fin%=\ns_mov_b32 s89, 64\ns_mov_b32 s40, 0\ns_xor_b32 s86, s86, 0x2000\ns_xor_b32 s87, s87, 0x2000\ns_mov_b32 s88, %[fh1]\nv_xor_b32 v3, 0x2000, v3\n" \
"s_branch .Lgg_next%=\n.Lgg_fin%=:\ns_waitcnt vmcnt(0)\ns_barrier\nbuffer_store_dword v90, off, %[rs], s56 sc1\ns_branch .Lgg_end%=\n.Lgg_abort%=:\ns_mov_b64 exec, -1\n" \
"v_mov_b32 v16, 0\ns_movk_i32 s44, 6400\nbuffer_store_dword v16, off, %[rs], s44 sc1\n.Lgg_end%=:\ns_waitcnt vmcnt(0) lgkmcnt(0)\n" \
""
#define DD_ASM \
"v_mbcnt_lo_u32_b32 v255, -1, 0\nv_mbcnt_hi_u32_b32 v255, -1, v255\nv_lshlrev_b32 v255, 2, v255\nv_add_u32 v255, %[tb], v255\nds_read_b32 v0, v255 offset:0\nds_read_b32 v1, v255 offset:256\nds_read_b32 v2, v255 offset:512\nds_read_b32 v3, v255 offset:768\n" \
"ds_read_b32 v4, v255 offset:1024\nds_read_b32 v5, v255 offset:1280\nds_read_b32 v6, v255 offset:1536\nds_read_b32 v7, v255 offset:1792\nds_read_b32 v125, v255 offset:2048\nds_read_b32 v126, v255 offset:2304\nds_read_b32 v127, v255 offset:2560\ns_waitcnt lgkmcnt(0)\n" \
"s_add_u32 m0, %[tb], 8192\ns_add_u32 s44, %[wo], 0\nbuffer_load_dwordx4 v125, %[rw], s44 offen nt lds\ns_add_u32 m0, %[tb], 9216\ns_add_u32 s44, %[wo], 16384\nbuffer_load_dwordx4 v125, %[rw], s44 offen nt lds\ns_add_u32 m0, %[tb], 10240\ns_add_u32 s44, %[wo], 32768\n" \
"buffer_load_dwordx4 v125, %[rw], s44 offen nt lds\ns_add_u32 m0, %[tb], 11264\ns_add_u32 s44, %[wo], 49152\nbuffer_load_dwordx4 v125, %[rw], s44 offen nt lds\ns_add_u32 m0, %[lw], 0\ns_add_u32 s44, %[wo], 65536\nbuffer_load_dwordx4 v125, %[rw], s44 offen nt lds\ns_add_u32 m0, %[lw], 1024\n" \
"s_add_u32 s44, %[wo], 81920\nbuffer_load_dwordx4 v125, %[rw], s44 offen nt lds\ns_add_u32 m0, %[lw], 2048\ns_add_u32 s44, %[wo], 98304\nbuffer_load_dwordx4 v125, %[rw], s44 offen nt lds\ns_add_u32 m0, %[lw], 3072\ns_add_u32 s44, %[wo], 114688\nbuffer_load_dwordx4 v125, %[rw], s44 offen nt lds\n" \
"s_add_u32 m0, %[lw], 4096\ns_add_u32 s44, %[wo], 131072\nbuffer_load_dwordx4 v125, %[rw], s44 offen nt lds\ns_add_u32 m0, %[lw], 5120\ns_add_u32 s44, %[wo], 147456\nbuffer_load_dwordx4 v125, %[rw], s44 offen nt lds\ns_add_u32 m0, %[lw], 6144\ns_add_u32 s44, %[wo], 163840\n" \
"buffer_load_dwordx4 v125, %[rw], s44 offen nt lds\ns_add_u32 m0, %[lw], 7168\ns_add_u32 s44, %[wo], 180224\nbuffer_load_dwordx4 v125, %[rw], s44 offen nt lds\ns_add_u32 m0, %[lw], 8192\ns_add_u32 s44, %[wo], 196608\nbuffer_load_dwordx4 v125, %[rw], s44 offen nt lds\ns_add_u32 m0, %[lw], 9216\n" \
"s_add_u32 s44, %[wo], 212992\nbuffer_load_dwordx4 v125, %[rw], s44 offen nt lds\ns_add_u32 m0, %[lw], 10240\ns_add_u32 s44, %[wo], 229376\nbuffer_load_dwordx4 v125, %[rw], s44 offen nt lds\ns_add_u32 m0, %[lw], 11264\ns_add_u32 s44, %[wo], 245760\nbuffer_load_dwordx4 v125, %[rw], s44 offen nt lds\n" \
"s_add_u32 m0, %[lw], 12288\ns_add_u32 s44, %[wo], 262144\nbuffer_load_dwordx4 v125, %[rw], s44 offen nt lds\ns_add_u32 m0, %[lw], 13312\ns_add_u32 s44, %[wo], 278528\nbuffer_load_dwordx4 v125, %[rw], s44 offen nt lds\ns_add_u32 m0, %[lw], 14336\ns_add_u32 s44, %[wo], 294912\n" \
"buffer_load_dwordx4 v125, %[rw], s44 offen nt lds\ns_add_u32 m0, %[lw], 15360\ns_add_u32 s44, %[wo], 311296\nbuffer_load_dwordx4 v125, %[rw], s44 offen nt lds\ns_add_u32 m0, %[lw], 16384\ns_add_u32 s44, %[wo], 327680\nbuffer_load_dwordx4 v125, %[rw], s44 offen nt lds\ns_add_u32 m0, %[lw], 17408\n" \
"s_add_u32 s44, %[wo], 344064\nbuffer_load_dwordx4 v125, %[rw], s44 offen nt lds\ns_add_u32 m0, %[lw], 18432\ns_add_u32 s44, %[wo], 360448\nbuffer_load_dwordx4 v125, %[rw], s44 offen nt lds\ns_add_u32 m0, %[lw], 19456\ns_add_u32 s44, %[wo], 376832\nbuffer_load_dwordx4 v125, %[rw], s44 offen nt lds\n" \
"s_add_u32 m0, %[lw], 20480\ns_add_u32 s44, %[wo], 393216\nbuffer_load_dwordx4 v125, %[rw], s44 offen nt lds\ns_add_u32 m0, %[lw], 21504\ns_add_u32 s44, %[wo], 409600\nbuffer_load_dwordx4 v125, %[rw], s44 offen nt lds\ns_add_u32 m0, %[lw], 22528\ns_add_u32 s44, %[wo], 425984\n" \
"buffer_load_dwordx4 v125, %[rw], s44 offen nt lds\ns_add_u32 m0, %[lw], 23552\ns_add_u32 s44, %[wo], 442368\nbuffer_load_dwordx4 v125, %[rw], s44 offen nt lds\ns_add_u32 m0, %[tb], 0\ns_add_u32 s44, %[wo], 458752\nbuffer_load_dwordx4 v125, %[rw], s44 offen nt lds\ns_add_u32 m0, %[tb], 1024\n" \
"s_add_u32 s44, %[wo], 475136\nbuffer_load_dwordx4 v125, %[rw], s44 offen nt lds\ns_add_u32 m0, %[tb], 2048\ns_add_u32 s44, %[wo], 491520\nbuffer_load_dwordx4 v125, %[rw], s44 offen nt lds\ns_add_u32 m0, %[tb], 3072\ns_add_u32 s44, %[wo], 507904\nbuffer_load_dwordx4 v125, %[rw], s44 offen nt lds\n" \
"s_add_u32 m0, %[tb], 4096\ns_add_u32 s44, %[wo], 524288\nbuffer_load_dwordx4 v125, %[rw], s44 offen nt lds\ns_add_u32 m0, %[tb], 5120\ns_add_u32 s44, %[wo], 540672\nbuffer_load_dwordx4 v125, %[rw], s44 offen nt lds\ns_add_u32 m0, %[tb], 6144\ns_add_u32 s44, %[wo], 557056\n" \
"buffer_load_dwordx4 v125, %[rw], s44 offen nt lds\ns_add_u32 m0, %[tb], 7168\ns_add_u32 s44, %[wo], 573440\nbuffer_load_dwordx4 v125, %[rw], s44 offen nt lds\ns_waitcnt vmcnt(32)\nds_read_b64_tr_b16 a[0:1], v126 offset:8192\nds_read_b64_tr_b16 a[2:3], v126 offset:8704\nds_read_b64_tr_b16 a[8:9], v126 offset:8224\n" \
"ds_read_b64_tr_b16 a[10:11], v126 offset:8736\nds_read_b64_tr_b16 a[16:17], v126 offset:8256\nds_read_b64_tr_b16 a[18:19], v126 offset:8768\nds_read_b64_tr_b16 a[24:25], v126 offset:8288\nds_read_b64_tr_b16 a[26:27], v126 offset:8800\ns_waitcnt lgkmcnt(0)\ns_add_u32 m0, %[tb], 8192\ns_add_u32 s44, %[wo], 589824\n" \
"buffer_load_dwordx4 v125, %[rw], s44 offen nt lds\ns_add_u32 m0, %[tb], 9216\ns_add_u32 s44, %[wo], 606208\nbuffer_load_dwordx4 v125, %[rw], s44 offen nt lds\ns_add_u32 m0, %[tb], 10240\ns_add_u32 s44, %[wo], 622592\nbuffer_load_dwordx4 v125, %[rw], s44 offen nt lds\ns_add_u32 m0, %[tb], 11264\n" \
"s_add_u32 s44, %[wo], 638976\nbuffer_load_dwordx4 v125, %[rw], s44 offen nt lds\ns_waitcnt vmcnt(32)\nds_read_b64_tr_b16 a[4:5], v127 offset:0\nds_read_b64_tr_b16 a[6:7], v127 offset:512\nds_read_b64_tr_b16 a[12:13], v127 offset:32\nds_read_b64_tr_b16 a[14:15], v127 offset:544\nds_read_b64_tr_b16 a[20:21], v127 offset:64\n" \
"ds_read_b64_tr_b16 a[22:23], v127 offset:576\nds_read_b64_tr_b16 a[28:29], v127 offset:96\nds_read_b64_tr_b16 a[30:31], v127 offset:608\ns_waitcnt lgkmcnt(0)\ns_add_u32 m0, %[lw], 0\ns_add_u32 s44, %[wo], 655360\nbuffer_load_dwordx4 v125, %[rw], s44 offen nt lds\ns_add_u32 m0, %[lw], 1024\n" \
"s_add_u32 s44, %[wo], 671744\nbuffer_load_dwordx4 v125, %[rw], s44 offen nt lds\ns_add_u32 m0, %[lw], 2048\ns_add_u32 s44, %[wo], 688128\nbuffer_load_dwordx4 v125, %[rw], s44 offen nt lds\ns_add_u32 m0, %[lw], 3072\ns_add_u32 s44, %[wo], 704512\nbuffer_load_dwordx4 v125, %[rw], s44 offen nt lds\n" \
"s_waitcnt vmcnt(32)\nds_read_b64_tr_b16 a[32:33], v127 offset:4096\nds_read_b64_tr_b16 a[34:35], v127 offset:4608\nds_read_b64_tr_b16 v[92:93], v127 offset:4128\nds_read_b64_tr_b16 v[94:95], v127 offset:4640\nds_read_b64_tr_b16 a[40:41], v127 offset:4160\nds_read_b64_tr_b16 a[42:43], v127 offset:4672\nds_read_b64_tr_b16 a[48:49], v127 offset:4192\n" \
"ds_read_b64_tr_b16 a[50:51], v127 offset:4704\ns_waitcnt lgkmcnt(0)\ns_add_u32 m0, %[lw], 4096\ns_add_u32 s44, %[wo], 720896\nbuffer_load_dwordx4 v125, %[rw], s44 offen nt lds\ns_add_u32 m0, %[lw], 5120\ns_add_u32 s44, %[wo], 737280\nbuffer_load_dwordx4 v125, %[rw], s44 offen nt lds\n" \
"s_add_u32 m0, %[lw], 6144\ns_add_u32 s44, %[wo], 753664\nbuffer_load_dwordx4 v125, %[rw], s44 offen nt lds\ns_add_u32 m0, %[lw], 7168\ns_add_u32 s44, %[wo], 770048\nbuffer_load_dwordx4 v125, %[rw], s44 offen nt lds\ns_waitcnt vmcnt(32)\nds_read_b64_tr_b16 a[36:37], v127 offset:8192\n" \
"ds_read_b64_tr_b16 a[38:39], v127 offset:8704\nds_read_b64_tr_b16 v[88:89], v127 offset:8224\nds_read_b64_tr_b16 v[90:91], v127 offset:8736\nds_read_b64_tr_b16 a[44:45], v127 offset:8256\nds_read_b64_tr_b16 a[46:47], v127 offset:8768\nds_read_b64_tr_b16 a[52:53], v127 offset:8288\nds_read_b64_tr_b16 a[54:55], v127 offset:8800\ns_waitcnt lgkmcnt(0)\n" \
"s_add_u32 m0, %[lw], 8192\ns_add_u32 s44, %[wo], 786432\nbuffer_load_dwordx4 v125, %[rw], s44 offen nt lds\ns_add_u32 m0, %[lw], 9216\ns_add_u32 s44, %[wo], 802816\nbuffer_load_dwordx4 v125, %[rw], s44 offen nt lds\ns_add_u32 m0, %[lw], 10240\ns_add_u32 s44, %[wo], 819200\n" \
"buffer_load_dwordx4 v125, %[rw], s44 offen nt lds\ns_add_u32 m0, %[lw], 11264\ns_add_u32 s44, %[wo], 835584\nbuffer_load_dwordx4 v125, %[rw], s44 offen nt lds\ns_waitcnt vmcnt(32)\nds_read_b64_tr_b16 a[56:57], v127 offset:12288\nds_read_b64_tr_b16 a[58:59], v127 offset:12800\nds_read_b64_tr_b16 a[64:65], v127 offset:12320\n" \
"ds_read_b64_tr_b16 a[66:67], v127 offset:12832\nds_read_b64_tr_b16 a[72:73], v127 offset:12352\nds_read_b64_tr_b16 a[74:75], v127 offset:12864\nds_read_b64_tr_b16 a[80:81], v127 offset:12384\nds_read_b64_tr_b16 a[82:83], v127 offset:12896\ns_waitcnt lgkmcnt(0)\ns_add_u32 m0, %[lw], 12288\ns_add_u32 s44, %[wo], 851968\n" \
"buffer_load_dwordx4 v125, %[rw], s44 offen nt lds\ns_add_u32 m0, %[lw], 13312\ns_add_u32 s44, %[wo], 868352\nbuffer_load_dwordx4 v125, %[rw], s44 offen nt lds\ns_add_u32 m0, %[lw], 14336\ns_add_u32 s44, %[wo], 884736\nbuffer_load_dwordx4 v125, %[rw], s44 offen nt lds\ns_add_u32 m0, %[lw], 15360\n" \
"s_add_u32 s44, %[wo], 901120\nbuffer_load_dwordx4 v125, %[rw], s44 offen nt lds\ns_waitcnt vmcnt(32)\nds_read_b64_tr_b16 a[60:61], v127 offset:16384\nds_read_b64_tr_b16 a[62:63], v127 offset:16896\nds_read_b64_tr_b16 a[68:69], v127 offset:16416\nds_read_b64_tr_b16 a[70:71], v127 offset:16928\nds_read_b64_tr_b16 a[76:77], v127 offset:16448\n" \
"ds_read_b64_tr_b16 a[78:79], v127 offset:16960\nds_read_b64_tr_b16 a[84:85], v127 offset:16480\nds_read_b64_tr_b16 a[86:87], v127 offset:16992\ns_waitcnt lgkmcnt(0)\ns_add_u32 m0, %[lw], 16384\ns_add_u32 s44, %[wo], 917504\nbuffer_load_dwordx4 v125, %[rw], s44 offen nt lds\ns_add_u32 m0, %[lw], 17408\n" \
"s_add_u32 s44, %[wo], 933888\nbuffer_load_dwordx4 v125, %[rw], s44 offen nt lds\ns_add_u32 m0, %[lw], 18432\ns_add_u32 s44, %[wo], 950272\nbuffer_load_dwordx4 v125, %[rw], s44 offen nt lds\ns_add_u32 m0, %[lw], 19456\ns_add_u32 s44, %[wo], 966656\nbuffer_load_dwordx4 v125, %[rw], s44 offen nt lds\n" \
"s_waitcnt vmcnt(32)\nds_read_b64_tr_b16 a[88:89], v127 offset:20480\nds_read_b64_tr_b16 a[90:91], v127 offset:20992\nds_read_b64_tr_b16 a[96:97], v127 offset:20512\nds_read_b64_tr_b16 a[98:99], v127 offset:21024\nds_read_b64_tr_b16 a[104:105], v127 offset:20544\nds_read_b64_tr_b16 a[106:107], v127 offset:21056\nds_read_b64_tr_b16 v[84:85], v127 offset:20576\n" \
"ds_read_b64_tr_b16 v[86:87], v127 offset:21088\ns_waitcnt lgkmcnt(0)\ns_add_u32 m0, %[lw], 20480\ns_add_u32 s44, %[wo], 983040\nbuffer_load_dwordx4 v125, %[rw], s44 offen nt lds\ns_add_u32 m0, %[lw], 21504\ns_add_u32 s44, %[wo], 999424\nbuffer_load_dwordx4 v125, %[rw], s44 offen nt lds\n" \
"s_add_u32 m0, %[lw], 22528\ns_add_u32 s44, %[wo], 1015808\nbuffer_load_dwordx4 v125, %[rw], s44 offen nt lds\ns_add_u32 m0, %[lw], 23552\ns_add_u32 s44, %[wo], 1032192\nbuffer_load_dwordx4 v125, %[rw], s44 offen nt lds\ns_waitcnt vmcnt(32)\nds_read_b64_tr_b16 a[92:93], v126 offset:0\n" \
"ds_read_b64_tr_b16 a[94:95], v126 offset:512\nds_read_b64_tr_b16 a[100:101], v126 offset:32\nds_read_b64_tr_b16 a[102:103], v126 offset:544\nds_read_b64_tr_b16 a[108:109], v126 offset:64\nds_read_b64_tr_b16 a[110:111], v126 offset:576\nds_read_b64_tr_b16 v[80:81], v126 offset:96\nds_read_b64_tr_b16 v[82:83], v126 offset:608\ns_waitcnt lgkmcnt(0)\n" \
"s_add_u32 m0, %[tb], 0\ns_add_u32 s44, %[wo], 1048576\nbuffer_load_dwordx4 v125, %[rw], s44 offen nt lds\ns_add_u32 m0, %[tb], 1024\ns_add_u32 s44, %[wo], 1064960\nbuffer_load_dwordx4 v125, %[rw], s44 offen nt lds\ns_add_u32 m0, %[tb], 2048\ns_add_u32 s44, %[wo], 1081344\n" \
"buffer_load_dwordx4 v125, %[rw], s44 offen nt lds\ns_add_u32 m0, %[tb], 3072\ns_add_u32 s44, %[wo], 1097728\nbuffer_load_dwordx4 v125, %[rw], s44 offen nt lds\ns_waitcnt vmcnt(32)\nds_read_b64_tr_b16 a[112:113], v126 offset:4096\nds_read_b64_tr_b16 a[114:115], v126 offset:4608\nds_read_b64_tr_b16 a[120:121], v126 offset:4128\n" \
"ds_read_b64_tr_b16 a[122:123], v126 offset:4640\nds_read_b64_tr_b16 a[128:129], v126 offset:4160\nds_read_b64_tr_b16 a[130:131], v126 offset:4672\nds_read_b64_tr_b16 a[136:137], v126 offset:4192\nds_read_b64_tr_b16 a[138:139], v126 offset:4704\ns_waitcnt lgkmcnt(0)\ns_add_u32 m0, %[tb], 4096\ns_add_u32 s44, %[wo], 1114112\n" \
"buffer_load_dwordx4 v125, %[rw], s44 offen nt lds\ns_add_u32 m0, %[tb], 5120\ns_add_u32 s44, %[wo], 1130496\nbuffer_load_dwordx4 v125, %[rw], s44 offen nt lds\ns_add_u32 m0, %[tb], 6144\ns_add_u32 s44, %[wo], 1146880\nbuffer_load_dwordx4 v125, %[rw], s44 offen nt lds\ns_add_u32 m0, %[tb], 7168\n" \
"s_add_u32 s44, %[wo], 1163264\nbuffer_load_dwordx4 v125, %[rw], s44 offen nt lds\ns_waitcnt vmcnt(32)\nds_read_b64_tr_b16 a[116:117], v126 offset:8192\nds_read_b64_tr_b16 a[118:119], v126 offset:8704\nds_read_b64_tr_b16 a[124:125], v126 offset:8224\nds_read_b64_tr_b16 a[126:127], v126 offset:8736\nds_read_b64_tr_b16 a[132:133], v126 offset:8256\n" \
"ds_read_b64_tr_b16 a[134:135], v126 offset:8768\nds_read_b64_tr_b16 a[140:141], v126 offset:8288\nds_read_b64_tr_b16 a[142:143], v126 offset:8800\ns_waitcnt lgkmcnt(0)\ns_add_u32 m0, %[tb], 8192\ns_add_u32 s44, %[wo], 1179648\nbuffer_load_dwordx4 v125, %[rw], s44 offen nt lds\ns_add_u32 m0, %[tb], 9216\n" \
"s_add_u32 s44, %[wo], 1196032\nbuffer_load_dwordx4 v125, %[rw], s44 offen nt lds\ns_add_u32 m0, %[tb], 10240\ns_add_u32 s44, %[wo], 1212416\nbuffer_load_dwordx4 v125, %[rw], s44 offen nt lds\ns_add_u32 m0, %[tb], 11264\ns_add_u32 s44, %[wo], 1228800\nbuffer_load_dwordx4 v125, %[rw], s44 offen nt lds\n" \
"s_waitcnt vmcnt(32)\nds_read_b64_tr_b16 a[144:145], v127 offset:0\nds_read_b64_tr_b16 a[146:147], v127 offset:512\nds_read_b64_tr_b16 v[76:77], v127 offset:32\nds_read_b64_tr_b16 v[78:79], v127 offset:544\nds_read_b64_tr_b16 a[152:153], v127 offset:64\nds_read_b64_tr_b16 a[154:155], v127 offset:576\nds_read_b64_tr_b16 a[160:161], v127 offset:96\n" \
"ds_read_b64_tr_b16 a[162:163], v127 offset:608\ns_waitcnt lgkmcnt(0)\ns_add_u32 m0, %[lw], 0\ns_add_u32 s44, %[wo], 1245184\nbuffer_load_dwordx4 v125, %[rw], s44 offen nt lds\ns_add_u32 m0, %[lw], 1024\ns_add_u32 s44, %[wo], 1261568\nbuffer_load_dwordx4 v125, %[rw], s44 offen nt lds\n" \
"s_add_u32 m0, %[lw], 2048\ns_add_u32 s44, %[wo], 1277952\nbuffer_load_dwordx4 v125, %[rw], s44 offen nt lds\ns_add_u32 m0, %[lw], 3072\ns_add_u32 s44, %[wo], 1294336\nbuffer_load_dwordx4 v125, %[rw], s44 offen nt lds\ns_waitcnt vmcnt(32)\nds_read_b64_tr_b16 a[148:149], v127 offset:4096\n" \
"ds_read_b64_tr_b16 a[150:151], v127 offset:4608\nds_read_b64_tr_b16 v[72:73], v127 offset:4128\nds_read_b64_tr_b16 v[74:75], v127 offset:4640\nds_read_b64_tr_b16 a[156:157], v127 offset:4160\nds_read_b64_tr_b16 a[158:159], v127 offset:4672\nds_read_b64_tr_b16 a[164:165], v127 offset:4192\nds_read_b64_tr_b16 a[166:167], v127 offset:4704\ns_waitcnt lgkmcnt(0)\n" \
"s_add_u32 m0, %[lw], 4096\ns_add_u32 s44, %[wo], 1310720\nbuffer_load_dwordx4 v125, %[rw], s44 offen nt lds\ns_add_u32 m0, %[lw], 5120\ns_add_u32 s44, %[wo], 1327104\nbuffer_load_dwordx4 v125, %[rw], s44 offen nt lds\ns_add_u32 m0, %[lw], 6144\ns_add_u32 s44, %[wo], 1343488\n" \
"buffer_load_dwordx4 v125, %[rw], s44 offen nt lds\ns_add_u32 m0, %[lw], 7168\ns_add_u32 s44, %[wo], 1359872\nbuffer_load_dwordx4 v125, %[rw], s44 offen nt lds\ns_waitcnt vmcnt(32)\nds_read_b64_tr_b16 a[168:169], v127 offset:8192\nds_read_b64_tr_b16 a[170:171], v127 offset:8704\nds_read_b64_tr_b16 a[176:177], v127 offset:8224\n" \
"ds_read_b64_tr_b16 a[178:179], v127 offset:8736\nds_read_b64_tr_b16 v[68:69], v127 offset:8256\nds_read_b64_tr_b16 v[70:71], v127 offset:8768\nds_read_b64_tr_b16 a[184:185], v127 offset:8288\nds_read_b64_tr_b16 a[186:187], v127 offset:8800\ns_waitcnt lgkmcnt(0)\ns_add_u32 m0, %[lw], 8192\ns_add_u32 s44, %[wo], 1376256\n" \
"buffer_load_dwordx4 v125, %[rw], s44 offen nt lds\ns_add_u32 m0, %[lw], 9216\ns_add_u32 s44, %[wo], 1392640\nbuffer_load_dwordx4 v125, %[rw], s44 offen nt lds\ns_add_u32 m0, %[lw], 10240\ns_add_u32 s44, %[wo], 1409024\nbuffer_load_dwordx4 v125, %[rw], s44 offen nt lds\ns_add_u32 m0, %[lw], 11264\n" \
"s_add_u32 s44, %[wo], 1425408\nbuffer_load_dwordx4 v125, %[rw], s44 offen nt lds\ns_waitcnt vmcnt(32)\nds_read_b64_tr_b16 a[172:173], v127 offset:12288\nds_read_b64_tr_b16 a[174:175], v127 offset:12800\nds_read_b64_tr_b16 a[180:181], v127 offset:12320\nds_read_b64_tr_b16 a[182:183], v127 offset:12832\nds_read_b64_tr_b16 v[64:65], v127 offset:12352\n" \
"ds_read_b64_tr_b16 v[66:67], v127 offset:12864\nds_read_b64_tr_b16 a[188:189], v127 offset:12384\nds_read_b64_tr_b16 a[190:191], v127 offset:12896\ns_waitcnt lgkmcnt(0)\ns_add_u32 m0, %[lw], 12288\ns_add_u32 s44, %[wo], 1441792\nbuffer_load_dwordx4 v125, %[rw], s44 offen nt lds\ns_add_u32 m0, %[lw], 13312\n" \
"s_add_u32 s44, %[wo], 1458176\nbuffer_load_dwordx4 v125, %[rw], s44 offen nt lds\ns_add_u32 m0, %[lw], 14336\ns_add_u32 s44, %[wo], 1474560\nbuffer_load_dwordx4 v125, %[rw], s44 offen nt lds\ns_add_u32 m0, %[lw], 15360\ns_add_u32 s44, %[wo], 1490944\nbuffer_load_dwordx4 v125, %[rw], s44 offen nt lds\n" \
"s_waitcnt vmcnt(32)\nds_read_b64_tr_b16 a[192:193], v127 offset:16384\nds_read_b64_tr_b16 a[194:195], v127 offset:16896\nds_read_b64_tr_b16 a[200:201], v127 offset:16416\nds_read_b64_tr_b16 a[202:203], v127 offset:16928\nds_read_b64_tr_b16 a[208:209], v127 offset:16448\nds_read_b64_tr_b16 a[210:211], v127 offset:16960\nds_read_b64_tr_b16 a[216:217], v127 offset:16480\n" \
"ds_read_b64_tr_b16 a[218:219], v127 offset:16992\ns_waitcnt lgkmcnt(0)\ns_add_u32 m0, %[lw], 16384\ns_add_u32 s44, %[wo], 1507328\nbuffer_load_dwordx4 v125, %[rw], s44 offen nt lds\ns_add_u32 m0, %[lw], 17408\ns_add_u32 s44, %[wo], 1523712\nbuffer_load_dwordx4 v125, %[rw], s44 offen nt lds\n" \
"s_add_u32 m0, %[lw], 18432\ns_add_u32 s44, %[wo], 1540096\nbuffer_load_dwordx4 v125, %[rw], s44 offen nt lds\ns_add_u32 m0, %[lw], 19456\ns_add_u32 s44, %[wo], 1556480\nbuffer_load_dwordx4 v125, %[rw], s44 offen nt lds\ns_waitcnt vmcnt(32)\nds_read_b64_tr_b16 a[196:197], v127 offset:20480\n" \
"ds_read_b64_tr_b16 a[198:199], v127 offset:20992\nds_read_b64_tr_b16 a[204:205], v127 offset:20512\nds_read_b64_tr_b16 a[206:207], v127 offset:21024\nds_read_b64_tr_b16 a[212:213], v127 offset:20544\nds_read_b64_tr_b16 a[214:215], v127 offset:21056\nds_read_b64_tr_b16 a[220:221], v127 offset:20576\nds_read_b64_tr_b16 a[222:223], v127 offset:21088\ns_waitcnt lgkmcnt(0)\n" \
"s_add_u32 m0, %[lw], 20480\ns_add_u32 s44, %[wo], 1572864\nbuffer_load_dwordx4 v125, %[rw], s44 offen nt lds\ns_add_u32 m0, %[lw], 21504\ns_add_u32 s44, %[wo], 1589248\nbuffer_load_dwordx4 v125, %[rw], s44 offen nt lds\ns_add_u32 m0, %[lw], 22528\ns_add_u32 s44, %[wo], 1605632\n" \
"buffer_load_dwordx4 v125, %[rw], s44 offen nt lds\ns_add_u32 m0, %[lw], 23552\ns_add_u32 s44, %[wo], 1622016\nbuffer_load_dwordx4 v125, %[rw], s44 offen nt lds\ns_waitcnt vmcnt(32)\nds_read_b64_tr_b16 v[60:61], v126 offset:0\nds_read_b64_tr_b16 v[62:63], v126 offset:512\nds_read_b64_tr_b16 a[224:225], v126 offset:32\n" \
"ds_read_b64_tr_b16 a[226:227], v126 offset:544\nds_read_b64_tr_b16 a[232:233], v126 offset:64\nds_read_b64_tr_b16 a[234:235], v126 offset:576\nds_read_b64_tr_b16 a[240:241], v126 offset:96\nds_read_b64_tr_b16 a[242:243], v126 offset:608\ns_waitcnt lgkmcnt(0)\ns_add_u32 m0, %[tb], 0\ns_add_u32 s44, %[wo], 1638400\n" \
"buffer_load_dwordx4 v125, %[rw], s44 offen nt lds\ns_add_u32 m0, %[tb], 1024\ns_add_u32 s44, %[wo], 1654784\nbuffer_load_dwordx4 v125, %[rw], s44 offen nt lds\ns_add_u32 m0, %[tb], 2048\ns_add_u32 s44, %[wo], 1671168\nbuffer_load_dwordx4 v125, %[rw], s44 offen nt lds\ns_add_u32 m0, %[tb], 3072\n" \
"s_add_u32 s44, %[wo], 1687552\nbuffer_load_dwordx4 v125, %[rw], s44 offen nt lds\ns_waitcnt vmcnt(32)\nds_read_b64_tr_b16 v[56:57], v126 offset:4096\nds_read_b64_tr_b16 v[58:59], v126 offset:4608\nds_read_b64_tr_b16 a[228:229], v126 offset:4128\nds_read_b64_tr_b16 a[230:231], v126 offset:4640\nds_read_b64_tr_b16 a[236:237], v126 offset:4160\n" \
"ds_read_b64_tr_b16 a[238:239], v126 offset:4672\nds_read_b64_tr_b16 a[244:245], v126 offset:4192\nds_read_b64_tr_b16 a[246:247], v126 offset:4704\ns_waitcnt lgkmcnt(0)\ns_add_u32 m0, %[tb], 4096\ns_add_u32 s44, %[wo], 1703936\nbuffer_load_dwordx4 v125, %[rw], s44 offen nt lds\ns_add_u32 m0, %[tb], 5120\n" \
"s_add_u32 s44, %[wo], 1720320\nbuffer_load_dwordx4 v125, %[rw], s44 offen nt lds\ns_add_u32 m0, %[tb], 6144\ns_add_u32 s44, %[wo], 1736704\nbuffer_load_dwordx4 v125, %[rw], s44 offen nt lds\ns_add_u32 m0, %[tb], 7168\ns_add_u32 s44, %[wo], 1753088\nbuffer_load_dwordx4 v125, %[rw], s44 offen nt lds\n" \
"s_waitcnt vmcnt(32)\nds_read_b64_tr_b16 a[248:249], v126 offset:8192\nds_read_b64_tr_b16 a[250:251], v126 offset:8704\nds_read_b64_tr_b16 v[128:129], v126 offset:8224\nds_read_b64_tr_b16 v[130:131], v126 offset:8736\nds_read_b64_tr_b16 v[136:137], v126 offset:8256\nds_read_b64_tr_b16 v[138:139], v126 offset:8768\nds_read_b64_tr_b16 v[144:145], v126 offset:8288\n" \
"ds_read_b64_tr_b16 v[146:147], v126 offset:8800\ns_waitcnt lgkmcnt(0)\ns_add_u32 m0, %[tb], 8192\ns_add_u32 s44, %[wo], 1769472\nbuffer_load_dwordx4 v125, %[rw], s44 offen nt lds\ns_add_u32 m0, %[tb], 9216\ns_add_u32 s44, %[wo], 1785856\nbuffer_load_dwordx4 v125, %[rw], s44 offen nt lds\n" \
"s_add_u32 m0, %[tb], 10240\ns_add_u32 s44, %[wo], 1802240\nbuffer_load_dwordx4 v125, %[rw], s44 offen nt lds\ns_add_u32 m0, %[tb], 11264\ns_add_u32 s44, %[wo], 1818624\nbuffer_load_dwordx4 v125, %[rw], s44 offen nt lds\ns_waitcnt vmcnt(32)\nds_read_b64_tr_b16 a[252:253], v127 offset:0\n" \
"ds_read_b64_tr_b16 a[254:255], v127 offset:512\nds_read_b64_tr_b16 v[132:133], v127 offset:32\nds_read_b64_tr_b16 v[134:135], v127 offset:544\nds_read_b64_tr_b16 v[140:141], v127 offset:64\nds_read_b64_tr_b16 v[142:143], v127 offset:576\nds_read_b64_tr_b16 v[148:149], v127 offset:96\nds_read_b64_tr_b16 v[150:151], v127 offset:608\ns_waitcnt lgkmcnt(0)\n" \
"ds_write_b128 v1, v[92:95] offset:0\nds_write_b128 v1, v[88:91] offset:1024\nds_write_b128 v1, v[84:87] offset:2048\nds_write_b128 v1, v[80:83] offset:3072\ns_waitcnt vmcnt(28)\nds_read_b64_tr_b16 v[152:153], v127 offset:4096\nds_read_b64_tr_b16 v[154:155], v127 offset:4608\nds_read_b64_tr_b16 v[160:161], v127 offset:4128\n" \
"ds_read_b64_tr_b16 v[162:163], v127 offset:4640\nds_read_b64_tr_b16 v[80:81], v127 offset:4160\nds_read_b64_tr_b16 v[82:83], v127 offset:4672\nds_read_b64_tr_b16 v[168:169], v127 offset:4192\nds_read_b64_tr_b16 v[170:171], v127 offset:4704\ns_waitcnt lgkmcnt(0)\nds_write_b128 v1, v[76:79] offset:4096\nds_write_b128 v1, v[72:75] offset:5120\n" \
"ds_write_b128 v1, v[68:71] offset:6144\nds_write_b128 v1, v[64:67] offset:7168\ns_waitcnt vmcnt(24)\nds_read_b64_tr_b16 v[156:157], v127 offset:8192\nds_read_b64_tr_b16 v[158:159], v127 offset:8704\nds_read_b64_tr_b16 v[164:165], v127 offset:8224\nds_read_b64_tr_b16 v[166:167], v127 offset:8736\nds_read_b64_tr_b16 v[64:65], v127 offset:8256\n" \
"ds_read_b64_tr_b16 v[66:67], v127 offset:8768\nds_read_b64_tr_b16 v[172:173], v127 offset:8288\nds_read_b64_tr_b16 v[174:175], v127 offset:8800\ns_waitcnt lgkmcnt(0)\nds_write_b128 v1, v[60:63] offset:8192\nds_write_b128 v1, v[56:59] offset:9216\nds_write_b128 v1, v[80:83] offset:10240\nds_write_b128 v1, v[64:67] offset:11264\n" \
"s_waitcnt vmcnt(20)\nds_read_b64_tr_b16 v[176:177], v127 offset:12288\nds_read_b64_tr_b16 v[178:179], v127 offset:12800\nds_read_b64_tr_b16 v[184:185], v127 offset:12320\nds_read_b64_tr_b16 v[186:187], v127 offset:12832\nds_read_b64_tr_b16 v[192:193], v127 offset:12352\nds_read_b64_tr_b16 v[194:195], v127 offset:12864\nds_read_b64_tr_b16 v[200:201], v127 offset:12384\n" \
"ds_read_b64_tr_b16 v[202:203], v127 offset:12896\ns_waitcnt lgkmcnt(0)\ns_waitcnt vmcnt(16)\nds_read_b64_tr_b16 v[180:181], v127 offset:16384\nds_read_b64_tr_b16 v[182:183], v127 offset:16896\nds_read_b64_tr_b16 v[188:189], v127 offset:16416\nds_read_b64_tr_b16 v[190:191], v127 offset:16928\nds_read_b64_tr_b16 v[196:197], v127 offset:16448\n" \
"ds_read_b64_tr_b16 v[198:199], v127 offset:16960\nds_read_b64_tr_b16 v[204:205], v127 offset:16480\nds_read_b64_tr_b16 v[206:207], v127 offset:16992\ns_waitcnt lgkmcnt(0)\ns_waitcnt vmcnt(12)\nds_read_b64_tr_b16 v[64:65], v127 offset:20480\nds_read_b64_tr_b16 v[66:67], v127 offset:20992\nds_read_b64_tr_b16 v[208:209], v127 offset:20512\n" \
"ds_read_b64_tr_b16 v[210:211], v127 offset:21024\nds_read_b64_tr_b16 v[216:217], v127 offset:20544\nds_read_b64_tr_b16 v[218:219], v127 offset:21056\nds_read_b64_tr_b16 v[224:225], v127 offset:20576\nds_read_b64_tr_b16 v[226:227], v127 offset:21088\ns_waitcnt lgkmcnt(0)\nds_write_b128 v1, v[64:67] offset:12288\ns_waitcnt vmcnt(8)\n" \
"ds_read_b64_tr_b16 v[64:65], v126 offset:0\nds_read_b64_tr_b16 v[66:67], v126 offset:512\nds_read_b64_tr_b16 v[212:213], v126 offset:32\nds_read_b64_tr_b16 v[214:215], v126 offset:544\nds_read_b64_tr_b16 v[220:221], v126 offset:64\nds_read_b64_tr_b16 v[222:223], v126 offset:576\nds_read_b64_tr_b16 v[228:229], v126 offset:96\nds_read_b64_tr_b16 v[230:231], v126 offset:608\n" \
"s_waitcnt lgkmcnt(0)\nds_write_b128 v1, v[64:67] offset:13312\ns_waitcnt vmcnt(4)\nds_read_b64_tr_b16 v[232:233], v126 offset:4096\nds_read_b64_tr_b16 v[234:235], v126 offset:4608\nds_read_b64_tr_b16 v[64:65], v126 offset:4128\nds_read_b64_tr_b16 v[66:67], v126 offset:4640\nds_read_b64_tr_b16 v[240:241], v126 offset:4160\n" \
"ds_read_b64_tr_b16 v[242:243], v126 offset:4672\nds_read_b64_tr_b16 v[248:249], v126 offset:4192\nds_read_b64_tr_b16 v[250:251], v126 offset:4704\ns_waitcnt lgkmcnt(0)\nds_write_b128 v1, v[64:67] offset:14336\ns_waitcnt vmcnt(0)\nds_read_b64_tr_b16 v[236:237], v126 offset:8192\nds_read_b64_tr_b16 v[238:239], v126 offset:8704\n" \
"ds_read_b64_tr_b16 v[64:65], v126 offset:8224\nds_read_b64_tr_b16 v[66:67], v126 offset:8736\nds_read_b64_tr_b16 v[244:245], v126 offset:8256\nds_read_b64_tr_b16 v[246:247], v126 offset:8768\nds_read_b64_tr_b16 v[252:253], v126 offset:8288\nds_read_b64_tr_b16 v[254:255], v126 offset:8800\ns_waitcnt lgkmcnt(0)\nds_write_b128 v1, v[64:67] offset:15360\n" \
"s_waitcnt lgkmcnt(0)\ns_barrier\ns_mov_b32 s40, 0\ns_mov_b32 s43, 0\ns_mov_b32 s56, -2147483648\n.Ldd_next%=:\ns_cmp_eq_u32 s40, %[b1]\ns_cselect_b32 s44, 4, 0\n" \
"v_xor_b32 v7, s44, v7\nbuffer_load_dword v8, v7, %[rs], 0 offen sc1\ns_lshl_b32 s58, s40, 1\ns_add_u32 s44, s40, 0\ns_lshl_b32 s44, s44, 19\ns_add_u32 s44, s44, %[ho]\ns_lshr_b32 s45, s44, 8\ns_and_b32 s45, s45, 0x2000\n" \
"s_xor_b32 s55, s44, s45\ns_waitcnt vmcnt(0)\nbuffer_store_dword v9, off, %[rs], s56 sc1\ns_mov_b32 s56, -2147483648\n.Ldd_w0%=:\nv_cmp_gt_i32 vcc, s58, v8\ns_cbranch_vccz .Ldd_w0go%=\ns_cmp_ge_u32 s40, 62\n" \
"s_cbranch_scc1 .Ldd_ns671%=\ns_sleep 8\n.Ldd_ns671%=:\ns_sleep 1\nbuffer_load_dword v8, v7, %[rs], 0 offen sc1\ns_waitcnt vmcnt(0)\ns_add_u32 s43, s43, 1\ns_cmp_lt_u32 s43, 262144\n" \
"s_cbranch_scc1 .Ldd_w0%=\ns_branch .Ldd_abort%=\n.Ldd_w0go%=:\ns_add_u32 s60, s55, 0\nbuffer_load_dwordx4 v[48:51], v0, %[rs], s60 offen offset:0 sc1\nbuffer_load_dwordx4 v[52:55], v0, %[rs], s60 offen offset:2048 sc1\ns_add_u32 s60, s55, 4096\nbuffer_load_dwordx4 v[56:59], v0, %[rs], s60 offen offset:0 sc1\n" \
"buffer_load_dwordx4 v[60:63], v0, %[rs], s60 offen offset:2048 sc1\ns_add_u32 s60, s55, 16384\nbuffer_load_dwordx4 v[64:67], v0, %[rs], s60 offen offset:0 sc1\nbuffer_load_dwordx4 v[68:71], v0, %[rs], s60 offen offset:2048 sc1\ns_add_u32 s60, s55, 20480\nbuffer_load_dwordx4 v[72:75], v0, %[rs], s60 offen offset:0 sc1\nbuffer_load_dwordx4 v[76:79], v0, %[rs], s60 offen offset:2048 sc1\ns_add_u32 s60, s55, 32768\n" \
"buffer_load_dwordx4 v[80:83], v0, %[rs], s60 offen offset:0 sc1\nbuffer_load_dwordx4 v[84:87], v0, %[rs], s60 offen offset:2048 sc1\ns_add_u32 s60, s55, 36864\nbuffer_load_dwordx4 v[88:91], v0, %[rs], s60 offen offset:0 sc1\nbuffer_load_dwordx4 v[92:95], v0, %[rs], s60 offen offset:2048 sc1\ns_add_u32 s60, s55, 49152\nbuffer_load_dwordx4 v[96:99], v0, %[rs], s60 offen offset:0 sc1\nbuffer_load_dwordx4 v[100:103], v0, %[rs], s60 offen offset:2048 sc1\n" \
".Ldd_run%=:\ns_add_u32 s44, s40, 0\ns_lshl_b32 s44, s44, 17\ns_add_u32 s44, s44, %[xn]\ns_lshr_b32 s45, s44, 8\ns_and_b32 s45, s45, 0x2000\ns_xor_b32 s41, s44, s45\ns_lshl_b32 s44, s40, 16\n" \
"s_cmp_eq_u32 %[l], 0\ns_cselect_b32 s42, s41, s44\ns_add_u32 s58, s58, 1\nds_read_b128 v[120:123], v1 offset:0\ns_waitcnt vmcnt(13)\nv_mfma_f32_16x16x32_bf16 v[16:19], a[0:3], v[48:51], 0\nds_read_b128 v[124:127], v1 offset:1024\nv_mfma_f32_16x16x32_bf16 v[24:27], a[8:11], v[48:51], 0\n" \
"buffer_load_dword v8, v7, %[rs], 0 offen sc1\nv_mfma_f32_16x16x32_bf16 v[32:35], a[16:19], v[48:51], 0\ns_add_u32 s60, s55, 53248\nv_mfma_f32_16x16x32_bf16 v[40:43], a[24:27], v[48:51], 0\ns_waitcnt vmcnt(13)\nbuffer_load_dwordx4 v[48:51], v0, %[rs], s60 offen offset:0 sc1\nv_mfma_f32_16x16x32_bf16 v[16:19], a[4:7], v[52:55], v[16:19]\nv_mfma_f32_16x16x32_bf16 v[24:27], a[12:15], v[52:55], v[24:27]\n" \
"v_mfma_f32_16x16x32_bf16 v[32:35], a[20:23], v[52:55], v[32:35]\nv_mfma_f32_16x16x32_bf16 v[40:43], a[28:31], v[52:55], v[40:43]\ns_waitcnt vmcnt(13)\nbuffer_load_dwordx4 v[52:55], v0, %[rs], s60 offen offset:2048 sc1\nv_mfma_f32_16x16x32_bf16 v[16:19], a[32:35], v[56:59], v[16:19]\ns_waitcnt lgkmcnt(1)\nv_mfma_f32_16x16x32_bf16 v[24:27], v[120:123], v[56:59], v[24:27]\nv_mfma_f32_16x16x32_bf16 v[32:35], a[40:43], v[56:59], v[32:35]\n" \
"s_add_u32 s60, s55, 65536\nv_mfma_f32_16x16x32_bf16 v[40:43], a[48:51], v[56:59], v[40:43]\ns_waitcnt vmcnt(13)\nbuffer_load_dwordx4 v[56:59], v0, %[rs], s60 offen offset:0 sc1\nv_mfma_f32_16x16x32_bf16 v[16:19], a[36:39], v[60:63], v[16:19]\ns_waitcnt lgkmcnt(0)\nds_read_b128 v[120:123], v1 offset:2048\nv_mfma_f32_16x16x32_bf16 v[24:27], v[124:127], v[60:63], v[24:27]\n" \
"v_mfma_f32_16x16x32_bf16 v[32:35], a[44:47], v[60:63], v[32:35]\nv_mfma_f32_16x16x32_bf16 v[40:43], a[52:55], v[60:63], v[40:43]\ns_waitcnt vmcnt(13)\nbuffer_load_dwordx4 v[60:63], v0, %[rs], s60 offen offset:2048 sc1\nv_mfma_f32_16x16x32_bf16 v[16:19], a[56:59], v[64:67], v[16:19]\nds_read_b128 v[124:127], v1 offset:3072\nv_mfma_f32_16x16x32_bf16 v[24:27], a[64:67], v[64:67], v[24:27]\nv_mfma_f32_16x16x32_bf16 v[32:35], a[72:75], v[64:67], v[32:35]\n" \
"s_add_u32 s60, s55, 69632\nv_mfma_f32_16x16x32_bf16 v[40:43], a[80:83], v[64:67], v[40:43]\ns_waitcnt vmcnt(13)\nbuffer_load_dwordx4 v[64:67], v0, %[rs], s60 offen offset:0 sc1\nv_mfma_f32_16x16x32_bf16 v[16:19], a[60:63], v[68:71], v[16:19]\nv_mfma_f32_16x16x32_bf16 v[24:27], a[68:71], v[68:71], v[24:27]\nv_mfma_f32_16x16x32_bf16 v[32:35], a[76:79], v[68:71], v[32:35]\nv_mfma_f32_16x16x32_bf16 v[40:43], a[84:87], v[68:71], v[40:43]\n" \
"s_waitcnt vmcnt(13)\nbuffer_load_dwordx4 v[68:71], v0, %[rs], s60 offen offset:2048 sc1\nv_mfma_f32_16x16x32_bf16 v[16:19], a[88:91], v[72:75], v[16:19]\nv_mfma_f32_16x16x32_bf16 v[24:27], a[96:99], v[72:75], v[24:27]\nv_mfma_f32_16x16x32_bf16 v[32:35], a[104:107], v[72:75], v[32:35]\ns_waitcnt lgkmcnt(1)\ns_add_u32 s60, s55, 81920\nv_mfma_f32_16x16x32_bf16 v[40:43], v[120:123], v[72:75], v[40:43]\n" \
"s_waitcnt vmcnt(13)\nbuffer_load_dwordx4 v[72:75], v0, %[rs], s60 offen offset:0 sc1\nv_mfma_f32_16x16x32_bf16 v[16:19], a[92:95], v[76:79], v[16:19]\nds_read_b128 v[120:123], v1 offset:4096\nv_mfma_f32_16x16x32_bf16 v[24:27], a[100:103], v[76:79], v[24:27]\nv_mfma_f32_16x16x32_bf16 v[32:35], a[108:111], v[76:79], v[32:35]\ns_waitcnt lgkmcnt(1)\nv_mfma_f32_16x16x32_bf16 v[40:43], v[124:127], v[76:79], v[40:43]\n" \
"s_waitcnt vmcnt(13)\nbuffer_load_dwordx4 v[76:79], v0, %[rs], s60 offen offset:2048 sc1\nv_mfma_f32_16x16x32_bf16 v[16:19], a[112:115], v[80:83], v[16:19]\nds_read_b128 v[124:127], v1 offset:5120\nv_mfma_f32_16x16x32_bf16 v[24:27], a[120:123], v[80:83], v[24:27]\nv_mfma_f32_16x16x32_bf16 v[32:35], a[128:131], v[80:83], v[32:35]\ns_add_u32 s60, s55, 86016\nv_mfma_f32_16x16x32_bf16 v[40:43], a[136:139], v[80:83], v[40:43]\n" \
"s_waitcnt vmcnt(13)\nbuffer_load_dwordx4 v[80:83], v0, %[rs], s60 offen offset:0 sc1\nv_mfma_f32_16x16x32_bf16 v[16:19], a[116:119], v[84:87], v[16:19]\nds_read_b128 v[112:115], v1 offset:6144\nv_mfma_f32_16x16x32_bf16 v[24:27], a[124:127], v[84:87], v[24:27]\nv_mfma_f32_16x16x32_bf16 v[32:35], a[132:135], v[84:87], v[32:35]\nv_mfma_f32_16x16x32_bf16 v[40:43], a[140:143], v[84:87], v[40:43]\ns_waitcnt vmcnt(13)\n" \
"buffer_load_dwordx4 v[84:87], v0, %[rs], s60 offen offset:2048 sc1\nv_mfma_f32_16x16x32_bf16 v[16:19], a[144:147], v[88:91], v[16:19]\ns_waitcnt lgkmcnt(2)\nds_read_b128 v[116:119], v1 offset:7168\nv_mfma_f32_16x16x32_bf16 v[24:27], v[120:123], v[88:91], v[24:27]\nv_mfma_f32_16x16x32_bf16 v[32:35], a[152:155], v[88:91], v[32:35]\ns_add_u32 s60, s55, 98304\nv_mfma_f32_16x16x32_bf16 v[40:43], a[160:163], v[88:91], v[40:43]\n" \
"s_waitcnt vmcnt(13)\nbuffer_load_dwordx4 v[88:91], v0, %[rs], s60 offen offset:0 sc1\nv_mfma_f32_16x16x32_bf16 v[16:19], a[148:151], v[92:95], v[16:19]\ns_waitcnt lgkmcnt(2)\nv_mfma_f32_16x16x32_bf16 v[24:27], v[124:127], v[92:95], v[24:27]\nv_mfma_f32_16x16x32_bf16 v[32:35], a[156:159], v[92:95], v[32:35]\nv_mfma_f32_16x16x32_bf16 v[40:43], a[164:167], v[92:95], v[40:43]\ns_waitcnt vmcnt(13)\n" \
"buffer_load_dwordx4 v[92:95], v0, %[rs], s60 offen offset:2048 sc1\nv_mfma_f32_16x16x32_bf16 v[16:19], a[168:171], v[96:99], v[16:19]\nv_mfma_f32_16x16x32_bf16 v[24:27], a[176:179], v[96:99], v[24:27]\ns_waitcnt lgkmcnt(1)\nv_mfma_f32_16x16x32_bf16 v[32:35], v[112:115], v[96:99], v[32:35]\ns_add_u32 s60, s55, 102400\nv_mfma_f32_16x16x32_bf16 v[40:43], a[184:187], v[96:99], v[40:43]\ns_waitcnt vmcnt(13)\n" \
"buffer_load_dwordx4 v[96:99], v0, %[rs], s60 offen offset:0 sc1\nv_mfma_f32_16x16x32_bf16 v[16:19], a[172:175], v[100:103], v[16:19]\nds_read_b128 v[112:115], v1 offset:8192\nv_mfma_f32_16x16x32_bf16 v[24:27], a[180:183], v[100:103], v[24:27]\ns_waitcnt vmcnt(13)\ns_waitcnt lgkmcnt(1)\nv_cmp_gt_i32 vcc, s58, v8\nv_mfma_f32_16x16x32_bf16 v[32:35], v[116:119], v[100:103], v[32:35]\n" \
"s_cmp_eq_u64 vcc, 0\ns_cselect_b32 s61, s55, -2147483648\nv_mfma_f32_16x16x32_bf16 v[40:43], a[188:191], v[100:103], v[40:43]\ns_waitcnt vmcnt(12)\nbuffer_load_dwordx4 v[100:103], v0, %[rs], s60 offen offset:2048 sc1\nv_mfma_f32_16x16x32_bf16 v[16:19], a[192:195], v[48:51], v[16:19]\nds_read_b128 v[116:119], v1 offset:9216\nv_mfma_f32_16x16x32_bf16 v[24:27], a[200:203], v[48:51], v[24:27]\n" \
"v_mfma_f32_16x16x32_bf16 v[32:35], a[208:211], v[48:51], v[32:35]\ns_add_u32 s60, s61, 0\nv_mfma_f32_16x16x32_bf16 v[40:43], a[216:219], v[48:51], v[40:43]\ns_waitcnt vmcnt(12)\nbuffer_load_dwordx4 v[48:51], v0, %[rs], s60 offen offset:1024 sc1\nv_mfma_f32_16x16x32_bf16 v[16:19], a[196:199], v[52:55], v[16:19]\nv_mfma_f32_16x16x32_bf16 v[24:27], a[204:207], v[52:55], v[24:27]\nv_mfma_f32_16x16x32_bf16 v[32:35], a[212:215], v[52:55], v[32:35]\n" \
"v_mfma_f32_16x16x32_bf16 v[40:43], a[220:223], v[52:55], v[40:43]\ns_waitcnt vmcnt(12)\ns_waitcnt lgkmcnt(1)\nbuffer_load_dwordx4 v[52:55], v0, %[rs], s60 offen offset:3072 sc1\nv_mfma_f32_16x16x32_bf16 v[16:19], v[112:115], v[56:59], v[16:19]\nv_mfma_f32_16x16x32_bf16 v[24:27], a[224:227], v[56:59], v[24:27]\nv_mfma_f32_16x16x32_bf16 v[32:35], a[232:235], v[56:59], v[32:35]\ns_add_u32 s60, s61, 4096\n" \
"v_mfma_f32_16x16x32_bf16 v[40:43], a[240:243], v[56:59], v[40:43]\ns_waitcnt vmcnt(12)\ns_waitcnt lgkmcnt(0)\nbuffer_load_dwordx4 v[56:59], v0, %[rs], s60 offen offset:1024 sc1\nv_mfma_f32_16x16x32_bf16 v[16:19], v[116:119], v[60:63], v[16:19]\nds_read_b128 v[112:115], v1 offset:10240\nv_mfma_f32_16x16x32_bf16 v[24:27], a[228:231], v[60:63], v[24:27]\nv_mfma_f32_16x16x32_bf16 v[32:35], a[236:239], v[60:63], v[32:35]\n" \
"v_mfma_f32_16x16x32_bf16 v[40:43], a[244:247], v[60:63], v[40:43]\ns_waitcnt vmcnt(12)\nbuffer_load_dwordx4 v[60:63], v0, %[rs], s60 offen offset:3072 sc1\nv_mfma_f32_16x16x32_bf16 v[16:19], a[248:251], v[64:67], v[16:19]\nds_read_b128 v[116:119], v1 offset:11264\nv_mfma_f32_16x16x32_bf16 v[24:27], v[128:131], v[64:67], v[24:27]\nv_mfma_f32_16x16x32_bf16 v[32:35], v[136:139], v[64:67], v[32:35]\ns_add_u32 s60, s61, 16384\n" \
"v_mfma_f32_16x16x32_bf16 v[40:43], v[144:147], v[64:67], v[40:43]\ns_waitcnt vmcnt(12)\nbuffer_load_dwordx4 v[64:67], v0, %[rs], s60 offen offset:1024 sc1\nv_mfma_f32_16x16x32_bf16 v[16:19], a[252:255], v[68:71], v[16:19]\nv_mfma_f32_16x16x32_bf16 v[24:27], v[132:135], v[68:71], v[24:27]\nv_mfma_f32_16x16x32_bf16 v[32:35], v[140:143], v[68:71], v[32:35]\nv_mfma_f32_16x16x32_bf16 v[40:43], v[148:151], v[68:71], v[40:43]\ns_waitcnt vmcnt(12)\n" \
"buffer_load_dwordx4 v[68:71], v0, %[rs], s60 offen offset:3072 sc1\nv_mfma_f32_16x16x32_bf16 v[16:19], v[152:155], v[72:75], v[16:19]\nv_mfma_f32_16x16x32_bf16 v[24:27], v[160:163], v[72:75], v[24:27]\ns_waitcnt lgkmcnt(1)\nv_mfma_f32_16x16x32_bf16 v[32:35], v[112:115], v[72:75], v[32:35]\ns_add_u32 s60, s61, 20480\nv_mfma_f32_16x16x32_bf16 v[40:43], v[168:171], v[72:75], v[40:43]\ns_waitcnt vmcnt(12)\n" \
"buffer_load_dwordx4 v[72:75], v0, %[rs], s60 offen offset:1024 sc1\nv_mfma_f32_16x16x32_bf16 v[16:19], v[156:159], v[76:79], v[16:19]\nds_read_b128 v[112:115], v1 offset:12288\nv_mfma_f32_16x16x32_bf16 v[24:27], v[164:167], v[76:79], v[24:27]\ns_waitcnt lgkmcnt(1)\nv_mfma_f32_16x16x32_bf16 v[32:35], v[116:119], v[76:79], v[32:35]\nv_mfma_f32_16x16x32_bf16 v[40:43], v[172:175], v[76:79], v[40:43]\ns_waitcnt vmcnt(12)\n" \
"buffer_load_dwordx4 v[76:79], v0, %[rs], s60 offen offset:3072 sc1\nv_mfma_f32_16x16x32_bf16 v[16:19], v[176:179], v[80:83], v[16:19]\nds_read_b128 v[116:119], v1 offset:13312\nv_mfma_f32_16x16x32_bf16 v[24:27], v[184:187], v[80:83], v[24:27]\nv_mfma_f32_16x16x32_bf16 v[32:35], v[192:195], v[80:83], v[32:35]\ns_add_u32 s60, s61, 32768\nv_mfma_f32_16x16x32_bf16 v[40:43], v[200:203], v[80:83], v[40:43]\ns_waitcnt vmcnt(12)\n" \
"buffer_load_dwordx4 v[80:83], v0, %[rs], s60 offen offset:1024 sc1\nv_mfma_f32_16x16x32_bf16 v[16:19], v[180:183], v[84:87], v[16:19]\nds_read_b128 v[120:123], v1 offset:14336\nv_mfma_f32_16x16x32_bf16 v[24:27], v[188:191], v[84:87], v[24:27]\nv_mfma_f32_16x16x32_bf16 v[32:35], v[196:199], v[84:87], v[32:35]\nv_mfma_f32_16x16x32_bf16 v[40:43], v[204:207], v[84:87], v[40:43]\ns_waitcnt vmcnt(12)\ns_waitcnt lgkmcnt(2)\n" \
"buffer_load_dwordx4 v[84:87], v0, %[rs], s60 offen offset:3072 sc1\nv_mfma_f32_16x16x32_bf16 v[16:19], v[112:115], v[88:91], v[16:19]\nds_read_b128 v[124:127], v1 offset:15360\nv_mfma_f32_16x16x32_bf16 v[24:27], v[208:211], v[88:91], v[24:27]\nv_mfma_f32_16x16x32_bf16 v[32:35], v[216:219], v[88:91], v[32:35]\ns_add_u32 s60, s61, 36864\nv_mfma_f32_16x16x32_bf16 v[40:43], v[224:227], v[88:91], v[40:43]\ns_waitcnt vmcnt(12)\n" \
"s_waitcnt lgkmcnt(2)\nbuffer_load_dwordx4 v[88:91], v0, %[rs], s60 offen offset:1024 sc1\nv_mfma_f32_16x16x32_bf16 v[16:19], v[116:119], v[92:95], v[16:19]\nv_mfma_f32_16x16x32_bf16 v[24:27], v[212:215], v[92:95], v[24:27]\nv_mfma_f32_16x16x32_bf16 v[32:35], v[220:223], v[92:95], v[32:35]\nv_mfma_f32_16x16x32_bf16 v[40:43], v[228:231], v[92:95], v[40:43]\ns_waitcnt vmcnt(12)\nbuffer_load_dwordx4 v[92:95], v0, %[rs], s60 offen offset:3072 sc1\n" \
"v_mfma_f32_16x16x32_bf16 v[16:19], v[232:235], v[96:99], v[16:19]\ns_waitcnt lgkmcnt(1)\nv_mfma_f32_16x16x32_bf16 v[24:27], v[120:123], v[96:99], v[24:27]\nv_mfma_f32_16x16x32_bf16 v[32:35], v[240:243], v[96:99], v[32:35]\ns_add_u32 s60, s61, 49152\nv_mfma_f32_16x16x32_bf16 v[40:43], v[248:251], v[96:99], v[40:43]\ns_waitcnt vmcnt(12)\nbuffer_load_dwordx4 v[96:99], v0, %[rs], s60 offen offset:1024 sc1\n" \
"v_mfma_f32_16x16x32_bf16 v[16:19], v[236:239], v[100:103], v[16:19]\ns_waitcnt lgkmcnt(0)\nds_read_b128 v[120:123], v1 offset:0\nv_mfma_f32_16x16x32_bf16 v[24:27], v[124:127], v[100:103], v[24:27]\nv_mfma_f32_16x16x32_bf16 v[32:35], v[244:247], v[100:103], v[32:35]\nv_mfma_f32_16x16x32_bf16 v[40:43], v[252:255], v[100:103], v[40:43]\nbuffer_load_dwordx4 v[100:103], v0, %[rs], s60 offen offset:3072 sc1\ns_cbranch_vccnz .Ldd_slow%=\n" \
".Ldd_slowr%=:\ns_waitcnt vmcnt(13)\nv_mfma_f32_16x16x32_bf16 v[20:23], a[0:3], v[48:51], 0\nds_read_b128 v[124:127], v1 offset:1024\nv_mfma_f32_16x16x32_bf16 v[28:31], a[8:11], v[48:51], 0\nv_mfma_f32_16x16x32_bf16 v[36:39], a[16:19], v[48:51], 0\ns_add_u32 s60, s55, 53248\nv_mfma_f32_16x16x32_bf16 v[44:47], a[24:27], v[48:51], 0\n" \
"s_waitcnt vmcnt(12)\nbuffer_load_dwordx4 v[48:51], v0, %[rs], s60 offen offset:1024 sc1\nv_mfma_f32_16x16x32_bf16 v[20:23], a[4:7], v[52:55], v[20:23]\nv_mfma_f32_16x16x32_bf16 v[28:31], a[12:15], v[52:55], v[28:31]\nv_mfma_f32_16x16x32_bf16 v[36:39], a[20:23], v[52:55], v[36:39]\nv_mfma_f32_16x16x32_bf16 v[44:47], a[28:31], v[52:55], v[44:47]\ns_waitcnt vmcnt(12)\nbuffer_load_dwordx4 v[52:55], v0, %[rs], s60 offen offset:3072 sc1\n" \
"v_mfma_f32_16x16x32_bf16 v[20:23], a[32:35], v[56:59], v[20:23]\ns_waitcnt lgkmcnt(1)\nv_mfma_f32_16x16x32_bf16 v[28:31], v[120:123], v[56:59], v[28:31]\nds_write_b128 v2, v[16:19] offset:0\nv_mfma_f32_16x16x32_bf16 v[36:39], a[40:43], v[56:59], v[36:39]\ns_add_u32 s60, s55, 65536\nv_mfma_f32_16x16x32_bf16 v[44:47], a[48:51], v[56:59], v[44:47]\ns_waitcnt vmcnt(12)\n" \
"buffer_load_dwordx4 v[56:59], v0, %[rs], s60 offen offset:1024 sc1\nv_mfma_f32_16x16x32_bf16 v[20:23], a[36:39], v[60:63], v[20:23]\ns_waitcnt lgkmcnt(1)\nds_read_b128 v[120:123], v1 offset:2048\nv_mfma_f32_16x16x32_bf16 v[28:31], v[124:127], v[60:63], v[28:31]\nv_mfma_f32_16x16x32_bf16 v[36:39], a[44:47], v[60:63], v[36:39]\nv_mfma_f32_16x16x32_bf16 v[44:47], a[52:55], v[60:63], v[44:47]\ns_waitcnt vmcnt(12)\n" \
"buffer_load_dwordx4 v[60:63], v0, %[rs], s60 offen offset:3072 sc1\nv_mfma_f32_16x16x32_bf16 v[20:23], a[56:59], v[64:67], v[20:23]\nds_read_b128 v[124:127], v1 offset:3072\nv_mfma_f32_16x16x32_bf16 v[28:31], a[64:67], v[64:67], v[28:31]\nds_write_b128 v2, v[24:27] offset:2048\nv_mfma_f32_16x16x32_bf16 v[36:39], a[72:75], v[64:67], v[36:39]\ns_add_u32 s60, s55, 69632\nv_mfma_f32_16x16x32_bf16 v[44:47], a[80:83], v[64:67], v[44:47]\n" \
"s_waitcnt vmcnt(12)\nbuffer_load_dwordx4 v[64:67], v0, %[rs], s60 offen offset:1024 sc1\nv_mfma_f32_16x16x32_bf16 v[20:23], a[60:63], v[68:71], v[20:23]\nv_mfma_f32_16x16x32_bf16 v[28:31], a[68:71], v[68:71], v[28:31]\nv_mfma_f32_16x16x32_bf16 v[36:39], a[76:79], v[68:71], v[36:39]\nv_mfma_f32_16x16x32_bf16 v[44:47], a[84:87], v[68:71], v[44:47]\ns_waitcnt vmcnt(12)\nbuffer_load_dwordx4 v[68:71], v0, %[rs], s60 offen offset:3072 sc1\n" \
"v_mfma_f32_16x16x32_bf16 v[20:23], a[88:91], v[72:75], v[20:23]\nv_mfma_f32_16x16x32_bf16 v[28:31], a[96:99], v[72:75], v[28:31]\nds_write_b128 v2, v[32:35] offset:4096\nv_mfma_f32_16x16x32_bf16 v[36:39], a[104:107], v[72:75], v[36:39]\ns_waitcnt lgkmcnt(3)\ns_add_u32 s60, s55, 81920\nv_mfma_f32_16x16x32_bf16 v[44:47], v[120:123], v[72:75], v[44:47]\ns_waitcnt vmcnt(12)\n" \
"buffer_load_dwordx4 v[72:75], v0, %[rs], s60 offen offset:1024 sc1\nv_mfma_f32_16x16x32_bf16 v[20:23], a[92:95], v[76:79], v[20:23]\nds_read_b128 v[120:123], v1 offset:4096\nv_mfma_f32_16x16x32_bf16 v[28:31], a[100:103], v[76:79], v[28:31]\nv_mfma_f32_16x16x32_bf16 v[36:39], a[108:111], v[76:79], v[36:39]\ns_waitcnt lgkmcnt(3)\nv_mfma_f32_16x16x32_bf16 v[44:47], v[124:127], v[76:79], v[44:47]\ns_waitcnt vmcnt(12)\n" \
"buffer_load_dwordx4 v[76:79], v0, %[rs], s60 offen offset:3072 sc1\nv_mfma_f32_16x16x32_bf16 v[20:23], a[112:115], v[80:83], v[20:23]\nds_read_b128 v[124:127], v1 offset:5120\nv_mfma_f32_16x16x32_bf16 v[28:31], a[120:123], v[80:83], v[28:31]\nds_write_b128 v2, v[40:43] offset:6144\nv_mfma_f32_16x16x32_bf16 v[36:39], a[128:131], v[80:83], v[36:39]\ns_add_u32 s60, s55, 86016\nv_mfma_f32_16x16x32_bf16 v[44:47], a[136:139], v[80:83], v[44:47]\n" \
"s_waitcnt vmcnt(12)\nbuffer_load_dwordx4 v[80:83], v0, %[rs], s60 offen offset:1024 sc1\nv_mfma_f32_16x16x32_bf16 v[20:23], a[116:119], v[84:87], v[20:23]\nds_read_b128 v[112:115], v1 offset:6144\nv_mfma_f32_16x16x32_bf16 v[28:31], a[124:127], v[84:87], v[28:31]\nv_mfma_f32_16x16x32_bf16 v[36:39], a[132:135], v[84:87], v[36:39]\nv_mfma_f32_16x16x32_bf16 v[44:47], a[140:143], v[84:87], v[44:47]\ns_waitcnt vmcnt(12)\n" \
"buffer_load_dwordx4 v[84:87], v0, %[rs], s60 offen offset:3072 sc1\nv_mfma_f32_16x16x32_bf16 v[20:23], a[144:147], v[88:91], v[20:23]\ns_waitcnt lgkmcnt(3)\nds_read_b128 v[116:119], v1 offset:7168\nv_mfma_f32_16x16x32_bf16 v[28:31], v[120:123], v[88:91], v[28:31]\nv_mfma_f32_16x16x32_bf16 v[36:39], a[152:155], v[88:91], v[36:39]\ns_add_u32 s60, s55, 98304\nv_mfma_f32_16x16x32_bf16 v[44:47], a[160:163], v[88:91], v[44:47]\n" \
"s_waitcnt vmcnt(12)\nbuffer_load_dwordx4 v[88:91], v0, %[rs], s60 offen offset:1024 sc1\nv_mfma_f32_16x16x32_bf16 v[20:23], a[148:151], v[92:95], v[20:23]\ns_waitcnt lgkmcnt(3)\nv_mfma_f32_16x16x32_bf16 v[28:31], v[124:127], v[92:95], v[28:31]\nbuffer_load_dwordx4 v[12:15], v4, %[rs], s41 offen sc1\nv_mfma_f32_16x16x32_bf16 v[36:39], a[156:159], v[92:95], v[36:39]\nv_mfma_f32_16x16x32_bf16 v[44:47], a[164:167], v[92:95], v[44:47]\n" \
"s_waitcnt vmcnt(13)\nbuffer_load_dwordx4 v[92:95], v0, %[rs], s60 offen offset:3072 sc1\nv_mfma_f32_16x16x32_bf16 v[20:23], a[168:171], v[96:99], v[20:23]\nv_mfma_f32_16x16x32_bf16 v[28:31], a[176:179], v[96:99], v[28:31]\ns_waitcnt lgkmcnt(1)\nv_mfma_f32_16x16x32_bf16 v[36:39], v[112:115], v[96:99], v[36:39]\ns_add_u32 s60, s55, 102400\nv_mfma_f32_16x16x32_bf16 v[44:47], a[184:187], v[96:99], v[44:47]\n" \
"s_waitcnt vmcnt(13)\nbuffer_load_dwordx4 v[96:99], v0, %[rs], s60 offen offset:1024 sc1\nv_mfma_f32_16x16x32_bf16 v[20:23], a[172:175], v[100:103], v[20:23]\nds_read_b128 v[112:115], v1 offset:8192\nv_mfma_f32_16x16x32_bf16 v[28:31], a[180:183], v[100:103], v[28:31]\ns_waitcnt lgkmcnt(1)\nv_mfma_f32_16x16x32_bf16 v[36:39], v[116:119], v[100:103], v[36:39]\nv_mfma_f32_16x16x32_bf16 v[44:47], a[188:191], v[100:103], v[44:47]\n" \
"s_waitcnt vmcnt(13)\nbuffer_load_dwordx4 v[100:103], v0, %[rs], s60 offen offset:3072 sc1\nv_mfma_f32_16x16x32_bf16 v[20:23], a[192:195], v[48:51], v[20:23]\nds_read_b128 v[116:119], v1 offset:9216\nv_mfma_f32_16x16x32_bf16 v[28:31], a[200:203], v[48:51], v[28:31]\nv_mfma_f32_16x16x32_bf16 v[36:39], a[208:211], v[48:51], v[36:39]\nv_mfma_f32_16x16x32_bf16 v[44:47], a[216:219], v[48:51], v[44:47]\ns_waitcnt vmcnt(13)\n" \
"v_mfma_f32_16x16x32_bf16 v[20:23], a[196:199], v[52:55], v[20:23]\nv_mfma_f32_16x16x32_bf16 v[28:31], a[204:207], v[52:55], v[28:31]\nv_mfma_f32_16x16x32_bf16 v[36:39], a[212:215], v[52:55], v[36:39]\nv_mfma_f32_16x16x32_bf16 v[44:47], a[220:223], v[52:55], v[44:47]\ns_waitcnt vmcnt(12)\ns_waitcnt lgkmcnt(1)\nv_mfma_f32_16x16x32_bf16 v[20:23], v[112:115], v[56:59], v[20:23]\nv_mfma_f32_16x16x32_bf16 v[28:31], a[224:227], v[56:59], v[28:31]\n" \
"v_mfma_f32_16x16x32_bf16 v[36:39], a[232:235], v[56:59], v[36:39]\nv_mfma_f32_16x16x32_bf16 v[44:47], a[240:243], v[56:59], v[44:47]\ns_waitcnt vmcnt(11)\ns_waitcnt lgkmcnt(0)\nv_mfma_f32_16x16x32_bf16 v[20:23], v[116:119], v[60:63], v[20:23]\nds_read_b128 v[112:115], v1 offset:10240\nv_mfma_f32_16x16x32_bf16 v[28:31], a[228:231], v[60:63], v[28:31]\nv_mfma_f32_16x16x32_bf16 v[36:39], a[236:239], v[60:63], v[36:39]\n" \
"v_mfma_f32_16x16x32_bf16 v[44:47], a[244:247], v[60:63], v[44:47]\ns_waitcnt vmcnt(10)\nv_mfma_f32_16x16x32_bf16 v[20:23], a[248:251], v[64:67], v[20:23]\nds_read_b128 v[116:119], v1 offset:11264\nv_mfma_f32_16x16x32_bf16 v[28:31], v[128:131], v[64:67], v[28:31]\nv_mfma_f32_16x16x32_bf16 v[36:39], v[136:139], v[64:67], v[36:39]\nv_mfma_f32_16x16x32_bf16 v[44:47], v[144:147], v[64:67], v[44:47]\ns_waitcnt vmcnt(9)\n" \
"v_mfma_f32_16x16x32_bf16 v[20:23], a[252:255], v[68:71], v[20:23]\nv_mfma_f32_16x16x32_bf16 v[28:31], v[132:135], v[68:71], v[28:31]\nv_mfma_f32_16x16x32_bf16 v[36:39], v[140:143], v[68:71], v[36:39]\nv_mfma_f32_16x16x32_bf16 v[44:47], v[148:151], v[68:71], v[44:47]\ns_waitcnt vmcnt(8)\nv_mfma_f32_16x16x32_bf16 v[20:23], v[152:155], v[72:75], v[20:23]\nv_mfma_f32_16x16x32_bf16 v[28:31], v[160:163], v[72:75], v[28:31]\ns_waitcnt lgkmcnt(1)\n" \
"v_mfma_f32_16x16x32_bf16 v[36:39], v[112:115], v[72:75], v[36:39]\nv_mfma_f32_16x16x32_bf16 v[44:47], v[168:171], v[72:75], v[44:47]\ns_waitcnt vmcnt(7)\nv_mfma_f32_16x16x32_bf16 v[20:23], v[156:159], v[76:79], v[20:23]\nds_read_b128 v[112:115], v1 offset:12288\nv_mfma_f32_16x16x32_bf16 v[28:31], v[164:167], v[76:79], v[28:31]\ns_waitcnt lgkmcnt(1)\nv_mfma_f32_16x16x32_bf16 v[36:39], v[116:119], v[76:79], v[36:39]\n" \
"v_mfma_f32_16x16x32_bf16 v[44:47], v[172:175], v[76:79], v[44:47]\ns_waitcnt vmcnt(6)\nv_mfma_f32_16x16x32_bf16 v[20:23], v[176:179], v[80:83], v[20:23]\nds_read_b128 v[116:119], v1 offset:13312\nv_mfma_f32_16x16x32_bf16 v[28:31], v[184:187], v[80:83], v[28:31]\nv_mfma_f32_16x16x32_bf16 v[36:39], v[192:195], v[80:83], v[36:39]\nv_mfma_f32_16x16x32_bf16 v[44:47], v[200:203], v[80:83], v[44:47]\ns_waitcnt vmcnt(5)\n" \
"v_mfma_f32_16x16x32_bf16 v[20:23], v[180:183], v[84:87], v[20:23]\nds_read_b128 v[120:123], v1 offset:14336\nv_mfma_f32_16x16x32_bf16 v[28:31], v[188:191], v[84:87], v[28:31]\nv_mfma_f32_16x16x32_bf16 v[36:39], v[196:199], v[84:87], v[36:39]\nv_mfma_f32_16x16x32_bf16 v[44:47], v[204:207], v[84:87], v[44:47]\ns_waitcnt vmcnt(4)\ns_waitcnt lgkmcnt(2)\nv_mfma_f32_16x16x32_bf16 v[20:23], v[112:115], v[88:91], v[20:23]\n" \
"ds_read_b128 v[124:127], v1 offset:15360\nv_mfma_f32_16x16x32_bf16 v[28:31], v[208:211], v[88:91], v[28:31]\nv_mfma_f32_16x16x32_bf16 v[36:39], v[216:219], v[88:91], v[36:39]\nv_mfma_f32_16x16x32_bf16 v[44:47], v[224:227], v[88:91], v[44:47]\ns_waitcnt vmcnt(2)\ns_waitcnt lgkmcnt(2)\nv_mfma_f32_16x16x32_bf16 v[20:23], v[116:119], v[92:95], v[20:23]\nv_mfma_f32_16x16x32_bf16 v[28:31], v[212:215], v[92:95], v[28:31]\n" \
"v_mfma_f32_16x16x32_bf16 v[36:39], v[220:223], v[92:95], v[36:39]\nv_mfma_f32_16x16x32_bf16 v[44:47], v[228:231], v[92:95], v[44:47]\ns_waitcnt vmcnt(1)\nv_mfma_f32_16x16x32_bf16 v[20:23], v[232:235], v[96:99], v[20:23]\ns_waitcnt lgkmcnt(1)\nv_mfma_f32_16x16x32_bf16 v[28:31], v[120:123], v[96:99], v[28:31]\nv_mfma_f32_16x16x32_bf16 v[36:39], v[240:243], v[96:99], v[36:39]\nv_mfma_f32_16x16x32_bf16 v[44:47], v[248:251], v[96:99], v[44:47]\n" \
"s_waitcnt vmcnt(0)\nv_mfma_f32_16x16x32_bf16 v[20:23], v[236:239], v[100:103], v[20:23]\ns_waitcnt lgkmcnt(0)\nv_lshlrev_b32 v104, 16, v12\nv_and_b32 v105, 0xffff0000, v12\nv_mfma_f32_16x16x32_bf16 v[28:31], v[124:127], v[100:103], v[28:31]\nv_lshlrev_b32 v106, 16, v13\nv_and_b32 v107, 0xffff0000, v13\n" \
"v_mfma_f32_16x16x32_bf16 v[36:39], v[244:247], v[100:103], v[36:39]\nv_lshlrev_b32 v108, 16, v14\nv_and_b32 v109, 0xffff0000, v14\nv_mfma_f32_16x16x32_bf16 v[44:47], v[252:255], v[100:103], v[44:47]\ns_mul_i32 s44, s40, %[l]\nds_write_b128 v2, v[20:23] offset:1024\nds_write_b128 v2, v[28:31] offset:3072\ns_nop 0\n" \
"ds_write_b128 v2, v[36:39] offset:5120\ns_nop 2\nds_write_b128 v2, v[44:47] offset:7168\nv_lshlrev_b32 v110, 16, v15\nv_and_b32 v111, 0xffff0000, v15\nv_mov_b32 v9, s40\ns_cmp_eq_u32 s44, 0\ns_cselect_b32 s56, %[fd], -2147483648\n" \
"s_add_u32 s40, s40, 1\ns_waitcnt lgkmcnt(0)\ns_barrier\nds_read_b128 v[16:19], v3 offset:0\nds_read_b128 v[20:23], v3 offset:256\nds_read_b128 v[24:27], v3 offset:8192\nds_read_b128 v[28:31], v3 offset:8448\nds_read_b128 v[32:35], v3 offset:16384\n" \
"ds_read_b128 v[36:39], v3 offset:16640\nds_read_b128 v[40:43], v3 offset:24576\nds_read_b128 v[44:47], v3 offset:24832\ns_waitcnt lgkmcnt(6)\nv_pk_add_f32 v[16:17], v[16:17], v[104:105]\nv_pk_add_f32 v[18:19], v[18:19], v[106:107]\nv_pk_add_f32 v[20:21], v[20:21], v[108:109]\nv_pk_add_f32 v[22:23], v[22:23], v[110:111]\n" \
"s_waitcnt lgkmcnt(4)\nv_pk_add_f32 v[16:17], v[16:17], v[24:25]\nv_pk_add_f32 v[18:19], v[18:19], v[26:27]\nv_pk_add_f32 v[20:21], v[20:21], v[28:29]\nv_pk_add_f32 v[22:23], v[22:23], v[30:31]\ns_waitcnt lgkmcnt(2)\nv_pk_add_f32 v[16:17], v[16:17], v[32:33]\nv_pk_add_f32 v[18:19], v[18:19], v[34:35]\n" \
"v_pk_add_f32 v[20:21], v[20:21], v[36:37]\nv_pk_add_f32 v[22:23], v[22:23], v[38:39]\ns_waitcnt lgkmcnt(0)\nv_pk_add_f32 v[16:17], v[16:17], v[40:41]\nv_pk_add_f32 v[18:19], v[18:19], v[42:43]\nv_pk_add_f32 v[20:21], v[20:21], v[44:45]\nv_pk_add_f32 v[22:23], v[22:23], v[46:47]\nv_cvt_pk_bf16_f32 v12, v16, v17\n" \
"v_cvt_pk_bf16_f32 v13, v18, v19\nv_cvt_pk_bf16_f32 v14, v20, v21\nv_cvt_pk_bf16_f32 v15, v22, v23\nbuffer_store_dwordx4 v[12:15], v5, %[ro], s42 offen sc1\nv_pk_add_f32 v[24:25], v[16:17], v[18:19]\nv_pk_add_f32 v[26:27], v[20:21], v[22:23]\nv_pk_add_f32 v[24:25], v[24:25], v[26:27]\nv_add_f32 v24, v24, v25\n" \
"s_nop 1\nv_add_f32_dpp v25, v24, v24 row_ror:8 row_mask:0xf bank_mask:0xf\nv_add_f32_dpp v26, v24, v24 row_ror:8 row_mask:0xf bank_mask:0xf\ns_nop 1\nv_permlane16_swap_b32 v25, v26\nv_add_f32 v24, v25, v26\nv_add_f32 v25, v25, v26\ns_nop 1\n" \
"v_permlane32_swap_b32 v24, v25\nv_add_f32 v24, v24, v25\nv_mul_f32 v24, 0x3c800000, v24\nv_pk_add_f32 v[16:17], v[16:17], v[24:25] op_sel_hi:[1,0] neg_lo:[0,1] neg_hi:[0,1]\nv_pk_add_f32 v[18:19], v[18:19], v[24:25] op_sel_hi:[1,0] neg_lo:[0,1] neg_hi:[0,1]\nv_pk_add_f32 v[20:21], v[20:21], v[24:25] op_sel_hi:[1,0] neg_lo:[0,1] neg_hi:[0,1]\nv_pk_add_f32 v[22:23], v[22:23], v[24:25] op_sel_hi:[1,0] neg_lo:[0,1] neg_hi:[0,1]\nv_pk_mul_f32 v[28:29], v[16:17], v[16:17]\n" \
"v_pk_mul_f32 v[30:31], v[18:19], v[18:19]\nv_pk_fma_f32 v[28:29], v[20:21], v[20:21], v[28:29]\nv_pk_fma_f32 v[30:31], v[22:23], v[22:23], v[30:31]\nv_pk_add_f32 v[28:29], v[28:29], v[30:31]\nv_add_f32 v28, v28, v29\ns_nop 1\nv_add_f32_dpp v29, v28, v28 row_ror:8 row_mask:0xf bank_mask:0xf\nv_add_f32_dpp v30, v28, v28 row_ror:8 row_mask:0xf bank_mask:0xf\n" \
"s_nop 1\nv_permlane16_swap_b32 v29, v30\nv_add_f32 v28, v29, v30\nv_add_f32 v29, v29, v30\ns_nop 1\nv_permlane32_swap_b32 v28, v29\nv_add_f32 v28, v28, v29\nv_mov_b32 v25, v28\n" \
"buffer_store_dwordx2 v[24:25], v6, %[rs], s41 offen sc1\nv_xor_b32 v2, 0x8000, v2\nv_xor_b32 v3, 0x8000, v3\ns_cmp_lt_u32 s40, 64\ns_cbranch_scc1 .Ldd_next%=\ns_waitcnt vmcnt(0)\nbuffer_store_dword v9, off, %[rs], s56 sc1\ns_branch .Ldd_end%=\n" \
".Ldd_slow%=:\n.Ldd_w1%=:\nv_cmp_gt_i32 vcc, s58, v8\ns_cbranch_vccz .Ldd_w1go%=\ns_cmp_ge_u32 s40, 62\ns_cbranch_scc1 .Ldd_ns1228%=\ns_sleep 8\n.Ldd_ns1228%=:\n" \
"s_sleep 1\nbuffer_load_dword v8, v7, %[rs], 0 offen sc1\ns_waitcnt vmcnt(0)\ns_add_u32 s43, s43, 1\ns_cmp_lt_u32 s43, 262144\ns_cbranch_scc1 .Ldd_w1%=\ns_branch .Ldd_abort%=\n.Ldd_w1go%=:\n" \
"s_add_u32 s60, s55, 0\nbuffer_load_dwordx4 v[48:51], v0, %[rs], s60 offen offset:1024 sc1\nbuffer_load_dwordx4 v[52:55], v0, %[rs], s60 offen offset:3072 sc1\ns_add_u32 s60, s55, 4096\nbuffer_load_dwordx4 v[56:59], v0, %[rs], s60 offen offset:1024 sc1\nbuffer_load_dwordx4 v[60:63], v0, %[rs], s60 offen offset:3072 sc1\ns_add_u32 s60, s55, 16384\nbuffer_load_dwordx4 v[64:67], v0, %[rs], s60 offen offset:1024 sc1\n" \
"buffer_load_dwordx4 v[68:71], v0, %[rs], s60 offen offset:3072 sc1\ns_add_u32 s60, s55, 20480\nbuffer_load_dwordx4 v[72:75], v0, %[rs], s60 offen offset:1024 sc1\nbuffer_load_dwordx4 v[76:79], v0, %[rs], s60 offen offset:3072 sc1\ns_add_u32 s60, s55, 32768\nbuffer_load_dwordx4 v[80:83], v0, %[rs], s60 offen offset:1024 sc1\nbuffer_load_dwordx4 v[84:87], v0, %[rs], s60 offen offset:3072 sc1\ns_add_u32 s60, s55, 36864\n" \
"buffer_load_dwordx4 v[88:91], v0, %[rs], s60 offen offset:1024 sc1\nbuffer_load_dwordx4 v[92:95], v0, %[rs], s60 offen offset:3072 sc1\ns_add_u32 s60, s55, 49152\nbuffer_load_dwordx4 v[96:99], v0, %[rs], s60 offen offset:1024 sc1\nbuffer_load_dwordx4 v[100:103], v0, %[rs], s60 offen offset:3072 sc1\ns_branch .Ldd_slowr%=\n.Ldd_abort%=:\ns_mov_b64 exec, -1\n" \
"v_mov_b32 v16, 0\ns_movk_i32 s44, 6400\nbuffer_store_dword v16, off, %[rs], s44 sc1\n.Ldd_end%=:\ns_waitcnt vmcnt(0) lgkmcnt(0)\n" \
""
#define GSW 0
#define PX4 1
#define QX4 7
#define G1F 1
#define A1F 0

#define V10(p, x) p #x "0", p #x "1", p #x "2", p #x "3", p #x "4", p #x "5", p #x "6", p #x "7", p #x "8", p #x "9"
#define V256(p) V10(p, ), V10(p, 1), V10(p, 2), V10(p, 3), V10(p, 4), V10(p, 5), V10(p, 6), V10(p, 7), V10(p, 8), V10(p, 9), V10(p, 10), V10(p, 11), V10(p, 12), V10(p, 13), V10(p, 14), V10(p, 15), V10(p, 16), \
    V10(p, 17), V10(p, 18), V10(p, 19), V10(p, 20), V10(p, 21), V10(p, 22), V10(p, 23), V10(p, 24), p "250", p "251", p "252", p "253", p "254", p "255"
#define CLOB "memory", "vcc", "scc", V256("v"), V256("a"), V10("s", 4), V10("s", 5), V10("s", 6), V10("s", 7), V10("s", 8)
template <int N> DEVI void table(lds_char* p, const unsigned (&t)[N]) {
#pragma unroll
    for (int i = 0; i < N; ++i) *(l1*)(p + i * 256) = t[i];
    WL0;
}
DEVI unsigned prof_at(int cu, int w) { return O_PROF + (262144 + cu * 2560 + w * 8) * 4; }
DEVI unsigned sp_idx(int lane) { return (lane & 2) ? 0xEEEEu : 0x4444u; }
DEVI int sp_row(int r, int hf) { return 4 * (r >> 1) + 2 * hf + (r & 1); }

DEVI void qk_role(const Args& a, const int w, lds_char* smem, int l, int h) {
    const int lane = lane_id(), g = lane >> 4, n = lane & 15, b = n & 3, p = n >> 2;
    const unsigned l0 = (unsigned)(size_t)smem, tb = l0 + w * 12288, lw = l0 + 55296 + w * 24576, ls = l * SD, IMG = l0 + 32768, MIM = l0 + 49152;
    const unsigned xw = (w >> 1) * 16384 + (w & 1) * 4096, oc = (h >> 3) * 16384 + ls, tr = (8 * g + p) * 128 + b * 16;
    const unsigned cm[4] = {4 * g > n ? 0xF149F2CAu : 0u, 4 * g + 1 > n ? 0xF149F2CAu : 0u, 4 * g + 2 > n ? 0xF149F2CAu : 0u, 4 * g + 3 > n ? 0xF149F2CAu : 0u};
    const unsigned t[19] = {
        l0 + lane * 16, lw + lane * 16, l0 + (g >> 1) * 1024 + (g & 1) * 512 + n * 16, O_E1 + ls + xw + lane * 16, sp_idx(lane),
        IMG + (w >> 1) * 8192 + ((w & 1) * 8 + 2 * g) * 512 + n * 16,
        MIM + (w * 8 + 2 * p) * 144 + g * 32 + b * 4,
        IMG + g * 512 + (w ? n * 16 : 8192 + (16 * (b >> 1) + 8 * (p >> 1) + 2 * (p & 1) + (b & 1)) * 16),
        MIM + g * 144 + (n & 7) * 16, MIM + 2304 + g * 144 + (2 * p + (b & 1)) * 16,
        w ? O_PT + (h >> 3) * 32768 + ls + lane * 16 + (w == 1 ? 16384 + ((h & 7) >> 1) * 1024 + (h & 1) * 8 : (h & 7) * 1024)
          : (QX4 & 1) && n >> 3 ? OOB : O_PM + oc + ((h >> 1) & 3) * 1024 + ((2 * (h & 1) + (g >> 1)) * 16 + sp_row(n & 7, g & 1)) * 16 + (n >> 3) * 8,
        w ? (QX4 & 4) && g ? OOB : O_PT + (h >> 3) * 32768 + ls + 20480 + ((h >> 2) & 1) * 2048 + (w - 1) * 1024 + (h & 3) * 256 + n * 16 + g * 4
          : n >> 3 || ((QX4 & 2) && g >> 1) ? OOB : O_PM + oc + 4096 + ((h & 7) >> 1) * 256 + sp_row(n & 7, g & 1) * 16 + (h & 1) * 8 + (g >> 1) * 4,
        w == 3 ? (l ? F_X + SD + p * 128 + (X1F ? 0 : b * 4) : F_X0 + b * 4) : cm[0], cm[1], cm[2], cm[3],
        (lane >> 3) * 8192u + (lane & 7) * 16, tb + tr, lw + tr};
    table(smem + w * 12288 + lane * 4, t);
    asm volatile(QK_ASM : : [ws] "s"(a.ws), [sz] "n"(WS_RS), [rw] "s"(mkrs(w < 2 ? a.Wq : a.Wk, 8u << 20)), [wo] "s"(h * 256 + (w & 1) * 128), [tb] "s"(tb), [lw] "s"(lw), [l0] "s"(l0), [w] "s"(w),
        [xn] "s"(O_XN + ls), [xw] "s"(xw), [fs] "s"(l ? 0 : 128), [sg] "s"(F_Y0 + 4096 + ls + h * 128 + 16), [pr] "s"(prof_at(l * 80 + h, w)) : CLOB, "s36", "s37", "s38", "s39");
    __builtin_amdgcn_endpgm();
}
DEVI void vu_role(const Args& a, const int w, lds_char* smem, int l, int h) {
    const int lane = lane_id(), g = lane >> 4, n = lane & 15, b = n & 3, p = n >> 2;
    const unsigned l0 = (unsigned)(size_t)smem, tb = l0 + w * 12288, lw = l0 + 61440 + w * 24576, ls = l * SD;
    const unsigned xw = (w >> 1) * 16384 + (w & 1) * 4096, tv = (8 * g + p) * 128 + b * 16 + (w & 1) * 64, to = (4 * g + (p & 1)) * 128 + (p >> 1) * 32 + b * 8;
    const unsigned t[18] = {
        l0 + lane * 16, lw + lane * 16, l0 + (g >> 1) * 1024 + (g & 1) * 512 + n * 16, O_E1 + ls + xw + lane * 16, sp_idx(lane),
        l0 + 57344 + (2 * w + (n >> 3)) * 256 + (n >> 3) * 128 + g * 32 + (n & 7) * 2,
        O_UM + ls + (4 * w + (g & 1)) * 65536 + (h >> 3) * 16384 + (g >> 1) * 2048 + (h & 7) * 256 + n * 16,
        sd(O_Y + h * 131072 + ls) + w * 32768 + (g >> 1) * 1024 + (g & 1) * 256 + n * 16,
        l ? F_X + SD + p * 128 + (X1F ? 0 : b * 4) : F_X0 + b * 4, l0 + 49152 + (w >> 1) * 2048 + (2 * (w & 1) + (g >> 1)) * 256 + (n ^ (g >> 1) * 8) * 16 + (g & 1) * 8, sp_idx(lane >> 2), l0 + g * 256 + (g & 1) * 128 + (n & 7) * 16,
        (lane >> 3) * 8192u + (lane & 7) * 16, (8 * (lane >> 5) + ((lane >> 3) & 3)) * 2048u + (lane & 7) * 16, tb + tv - (w & 1) * 12288, lw + tv - (w & 1) * 24576, tb + to, lw + to};
    table(smem + w * 12288 + lane * 4, t);
    asm volatile(VU_ASM : : [ws] "s"(a.ws), [sz] "n"(WS_RS), [rw] "s"(mkrs(a.Wv, 8u << 20)), [ro] "s"(mkrs(a.Wo, 8u << 20)), [wo] "s"(h * 256 + (w >> 1) * 128 + (w & 1) * 262144), [w2] "s"(h * 262144 + w * 512), [tb] "s"(tb), [lw] "s"(lw), [w] "s"(w),
        [xn] "s"(O_XN + ls), [xw] "s"(xw), [fs] "s"(l ? 0 : 128), [xt] "s"(w * 16384 - xw), [fy] "s"(F_Y0 + ls + h * 128 + w * 4), [sg] "s"(w == 1 ? F_Y0 + ls + h * 128 + 16 : OOB), [pr] "s"(prof_at(l * 80 + 32 + h, w) - 128) : CLOB, "s36", "s37", "s38", "s39");
    __builtin_amdgcn_endpgm();
}
template <class F> DEVI void rd_role(const Args& a, const int w, lds_char* smem, int l, int c, F&& idle_work) {
    const int lane = lane_id(), g = lane >> 4, n = lane & 15, r = 2 * (n >> 2) + (n & 1), hf = (n >> 1) & 1;
    const rsrc_t rs = mkrs(a.ws, WS_RS);
    const unsigned l0 = (unsigned)(size_t)smem, tb = l0 + 65536 + w * 4096, ls = l * SD, vp = O_E1 + ls + (c >> 3) * 16384 + (c & 7) * 1024 + w * 256 + n * 16;
    {
        const v4u ff = {SENT, SENT, SENT, SENT};
#pragma unroll
        for (int i = 0; i < 8; ++i) st16(ff, rs, O_UM + ls + c * 65536 + w * 16384 + i * 1024 + lane * 16);
#pragma unroll
        for (int i = 0; i < 2; ++i) st16(ff, rs, O_PT + ls + c * 16384 + w * 2048 + i * 1024 + lane * 16);
        st16(ff, rs, c < 8 ? O_PM + ls + w * 16384 + c * 1024 + lane * 16 : OOB);
        st16(ff, rs, g ? vp + g * 32768 : OOB);
        WV0; BAR;
    }
    st4(g & 1 ? *(const unsigned*)(a.mem0 + r * 1024 + c * 64 + w * 16 + 4 * g + 2 * hf) : nosent(*(const unsigned*)(a.mem0 + r * 1024 + c * 64 + w * 16 + 4 * g + 2 * hf)), rs, g & 1 ? vp + g * 4 : OOB, 0);
    WV0;
    st4(nosent(*(const unsigned*)(a.mem0 + r * 1024 + c * 64 + w * 16 + 4 * g + 2 * hf)), rs, g & 1 ? OOB : vp + g * 4, 0);
    idle_work();
    const int gi = lane >> 3, r8 = lane & 7;
    const unsigned t[12] = {
        (unsigned)lane * 16, sp_idx(lane), l0 + w * 4096 + g * 576 + ((n >> 3) * 16 + hf * 4 + 2 * ((n >> 2) & 1) + (n & 1)) * 4,
        l0 + (r >> 1) * 576 + w * 128 + (g >> 1) * 64 + (r & 1) * 32 + hf * 16,
        PX4 && g >> 1 ? OOB : (vp ^ ((g & 1) * 65536)) + (g >> 1) * 8, g & 1 ? SENT : 0u,
        l0 + 16384 + w * 8192 + lane * 16, l0 + 16384 + ((gi >> 1) * 2 + (w >> 1)) * 1024 + ((gi & 1) * 32 + (w & 1) * 8 + r8) * 16,
        (c >> 1) * 16384u + (c & 1) * 4096 + gi * 512 + (w * 8 + r8) * 16,
        gi ? OOB : O_ST - O_XR + c * 256 + ((w & 1) * 8 + r8) * 16 + (w >> 1) * 8,
        F_Y0 + ls + (w * 8 + r8) * 128 + (c >> 2) * 4, F_Y0 + ls + (gi & 1) * 4096 + (w * 8 + r8) * 128 + 16};
    table(smem + 65536 + w * 4096 + lane * 4, t);
    asm volatile(RD_ASM : : [rs] "s"(rs), [tb] "s"(tb), [su] "s"(O_UM + ls + c * 65536 + w * 16384), [sp] "s"(O_PM + ls + w * 16384), [st] "s"(O_PT + ls + w * 32768),
        [sy] "s"(sd(O_Y + w * (1u << 20) + ls) + (c >> 1) * 16384 + (c & 1) * 4096), [at] "s"(O_XR + ls), [du] "s"((c & 2) * 5 / 2), [fa] "s"(A1F && w ? OOB : F_A + ls + c * 128 + w * 4), [pr] "s"(prof_at(l * 80 + 64 + c, w) + 128) : CLOB);
    __builtin_amdgcn_endpgm();
}
DEVI void gg_role(const Args& a, const int w, lds_char* smem, int l, int gi) {
    const int lane = lane_id(), g = lane >> 4, n = lane & 15, b = n & 3, p = n >> 2;
    const unsigned l0 = (unsigned)(size_t)smem, tb = l0 + w * 12288, lw = l0 + 65536 + w * 24576, ls = l * SD, tv = (8 * g + p) * 128 + b * 16 + (w & 1) * 64, fh = (g >> 1) * 1024 + (g & 1) * 512 + n * 16;
    const unsigned lf = GSW ? 0 : ls, fo = w ? OOB : F_H + (gi / 7) * 1024 + (gi % 7) * 128;
    const unsigned t[9] = {l0 + lane * 16, lw + lane * 16, l0 + fh, w ? OOB : F_A + lf + (lane >> 2) * 128 + (A1F ? 0 : (lane & 3) * 4), w * 2048u + lane * 16, O_ST - O_XR + g * 1024 + n * 16, (lane >> 3) * 7168u + (lane & 7) * 16, tb + tv - (w & 1) * 12288, lw + tv - (w & 1) * 24576};
    table(smem + w * 12288 + lane * 4, t);
    asm volatile(GG_ASM : : [rs] "s"(mkrs(a.ws, WS_RS)), [rw] "s"(mkrs(w & 1 ? a.Wu : a.Wg, 3584 * 2048)), [wo] "s"(gi * 256 + (w >> 1) * 128), [tb] "s"(tb), [lw] "s"(lw), [w] "s"(w),
        [xn0] "s"(O_XR + lf), [xw] "s"((w >> 1) * 16384 + (w & 1) * 4096), [ho0] "s"(O_H + lf + gi * 16384), [fh0] "s"(fo + (GSW ? l * 4 : ls)), [fh1] "s"(fo + ls), [b0] "s"(l ? 0 : GSW), [b1] "s"(l && GSW ? GSW : 64), [pr] "s"(prof_at(160 + l * 28 + gi, w) + 128) : CLOB);
    __builtin_amdgcn_endpgm();
}
DEVI void dd_role(const Args& a, const int w, lds_char* smem, int l, int c) {
    const int lane = lane_id(), g = lane >> 4, b = lane & 3, p = (lane >> 2) & 3, gi = lane >> 3, r8 = lane & 7;
    const unsigned l0 = (unsigned)(size_t)smem, tb = l0 + w * 12288, lw = l0 + 65536 + w * 24576, ls = l * SD, tr = (8 * g + p) * 128 + b * 8;
    const unsigned ta = (c >> 1) * 16384u + (c & 1) * 4096 + gi * 512 + (w * 8 + r8) * 16;
    const rsrc_t rs = mkrs(a.ws, WS_RS);
    const unsigned t[11] = {(unsigned)lane * 16, lw + lane * 16, l0 + w * 8192 + lane * 16, l0 + ((gi >> 1) * 2 + (w >> 1)) * 1024 + ((gi & 1) * 32 + (w & 1) * 8 + r8) * 16,
        ta, l ? (w * 8 + r8) * 2048 + c * 128 + gi * 16 : ta, gi || l ? OOB : O_ST - O_XR + 4096 + c * 256 + (w * 8 + r8) * 8,
        F_H + ls + w * 1024 + ((lane >> 2) % 7) * 128 + (GSW && !l ? 4 : 0), gi * 2048u + r8 * 16, tb + tr, lw + tr};
    table(smem + w * 12288 + lane * 4, t);
    asm volatile(DD_ASM : : [rs] "s"(rs), [rw] "s"(mkrs(a.Wd, 3584 * 2048)), [ro] "s"(mkrs(l ? (char*)a.out : a.ws, l ? 2048 * 2048 : WS_RS)), [wo] "s"(w * (896 * 2048) + c * 128), [tb] "s"(tb), [lw] "s"(lw), [l] "s"(l), [b1] "s"(GSW && !l ? GSW : 99),
        [xn] "s"(O_XR + ls), [ho] "s"(O_H + ls + w * 7 * 16384), [fd] "s"(D1F && w ? OOB : F_D + ls + c * 128 + w * 4), [lt] "s"(F_D + ls + c * 128 + w * 4 + 64), [pr] "s"(prof_at(216 + l * 16 + c, w) + 128) : CLOB);
    __builtin_amdgcn_endpgm();
}

#define PFG 0
#define PFT 1000
#define PFX 0
#define CPF 74
#define CPJ 0
#define LNW 0
#define DLQ 150
#define DLS 400
#define SPL 0
#define PLAN 3
#define PFA 0
#define PFB 3
#define PFL 1
#define VUX 1
#define LNS 40
#define LN1 700
#define DLG 1100
#define DLD 1500
#define DL1 1500
#define DLG1 2600
#define DLD1 3000
DEVI Args args(unsigned sh) {
    auto p = (const __attribute__((address_space(4))) Args*)__builtin_amdgcn_kernarg_segment_ptr();
    asm volatile("" : "+s"(p));
    Args a;
    a.x = p->x; a.mem0 = p->mem0; a.Wq = p->Wq; a.Wk = p->Wk; a.Wv = p->Wv; a.Wo = p->Wo; a.Wg = p->Wg; a.Wu = p->Wu; a.Wd = p->Wd; a.ws = p->ws + sh; a.out = p->out;
    return a;
}
DEVI unsigned beat_wait(unsigned x0) {
    const rsrc_t r0 = mkrs(args(0).ws, 2u << 20);
    for (unsigned spins = 0; spins < SPIN_CAP; ++spins) {
        CB;
        const v2u t = __builtin_amdgcn_raw_buffer_load_b64(r0, 0, O_HB, AUX_SC1);
        if (RFL(t[0]) != x0) return RFL(t[1]) & (2u << 20);
        __builtin_amdgcn_s_sleep(8);
    }
    __builtin_amdgcn_endpgm();
}
#define MPF 0
DEVI void mpf(rsrc_t rs, unsigned o) {
    o += O_E1 + lane_id() * 128;
    unsigned spins = 0;
    for (int s = 0; s < NSEG && spins < SPIN_CAP; ++spins) {
        CB;
        if (__ballot(ld4(rs, o, (s & 3) * 32768) == SENT) == 0ull) { ++s; for (int i = 0; i < MPF; ++i) __builtin_amdgcn_s_sleep(8); }
    }
}
DEVI void leader(const Args& a, const int w, lds_char* smem, unsigned x0) {
    const int lane = lane_id();
    const rsrc_t r0 = mkrs(a.ws, 4u << 20);
    unsigned t[2] = {~0u, ~0u};
    for (int i = 0; i < 2 * NPRB; ++i) {
        const unsigned t0 = (unsigned)__builtin_amdgcn_s_memtime();
        WL0;
        if (lane == 0) st4(0u, r0, 0, O_PRB + (i & 1) * SD + w * 1024 + (i >> 1) * 128);
        WV0;
        const unsigned d = (unsigned)__builtin_amdgcn_s_memtime() - t0;
        if (i & 1) t[1] = min(t[1], d); else t[0] = min(t[0], d);
    }
    *(l2*)(smem + w * 8) = v2u{t[0], t[1]};
    WL0; BAR;
    const v4u p = *(l4*)smem, q = *(l4*)(smem + 16);
    const unsigned sh = RFL(min(min(p[0], p[2]), min(q[0], q[2])) < min(min(p[1], p[3]), min(q[1], q[3])) ? 2u << 20 : 0u);
    const unsigned o = sh + w * 1024 + lane * 16;
    const v4u ff = {SENT, SENT, SENT, SENT};
#pragma unroll
    for (int i = 0; i < 8; ++i) st16(ff, r0, o + O_E1 + i * 4096);
#pragma unroll
    for (int i = 1; i < 16; ++i) if (i != 2 && i != 11) st16(ff, r0, o + i * 4096);
    WV0; BAR;
    if (w) { if (MPF && w > 1) mpf(r0, sh + SD + (w & 1) * 16384); return; }
    for (unsigned spins = 0; spins < SPIN_CAP; ++spins) {
        if (lane == 0) st8(v2u{++x0, sh}, r0, 0, O_HB);
        if (__ballot((int)ld4(r0, sh + F_D + SD + (lane >> 2) * 128 + (D1F ? 0 : (lane & 3) * 4), 0) < 0) == 0ull) break;
        __builtin_amdgcn_s_sleep(16);
    }
}
__global__ __launch_bounds__(256, 1) void sys_kernel(Args) {
    extern __shared__ __attribute__((aligned(16))) char smem_[];
    lds_char* smem = (lds_char*)smem_;
    const int x = blockIdx.x & 7, j = blockIdx.x >> 3, wv = RFL(threadIdx.x >> 6), l = x >> 2, xx = x & 3;
    const unsigned t0 = (unsigned)__builtin_amdgcn_s_memrealtime();
    auto until = [&](unsigned dl) AI { while ((unsigned)__builtin_amdgcn_s_memrealtime() - t0 < dl) __builtin_amdgcn_s_sleep(8); };
    unsigned x0 = 0;
    if (l || j >= 16) x0 = RFL(ld4(mkrs(args(0).ws, 2u << 20), 0, O_HB));
    if (j < 16) {
        if (l) { until(j ? LN1 + j * LNS : 0); ln0(args(0), wv, j * 4 + xx, [&]() AI { return beat_wait(x0); }); until(DL1); }
        if (DLQ && !l && j < 8) until(DLQ);
        if (j < 8) qk_role(args(0), wv, smem, l, xx * 8 + j); else vu_role(args(0), wv, smem, l, (xx ^ VUX) * 8 + j - 8);
        return;
    }
    if (j >= 16 + CPJ && j < 20 + CPJ) {
        unsigned long long pc; unsigned n;
        asm volatile("s_getpc_b64 %0\n\ts_mov_b32 %1, .-_Z10sys_kernel4Args" : "=s"(pc), "=s"(n));
        const rsrc_t rc = mkrs((const void*)(pc - n), CPF * 1024);
        __builtin_amdgcn_raw_ptr_buffer_load_lds(rc, smem + LDSB - 1024 + wv * 256, 4, lane_id() * 128, ((j - 16 - CPJ) * 4 + wv) * 8192, 0, 0);
    }
    {
        const int lane = lane_id(), q = (j - 16) * 4 + wv, m = SPL && !l ? ((xx & 1) != (SPL & 1)) * 2 + (q >> 5) : q >> 4;
        const Args a = args(0);
        const u16 *W0 = a.Wq, *W1 = a.Wk, *W2 = a.Wv, *W3 = a.Wo;
        asm volatile("" : "+s"(W0), "+s"(W1), "+s"(W2), "+s"(W3));
        const u16* W = m == 0 ? W0 : m == 1 ? W1 : m == 2 ? W2 : W3;
        const rsrc_t rw = mkrs(W, 8u << 20);
        if (l) {
#pragma unroll
            for (int i = 0; i < PFA; ++i) __builtin_amdgcn_raw_ptr_buffer_load_lds(rw, smem + LDSB - 1024 + wv * 256, 4, lane * 256, i * (1u << 20) + (xx * 16 + (q & 15)) * 16384, 0, 0);
        } else {
            const unsigned po = (0x100u - (unsigned)(size_t)W) & 0x300u, hx = SPL ? (xx & 2) + ((q >> 4) & 1) : m < 2 ? xx : xx ^ VUX;
            const unsigned vo = m == 3 ? (lane >> 1) * 1024 + (lane & 1) * 128 : (lane >> 2) * 8192 + ((lane >> 1) & 1) * 1024 + (lane & 1) * 128;
            const unsigned so = po + (m == 3 ? hx * (2u << 20) + (q & 15) * 32768 : hx * 2048 + (q & 15) * 131072);
#pragma unroll
            for (int i = 0; i < PFB; ++i) __builtin_amdgcn_raw_ptr_buffer_load_lds(rw, smem + LDSB - 1024 + wv * 256, 4, vo, so + i * (m == 3 ? 524288 : 2u << 20), 0, 0);
            const unsigned pl = (0x1F00u - (unsigned)(size_t)W) & 0x1F00u;
#pragma unroll
            for (int i = 0; i < PFL; ++i) __builtin_amdgcn_raw_ptr_buffer_load_lds(rw, smem + LDSB - 1024 + wv * 256, 4, (lane >> 1) * 8192 + (lane & 1) * 128, (pl >> 11) == hx && m < 3 ? pl + ((q & 15) + 16 * i) * 262144 : OOB, 0, 0);
        }
    }
    int ro, ix;
    if ((xx & 1) == (PLAN & 1)) { if (j < 24) { ro = 0; ix = (xx >> 1) * 8 + j - 16; } else if (j < 26) { ro = 3; ix = (xx >> 1) * 2 + j - 24; } else { ro = 1; ix = (xx >> 1) * 14 + j - 26; } }
    else if (j < 24) { ro = 1; ix = (xx >> 1) * 14 + j - 10; } else { ro = 2; ix = (xx >> 1) * 8 + j - 24; }
    if (ro == 3 && l) { if (ix == 0) leader(args(0), wv, smem, x0); else if (MPF && wv > 1) mpf(mkrs(args(0).ws, 4u << 20), beat_wait(x0) + SD + (wv & 1) * 16384); return; }
    const unsigned sh = beat_wait(x0);
    auto pfg = [&]() AI {
        if (!PFG || l) return;
        until(PFT);
        const Args a = args(0);
        const int lane = lane_id(), q = (ix & 7) * 4 + wv;
        const rsrc_t rw = mkrs(q & 1 ? a.Wu : a.Wg, 3584 * 2048);
        const unsigned vo = (lane & 15) < ((xx & 1) ? 12 : 16) ? (lane >> 4) * 7168 + (lane & 15) * 128 : OOB, so = ((xx >> 1) * 14 + ((xx & 1) ? 0 : 6)) * 256 + (1024 - 64 * PFG + (q >> 1) * 4) * 7168;
#pragma unroll
        for (int i = 0; i < PFG; ++i) __builtin_amdgcn_raw_ptr_buffer_load_lds(rw, smem + LDSB - 1024 + wv * 256, 4, vo, so + i * 64 * 7168, 0, PFX);
    };
    if (ro == 0) { rd_role(args(sh), wv, smem, l, ix, pfg); return; }
    if (ro == 1) { const Args a = args(sh); until((l ? DLG1 : DLG) - (((((unsigned)(size_t)a.Wg >> 8) + ix) & 3) == 1 || ((((unsigned)(size_t)a.Wu >> 8) + ix) & 3) == 1 ? DLS : 0)); WV0; BAR; gg_role(a, wv, smem, l, ix); return; }
    if (ro == 2) { pfg(); const Args a = args(sh); until((l ? DLD1 : DLD) - (((((unsigned)(size_t)a.Wd >> 8) + (ix >> 1)) & 3) == 1 ? DLS + 200 : 0)); WV0; BAR; dd_role(a, wv, smem, l, ix); return; }
    r_role(args(sh), wv, ix);
}

extern "C" void kernel_launch(void* const* d_in, const int* in_sizes, int n_in, void* d_out, int out_size, void* d_ws, size_t ws_size, hipStream_t stream) {
    (void)in_sizes; (void)n_in; (void)out_size; (void)ws_size;
    Args a;
    a.x = (const u16*)d_in[0]; a.mem0 = (const u16*)d_in[1]; a.Wq = (const u16*)d_in[2]; a.Wk = (const u16*)d_in[3]; a.Wv = (const u16*)d_in[4]; a.Wo = (const u16*)d_in[5];
    a.Wg = (const u16*)d_in[8]; a.Wu = (const u16*)d_in[9]; a.Wd = (const u16*)d_in[10];
    a.ws = (char*)(((uintptr_t)d_ws + (2u << 20) - 1) & ~(uintptr_t)((2u << 20) - 1)); a.out = (u16*)d_out;
    (void)hipFuncSetAttribute((const void*)sys_kernel, hipFuncAttributeMaxDynamicSharedMemorySize, LDSB);
    hipLaunchKernelGGL(sys_kernel, dim3(256), dim3(256), LDSB, stream, a);
}
```

```cpp
#include <hip/hip_runtime.h>
#include <stdint.h>

typedef unsigned short u16;
typedef unsigned long long u64;
typedef __attribute__((ext_vector_type(8))) short bf16x8;
typedef __attribute__((ext_vector_type(4))) short s16x4;
typedef __attribute__((ext_vector_type(4))) float f32x4;
typedef __attribute__((ext_vector_type(16))) float f32x16;
typedef __attribute__((ext_vector_type(2))) float f32x2;
typedef __attribute__((ext_vector_type(4))) unsigned v4u;
typedef __attribute__((ext_vector_type(2))) unsigned v2u;
typedef __amdgpu_buffer_rsrc_t rsrc_t;
typedef __attribute__((address_space(3))) char lds_char;
typedef __attribute__((address_space(3))) bf16x8 l8;
typedef __attribute__((address_space(3))) v4u l4;
typedef __attribute__((address_space(3))) v2u l2;
typedef __attribute__((address_space(3))) unsigned l1;
typedef __attribute__((address_space(3))) f32x4 lf4;
typedef __attribute__((address_space(3))) float lf1;
#define DEVI __device__ __forceinline__
#define AI __attribute__((always_inline))

constexpr int NSEG = 64;
constexpr float ATT_SCALE = 0.08838834764831845f;
constexpr float LOG2E = 1.4426950408889634f;
#define NPRB 4
constexpr unsigned SD = 8192;
DEVI unsigned sd(unsigned o) { return o ^ ((o >> 8) & SD); }
constexpr unsigned F_X0 = 32768;
constexpr unsigned F_X = 4096;
constexpr unsigned F_A = 16384;
constexpr unsigned F_H = F_A + 2048;
constexpr unsigned F_D = F_H + 4096;
constexpr unsigned F_Y0 = 49152;
constexpr unsigned F_ABORT = 6400;
constexpr unsigned O_PM = 65536;
constexpr unsigned O_E1 = 131072;
constexpr unsigned O_PT = 262144;
constexpr unsigned O_UM = 524288;
constexpr unsigned CTRL_END = O_UM + (1u << 20);
constexpr unsigned O_PRB = 0x1F0000, O_HB = 0x1FFF00;
constexpr unsigned O_XN = 4u << 20;
constexpr unsigned O_XR = O_XN + (8u << 20);
constexpr unsigned O_ST = O_XR + (8u << 20);
constexpr unsigned O_H = O_ST + (8u << 20);
constexpr unsigned O_Y = O_H + (36u << 20);
constexpr unsigned WS_NEED = O_Y + (8u << 20);
constexpr unsigned O_PROF = WS_NEED;
constexpr unsigned WS_RS = WS_NEED;
static_assert(CTRL_END <= O_PRB && CTRL_END % 16384 == 0 && O_Y % (8u << 20) == 0, "workspace");
constexpr unsigned OOB = 0x80000000u, OOB2 = 0x90000000u, SENT = 0xFFFFFFFFu;

struct Args { const u16 *x, *mem0, *Wq, *Wk, *Wv, *Wo, *Wg, *Wu, *Wd; char* ws; u16* out; };

typedef __attribute__((ext_vector_type(2))) __bf16 bf16x2_t;
DEVI unsigned pack2(float lo, float hi) { f32x2 v = {lo, hi}; bf16x2_t b = __builtin_convertvector(v, bf16x2_t); return __builtin_bit_cast(unsigned, b); }
DEVI float bflo(unsigned u) { return __uint_as_float(u << 16); }
DEVI float bfhi(unsigned u) { return __uint_as_float(u & 0xffff0000u); }
DEVI float rbf(float x) { return bflo(pack2(x, x)); }
DEVI bf16x8 as8(v4u v) { return __builtin_bit_cast(bf16x8, v); }
DEVI f32x4 mfma16(bf16x8 a, bf16x8 b, f32x4 c) { return __builtin_amdgcn_mfma_f32_16x16x32_bf16(a, b, c, 0, 0, 0); }
DEVI f32x16 mfma32(bf16x8 a, bf16x8 b, f32x16 c) { return __builtin_amdgcn_mfma_f32_32x32x16_bf16(a, b, c, 0, 0, 0); }
constexpr int AUX_SC1 = 16;
DEVI v4u ld16(rsrc_t r, unsigned vo, unsigned so) { return __builtin_amdgcn_raw_buffer_load_b128(r, vo, so, AUX_SC1); }
DEVI unsigned ld4(rsrc_t r, unsigned vo, unsigned so) { return __builtin_amdgcn_raw_buffer_load_b32(r, vo, so, AUX_SC1); }
DEVI void st16(v4u v, rsrc_t r, unsigned vo) { __builtin_amdgcn_raw_buffer_store_b128(v, r, vo, 0, AUX_SC1); }
DEVI void st8(v2u v, rsrc_t r, unsigned vo, unsigned so) { __builtin_amdgcn_raw_buffer_store_b64(v, r, vo, so, AUX_SC1); }
DEVI void st4(unsigned v, rsrc_t r, unsigned vo, unsigned so) { __builtin_amdgcn_raw_buffer_store_b32(v, r, vo, so, AUX_SC1); }
DEVI v2u trd(lds_char* p) { return __builtin_bit_cast(v2u, __builtin_amdgcn_ds_read_tr16_b64_v4i16((__attribute__((address_space(3))) s16x4*)p)); }
template <int O1, int O2> DEVI void tra(v2u& lo, v2u& hi, unsigned a) { asm volatile("ds_read_b64_tr_b16 %0, %2 offset:%3\n\tds_read_b64_tr_b16 %1, %2 offset:%4" : "=&v"(lo), "=&v"(hi) : "v"(a), "n"(O1), "n"(O2) : "memory"); }
DEVI void trw(v2u& a, v2u& b, v2u& c, v2u& d) { asm volatile("s_waitcnt lgkmcnt(0)" : "+v"(a), "+v"(b), "+v"(c), "+v"(d)::"memory"); }
DEVI rsrc_t mkrs(const void* p, unsigned n) { return __builtin_amdgcn_make_buffer_rsrc((void*)p, 0, (int)n, 0x00020000); }
#define CB asm volatile("" ::: "memory")
#define SB __builtin_amdgcn_sched_barrier(0)
#define WL0 asm volatile("s_waitcnt lgkmcnt(0)" ::: "memory")
#define WV0 asm volatile("s_waitcnt vmcnt(0)" ::: "memory")
#define WV(n) asm volatile("s_waitcnt vmcnt(%0)" ::"n"(n) : "memory")
#define BAR do { __builtin_amdgcn_s_barrier(); CB; } while (0)
#define RFL(x) __builtin_amdgcn_readfirstlane(x)
DEVI int lane_id() { int x; asm volatile("v_mbcnt_lo_u32_b32 %0, -1, 0\n\tv_mbcnt_hi_u32_b32 %0, -1, %0" : "=v"(x)); return x; }
template <int I> struct IC { static constexpr int v = I; };
template <int B, int E_, class F> DEVI void sfor(F&& f) { if constexpr (B < E_) { f(IC<B>{}); sfor<B + 1, E_>(f); } }
DEVI float red_x32(float x, bool mx) { unsigned u = __float_as_uint(x); auto r = __builtin_amdgcn_permlane32_swap(u, u, false, false); float a = __uint_as_float(r[0]), b = __uint_as_float(r[1]); return mx ? fmaxf(a, b) : a + b; }
DEVI float red_x16(float x, bool mx) { unsigned u = __float_as_uint(x); auto r = __builtin_amdgcn_permlane16_swap(u, u, false, false); float a = __uint_as_float(r[0]), b = __uint_as_float(r[1]); return mx ? fmaxf(a, b) : a + b; }
DEVI unsigned vmax4(v4u d) { return max(max(d[0], d[1]), max(d[2], d[3])); }
DEVI unsigned nosent(unsigned v) { return min(v, SENT - 1u); }
constexpr unsigned SPIN_CAP = 1u << 18;
DEVI bool spin_bail(unsigned& spins, rsrc_t rs) {
    if (++spins > SPIN_CAP) { st4(0u, rs, 0, F_ABORT); return true; }
    return (spins & 255u) == 0u && RFL(ld4(rs, 0, F_ABORT)) != SENT;
}
#define TSTAMP(idx)
constexpr int LDSB = 160 * 1024;
DEVI void idle(unsigned dl) { const unsigned t0 = (unsigned)__builtin_amdgcn_s_memrealtime(); while ((unsigned)__builtin_amdgcn_s_memrealtime() - t0 < dl) __builtin_amdgcn_s_sleep(8); }
#define RXE 0
#define D1F 0
#define X1F 0

DEVI float red_rows(float s) {
    s += __uint_as_float(__builtin_amdgcn_update_dpp(0, __float_as_uint(s), 0x128, 0xf, 0xf, false));
    s = red_x16(s, false); return red_x32(s, false);
}
DEVI f32x2 up2(unsigned u) { return f32x2{bflo(u), bfhi(u)}; }
DEVI float ln_inv(float var) { return __builtin_amdgcn_rcpf(rbf(__builtin_amdgcn_sqrtf(rbf(rbf(fmaxf(var, 0.f)) + 1e-5f)))); }
template <class F> DEVI void ln0(const Args& a, const int w, int b, F&& base) {
    const int lane = lane_id(), row = w * 8 + (lane & 7);
    const rsrc_t rx = mkrs(a.x, 2048 * 2048);
    v4u u[16];
#pragma unroll
    for (int i = 0; i < 16; ++i) u[i] = __builtin_amdgcn_raw_buffer_load_b128(rx, row * 2048 + (lane >> 3) * 16 + i * 128, b * 65536, 0);
    const float k0 = __shfl(bflo(u[0][0]), lane & 7);
    f32x2 s1 = {0.f, 0.f}, s2 = {0.f, 0.f};
#pragma unroll
    for (int i = 0; i < 16; ++i)
#pragma unroll
        for (int e = 0; e < 4; ++e) { const f32x2 d = up2(u[i][e]) - k0; s1 += d; s2 += d * d; }
    const float s = red_rows(s1[0] + s1[1]) * (1.0f / 1024.0f), q = red_rows(s2[0] + s2[1]) * (1.0f / 1024.0f);
    float inv = ln_inv(q - s * s), c = -rbf(k0 + s) * inv;
    asm volatile("" : "+v"(inv), "+v"(c));
    const rsrc_t rs = mkrs(a.ws + base(), WS_RS);
    const unsigned o = sd(O_XN + b * 131072) + (lane >> 3) * 512 + row * 16;
#pragma unroll
    for (int i = 0; i < 16; ++i) {
        v4u r;
#pragma unroll
        for (int e = 0; e < 4; ++e) { const f32x2 z = up2(u[i][e]) * inv + c; r[e] = pack2(z[0], z[1]); }
        st16(r, rs, o + (i >> 1) * 16384 + (i & 1) * 4096);
    }
#pragma unroll
    for (int i = 0; i < 16; ++i) st16(u[i], rs, o + (O_XR - O_XN) + (i >> 1) * 16384 + (i & 1) * 4096);
    st16(u[0], rs, sd(O_ST + b * 131072) + w * 1024 + lane * 16); st16(u[0], rs, sd(O_ST + b * 131072) + 4096 + w * 1024 + lane * 16); st16(u[0], rs, sd(O_ST + SD + b * 131072) + w * 1024 + lane * 16);
    WV0;
    st4(b, rs, 0, F_X0 + b * 128 + w * 4);
}
#define RSLP 8
DEVI void r_role(const Args& a, const int w, int rq) {
    const int lane = lane_id(), row = (w >> 1) * 16 + (rq & 1) * 8 + (lane & 7), cq = (rq >> 1) * 2 + (w & 1);
    const rsrc_t rs = mkrs(a.ws, WS_RS);
    const unsigned fo = cq * 32768 + (lane >> 3) * 512 + row * 16, so = O_ST - O_XR + 4096 + (lane >> 3) * 512 + row * 8;
    auto io = [](int i) -> unsigned { return (i >> 1) * 16384 + (i & 1) * 4096; };
    const unsigned fd = F_D + (lane & 15) * 128 + (D1F ? 0 : (row >> 3) * 4);
    for (int b = 0; b < NSEG; ++b) {
        unsigned spins = 0;
        for (;;) {
            CB;
            if (__ballot((int)ld4(rs, fd, 0) < b) == 0ull) break;
            if (spin_bail(spins, rs)) return;
            __builtin_amdgcn_s_sleep(RSLP);
        }
        const unsigned sx = sd(O_XR + b * 131072);
        const v2u s0 = __builtin_amdgcn_raw_buffer_load_b64(rs, so, sx, AUX_SC1), s1 = __builtin_amdgcn_raw_buffer_load_b64(rs, so + 256, sx, AUX_SC1);
        v4u u[4];
#pragma unroll
        for (int i = 0; i < 4; ++i) u[i] = ld16(rs, fo + io(i), sx);
        const float m0 = __uint_as_float(s0[0]), m1 = __uint_as_float(s1[0]), mean = red_rows(m0 + m1) * 0.0625f;
        const float var = red_rows(__uint_as_float(s0[1]) + __uint_as_float(s1[1]) + 64.f * ((m0 - mean) * (m0 - mean) + (m1 - mean) * (m1 - mean))) * (1.0f / 1024.0f);
        const float inv = ln_inv(var), c = -rbf(mean) * inv;
#pragma unroll
        for (int i = 0; i < 4; ++i) {
            v4u o;
#pragma unroll
            for (int e = 0; e < 4; ++e) { const f32x2 z = up2(u[i][e]) * inv + c; o[e] = pack2(z[0], z[1]); }
            st16(o, rs, fo + io(i) + ((sx ^ SD) - (O_XR - O_XN)));
        }
        CB;
#pragma unroll
        for (int i = 0; i < 4; ++i) st16(u[i], rs, fo + io(i) + (sx ^ SD));
        WV0;
        if (X1F) BAR;
        st4(b, rs, 0, X1F && w ? OOB : F_X + SD + rq * 128 + w * 4);
        if (rq == 0 && w == 0) TSTAMP(200000 + 64 + b);
    }
}
DEVI void r_role2(const Args& a, const int w, int rq) {
    const int lane = lane_id(), row = w * 16 + (rq & 1) * 8 + (lane & 7), cq = (rq >> 1) * 2;
    const rsrc_t rs = mkrs(a.ws, WS_RS);
    const unsigned fo = cq * 32768 + (lane >> 3) * 512 + row * 16, so = O_ST - O_XR + 4096 + (lane >> 3) * 512 + row * 8;
    auto io = [](int i) -> unsigned { return (i >> 1) * 16384 + (i & 1) * 4096; };
    const unsigned fd = F_D + (lane & 15) * 128 + (D1F ? 0 : (row >> 3) * 4);
    for (int b = 0; b < NSEG; ++b) {
        unsigned spins = 0;
        for (;;) {
            CB;
            if (__ballot((int)ld4(rs, fd, 0) < b) == 0ull) break;
            if (spin_bail(spins, rs)) return;
            __builtin_amdgcn_s_sleep(RSLP);
        }
        const unsigned sx = sd(O_XR + b * 131072);
        const v2u s0 = __builtin_amdgcn_raw_buffer_load_b64(rs, so, sx, AUX_SC1), s1 = __builtin_amdgcn_raw_buffer_load_b64(rs, so + 256, sx, AUX_SC1);
        v4u u[8];
#pragma unroll
        for (int i = 0; i < 8; ++i) u[i] = ld16(rs, fo + io(i), sx);
        const float m0 = __uint_as_float(s0[0]), m1 = __uint_as_float(s1[0]), mean = red_rows(m0 + m1) * 0.0625f;
        const float var = red_rows(__uint_as_float(s0[1]) + __uint_as_float(s1[1]) + 64.f * ((m0 - mean) * (m0 - mean) + (m1 - mean) * (m1 - mean))) * (1.0f / 1024.0f);
        const float inv = ln_inv(var), c = -rbf(mean) * inv;
#pragma unroll
        for (int i = 0; i < 8; ++i) {
            v4u o;
#pragma unroll
            for (int e = 0; e < 4; ++e) { const f32x2 z = up2(u[i][e]) * inv + c; o[e] = pack2(z[0], z[1]); }
            st16(o, rs, fo + io(i) + ((sx ^ SD) - (O_XR - O_XN)));
        }
        CB;
#pragma unroll
        for (int i = 0; i < 8; ++i) st16(u[i], rs, fo + io(i) + (sx ^ SD));
        WV0;
        if (X1F) BAR;
        st8(v2u{(unsigned)b, (unsigned)b}, rs, 0, F_X + SD + rq * 128 + w * 8);
        if (rq == 0 && w == 0) TSTAMP(200000 + 64 + b);
    }
}
#define QK_ASM \
"v_mbcnt_lo_u32_b32 v255, -1, 0\nv_mbcnt_hi_u32_b32 v255, -1, v255\nv_lshlrev_b32 v255, 2, v255\nv_add_u32 v255, %[tb], v255\nds_read_b32 v0, v255 offset:0\nds_read_b32 v1, v255 offset:256\nds_read_b32 v2, v255 offset:512\nds_read_b32 v3, v255 offset:768\n" \
"ds_read_b32 v4, v255 offset:1024\nds_read_b32 v5, v255 offset:1280\nds_read_b32 v6, v255 offset:1536\nds_read_b32 v7, v255 offset:1792\nds_read_b32 v8, v255 offset:2048\nds_read_b32 v9, v255 offset:2304\nds_read_b32 v10, v255 offset:2560\nds_read_b32 v11, v255 offset:2816\n" \
"ds_read_b32 v12, v255 offset:3072\nds_read_b32 v13, v255 offset:3328\nds_read_b32 v14, v255 offset:3584\nds_read_b32 v15, v255 offset:3840\nds_read_b32 v92, v255 offset:4096\nds_read_b32 v93, v255 offset:4352\nds_read_b32 v94, v255 offset:4608\ns_waitcnt lgkmcnt(0)\n" \
"s_mov_b64 s[36:37], %[ws]\ns_mov_b32 s38, %[sz]\ns_mov_b32 s39, 0x20000\ns_mov_b32 s45, 2096896\nbuffer_load_dword v95, off, s[36:39], s45 sc1\ns_add_u32 m0, %[lw], 16384\ns_add_u32 s44, %[wo], 0\nbuffer_load_dwordx4 v92, %[rw], s44 offen nt lds\n" \
"s_add_u32 m0, %[lw], 17408\ns_add_u32 s44, %[wo], 65536\nbuffer_load_dwordx4 v92, %[rw], s44 offen nt lds\ns_add_u32 m0, %[lw], 18432\ns_add_u32 s44, %[wo], 131072\nbuffer_load_dwordx4 v92, %[rw], s44 offen nt lds\ns_add_u32 m0, %[lw], 19456\ns_add_u32 s44, %[wo], 196608\n" \
"buffer_load_dwordx4 v92, %[rw], s44 offen nt lds\ns_add_u32 m0, %[lw], 20480\ns_add_u32 s44, %[wo], 262144\nbuffer_load_dwordx4 v92, %[rw], s44 offen nt lds\ns_add_u32 m0, %[lw], 21504\ns_add_u32 s44, %[wo], 327680\nbuffer_load_dwordx4 v92, %[rw], s44 offen nt lds\ns_add_u32 m0, %[lw], 22528\n" \
"s_add_u32 s44, %[wo], 393216\nbuffer_load_dwordx4 v92, %[rw], s44 offen nt lds\ns_add_u32 m0, %[lw], 23552\ns_add_u32 s44, %[wo], 458752\nbuffer_load_dwordx4 v92, %[rw], s44 offen nt lds\ns_add_u32 m0, %[tb], 0\ns_add_u32 s44, %[wo], 524288\nbuffer_load_dwordx4 v92, %[rw], s44 offen nt lds\n" \
"s_add_u32 m0, %[tb], 1024\ns_add_u32 s44, %[wo], 589824\nbuffer_load_dwordx4 v92, %[rw], s44 offen nt lds\ns_add_u32 m0, %[tb], 2048\ns_add_u32 s44, %[wo], 655360\nbuffer_load_dwordx4 v92, %[rw], s44 offen nt lds\ns_add_u32 m0, %[tb], 3072\ns_add_u32 s44, %[wo], 720896\n" \
"buffer_load_dwordx4 v92, %[rw], s44 offen nt lds\ns_add_u32 m0, %[tb], 4096\ns_add_u32 s44, %[wo], 786432\nbuffer_load_dwordx4 v92, %[rw], s44 offen nt lds\ns_add_u32 m0, %[tb], 5120\ns_add_u32 s44, %[wo], 851968\nbuffer_load_dwordx4 v92, %[rw], s44 offen nt lds\ns_add_u32 m0, %[tb], 6144\n" \
"s_add_u32 s44, %[wo], 917504\nbuffer_load_dwordx4 v92, %[rw], s44 offen nt lds\ns_add_u32 m0, %[tb], 7168\ns_add_u32 s44, %[wo], 983040\nbuffer_load_dwordx4 v92, %[rw], s44 offen nt lds\ns_add_u32 m0, %[tb], 8192\ns_add_u32 s44, %[wo], 1048576\nbuffer_load_dwordx4 v92, %[rw], s44 offen nt lds\n" \
"s_add_u32 m0, %[tb], 9216\ns_add_u32 s44, %[wo], 1114112\nbuffer_load_dwordx4 v92, %[rw], s44 offen nt lds\ns_add_u32 m0, %[tb], 10240\ns_add_u32 s44, %[wo], 1179648\nbuffer_load_dwordx4 v92, %[rw], s44 offen nt lds\ns_add_u32 m0, %[tb], 11264\ns_add_u32 s44, %[wo], 1245184\n" \
"buffer_load_dwordx4 v92, %[rw], s44 offen nt lds\ns_add_u32 m0, %[lw], 0\ns_add_u32 s44, %[wo], 1310720\nbuffer_load_dwordx4 v92, %[rw], s44 offen nt lds\ns_add_u32 m0, %[lw], 1024\ns_add_u32 s44, %[wo], 1376256\nbuffer_load_dwordx4 v92, %[rw], s44 offen nt lds\ns_add_u32 m0, %[lw], 2048\n" \
"s_add_u32 s44, %[wo], 1441792\nbuffer_load_dwordx4 v92, %[rw], s44 offen nt lds\ns_add_u32 m0, %[lw], 3072\ns_add_u32 s44, %[wo], 1507328\nbuffer_load_dwordx4 v92, %[rw], s44 offen nt lds\ns_add_u32 m0, %[lw], 4096\ns_add_u32 s44, %[wo], 1572864\nbuffer_load_dwordx4 v92, %[rw], s44 offen nt lds\n" \
"s_add_u32 m0, %[lw], 5120\ns_add_u32 s44, %[wo], 1638400\nbuffer_load_dwordx4 v92, %[rw], s44 offen nt lds\ns_add_u32 m0, %[lw], 6144\ns_add_u32 s44, %[wo], 1703936\nbuffer_load_dwordx4 v92, %[rw], s44 offen nt lds\ns_add_u32 m0, %[lw], 7168\ns_add_u32 s44, %[wo], 1769472\n" \
"buffer_load_dwordx4 v92, %[rw], s44 offen nt lds\ns_add_u32 m0, %[lw], 8192\ns_add_u32 s44, %[wo], 1835008\nbuffer_load_dwordx4 v92, %[rw], s44 offen nt lds\ns_add_u32 m0, %[lw], 9216\ns_add_u32 s44, %[wo], 1900544\nbuffer_load_dwordx4 v92, %[rw], s44 offen nt lds\ns_add_u32 m0, %[lw], 10240\n" \
"s_add_u32 s44, %[wo], 1966080\nbuffer_load_dwordx4 v92, %[rw], s44 offen nt lds\ns_add_u32 m0, %[lw], 11264\ns_add_u32 s44, %[wo], 2031616\nbuffer_load_dwordx4 v92, %[rw], s44 offen nt lds\ns_add_u32 m0, %[lw], 12288\ns_add_u32 s44, %[wo], 2097152\nbuffer_load_dwordx4 v92, %[rw], s44 offen nt lds\n" \
"s_add_u32 m0, %[lw], 13312\ns_add_u32 s44, %[wo], 2162688\nbuffer_load_dwordx4 v92, %[rw], s44 offen nt lds\ns_add_u32 m0, %[lw], 14336\ns_add_u32 s44, %[wo], 2228224\nbuffer_load_dwordx4 v92, %[rw], s44 offen nt lds\ns_add_u32 m0, %[lw], 15360\ns_add_u32 s44, %[wo], 2293760\n" \
"buffer_load_dwordx4 v92, %[rw], s44 offen nt lds\ns_waitcnt vmcnt(32)\nds_read_b64_tr_b16 a[0:1], v94 offset:16384\nds_read_b64_tr_b16 a[2:3], v94 offset:16896\nds_read_b64_tr_b16 a[8:9], v94 offset:16392\nds_read_b64_tr_b16 a[10:11], v94 offset:16904\nds_read_b64_tr_b16 a[16:17], v94 offset:16448\nds_read_b64_tr_b16 a[18:19], v94 offset:16960\n" \
"ds_read_b64_tr_b16 a[24:25], v94 offset:16456\nds_read_b64_tr_b16 a[26:27], v94 offset:16968\ns_waitcnt lgkmcnt(0)\ns_add_u32 m0, %[lw], 16384\ns_add_u32 s44, %[wo], 2359296\nbuffer_load_dwordx4 v92, %[rw], s44 offen nt lds\ns_add_u32 m0, %[lw], 17408\ns_add_u32 s44, %[wo], 2424832\n" \
"buffer_load_dwordx4 v92, %[rw], s44 offen nt lds\ns_add_u32 m0, %[lw], 18432\ns_add_u32 s44, %[wo], 2490368\nbuffer_load_dwordx4 v92, %[rw], s44 offen nt lds\ns_add_u32 m0, %[lw], 19456\ns_add_u32 s44, %[wo], 2555904\nbuffer_load_dwordx4 v92, %[rw], s44 offen nt lds\ns_waitcnt vmcnt(32)\n" \
"ds_read_b64_tr_b16 a[4:5], v94 offset:20480\nds_read_b64_tr_b16 a[6:7], v94 offset:20992\nds_read_b64_tr_b16 a[12:13], v94 offset:20488\nds_read_b64_tr_b16 a[14:15], v94 offset:21000\nds_read_b64_tr_b16 a[20:21], v94 offset:20544\nds_read_b64_tr_b16 a[22:23], v94 offset:21056\nds_read_b64_tr_b16 a[28:29], v94 offset:20552\nds_read_b64_tr_b16 a[30:31], v94 offset:21064\n" \
"s_waitcnt lgkmcnt(0)\ns_add_u32 m0, %[lw], 20480\ns_add_u32 s44, %[wo], 2621440\nbuffer_load_dwordx4 v92, %[rw], s44 offen nt lds\ns_add_u32 m0, %[lw], 21504\ns_add_u32 s44, %[wo], 2686976\nbuffer_load_dwordx4 v92, %[rw], s44 offen nt lds\ns_add_u32 m0, %[lw], 22528\n" \
"s_add_u32 s44, %[wo], 2752512\nbuffer_load_dwordx4 v92, %[rw], s44 offen nt lds\ns_add_u32 m0, %[lw], 23552\ns_add_u32 s44, %[wo], 2818048\nbuffer_load_dwordx4 v92, %[rw], s44 offen nt lds\ns_waitcnt vmcnt(32)\nds_read_b64_tr_b16 a[32:33], v93 offset:0\nds_read_b64_tr_b16 a[34:35], v93 offset:512\n" \
"ds_read_b64_tr_b16 v[84:85], v93 offset:8\nds_read_b64_tr_b16 v[86:87], v93 offset:520\nds_read_b64_tr_b16 a[40:41], v93 offset:64\nds_read_b64_tr_b16 a[42:43], v93 offset:576\nds_read_b64_tr_b16 a[48:49], v93 offset:72\nds_read_b64_tr_b16 a[50:51], v93 offset:584\ns_waitcnt lgkmcnt(0)\ns_add_u32 m0, %[tb], 0\n" \
"s_add_u32 s44, %[wo], 2883584\nbuffer_load_dwordx4 v92, %[rw], s44 offen nt lds\ns_add_u32 m0, %[tb], 1024\ns_add_u32 s44, %[wo], 2949120\nbuffer_load_dwordx4 v92, %[rw], s44 offen nt lds\ns_add_u32 m0, %[tb], 2048\ns_add_u32 s44, %[wo], 3014656\nbuffer_load_dwordx4 v92, %[rw], s44 offen nt lds\n" \
"s_add_u32 m0, %[tb], 3072\ns_add_u32 s44, %[wo], 3080192\nbuffer_load_dwordx4 v92, %[rw], s44 offen nt lds\ns_waitcnt vmcnt(32)\nds_read_b64_tr_b16 a[36:37], v93 offset:4096\nds_read_b64_tr_b16 a[38:39], v93 offset:4608\nds_read_b64_tr_b16 v[80:81], v93 offset:4104\nds_read_b64_tr_b16 v[82:83], v93 offset:4616\n" \
"ds_read_b64_tr_b16 a[44:45], v93 offset:4160\nds_read_b64_tr_b16 a[46:47], v93 offset:4672\nds_read_b64_tr_b16 a[52:53], v93 offset:4168\nds_read_b64_tr_b16 a[54:55], v93 offset:4680\ns_waitcnt lgkmcnt(0)\ns_add_u32 m0, %[tb], 4096\ns_add_u32 s44, %[wo], 3145728\nbuffer_load_dwordx4 v92, %[rw], s44 offen nt lds\n" \
"s_add_u32 m0, %[tb], 5120\ns_add_u32 s44, %[wo], 3211264\nbuffer_load_dwordx4 v92, %[rw], s44 offen nt lds\ns_add_u32 m0, %[tb], 6144\ns_add_u32 s44, %[wo], 3276800\nbuffer_load_dwordx4 v92, %[rw], s44 offen nt lds\ns_add_u32 m0, %[tb], 7168\ns_add_u32 s44, %[wo], 3342336\n" \
"buffer_load_dwordx4 v92, %[rw], s44 offen nt lds\ns_waitcnt vmcnt(32)\nds_read_b64_tr_b16 a[56:57], v93 offset:8192\nds_read_b64_tr_b16 a[58:59], v93 offset:8704\nds_read_b64_tr_b16 a[64:65], v93 offset:8200\nds_read_b64_tr_b16 a[66:67], v93 offset:8712\nds_read_b64_tr_b16 v[76:77], v93 offset:8256\nds_read_b64_tr_b16 v[78:79], v93 offset:8768\n" \
"ds_read_b64_tr_b16 a[72:73], v93 offset:8264\nds_read_b64_tr_b16 a[74:75], v93 offset:8776\ns_waitcnt lgkmcnt(0)\ns_add_u32 m0, %[tb], 8192\ns_add_u32 s44, %[wo], 3407872\nbuffer_load_dwordx4 v92, %[rw], s44 offen nt lds\ns_add_u32 m0, %[tb], 9216\ns_add_u32 s44, %[wo], 3473408\n" \
"buffer_load_dwordx4 v92, %[rw], s44 offen nt lds\ns_add_u32 m0, %[tb], 10240\ns_add_u32 s44, %[wo], 3538944\nbuffer_load_dwordx4 v92, %[rw], s44 offen nt lds\ns_add_u32 m0, %[tb], 11264\ns_add_u32 s44, %[wo], 3604480\nbuffer_load_dwordx4 v92, %[rw], s44 offen nt lds\ns_waitcnt vmcnt(32)\n" \
"ds_read_b64_tr_b16 a[60:61], v94 offset:0\nds_read_b64_tr_b16 a[62:63], v94 offset:512\nds_read_b64_tr_b16 a[68:69], v94 offset:8\nds_read_b64_tr_b16 a[70:71], v94 offset:520\nds_read_b64_tr_b16 v[72:73], v94 offset:64\nds_read_b64_tr_b16 v[74:75], v94 offset:576\nds_read_b64_tr_b16 a[76:77], v94 offset:72\nds_read_b64_tr_b16 a[78:79], v94 offset:584\n" \
"s_waitcnt lgkmcnt(0)\ns_add_u32 m0, %[lw], 0\ns_add_u32 s44, %[wo], 3670016\nbuffer_load_dwordx4 v92, %[rw], s44 offen nt lds\ns_add_u32 m0, %[lw], 1024\ns_add_u32 s44, %[wo], 3735552\nbuffer_load_dwordx4 v92, %[rw], s44 offen nt lds\ns_add_u32 m0, %[lw], 2048\n" \
"s_add_u32 s44, %[wo], 3801088\nbuffer_load_dwordx4 v92, %[rw], s44 offen nt lds\ns_add_u32 m0, %[lw], 3072\ns_add_u32 s44, %[wo], 3866624\nbuffer_load_dwordx4 v92, %[rw], s44 offen nt lds\ns_waitcnt vmcnt(32)\nds_read_b64_tr_b16 a[80:81], v94 offset:4096\nds_read_b64_tr_b16 a[82:83], v94 offset:4608\n" \
"ds_read_b64_tr_b16 a[88:89], v94 offset:4104\nds_read_b64_tr_b16 a[90:91], v94 offset:4616\nds_read_b64_tr_b16 a[96:97], v94 offset:4160\nds_read_b64_tr_b16 a[98:99], v94 offset:4672\nds_read_b64_tr_b16 v[68:69], v94 offset:4168\nds_read_b64_tr_b16 v[70:71], v94 offset:4680\ns_waitcnt lgkmcnt(0)\ns_add_u32 m0, %[lw], 4096\n" \
"s_add_u32 s44, %[wo], 3932160\nbuffer_load_dwordx4 v92, %[rw], s44 offen nt lds\ns_add_u32 m0, %[lw], 5120\ns_add_u32 s44, %[wo], 3997696\nbuffer_load_dwordx4 v92, %[rw], s44 offen nt lds\ns_add_u32 m0, %[lw], 6144\ns_add_u32 s44, %[wo], 4063232\nbuffer_load_dwordx4 v92, %[rw], s44 offen nt lds\n" \
"s_add_u32 m0, %[lw], 7168\ns_add_u32 s44, %[wo], 4128768\nbuffer_load_dwordx4 v92, %[rw], s44 offen nt lds\ns_waitcnt vmcnt(32)\nds_read_b64_tr_b16 a[84:85], v94 offset:8192\nds_read_b64_tr_b16 a[86:87], v94 offset:8704\nds_read_b64_tr_b16 a[92:93], v94 offset:8200\nds_read_b64_tr_b16 a[94:95], v94 offset:8712\n" \
"ds_read_b64_tr_b16 a[100:101], v94 offset:8256\nds_read_b64_tr_b16 a[102:103], v94 offset:8768\nds_read_b64_tr_b16 v[64:65], v94 offset:8264\nds_read_b64_tr_b16 v[66:67], v94 offset:8776\ns_waitcnt lgkmcnt(0)\ns_add_u32 m0, %[lw], 8192\ns_add_u32 s44, %[wo], 4194304\nbuffer_load_dwordx4 v92, %[rw], s44 offen nt lds\n" \
"s_add_u32 m0, %[lw], 9216\ns_add_u32 s44, %[wo], 4259840\nbuffer_load_dwordx4 v92, %[rw], s44 offen nt lds\ns_add_u32 m0, %[lw], 10240\ns_add_u32 s44, %[wo], 4325376\nbuffer_load_dwordx4 v92, %[rw], s44 offen nt lds\ns_add_u32 m0, %[lw], 11264\ns_add_u32 s44, %[wo], 4390912\n" \
"buffer_load_dwordx4 v92, %[rw], s44 offen nt lds\ns_waitcnt vmcnt(32)\nds_read_b64_tr_b16 a[104:105], v94 offset:12288\nds_read_b64_tr_b16 a[106:107], v94 offset:12800\nds_read_b64_tr_b16 a[112:113], v94 offset:12296\nds_read_b64_tr_b16 a[114:115], v94 offset:12808\nds_read_b64_tr_b16 a[120:121], v94 offset:12352\nds_read_b64_tr_b16 a[122:123], v94 offset:12864\n" \
"ds_read_b64_tr_b16 a[128:129], v94 offset:12360\nds_read_b64_tr_b16 a[130:131], v94 offset:12872\ns_waitcnt lgkmcnt(0)\ns_add_u32 m0, %[lw], 12288\ns_add_u32 s44, %[wo], 4456448\nbuffer_load_dwordx4 v92, %[rw], s44 offen nt lds\ns_add_u32 m0, %[lw], 13312\ns_add_u32 s44, %[wo], 4521984\n" \
"buffer_load_dwordx4 v92, %[rw], s44 offen nt lds\ns_add_u32 m0, %[lw], 14336\ns_add_u32 s44, %[wo], 4587520\nbuffer_load_dwordx4 v92, %[rw], s44 offen nt lds\ns_add_u32 m0, %[lw], 15360\ns_add_u32 s44, %[wo], 4653056\nbuffer_load_dwordx4 v92, %[rw], s44 offen nt lds\ns_waitcnt vmcnt(32)\n" \
"ds_read_b64_tr_b16 a[108:109], v94 offset:16384\nds_read_b64_tr_b16 a[110:111], v94 offset:16896\nds_read_b64_tr_b16 a[116:117], v94 offset:16392\nds_read_b64_tr_b16 a[118:119], v94 offset:16904\nds_read_b64_tr_b16 a[124:125], v94 offset:16448\nds_read_b64_tr_b16 a[126:127], v94 offset:16960\nds_read_b64_tr_b16 a[132:133], v94 offset:16456\nds_read_b64_tr_b16 a[134:135], v94 offset:16968\n" \
"s_waitcnt lgkmcnt(0)\ns_add_u32 m0, %[lw], 16384\ns_add_u32 s44, %[wo], 4718592\nbuffer_load_dwordx4 v92, %[rw], s44 offen nt lds\ns_add_u32 m0, %[lw], 17408\ns_add_u32 s44, %[wo], 4784128\nbuffer_load_dwordx4 v92, %[rw], s44 offen nt lds\ns_add_u32 m0, %[lw], 18432\n" \
"s_add_u32 s44, %[wo], 4849664\nbuffer_load_dwordx4 v92, %[rw], s44 offen nt lds\ns_add_u32 m0, %[lw], 19456\ns_add_u32 s44, %[wo], 4915200\nbuffer_load_dwordx4 v92, %[rw], s44 offen nt lds\ns_waitcnt vmcnt(32)\nds_read_b64_tr_b16 a[136:137], v94 offset:20480\nds_read_b64_tr_b16 a[138:139], v94 offset:20992\n" \
"ds_read_b64_tr_b16 v[60:61], v94 offset:20488\nds_read_b64_tr_b16 v[62:63], v94 offset:21000\nds_read_b64_tr_b16 a[144:145], v94 offset:20544\nds_read_b64_tr_b16 a[146:147], v94 offset:21056\nds_read_b64_tr_b16 a[152:153], v94 offset:20552\nds_read_b64_tr_b16 a[154:155], v94 offset:21064\ns_waitcnt lgkmcnt(0)\ns_add_u32 m0, %[lw], 20480\n" \
"s_add_u32 s44, %[wo], 4980736\nbuffer_load_dwordx4 v92, %[rw], s44 offen nt lds\ns_add_u32 m0, %[lw], 21504\ns_add_u32 s44, %[wo], 5046272\nbuffer_load_dwordx4 v92, %[rw], s44 offen nt lds\ns_add_u32 m0, %[lw], 22528\ns_add_u32 s44, %[wo], 5111808\nbuffer_load_dwordx4 v92, %[rw], s44 offen nt lds\n" \
"s_add_u32 m0, %[lw], 23552\ns_add_u32 s44, %[wo], 5177344\nbuffer_load_dwordx4 v92, %[rw], s44 offen nt lds\ns_waitcnt vmcnt(32)\nds_read_b64_tr_b16 a[140:141], v93 offset:0\nds_read_b64_tr_b16 a[142:143], v93 offset:512\nds_read_b64_tr_b16 v[56:57], v93 offset:8\nds_read_b64_tr_b16 v[58:59], v93 offset:520\n" \
"ds_read_b64_tr_b16 a[148:149], v93 offset:64\nds_read_b64_tr_b16 a[150:151], v93 offset:576\nds_read_b64_tr_b16 a[156:157], v93 offset:72\nds_read_b64_tr_b16 a[158:159], v93 offset:584\ns_waitcnt lgkmcnt(0)\ns_add_u32 m0, %[tb], 0\ns_add_u32 s44, %[wo], 5242880\nbuffer_load_dwordx4 v92, %[rw], s44 offen nt lds\n" \
"s_add_u32 m0, %[tb], 1024\ns_add_u32 s44, %[wo], 5308416\nbuffer_load_dwordx4 v92, %[rw], s44 offen nt lds\ns_add_u32 m0, %[tb], 2048\ns_add_u32 s44, %[wo], 5373952\nbuffer_load_dwordx4 v92, %[rw], s44 offen nt lds\ns_add_u32 m0, %[tb], 3072\ns_add_u32 s44, %[wo], 5439488\n" \
"buffer_load_dwordx4 v92, %[rw], s44 offen nt lds\ns_waitcnt vmcnt(32)\nds_read_b64_tr_b16 a[160:161], v93 offset:4096\nds_read_b64_tr_b16 a[162:163], v93 offset:4608\nds_read_b64_tr_b16 a[168:169], v93 offset:4104\nds_read_b64_tr_b16 a[170:171], v93 offset:4616\nds_read_b64_tr_b16 v[52:53], v93 offset:4160\nds_read_b64_tr_b16 v[54:55], v93 offset:4672\n" \
"ds_read_b64_tr_b16 a[176:177], v93 offset:4168\nds_read_b64_tr_b16 a[178:179], v93 offset:4680\ns_waitcnt lgkmcnt(0)\ns_add_u32 m0, %[tb], 4096\ns_add_u32 s44, %[wo], 5505024\nbuffer_load_dwordx4 v92, %[rw], s44 offen nt lds\ns_add_u32 m0, %[tb], 5120\ns_add_u32 s44, %[wo], 5570560\n" \
"buffer_load_dwordx4 v92, %[rw], s44 offen nt lds\ns_add_u32 m0, %[tb], 6144\ns_add_u32 s44, %[wo], 5636096\nbuffer_load_dwordx4 v92, %[rw], s44 offen nt lds\ns_add_u32 m0, %[tb], 7168\ns_add_u32 s44, %[wo], 5701632\nbuffer_load_dwordx4 v92, %[rw], s44 offen nt lds\ns_waitcnt vmcnt(32)\n" \
"ds_read_b64_tr_b16 a[164:165], v93 offset:8192\nds_read_b64_tr_b16 a[166:167], v93 offset:8704\nds_read_b64_tr_b16 a[172:173], v93 offset:8200\nds_read_b64_tr_b16 a[174:175], v93 offset:8712\nds_read_b64_tr_b16 v[48:49], v93 offset:8256\nds_read_b64_tr_b16 v[50:51], v93 offset:8768\nds_read_b64_tr_b16 a[180:181], v93 offset:8264\nds_read_b64_tr_b16 a[182:183], v93 offset:8776\n" \
"s_waitcnt lgkmcnt(0)\ns_add_u32 m0, %[tb], 8192\ns_add_u32 s44, %[wo], 5767168\nbuffer_load_dwordx4 v92, %[rw], s44 offen nt lds\ns_add_u32 m0, %[tb], 9216\ns_add_u32 s44, %[wo], 5832704\nbuffer_load_dwordx4 v92, %[rw], s44 offen nt lds\ns_add_u32 m0, %[tb], 10240\n" \
"s_add_u32 s44, %[wo], 5898240\nbuffer_load_dwordx4 v92, %[rw], s44 offen nt lds\ns_add_u32 m0, %[tb], 11264\ns_add_u32 s44, %[wo], 5963776\nbuffer_load_dwordx4 v92, %[rw], s44 offen nt lds\ns_waitcnt vmcnt(32)\nds_read_b64_tr_b16 a[184:185], v94 offset:0\nds_read_b64_tr_b16 a[186:187], v94 offset:512\n" \
"ds_read_b64_tr_b16 a[192:193], v94 offset:8\nds_read_b64_tr_b16 a[194:195], v94 offset:520\nds_read_b64_tr_b16 a[200:201], v94 offset:64\nds_read_b64_tr_b16 a[202:203], v94 offset:576\nds_read_b64_tr_b16 v[44:45], v94 offset:72\nds_read_b64_tr_b16 v[46:47], v94 offset:584\ns_waitcnt lgkmcnt(0)\ns_add_u32 m0, %[lw], 0\n" \
"s_add_u32 s44, %[wo], 6029312\nbuffer_load_dwordx4 v92, %[rw], s44 offen nt lds\ns_add_u32 m0, %[lw], 1024\ns_add_u32 s44, %[wo], 6094848\nbuffer_load_dwordx4 v92, %[rw], s44 offen nt lds\ns_add_u32 m0, %[lw], 2048\ns_add_u32 s44, %[wo], 6160384\nbuffer_load_dwordx4 v92, %[rw], s44 offen nt lds\n" \
"s_add_u32 m0, %[lw], 3072\ns_add_u32 s44, %[wo], 6225920\nbuffer_load_dwordx4 v92, %[rw], s44 offen nt lds\ns_waitcnt vmcnt(32)\nds_read_b64_tr_b16 a[188:189], v94 offset:4096\nds_read_b64_tr_b16 a[190:191], v94 offset:4608\nds_read_b64_tr_b16 a[196:197], v94 offset:4104\nds_read_b64_tr_b16 a[198:199], v94 offset:4616\n" \
"ds_read_b64_tr_b16 a[204:205], v94 offset:4160\nds_read_b64_tr_b16 a[206:207], v94 offset:4672\nds_read_b64_tr_b16 v[40:41], v94 offset:4168\nds_read_b64_tr_b16 v[42:43], v94 offset:4680\ns_waitcnt lgkmcnt(0)\ns_add_u32 m0, %[lw], 4096\ns_add_u32 s44, %[wo], 6291456\nbuffer_load_dwordx4 v92, %[rw], s44 offen nt lds\n" \
"s_add_u32 m0, %[lw], 5120\ns_add_u32 s44, %[wo], 6356992\nbuffer_load_dwordx4 v92, %[rw], s44 offen nt lds\ns_add_u32 m0, %[lw], 6144\ns_add_u32 s44, %[wo], 6422528\nbuffer_load_dwordx4 v92, %[rw], s44 offen nt lds\ns_add_u32 m0, %[lw], 7168\ns_add_u32 s44, %[wo], 6488064\n" \
"buffer_load_dwordx4 v92, %[rw], s44 offen nt lds\ns_waitcnt vmcnt(32)\nds_read_b64_tr_b16 a[208:209], v94 offset:8192\nds_read_b64_tr_b16 a[210:211], v94 offset:8704\nds_read_b64_tr_b16 a[216:217], v94 offset:8200\nds_read_b64_tr_b16 a[218:219], v94 offset:8712\nds_read_b64_tr_b16 a[224:225], v94 offset:8256\nds_read_b64_tr_b16 a[226:227], v94 offset:8768\n" \
"ds_read_b64_tr_b16 a[232:233], v94 offset:8264\nds_read_b64_tr_b16 a[234:235], v94 offset:8776\ns_waitcnt lgkmcnt(0)\ns_add_u32 m0, %[lw], 8192\ns_add_u32 s44, %[wo], 6553600\nbuffer_load_dwordx4 v92, %[rw], s44 offen nt lds\ns_add_u32 m0, %[lw], 9216\ns_add_u32 s44, %[wo], 6619136\n" \
"buffer_load_dwordx4 v92, %[rw], s44 offen nt lds\ns_add_u32 m0, %[lw], 10240\ns_add_u32 s44, %[wo], 6684672\nbuffer_load_dwordx4 v92, %[rw], s44 offen nt lds\ns_add_u32 m0, %[lw], 11264\ns_add_u32 s44, %[wo], 6750208\nbuffer_load_dwordx4 v92, %[rw], s44 offen nt lds\ns_waitcnt vmcnt(32)\n" \
"ds_read_b64_tr_b16 a[212:213], v94 offset:12288\nds_read_b64_tr_b16 a[214:215], v94 offset:12800\nds_read_b64_tr_b16 a[220:221], v94 offset:12296\nds_read_b64_tr_b16 a[222:223], v94 offset:12808\nds_read_b64_tr_b16 a[228:229], v94 offset:12352\nds_read_b64_tr_b16 a[230:231], v94 offset:12864\nds_read_b64_tr_b16 a[236:237], v94 offset:12360\nds_read_b64_tr_b16 a[238:239], v94 offset:12872\n" \
"s_waitcnt lgkmcnt(0)\ns_add_u32 m0, %[lw], 12288\ns_add_u32 s44, %[wo], 6815744\nbuffer_load_dwordx4 v92, %[rw], s44 offen nt lds\ns_add_u32 m0, %[lw], 13312\ns_add_u32 s44, %[wo], 6881280\nbuffer_load_dwordx4 v92, %[rw], s44 offen nt lds\ns_add_u32 m0, %[lw], 14336\n" \
"s_add_u32 s44, %[wo], 6946816\nbuffer_load_dwordx4 v92, %[rw], s44 offen nt lds\ns_add_u32 m0, %[lw], 15360\ns_add_u32 s44, %[wo], 7012352\nbuffer_load_dwordx4 v92, %[rw], s44 offen nt lds\ns_waitcnt vmcnt(32)\nds_read_b64_tr_b16 a[240:241], v94 offset:16384\nds_read_b64_tr_b16 a[242:243], v94 offset:16896\n" \
"ds_read_b64_tr_b16 v[36:37], v94 offset:16392\nds_read_b64_tr_b16 v[38:39], v94 offset:16904\nds_read_b64_tr_b16 a[248:249], v94 offset:16448\nds_read_b64_tr_b16 a[250:251], v94 offset:16960\nds_read_b64_tr_b16 v[96:97], v94 offset:16456\nds_read_b64_tr_b16 v[98:99], v94 offset:16968\ns_waitcnt lgkmcnt(0)\ns_add_u32 m0, %[lw], 16384\n" \
"s_add_u32 s44, %[wo], 7077888\nbuffer_load_dwordx4 v92, %[rw], s44 offen nt lds\ns_add_u32 m0, %[lw], 17408\ns_add_u32 s44, %[wo], 7143424\nbuffer_load_dwordx4 v92, %[rw], s44 offen nt lds\ns_add_u32 m0, %[lw], 18432\ns_add_u32 s44, %[wo], 7208960\nbuffer_load_dwordx4 v92, %[rw], s44 offen nt lds\n" \
"s_add_u32 m0, %[lw], 19456\ns_add_u32 s44, %[wo], 7274496\nbuffer_load_dwordx4 v92, %[rw], s44 offen nt lds\ns_waitcnt vmcnt(32)\nds_read_b64_tr_b16 a[244:245], v94 offset:20480\nds_read_b64_tr_b16 a[246:247], v94 offset:20992\nds_read_b64_tr_b16 v[32:33], v94 offset:20488\nds_read_b64_tr_b16 v[34:35], v94 offset:21000\n" \
"ds_read_b64_tr_b16 a[252:253], v94 offset:20544\nds_read_b64_tr_b16 a[254:255], v94 offset:21056\nds_read_b64_tr_b16 v[100:101], v94 offset:20552\nds_read_b64_tr_b16 v[102:103], v94 offset:21064\ns_waitcnt lgkmcnt(0)\ns_add_u32 m0, %[lw], 20480\ns_add_u32 s44, %[wo], 7340032\nbuffer_load_dwordx4 v92, %[rw], s44 offen nt lds\n" \
"s_add_u32 m0, %[lw], 21504\ns_add_u32 s44, %[wo], 7405568\nbuffer_load_dwordx4 v92, %[rw], s44 offen nt lds\ns_add_u32 m0, %[lw], 22528\ns_add_u32 s44, %[wo], 7471104\nbuffer_load_dwordx4 v92, %[rw], s44 offen nt lds\ns_add_u32 m0, %[lw], 23552\ns_add_u32 s44, %[wo], 7536640\n" \
"buffer_load_dwordx4 v92, %[rw], s44 offen nt lds\ns_waitcnt vmcnt(32)\nds_read_b64_tr_b16 v[104:105], v93 offset:0\nds_read_b64_tr_b16 v[106:107], v93 offset:512\nds_read_b64_tr_b16 v[112:113], v93 offset:8\nds_read_b64_tr_b16 v[114:115], v93 offset:520\nds_read_b64_tr_b16 v[28:29], v93 offset:64\nds_read_b64_tr_b16 v[30:31], v93 offset:576\n" \
"ds_read_b64_tr_b16 v[120:121], v93 offset:72\nds_read_b64_tr_b16 v[122:123], v93 offset:584\ns_waitcnt lgkmcnt(0)\ns_add_u32 m0, %[tb], 0\ns_add_u32 s44, %[wo], 7602176\nbuffer_load_dwordx4 v92, %[rw], s44 offen nt lds\ns_add_u32 m0, %[tb], 1024\ns_add_u32 s44, %[wo], 7667712\n" \
"buffer_load_dwordx4 v92, %[rw], s44 offen nt lds\ns_add_u32 m0, %[tb], 2048\ns_add_u32 s44, %[wo], 7733248\nbuffer_load_dwordx4 v92, %[rw], s44 offen nt lds\ns_add_u32 m0, %[tb], 3072\ns_add_u32 s44, %[wo], 7798784\nbuffer_load_dwordx4 v92, %[rw], s44 offen nt lds\ns_waitcnt vmcnt(32)\n" \
"ds_read_b64_tr_b16 v[108:109], v93 offset:4096\nds_read_b64_tr_b16 v[110:111], v93 offset:4608\nds_read_b64_tr_b16 v[116:117], v93 offset:4104\nds_read_b64_tr_b16 v[118:119], v93 offset:4616\nds_read_b64_tr_b16 v[24:25], v93 offset:4160\nds_read_b64_tr_b16 v[26:27], v93 offset:4672\nds_read_b64_tr_b16 v[124:125], v93 offset:4168\nds_read_b64_tr_b16 v[126:127], v93 offset:4680\n" \
"s_waitcnt lgkmcnt(0)\nbuffer_load_dwordx2 v[88:89], off, s[36:39], s45 sc1\ns_add_u32 m0, %[tb], 4096\ns_add_u32 s44, %[wo], 7864320\nbuffer_load_dwordx4 v92, %[rw], s44 offen nt lds\ns_add_u32 m0, %[tb], 5120\ns_add_u32 s44, %[wo], 7929856\nbuffer_load_dwordx4 v92, %[rw], s44 offen nt lds\n" \
"s_add_u32 m0, %[tb], 6144\ns_add_u32 s44, %[wo], 7995392\nbuffer_load_dwordx4 v92, %[rw], s44 offen nt lds\ns_add_u32 m0, %[tb], 7168\ns_add_u32 s44, %[wo], 8060928\nbuffer_load_dwordx4 v92, %[rw], s44 offen nt lds\ns_waitcnt vmcnt(33)\nds_read_b64_tr_b16 v[128:129], v93 offset:8192\n" \
"ds_read_b64_tr_b16 v[130:131], v93 offset:8704\nds_read_b64_tr_b16 v[136:137], v93 offset:8200\nds_read_b64_tr_b16 v[138:139], v93 offset:8712\nds_read_b64_tr_b16 v[144:145], v93 offset:8256\nds_read_b64_tr_b16 v[146:147], v93 offset:8768\nds_read_b64_tr_b16 v[20:21], v93 offset:8264\nds_read_b64_tr_b16 v[22:23], v93 offset:8776\ns_waitcnt lgkmcnt(0)\n" \
"s_add_u32 m0, %[tb], 8192\ns_add_u32 s44, %[wo], 8126464\nbuffer_load_dwordx4 v92, %[rw], s44 offen nt lds\ns_add_u32 m0, %[tb], 9216\ns_add_u32 s44, %[wo], 8192000\nbuffer_load_dwordx4 v92, %[rw], s44 offen nt lds\ns_add_u32 m0, %[tb], 10240\ns_add_u32 s44, %[wo], 8257536\n" \
"buffer_load_dwordx4 v92, %[rw], s44 offen nt lds\ns_add_u32 m0, %[tb], 11264\ns_add_u32 s44, %[wo], 8323072\nbuffer_load_dwordx4 v92, %[rw], s44 offen nt lds\ns_waitcnt vmcnt(33)\nds_read_b64_tr_b16 v[132:133], v94 offset:0\nds_read_b64_tr_b16 v[134:135], v94 offset:512\nds_read_b64_tr_b16 v[140:141], v94 offset:8\n" \
"ds_read_b64_tr_b16 v[142:143], v94 offset:520\nds_read_b64_tr_b16 v[148:149], v94 offset:64\nds_read_b64_tr_b16 v[150:151], v94 offset:576\nds_read_b64_tr_b16 v[16:17], v94 offset:72\nds_read_b64_tr_b16 v[18:19], v94 offset:584\ns_waitcnt lgkmcnt(0)\nds_write_b128 v1, v[84:87] offset:0\nds_write_b128 v1, v[80:83] offset:1024\n" \
"ds_write_b128 v1, v[76:79] offset:2048\nds_write_b128 v1, v[72:75] offset:3072\ns_waitcnt vmcnt(29)\nds_read_b64_tr_b16 v[152:153], v94 offset:4096\nds_read_b64_tr_b16 v[154:155], v94 offset:4608\nds_read_b64_tr_b16 v[160:161], v94 offset:4104\nds_read_b64_tr_b16 v[162:163], v94 offset:4616\nds_read_b64_tr_b16 v[168:169], v94 offset:4160\n" \
"ds_read_b64_tr_b16 v[170:171], v94 offset:4672\nds_read_b64_tr_b16 v[176:177], v94 offset:4168\nds_read_b64_tr_b16 v[178:179], v94 offset:4680\ns_waitcnt lgkmcnt(0)\nds_write_b128 v1, v[68:71] offset:4096\nds_write_b128 v1, v[64:67] offset:5120\nds_write_b128 v1, v[60:63] offset:6144\nds_write_b128 v1, v[56:59] offset:7168\n" \
"s_waitcnt vmcnt(25)\nds_read_b64_tr_b16 v[156:157], v94 offset:8192\nds_read_b64_tr_b16 v[158:159], v94 offset:8704\nds_read_b64_tr_b16 v[164:165], v94 offset:8200\nds_read_b64_tr_b16 v[166:167], v94 offset:8712\nds_read_b64_tr_b16 v[172:173], v94 offset:8256\nds_read_b64_tr_b16 v[174:175], v94 offset:8768\nds_read_b64_tr_b16 v[180:181], v94 offset:8264\n" \
"ds_read_b64_tr_b16 v[182:183], v94 offset:8776\ns_waitcnt lgkmcnt(0)\nds_write_b128 v1, v[52:55] offset:8192\nds_write_b128 v1, v[48:51] offset:9216\nds_write_b128 v1, v[44:47] offset:10240\nds_write_b128 v1, v[40:43] offset:11264\ns_waitcnt vmcnt(21)\nds_read_b64_tr_b16 v[184:185], v94 offset:12288\n" \
"ds_read_b64_tr_b16 v[186:187], v94 offset:12800\nds_read_b64_tr_b16 v[40:41], v94 offset:12296\nds_read_b64_tr_b16 v[42:43], v94 offset:12808\nds_read_b64_tr_b16 v[192:193], v94 offset:12352\nds_read_b64_tr_b16 v[194:195], v94 offset:12864\nds_read_b64_tr_b16 v[200:201], v94 offset:12360\nds_read_b64_tr_b16 v[202:203], v94 offset:12872\ns_waitcnt lgkmcnt(0)\n" \
"ds_write_b128 v1, v[36:39] offset:12288\nds_write_b128 v1, v[32:35] offset:13312\nds_write_b128 v1, v[28:31] offset:14336\nds_write_b128 v1, v[24:27] offset:15360\ns_waitcnt vmcnt(17)\nds_read_b64_tr_b16 v[188:189], v94 offset:16384\nds_read_b64_tr_b16 v[190:191], v94 offset:16896\nds_read_b64_tr_b16 v[24:25], v94 offset:16392\n" \
"ds_read_b64_tr_b16 v[26:27], v94 offset:16904\nds_read_b64_tr_b16 v[196:197], v94 offset:16448\nds_read_b64_tr_b16 v[198:199], v94 offset:16960\nds_read_b64_tr_b16 v[204:205], v94 offset:16456\nds_read_b64_tr_b16 v[206:207], v94 offset:16968\ns_waitcnt lgkmcnt(0)\nds_write_b128 v1, v[20:23] offset:16384\nds_write_b128 v1, v[16:19] offset:17408\n" \
"ds_write_b128 v1, v[40:43] offset:18432\nds_write_b128 v1, v[24:27] offset:19456\ns_waitcnt vmcnt(13)\nds_read_b64_tr_b16 v[208:209], v94 offset:20480\nds_read_b64_tr_b16 v[210:211], v94 offset:20992\nds_read_b64_tr_b16 v[216:217], v94 offset:20488\nds_read_b64_tr_b16 v[218:219], v94 offset:21000\nds_read_b64_tr_b16 v[24:25], v94 offset:20544\n" \
"ds_read_b64_tr_b16 v[26:27], v94 offset:21056\nds_read_b64_tr_b16 v[224:225], v94 offset:20552\nds_read_b64_tr_b16 v[226:227], v94 offset:21064\ns_waitcnt lgkmcnt(0)\nds_write_b128 v1, v[24:27] offset:20480\ns_waitcnt vmcnt(9)\nds_read_b64_tr_b16 v[212:213], v93 offset:0\nds_read_b64_tr_b16 v[214:215], v93 offset:512\n" \
"ds_read_b64_tr_b16 v[220:221], v93 offset:8\nds_read_b64_tr_b16 v[222:223], v93 offset:520\nds_read_b64_tr_b16 v[24:25], v93 offset:64\nds_read_b64_tr_b16 v[26:27], v93 offset:576\nds_read_b64_tr_b16 v[228:229], v93 offset:72\nds_read_b64_tr_b16 v[230:231], v93 offset:584\ns_waitcnt lgkmcnt(0)\nds_write_b128 v1, v[24:27] offset:21504\n" \
"s_waitcnt vmcnt(4)\nds_read_b64_tr_b16 v[232:233], v93 offset:4096\nds_read_b64_tr_b16 v[234:235], v93 offset:4608\nds_read_b64_tr_b16 v[240:241], v93 offset:4104\nds_read_b64_tr_b16 v[242:243], v93 offset:4616\nds_read_b64_tr_b16 v[248:249], v93 offset:4160\nds_read_b64_tr_b16 v[250:251], v93 offset:4672\nds_read_b64_tr_b16 v[24:25], v93 offset:4168\n" \
"ds_read_b64_tr_b16 v[26:27], v93 offset:4680\ns_waitcnt lgkmcnt(0)\nds_write_b128 v1, v[24:27] offset:22528\ns_waitcnt vmcnt(0)\nds_read_b64_tr_b16 v[236:237], v93 offset:8192\nds_read_b64_tr_b16 v[238:239], v93 offset:8704\nds_read_b64_tr_b16 v[244:245], v93 offset:8200\nds_read_b64_tr_b16 v[246:247], v93 offset:8712\n" \
"ds_read_b64_tr_b16 v[252:253], v93 offset:8256\nds_read_b64_tr_b16 v[254:255], v93 offset:8768\nds_read_b64_tr_b16 v[24:25], v93 offset:8264\nds_read_b64_tr_b16 v[26:27], v93 offset:8776\ns_waitcnt lgkmcnt(0)\ns_mov_b32 s43, 0\ns_mov_b32 s86, 110\ns_mov_b32 s89, 0\n" \
".Lqk_hb%=:\nv_cmp_ne_u32 vcc, v95, v88\ns_cbranch_vccnz .Lqk_hb1%=\ns_sleep 4\nbuffer_load_dwordx2 v[88:89], off, s[36:39], s45 sc1\ns_waitcnt vmcnt(0)\ns_add_u32 s43, s43, 1\ns_cmp_lt_u32 s43, 262144\n" \
"s_cbranch_scc1 .Lqk_hb%=\ns_branch .Lqk_abort%=\n.Lqk_hb1%=:\nv_readfirstlane_b32 s44, v89\ns_and_b32 s44, s44, 0x200000\ns_add_u32 s36, s36, s44\ns_addc_u32 s37, s37, 0\nds_write_b128 v1, v[24:27] offset:23552\n" \
"s_waitcnt lgkmcnt(0)\ns_barrier\ns_mov_b32 s40, -1\ns_mov_b32 s43, 0\ns_mov_b32 s61, 0x7fff7fff\ns_mov_b32 s58, 0x3e0293ee\ns_mov_b32 s59, 0xbe0293ee\ns_lshl_b32 s57, %[w], 12\n" \
"s_mov_b32 s62, 0xff00ff00\ns_mov_b32 s63, 0xff00ff00\ns_mov_b32 s56, 0\ns_cmp_eq_u32 %[w], 3\ns_cbranch_scc1 .Lqk_fetch0%=\n.Lqk_pwait%=:\ns_barrier\nds_read_b128 v[80:83], v2 offset:0\n" \
"ds_read_b128 v[84:87], v2 offset:256\nds_read_b128 v[88:91], v2 offset:2048\n.Lqk_proj%=:\ns_waitcnt lgkmcnt(2)\nv_mfma_f32_16x16x32_bf16 v[16:19], a[0:3], v[80:83], 0\nds_read_b128 v[92:95], v2 offset:2304\nv_mfma_f32_16x16x32_bf16 v[20:23], a[8:11], v[80:83], 0\nds_read_b128 v[72:75], v1 offset:0\n" \
"v_mfma_f32_16x16x32_bf16 v[24:27], a[16:19], v[80:83], 0\nv_mfma_f32_16x16x32_bf16 v[28:31], a[24:27], v[80:83], 0\ns_waitcnt lgkmcnt(3)\nv_mfma_f32_16x16x32_bf16 v[32:35], a[0:3], v[84:87], 0\nds_read_b128 v[80:83], v2 offset:4096\nv_mfma_f32_16x16x32_bf16 v[36:39], a[8:11], v[84:87], 0\nv_mfma_f32_16x16x32_bf16 v[40:43], a[16:19], v[84:87], 0\nv_mfma_f32_16x16x32_bf16 v[44:47], a[24:27], v[84:87], 0\n" \
"s_waitcnt lgkmcnt(3)\nv_mfma_f32_16x16x32_bf16 v[16:19], a[4:7], v[88:91], v[16:19]\nds_read_b128 v[84:87], v2 offset:4352\nv_mfma_f32_16x16x32_bf16 v[20:23], a[12:15], v[88:91], v[20:23]\nds_read_b128 v[76:79], v1 offset:1024\nv_mfma_f32_16x16x32_bf16 v[24:27], a[20:23], v[88:91], v[24:27]\nv_mfma_f32_16x16x32_bf16 v[28:31], a[28:31], v[88:91], v[28:31]\ns_waitcnt lgkmcnt(4)\n" \
"v_mfma_f32_16x16x32_bf16 v[32:35], a[4:7], v[92:95], v[32:35]\nds_read_b128 v[88:91], v2 offset:6144\nv_mfma_f32_16x16x32_bf16 v[36:39], a[12:15], v[92:95], v[36:39]\nv_mfma_f32_16x16x32_bf16 v[40:43], a[20:23], v[92:95], v[40:43]\nv_mfma_f32_16x16x32_bf16 v[44:47], a[28:31], v[92:95], v[44:47]\ns_waitcnt lgkmcnt(3)\nv_mfma_f32_16x16x32_bf16 v[16:19], a[32:35], v[80:83], v[16:19]\nds_read_b128 v[92:95], v2 offset:6400\n" \
"v_mfma_f32_16x16x32_bf16 v[20:23], v[72:75], v[80:83], v[20:23]\nds_read_b128 v[64:67], v1 offset:2048\nv_mfma_f32_16x16x32_bf16 v[24:27], a[40:43], v[80:83], v[24:27]\nv_mfma_f32_16x16x32_bf16 v[28:31], a[48:51], v[80:83], v[28:31]\ns_waitcnt lgkmcnt(4)\nv_mfma_f32_16x16x32_bf16 v[32:35], a[32:35], v[84:87], v[32:35]\nds_read_b128 v[80:83], v2 offset:8192\nv_mfma_f32_16x16x32_bf16 v[36:39], v[72:75], v[84:87], v[36:39]\n" \
"v_mfma_f32_16x16x32_bf16 v[40:43], a[40:43], v[84:87], v[40:43]\nv_mfma_f32_16x16x32_bf16 v[44:47], a[48:51], v[84:87], v[44:47]\ns_waitcnt lgkmcnt(3)\nv_mfma_f32_16x16x32_bf16 v[16:19], a[36:39], v[88:91], v[16:19]\nds_read_b128 v[84:87], v2 offset:8448\nv_mfma_f32_16x16x32_bf16 v[20:23], v[76:79], v[88:91], v[20:23]\nds_read_b128 v[68:71], v1 offset:3072\nv_mfma_f32_16x16x32_bf16 v[24:27], a[44:47], v[88:91], v[24:27]\n" \
"v_mfma_f32_16x16x32_bf16 v[28:31], a[52:55], v[88:91], v[28:31]\ns_waitcnt lgkmcnt(4)\nv_mfma_f32_16x16x32_bf16 v[32:35], a[36:39], v[92:95], v[32:35]\nds_read_b128 v[88:91], v2 offset:10240\nv_mfma_f32_16x16x32_bf16 v[36:39], v[76:79], v[92:95], v[36:39]\nv_mfma_f32_16x16x32_bf16 v[40:43], a[44:47], v[92:95], v[40:43]\nv_mfma_f32_16x16x32_bf16 v[44:47], a[52:55], v[92:95], v[44:47]\ns_waitcnt lgkmcnt(3)\n" \
"v_mfma_f32_16x16x32_bf16 v[16:19], a[56:59], v[80:83], v[16:19]\nds_read_b128 v[92:95], v2 offset:10496\nv_mfma_f32_16x16x32_bf16 v[20:23], a[64:67], v[80:83], v[20:23]\nds_read_b128 v[72:75], v1 offset:4096\nv_mfma_f32_16x16x32_bf16 v[24:27], v[64:67], v[80:83], v[24:27]\nv_mfma_f32_16x16x32_bf16 v[28:31], a[72:75], v[80:83], v[28:31]\ns_waitcnt lgkmcnt(4)\nv_mfma_f32_16x16x32_bf16 v[32:35], a[56:59], v[84:87], v[32:35]\n" \
"ds_read_b128 v[80:83], v2 offset:12288\nv_mfma_f32_16x16x32_bf16 v[36:39], a[64:67], v[84:87], v[36:39]\nv_mfma_f32_16x16x32_bf16 v[40:43], v[64:67], v[84:87], v[40:43]\nv_mfma_f32_16x16x32_bf16 v[44:47], a[72:75], v[84:87], v[44:47]\ns_waitcnt lgkmcnt(3)\nv_mfma_f32_16x16x32_bf16 v[16:19], a[60:63], v[88:91], v[16:19]\nds_read_b128 v[84:87], v2 offset:12544\nv_mfma_f32_16x16x32_bf16 v[20:23], a[68:71], v[88:91], v[20:23]\n" \
"ds_read_b128 v[76:79], v1 offset:5120\nv_mfma_f32_16x16x32_bf16 v[24:27], v[68:71], v[88:91], v[24:27]\nv_mfma_f32_16x16x32_bf16 v[28:31], a[76:79], v[88:91], v[28:31]\ns_waitcnt lgkmcnt(4)\nv_mfma_f32_16x16x32_bf16 v[32:35], a[60:63], v[92:95], v[32:35]\nds_read_b128 v[88:91], v2 offset:14336\nv_mfma_f32_16x16x32_bf16 v[36:39], a[68:71], v[92:95], v[36:39]\nv_mfma_f32_16x16x32_bf16 v[40:43], v[68:71], v[92:95], v[40:43]\n" \
"v_mfma_f32_16x16x32_bf16 v[44:47], a[76:79], v[92:95], v[44:47]\n.p2align 6\n.Lqk_pj%=:\ns_waitcnt lgkmcnt(3)\nv_mfma_f32_16x16x32_bf16 v[16:19], a[80:83], v[80:83], v[16:19]\nds_read_b128 v[92:95], v2 offset:14592\nv_mfma_f32_16x16x32_bf16 v[20:23], a[88:91], v[80:83], v[20:23]\ns_add_u32 s40, s40, 1\n" \
"v_mfma_f32_16x16x32_bf16 v[24:27], a[96:99], v[80:83], v[24:27]\ns_lshl_b32 s44, s40, 17\nv_mfma_f32_16x16x32_bf16 v[28:31], v[72:75], v[80:83], v[28:31]\ns_add_u32 s44, s44, %[xn]\ns_waitcnt lgkmcnt(3)\nv_mfma_f32_16x16x32_bf16 v[32:35], a[80:83], v[84:87], v[32:35]\ns_barrier\nds_read_b128 v[80:83], v2 offset:16384\n" \
"v_mfma_f32_16x16x32_bf16 v[36:39], a[88:91], v[84:87], v[36:39]\ns_lshr_b32 s45, s44, 8\nv_mfma_f32_16x16x32_bf16 v[40:43], a[96:99], v[84:87], v[40:43]\ns_and_b32 s45, s45, 0x2000\nv_mfma_f32_16x16x32_bf16 v[44:47], v[72:75], v[84:87], v[44:47]\ns_xor_b32 s41, s44, s45\ns_waitcnt lgkmcnt(2)\nv_mfma_f32_16x16x32_bf16 v[16:19], a[84:87], v[88:91], v[16:19]\n" \
"ds_read_b128 v[84:87], v2 offset:16640\nv_mfma_f32_16x16x32_bf16 v[20:23], a[92:95], v[88:91], v[20:23]\ns_add_u32 s41, s41, %[xw]\ns_and_b32 s42, s40, 3\ns_add_u32 m0, s57, 32768\nv_mfma_f32_16x16x32_bf16 v[24:27], a[100:103], v[88:91], v[24:27]\ns_add_u32 s60, s41, 65536\nbuffer_load_dwordx4 v0, s[36:39], s60 offen offset:0 sc1 lds\n" \
"v_mfma_f32_16x16x32_bf16 v[28:31], v[76:79], v[88:91], v[28:31]\ns_lshl_b32 s42, s42, 15\ns_waitcnt lgkmcnt(2)\nv_mfma_f32_16x16x32_bf16 v[32:35], a[84:87], v[92:95], v[32:35]\nds_read_b128 v[88:91], v2 offset:18432\nv_mfma_f32_16x16x32_bf16 v[36:39], a[92:95], v[92:95], v[36:39]\ns_and_b32 s44, s40, 1\nv_mfma_f32_16x16x32_bf16 v[40:43], a[100:103], v[92:95], v[40:43]\n" \
"s_lshl_b32 s55, s44, 17\nbuffer_load_dwordx4 v0, s[36:39], s60 offen offset:1024 sc1 lds\nv_mfma_f32_16x16x32_bf16 v[44:47], v[76:79], v[92:95], v[44:47]\ns_add_u32 s56, s56, %[fs]\ns_waitcnt lgkmcnt(2)\nv_mfma_f32_16x16x32_bf16 v[16:19], a[104:107], v[80:83], v[16:19]\nds_read_b128 v[92:95], v2 offset:18688\nv_mfma_f32_16x16x32_bf16 v[20:23], a[112:115], v[80:83], v[20:23]\n" \
"ds_read_b128 v[72:75], v1 offset:6144\nv_mfma_f32_16x16x32_bf16 v[24:27], a[120:123], v[80:83], v[24:27]\nbuffer_load_dwordx4 v0, s[36:39], s60 offen offset:2048 sc1 lds\nv_mfma_f32_16x16x32_bf16 v[28:31], a[128:131], v[80:83], v[28:31]\ns_waitcnt lgkmcnt(3)\nv_mfma_f32_16x16x32_bf16 v[32:35], a[104:107], v[84:87], v[32:35]\nds_read_b128 v[80:83], v2 offset:20480\nv_mfma_f32_16x16x32_bf16 v[36:39], a[112:115], v[84:87], v[36:39]\n" \
"v_mfma_f32_16x16x32_bf16 v[40:43], a[120:123], v[84:87], v[40:43]\nbuffer_load_dwordx4 v0, s[36:39], s60 offen offset:3072 sc1 lds\nv_mfma_f32_16x16x32_bf16 v[44:47], a[128:131], v[84:87], v[44:47]\ns_waitcnt lgkmcnt(3)\nv_mfma_f32_16x16x32_bf16 v[16:19], a[108:111], v[88:91], v[16:19]\nds_read_b128 v[84:87], v2 offset:20736\nv_mfma_f32_16x16x32_bf16 v[20:23], a[116:119], v[88:91], v[20:23]\nds_read_b128 v[76:79], v1 offset:7168\n" \
"v_mfma_f32_16x16x32_bf16 v[24:27], a[124:127], v[88:91], v[24:27]\nv_mfma_f32_16x16x32_bf16 v[28:31], a[132:135], v[88:91], v[28:31]\ns_waitcnt lgkmcnt(4)\nv_mfma_f32_16x16x32_bf16 v[32:35], a[108:111], v[92:95], v[32:35]\nds_read_b128 v[88:91], v2 offset:22528\nv_mfma_f32_16x16x32_bf16 v[36:39], a[116:119], v[92:95], v[36:39]\nv_mfma_f32_16x16x32_bf16 v[40:43], a[124:127], v[92:95], v[40:43]\nv_mfma_f32_16x16x32_bf16 v[44:47], a[132:135], v[92:95], v[44:47]\n" \
"s_waitcnt lgkmcnt(3)\nv_mfma_f32_16x16x32_bf16 v[16:19], a[136:139], v[80:83], v[16:19]\nds_read_b128 v[92:95], v2 offset:22784\nv_mfma_f32_16x16x32_bf16 v[20:23], v[72:75], v[80:83], v[20:23]\nds_read_b128 v[64:67], v1 offset:8192\nv_mfma_f32_16x16x32_bf16 v[24:27], a[144:147], v[80:83], v[24:27]\nv_mfma_f32_16x16x32_bf16 v[28:31], a[152:155], v[80:83], v[28:31]\ns_waitcnt lgkmcnt(4)\n" \
"v_mfma_f32_16x16x32_bf16 v[32:35], a[136:139], v[84:87], v[32:35]\nds_read_b128 v[80:83], v2 offset:24576\nv_mfma_f32_16x16x32_bf16 v[36:39], v[72:75], v[84:87], v[36:39]\nv_mfma_f32_16x16x32_bf16 v[40:43], a[144:147], v[84:87], v[40:43]\nv_mfma_f32_16x16x32_bf16 v[44:47], a[152:155], v[84:87], v[44:47]\ns_waitcnt lgkmcnt(3)\nv_mfma_f32_16x16x32_bf16 v[16:19], a[140:143], v[88:91], v[16:19]\nds_read_b128 v[84:87], v2 offset:24832\n" \
"v_mfma_f32_16x16x32_bf16 v[20:23], v[76:79], v[88:91], v[20:23]\nds_read_b128 v[68:71], v1 offset:9216\nv_mfma_f32_16x16x32_bf16 v[24:27], a[148:151], v[88:91], v[24:27]\nv_mfma_f32_16x16x32_bf16 v[28:31], a[156:159], v[88:91], v[28:31]\ns_waitcnt lgkmcnt(4)\nv_mfma_f32_16x16x32_bf16 v[32:35], a[140:143], v[92:95], v[32:35]\nds_read_b128 v[88:91], v2 offset:26624\nv_mfma_f32_16x16x32_bf16 v[36:39], v[76:79], v[92:95], v[36:39]\n" \
"v_mfma_f32_16x16x32_bf16 v[40:43], a[148:151], v[92:95], v[40:43]\nv_mfma_f32_16x16x32_bf16 v[44:47], a[156:159], v[92:95], v[44:47]\ns_waitcnt lgkmcnt(3)\nv_mfma_f32_16x16x32_bf16 v[16:19], a[160:163], v[80:83], v[16:19]\nds_read_b128 v[92:95], v2 offset:26880\nv_mfma_f32_16x16x32_bf16 v[20:23], a[168:171], v[80:83], v[20:23]\nds_read_b128 v[72:75], v1 offset:10240\nv_mfma_f32_16x16x32_bf16 v[24:27], v[64:67], v[80:83], v[24:27]\n" \
"v_mfma_f32_16x16x32_bf16 v[28:31], a[176:179], v[80:83], v[28:31]\ns_waitcnt lgkmcnt(4)\nv_mfma_f32_16x16x32_bf16 v[32:35], a[160:163], v[84:87], v[32:35]\nds_read_b128 v[80:83], v2 offset:28672\nv_mfma_f32_16x16x32_bf16 v[36:39], a[168:171], v[84:87], v[36:39]\nv_mfma_f32_16x16x32_bf16 v[40:43], v[64:67], v[84:87], v[40:43]\nv_mfma_f32_16x16x32_bf16 v[44:47], a[176:179], v[84:87], v[44:47]\ns_waitcnt lgkmcnt(3)\n" \
"v_mfma_f32_16x16x32_bf16 v[16:19], a[164:167], v[88:91], v[16:19]\nds_read_b128 v[84:87], v2 offset:28928\nv_mfma_f32_16x16x32_bf16 v[20:23], a[172:175], v[88:91], v[20:23]\nds_read_b128 v[76:79], v1 offset:11264\nv_mfma_f32_16x16x32_bf16 v[24:27], v[68:71], v[88:91], v[24:27]\nv_mfma_f32_16x16x32_bf16 v[28:31], a[180:183], v[88:91], v[28:31]\ns_waitcnt lgkmcnt(4)\nv_mfma_f32_16x16x32_bf16 v[32:35], a[164:167], v[92:95], v[32:35]\n" \
"ds_read_b128 v[88:91], v2 offset:30720\nv_mfma_f32_16x16x32_bf16 v[36:39], a[172:175], v[92:95], v[36:39]\nv_mfma_f32_16x16x32_bf16 v[40:43], v[68:71], v[92:95], v[40:43]\nv_mfma_f32_16x16x32_bf16 v[44:47], a[180:183], v[92:95], v[44:47]\ns_waitcnt lgkmcnt(3)\nv_mfma_f32_16x16x32_bf16 v[16:19], a[184:187], v[80:83], v[16:19]\nds_read_b128 v[92:95], v2 offset:30976\nv_mfma_f32_16x16x32_bf16 v[20:23], a[192:195], v[80:83], v[20:23]\n" \
"v_mfma_f32_16x16x32_bf16 v[24:27], a[200:203], v[80:83], v[24:27]\nv_mfma_f32_16x16x32_bf16 v[28:31], v[72:75], v[80:83], v[28:31]\ns_waitcnt lgkmcnt(3)\nv_mfma_f32_16x16x32_bf16 v[32:35], a[184:187], v[84:87], v[32:35]\ns_waitcnt vmcnt(0)\ns_barrier\nds_read_b128 v[48:51], v2 offset:32768\nv_mfma_f32_16x16x32_bf16 v[36:39], a[192:195], v[84:87], v[36:39]\n" \
"s_add_u32 m0, s57, 0\nv_mfma_f32_16x16x32_bf16 v[40:43], a[200:203], v[84:87], v[40:43]\ns_add_u32 s60, s41, 98304\nbuffer_load_dwordx4 v0, s[36:39], s60 offen offset:0 sc1 lds\nv_mfma_f32_16x16x32_bf16 v[44:47], v[72:75], v[84:87], v[44:47]\ns_waitcnt lgkmcnt(2)\nv_mfma_f32_16x16x32_bf16 v[16:19], a[188:191], v[88:91], v[16:19]\nds_read_b128 v[52:55], v2 offset:33024\n" \
"v_mfma_f32_16x16x32_bf16 v[20:23], a[196:199], v[88:91], v[20:23]\nv_mfma_f32_16x16x32_bf16 v[24:27], a[204:207], v[88:91], v[24:27]\nbuffer_load_dwordx4 v0, s[36:39], s60 offen offset:1024 sc1 lds\nv_mfma_f32_16x16x32_bf16 v[28:31], v[76:79], v[88:91], v[28:31]\ns_waitcnt lgkmcnt(2)\nv_mfma_f32_16x16x32_bf16 v[32:35], a[188:191], v[92:95], v[32:35]\nds_read_b128 v[56:59], v2 offset:34816\nv_mfma_f32_16x16x32_bf16 v[36:39], a[196:199], v[92:95], v[36:39]\n" \
"v_mfma_f32_16x16x32_bf16 v[40:43], a[204:207], v[92:95], v[40:43]\nbuffer_load_dwordx4 v0, s[36:39], s60 offen offset:2048 sc1 lds\nv_mfma_f32_16x16x32_bf16 v[44:47], v[76:79], v[92:95], v[44:47]\ns_waitcnt lgkmcnt(2)\nv_mfma_f32_16x16x32_bf16 v[16:19], a[208:211], v[48:51], v[16:19]\nds_read_b128 v[60:63], v2 offset:35072\nv_mfma_f32_16x16x32_bf16 v[20:23], a[216:219], v[48:51], v[20:23]\nds_read_b128 v[72:75], v1 offset:12288\n" \
"v_mfma_f32_16x16x32_bf16 v[24:27], a[224:227], v[48:51], v[24:27]\nbuffer_load_dwordx4 v0, s[36:39], s60 offen offset:3072 sc1 lds\nv_mfma_f32_16x16x32_bf16 v[28:31], a[232:235], v[48:51], v[28:31]\ns_waitcnt lgkmcnt(3)\nv_mfma_f32_16x16x32_bf16 v[32:35], a[208:211], v[52:55], v[32:35]\nds_read_b128 v[48:51], v2 offset:36864\nv_mfma_f32_16x16x32_bf16 v[36:39], a[216:219], v[52:55], v[36:39]\nv_mfma_f32_16x16x32_bf16 v[40:43], a[224:227], v[52:55], v[40:43]\n" \
"v_mfma_f32_16x16x32_bf16 v[44:47], a[232:235], v[52:55], v[44:47]\ns_waitcnt lgkmcnt(3)\nv_mfma_f32_16x16x32_bf16 v[16:19], a[212:215], v[56:59], v[16:19]\nds_read_b128 v[52:55], v2 offset:37120\nv_mfma_f32_16x16x32_bf16 v[20:23], a[220:223], v[56:59], v[20:23]\nds_read_b128 v[76:79], v1 offset:13312\nv_mfma_f32_16x16x32_bf16 v[24:27], a[228:231], v[56:59], v[24:27]\nv_mfma_f32_16x16x32_bf16 v[28:31], a[236:239], v[56:59], v[28:31]\n" \
"s_waitcnt lgkmcnt(4)\nv_mfma_f32_16x16x32_bf16 v[32:35], a[212:215], v[60:63], v[32:35]\nds_read_b128 v[56:59], v2 offset:38912\nv_mfma_f32_16x16x32_bf16 v[36:39], a[220:223], v[60:63], v[36:39]\nv_mfma_f32_16x16x32_bf16 v[40:43], a[228:231], v[60:63], v[40:43]\nv_mfma_f32_16x16x32_bf16 v[44:47], a[236:239], v[60:63], v[44:47]\ns_waitcnt lgkmcnt(3)\nv_mfma_f32_16x16x32_bf16 v[16:19], a[240:243], v[48:51], v[16:19]\n" \
"ds_read_b128 v[60:63], v2 offset:39168\nv_mfma_f32_16x16x32_bf16 v[20:23], v[72:75], v[48:51], v[20:23]\nds_read_b128 v[64:67], v1 offset:14336\nv_mfma_f32_16x16x32_bf16 v[24:27], a[248:251], v[48:51], v[24:27]\nv_mfma_f32_16x16x32_bf16 v[28:31], v[96:99], v[48:51], v[28:31]\ns_waitcnt lgkmcnt(4)\nv_mfma_f32_16x16x32_bf16 v[32:35], a[240:243], v[52:55], v[32:35]\nds_read_b128 v[48:51], v2 offset:40960\n" \
"v_mfma_f32_16x16x32_bf16 v[36:39], v[72:75], v[52:55], v[36:39]\nv_mfma_f32_16x16x32_bf16 v[40:43], a[248:251], v[52:55], v[40:43]\nv_mfma_f32_16x16x32_bf16 v[44:47], v[96:99], v[52:55], v[44:47]\ns_waitcnt lgkmcnt(3)\nv_mfma_f32_16x16x32_bf16 v[16:19], a[244:247], v[56:59], v[16:19]\nds_read_b128 v[52:55], v2 offset:41216\nv_mfma_f32_16x16x32_bf16 v[20:23], v[76:79], v[56:59], v[20:23]\nds_read_b128 v[68:71], v1 offset:15360\n" \
"v_mfma_f32_16x16x32_bf16 v[24:27], a[252:255], v[56:59], v[24:27]\nv_mfma_f32_16x16x32_bf16 v[28:31], v[100:103], v[56:59], v[28:31]\ns_waitcnt lgkmcnt(4)\nv_mfma_f32_16x16x32_bf16 v[32:35], a[244:247], v[60:63], v[32:35]\nds_read_b128 v[56:59], v2 offset:43008\nv_mfma_f32_16x16x32_bf16 v[36:39], v[76:79], v[60:63], v[36:39]\nv_mfma_f32_16x16x32_bf16 v[40:43], a[252:255], v[60:63], v[40:43]\nv_mfma_f32_16x16x32_bf16 v[44:47], v[100:103], v[60:63], v[44:47]\n" \
"s_waitcnt lgkmcnt(3)\nv_mfma_f32_16x16x32_bf16 v[16:19], v[104:107], v[48:51], v[16:19]\nds_read_b128 v[60:63], v2 offset:43264\nv_mfma_f32_16x16x32_bf16 v[20:23], v[112:115], v[48:51], v[20:23]\nds_read_b128 v[72:75], v1 offset:16384\nv_mfma_f32_16x16x32_bf16 v[24:27], v[64:67], v[48:51], v[24:27]\nv_mfma_f32_16x16x32_bf16 v[28:31], v[120:123], v[48:51], v[28:31]\ns_waitcnt lgkmcnt(4)\n" \
"v_mfma_f32_16x16x32_bf16 v[32:35], v[104:107], v[52:55], v[32:35]\nds_read_b128 v[48:51], v2 offset:45056\nv_mfma_f32_16x16x32_bf16 v[36:39], v[112:115], v[52:55], v[36:39]\nv_mfma_f32_16x16x32_bf16 v[40:43], v[64:67], v[52:55], v[40:43]\nv_mfma_f32_16x16x32_bf16 v[44:47], v[120:123], v[52:55], v[44:47]\ns_waitcnt lgkmcnt(3)\nv_mfma_f32_16x16x32_bf16 v[16:19], v[108:111], v[56:59], v[16:19]\nds_read_b128 v[52:55], v2 offset:45312\n" \
"v_mfma_f32_16x16x32_bf16 v[20:23], v[116:119], v[56:59], v[20:23]\nds_read_b128 v[76:79], v1 offset:17408\nv_mfma_f32_16x16x32_bf16 v[24:27], v[68:71], v[56:59], v[24:27]\nv_mfma_f32_16x16x32_bf16 v[28:31], v[124:127], v[56:59], v[28:31]\ns_waitcnt lgkmcnt(4)\nv_mfma_f32_16x16x32_bf16 v[32:35], v[108:111], v[60:63], v[32:35]\nds_read_b128 v[56:59], v2 offset:47104\nv_mfma_f32_16x16x32_bf16 v[36:39], v[116:119], v[60:63], v[36:39]\n" \
"v_mfma_f32_16x16x32_bf16 v[40:43], v[68:71], v[60:63], v[40:43]\nv_mfma_f32_16x16x32_bf16 v[44:47], v[124:127], v[60:63], v[44:47]\ns_waitcnt lgkmcnt(3)\nv_mfma_f32_16x16x32_bf16 v[16:19], v[128:131], v[48:51], v[16:19]\nds_read_b128 v[60:63], v2 offset:47360\nv_mfma_f32_16x16x32_bf16 v[20:23], v[136:139], v[48:51], v[20:23]\nv_mfma_f32_16x16x32_bf16 v[24:27], v[144:147], v[48:51], v[24:27]\nv_mfma_f32_16x16x32_bf16 v[28:31], v[72:75], v[48:51], v[28:31]\n" \
"s_waitcnt lgkmcnt(3)\nv_mfma_f32_16x16x32_bf16 v[32:35], v[128:131], v[52:55], v[32:35]\ns_waitcnt lgkmcnt(0)\ns_waitcnt vmcnt(0)\ns_barrier\nds_read_b128 v[48:51], v2 offset:0\nv_mfma_f32_16x16x32_bf16 v[36:39], v[136:139], v[52:55], v[36:39]\nv_mfma_f32_16x16x32_bf16 v[40:43], v[144:147], v[52:55], v[40:43]\n" \
"v_mfma_f32_16x16x32_bf16 v[44:47], v[72:75], v[52:55], v[44:47]\nv_mfma_f32_16x16x32_bf16 v[16:19], v[132:135], v[56:59], v[16:19]\nds_read_b128 v[52:55], v2 offset:256\nv_mfma_f32_16x16x32_bf16 v[20:23], v[140:143], v[56:59], v[20:23]\nv_mfma_f32_16x16x32_bf16 v[24:27], v[148:151], v[56:59], v[24:27]\nv_mfma_f32_16x16x32_bf16 v[28:31], v[76:79], v[56:59], v[28:31]\nv_mfma_f32_16x16x32_bf16 v[32:35], v[132:135], v[60:63], v[32:35]\nds_read_b128 v[56:59], v2 offset:2048\n" \
"v_mfma_f32_16x16x32_bf16 v[36:39], v[140:143], v[60:63], v[36:39]\nv_mfma_f32_16x16x32_bf16 v[40:43], v[148:151], v[60:63], v[40:43]\nv_mfma_f32_16x16x32_bf16 v[44:47], v[76:79], v[60:63], v[44:47]\ns_waitcnt lgkmcnt(2)\nv_mfma_f32_16x16x32_bf16 v[16:19], v[152:155], v[48:51], v[16:19]\nds_read_b128 v[60:63], v2 offset:2304\nv_mfma_f32_16x16x32_bf16 v[20:23], v[160:163], v[48:51], v[20:23]\nds_read_b128 v[72:75], v1 offset:18432\n" \
"v_mfma_f32_16x16x32_bf16 v[24:27], v[168:171], v[48:51], v[24:27]\nv_mfma_f32_16x16x32_bf16 v[28:31], v[176:179], v[48:51], v[28:31]\ns_waitcnt lgkmcnt(3)\nv_mfma_f32_16x16x32_bf16 v[32:35], v[152:155], v[52:55], v[32:35]\nds_read_b128 v[48:51], v2 offset:4096\nv_mfma_f32_16x16x32_bf16 v[36:39], v[160:163], v[52:55], v[36:39]\nv_mfma_f32_16x16x32_bf16 v[40:43], v[168:171], v[52:55], v[40:43]\nv_mfma_f32_16x16x32_bf16 v[44:47], v[176:179], v[52:55], v[44:47]\n" \
"s_waitcnt lgkmcnt(3)\nv_mfma_f32_16x16x32_bf16 v[16:19], v[156:159], v[56:59], v[16:19]\nds_read_b128 v[52:55], v2 offset:4352\nv_mfma_f32_16x16x32_bf16 v[20:23], v[164:167], v[56:59], v[20:23]\nds_read_b128 v[76:79], v1 offset:19456\nv_mfma_f32_16x16x32_bf16 v[24:27], v[172:175], v[56:59], v[24:27]\nv_mfma_f32_16x16x32_bf16 v[28:31], v[180:183], v[56:59], v[28:31]\ns_waitcnt lgkmcnt(4)\n" \
"v_mfma_f32_16x16x32_bf16 v[32:35], v[156:159], v[60:63], v[32:35]\nds_read_b128 v[56:59], v2 offset:6144\nv_mfma_f32_16x16x32_bf16 v[36:39], v[164:167], v[60:63], v[36:39]\nv_mfma_f32_16x16x32_bf16 v[40:43], v[172:175], v[60:63], v[40:43]\nv_mfma_f32_16x16x32_bf16 v[44:47], v[180:183], v[60:63], v[44:47]\ns_waitcnt lgkmcnt(3)\nv_mfma_f32_16x16x32_bf16 v[16:19], v[184:187], v[48:51], v[16:19]\nds_read_b128 v[60:63], v2 offset:6400\n" \
"v_mfma_f32_16x16x32_bf16 v[20:23], v[72:75], v[48:51], v[20:23]\nds_read_b128 v[64:67], v1 offset:20480\nv_mfma_f32_16x16x32_bf16 v[24:27], v[192:195], v[48:51], v[24:27]\nv_mfma_f32_16x16x32_bf16 v[28:31], v[200:203], v[48:51], v[28:31]\ns_waitcnt lgkmcnt(4)\nv_mfma_f32_16x16x32_bf16 v[32:35], v[184:187], v[52:55], v[32:35]\nds_read_b128 v[48:51], v2 offset:8192\nv_mfma_f32_16x16x32_bf16 v[36:39], v[72:75], v[52:55], v[36:39]\n" \
"v_mfma_f32_16x16x32_bf16 v[40:43], v[192:195], v[52:55], v[40:43]\nv_mfma_f32_16x16x32_bf16 v[44:47], v[200:203], v[52:55], v[44:47]\ns_waitcnt lgkmcnt(3)\nv_mfma_f32_16x16x32_bf16 v[16:19], v[188:191], v[56:59], v[16:19]\nds_read_b128 v[52:55], v2 offset:10240\nv_mfma_f32_16x16x32_bf16 v[20:23], v[76:79], v[56:59], v[20:23]\nds_read_b128 v[68:71], v1 offset:21504\nv_mfma_f32_16x16x32_bf16 v[24:27], v[196:199], v[56:59], v[24:27]\n" \
"buffer_load_dwordx4 v[80:83], v3, s[36:39], s42 offen offset:0 sc1\nv_mfma_f32_16x16x32_bf16 v[28:31], v[204:207], v[56:59], v[28:31]\ns_waitcnt lgkmcnt(4)\nv_mfma_f32_16x16x32_bf16 v[32:35], v[188:191], v[60:63], v[32:35]\nds_read_b128 v[56:59], v2 offset:12288\nv_mfma_f32_16x16x32_bf16 v[36:39], v[76:79], v[60:63], v[36:39]\nds_read_b128 v[72:75], v1 offset:22528\nv_mfma_f32_16x16x32_bf16 v[40:43], v[196:199], v[60:63], v[40:43]\n" \
"buffer_load_dwordx4 v[84:87], v3, s[36:39], s42 offen offset:1024 sc1\nv_mfma_f32_16x16x32_bf16 v[44:47], v[204:207], v[60:63], v[44:47]\ns_waitcnt lgkmcnt(4)\nv_mfma_f32_16x16x32_bf16 v[16:19], v[208:211], v[48:51], v[16:19]\nds_read_b128 v[60:63], v2 offset:14336\nv_mfma_f32_16x16x32_bf16 v[20:23], v[216:219], v[48:51], v[20:23]\nds_read_b128 v[76:79], v1 offset:23552\nv_mfma_f32_16x16x32_bf16 v[24:27], v[64:67], v[48:51], v[24:27]\n" \
"buffer_load_dwordx4 v[88:91], v3, s[36:39], s42 offen offset:2048 sc1\nv_mfma_f32_16x16x32_bf16 v[28:31], v[224:227], v[48:51], v[28:31]\ns_waitcnt lgkmcnt(5)\nv_mfma_f32_16x16x32_bf16 v[16:19], v[212:215], v[52:55], v[16:19]\nds_read_b128 v[48:51], v2 offset:8448\nv_mfma_f32_16x16x32_bf16 v[20:23], v[220:223], v[52:55], v[20:23]\ns_waitcnt lgkmcnt(5)\nv_mfma_f32_16x16x32_bf16 v[24:27], v[68:71], v[52:55], v[24:27]\n" \
"buffer_load_dwordx4 v[92:95], v3, s[36:39], s42 offen offset:3072 sc1\nv_mfma_f32_16x16x32_bf16 v[28:31], v[228:231], v[52:55], v[28:31]\ns_waitcnt lgkmcnt(4)\nv_mfma_f32_16x16x32_bf16 v[16:19], v[232:235], v[56:59], v[16:19]\nds_read_b128 v[52:55], v2 offset:10496\nv_mfma_f32_16x16x32_bf16 v[20:23], v[240:243], v[56:59], v[20:23]\nv_mfma_f32_16x16x32_bf16 v[24:27], v[248:251], v[56:59], v[24:27]\ns_waitcnt lgkmcnt(4)\n" \
"v_mfma_f32_16x16x32_bf16 v[28:31], v[72:75], v[56:59], v[28:31]\ns_waitcnt lgkmcnt(3)\nv_mfma_f32_16x16x32_bf16 v[16:19], v[236:239], v[60:63], v[16:19]\nds_read_b128 v[56:59], v2 offset:12544\nv_mfma_f32_16x16x32_bf16 v[20:23], v[244:247], v[60:63], v[20:23]\nv_mfma_f32_16x16x32_bf16 v[24:27], v[252:255], v[60:63], v[24:27]\ns_waitcnt lgkmcnt(3)\nv_mfma_f32_16x16x32_bf16 v[28:31], v[76:79], v[60:63], v[28:31]\n" \
"s_waitcnt lgkmcnt(2)\nv_mfma_f32_16x16x32_bf16 v[32:35], v[208:211], v[48:51], v[32:35]\nds_read_b128 v[60:63], v2 offset:14592\nv_mfma_f32_16x16x32_bf16 v[36:39], v[216:219], v[48:51], v[36:39]\nv_cvt_pk_bf16_f32 v20, v16, v20\nv_cvt_pk_bf16_f32 v21, v17, v21\nv_mfma_f32_16x16x32_bf16 v[40:43], v[64:67], v[48:51], v[40:43]\nv_cvt_pk_bf16_f32 v22, v18, v22\n" \
"v_cvt_pk_bf16_f32 v23, v19, v23\nv_mfma_f32_16x16x32_bf16 v[44:47], v[224:227], v[48:51], v[44:47]\ns_waitcnt lgkmcnt(2)\nds_write_b128 v5, v[20:23] offset:0\nv_mfma_f32_16x16x32_bf16 v[32:35], v[212:215], v[52:55], v[32:35]\nv_cvt_pk_bf16_f32 v28, v24, v28\nv_cvt_pk_bf16_f32 v29, v25, v29\nv_mfma_f32_16x16x32_bf16 v[36:39], v[220:223], v[52:55], v[36:39]\n" \
"v_cvt_pk_bf16_f32 v30, v26, v30\nv_cvt_pk_bf16_f32 v31, v27, v31\nv_mfma_f32_16x16x32_bf16 v[40:43], v[68:71], v[52:55], v[40:43]\nv_mov_b64 v[48:49], 0\nv_mov_b64 v[50:51], 0\nv_mfma_f32_16x16x32_bf16 v[44:47], v[228:231], v[52:55], v[44:47]\ns_cmp_eq_u32 %[w], 1\nds_write_b128 v5, v[28:31] offset:512\n" \
"s_cbranch_scc1 .Lqk_c1%=\ns_cmp_eq_u32 %[w], 2\ns_cbranch_scc1 .Lqk_c2%=\ns_cmp_eq_u32 %[w], 3\ns_cbranch_scc1 .Lqk_c3%=\n.Lqk_c0%=:\ns_waitcnt lgkmcnt(3)\nv_mfma_f32_16x16x32_bf16 v[32:35], v[232:235], v[56:59], v[32:35]\n" \
"v_mov_b64 v[52:53], 0\nv_mov_b64 v[54:55], 0\nv_mfma_f32_16x16x32_bf16 v[36:39], v[240:243], v[56:59], v[36:39]\ns_waitcnt lgkmcnt(2)\nds_read_b128 v[64:67], v1 offset:0\nv_mfma_f32_16x16x32_bf16 v[32:35], v[236:239], v[60:63], v[32:35]\nds_read_b128 v[68:71], v1 offset:1024\nv_mfma_f32_16x16x32_bf16 v[36:39], v[244:247], v[60:63], v[36:39]\n" \
"s_bfe_i32 s54, s40, 0x10000\nv_mfma_f32_16x16x32_bf16 v[40:43], v[248:251], v[56:59], v[40:43]\nv_mfma_f32_16x16x32_bf16 v[44:47], v[72:75], v[56:59], v[44:47]\nv_cvt_pk_bf16_f32 v36, v32, v36\nv_cvt_pk_bf16_f32 v37, v33, v37\nv_mfma_f32_16x16x32_bf16 v[40:43], v[252:255], v[60:63], v[40:43]\nv_cvt_pk_bf16_f32 v38, v34, v38\nv_cvt_pk_bf16_f32 v39, v35, v39\n" \
"v_mfma_f32_16x16x32_bf16 v[44:47], v[76:79], v[60:63], v[44:47]\nds_read_b128 v[72:75], v1 offset:2048\nds_read_b128 v[76:79], v1 offset:3072\nv_mov_b64 v[56:57], 0\nv_mov_b64 v[58:59], 0\nv_mov_b64 v[60:61], 0\nv_mov_b64 v[62:63], 0\n.Lqk_poll0%=:\n" \
"s_waitcnt vmcnt(0)\nv_smfmac_f32_16x16x64_bf16 v[48:51], v[80:83], a[0:7], v4\nds_write_b128 v0, v[80:83] offset:16384\nv_max3_u32 v16, v80, v82, v84\nv_smfmac_f32_16x16x64_bf16 v[52:55], v[80:83], a[8:15], v4\nv_max3_u32 v16, v16, v86, v88\nv_smfmac_f32_16x16x64_bf16 v[56:59], v[80:83], a[16:23], v4\nds_write_b128 v0, v[84:87] offset:17408\n" \
"v_max3_u32 v16, v16, v90, v92\nv_max_u32 v16, v16, v94\nv_smfmac_f32_16x16x64_bf16 v[60:63], v[80:83], a[24:31], v4\nv_cmp_eq_u32 vcc, -1, v16\nv_smfmac_f32_16x16x64_bf16 v[48:51], v[84:87], a[32:39], v4\nds_write_b128 v0, v[88:91] offset:18432\ns_cbranch_vccnz .Lqk_re0%=\ns_waitcnt lgkmcnt(5)\n" \
"v_smfmac_f32_16x16x64_bf16 v[52:55], v[84:87], v[64:71], v4\nv_smfmac_f32_16x16x64_bf16 v[56:59], v[84:87], a[40:47], v4\nds_write_b128 v0, v[92:95] offset:19456\nv_smfmac_f32_16x16x64_bf16 v[60:63], v[84:87], a[48:55], v4\nv_smfmac_f32_16x16x64_bf16 v[48:51], v[88:91], a[56:63], v4\nv_smfmac_f32_16x16x64_bf16 v[52:55], v[88:91], a[64:71], v4\ns_waitcnt lgkmcnt(0)\n.Lqk_jn0%=:\n" \
"s_barrier\ns_memrealtime s[68:69]\nds_read_b128 v[80:83], v0 offset:20480\nds_read_b128 v[64:67], v1 offset:4096\nds_read_b128 v[68:71], v1 offset:5120\nv_mov_b32 v24, s40\nv_smfmac_f32_16x16x64_bf16 v[56:59], v[88:91], v[72:79], v4\nds_read_b128 v[72:75], v1 offset:6144\n" \
"ds_read_b128 v[76:79], v1 offset:7168\nv_smfmac_f32_16x16x64_bf16 v[60:63], v[88:91], a[72:79], v4\nds_read_b128 v[84:87], v0 offset:21504\nds_read_b128 v[88:91], v0 offset:22528\nv_smfmac_f32_16x16x64_bf16 v[48:51], v[92:95], a[80:87], v4\nds_write_b128 v5, v[36:39] offset:256\nv_cvt_pk_bf16_f32 v44, v40, v44\nv_cvt_pk_bf16_f32 v45, v41, v45\n" \
"v_cvt_pk_bf16_f32 v46, v42, v46\nv_cvt_pk_bf16_f32 v47, v43, v47\nv_smfmac_f32_16x16x64_bf16 v[52:55], v[92:95], a[88:95], v4\nv_smfmac_f32_16x16x64_bf16 v[56:59], v[92:95], a[96:103], v4\ns_waitcnt lgkmcnt(5)\nv_smfmac_f32_16x16x64_bf16 v[60:63], v[92:95], v[64:71], v4\ns_sub_u32 s88, s86, s89\nds_read_b128 v[92:95], v0 offset:23552\n" \
"ds_read_b128 v[64:67], v1 offset:8192\nds_read_b128 v[68:71], v1 offset:9216\nv_smfmac_f32_16x16x64_bf16 v[48:51], v[80:83], a[104:111], v4\ns_min_u32 s88, s88, 150\nds_write_b128 v5, v[44:47] offset:768\nv_smfmac_f32_16x16x64_bf16 v[52:55], v[80:83], a[112:119], v4\ns_add_u32 s88, s88, s68\ns_mov_b32 s89, s68\n" \
"v_mov_b32 v25, s88\nv_smfmac_f32_16x16x64_bf16 v[56:59], v[80:83], a[120:127], v4\nbuffer_store_dwordx2 v[24:25], off, s[36:39], %[sg] sc1\nv_smfmac_f32_16x16x64_bf16 v[60:63], v[80:83], a[128:135], v4\ns_waitcnt lgkmcnt(6)\nds_read_b128 v[80:83], v0 offset:24576\nv_smfmac_f32_16x16x64_bf16 v[48:51], v[84:87], a[136:143], v4\nv_smfmac_f32_16x16x64_bf16 v[52:55], v[84:87], v[72:79], v4\n" \
"ds_read_b128 v[72:75], v1 offset:10240\nds_read_b128 v[76:79], v1 offset:11264\nv_smfmac_f32_16x16x64_bf16 v[56:59], v[84:87], a[144:151], v4\nv_smfmac_f32_16x16x64_bf16 v[60:63], v[84:87], a[152:159], v4\ns_waitcnt lgkmcnt(8)\nds_read_b128 v[84:87], v0 offset:25600\nv_smfmac_f32_16x16x64_bf16 v[48:51], v[88:91], a[160:167], v4\ns_waitcnt lgkmcnt(4)\n" \
"s_barrier\nv_smfmac_f32_16x16x64_bf16 v[52:55], v[88:91], a[168:175], v4\nv_smfmac_f32_16x16x64_bf16 v[56:59], v[88:91], v[64:71], v4\nds_read_b128 v[64:67], v1 offset:12288\nds_read_b128 v[68:71], v1 offset:13312\nv_smfmac_f32_16x16x64_bf16 v[60:63], v[88:91], a[176:183], v4\nds_read_b128 v[88:91], v0 offset:26624\nv_smfmac_f32_16x16x64_bf16 v[48:51], v[92:95], a[184:191], v4\n" \
"ds_read_b128 v[16:19], v7 offset:0\nv_smfmac_f32_16x16x64_bf16 v[52:55], v[92:95], a[192:199], v4\nv_smfmac_f32_16x16x64_bf16 v[56:59], v[92:95], a[200:207], v4\ns_waitcnt lgkmcnt(5)\nds_read_b128 v[20:23], v7 offset:64\nv_smfmac_f32_16x16x64_bf16 v[60:63], v[92:95], v[72:79], v4\nds_read_b128 v[92:95], v0 offset:27648\nds_read_b128 v[72:75], v1 offset:14336\n" \
"ds_read_b128 v[76:79], v1 offset:15360\nv_smfmac_f32_16x16x64_bf16 v[48:51], v[80:83], a[208:215], v4\nds_read_b128 v[24:27], v7 offset:2048\nv_smfmac_f32_16x16x64_bf16 v[52:55], v[80:83], a[216:223], v4\nv_smfmac_f32_16x16x64_bf16 v[56:59], v[80:83], a[224:231], v4\nds_read_b128 v[28:31], v7 offset:2112\nv_smfmac_f32_16x16x64_bf16 v[60:63], v[80:83], a[232:239], v4\ns_waitcnt lgkmcnt(10)\n" \
"ds_read_b128 v[80:83], v0 offset:28672\nv_smfmac_f32_16x16x64_bf16 v[48:51], v[84:87], a[240:247], v4\ns_waitcnt lgkmcnt(9)\nds_read_b128 v[32:35], v7 offset:4096\nv_smfmac_f32_16x16x64_bf16 v[52:55], v[84:87], v[64:71], v4\nds_read_b128 v[64:67], v1 offset:16384\nds_read_b128 v[68:71], v1 offset:17408\nv_smfmac_f32_16x16x64_bf16 v[56:59], v[84:87], a[248:255], v4\n" \
"ds_read_b128 v[36:39], v7 offset:4160\nv_smfmac_f32_16x16x64_bf16 v[60:63], v[84:87], v[96:103], v4\ns_waitcnt lgkmcnt(12)\nds_read_b128 v[84:87], v0 offset:29696\nv_smfmac_f32_16x16x64_bf16 v[48:51], v[88:91], v[104:111], v4\nds_read_b128 v[40:43], v7 offset:6144\nv_smfmac_f32_16x16x64_bf16 v[52:55], v[88:91], v[112:119], v4\ns_waitcnt lgkmcnt(9)\n" \
"v_smfmac_f32_16x16x64_bf16 v[56:59], v[88:91], v[72:79], v4\nds_read_b128 v[72:75], v1 offset:18432\nds_read_b128 v[76:79], v1 offset:19456\nv_smfmac_f32_16x16x64_bf16 v[60:63], v[88:91], v[120:127], v4\nds_read_b128 v[88:91], v0 offset:30720\nv_smfmac_f32_16x16x64_bf16 v[48:51], v[92:95], v[128:135], v4\nds_read_b128 v[44:47], v7 offset:6208\nv_smfmac_f32_16x16x64_bf16 v[52:55], v[92:95], v[136:143], v4\n" \
"v_smfmac_f32_16x16x64_bf16 v[56:59], v[92:95], v[144:151], v4\ns_waitcnt lgkmcnt(7)\nv_smfmac_f32_16x16x64_bf16 v[60:63], v[92:95], v[64:71], v4\nds_read_b128 v[92:95], v0 offset:31744\nds_read_b128 v[64:67], v1 offset:20480\nds_read_b128 v[68:71], v1 offset:21504\nv_smfmac_f32_16x16x64_bf16 v[48:51], v[80:83], v[152:159], v4\nv_smfmac_f32_16x16x64_bf16 v[52:55], v[80:83], v[160:167], v4\n" \
"v_smfmac_f32_16x16x64_bf16 v[56:59], v[80:83], v[168:175], v4\nv_smfmac_f32_16x16x64_bf16 v[60:63], v[80:83], v[176:183], v4\ns_waitcnt lgkmcnt(8)\nv_smfmac_f32_16x16x64_bf16 v[48:51], v[84:87], v[184:191], v4\ns_waitcnt lgkmcnt(5)\nv_smfmac_f32_16x16x64_bf16 v[52:55], v[84:87], v[72:79], v4\nds_read_b128 v[72:75], v1 offset:22528\nds_read_b128 v[76:79], v1 offset:23552\n" \
"v_smfmac_f32_16x16x64_bf16 v[56:59], v[84:87], v[192:199], v4\nv_smfmac_f32_16x16x64_bf16 v[60:63], v[84:87], v[200:207], v4\ns_waitcnt lgkmcnt(6)\nv_smfmac_f32_16x16x64_bf16 v[48:51], v[88:91], v[208:215], v4\nv_smfmac_f32_16x16x64_bf16 v[52:55], v[88:91], v[216:223], v4\ns_waitcnt lgkmcnt(4)\nv_smfmac_f32_16x16x64_bf16 v[48:51], v[92:95], v[232:239], v4\nv_smfmac_f32_16x16x64_bf16 v[52:55], v[92:95], v[240:247], v4\n" \
"s_waitcnt lgkmcnt(2)\nv_smfmac_f32_16x16x64_bf16 v[56:59], v[88:91], v[64:71], v4\nv_add_f32 v48, v48, v50\nv_smfmac_f32_16x16x64_bf16 v[60:63], v[88:91], v[224:231], v4\nv_add_f32 v52, v52, v54\nv_add_f32 v49, v49, v51\nv_smfmac_f32_16x16x64_bf16 v[56:59], v[92:95], v[248:255], v4\ns_waitcnt lgkmcnt(0)\n" \
"v_add_f32 v53, v53, v55\nv_cvt_pk_bf16_f32 v50, v48, v52\nv_smfmac_f32_16x16x64_bf16 v[60:63], v[92:95], v[72:79], v4\nv_cvt_pk_bf16_f32 v51, v49, v53\nds_write2_b32 v6, v50, v51 offset0:0 offset1:4\ns_nop 1\nv_add_f32 v56, v56, v58\nv_add_f32 v57, v57, v59\n" \
"s_nop 1\nv_add_f32 v60, v60, v62\nv_add_f32 v61, v61, v63\nv_cvt_pk_bf16_f32 v62, v56, v60\nv_cvt_pk_bf16_f32 v63, v57, v61\nds_write2_b32 v6, v62, v63 offset0:36 offset1:40\nv_mov_b32 v61, s54\ns_waitcnt lgkmcnt(0)\n" \
"s_barrier\nds_read_b128 v[80:83], v8 offset:0\nds_read_b128 v[84:87], v8 offset:576\nds_read_b128 v[88:91], v8 offset:1152\nds_read_b128 v[92:95], v8 offset:1728\nds_read_b128 v[64:67], v9 offset:0\nds_read_b128 v[68:71], v9 offset:576\nds_read_b128 v[72:75], v9 offset:1152\n" \
"ds_read_b128 v[76:79], v9 offset:1728\nds_read_b32 v60, v0 offset:53760\ns_waitcnt lgkmcnt(8)\nv_mfma_f32_16x16x32_bf16 v[52:55], v[16:19], v[80:83], 0\nv_mfma_f32_16x16x32_bf16 v[56:59], v[20:23], v[80:83], 0\ns_waitcnt lgkmcnt(7)\nv_mfma_f32_16x16x32_bf16 v[52:55], v[24:27], v[84:87], v[52:55]\nv_mfma_f32_16x16x32_bf16 v[56:59], v[28:31], v[84:87], v[56:59]\n" \
"s_waitcnt lgkmcnt(6)\nv_mfma_f32_16x16x32_bf16 v[52:55], v[32:35], v[88:91], v[52:55]\nv_mfma_f32_16x16x32_bf16 v[56:59], v[36:39], v[88:91], v[56:59]\ns_waitcnt lgkmcnt(5)\nv_mfma_f32_16x16x32_bf16 v[52:55], v[40:43], v[92:95], v[52:55]\nv_mfma_f32_16x16x32_bf16 v[56:59], v[44:47], v[92:95], v[56:59]\ns_waitcnt lgkmcnt(4)\nv_mfma_f32_16x16x32_bf16 v[48:51], v[64:67], v[80:83], 0\n" \
"s_waitcnt lgkmcnt(3)\nv_mfma_f32_16x16x32_bf16 v[48:51], v[68:71], v[84:87], v[48:51]\ns_waitcnt lgkmcnt(2)\nds_read_b128 v[80:83], v2 offset:0\nv_mfma_f32_16x16x32_bf16 v[48:51], v[72:75], v[88:91], v[48:51]\ns_waitcnt lgkmcnt(2)\nds_read_b128 v[84:87], v2 offset:256\nv_mfma_f32_16x16x32_bf16 v[48:51], v[76:79], v[92:95], v[48:51]\n" \
"ds_read_b128 v[88:91], v2 offset:2048\nv_max3_f32 v62, v52, v53, v54\nv_max3_f32 v63, v55, v56, v57\nv_max3_f32 v62, v62, v58, v59\nv_cndmask_b32 v52, v52, v56, s[62:63]\nv_cndmask_b32 v53, v53, v57, s[62:63]\nv_cndmask_b32 v54, v54, v58, s[62:63]\nv_cndmask_b32 v55, v55, v59, s[62:63]\n" \
"v_max_f32 v62, v62, v63\nv_max3_f32 v63, v62, v48, v49\nv_max3_f32 v62, v62, v48, v49\ns_waitcnt lgkmcnt(2)\nv_mfma_f32_16x16x32_bf16 v[16:19], a[0:3], v[80:83], 0\nds_read_b128 v[92:95], v2 offset:2304\nv_permlane16_swap_b32 v62, v63\nv_mfma_f32_16x16x32_bf16 v[20:23], a[8:11], v[80:83], 0\n" \
"ds_read_b128 v[72:75], v1 offset:0\nv_max_f32 v50, v62, v63\nv_max_f32 v62, v62, v63\nv_mfma_f32_16x16x32_bf16 v[24:27], a[16:19], v[80:83], 0\ns_nop 0\nv_permlane32_swap_b32 v62, v50\nv_max_f32 v62, v62, v50\nv_mfma_f32_16x16x32_bf16 v[28:31], a[24:27], v[80:83], 0\n" \
"s_waitcnt lgkmcnt(3)\nv_mul_f32 v62, s59, v62\nv_fma_f32 v52, v52, s58, v62\nv_fma_f32 v53, v53, s58, v62\nv_mfma_f32_16x16x32_bf16 v[32:35], a[0:3], v[84:87], 0\nds_read_b128 v[80:83], v2 offset:4096\nv_exp_f32 v52, v52\nv_mfma_f32_16x16x32_bf16 v[36:39], a[8:11], v[84:87], 0\n" \
"v_fma_f32 v54, v54, s58, v62\nv_exp_f32 v53, v53\nv_mfma_f32_16x16x32_bf16 v[40:43], a[16:19], v[84:87], 0\nv_fma_f32 v55, v55, s58, v62\nv_exp_f32 v54, v54\nv_mfma_f32_16x16x32_bf16 v[44:47], a[24:27], v[84:87], 0\ns_waitcnt lgkmcnt(3)\nv_add_f32 v63, v52, v53\n" \
"v_fma_f32 v48, v48, s58, v62\nv_add_f32 v63, v63, v54\nv_mfma_f32_16x16x32_bf16 v[16:19], a[4:7], v[88:91], v[16:19]\nds_read_b128 v[84:87], v2 offset:4352\nv_exp_f32 v55, v55\nv_mfma_f32_16x16x32_bf16 v[20:23], a[12:15], v[88:91], v[20:23]\nds_read_b128 v[76:79], v1 offset:1024\nv_fma_f32 v49, v49, s58, v62\n" \
"v_add_f32 v63, v63, v55\nv_mfma_f32_16x16x32_bf16 v[24:27], a[20:23], v[88:91], v[24:27]\nv_exp_f32 v48, v48\nv_mfma_f32_16x16x32_bf16 v[28:31], a[28:31], v[88:91], v[28:31]\ns_waitcnt lgkmcnt(4)\nv_exp_f32 v49, v49\nv_add_f32 v50, v48, v63\nv_mfma_f32_16x16x32_bf16 v[32:35], a[4:7], v[92:95], v[32:35]\n" \
"ds_read_b128 v[88:91], v2 offset:6144\nv_add_f32 v50, v50, v49\nv_mfma_f32_16x16x32_bf16 v[36:39], a[12:15], v[92:95], v[36:39]\ns_nop 0\nv_add_f32_dpp v62, v63, v50 row_ror:8 row_mask:0xf bank_mask:0xf\nv_add_f32_dpp v50, v63, v50 row_ror:8 row_mask:0xf bank_mask:0xf\nv_mfma_f32_16x16x32_bf16 v[40:43], a[20:23], v[92:95], v[40:43]\ns_nop 0\n" \
"v_permlane16_swap_b32 v62, v50\nv_add_f32 v63, v62, v50\nv_mfma_f32_16x16x32_bf16 v[44:47], a[28:31], v[92:95], v[44:47]\ns_waitcnt lgkmcnt(3)\nv_add_f32 v50, v62, v50\nv_mfma_f32_16x16x32_bf16 v[16:19], a[32:35], v[80:83], v[16:19]\nds_read_b128 v[92:95], v2 offset:6400\nv_permlane32_swap_b32 v63, v50\n" \
"v_mfma_f32_16x16x32_bf16 v[20:23], v[72:75], v[80:83], v[20:23]\nds_read_b128 v[64:67], v1 offset:2048\nv_add_f32 v63, v63, v50\nv_mfma_f32_16x16x32_bf16 v[24:27], a[40:43], v[80:83], v[24:27]\nv_rcp_f32 v63, v63\nv_mfma_f32_16x16x32_bf16 v[28:31], a[48:51], v[80:83], v[28:31]\ns_waitcnt lgkmcnt(4)\nv_mul_f32 v52, v52, v63\n" \
"v_mul_f32 v53, v53, v63\nv_mul_f32 v54, v54, v63\nv_mfma_f32_16x16x32_bf16 v[32:35], a[32:35], v[84:87], v[32:35]\nds_read_b128 v[80:83], v2 offset:8192\nv_mul_f32 v55, v55, v63\nv_mul_f32 v48, v48, v63\nv_mfma_f32_16x16x32_bf16 v[36:39], v[72:75], v[84:87], v[36:39]\nv_mul_f32 v49, v49, v63\n" \
"v_cvt_pk_bf16_f32 v52, v52, v53\nv_cvt_pk_bf16_f32 v53, v54, v55\nv_mfma_f32_16x16x32_bf16 v[40:43], a[40:43], v[84:87], v[40:43]\nv_cvt_pk_bf16_f32 v48, v48, v49\nv_bfi_b32 v52, s61, v52, v61\nv_bfi_b32 v53, s61, v53, v61\nv_mfma_f32_16x16x32_bf16 v[44:47], a[48:51], v[84:87], v[44:47]\ns_waitcnt lgkmcnt(3)\n" \
"v_bfi_b32 v48, s61, v48, v61\nv_mov_b32_dpp v54, v52 row_ror:8 row_mask:0xf bank_mask:0xf\nv_mov_b32_dpp v55, v53 row_ror:8 row_mask:0xf bank_mask:0xf\nv_mfma_f32_16x16x32_bf16 v[16:19], a[36:39], v[88:91], v[16:19]\nds_read_b128 v[84:87], v2 offset:8448\nv_mfma_f32_16x16x32_bf16 v[20:23], v[76:79], v[88:91], v[20:23]\nds_read_b128 v[68:71], v1 offset:3072\nv_mfma_f32_16x16x32_bf16 v[24:27], a[44:47], v[88:91], v[24:27]\n" \
"buffer_store_dwordx4 v[52:55], v10, s[36:39], 0 offen sc1\nv_mov_b32 v49, v48\nv_mfma_f32_16x16x32_bf16 v[28:31], a[52:55], v[88:91], v[28:31]\ns_waitcnt lgkmcnt(4)\nv_permlane32_swap_b32 v48, v49\nv_mfma_f32_16x16x32_bf16 v[32:35], a[36:39], v[92:95], v[32:35]\nds_read_b128 v[88:91], v2 offset:10240\nv_mfma_f32_16x16x32_bf16 v[36:39], v[76:79], v[92:95], v[36:39]\n" \
"s_memrealtime s[86:87]\nbuffer_store_dwordx2 v[48:49], v11, s[36:39], 0 offen sc1\nv_cmp_ne_u32 vcc, 0, v60\nv_mfma_f32_16x16x32_bf16 v[40:43], a[44:47], v[92:95], v[40:43]\nv_mfma_f32_16x16x32_bf16 v[44:47], a[52:55], v[92:95], v[44:47]\ns_waitcnt lgkmcnt(3)\nv_mfma_f32_16x16x32_bf16 v[16:19], a[56:59], v[80:83], v[16:19]\nds_read_b128 v[92:95], v2 offset:10496\n" \
"v_mfma_f32_16x16x32_bf16 v[20:23], a[64:67], v[80:83], v[20:23]\nds_read_b128 v[72:75], v1 offset:4096\nv_mfma_f32_16x16x32_bf16 v[24:27], v[64:67], v[80:83], v[24:27]\nv_mfma_f32_16x16x32_bf16 v[28:31], a[72:75], v[80:83], v[28:31]\ns_waitcnt lgkmcnt(4)\nv_mfma_f32_16x16x32_bf16 v[32:35], a[56:59], v[84:87], v[32:35]\nds_read_b128 v[80:83], v2 offset:12288\nv_mfma_f32_16x16x32_bf16 v[36:39], a[64:67], v[84:87], v[36:39]\n" \
"v_mfma_f32_16x16x32_bf16 v[40:43], v[64:67], v[84:87], v[40:43]\nv_mfma_f32_16x16x32_bf16 v[44:47], a[72:75], v[84:87], v[44:47]\ns_waitcnt lgkmcnt(3)\nv_mfma_f32_16x16x32_bf16 v[16:19], a[60:63], v[88:91], v[16:19]\nds_read_b128 v[84:87], v2 offset:12544\nv_mfma_f32_16x16x32_bf16 v[20:23], a[68:71], v[88:91], v[20:23]\nds_read_b128 v[76:79], v1 offset:5120\nv_mfma_f32_16x16x32_bf16 v[24:27], v[68:71], v[88:91], v[24:27]\n" \
"v_mfma_f32_16x16x32_bf16 v[28:31], a[76:79], v[88:91], v[28:31]\ns_waitcnt lgkmcnt(4)\nv_mfma_f32_16x16x32_bf16 v[32:35], a[60:63], v[92:95], v[32:35]\nds_read_b128 v[88:91], v2 offset:14336\nv_mfma_f32_16x16x32_bf16 v[36:39], a[68:71], v[92:95], v[36:39]\nv_mfma_f32_16x16x32_bf16 v[40:43], v[68:71], v[92:95], v[40:43]\nv_mfma_f32_16x16x32_bf16 v[44:47], a[76:79], v[92:95], v[44:47]\ns_cbranch_vccnz .Lqk_pj%=\n" \
"s_cmp_lt_i32 s40, 63\ns_waitcnt lgkmcnt(0)\ns_cbranch_scc1 .Lqk_pwait%=\ns_branch .Lqk_end%=\n.Lqk_re0%=:\nv_max_u32 v17, v80, v82\nv_cmp_eq_u32 vcc, -1, v17\ns_cbranch_vccz .Lqk_sk1617%=\n" \
"s_mov_b64 exec, vcc\nbuffer_load_dwordx4 v[80:83], v3, s[36:39], s42 offen offset:0 sc1\ns_mov_b64 exec, -1\n.Lqk_sk1617%=:\nv_max_u32 v17, v84, v86\nv_cmp_eq_u32 vcc, -1, v17\ns_cbranch_vccz .Lqk_sk1624%=\ns_mov_b64 exec, vcc\n" \
"buffer_load_dwordx4 v[84:87], v3, s[36:39], s42 offen offset:1024 sc1\ns_mov_b64 exec, -1\n.Lqk_sk1624%=:\nv_max_u32 v17, v88, v90\nv_cmp_eq_u32 vcc, -1, v17\ns_cbranch_vccz .Lqk_sk1631%=\ns_mov_b64 exec, vcc\nbuffer_load_dwordx4 v[88:91], v3, s[36:39], s42 offen offset:2048 sc1\n" \
"s_mov_b64 exec, -1\n.Lqk_sk1631%=:\nv_max_u32 v17, v92, v94\nv_cmp_eq_u32 vcc, -1, v17\ns_cbranch_vccz .Lqk_sk1638%=\ns_mov_b64 exec, vcc\nbuffer_load_dwordx4 v[92:95], v3, s[36:39], s42 offen offset:3072 sc1\ns_mov_b64 exec, -1\n" \
".Lqk_sk1638%=:\nv_mov_b64 v[48:49], 0\nv_mov_b64 v[50:51], 0\nv_mov_b64 v[52:53], 0\nv_mov_b64 v[54:55], 0\nv_mov_b64 v[56:57], 0\nv_mov_b64 v[58:59], 0\nv_mov_b64 v[60:61], 0\n" \
"v_mov_b64 v[62:63], 0\ns_add_u32 s43, s43, 1\ns_cmp_lt_u32 s43, 262144\ns_cbranch_scc1 .Lqk_poll0%=\ns_branch .Lqk_abort%=\n.p2align 6\n.Lqk_c1%=:\ns_waitcnt lgkmcnt(3)\n" \
"v_mfma_f32_16x16x32_bf16 v[32:35], v[232:235], v[56:59], v[32:35]\nv_mov_b64 v[52:53], 0\nv_mov_b64 v[54:55], 0\nv_mfma_f32_16x16x32_bf16 v[36:39], v[240:243], v[56:59], v[36:39]\ns_waitcnt lgkmcnt(2)\nds_read_b128 v[64:67], v1 offset:6144\nv_mfma_f32_16x16x32_bf16 v[32:35], v[236:239], v[60:63], v[32:35]\nds_read_b128 v[68:71], v1 offset:7168\n" \
"v_mfma_f32_16x16x32_bf16 v[36:39], v[244:247], v[60:63], v[36:39]\ns_bfe_i32 s54, s40, 0x10001\nv_mfma_f32_16x16x32_bf16 v[40:43], v[248:251], v[56:59], v[40:43]\nv_mfma_f32_16x16x32_bf16 v[44:47], v[72:75], v[56:59], v[44:47]\nv_cvt_pk_bf16_f32 v36, v32, v36\nv_cvt_pk_bf16_f32 v37, v33, v37\nv_mfma_f32_16x16x32_bf16 v[40:43], v[252:255], v[60:63], v[40:43]\nv_cvt_pk_bf16_f32 v38, v34, v38\n" \
"v_cvt_pk_bf16_f32 v39, v35, v39\nv_mfma_f32_16x16x32_bf16 v[44:47], v[76:79], v[60:63], v[44:47]\nds_read_b128 v[72:75], v1 offset:8192\nds_read_b128 v[76:79], v1 offset:9216\nv_mov_b64 v[56:57], 0\nv_mov_b64 v[58:59], 0\nv_mov_b64 v[60:61], 0\nv_mov_b64 v[62:63], 0\n" \
".Lqk_poll1%=:\ns_waitcnt vmcnt(0)\nv_smfmac_f32_16x16x64_bf16 v[48:51], v[80:83], a[104:111], v4\nds_write_b128 v0, v[80:83] offset:20480\nv_max3_u32 v16, v80, v82, v84\nv_smfmac_f32_16x16x64_bf16 v[52:55], v[80:83], a[112:119], v4\nv_max3_u32 v16, v16, v86, v88\nv_smfmac_f32_16x16x64_bf16 v[56:59], v[80:83], a[120:127], v4\n" \
"ds_write_b128 v0, v[84:87] offset:21504\nv_max3_u32 v16, v16, v90, v92\nv_max_u32 v16, v16, v94\nv_smfmac_f32_16x16x64_bf16 v[60:63], v[80:83], a[128:135], v4\nv_cmp_eq_u32 vcc, -1, v16\nv_smfmac_f32_16x16x64_bf16 v[48:51], v[84:87], a[136:143], v4\nds_write_b128 v0, v[88:91] offset:22528\ns_cbranch_vccnz .Lqk_re1%=\n" \
"s_waitcnt lgkmcnt(5)\nv_smfmac_f32_16x16x64_bf16 v[52:55], v[84:87], v[64:71], v4\nv_smfmac_f32_16x16x64_bf16 v[56:59], v[84:87], a[144:151], v4\nds_write_b128 v0, v[92:95] offset:23552\nv_smfmac_f32_16x16x64_bf16 v[60:63], v[84:87], a[152:159], v4\nv_smfmac_f32_16x16x64_bf16 v[48:51], v[88:91], a[160:167], v4\nv_smfmac_f32_16x16x64_bf16 v[52:55], v[88:91], a[168:175], v4\ns_waitcnt lgkmcnt(0)\n" \
".Lqk_jn1%=:\ns_barrier\nds_read_b128 v[80:83], v0 offset:24576\nds_read_b128 v[64:67], v1 offset:10240\nds_read_b128 v[68:71], v1 offset:11264\nv_smfmac_f32_16x16x64_bf16 v[56:59], v[88:91], v[72:79], v4\nds_read_b128 v[72:75], v1 offset:12288\nds_read_b128 v[76:79], v1 offset:13312\n" \
"v_smfmac_f32_16x16x64_bf16 v[60:63], v[88:91], a[176:183], v4\nds_read_b128 v[84:87], v0 offset:25600\nds_read_b128 v[88:91], v0 offset:26624\nv_smfmac_f32_16x16x64_bf16 v[48:51], v[92:95], a[184:191], v4\nds_write_b128 v5, v[36:39] offset:256\nv_cvt_pk_bf16_f32 v44, v40, v44\nv_cvt_pk_bf16_f32 v45, v41, v45\nv_cvt_pk_bf16_f32 v46, v42, v46\n" \
"v_cvt_pk_bf16_f32 v47, v43, v47\nv_smfmac_f32_16x16x64_bf16 v[52:55], v[92:95], a[192:199], v4\nv_smfmac_f32_16x16x64_bf16 v[56:59], v[92:95], a[200:207], v4\ns_waitcnt lgkmcnt(5)\nv_smfmac_f32_16x16x64_bf16 v[60:63], v[92:95], v[64:71], v4\nds_read_b128 v[92:95], v0 offset:27648\nds_read_b128 v[64:67], v1 offset:14336\nds_read_b128 v[68:71], v1 offset:15360\n" \
"v_smfmac_f32_16x16x64_bf16 v[48:51], v[80:83], a[208:215], v4\nds_write_b128 v5, v[44:47] offset:768\nv_smfmac_f32_16x16x64_bf16 v[52:55], v[80:83], a[216:223], v4\nv_smfmac_f32_16x16x64_bf16 v[56:59], v[80:83], a[224:231], v4\nv_smfmac_f32_16x16x64_bf16 v[60:63], v[80:83], a[232:239], v4\ns_waitcnt lgkmcnt(6)\nds_read_b128 v[80:83], v0 offset:28672\nv_smfmac_f32_16x16x64_bf16 v[48:51], v[84:87], a[240:247], v4\n" \
"v_smfmac_f32_16x16x64_bf16 v[52:55], v[84:87], v[72:79], v4\nds_read_b128 v[72:75], v1 offset:16384\nds_read_b128 v[76:79], v1 offset:17408\nv_smfmac_f32_16x16x64_bf16 v[56:59], v[84:87], a[248:255], v4\nv_smfmac_f32_16x16x64_bf16 v[60:63], v[84:87], v[96:103], v4\ns_waitcnt lgkmcnt(8)\nds_read_b128 v[84:87], v0 offset:29696\nv_smfmac_f32_16x16x64_bf16 v[48:51], v[88:91], v[104:111], v4\n" \
"s_waitcnt lgkmcnt(4)\ns_barrier\nv_smfmac_f32_16x16x64_bf16 v[52:55], v[88:91], v[112:119], v4\nv_smfmac_f32_16x16x64_bf16 v[56:59], v[88:91], v[64:71], v4\nds_read_b128 v[64:67], v1 offset:18432\nds_read_b128 v[68:71], v1 offset:19456\nv_smfmac_f32_16x16x64_bf16 v[60:63], v[88:91], v[120:127], v4\nds_read_b128 v[88:91], v0 offset:30720\n" \
"v_smfmac_f32_16x16x64_bf16 v[48:51], v[92:95], v[128:135], v4\nds_read_b128 v[16:19], v7 offset:0\nv_smfmac_f32_16x16x64_bf16 v[52:55], v[92:95], v[136:143], v4\nv_smfmac_f32_16x16x64_bf16 v[56:59], v[92:95], v[144:151], v4\ns_waitcnt lgkmcnt(5)\nds_read_b128 v[20:23], v7 offset:2048\nv_smfmac_f32_16x16x64_bf16 v[60:63], v[92:95], v[72:79], v4\nds_read_b128 v[92:95], v0 offset:31744\n" \
"ds_read_b128 v[72:75], v1 offset:20480\nds_read_b128 v[76:79], v1 offset:21504\nv_smfmac_f32_16x16x64_bf16 v[48:51], v[80:83], v[152:159], v4\nds_read_b128 v[24:27], v7 offset:4096\nv_smfmac_f32_16x16x64_bf16 v[52:55], v[80:83], v[160:167], v4\nv_smfmac_f32_16x16x64_bf16 v[56:59], v[80:83], v[168:175], v4\nds_read_b128 v[28:31], v7 offset:6144\nv_smfmac_f32_16x16x64_bf16 v[60:63], v[80:83], v[176:183], v4\n" \
"s_waitcnt lgkmcnt(10)\nds_read_b128 v[80:83], v0 offset:16384\nv_smfmac_f32_16x16x64_bf16 v[48:51], v[84:87], v[184:191], v4\ns_waitcnt lgkmcnt(9)\nds_read_b128 v[32:35], v7 offset:8192\nv_smfmac_f32_16x16x64_bf16 v[52:55], v[84:87], v[64:71], v4\nds_read_b128 v[64:67], v1 offset:22528\nds_read_b128 v[68:71], v1 offset:23552\n" \
"v_smfmac_f32_16x16x64_bf16 v[56:59], v[84:87], v[192:199], v4\nds_read_b128 v[36:39], v7 offset:10240\nv_smfmac_f32_16x16x64_bf16 v[60:63], v[84:87], v[200:207], v4\ns_waitcnt lgkmcnt(12)\nds_read_b128 v[84:87], v0 offset:17408\nv_smfmac_f32_16x16x64_bf16 v[48:51], v[88:91], v[208:215], v4\nds_read_b128 v[40:43], v7 offset:12288\nv_smfmac_f32_16x16x64_bf16 v[52:55], v[88:91], v[216:223], v4\n" \
"s_waitcnt lgkmcnt(9)\nv_smfmac_f32_16x16x64_bf16 v[56:59], v[88:91], v[72:79], v4\nds_read_b128 v[72:75], v1 offset:0\nds_read_b128 v[76:79], v1 offset:1024\nv_smfmac_f32_16x16x64_bf16 v[60:63], v[88:91], v[224:231], v4\nds_read_b128 v[88:91], v0 offset:18432\nv_smfmac_f32_16x16x64_bf16 v[48:51], v[92:95], v[232:239], v4\nds_read_b128 v[44:47], v7 offset:14336\n" \
"v_smfmac_f32_16x16x64_bf16 v[52:55], v[92:95], v[240:247], v4\nv_smfmac_f32_16x16x64_bf16 v[56:59], v[92:95], v[248:255], v4\ns_waitcnt lgkmcnt(7)\nv_smfmac_f32_16x16x64_bf16 v[60:63], v[92:95], v[64:71], v4\nds_read_b128 v[92:95], v0 offset:19456\nds_read_b128 v[64:67], v1 offset:2048\nds_read_b128 v[68:71], v1 offset:3072\nv_smfmac_f32_16x16x64_bf16 v[48:51], v[80:83], a[0:7], v4\n" \
"v_smfmac_f32_16x16x64_bf16 v[52:55], v[80:83], a[8:15], v4\nv_smfmac_f32_16x16x64_bf16 v[56:59], v[80:83], a[16:23], v4\nv_smfmac_f32_16x16x64_bf16 v[60:63], v[80:83], a[24:31], v4\ns_waitcnt lgkmcnt(8)\nv_smfmac_f32_16x16x64_bf16 v[48:51], v[84:87], a[32:39], v4\ns_waitcnt lgkmcnt(5)\nv_smfmac_f32_16x16x64_bf16 v[52:55], v[84:87], v[72:79], v4\nds_read_b128 v[72:75], v1 offset:4096\n" \
"ds_read_b128 v[76:79], v1 offset:5120\nv_smfmac_f32_16x16x64_bf16 v[56:59], v[84:87], a[40:47], v4\nv_smfmac_f32_16x16x64_bf16 v[60:63], v[84:87], a[48:55], v4\ns_waitcnt lgkmcnt(6)\nv_smfmac_f32_16x16x64_bf16 v[48:51], v[88:91], a[56:63], v4\nv_smfmac_f32_16x16x64_bf16 v[52:55], v[88:91], a[64:71], v4\ns_waitcnt lgkmcnt(4)\nv_smfmac_f32_16x16x64_bf16 v[48:51], v[92:95], a[80:87], v4\n" \
"v_smfmac_f32_16x16x64_bf16 v[52:55], v[92:95], a[88:95], v4\ns_waitcnt lgkmcnt(2)\nv_smfmac_f32_16x16x64_bf16 v[56:59], v[88:91], v[64:71], v4\nv_add_f32 v48, v48, v50\nv_smfmac_f32_16x16x64_bf16 v[60:63], v[88:91], a[72:79], v4\nv_add_f32 v52, v52, v54\nv_add_f32 v49, v49, v51\nv_smfmac_f32_16x16x64_bf16 v[56:59], v[92:95], a[96:103], v4\n" \
"s_waitcnt lgkmcnt(0)\nv_add_f32 v53, v53, v55\nv_cvt_pk_bf16_f32 v50, v48, v52\nv_smfmac_f32_16x16x64_bf16 v[60:63], v[92:95], v[72:79], v4\nv_cvt_pk_bf16_f32 v51, v49, v53\nds_write2_b32 v6, v50, v51 offset0:0 offset1:4\ns_nop 1\nv_add_f32 v56, v56, v58\n" \
"v_add_f32 v57, v57, v59\ns_nop 1\nv_add_f32 v60, v60, v62\nv_add_f32 v61, v61, v63\nv_cvt_pk_bf16_f32 v62, v56, v60\nv_cvt_pk_bf16_f32 v63, v57, v61\nds_write2_b32 v6, v62, v63 offset0:36 offset1:40\nv_mov_b32 v61, s54\n" \
"v_mfma_f32_16x16x32_bf16 v[52:55], v[32:35], v[16:19], v[12:15]\nv_mfma_f32_16x16x32_bf16 v[52:55], v[36:39], v[20:23], v[52:55]\nv_mfma_f32_16x16x32_bf16 v[52:55], v[40:43], v[24:27], v[52:55]\nv_mfma_f32_16x16x32_bf16 v[52:55], v[44:47], v[28:31], v[52:55]\ns_waitcnt lgkmcnt(0)\ns_barrier\ns_nop 10\nds_read_b128 v[32:35], v9 offset:0\n" \
"ds_read_b128 v[36:39], v9 offset:576\nds_read_b128 v[40:43], v9 offset:1152\nds_read_b128 v[44:47], v9 offset:1728\nds_read_b32 v60, v0 offset:53760\nds_read_b128 v[80:83], v2 offset:0\nds_read_b128 v[84:87], v2 offset:256\nds_read_b128 v[88:91], v2 offset:2048\ns_waitcnt lgkmcnt(7)\n" \
"v_mfma_f32_16x16x32_bf16 v[48:51], v[32:35], v[16:19], 0\ns_waitcnt lgkmcnt(6)\nv_mfma_f32_16x16x32_bf16 v[48:51], v[36:39], v[20:23], v[48:51]\ns_waitcnt lgkmcnt(5)\nv_mfma_f32_16x16x32_bf16 v[48:51], v[40:43], v[24:27], v[48:51]\ns_waitcnt lgkmcnt(4)\nv_mfma_f32_16x16x32_bf16 v[48:51], v[44:47], v[28:31], v[48:51]\ns_waitcnt lgkmcnt(2)\n" \
"v_mfma_f32_16x16x32_bf16 v[16:19], a[0:3], v[80:83], 0\nds_read_b128 v[92:95], v2 offset:2304\nv_max3_f32 v62, v52, v53, v54\nv_max_f32 v62, v62, v55\nv_mfma_f32_16x16x32_bf16 v[20:23], a[8:11], v[80:83], 0\nds_read_b128 v[72:75], v1 offset:0\nv_max3_f32 v63, v62, v48, v49\nv_max3_f32 v62, v62, v48, v49\n" \
"v_mfma_f32_16x16x32_bf16 v[24:27], a[16:19], v[80:83], 0\ns_nop 0\nv_permlane16_swap_b32 v62, v63\nv_max_f32 v50, v62, v63\nv_mfma_f32_16x16x32_bf16 v[28:31], a[24:27], v[80:83], 0\ns_waitcnt lgkmcnt(3)\nv_max_f32 v62, v62, v63\nv_mfma_f32_16x16x32_bf16 v[32:35], a[0:3], v[84:87], 0\n" \
"ds_read_b128 v[80:83], v2 offset:4096\nv_permlane32_swap_b32 v62, v50\nv_mfma_f32_16x16x32_bf16 v[36:39], a[8:11], v[84:87], 0\nv_max_f32 v62, v62, v50\nv_mul_f32 v62, s59, v62\nv_fma_f32 v52, v52, s58, v62\nv_mfma_f32_16x16x32_bf16 v[40:43], a[16:19], v[84:87], 0\nv_fma_f32 v53, v53, s58, v62\n" \
"v_exp_f32 v52, v52\nv_mfma_f32_16x16x32_bf16 v[44:47], a[24:27], v[84:87], 0\ns_waitcnt lgkmcnt(3)\nv_fma_f32 v54, v54, s58, v62\nv_exp_f32 v53, v53\nv_mfma_f32_16x16x32_bf16 v[16:19], a[4:7], v[88:91], v[16:19]\nds_read_b128 v[84:87], v2 offset:4352\nv_fma_f32 v55, v55, s58, v62\n" \
"v_add_f32 v63, v52, v53\nv_mfma_f32_16x16x32_bf16 v[20:23], a[12:15], v[88:91], v[20:23]\nds_read_b128 v[76:79], v1 offset:1024\nv_exp_f32 v54, v54\nv_mfma_f32_16x16x32_bf16 v[24:27], a[20:23], v[88:91], v[24:27]\nv_fma_f32 v48, v48, s58, v62\nv_exp_f32 v55, v55\nv_mfma_f32_16x16x32_bf16 v[28:31], a[28:31], v[88:91], v[28:31]\n" \
"s_waitcnt lgkmcnt(4)\nv_add_f32 v63, v63, v54\nv_fma_f32 v49, v49, s58, v62\nv_add_f32 v63, v63, v55\nv_mfma_f32_16x16x32_bf16 v[32:35], a[4:7], v[92:95], v[32:35]\nds_read_b128 v[88:91], v2 offset:6144\nv_exp_f32 v48, v48\nv_mfma_f32_16x16x32_bf16 v[36:39], a[12:15], v[92:95], v[36:39]\n" \
"v_exp_f32 v49, v49\nv_add_f32 v63, v63, v48\nv_mfma_f32_16x16x32_bf16 v[40:43], a[20:23], v[92:95], v[40:43]\nv_add_f32 v62, v63, v49\nv_add_f32 v50, v63, v49\nv_mfma_f32_16x16x32_bf16 v[44:47], a[28:31], v[92:95], v[44:47]\ns_waitcnt lgkmcnt(3)\nv_permlane16_swap_b32 v62, v50\n" \
"v_add_f32 v63, v62, v50\nv_mfma_f32_16x16x32_bf16 v[16:19], a[32:35], v[80:83], v[16:19]\nds_read_b128 v[92:95], v2 offset:6400\nv_add_f32 v50, v62, v50\nv_mfma_f32_16x16x32_bf16 v[20:23], v[72:75], v[80:83], v[20:23]\nds_read_b128 v[64:67], v1 offset:2048\nv_permlane32_swap_b32 v63, v50\nv_mfma_f32_16x16x32_bf16 v[24:27], a[40:43], v[80:83], v[24:27]\n" \
"v_add_f32 v63, v63, v50\nv_rcp_f32 v63, v63\nv_mfma_f32_16x16x32_bf16 v[28:31], a[48:51], v[80:83], v[28:31]\ns_waitcnt lgkmcnt(4)\nv_mul_f32 v52, v52, v63\nv_mul_f32 v53, v53, v63\nv_mul_f32 v54, v54, v63\nv_mfma_f32_16x16x32_bf16 v[32:35], a[32:35], v[84:87], v[32:35]\n" \
"ds_read_b128 v[80:83], v2 offset:8192\nv_mul_f32 v55, v55, v63\nv_mul_f32 v48, v48, v63\nv_mfma_f32_16x16x32_bf16 v[36:39], v[72:75], v[84:87], v[36:39]\nv_mul_f32 v49, v49, v63\nv_cvt_pk_bf16_f32 v52, v52, v53\nv_cvt_pk_bf16_f32 v53, v54, v55\nv_mfma_f32_16x16x32_bf16 v[40:43], a[40:43], v[84:87], v[40:43]\n" \
"v_cvt_pk_bf16_f32 v48, v48, v49\nv_bfi_b32 v52, s61, v52, v61\nv_bfi_b32 v53, s61, v53, v61\nv_mfma_f32_16x16x32_bf16 v[44:47], a[48:51], v[84:87], v[44:47]\ns_waitcnt lgkmcnt(3)\nv_bfi_b32 v48, s61, v48, v61\nv_mfma_f32_16x16x32_bf16 v[16:19], a[36:39], v[88:91], v[16:19]\nds_read_b128 v[84:87], v2 offset:8448\n" \
"v_mfma_f32_16x16x32_bf16 v[20:23], v[76:79], v[88:91], v[20:23]\nds_read_b128 v[68:71], v1 offset:3072\nv_mfma_f32_16x16x32_bf16 v[24:27], a[44:47], v[88:91], v[24:27]\nbuffer_store_dwordx2 v[52:53], v10, s[36:39], s55 offen sc1\nv_mov_b32 v49, v48\nv_mfma_f32_16x16x32_bf16 v[28:31], a[52:55], v[88:91], v[28:31]\ns_waitcnt lgkmcnt(4)\nv_permlane16_swap_b32 v48, v49\n" \
"v_mov_b32 v50, v48\nv_mfma_f32_16x16x32_bf16 v[32:35], a[36:39], v[92:95], v[32:35]\nds_read_b128 v[88:91], v2 offset:10240\nv_mov_b32 v51, v49\nv_mfma_f32_16x16x32_bf16 v[36:39], v[76:79], v[92:95], v[36:39]\nv_permlane32_swap_b32 v48, v50\nv_mfma_f32_16x16x32_bf16 v[40:43], a[44:47], v[92:95], v[40:43]\nv_permlane32_swap_b32 v49, v51\n" \
"v_mfma_f32_16x16x32_bf16 v[44:47], a[52:55], v[92:95], v[44:47]\ns_waitcnt lgkmcnt(3)\nbuffer_store_dwordx4 v[48:51], v11, s[36:39], s55 offen sc1\nv_cmp_ne_u32 vcc, 0, v60\nv_mfma_f32_16x16x32_bf16 v[16:19], a[56:59], v[80:83], v[16:19]\nds_read_b128 v[92:95], v2 offset:10496\nv_mfma_f32_16x16x32_bf16 v[20:23], a[64:67], v[80:83], v[20:23]\nds_read_b128 v[72:75], v1 offset:4096\n" \
"v_mfma_f32_16x16x32_bf16 v[24:27], v[64:67], v[80:83], v[24:27]\nv_mfma_f32_16x16x32_bf16 v[28:31], a[72:75], v[80:83], v[28:31]\ns_waitcnt lgkmcnt(4)\nv_mfma_f32_16x16x32_bf16 v[32:35], a[56:59], v[84:87], v[32:35]\nds_read_b128 v[80:83], v2 offset:12288\nv_mfma_f32_16x16x32_bf16 v[36:39], a[64:67], v[84:87], v[36:39]\nv_mfma_f32_16x16x32_bf16 v[40:43], v[64:67], v[84:87], v[40:43]\nv_mfma_f32_16x16x32_bf16 v[44:47], a[72:75], v[84:87], v[44:47]\n" \
"s_waitcnt lgkmcnt(3)\nv_mfma_f32_16x16x32_bf16 v[16:19], a[60:63], v[88:91], v[16:19]\nds_read_b128 v[84:87], v2 offset:12544\nv_mfma_f32_16x16x32_bf16 v[20:23], a[68:71], v[88:91], v[20:23]\nds_read_b128 v[76:79], v1 offset:5120\nv_mfma_f32_16x16x32_bf16 v[24:27], v[68:71], v[88:91], v[24:27]\nv_mfma_f32_16x16x32_bf16 v[28:31], a[76:79], v[88:91], v[28:31]\ns_waitcnt lgkmcnt(4)\n" \
"v_mfma_f32_16x16x32_bf16 v[32:35], a[60:63], v[92:95], v[32:35]\nds_read_b128 v[88:91], v2 offset:14336\nv_mfma_f32_16x16x32_bf16 v[36:39], a[68:71], v[92:95], v[36:39]\nv_mfma_f32_16x16x32_bf16 v[40:43], v[68:71], v[92:95], v[40:43]\nv_mfma_f32_16x16x32_bf16 v[44:47], a[76:79], v[92:95], v[44:47]\ns_cbranch_vccnz .Lqk_pj%=\ns_cmp_lt_i32 s40, 63\ns_waitcnt lgkmcnt(0)\n" \
"s_cbranch_scc1 .Lqk_pwait%=\ns_branch .Lqk_end%=\n.Lqk_re1%=:\nv_max_u32 v17, v80, v82\nv_cmp_eq_u32 vcc, -1, v17\ns_cbranch_vccz .Lqk_sk2003%=\ns_mov_b64 exec, vcc\nbuffer_load_dwordx4 v[80:83], v3, s[36:39], s42 offen offset:0 sc1\n" \
"s_mov_b64 exec, -1\n.Lqk_sk2003%=:\nv_max_u32 v17, v84, v86\nv_cmp_eq_u32 vcc, -1, v17\ns_cbranch_vccz .Lqk_sk2010%=\ns_mov_b64 exec, vcc\nbuffer_load_dwordx4 v[84:87], v3, s[36:39], s42 offen offset:1024 sc1\ns_mov_b64 exec, -1\n" \
".Lqk_sk2010%=:\nv_max_u32 v17, v88, v90\nv_cmp_eq_u32 vcc, -1, v17\ns_cbranch_vccz .Lqk_sk2017%=\ns_mov_b64 exec, vcc\nbuffer_load_dwordx4 v[88:91], v3, s[36:39], s42 offen offset:2048 sc1\ns_mov_b64 exec, -1\n.Lqk_sk2017%=:\n" \
"v_max_u32 v17, v92, v94\nv_cmp_eq_u32 vcc, -1, v17\ns_cbranch_vccz .Lqk_sk2024%=\ns_mov_b64 exec, vcc\nbuffer_load_dwordx4 v[92:95], v3, s[36:39], s42 offen offset:3072 sc1\ns_mov_b64 exec, -1\n.Lqk_sk2024%=:\nv_mov_b64 v[48:49], 0\n" \
"v_mov_b64 v[50:51], 0\nv_mov_b64 v[52:53], 0\nv_mov_b64 v[54:55], 0\nv_mov_b64 v[56:57], 0\nv_mov_b64 v[58:59], 0\nv_mov_b64 v[60:61], 0\nv_mov_b64 v[62:63], 0\ns_add_u32 s43, s43, 1\n" \
"s_cmp_lt_u32 s43, 262144\ns_cbranch_scc1 .Lqk_poll1%=\ns_branch .Lqk_abort%=\n.p2align 6\n.Lqk_c2%=:\ns_waitcnt lgkmcnt(3)\nv_mfma_f32_16x16x32_bf16 v[32:35], v[232:235], v[56:59], v[32:35]\nv_mov_b64 v[52:53], 0\n" \
"v_mov_b64 v[54:55], 0\nv_mfma_f32_16x16x32_bf16 v[36:39], v[240:243], v[56:59], v[36:39]\ns_waitcnt lgkmcnt(2)\nds_read_b128 v[64:67], v1 offset:12288\nv_mfma_f32_16x16x32_bf16 v[32:35], v[236:239], v[60:63], v[32:35]\nds_read_b128 v[68:71], v1 offset:13312\nv_mfma_f32_16x16x32_bf16 v[36:39], v[244:247], v[60:63], v[36:39]\ns_bfe_i32 s54, s40, 0x10001\n" \
"v_mfma_f32_16x16x32_bf16 v[40:43], v[248:251], v[56:59], v[40:43]\nv_mfma_f32_16x16x32_bf16 v[44:47], v[72:75], v[56:59], v[44:47]\nv_cvt_pk_bf16_f32 v36, v32, v36\nv_cvt_pk_bf16_f32 v37, v33, v37\nv_mfma_f32_16x16x32_bf16 v[40:43], v[252:255], v[60:63], v[40:43]\nv_cvt_pk_bf16_f32 v38, v34, v38\nv_cvt_pk_bf16_f32 v39, v35, v39\nv_mfma_f32_16x16x32_bf16 v[44:47], v[76:79], v[60:63], v[44:47]\n" \
"ds_read_b128 v[72:75], v1 offset:14336\nds_read_b128 v[76:79], v1 offset:15360\nv_mov_b64 v[56:57], 0\nv_mov_b64 v[58:59], 0\nv_mov_b64 v[60:61], 0\nv_mov_b64 v[62:63], 0\n.Lqk_poll2%=:\ns_waitcnt vmcnt(0)\n" \
"v_smfmac_f32_16x16x64_bf16 v[48:51], v[80:83], a[208:215], v4\nds_write_b128 v0, v[80:83] offset:24576\nv_max3_u32 v16, v80, v82, v84\nv_smfmac_f32_16x16x64_bf16 v[52:55], v[80:83], a[216:223], v4\nv_max3_u32 v16, v16, v86, v88\nv_smfmac_f32_16x16x64_bf16 v[56:59], v[80:83], a[224:231], v4\nds_write_b128 v0, v[84:87] offset:25600\nv_max3_u32 v16, v16, v90, v92\n" \
"v_max_u32 v16, v16, v94\nv_smfmac_f32_16x16x64_bf16 v[60:63], v[80:83], a[232:239], v4\nv_cmp_eq_u32 vcc, -1, v16\nv_smfmac_f32_16x16x64_bf16 v[48:51], v[84:87], a[240:247], v4\nds_write_b128 v0, v[88:91] offset:26624\ns_cbranch_vccnz .Lqk_re2%=\ns_waitcnt lgkmcnt(5)\nv_smfmac_f32_16x16x64_bf16 v[52:55], v[84:87], v[64:71], v4\n" \
"v_smfmac_f32_16x16x64_bf16 v[56:59], v[84:87], a[248:255], v4\nds_write_b128 v0, v[92:95] offset:27648\nv_smfmac_f32_16x16x64_bf16 v[60:63], v[84:87], v[96:103], v4\nv_smfmac_f32_16x16x64_bf16 v[48:51], v[88:91], v[104:111], v4\nv_smfmac_f32_16x16x64_bf16 v[52:55], v[88:91], v[112:119], v4\ns_waitcnt lgkmcnt(0)\n.Lqk_jn2%=:\ns_barrier\n" \
"ds_read_b128 v[80:83], v0 offset:28672\nds_read_b128 v[64:67], v1 offset:16384\nds_read_b128 v[68:71], v1 offset:17408\nv_smfmac_f32_16x16x64_bf16 v[56:59], v[88:91], v[72:79], v4\nds_read_b128 v[72:75], v1 offset:18432\nds_read_b128 v[76:79], v1 offset:19456\nv_smfmac_f32_16x16x64_bf16 v[60:63], v[88:91], v[120:127], v4\nds_read_b128 v[84:87], v0 offset:29696\n" \
"ds_read_b128 v[88:91], v0 offset:30720\nv_smfmac_f32_16x16x64_bf16 v[48:51], v[92:95], v[128:135], v4\nds_write_b128 v5, v[36:39] offset:256\nv_cvt_pk_bf16_f32 v44, v40, v44\nv_cvt_pk_bf16_f32 v45, v41, v45\nv_cvt_pk_bf16_f32 v46, v42, v46\nv_cvt_pk_bf16_f32 v47, v43, v47\nv_smfmac_f32_16x16x64_bf16 v[52:55], v[92:95], v[136:143], v4\n" \
"v_smfmac_f32_16x16x64_bf16 v[56:59], v[92:95], v[144:151], v4\ns_waitcnt lgkmcnt(5)\nv_smfmac_f32_16x16x64_bf16 v[60:63], v[92:95], v[64:71], v4\nds_read_b128 v[92:95], v0 offset:31744\nds_read_b128 v[64:67], v1 offset:20480\nds_read_b128 v[68:71], v1 offset:21504\nv_smfmac_f32_16x16x64_bf16 v[48:51], v[80:83], v[152:159], v4\nds_write_b128 v5, v[44:47] offset:768\n" \
"v_smfmac_f32_16x16x64_bf16 v[52:55], v[80:83], v[160:167], v4\nv_smfmac_f32_16x16x64_bf16 v[56:59], v[80:83], v[168:175], v4\nv_smfmac_f32_16x16x64_bf16 v[60:63], v[80:83], v[176:183], v4\ns_waitcnt lgkmcnt(6)\nds_read_b128 v[80:83], v0 offset:16384\nv_smfmac_f32_16x16x64_bf16 v[48:51], v[84:87], v[184:191], v4\nv_smfmac_f32_16x16x64_bf16 v[52:55], v[84:87], v[72:79], v4\nds_read_b128 v[72:75], v1 offset:22528\n" \
"ds_read_b128 v[76:79], v1 offset:23552\nv_smfmac_f32_16x16x64_bf16 v[56:59], v[84:87], v[192:199], v4\nv_smfmac_f32_16x16x64_bf16 v[60:63], v[84:87], v[200:207], v4\ns_waitcnt lgkmcnt(8)\nds_read_b128 v[84:87], v0 offset:17408\nv_smfmac_f32_16x16x64_bf16 v[48:51], v[88:91], v[208:215], v4\ns_waitcnt lgkmcnt(4)\ns_barrier\n" \
"v_smfmac_f32_16x16x64_bf16 v[52:55], v[88:91], v[216:223], v4\nv_smfmac_f32_16x16x64_bf16 v[56:59], v[88:91], v[64:71], v4\nds_read_b128 v[64:67], v1 offset:0\nds_read_b128 v[68:71], v1 offset:1024\nv_smfmac_f32_16x16x64_bf16 v[60:63], v[88:91], v[224:231], v4\nds_read_b128 v[88:91], v0 offset:18432\nv_smfmac_f32_16x16x64_bf16 v[48:51], v[92:95], v[232:239], v4\nds_read_b128 v[16:19], v7 offset:256\n" \
"v_smfmac_f32_16x16x64_bf16 v[52:55], v[92:95], v[240:247], v4\nv_smfmac_f32_16x16x64_bf16 v[56:59], v[92:95], v[248:255], v4\ns_waitcnt lgkmcnt(5)\nds_read_b128 v[20:23], v7 offset:2304\nv_smfmac_f32_16x16x64_bf16 v[60:63], v[92:95], v[72:79], v4\nds_read_b128 v[92:95], v0 offset:19456\nds_read_b128 v[72:75], v1 offset:2048\nds_read_b128 v[76:79], v1 offset:3072\n" \
"v_smfmac_f32_16x16x64_bf16 v[48:51], v[80:83], a[0:7], v4\nds_read_b128 v[24:27], v7 offset:4352\nv_smfmac_f32_16x16x64_bf16 v[52:55], v[80:83], a[8:15], v4\nv_smfmac_f32_16x16x64_bf16 v[56:59], v[80:83], a[16:23], v4\nds_read_b128 v[28:31], v7 offset:6400\nv_smfmac_f32_16x16x64_bf16 v[60:63], v[80:83], a[24:31], v4\ns_waitcnt lgkmcnt(10)\nds_read_b128 v[80:83], v0 offset:20480\n" \
"v_smfmac_f32_16x16x64_bf16 v[48:51], v[84:87], a[32:39], v4\ns_waitcnt lgkmcnt(9)\nds_read_b128 v[32:35], v7 offset:8192\nv_smfmac_f32_16x16x64_bf16 v[52:55], v[84:87], v[64:71], v4\nds_read_b128 v[64:67], v1 offset:4096\nds_read_b128 v[68:71], v1 offset:5120\nv_smfmac_f32_16x16x64_bf16 v[56:59], v[84:87], a[40:47], v4\nds_read_b128 v[36:39], v7 offset:10240\n" \
"v_smfmac_f32_16x16x64_bf16 v[60:63], v[84:87], a[48:55], v4\ns_waitcnt lgkmcnt(12)\nds_read_b128 v[84:87], v0 offset:21504\nv_smfmac_f32_16x16x64_bf16 v[48:51], v[88:91], a[56:63], v4\nds_read_b128 v[40:43], v7 offset:12288\nv_smfmac_f32_16x16x64_bf16 v[52:55], v[88:91], a[64:71], v4\ns_waitcnt lgkmcnt(9)\nv_smfmac_f32_16x16x64_bf16 v[56:59], v[88:91], v[72:79], v4\n" \
"ds_read_b128 v[72:75], v1 offset:6144\nds_read_b128 v[76:79], v1 offset:7168\nv_smfmac_f32_16x16x64_bf16 v[60:63], v[88:91], a[72:79], v4\nds_read_b128 v[88:91], v0 offset:22528\nv_smfmac_f32_16x16x64_bf16 v[48:51], v[92:95], a[80:87], v4\nds_read_b128 v[44:47], v7 offset:14336\nv_smfmac_f32_16x16x64_bf16 v[52:55], v[92:95], a[88:95], v4\nv_smfmac_f32_16x16x64_bf16 v[56:59], v[92:95], a[96:103], v4\n" \
"s_waitcnt lgkmcnt(7)\nv_smfmac_f32_16x16x64_bf16 v[60:63], v[92:95], v[64:71], v4\nds_read_b128 v[92:95], v0 offset:23552\nds_read_b128 v[64:67], v1 offset:8192\nds_read_b128 v[68:71], v1 offset:9216\nv_smfmac_f32_16x16x64_bf16 v[48:51], v[80:83], a[104:111], v4\nv_smfmac_f32_16x16x64_bf16 v[52:55], v[80:83], a[112:119], v4\nv_smfmac_f32_16x16x64_bf16 v[56:59], v[80:83], a[120:127], v4\n" \
"v_smfmac_f32_16x16x64_bf16 v[60:63], v[80:83], a[128:135], v4\ns_waitcnt lgkmcnt(8)\nv_smfmac_f32_16x16x64_bf16 v[48:51], v[84:87], a[136:143], v4\ns_waitcnt lgkmcnt(5)\nds_read_b128 v[80:83], v7 offset:8448\nv_smfmac_f32_16x16x64_bf16 v[52:55], v[84:87], v[72:79], v4\nds_read_b128 v[72:75], v1 offset:10240\nds_read_b128 v[76:79], v1 offset:11264\n" \
"v_smfmac_f32_16x16x64_bf16 v[56:59], v[84:87], a[144:151], v4\nv_smfmac_f32_16x16x64_bf16 v[60:63], v[84:87], a[152:159], v4\ns_waitcnt lgkmcnt(7)\nv_smfmac_f32_16x16x64_bf16 v[48:51], v[88:91], a[160:167], v4\nds_read_b128 v[84:87], v7 offset:10496\nv_smfmac_f32_16x16x64_bf16 v[52:55], v[88:91], a[168:175], v4\ns_waitcnt lgkmcnt(6)\nv_smfmac_f32_16x16x64_bf16 v[48:51], v[92:95], a[184:191], v4\n" \
"v_smfmac_f32_16x16x64_bf16 v[52:55], v[92:95], a[192:199], v4\ns_waitcnt lgkmcnt(4)\nv_smfmac_f32_16x16x64_bf16 v[56:59], v[88:91], v[64:71], v4\nds_read_b128 v[64:67], v7 offset:12544\nv_add_f32 v48, v48, v50\nv_smfmac_f32_16x16x64_bf16 v[60:63], v[88:91], a[176:183], v4\nds_read_b128 v[68:71], v7 offset:14592\nv_add_f32 v52, v52, v54\n" \
"v_add_f32 v49, v49, v51\nv_smfmac_f32_16x16x64_bf16 v[56:59], v[92:95], a[200:207], v4\ns_waitcnt lgkmcnt(3)\nv_add_f32 v53, v53, v55\nv_cvt_pk_bf16_f32 v50, v48, v52\nv_smfmac_f32_16x16x64_bf16 v[60:63], v[92:95], v[72:79], v4\nv_cvt_pk_bf16_f32 v51, v49, v53\nds_write2_b32 v6, v50, v51 offset0:0 offset1:4\n" \
"s_nop 1\nv_add_f32 v56, v56, v58\nv_add_f32 v57, v57, v59\ns_nop 1\nv_add_f32 v60, v60, v62\nv_add_f32 v61, v61, v63\nv_cvt_pk_bf16_f32 v62, v56, v60\nv_cvt_pk_bf16_f32 v63, v57, v61\n" \
"ds_write2_b32 v6, v62, v63 offset0:36 offset1:40\nv_mov_b32 v61, s54\nv_mfma_f32_16x16x32_bf16 v[52:55], v[32:35], v[16:19], 0\nv_mfma_f32_16x16x32_bf16 v[52:55], v[36:39], v[20:23], v[52:55]\nv_mfma_f32_16x16x32_bf16 v[52:55], v[40:43], v[24:27], v[52:55]\nv_mfma_f32_16x16x32_bf16 v[52:55], v[44:47], v[28:31], v[52:55]\ns_waitcnt lgkmcnt(0)\ns_barrier\n" \
"v_mfma_f32_16x16x32_bf16 v[56:59], v[80:83], v[16:19], v[12:15]\nv_mfma_f32_16x16x32_bf16 v[56:59], v[84:87], v[20:23], v[56:59]\nds_read_b128 v[32:35], v9 offset:0\nds_read_b128 v[36:39], v9 offset:576\nds_read_b128 v[40:43], v9 offset:1152\nds_read_b128 v[44:47], v9 offset:1728\nds_read_b32 v60, v0 offset:53760\nv_mfma_f32_16x16x32_bf16 v[56:59], v[64:67], v[24:27], v[56:59]\n" \
"v_mfma_f32_16x16x32_bf16 v[56:59], v[68:71], v[28:31], v[56:59]\ns_waitcnt lgkmcnt(4)\nds_read_b128 v[80:83], v2 offset:0\nds_read_b128 v[84:87], v2 offset:256\nds_read_b128 v[88:91], v2 offset:2048\nv_mfma_f32_16x16x32_bf16 v[48:51], v[32:35], v[16:19], 0\ns_waitcnt lgkmcnt(6)\nv_mfma_f32_16x16x32_bf16 v[48:51], v[36:39], v[20:23], v[48:51]\n" \
"s_waitcnt lgkmcnt(5)\nv_mfma_f32_16x16x32_bf16 v[48:51], v[40:43], v[24:27], v[48:51]\ns_waitcnt lgkmcnt(4)\nv_mfma_f32_16x16x32_bf16 v[48:51], v[44:47], v[28:31], v[48:51]\ns_waitcnt lgkmcnt(2)\nv_mfma_f32_16x16x32_bf16 v[16:19], a[0:3], v[80:83], 0\nds_read_b128 v[92:95], v2 offset:2304\nv_max3_f32 v62, v52, v53, v54\n" \
"v_max3_f32 v62, v62, v56, v57\nv_mfma_f32_16x16x32_bf16 v[20:23], a[8:11], v[80:83], 0\nds_read_b128 v[72:75], v1 offset:0\nv_max3_f32 v62, v62, v58, v59\nv_max_f32 v62, v62, v55\nv_mfma_f32_16x16x32_bf16 v[24:27], a[16:19], v[80:83], 0\nv_max3_f32 v63, v62, v48, v49\nv_max3_f32 v62, v62, v48, v49\n" \
"v_mfma_f32_16x16x32_bf16 v[28:31], a[24:27], v[80:83], 0\ns_waitcnt lgkmcnt(3)\nv_permlane16_swap_b32 v62, v63\nv_max_f32 v50, v62, v63\nv_mfma_f32_16x16x32_bf16 v[32:35], a[0:3], v[84:87], 0\nds_read_b128 v[80:83], v2 offset:4096\nv_max_f32 v62, v62, v63\nv_mfma_f32_16x16x32_bf16 v[36:39], a[8:11], v[84:87], 0\n" \
"s_nop 0\nv_permlane32_swap_b32 v62, v50\nv_max_f32 v62, v62, v50\nv_mfma_f32_16x16x32_bf16 v[40:43], a[16:19], v[84:87], 0\nv_mul_f32 v62, s59, v62\nv_fma_f32 v52, v52, s58, v62\nv_fma_f32 v53, v53, s58, v62\nv_mfma_f32_16x16x32_bf16 v[44:47], a[24:27], v[84:87], 0\n" \
"s_waitcnt lgkmcnt(3)\nv_exp_f32 v52, v52\nv_fma_f32 v54, v54, s58, v62\nv_mfma_f32_16x16x32_bf16 v[16:19], a[4:7], v[88:91], v[16:19]\nds_read_b128 v[84:87], v2 offset:4352\nv_exp_f32 v53, v53\nv_mfma_f32_16x16x32_bf16 v[20:23], a[12:15], v[88:91], v[20:23]\nds_read_b128 v[76:79], v1 offset:1024\n" \
"v_fma_f32 v55, v55, s58, v62\nv_add_f32 v63, v52, v53\nv_mfma_f32_16x16x32_bf16 v[24:27], a[20:23], v[88:91], v[24:27]\nv_exp_f32 v54, v54\nv_fma_f32 v56, v56, s58, v62\nv_mfma_f32_16x16x32_bf16 v[28:31], a[28:31], v[88:91], v[28:31]\ns_waitcnt lgkmcnt(4)\nv_exp_f32 v55, v55\n" \
"v_add_f32 v63, v63, v54\nv_mfma_f32_16x16x32_bf16 v[32:35], a[4:7], v[92:95], v[32:35]\nds_read_b128 v[88:91], v2 offset:6144\nv_fma_f32 v57, v57, s58, v62\nv_add_f32 v63, v63, v55\nv_mfma_f32_16x16x32_bf16 v[36:39], a[12:15], v[92:95], v[36:39]\nv_exp_f32 v56, v56\nv_fma_f32 v58, v58, s58, v62\n" \
"v_mfma_f32_16x16x32_bf16 v[40:43], a[20:23], v[92:95], v[40:43]\nv_exp_f32 v57, v57\nv_add_f32 v63, v63, v56\nv_mfma_f32_16x16x32_bf16 v[44:47], a[28:31], v[92:95], v[44:47]\ns_waitcnt lgkmcnt(3)\nv_fma_f32 v59, v59, s58, v62\nv_exp_f32 v58, v58\nv_mfma_f32_16x16x32_bf16 v[16:19], a[32:35], v[80:83], v[16:19]\n" \
"ds_read_b128 v[92:95], v2 offset:6400\nv_add_f32 v63, v63, v57\nv_fma_f32 v48, v48, s58, v62\nv_mfma_f32_16x16x32_bf16 v[20:23], v[72:75], v[80:83], v[20:23]\nds_read_b128 v[64:67], v1 offset:2048\nv_exp_f32 v59, v59\nv_mfma_f32_16x16x32_bf16 v[24:27], a[40:43], v[80:83], v[24:27]\nv_add_f32 v63, v63, v58\n" \
"v_fma_f32 v49, v49, s58, v62\nv_add_f32 v63, v63, v59\nv_mfma_f32_16x16x32_bf16 v[28:31], a[48:51], v[80:83], v[28:31]\ns_waitcnt lgkmcnt(4)\nv_exp_f32 v48, v48\nv_mfma_f32_16x16x32_bf16 v[32:35], a[32:35], v[84:87], v[32:35]\nds_read_b128 v[80:83], v2 offset:8192\nv_exp_f32 v49, v49\n" \
"v_mfma_f32_16x16x32_bf16 v[36:39], v[72:75], v[84:87], v[36:39]\nv_add_f32 v63, v63, v48\nv_add_f32 v62, v63, v49\nv_add_f32 v50, v63, v49\nv_mfma_f32_16x16x32_bf16 v[40:43], a[40:43], v[84:87], v[40:43]\ns_nop 0\nv_permlane16_swap_b32 v62, v50\nv_add_f32 v63, v62, v50\n" \
"v_mfma_f32_16x16x32_bf16 v[44:47], a[48:51], v[84:87], v[44:47]\ns_waitcnt lgkmcnt(3)\nv_add_f32 v50, v62, v50\nv_mfma_f32_16x16x32_bf16 v[16:19], a[36:39], v[88:91], v[16:19]\nds_read_b128 v[84:87], v2 offset:8448\nv_permlane32_swap_b32 v63, v50\nv_mfma_f32_16x16x32_bf16 v[20:23], v[76:79], v[88:91], v[20:23]\nds_read_b128 v[68:71], v1 offset:3072\n" \
"v_add_f32 v63, v63, v50\nv_mfma_f32_16x16x32_bf16 v[24:27], a[44:47], v[88:91], v[24:27]\nv_rcp_f32 v63, v63\nv_mfma_f32_16x16x32_bf16 v[28:31], a[52:55], v[88:91], v[28:31]\ns_waitcnt lgkmcnt(4)\nv_mul_f32 v52, v52, v63\nv_mul_f32 v53, v53, v63\nv_mul_f32 v54, v54, v63\n" \
"v_mfma_f32_16x16x32_bf16 v[32:35], a[36:39], v[92:95], v[32:35]\nds_read_b128 v[88:91], v2 offset:10240\nv_mul_f32 v55, v55, v63\nv_mul_f32 v56, v56, v63\nv_mfma_f32_16x16x32_bf16 v[36:39], v[76:79], v[92:95], v[36:39]\nv_mul_f32 v57, v57, v63\nv_mul_f32 v58, v58, v63\nv_mul_f32 v59, v59, v63\n" \
"v_mfma_f32_16x16x32_bf16 v[40:43], a[44:47], v[92:95], v[40:43]\nv_mul_f32 v48, v48, v63\nv_mul_f32 v49, v49, v63\nv_cvt_pk_bf16_f32 v52, v52, v53\nv_mfma_f32_16x16x32_bf16 v[44:47], a[52:55], v[92:95], v[44:47]\ns_waitcnt lgkmcnt(3)\nv_cvt_pk_bf16_f32 v53, v54, v55\nv_cvt_pk_bf16_f32 v48, v48, v49\n" \
"v_cvt_pk_bf16_f32 v54, v56, v57\nv_mfma_f32_16x16x32_bf16 v[16:19], a[56:59], v[80:83], v[16:19]\nds_read_b128 v[92:95], v2 offset:10496\nv_cvt_pk_bf16_f32 v55, v58, v59\nv_bfi_b32 v52, s61, v52, v61\nv_mfma_f32_16x16x32_bf16 v[20:23], a[64:67], v[80:83], v[20:23]\nds_read_b128 v[72:75], v1 offset:4096\nv_bfi_b32 v53, s61, v53, v61\n" \
"v_bfi_b32 v54, s61, v54, v61\nv_mfma_f32_16x16x32_bf16 v[24:27], v[64:67], v[80:83], v[24:27]\nv_bfi_b32 v55, s61, v55, v61\nv_bfi_b32 v48, s61, v48, v61\nv_mfma_f32_16x16x32_bf16 v[28:31], a[72:75], v[80:83], v[28:31]\ns_waitcnt lgkmcnt(4)\nbuffer_store_dwordx4 v[52:55], v10, s[36:39], s55 offen sc1\nv_mov_b32 v49, v48\n" \
"v_mfma_f32_16x16x32_bf16 v[32:35], a[56:59], v[84:87], v[32:35]\nds_read_b128 v[80:83], v2 offset:12288\nv_permlane16_swap_b32 v48, v49\nv_mfma_f32_16x16x32_bf16 v[36:39], a[64:67], v[84:87], v[36:39]\nv_mov_b32 v50, v48\nv_mov_b32 v51, v49\nv_mfma_f32_16x16x32_bf16 v[40:43], v[64:67], v[84:87], v[40:43]\nv_permlane32_swap_b32 v48, v50\n" \
"v_mfma_f32_16x16x32_bf16 v[44:47], a[72:75], v[84:87], v[44:47]\ns_waitcnt lgkmcnt(3)\nv_permlane32_swap_b32 v49, v51\nv_mfma_f32_16x16x32_bf16 v[16:19], a[60:63], v[88:91], v[16:19]\nds_read_b128 v[84:87], v2 offset:12544\nv_mfma_f32_16x16x32_bf16 v[20:23], a[68:71], v[88:91], v[20:23]\nds_read_b128 v[76:79], v1 offset:5120\nv_mfma_f32_16x16x32_bf16 v[24:27], v[68:71], v[88:91], v[24:27]\n" \
"buffer_store_dwordx4 v[48:51], v11, s[36:39], s55 offen sc1\nv_cmp_ne_u32 vcc, 0, v60\nv_mfma_f32_16x16x32_bf16 v[28:31], a[76:79], v[88:91], v[28:31]\ns_waitcnt lgkmcnt(4)\nv_mfma_f32_16x16x32_bf16 v[32:35], a[60:63], v[92:95], v[32:35]\nds_read_b128 v[88:91], v2 offset:14336\nv_mfma_f32_16x16x32_bf16 v[36:39], a[68:71], v[92:95], v[36:39]\nv_mfma_f32_16x16x32_bf16 v[40:43], v[68:71], v[92:95], v[40:43]\n" \
"v_mfma_f32_16x16x32_bf16 v[44:47], a[76:79], v[92:95], v[44:47]\ns_cbranch_vccnz .Lqk_pj%=\ns_cmp_lt_i32 s40, 63\ns_waitcnt lgkmcnt(0)\ns_cbranch_scc1 .Lqk_pwait%=\ns_branch .Lqk_end%=\n.Lqk_re2%=:\nv_max_u32 v17, v80, v82\n" \
"v_cmp_eq_u32 vcc, -1, v17\ns_cbranch_vccz .Lqk_sk2418%=\ns_mov_b64 exec, vcc\nbuffer_load_dwordx4 v[80:83], v3, s[36:39], s42 offen offset:0 sc1\ns_mov_b64 exec, -1\n.Lqk_sk2418%=:\nv_max_u32 v17, v84, v86\nv_cmp_eq_u32 vcc, -1, v17\n" \
"s_cbranch_vccz .Lqk_sk2425%=\ns_mov_b64 exec, vcc\nbuffer_load_dwordx4 v[84:87], v3, s[36:39], s42 offen offset:1024 sc1\ns_mov_b64 exec, -1\n.Lqk_sk2425%=:\nv_max_u32 v17, v88, v90\nv_cmp_eq_u32 vcc, -1, v17\ns_cbranch_vccz .Lqk_sk2432%=\n" \
"s_mov_b64 exec, vcc\nbuffer_load_dwordx4 v[88:91], v3, s[36:39], s42 offen offset:2048 sc1\ns_mov_b64 exec, -1\n.Lqk_sk2432%=:\nv_max_u32 v17, v92, v94\nv_cmp_eq_u32 vcc, -1, v17\ns_cbranch_vccz .Lqk_sk2439%=\ns_mov_b64 exec, vcc\n" \
"buffer_load_dwordx4 v[92:95], v3, s[36:39], s42 offen offset:3072 sc1\ns_mov_b64 exec, -1\n.Lqk_sk2439%=:\nv_mov_b64 v[48:49], 0\nv_mov_b64 v[50:51], 0\nv_mov_b64 v[52:53], 0\nv_mov_b64 v[54:55], 0\nv_mov_b64 v[56:57], 0\n" \
"v_mov_b64 v[58:59], 0\nv_mov_b64 v[60:61], 0\nv_mov_b64 v[62:63], 0\ns_add_u32 s43, s43, 1\ns_cmp_lt_u32 s43, 262144\ns_cbranch_scc1 .Lqk_poll2%=\ns_branch .Lqk_abort%=\n.p2align 6\n" \
".Lqk_c3%=:\ns_waitcnt lgkmcnt(3)\nv_mfma_f32_16x16x32_bf16 v[32:35], v[232:235], v[56:59], v[32:35]\ns_add_u32 s44, s40, 1\nv_mov_b64 v[52:53], 0\nv_mov_b64 v[54:55], 0\nv_mfma_f32_16x16x32_bf16 v[36:39], v[240:243], v[56:59], v[36:39]\ns_lshl_b32 s44, s44, 17\n" \
"s_waitcnt lgkmcnt(2)\nds_read_b128 v[64:67], v1 offset:18432\nv_mfma_f32_16x16x32_bf16 v[32:35], v[236:239], v[60:63], v[32:35]\ns_add_u32 s44, s44, %[xn]\ns_lshr_b32 s45, s44, 8\nds_read_b128 v[68:71], v1 offset:19456\nv_mfma_f32_16x16x32_bf16 v[36:39], v[244:247], v[60:63], v[36:39]\ns_and_b32 s45, s45, 0x2000\n" \
"s_xor_b32 s41, s44, s45\nv_mfma_f32_16x16x32_bf16 v[40:43], v[248:251], v[56:59], v[40:43]\nbuffer_load_dword v16, v12, s[36:39], s56 offen sc1\nv_mfma_f32_16x16x32_bf16 v[44:47], v[72:75], v[56:59], v[44:47]\nv_cvt_pk_bf16_f32 v36, v32, v36\nv_cvt_pk_bf16_f32 v37, v33, v37\nv_mfma_f32_16x16x32_bf16 v[40:43], v[252:255], v[60:63], v[40:43]\nv_cvt_pk_bf16_f32 v38, v34, v38\n" \
"v_cvt_pk_bf16_f32 v39, v35, v39\nv_mfma_f32_16x16x32_bf16 v[44:47], v[76:79], v[60:63], v[44:47]\nds_read_b128 v[72:75], v1 offset:20480\nds_read_b128 v[76:79], v1 offset:21504\nv_mov_b64 v[56:57], 0\nv_mov_b64 v[58:59], 0\nv_mov_b64 v[60:61], 0\nv_mov_b64 v[62:63], 0\n" \
".Lqk_poll3%=:\ns_waitcnt vmcnt(1)\nv_smfmac_f32_16x16x64_bf16 v[48:51], v[80:83], v[152:159], v4\nds_write_b128 v0, v[80:83] offset:28672\nv_max3_u32 v17, v80, v82, v84\nv_smfmac_f32_16x16x64_bf16 v[52:55], v[80:83], v[160:167], v4\nv_max3_u32 v17, v17, v86, v88\nv_smfmac_f32_16x16x64_bf16 v[56:59], v[80:83], v[168:175], v4\n" \
"ds_write_b128 v0, v[84:87] offset:29696\nv_max3_u32 v17, v17, v90, v92\nv_max_u32 v17, v17, v94\nv_smfmac_f32_16x16x64_bf16 v[60:63], v[80:83], v[176:183], v4\nv_cmp_eq_u32 vcc, -1, v17\nv_smfmac_f32_16x16x64_bf16 v[48:51], v[84:87], v[184:191], v4\nds_write_b128 v0, v[88:91] offset:30720\ns_cbranch_vccnz .Lqk_re3%=\n" \
"s_waitcnt lgkmcnt(5)\nv_smfmac_f32_16x16x64_bf16 v[52:55], v[84:87], v[64:71], v4\nv_smfmac_f32_16x16x64_bf16 v[56:59], v[84:87], v[192:199], v4\nds_write_b128 v0, v[92:95] offset:31744\nv_smfmac_f32_16x16x64_bf16 v[60:63], v[84:87], v[200:207], v4\nv_smfmac_f32_16x16x64_bf16 v[48:51], v[88:91], v[208:215], v4\nv_smfmac_f32_16x16x64_bf16 v[52:55], v[88:91], v[216:223], v4\ns_waitcnt lgkmcnt(0)\n" \
".Lqk_jn3%=:\ns_barrier\nds_read_b128 v[80:83], v0 offset:16384\nds_read_b128 v[64:67], v1 offset:22528\nds_read_b128 v[68:71], v1 offset:23552\nv_smfmac_f32_16x16x64_bf16 v[56:59], v[88:91], v[72:79], v4\ns_waitcnt vmcnt(0)\nds_read_b128 v[72:75], v1 offset:0\n" \
"ds_read_b128 v[76:79], v1 offset:1024\nv_cmp_ge_i32 vcc, s40, v16\nv_smfmac_f32_16x16x64_bf16 v[60:63], v[88:91], v[224:231], v4\ns_cmp_eq_u64 vcc, 0\ns_cselect_b32 s61, s41, -2147483648\ns_cselect_b32 s44, 1, 0\nds_read_b128 v[84:87], v0 offset:17408\nds_read_b128 v[88:91], v0 offset:18432\n" \
"v_smfmac_f32_16x16x64_bf16 v[48:51], v[92:95], v[232:239], v4\ns_add_u32 m0, %[l0], 0\ns_add_u32 s60, s61, 0\nds_write_b128 v5, v[36:39] offset:256\nbuffer_load_dwordx4 v0, s[36:39], s60 offen offset:0 sc1 lds\nv_cvt_pk_bf16_f32 v44, v40, v44\nv_cvt_pk_bf16_f32 v45, v41, v45\nv_cvt_pk_bf16_f32 v46, v42, v46\n" \
"v_cvt_pk_bf16_f32 v47, v43, v47\nv_smfmac_f32_16x16x64_bf16 v[52:55], v[92:95], v[240:247], v4\nv_mov_b32 v16, s44\nv_smfmac_f32_16x16x64_bf16 v[56:59], v[92:95], v[248:255], v4\ns_waitcnt lgkmcnt(5)\nbuffer_load_dwordx4 v0, s[36:39], s60 offen offset:1024 sc1 lds\nv_smfmac_f32_16x16x64_bf16 v[60:63], v[92:95], v[64:71], v4\nds_read_b128 v[92:95], v0 offset:19456\n" \
"ds_read_b128 v[64:67], v1 offset:2048\nds_read_b128 v[68:71], v1 offset:3072\nds_write_b32 v0, v16 offset:53760\nv_smfmac_f32_16x16x64_bf16 v[48:51], v[80:83], a[0:7], v4\nds_write_b128 v5, v[44:47] offset:768\nbuffer_load_dwordx4 v0, s[36:39], s60 offen offset:2048 sc1 lds\nv_smfmac_f32_16x16x64_bf16 v[52:55], v[80:83], a[8:15], v4\nv_smfmac_f32_16x16x64_bf16 v[56:59], v[80:83], a[16:23], v4\n" \
"buffer_load_dwordx4 v0, s[36:39], s60 offen offset:3072 sc1 lds\nv_smfmac_f32_16x16x64_bf16 v[60:63], v[80:83], a[24:31], v4\ns_waitcnt lgkmcnt(7)\ns_add_u32 m0, %[l0], 4096\nds_read_b128 v[80:83], v0 offset:20480\nv_smfmac_f32_16x16x64_bf16 v[48:51], v[84:87], a[32:39], v4\ns_add_u32 s60, s61, 4096\nbuffer_load_dwordx4 v0, s[36:39], s60 offen offset:0 sc1 lds\n" \
"v_smfmac_f32_16x16x64_bf16 v[52:55], v[84:87], v[72:79], v4\nds_read_b128 v[72:75], v1 offset:4096\nds_read_b128 v[76:79], v1 offset:5120\nv_smfmac_f32_16x16x64_bf16 v[56:59], v[84:87], a[40:47], v4\nbuffer_load_dwordx4 v0, s[36:39], s60 offen offset:1024 sc1 lds\nv_smfmac_f32_16x16x64_bf16 v[60:63], v[84:87], a[48:55], v4\ns_waitcnt lgkmcnt(9)\nds_read_b128 v[84:87], v0 offset:21504\n" \
"v_smfmac_f32_16x16x64_bf16 v[48:51], v[88:91], a[56:63], v4\ns_waitcnt lgkmcnt(4)\ns_barrier\nbuffer_load_dwordx4 v0, s[36:39], s60 offen offset:2048 sc1 lds\nv_smfmac_f32_16x16x64_bf16 v[52:55], v[88:91], a[64:71], v4\nv_smfmac_f32_16x16x64_bf16 v[56:59], v[88:91], v[64:71], v4\nds_read_b128 v[64:67], v1 offset:6144\nds_read_b128 v[68:71], v1 offset:7168\n" \
"buffer_load_dwordx4 v0, s[36:39], s60 offen offset:3072 sc1 lds\nv_smfmac_f32_16x16x64_bf16 v[60:63], v[88:91], a[72:79], v4\ns_add_u32 m0, %[l0], 8192\nds_read_b128 v[88:91], v0 offset:22528\nv_smfmac_f32_16x16x64_bf16 v[48:51], v[92:95], a[80:87], v4\ns_add_u32 s60, s61, 16384\nbuffer_load_dwordx4 v0, s[36:39], s60 offen offset:0 sc1 lds\nv_smfmac_f32_16x16x64_bf16 v[52:55], v[92:95], a[88:95], v4\n" \
"v_smfmac_f32_16x16x64_bf16 v[56:59], v[92:95], a[96:103], v4\ns_waitcnt lgkmcnt(4)\nbuffer_load_dwordx4 v0, s[36:39], s60 offen offset:1024 sc1 lds\nv_smfmac_f32_16x16x64_bf16 v[60:63], v[92:95], v[72:79], v4\nds_read_b128 v[92:95], v0 offset:23552\nds_read_b128 v[72:75], v1 offset:8192\nds_read_b128 v[76:79], v1 offset:9216\nv_smfmac_f32_16x16x64_bf16 v[48:51], v[80:83], a[104:111], v4\n" \
"buffer_load_dwordx4 v0, s[36:39], s60 offen offset:2048 sc1 lds\nv_smfmac_f32_16x16x64_bf16 v[52:55], v[80:83], a[112:119], v4\nv_smfmac_f32_16x16x64_bf16 v[56:59], v[80:83], a[120:127], v4\nbuffer_load_dwordx4 v0, s[36:39], s60 offen offset:3072 sc1 lds\nv_smfmac_f32_16x16x64_bf16 v[60:63], v[80:83], a[128:135], v4\ns_waitcnt lgkmcnt(6)\ns_add_u32 m0, %[l0], 12288\nds_read_b128 v[80:83], v0 offset:24576\n" \
"v_smfmac_f32_16x16x64_bf16 v[48:51], v[84:87], a[136:143], v4\ns_add_u32 s60, s61, 20480\ns_waitcnt lgkmcnt(5)\nbuffer_load_dwordx4 v0, s[36:39], s60 offen offset:0 sc1 lds\nv_smfmac_f32_16x16x64_bf16 v[52:55], v[84:87], v[64:71], v4\nds_read_b128 v[64:67], v1 offset:10240\nds_read_b128 v[68:71], v1 offset:11264\nv_smfmac_f32_16x16x64_bf16 v[56:59], v[84:87], a[144:151], v4\n" \
"buffer_load_dwordx4 v0, s[36:39], s60 offen offset:1024 sc1 lds\nv_smfmac_f32_16x16x64_bf16 v[60:63], v[84:87], a[152:159], v4\ns_waitcnt lgkmcnt(6)\nds_read_b128 v[84:87], v0 offset:25600\nv_smfmac_f32_16x16x64_bf16 v[48:51], v[88:91], a[160:167], v4\nbuffer_load_dwordx4 v0, s[36:39], s60 offen offset:2048 sc1 lds\nv_smfmac_f32_16x16x64_bf16 v[52:55], v[88:91], a[168:175], v4\ns_waitcnt lgkmcnt(4)\n" \
"v_smfmac_f32_16x16x64_bf16 v[56:59], v[88:91], v[72:79], v4\nds_read_b128 v[72:75], v1 offset:12288\nds_read_b128 v[76:79], v1 offset:13312\nbuffer_load_dwordx4 v0, s[36:39], s60 offen offset:3072 sc1 lds\nv_smfmac_f32_16x16x64_bf16 v[60:63], v[88:91], a[176:183], v4\nds_read_b128 v[88:91], v0 offset:26624\nv_smfmac_f32_16x16x64_bf16 v[48:51], v[92:95], a[184:191], v4\nv_smfmac_f32_16x16x64_bf16 v[52:55], v[92:95], a[192:199], v4\n" \
"v_smfmac_f32_16x16x64_bf16 v[56:59], v[92:95], a[200:207], v4\ns_waitcnt lgkmcnt(4)\nv_smfmac_f32_16x16x64_bf16 v[60:63], v[92:95], v[64:71], v4\nds_read_b128 v[92:95], v0 offset:27648\nds_read_b128 v[64:67], v1 offset:14336\nds_read_b128 v[68:71], v1 offset:15360\nv_smfmac_f32_16x16x64_bf16 v[48:51], v[80:83], a[208:215], v4\nv_smfmac_f32_16x16x64_bf16 v[52:55], v[80:83], a[216:223], v4\n" \
"v_smfmac_f32_16x16x64_bf16 v[56:59], v[80:83], a[224:231], v4\nv_smfmac_f32_16x16x64_bf16 v[60:63], v[80:83], a[232:239], v4\ns_waitcnt lgkmcnt(6)\nv_smfmac_f32_16x16x64_bf16 v[48:51], v[84:87], a[240:247], v4\ns_waitcnt lgkmcnt(4)\nv_smfmac_f32_16x16x64_bf16 v[52:55], v[84:87], v[72:79], v4\nds_read_b128 v[72:75], v1 offset:16384\nds_read_b128 v[76:79], v1 offset:17408\n" \
"v_smfmac_f32_16x16x64_bf16 v[56:59], v[84:87], a[248:255], v4\nv_smfmac_f32_16x16x64_bf16 v[60:63], v[84:87], v[96:103], v4\ns_waitcnt lgkmcnt(5)\nv_smfmac_f32_16x16x64_bf16 v[48:51], v[88:91], v[104:111], v4\nv_smfmac_f32_16x16x64_bf16 v[52:55], v[88:91], v[112:119], v4\ns_waitcnt lgkmcnt(4)\nv_smfmac_f32_16x16x64_bf16 v[48:51], v[92:95], v[128:135], v4\nv_smfmac_f32_16x16x64_bf16 v[52:55], v[92:95], v[136:143], v4\n" \
"s_waitcnt lgkmcnt(2)\nv_smfmac_f32_16x16x64_bf16 v[56:59], v[88:91], v[64:71], v4\nv_add_f32 v48, v48, v50\nv_smfmac_f32_16x16x64_bf16 v[60:63], v[88:91], v[120:127], v4\nv_add_f32 v52, v52, v54\nv_add_f32 v49, v49, v51\nv_smfmac_f32_16x16x64_bf16 v[56:59], v[92:95], v[144:151], v4\ns_waitcnt lgkmcnt(0)\n" \
"v_add_f32 v53, v53, v55\nv_cvt_pk_bf16_f32 v50, v48, v52\nv_smfmac_f32_16x16x64_bf16 v[60:63], v[92:95], v[72:79], v4\nv_cvt_pk_bf16_f32 v51, v49, v53\nds_write2_b32 v6, v50, v51 offset0:0 offset1:4\ns_nop 1\nv_add_f32 v56, v56, v58\nv_add_f32 v57, v57, v59\n" \
"s_nop 1\nv_add_f32 v60, v60, v62\nv_add_f32 v61, v61, v63\nv_cvt_pk_bf16_f32 v62, v56, v60\nv_cvt_pk_bf16_f32 v63, v57, v61\nds_write2_b32 v6, v62, v63 offset0:36 offset1:40\ns_waitcnt vmcnt(0)\ns_waitcnt lgkmcnt(0)\n" \
"s_barrier\ns_cbranch_vccnz .Lqk_slow%=\ns_add_u32 m0, %[l0], 16384\ns_add_u32 s60, s41, 32768\nbuffer_load_dwordx4 v0, s[36:39], s60 offen offset:0 sc1 lds\nbuffer_load_dwordx4 v0, s[36:39], s60 offen offset:1024 sc1 lds\nbuffer_load_dwordx4 v0, s[36:39], s60 offen offset:2048 sc1 lds\nbuffer_load_dwordx4 v0, s[36:39], s60 offen offset:3072 sc1 lds\n" \
"s_add_u32 m0, %[l0], 20480\ns_add_u32 s60, s41, 36864\nbuffer_load_dwordx4 v0, s[36:39], s60 offen offset:0 sc1 lds\nbuffer_load_dwordx4 v0, s[36:39], s60 offen offset:1024 sc1 lds\nds_read_b128 v[80:83], v2 offset:0\nds_read_b128 v[84:87], v2 offset:256\nds_read_b128 v[88:91], v2 offset:2048\nbuffer_load_dwordx4 v0, s[36:39], s60 offen offset:2048 sc1 lds\n" \
"buffer_load_dwordx4 v0, s[36:39], s60 offen offset:3072 sc1 lds\ns_add_u32 m0, %[l0], 24576\ns_add_u32 s60, s41, 49152\nbuffer_load_dwordx4 v0, s[36:39], s60 offen offset:0 sc1 lds\nbuffer_load_dwordx4 v0, s[36:39], s60 offen offset:1024 sc1 lds\nbuffer_load_dwordx4 v0, s[36:39], s60 offen offset:2048 sc1 lds\nbuffer_load_dwordx4 v0, s[36:39], s60 offen offset:3072 sc1 lds\ns_add_u32 m0, %[l0], 28672\n" \
"s_add_u32 s60, s41, 53248\nbuffer_load_dwordx4 v0, s[36:39], s60 offen offset:0 sc1 lds\nbuffer_load_dwordx4 v0, s[36:39], s60 offen offset:1024 sc1 lds\nbuffer_load_dwordx4 v0, s[36:39], s60 offen offset:2048 sc1 lds\nbuffer_load_dwordx4 v0, s[36:39], s60 offen offset:3072 sc1 lds\ns_waitcnt lgkmcnt(2)\nv_mfma_f32_16x16x32_bf16 v[16:19], a[0:3], v[80:83], 0\nds_read_b128 v[92:95], v2 offset:2304\n" \
"v_mfma_f32_16x16x32_bf16 v[20:23], a[8:11], v[80:83], 0\nds_read_b128 v[72:75], v1 offset:0\nv_mfma_f32_16x16x32_bf16 v[24:27], a[16:19], v[80:83], 0\nv_mfma_f32_16x16x32_bf16 v[28:31], a[24:27], v[80:83], 0\ns_waitcnt lgkmcnt(3)\nv_mfma_f32_16x16x32_bf16 v[32:35], a[0:3], v[84:87], 0\nds_read_b128 v[80:83], v2 offset:4096\nv_mfma_f32_16x16x32_bf16 v[36:39], a[8:11], v[84:87], 0\n" \
"v_mfma_f32_16x16x32_bf16 v[40:43], a[16:19], v[84:87], 0\nv_mfma_f32_16x16x32_bf16 v[44:47], a[24:27], v[84:87], 0\ns_waitcnt lgkmcnt(3)\nv_mfma_f32_16x16x32_bf16 v[16:19], a[4:7], v[88:91], v[16:19]\nds_read_b128 v[84:87], v2 offset:4352\nv_mfma_f32_16x16x32_bf16 v[20:23], a[12:15], v[88:91], v[20:23]\nds_read_b128 v[76:79], v1 offset:1024\nv_mfma_f32_16x16x32_bf16 v[24:27], a[20:23], v[88:91], v[24:27]\n" \
"v_mfma_f32_16x16x32_bf16 v[28:31], a[28:31], v[88:91], v[28:31]\ns_waitcnt lgkmcnt(4)\nv_mfma_f32_16x16x32_bf16 v[32:35], a[4:7], v[92:95], v[32:35]\nds_read_b128 v[88:91], v2 offset:6144\nv_mfma_f32_16x16x32_bf16 v[36:39], a[12:15], v[92:95], v[36:39]\nv_mfma_f32_16x16x32_bf16 v[40:43], a[20:23], v[92:95], v[40:43]\nv_mfma_f32_16x16x32_bf16 v[44:47], a[28:31], v[92:95], v[44:47]\ns_waitcnt lgkmcnt(3)\n" \
"v_mfma_f32_16x16x32_bf16 v[16:19], a[32:35], v[80:83], v[16:19]\nds_read_b128 v[92:95], v2 offset:6400\nv_mfma_f32_16x16x32_bf16 v[20:23], v[72:75], v[80:83], v[20:23]\nds_read_b128 v[64:67], v1 offset:2048\nv_mfma_f32_16x16x32_bf16 v[24:27], a[40:43], v[80:83], v[24:27]\nv_mfma_f32_16x16x32_bf16 v[28:31], a[48:51], v[80:83], v[28:31]\ns_waitcnt lgkmcnt(4)\nv_mfma_f32_16x16x32_bf16 v[32:35], a[32:35], v[84:87], v[32:35]\n" \
"ds_read_b128 v[80:83], v2 offset:8192\nv_mfma_f32_16x16x32_bf16 v[36:39], v[72:75], v[84:87], v[36:39]\nv_mfma_f32_16x16x32_bf16 v[40:43], a[40:43], v[84:87], v[40:43]\nv_mfma_f32_16x16x32_bf16 v[44:47], a[48:51], v[84:87], v[44:47]\ns_waitcnt lgkmcnt(3)\nv_mfma_f32_16x16x32_bf16 v[16:19], a[36:39], v[88:91], v[16:19]\nds_read_b128 v[84:87], v2 offset:8448\nv_mfma_f32_16x16x32_bf16 v[20:23], v[76:79], v[88:91], v[20:23]\n" \
"ds_read_b128 v[68:71], v1 offset:3072\nv_mfma_f32_16x16x32_bf16 v[24:27], a[44:47], v[88:91], v[24:27]\nv_mfma_f32_16x16x32_bf16 v[28:31], a[52:55], v[88:91], v[28:31]\ns_waitcnt lgkmcnt(4)\nv_mfma_f32_16x16x32_bf16 v[32:35], a[36:39], v[92:95], v[32:35]\nds_read_b128 v[88:91], v2 offset:10240\nv_mfma_f32_16x16x32_bf16 v[36:39], v[76:79], v[92:95], v[36:39]\nv_mfma_f32_16x16x32_bf16 v[40:43], a[44:47], v[92:95], v[40:43]\n" \
"v_mfma_f32_16x16x32_bf16 v[44:47], a[52:55], v[92:95], v[44:47]\ns_waitcnt lgkmcnt(3)\nv_mfma_f32_16x16x32_bf16 v[16:19], a[56:59], v[80:83], v[16:19]\nds_read_b128 v[92:95], v2 offset:10496\nv_mfma_f32_16x16x32_bf16 v[20:23], a[64:67], v[80:83], v[20:23]\nds_read_b128 v[72:75], v1 offset:4096\nv_mfma_f32_16x16x32_bf16 v[24:27], v[64:67], v[80:83], v[24:27]\nv_mfma_f32_16x16x32_bf16 v[28:31], a[72:75], v[80:83], v[28:31]\n" \
"s_waitcnt lgkmcnt(4)\nv_mfma_f32_16x16x32_bf16 v[32:35], a[56:59], v[84:87], v[32:35]\nds_read_b128 v[80:83], v2 offset:12288\nv_mfma_f32_16x16x32_bf16 v[36:39], a[64:67], v[84:87], v[36:39]\nv_mfma_f32_16x16x32_bf16 v[40:43], v[64:67], v[84:87], v[40:43]\nv_mfma_f32_16x16x32_bf16 v[44:47], a[72:75], v[84:87], v[44:47]\ns_waitcnt lgkmcnt(3)\nv_mfma_f32_16x16x32_bf16 v[16:19], a[60:63], v[88:91], v[16:19]\n" \
"ds_read_b128 v[84:87], v2 offset:12544\nv_mfma_f32_16x16x32_bf16 v[20:23], a[68:71], v[88:91], v[20:23]\nds_read_b128 v[76:79], v1 offset:5120\nv_mfma_f32_16x16x32_bf16 v[24:27], v[68:71], v[88:91], v[24:27]\nv_mfma_f32_16x16x32_bf16 v[28:31], a[76:79], v[88:91], v[28:31]\ns_waitcnt lgkmcnt(4)\nv_mfma_f32_16x16x32_bf16 v[32:35], a[60:63], v[92:95], v[32:35]\nds_read_b128 v[88:91], v2 offset:14336\n" \
"v_mfma_f32_16x16x32_bf16 v[36:39], a[68:71], v[92:95], v[36:39]\nv_mfma_f32_16x16x32_bf16 v[40:43], v[68:71], v[92:95], v[40:43]\nv_mfma_f32_16x16x32_bf16 v[44:47], a[76:79], v[92:95], v[44:47]\ns_waitcnt vmcnt(0)\ns_branch .Lqk_pj%=\n.Lqk_slow%=:\ns_cmp_ge_i32 s40, 63\ns_cbranch_scc1 .Lqk_end%=\n" \
".Lqk_fetch0%=:\nbuffer_load_dword v16, v12, s[36:39], s56 offen sc1\ns_waitcnt vmcnt(0)\nv_cmp_ge_i32 vcc, s40, v16\ns_cbranch_vccz .Lqk_fok%=\ns_add_u32 s43, s43, 1\ns_cmp_lt_u32 s43, 262144\ns_cbranch_scc1 .Lqk_fetch0%=\n" \
"s_branch .Lqk_abort%=\n.Lqk_fok%=:\ns_add_u32 s44, s40, 1\ns_lshl_b32 s44, s44, 17\ns_add_u32 s44, s44, %[xn]\ns_lshr_b32 s45, s44, 8\ns_and_b32 s45, s45, 0x2000\ns_xor_b32 s41, s44, s45\n" \
"s_add_u32 m0, %[l0], 0\ns_add_u32 s60, s41, 0\nbuffer_load_dwordx4 v0, s[36:39], s60 offen offset:0 sc1 lds\nbuffer_load_dwordx4 v0, s[36:39], s60 offen offset:1024 sc1 lds\nbuffer_load_dwordx4 v0, s[36:39], s60 offen offset:2048 sc1 lds\nbuffer_load_dwordx4 v0, s[36:39], s60 offen offset:3072 sc1 lds\ns_add_u32 m0, %[l0], 4096\ns_add_u32 s60, s41, 4096\n" \
"buffer_load_dwordx4 v0, s[36:39], s60 offen offset:0 sc1 lds\nbuffer_load_dwordx4 v0, s[36:39], s60 offen offset:1024 sc1 lds\nbuffer_load_dwordx4 v0, s[36:39], s60 offen offset:2048 sc1 lds\nbuffer_load_dwordx4 v0, s[36:39], s60 offen offset:3072 sc1 lds\ns_add_u32 m0, %[l0], 8192\ns_add_u32 s60, s41, 16384\nbuffer_load_dwordx4 v0, s[36:39], s60 offen offset:0 sc1 lds\nbuffer_load_dwordx4 v0, s[36:39], s60 offen offset:1024 sc1 lds\n" \
"buffer_load_dwordx4 v0, s[36:39], s60 offen offset:2048 sc1 lds\nbuffer_load_dwordx4 v0, s[36:39], s60 offen offset:3072 sc1 lds\ns_add_u32 m0, %[l0], 12288\ns_add_u32 s60, s41, 20480\nbuffer_load_dwordx4 v0, s[36:39], s60 offen offset:0 sc1 lds\nbuffer_load_dwordx4 v0, s[36:39], s60 offen offset:1024 sc1 lds\nbuffer_load_dwordx4 v0, s[36:39], s60 offen offset:2048 sc1 lds\nbuffer_load_dwordx4 v0, s[36:39], s60 offen offset:3072 sc1 lds\n" \
"s_add_u32 m0, %[l0], 16384\ns_add_u32 s60, s41, 32768\nbuffer_load_dwordx4 v0, s[36:39], s60 offen offset:0 sc1 lds\nbuffer_load_dwordx4 v0, s[36:39], s60 offen offset:1024 sc1 lds\nbuffer_load_dwordx4 v0, s[36:39], s60 offen offset:2048 sc1 lds\nbuffer_load_dwordx4 v0, s[36:39], s60 offen offset:3072 sc1 lds\ns_add_u32 m0, %[l0], 20480\ns_add_u32 s60, s41, 36864\n" \
"buffer_load_dwordx4 v0, s[36:39], s60 offen offset:0 sc1 lds\nbuffer_load_dwordx4 v0, s[36:39], s60 offen offset:1024 sc1 lds\nbuffer_load_dwordx4 v0, s[36:39], s60 offen offset:2048 sc1 lds\nbuffer_load_dwordx4 v0, s[36:39], s60 offen offset:3072 sc1 lds\ns_add_u32 m0, %[l0], 24576\ns_add_u32 s60, s41, 49152\nbuffer_load_dwordx4 v0, s[36:39], s60 offen offset:0 sc1 lds\nbuffer_load_dwordx4 v0, s[36:39], s60 offen offset:1024 sc1 lds\n" \
"buffer_load_dwordx4 v0, s[36:39], s60 offen offset:2048 sc1 lds\nbuffer_load_dwordx4 v0, s[36:39], s60 offen offset:3072 sc1 lds\ns_add_u32 m0, %[l0], 28672\ns_add_u32 s60, s41, 53248\nbuffer_load_dwordx4 v0, s[36:39], s60 offen offset:0 sc1 lds\nbuffer_load_dwordx4 v0, s[36:39], s60 offen offset:1024 sc1 lds\nbuffer_load_dwordx4 v0, s[36:39], s60 offen offset:2048 sc1 lds\nbuffer_load_dwordx4 v0, s[36:39], s60 offen offset:3072 sc1 lds\n" \
"s_waitcnt vmcnt(0)\ns_branch .Lqk_pwait%=\n.Lqk_re3%=:\nv_max_u32 v17, v80, v82\nv_cmp_eq_u32 vcc, -1, v17\ns_cbranch_vccz .Lqk_sk2851%=\ns_mov_b64 exec, vcc\nbuffer_load_dwordx4 v[80:83], v3, s[36:39], s42 offen offset:0 sc1\n" \
"s_mov_b64 exec, -1\n.Lqk_sk2851%=:\nv_max_u32 v17, v84, v86\nv_cmp_eq_u32 vcc, -1, v17\ns_cbranch_vccz .Lqk_sk2858%=\ns_mov_b64 exec, vcc\nbuffer_load_dwordx4 v[84:87], v3, s[36:39], s42 offen offset:1024 sc1\ns_mov_b64 exec, -1\n" \
".Lqk_sk2858%=:\nv_max_u32 v17, v88, v90\nv_cmp_eq_u32 vcc, -1, v17\ns_cbranch_vccz .Lqk_sk2865%=\ns_mov_b64 exec, vcc\nbuffer_load_dwordx4 v[88:91], v3, s[36:39], s42 offen offset:2048 sc1\ns_mov_b64 exec, -1\n.Lqk_sk2865%=:\n" \
"v_max_u32 v17, v92, v94\nv_cmp_eq_u32 vcc, -1, v17\ns_cbranch_vccz .Lqk_sk2872%=\ns_mov_b64 exec, vcc\nbuffer_load_dwordx4 v[92:95], v3, s[36:39], s42 offen offset:3072 sc1\ns_mov_b64 exec, -1\n.Lqk_sk2872%=:\nbuffer_load_dword v16, v12, s[36:39], s56 offen sc1\n" \
"v_mov_b64 v[48:49], 0\nv_mov_b64 v[50:51], 0\nv_mov_b64 v[52:53], 0\nv_mov_b64 v[54:55], 0\nv_mov_b64 v[56:57], 0\nv_mov_b64 v[58:59], 0\nv_mov_b64 v[60:61], 0\nv_mov_b64 v[62:63], 0\n" \
"s_add_u32 s43, s43, 1\ns_cmp_lt_u32 s43, 262144\ns_cbranch_scc1 .Lqk_poll3%=\ns_branch .Lqk_abort%=\n.Lqk_abort%=:\ns_mov_b64 exec, -1\nv_mov_b32 v16, 0\ns_movk_i32 s44, 6400\n" \
"buffer_store_dword v16, off, s[36:39], s44 sc1\n.Lqk_end%=:\ns_waitcnt vmcnt(0) lgkmcnt(0)\n" \
""
#define VU_ASM \
"v_mbcnt_lo_u32_b32 v255, -1, 0\nv_mbcnt_hi_u32_b32 v255, -1, v255\nv_lshlrev_b32 v255, 2, v255\nv_add_u32 v255, %[tb], v255\nds_read_b32 v0, v255 offset:0\nds_read_b32 v1, v255 offset:256\nds_read_b32 v2, v255 offset:512\nds_read_b32 v3, v255 offset:768\n" \
"ds_read_b32 v4, v255 offset:1024\nds_read_b32 v5, v255 offset:1280\nds_read_b32 v6, v255 offset:1536\nds_read_b32 v7, v255 offset:1792\nds_read_b32 v8, v255 offset:2048\nds_read_b32 v9, v255 offset:2304\nds_read_b32 v10, v255 offset:2560\nds_read_b32 v11, v255 offset:2816\n" \
"ds_read_b32 v90, v255 offset:3072\nds_read_b32 v91, v255 offset:3328\nds_read_b32 v92, v255 offset:3584\nds_read_b32 v93, v255 offset:3840\nds_read_b32 v94, v255 offset:4096\nds_read_b32 v95, v255 offset:4352\ns_waitcnt lgkmcnt(0)\ns_mov_b64 s[36:37], %[ws]\n" \
"s_mov_b32 s38, %[sz]\ns_mov_b32 s39, 0x20000\ns_mov_b32 s45, 2096896\nbuffer_load_dword v15, off, s[36:39], s45 sc1\ns_add_u32 m0, %[lw], 16384\ns_add_u32 s44, %[wo], 0\nbuffer_load_dwordx4 v90, %[rw], s44 offen nt lds\ns_add_u32 m0, %[lw], 17408\n" \
"s_add_u32 s44, %[wo], 65536\nbuffer_load_dwordx4 v90, %[rw], s44 offen nt lds\ns_add_u32 m0, %[lw], 18432\ns_add_u32 s44, %[wo], 131072\nbuffer_load_dwordx4 v90, %[rw], s44 offen nt lds\ns_add_u32 m0, %[lw], 19456\ns_add_u32 s44, %[wo], 196608\nbuffer_load_dwordx4 v90, %[rw], s44 offen nt lds\n" \
"s_add_u32 m0, %[lw], 20480\ns_add_u32 s44, %[wo], 524288\nbuffer_load_dwordx4 v90, %[rw], s44 offen nt lds\ns_add_u32 m0, %[lw], 21504\ns_add_u32 s44, %[wo], 589824\nbuffer_load_dwordx4 v90, %[rw], s44 offen nt lds\ns_add_u32 m0, %[lw], 22528\ns_add_u32 s44, %[wo], 655360\n" \
"buffer_load_dwordx4 v90, %[rw], s44 offen nt lds\ns_add_u32 m0, %[lw], 23552\ns_add_u32 s44, %[wo], 720896\nbuffer_load_dwordx4 v90, %[rw], s44 offen nt lds\ns_add_u32 m0, %[tb], 0\ns_add_u32 s44, %[wo], 1048576\nbuffer_load_dwordx4 v90, %[rw], s44 offen nt lds\ns_add_u32 m0, %[tb], 1024\n" \
"s_add_u32 s44, %[wo], 1114112\nbuffer_load_dwordx4 v90, %[rw], s44 offen nt lds\ns_add_u32 m0, %[tb], 2048\ns_add_u32 s44, %[wo], 1179648\nbuffer_load_dwordx4 v90, %[rw], s44 offen nt lds\ns_add_u32 m0, %[tb], 3072\ns_add_u32 s44, %[wo], 1245184\nbuffer_load_dwordx4 v90, %[rw], s44 offen nt lds\n" \
"s_add_u32 m0, %[tb], 4096\ns_add_u32 s44, %[wo], 1572864\nbuffer_load_dwordx4 v90, %[rw], s44 offen nt lds\ns_add_u32 m0, %[tb], 5120\ns_add_u32 s44, %[wo], 1638400\nbuffer_load_dwordx4 v90, %[rw], s44 offen nt lds\ns_add_u32 m0, %[tb], 6144\ns_add_u32 s44, %[wo], 1703936\n" \
"buffer_load_dwordx4 v90, %[rw], s44 offen nt lds\ns_add_u32 m0, %[tb], 7168\ns_add_u32 s44, %[wo], 1769472\nbuffer_load_dwordx4 v90, %[rw], s44 offen nt lds\ns_add_u32 m0, %[tb], 8192\ns_add_u32 s44, %[wo], 2097152\nbuffer_load_dwordx4 v90, %[rw], s44 offen nt lds\ns_add_u32 m0, %[tb], 9216\n" \
"s_add_u32 s44, %[wo], 2162688\nbuffer_load_dwordx4 v90, %[rw], s44 offen nt lds\ns_add_u32 m0, %[tb], 10240\ns_add_u32 s44, %[wo], 2228224\nbuffer_load_dwordx4 v90, %[rw], s44 offen nt lds\ns_add_u32 m0, %[tb], 11264\ns_add_u32 s44, %[wo], 2293760\nbuffer_load_dwordx4 v90, %[rw], s44 offen nt lds\n" \
"s_add_u32 m0, %[lw], 0\ns_add_u32 s44, %[wo], 2621440\nbuffer_load_dwordx4 v90, %[rw], s44 offen nt lds\ns_add_u32 m0, %[lw], 1024\ns_add_u32 s44, %[wo], 2686976\nbuffer_load_dwordx4 v90, %[rw], s44 offen nt lds\ns_add_u32 m0, %[lw], 2048\ns_add_u32 s44, %[wo], 2752512\n" \
"buffer_load_dwordx4 v90, %[rw], s44 offen nt lds\ns_add_u32 m0, %[lw], 3072\ns_add_u32 s44, %[wo], 2818048\nbuffer_load_dwordx4 v90, %[rw], s44 offen nt lds\ns_add_u32 m0, %[lw], 4096\ns_add_u32 s44, %[wo], 3145728\nbuffer_load_dwordx4 v90, %[rw], s44 offen nt lds\ns_add_u32 m0, %[lw], 5120\n" \
"s_add_u32 s44, %[wo], 3211264\nbuffer_load_dwordx4 v90, %[rw], s44 offen nt lds\ns_add_u32 m0, %[lw], 6144\ns_add_u32 s44, %[wo], 3276800\nbuffer_load_dwordx4 v90, %[rw], s44 offen nt lds\ns_add_u32 m0, %[lw], 7168\ns_add_u32 s44, %[wo], 3342336\nbuffer_load_dwordx4 v90, %[rw], s44 offen nt lds\n" \
"s_add_u32 m0, %[lw], 8192\ns_add_u32 s44, %[wo], 3670016\nbuffer_load_dwordx4 v90, %[rw], s44 offen nt lds\ns_add_u32 m0, %[lw], 9216\ns_add_u32 s44, %[wo], 3735552\nbuffer_load_dwordx4 v90, %[rw], s44 offen nt lds\ns_add_u32 m0, %[lw], 10240\ns_add_u32 s44, %[wo], 3801088\n" \
"buffer_load_dwordx4 v90, %[rw], s44 offen nt lds\ns_add_u32 m0, %[lw], 11264\ns_add_u32 s44, %[wo], 3866624\nbuffer_load_dwordx4 v90, %[rw], s44 offen nt lds\ns_waitcnt vmcnt(28)\ns_barrier\ns_add_u32 m0, %[lw], 12288\ns_add_u32 s44, %[wo], 4194304\n" \
"buffer_load_dwordx4 v90, %[rw], s44 offen nt lds\ns_add_u32 m0, %[lw], 13312\ns_add_u32 s44, %[wo], 4259840\nbuffer_load_dwordx4 v90, %[rw], s44 offen nt lds\ns_add_u32 m0, %[lw], 14336\ns_add_u32 s44, %[wo], 4325376\nbuffer_load_dwordx4 v90, %[rw], s44 offen nt lds\ns_add_u32 m0, %[lw], 15360\n" \
"s_add_u32 s44, %[wo], 4390912\nbuffer_load_dwordx4 v90, %[rw], s44 offen nt lds\nds_read_b64_tr_b16 a[0:1], v93 offset:16384\nds_read_b64_tr_b16 a[2:3], v93 offset:16896\nds_read_b64_tr_b16 a[16:17], v93 offset:16392\nds_read_b64_tr_b16 a[18:19], v93 offset:16904\nds_read_b64_tr_b16 a[4:5], v93 offset:40960\nds_read_b64_tr_b16 a[6:7], v93 offset:41472\n" \
"ds_read_b64_tr_b16 a[20:21], v93 offset:40968\nds_read_b64_tr_b16 a[22:23], v93 offset:41480\ns_waitcnt lgkmcnt(0)\ns_waitcnt vmcnt(28)\ns_barrier\ns_add_u32 m0, %[lw], 16384\ns_add_u32 s44, %[wo], 4718592\nbuffer_load_dwordx4 v90, %[rw], s44 offen nt lds\n" \
"s_add_u32 m0, %[lw], 17408\ns_add_u32 s44, %[wo], 4784128\nbuffer_load_dwordx4 v90, %[rw], s44 offen nt lds\ns_add_u32 m0, %[lw], 18432\ns_add_u32 s44, %[wo], 4849664\nbuffer_load_dwordx4 v90, %[rw], s44 offen nt lds\ns_add_u32 m0, %[lw], 19456\ns_add_u32 s44, %[wo], 4915200\n" \
"buffer_load_dwordx4 v90, %[rw], s44 offen nt lds\nds_read_b64_tr_b16 a[24:25], v93 offset:20480\nds_read_b64_tr_b16 a[26:27], v93 offset:20992\nds_read_b64_tr_b16 a[40:41], v93 offset:20488\nds_read_b64_tr_b16 a[42:43], v93 offset:21000\nds_read_b64_tr_b16 a[28:29], v93 offset:45056\nds_read_b64_tr_b16 a[30:31], v93 offset:45568\nds_read_b64_tr_b16 a[44:45], v93 offset:45064\n" \
"ds_read_b64_tr_b16 a[46:47], v93 offset:45576\ns_waitcnt lgkmcnt(0)\ns_waitcnt vmcnt(28)\ns_barrier\ns_add_u32 m0, %[lw], 20480\ns_add_u32 s44, %[wo], 5242880\nbuffer_load_dwordx4 v90, %[rw], s44 offen nt lds\ns_add_u32 m0, %[lw], 21504\n" \
"s_add_u32 s44, %[wo], 5308416\nbuffer_load_dwordx4 v90, %[rw], s44 offen nt lds\ns_add_u32 m0, %[lw], 22528\ns_add_u32 s44, %[wo], 5373952\nbuffer_load_dwordx4 v90, %[rw], s44 offen nt lds\ns_add_u32 m0, %[lw], 23552\ns_add_u32 s44, %[wo], 5439488\nbuffer_load_dwordx4 v90, %[rw], s44 offen nt lds\n" \
"ds_read_b64_tr_b16 v[84:85], v92 offset:0\nds_read_b64_tr_b16 v[86:87], v92 offset:512\nds_read_b64_tr_b16 a[64:65], v92 offset:8\nds_read_b64_tr_b16 a[66:67], v92 offset:520\nds_read_b64_tr_b16 v[80:81], v92 offset:12288\nds_read_b64_tr_b16 v[82:83], v92 offset:12800\nds_read_b64_tr_b16 a[68:69], v92 offset:12296\nds_read_b64_tr_b16 a[70:71], v92 offset:12808\n" \
"s_waitcnt lgkmcnt(0)\ns_waitcnt vmcnt(28)\ns_barrier\ns_add_u32 m0, %[tb], 0\ns_add_u32 s44, %[wo], 5767168\nbuffer_load_dwordx4 v90, %[rw], s44 offen nt lds\ns_add_u32 m0, %[tb], 1024\ns_add_u32 s44, %[wo], 5832704\n" \
"buffer_load_dwordx4 v90, %[rw], s44 offen nt lds\ns_add_u32 m0, %[tb], 2048\ns_add_u32 s44, %[wo], 5898240\nbuffer_load_dwordx4 v90, %[rw], s44 offen nt lds\ns_add_u32 m0, %[tb], 3072\ns_add_u32 s44, %[wo], 5963776\nbuffer_load_dwordx4 v90, %[rw], s44 offen nt lds\nds_read_b64_tr_b16 a[80:81], v92 offset:4096\n" \
"ds_read_b64_tr_b16 a[82:83], v92 offset:4608\nds_read_b64_tr_b16 a[88:89], v92 offset:4104\nds_read_b64_tr_b16 a[90:91], v92 offset:4616\nds_read_b64_tr_b16 a[84:85], v92 offset:16384\nds_read_b64_tr_b16 a[86:87], v92 offset:16896\nds_read_b64_tr_b16 a[92:93], v92 offset:16392\nds_read_b64_tr_b16 a[94:95], v92 offset:16904\ns_waitcnt lgkmcnt(0)\n" \
"s_waitcnt vmcnt(28)\ns_barrier\ns_add_u32 m0, %[tb], 4096\ns_add_u32 s44, %[wo], 6291456\nbuffer_load_dwordx4 v90, %[rw], s44 offen nt lds\ns_add_u32 m0, %[tb], 5120\ns_add_u32 s44, %[wo], 6356992\nbuffer_load_dwordx4 v90, %[rw], s44 offen nt lds\n" \
"s_add_u32 m0, %[tb], 6144\ns_add_u32 s44, %[wo], 6422528\nbuffer_load_dwordx4 v90, %[rw], s44 offen nt lds\ns_add_u32 m0, %[tb], 7168\ns_add_u32 s44, %[wo], 6488064\nbuffer_load_dwordx4 v90, %[rw], s44 offen nt lds\nds_read_b64_tr_b16 a[104:105], v92 offset:8192\nds_read_b64_tr_b16 a[106:107], v92 offset:8704\n" \
"ds_read_b64_tr_b16 a[120:121], v92 offset:8200\nds_read_b64_tr_b16 a[122:123], v92 offset:8712\nds_read_b64_tr_b16 a[108:109], v92 offset:20480\nds_read_b64_tr_b16 a[110:111], v92 offset:20992\nds_read_b64_tr_b16 a[124:125], v92 offset:20488\nds_read_b64_tr_b16 a[126:127], v92 offset:21000\ns_waitcnt lgkmcnt(0)\ns_waitcnt vmcnt(28)\n" \
"s_barrier\ns_add_u32 m0, %[tb], 8192\ns_add_u32 s44, %[wo], 6815744\nbuffer_load_dwordx4 v90, %[rw], s44 offen nt lds\ns_add_u32 m0, %[tb], 9216\ns_add_u32 s44, %[wo], 6881280\nbuffer_load_dwordx4 v90, %[rw], s44 offen nt lds\ns_add_u32 m0, %[tb], 10240\n" \
"s_add_u32 s44, %[wo], 6946816\nbuffer_load_dwordx4 v90, %[rw], s44 offen nt lds\ns_add_u32 m0, %[tb], 11264\ns_add_u32 s44, %[wo], 7012352\nbuffer_load_dwordx4 v90, %[rw], s44 offen nt lds\nds_read_b64_tr_b16 a[128:129], v93 offset:0\nds_read_b64_tr_b16 a[130:131], v93 offset:512\nds_read_b64_tr_b16 v[76:77], v93 offset:8\n" \
"ds_read_b64_tr_b16 v[78:79], v93 offset:520\nds_read_b64_tr_b16 a[132:133], v93 offset:24576\nds_read_b64_tr_b16 a[134:135], v93 offset:25088\nds_read_b64_tr_b16 v[72:73], v93 offset:24584\nds_read_b64_tr_b16 v[74:75], v93 offset:25096\ns_waitcnt lgkmcnt(0)\ns_waitcnt vmcnt(28)\ns_barrier\n" \
"s_add_u32 m0, %[lw], 0\ns_add_u32 s44, %[wo], 7340032\nbuffer_load_dwordx4 v90, %[rw], s44 offen nt lds\ns_add_u32 m0, %[lw], 1024\ns_add_u32 s44, %[wo], 7405568\nbuffer_load_dwordx4 v90, %[rw], s44 offen nt lds\ns_add_u32 m0, %[lw], 2048\ns_add_u32 s44, %[wo], 7471104\n" \
"buffer_load_dwordx4 v90, %[rw], s44 offen nt lds\ns_add_u32 m0, %[lw], 3072\ns_add_u32 s44, %[wo], 7536640\nbuffer_load_dwordx4 v90, %[rw], s44 offen nt lds\nds_read_b64_tr_b16 a[152:153], v93 offset:4096\nds_read_b64_tr_b16 a[154:155], v93 offset:4608\nds_read_b64_tr_b16 a[168:169], v93 offset:4104\nds_read_b64_tr_b16 a[170:171], v93 offset:4616\n" \
"ds_read_b64_tr_b16 a[156:157], v93 offset:28672\nds_read_b64_tr_b16 a[158:159], v93 offset:29184\nds_read_b64_tr_b16 a[172:173], v93 offset:28680\nds_read_b64_tr_b16 a[174:175], v93 offset:29192\ns_waitcnt lgkmcnt(0)\ns_waitcnt vmcnt(28)\ns_barrier\ns_add_u32 m0, %[lw], 4096\n" \
"s_add_u32 s44, %[wo], 7864320\nbuffer_load_dwordx4 v90, %[rw], s44 offen nt lds\ns_add_u32 m0, %[lw], 5120\ns_add_u32 s44, %[wo], 7929856\nbuffer_load_dwordx4 v90, %[rw], s44 offen nt lds\ns_add_u32 m0, %[lw], 6144\ns_add_u32 s44, %[wo], 7995392\nbuffer_load_dwordx4 v90, %[rw], s44 offen nt lds\n" \
"s_add_u32 m0, %[lw], 7168\ns_add_u32 s44, %[wo], 8060928\nbuffer_load_dwordx4 v90, %[rw], s44 offen nt lds\nds_read_b64_tr_b16 a[184:185], v93 offset:8192\nds_read_b64_tr_b16 a[186:187], v93 offset:8704\nds_read_b64_tr_b16 a[192:193], v93 offset:8200\nds_read_b64_tr_b16 a[194:195], v93 offset:8712\nds_read_b64_tr_b16 a[188:189], v93 offset:32768\n" \
"ds_read_b64_tr_b16 a[190:191], v93 offset:33280\nds_read_b64_tr_b16 a[196:197], v93 offset:32776\nds_read_b64_tr_b16 a[198:199], v93 offset:33288\ns_waitcnt lgkmcnt(0)\ns_waitcnt vmcnt(28)\ns_barrier\ns_add_u32 m0, %[lw], 8192\ns_add_u32 s44, %[w2], 0\n" \
"buffer_load_dwordx4 v91, %[ro], s44 offen nt lds\ns_add_u32 m0, %[lw], 9216\ns_add_u32 s44, %[w2], 32768\nbuffer_load_dwordx4 v91, %[ro], s44 offen nt lds\ns_add_u32 m0, %[lw], 10240\ns_add_u32 s44, %[w2], 65536\nbuffer_load_dwordx4 v91, %[ro], s44 offen nt lds\ns_add_u32 m0, %[lw], 11264\n" \
"s_add_u32 s44, %[w2], 98304\nbuffer_load_dwordx4 v91, %[ro], s44 offen nt lds\nds_read_b64_tr_b16 v[68:69], v93 offset:12288\nds_read_b64_tr_b16 v[70:71], v93 offset:12800\nds_read_b64_tr_b16 a[216:217], v93 offset:12296\nds_read_b64_tr_b16 a[218:219], v93 offset:12808\nds_read_b64_tr_b16 v[64:65], v93 offset:36864\nds_read_b64_tr_b16 v[66:67], v93 offset:37376\n" \
"ds_read_b64_tr_b16 a[220:221], v93 offset:36872\nds_read_b64_tr_b16 a[222:223], v93 offset:37384\ns_waitcnt lgkmcnt(0)\ns_waitcnt vmcnt(28)\ns_barrier\ns_add_u32 m0, %[lw], 12288\ns_add_u32 s44, %[w2], 128\nbuffer_load_dwordx4 v91, %[ro], s44 offen nt lds\n" \
"s_add_u32 m0, %[lw], 13312\ns_add_u32 s44, %[w2], 32896\nbuffer_load_dwordx4 v91, %[ro], s44 offen nt lds\ns_add_u32 m0, %[lw], 14336\ns_add_u32 s44, %[w2], 65664\nbuffer_load_dwordx4 v91, %[ro], s44 offen nt lds\ns_add_u32 m0, %[lw], 15360\ns_add_u32 s44, %[w2], 98432\n" \
"buffer_load_dwordx4 v91, %[ro], s44 offen nt lds\nds_read_b64_tr_b16 a[224:225], v93 offset:16384\nds_read_b64_tr_b16 a[226:227], v93 offset:16896\nds_read_b64_tr_b16 a[240:241], v93 offset:16392\nds_read_b64_tr_b16 a[242:243], v93 offset:16904\nds_read_b64_tr_b16 a[228:229], v93 offset:40960\nds_read_b64_tr_b16 a[230:231], v93 offset:41472\nds_read_b64_tr_b16 a[244:245], v93 offset:40968\n" \
"ds_read_b64_tr_b16 a[246:247], v93 offset:41480\ns_waitcnt lgkmcnt(0)\ns_waitcnt vmcnt(28)\ns_barrier\ns_add_u32 m0, %[lw], 16384\ns_add_u32 s44, %[w2], 256\nbuffer_load_dwordx4 v91, %[ro], s44 offen nt lds\ns_add_u32 m0, %[lw], 17408\n" \
"s_add_u32 s44, %[w2], 33024\nbuffer_load_dwordx4 v91, %[ro], s44 offen nt lds\ns_add_u32 m0, %[lw], 18432\ns_add_u32 s44, %[w2], 65792\nbuffer_load_dwordx4 v91, %[ro], s44 offen nt lds\ns_add_u32 m0, %[lw], 19456\ns_add_u32 s44, %[w2], 98560\nbuffer_load_dwordx4 v91, %[ro], s44 offen nt lds\n" \
"ds_read_b64_tr_b16 v[96:97], v93 offset:20480\nds_read_b64_tr_b16 v[98:99], v93 offset:20992\nds_read_b64_tr_b16 v[112:113], v93 offset:20488\nds_read_b64_tr_b16 v[114:115], v93 offset:21000\nds_read_b64_tr_b16 v[100:101], v93 offset:45056\nds_read_b64_tr_b16 v[102:103], v93 offset:45568\nds_read_b64_tr_b16 v[116:117], v93 offset:45064\nds_read_b64_tr_b16 v[118:119], v93 offset:45576\n" \
"s_waitcnt lgkmcnt(0)\ns_waitcnt vmcnt(28)\ns_barrier\ns_add_u32 m0, %[lw], 20480\ns_add_u32 s44, %[w2], 384\nbuffer_load_dwordx4 v91, %[ro], s44 offen nt lds\ns_add_u32 m0, %[lw], 21504\ns_add_u32 s44, %[w2], 33152\n" \
"buffer_load_dwordx4 v91, %[ro], s44 offen nt lds\ns_add_u32 m0, %[lw], 22528\ns_add_u32 s44, %[w2], 65920\nbuffer_load_dwordx4 v91, %[ro], s44 offen nt lds\ns_add_u32 m0, %[lw], 23552\ns_add_u32 s44, %[w2], 98688\nbuffer_load_dwordx4 v91, %[ro], s44 offen nt lds\nds_read_b64_tr_b16 v[128:129], v92 offset:0\n" \
"ds_read_b64_tr_b16 v[130:131], v92 offset:512\nds_read_b64_tr_b16 v[60:61], v92 offset:8\nds_read_b64_tr_b16 v[62:63], v92 offset:520\nds_read_b64_tr_b16 v[132:133], v92 offset:12288\nds_read_b64_tr_b16 v[134:135], v92 offset:12800\nds_read_b64_tr_b16 v[56:57], v92 offset:12296\nds_read_b64_tr_b16 v[58:59], v92 offset:12808\ns_waitcnt lgkmcnt(0)\n" \
"s_waitcnt vmcnt(28)\ns_barrier\ns_add_u32 m0, %[tb], 0\ns_add_u32 s44, %[w2], 139264\nbuffer_load_dwordx4 v91, %[ro], s44 offen nt lds\ns_add_u32 m0, %[tb], 1024\ns_add_u32 s44, %[w2], 172032\nbuffer_load_dwordx4 v91, %[ro], s44 offen nt lds\n" \
"s_add_u32 m0, %[tb], 2048\ns_add_u32 s44, %[w2], 204800\nbuffer_load_dwordx4 v91, %[ro], s44 offen nt lds\ns_add_u32 m0, %[tb], 3072\ns_add_u32 s44, %[w2], 237568\nbuffer_load_dwordx4 v91, %[ro], s44 offen nt lds\nds_read_b64_tr_b16 v[144:145], v92 offset:4096\nds_read_b64_tr_b16 v[146:147], v92 offset:4608\n" \
"ds_read_b64_tr_b16 v[160:161], v92 offset:4104\nds_read_b64_tr_b16 v[162:163], v92 offset:4616\nds_read_b64_tr_b16 v[148:149], v92 offset:16384\nds_read_b64_tr_b16 v[150:151], v92 offset:16896\nds_read_b64_tr_b16 v[164:165], v92 offset:16392\nds_read_b64_tr_b16 v[166:167], v92 offset:16904\ns_waitcnt lgkmcnt(0)\ns_waitcnt vmcnt(28)\n" \
"s_barrier\ns_add_u32 m0, %[tb], 4096\ns_add_u32 s44, %[w2], 139392\nbuffer_load_dwordx4 v91, %[ro], s44 offen nt lds\ns_add_u32 m0, %[tb], 5120\ns_add_u32 s44, %[w2], 172160\nbuffer_load_dwordx4 v91, %[ro], s44 offen nt lds\ns_add_u32 m0, %[tb], 6144\n" \
"s_add_u32 s44, %[w2], 204928\nbuffer_load_dwordx4 v91, %[ro], s44 offen nt lds\ns_add_u32 m0, %[tb], 7168\ns_add_u32 s44, %[w2], 237696\nbuffer_load_dwordx4 v91, %[ro], s44 offen nt lds\nds_read_b64_tr_b16 v[168:169], v92 offset:8192\nds_read_b64_tr_b16 v[170:171], v92 offset:8704\nds_read_b64_tr_b16 v[184:185], v92 offset:8200\n" \
"ds_read_b64_tr_b16 v[186:187], v92 offset:8712\nds_read_b64_tr_b16 v[172:173], v92 offset:20480\nds_read_b64_tr_b16 v[174:175], v92 offset:20992\nds_read_b64_tr_b16 v[188:189], v92 offset:20488\nds_read_b64_tr_b16 v[190:191], v92 offset:21000\ns_waitcnt lgkmcnt(0)\ns_waitcnt vmcnt(28)\ns_barrier\n" \
"s_add_u32 m0, %[tb], 8192\ns_add_u32 s44, %[w2], 139520\nbuffer_load_dwordx4 v91, %[ro], s44 offen nt lds\ns_add_u32 m0, %[tb], 9216\ns_add_u32 s44, %[w2], 172288\nbuffer_load_dwordx4 v91, %[ro], s44 offen nt lds\ns_add_u32 m0, %[tb], 10240\ns_add_u32 s44, %[w2], 205056\n" \
"buffer_load_dwordx4 v91, %[ro], s44 offen nt lds\ns_add_u32 m0, %[tb], 11264\ns_add_u32 s44, %[w2], 237824\nbuffer_load_dwordx4 v91, %[ro], s44 offen nt lds\nds_read_b64_tr_b16 v[200:201], v93 offset:0\nds_read_b64_tr_b16 v[202:203], v93 offset:512\nds_read_b64_tr_b16 v[216:217], v93 offset:8\nds_read_b64_tr_b16 v[218:219], v93 offset:520\n" \
"ds_read_b64_tr_b16 v[204:205], v93 offset:24576\nds_read_b64_tr_b16 v[206:207], v93 offset:25088\nds_read_b64_tr_b16 v[220:221], v93 offset:24584\nds_read_b64_tr_b16 v[222:223], v93 offset:25096\ns_waitcnt lgkmcnt(0)\ns_waitcnt vmcnt(28)\ns_barrier\ns_add_u32 m0, %[lw], 0\n" \
"s_add_u32 s44, %[w2], 139648\nbuffer_load_dwordx4 v91, %[ro], s44 offen nt lds\ns_add_u32 m0, %[lw], 1024\ns_add_u32 s44, %[w2], 172416\nbuffer_load_dwordx4 v91, %[ro], s44 offen nt lds\ns_add_u32 m0, %[lw], 2048\ns_add_u32 s44, %[w2], 205184\nbuffer_load_dwordx4 v91, %[ro], s44 offen nt lds\n" \
"s_add_u32 m0, %[lw], 3072\ns_add_u32 s44, %[w2], 237952\nbuffer_load_dwordx4 v91, %[ro], s44 offen nt lds\nds_read_b64_tr_b16 v[232:233], v93 offset:4096\nds_read_b64_tr_b16 v[234:235], v93 offset:4608\nds_read_b64_tr_b16 v[240:241], v93 offset:4104\nds_read_b64_tr_b16 v[242:243], v93 offset:4616\nds_read_b64_tr_b16 v[236:237], v93 offset:28672\n" \
"ds_read_b64_tr_b16 v[238:239], v93 offset:29184\nds_read_b64_tr_b16 v[244:245], v93 offset:28680\nds_read_b64_tr_b16 v[246:247], v93 offset:29192\ns_waitcnt lgkmcnt(0)\ns_waitcnt vmcnt(28)\ns_barrier\ns_add_u32 m0, %[lw], 4096\ns_add_u32 s44, %[w2], 131072\n" \
"buffer_load_dwordx4 v91, %[ro], s44 offen nt lds\ns_add_u32 m0, %[lw], 5120\ns_add_u32 s44, %[w2], 163840\nbuffer_load_dwordx4 v91, %[ro], s44 offen nt lds\ns_add_u32 m0, %[lw], 6144\ns_add_u32 s44, %[w2], 196608\nbuffer_load_dwordx4 v91, %[ro], s44 offen nt lds\ns_add_u32 m0, %[lw], 7168\n" \
"s_add_u32 s44, %[w2], 229376\nbuffer_load_dwordx4 v91, %[ro], s44 offen nt lds\nds_read_b64_tr_b16 a[8:9], v95 offset:8192\nds_read_b64_tr_b16 a[10:11], v95 offset:8448\nds_read_b64_tr_b16 a[12:13], v95 offset:10240\nds_read_b64_tr_b16 a[14:15], v95 offset:10496\nds_read_b64_tr_b16 a[208:209], v95 offset:8256\nds_read_b64_tr_b16 a[210:211], v95 offset:8512\n" \
"ds_read_b64_tr_b16 a[212:213], v95 offset:10304\nds_read_b64_tr_b16 a[214:215], v95 offset:10560\ns_waitcnt lgkmcnt(0)\ns_waitcnt vmcnt(28)\ns_barrier\ns_add_u32 m0, %[lw], 8192\ns_add_u32 s44, %[w2], 131200\nbuffer_load_dwordx4 v91, %[ro], s44 offen nt lds\n" \
"s_add_u32 m0, %[lw], 9216\ns_add_u32 s44, %[w2], 163968\nbuffer_load_dwordx4 v91, %[ro], s44 offen nt lds\ns_add_u32 m0, %[lw], 10240\ns_add_u32 s44, %[w2], 196736\nbuffer_load_dwordx4 v91, %[ro], s44 offen nt lds\ns_add_u32 m0, %[lw], 11264\ns_add_u32 s44, %[w2], 229504\n" \
"buffer_load_dwordx4 v91, %[ro], s44 offen nt lds\nds_read_b64_tr_b16 a[56:57], v95 offset:12288\nds_read_b64_tr_b16 a[58:59], v95 offset:12544\nds_read_b64_tr_b16 a[60:61], v95 offset:14336\nds_read_b64_tr_b16 a[62:63], v95 offset:14592\nds_read_b64_tr_b16 v[104:105], v95 offset:12352\nds_read_b64_tr_b16 v[106:107], v95 offset:12608\nds_read_b64_tr_b16 v[108:109], v95 offset:14400\n" \
"ds_read_b64_tr_b16 v[110:111], v95 offset:14656\ns_waitcnt lgkmcnt(0)\ns_waitcnt vmcnt(28)\ns_barrier\ns_add_u32 m0, %[lw], 12288\ns_add_u32 s44, %[w2], 131328\nbuffer_load_dwordx4 v91, %[ro], s44 offen nt lds\ns_add_u32 m0, %[lw], 13312\n" \
"s_add_u32 s44, %[w2], 164096\nbuffer_load_dwordx4 v91, %[ro], s44 offen nt lds\ns_add_u32 m0, %[lw], 14336\ns_add_u32 s44, %[w2], 196864\nbuffer_load_dwordx4 v91, %[ro], s44 offen nt lds\ns_add_u32 m0, %[lw], 15360\ns_add_u32 s44, %[w2], 229632\nbuffer_load_dwordx4 v91, %[ro], s44 offen nt lds\n" \
"ds_read_b64_tr_b16 a[112:113], v95 offset:16384\nds_read_b64_tr_b16 a[114:115], v95 offset:16640\nds_read_b64_tr_b16 a[116:117], v95 offset:18432\nds_read_b64_tr_b16 a[118:119], v95 offset:18688\nds_read_b64_tr_b16 v[152:153], v95 offset:16448\nds_read_b64_tr_b16 v[154:155], v95 offset:16704\nds_read_b64_tr_b16 v[156:157], v95 offset:18496\nds_read_b64_tr_b16 v[158:159], v95 offset:18752\n" \
"s_waitcnt lgkmcnt(0)\ns_waitcnt vmcnt(28)\ns_barrier\ns_add_u32 m0, %[lw], 16384\ns_add_u32 s44, %[w2], 131456\nbuffer_load_dwordx4 v91, %[ro], s44 offen nt lds\ns_add_u32 m0, %[lw], 17408\ns_add_u32 s44, %[w2], 164224\n" \
"buffer_load_dwordx4 v91, %[ro], s44 offen nt lds\ns_add_u32 m0, %[lw], 18432\ns_add_u32 s44, %[w2], 196992\nbuffer_load_dwordx4 v91, %[ro], s44 offen nt lds\ns_add_u32 m0, %[lw], 19456\ns_add_u32 s44, %[w2], 229760\nbuffer_load_dwordx4 v91, %[ro], s44 offen nt lds\nds_read_b64_tr_b16 a[160:161], v95 offset:20480\n" \
"ds_read_b64_tr_b16 a[162:163], v95 offset:20736\nds_read_b64_tr_b16 a[164:165], v95 offset:22528\nds_read_b64_tr_b16 a[166:167], v95 offset:22784\nds_read_b64_tr_b16 v[208:209], v95 offset:20544\nds_read_b64_tr_b16 v[210:211], v95 offset:20800\nds_read_b64_tr_b16 v[212:213], v95 offset:22592\nds_read_b64_tr_b16 v[214:215], v95 offset:22848\ns_waitcnt lgkmcnt(0)\n" \
"s_waitcnt vmcnt(28)\ns_barrier\ns_add_u32 m0, %[lw], 20480\ns_add_u32 s44, %[w2], 8192\nbuffer_load_dwordx4 v91, %[ro], s44 offen nt lds\ns_add_u32 m0, %[lw], 21504\ns_add_u32 s44, %[w2], 40960\nbuffer_load_dwordx4 v91, %[ro], s44 offen nt lds\n" \
"s_add_u32 m0, %[lw], 22528\ns_add_u32 s44, %[w2], 73728\nbuffer_load_dwordx4 v91, %[ro], s44 offen nt lds\ns_add_u32 m0, %[lw], 23552\ns_add_u32 s44, %[w2], 106496\nbuffer_load_dwordx4 v91, %[ro], s44 offen nt lds\nds_read_b64_tr_b16 a[48:49], v94 offset:0\nds_read_b64_tr_b16 a[50:51], v94 offset:256\n" \
"ds_read_b64_tr_b16 a[52:53], v94 offset:2048\nds_read_b64_tr_b16 a[54:55], v94 offset:2304\nds_read_b64_tr_b16 a[248:249], v94 offset:64\nds_read_b64_tr_b16 a[250:251], v94 offset:320\nds_read_b64_tr_b16 a[252:253], v94 offset:2112\nds_read_b64_tr_b16 a[254:255], v94 offset:2368\ns_waitcnt lgkmcnt(0)\ns_waitcnt vmcnt(28)\n" \
"s_barrier\ns_add_u32 m0, %[tb], 0\ns_add_u32 s44, %[w2], 8320\nbuffer_load_dwordx4 v91, %[ro], s44 offen nt lds\ns_add_u32 m0, %[tb], 1024\ns_add_u32 s44, %[w2], 41088\nbuffer_load_dwordx4 v91, %[ro], s44 offen nt lds\ns_add_u32 m0, %[tb], 2048\n" \
"s_add_u32 s44, %[w2], 73856\nbuffer_load_dwordx4 v91, %[ro], s44 offen nt lds\ns_add_u32 m0, %[tb], 3072\ns_add_u32 s44, %[w2], 106624\nbuffer_load_dwordx4 v91, %[ro], s44 offen nt lds\nds_read_b64_tr_b16 a[96:97], v94 offset:4096\nds_read_b64_tr_b16 a[98:99], v94 offset:4352\nds_read_b64_tr_b16 a[100:101], v94 offset:6144\n" \
"ds_read_b64_tr_b16 a[102:103], v94 offset:6400\nds_read_b64_tr_b16 v[136:137], v94 offset:4160\nds_read_b64_tr_b16 v[138:139], v94 offset:4416\nds_read_b64_tr_b16 v[140:141], v94 offset:6208\nds_read_b64_tr_b16 v[142:143], v94 offset:6464\ns_waitcnt lgkmcnt(0)\nbuffer_load_dwordx2 v[12:13], off, s[36:39], s45 sc1\ns_waitcnt vmcnt(29)\n" \
"s_barrier\ns_add_u32 m0, %[tb], 4096\ns_add_u32 s44, %[w2], 8448\nbuffer_load_dwordx4 v91, %[ro], s44 offen nt lds\ns_add_u32 m0, %[tb], 5120\ns_add_u32 s44, %[w2], 41216\nbuffer_load_dwordx4 v91, %[ro], s44 offen nt lds\ns_add_u32 m0, %[tb], 6144\n" \
"s_add_u32 s44, %[w2], 73984\nbuffer_load_dwordx4 v91, %[ro], s44 offen nt lds\ns_add_u32 m0, %[tb], 7168\ns_add_u32 s44, %[w2], 106752\nbuffer_load_dwordx4 v91, %[ro], s44 offen nt lds\nds_read_b64_tr_b16 a[144:145], v94 offset:8192\nds_read_b64_tr_b16 a[146:147], v94 offset:8448\nds_read_b64_tr_b16 a[148:149], v94 offset:10240\n" \
"ds_read_b64_tr_b16 a[150:151], v94 offset:10496\nds_read_b64_tr_b16 v[192:193], v94 offset:8256\nds_read_b64_tr_b16 v[194:195], v94 offset:8512\nds_read_b64_tr_b16 v[196:197], v94 offset:10304\nds_read_b64_tr_b16 v[198:199], v94 offset:10560\ns_waitcnt lgkmcnt(0)\ns_waitcnt vmcnt(29)\ns_barrier\n" \
"s_add_u32 m0, %[tb], 8192\ns_add_u32 s44, %[w2], 8576\nbuffer_load_dwordx4 v91, %[ro], s44 offen nt lds\ns_add_u32 m0, %[tb], 9216\ns_add_u32 s44, %[w2], 41344\nbuffer_load_dwordx4 v91, %[ro], s44 offen nt lds\ns_add_u32 m0, %[tb], 10240\ns_add_u32 s44, %[w2], 74112\n" \
"buffer_load_dwordx4 v91, %[ro], s44 offen nt lds\ns_add_u32 m0, %[tb], 11264\ns_add_u32 s44, %[w2], 106880\nbuffer_load_dwordx4 v91, %[ro], s44 offen nt lds\nds_read_b64_tr_b16 a[200:201], v95 offset:0\nds_read_b64_tr_b16 a[202:203], v95 offset:256\nds_read_b64_tr_b16 a[204:205], v95 offset:2048\nds_read_b64_tr_b16 a[206:207], v95 offset:2304\n" \
"ds_read_b64_tr_b16 v[248:249], v95 offset:64\nds_read_b64_tr_b16 v[250:251], v95 offset:320\nds_read_b64_tr_b16 v[252:253], v95 offset:2112\nds_read_b64_tr_b16 v[254:255], v95 offset:2368\ns_waitcnt lgkmcnt(0)\ns_waitcnt vmcnt(29)\ns_barrier\nds_read_b64_tr_b16 v[52:53], v95 offset:4096\n" \
"ds_read_b64_tr_b16 v[54:55], v95 offset:4352\nds_read_b64_tr_b16 v[48:49], v95 offset:6144\nds_read_b64_tr_b16 v[50:51], v95 offset:6400\nds_read_b64_tr_b16 v[44:45], v95 offset:4160\nds_read_b64_tr_b16 v[46:47], v95 offset:4416\nds_read_b64_tr_b16 v[40:41], v95 offset:6208\nds_read_b64_tr_b16 v[42:43], v95 offset:6464\ns_waitcnt lgkmcnt(0)\n" \
"ds_write_b128 v1, v[84:87] offset:0\nds_write_b128 v1, v[80:83] offset:1024\nds_write_b128 v1, v[76:79] offset:2048\nds_write_b128 v1, v[72:75] offset:3072\ns_waitcnt vmcnt(25)\ns_barrier\nds_read_b64_tr_b16 a[72:73], v95 offset:8192\nds_read_b64_tr_b16 a[74:75], v95 offset:8448\n" \
"ds_read_b64_tr_b16 a[76:77], v95 offset:10240\nds_read_b64_tr_b16 a[78:79], v95 offset:10496\nds_read_b64_tr_b16 v[120:121], v95 offset:8256\nds_read_b64_tr_b16 v[122:123], v95 offset:8512\nds_read_b64_tr_b16 v[124:125], v95 offset:10304\nds_read_b64_tr_b16 v[126:127], v95 offset:10560\ns_waitcnt lgkmcnt(0)\nds_write_b128 v1, v[68:71] offset:4096\n" \
"ds_write_b128 v1, v[64:67] offset:5120\nds_write_b128 v1, v[60:63] offset:6144\nds_write_b128 v1, v[56:59] offset:7168\ns_waitcnt vmcnt(21)\ns_barrier\nds_read_b64_tr_b16 v[56:57], v95 offset:12288\nds_read_b64_tr_b16 v[58:59], v95 offset:12544\nds_read_b64_tr_b16 v[60:61], v95 offset:14336\n" \
"ds_read_b64_tr_b16 v[62:63], v95 offset:14592\nds_read_b64_tr_b16 v[64:65], v95 offset:12352\nds_read_b64_tr_b16 v[66:67], v95 offset:12608\nds_read_b64_tr_b16 v[68:69], v95 offset:14400\nds_read_b64_tr_b16 v[70:71], v95 offset:14656\ns_waitcnt lgkmcnt(0)\nds_write_b128 v1, v[52:55] offset:8192\nds_write_b128 v1, v[48:51] offset:9216\n" \
"ds_write_b128 v1, v[44:47] offset:10240\nds_write_b128 v1, v[40:43] offset:11264\ns_waitcnt vmcnt(17)\ns_barrier\nds_read_b64_tr_b16 a[176:177], v95 offset:16384\nds_read_b64_tr_b16 a[178:179], v95 offset:16640\nds_read_b64_tr_b16 a[180:181], v95 offset:18432\nds_read_b64_tr_b16 a[182:183], v95 offset:18688\n" \
"ds_read_b64_tr_b16 v[224:225], v95 offset:16448\nds_read_b64_tr_b16 v[226:227], v95 offset:16704\nds_read_b64_tr_b16 v[228:229], v95 offset:18496\nds_read_b64_tr_b16 v[230:231], v95 offset:18752\ns_waitcnt lgkmcnt(0)\nds_write_b128 v1, v[56:59] offset:12288\nds_write_b128 v1, v[60:63] offset:13312\nds_write_b128 v1, v[64:67] offset:14336\n" \
"ds_write_b128 v1, v[68:71] offset:15360\ns_waitcnt vmcnt(13)\ns_barrier\nds_read_b64_tr_b16 a[32:33], v95 offset:20480\nds_read_b64_tr_b16 a[34:35], v95 offset:20736\nds_read_b64_tr_b16 a[36:37], v95 offset:22528\nds_read_b64_tr_b16 a[38:39], v95 offset:22784\nds_read_b64_tr_b16 a[232:233], v95 offset:20544\n" \
"ds_read_b64_tr_b16 a[234:235], v95 offset:20800\nds_read_b64_tr_b16 a[236:237], v95 offset:22592\nds_read_b64_tr_b16 a[238:239], v95 offset:22848\ns_waitcnt lgkmcnt(0)\ns_waitcnt vmcnt(9)\ns_barrier\nds_read_b64_tr_b16 v[68:69], v94 offset:0\nds_read_b64_tr_b16 v[70:71], v94 offset:256\n" \
"ds_read_b64_tr_b16 v[64:65], v94 offset:2048\nds_read_b64_tr_b16 v[66:67], v94 offset:2304\nds_read_b64_tr_b16 v[60:61], v94 offset:64\nds_read_b64_tr_b16 v[62:63], v94 offset:320\nds_read_b64_tr_b16 v[56:57], v94 offset:2112\nds_read_b64_tr_b16 v[58:59], v94 offset:2368\ns_waitcnt lgkmcnt(0)\nds_write_b128 v1, v[68:71] offset:16384\n" \
"ds_write_b128 v1, v[64:67] offset:17408\nds_write_b128 v1, v[60:63] offset:18432\nds_write_b128 v1, v[56:59] offset:19456\ns_waitcnt vmcnt(4)\ns_barrier\nds_read_b64_tr_b16 a[136:137], v94 offset:4096\nds_read_b64_tr_b16 a[138:139], v94 offset:4352\nds_read_b64_tr_b16 a[140:141], v94 offset:6144\n" \
"ds_read_b64_tr_b16 a[142:143], v94 offset:6400\nds_read_b64_tr_b16 v[176:177], v94 offset:4160\nds_read_b64_tr_b16 v[178:179], v94 offset:4416\nds_read_b64_tr_b16 v[180:181], v94 offset:6208\nds_read_b64_tr_b16 v[182:183], v94 offset:6464\ns_waitcnt lgkmcnt(0)\ns_waitcnt vmcnt(0)\ns_barrier\n" \
"ds_read_b64_tr_b16 v[56:57], v94 offset:8192\nds_read_b64_tr_b16 v[58:59], v94 offset:8448\nds_read_b64_tr_b16 v[60:61], v94 offset:10240\nds_read_b64_tr_b16 v[62:63], v94 offset:10496\nds_read_b64_tr_b16 v[64:65], v94 offset:8256\nds_read_b64_tr_b16 v[66:67], v94 offset:8512\nds_read_b64_tr_b16 v[68:69], v94 offset:10304\nds_read_b64_tr_b16 v[70:71], v94 offset:10560\n" \
"s_waitcnt lgkmcnt(0)\ns_mov_b32 s43, 0\ns_mov_b32 s86, 110\ns_mov_b32 s89, 0\n.Lvu_hb%=:\nv_cmp_ne_u32 vcc, v15, v12\ns_cbranch_vccnz .Lvu_hb1%=\ns_sleep 4\n" \
"buffer_load_dwordx2 v[12:13], off, s[36:39], s45 sc1\ns_waitcnt vmcnt(0)\ns_add_u32 s43, s43, 1\ns_cmp_lt_u32 s43, 262144\ns_cbranch_scc1 .Lvu_hb%=\ns_branch .Lvu_abort%=\n.Lvu_hb1%=:\nv_readfirstlane_b32 s44, v13\n" \
"s_and_b32 s44, s44, 0x200000\ns_add_u32 s36, s36, s44\ns_addc_u32 s37, s37, 0\nds_write_b128 v1, v[56:59] offset:20480\nds_write_b128 v1, v[60:63] offset:21504\nds_write_b128 v1, v[64:67] offset:22528\nds_write_b128 v1, v[68:71] offset:23552\ns_waitcnt lgkmcnt(0)\n" \
"s_barrier\ns_mov_b32 s40, -1\ns_mov_b32 s43, 0\ns_lshl_b32 s57, %[w], 12\ns_mov_b32 s56, 0\ns_mov_b32 s42, 0\ns_mov_b32 s60, -2147483648\ns_mov_b32 s54, -2147483648\n" \
"s_add_u32 s44, s40, 1\ns_lshl_b32 s44, s44, 17\ns_add_u32 s44, s44, %[xn]\ns_lshr_b32 s45, s44, 8\ns_and_b32 s45, s45, 0x2000\ns_xor_b32 s41, s44, s45\ns_add_u32 s41, s41, %[xw]\n.Lvu_f0%=:\n" \
"buffer_load_dword v14, v8, s[36:39], s56 offen sc1\ns_waitcnt vmcnt(0)\nv_cmp_ge_i32 vcc, s40, v14\ns_cbranch_vccz .Lvu_f0fok%=\ns_add_u32 s43, s43, 1\ns_cmp_lt_u32 s43, 262144\ns_cbranch_scc1 .Lvu_f0%=\ns_branch .Lvu_abort%=\n" \
".Lvu_f0fok%=:\ns_add_u32 m0, s57, 0\ns_add_u32 s58, s41, 0\nbuffer_load_dwordx4 v0, s[36:39], s58 offen offset:0 sc1 lds\nbuffer_load_dwordx4 v0, s[36:39], s58 offen offset:1024 sc1 lds\nbuffer_load_dwordx4 v0, s[36:39], s58 offen offset:2048 sc1 lds\nbuffer_load_dwordx4 v0, s[36:39], s58 offen offset:3072 sc1 lds\ns_add_u32 m0, s57, 32768\n" \
"s_add_u32 s58, s41, 32768\nbuffer_load_dwordx4 v0, s[36:39], s58 offen offset:0 sc1 lds\nbuffer_load_dwordx4 v0, s[36:39], s58 offen offset:1024 sc1 lds\nbuffer_load_dwordx4 v0, s[36:39], s58 offen offset:2048 sc1 lds\nbuffer_load_dwordx4 v0, s[36:39], s58 offen offset:3072 sc1 lds\ns_waitcnt vmcnt(0)\ns_barrier\nds_read_b128 v[32:35], v2 offset:0\n" \
"ds_read_b128 v[36:39], v2 offset:256\nds_read_b128 v[40:43], v2 offset:2048\nds_read_b128 v[44:47], v2 offset:2304\n.p2align 6\n.Lvu_tok%=:\nds_read_b128 v[64:67], v2 offset:4096\nds_read_b128 v[68:71], v2 offset:4352\ns_waitcnt lgkmcnt(5)\n" \
"v_mfma_f32_16x16x32_bf16 v[16:19], a[0:3], v[32:35], 0\nv_cvt_pk_bf16_f32 v92, v80, v81\nv_cvt_pk_bf16_f32 v93, v82, v83\nv_mfma_f32_16x16x32_bf16 v[20:23], a[16:19], v[32:35], 0\ns_waitcnt lgkmcnt(4)\nds_read_b128 v[32:35], v2 offset:6144\nv_cvt_pk_bf16_f32 v94, v84, v85\nv_cvt_pk_bf16_f32 v95, v86, v87\n" \
"v_mfma_f32_16x16x32_bf16 v[24:27], a[0:3], v[36:39], 0\nv_and_or_b32 v92, v92, -2, v13\nv_and_or_b32 v94, v94, -2, v13\nv_mfma_f32_16x16x32_bf16 v[28:31], a[16:19], v[36:39], 0\ns_waitcnt lgkmcnt(4)\nbuffer_store_dwordx4 v[92:95], v7, s[36:39], s60 offen offset:2560 sc1\nds_read_b128 v[36:39], v2 offset:6400\nv_mfma_f32_16x16x32_bf16 v[16:19], a[4:7], v[40:43], v[16:19]\n" \
"ds_read_b128 v[56:59], v1 offset:0\nds_read_b128 v[60:63], v1 offset:1024\nv_mfma_f32_16x16x32_bf16 v[20:23], a[20:23], v[40:43], v[20:23]\ns_waitcnt lgkmcnt(6)\ns_add_u32 m0, s57, 16384\nds_read_b128 v[40:43], v2 offset:8192\nv_mfma_f32_16x16x32_bf16 v[24:27], a[4:7], v[44:47], v[24:27]\ns_add_u32 s60, s41, 65536\n" \
"buffer_load_dwordx4 v0, s[36:39], s60 offen offset:0 sc1 lds\nv_mfma_f32_16x16x32_bf16 v[28:31], a[20:23], v[44:47], v[28:31]\ns_waitcnt lgkmcnt(6)\nds_read_b128 v[44:47], v2 offset:8448\nv_mfma_f32_16x16x32_bf16 v[16:19], a[24:27], v[64:67], v[16:19]\nv_mfma_f32_16x16x32_bf16 v[20:23], a[40:43], v[64:67], v[20:23]\ns_waitcnt lgkmcnt(6)\nds_read_b128 v[64:67], v2 offset:10240\n" \
"v_mfma_f32_16x16x32_bf16 v[24:27], a[24:27], v[68:71], v[24:27]\nbuffer_load_dwordx4 v0, s[36:39], s60 offen offset:1024 sc1 lds\nv_mfma_f32_16x16x32_bf16 v[28:31], a[40:43], v[68:71], v[28:31]\ns_waitcnt lgkmcnt(6)\nds_read_b128 v[68:71], v2 offset:10496\nv_mfma_f32_16x16x32_bf16 v[16:19], a[28:31], v[32:35], v[16:19]\nv_mfma_f32_16x16x32_bf16 v[20:23], a[44:47], v[32:35], v[20:23]\ns_waitcnt lgkmcnt(6)\n" \
"ds_read_b128 v[32:35], v2 offset:12288\nv_mfma_f32_16x16x32_bf16 v[24:27], a[28:31], v[36:39], v[24:27]\nbuffer_load_dwordx4 v0, s[36:39], s60 offen offset:2048 sc1 lds\nv_mfma_f32_16x16x32_bf16 v[28:31], a[44:47], v[36:39], v[28:31]\ns_waitcnt lgkmcnt(4)\nds_read_b128 v[36:39], v2 offset:12544\nv_mfma_f32_16x16x32_bf16 v[16:19], v[56:59], v[40:43], v[16:19]\nv_mfma_f32_16x16x32_bf16 v[20:23], a[64:67], v[40:43], v[20:23]\n" \
"s_waitcnt lgkmcnt(4)\nds_read_b128 v[40:43], v2 offset:14336\nv_mfma_f32_16x16x32_bf16 v[24:27], v[56:59], v[44:47], v[24:27]\nbuffer_load_dwordx4 v0, s[36:39], s60 offen offset:3072 sc1 lds\nv_mfma_f32_16x16x32_bf16 v[28:31], a[64:67], v[44:47], v[28:31]\ns_waitcnt lgkmcnt(4)\nds_read_b128 v[44:47], v2 offset:14592\nv_mfma_f32_16x16x32_bf16 v[16:19], v[60:63], v[64:67], v[16:19]\n" \
"s_add_u32 s56, s56, %[fs]\ns_add_u32 s62, s40, 1\ns_lshr_b32 m0, s57, 4\nv_mfma_f32_16x16x32_bf16 v[20:23], a[68:71], v[64:67], v[20:23]\ns_add_u32 m0, m0, 159744\ns_waitcnt lgkmcnt(4)\nds_read_b128 v[64:67], v2 offset:32768\nv_mfma_f32_16x16x32_bf16 v[24:27], v[60:63], v[68:71], v[24:27]\n" \
"buffer_load_dword v8, s[36:39], s56 offen sc1 lds\nv_lshrrev_b32 v14, 2, v0\nv_mfma_f32_16x16x32_bf16 v[28:31], a[68:71], v[68:71], v[28:31]\ns_waitcnt lgkmcnt(4)\nds_read_b128 v[68:71], v2 offset:33024\nv_mfma_f32_16x16x32_bf16 v[16:19], a[80:83], v[32:35], v[16:19]\nv_add_u32 v14, 159744, v14\nv_mfma_f32_16x16x32_bf16 v[20:23], a[88:91], v[32:35], v[20:23]\n" \
"s_waitcnt lgkmcnt(4)\nds_read_b128 v[32:35], v2 offset:34816\nv_mfma_f32_16x16x32_bf16 v[24:27], a[80:83], v[36:39], v[24:27]\nds_read_b128 v[56:59], v1 offset:2048\nds_read_b128 v[60:63], v1 offset:3072\nv_mfma_f32_16x16x32_bf16 v[28:31], a[88:91], v[36:39], v[28:31]\ns_waitcnt lgkmcnt(6)\nds_read_b128 v[36:39], v2 offset:35072\n" \
"v_mfma_f32_16x16x32_bf16 v[16:19], a[84:87], v[40:43], v[16:19]\nv_mfma_f32_16x16x32_bf16 v[20:23], a[92:95], v[40:43], v[20:23]\ns_waitcnt lgkmcnt(6)\nds_read_b128 v[40:43], v2 offset:36864\nv_mfma_f32_16x16x32_bf16 v[24:27], a[84:87], v[44:47], v[24:27]\nv_mfma_f32_16x16x32_bf16 v[28:31], a[92:95], v[44:47], v[28:31]\ns_waitcnt lgkmcnt(6)\nds_read_b128 v[44:47], v2 offset:37120\n" \
"v_mfma_f32_16x16x32_bf16 v[16:19], a[104:107], v[64:67], v[16:19]\nv_mfma_f32_16x16x32_bf16 v[20:23], a[120:123], v[64:67], v[20:23]\ns_waitcnt lgkmcnt(6)\nds_read_b128 v[64:67], v2 offset:38912\nv_mfma_f32_16x16x32_bf16 v[24:27], a[104:107], v[68:71], v[24:27]\ns_barrier\nv_mfma_f32_16x16x32_bf16 v[28:31], a[120:123], v[68:71], v[28:31]\ns_waitcnt lgkmcnt(6)\n" \
"s_add_u32 m0, s57, 0\nds_read_b128 v[68:71], v2 offset:39168\nv_mfma_f32_16x16x32_bf16 v[16:19], a[108:111], v[32:35], v[16:19]\ns_add_u32 s60, s41, 98304\nbuffer_load_dwordx4 v0, s[36:39], s60 offen offset:0 sc1 lds\nv_mfma_f32_16x16x32_bf16 v[20:23], a[124:127], v[32:35], v[20:23]\ns_waitcnt lgkmcnt(4)\nds_read_b128 v[32:35], v2 offset:40960\n" \
"v_mfma_f32_16x16x32_bf16 v[24:27], a[108:111], v[36:39], v[24:27]\nv_mfma_f32_16x16x32_bf16 v[28:31], a[124:127], v[36:39], v[28:31]\ns_waitcnt lgkmcnt(4)\nds_read_b128 v[36:39], v2 offset:41216\nv_mfma_f32_16x16x32_bf16 v[16:19], a[128:131], v[40:43], v[16:19]\nbuffer_load_dwordx4 v0, s[36:39], s60 offen offset:1024 sc1 lds\nv_mfma_f32_16x16x32_bf16 v[20:23], v[56:59], v[40:43], v[20:23]\ns_waitcnt lgkmcnt(4)\n" \
"ds_read_b128 v[40:43], v2 offset:43008\nv_mfma_f32_16x16x32_bf16 v[24:27], a[128:131], v[44:47], v[24:27]\nv_mfma_f32_16x16x32_bf16 v[28:31], v[56:59], v[44:47], v[28:31]\ns_waitcnt lgkmcnt(4)\nds_read_b128 v[44:47], v2 offset:43264\nv_mfma_f32_16x16x32_bf16 v[16:19], a[132:135], v[64:67], v[16:19]\nbuffer_load_dwordx4 v0, s[36:39], s60 offen offset:2048 sc1 lds\nv_mfma_f32_16x16x32_bf16 v[20:23], v[60:63], v[64:67], v[20:23]\n" \
"s_waitcnt lgkmcnt(4)\nds_read_b128 v[64:67], v2 offset:45056\nv_mfma_f32_16x16x32_bf16 v[24:27], a[132:135], v[68:71], v[24:27]\nv_mfma_f32_16x16x32_bf16 v[28:31], v[60:63], v[68:71], v[28:31]\ns_waitcnt lgkmcnt(4)\nds_read_b128 v[68:71], v2 offset:45312\nv_mfma_f32_16x16x32_bf16 v[16:19], a[152:155], v[32:35], v[16:19]\nbuffer_load_dwordx4 v0, s[36:39], s60 offen offset:3072 sc1 lds\n" \
"v_mfma_f32_16x16x32_bf16 v[20:23], a[168:171], v[32:35], v[20:23]\ns_waitcnt lgkmcnt(4)\ns_add_u32 s44, s40, 2\nds_read_b128 v[32:35], v2 offset:47104\nv_mfma_f32_16x16x32_bf16 v[24:27], a[152:155], v[36:39], v[24:27]\ns_lshl_b32 s44, s44, 17\nds_read_b128 v[56:59], v1 offset:4096\nds_read_b128 v[60:63], v1 offset:5120\n" \
"v_mfma_f32_16x16x32_bf16 v[28:31], a[168:171], v[36:39], v[28:31]\ns_waitcnt lgkmcnt(6)\nds_read_b128 v[36:39], v2 offset:47360\nv_mfma_f32_16x16x32_bf16 v[16:19], a[156:159], v[40:43], v[16:19]\ns_add_u32 s44, s44, %[xn]\ns_lshr_b32 s45, s44, 8\nv_mfma_f32_16x16x32_bf16 v[20:23], a[172:175], v[40:43], v[20:23]\ns_waitcnt lgkmcnt(6)\n" \
"v_mfma_f32_16x16x32_bf16 v[24:27], a[156:159], v[44:47], v[24:27]\ns_and_b32 s45, s45, 0x2000\ns_xor_b32 s41, s44, s45\nv_mfma_f32_16x16x32_bf16 v[28:31], a[172:175], v[44:47], v[28:31]\ns_waitcnt lgkmcnt(0)\ns_waitcnt vmcnt(4)\ns_barrier\nds_read_b128 v[40:43], v2 offset:16384\n" \
"ds_read_b128 v[44:47], v2 offset:16640\nv_mfma_f32_16x16x32_bf16 v[16:19], a[184:187], v[64:67], v[16:19]\nds_read_b32 v14, v14\nv_mfma_f32_16x16x32_bf16 v[20:23], a[192:195], v[64:67], v[20:23]\nds_read_b128 v[64:67], v2 offset:18432\nv_mfma_f32_16x16x32_bf16 v[24:27], a[184:187], v[68:71], v[24:27]\ns_add_u32 s41, s41, %[xw]\nv_mfma_f32_16x16x32_bf16 v[28:31], a[192:195], v[68:71], v[28:31]\n" \
"ds_read_b128 v[68:71], v2 offset:18688\nv_mfma_f32_16x16x32_bf16 v[16:19], a[188:191], v[32:35], v[16:19]\nv_mfma_f32_16x16x32_bf16 v[20:23], a[196:199], v[32:35], v[20:23]\nds_read_b128 v[32:35], v2 offset:20480\nv_mfma_f32_16x16x32_bf16 v[24:27], a[188:191], v[36:39], v[24:27]\ns_waitcnt lgkmcnt(3)\nv_cmp_ge_i32 s[64:65], s62, v14\nv_mfma_f32_16x16x32_bf16 v[28:31], a[196:199], v[36:39], v[28:31]\n" \
"s_cmp_eq_u64 s[64:65], 0\ns_cselect_b32 s61, s41, -2147483648\nds_read_b128 v[36:39], v2 offset:20736\nv_mfma_f32_16x16x32_bf16 v[16:19], v[56:59], v[40:43], v[16:19]\ns_waitcnt vmcnt(4)\nbuffer_store_dword v15, off, s[36:39], s54 sc1\nv_mfma_f32_16x16x32_bf16 v[20:23], a[216:219], v[40:43], v[20:23]\ns_add_u32 m0, s57, 32768\n" \
"ds_read_b128 v[40:43], v2 offset:22528\nv_mfma_f32_16x16x32_bf16 v[24:27], v[56:59], v[44:47], v[24:27]\ns_add_u32 s58, s61, 32768\nbuffer_load_dwordx4 v0, s[36:39], s58 offen offset:0 sc1 lds\nv_mfma_f32_16x16x32_bf16 v[28:31], a[216:219], v[44:47], v[28:31]\ns_waitcnt lgkmcnt(4)\nds_read_b128 v[44:47], v2 offset:22784\nv_mfma_f32_16x16x32_bf16 v[16:19], v[60:63], v[64:67], v[16:19]\n" \
"v_mfma_f32_16x16x32_bf16 v[20:23], a[220:223], v[64:67], v[20:23]\ns_waitcnt lgkmcnt(4)\nds_read_b128 v[64:67], v2 offset:24576\nv_mfma_f32_16x16x32_bf16 v[24:27], v[60:63], v[68:71], v[24:27]\nbuffer_load_dwordx4 v0, s[36:39], s58 offen offset:1024 sc1 lds\nv_mfma_f32_16x16x32_bf16 v[28:31], a[220:223], v[68:71], v[28:31]\ns_waitcnt lgkmcnt(4)\nds_read_b128 v[68:71], v2 offset:24832\n" \
"v_mfma_f32_16x16x32_bf16 v[16:19], a[224:227], v[32:35], v[16:19]\nds_read_b128 v[56:59], v1 offset:6144\nds_read_b128 v[60:63], v1 offset:7168\nv_mfma_f32_16x16x32_bf16 v[20:23], a[240:243], v[32:35], v[20:23]\ns_waitcnt lgkmcnt(6)\nds_read_b128 v[32:35], v2 offset:26624\nv_mfma_f32_16x16x32_bf16 v[24:27], a[224:227], v[36:39], v[24:27]\nbuffer_load_dwordx4 v0, s[36:39], s58 offen offset:2048 sc1 lds\n" \
"v_mfma_f32_16x16x32_bf16 v[28:31], a[240:243], v[36:39], v[28:31]\ns_waitcnt lgkmcnt(6)\nds_read_b128 v[36:39], v2 offset:26880\nv_mfma_f32_16x16x32_bf16 v[16:19], a[228:231], v[40:43], v[16:19]\nv_mfma_f32_16x16x32_bf16 v[20:23], a[244:247], v[40:43], v[20:23]\ns_waitcnt lgkmcnt(6)\nds_read_b128 v[40:43], v2 offset:28672\nv_mfma_f32_16x16x32_bf16 v[24:27], a[228:231], v[44:47], v[24:27]\n" \
"buffer_load_dwordx4 v0, s[36:39], s58 offen offset:3072 sc1 lds\nv_mfma_f32_16x16x32_bf16 v[28:31], a[244:247], v[44:47], v[28:31]\ns_waitcnt lgkmcnt(6)\nds_read_b128 v[44:47], v2 offset:28928\nv_mfma_f32_16x16x32_bf16 v[16:19], v[96:99], v[64:67], v[16:19]\nv_mfma_f32_16x16x32_bf16 v[20:23], v[112:115], v[64:67], v[20:23]\ns_waitcnt lgkmcnt(6)\nds_read_b128 v[64:67], v2 offset:30720\n" \
"v_mfma_f32_16x16x32_bf16 v[24:27], v[96:99], v[68:71], v[24:27]\nv_mfma_f32_16x16x32_bf16 v[28:31], v[112:115], v[68:71], v[28:31]\ns_waitcnt lgkmcnt(4)\nds_read_b128 v[68:71], v2 offset:30976\nv_mfma_f32_16x16x32_bf16 v[16:19], v[100:103], v[32:35], v[16:19]\nv_mfma_f32_16x16x32_bf16 v[20:23], v[116:119], v[32:35], v[20:23]\ns_waitcnt lgkmcnt(4)\nv_mfma_f32_16x16x32_bf16 v[24:27], v[100:103], v[36:39], v[24:27]\n" \
"v_mfma_f32_16x16x32_bf16 v[28:31], v[116:119], v[36:39], v[28:31]\ns_waitcnt lgkmcnt(0)\ns_waitcnt vmcnt(5)\ns_barrier\nds_read_b128 v[32:35], v2 offset:0\nds_read_b128 v[36:39], v2 offset:2048\nv_mfma_f32_16x16x32_bf16 v[16:19], v[128:131], v[40:43], v[16:19]\ns_add_u32 m0, s57, 16384\n" \
"s_nop 0\nbuffer_load_dwordx4 v3, s[36:39], s42 offen offset:0 sc1 lds\nv_mfma_f32_16x16x32_bf16 v[20:23], v[56:59], v[40:43], v[20:23]\nds_read_b128 v[40:43], v2 offset:4096\nv_mfma_f32_16x16x32_bf16 v[24:27], v[128:131], v[44:47], v[24:27]\nv_mfma_f32_16x16x32_bf16 v[28:31], v[56:59], v[44:47], v[28:31]\nds_read_b128 v[44:47], v2 offset:6144\nv_mfma_f32_16x16x32_bf16 v[16:19], v[132:135], v[64:67], v[16:19]\n" \
"buffer_load_dwordx4 v3, s[36:39], s42 offen offset:1024 sc1 lds\nv_mfma_f32_16x16x32_bf16 v[20:23], v[60:63], v[64:67], v[20:23]\nds_read_b128 v[64:67], v2 offset:8192\nv_mfma_f32_16x16x32_bf16 v[24:27], v[132:135], v[68:71], v[24:27]\nv_mfma_f32_16x16x32_bf16 v[28:31], v[60:63], v[68:71], v[28:31]\ns_waitcnt lgkmcnt(4)\nds_read_b128 v[68:71], v2 offset:10240\nv_mfma_f32_16x16x32_bf16 v[16:19], v[144:147], v[32:35], v[16:19]\n" \
"buffer_load_dwordx4 v3, s[36:39], s42 offen offset:2048 sc1 lds\nv_mfma_f32_16x16x32_bf16 v[20:23], v[160:163], v[32:35], v[20:23]\ns_waitcnt lgkmcnt(4)\nds_read_b128 v[32:35], v2 offset:12288\nv_mfma_f32_16x16x32_bf16 v[16:19], v[148:151], v[36:39], v[16:19]\nv_mfma_f32_16x16x32_bf16 v[20:23], v[164:167], v[36:39], v[20:23]\ns_waitcnt lgkmcnt(4)\nds_read_b128 v[36:39], v2 offset:14336\n" \
"v_mfma_f32_16x16x32_bf16 v[16:19], v[168:171], v[40:43], v[16:19]\nbuffer_load_dwordx4 v3, s[36:39], s42 offen offset:3072 sc1 lds\nv_add_u32 v14, s57, v0\nv_mfma_f32_16x16x32_bf16 v[20:23], v[184:187], v[40:43], v[20:23]\ns_waitcnt lgkmcnt(4)\nds_read_b128 v[40:43], v2 offset:256\nv_mfma_f32_16x16x32_bf16 v[16:19], v[172:175], v[44:47], v[16:19]\nv_mov_b64 v[48:49], 0\n" \
"v_mfma_f32_16x16x32_bf16 v[20:23], v[188:191], v[44:47], v[20:23]\ns_waitcnt lgkmcnt(4)\nds_read_b128 v[44:47], v2 offset:2304\nv_mov_b64 v[50:51], 0\nv_mfma_f32_16x16x32_bf16 v[16:19], v[200:203], v[64:67], v[16:19]\nv_mov_b64 v[52:53], 0\nv_mfma_f32_16x16x32_bf16 v[20:23], v[216:219], v[64:67], v[20:23]\ns_waitcnt lgkmcnt(4)\n" \
"ds_read_b128 v[64:67], v2 offset:4352\nv_mov_b64 v[54:55], 0\nv_mfma_f32_16x16x32_bf16 v[16:19], v[204:207], v[68:71], v[16:19]\nds_read_b128 v[56:59], v1 offset:0\nds_read_b128 v[60:63], v1 offset:1024\nv_mfma_f32_16x16x32_bf16 v[20:23], v[220:223], v[68:71], v[20:23]\ns_waitcnt lgkmcnt(6)\nds_read_b128 v[68:71], v2 offset:6400\n" \
"v_mfma_f32_16x16x32_bf16 v[16:19], v[232:235], v[32:35], v[16:19]\ns_waitcnt vmcnt(0)\nds_read_b128 v[80:83], v14 offset:16384\nds_read_b128 v[84:87], v14 offset:17408\nv_mfma_f32_16x16x32_bf16 v[20:23], v[240:243], v[32:35], v[20:23]\ns_waitcnt lgkmcnt(8)\nds_read_b128 v[32:35], v2 offset:8448\nv_mfma_f32_16x16x32_bf16 v[16:19], v[236:239], v[36:39], v[16:19]\n" \
"ds_read_b128 v[88:91], v14 offset:18432\nds_read_b128 v[92:95], v14 offset:19456\nv_mfma_f32_16x16x32_bf16 v[20:23], v[244:247], v[36:39], v[20:23]\ns_waitcnt lgkmcnt(10)\nds_read_b128 v[36:39], v2 offset:10496\nv_mfma_f32_16x16x32_bf16 v[24:27], v[144:147], v[40:43], v[24:27]\nv_mfma_f32_16x16x32_bf16 v[28:31], v[160:163], v[40:43], v[28:31]\ns_waitcnt lgkmcnt(10)\n" \
"ds_read_b128 v[40:43], v2 offset:12544\nv_cvt_pk_bf16_f32 v72, v16, v17\nv_mfma_f32_16x16x32_bf16 v[24:27], v[148:151], v[44:47], v[24:27]\nv_cvt_pk_bf16_f32 v73, v18, v19\nv_mfma_f32_16x16x32_bf16 v[28:31], v[164:167], v[44:47], v[28:31]\ns_waitcnt lgkmcnt(10)\nds_read_b128 v[44:47], v2 offset:14592\nv_cvt_pk_bf16_f32 v74, v20, v21\n" \
"v_mfma_f32_16x16x32_bf16 v[24:27], v[168:171], v[64:67], v[24:27]\ns_waitcnt lgkmcnt(6)\nv_max3_u32 v76, v80, v82, v84\nv_cvt_pk_bf16_f32 v75, v22, v23\nv_mfma_f32_16x16x32_bf16 v[28:31], v[184:187], v[64:67], v[28:31]\ns_waitcnt lgkmcnt(4)\nds_write2st64_b64 v9, v[72:73], v[74:75] offset0:0 offset1:8\nv_max3_u32 v76, v76, v86, v88\n" \
"v_mfma_f32_16x16x32_bf16 v[24:27], v[172:175], v[68:71], v[24:27]\ns_waitcnt lgkmcnt(4)\nv_max3_u32 v76, v76, v90, v92\nv_max_u32 v76, v76, v94\nv_mfma_f32_16x16x32_bf16 v[28:31], v[188:191], v[68:71], v[28:31]\nv_cmp_eq_u32 vcc, -1, v76\nv_mfma_f32_16x16x32_bf16 v[24:27], v[200:203], v[32:35], v[24:27]\ns_cbranch_vccnz .Lvu_re%=\n" \
".Lvu_rer%=:\ns_barrier\ns_memrealtime s[68:69]\nds_read_b128 v[80:83], v0 offset:16384\nv_mfma_f32_16x16x32_bf16 v[28:31], v[216:219], v[32:35], v[28:31]\ns_waitcnt lgkmcnt(4)\nds_read_b128 v[84:87], v0 offset:17408\nv_mfma_f32_16x16x32_bf16 v[24:27], v[204:207], v[36:39], v[24:27]\n" \
"ds_read_b128 v[88:91], v0 offset:18432\nv_mfma_f32_16x16x32_bf16 v[28:31], v[220:223], v[36:39], v[28:31]\ns_waitcnt lgkmcnt(5)\nds_read_b128 v[92:95], v0 offset:19456\nv_mfma_f32_16x16x32_bf16 v[24:27], v[232:235], v[40:43], v[24:27]\nds_read_b128 v[16:19], v0 offset:20480\nv_mfma_f32_16x16x32_bf16 v[28:31], v[240:243], v[40:43], v[28:31]\ns_waitcnt lgkmcnt(6)\n" \
"ds_read_b128 v[20:23], v0 offset:21504\nv_mfma_f32_16x16x32_bf16 v[24:27], v[236:239], v[44:47], v[24:27]\nds_read_b128 v[32:35], v0 offset:22528\nv_mfma_f32_16x16x32_bf16 v[28:31], v[244:247], v[44:47], v[28:31]\ns_cmp_lt_i32 s40, 0\nds_read_b128 v[36:39], v0 offset:23552\ns_cbranch_scc1 .Lvu_first%=\n.Lvu_crit%=:\n" \
"s_waitcnt lgkmcnt(7)\nv_smfmac_f32_16x16x64_bf16 v[48:51], v[80:83], a[0:7], v4\ns_waitcnt lgkmcnt(6)\nds_read_b128 v[64:67], v1 offset:4096\nds_read_b128 v[40:43], v0 offset:24576\nv_mov_b32 v14, s40\nv_smfmac_f32_16x16x64_bf16 v[48:51], v[84:87], a[24:31], v4\ns_waitcnt lgkmcnt(7)\n" \
"ds_read_b128 v[68:71], v1 offset:5120\nds_read_b128 v[44:47], v0 offset:25600\nv_cvt_pk_bf16_f32 v76, v24, v25\nv_smfmac_f32_16x16x64_bf16 v[48:51], v[88:91], v[56:63], v4\ns_sub_u32 s88, s86, s89\ns_min_u32 s88, s88, 150\ns_waitcnt lgkmcnt(8)\nds_read_b128 v[80:83], v0 offset:28672\n" \
"v_cvt_pk_bf16_f32 v77, v26, v27\nv_smfmac_f32_16x16x64_bf16 v[48:51], v[92:95], a[80:87], v4\ns_waitcnt lgkmcnt(8)\nds_read_b128 v[84:87], v0 offset:29696\nv_cvt_pk_bf16_f32 v78, v28, v29\nv_smfmac_f32_16x16x64_bf16 v[48:51], v[16:19], a[104:111], v4\ns_add_u32 s88, s88, s68\ns_mov_b32 s89, s68\n" \
"s_waitcnt lgkmcnt(8)\nds_read_b128 v[88:91], v0 offset:30720\nv_cvt_pk_bf16_f32 v79, v30, v31\nv_smfmac_f32_16x16x64_bf16 v[48:51], v[20:23], a[128:135], v4\ns_waitcnt lgkmcnt(8)\nds_read_b128 v[92:95], v0 offset:31744\nv_mov_b32 v15, s88\nv_smfmac_f32_16x16x64_bf16 v[48:51], v[32:35], a[152:159], v4\n" \
"s_waitcnt lgkmcnt(8)\nds_read_b128 v[24:27], v0 offset:26624\nbuffer_store_dwordx2 v[14:15], off, s[36:39], %[sg] sc1\nv_smfmac_f32_16x16x64_bf16 v[48:51], v[36:39], a[184:191], v4\ns_waitcnt lgkmcnt(7)\ns_waitcnt lgkmcnt(6)\nds_read_b128 v[28:31], v0 offset:27648\nv_smfmac_f32_16x16x64_bf16 v[48:51], v[40:43], v[64:71], v4\n" \
"s_waitcnt lgkmcnt(6)\nds_read_b128 v[56:59], v1 offset:2048\nv_smfmac_f32_16x16x64_bf16 v[48:51], v[44:47], a[224:231], v4\ns_waitcnt lgkmcnt(6)\nds_read_b128 v[60:63], v1 offset:3072\nv_smfmac_f32_16x16x64_bf16 v[48:51], v[80:83], v[144:151], v4\ns_waitcnt lgkmcnt(6)\nds_write2st64_b64 v9, v[76:77], v[78:79] offset0:2 offset1:10\n" \
"v_smfmac_f32_16x16x64_bf16 v[48:51], v[84:87], v[168:175], v4\ns_waitcnt lgkmcnt(6)\nv_smfmac_f32_16x16x64_bf16 v[48:51], v[88:91], v[200:207], v4\ns_waitcnt lgkmcnt(5)\nv_smfmac_f32_16x16x64_bf16 v[48:51], v[92:95], v[232:239], v4\ns_waitcnt lgkmcnt(4)\nv_smfmac_f32_16x16x64_bf16 v[48:51], v[24:27], v[96:103], v4\ns_waitcnt lgkmcnt(3)\n" \
"ds_read_b128 v[64:67], v1 offset:6144\nv_smfmac_f32_16x16x64_bf16 v[48:51], v[28:31], v[128:135], v4\nds_read_b128 v[68:71], v1 offset:7168\nv_smfmac_f32_16x16x64_bf16 v[52:55], v[80:83], v[160:167], v4\nv_smfmac_f32_16x16x64_bf16 v[52:55], v[84:87], v[184:191], v4\nv_add_f32 v48, v48, v50\nv_smfmac_f32_16x16x64_bf16 v[52:55], v[88:91], v[216:223], v4\nds_read_b128 v[80:83], v0 offset:16384\n" \
"v_add_f32 v49, v49, v51\nv_smfmac_f32_16x16x64_bf16 v[52:55], v[92:95], v[240:247], v4\nds_read_b128 v[84:87], v0 offset:17408\nv_cvt_pk_bf16_f32 v14, v48, v49\nv_smfmac_f32_16x16x64_bf16 v[52:55], v[16:19], a[120:127], v4\ns_waitcnt lgkmcnt(5)\nds_write_b16 v5, v14 offset:0\nds_write_b16_d16_hi v5, v14 offset:16\n" \
"v_smfmac_f32_16x16x64_bf16 v[52:55], v[20:23], v[56:63], v4\nds_read_b128 v[88:91], v0 offset:18432\nv_mov_b64 v[16:17], 0\nv_smfmac_f32_16x16x64_bf16 v[52:55], v[24:27], v[112:119], v4\ns_waitcnt lgkmcnt(5)\nds_read_b128 v[92:95], v0 offset:19456\nv_mov_b64 v[18:19], 0\nv_smfmac_f32_16x16x64_bf16 v[52:55], v[28:31], v[64:71], v4\n" \
"ds_read_b128 v[56:59], v1 offset:8192\nv_mov_b64 v[20:21], 0\nv_smfmac_f32_16x16x64_bf16 v[52:55], v[32:35], a[168:175], v4\ns_waitcnt lgkmcnt(3)\nds_read_b128 v[60:63], v1 offset:9216\ns_barrier\nds_read_b128 v[72:75], v11 offset:57344\nv_smfmac_f32_16x16x64_bf16 v[52:55], v[36:39], a[192:199], v4\n" \
"ds_read_b128 v[76:79], v11 offset:58368\nv_mov_b64 v[22:23], 0\nv_smfmac_f32_16x16x64_bf16 v[52:55], v[40:43], a[216:223], v4\nv_mov_b64 v[24:25], 0\nv_smfmac_f32_16x16x64_bf16 v[52:55], v[44:47], a[240:247], v4\nds_read_b128 v[64:67], v1 offset:12288\nv_mov_b64 v[26:27], 0\nv_smfmac_f32_16x16x64_bf16 v[52:55], v[80:83], a[16:23], v4\n" \
"ds_read_b128 v[68:71], v1 offset:13312\nv_mov_b64 v[28:29], 0\nv_smfmac_f32_16x16x64_bf16 v[52:55], v[84:87], a[40:47], v4\ns_waitcnt lgkmcnt(7)\nv_mov_b64 v[30:31], 0\nv_smfmac_f32_16x16x64_bf16 v[52:55], v[88:91], a[64:71], v4\ns_waitcnt lgkmcnt(6)\nv_mov_b64 v[32:33], 0\n" \
"v_smfmac_f32_16x16x64_bf16 v[52:55], v[92:95], a[88:95], v4\ns_waitcnt lgkmcnt(3)\nv_mov_b64 v[34:35], 0\nv_smfmac_f32_16x16x64_bf16 v[16:19], v[72:75], a[8:15], v10\ns_waitcnt lgkmcnt(2)\nv_mov_b64 v[36:37], 0\nv_smfmac_f32_16x16x64_bf16 v[16:19], v[76:79], v[56:63], v10\nds_read_b128 v[56:59], v1 offset:10240\n" \
"v_add_f32 v52, v52, v54\nv_smfmac_f32_16x16x64_bf16 v[20:23], v[72:75], a[56:63], v10\nds_read_b128 v[60:63], v1 offset:11264\nv_add_f32 v53, v53, v55\nv_smfmac_f32_16x16x64_bf16 v[20:23], v[76:79], a[72:79], v10\nv_cvt_pk_bf16_f32 v14, v52, v53\nv_smfmac_f32_16x16x64_bf16 v[24:27], v[72:75], a[112:119], v10\ns_waitcnt lgkmcnt(2)\n" \
"ds_write_b16 v5, v14 offset:2048\nds_write_b16_d16_hi v5, v14 offset:2064\nv_smfmac_f32_16x16x64_bf16 v[24:27], v[76:79], v[64:71], v10\nds_read_b128 v[64:67], v1 offset:14336\nv_mov_b64 v[38:39], 0\nv_smfmac_f32_16x16x64_bf16 v[28:31], v[72:75], a[160:167], v10\nds_read_b128 v[68:71], v1 offset:15360\nv_mov_b64 v[40:41], 0\n" \
"v_smfmac_f32_16x16x64_bf16 v[28:31], v[76:79], a[176:183], v10\nv_mov_b64 v[42:43], 0\nv_smfmac_f32_16x16x64_bf16 v[32:35], v[72:75], a[208:215], v10\ns_waitcnt lgkmcnt(2)\ns_barrier\ns_mov_b32 s59, 0\nds_read_b128 v[80:83], v11 offset:59392\nv_smfmac_f32_16x16x64_bf16 v[32:35], v[76:79], v[56:63], v10\n" \
"s_add_u32 m0, s57, 0\ns_add_u32 s58, s61, 0\nds_read_b128 v[56:59], v1 offset:16384\nbuffer_load_dwordx4 v0, s[36:39], s58 offen offset:0 sc1 lds\nds_read_b128 v[84:87], v11 offset:60416\nv_smfmac_f32_16x16x64_bf16 v[36:39], v[72:75], v[104:111], v10\nds_read_b128 v[60:63], v1 offset:17408\nv_mov_b64 v[44:45], 0\n" \
"v_smfmac_f32_16x16x64_bf16 v[36:39], v[76:79], v[120:127], v10\nv_mov_b64 v[46:47], 0\nv_smfmac_f32_16x16x64_bf16 v[40:43], v[72:75], v[152:159], v10\ns_waitcnt lgkmcnt(4)\nbuffer_load_dwordx4 v0, s[36:39], s58 offen offset:1024 sc1 lds\nv_smfmac_f32_16x16x64_bf16 v[40:43], v[76:79], v[64:71], v10\nds_read_b128 v[64:67], v1 offset:20480\nv_mov_b64 v[48:49], 0\n" \
"v_smfmac_f32_16x16x64_bf16 v[44:47], v[72:75], v[208:215], v10\nds_read_b128 v[68:71], v1 offset:21504\nv_mov_b64 v[50:51], 0\nv_smfmac_f32_16x16x64_bf16 v[44:47], v[76:79], v[224:231], v10\ns_waitcnt lgkmcnt(5)\nbuffer_load_dwordx4 v0, s[36:39], s58 offen offset:2048 sc1 lds\nv_smfmac_f32_16x16x64_bf16 v[16:19], v[80:83], a[32:39], v10\ns_waitcnt lgkmcnt(3)\n" \
"ds_read_b128 v[72:75], v11 offset:49152\nv_mov_b64 v[52:53], 0\nv_smfmac_f32_16x16x64_bf16 v[16:19], v[84:87], a[48:55], v10\ns_waitcnt lgkmcnt(3)\nds_read_b128 v[76:79], v11 offset:50176\nv_mov_b64 v[54:55], 0\nv_smfmac_f32_16x16x64_bf16 v[20:23], v[80:83], v[56:63], v10\nds_read_b128 v[56:59], v1 offset:18432\n" \
"buffer_load_dwordx4 v0, s[36:39], s58 offen offset:3072 sc1 lds\nv_smfmac_f32_16x16x64_bf16 v[20:23], v[84:87], a[96:103], v10\nds_read_b128 v[60:63], v1 offset:19456\nv_cvt_pk_bf16_f32 v88, v16, v17\nv_smfmac_f32_16x16x64_bf16 v[24:27], v[80:83], a[136:143], v10\nv_and_or_b32 v88, v88, -2, v12\nv_cvt_pk_bf16_f32 v89, v18, v19\nv_smfmac_f32_16x16x64_bf16 v[24:27], v[84:87], a[144:151], v10\n" \
"s_waitcnt lgkmcnt(4)\nv_cvt_pk_bf16_f32 v90, v20, v21\nv_cvt_pk_bf16_f32 v91, v22, v23\nv_and_or_b32 v90, v90, -2, v12\nv_smfmac_f32_16x16x64_bf16 v[28:31], v[80:83], v[64:71], v10\nds_read_b128 v[64:67], v1 offset:22528\nv_permlane16_swap_b32 v89, v91\nv_smfmac_f32_16x16x64_bf16 v[28:31], v[84:87], a[200:207], v10\n" \
"ds_read_b128 v[68:71], v1 offset:23552\nv_permlane16_swap_b32 v88, v90\nv_smfmac_f32_16x16x64_bf16 v[32:35], v[80:83], a[232:239], v10\ns_add_u32 s63, s59, 0\nds_read_b128 v[16:19], v11 offset:51200\nv_cvt_pk_bf16_f32 v92, v24, v25\nv_and_or_b32 v92, v92, -2, v12\nv_smfmac_f32_16x16x64_bf16 v[32:35], v[84:87], a[248:255], v10\n" \
"s_waitcnt lgkmcnt(3)\nds_read_b128 v[20:23], v11 offset:52224\nbuffer_store_dwordx4 v[88:91], v6, s[36:39], s63 offen sc1\nv_cvt_pk_bf16_f32 v93, v26, v27\nv_cvt_pk_bf16_f32 v94, v28, v29\nv_cvt_pk_bf16_f32 v95, v30, v31\nv_and_or_b32 v94, v94, -2, v12\nv_smfmac_f32_16x16x64_bf16 v[36:39], v[80:83], v[56:63], v10\n" \
"ds_read_b128 v[56:59], v1 offset:8192\nv_permlane16_swap_b32 v93, v95\nv_smfmac_f32_16x16x64_bf16 v[36:39], v[84:87], v[136:143], v10\nds_read_b128 v[60:63], v1 offset:9216\nv_permlane16_swap_b32 v92, v94\nv_smfmac_f32_16x16x64_bf16 v[40:43], v[80:83], v[176:183], v10\ns_add_u32 s63, s59, 131072\nds_read_b128 v[24:27], v11 offset:53248\n" \
"v_cvt_pk_bf16_f32 v88, v32, v33\nv_and_or_b32 v88, v88, -2, v12\nv_smfmac_f32_16x16x64_bf16 v[40:43], v[84:87], v[192:199], v10\ns_waitcnt lgkmcnt(5)\nds_read_b128 v[28:31], v11 offset:54272\nbuffer_store_dwordx4 v[92:95], v6, s[36:39], s63 offen sc1\nv_cvt_pk_bf16_f32 v89, v34, v35\nv_cvt_pk_bf16_f32 v90, v36, v37\n" \
"v_cvt_pk_bf16_f32 v91, v38, v39\nv_and_or_b32 v90, v90, -2, v12\nv_smfmac_f32_16x16x64_bf16 v[44:47], v[80:83], v[64:71], v10\nds_read_b128 v[64:67], v1 offset:16384\nv_permlane16_swap_b32 v89, v91\nv_smfmac_f32_16x16x64_bf16 v[44:47], v[84:87], v[248:255], v10\ns_cmp_ge_i32 s40, 63\nds_read_b128 v[68:71], v1 offset:17408\n" \
"v_permlane16_swap_b32 v88, v90\ns_cbranch_scc1 .Lvu_last%=\n.Lvu_y%=:\nv_smfmac_f32_16x16x64_bf16 v[48:51], v[72:75], a[8:15], v10\ns_add_u32 s63, s59, 4096\nds_read_b128 v[32:35], v11 offset:55296\nds_read_b128 v[36:39], v11 offset:56320\nv_cvt_pk_bf16_f32 v92, v40, v41\n" \
"v_and_or_b32 v92, v92, -2, v12\nv_smfmac_f32_16x16x64_bf16 v[52:55], v[76:79], a[8:15], v10\ns_waitcnt lgkmcnt(9)\ns_waitcnt lgkmcnt(6)\nbuffer_store_dwordx4 v[88:91], v6, s[36:39], s63 offen sc1\nv_cvt_pk_bf16_f32 v93, v42, v43\nv_cvt_pk_bf16_f32 v94, v44, v45\nv_cvt_pk_bf16_f32 v95, v46, v47\n" \
"v_and_or_b32 v94, v94, -2, v12\nv_smfmac_f32_16x16x64_bf16 v[48:51], v[16:19], v[56:63], v10\ns_add_u32 s40, s40, 1\ns_and_b32 s44, s40, 1\nv_permlane16_swap_b32 v93, v95\nv_smfmac_f32_16x16x64_bf16 v[52:55], v[20:23], v[56:63], v10\ns_lshl_b32 s55, s44, 22\ns_waitcnt lgkmcnt(5)\n" \
"v_permlane16_swap_b32 v92, v94\nv_smfmac_f32_16x16x64_bf16 v[48:51], v[24:27], a[32:39], v10\ns_waitcnt lgkmcnt(4)\ns_add_u32 s63, s59, 135168\nds_read_b128 v[56:59], v1 offset:12288\nv_smfmac_f32_16x16x64_bf16 v[52:55], v[28:31], a[32:39], v10\ns_waitcnt lgkmcnt(2)\nds_read_b128 v[60:63], v1 offset:13312\n" \
"buffer_store_dwordx4 v[92:95], v6, s[36:39], s63 offen sc1\nv_mov_b64 v[80:81], 0\nv_mov_b64 v[82:83], 0\nv_mov_b64 v[84:85], 0\nv_mov_b64 v[86:87], 0\nv_smfmac_f32_16x16x64_bf16 v[48:51], v[32:35], a[48:55], v10\ns_memrealtime s[86:87]\ns_bfe_u32 s45, s40, 0x10001\n" \
"s_waitcnt lgkmcnt(2)\nv_smfmac_f32_16x16x64_bf16 v[52:55], v[36:39], a[48:55], v10\nv_mov_b32 v13, s45\nv_smfmac_f32_16x16x64_bf16 v[80:83], v[72:75], a[56:63], v10\nv_cvt_pk_bf16_f32 v88, v48, v49\nv_smfmac_f32_16x16x64_bf16 v[84:87], v[76:79], a[56:63], v10\nv_cvt_pk_bf16_f32 v89, v50, v51\nv_smfmac_f32_16x16x64_bf16 v[80:83], v[16:19], a[72:79], v10\n" \
"v_cvt_pk_bf16_f32 v90, v52, v53\nv_smfmac_f32_16x16x64_bf16 v[84:87], v[20:23], a[72:79], v10\ns_add_u32 s60, s55, 0\nv_cvt_pk_bf16_f32 v91, v54, v55\nv_smfmac_f32_16x16x64_bf16 v[80:83], v[24:27], v[64:71], v10\nv_and_or_b32 v88, v88, -2, v13\nv_smfmac_f32_16x16x64_bf16 v[84:87], v[28:31], v[64:71], v10\ns_and_b32 s42, s40, 3\n" \
"s_lshl_b32 s42, s42, 15\nv_and_or_b32 v90, v90, -2, v13\nv_smfmac_f32_16x16x64_bf16 v[80:83], v[32:35], a[96:103], v10\nbuffer_store_dwordx4 v[88:91], v7, s[36:39], s60 offen offset:0 sc1\nv_mov_b64 v[48:49], 0\nv_mov_b64 v[50:51], 0\nv_mov_b64 v[52:53], 0\nv_mov_b32 v12, s44\n" \
"v_smfmac_f32_16x16x64_bf16 v[84:87], v[36:39], a[96:103], v10\nds_read_b128 v[64:67], v1 offset:20480\nv_mov_b64 v[54:55], 0\nv_smfmac_f32_16x16x64_bf16 v[48:51], v[72:75], a[112:119], v10\nds_read_b128 v[68:71], v1 offset:21504\nv_cvt_pk_bf16_f32 v92, v80, v81\nv_smfmac_f32_16x16x64_bf16 v[52:55], v[76:79], a[112:119], v10\ns_waitcnt lgkmcnt(2)\n" \
"v_cvt_pk_bf16_f32 v93, v82, v83\nv_smfmac_f32_16x16x64_bf16 v[48:51], v[16:19], v[56:63], v10\nv_cvt_pk_bf16_f32 v94, v84, v85\nv_smfmac_f32_16x16x64_bf16 v[52:55], v[20:23], v[56:63], v10\ns_add_u32 s60, s55, 4096\nv_cvt_pk_bf16_f32 v95, v86, v87\nv_smfmac_f32_16x16x64_bf16 v[48:51], v[24:27], a[136:143], v10\nds_read_b128 v[56:59], v1 offset:10240\n" \
"v_and_or_b32 v92, v92, -2, v13\nv_smfmac_f32_16x16x64_bf16 v[52:55], v[28:31], a[136:143], v10\nds_read_b128 v[60:63], v1 offset:11264\nv_and_or_b32 v94, v94, -2, v13\nv_smfmac_f32_16x16x64_bf16 v[48:51], v[32:35], a[144:151], v10\ns_waitcnt vmcnt(5)\nbuffer_store_dwordx4 v[92:95], v7, s[36:39], s60 offen offset:0 sc1\nv_mov_b64 v[80:81], 0\n" \
"v_mov_b64 v[82:83], 0\nv_mov_b64 v[84:85], 0\ns_barrier\nv_smfmac_f32_16x16x64_bf16 v[52:55], v[36:39], a[144:151], v10\nv_mov_b64 v[86:87], 0\nv_smfmac_f32_16x16x64_bf16 v[80:83], v[72:75], a[160:167], v10\nv_cvt_pk_bf16_f32 v88, v48, v49\nv_smfmac_f32_16x16x64_bf16 v[84:87], v[76:79], a[160:167], v10\n" \
"v_cvt_pk_bf16_f32 v89, v50, v51\nv_smfmac_f32_16x16x64_bf16 v[80:83], v[16:19], a[176:183], v10\ns_cmp_lt_u32 s40, 2\ns_cselect_b32 s54, %[fy], -2147483648\nv_cvt_pk_bf16_f32 v90, v52, v53\nv_smfmac_f32_16x16x64_bf16 v[84:87], v[20:23], a[176:183], v10\ns_add_u32 s60, s55, 16384\ns_waitcnt lgkmcnt(2)\n" \
"v_cvt_pk_bf16_f32 v91, v54, v55\nv_smfmac_f32_16x16x64_bf16 v[80:83], v[24:27], v[64:71], v10\nv_and_or_b32 v88, v88, -2, v13\nv_smfmac_f32_16x16x64_bf16 v[84:87], v[28:31], v[64:71], v10\nv_and_or_b32 v90, v90, -2, v13\nv_smfmac_f32_16x16x64_bf16 v[80:83], v[32:35], a[200:207], v10\nbuffer_store_dwordx4 v[88:91], v7, s[36:39], s60 offen offset:0 sc1\nv_mov_b64 v[48:49], 0\n" \
"v_mov_b64 v[50:51], 0\nv_mov_b64 v[52:53], 0\nv_smfmac_f32_16x16x64_bf16 v[84:87], v[36:39], a[200:207], v10\nds_read_b128 v[64:67], v1 offset:18432\nv_mov_b64 v[54:55], 0\nv_smfmac_f32_16x16x64_bf16 v[48:51], v[72:75], a[208:215], v10\nds_read_b128 v[68:71], v1 offset:19456\nv_cvt_pk_bf16_f32 v92, v80, v81\n" \
"v_smfmac_f32_16x16x64_bf16 v[52:55], v[76:79], a[208:215], v10\ns_waitcnt lgkmcnt(2)\nv_cvt_pk_bf16_f32 v93, v82, v83\nv_smfmac_f32_16x16x64_bf16 v[48:51], v[16:19], v[56:63], v10\nv_cvt_pk_bf16_f32 v94, v84, v85\nv_smfmac_f32_16x16x64_bf16 v[52:55], v[20:23], v[56:63], v10\ns_add_u32 s60, s55, 20480\nv_cvt_pk_bf16_f32 v95, v86, v87\n" \
"v_smfmac_f32_16x16x64_bf16 v[48:51], v[24:27], a[232:239], v10\nds_read_b128 v[56:59], v1 offset:14336\nv_and_or_b32 v92, v92, -2, v13\nv_smfmac_f32_16x16x64_bf16 v[52:55], v[28:31], a[232:239], v10\nds_read_b128 v[60:63], v1 offset:15360\nv_and_or_b32 v94, v94, -2, v13\nv_smfmac_f32_16x16x64_bf16 v[48:51], v[32:35], a[248:255], v10\nbuffer_store_dwordx4 v[92:95], v7, s[36:39], s60 offen offset:0 sc1\n" \
"v_mov_b64 v[80:81], 0\nv_mov_b64 v[82:83], 0\nv_mov_b64 v[84:85], 0\nv_smfmac_f32_16x16x64_bf16 v[52:55], v[36:39], a[248:255], v10\nv_mov_b64 v[86:87], 0\nv_smfmac_f32_16x16x64_bf16 v[80:83], v[72:75], v[104:111], v10\nv_cvt_pk_bf16_f32 v88, v48, v49\nv_smfmac_f32_16x16x64_bf16 v[84:87], v[76:79], v[104:111], v10\n" \
"v_cvt_pk_bf16_f32 v89, v50, v51\nv_smfmac_f32_16x16x64_bf16 v[80:83], v[16:19], v[120:127], v10\nv_cvt_pk_bf16_f32 v90, v52, v53\nv_smfmac_f32_16x16x64_bf16 v[84:87], v[20:23], v[120:127], v10\ns_add_u32 s60, s55, 0\ns_waitcnt lgkmcnt(2)\nv_cvt_pk_bf16_f32 v91, v54, v55\nv_smfmac_f32_16x16x64_bf16 v[80:83], v[24:27], v[64:71], v10\n" \
"v_and_or_b32 v88, v88, -2, v13\nv_smfmac_f32_16x16x64_bf16 v[84:87], v[28:31], v[64:71], v10\nv_and_or_b32 v90, v90, -2, v13\nv_smfmac_f32_16x16x64_bf16 v[80:83], v[32:35], v[136:143], v10\nbuffer_store_dwordx4 v[88:91], v7, s[36:39], s60 offen offset:2048 sc1\nv_xor_b32 v11, 128, v11\nv_mov_b64 v[48:49], 0\nv_mov_b64 v[50:51], 0\n" \
"v_mov_b64 v[52:53], 0\nv_smfmac_f32_16x16x64_bf16 v[84:87], v[36:39], v[136:143], v10\nds_read_b128 v[64:67], v1 offset:22528\nv_mov_b64 v[54:55], 0\nv_smfmac_f32_16x16x64_bf16 v[48:51], v[72:75], v[152:159], v10\nds_read_b128 v[68:71], v1 offset:23552\nv_cvt_pk_bf16_f32 v92, v80, v81\nv_smfmac_f32_16x16x64_bf16 v[52:55], v[76:79], v[152:159], v10\n" \
"s_waitcnt lgkmcnt(2)\nds_read_b128 v[40:43], v11 offset:49152\nv_cvt_pk_bf16_f32 v93, v82, v83\nv_smfmac_f32_16x16x64_bf16 v[48:51], v[16:19], v[56:63], v10\nds_read_b128 v[44:47], v11 offset:50176\nv_cvt_pk_bf16_f32 v94, v84, v85\nv_smfmac_f32_16x16x64_bf16 v[52:55], v[20:23], v[56:63], v10\ns_add_u32 s60, s55, 4096\n" \
"v_cvt_pk_bf16_f32 v95, v86, v87\nv_smfmac_f32_16x16x64_bf16 v[48:51], v[24:27], v[176:183], v10\nds_read_b128 v[56:59], v1 offset:8192\nv_and_or_b32 v92, v92, -2, v13\nv_smfmac_f32_16x16x64_bf16 v[52:55], v[28:31], v[176:183], v10\nds_read_b128 v[60:63], v1 offset:9216\nv_and_or_b32 v94, v94, -2, v13\nv_smfmac_f32_16x16x64_bf16 v[48:51], v[32:35], v[192:199], v10\n" \
"buffer_store_dwordx4 v[92:95], v7, s[36:39], s60 offen offset:2048 sc1\nv_mov_b64 v[80:81], 0\nv_mov_b64 v[82:83], 0\nv_mov_b64 v[84:85], 0\nv_smfmac_f32_16x16x64_bf16 v[52:55], v[36:39], v[192:199], v10\nv_mov_b64 v[86:87], 0\nv_smfmac_f32_16x16x64_bf16 v[80:83], v[72:75], v[208:215], v10\nds_read_b128 v[72:75], v11 offset:51200\n" \
"v_cvt_pk_bf16_f32 v88, v48, v49\nv_smfmac_f32_16x16x64_bf16 v[84:87], v[76:79], v[208:215], v10\nds_read_b128 v[76:79], v11 offset:52224\nv_cvt_pk_bf16_f32 v89, v50, v51\nv_smfmac_f32_16x16x64_bf16 v[80:83], v[16:19], v[224:231], v10\ns_add_u32 s60, s55, 16384\nds_read_b128 v[16:19], v11 offset:53248\nv_cvt_pk_bf16_f32 v90, v52, v53\n" \
"v_smfmac_f32_16x16x64_bf16 v[84:87], v[20:23], v[224:231], v10\ns_waitcnt lgkmcnt(7)\nds_read_b128 v[20:23], v11 offset:54272\nv_cvt_pk_bf16_f32 v91, v54, v55\nv_smfmac_f32_16x16x64_bf16 v[80:83], v[24:27], v[64:71], v10\nds_read_b128 v[24:27], v11 offset:55296\nv_and_or_b32 v88, v88, -2, v13\nv_smfmac_f32_16x16x64_bf16 v[84:87], v[28:31], v[64:71], v10\n" \
"ds_read_b128 v[28:31], v11 offset:56320\nv_and_or_b32 v90, v90, -2, v13\nv_smfmac_f32_16x16x64_bf16 v[80:83], v[32:35], v[248:255], v10\nbuffer_store_dwordx4 v[88:91], v7, s[36:39], s60 offen offset:2048 sc1\nv_mov_b64 v[48:49], 0\nv_mov_b64 v[50:51], 0\nv_mov_b64 v[52:53], 0\nv_smfmac_f32_16x16x64_bf16 v[84:87], v[36:39], v[248:255], v10\n" \
"s_waitcnt lgkmcnt(9)\nds_read_b128 v[64:67], v1 offset:16384\nv_mov_b64 v[54:55], 0\nv_smfmac_f32_16x16x64_bf16 v[48:51], v[40:43], a[8:15], v10\ns_waitcnt lgkmcnt(9)\nds_read_b128 v[68:71], v1 offset:17408\nv_cvt_pk_bf16_f32 v92, v80, v81\nv_smfmac_f32_16x16x64_bf16 v[52:55], v[44:47], a[8:15], v10\n" \
"s_waitcnt lgkmcnt(7)\nv_cvt_pk_bf16_f32 v93, v82, v83\nv_smfmac_f32_16x16x64_bf16 v[48:51], v[72:75], v[56:63], v10\ns_waitcnt lgkmcnt(6)\nv_cvt_pk_bf16_f32 v94, v84, v85\nv_smfmac_f32_16x16x64_bf16 v[52:55], v[76:79], v[56:63], v10\ns_add_u32 s60, s55, 20480\ns_waitcnt lgkmcnt(5)\n" \
"v_cvt_pk_bf16_f32 v95, v86, v87\nv_smfmac_f32_16x16x64_bf16 v[48:51], v[16:19], a[32:39], v10\ns_waitcnt lgkmcnt(4)\nds_read_b128 v[56:59], v1 offset:12288\nv_and_or_b32 v92, v92, -2, v13\nv_smfmac_f32_16x16x64_bf16 v[52:55], v[20:23], a[32:39], v10\ns_waitcnt lgkmcnt(4)\nds_read_b128 v[60:63], v1 offset:13312\n" \
"v_and_or_b32 v94, v94, -2, v13\nv_smfmac_f32_16x16x64_bf16 v[48:51], v[24:27], a[48:55], v10\ns_waitcnt lgkmcnt(4)\nbuffer_store_dwordx4 v[92:95], v7, s[36:39], s60 offen offset:2048 sc1\nv_xor_b32 v11, 128, v11\nv_mov_b64 v[80:81], 0\nv_mov_b64 v[82:83], 0\nv_mov_b64 v[84:85], 0\n" \
"v_smfmac_f32_16x16x64_bf16 v[52:55], v[28:31], a[48:55], v10\nv_mov_b64 v[86:87], 0\nv_smfmac_f32_16x16x64_bf16 v[80:83], v[40:43], a[56:63], v10\nv_cvt_pk_bf16_f32 v88, v48, v49\nv_smfmac_f32_16x16x64_bf16 v[84:87], v[44:47], a[56:63], v10\nv_cvt_pk_bf16_f32 v89, v50, v51\nv_smfmac_f32_16x16x64_bf16 v[80:83], v[72:75], a[72:79], v10\nv_cvt_pk_bf16_f32 v90, v52, v53\n" \
"v_smfmac_f32_16x16x64_bf16 v[84:87], v[76:79], a[72:79], v10\ns_add_u32 s60, s55, 0\ns_waitcnt lgkmcnt(2)\nv_cvt_pk_bf16_f32 v91, v54, v55\nv_smfmac_f32_16x16x64_bf16 v[80:83], v[16:19], v[64:71], v10\nv_and_or_b32 v88, v88, -2, v13\nv_smfmac_f32_16x16x64_bf16 v[84:87], v[20:23], v[64:71], v10\nv_and_or_b32 v90, v90, -2, v13\n" \
"v_smfmac_f32_16x16x64_bf16 v[80:83], v[24:27], a[96:103], v10\nbuffer_store_dwordx4 v[88:91], v7, s[36:39], s60 offen offset:512 sc1\nv_mov_b64 v[48:49], 0\nv_mov_b64 v[50:51], 0\nv_mov_b64 v[52:53], 0\nv_smfmac_f32_16x16x64_bf16 v[84:87], v[28:31], a[96:103], v10\nds_read_b128 v[64:67], v1 offset:20480\nv_mov_b64 v[54:55], 0\n" \
"v_smfmac_f32_16x16x64_bf16 v[48:51], v[40:43], a[112:119], v10\nds_read_b128 v[68:71], v1 offset:21504\nv_cvt_pk_bf16_f32 v92, v80, v81\nv_smfmac_f32_16x16x64_bf16 v[52:55], v[44:47], a[112:119], v10\ns_waitcnt lgkmcnt(2)\nv_cvt_pk_bf16_f32 v93, v82, v83\nv_smfmac_f32_16x16x64_bf16 v[48:51], v[72:75], v[56:63], v10\nv_cvt_pk_bf16_f32 v94, v84, v85\n" \
"v_smfmac_f32_16x16x64_bf16 v[52:55], v[76:79], v[56:63], v10\ns_add_u32 s60, s55, 4096\nv_cvt_pk_bf16_f32 v95, v86, v87\nv_smfmac_f32_16x16x64_bf16 v[48:51], v[16:19], a[136:143], v10\nds_read_b128 v[56:59], v1 offset:10240\nv_and_or_b32 v92, v92, -2, v13\nv_smfmac_f32_16x16x64_bf16 v[52:55], v[20:23], a[136:143], v10\nds_read_b128 v[60:63], v1 offset:11264\n" \
"v_and_or_b32 v94, v94, -2, v13\nv_smfmac_f32_16x16x64_bf16 v[48:51], v[24:27], a[144:151], v10\nbuffer_store_dwordx4 v[92:95], v7, s[36:39], s60 offen offset:512 sc1\nv_mov_b64 v[80:81], 0\nv_mov_b64 v[82:83], 0\nv_mov_b64 v[84:85], 0\nv_smfmac_f32_16x16x64_bf16 v[52:55], v[28:31], a[144:151], v10\nv_mov_b64 v[86:87], 0\n" \
"v_smfmac_f32_16x16x64_bf16 v[80:83], v[40:43], a[160:167], v10\nv_cvt_pk_bf16_f32 v88, v48, v49\nv_smfmac_f32_16x16x64_bf16 v[84:87], v[44:47], a[160:167], v10\nv_cvt_pk_bf16_f32 v89, v50, v51\nv_smfmac_f32_16x16x64_bf16 v[80:83], v[72:75], a[176:183], v10\nv_cvt_pk_bf16_f32 v90, v52, v53\nv_smfmac_f32_16x16x64_bf16 v[84:87], v[76:79], a[176:183], v10\ns_add_u32 s60, s55, 16384\n" \
"s_waitcnt lgkmcnt(2)\nv_cvt_pk_bf16_f32 v91, v54, v55\nv_smfmac_f32_16x16x64_bf16 v[80:83], v[16:19], v[64:71], v10\nv_and_or_b32 v88, v88, -2, v13\nv_smfmac_f32_16x16x64_bf16 v[84:87], v[20:23], v[64:71], v10\nv_and_or_b32 v90, v90, -2, v13\nv_smfmac_f32_16x16x64_bf16 v[80:83], v[24:27], a[200:207], v10\nbuffer_store_dwordx4 v[88:91], v7, s[36:39], s60 offen offset:512 sc1\n" \
"v_mov_b64 v[48:49], 0\nv_mov_b64 v[50:51], 0\nv_mov_b64 v[52:53], 0\nv_smfmac_f32_16x16x64_bf16 v[84:87], v[28:31], a[200:207], v10\nds_read_b128 v[64:67], v1 offset:18432\nv_mov_b64 v[54:55], 0\nv_smfmac_f32_16x16x64_bf16 v[48:51], v[40:43], a[208:215], v10\nds_read_b128 v[68:71], v1 offset:19456\n" \
"v_cvt_pk_bf16_f32 v92, v80, v81\nv_smfmac_f32_16x16x64_bf16 v[52:55], v[44:47], a[208:215], v10\ns_waitcnt lgkmcnt(2)\nv_cvt_pk_bf16_f32 v93, v82, v83\nv_smfmac_f32_16x16x64_bf16 v[48:51], v[72:75], v[56:63], v10\nv_cvt_pk_bf16_f32 v94, v84, v85\nv_smfmac_f32_16x16x64_bf16 v[52:55], v[76:79], v[56:63], v10\ns_add_u32 s60, s55, 20480\n" \
"v_cvt_pk_bf16_f32 v95, v86, v87\nv_smfmac_f32_16x16x64_bf16 v[48:51], v[16:19], a[232:239], v10\nds_read_b128 v[56:59], v1 offset:14336\nv_and_or_b32 v92, v92, -2, v13\nv_smfmac_f32_16x16x64_bf16 v[52:55], v[20:23], a[232:239], v10\nds_read_b128 v[60:63], v1 offset:15360\nv_and_or_b32 v94, v94, -2, v13\nv_smfmac_f32_16x16x64_bf16 v[48:51], v[24:27], a[248:255], v10\n" \
"buffer_store_dwordx4 v[92:95], v7, s[36:39], s60 offen offset:512 sc1\nv_mov_b64 v[80:81], 0\nv_mov_b64 v[82:83], 0\nv_mov_b64 v[84:85], 0\nv_smfmac_f32_16x16x64_bf16 v[52:55], v[28:31], a[248:255], v10\nv_mov_b64 v[86:87], 0\nv_smfmac_f32_16x16x64_bf16 v[80:83], v[40:43], v[104:111], v10\nv_cvt_pk_bf16_f32 v88, v48, v49\n" \
"v_smfmac_f32_16x16x64_bf16 v[84:87], v[44:47], v[104:111], v10\nv_cvt_pk_bf16_f32 v89, v50, v51\nv_smfmac_f32_16x16x64_bf16 v[80:83], v[72:75], v[120:127], v10\nv_cvt_pk_bf16_f32 v90, v52, v53\nv_smfmac_f32_16x16x64_bf16 v[84:87], v[76:79], v[120:127], v10\ns_add_u32 s60, s55, 0\ns_waitcnt lgkmcnt(2)\nv_cvt_pk_bf16_f32 v91, v54, v55\n" \
"v_smfmac_f32_16x16x64_bf16 v[80:83], v[16:19], v[64:71], v10\nv_and_or_b32 v88, v88, -2, v13\nv_smfmac_f32_16x16x64_bf16 v[84:87], v[20:23], v[64:71], v10\nv_and_or_b32 v90, v90, -2, v13\nv_smfmac_f32_16x16x64_bf16 v[80:83], v[24:27], v[136:143], v10\nbuffer_store_dwordx4 v[88:91], v7, s[36:39], s60 offen offset:2560 sc1\nv_mov_b64 v[48:49], 0\nv_mov_b64 v[50:51], 0\n" \
"v_mov_b64 v[52:53], 0\nv_smfmac_f32_16x16x64_bf16 v[84:87], v[28:31], v[136:143], v10\nds_read_b128 v[64:67], v1 offset:22528\nv_mov_b64 v[54:55], 0\nv_smfmac_f32_16x16x64_bf16 v[48:51], v[40:43], v[152:159], v10\nds_read_b128 v[68:71], v1 offset:23552\nv_cvt_pk_bf16_f32 v92, v80, v81\nv_smfmac_f32_16x16x64_bf16 v[52:55], v[44:47], v[152:159], v10\n" \
"s_waitcnt lgkmcnt(2)\nv_cvt_pk_bf16_f32 v93, v82, v83\nv_smfmac_f32_16x16x64_bf16 v[48:51], v[72:75], v[56:63], v10\nv_cvt_pk_bf16_f32 v94, v84, v85\nv_smfmac_f32_16x16x64_bf16 v[52:55], v[76:79], v[56:63], v10\ns_add_u32 s60, s55, 4096\nv_cvt_pk_bf16_f32 v95, v86, v87\nv_smfmac_f32_16x16x64_bf16 v[48:51], v[16:19], v[176:183], v10\n" \
"v_and_or_b32 v92, v92, -2, v13\nv_smfmac_f32_16x16x64_bf16 v[52:55], v[20:23], v[176:183], v10\nv_and_or_b32 v94, v94, -2, v13\nv_smfmac_f32_16x16x64_bf16 v[48:51], v[24:27], v[192:199], v10\nbuffer_store_dwordx4 v[92:95], v7, s[36:39], s60 offen offset:2560 sc1\nv_mov_b64 v[80:81], 0\nv_mov_b64 v[82:83], 0\nv_mov_b64 v[84:85], 0\n" \
"v_smfmac_f32_16x16x64_bf16 v[52:55], v[28:31], v[192:199], v10\nds_read_b128 v[32:35], v2 offset:0\nv_mov_b64 v[86:87], 0\nv_smfmac_f32_16x16x64_bf16 v[80:83], v[40:43], v[208:215], v10\nds_read_b128 v[36:39], v2 offset:256\nv_cvt_pk_bf16_f32 v88, v48, v49\nv_smfmac_f32_16x16x64_bf16 v[84:87], v[44:47], v[208:215], v10\nv_cvt_pk_bf16_f32 v89, v50, v51\n" \
"v_smfmac_f32_16x16x64_bf16 v[80:83], v[72:75], v[224:231], v10\ns_add_u32 s60, s55, 16384\nds_read_b128 v[40:43], v2 offset:2048\nv_cvt_pk_bf16_f32 v90, v52, v53\nv_smfmac_f32_16x16x64_bf16 v[84:87], v[76:79], v[224:231], v10\ns_waitcnt lgkmcnt(3)\nds_read_b128 v[44:47], v2 offset:2304\nv_cvt_pk_bf16_f32 v91, v54, v55\n" \
"v_smfmac_f32_16x16x64_bf16 v[80:83], v[16:19], v[64:71], v10\nv_and_or_b32 v88, v88, -2, v13\nv_smfmac_f32_16x16x64_bf16 v[84:87], v[20:23], v[64:71], v10\nv_and_or_b32 v90, v90, -2, v13\nv_smfmac_f32_16x16x64_bf16 v[80:83], v[24:27], v[248:255], v10\nbuffer_store_dwordx4 v[88:91], v7, s[36:39], s60 offen offset:2560 sc1\nv_mov_b64 v[48:49], 0\nv_mov_b64 v[50:51], 0\n" \
"v_mov_b64 v[52:53], 0\nv_mov_b32 v15, s40\nv_smfmac_f32_16x16x64_bf16 v[84:87], v[28:31], v[248:255], v10\ns_add_u32 s60, s55, 20480\ns_cmp_eq_u64 s[64:65], 0\nv_mov_b64 v[54:55], 0\ns_cbranch_scc1 .Lvu_tok%=\nv_cvt_pk_bf16_f32 v92, v80, v81\n" \
"v_cvt_pk_bf16_f32 v93, v82, v83\ns_nop 1\nv_cvt_pk_bf16_f32 v94, v84, v85\nv_cvt_pk_bf16_f32 v95, v86, v87\nv_and_or_b32 v92, v92, -2, v13\nv_and_or_b32 v94, v94, -2, v13\nbuffer_store_dwordx4 v[92:95], v7, s[36:39], s60 offen offset:2560 sc1\ns_mov_b32 s60, -2147483648\n" \
"s_cmp_lt_i32 s40, 63\ns_cbranch_scc1 .Lvu_f0%=\ns_mov_b32 s61, -2147483648\nv_add_u32 v14, s57, v0\ns_waitcnt lgkmcnt(0)\n.Lvu_nov%=:\ns_add_u32 m0, s57, 16384\ns_nop 0\n" \
"buffer_load_dwordx4 v3, s[36:39], s42 offen offset:0 sc1 lds\nbuffer_load_dwordx4 v3, s[36:39], s42 offen offset:1024 sc1 lds\nbuffer_load_dwordx4 v3, s[36:39], s42 offen offset:2048 sc1 lds\nbuffer_load_dwordx4 v3, s[36:39], s42 offen offset:3072 sc1 lds\ns_waitcnt vmcnt(0)\nds_read_b128 v[80:83], v14 offset:16384\nds_read_b128 v[84:87], v14 offset:17408\nds_read_b128 v[88:91], v14 offset:18432\n" \
"ds_read_b128 v[92:95], v14 offset:19456\ns_waitcnt lgkmcnt(2)\nv_max3_u32 v76, v80, v82, v84\ns_waitcnt lgkmcnt(1)\nv_max3_u32 v76, v76, v86, v88\ns_waitcnt lgkmcnt(0)\nv_max3_u32 v76, v76, v90, v92\nv_max_u32 v76, v76, v94\n" \
"v_cmp_eq_u32 vcc, -1, v76\ns_cbranch_vccz .Lvu_novfok%=\ns_add_u32 s43, s43, 1\ns_cmp_lt_u32 s43, 262144\ns_cbranch_scc1 .Lvu_nov%=\ns_branch .Lvu_abort%=\n.Lvu_novfok%=:\ns_waitcnt lgkmcnt(0)\n" \
"s_barrier\ns_memrealtime s[68:69]\nds_read_b128 v[56:59], v1 offset:0\nds_read_b128 v[60:63], v1 offset:1024\nds_read_b128 v[80:83], v0 offset:16384\nds_read_b128 v[84:87], v0 offset:17408\nds_read_b128 v[88:91], v0 offset:18432\nds_read_b128 v[92:95], v0 offset:19456\n" \
"ds_read_b128 v[16:19], v0 offset:20480\nds_read_b128 v[20:23], v0 offset:21504\nds_read_b128 v[32:35], v0 offset:22528\nds_read_b128 v[36:39], v0 offset:23552\ns_waitcnt lgkmcnt(0)\ns_branch .Lvu_crit%=\n.Lvu_re%=:\n.Lvu_re2%=:\n" \
"s_add_u32 m0, s57, 16384\nv_max_u32 v76, v80, v82\nv_cmp_eq_u32 vcc, -1, v76\ns_cbranch_vccz .Lvu_sk1893%=\ns_mov_b64 exec, vcc\nbuffer_load_dwordx4 v3, s[36:39], s42 offen offset:0 sc1 lds\ns_mov_b64 exec, -1\n.Lvu_sk1893%=:\n" \
"v_max_u32 v76, v84, v86\nv_cmp_eq_u32 vcc, -1, v76\ns_cbranch_vccz .Lvu_sk1900%=\ns_mov_b64 exec, vcc\nbuffer_load_dwordx4 v3, s[36:39], s42 offen offset:1024 sc1 lds\ns_mov_b64 exec, -1\n.Lvu_sk1900%=:\nv_max_u32 v76, v88, v90\n" \
"v_cmp_eq_u32 vcc, -1, v76\ns_cbranch_vccz .Lvu_sk1907%=\ns_mov_b64 exec, vcc\nbuffer_load_dwordx4 v3, s[36:39], s42 offen offset:2048 sc1 lds\ns_mov_b64 exec, -1\n.Lvu_sk1907%=:\nv_max_u32 v76, v92, v94\nv_cmp_eq_u32 vcc, -1, v76\n" \
"s_cbranch_vccz .Lvu_sk1914%=\ns_mov_b64 exec, vcc\nbuffer_load_dwordx4 v3, s[36:39], s42 offen offset:3072 sc1 lds\ns_mov_b64 exec, -1\n.Lvu_sk1914%=:\n.Lvu_skf%=:\ns_waitcnt vmcnt(0)\nds_read_b128 v[80:83], v14 offset:16384\n" \
"ds_read_b128 v[84:87], v14 offset:17408\nds_read_b128 v[88:91], v14 offset:18432\nds_read_b128 v[92:95], v14 offset:19456\ns_waitcnt lgkmcnt(2)\nv_max3_u32 v76, v80, v82, v84\ns_waitcnt lgkmcnt(1)\nv_max3_u32 v76, v76, v86, v88\ns_waitcnt lgkmcnt(0)\n" \
"v_max3_u32 v76, v76, v90, v92\nv_max_u32 v76, v76, v94\nv_cmp_eq_u32 vcc, -1, v76\ns_cbranch_vccz .Lvu_rer%=\ns_add_u32 s43, s43, 1\ns_cmp_lt_u32 s43, 262144\ns_cbranch_scc1 .Lvu_re2%=\ns_branch .Lvu_abort%=\n" \
".Lvu_rew%=:\nds_read_b128 v[80:83], v14 offset:16384\nds_read_b128 v[84:87], v14 offset:17408\nds_read_b128 v[88:91], v14 offset:18432\nds_read_b128 v[92:95], v14 offset:19456\ns_waitcnt lgkmcnt(0)\ns_branch .Lvu_rer%=\n.Lvu_first%=:\n" \
"s_nop 7\nv_cvt_pk_bf16_f32 v76, v24, v25\nv_cvt_pk_bf16_f32 v77, v26, v27\nv_cvt_pk_bf16_f32 v78, v28, v29\nv_cvt_pk_bf16_f32 v79, v30, v31\nds_write2st64_b64 v9, v[76:77], v[78:79] offset0:2 offset1:10\ns_waitcnt vmcnt(0) lgkmcnt(0)\ns_barrier\n" \
"ds_read_b128 v[56:59], v1 offset:8192\nds_read_b128 v[60:63], v1 offset:9216\nds_read_b128 v[64:67], v1 offset:16384\nds_read_b128 v[68:71], v1 offset:17408\nds_read_b128 v[72:75], v11 offset:49152\nds_read_b128 v[76:79], v11 offset:50176\nds_read_b128 v[16:19], v11 offset:51200\nds_read_b128 v[20:23], v11 offset:52224\n" \
"ds_read_b128 v[24:27], v11 offset:53248\nds_read_b128 v[28:31], v11 offset:54272\nds_read_b128 v[32:35], v11 offset:55296\nds_read_b128 v[36:39], v11 offset:56320\ns_waitcnt lgkmcnt(0)\ns_add_u32 m0, s57, 0\ns_add_u32 s58, s61, 0\nbuffer_load_dwordx4 v0, s[36:39], s58 offen offset:0 sc1 lds\n" \
"buffer_load_dwordx4 v0, s[36:39], s58 offen offset:1024 sc1 lds\nbuffer_load_dwordx4 v0, s[36:39], s58 offen offset:2048 sc1 lds\nbuffer_load_dwordx4 v0, s[36:39], s58 offen offset:3072 sc1 lds\ns_mov_b32 s59, -2147483648\nbuffer_store_dword v14, off, s[36:39], s59\nbuffer_store_dword v14, off, s[36:39], s59\nbuffer_store_dword v14, off, s[36:39], s59\ns_branch .Lvu_y%=\n" \
".Lvu_last%=:\ns_nop 7\nv_cvt_pk_bf16_f32 v92, v40, v41\nv_and_or_b32 v92, v92, -2, v12\ns_add_u32 s63, s59, 4096\nbuffer_store_dwordx4 v[88:91], v6, s[36:39], s63 offen sc1\nv_cvt_pk_bf16_f32 v93, v42, v43\nv_cvt_pk_bf16_f32 v94, v44, v45\n" \
"v_cvt_pk_bf16_f32 v95, v46, v47\nv_and_or_b32 v94, v94, -2, v12\ns_nop 0\nv_permlane16_swap_b32 v93, v95\nv_permlane16_swap_b32 v92, v94\ns_add_u32 s63, s59, 135168\nbuffer_store_dwordx4 v[92:95], v6, s[36:39], s63 offen sc1\ns_branch .Lvu_end%=\n" \
".Lvu_abort%=:\ns_mov_b64 exec, -1\nv_mov_b32 v16, 0\ns_movk_i32 s44, 6400\nbuffer_store_dword v16, off, s[36:39], s44 sc1\n.Lvu_end%=:\ns_waitcnt vmcnt(0) lgkmcnt(0)\n" \
""
#define RD_ASM \
"v_mbcnt_lo_u32_b32 v255, -1, 0\nv_mbcnt_hi_u32_b32 v255, -1, v255\nv_lshlrev_b32 v255, 2, v255\nv_add_u32 v255, %[tb], v255\nds_read_b32 v196, v255 offset:0\nds_read_b32 v197, v255 offset:256\nds_read_b32 v198, v255 offset:512\nds_read_b32 v199, v255 offset:768\n" \
"ds_read_b32 v200, v255 offset:1024\nds_read_b32 v201, v255 offset:1280\nds_read_b32 v202, v255 offset:1536\nds_read_b32 v203, v255 offset:1792\nds_read_b32 v204, v255 offset:2048\nds_read_b32 v205, v255 offset:2304\nds_read_b32 v206, v255 offset:2560\nds_read_b32 v207, v255 offset:2816\n" \
"s_waitcnt lgkmcnt(0)\nv_accvgpr_write_b32 a32, v206\nv_accvgpr_write_b32 a33, v207\ns_mov_b32 s40, 0\ns_mov_b32 s43, 0\ns_add_u32 s57, %[su], 4096\ns_add_u32 s56, %[sp], 4096\ns_mov_b32 s41, %[sy]\n" \
"s_mov_b32 s59, -2147483648\n.Lrd_yflag%=:\nv_accvgpr_read_b32 v206, a32\nbuffer_load_dword v208, v206, %[rs], 0 offen sc1\ns_waitcnt vmcnt(0)\nv_cmp_gt_i32 vcc, s40, v208\ns_cbranch_vccz .Lrd_yld%=\ns_add_u32 s43, s43, 1\n" \
"s_cmp_lt_u32 s43, 262144\ns_cbranch_scc1 .Lrd_yflag%=\ns_branch .Lrd_abort%=\n.Lrd_yld%=:\ns_add_u32 s60, s41, 0\nbuffer_load_dwordx4 v[0:3], v196, %[rs], s60 offen offset:0 sc1\ns_add_u32 s60, s41, 0\nbuffer_load_dwordx4 v[32:35], v196, %[rs], s60 offen offset:1024 sc1\n" \
"s_add_u32 s60, s41, 0\nbuffer_load_dwordx4 v[64:67], v196, %[rs], s60 offen offset:2048 sc1\ns_add_u32 s60, s41, 131072\nbuffer_load_dwordx4 v[4:7], v196, %[rs], s60 offen offset:0 sc1\ns_add_u32 s60, s41, 131072\nbuffer_load_dwordx4 v[36:39], v196, %[rs], s60 offen offset:1024 sc1\ns_add_u32 s60, s41, 131072\nbuffer_load_dwordx4 v[68:71], v196, %[rs], s60 offen offset:2048 sc1\n" \
"s_add_u32 s60, s41, 262144\nbuffer_load_dwordx4 v[8:11], v196, %[rs], s60 offen offset:0 sc1\ns_add_u32 s60, s41, 262144\nbuffer_load_dwordx4 v[40:43], v196, %[rs], s60 offen offset:1024 sc1\ns_add_u32 s60, s41, 262144\nbuffer_load_dwordx4 v[72:75], v196, %[rs], s60 offen offset:2048 sc1\ns_add_u32 s60, s41, 393216\nbuffer_load_dwordx4 v[12:15], v196, %[rs], s60 offen offset:0 sc1\n" \
"s_add_u32 s60, s41, 393216\nbuffer_load_dwordx4 v[44:47], v196, %[rs], s60 offen offset:1024 sc1\ns_add_u32 s60, s41, 393216\nbuffer_load_dwordx4 v[76:79], v196, %[rs], s60 offen offset:2048 sc1\ns_add_u32 s60, s41, 524288\nbuffer_load_dwordx4 v[16:19], v196, %[rs], s60 offen offset:0 sc1\ns_add_u32 s60, s41, 524288\nbuffer_load_dwordx4 v[48:51], v196, %[rs], s60 offen offset:1024 sc1\n" \
"s_add_u32 s60, s41, 524288\nbuffer_load_dwordx4 v[80:83], v196, %[rs], s60 offen offset:2048 sc1\ns_add_u32 s60, s41, 655360\nbuffer_load_dwordx4 v[20:23], v196, %[rs], s60 offen offset:0 sc1\ns_add_u32 s60, s41, 655360\nbuffer_load_dwordx4 v[52:55], v196, %[rs], s60 offen offset:1024 sc1\ns_add_u32 s60, s41, 655360\nbuffer_load_dwordx4 v[84:87], v196, %[rs], s60 offen offset:2048 sc1\n" \
"s_add_u32 s60, s41, 786432\nbuffer_load_dwordx4 v[24:27], v196, %[rs], s60 offen offset:0 sc1\ns_add_u32 s60, s41, 786432\nbuffer_load_dwordx4 v[56:59], v196, %[rs], s60 offen offset:1024 sc1\ns_add_u32 s60, s41, 786432\nbuffer_load_dwordx4 v[88:91], v196, %[rs], s60 offen offset:2048 sc1\ns_add_u32 s60, s41, 917504\nbuffer_load_dwordx4 v[28:31], v196, %[rs], s60 offen offset:0 sc1\n" \
"s_add_u32 s60, s41, 917504\nbuffer_load_dwordx4 v[60:63], v196, %[rs], s60 offen offset:1024 sc1\ns_add_u32 s60, s41, 917504\nbuffer_load_dwordx4 v[92:95], v196, %[rs], s60 offen offset:2048 sc1\ns_add_u32 s60, s41, 0\nbuffer_load_dwordx4 v[96:99], v196, %[rs], s60 offen offset:3072 sc1\ns_add_u32 s60, s41, 131072\nbuffer_load_dwordx4 v[100:103], v196, %[rs], s60 offen offset:3072 sc1\n" \
"s_add_u32 s60, s41, 262144\nbuffer_load_dwordx4 v[104:107], v196, %[rs], s60 offen offset:3072 sc1\ns_add_u32 s60, s41, 393216\nbuffer_load_dwordx4 v[108:111], v196, %[rs], s60 offen offset:3072 sc1\ns_add_u32 s60, s41, 524288\nbuffer_load_dwordx4 v[112:115], v196, %[rs], s60 offen offset:3072 sc1\ns_add_u32 s60, s41, 655360\nbuffer_load_dwordx4 v[116:119], v196, %[rs], s60 offen offset:3072 sc1\n" \
"s_add_u32 s60, s41, 786432\nbuffer_load_dwordx4 v[120:123], v196, %[rs], s60 offen offset:3072 sc1\ns_add_u32 s60, s41, 917504\nbuffer_load_dwordx4 v[124:127], v196, %[rs], s60 offen offset:3072 sc1\ns_mov_b64 exec, -1\nv_accvgpr_read_b32 v212, a33\nbuffer_load_dwordx2 v[214:215], v212, %[rs], 0 offen sc1\ns_waitcnt vmcnt(0)\n" \
"s_branch .Lrd_ychk%=\n.Lrd_u2%=:\nbuffer_load_dwordx4 v[136:139], v196, %[rs], %[su] offen offset:2048 sc1\nbuffer_load_dwordx4 v[140:143], v196, %[rs], %[su] offen offset:3072 sc1\ns_add_u32 s43, s43, 1\ns_cmp_ge_u32 s43, 262144\ns_cbranch_scc1 .Lrd_abort%=\ns_waitcnt vmcnt(0)\n" \
"v_bitop3_b32 v208, v136, v138, s54 bitop3:0x7e\nv_bitop3_b32 v209, v140, v142, s54 bitop3:0x7e\nv_bitop3_b32 v208, v208, 1, v209 bitop3:0xc8\nv_cmp_ne_u32 vcc, 0, v208\ns_cbranch_vccnz .Lrd_u2%=\n.Lrd_u3%=:\nbuffer_load_dwordx4 v[128:131], v196, %[rs], %[su] offen offset:0 sc1\nbuffer_load_dwordx4 v[132:135], v196, %[rs], %[su] offen offset:1024 sc1\n" \
"buffer_load_dwordx4 v[144:147], v196, %[rs], s57 offen offset:0 sc1\nbuffer_load_dwordx4 v[148:151], v196, %[rs], s57 offen offset:1024 sc1\nbuffer_load_dwordx4 v[152:155], v196, %[rs], s57 offen offset:2048 sc1\nbuffer_load_dwordx4 v[156:159], v196, %[rs], s57 offen offset:3072 sc1\nbuffer_load_dwordx4 v[160:163], v196, %[rs], %[sp] offen offset:0 sc1\nbuffer_load_dwordx4 v[164:167], v196, %[rs], %[sp] offen offset:1024 sc1\nbuffer_load_dwordx4 v[168:171], v196, %[rs], %[sp] offen offset:2048 sc1\nbuffer_load_dwordx4 v[172:175], v196, %[rs], %[sp] offen offset:3072 sc1\n" \
"buffer_load_dwordx4 v[176:179], v196, %[rs], s56 offen offset:0 sc1\n.p2align 6\n.Lrd_ra%=:\ns_waitcnt vmcnt(10)\nv_bitop3_b32 v208, v128, v130, s54 bitop3:0x7e\nv_bitop3_b32 v209, v136, v138, s54 bitop3:0x7e\nv_bitop3_b32 v208, v208, 1, v209 bitop3:0xc8\nv_cmp_ne_u32 s[86:87], 0, v208\n" \
"v_bitop3_b32 v208, v144, v146, s54 bitop3:0x7e\nv_bitop3_b32 v209, v152, v154, s54 bitop3:0x7e\nv_bitop3_b32 v208, v208, 1, v209 bitop3:0xc8\nv_cmp_ne_u32 s[88:89], 0, v208\nv_bitop3_b32 v208, v132, v134, s54 bitop3:0x7e\nv_bitop3_b32 v209, v140, v142, s54 bitop3:0x7e\nv_bitop3_b32 v208, v208, 1, v209 bitop3:0xc8\nv_cmp_ne_u32 s[68:69], 0, v208\n" \
"v_bitop3_b32 v208, v148, v150, s54 bitop3:0x7e\nv_bitop3_b32 v209, v156, v158, s54 bitop3:0x7e\nv_bitop3_b32 v208, v208, 1, v209 bitop3:0xc8\nv_cmp_ne_u32 s[62:63], 0, v208\ns_or_b64 vcc, s[86:87], s[88:89]\ns_or_b64 vcc, vcc, s[68:69]\ns_or_b64 vcc, vcc, s[62:63]\ns_waitcnt vmcnt(9)\n" \
"v_smfmac_f32_16x16x64_bf16 v[180:183], v[160:163], v[0:7], v197\nv_bitop3_b32 v208, v160, v162, s54 bitop3:0x7e\nv_smfmac_f32_16x16x64_bf16 v[184:187], v[160:163], v[32:39], v197\nv_cmp_gt_i32 s[46:47], 0, v208\nv_smfmac_f32_16x16x64_bf16 v[188:191], v[160:163], v[64:71], v197\nv_smfmac_f32_16x16x64_bf16 v[192:195], v[160:163], v[96:103], v197\ns_or_b64 vcc, vcc, s[46:47]\ns_waitcnt vmcnt(8)\n" \
"v_smfmac_f32_16x16x64_bf16 v[180:183], v[164:167], v[8:15], v197\nv_bitop3_b32 v208, v164, v166, s54 bitop3:0x7e\nv_smfmac_f32_16x16x64_bf16 v[184:187], v[164:167], v[40:47], v197\nv_cmp_gt_i32 s[48:49], 0, v208\nv_smfmac_f32_16x16x64_bf16 v[188:191], v[164:167], v[72:79], v197\nv_smfmac_f32_16x16x64_bf16 v[192:195], v[164:167], v[104:111], v197\ns_or_b64 vcc, vcc, s[48:49]\ns_waitcnt vmcnt(7)\n" \
"v_smfmac_f32_16x16x64_bf16 v[180:183], v[168:171], v[16:23], v197\nv_bitop3_b32 v208, v168, v170, s54 bitop3:0x7e\nv_smfmac_f32_16x16x64_bf16 v[184:187], v[168:171], v[48:55], v197\nv_cmp_gt_i32 s[50:51], 0, v208\nv_smfmac_f32_16x16x64_bf16 v[188:191], v[168:171], v[80:87], v197\nv_smfmac_f32_16x16x64_bf16 v[192:195], v[168:171], v[112:119], v197\ns_or_b64 vcc, vcc, s[50:51]\ns_waitcnt vmcnt(6)\n" \
"v_smfmac_f32_16x16x64_bf16 v[180:183], v[172:175], v[24:31], v197\nv_bitop3_b32 v208, v172, v174, s54 bitop3:0x7e\nv_smfmac_f32_16x16x64_bf16 v[184:187], v[172:175], v[56:63], v197\nv_cmp_gt_i32 s[52:53], 0, v208\nv_smfmac_f32_16x16x64_bf16 v[188:191], v[172:175], v[88:95], v197\nv_smfmac_f32_16x16x64_bf16 v[192:195], v[172:175], v[120:127], v197\ns_or_b64 vcc, vcc, s[52:53]\ns_waitcnt vmcnt(5)\n" \
"v_smfmac_f32_16x16x64_bf16 v[180:183], v[176:179], v[128:135], v197\nv_bitop3_b32 v208, v176, v178, s54 bitop3:0x7e\nv_smfmac_f32_16x16x64_bf16 v[184:187], v[176:179], v[136:143], v197\nv_cmp_gt_i32 s[64:65], 0, v208\nv_smfmac_f32_16x16x64_bf16 v[188:191], v[176:179], v[144:151], v197\ns_or_b64 vcc, vcc, s[64:65]\ns_cbranch_scc1 .Lrd_rb%=\nv_smfmac_f32_16x16x64_bf16 v[192:195], v[176:179], v[152:159], v197\n" \
".Lrd_rd0%=:\nv_add_f32 v180, v180, v182\nv_add_f32 v181, v181, v183\nds_write2_b32 v198, v180, v181 offset0:0 offset1:8\nv_add_f32 v184, v184, v186\nv_add_f32 v185, v185, v187\nv_add_f32 v188, v188, v190\nv_add_f32 v189, v189, v191\n" \
"ds_write2_b32 v198, v184, v185 offset0:32 offset1:40\nv_add_f32 v192, v192, v194\nv_add_f32 v193, v193, v195\nds_write2_b32 v198, v188, v189 offset0:64 offset1:72\nds_write2_b32 v198, v192, v193 offset0:96 offset1:104\ns_add_u32 s60, s58, 0\nbuffer_load_dwordx4 v[232:235], v196, %[rs], s60 offen offset:3072 sc1\ns_add_u32 s60, s58, 16384\n" \
"buffer_load_dwordx4 v[236:239], v196, %[rs], s60 offen offset:2048 sc1\ns_waitcnt lgkmcnt(0)\ns_barrier\nds_read_b128 v[180:183], v199 offset:0\nds_read_b128 v[184:187], v199 offset:4096\nds_read_b128 v[188:191], v199 offset:8192\nds_read_b128 v[192:195], v199 offset:12288\ns_and_b32 s44, s54, 0x80008000\n" \
"s_add_u32 s60, s58, 4096\nbuffer_load_dwordx4 v[240:243], v196, %[rs], s60 offen offset:0 sc1\ns_add_u32 s60, s58, 4096\nbuffer_load_dwordx4 v[244:247], v196, %[rs], s60 offen offset:1024 sc1\ns_waitcnt lgkmcnt(2)\nv_pk_add_f32 v[182:183], v[182:183], v[186:187]\nv_pk_add_f32 v[180:181], v[180:181], v[184:185]\ns_waitcnt lgkmcnt(0)\n" \
"v_pk_add_f32 v[186:187], v[188:189], v[192:193]\nv_pk_add_f32 v[184:185], v[190:191], v[194:195]\nv_pk_add_f32 v[180:181], v[180:181], v[186:187]\nv_pk_add_f32 v[182:183], v[182:183], v[184:185]\nv_cvt_pk_bf16_f32 v180, v180, v181\nv_xor_b32 v180, s44, v180\nv_cvt_pk_bf16_f32 v181, v182, v183\nv_min_u32 v180, -2, v180\n" \
"v_xor_b32 v209, s44, v181\nv_or_b32 v208, v180, v201\nv_mov_b32 v211, v209\nv_or_b32 v210, v180, v201\ns_nop 0\nv_permlane32_swap_b32 v209, v211\nv_permlane32_swap_b32 v208, v210\nbuffer_store_dwordx4 v[208:211], v206, %[rs], 0 offen sc1\n" \
"s_add_u32 s60, s58, 16384\nbuffer_load_dwordx4 v[248:251], v196, %[rs], s60 offen offset:3072 sc1\ns_add_u32 s60, s58, 20480\nbuffer_load_dwordx4 v[252:255], v196, %[rs], s60 offen offset:0 sc1\ns_add_u32 s60, s58, 20480\nbuffer_load_dwordx4 v[160:163], v196, %[rs], s60 offen offset:2048 sc1\ns_xor_b32 s41, s41, 4194304\nv_mov_b32 v207, s40\n" \
"s_waitcnt vmcnt(12)\nv_bitop3_b32 v209, v212, v214, s61 bitop3:0x7e\nv_cmp_gt_i32 vcc, 0, v209\ns_cbranch_vccnz .Lrd_tre0%=\n.Lrd_tl0%=:\nv_mfma_f32_16x16x16_bf16 a[0:3], v[0:1], v[212:213], 0\ns_add_u32 s60, s58, 4096\nbuffer_load_dwordx4 v[164:167], v196, %[rs], s60 offen offset:2048 sc1\n" \
"v_mfma_f32_16x16x16_bf16 a[8:11], v[32:33], v[212:213], 0\nv_mfma_f32_16x16x16_bf16 a[16:19], v[64:65], v[212:213], 0\nv_mfma_f32_16x16x16_bf16 a[24:27], v[96:97], v[212:213], 0\ns_waitcnt vmcnt(12)\nv_mfma_f32_16x16x16_bf16 a[0:3], v[4:5], v[214:215], a[0:3]\ns_add_u32 s60, s58, 4096\nbuffer_load_dwordx4 v[168:171], v196, %[rs], s60 offen offset:3072 sc1\nv_bitop3_b32 v209, v216, v218, s61 bitop3:0x7e\n" \
"v_mfma_f32_16x16x16_bf16 a[8:11], v[36:37], v[214:215], a[8:11]\ns_lshl_b32 s42, s40, 17\ns_add_u32 s42, s42, %[at]\nv_mfma_f32_16x16x16_bf16 a[16:19], v[68:69], v[214:215], a[16:19]\nv_cmp_gt_i32 vcc, 0, v209\nv_mfma_f32_16x16x16_bf16 a[24:27], v[100:101], v[214:215], a[24:27]\ns_cbranch_vccnz .Lrd_tre1%=\n.Lrd_tl1%=:\n" \
"s_waitcnt vmcnt(12)\nv_mfma_f32_16x16x32_bf16 a[4:7], v[0:3], v[216:219], 0\ns_add_u32 s60, s58, 20480\nbuffer_load_dwordx4 v[172:175], v196, %[rs], s60 offen offset:1024 sc1\nv_bitop3_b32 v209, v220, v222, s61 bitop3:0x7e\nv_mfma_f32_16x16x32_bf16 a[12:15], v[32:35], v[216:219], 0\ns_lshr_b32 s45, s42, 8\ns_and_b32 s45, s45, 0x2000\n" \
"v_mfma_f32_16x16x32_bf16 a[20:23], v[64:67], v[216:219], 0\nv_cmp_gt_i32 vcc, 0, v209\nv_mfma_f32_16x16x32_bf16 a[28:31], v[96:99], v[216:219], 0\ns_cbranch_vccnz .Lrd_tre2%=\n.Lrd_tl2%=:\ns_waitcnt vmcnt(12)\nv_mfma_f32_16x16x32_bf16 a[4:7], v[4:7], v[220:223], a[4:7]\ns_add_u32 s60, s58, 20480\n" \
"buffer_load_dwordx4 v[176:179], v196, %[rs], s60 offen offset:3072 sc1\nv_bitop3_b32 v209, v224, v226, s61 bitop3:0x7e\nv_mfma_f32_16x16x32_bf16 a[12:15], v[36:39], v[220:223], a[12:15]\ns_xor_b32 s42, s42, s45\ns_add_u32 s40, s40, 1\nv_mfma_f32_16x16x32_bf16 a[20:23], v[68:71], v[220:223], a[20:23]\nv_cmp_gt_i32 vcc, 0, v209\nv_mfma_f32_16x16x32_bf16 a[28:31], v[100:103], v[220:223], a[28:31]\n" \
"s_cbranch_vccnz .Lrd_tre3%=\n.Lrd_tl3%=:\nv_mfma_f32_16x16x16_bf16 a[0:3], v[8:9], v[224:225], a[0:3]\ns_cmp_eq_u32 s40, 1\ns_cbranch_scc1 .Lrd_y1%=\n.Lrd_y1r%=:\ns_add_u32 s60, s41, 0\nbuffer_load_dwordx4 v[0:3], v196, %[rs], s60 offen offset:0 sc1\n" \
"v_mfma_f32_16x16x16_bf16 a[8:11], v[40:41], v[224:225], a[8:11]\nv_mfma_f32_16x16x16_bf16 a[16:19], v[72:73], v[224:225], a[16:19]\nbuffer_load_dwordx4 v[212:215], v204, %[rs], s42 offen sc1\nv_mfma_f32_16x16x16_bf16 a[24:27], v[104:105], v[224:225], a[24:27]\ns_waitcnt vmcnt(14)\nv_mfma_f32_16x16x16_bf16 a[0:3], v[12:13], v[226:227], a[0:3]\ns_add_u32 s60, s41, 0\nbuffer_load_dwordx4 v[32:35], v196, %[rs], s60 offen offset:1024 sc1\n" \
"v_bitop3_b32 v209, v228, v230, s61 bitop3:0x7e\nv_mfma_f32_16x16x16_bf16 a[8:11], v[44:45], v[226:227], a[8:11]\ns_lshl_b32 s62, s55, 31\ns_or_b32 s62, s62, 1.0\ns_mov_b32 s63, s62\nv_mfma_f32_16x16x16_bf16 a[16:19], v[76:77], v[226:227], a[16:19]\nv_cmp_gt_i32 vcc, 0, v209\nv_mfma_f32_16x16x16_bf16 a[24:27], v[108:109], v[226:227], a[24:27]\n" \
"s_cbranch_vccnz .Lrd_tre4%=\n.Lrd_tl4%=:\ns_waitcnt vmcnt(14)\nv_mfma_f32_16x16x32_bf16 a[4:7], v[8:11], v[228:231], a[4:7]\ns_add_u32 s60, s41, 0\nbuffer_load_dwordx4 v[64:67], v196, %[rs], s60 offen offset:2048 sc1\nv_bitop3_b32 v209, v232, v234, s61 bitop3:0x7e\nv_mfma_f32_16x16x32_bf16 a[12:15], v[40:43], v[228:231], a[12:15]\n" \
"v_mfma_f32_16x16x32_bf16 a[20:23], v[72:75], v[228:231], a[20:23]\nv_cmp_gt_i32 vcc, 0, v209\nv_mfma_f32_16x16x32_bf16 a[28:31], v[104:107], v[228:231], a[28:31]\ns_cbranch_vccnz .Lrd_tre5%=\n.Lrd_tl5%=:\ns_waitcnt vmcnt(14)\nv_mfma_f32_16x16x32_bf16 a[4:7], v[12:15], v[232:235], a[4:7]\ns_add_u32 s60, s41, 131072\n" \
"buffer_load_dwordx4 v[4:7], v196, %[rs], s60 offen offset:0 sc1\nv_bitop3_b32 v209, v236, v238, s61 bitop3:0x7e\nv_mfma_f32_16x16x32_bf16 a[12:15], v[44:47], v[232:235], a[12:15]\nv_mfma_f32_16x16x32_bf16 a[20:23], v[76:79], v[232:235], a[20:23]\nv_cmp_gt_i32 vcc, 0, v209\nv_mfma_f32_16x16x32_bf16 a[28:31], v[108:111], v[232:235], a[28:31]\ns_cbranch_vccnz .Lrd_tre6%=\n.Lrd_tl6%=:\n" \
"v_mfma_f32_16x16x16_bf16 a[0:3], v[16:17], v[236:237], a[0:3]\ns_add_u32 s60, s41, 131072\nbuffer_load_dwordx4 v[36:39], v196, %[rs], s60 offen offset:1024 sc1\nv_mfma_f32_16x16x16_bf16 a[8:11], v[48:49], v[236:237], a[8:11]\nv_mfma_f32_16x16x16_bf16 a[16:19], v[80:81], v[236:237], a[16:19]\nv_mfma_f32_16x16x16_bf16 a[24:27], v[112:113], v[236:237], a[24:27]\ns_waitcnt vmcnt(15)\nv_mfma_f32_16x16x16_bf16 a[0:3], v[20:21], v[238:239], a[0:3]\n" \
"s_add_u32 s60, s41, 131072\nbuffer_load_dwordx4 v[68:71], v196, %[rs], s60 offen offset:2048 sc1\nv_bitop3_b32 v209, v240, v242, s61 bitop3:0x7e\nv_mfma_f32_16x16x16_bf16 a[8:11], v[52:53], v[238:239], a[8:11]\nv_mfma_f32_16x16x16_bf16 a[16:19], v[84:85], v[238:239], a[16:19]\nv_cmp_gt_i32 vcc, 0, v209\nv_mfma_f32_16x16x16_bf16 a[24:27], v[116:117], v[238:239], a[24:27]\ns_cbranch_vccnz .Lrd_tre7%=\n" \
".Lrd_tl7%=:\ns_waitcnt vmcnt(15)\nv_mfma_f32_16x16x32_bf16 a[4:7], v[16:19], v[240:243], a[4:7]\ns_add_u32 s60, s41, 262144\nbuffer_load_dwordx4 v[8:11], v196, %[rs], s60 offen offset:0 sc1\nv_bitop3_b32 v209, v244, v246, s61 bitop3:0x7e\nv_mfma_f32_16x16x32_bf16 a[12:15], v[48:51], v[240:243], a[12:15]\nv_mfma_f32_16x16x32_bf16 a[20:23], v[80:83], v[240:243], a[20:23]\n" \
"v_cmp_gt_i32 vcc, 0, v209\nv_mfma_f32_16x16x32_bf16 a[28:31], v[112:115], v[240:243], a[28:31]\ns_cbranch_vccnz .Lrd_tre8%=\n.Lrd_tl8%=:\ns_waitcnt vmcnt(14)\nv_mfma_f32_16x16x32_bf16 a[4:7], v[20:23], v[244:247], a[4:7]\ns_add_u32 s60, s41, 262144\nbuffer_load_dwordx4 v[40:43], v196, %[rs], s60 offen offset:1024 sc1\n" \
"v_bitop3_b32 v209, v248, v250, s61 bitop3:0x7e\nv_mfma_f32_16x16x32_bf16 a[12:15], v[52:55], v[244:247], a[12:15]\nv_mfma_f32_16x16x32_bf16 a[20:23], v[84:87], v[244:247], a[20:23]\nv_cmp_gt_i32 vcc, 0, v209\nv_mfma_f32_16x16x32_bf16 a[28:31], v[116:119], v[244:247], a[28:31]\ns_cbranch_vccnz .Lrd_tre9%=\n.Lrd_tl9%=:\nv_mfma_f32_16x16x16_bf16 a[0:3], v[24:25], v[248:249], a[0:3]\n" \
"s_add_u32 s60, s41, 262144\nbuffer_load_dwordx4 v[72:75], v196, %[rs], s60 offen offset:2048 sc1\nv_mfma_f32_16x16x16_bf16 a[8:11], v[56:57], v[248:249], a[8:11]\nv_mfma_f32_16x16x16_bf16 a[16:19], v[88:89], v[248:249], a[16:19]\nv_mfma_f32_16x16x16_bf16 a[24:27], v[120:121], v[248:249], a[24:27]\ns_waitcnt vmcnt(15)\nv_mfma_f32_16x16x16_bf16 a[0:3], v[28:29], v[250:251], a[0:3]\ns_add_u32 s60, s41, 393216\n" \
"buffer_load_dwordx4 v[12:15], v196, %[rs], s60 offen offset:0 sc1\nv_bitop3_b32 v209, v252, v254, s61 bitop3:0x7e\nv_mfma_f32_16x16x16_bf16 a[8:11], v[60:61], v[250:251], a[8:11]\nv_mfma_f32_16x16x16_bf16 a[16:19], v[92:93], v[250:251], a[16:19]\nv_cmp_gt_i32 vcc, 0, v209\nv_mfma_f32_16x16x16_bf16 a[24:27], v[124:125], v[250:251], a[24:27]\ns_cbranch_vccnz .Lrd_tre10%=\n.Lrd_tl10%=:\n" \
"s_waitcnt vmcnt(15)\nv_mfma_f32_16x16x32_bf16 a[0:3], v[128:131], v[252:255], a[0:3]\ns_add_u32 s60, s41, 393216\nbuffer_load_dwordx4 v[44:47], v196, %[rs], s60 offen offset:1024 sc1\nv_bitop3_b32 v209, v160, v162, s61 bitop3:0x7e\nv_mfma_f32_16x16x32_bf16 a[8:11], v[136:139], v[252:255], a[8:11]\nv_mfma_f32_16x16x32_bf16 a[16:19], v[144:147], v[252:255], a[16:19]\nv_cmp_gt_i32 vcc, 0, v209\n" \
"v_mfma_f32_16x16x32_bf16 a[24:27], v[152:155], v[252:255], a[24:27]\ns_cbranch_vccnz .Lrd_tre11%=\n.Lrd_tl11%=:\ns_waitcnt vmcnt(15)\nv_mfma_f32_16x16x32_bf16 a[0:3], v[132:135], v[160:163], a[0:3]\ns_add_u32 s60, s41, 393216\nbuffer_load_dwordx4 v[76:79], v196, %[rs], s60 offen offset:2048 sc1\nv_bitop3_b32 v209, v164, v166, s61 bitop3:0x7e\n" \
"v_mfma_f32_16x16x32_bf16 a[8:11], v[140:143], v[160:163], a[8:11]\nv_mfma_f32_16x16x32_bf16 a[16:19], v[148:151], v[160:163], a[16:19]\nv_cmp_gt_i32 vcc, 0, v209\nv_mfma_f32_16x16x32_bf16 a[24:27], v[156:159], v[160:163], a[24:27]\ns_cbranch_vccnz .Lrd_tre12%=\n.Lrd_tl12%=:\ns_waitcnt vmcnt(15)\nv_mfma_f32_16x16x32_bf16 a[4:7], v[24:27], v[164:167], a[4:7]\n" \
"s_add_u32 s60, s41, 524288\nbuffer_load_dwordx4 v[16:19], v196, %[rs], s60 offen offset:0 sc1\nv_bitop3_b32 v209, v168, v170, s61 bitop3:0x7e\nv_mfma_f32_16x16x32_bf16 a[12:15], v[56:59], v[164:167], a[12:15]\nv_mfma_f32_16x16x32_bf16 a[20:23], v[88:91], v[164:167], a[20:23]\nds_write_b128 v202, a[0:3] offset:0\nv_cmp_gt_i32 vcc, 0, v209\nv_mfma_f32_16x16x32_bf16 a[28:31], v[120:123], v[164:167], a[28:31]\n" \
"s_cbranch_vccnz .Lrd_tre13%=\n.Lrd_tl13%=:\ns_waitcnt vmcnt(15)\nv_mfma_f32_16x16x32_bf16 a[4:7], v[28:31], v[168:171], a[4:7]\ns_add_u32 s60, s41, 524288\nbuffer_load_dwordx4 v[48:51], v196, %[rs], s60 offen offset:1024 sc1\nv_bitop3_b32 v209, v172, v174, s61 bitop3:0x7e\nv_mfma_f32_16x16x32_bf16 a[12:15], v[60:63], v[168:171], a[12:15]\n" \
"v_mfma_f32_16x16x32_bf16 a[20:23], v[92:95], v[168:171], a[20:23]\nds_write_b128 v202, a[8:11] offset:2048\nv_cmp_gt_i32 vcc, 0, v209\nv_mfma_f32_16x16x32_bf16 a[28:31], v[124:127], v[168:171], a[28:31]\ns_cbranch_vccnz .Lrd_tre14%=\n.Lrd_tl14%=:\ns_waitcnt vmcnt(15)\nv_mfma_f32_16x16x32_bf16 a[4:7], v[128:131], v[172:175], a[4:7]\n" \
"s_add_u32 s60, s41, 524288\nbuffer_load_dwordx4 v[80:83], v196, %[rs], s60 offen offset:2048 sc1\nv_bitop3_b32 v209, v176, v178, s61 bitop3:0x7e\nv_mfma_f32_16x16x32_bf16 a[12:15], v[136:139], v[172:175], a[12:15]\nv_mfma_f32_16x16x32_bf16 a[20:23], v[144:147], v[172:175], a[20:23]\nds_write_b128 v202, a[16:19] offset:4096\nv_cmp_gt_i32 vcc, 0, v209\nv_mfma_f32_16x16x32_bf16 a[28:31], v[152:155], v[172:175], a[28:31]\n" \
"s_cbranch_vccnz .Lrd_tre15%=\n.Lrd_tl15%=:\nv_mfma_f32_16x16x32_bf16 a[4:7], v[132:135], v[176:179], a[4:7]\ns_add_u32 s60, s41, 655360\ns_waitcnt vmcnt(14)\nbuffer_load_dwordx4 v[20:23], v196, %[rs], s60 offen offset:0 sc1\nv_lshlrev_b32 v216, 16, v212\nv_and_b32 v217, 0xffff0000, v212\n" \
"v_mfma_f32_16x16x32_bf16 a[12:15], v[140:143], v[176:179], a[12:15]\nv_lshlrev_b32 v218, 16, v213\nv_and_b32 v219, 0xffff0000, v213\nv_mfma_f32_16x16x32_bf16 a[20:23], v[148:151], v[176:179], a[20:23]\nds_write_b128 v202, a[24:27] offset:6144\nv_lshlrev_b32 v220, 16, v214\nv_and_b32 v221, 0xffff0000, v214\nv_mfma_f32_16x16x32_bf16 a[28:31], v[156:159], v[176:179], a[28:31]\n" \
"s_add_u32 s60, s41, 655360\nbuffer_load_dwordx4 v[52:55], v196, %[rs], s60 offen offset:1024 sc1\ns_add_u32 s60, s41, 655360\nds_write_b128 v202, a[4:7] offset:1024\nbuffer_load_dwordx4 v[84:87], v196, %[rs], s60 offen offset:2048 sc1\nds_write_b128 v202, a[12:15] offset:3072\nv_lshlrev_b32 v222, 16, v215\nv_and_b32 v223, 0xffff0000, v215\n" \
"s_add_u32 s60, s41, 786432\nbuffer_load_dwordx4 v[24:27], v196, %[rs], s60 offen offset:0 sc1\nds_write_b128 v202, a[20:23] offset:5120\ns_add_u32 s60, s41, 786432\nbuffer_load_dwordx4 v[56:59], v196, %[rs], s60 offen offset:1024 sc1\nds_write_b128 v202, a[28:31] offset:7168\ns_waitcnt lgkmcnt(0)\ns_barrier\n" \
"ds_read_b128 v[224:227], v203 offset:0\nds_read_b128 v[228:231], v203 offset:256\nds_read_b128 v[232:235], v203 offset:8192\nds_read_b128 v[236:239], v203 offset:8448\nds_read_b128 v[240:243], v203 offset:16384\nds_read_b128 v[244:247], v203 offset:16640\nds_read_b128 v[248:251], v203 offset:24576\nds_read_b128 v[252:255], v203 offset:24832\n" \
"s_add_u32 s60, s41, 786432\nbuffer_load_dwordx4 v[88:91], v196, %[rs], s60 offen offset:2048 sc1\ns_waitcnt lgkmcnt(4)\nv_pk_add_f32 v[224:225], v[224:225], v[232:233]\nv_pk_add_f32 v[226:227], v[226:227], v[234:235]\nv_pk_add_f32 v[228:229], v[228:229], v[236:237]\nv_pk_add_f32 v[230:231], v[230:231], v[238:239]\ns_add_u32 s60, s41, 917504\n" \
"buffer_load_dwordx4 v[28:31], v196, %[rs], s60 offen offset:0 sc1\ns_waitcnt lgkmcnt(2)\nv_pk_add_f32 v[224:225], v[224:225], v[240:241]\nv_pk_add_f32 v[226:227], v[226:227], v[242:243]\nv_pk_add_f32 v[228:229], v[228:229], v[244:245]\nv_pk_add_f32 v[230:231], v[230:231], v[246:247]\ns_add_u32 s60, s41, 917504\nbuffer_load_dwordx4 v[60:63], v196, %[rs], s60 offen offset:1024 sc1\n" \
"s_waitcnt lgkmcnt(0)\nv_pk_add_f32 v[224:225], v[224:225], v[248:249]\nv_pk_add_f32 v[226:227], v[226:227], v[250:251]\nv_pk_add_f32 v[228:229], v[228:229], v[252:253]\nv_pk_add_f32 v[230:231], v[230:231], v[254:255]\nv_pk_fma_f32 v[224:225], v[224:225], s[62:63], v[216:217]\nv_pk_fma_f32 v[226:227], v[226:227], s[62:63], v[218:219]\nv_pk_fma_f32 v[228:229], v[228:229], s[62:63], v[220:221]\n" \
"v_pk_fma_f32 v[230:231], v[230:231], s[62:63], v[222:223]\nv_cvt_pk_bf16_f32 v208, v224, v225\nv_cvt_pk_bf16_f32 v209, v226, v227\nv_cvt_pk_bf16_f32 v210, v228, v229\nv_cvt_pk_bf16_f32 v211, v230, v231\nbuffer_store_dwordx4 v[208:211], v204, %[rs], s42 offen sc1\nv_pk_add_f32 v[232:233], v[224:225], v[226:227]\nv_pk_add_f32 v[234:235], v[228:229], v[230:231]\n" \
"v_pk_add_f32 v[232:233], v[232:233], v[234:235]\ns_add_u32 s60, s41, 917504\nbuffer_load_dwordx4 v[92:95], v196, %[rs], s60 offen offset:2048 sc1\ns_add_u32 s60, s41, 0\nbuffer_load_dwordx4 v[96:99], v196, %[rs], s60 offen offset:3072 sc1\nv_add_f32 v232, v232, v233\ns_nop 1\nv_add_f32_dpp v233, v232, v232 row_ror:8 row_mask:0xf bank_mask:0xf\n" \
"v_add_f32_dpp v234, v232, v232 row_ror:8 row_mask:0xf bank_mask:0xf\ns_nop 1\nv_permlane16_swap_b32 v233, v234\nv_add_f32 v232, v233, v234\nv_add_f32 v233, v233, v234\ns_nop 1\nv_permlane32_swap_b32 v232, v233\nv_add_f32 v232, v232, v233\n" \
"v_mul_f32 v232, 0x3c800000, v232\nv_pk_add_f32 v[224:225], v[224:225], v[232:233] op_sel_hi:[1,0] neg_lo:[0,1] neg_hi:[0,1]\nv_pk_add_f32 v[226:227], v[226:227], v[232:233] op_sel_hi:[1,0] neg_lo:[0,1] neg_hi:[0,1]\nv_pk_add_f32 v[228:229], v[228:229], v[232:233] op_sel_hi:[1,0] neg_lo:[0,1] neg_hi:[0,1]\nv_pk_add_f32 v[230:231], v[230:231], v[232:233] op_sel_hi:[1,0] neg_lo:[0,1] neg_hi:[0,1]\ns_add_u32 s60, s41, 131072\nbuffer_load_dwordx4 v[100:103], v196, %[rs], s60 offen offset:3072 sc1\nv_accvgpr_read_b32 v212, a33\n" \
"buffer_load_dwordx2 v[214:215], v212, %[rs], 0 offen sc1\nv_pk_mul_f32 v[236:237], v[224:225], v[224:225]\nv_pk_mul_f32 v[238:239], v[226:227], v[226:227]\nv_pk_fma_f32 v[236:237], v[228:229], v[228:229], v[236:237]\nv_pk_fma_f32 v[238:239], v[230:231], v[230:231], v[238:239]\nv_pk_add_f32 v[236:237], v[236:237], v[238:239]\nv_add_f32 v236, v236, v237\ns_nop 1\n" \
"v_add_f32_dpp v237, v236, v236 row_ror:8 row_mask:0xf bank_mask:0xf\nv_add_f32_dpp v238, v236, v236 row_ror:8 row_mask:0xf bank_mask:0xf\ns_nop 1\nv_permlane16_swap_b32 v237, v238\nv_add_f32 v236, v237, v238\nv_add_f32 v237, v237, v238\ns_nop 1\nv_permlane32_swap_b32 v236, v237\n" \
"v_add_f32 v236, v236, v237\nv_mov_b32 v233, v236\nbuffer_store_dwordx2 v[232:233], v205, %[rs], s42 offen sc1\ns_mov_b32 s59, %[fa]\ns_add_u32 s60, s41, 262144\nbuffer_load_dwordx4 v[104:107], v196, %[rs], s60 offen offset:3072 sc1\ns_add_u32 s60, s41, 393216\nbuffer_load_dwordx4 v[108:111], v196, %[rs], s60 offen offset:3072 sc1\n" \
"s_add_u32 s60, s41, 524288\nbuffer_load_dwordx4 v[112:115], v196, %[rs], s60 offen offset:3072 sc1\ns_add_u32 s60, s41, 655360\nbuffer_load_dwordx4 v[116:119], v196, %[rs], s60 offen offset:3072 sc1\ns_add_u32 s60, s41, 786432\nbuffer_load_dwordx4 v[120:123], v196, %[rs], s60 offen offset:3072 sc1\ns_add_u32 s60, s41, 917504\nbuffer_load_dwordx4 v[124:127], v196, %[rs], s60 offen offset:3072 sc1\n" \
"s_cmp_ge_u32 s40, 64\ns_cbranch_scc1 .Lrd_fin%=\n.Lrd_ychk%=:\ns_and_b32 s44, s40, 1\ns_bfe_u32 s55, s40, 0x10001\ns_mul_i32 s54, s44, 0x80008001\ns_mul_i32 s61, s55, 0x80008000\ns_lshl_b32 s58, s44, 17\n" \
"s_add_u32 s58, s58, %[st]\ns_add_u32 s44, s40, 1\ns_and_b32 s44, s44, 3\ns_lshl_b32 s44, s44, 15\nv_xor_b32 v206, s44, v200\nv_mov_b64 v[180:181], 0\nv_mov_b64 v[182:183], 0\nv_mov_b64 v[184:185], 0\n" \
"v_mov_b64 v[186:187], 0\nv_mov_b64 v[188:189], 0\nv_mov_b64 v[190:191], 0\nv_mov_b64 v[192:193], 0\nv_mov_b64 v[194:195], 0\ns_waitcnt vmcnt(7)\n.Lrd_sg0%=:\ns_memrealtime s[68:69]\n" \
"v_cmp_ne_u32 vcc, s40, v214\ns_cbranch_vccnz .Lrd_sg1%=\nv_max_u32_dpp v215, v215, v215 quad_perm:[1,0,3,2] row_mask:0xf bank_mask:0xf\ns_nop 1\nv_max_u32_dpp v215, v215, v215 quad_perm:[2,3,0,1] row_mask:0xf bank_mask:0xf\ns_nop 1\nv_max_u32_dpp v215, v215, v215 row_half_mirror row_mask:0xf bank_mask:0xf\ns_nop 0\n" \
"v_readlane_b32 s86, v215, 0\nv_readlane_b32 s87, v215, 8\ns_add_u32 s86, s86, %[du]\n.Lrd_tw608%=:\ns_waitcnt lgkmcnt(0)\ns_sub_u32 s44, s86, s68\ns_add_u32 s44, s44, 11\ns_cmp_lt_u32 s44, 250\n" \
"s_cbranch_scc1 .Lrd_tw608x%=\nbuffer_load_dwordx4 v[128:131], v196, %[rs], %[su] offen offset:0 sc1\nbuffer_load_dwordx4 v[132:135], v196, %[rs], %[su] offen offset:1024 sc1\ns_memrealtime s[68:69]\nbuffer_load_dwordx4 v[136:139], v196, %[rs], %[su] offen offset:2048 sc1\nbuffer_load_dwordx4 v[140:143], v196, %[rs], %[su] offen offset:3072 sc1\n.Lrd_tw619%=:\ns_waitcnt lgkmcnt(0)\n" \
"s_sub_u32 s44, s86, s68\ns_add_u32 s44, s44, 21\ns_cmp_lt_u32 s44, 250\ns_cbranch_scc1 .Lrd_tw619x%=\nbuffer_load_dwordx4 v[144:147], v196, %[rs], s57 offen offset:0 sc1\nbuffer_load_dwordx4 v[148:151], v196, %[rs], s57 offen offset:1024 sc1\ns_memrealtime s[68:69]\nbuffer_load_dwordx4 v[152:155], v196, %[rs], s57 offen offset:2048 sc1\n" \
"buffer_load_dwordx4 v[156:159], v196, %[rs], s57 offen offset:3072 sc1\ns_waitcnt vmcnt(14)\nbuffer_store_dword v207, off, %[rs], s59 sc1\ns_mov_b32 s59, -2147483648\n.Lrd_tw633%=:\ns_waitcnt lgkmcnt(0)\ns_sub_u32 s44, s87, s68\ns_add_u32 s44, s44, 24\n" \
"s_cmp_lt_u32 s44, 250\ns_cbranch_scc1 .Lrd_tw633x%=\nbuffer_load_dwordx4 v[160:163], v196, %[rs], %[sp] offen offset:0 sc1\nbuffer_load_dwordx4 v[164:167], v196, %[rs], %[sp] offen offset:1024 sc1\nbuffer_load_dwordx4 v[168:171], v196, %[rs], %[sp] offen offset:2048 sc1\nbuffer_load_dwordx4 v[172:175], v196, %[rs], %[sp] offen offset:3072 sc1\nbuffer_load_dwordx4 v[176:179], v196, %[rs], s56 offen offset:0 sc1\ns_add_u32 s60, s58, 16384\n" \
"buffer_load_dwordx4 v[212:215], v196, %[rs], s60 offen offset:0 sc1\nv_bitop3_b32 v209, v0, v2, s55 bitop3:0x7e\nv_bitop3_b32 v210, v32, v34, s55 bitop3:0x7e\nv_or3_b32 v208, v209, v210, 0\nv_bitop3_b32 v209, v64, v66, s55 bitop3:0x7e\nv_bitop3_b32 v210, v4, v6, s55 bitop3:0x7e\nv_or3_b32 v208, v209, v210, v208\ns_add_u32 s60, s58, 0\n" \
"buffer_load_dwordx4 v[216:219], v196, %[rs], s60 offen offset:0 sc1\nv_bitop3_b32 v209, v36, v38, s55 bitop3:0x7e\nv_bitop3_b32 v210, v68, v70, s55 bitop3:0x7e\nv_or3_b32 v208, v209, v210, v208\nv_bitop3_b32 v209, v8, v10, s55 bitop3:0x7e\nv_bitop3_b32 v210, v40, v42, s55 bitop3:0x7e\nv_or3_b32 v208, v209, v210, v208\ns_add_u32 s60, s58, 0\n" \
"buffer_load_dwordx4 v[220:223], v196, %[rs], s60 offen offset:1024 sc1\nv_bitop3_b32 v209, v72, v74, s55 bitop3:0x7e\nv_bitop3_b32 v210, v12, v14, s55 bitop3:0x7e\nv_or3_b32 v208, v209, v210, v208\nv_bitop3_b32 v209, v44, v46, s55 bitop3:0x7e\nv_bitop3_b32 v210, v76, v78, s55 bitop3:0x7e\nv_or3_b32 v208, v209, v210, v208\ns_add_u32 s60, s58, 16384\n" \
"buffer_load_dwordx4 v[224:227], v196, %[rs], s60 offen offset:1024 sc1\nv_bitop3_b32 v209, v16, v18, s55 bitop3:0x7e\nv_bitop3_b32 v210, v48, v50, s55 bitop3:0x7e\nv_or3_b32 v208, v209, v210, v208\nv_bitop3_b32 v209, v80, v82, s55 bitop3:0x7e\nv_bitop3_b32 v210, v20, v22, s55 bitop3:0x7e\nv_or3_b32 v208, v209, v210, v208\ns_add_u32 s60, s58, 0\n" \
"buffer_load_dwordx4 v[228:231], v196, %[rs], s60 offen offset:2048 sc1\nv_bitop3_b32 v209, v52, v54, s55 bitop3:0x7e\nv_bitop3_b32 v210, v84, v86, s55 bitop3:0x7e\nv_or3_b32 v208, v209, v210, v208\nv_bitop3_b32 v209, v24, v26, s55 bitop3:0x7e\nv_bitop3_b32 v210, v56, v58, s55 bitop3:0x7e\nv_or3_b32 v208, v209, v210, v208\nv_bitop3_b32 v209, v88, v90, s55 bitop3:0x7e\n" \
"v_bitop3_b32 v210, v28, v30, s55 bitop3:0x7e\nv_or3_b32 v208, v209, v210, v208\nv_bitop3_b32 v209, v60, v62, s55 bitop3:0x7e\nv_bitop3_b32 v210, v92, v94, s55 bitop3:0x7e\nv_or3_b32 v208, v209, v210, v208\ns_waitcnt vmcnt(23)\nv_bitop3_b32 v209, v96, v98, s55 bitop3:0x7e\nv_bitop3_b32 v210, v100, v102, s55 bitop3:0x7e\n" \
"v_or3_b32 v208, v209, v210, v208\nv_bitop3_b32 v209, v104, v106, s55 bitop3:0x7e\nv_bitop3_b32 v210, v108, v110, s55 bitop3:0x7e\nv_or3_b32 v208, v209, v210, v208\ns_waitcnt vmcnt(19)\nv_bitop3_b32 v209, v112, v114, s55 bitop3:0x7e\nv_bitop3_b32 v210, v116, v118, s55 bitop3:0x7e\nv_or3_b32 v208, v209, v210, v208\n" \
"v_bitop3_b32 v209, v120, v122, s55 bitop3:0x7e\nv_bitop3_b32 v210, v124, v126, s55 bitop3:0x7e\nv_or3_b32 v208, v209, v210, v208\nv_and_b32 v208, 1, v208\nv_cmp_ne_u32 vcc, 0, v208\ns_cbranch_vccnz .Lrd_yre%=\ns_branch .Lrd_ra%=\n.Lrd_sg1%=:\n" \
"buffer_load_dwordx2 v[214:215], v212, %[rs], 0 offen sc1\ns_waitcnt vmcnt(0)\nbuffer_store_dword v207, off, %[rs], s59 sc1\ns_mov_b32 s59, -2147483648\ns_add_u32 s43, s43, 1\ns_cmp_lt_u32 s43, 262144\ns_cbranch_scc1 .Lrd_sg0%=\ns_branch .Lrd_abort%=\n" \
".Lrd_tw608x%=:\ns_memrealtime s[68:69]\ns_branch .Lrd_tw608%=\n.Lrd_tw619x%=:\ns_memrealtime s[68:69]\ns_branch .Lrd_tw619%=\n.Lrd_tw633x%=:\ns_memrealtime s[68:69]\n" \
"s_branch .Lrd_tw633%=\n.Lrd_fin%=:\ns_waitcnt vmcnt(0)\nbuffer_store_dword v207, off, %[rs], s59 sc1\ns_mov_b32 s59, -2147483648\ns_branch .Lrd_end%=\n.Lrd_yre%=:\ns_mov_b64 exec, vcc\n" \
"s_waitcnt vmcnt(0)\ns_add_u32 s43, s43, 1\ns_cmp_lt_u32 s43, 262144\ns_cbranch_scc1 .Lrd_yld%=\ns_branch .Lrd_abort%=\n.Lrd_rb%=:\ns_mov_b64 exec, s[46:47]\ns_cbranch_execz .Lrd_rs0%=\n" \
"buffer_load_dwordx4 v[160:163], v196, %[rs], %[sp] offen offset:0 sc1\n.Lrd_rs0%=:\ns_mov_b64 exec, s[48:49]\ns_cbranch_execz .Lrd_rs1%=\nbuffer_load_dwordx4 v[164:167], v196, %[rs], %[sp] offen offset:1024 sc1\n.Lrd_rs1%=:\ns_mov_b64 exec, s[50:51]\ns_cbranch_execz .Lrd_rs2%=\n" \
"buffer_load_dwordx4 v[168:171], v196, %[rs], %[sp] offen offset:2048 sc1\n.Lrd_rs2%=:\ns_mov_b64 exec, s[52:53]\ns_cbranch_execz .Lrd_rs3%=\nbuffer_load_dwordx4 v[172:175], v196, %[rs], %[sp] offen offset:3072 sc1\n.Lrd_rs3%=:\ns_mov_b64 exec, s[64:65]\ns_cbranch_execz .Lrd_rs4%=\n" \
"buffer_load_dwordx4 v[176:179], v196, %[rs], s56 offen offset:0 sc1\n.Lrd_rs4%=:\ns_mov_b64 exec, s[86:87]\ns_cbranch_execz .Lrd_rs5%=\nbuffer_load_dwordx4 v[128:131], v196, %[rs], %[su] offen offset:0 sc1\nbuffer_load_dwordx4 v[136:139], v196, %[rs], %[su] offen offset:2048 sc1\n.Lrd_rs5%=:\ns_mov_b64 exec, s[88:89]\n" \
"s_cbranch_execz .Lrd_rs6%=\nbuffer_load_dwordx4 v[144:147], v196, %[rs], s57 offen offset:0 sc1\nbuffer_load_dwordx4 v[152:155], v196, %[rs], s57 offen offset:2048 sc1\n.Lrd_rs6%=:\ns_mov_b64 exec, s[68:69]\ns_cbranch_execz .Lrd_rs7%=\nbuffer_load_dwordx4 v[132:135], v196, %[rs], %[su] offen offset:1024 sc1\nbuffer_load_dwordx4 v[140:143], v196, %[rs], %[su] offen offset:3072 sc1\n" \
".Lrd_rs7%=:\ns_mov_b64 exec, s[62:63]\ns_cbranch_execz .Lrd_rs8%=\nbuffer_load_dwordx4 v[148:151], v196, %[rs], s57 offen offset:1024 sc1\nbuffer_load_dwordx4 v[156:159], v196, %[rs], s57 offen offset:3072 sc1\n.Lrd_rs8%=:\ns_mov_b64 exec, -1\nv_mov_b64 v[180:181], 0\n" \
"v_mov_b64 v[182:183], 0\nv_mov_b64 v[184:185], 0\nv_mov_b64 v[186:187], 0\nv_mov_b64 v[188:189], 0\nv_mov_b64 v[190:191], 0\nv_mov_b64 v[192:193], 0\nv_mov_b64 v[194:195], 0\ns_waitcnt vmcnt(0)\n" \
"s_add_u32 s43, s43, 1\ns_cmp_lt_u32 s43, 262144\ns_cbranch_scc1 .Lrd_ra%=\ns_branch .Lrd_abort%=\n.Lrd_y1%=:\nv_accvgpr_read_b32 v206, a32\nbuffer_load_dword v208, v206, %[rs], 0 offen sc1\ns_waitcnt vmcnt(0)\n" \
"v_cmp_gt_i32 vcc, 1, v208\ns_cbranch_vccz .Lrd_y1r%=\ns_add_u32 s43, s43, 1\ns_cmp_lt_u32 s43, 262144\ns_cbranch_scc1 .Lrd_y1%=\ns_branch .Lrd_abort%=\n.Lrd_tre0%=:\ns_add_u32 s60, s58, 16384\n" \
"buffer_load_dwordx4 v[212:215], v196, %[rs], s60 offen offset:0 sc1\ns_waitcnt vmcnt(0)\nv_bitop3_b32 v209, v212, v214, s61 bitop3:0x7e\nv_cmp_gt_i32 vcc, 0, v209\ns_cbranch_vccz .Lrd_tl0%=\ns_add_u32 s43, s43, 1\ns_cmp_lt_u32 s43, 262144\ns_cbranch_scc1 .Lrd_tre0%=\n" \
"s_branch .Lrd_abort%=\n.Lrd_tre1%=:\ns_add_u32 s60, s58, 0\nbuffer_load_dwordx4 v[216:219], v196, %[rs], s60 offen offset:0 sc1\ns_waitcnt vmcnt(0)\nv_bitop3_b32 v209, v216, v218, s61 bitop3:0x7e\nv_cmp_gt_i32 vcc, 0, v209\ns_cbranch_vccz .Lrd_tl1%=\n" \
"s_add_u32 s43, s43, 1\ns_cmp_lt_u32 s43, 262144\ns_cbranch_scc1 .Lrd_tre1%=\ns_branch .Lrd_abort%=\n.Lrd_tre2%=:\ns_add_u32 s60, s58, 0\nbuffer_load_dwordx4 v[220:223], v196, %[rs], s60 offen offset:1024 sc1\ns_waitcnt vmcnt(0)\n" \
"v_bitop3_b32 v209, v220, v222, s61 bitop3:0x7e\nv_cmp_gt_i32 vcc, 0, v209\ns_cbranch_vccz .Lrd_tl2%=\ns_add_u32 s43, s43, 1\ns_cmp_lt_u32 s43, 262144\ns_cbranch_scc1 .Lrd_tre2%=\ns_branch .Lrd_abort%=\n.Lrd_tre3%=:\n" \
"s_add_u32 s60, s58, 16384\nbuffer_load_dwordx4 v[224:227], v196, %[rs], s60 offen offset:1024 sc1\ns_waitcnt vmcnt(0)\nv_bitop3_b32 v209, v224, v226, s61 bitop3:0x7e\nv_cmp_gt_i32 vcc, 0, v209\ns_cbranch_vccz .Lrd_tl3%=\ns_add_u32 s43, s43, 1\ns_cmp_lt_u32 s43, 262144\n" \
"s_cbranch_scc1 .Lrd_tre3%=\ns_branch .Lrd_abort%=\n.Lrd_tre4%=:\ns_add_u32 s60, s58, 0\nbuffer_load_dwordx4 v[228:231], v196, %[rs], s60 offen offset:2048 sc1\ns_waitcnt vmcnt(0)\nv_bitop3_b32 v209, v228, v230, s61 bitop3:0x7e\nv_cmp_gt_i32 vcc, 0, v209\n" \
"s_cbranch_vccz .Lrd_tl4%=\ns_add_u32 s43, s43, 1\ns_cmp_lt_u32 s43, 262144\ns_cbranch_scc1 .Lrd_tre4%=\ns_branch .Lrd_abort%=\n.Lrd_tre5%=:\ns_add_u32 s60, s58, 0\nbuffer_load_dwordx4 v[232:235], v196, %[rs], s60 offen offset:3072 sc1\n" \
"s_waitcnt vmcnt(0)\nv_bitop3_b32 v209, v232, v234, s61 bitop3:0x7e\nv_cmp_gt_i32 vcc, 0, v209\ns_cbranch_vccz .Lrd_tl5%=\ns_add_u32 s43, s43, 1\ns_cmp_lt_u32 s43, 262144\ns_cbranch_scc1 .Lrd_tre5%=\ns_branch .Lrd_abort%=\n" \
".Lrd_tre6%=:\ns_add_u32 s60, s58, 16384\nbuffer_load_dwordx4 v[236:239], v196, %[rs], s60 offen offset:2048 sc1\ns_waitcnt vmcnt(0)\nv_bitop3_b32 v209, v236, v238, s61 bitop3:0x7e\nv_cmp_gt_i32 vcc, 0, v209\ns_cbranch_vccz .Lrd_tl6%=\ns_add_u32 s43, s43, 1\n" \
"s_cmp_lt_u32 s43, 262144\ns_cbranch_scc1 .Lrd_tre6%=\ns_branch .Lrd_abort%=\n.Lrd_tre7%=:\ns_add_u32 s60, s58, 4096\nbuffer_load_dwordx4 v[240:243], v196, %[rs], s60 offen offset:0 sc1\ns_waitcnt vmcnt(0)\nv_bitop3_b32 v209, v240, v242, s61 bitop3:0x7e\n" \
"v_cmp_gt_i32 vcc, 0, v209\ns_cbranch_vccz .Lrd_tl7%=\ns_add_u32 s43, s43, 1\ns_cmp_lt_u32 s43, 262144\ns_cbranch_scc1 .Lrd_tre7%=\ns_branch .Lrd_abort%=\n.Lrd_tre8%=:\ns_add_u32 s60, s58, 4096\n" \
"buffer_load_dwordx4 v[244:247], v196, %[rs], s60 offen offset:1024 sc1\ns_waitcnt vmcnt(0)\nv_bitop3_b32 v209, v244, v246, s61 bitop3:0x7e\nv_cmp_gt_i32 vcc, 0, v209\ns_cbranch_vccz .Lrd_tl8%=\ns_add_u32 s43, s43, 1\ns_cmp_lt_u32 s43, 262144\ns_cbranch_scc1 .Lrd_tre8%=\n" \
"s_branch .Lrd_abort%=\n.Lrd_tre9%=:\ns_add_u32 s60, s58, 16384\nbuffer_load_dwordx4 v[248:251], v196, %[rs], s60 offen offset:3072 sc1\ns_waitcnt vmcnt(0)\nv_bitop3_b32 v209, v248, v250, s61 bitop3:0x7e\nv_cmp_gt_i32 vcc, 0, v209\ns_cbranch_vccz .Lrd_tl9%=\n" \
"s_add_u32 s43, s43, 1\ns_cmp_lt_u32 s43, 262144\ns_cbranch_scc1 .Lrd_tre9%=\ns_branch .Lrd_abort%=\n.Lrd_tre10%=:\ns_add_u32 s60, s58, 20480\nbuffer_load_dwordx4 v[252:255], v196, %[rs], s60 offen offset:0 sc1\ns_waitcnt vmcnt(0)\n" \
"v_bitop3_b32 v209, v252, v254, s61 bitop3:0x7e\nv_cmp_gt_i32 vcc, 0, v209\ns_cbranch_vccz .Lrd_tl10%=\ns_add_u32 s43, s43, 1\ns_cmp_lt_u32 s43, 262144\ns_cbranch_scc1 .Lrd_tre10%=\ns_branch .Lrd_abort%=\n.Lrd_tre11%=:\n" \
"s_add_u32 s60, s58, 20480\nbuffer_load_dwordx4 v[160:163], v196, %[rs], s60 offen offset:2048 sc1\ns_waitcnt vmcnt(0)\nv_bitop3_b32 v209, v160, v162, s61 bitop3:0x7e\nv_cmp_gt_i32 vcc, 0, v209\ns_cbranch_vccz .Lrd_tl11%=\ns_add_u32 s43, s43, 1\ns_cmp_lt_u32 s43, 262144\n" \
"s_cbranch_scc1 .Lrd_tre11%=\ns_branch .Lrd_abort%=\n.Lrd_tre12%=:\ns_add_u32 s60, s58, 4096\nbuffer_load_dwordx4 v[164:167], v196, %[rs], s60 offen offset:2048 sc1\ns_waitcnt vmcnt(0)\nv_bitop3_b32 v209, v164, v166, s61 bitop3:0x7e\nv_cmp_gt_i32 vcc, 0, v209\n" \
"s_cbranch_vccz .Lrd_tl12%=\ns_add_u32 s43, s43, 1\ns_cmp_lt_u32 s43, 262144\ns_cbranch_scc1 .Lrd_tre12%=\ns_branch .Lrd_abort%=\n.Lrd_tre13%=:\ns_add_u32 s60, s58, 4096\nbuffer_load_dwordx4 v[168:171], v196, %[rs], s60 offen offset:3072 sc1\n" \
"s_waitcnt vmcnt(0)\nv_bitop3_b32 v209, v168, v170, s61 bitop3:0x7e\nv_cmp_gt_i32 vcc, 0, v209\ns_cbranch_vccz .Lrd_tl13%=\ns_add_u32 s43, s43, 1\ns_cmp_lt_u32 s43, 262144\ns_cbranch_scc1 .Lrd_tre13%=\ns_branch .Lrd_abort%=\n" \
".Lrd_tre14%=:\ns_add_u32 s60, s58, 20480\nbuffer_load_dwordx4 v[172:175], v196, %[rs], s60 offen offset:1024 sc1\ns_waitcnt vmcnt(0)\nv_bitop3_b32 v209, v172, v174, s61 bitop3:0x7e\nv_cmp_gt_i32 vcc, 0, v209\ns_cbranch_vccz .Lrd_tl14%=\ns_add_u32 s43, s43, 1\n" \
"s_cmp_lt_u32 s43, 262144\ns_cbranch_scc1 .Lrd_tre14%=\ns_branch .Lrd_abort%=\n.Lrd_tre15%=:\ns_add_u32 s60, s58, 20480\nbuffer_load_dwordx4 v[176:179], v196, %[rs], s60 offen offset:3072 sc1\ns_waitcnt vmcnt(0)\nv_bitop3_b32 v209, v176, v178, s61 bitop3:0x7e\n" \
"v_cmp_gt_i32 vcc, 0, v209\ns_cbranch_vccz .Lrd_tl15%=\ns_add_u32 s43, s43, 1\ns_cmp_lt_u32 s43, 262144\ns_cbranch_scc1 .Lrd_tre15%=\ns_branch .Lrd_abort%=\n.Lrd_abort%=:\ns_mov_b64 exec, -1\n" \
"v_mov_b32 v16, 0\ns_movk_i32 s44, 6400\nbuffer_store_dword v16, off, %[rs], s44 sc1\n.Lrd_end%=:\ns_waitcnt vmcnt(0) lgkmcnt(0)\n" \
""
#define GG_ASM \
"v_mbcnt_lo_u32_b32 v255, -1, 0\nv_mbcnt_hi_u32_b32 v255, -1, v255\nv_lshlrev_b32 v255, 2, v255\nv_add_u32 v255, %[tb], v255\nds_read_b32 v0, v255 offset:0\nds_read_b32 v1, v255 offset:256\nds_read_b32 v2, v255 offset:512\nds_read_b32 v3, v255 offset:768\n" \
"ds_read_b32 v4, v255 offset:1024\nds_read_b32 v5, v255 offset:1280\nds_read_b32 v92, v255 offset:1536\nds_read_b32 v93, v255 offset:1792\nds_read_b32 v94, v255 offset:2048\ns_waitcnt lgkmcnt(0)\nv_mov_b32 v6, 0x3f803f80\nv_mov_b32 v7, 0x3f803f80\n" \
"s_add_u32 m0, %[lw], 16384\ns_add_u32 s44, %[wo], 0\nbuffer_load_dwordx4 v92, %[rw], s44 offen nt lds\ns_add_u32 m0, %[lw], 17408\ns_add_u32 s44, %[wo], 57344\nbuffer_load_dwordx4 v92, %[rw], s44 offen nt lds\ns_add_u32 m0, %[lw], 18432\ns_add_u32 s44, %[wo], 114688\n" \
"buffer_load_dwordx4 v92, %[rw], s44 offen nt lds\ns_add_u32 m0, %[lw], 19456\ns_add_u32 s44, %[wo], 172032\nbuffer_load_dwordx4 v92, %[rw], s44 offen nt lds\ns_add_u32 m0, %[lw], 20480\ns_add_u32 s44, %[wo], 229376\nbuffer_load_dwordx4 v92, %[rw], s44 offen nt lds\ns_add_u32 m0, %[lw], 21504\n" \
"s_add_u32 s44, %[wo], 286720\nbuffer_load_dwordx4 v92, %[rw], s44 offen nt lds\ns_add_u32 m0, %[lw], 22528\ns_add_u32 s44, %[wo], 344064\nbuffer_load_dwordx4 v92, %[rw], s44 offen nt lds\ns_add_u32 m0, %[lw], 23552\ns_add_u32 s44, %[wo], 401408\nbuffer_load_dwordx4 v92, %[rw], s44 offen nt lds\n" \
"s_add_u32 m0, %[tb], 0\ns_add_u32 s44, %[wo], 1835008\nbuffer_load_dwordx4 v92, %[rw], s44 offen nt lds\ns_add_u32 m0, %[tb], 1024\ns_add_u32 s44, %[wo], 1892352\nbuffer_load_dwordx4 v92, %[rw], s44 offen nt lds\ns_add_u32 m0, %[tb], 2048\ns_add_u32 s44, %[wo], 1949696\n" \
"buffer_load_dwordx4 v92, %[rw], s44 offen nt lds\ns_add_u32 m0, %[tb], 3072\ns_add_u32 s44, %[wo], 2007040\nbuffer_load_dwordx4 v92, %[rw], s44 offen nt lds\ns_add_u32 m0, %[tb], 4096\ns_add_u32 s44, %[wo], 2064384\nbuffer_load_dwordx4 v92, %[rw], s44 offen nt lds\ns_add_u32 m0, %[tb], 5120\n" \
"s_add_u32 s44, %[wo], 2121728\nbuffer_load_dwordx4 v92, %[rw], s44 offen nt lds\ns_add_u32 m0, %[tb], 6144\ns_add_u32 s44, %[wo], 2179072\nbuffer_load_dwordx4 v92, %[rw], s44 offen nt lds\ns_add_u32 m0, %[tb], 7168\ns_add_u32 s44, %[wo], 2236416\nbuffer_load_dwordx4 v92, %[rw], s44 offen nt lds\n" \
"s_add_u32 m0, %[tb], 8192\ns_add_u32 s44, %[wo], 3670016\nbuffer_load_dwordx4 v92, %[rw], s44 offen nt lds\ns_add_u32 m0, %[tb], 9216\ns_add_u32 s44, %[wo], 3727360\nbuffer_load_dwordx4 v92, %[rw], s44 offen nt lds\ns_add_u32 m0, %[tb], 10240\ns_add_u32 s44, %[wo], 3784704\n" \
"buffer_load_dwordx4 v92, %[rw], s44 offen nt lds\ns_add_u32 m0, %[tb], 11264\ns_add_u32 s44, %[wo], 3842048\nbuffer_load_dwordx4 v92, %[rw], s44 offen nt lds\ns_add_u32 m0, %[lw], 0\ns_add_u32 s44, %[wo], 3899392\nbuffer_load_dwordx4 v92, %[rw], s44 offen nt lds\ns_add_u32 m0, %[lw], 1024\n" \
"s_add_u32 s44, %[wo], 3956736\nbuffer_load_dwordx4 v92, %[rw], s44 offen nt lds\ns_add_u32 m0, %[lw], 2048\ns_add_u32 s44, %[wo], 4014080\nbuffer_load_dwordx4 v92, %[rw], s44 offen nt lds\ns_add_u32 m0, %[lw], 3072\ns_add_u32 s44, %[wo], 4071424\nbuffer_load_dwordx4 v92, %[rw], s44 offen nt lds\n" \
"s_add_u32 m0, %[lw], 4096\ns_add_u32 s44, %[wo], 5505024\nbuffer_load_dwordx4 v92, %[rw], s44 offen nt lds\ns_add_u32 m0, %[lw], 5120\ns_add_u32 s44, %[wo], 5562368\nbuffer_load_dwordx4 v92, %[rw], s44 offen nt lds\ns_add_u32 m0, %[lw], 6144\ns_add_u32 s44, %[wo], 5619712\n" \
"buffer_load_dwordx4 v92, %[rw], s44 offen nt lds\ns_add_u32 m0, %[lw], 7168\ns_add_u32 s44, %[wo], 5677056\nbuffer_load_dwordx4 v92, %[rw], s44 offen nt lds\ns_add_u32 m0, %[lw], 8192\ns_add_u32 s44, %[wo], 5734400\nbuffer_load_dwordx4 v92, %[rw], s44 offen nt lds\ns_add_u32 m0, %[lw], 9216\n" \
"s_add_u32 s44, %[wo], 5791744\nbuffer_load_dwordx4 v92, %[rw], s44 offen nt lds\ns_add_u32 m0, %[lw], 10240\ns_add_u32 s44, %[wo], 5849088\nbuffer_load_dwordx4 v92, %[rw], s44 offen nt lds\ns_add_u32 m0, %[lw], 11264\ns_add_u32 s44, %[wo], 5906432\nbuffer_load_dwordx4 v92, %[rw], s44 offen nt lds\n" \
"s_waitcnt vmcnt(28)\ns_barrier\ns_add_u32 m0, %[lw], 12288\ns_add_u32 s44, %[wo], 458752\nbuffer_load_dwordx4 v92, %[rw], s44 offen nt lds\ns_add_u32 m0, %[lw], 13312\ns_add_u32 s44, %[wo], 516096\nbuffer_load_dwordx4 v92, %[rw], s44 offen nt lds\n" \
"s_add_u32 m0, %[lw], 14336\ns_add_u32 s44, %[wo], 573440\nbuffer_load_dwordx4 v92, %[rw], s44 offen nt lds\ns_add_u32 m0, %[lw], 15360\ns_add_u32 s44, %[wo], 630784\nbuffer_load_dwordx4 v92, %[rw], s44 offen nt lds\nds_read_b64_tr_b16 a[0:1], v94 offset:16384\nds_read_b64_tr_b16 a[2:3], v94 offset:16896\n" \
"ds_read_b64_tr_b16 a[8:9], v94 offset:16392\nds_read_b64_tr_b16 a[10:11], v94 offset:16904\nds_read_b64_tr_b16 a[16:17], v94 offset:40960\nds_read_b64_tr_b16 a[18:19], v94 offset:41472\nds_read_b64_tr_b16 a[24:25], v94 offset:40968\nds_read_b64_tr_b16 a[26:27], v94 offset:41480\ns_waitcnt lgkmcnt(0)\nv_mfma_f32_16x16x16_bf16 v[8:11], a[0:1], v[6:7], 0\n" \
"v_mfma_f32_16x16x16_bf16 v[12:15], a[8:9], v[6:7], 0\nv_mfma_f32_16x16x16_bf16 v[16:19], a[16:17], v[6:7], 0\nv_mfma_f32_16x16x16_bf16 v[20:23], a[24:25], v[6:7], 0\nv_mfma_f32_16x16x16_bf16 v[8:11], a[2:3], v[6:7], v[8:11]\nv_mfma_f32_16x16x16_bf16 v[12:15], a[10:11], v[6:7], v[12:15]\nv_mfma_f32_16x16x16_bf16 v[16:19], a[18:19], v[6:7], v[16:19]\nv_mfma_f32_16x16x16_bf16 v[20:23], a[26:27], v[6:7], v[20:23]\ns_waitcnt vmcnt(28)\n" \
"s_barrier\ns_add_u32 m0, %[lw], 16384\ns_add_u32 s44, %[wo], 688128\nbuffer_load_dwordx4 v92, %[rw], s44 offen nt lds\ns_add_u32 m0, %[lw], 17408\ns_add_u32 s44, %[wo], 745472\nbuffer_load_dwordx4 v92, %[rw], s44 offen nt lds\ns_add_u32 m0, %[lw], 18432\n" \
"s_add_u32 s44, %[wo], 802816\nbuffer_load_dwordx4 v92, %[rw], s44 offen nt lds\ns_add_u32 m0, %[lw], 19456\ns_add_u32 s44, %[wo], 860160\nbuffer_load_dwordx4 v92, %[rw], s44 offen nt lds\nds_read_b64_tr_b16 a[4:5], v94 offset:20480\nds_read_b64_tr_b16 a[6:7], v94 offset:20992\nds_read_b64_tr_b16 a[12:13], v94 offset:20488\n" \
"ds_read_b64_tr_b16 a[14:15], v94 offset:21000\nds_read_b64_tr_b16 a[20:21], v94 offset:45056\nds_read_b64_tr_b16 a[22:23], v94 offset:45568\nds_read_b64_tr_b16 a[28:29], v94 offset:45064\nds_read_b64_tr_b16 a[30:31], v94 offset:45576\ns_waitcnt lgkmcnt(0)\nv_mfma_f32_16x16x16_bf16 v[8:11], a[4:5], v[6:7], v[8:11]\nv_mfma_f32_16x16x16_bf16 v[12:15], a[12:13], v[6:7], v[12:15]\n" \
"v_mfma_f32_16x16x16_bf16 v[16:19], a[20:21], v[6:7], v[16:19]\nv_mfma_f32_16x16x16_bf16 v[20:23], a[28:29], v[6:7], v[20:23]\nv_mfma_f32_16x16x16_bf16 v[8:11], a[6:7], v[6:7], v[8:11]\nv_mfma_f32_16x16x16_bf16 v[12:15], a[14:15], v[6:7], v[12:15]\nv_mfma_f32_16x16x16_bf16 v[16:19], a[22:23], v[6:7], v[16:19]\nv_mfma_f32_16x16x16_bf16 v[20:23], a[30:31], v[6:7], v[20:23]\ns_waitcnt vmcnt(28)\ns_barrier\n" \
"s_add_u32 m0, %[lw], 20480\ns_add_u32 s44, %[wo], 917504\nbuffer_load_dwordx4 v92, %[rw], s44 offen nt lds\ns_add_u32 m0, %[lw], 21504\ns_add_u32 s44, %[wo], 974848\nbuffer_load_dwordx4 v92, %[rw], s44 offen nt lds\ns_add_u32 m0, %[lw], 22528\ns_add_u32 s44, %[wo], 1032192\n" \
"buffer_load_dwordx4 v92, %[rw], s44 offen nt lds\ns_add_u32 m0, %[lw], 23552\ns_add_u32 s44, %[wo], 1089536\nbuffer_load_dwordx4 v92, %[rw], s44 offen nt lds\nds_read_b64_tr_b16 a[104:105], v93 offset:0\nds_read_b64_tr_b16 a[106:107], v93 offset:512\nds_read_b64_tr_b16 a[112:113], v93 offset:8\nds_read_b64_tr_b16 a[114:115], v93 offset:520\n" \
"ds_read_b64_tr_b16 a[120:121], v93 offset:12288\nds_read_b64_tr_b16 a[122:123], v93 offset:12800\nds_read_b64_tr_b16 a[128:129], v93 offset:12296\nds_read_b64_tr_b16 a[130:131], v93 offset:12808\ns_waitcnt lgkmcnt(0)\nv_mfma_f32_16x16x16_bf16 v[8:11], a[104:105], v[6:7], v[8:11]\nv_mfma_f32_16x16x16_bf16 v[12:15], a[112:113], v[6:7], v[12:15]\nv_mfma_f32_16x16x16_bf16 v[16:19], a[120:121], v[6:7], v[16:19]\n" \
"v_mfma_f32_16x16x16_bf16 v[20:23], a[128:129], v[6:7], v[20:23]\nv_mfma_f32_16x16x16_bf16 v[8:11], a[106:107], v[6:7], v[8:11]\nv_mfma_f32_16x16x16_bf16 v[12:15], a[114:115], v[6:7], v[12:15]\nv_mfma_f32_16x16x16_bf16 v[16:19], a[122:123], v[6:7], v[16:19]\nv_mfma_f32_16x16x16_bf16 v[20:23], a[130:131], v[6:7], v[20:23]\ns_waitcnt vmcnt(28)\ns_barrier\ns_add_u32 m0, %[tb], 0\n" \
"s_add_u32 s44, %[wo], 1146880\nbuffer_load_dwordx4 v92, %[rw], s44 offen nt lds\ns_add_u32 m0, %[tb], 1024\ns_add_u32 s44, %[wo], 1204224\nbuffer_load_dwordx4 v92, %[rw], s44 offen nt lds\ns_add_u32 m0, %[tb], 2048\ns_add_u32 s44, %[wo], 1261568\nbuffer_load_dwordx4 v92, %[rw], s44 offen nt lds\n" \
"s_add_u32 m0, %[tb], 3072\ns_add_u32 s44, %[wo], 1318912\nbuffer_load_dwordx4 v92, %[rw], s44 offen nt lds\nds_read_b64_tr_b16 a[108:109], v93 offset:4096\nds_read_b64_tr_b16 a[110:111], v93 offset:4608\nds_read_b64_tr_b16 a[116:117], v93 offset:4104\nds_read_b64_tr_b16 a[118:119], v93 offset:4616\nds_read_b64_tr_b16 a[124:125], v93 offset:16384\n" \
"ds_read_b64_tr_b16 a[126:127], v93 offset:16896\nds_read_b64_tr_b16 a[132:133], v93 offset:16392\nds_read_b64_tr_b16 a[134:135], v93 offset:16904\ns_waitcnt lgkmcnt(0)\nv_mfma_f32_16x16x16_bf16 v[8:11], a[108:109], v[6:7], v[8:11]\nv_mfma_f32_16x16x16_bf16 v[12:15], a[116:117], v[6:7], v[12:15]\nv_mfma_f32_16x16x16_bf16 v[16:19], a[124:125], v[6:7], v[16:19]\nv_mfma_f32_16x16x16_bf16 v[20:23], a[132:133], v[6:7], v[20:23]\n" \
"v_mfma_f32_16x16x16_bf16 v[8:11], a[110:111], v[6:7], v[8:11]\nv_mfma_f32_16x16x16_bf16 v[12:15], a[118:119], v[6:7], v[12:15]\nv_mfma_f32_16x16x16_bf16 v[16:19], a[126:127], v[6:7], v[16:19]\nv_mfma_f32_16x16x16_bf16 v[20:23], a[134:135], v[6:7], v[20:23]\ns_waitcnt vmcnt(28)\ns_barrier\ns_add_u32 m0, %[tb], 4096\ns_add_u32 s44, %[wo], 1376256\n" \
"buffer_load_dwordx4 v92, %[rw], s44 offen nt lds\ns_add_u32 m0, %[tb], 5120\ns_add_u32 s44, %[wo], 1433600\nbuffer_load_dwordx4 v92, %[rw], s44 offen nt lds\ns_add_u32 m0, %[tb], 6144\ns_add_u32 s44, %[wo], 1490944\nbuffer_load_dwordx4 v92, %[rw], s44 offen nt lds\ns_add_u32 m0, %[tb], 7168\n" \
"s_add_u32 s44, %[wo], 1548288\nbuffer_load_dwordx4 v92, %[rw], s44 offen nt lds\nds_read_b64_tr_b16 a[208:209], v93 offset:8192\nds_read_b64_tr_b16 a[210:211], v93 offset:8704\nds_read_b64_tr_b16 a[216:217], v93 offset:8200\nds_read_b64_tr_b16 a[218:219], v93 offset:8712\nds_read_b64_tr_b16 a[224:225], v93 offset:20480\nds_read_b64_tr_b16 a[226:227], v93 offset:20992\n" \
"ds_read_b64_tr_b16 a[232:233], v93 offset:20488\nds_read_b64_tr_b16 a[234:235], v93 offset:21000\ns_waitcnt lgkmcnt(0)\nv_mfma_f32_16x16x16_bf16 v[8:11], a[208:209], v[6:7], v[8:11]\nv_mfma_f32_16x16x16_bf16 v[12:15], a[216:217], v[6:7], v[12:15]\nv_mfma_f32_16x16x16_bf16 v[16:19], a[224:225], v[6:7], v[16:19]\nv_mfma_f32_16x16x16_bf16 v[20:23], a[232:233], v[6:7], v[20:23]\nv_mfma_f32_16x16x16_bf16 v[8:11], a[210:211], v[6:7], v[8:11]\n" \
"v_mfma_f32_16x16x16_bf16 v[12:15], a[218:219], v[6:7], v[12:15]\nv_mfma_f32_16x16x16_bf16 v[16:19], a[226:227], v[6:7], v[16:19]\nv_mfma_f32_16x16x16_bf16 v[20:23], a[234:235], v[6:7], v[20:23]\ns_waitcnt vmcnt(28)\ns_barrier\ns_add_u32 m0, %[tb], 8192\ns_add_u32 s44, %[wo], 1605632\nbuffer_load_dwordx4 v92, %[rw], s44 offen nt lds\n" \
"s_add_u32 m0, %[tb], 9216\ns_add_u32 s44, %[wo], 1662976\nbuffer_load_dwordx4 v92, %[rw], s44 offen nt lds\ns_add_u32 m0, %[tb], 10240\ns_add_u32 s44, %[wo], 1720320\nbuffer_load_dwordx4 v92, %[rw], s44 offen nt lds\ns_add_u32 m0, %[tb], 11264\ns_add_u32 s44, %[wo], 1777664\n" \
"buffer_load_dwordx4 v92, %[rw], s44 offen nt lds\nds_read_b64_tr_b16 a[212:213], v94 offset:0\nds_read_b64_tr_b16 a[214:215], v94 offset:512\nds_read_b64_tr_b16 a[220:221], v94 offset:8\nds_read_b64_tr_b16 a[222:223], v94 offset:520\nds_read_b64_tr_b16 a[228:229], v94 offset:24576\nds_read_b64_tr_b16 a[230:231], v94 offset:25088\nds_read_b64_tr_b16 a[236:237], v94 offset:24584\n" \
"ds_read_b64_tr_b16 a[238:239], v94 offset:25096\ns_waitcnt lgkmcnt(0)\nv_mfma_f32_16x16x16_bf16 v[8:11], a[212:213], v[6:7], v[8:11]\nv_mfma_f32_16x16x16_bf16 v[12:15], a[220:221], v[6:7], v[12:15]\nv_mfma_f32_16x16x16_bf16 v[16:19], a[228:229], v[6:7], v[16:19]\nv_mfma_f32_16x16x16_bf16 v[20:23], a[236:237], v[6:7], v[20:23]\nv_mfma_f32_16x16x16_bf16 v[8:11], a[214:215], v[6:7], v[8:11]\nv_mfma_f32_16x16x16_bf16 v[12:15], a[222:223], v[6:7], v[12:15]\n" \
"v_mfma_f32_16x16x16_bf16 v[16:19], a[230:231], v[6:7], v[16:19]\nv_mfma_f32_16x16x16_bf16 v[20:23], a[238:239], v[6:7], v[20:23]\ns_waitcnt vmcnt(28)\ns_barrier\ns_add_u32 m0, %[lw], 0\ns_add_u32 s44, %[wo], 2293760\nbuffer_load_dwordx4 v92, %[rw], s44 offen nt lds\ns_add_u32 m0, %[lw], 1024\n" \
"s_add_u32 s44, %[wo], 2351104\nbuffer_load_dwordx4 v92, %[rw], s44 offen nt lds\ns_add_u32 m0, %[lw], 2048\ns_add_u32 s44, %[wo], 2408448\nbuffer_load_dwordx4 v92, %[rw], s44 offen nt lds\ns_add_u32 m0, %[lw], 3072\ns_add_u32 s44, %[wo], 2465792\nbuffer_load_dwordx4 v92, %[rw], s44 offen nt lds\n" \
"ds_read_b64_tr_b16 v[152:153], v94 offset:4096\nds_read_b64_tr_b16 v[154:155], v94 offset:4608\nds_read_b64_tr_b16 v[160:161], v94 offset:4104\nds_read_b64_tr_b16 v[162:163], v94 offset:4616\nds_read_b64_tr_b16 v[168:169], v94 offset:28672\nds_read_b64_tr_b16 v[170:171], v94 offset:29184\nds_read_b64_tr_b16 v[176:177], v94 offset:28680\nds_read_b64_tr_b16 v[178:179], v94 offset:29192\n" \
"s_waitcnt lgkmcnt(0)\nv_mfma_f32_16x16x16_bf16 v[8:11], v[152:153], v[6:7], v[8:11]\nv_mfma_f32_16x16x16_bf16 v[12:15], v[160:161], v[6:7], v[12:15]\nv_mfma_f32_16x16x16_bf16 v[16:19], v[168:169], v[6:7], v[16:19]\nv_mfma_f32_16x16x16_bf16 v[20:23], v[176:177], v[6:7], v[20:23]\nv_mfma_f32_16x16x16_bf16 v[8:11], v[154:155], v[6:7], v[8:11]\nv_mfma_f32_16x16x16_bf16 v[12:15], v[162:163], v[6:7], v[12:15]\nv_mfma_f32_16x16x16_bf16 v[16:19], v[170:171], v[6:7], v[16:19]\n" \
"v_mfma_f32_16x16x16_bf16 v[20:23], v[178:179], v[6:7], v[20:23]\ns_waitcnt vmcnt(28)\ns_barrier\ns_add_u32 m0, %[lw], 4096\ns_add_u32 s44, %[wo], 2523136\nbuffer_load_dwordx4 v92, %[rw], s44 offen nt lds\ns_add_u32 m0, %[lw], 5120\ns_add_u32 s44, %[wo], 2580480\n" \
"buffer_load_dwordx4 v92, %[rw], s44 offen nt lds\ns_add_u32 m0, %[lw], 6144\ns_add_u32 s44, %[wo], 2637824\nbuffer_load_dwordx4 v92, %[rw], s44 offen nt lds\ns_add_u32 m0, %[lw], 7168\ns_add_u32 s44, %[wo], 2695168\nbuffer_load_dwordx4 v92, %[rw], s44 offen nt lds\nds_read_b64_tr_b16 v[156:157], v94 offset:8192\n" \
"ds_read_b64_tr_b16 v[158:159], v94 offset:8704\nds_read_b64_tr_b16 v[164:165], v94 offset:8200\nds_read_b64_tr_b16 v[166:167], v94 offset:8712\nds_read_b64_tr_b16 v[172:173], v94 offset:32768\nds_read_b64_tr_b16 v[174:175], v94 offset:33280\nds_read_b64_tr_b16 v[180:181], v94 offset:32776\nds_read_b64_tr_b16 v[182:183], v94 offset:33288\ns_waitcnt lgkmcnt(0)\n" \
"v_mfma_f32_16x16x16_bf16 v[8:11], v[156:157], v[6:7], v[8:11]\nv_mfma_f32_16x16x16_bf16 v[12:15], v[164:165], v[6:7], v[12:15]\nv_mfma_f32_16x16x16_bf16 v[16:19], v[172:173], v[6:7], v[16:19]\nv_mfma_f32_16x16x16_bf16 v[20:23], v[180:181], v[6:7], v[20:23]\nv_mfma_f32_16x16x16_bf16 v[8:11], v[158:159], v[6:7], v[8:11]\nv_mfma_f32_16x16x16_bf16 v[12:15], v[166:167], v[6:7], v[12:15]\nv_mfma_f32_16x16x16_bf16 v[16:19], v[174:175], v[6:7], v[16:19]\nv_mfma_f32_16x16x16_bf16 v[20:23], v[182:183], v[6:7], v[20:23]\n" \
"s_waitcnt vmcnt(28)\ns_barrier\ns_add_u32 m0, %[lw], 8192\ns_add_u32 s44, %[wo], 2752512\nbuffer_load_dwordx4 v92, %[rw], s44 offen nt lds\ns_add_u32 m0, %[lw], 9216\ns_add_u32 s44, %[wo], 2809856\nbuffer_load_dwordx4 v92, %[rw], s44 offen nt lds\n" \
"s_add_u32 m0, %[lw], 10240\ns_add_u32 s44, %[wo], 2867200\nbuffer_load_dwordx4 v92, %[rw], s44 offen nt lds\ns_add_u32 m0, %[lw], 11264\ns_add_u32 s44, %[wo], 2924544\nbuffer_load_dwordx4 v92, %[rw], s44 offen nt lds\nds_read_b64_tr_b16 a[32:33], v94 offset:12288\nds_read_b64_tr_b16 a[34:35], v94 offset:12800\n" \
"ds_read_b64_tr_b16 v[88:89], v94 offset:12296\nds_read_b64_tr_b16 v[90:91], v94 offset:12808\nds_read_b64_tr_b16 a[40:41], v94 offset:36864\nds_read_b64_tr_b16 a[42:43], v94 offset:37376\nds_read_b64_tr_b16 a[48:49], v94 offset:36872\nds_read_b64_tr_b16 a[50:51], v94 offset:37384\ns_waitcnt lgkmcnt(0)\nv_mfma_f32_16x16x16_bf16 v[8:11], a[32:33], v[6:7], v[8:11]\n" \
"v_mfma_f32_16x16x16_bf16 v[12:15], v[88:89], v[6:7], v[12:15]\nv_mfma_f32_16x16x16_bf16 v[16:19], a[40:41], v[6:7], v[16:19]\nv_mfma_f32_16x16x16_bf16 v[20:23], a[48:49], v[6:7], v[20:23]\nv_mfma_f32_16x16x16_bf16 v[8:11], a[34:35], v[6:7], v[8:11]\nv_mfma_f32_16x16x16_bf16 v[12:15], v[90:91], v[6:7], v[12:15]\nv_mfma_f32_16x16x16_bf16 v[16:19], a[42:43], v[6:7], v[16:19]\nv_mfma_f32_16x16x16_bf16 v[20:23], a[50:51], v[6:7], v[20:23]\ns_waitcnt vmcnt(28)\n" \
"s_barrier\ns_add_u32 m0, %[lw], 12288\ns_add_u32 s44, %[wo], 2981888\nbuffer_load_dwordx4 v92, %[rw], s44 offen nt lds\ns_add_u32 m0, %[lw], 13312\ns_add_u32 s44, %[wo], 3039232\nbuffer_load_dwordx4 v92, %[rw], s44 offen nt lds\ns_add_u32 m0, %[lw], 14336\n" \
"s_add_u32 s44, %[wo], 3096576\nbuffer_load_dwordx4 v92, %[rw], s44 offen nt lds\ns_add_u32 m0, %[lw], 15360\ns_add_u32 s44, %[wo], 3153920\nbuffer_load_dwordx4 v92, %[rw], s44 offen nt lds\nds_read_b64_tr_b16 a[36:37], v94 offset:16384\nds_read_b64_tr_b16 a[38:39], v94 offset:16896\nds_read_b64_tr_b16 v[84:85], v94 offset:16392\n" \
"ds_read_b64_tr_b16 v[86:87], v94 offset:16904\nds_read_b64_tr_b16 a[44:45], v94 offset:40960\nds_read_b64_tr_b16 a[46:47], v94 offset:41472\nds_read_b64_tr_b16 a[52:53], v94 offset:40968\nds_read_b64_tr_b16 a[54:55], v94 offset:41480\ns_waitcnt lgkmcnt(0)\nv_mfma_f32_16x16x16_bf16 v[8:11], a[36:37], v[6:7], v[8:11]\nv_mfma_f32_16x16x16_bf16 v[12:15], v[84:85], v[6:7], v[12:15]\n" \
"v_mfma_f32_16x16x16_bf16 v[16:19], a[44:45], v[6:7], v[16:19]\nv_mfma_f32_16x16x16_bf16 v[20:23], a[52:53], v[6:7], v[20:23]\nv_mfma_f32_16x16x16_bf16 v[8:11], a[38:39], v[6:7], v[8:11]\nv_mfma_f32_16x16x16_bf16 v[12:15], v[86:87], v[6:7], v[12:15]\nv_mfma_f32_16x16x16_bf16 v[16:19], a[46:47], v[6:7], v[16:19]\nv_mfma_f32_16x16x16_bf16 v[20:23], a[54:55], v[6:7], v[20:23]\ns_waitcnt vmcnt(28)\ns_barrier\n" \
"s_add_u32 m0, %[lw], 16384\ns_add_u32 s44, %[wo], 3211264\nbuffer_load_dwordx4 v92, %[rw], s44 offen nt lds\ns_add_u32 m0, %[lw], 17408\ns_add_u32 s44, %[wo], 3268608\nbuffer_load_dwordx4 v92, %[rw], s44 offen nt lds\ns_add_u32 m0, %[lw], 18432\ns_add_u32 s44, %[wo], 3325952\n" \
"buffer_load_dwordx4 v92, %[rw], s44 offen nt lds\ns_add_u32 m0, %[lw], 19456\ns_add_u32 s44, %[wo], 3383296\nbuffer_load_dwordx4 v92, %[rw], s44 offen nt lds\nds_read_b64_tr_b16 a[56:57], v94 offset:20480\nds_read_b64_tr_b16 a[58:59], v94 offset:20992\nds_read_b64_tr_b16 a[64:65], v94 offset:20488\nds_read_b64_tr_b16 a[66:67], v94 offset:21000\n" \
"ds_read_b64_tr_b16 v[80:81], v94 offset:45056\nds_read_b64_tr_b16 v[82:83], v94 offset:45568\nds_read_b64_tr_b16 a[72:73], v94 offset:45064\nds_read_b64_tr_b16 a[74:75], v94 offset:45576\ns_waitcnt lgkmcnt(0)\nv_mfma_f32_16x16x16_bf16 v[8:11], a[56:57], v[6:7], v[8:11]\nv_mfma_f32_16x16x16_bf16 v[12:15], a[64:65], v[6:7], v[12:15]\nv_mfma_f32_16x16x16_bf16 v[16:19], v[80:81], v[6:7], v[16:19]\n" \
"v_mfma_f32_16x16x16_bf16 v[20:23], a[72:73], v[6:7], v[20:23]\nv_mfma_f32_16x16x16_bf16 v[8:11], a[58:59], v[6:7], v[8:11]\nv_mfma_f32_16x16x16_bf16 v[12:15], a[66:67], v[6:7], v[12:15]\nv_mfma_f32_16x16x16_bf16 v[16:19], v[82:83], v[6:7], v[16:19]\nv_mfma_f32_16x16x16_bf16 v[20:23], a[74:75], v[6:7], v[20:23]\ns_waitcnt vmcnt(28)\ns_barrier\ns_add_u32 m0, %[lw], 20480\n" \
"s_add_u32 s44, %[wo], 3440640\nbuffer_load_dwordx4 v92, %[rw], s44 offen nt lds\ns_add_u32 m0, %[lw], 21504\ns_add_u32 s44, %[wo], 3497984\nbuffer_load_dwordx4 v92, %[rw], s44 offen nt lds\ns_add_u32 m0, %[lw], 22528\ns_add_u32 s44, %[wo], 3555328\nbuffer_load_dwordx4 v92, %[rw], s44 offen nt lds\n" \
"s_add_u32 m0, %[lw], 23552\ns_add_u32 s44, %[wo], 3612672\nbuffer_load_dwordx4 v92, %[rw], s44 offen nt lds\nds_read_b64_tr_b16 a[60:61], v93 offset:0\nds_read_b64_tr_b16 a[62:63], v93 offset:512\nds_read_b64_tr_b16 a[68:69], v93 offset:8\nds_read_b64_tr_b16 a[70:71], v93 offset:520\nds_read_b64_tr_b16 v[76:77], v93 offset:12288\n" \
"ds_read_b64_tr_b16 v[78:79], v93 offset:12800\nds_read_b64_tr_b16 a[76:77], v93 offset:12296\nds_read_b64_tr_b16 a[78:79], v93 offset:12808\ns_waitcnt lgkmcnt(0)\nv_mfma_f32_16x16x16_bf16 v[8:11], a[60:61], v[6:7], v[8:11]\nv_mfma_f32_16x16x16_bf16 v[12:15], a[68:69], v[6:7], v[12:15]\nv_mfma_f32_16x16x16_bf16 v[16:19], v[76:77], v[6:7], v[16:19]\nv_mfma_f32_16x16x16_bf16 v[20:23], a[76:77], v[6:7], v[20:23]\n" \
"v_mfma_f32_16x16x16_bf16 v[8:11], a[62:63], v[6:7], v[8:11]\nv_mfma_f32_16x16x16_bf16 v[12:15], a[70:71], v[6:7], v[12:15]\nv_mfma_f32_16x16x16_bf16 v[16:19], v[78:79], v[6:7], v[16:19]\nv_mfma_f32_16x16x16_bf16 v[20:23], a[78:79], v[6:7], v[20:23]\ns_waitcnt vmcnt(28)\ns_barrier\ns_add_u32 m0, %[tb], 0\ns_add_u32 s44, %[wo], 4128768\n" \
"buffer_load_dwordx4 v92, %[rw], s44 offen nt lds\ns_add_u32 m0, %[tb], 1024\ns_add_u32 s44, %[wo], 4186112\nbuffer_load_dwordx4 v92, %[rw], s44 offen nt lds\ns_add_u32 m0, %[tb], 2048\ns_add_u32 s44, %[wo], 4243456\nbuffer_load_dwordx4 v92, %[rw], s44 offen nt lds\ns_add_u32 m0, %[tb], 3072\n" \
"s_add_u32 s44, %[wo], 4300800\nbuffer_load_dwordx4 v92, %[rw], s44 offen nt lds\nds_read_b64_tr_b16 a[80:81], v93 offset:4096\nds_read_b64_tr_b16 a[82:83], v93 offset:4608\nds_read_b64_tr_b16 a[88:89], v93 offset:4104\nds_read_b64_tr_b16 a[90:91], v93 offset:4616\nds_read_b64_tr_b16 a[96:97], v93 offset:16384\nds_read_b64_tr_b16 a[98:99], v93 offset:16896\n" \
"ds_read_b64_tr_b16 v[72:73], v93 offset:16392\nds_read_b64_tr_b16 v[74:75], v93 offset:16904\ns_waitcnt lgkmcnt(0)\nv_mfma_f32_16x16x16_bf16 v[8:11], a[80:81], v[6:7], v[8:11]\nv_mfma_f32_16x16x16_bf16 v[12:15], a[88:89], v[6:7], v[12:15]\nv_mfma_f32_16x16x16_bf16 v[16:19], a[96:97], v[6:7], v[16:19]\nv_mfma_f32_16x16x16_bf16 v[20:23], v[72:73], v[6:7], v[20:23]\nv_mfma_f32_16x16x16_bf16 v[8:11], a[82:83], v[6:7], v[8:11]\n" \
"v_mfma_f32_16x16x16_bf16 v[12:15], a[90:91], v[6:7], v[12:15]\nv_mfma_f32_16x16x16_bf16 v[16:19], a[98:99], v[6:7], v[16:19]\nv_mfma_f32_16x16x16_bf16 v[20:23], v[74:75], v[6:7], v[20:23]\ns_waitcnt vmcnt(28)\ns_barrier\ns_add_u32 m0, %[tb], 4096\ns_add_u32 s44, %[wo], 4358144\nbuffer_load_dwordx4 v92, %[rw], s44 offen nt lds\n" \
"s_add_u32 m0, %[tb], 5120\ns_add_u32 s44, %[wo], 4415488\nbuffer_load_dwordx4 v92, %[rw], s44 offen nt lds\ns_add_u32 m0, %[tb], 6144\ns_add_u32 s44, %[wo], 4472832\nbuffer_load_dwordx4 v92, %[rw], s44 offen nt lds\ns_add_u32 m0, %[tb], 7168\ns_add_u32 s44, %[wo], 4530176\n" \
"buffer_load_dwordx4 v92, %[rw], s44 offen nt lds\nds_read_b64_tr_b16 a[84:85], v93 offset:8192\nds_read_b64_tr_b16 a[86:87], v93 offset:8704\nds_read_b64_tr_b16 a[92:93], v93 offset:8200\nds_read_b64_tr_b16 a[94:95], v93 offset:8712\nds_read_b64_tr_b16 a[100:101], v93 offset:20480\nds_read_b64_tr_b16 a[102:103], v93 offset:20992\nds_read_b64_tr_b16 v[68:69], v93 offset:20488\n" \
"ds_read_b64_tr_b16 v[70:71], v93 offset:21000\ns_waitcnt lgkmcnt(0)\nv_mfma_f32_16x16x16_bf16 v[8:11], a[84:85], v[6:7], v[8:11]\nv_mfma_f32_16x16x16_bf16 v[12:15], a[92:93], v[6:7], v[12:15]\nv_mfma_f32_16x16x16_bf16 v[16:19], a[100:101], v[6:7], v[16:19]\nv_mfma_f32_16x16x16_bf16 v[20:23], v[68:69], v[6:7], v[20:23]\nv_mfma_f32_16x16x16_bf16 v[8:11], a[86:87], v[6:7], v[8:11]\nv_mfma_f32_16x16x16_bf16 v[12:15], a[94:95], v[6:7], v[12:15]\n" \
"v_mfma_f32_16x16x16_bf16 v[16:19], a[102:103], v[6:7], v[16:19]\nv_mfma_f32_16x16x16_bf16 v[20:23], v[70:71], v[6:7], v[20:23]\ns_waitcnt vmcnt(28)\ns_barrier\ns_add_u32 m0, %[tb], 8192\ns_add_u32 s44, %[wo], 4587520\nbuffer_load_dwordx4 v92, %[rw], s44 offen nt lds\ns_add_u32 m0, %[tb], 9216\n" \
"s_add_u32 s44, %[wo], 4644864\nbuffer_load_dwordx4 v92, %[rw], s44 offen nt lds\ns_add_u32 m0, %[tb], 10240\ns_add_u32 s44, %[wo], 4702208\nbuffer_load_dwordx4 v92, %[rw], s44 offen nt lds\ns_add_u32 m0, %[tb], 11264\ns_add_u32 s44, %[wo], 4759552\nbuffer_load_dwordx4 v92, %[rw], s44 offen nt lds\n" \
"ds_read_b64_tr_b16 a[136:137], v94 offset:0\nds_read_b64_tr_b16 a[138:139], v94 offset:512\nds_read_b64_tr_b16 v[64:65], v94 offset:8\nds_read_b64_tr_b16 v[66:67], v94 offset:520\nds_read_b64_tr_b16 a[144:145], v94 offset:24576\nds_read_b64_tr_b16 a[146:147], v94 offset:25088\nds_read_b64_tr_b16 a[152:153], v94 offset:24584\nds_read_b64_tr_b16 a[154:155], v94 offset:25096\n" \
"s_waitcnt lgkmcnt(0)\nv_mfma_f32_16x16x16_bf16 v[8:11], a[136:137], v[6:7], v[8:11]\nv_mfma_f32_16x16x16_bf16 v[12:15], v[64:65], v[6:7], v[12:15]\nv_mfma_f32_16x16x16_bf16 v[16:19], a[144:145], v[6:7], v[16:19]\nv_mfma_f32_16x16x16_bf16 v[20:23], a[152:153], v[6:7], v[20:23]\nv_mfma_f32_16x16x16_bf16 v[8:11], a[138:139], v[6:7], v[8:11]\nv_mfma_f32_16x16x16_bf16 v[12:15], v[66:67], v[6:7], v[12:15]\nv_mfma_f32_16x16x16_bf16 v[16:19], a[146:147], v[6:7], v[16:19]\n" \
"v_mfma_f32_16x16x16_bf16 v[20:23], a[154:155], v[6:7], v[20:23]\ns_waitcnt vmcnt(28)\ns_barrier\ns_add_u32 m0, %[lw], 0\ns_add_u32 s44, %[wo], 4816896\nbuffer_load_dwordx4 v92, %[rw], s44 offen nt lds\ns_add_u32 m0, %[lw], 1024\ns_add_u32 s44, %[wo], 4874240\n" \
"buffer_load_dwordx4 v92, %[rw], s44 offen nt lds\ns_add_u32 m0, %[lw], 2048\ns_add_u32 s44, %[wo], 4931584\nbuffer_load_dwordx4 v92, %[rw], s44 offen nt lds\ns_add_u32 m0, %[lw], 3072\ns_add_u32 s44, %[wo], 4988928\nbuffer_load_dwordx4 v92, %[rw], s44 offen nt lds\nds_read_b64_tr_b16 a[140:141], v94 offset:4096\n" \
"ds_read_b64_tr_b16 a[142:143], v94 offset:4608\nds_read_b64_tr_b16 v[60:61], v94 offset:4104\nds_read_b64_tr_b16 v[62:63], v94 offset:4616\nds_read_b64_tr_b16 a[148:149], v94 offset:28672\nds_read_b64_tr_b16 a[150:151], v94 offset:29184\nds_read_b64_tr_b16 a[156:157], v94 offset:28680\nds_read_b64_tr_b16 a[158:159], v94 offset:29192\ns_waitcnt lgkmcnt(0)\n" \
"v_mfma_f32_16x16x16_bf16 v[8:11], a[140:141], v[6:7], v[8:11]\nv_mfma_f32_16x16x16_bf16 v[12:15], v[60:61], v[6:7], v[12:15]\nv_mfma_f32_16x16x16_bf16 v[16:19], a[148:149], v[6:7], v[16:19]\nv_mfma_f32_16x16x16_bf16 v[20:23], a[156:157], v[6:7], v[20:23]\nv_mfma_f32_16x16x16_bf16 v[8:11], a[142:143], v[6:7], v[8:11]\nv_mfma_f32_16x16x16_bf16 v[12:15], v[62:63], v[6:7], v[12:15]\nv_mfma_f32_16x16x16_bf16 v[16:19], a[150:151], v[6:7], v[16:19]\nv_mfma_f32_16x16x16_bf16 v[20:23], a[158:159], v[6:7], v[20:23]\n" \
"s_waitcnt vmcnt(28)\ns_barrier\ns_add_u32 m0, %[lw], 4096\ns_add_u32 s44, %[wo], 5046272\nbuffer_load_dwordx4 v92, %[rw], s44 offen nt lds\ns_add_u32 m0, %[lw], 5120\ns_add_u32 s44, %[wo], 5103616\nbuffer_load_dwordx4 v92, %[rw], s44 offen nt lds\n" \
"s_add_u32 m0, %[lw], 6144\ns_add_u32 s44, %[wo], 5160960\nbuffer_load_dwordx4 v92, %[rw], s44 offen nt lds\ns_add_u32 m0, %[lw], 7168\ns_add_u32 s44, %[wo], 5218304\nbuffer_load_dwordx4 v92, %[rw], s44 offen nt lds\nds_read_b64_tr_b16 a[160:161], v94 offset:8192\nds_read_b64_tr_b16 a[162:163], v94 offset:8704\n" \
"ds_read_b64_tr_b16 a[168:169], v94 offset:8200\nds_read_b64_tr_b16 a[170:171], v94 offset:8712\nds_read_b64_tr_b16 v[56:57], v94 offset:32768\nds_read_b64_tr_b16 v[58:59], v94 offset:33280\nds_read_b64_tr_b16 a[176:177], v94 offset:32776\nds_read_b64_tr_b16 a[178:179], v94 offset:33288\ns_waitcnt lgkmcnt(0)\nv_mfma_f32_16x16x16_bf16 v[8:11], a[160:161], v[6:7], v[8:11]\n" \
"v_mfma_f32_16x16x16_bf16 v[12:15], a[168:169], v[6:7], v[12:15]\nv_mfma_f32_16x16x16_bf16 v[16:19], v[56:57], v[6:7], v[16:19]\nv_mfma_f32_16x16x16_bf16 v[20:23], a[176:177], v[6:7], v[20:23]\nv_mfma_f32_16x16x16_bf16 v[8:11], a[162:163], v[6:7], v[8:11]\nv_mfma_f32_16x16x16_bf16 v[12:15], a[170:171], v[6:7], v[12:15]\nv_mfma_f32_16x16x16_bf16 v[16:19], v[58:59], v[6:7], v[16:19]\nv_mfma_f32_16x16x16_bf16 v[20:23], a[178:179], v[6:7], v[20:23]\ns_waitcnt vmcnt(28)\n" \
"s_barrier\ns_add_u32 m0, %[lw], 8192\ns_add_u32 s44, %[wo], 5275648\nbuffer_load_dwordx4 v92, %[rw], s44 offen nt lds\ns_add_u32 m0, %[lw], 9216\ns_add_u32 s44, %[wo], 5332992\nbuffer_load_dwordx4 v92, %[rw], s44 offen nt lds\ns_add_u32 m0, %[lw], 10240\n" \
"s_add_u32 s44, %[wo], 5390336\nbuffer_load_dwordx4 v92, %[rw], s44 offen nt lds\ns_add_u32 m0, %[lw], 11264\ns_add_u32 s44, %[wo], 5447680\nbuffer_load_dwordx4 v92, %[rw], s44 offen nt lds\nds_read_b64_tr_b16 a[164:165], v94 offset:12288\nds_read_b64_tr_b16 a[166:167], v94 offset:12800\nds_read_b64_tr_b16 a[172:173], v94 offset:12296\n" \
"ds_read_b64_tr_b16 a[174:175], v94 offset:12808\nds_read_b64_tr_b16 v[52:53], v94 offset:36864\nds_read_b64_tr_b16 v[54:55], v94 offset:37376\nds_read_b64_tr_b16 a[180:181], v94 offset:36872\nds_read_b64_tr_b16 a[182:183], v94 offset:37384\ns_waitcnt lgkmcnt(0)\nv_mfma_f32_16x16x16_bf16 v[8:11], a[164:165], v[6:7], v[8:11]\nv_mfma_f32_16x16x16_bf16 v[12:15], a[172:173], v[6:7], v[12:15]\n" \
"v_mfma_f32_16x16x16_bf16 v[16:19], v[52:53], v[6:7], v[16:19]\nv_mfma_f32_16x16x16_bf16 v[20:23], a[180:181], v[6:7], v[20:23]\nv_mfma_f32_16x16x16_bf16 v[8:11], a[166:167], v[6:7], v[8:11]\nv_mfma_f32_16x16x16_bf16 v[12:15], a[174:175], v[6:7], v[12:15]\nv_mfma_f32_16x16x16_bf16 v[16:19], v[54:55], v[6:7], v[16:19]\nv_mfma_f32_16x16x16_bf16 v[20:23], a[182:183], v[6:7], v[20:23]\ns_waitcnt vmcnt(28)\ns_barrier\n" \
"s_add_u32 m0, %[lw], 12288\ns_add_u32 s44, %[wo], 5963776\nbuffer_load_dwordx4 v92, %[rw], s44 offen nt lds\ns_add_u32 m0, %[lw], 13312\ns_add_u32 s44, %[wo], 6021120\nbuffer_load_dwordx4 v92, %[rw], s44 offen nt lds\ns_add_u32 m0, %[lw], 14336\ns_add_u32 s44, %[wo], 6078464\n" \
"buffer_load_dwordx4 v92, %[rw], s44 offen nt lds\ns_add_u32 m0, %[lw], 15360\ns_add_u32 s44, %[wo], 6135808\nbuffer_load_dwordx4 v92, %[rw], s44 offen nt lds\nds_read_b64_tr_b16 a[184:185], v94 offset:16384\nds_read_b64_tr_b16 a[186:187], v94 offset:16896\nds_read_b64_tr_b16 a[192:193], v94 offset:16392\nds_read_b64_tr_b16 a[194:195], v94 offset:16904\n" \
"ds_read_b64_tr_b16 a[200:201], v94 offset:40960\nds_read_b64_tr_b16 a[202:203], v94 offset:41472\nds_read_b64_tr_b16 v[48:49], v94 offset:40968\nds_read_b64_tr_b16 v[50:51], v94 offset:41480\ns_waitcnt lgkmcnt(0)\nv_mfma_f32_16x16x16_bf16 v[8:11], a[184:185], v[6:7], v[8:11]\nv_mfma_f32_16x16x16_bf16 v[12:15], a[192:193], v[6:7], v[12:15]\nv_mfma_f32_16x16x16_bf16 v[16:19], a[200:201], v[6:7], v[16:19]\n" \
"v_mfma_f32_16x16x16_bf16 v[20:23], v[48:49], v[6:7], v[20:23]\nv_mfma_f32_16x16x16_bf16 v[8:11], a[186:187], v[6:7], v[8:11]\nv_mfma_f32_16x16x16_bf16 v[12:15], a[194:195], v[6:7], v[12:15]\nv_mfma_f32_16x16x16_bf16 v[16:19], a[202:203], v[6:7], v[16:19]\nv_mfma_f32_16x16x16_bf16 v[20:23], v[50:51], v[6:7], v[20:23]\ns_waitcnt vmcnt(28)\ns_barrier\ns_add_u32 m0, %[lw], 16384\n" \
"s_add_u32 s44, %[wo], 6193152\nbuffer_load_dwordx4 v92, %[rw], s44 offen nt lds\ns_add_u32 m0, %[lw], 17408\ns_add_u32 s44, %[wo], 6250496\nbuffer_load_dwordx4 v92, %[rw], s44 offen nt lds\ns_add_u32 m0, %[lw], 18432\ns_add_u32 s44, %[wo], 6307840\nbuffer_load_dwordx4 v92, %[rw], s44 offen nt lds\n" \
"s_add_u32 m0, %[lw], 19456\ns_add_u32 s44, %[wo], 6365184\nbuffer_load_dwordx4 v92, %[rw], s44 offen nt lds\nds_read_b64_tr_b16 a[188:189], v94 offset:20480\nds_read_b64_tr_b16 a[190:191], v94 offset:20992\nds_read_b64_tr_b16 a[196:197], v94 offset:20488\nds_read_b64_tr_b16 a[198:199], v94 offset:21000\nds_read_b64_tr_b16 a[204:205], v94 offset:45056\n" \
"ds_read_b64_tr_b16 a[206:207], v94 offset:45568\nds_read_b64_tr_b16 v[44:45], v94 offset:45064\nds_read_b64_tr_b16 v[46:47], v94 offset:45576\ns_waitcnt lgkmcnt(0)\nv_mfma_f32_16x16x16_bf16 v[8:11], a[188:189], v[6:7], v[8:11]\nv_mfma_f32_16x16x16_bf16 v[12:15], a[196:197], v[6:7], v[12:15]\nv_mfma_f32_16x16x16_bf16 v[16:19], a[204:205], v[6:7], v[16:19]\nv_mfma_f32_16x16x16_bf16 v[20:23], v[44:45], v[6:7], v[20:23]\n" \
"v_mfma_f32_16x16x16_bf16 v[8:11], a[190:191], v[6:7], v[8:11]\nv_mfma_f32_16x16x16_bf16 v[12:15], a[198:199], v[6:7], v[12:15]\nv_mfma_f32_16x16x16_bf16 v[16:19], a[206:207], v[6:7], v[16:19]\nv_mfma_f32_16x16x16_bf16 v[20:23], v[46:47], v[6:7], v[20:23]\ns_waitcnt vmcnt(28)\ns_barrier\ns_add_u32 m0, %[lw], 20480\ns_add_u32 s44, %[wo], 6422528\n" \
"buffer_load_dwordx4 v92, %[rw], s44 offen nt lds\ns_add_u32 m0, %[lw], 21504\ns_add_u32 s44, %[wo], 6479872\nbuffer_load_dwordx4 v92, %[rw], s44 offen nt lds\ns_add_u32 m0, %[lw], 22528\ns_add_u32 s44, %[wo], 6537216\nbuffer_load_dwordx4 v92, %[rw], s44 offen nt lds\ns_add_u32 m0, %[lw], 23552\n" \
"s_add_u32 s44, %[wo], 6594560\nbuffer_load_dwordx4 v92, %[rw], s44 offen nt lds\nds_read_b64_tr_b16 a[240:241], v93 offset:0\nds_read_b64_tr_b16 a[242:243], v93 offset:512\nds_read_b64_tr_b16 v[40:41], v93 offset:8\nds_read_b64_tr_b16 v[42:43], v93 offset:520\nds_read_b64_tr_b16 a[248:249], v93 offset:12288\nds_read_b64_tr_b16 a[250:251], v93 offset:12800\n" \
"ds_read_b64_tr_b16 v[96:97], v93 offset:12296\nds_read_b64_tr_b16 v[98:99], v93 offset:12808\ns_waitcnt lgkmcnt(0)\nv_mfma_f32_16x16x16_bf16 v[8:11], a[240:241], v[6:7], v[8:11]\nv_mfma_f32_16x16x16_bf16 v[12:15], v[40:41], v[6:7], v[12:15]\nv_mfma_f32_16x16x16_bf16 v[16:19], a[248:249], v[6:7], v[16:19]\nv_mfma_f32_16x16x16_bf16 v[20:23], v[96:97], v[6:7], v[20:23]\nv_mfma_f32_16x16x16_bf16 v[8:11], a[242:243], v[6:7], v[8:11]\n" \
"v_mfma_f32_16x16x16_bf16 v[12:15], v[42:43], v[6:7], v[12:15]\nv_mfma_f32_16x16x16_bf16 v[16:19], a[250:251], v[6:7], v[16:19]\nv_mfma_f32_16x16x16_bf16 v[20:23], v[98:99], v[6:7], v[20:23]\ns_waitcnt vmcnt(28)\ns_barrier\ns_add_u32 m0, %[tb], 0\ns_add_u32 s44, %[wo], 6651904\nbuffer_load_dwordx4 v92, %[rw], s44 offen nt lds\n" \
"s_add_u32 m0, %[tb], 1024\ns_add_u32 s44, %[wo], 6709248\nbuffer_load_dwordx4 v92, %[rw], s44 offen nt lds\ns_add_u32 m0, %[tb], 2048\ns_add_u32 s44, %[wo], 6766592\nbuffer_load_dwordx4 v92, %[rw], s44 offen nt lds\ns_add_u32 m0, %[tb], 3072\ns_add_u32 s44, %[wo], 6823936\n" \
"buffer_load_dwordx4 v92, %[rw], s44 offen nt lds\nds_read_b64_tr_b16 a[244:245], v93 offset:4096\nds_read_b64_tr_b16 a[246:247], v93 offset:4608\nds_read_b64_tr_b16 v[36:37], v93 offset:4104\nds_read_b64_tr_b16 v[38:39], v93 offset:4616\nds_read_b64_tr_b16 a[252:253], v93 offset:16384\nds_read_b64_tr_b16 a[254:255], v93 offset:16896\nds_read_b64_tr_b16 v[100:101], v93 offset:16392\n" \
"ds_read_b64_tr_b16 v[102:103], v93 offset:16904\ns_waitcnt lgkmcnt(0)\nv_mfma_f32_16x16x16_bf16 v[8:11], a[244:245], v[6:7], v[8:11]\nv_mfma_f32_16x16x16_bf16 v[12:15], v[36:37], v[6:7], v[12:15]\nv_mfma_f32_16x16x16_bf16 v[16:19], a[252:253], v[6:7], v[16:19]\nv_mfma_f32_16x16x16_bf16 v[20:23], v[100:101], v[6:7], v[20:23]\nv_mfma_f32_16x16x16_bf16 v[8:11], a[246:247], v[6:7], v[8:11]\nv_mfma_f32_16x16x16_bf16 v[12:15], v[38:39], v[6:7], v[12:15]\n" \
"v_mfma_f32_16x16x16_bf16 v[16:19], a[254:255], v[6:7], v[16:19]\nv_mfma_f32_16x16x16_bf16 v[20:23], v[102:103], v[6:7], v[20:23]\ns_waitcnt vmcnt(28)\ns_barrier\ns_add_u32 m0, %[tb], 4096\ns_add_u32 s44, %[wo], 6881280\nbuffer_load_dwordx4 v92, %[rw], s44 offen nt lds\ns_add_u32 m0, %[tb], 5120\n" \
"s_add_u32 s44, %[wo], 6938624\nbuffer_load_dwordx4 v92, %[rw], s44 offen nt lds\ns_add_u32 m0, %[tb], 6144\ns_add_u32 s44, %[wo], 6995968\nbuffer_load_dwordx4 v92, %[rw], s44 offen nt lds\ns_add_u32 m0, %[tb], 7168\ns_add_u32 s44, %[wo], 7053312\nbuffer_load_dwordx4 v92, %[rw], s44 offen nt lds\n" \
"ds_read_b64_tr_b16 v[104:105], v93 offset:8192\nds_read_b64_tr_b16 v[106:107], v93 offset:8704\nds_read_b64_tr_b16 v[112:113], v93 offset:8200\nds_read_b64_tr_b16 v[114:115], v93 offset:8712\nds_read_b64_tr_b16 v[32:33], v93 offset:20480\nds_read_b64_tr_b16 v[34:35], v93 offset:20992\nds_read_b64_tr_b16 v[120:121], v93 offset:20488\nds_read_b64_tr_b16 v[122:123], v93 offset:21000\n" \
"s_waitcnt lgkmcnt(0)\nv_mfma_f32_16x16x16_bf16 v[8:11], v[104:105], v[6:7], v[8:11]\nv_mfma_f32_16x16x16_bf16 v[12:15], v[112:113], v[6:7], v[12:15]\nv_mfma_f32_16x16x16_bf16 v[16:19], v[32:33], v[6:7], v[16:19]\nv_mfma_f32_16x16x16_bf16 v[20:23], v[120:121], v[6:7], v[20:23]\nv_mfma_f32_16x16x16_bf16 v[8:11], v[106:107], v[6:7], v[8:11]\nv_mfma_f32_16x16x16_bf16 v[12:15], v[114:115], v[6:7], v[12:15]\nv_mfma_f32_16x16x16_bf16 v[16:19], v[34:35], v[6:7], v[16:19]\n" \
"v_mfma_f32_16x16x16_bf16 v[20:23], v[122:123], v[6:7], v[20:23]\ns_waitcnt vmcnt(28)\ns_barrier\ns_add_u32 m0, %[tb], 8192\ns_add_u32 s44, %[wo], 7110656\nbuffer_load_dwordx4 v92, %[rw], s44 offen nt lds\ns_add_u32 m0, %[tb], 9216\ns_add_u32 s44, %[wo], 7168000\n" \
"buffer_load_dwordx4 v92, %[rw], s44 offen nt lds\ns_add_u32 m0, %[tb], 10240\ns_add_u32 s44, %[wo], 7225344\nbuffer_load_dwordx4 v92, %[rw], s44 offen nt lds\ns_add_u32 m0, %[tb], 11264\ns_add_u32 s44, %[wo], 7282688\nbuffer_load_dwordx4 v92, %[rw], s44 offen nt lds\nds_read_b64_tr_b16 v[108:109], v94 offset:0\n" \
"ds_read_b64_tr_b16 v[110:111], v94 offset:512\nds_read_b64_tr_b16 v[116:117], v94 offset:8\nds_read_b64_tr_b16 v[118:119], v94 offset:520\nds_read_b64_tr_b16 v[28:29], v94 offset:24576\nds_read_b64_tr_b16 v[30:31], v94 offset:25088\nds_read_b64_tr_b16 v[124:125], v94 offset:24584\nds_read_b64_tr_b16 v[126:127], v94 offset:25096\ns_waitcnt lgkmcnt(0)\n" \
"v_mfma_f32_16x16x16_bf16 v[8:11], v[108:109], v[6:7], v[8:11]\nv_mfma_f32_16x16x16_bf16 v[12:15], v[116:117], v[6:7], v[12:15]\nv_mfma_f32_16x16x16_bf16 v[16:19], v[28:29], v[6:7], v[16:19]\nv_mfma_f32_16x16x16_bf16 v[20:23], v[124:125], v[6:7], v[20:23]\nv_mfma_f32_16x16x16_bf16 v[8:11], v[110:111], v[6:7], v[8:11]\nv_mfma_f32_16x16x16_bf16 v[12:15], v[118:119], v[6:7], v[12:15]\nv_mfma_f32_16x16x16_bf16 v[16:19], v[30:31], v[6:7], v[16:19]\nv_mfma_f32_16x16x16_bf16 v[20:23], v[126:127], v[6:7], v[20:23]\n" \
"s_waitcnt vmcnt(28)\ns_barrier\nds_read_b64_tr_b16 v[128:129], v94 offset:4096\nds_read_b64_tr_b16 v[130:131], v94 offset:4608\nds_read_b64_tr_b16 v[136:137], v94 offset:4104\nds_read_b64_tr_b16 v[138:139], v94 offset:4616\nds_read_b64_tr_b16 v[144:145], v94 offset:28672\nds_read_b64_tr_b16 v[146:147], v94 offset:29184\n" \
"ds_read_b64_tr_b16 v[24:25], v94 offset:28680\nds_read_b64_tr_b16 v[26:27], v94 offset:29192\ns_waitcnt lgkmcnt(0)\nv_mfma_f32_16x16x16_bf16 v[8:11], v[128:129], v[6:7], v[8:11]\nv_mfma_f32_16x16x16_bf16 v[12:15], v[136:137], v[6:7], v[12:15]\nv_mfma_f32_16x16x16_bf16 v[16:19], v[144:145], v[6:7], v[16:19]\nv_mfma_f32_16x16x16_bf16 v[20:23], v[24:25], v[6:7], v[20:23]\nv_mfma_f32_16x16x16_bf16 v[8:11], v[130:131], v[6:7], v[8:11]\n" \
"v_mfma_f32_16x16x16_bf16 v[12:15], v[138:139], v[6:7], v[12:15]\nv_mfma_f32_16x16x16_bf16 v[16:19], v[146:147], v[6:7], v[16:19]\nv_mfma_f32_16x16x16_bf16 v[20:23], v[26:27], v[6:7], v[20:23]\ns_waitcnt vmcnt(24)\nds_write_b128 v1, v[88:91] offset:0\nds_write_b128 v1, v[84:87] offset:1024\nds_write_b128 v1, v[80:83] offset:2048\nds_write_b128 v1, v[76:79] offset:3072\n" \
"s_barrier\nds_read_b64_tr_b16 v[132:133], v94 offset:8192\nds_read_b64_tr_b16 v[134:135], v94 offset:8704\nds_read_b64_tr_b16 v[140:141], v94 offset:8200\nds_read_b64_tr_b16 v[142:143], v94 offset:8712\nds_read_b64_tr_b16 v[148:149], v94 offset:32768\nds_read_b64_tr_b16 v[150:151], v94 offset:33280\nds_read_b64_tr_b16 v[76:77], v94 offset:32776\n" \
"ds_read_b64_tr_b16 v[78:79], v94 offset:33288\ns_waitcnt lgkmcnt(0)\nv_mfma_f32_16x16x16_bf16 v[8:11], v[132:133], v[6:7], v[8:11]\nv_mfma_f32_16x16x16_bf16 v[12:15], v[140:141], v[6:7], v[12:15]\nv_mfma_f32_16x16x16_bf16 v[16:19], v[148:149], v[6:7], v[16:19]\nv_mfma_f32_16x16x16_bf16 v[20:23], v[76:77], v[6:7], v[20:23]\nv_mfma_f32_16x16x16_bf16 v[8:11], v[134:135], v[6:7], v[8:11]\nv_mfma_f32_16x16x16_bf16 v[12:15], v[142:143], v[6:7], v[12:15]\n" \
"v_mfma_f32_16x16x16_bf16 v[16:19], v[150:151], v[6:7], v[16:19]\nv_mfma_f32_16x16x16_bf16 v[20:23], v[78:79], v[6:7], v[20:23]\ns_waitcnt vmcnt(20)\nds_write_b128 v1, v[72:75] offset:4096\nds_write_b128 v1, v[68:71] offset:5120\nds_write_b128 v1, v[64:67] offset:6144\nds_write_b128 v1, v[60:63] offset:7168\ns_barrier\n" \
"ds_read_b64_tr_b16 v[184:185], v94 offset:12288\nds_read_b64_tr_b16 v[186:187], v94 offset:12800\nds_read_b64_tr_b16 v[60:61], v94 offset:12296\nds_read_b64_tr_b16 v[62:63], v94 offset:12808\nds_read_b64_tr_b16 v[192:193], v94 offset:36864\nds_read_b64_tr_b16 v[194:195], v94 offset:37376\nds_read_b64_tr_b16 v[200:201], v94 offset:36872\nds_read_b64_tr_b16 v[202:203], v94 offset:37384\n" \
"s_waitcnt lgkmcnt(0)\nv_mfma_f32_16x16x16_bf16 v[8:11], v[184:185], v[6:7], v[8:11]\nv_mfma_f32_16x16x16_bf16 v[12:15], v[60:61], v[6:7], v[12:15]\nv_mfma_f32_16x16x16_bf16 v[16:19], v[192:193], v[6:7], v[16:19]\nv_mfma_f32_16x16x16_bf16 v[20:23], v[200:201], v[6:7], v[20:23]\nv_mfma_f32_16x16x16_bf16 v[8:11], v[186:187], v[6:7], v[8:11]\nv_mfma_f32_16x16x16_bf16 v[12:15], v[62:63], v[6:7], v[12:15]\nv_mfma_f32_16x16x16_bf16 v[16:19], v[194:195], v[6:7], v[16:19]\n" \
"v_mfma_f32_16x16x16_bf16 v[20:23], v[202:203], v[6:7], v[20:23]\ns_waitcnt vmcnt(16)\nds_write_b128 v1, v[56:59] offset:8192\nds_write_b128 v1, v[52:55] offset:9216\nds_write_b128 v1, v[48:51] offset:10240\nds_write_b128 v1, v[44:47] offset:11264\ns_barrier\nds_read_b64_tr_b16 v[188:189], v94 offset:16384\n" \
"ds_read_b64_tr_b16 v[190:191], v94 offset:16896\nds_read_b64_tr_b16 v[44:45], v94 offset:16392\nds_read_b64_tr_b16 v[46:47], v94 offset:16904\nds_read_b64_tr_b16 v[196:197], v94 offset:40960\nds_read_b64_tr_b16 v[198:199], v94 offset:41472\nds_read_b64_tr_b16 v[204:205], v94 offset:40968\nds_read_b64_tr_b16 v[206:207], v94 offset:41480\ns_waitcnt lgkmcnt(0)\n" \
"v_mfma_f32_16x16x16_bf16 v[8:11], v[188:189], v[6:7], v[8:11]\nv_mfma_f32_16x16x16_bf16 v[12:15], v[44:45], v[6:7], v[12:15]\nv_mfma_f32_16x16x16_bf16 v[16:19], v[196:197], v[6:7], v[16:19]\nv_mfma_f32_16x16x16_bf16 v[20:23], v[204:205], v[6:7], v[20:23]\nv_mfma_f32_16x16x16_bf16 v[8:11], v[190:191], v[6:7], v[8:11]\nv_mfma_f32_16x16x16_bf16 v[12:15], v[46:47], v[6:7], v[12:15]\nv_mfma_f32_16x16x16_bf16 v[16:19], v[198:199], v[6:7], v[16:19]\nv_mfma_f32_16x16x16_bf16 v[20:23], v[206:207], v[6:7], v[20:23]\n" \
"s_waitcnt vmcnt(12)\nds_write_b128 v1, v[40:43] offset:12288\nds_write_b128 v1, v[36:39] offset:13312\nds_write_b128 v1, v[32:35] offset:14336\nds_write_b128 v1, v[28:31] offset:15360\ns_barrier\nds_read_b64_tr_b16 v[208:209], v94 offset:20480\nds_read_b64_tr_b16 v[210:211], v94 offset:20992\n" \
"ds_read_b64_tr_b16 v[216:217], v94 offset:20488\nds_read_b64_tr_b16 v[218:219], v94 offset:21000\nds_read_b64_tr_b16 v[28:29], v94 offset:45056\nds_read_b64_tr_b16 v[30:31], v94 offset:45568\nds_read_b64_tr_b16 v[224:225], v94 offset:45064\nds_read_b64_tr_b16 v[226:227], v94 offset:45576\ns_waitcnt lgkmcnt(0)\nv_mfma_f32_16x16x16_bf16 v[8:11], v[208:209], v[6:7], v[8:11]\n" \
"v_mfma_f32_16x16x16_bf16 v[12:15], v[216:217], v[6:7], v[12:15]\nv_mfma_f32_16x16x16_bf16 v[16:19], v[28:29], v[6:7], v[16:19]\nv_mfma_f32_16x16x16_bf16 v[20:23], v[224:225], v[6:7], v[20:23]\nv_mfma_f32_16x16x16_bf16 v[8:11], v[210:211], v[6:7], v[8:11]\nv_mfma_f32_16x16x16_bf16 v[12:15], v[218:219], v[6:7], v[12:15]\nv_mfma_f32_16x16x16_bf16 v[16:19], v[30:31], v[6:7], v[16:19]\nv_mfma_f32_16x16x16_bf16 v[20:23], v[226:227], v[6:7], v[20:23]\ns_waitcnt vmcnt(8)\n" \
"ds_write_b128 v1, v[24:27] offset:16384\nds_write_b128 v1, v[76:79] offset:17408\nds_write_b128 v1, v[60:63] offset:18432\nds_write_b128 v1, v[44:47] offset:19456\ns_barrier\nds_read_b64_tr_b16 v[212:213], v93 offset:0\nds_read_b64_tr_b16 v[214:215], v93 offset:512\nds_read_b64_tr_b16 v[220:221], v93 offset:8\n" \
"ds_read_b64_tr_b16 v[222:223], v93 offset:520\nds_read_b64_tr_b16 v[44:45], v93 offset:12288\nds_read_b64_tr_b16 v[46:47], v93 offset:12800\nds_read_b64_tr_b16 v[228:229], v93 offset:12296\nds_read_b64_tr_b16 v[230:231], v93 offset:12808\ns_waitcnt lgkmcnt(0)\nv_mfma_f32_16x16x16_bf16 v[8:11], v[212:213], v[6:7], v[8:11]\nv_mfma_f32_16x16x16_bf16 v[12:15], v[220:221], v[6:7], v[12:15]\n" \
"v_mfma_f32_16x16x16_bf16 v[16:19], v[44:45], v[6:7], v[16:19]\nv_mfma_f32_16x16x16_bf16 v[20:23], v[228:229], v[6:7], v[20:23]\nv_mfma_f32_16x16x16_bf16 v[8:11], v[214:215], v[6:7], v[8:11]\nv_mfma_f32_16x16x16_bf16 v[12:15], v[222:223], v[6:7], v[12:15]\nv_mfma_f32_16x16x16_bf16 v[16:19], v[46:47], v[6:7], v[16:19]\nv_mfma_f32_16x16x16_bf16 v[20:23], v[230:231], v[6:7], v[20:23]\ns_waitcnt vmcnt(4)\nds_write_b128 v1, v[28:31] offset:20480\n" \
"ds_write_b128 v1, v[44:47] offset:21504\ns_barrier\nds_read_b64_tr_b16 v[232:233], v93 offset:4096\nds_read_b64_tr_b16 v[234:235], v93 offset:4608\nds_read_b64_tr_b16 v[240:241], v93 offset:4104\nds_read_b64_tr_b16 v[242:243], v93 offset:4616\nds_read_b64_tr_b16 v[248:249], v93 offset:16384\nds_read_b64_tr_b16 v[250:251], v93 offset:16896\n" \
"ds_read_b64_tr_b16 v[44:45], v93 offset:16392\nds_read_b64_tr_b16 v[46:47], v93 offset:16904\ns_waitcnt lgkmcnt(0)\nv_mfma_f32_16x16x16_bf16 v[8:11], v[232:233], v[6:7], v[8:11]\nv_mfma_f32_16x16x16_bf16 v[12:15], v[240:241], v[6:7], v[12:15]\nv_mfma_f32_16x16x16_bf16 v[16:19], v[248:249], v[6:7], v[16:19]\nv_mfma_f32_16x16x16_bf16 v[20:23], v[44:45], v[6:7], v[20:23]\nv_mfma_f32_16x16x16_bf16 v[8:11], v[234:235], v[6:7], v[8:11]\n" \
"v_mfma_f32_16x16x16_bf16 v[12:15], v[242:243], v[6:7], v[12:15]\nv_mfma_f32_16x16x16_bf16 v[16:19], v[250:251], v[6:7], v[16:19]\nv_mfma_f32_16x16x16_bf16 v[20:23], v[46:47], v[6:7], v[20:23]\ns_waitcnt vmcnt(0)\nds_write_b128 v1, v[44:47] offset:22528\ns_barrier\nds_read_b64_tr_b16 v[236:237], v93 offset:8192\nds_read_b64_tr_b16 v[238:239], v93 offset:8704\n" \
"ds_read_b64_tr_b16 v[244:245], v93 offset:8200\nds_read_b64_tr_b16 v[246:247], v93 offset:8712\nds_read_b64_tr_b16 v[252:253], v93 offset:20480\nds_read_b64_tr_b16 v[254:255], v93 offset:20992\nds_read_b64_tr_b16 v[44:45], v93 offset:20488\nds_read_b64_tr_b16 v[46:47], v93 offset:21000\ns_waitcnt lgkmcnt(0)\nv_mfma_f32_16x16x16_bf16 v[8:11], v[236:237], v[6:7], v[8:11]\n" \
"v_mfma_f32_16x16x16_bf16 v[12:15], v[244:245], v[6:7], v[12:15]\nv_mfma_f32_16x16x16_bf16 v[16:19], v[252:253], v[6:7], v[16:19]\nv_mfma_f32_16x16x16_bf16 v[20:23], v[44:45], v[6:7], v[20:23]\nv_mfma_f32_16x16x16_bf16 v[8:11], v[238:239], v[6:7], v[8:11]\nv_mfma_f32_16x16x16_bf16 v[12:15], v[246:247], v[6:7], v[12:15]\nv_mfma_f32_16x16x16_bf16 v[16:19], v[254:255], v[6:7], v[16:19]\nv_mfma_f32_16x16x16_bf16 v[20:23], v[46:47], v[6:7], v[20:23]\nds_write_b128 v1, v[44:47] offset:23552\n" \
"s_waitcnt lgkmcnt(0)\ns_barrier\ns_mov_b32 s86, %[xn0]\ns_mov_b32 s87, %[ho0]\ns_mov_b32 s88, %[fh0]\ns_mov_b32 s89, %[b1]\ns_mov_b32 s40, %[b0]\ns_mov_b32 s43, 0\n" \
"s_lshl_b32 s57, %[w], 12\ns_mov_b32 s58, 0xbeeff558\ns_mov_b32 s59, 0x409d30fc\ns_mov_b32 s56, -2147483648\n.p2align 6\n.Lgg_next%=:\ns_cmp_lg_u32 %[w], 0\ns_cbranch_scc1 .Lgg_w1%=\n" \
"buffer_load_dword v56, v3, %[rs], 0 offen sc1\ns_waitcnt vmcnt(0)\ns_barrier\nbuffer_store_dword v90, off, %[rs], s56 sc1\ns_mov_b32 s56, -2147483648\n.Lgg_poll%=:\nv_cmp_gt_i32 vcc, s40, v56\ns_cbranch_vccz .Lgg_go%=\n" \
"s_cmp_ge_u32 s40, 62\ns_cbranch_scc1 .Lgg_ns1055%=\ns_sleep 6\n.Lgg_ns1055%=:\ns_sleep 1\nbuffer_load_dword v56, v3, %[rs], 0 offen sc1\ns_waitcnt vmcnt(0)\ns_add_u32 s43, s43, 1\n" \
"s_cmp_lt_u32 s43, 262144\ns_cbranch_scc1 .Lgg_poll%=\ns_branch .Lgg_abort%=\n.p2align 6\n.Lgg_w1%=:\ns_waitcnt vmcnt(0)\ns_barrier\n.p2align 6\n" \
".Lgg_go%=:\ns_add_u32 s44, s40, 0\ns_lshl_b32 s44, s44, 17\ns_add_u32 s44, s44, s86\ns_lshr_b32 s45, s44, 8\ns_and_b32 s45, s45, 0x2000\ns_xor_b32 s41, s44, s45\ns_add_u32 s61, s41, %[xw]\n" \
"s_barrier\ns_add_u32 m0, s57, 0\ns_add_u32 s60, s61, 0\nbuffer_load_dwordx4 v0, %[rs], s60 offen offset:0 sc1 lds\nbuffer_load_dwordx4 v0, %[rs], s60 offen offset:1024 sc1 lds\nbuffer_load_dwordx4 v0, %[rs], s60 offen offset:2048 sc1 lds\nbuffer_load_dwordx4 v0, %[rs], s60 offen offset:3072 sc1 lds\ns_add_u32 m0, s57, 16384\n" \
"s_add_u32 s60, s61, 32768\nbuffer_load_dwordx4 v0, %[rs], s60 offen offset:0 sc1 lds\nbuffer_load_dwordx4 v0, %[rs], s60 offen offset:1024 sc1 lds\nbuffer_load_dwordx4 v0, %[rs], s60 offen offset:2048 sc1 lds\nbuffer_load_dwordx4 v0, %[rs], s60 offen offset:3072 sc1 lds\nbuffer_load_dwordx4 v[40:43], v5, %[rs], s41 offen offset:0 sc1\nbuffer_load_dwordx4 v[44:47], v5, %[rs], s41 offen offset:256 sc1\ns_lshl_b32 s44, s40, 19\n" \
"s_add_u32 s44, s44, s87\ns_lshr_b32 s45, s44, 8\ns_and_b32 s45, s45, 0x2000\ns_xor_b32 s55, s44, s45\ns_waitcnt vmcnt(6)\ns_barrier\nds_read_b128 v[56:59], v2 offset:0\nds_read_b128 v[60:63], v2 offset:2048\n" \
"ds_read_b128 v[64:67], v2 offset:4096\ns_waitcnt lgkmcnt(2)\nv_mfma_f32_16x16x32_bf16 v[24:27], a[0:3], v[56:59], 0\nds_read_b128 v[68:71], v2 offset:6144\nv_mfma_f32_16x16x32_bf16 v[28:31], a[8:11], v[56:59], 0\nds_read_b128 v[72:75], v1 offset:0\nbuffer_load_dwordx4 v[48:51], v5, %[rs], s41 offen offset:512 sc1\nv_mfma_f32_16x16x32_bf16 v[32:35], a[16:19], v[56:59], 0\n" \
"ds_read_b128 v[76:79], v1 offset:1024\nv_mfma_f32_16x16x32_bf16 v[36:39], a[24:27], v[56:59], 0\ns_waitcnt lgkmcnt(4)\nv_mfma_f32_16x16x32_bf16 v[24:27], a[4:7], v[60:63], v[24:27]\nds_read_b128 v[56:59], v2 offset:8192\nv_mfma_f32_16x16x32_bf16 v[28:31], a[12:15], v[60:63], v[28:31]\nds_read_b128 v[80:83], v1 offset:2048\nbuffer_load_dwordx4 v[52:55], v5, %[rs], s41 offen offset:768 sc1\n" \
"v_mfma_f32_16x16x32_bf16 v[32:35], a[20:23], v[60:63], v[32:35]\nv_mfma_f32_16x16x32_bf16 v[36:39], a[28:31], v[60:63], v[36:39]\ns_waitcnt lgkmcnt(5)\nv_mfma_f32_16x16x32_bf16 v[24:27], a[32:35], v[64:67], v[24:27]\ns_waitcnt lgkmcnt(3)\ns_add_u32 m0, s57, 32768\nds_read_b128 v[60:63], v2 offset:10240\nv_mfma_f32_16x16x32_bf16 v[28:31], v[72:75], v[64:67], v[28:31]\n" \
"s_add_u32 s60, s61, 65536\nds_read_b128 v[72:75], v1 offset:3072\nbuffer_load_dwordx4 v0, %[rs], s60 offen offset:0 sc1 lds\nv_mfma_f32_16x16x32_bf16 v[32:35], a[40:43], v[64:67], v[32:35]\nv_mfma_f32_16x16x32_bf16 v[36:39], a[48:51], v[64:67], v[36:39]\nv_mfma_f32_16x16x32_bf16 v[24:27], a[36:39], v[68:71], v[24:27]\ns_waitcnt lgkmcnt(4)\nds_read_b128 v[64:67], v2 offset:12288\n" \
"v_mfma_f32_16x16x32_bf16 v[28:31], v[76:79], v[68:71], v[28:31]\nbuffer_load_dwordx4 v0, %[rs], s60 offen offset:1024 sc1 lds\nv_mfma_f32_16x16x32_bf16 v[32:35], a[44:47], v[68:71], v[32:35]\nv_mfma_f32_16x16x32_bf16 v[36:39], a[52:55], v[68:71], v[36:39]\ns_waitcnt lgkmcnt(4)\nds_read_b128 v[76:79], v1 offset:4096\nv_mfma_f32_16x16x32_bf16 v[24:27], a[56:59], v[56:59], v[24:27]\nds_read_b128 v[68:71], v2 offset:14336\n" \
"v_mfma_f32_16x16x32_bf16 v[28:31], a[64:67], v[56:59], v[28:31]\ns_waitcnt lgkmcnt(5)\nbuffer_load_dwordx4 v0, %[rs], s60 offen offset:2048 sc1 lds\nv_mfma_f32_16x16x32_bf16 v[32:35], v[80:83], v[56:59], v[32:35]\nv_mfma_f32_16x16x32_bf16 v[36:39], a[72:75], v[56:59], v[36:39]\ns_waitcnt lgkmcnt(4)\nds_read_b128 v[80:83], v1 offset:5120\nv_mfma_f32_16x16x32_bf16 v[24:27], a[60:63], v[60:63], v[24:27]\n" \
"s_waitcnt vmcnt(7)\ns_barrier\nds_read_b128 v[56:59], v2 offset:16384\nv_mfma_f32_16x16x32_bf16 v[28:31], a[68:71], v[60:63], v[28:31]\ns_waitcnt lgkmcnt(5)\nbuffer_load_dwordx4 v0, %[rs], s60 offen offset:3072 sc1 lds\nv_mfma_f32_16x16x32_bf16 v[32:35], v[72:75], v[60:63], v[32:35]\nv_mfma_f32_16x16x32_bf16 v[36:39], a[76:79], v[60:63], v[36:39]\n" \
"s_waitcnt lgkmcnt(4)\nv_mfma_f32_16x16x32_bf16 v[24:27], a[80:83], v[64:67], v[24:27]\ns_add_u32 m0, s57, 49152\nds_read_b128 v[60:63], v2 offset:18432\nv_mfma_f32_16x16x32_bf16 v[28:31], a[88:91], v[64:67], v[28:31]\ns_add_u32 s60, s61, 98304\nbuffer_load_dwordx4 v0, %[rs], s60 offen offset:0 sc1 lds\nv_mfma_f32_16x16x32_bf16 v[32:35], a[96:99], v[64:67], v[32:35]\n" \
"s_waitcnt lgkmcnt(4)\nv_mfma_f32_16x16x32_bf16 v[36:39], v[76:79], v[64:67], v[36:39]\ns_waitcnt lgkmcnt(3)\nv_mfma_f32_16x16x32_bf16 v[24:27], a[84:87], v[68:71], v[24:27]\nds_read_b128 v[64:67], v2 offset:20480\nv_mfma_f32_16x16x32_bf16 v[28:31], a[92:95], v[68:71], v[28:31]\nds_read_b128 v[72:75], v1 offset:6144\nbuffer_load_dwordx4 v0, %[rs], s60 offen offset:1024 sc1 lds\n" \
"v_mfma_f32_16x16x32_bf16 v[32:35], a[100:103], v[68:71], v[32:35]\ns_waitcnt lgkmcnt(4)\nv_mfma_f32_16x16x32_bf16 v[36:39], v[80:83], v[68:71], v[36:39]\ns_waitcnt lgkmcnt(3)\nv_mfma_f32_16x16x32_bf16 v[24:27], a[104:107], v[56:59], v[24:27]\nds_read_b128 v[68:71], v2 offset:22528\nv_mfma_f32_16x16x32_bf16 v[28:31], a[112:115], v[56:59], v[28:31]\nds_read_b128 v[76:79], v1 offset:7168\n" \
"buffer_load_dwordx4 v0, %[rs], s60 offen offset:2048 sc1 lds\nv_mfma_f32_16x16x32_bf16 v[32:35], a[120:123], v[56:59], v[32:35]\nv_mfma_f32_16x16x32_bf16 v[36:39], a[128:131], v[56:59], v[36:39]\ns_waitcnt lgkmcnt(4)\nv_mfma_f32_16x16x32_bf16 v[24:27], a[108:111], v[60:63], v[24:27]\nds_read_b128 v[56:59], v2 offset:24576\nv_mfma_f32_16x16x32_bf16 v[28:31], a[116:119], v[60:63], v[28:31]\nds_read_b128 v[80:83], v1 offset:8192\n" \
"buffer_load_dwordx4 v0, %[rs], s60 offen offset:3072 sc1 lds\nv_mfma_f32_16x16x32_bf16 v[32:35], a[124:127], v[60:63], v[32:35]\nv_mfma_f32_16x16x32_bf16 v[36:39], a[132:135], v[60:63], v[36:39]\ns_waitcnt lgkmcnt(5)\nv_mfma_f32_16x16x32_bf16 v[24:27], a[136:139], v[64:67], v[24:27]\ns_waitcnt vmcnt(8)\ns_waitcnt lgkmcnt(4)\nds_read_b128 v[60:63], v2 offset:26624\n" \
"v_add_f32 v84, v40, v44\nv_mfma_f32_16x16x32_bf16 v[28:31], v[72:75], v[64:67], v[28:31]\nds_read_b128 v[72:75], v1 offset:9216\nv_add_f32 v85, v48, v52\nv_mfma_f32_16x16x32_bf16 v[32:35], a[144:147], v[64:67], v[32:35]\nv_add_f32 v84, v84, v85\nv_mov_b32 v85, v84\nv_mfma_f32_16x16x32_bf16 v[36:39], a[152:155], v[64:67], v[36:39]\n" \
"s_waitcnt lgkmcnt(5)\nv_permlane16_swap_b32 v84, v85\nv_mfma_f32_16x16x32_bf16 v[24:27], a[140:143], v[68:71], v[24:27]\ns_waitcnt lgkmcnt(4)\nds_read_b128 v[64:67], v2 offset:28672\nv_add_f32 v84, v84, v85\nv_mfma_f32_16x16x32_bf16 v[28:31], v[76:79], v[68:71], v[28:31]\nv_mov_b32 v85, v84\n" \
"v_mfma_f32_16x16x32_bf16 v[32:35], a[148:151], v[68:71], v[32:35]\ns_nop 0\nv_permlane32_swap_b32 v84, v85\nv_mfma_f32_16x16x32_bf16 v[36:39], a[156:159], v[68:71], v[36:39]\ns_waitcnt lgkmcnt(4)\nds_read_b128 v[76:79], v1 offset:10240\nv_add_f32 v84, v84, v85\nv_mfma_f32_16x16x32_bf16 v[24:27], a[160:163], v[56:59], v[24:27]\n" \
"ds_read_b128 v[68:71], v2 offset:30720\nv_mul_f32 v84, 0xbd800000, v84\nv_mfma_f32_16x16x32_bf16 v[28:31], a[168:171], v[56:59], v[28:31]\ns_waitcnt lgkmcnt(5)\nv_add_f32 v40, v40, v84\nv_mul_f32 v40, 0x41000000, v40\nv_mfma_f32_16x16x32_bf16 v[32:35], v[80:83], v[56:59], v[32:35]\nv_fma_f32 v41, v40, v40, v41\n" \
"v_add_f32 v44, v44, v84\nv_mfma_f32_16x16x32_bf16 v[36:39], a[176:179], v[56:59], v[36:39]\ns_waitcnt lgkmcnt(4)\nds_read_b128 v[80:83], v1 offset:11264\nv_mul_f32 v44, 0x41000000, v44\nv_mfma_f32_16x16x32_bf16 v[24:27], a[164:167], v[60:63], v[24:27]\ns_waitcnt vmcnt(4)\ns_barrier\n" \
"ds_read_b128 v[56:59], v2 offset:32768\nv_mfma_f32_16x16x32_bf16 v[28:31], a[172:175], v[60:63], v[28:31]\ns_waitcnt lgkmcnt(5)\nv_fma_f32 v45, v44, v44, v45\nv_add_f32 v48, v48, v84\nv_mfma_f32_16x16x32_bf16 v[32:35], v[72:75], v[60:63], v[32:35]\nv_mul_f32 v48, 0x41000000, v48\nv_fma_f32 v49, v48, v48, v49\n" \
"v_mfma_f32_16x16x32_bf16 v[36:39], a[180:183], v[60:63], v[36:39]\ns_waitcnt lgkmcnt(4)\nv_add_f32 v52, v52, v84\nv_mul_f32 v52, 0x41000000, v52\nv_mfma_f32_16x16x32_bf16 v[24:27], a[184:187], v[64:67], v[24:27]\nds_read_b128 v[60:63], v2 offset:34816\nv_fma_f32 v53, v52, v52, v53\nv_mfma_f32_16x16x32_bf16 v[28:31], a[192:195], v[64:67], v[28:31]\n" \
"v_add_f32 v85, v41, v45\nv_add_f32 v86, v49, v53\nv_mfma_f32_16x16x32_bf16 v[32:35], a[200:203], v[64:67], v[32:35]\ns_waitcnt lgkmcnt(4)\nv_add_f32 v85, v85, v86\nv_mov_b32 v86, v85\nv_mfma_f32_16x16x32_bf16 v[36:39], v[76:79], v[64:67], v[36:39]\ns_waitcnt lgkmcnt(3)\n" \
"v_permlane16_swap_b32 v85, v86\nv_mfma_f32_16x16x32_bf16 v[24:27], a[188:191], v[68:71], v[24:27]\nds_read_b128 v[64:67], v2 offset:36864\nv_add_f32 v85, v85, v86\nv_mfma_f32_16x16x32_bf16 v[28:31], a[196:199], v[68:71], v[28:31]\nds_read_b128 v[72:75], v1 offset:12288\nv_mov_b32 v86, v85\nv_mfma_f32_16x16x32_bf16 v[32:35], a[204:207], v[68:71], v[32:35]\n" \
"s_waitcnt lgkmcnt(4)\nv_permlane32_swap_b32 v85, v86\nv_mfma_f32_16x16x32_bf16 v[36:39], v[80:83], v[68:71], v[36:39]\ns_waitcnt lgkmcnt(3)\nv_add_f32 v85, v85, v86\nv_mul_f32 v85, 0x3a800000, v85\nv_mfma_f32_16x16x32_bf16 v[24:27], a[208:211], v[56:59], v[24:27]\nds_read_b128 v[68:71], v2 offset:38912\n" \
"v_cvt_pk_bf16_f32 v85, v85, v85\nv_mfma_f32_16x16x32_bf16 v[28:31], a[216:219], v[56:59], v[28:31]\nds_read_b128 v[76:79], v1 offset:13312\nv_and_b32 v85, 0xffff0000, v85\nv_mfma_f32_16x16x32_bf16 v[32:35], a[224:227], v[56:59], v[32:35]\nv_add_f32 v85, 0x3727c5ac, v85\nv_cvt_pk_bf16_f32 v85, v85, v85\nv_mfma_f32_16x16x32_bf16 v[36:39], a[232:235], v[56:59], v[36:39]\n" \
"s_waitcnt lgkmcnt(4)\nv_and_b32 v85, 0xffff0000, v85\nv_cvt_pk_bf16_f32 v84, v84, v84\nv_mfma_f32_16x16x32_bf16 v[24:27], a[212:215], v[60:63], v[24:27]\nds_read_b128 v[56:59], v2 offset:40960\nv_and_b32 v84, 0xffff0000, v84\nv_mfma_f32_16x16x32_bf16 v[28:31], a[220:223], v[60:63], v[28:31]\nds_read_b128 v[80:83], v1 offset:14336\n" \
"v_mfma_f32_16x16x32_bf16 v[32:35], a[228:231], v[60:63], v[32:35]\nv_sqrt_f32 v85, v85\nv_mfma_f32_16x16x32_bf16 v[36:39], a[236:239], v[60:63], v[36:39]\ns_waitcnt lgkmcnt(5)\nv_cvt_pk_bf16_f32 v85, v85, v85\nv_and_b32 v85, 0xffff0000, v85\nv_mfma_f32_16x16x32_bf16 v[24:27], a[240:243], v[64:67], v[24:27]\ns_waitcnt lgkmcnt(4)\n" \
"ds_read_b128 v[60:63], v2 offset:43008\nv_mul_f32 v86, s58, v85\nv_mfma_f32_16x16x32_bf16 v[28:31], v[72:75], v[64:67], v[28:31]\nds_read_b128 v[72:75], v1 offset:15360\nv_add_f32 v87, v42, v46\nv_mfma_f32_16x16x32_bf16 v[32:35], a[248:251], v[64:67], v[32:35]\nv_rcp_f32 v85, v85\nv_mfma_f32_16x16x32_bf16 v[36:39], v[96:99], v[64:67], v[36:39]\n" \
"s_waitcnt lgkmcnt(5)\nv_mul_f32 v85, s58, v85\nv_add_f32 v88, v50, v54\nv_mfma_f32_16x16x32_bf16 v[24:27], a[244:247], v[68:71], v[24:27]\ns_waitcnt lgkmcnt(4)\nds_read_b128 v[64:67], v2 offset:45056\nv_add_f32 v87, v87, v88\nv_mfma_f32_16x16x32_bf16 v[28:31], v[76:79], v[68:71], v[28:31]\n" \
"v_mov_b32 v88, v87\nv_mfma_f32_16x16x32_bf16 v[32:35], a[252:255], v[68:71], v[32:35]\ns_nop 0\nv_permlane16_swap_b32 v87, v88\nv_mfma_f32_16x16x32_bf16 v[36:39], v[100:103], v[68:71], v[36:39]\ns_waitcnt lgkmcnt(4)\nds_read_b128 v[76:79], v1 offset:16384\nv_add_f32 v87, v87, v88\n" \
"v_mfma_f32_16x16x32_bf16 v[24:27], v[104:107], v[56:59], v[24:27]\nds_read_b128 v[68:71], v2 offset:47104\nv_mov_b32 v88, v87\nv_mfma_f32_16x16x32_bf16 v[28:31], v[112:115], v[56:59], v[28:31]\ns_waitcnt lgkmcnt(5)\nv_permlane32_swap_b32 v87, v88\nv_mfma_f32_16x16x32_bf16 v[32:35], v[80:83], v[56:59], v[32:35]\nv_add_f32 v87, v87, v88\n" \
"v_mul_f32 v87, 0xbd800000, v87\nv_mfma_f32_16x16x32_bf16 v[36:39], v[120:123], v[56:59], v[36:39]\ns_waitcnt lgkmcnt(4)\nds_read_b128 v[80:83], v1 offset:17408\nv_add_f32 v42, v42, v87\nv_mfma_f32_16x16x32_bf16 v[24:27], v[108:111], v[60:63], v[24:27]\ns_waitcnt vmcnt(0)\ns_barrier\n" \
"ds_read_b128 v[56:59], v2 offset:49152\nv_mfma_f32_16x16x32_bf16 v[28:31], v[116:119], v[60:63], v[28:31]\ns_waitcnt lgkmcnt(5)\nv_mul_f32 v42, 0x41000000, v42\nv_fma_f32 v43, v42, v42, v43\nv_mfma_f32_16x16x32_bf16 v[32:35], v[72:75], v[60:63], v[32:35]\nv_add_f32 v46, v46, v87\nv_mul_f32 v46, 0x41000000, v46\n" \
"v_mfma_f32_16x16x32_bf16 v[36:39], v[124:127], v[60:63], v[36:39]\ns_waitcnt lgkmcnt(4)\nv_fma_f32 v47, v46, v46, v47\nv_add_f32 v50, v50, v87\nv_mfma_f32_16x16x32_bf16 v[24:27], v[128:131], v[64:67], v[24:27]\nds_read_b128 v[60:63], v2 offset:51200\nv_mul_f32 v50, 0x41000000, v50\nv_mfma_f32_16x16x32_bf16 v[28:31], v[136:139], v[64:67], v[28:31]\n" \
"v_fma_f32 v51, v50, v50, v51\nv_add_f32 v54, v54, v87\nv_mfma_f32_16x16x32_bf16 v[32:35], v[144:147], v[64:67], v[32:35]\ns_waitcnt lgkmcnt(4)\nv_mul_f32 v54, 0x41000000, v54\nv_fma_f32 v55, v54, v54, v55\nv_mfma_f32_16x16x32_bf16 v[36:39], v[76:79], v[64:67], v[36:39]\ns_waitcnt lgkmcnt(3)\n" \
"v_add_f32 v88, v43, v47\nv_add_f32 v89, v51, v55\nv_mfma_f32_16x16x32_bf16 v[24:27], v[132:135], v[68:71], v[24:27]\nds_read_b128 v[64:67], v2 offset:53248\nv_add_f32 v88, v88, v89\nv_mfma_f32_16x16x32_bf16 v[28:31], v[140:143], v[68:71], v[28:31]\nds_read_b128 v[72:75], v1 offset:18432\nv_mov_b32 v89, v88\n" \
"v_mfma_f32_16x16x32_bf16 v[32:35], v[148:151], v[68:71], v[32:35]\ns_waitcnt lgkmcnt(4)\nv_permlane16_swap_b32 v88, v89\nv_mfma_f32_16x16x32_bf16 v[36:39], v[80:83], v[68:71], v[36:39]\ns_waitcnt lgkmcnt(3)\nv_add_f32 v88, v88, v89\nv_mov_b32 v89, v88\nv_mfma_f32_16x16x32_bf16 v[24:27], v[152:155], v[56:59], v[24:27]\n" \
"ds_read_b128 v[68:71], v2 offset:55296\nv_mfma_f32_16x16x32_bf16 v[28:31], v[160:163], v[56:59], v[28:31]\nds_read_b128 v[76:79], v1 offset:19456\nv_mfma_f32_16x16x32_bf16 v[32:35], v[168:171], v[56:59], v[32:35]\nv_permlane32_swap_b32 v88, v89\nv_mfma_f32_16x16x32_bf16 v[36:39], v[176:179], v[56:59], v[36:39]\ns_waitcnt lgkmcnt(4)\nv_add_f32 v88, v88, v89\n" \
"v_mul_f32 v88, 0x3a800000, v88\nv_mfma_f32_16x16x32_bf16 v[24:27], v[156:159], v[60:63], v[24:27]\nds_read_b128 v[56:59], v2 offset:57344\nv_cvt_pk_bf16_f32 v88, v88, v88\nv_mfma_f32_16x16x32_bf16 v[28:31], v[164:167], v[60:63], v[28:31]\nds_read_b128 v[80:83], v1 offset:20480\nv_and_b32 v88, 0xffff0000, v88\nv_mfma_f32_16x16x32_bf16 v[32:35], v[172:175], v[60:63], v[32:35]\n" \
"v_add_f32 v88, 0x3727c5ac, v88\nv_cvt_pk_bf16_f32 v88, v88, v88\nv_mfma_f32_16x16x32_bf16 v[36:39], v[180:183], v[60:63], v[36:39]\ns_waitcnt lgkmcnt(5)\nv_and_b32 v88, 0xffff0000, v88\nv_cvt_pk_bf16_f32 v87, v87, v87\nv_mfma_f32_16x16x32_bf16 v[24:27], v[184:187], v[64:67], v[24:27]\ns_waitcnt lgkmcnt(4)\n" \
"ds_read_b128 v[60:63], v2 offset:59392\nv_and_b32 v87, 0xffff0000, v87\nv_mfma_f32_16x16x32_bf16 v[28:31], v[72:75], v[64:67], v[28:31]\nds_read_b128 v[72:75], v1 offset:21504\nv_mfma_f32_16x16x32_bf16 v[32:35], v[192:195], v[64:67], v[32:35]\nv_sqrt_f32 v88, v88\nv_mfma_f32_16x16x32_bf16 v[36:39], v[200:203], v[64:67], v[36:39]\ns_waitcnt lgkmcnt(5)\n" \
"v_cvt_pk_bf16_f32 v88, v88, v88\nv_and_b32 v88, 0xffff0000, v88\nv_mfma_f32_16x16x32_bf16 v[24:27], v[188:191], v[68:71], v[24:27]\ns_waitcnt lgkmcnt(4)\nds_read_b128 v[64:67], v2 offset:61440\nv_mul_f32 v89, s58, v88\nv_mfma_f32_16x16x32_bf16 v[28:31], v[76:79], v[68:71], v[28:31]\nv_rcp_f32 v88, v88\n" \
"v_mfma_f32_16x16x32_bf16 v[32:35], v[196:199], v[68:71], v[32:35]\nv_mul_f32 v88, s58, v88\nv_mfma_f32_16x16x32_bf16 v[36:39], v[204:207], v[68:71], v[36:39]\ns_waitcnt lgkmcnt(4)\nds_read_b128 v[76:79], v1 offset:22528\nv_mfma_f32_16x16x32_bf16 v[24:27], v[208:211], v[56:59], v[24:27]\nds_read_b128 v[68:71], v2 offset:63488\nv_mfma_f32_16x16x32_bf16 v[28:31], v[216:219], v[56:59], v[28:31]\n" \
"s_waitcnt lgkmcnt(5)\nv_mfma_f32_16x16x32_bf16 v[32:35], v[80:83], v[56:59], v[32:35]\nv_mfma_f32_16x16x32_bf16 v[36:39], v[224:227], v[56:59], v[36:39]\ns_waitcnt lgkmcnt(4)\nds_read_b128 v[80:83], v1 offset:23552\nv_mfma_f32_16x16x32_bf16 v[24:27], v[212:215], v[60:63], v[24:27]\nds_read_b128 v[56:59], v2 offset:256\nv_mfma_f32_16x16x32_bf16 v[28:31], v[220:223], v[60:63], v[28:31]\n" \
"s_waitcnt lgkmcnt(5)\nv_mfma_f32_16x16x32_bf16 v[32:35], v[72:75], v[60:63], v[32:35]\nv_mfma_f32_16x16x32_bf16 v[36:39], v[228:231], v[60:63], v[36:39]\ns_waitcnt lgkmcnt(4)\nv_mfma_f32_16x16x32_bf16 v[24:27], v[232:235], v[64:67], v[24:27]\nds_read_b128 v[60:63], v2 offset:2304\nv_mfma_f32_16x16x32_bf16 v[28:31], v[240:243], v[64:67], v[28:31]\nv_mfma_f32_16x16x32_bf16 v[32:35], v[248:251], v[64:67], v[32:35]\n" \
"s_waitcnt lgkmcnt(4)\nv_mfma_f32_16x16x32_bf16 v[36:39], v[76:79], v[64:67], v[36:39]\ns_waitcnt lgkmcnt(3)\nv_mfma_f32_16x16x32_bf16 v[24:27], v[236:239], v[68:71], v[24:27]\nds_read_b128 v[64:67], v2 offset:4352\nv_mfma_f32_16x16x32_bf16 v[28:31], v[244:247], v[68:71], v[28:31]\nds_read_b128 v[72:75], v1 offset:0\nv_mfma_f32_16x16x32_bf16 v[32:35], v[252:255], v[68:71], v[32:35]\n" \
"s_waitcnt lgkmcnt(4)\nv_mfma_f32_16x16x32_bf16 v[36:39], v[80:83], v[68:71], v[36:39]\ns_waitcnt lgkmcnt(3)\nv_mfma_f32_16x16x32_bf16 v[40:43], a[0:3], v[56:59], 0\nds_read_b128 v[68:71], v2 offset:6400\nv_mfma_f32_16x16x32_bf16 v[44:47], a[8:11], v[56:59], 0\ns_lshl_b32 s44, s40, 1\nds_read_b128 v[76:79], v1 offset:1024\n" \
"v_mov_b32 v90, s44\nv_mfma_f32_16x16x32_bf16 v[48:51], a[16:19], v[56:59], 0\nv_mfma_f32_16x16x32_bf16 v[52:55], a[24:27], v[56:59], 0\ns_waitcnt lgkmcnt(4)\nv_fma_f32 v24, v8, v84, v24\nv_fma_f32 v25, v9, v84, v25\nv_mfma_f32_16x16x32_bf16 v[40:43], a[4:7], v[60:63], v[40:43]\nds_read_b128 v[56:59], v2 offset:8448\n" \
"v_fma_f32 v32, v16, v84, v32\nv_mfma_f32_16x16x32_bf16 v[44:47], a[12:15], v[60:63], v[44:47]\nds_read_b128 v[80:83], v1 offset:2048\nv_fma_f32 v33, v17, v84, v33\nv_mfma_f32_16x16x32_bf16 v[48:51], a[20:23], v[60:63], v[48:51]\nv_mul_f32 v24, v24, v85\nv_mul_f32 v25, v25, v85\nv_mfma_f32_16x16x32_bf16 v[52:55], a[28:31], v[60:63], v[52:55]\n" \
"s_waitcnt lgkmcnt(5)\nv_fma_f32 v91, v24, v24, s59\nv_fma_f32 v92, v25, v25, s59\nv_mfma_f32_16x16x32_bf16 v[40:43], a[32:35], v[64:67], v[40:43]\ns_waitcnt lgkmcnt(4)\nds_read_b128 v[60:63], v2 offset:10496\nv_mul_f32 v91, v91, v24\nv_mfma_f32_16x16x32_bf16 v[44:47], v[72:75], v[64:67], v[44:47]\n" \
"ds_read_b128 v[72:75], v1 offset:3072\nv_mul_f32 v92, v92, v25\nv_mfma_f32_16x16x32_bf16 v[48:51], a[40:43], v[64:67], v[48:51]\nv_exp_f32 v91, v91\nv_mfma_f32_16x16x32_bf16 v[52:55], a[48:51], v[64:67], v[52:55]\ns_waitcnt lgkmcnt(5)\nv_exp_f32 v92, v92\nv_mfma_f32_16x16x32_bf16 v[40:43], a[36:39], v[68:71], v[40:43]\n" \
"s_waitcnt lgkmcnt(4)\nds_read_b128 v[64:67], v2 offset:12544\nv_mul_f32 v24, v24, v32\nv_mfma_f32_16x16x32_bf16 v[44:47], v[76:79], v[68:71], v[44:47]\nv_mul_f32 v25, v25, v33\nv_fma_f32 v91, v91, v86, v86\nv_mfma_f32_16x16x32_bf16 v[48:51], a[44:47], v[68:71], v[48:51]\nv_fma_f32 v92, v92, v86, v86\n" \
"v_fma_f32 v26, v10, v84, v26\nv_mfma_f32_16x16x32_bf16 v[52:55], a[52:55], v[68:71], v[52:55]\ns_waitcnt lgkmcnt(4)\nds_read_b128 v[76:79], v1 offset:4096\nv_fma_f32 v27, v11, v84, v27\nv_mfma_f32_16x16x32_bf16 v[40:43], a[56:59], v[56:59], v[40:43]\nds_read_b128 v[68:71], v2 offset:14592\nv_fma_f32 v34, v18, v84, v34\n" \
"v_mfma_f32_16x16x32_bf16 v[44:47], a[64:67], v[56:59], v[44:47]\ns_waitcnt lgkmcnt(5)\nv_rcp_f32 v91, v91\nv_mfma_f32_16x16x32_bf16 v[48:51], v[80:83], v[56:59], v[48:51]\nv_rcp_f32 v92, v92\nv_mfma_f32_16x16x32_bf16 v[52:55], a[72:75], v[56:59], v[52:55]\ns_waitcnt lgkmcnt(4)\nds_read_b128 v[80:83], v1 offset:5120\n" \
"v_mul_f32 v24, v24, v91\nv_mfma_f32_16x16x32_bf16 v[40:43], a[60:63], v[60:63], v[40:43]\nds_read_b128 v[56:59], v2 offset:16640\nv_mul_f32 v25, v25, v92\nv_mfma_f32_16x16x32_bf16 v[44:47], a[68:71], v[60:63], v[44:47]\ns_waitcnt lgkmcnt(5)\nv_cvt_pk_bf16_f32 v32, v24, v25\nv_fma_f32 v35, v19, v84, v35\n" \
"v_mfma_f32_16x16x32_bf16 v[48:51], v[72:75], v[60:63], v[48:51]\nv_mul_f32 v26, v26, v85\nv_mul_f32 v27, v27, v85\nv_mfma_f32_16x16x32_bf16 v[52:55], a[76:79], v[60:63], v[52:55]\ns_waitcnt lgkmcnt(4)\nv_fma_f32 v91, v26, v26, s59\nv_fma_f32 v92, v27, v27, s59\nv_mfma_f32_16x16x32_bf16 v[40:43], a[80:83], v[64:67], v[40:43]\n" \
"ds_read_b128 v[60:63], v2 offset:18688\nv_mul_f32 v91, v91, v26\nv_mfma_f32_16x16x32_bf16 v[44:47], a[88:91], v[64:67], v[44:47]\nv_mul_f32 v92, v92, v27\nv_mul_f32 v26, v26, v34\nv_mfma_f32_16x16x32_bf16 v[48:51], a[96:99], v[64:67], v[48:51]\ns_waitcnt lgkmcnt(4)\nv_exp_f32 v91, v91\n" \
"v_mfma_f32_16x16x32_bf16 v[52:55], v[76:79], v[64:67], v[52:55]\ns_waitcnt lgkmcnt(3)\nv_exp_f32 v92, v92\nv_mfma_f32_16x16x32_bf16 v[40:43], a[84:87], v[68:71], v[40:43]\nds_read_b128 v[64:67], v2 offset:20736\nv_mul_f32 v27, v27, v35\nv_mfma_f32_16x16x32_bf16 v[44:47], a[92:95], v[68:71], v[44:47]\nds_read_b128 v[72:75], v1 offset:6144\n" \
"v_fma_f32 v91, v91, v86, v86\nv_mfma_f32_16x16x32_bf16 v[48:51], a[100:103], v[68:71], v[48:51]\ns_waitcnt lgkmcnt(4)\nv_fma_f32 v92, v92, v86, v86\nv_fma_f32 v28, v12, v84, v28\nv_mfma_f32_16x16x32_bf16 v[52:55], v[80:83], v[68:71], v[52:55]\ns_waitcnt lgkmcnt(3)\nv_rcp_f32 v91, v91\n" \
"v_mfma_f32_16x16x32_bf16 v[40:43], a[104:107], v[56:59], v[40:43]\nds_read_b128 v[68:71], v2 offset:22784\nv_mul_f32 v26, v26, v91\nv_mfma_f32_16x16x32_bf16 v[44:47], a[112:115], v[56:59], v[44:47]\nds_read_b128 v[76:79], v1 offset:7168\nv_fma_f32 v29, v13, v84, v29\nv_mfma_f32_16x16x32_bf16 v[48:51], a[120:123], v[56:59], v[48:51]\nv_rcp_f32 v92, v92\n" \
"v_mfma_f32_16x16x32_bf16 v[52:55], a[128:131], v[56:59], v[52:55]\ns_waitcnt lgkmcnt(4)\nv_mul_f32 v27, v27, v92\nv_cvt_pk_bf16_f32 v33, v26, v27\nv_mfma_f32_16x16x32_bf16 v[40:43], a[108:111], v[60:63], v[40:43]\nds_read_b128 v[56:59], v2 offset:24832\nv_fma_f32 v36, v20, v84, v36\nv_mfma_f32_16x16x32_bf16 v[44:47], a[116:119], v[60:63], v[44:47]\n" \
"ds_read_b128 v[80:83], v1 offset:8192\nv_fma_f32 v37, v21, v84, v37\nv_mfma_f32_16x16x32_bf16 v[48:51], a[124:127], v[60:63], v[48:51]\nv_mul_f32 v28, v28, v85\nv_mul_f32 v29, v29, v85\nv_mfma_f32_16x16x32_bf16 v[52:55], a[132:135], v[60:63], v[52:55]\ns_waitcnt lgkmcnt(5)\nv_fma_f32 v91, v28, v28, s59\n" \
"v_fma_f32 v92, v29, v29, s59\nv_mfma_f32_16x16x32_bf16 v[40:43], a[136:139], v[64:67], v[40:43]\ns_waitcnt lgkmcnt(4)\nds_read_b128 v[60:63], v2 offset:26880\nv_mul_f32 v91, v91, v28\nv_mfma_f32_16x16x32_bf16 v[44:47], v[72:75], v[64:67], v[44:47]\nds_read_b128 v[72:75], v1 offset:9216\nv_mul_f32 v92, v92, v29\n" \
"v_mfma_f32_16x16x32_bf16 v[48:51], a[144:147], v[64:67], v[48:51]\nv_exp_f32 v91, v91\nv_mfma_f32_16x16x32_bf16 v[52:55], a[152:155], v[64:67], v[52:55]\ns_waitcnt lgkmcnt(5)\nv_exp_f32 v92, v92\nv_mfma_f32_16x16x32_bf16 v[40:43], a[140:143], v[68:71], v[40:43]\ns_waitcnt lgkmcnt(4)\nds_read_b128 v[64:67], v2 offset:28928\n" \
"v_mul_f32 v28, v28, v36\nv_mfma_f32_16x16x32_bf16 v[44:47], v[76:79], v[68:71], v[44:47]\nv_mul_f32 v29, v29, v37\nv_fma_f32 v91, v91, v86, v86\nv_mfma_f32_16x16x32_bf16 v[48:51], a[148:151], v[68:71], v[48:51]\nv_fma_f32 v92, v92, v86, v86\nv_fma_f32 v30, v14, v84, v30\nv_mfma_f32_16x16x32_bf16 v[52:55], a[156:159], v[68:71], v[52:55]\n" \
"s_waitcnt lgkmcnt(4)\nds_read_b128 v[76:79], v1 offset:10240\nv_fma_f32 v31, v15, v84, v31\nv_mfma_f32_16x16x32_bf16 v[40:43], a[160:163], v[56:59], v[40:43]\nds_read_b128 v[68:71], v2 offset:30976\nv_fma_f32 v38, v22, v84, v38\nv_mfma_f32_16x16x32_bf16 v[44:47], a[168:171], v[56:59], v[44:47]\ns_waitcnt lgkmcnt(5)\n" \
"v_rcp_f32 v91, v91\nv_mfma_f32_16x16x32_bf16 v[48:51], v[80:83], v[56:59], v[48:51]\nv_rcp_f32 v92, v92\nv_mfma_f32_16x16x32_bf16 v[52:55], a[176:179], v[56:59], v[52:55]\ns_waitcnt lgkmcnt(4)\nds_read_b128 v[80:83], v1 offset:11264\nv_mul_f32 v28, v28, v91\nv_mfma_f32_16x16x32_bf16 v[40:43], a[164:167], v[60:63], v[40:43]\n" \
"ds_read_b128 v[56:59], v2 offset:33024\nv_mul_f32 v29, v29, v92\nv_mfma_f32_16x16x32_bf16 v[44:47], a[172:175], v[60:63], v[44:47]\ns_waitcnt lgkmcnt(5)\nv_cvt_pk_bf16_f32 v34, v28, v29\nv_fma_f32 v39, v23, v84, v39\nv_mfma_f32_16x16x32_bf16 v[48:51], v[72:75], v[60:63], v[48:51]\nv_mul_f32 v30, v30, v85\n" \
"v_mul_f32 v31, v31, v85\nv_mfma_f32_16x16x32_bf16 v[52:55], a[180:183], v[60:63], v[52:55]\ns_waitcnt lgkmcnt(4)\nv_fma_f32 v91, v30, v30, s59\nv_fma_f32 v92, v31, v31, s59\nv_mfma_f32_16x16x32_bf16 v[40:43], a[184:187], v[64:67], v[40:43]\nds_read_b128 v[60:63], v2 offset:35072\nv_mul_f32 v91, v91, v30\n" \
"v_mfma_f32_16x16x32_bf16 v[44:47], a[192:195], v[64:67], v[44:47]\nv_mul_f32 v92, v92, v31\nv_mul_f32 v30, v30, v38\nv_mfma_f32_16x16x32_bf16 v[48:51], a[200:203], v[64:67], v[48:51]\ns_waitcnt lgkmcnt(4)\nv_exp_f32 v91, v91\nv_mfma_f32_16x16x32_bf16 v[52:55], v[76:79], v[64:67], v[52:55]\ns_waitcnt lgkmcnt(3)\n" \
"v_exp_f32 v92, v92\nv_mfma_f32_16x16x32_bf16 v[40:43], a[188:191], v[68:71], v[40:43]\nds_read_b128 v[64:67], v2 offset:37120\nv_mul_f32 v31, v31, v39\nv_mfma_f32_16x16x32_bf16 v[44:47], a[196:199], v[68:71], v[44:47]\nds_read_b128 v[72:75], v1 offset:12288\nv_fma_f32 v91, v91, v86, v86\nv_mfma_f32_16x16x32_bf16 v[48:51], a[204:207], v[68:71], v[48:51]\n" \
"s_waitcnt lgkmcnt(4)\nv_fma_f32 v92, v92, v86, v86\nv_mfma_f32_16x16x32_bf16 v[52:55], v[80:83], v[68:71], v[52:55]\ns_waitcnt lgkmcnt(3)\nv_rcp_f32 v91, v91\nv_mfma_f32_16x16x32_bf16 v[40:43], a[208:211], v[56:59], v[40:43]\nds_read_b128 v[68:71], v2 offset:39168\nv_mul_f32 v30, v30, v91\n" \
"v_mfma_f32_16x16x32_bf16 v[44:47], a[216:219], v[56:59], v[44:47]\nds_read_b128 v[76:79], v1 offset:13312\nv_mfma_f32_16x16x32_bf16 v[48:51], a[224:227], v[56:59], v[48:51]\nv_rcp_f32 v92, v92\nv_mfma_f32_16x16x32_bf16 v[52:55], a[232:235], v[56:59], v[52:55]\ns_waitcnt lgkmcnt(4)\nv_mul_f32 v31, v31, v92\nv_cvt_pk_bf16_f32 v35, v30, v31\n" \
"v_mfma_f32_16x16x32_bf16 v[40:43], a[212:215], v[60:63], v[40:43]\nds_read_b128 v[56:59], v2 offset:41216\nv_mfma_f32_16x16x32_bf16 v[44:47], a[220:223], v[60:63], v[44:47]\nds_read_b128 v[80:83], v1 offset:14336\nv_mfma_f32_16x16x32_bf16 v[48:51], a[228:231], v[60:63], v[48:51]\nbuffer_store_dwordx4 v[32:35], v4, %[rs], s55 offen offset:0 sc1\nv_mfma_f32_16x16x32_bf16 v[52:55], a[236:239], v[60:63], v[52:55]\ns_waitcnt lgkmcnt(5)\n" \
"v_mfma_f32_16x16x32_bf16 v[40:43], a[240:243], v[64:67], v[40:43]\ns_waitcnt lgkmcnt(4)\nds_read_b128 v[60:63], v2 offset:43264\nv_mfma_f32_16x16x32_bf16 v[44:47], v[72:75], v[64:67], v[44:47]\nds_read_b128 v[72:75], v1 offset:15360\nv_mfma_f32_16x16x32_bf16 v[48:51], a[248:251], v[64:67], v[48:51]\nv_mfma_f32_16x16x32_bf16 v[52:55], v[96:99], v[64:67], v[52:55]\ns_waitcnt lgkmcnt(5)\n" \
"v_mfma_f32_16x16x32_bf16 v[40:43], a[244:247], v[68:71], v[40:43]\ns_waitcnt lgkmcnt(4)\nds_read_b128 v[64:67], v2 offset:45312\nv_mfma_f32_16x16x32_bf16 v[44:47], v[76:79], v[68:71], v[44:47]\nds_read_b128 v[24:27], v2 offset:47360\nv_mfma_f32_16x16x32_bf16 v[48:51], a[252:255], v[68:71], v[48:51]\nds_read_b128 v[28:31], v2 offset:49408\nv_mfma_f32_16x16x32_bf16 v[52:55], v[100:103], v[68:71], v[52:55]\n" \
"s_waitcnt lgkmcnt(6)\nv_mfma_f32_16x16x32_bf16 v[40:43], v[104:107], v[56:59], v[40:43]\ns_waitcnt lgkmcnt(5)\nds_read_b128 v[68:71], v2 offset:51456\nv_mfma_f32_16x16x32_bf16 v[48:51], v[80:83], v[56:59], v[48:51]\ns_waitcnt lgkmcnt(5)\nv_mfma_f32_16x16x32_bf16 v[40:43], v[108:111], v[60:63], v[40:43]\ns_waitcnt lgkmcnt(4)\n" \
"ds_read_b128 v[56:59], v2 offset:53504\nv_mfma_f32_16x16x32_bf16 v[48:51], v[72:75], v[60:63], v[48:51]\ns_waitcnt lgkmcnt(4)\nv_mfma_f32_16x16x32_bf16 v[40:43], v[128:131], v[64:67], v[40:43]\nds_read_b128 v[60:63], v2 offset:55552\nv_mfma_f32_16x16x32_bf16 v[48:51], v[144:147], v[64:67], v[48:51]\ns_waitcnt lgkmcnt(4)\nds_read_b128 v[76:79], v1 offset:20480\n" \
"v_mfma_f32_16x16x32_bf16 v[40:43], v[132:135], v[24:27], v[40:43]\nds_read_b128 v[64:67], v2 offset:57600\nv_mfma_f32_16x16x32_bf16 v[48:51], v[148:151], v[24:27], v[48:51]\ns_waitcnt lgkmcnt(5)\nds_read_b128 v[80:83], v1 offset:21504\nv_mfma_f32_16x16x32_bf16 v[40:43], v[152:155], v[28:31], v[40:43]\nds_read_b128 v[24:27], v2 offset:59648\nv_mfma_f32_16x16x32_bf16 v[48:51], v[168:171], v[28:31], v[48:51]\n" \
"s_waitcnt lgkmcnt(6)\nv_mfma_f32_16x16x32_bf16 v[40:43], v[156:159], v[68:71], v[40:43]\nds_read_b128 v[28:31], v2 offset:61696\nv_mfma_f32_16x16x32_bf16 v[48:51], v[172:175], v[68:71], v[48:51]\ns_waitcnt lgkmcnt(6)\nv_mfma_f32_16x16x32_bf16 v[40:43], v[184:187], v[56:59], v[40:43]\nds_read_b128 v[68:71], v2 offset:63744\nv_mfma_f32_16x16x32_bf16 v[48:51], v[192:195], v[56:59], v[48:51]\n" \
"s_waitcnt lgkmcnt(6)\nv_mfma_f32_16x16x32_bf16 v[40:43], v[188:191], v[60:63], v[40:43]\nds_read_b128 v[56:59], v2 offset:41216\nv_mfma_f32_16x16x32_bf16 v[48:51], v[196:199], v[60:63], v[48:51]\ns_waitcnt lgkmcnt(5)\nv_mfma_f32_16x16x32_bf16 v[40:43], v[208:211], v[64:67], v[40:43]\nds_read_b128 v[60:63], v2 offset:43264\nv_mfma_f32_16x16x32_bf16 v[48:51], v[76:79], v[64:67], v[48:51]\n" \
"s_waitcnt lgkmcnt(4)\nds_read_b128 v[72:75], v1 offset:16384\nv_mfma_f32_16x16x32_bf16 v[40:43], v[212:215], v[24:27], v[40:43]\nds_read_b128 v[64:67], v2 offset:45312\nv_mfma_f32_16x16x32_bf16 v[48:51], v[80:83], v[24:27], v[48:51]\ns_waitcnt lgkmcnt(5)\nds_read_b128 v[76:79], v1 offset:17408\nv_mfma_f32_16x16x32_bf16 v[40:43], v[232:235], v[28:31], v[40:43]\n" \
"ds_read_b128 v[24:27], v2 offset:47360\nv_mfma_f32_16x16x32_bf16 v[48:51], v[248:251], v[28:31], v[48:51]\ns_waitcnt vmcnt(0)\ns_barrier\ns_waitcnt lgkmcnt(6)\nbuffer_store_dword v90, off, %[rs], s88 sc1\nv_mfma_f32_16x16x32_bf16 v[40:43], v[236:239], v[68:71], v[40:43]\nds_read_b128 v[28:31], v2 offset:49408\n" \
"v_mfma_f32_16x16x32_bf16 v[48:51], v[252:255], v[68:71], v[48:51]\ns_waitcnt lgkmcnt(6)\nds_read_b128 v[80:83], v1 offset:18432\nv_mfma_f32_16x16x32_bf16 v[44:47], v[112:115], v[56:59], v[44:47]\nds_read_b128 v[68:71], v2 offset:51456\nv_mfma_f32_16x16x32_bf16 v[52:55], v[120:123], v[56:59], v[52:55]\ns_waitcnt lgkmcnt(7)\nv_fma_f32 v40, v8, v87, v40\n" \
"v_fma_f32 v41, v9, v87, v41\nv_mfma_f32_16x16x32_bf16 v[44:47], v[116:119], v[60:63], v[44:47]\nds_read_b128 v[56:59], v2 offset:53504\nv_fma_f32 v48, v16, v87, v48\nv_mfma_f32_16x16x32_bf16 v[52:55], v[124:127], v[60:63], v[52:55]\ns_waitcnt lgkmcnt(6)\nv_fma_f32 v49, v17, v87, v49\nv_mul_f32 v40, v40, v88\n" \
"v_mfma_f32_16x16x32_bf16 v[44:47], v[136:139], v[64:67], v[44:47]\nds_read_b128 v[60:63], v2 offset:55552\nv_mul_f32 v41, v41, v88\nv_mfma_f32_16x16x32_bf16 v[52:55], v[72:75], v[64:67], v[52:55]\ns_waitcnt lgkmcnt(5)\nds_read_b128 v[72:75], v1 offset:19456\nv_fma_f32 v91, v40, v40, s59\nv_mfma_f32_16x16x32_bf16 v[44:47], v[140:143], v[24:27], v[44:47]\n" \
"ds_read_b128 v[64:67], v2 offset:57600\nv_fma_f32 v92, v41, v41, s59\nv_mfma_f32_16x16x32_bf16 v[52:55], v[76:79], v[24:27], v[52:55]\ns_waitcnt lgkmcnt(6)\nv_mul_f32 v91, v91, v40\nv_mul_f32 v92, v92, v41\nv_mfma_f32_16x16x32_bf16 v[44:47], v[160:163], v[28:31], v[44:47]\nds_read_b128 v[24:27], v2 offset:59648\n" \
"v_mul_f32 v40, v40, v48\nv_mfma_f32_16x16x32_bf16 v[52:55], v[176:179], v[28:31], v[52:55]\ns_waitcnt lgkmcnt(5)\nds_read_b128 v[76:79], v1 offset:22528\nv_mul_f32 v41, v41, v49\nv_mfma_f32_16x16x32_bf16 v[44:47], v[164:167], v[68:71], v[44:47]\nds_read_b128 v[28:31], v2 offset:61696\nv_mfma_f32_16x16x32_bf16 v[52:55], v[180:183], v[68:71], v[52:55]\n" \
"s_waitcnt lgkmcnt(6)\nv_exp_f32 v91, v91\nv_mfma_f32_16x16x32_bf16 v[44:47], v[80:83], v[56:59], v[44:47]\nds_read_b128 v[68:71], v2 offset:63744\nds_read_b128 v[80:83], v1 offset:23552\nv_mfma_f32_16x16x32_bf16 v[52:55], v[200:203], v[56:59], v[52:55]\ns_waitcnt lgkmcnt(6)\nv_exp_f32 v92, v92\n" \
"v_mfma_f32_16x16x32_bf16 v[44:47], v[72:75], v[60:63], v[44:47]\nv_fma_f32 v91, v91, v89, v89\nv_fma_f32 v92, v92, v89, v89\nv_mfma_f32_16x16x32_bf16 v[52:55], v[204:207], v[60:63], v[52:55]\ns_waitcnt lgkmcnt(5)\nv_rcp_f32 v91, v91\nv_mfma_f32_16x16x32_bf16 v[44:47], v[216:219], v[64:67], v[44:47]\nv_rcp_f32 v92, v92\n" \
"v_mfma_f32_16x16x32_bf16 v[52:55], v[224:227], v[64:67], v[52:55]\ns_waitcnt lgkmcnt(4)\nv_mul_f32 v40, v40, v91\nv_mul_f32 v41, v41, v92\nv_mfma_f32_16x16x32_bf16 v[44:47], v[220:223], v[24:27], v[44:47]\nv_cvt_pk_bf16_f32 v48, v40, v41\nv_fma_f32 v42, v10, v87, v42\nv_mfma_f32_16x16x32_bf16 v[52:55], v[228:231], v[24:27], v[52:55]\n" \
"s_waitcnt lgkmcnt(2)\nv_fma_f32 v43, v11, v87, v43\nv_fma_f32 v50, v18, v87, v50\nv_mfma_f32_16x16x32_bf16 v[44:47], v[240:243], v[28:31], v[44:47]\nv_fma_f32 v51, v19, v87, v51\nv_mul_f32 v42, v42, v88\nv_mfma_f32_16x16x32_bf16 v[52:55], v[76:79], v[28:31], v[52:55]\ns_waitcnt lgkmcnt(1)\n" \
"v_mul_f32 v43, v43, v88\nv_fma_f32 v91, v42, v42, s59\nv_mfma_f32_16x16x32_bf16 v[44:47], v[244:247], v[68:71], v[44:47]\ns_waitcnt lgkmcnt(0)\nv_fma_f32 v92, v43, v43, s59\nv_mul_f32 v91, v91, v42\nv_mfma_f32_16x16x32_bf16 v[52:55], v[80:83], v[68:71], v[52:55]\nv_mul_f32 v92, v92, v43\n" \
"v_mul_f32 v42, v42, v50\nv_exp_f32 v91, v91\nv_exp_f32 v92, v92\nv_mul_f32 v43, v43, v51\nv_fma_f32 v91, v91, v89, v89\nv_fma_f32 v92, v92, v89, v89\nv_rcp_f32 v91, v91\nv_rcp_f32 v92, v92\n" \
"v_mul_f32 v42, v42, v91\nv_mul_f32 v43, v43, v92\nv_cvt_pk_bf16_f32 v49, v42, v43\nv_fma_f32 v44, v12, v87, v44\nv_fma_f32 v45, v13, v87, v45\nv_fma_f32 v52, v20, v87, v52\nv_fma_f32 v53, v21, v87, v53\nv_mul_f32 v44, v44, v88\n" \
"v_mul_f32 v45, v45, v88\nv_fma_f32 v91, v44, v44, s59\nv_fma_f32 v92, v45, v45, s59\nv_mul_f32 v91, v91, v44\nv_mul_f32 v92, v92, v45\nv_exp_f32 v91, v91\nv_exp_f32 v92, v92\nv_mul_f32 v44, v44, v52\n" \
"v_mul_f32 v45, v45, v53\nv_fma_f32 v91, v91, v89, v89\nv_fma_f32 v92, v92, v89, v89\nv_rcp_f32 v91, v91\nv_rcp_f32 v92, v92\nv_mul_f32 v44, v44, v91\nv_mul_f32 v45, v45, v92\nv_cvt_pk_bf16_f32 v50, v44, v45\n" \
"v_fma_f32 v46, v14, v87, v46\nv_fma_f32 v47, v15, v87, v47\nv_fma_f32 v54, v22, v87, v54\nv_fma_f32 v55, v23, v87, v55\nv_mul_f32 v46, v46, v88\nv_mul_f32 v47, v47, v88\nv_fma_f32 v91, v46, v46, s59\nv_fma_f32 v92, v47, v47, s59\n" \
"v_mul_f32 v91, v91, v46\nv_mul_f32 v92, v92, v47\nv_exp_f32 v91, v91\nv_exp_f32 v92, v92\nv_mul_f32 v46, v46, v54\nv_mul_f32 v47, v47, v55\nv_fma_f32 v91, v91, v89, v89\nv_fma_f32 v92, v92, v89, v89\n" \
"v_rcp_f32 v91, v91\nv_rcp_f32 v92, v92\nv_mul_f32 v46, v46, v91\nv_mul_f32 v47, v47, v92\nv_cvt_pk_bf16_f32 v51, v46, v47\nbuffer_store_dwordx4 v[48:51], v4, %[rs], s55 offen offset:1024 sc1\nv_add_u32 v90, 1, v90\ns_mov_b32 s56, s88\n" \
"s_add_u32 s40, s40, 1\ns_cmp_lt_u32 s40, s89\ns_cbranch_scc1 .Lgg_next%=\ns_cmp_eq_u32 s89, 64\ns_cbranch_scc1 .Lgg_fin%=\ns_mov_b32 s89, 64\ns_mov_b32 s40, 0\ns_xor_b32 s86, s86, 0x2000\n" \
"s_xor_b32 s87, s87, 0x2000\ns_mov_b32 s88, %[fh1]\nv_xor_b32 v3, 0x2000, v3\ns_branch .Lgg_next%=\n.Lgg_fin%=:\ns_waitcnt vmcnt(0)\ns_barrier\nbuffer_store_dword v90, off, %[rs], s56 sc1\n" \
"s_branch .Lgg_end%=\n.Lgg_abort%=:\ns_mov_b64 exec, -1\nv_mov_b32 v16, 0\ns_movk_i32 s44, 6400\nbuffer_store_dword v16, off, %[rs], s44 sc1\n.Lgg_end%=:\ns_waitcnt vmcnt(0) lgkmcnt(0)\n" \
""
#define DD_ASM \
"v_mbcnt_lo_u32_b32 v255, -1, 0\nv_mbcnt_hi_u32_b32 v255, -1, v255\nv_lshlrev_b32 v255, 2, v255\nv_add_u32 v255, %[tb], v255\nds_read_b32 v0, v255 offset:0\nds_read_b32 v1, v255 offset:256\nds_read_b32 v2, v255 offset:512\nds_read_b32 v3, v255 offset:768\n" \
"ds_read_b32 v4, v255 offset:1024\nds_read_b32 v5, v255 offset:1280\nds_read_b32 v6, v255 offset:1536\nds_read_b32 v7, v255 offset:1792\nds_read_b32 v125, v255 offset:2048\nds_read_b32 v126, v255 offset:2304\nds_read_b32 v127, v255 offset:2560\ns_waitcnt lgkmcnt(0)\n" \
"s_add_u32 m0, %[tb], 8192\ns_add_u32 s44, %[wo], 0\nbuffer_load_dwordx4 v125, %[rw], s44 offen nt lds\ns_add_u32 m0, %[tb], 9216\ns_add_u32 s44, %[wo], 16384\nbuffer_load_dwordx4 v125, %[rw], s44 offen nt lds\ns_add_u32 m0, %[tb], 10240\ns_add_u32 s44, %[wo], 32768\n" \
"buffer_load_dwordx4 v125, %[rw], s44 offen nt lds\ns_add_u32 m0, %[tb], 11264\ns_add_u32 s44, %[wo], 49152\nbuffer_load_dwordx4 v125, %[rw], s44 offen nt lds\ns_add_u32 m0, %[lw], 0\ns_add_u32 s44, %[wo], 65536\nbuffer_load_dwordx4 v125, %[rw], s44 offen nt lds\ns_add_u32 m0, %[lw], 1024\n" \
"s_add_u32 s44, %[wo], 81920\nbuffer_load_dwordx4 v125, %[rw], s44 offen nt lds\ns_add_u32 m0, %[lw], 2048\ns_add_u32 s44, %[wo], 98304\nbuffer_load_dwordx4 v125, %[rw], s44 offen nt lds\ns_add_u32 m0, %[lw], 3072\ns_add_u32 s44, %[wo], 114688\nbuffer_load_dwordx4 v125, %[rw], s44 offen nt lds\n" \
"s_add_u32 m0, %[lw], 4096\ns_add_u32 s44, %[wo], 131072\nbuffer_load_dwordx4 v125, %[rw], s44 offen nt lds\ns_add_u32 m0, %[lw], 5120\ns_add_u32 s44, %[wo], 147456\nbuffer_load_dwordx4 v125, %[rw], s44 offen nt lds\ns_add_u32 m0, %[lw], 6144\ns_add_u32 s44, %[wo], 163840\n" \
"buffer_load_dwordx4 v125, %[rw], s44 offen nt lds\ns_add_u32 m0, %[lw], 7168\ns_add_u32 s44, %[wo], 180224\nbuffer_load_dwordx4 v125, %[rw], s44 offen nt lds\ns_add_u32 m0, %[lw], 8192\ns_add_u32 s44, %[wo], 196608\nbuffer_load_dwordx4 v125, %[rw], s44 offen nt lds\ns_add_u32 m0, %[lw], 9216\n" \
"s_add_u32 s44, %[wo], 212992\nbuffer_load_dwordx4 v125, %[rw], s44 offen nt lds\ns_add_u32 m0, %[lw], 10240\ns_add_u32 s44, %[wo], 229376\nbuffer_load_dwordx4 v125, %[rw], s44 offen nt lds\ns_add_u32 m0, %[lw], 11264\ns_add_u32 s44, %[wo], 245760\nbuffer_load_dwordx4 v125, %[rw], s44 offen nt lds\n" \
"s_add_u32 m0, %[lw], 12288\ns_add_u32 s44, %[wo], 262144\nbuffer_load_dwordx4 v125, %[rw], s44 offen nt lds\ns_add_u32 m0, %[lw], 13312\ns_add_u32 s44, %[wo], 278528\nbuffer_load_dwordx4 v125, %[rw], s44 offen nt lds\ns_add_u32 m0, %[lw], 14336\ns_add_u32 s44, %[wo], 294912\n" \
"buffer_load_dwordx4 v125, %[rw], s44 offen nt lds\ns_add_u32 m0, %[lw], 15360\ns_add_u32 s44, %[wo], 311296\nbuffer_load_dwordx4 v125, %[rw], s44 offen nt lds\ns_add_u32 m0, %[lw], 16384\ns_add_u32 s44, %[wo], 327680\nbuffer_load_dwordx4 v125, %[rw], s44 offen nt lds\ns_add_u32 m0, %[lw], 17408\n" \
"s_add_u32 s44, %[wo], 344064\nbuffer_load_dwordx4 v125, %[rw], s44 offen nt lds\ns_add_u32 m0, %[lw], 18432\ns_add_u32 s44, %[wo], 360448\nbuffer_load_dwordx4 v125, %[rw], s44 offen nt lds\ns_add_u32 m0, %[lw], 19456\ns_add_u32 s44, %[wo], 376832\nbuffer_load_dwordx4 v125, %[rw], s44 offen nt lds\n" \
"s_add_u32 m0, %[lw], 20480\ns_add_u32 s44, %[wo], 393216\nbuffer_load_dwordx4 v125, %[rw], s44 offen nt lds\ns_add_u32 m0, %[lw], 21504\ns_add_u32 s44, %[wo], 409600\nbuffer_load_dwordx4 v125, %[rw], s44 offen nt lds\ns_add_u32 m0, %[lw], 22528\ns_add_u32 s44, %[wo], 425984\n" \
"buffer_load_dwordx4 v125, %[rw], s44 offen nt lds\ns_add_u32 m0, %[lw], 23552\ns_add_u32 s44, %[wo], 442368\nbuffer_load_dwordx4 v125, %[rw], s44 offen nt lds\ns_add_u32 m0, %[tb], 0\ns_add_u32 s44, %[wo], 458752\nbuffer_load_dwordx4 v125, %[rw], s44 offen nt lds\ns_add_u32 m0, %[tb], 1024\n" \
"s_add_u32 s44, %[wo], 475136\nbuffer_load_dwordx4 v125, %[rw], s44 offen nt lds\ns_add_u32 m0, %[tb], 2048\ns_add_u32 s44, %[wo], 491520\nbuffer_load_dwordx4 v125, %[rw], s44 offen nt lds\ns_add_u32 m0, %[tb], 3072\ns_add_u32 s44, %[wo], 507904\nbuffer_load_dwordx4 v125, %[rw], s44 offen nt lds\n" \
"s_add_u32 m0, %[tb], 4096\ns_add_u32 s44, %[wo], 524288\nbuffer_load_dwordx4 v125, %[rw], s44 offen nt lds\ns_add_u32 m0, %[tb], 5120\ns_add_u32 s44, %[wo], 540672\nbuffer_load_dwordx4 v125, %[rw], s44 offen nt lds\ns_add_u32 m0, %[tb], 6144\ns_add_u32 s44, %[wo], 557056\n" \
"buffer_load_dwordx4 v125, %[rw], s44 offen nt lds\ns_add_u32 m0, %[tb], 7168\ns_add_u32 s44, %[wo], 573440\nbuffer_load_dwordx4 v125, %[rw], s44 offen nt lds\ns_waitcnt vmcnt(32)\nds_read_b64_tr_b16 a[0:1], v126 offset:8192\nds_read_b64_tr_b16 a[2:3], v126 offset:8704\nds_read_b64_tr_b16 a[8:9], v126 offset:8224\n" \
"ds_read_b64_tr_b16 a[10:11], v126 offset:8736\nds_read_b64_tr_b16 a[16:17], v126 offset:8256\nds_read_b64_tr_b16 a[18:19], v126 offset:8768\nds_read_b64_tr_b16 a[24:25], v126 offset:8288\nds_read_b64_tr_b16 a[26:27], v126 offset:8800\ns_waitcnt lgkmcnt(0)\ns_add_u32 m0, %[tb], 8192\ns_add_u32 s44, %[wo], 589824\n" \
"buffer_load_dwordx4 v125, %[rw], s44 offen nt lds\ns_add_u32 m0, %[tb], 9216\ns_add_u32 s44, %[wo], 606208\nbuffer_load_dwordx4 v125, %[rw], s44 offen nt lds\ns_add_u32 m0, %[tb], 10240\ns_add_u32 s44, %[wo], 622592\nbuffer_load_dwordx4 v125, %[rw], s44 offen nt lds\ns_add_u32 m0, %[tb], 11264\n" \
"s_add_u32 s44, %[wo], 638976\nbuffer_load_dwordx4 v125, %[rw], s44 offen nt lds\ns_waitcnt vmcnt(32)\nds_read_b64_tr_b16 a[4:5], v127 offset:0\nds_read_b64_tr_b16 a[6:7], v127 offset:512\nds_read_b64_tr_b16 a[12:13], v127 offset:32\nds_read_b64_tr_b16 a[14:15], v127 offset:544\nds_read_b64_tr_b16 a[20:21], v127 offset:64\n" \
"ds_read_b64_tr_b16 a[22:23], v127 offset:576\nds_read_b64_tr_b16 a[28:29], v127 offset:96\nds_read_b64_tr_b16 a[30:31], v127 offset:608\ns_waitcnt lgkmcnt(0)\ns_add_u32 m0, %[lw], 0\ns_add_u32 s44, %[wo], 655360\nbuffer_load_dwordx4 v125, %[rw], s44 offen nt lds\ns_add_u32 m0, %[lw], 1024\n" \
"s_add_u32 s44, %[wo], 671744\nbuffer_load_dwordx4 v125, %[rw], s44 offen nt lds\ns_add_u32 m0, %[lw], 2048\ns_add_u32 s44, %[wo], 688128\nbuffer_load_dwordx4 v125, %[rw], s44 offen nt lds\ns_add_u32 m0, %[lw], 3072\ns_add_u32 s44, %[wo], 704512\nbuffer_load_dwordx4 v125, %[rw], s44 offen nt lds\n" \
"s_waitcnt vmcnt(32)\nds_read_b64_tr_b16 a[32:33], v127 offset:4096\nds_read_b64_tr_b16 a[34:35], v127 offset:4608\nds_read_b64_tr_b16 v[92:93], v127 offset:4128\nds_read_b64_tr_b16 v[94:95], v127 offset:4640\nds_read_b64_tr_b16 a[40:41], v127 offset:4160\nds_read_b64_tr_b16 a[42:43], v127 offset:4672\nds_read_b64_tr_b16 a[48:49], v127 offset:4192\n" \
"ds_read_b64_tr_b16 a[50:51], v127 offset:4704\ns_waitcnt lgkmcnt(0)\ns_add_u32 m0, %[lw], 4096\ns_add_u32 s44, %[wo], 720896\nbuffer_load_dwordx4 v125, %[rw], s44 offen nt lds\ns_add_u32 m0, %[lw], 5120\ns_add_u32 s44, %[wo], 737280\nbuffer_load_dwordx4 v125, %[rw], s44 offen nt lds\n" \
"s_add_u32 m0, %[lw], 6144\ns_add_u32 s44, %[wo], 753664\nbuffer_load_dwordx4 v125, %[rw], s44 offen nt lds\ns_add_u32 m0, %[lw], 7168\ns_add_u32 s44, %[wo], 770048\nbuffer_load_dwordx4 v125, %[rw], s44 offen nt lds\ns_waitcnt vmcnt(32)\nds_read_b64_tr_b16 a[36:37], v127 offset:8192\n" \
"ds_read_b64_tr_b16 a[38:39], v127 offset:8704\nds_read_b64_tr_b16 v[88:89], v127 offset:8224\nds_read_b64_tr_b16 v[90:91], v127 offset:8736\nds_read_b64_tr_b16 a[44:45], v127 offset:8256\nds_read_b64_tr_b16 a[46:47], v127 offset:8768\nds_read_b64_tr_b16 a[52:53], v127 offset:8288\nds_read_b64_tr_b16 a[54:55], v127 offset:8800\ns_waitcnt lgkmcnt(0)\n" \
"s_add_u32 m0, %[lw], 8192\ns_add_u32 s44, %[wo], 786432\nbuffer_load_dwordx4 v125, %[rw], s44 offen nt lds\ns_add_u32 m0, %[lw], 9216\ns_add_u32 s44, %[wo], 802816\nbuffer_load_dwordx4 v125, %[rw], s44 offen nt lds\ns_add_u32 m0, %[lw], 10240\ns_add_u32 s44, %[wo], 819200\n" \
"buffer_load_dwordx4 v125, %[rw], s44 offen nt lds\ns_add_u32 m0, %[lw], 11264\ns_add_u32 s44, %[wo], 835584\nbuffer_load_dwordx4 v125, %[rw], s44 offen nt lds\ns_waitcnt vmcnt(32)\nds_read_b64_tr_b16 a[56:57], v127 offset:12288\nds_read_b64_tr_b16 a[58:59], v127 offset:12800\nds_read_b64_tr_b16 a[64:65], v127 offset:12320\n" \
"ds_read_b64_tr_b16 a[66:67], v127 offset:12832\nds_read_b64_tr_b16 a[72:73], v127 offset:12352\nds_read_b64_tr_b16 a[74:75], v127 offset:12864\nds_read_b64_tr_b16 a[80:81], v127 offset:12384\nds_read_b64_tr_b16 a[82:83], v127 offset:12896\ns_waitcnt lgkmcnt(0)\ns_add_u32 m0, %[lw], 12288\ns_add_u32 s44, %[wo], 851968\n" \
"buffer_load_dwordx4 v125, %[rw], s44 offen nt lds\ns_add_u32 m0, %[lw], 13312\ns_add_u32 s44, %[wo], 868352\nbuffer_load_dwordx4 v125, %[rw], s44 offen nt lds\ns_add_u32 m0, %[lw], 14336\ns_add_u32 s44, %[wo], 884736\nbuffer_load_dwordx4 v125, %[rw], s44 offen nt lds\ns_add_u32 m0, %[lw], 15360\n" \
"s_add_u32 s44, %[wo], 901120\nbuffer_load_dwordx4 v125, %[rw], s44 offen nt lds\ns_waitcnt vmcnt(32)\nds_read_b64_tr_b16 a[60:61], v127 offset:16384\nds_read_b64_tr_b16 a[62:63], v127 offset:16896\nds_read_b64_tr_b16 a[68:69], v127 offset:16416\nds_read_b64_tr_b16 a[70:71], v127 offset:16928\nds_read_b64_tr_b16 a[76:77], v127 offset:16448\n" \
"ds_read_b64_tr_b16 a[78:79], v127 offset:16960\nds_read_b64_tr_b16 a[84:85], v127 offset:16480\nds_read_b64_tr_b16 a[86:87], v127 offset:16992\ns_waitcnt lgkmcnt(0)\ns_add_u32 m0, %[lw], 16384\ns_add_u32 s44, %[wo], 917504\nbuffer_load_dwordx4 v125, %[rw], s44 offen nt lds\ns_add_u32 m0, %[lw], 17408\n" \
"s_add_u32 s44, %[wo], 933888\nbuffer_load_dwordx4 v125, %[rw], s44 offen nt lds\ns_add_u32 m0, %[lw], 18432\ns_add_u32 s44, %[wo], 950272\nbuffer_load_dwordx4 v125, %[rw], s44 offen nt lds\ns_add_u32 m0, %[lw], 19456\ns_add_u32 s44, %[wo], 966656\nbuffer_load_dwordx4 v125, %[rw], s44 offen nt lds\n" \
"s_waitcnt vmcnt(32)\nds_read_b64_tr_b16 a[88:89], v127 offset:20480\nds_read_b64_tr_b16 a[90:91], v127 offset:20992\nds_read_b64_tr_b16 a[96:97], v127 offset:20512\nds_read_b64_tr_b16 a[98:99], v127 offset:21024\nds_read_b64_tr_b16 a[104:105], v127 offset:20544\nds_read_b64_tr_b16 a[106:107], v127 offset:21056\nds_read_b64_tr_b16 v[84:85], v127 offset:20576\n" \
"ds_read_b64_tr_b16 v[86:87], v127 offset:21088\ns_waitcnt lgkmcnt(0)\ns_add_u32 m0, %[lw], 20480\ns_add_u32 s44, %[wo], 983040\nbuffer_load_dwordx4 v125, %[rw], s44 offen nt lds\ns_add_u32 m0, %[lw], 21504\ns_add_u32 s44, %[wo], 999424\nbuffer_load_dwordx4 v125, %[rw], s44 offen nt lds\n" \
"s_add_u32 m0, %[lw], 22528\ns_add_u32 s44, %[wo], 1015808\nbuffer_load_dwordx4 v125, %[rw], s44 offen nt lds\ns_add_u32 m0, %[lw], 23552\ns_add_u32 s44, %[wo], 1032192\nbuffer_load_dwordx4 v125, %[rw], s44 offen nt lds\ns_waitcnt vmcnt(32)\nds_read_b64_tr_b16 a[92:93], v126 offset:0\n" \
"ds_read_b64_tr_b16 a[94:95], v126 offset:512\nds_read_b64_tr_b16 a[100:101], v126 offset:32\nds_read_b64_tr_b16 a[102:103], v126 offset:544\nds_read_b64_tr_b16 a[108:109], v126 offset:64\nds_read_b64_tr_b16 a[110:111], v126 offset:576\nds_read_b64_tr_b16 v[80:81], v126 offset:96\nds_read_b64_tr_b16 v[82:83], v126 offset:608\ns_waitcnt lgkmcnt(0)\n" \
"s_add_u32 m0, %[tb], 0\ns_add_u32 s44, %[wo], 1048576\nbuffer_load_dwordx4 v125, %[rw], s44 offen nt lds\ns_add_u32 m0, %[tb], 1024\ns_add_u32 s44, %[wo], 1064960\nbuffer_load_dwordx4 v125, %[rw], s44 offen nt lds\ns_add_u32 m0, %[tb], 2048\ns_add_u32 s44, %[wo], 1081344\n" \
"buffer_load_dwordx4 v125, %[rw], s44 offen nt lds\ns_add_u32 m0, %[tb], 3072\ns_add_u32 s44, %[wo], 1097728\nbuffer_load_dwordx4 v125, %[rw], s44 offen nt lds\ns_waitcnt vmcnt(32)\nds_read_b64_tr_b16 a[112:113], v126 offset:4096\nds_read_b64_tr_b16 a[114:115], v126 offset:4608\nds_read_b64_tr_b16 a[120:121], v126 offset:4128\n" \
"ds_read_b64_tr_b16 a[122:123], v126 offset:4640\nds_read_b64_tr_b16 a[128:129], v126 offset:4160\nds_read_b64_tr_b16 a[130:131], v126 offset:4672\nds_read_b64_tr_b16 a[136:137], v126 offset:4192\nds_read_b64_tr_b16 a[138:139], v126 offset:4704\ns_waitcnt lgkmcnt(0)\ns_add_u32 m0, %[tb], 4096\ns_add_u32 s44, %[wo], 1114112\n" \
"buffer_load_dwordx4 v125, %[rw], s44 offen nt lds\ns_add_u32 m0, %[tb], 5120\ns_add_u32 s44, %[wo], 1130496\nbuffer_load_dwordx4 v125, %[rw], s44 offen nt lds\ns_add_u32 m0, %[tb], 6144\ns_add_u32 s44, %[wo], 1146880\nbuffer_load_dwordx4 v125, %[rw], s44 offen nt lds\ns_add_u32 m0, %[tb], 7168\n" \
"s_add_u32 s44, %[wo], 1163264\nbuffer_load_dwordx4 v125, %[rw], s44 offen nt lds\ns_waitcnt vmcnt(32)\nds_read_b64_tr_b16 a[116:117], v126 offset:8192\nds_read_b64_tr_b16 a[118:119], v126 offset:8704\nds_read_b64_tr_b16 a[124:125], v126 offset:8224\nds_read_b64_tr_b16 a[126:127], v126 offset:8736\nds_read_b64_tr_b16 a[132:133], v126 offset:8256\n" \
"ds_read_b64_tr_b16 a[134:135], v126 offset:8768\nds_read_b64_tr_b16 a[140:141], v126 offset:8288\nds_read_b64_tr_b16 a[142:143], v126 offset:8800\ns_waitcnt lgkmcnt(0)\ns_add_u32 m0, %[tb], 8192\ns_add_u32 s44, %[wo], 1179648\nbuffer_load_dwordx4 v125, %[rw], s44 offen nt lds\ns_add_u32 m0, %[tb], 9216\n" \
"s_add_u32 s44, %[wo], 1196032\nbuffer_load_dwordx4 v125, %[rw], s44 offen nt lds\ns_add_u32 m0, %[tb], 10240\ns_add_u32 s44, %[wo], 1212416\nbuffer_load_dwordx4 v125, %[rw], s44 offen nt lds\ns_add_u32 m0, %[tb], 11264\ns_add_u32 s44, %[wo], 1228800\nbuffer_load_dwordx4 v125, %[rw], s44 offen nt lds\n" \
"s_waitcnt vmcnt(32)\nds_read_b64_tr_b16 a[144:145], v127 offset:0\nds_read_b64_tr_b16 a[146:147], v127 offset:512\nds_read_b64_tr_b16 v[76:77], v127 offset:32\nds_read_b64_tr_b16 v[78:79], v127 offset:544\nds_read_b64_tr_b16 a[152:153], v127 offset:64\nds_read_b64_tr_b16 a[154:155], v127 offset:576\nds_read_b64_tr_b16 a[160:161], v127 offset:96\n" \
"ds_read_b64_tr_b16 a[162:163], v127 offset:608\ns_waitcnt lgkmcnt(0)\ns_add_u32 m0, %[lw], 0\ns_add_u32 s44, %[wo], 1245184\nbuffer_load_dwordx4 v125, %[rw], s44 offen nt lds\ns_add_u32 m0, %[lw], 1024\ns_add_u32 s44, %[wo], 1261568\nbuffer_load_dwordx4 v125, %[rw], s44 offen nt lds\n" \
"s_add_u32 m0, %[lw], 2048\ns_add_u32 s44, %[wo], 1277952\nbuffer_load_dwordx4 v125, %[rw], s44 offen nt lds\ns_add_u32 m0, %[lw], 3072\ns_add_u32 s44, %[wo], 1294336\nbuffer_load_dwordx4 v125, %[rw], s44 offen nt lds\ns_waitcnt vmcnt(32)\nds_read_b64_tr_b16 a[148:149], v127 offset:4096\n" \
"ds_read_b64_tr_b16 a[150:151], v127 offset:4608\nds_read_b64_tr_b16 v[72:73], v127 offset:4128\nds_read_b64_tr_b16 v[74:75], v127 offset:4640\nds_read_b64_tr_b16 a[156:157], v127 offset:4160\nds_read_b64_tr_b16 a[158:159], v127 offset:4672\nds_read_b64_tr_b16 a[164:165], v127 offset:4192\nds_read_b64_tr_b16 a[166:167], v127 offset:4704\ns_waitcnt lgkmcnt(0)\n" \
"s_add_u32 m0, %[lw], 4096\ns_add_u32 s44, %[wo], 1310720\nbuffer_load_dwordx4 v125, %[rw], s44 offen nt lds\ns_add_u32 m0, %[lw], 5120\ns_add_u32 s44, %[wo], 1327104\nbuffer_load_dwordx4 v125, %[rw], s44 offen nt lds\ns_add_u32 m0, %[lw], 6144\ns_add_u32 s44, %[wo], 1343488\n" \
"buffer_load_dwordx4 v125, %[rw], s44 offen nt lds\ns_add_u32 m0, %[lw], 7168\ns_add_u32 s44, %[wo], 1359872\nbuffer_load_dwordx4 v125, %[rw], s44 offen nt lds\ns_waitcnt vmcnt(32)\nds_read_b64_tr_b16 a[168:169], v127 offset:8192\nds_read_b64_tr_b16 a[170:171], v127 offset:8704\nds_read_b64_tr_b16 a[176:177], v127 offset:8224\n" \
"ds_read_b64_tr_b16 a[178:179], v127 offset:8736\nds_read_b64_tr_b16 v[68:69], v127 offset:8256\nds_read_b64_tr_b16 v[70:71], v127 offset:8768\nds_read_b64_tr_b16 a[184:185], v127 offset:8288\nds_read_b64_tr_b16 a[186:187], v127 offset:8800\ns_waitcnt lgkmcnt(0)\ns_add_u32 m0, %[lw], 8192\ns_add_u32 s44, %[wo], 1376256\n" \
"buffer_load_dwordx4 v125, %[rw], s44 offen nt lds\ns_add_u32 m0, %[lw], 9216\ns_add_u32 s44, %[wo], 1392640\nbuffer_load_dwordx4 v125, %[rw], s44 offen nt lds\ns_add_u32 m0, %[lw], 10240\ns_add_u32 s44, %[wo], 1409024\nbuffer_load_dwordx4 v125, %[rw], s44 offen nt lds\ns_add_u32 m0, %[lw], 11264\n" \
"s_add_u32 s44, %[wo], 1425408\nbuffer_load_dwordx4 v125, %[rw], s44 offen nt lds\ns_waitcnt vmcnt(32)\nds_read_b64_tr_b16 a[172:173], v127 offset:12288\nds_read_b64_tr_b16 a[174:175], v127 offset:12800\nds_read_b64_tr_b16 a[180:181], v127 offset:12320\nds_read_b64_tr_b16 a[182:183], v127 offset:12832\nds_read_b64_tr_b16 v[64:65], v127 offset:12352\n" \
"ds_read_b64_tr_b16 v[66:67], v127 offset:12864\nds_read_b64_tr_b16 a[188:189], v127 offset:12384\nds_read_b64_tr_b16 a[190:191], v127 offset:12896\ns_waitcnt lgkmcnt(0)\ns_add_u32 m0, %[lw], 12288\ns_add_u32 s44, %[wo], 1441792\nbuffer_load_dwordx4 v125, %[rw], s44 offen nt lds\ns_add_u32 m0, %[lw], 13312\n" \
"s_add_u32 s44, %[wo], 1458176\nbuffer_load_dwordx4 v125, %[rw], s44 offen nt lds\ns_add_u32 m0, %[lw], 14336\ns_add_u32 s44, %[wo], 1474560\nbuffer_load_dwordx4 v125, %[rw], s44 offen nt lds\ns_add_u32 m0, %[lw], 15360\ns_add_u32 s44, %[wo], 1490944\nbuffer_load_dwordx4 v125, %[rw], s44 offen nt lds\n" \
"s_waitcnt vmcnt(32)\nds_read_b64_tr_b16 a[192:193], v127 offset:16384\nds_read_b64_tr_b16 a[194:195], v127 offset:16896\nds_read_b64_tr_b16 a[200:201], v127 offset:16416\nds_read_b64_tr_b16 a[202:203], v127 offset:16928\nds_read_b64_tr_b16 a[208:209], v127 offset:16448\nds_read_b64_tr_b16 a[210:211], v127 offset:16960\nds_read_b64_tr_b16 a[216:217], v127 offset:16480\n" \
"ds_read_b64_tr_b16 a[218:219], v127 offset:16992\ns_waitcnt lgkmcnt(0)\ns_add_u32 m0, %[lw], 16384\ns_add_u32 s44, %[wo], 1507328\nbuffer_load_dwordx4 v125, %[rw], s44 offen nt lds\ns_add_u32 m0, %[lw], 17408\ns_add_u32 s44, %[wo], 1523712\nbuffer_load_dwordx4 v125, %[rw], s44 offen nt lds\n" \
"s_add_u32 m0, %[lw], 18432\ns_add_u32 s44, %[wo], 1540096\nbuffer_load_dwordx4 v125, %[rw], s44 offen nt lds\ns_add_u32 m0, %[lw], 19456\ns_add_u32 s44, %[wo], 1556480\nbuffer_load_dwordx4 v125, %[rw], s44 offen nt lds\ns_waitcnt vmcnt(32)\nds_read_b64_tr_b16 a[196:197], v127 offset:20480\n" \
"ds_read_b64_tr_b16 a[198:199], v127 offset:20992\nds_read_b64_tr_b16 a[204:205], v127 offset:20512\nds_read_b64_tr_b16 a[206:207], v127 offset:21024\nds_read_b64_tr_b16 a[212:213], v127 offset:20544\nds_read_b64_tr_b16 a[214:215], v127 offset:21056\nds_read_b64_tr_b16 a[220:221], v127 offset:20576\nds_read_b64_tr_b16 a[222:223], v127 offset:21088\ns_waitcnt lgkmcnt(0)\n" \
"s_add_u32 m0, %[lw], 20480\ns_add_u32 s44, %[wo], 1572864\nbuffer_load_dwordx4 v125, %[rw], s44 offen nt lds\ns_add_u32 m0, %[lw], 21504\ns_add_u32 s44, %[wo], 1589248\nbuffer_load_dwordx4 v125, %[rw], s44 offen nt lds\ns_add_u32 m0, %[lw], 22528\ns_add_u32 s44, %[wo], 1605632\n" \
"buffer_load_dwordx4 v125, %[rw], s44 offen nt lds\ns_add_u32 m0, %[lw], 23552\ns_add_u32 s44, %[wo], 1622016\nbuffer_load_dwordx4 v125, %[rw], s44 offen nt lds\ns_waitcnt vmcnt(32)\nds_read_b64_tr_b16 v[60:61], v126 offset:0\nds_read_b64_tr_b16 v[62:63], v126 offset:512\nds_read_b64_tr_b16 a[224:225], v126 offset:32\n" \
"ds_read_b64_tr_b16 a[226:227], v126 offset:544\nds_read_b64_tr_b16 a[232:233], v126 offset:64\nds_read_b64_tr_b16 a[234:235], v126 offset:576\nds_read_b64_tr_b16 a[240:241], v126 offset:96\nds_read_b64_tr_b16 a[242:243], v126 offset:608\ns_waitcnt lgkmcnt(0)\ns_add_u32 m0, %[tb], 0\ns_add_u32 s44, %[wo], 1638400\n" \
"buffer_load_dwordx4 v125, %[rw], s44 offen nt lds\ns_add_u32 m0, %[tb], 1024\ns_add_u32 s44, %[wo], 1654784\nbuffer_load_dwordx4 v125, %[rw], s44 offen nt lds\ns_add_u32 m0, %[tb], 2048\ns_add_u32 s44, %[wo], 1671168\nbuffer_load_dwordx4 v125, %[rw], s44 offen nt lds\ns_add_u32 m0, %[tb], 3072\n" \
"s_add_u32 s44, %[wo], 1687552\nbuffer_load_dwordx4 v125, %[rw], s44 offen nt lds\ns_waitcnt vmcnt(32)\nds_read_b64_tr_b16 v[56:57], v126 offset:4096\nds_read_b64_tr_b16 v[58:59], v126 offset:4608\nds_read_b64_tr_b16 a[228:229], v126 offset:4128\nds_read_b64_tr_b16 a[230:231], v126 offset:4640\nds_read_b64_tr_b16 a[236:237], v126 offset:4160\n" \
"ds_read_b64_tr_b16 a[238:239], v126 offset:4672\nds_read_b64_tr_b16 a[244:245], v126 offset:4192\nds_read_b64_tr_b16 a[246:247], v126 offset:4704\ns_waitcnt lgkmcnt(0)\ns_add_u32 m0, %[tb], 4096\ns_add_u32 s44, %[wo], 1703936\nbuffer_load_dwordx4 v125, %[rw], s44 offen nt lds\ns_add_u32 m0, %[tb], 5120\n" \
"s_add_u32 s44, %[wo], 1720320\nbuffer_load_dwordx4 v125, %[rw], s44 offen nt lds\ns_add_u32 m0, %[tb], 6144\ns_add_u32 s44, %[wo], 1736704\nbuffer_load_dwordx4 v125, %[rw], s44 offen nt lds\ns_add_u32 m0, %[tb], 7168\ns_add_u32 s44, %[wo], 1753088\nbuffer_load_dwordx4 v125, %[rw], s44 offen nt lds\n" \
"s_waitcnt vmcnt(32)\nds_read_b64_tr_b16 a[248:249], v126 offset:8192\nds_read_b64_tr_b16 a[250:251], v126 offset:8704\nds_read_b64_tr_b16 v[128:129], v126 offset:8224\nds_read_b64_tr_b16 v[130:131], v126 offset:8736\nds_read_b64_tr_b16 v[136:137], v126 offset:8256\nds_read_b64_tr_b16 v[138:139], v126 offset:8768\nds_read_b64_tr_b16 v[144:145], v126 offset:8288\n" \
"ds_read_b64_tr_b16 v[146:147], v126 offset:8800\ns_waitcnt lgkmcnt(0)\ns_add_u32 m0, %[tb], 8192\ns_add_u32 s44, %[wo], 1769472\nbuffer_load_dwordx4 v125, %[rw], s44 offen nt lds\ns_add_u32 m0, %[tb], 9216\ns_add_u32 s44, %[wo], 1785856\nbuffer_load_dwordx4 v125, %[rw], s44 offen nt lds\n" \
"s_add_u32 m0, %[tb], 10240\ns_add_u32 s44, %[wo], 1802240\nbuffer_load_dwordx4 v125, %[rw], s44 offen nt lds\ns_add_u32 m0, %[tb], 11264\ns_add_u32 s44, %[wo], 1818624\nbuffer_load_dwordx4 v125, %[rw], s44 offen nt lds\ns_waitcnt vmcnt(32)\nds_read_b64_tr_b16 a[252:253], v127 offset:0\n" \
"ds_read_b64_tr_b16 a[254:255], v127 offset:512\nds_read_b64_tr_b16 v[132:133], v127 offset:32\nds_read_b64_tr_b16 v[134:135], v127 offset:544\nds_read_b64_tr_b16 v[140:141], v127 offset:64\nds_read_b64_tr_b16 v[142:143], v127 offset:576\nds_read_b64_tr_b16 v[148:149], v127 offset:96\nds_read_b64_tr_b16 v[150:151], v127 offset:608\ns_waitcnt lgkmcnt(0)\n" \
"ds_write_b128 v1, v[92:95] offset:0\nds_write_b128 v1, v[88:91] offset:1024\nds_write_b128 v1, v[84:87] offset:2048\nds_write_b128 v1, v[80:83] offset:3072\ns_waitcnt vmcnt(28)\nds_read_b64_tr_b16 v[152:153], v127 offset:4096\nds_read_b64_tr_b16 v[154:155], v127 offset:4608\nds_read_b64_tr_b16 v[160:161], v127 offset:4128\n" \
"ds_read_b64_tr_b16 v[162:163], v127 offset:4640\nds_read_b64_tr_b16 v[80:81], v127 offset:4160\nds_read_b64_tr_b16 v[82:83], v127 offset:4672\nds_read_b64_tr_b16 v[168:169], v127 offset:4192\nds_read_b64_tr_b16 v[170:171], v127 offset:4704\ns_waitcnt lgkmcnt(0)\nds_write_b128 v1, v[76:79] offset:4096\nds_write_b128 v1, v[72:75] offset:5120\n" \
"ds_write_b128 v1, v[68:71] offset:6144\nds_write_b128 v1, v[64:67] offset:7168\ns_waitcnt vmcnt(24)\nds_read_b64_tr_b16 v[156:157], v127 offset:8192\nds_read_b64_tr_b16 v[158:159], v127 offset:8704\nds_read_b64_tr_b16 v[164:165], v127 offset:8224\nds_read_b64_tr_b16 v[166:167], v127 offset:8736\nds_read_b64_tr_b16 v[64:65], v127 offset:8256\n" \
"ds_read_b64_tr_b16 v[66:67], v127 offset:8768\nds_read_b64_tr_b16 v[172:173], v127 offset:8288\nds_read_b64_tr_b16 v[174:175], v127 offset:8800\ns_waitcnt lgkmcnt(0)\nds_write_b128 v1, v[60:63] offset:8192\nds_write_b128 v1, v[56:59] offset:9216\nds_write_b128 v1, v[80:83] offset:10240\nds_write_b128 v1, v[64:67] offset:11264\n" \
"s_waitcnt vmcnt(20)\nds_read_b64_tr_b16 v[176:177], v127 offset:12288\nds_read_b64_tr_b16 v[178:179], v127 offset:12800\nds_read_b64_tr_b16 v[184:185], v127 offset:12320\nds_read_b64_tr_b16 v[186:187], v127 offset:12832\nds_read_b64_tr_b16 v[192:193], v127 offset:12352\nds_read_b64_tr_b16 v[194:195], v127 offset:12864\nds_read_b64_tr_b16 v[200:201], v127 offset:12384\n" \
"ds_read_b64_tr_b16 v[202:203], v127 offset:12896\ns_waitcnt lgkmcnt(0)\ns_waitcnt vmcnt(16)\nds_read_b64_tr_b16 v[180:181], v127 offset:16384\nds_read_b64_tr_b16 v[182:183], v127 offset:16896\nds_read_b64_tr_b16 v[188:189], v127 offset:16416\nds_read_b64_tr_b16 v[190:191], v127 offset:16928\nds_read_b64_tr_b16 v[196:197], v127 offset:16448\n" \
"ds_read_b64_tr_b16 v[198:199], v127 offset:16960\nds_read_b64_tr_b16 v[204:205], v127 offset:16480\nds_read_b64_tr_b16 v[206:207], v127 offset:16992\ns_waitcnt lgkmcnt(0)\ns_waitcnt vmcnt(12)\nds_read_b64_tr_b16 v[64:65], v127 offset:20480\nds_read_b64_tr_b16 v[66:67], v127 offset:20992\nds_read_b64_tr_b16 v[208:209], v127 offset:20512\n" \
"ds_read_b64_tr_b16 v[210:211], v127 offset:21024\nds_read_b64_tr_b16 v[216:217], v127 offset:20544\nds_read_b64_tr_b16 v[218:219], v127 offset:21056\nds_read_b64_tr_b16 v[224:225], v127 offset:20576\nds_read_b64_tr_b16 v[226:227], v127 offset:21088\ns_waitcnt lgkmcnt(0)\nds_write_b128 v1, v[64:67] offset:12288\ns_waitcnt vmcnt(8)\n" \
"ds_read_b64_tr_b16 v[64:65], v126 offset:0\nds_read_b64_tr_b16 v[66:67], v126 offset:512\nds_read_b64_tr_b16 v[212:213], v126 offset:32\nds_read_b64_tr_b16 v[214:215], v126 offset:544\nds_read_b64_tr_b16 v[220:221], v126 offset:64\nds_read_b64_tr_b16 v[222:223], v126 offset:576\nds_read_b64_tr_b16 v[228:229], v126 offset:96\nds_read_b64_tr_b16 v[230:231], v126 offset:608\n" \
"s_waitcnt lgkmcnt(0)\nds_write_b128 v1, v[64:67] offset:13312\ns_waitcnt vmcnt(4)\nds_read_b64_tr_b16 v[232:233], v126 offset:4096\nds_read_b64_tr_b16 v[234:235], v126 offset:4608\nds_read_b64_tr_b16 v[64:65], v126 offset:4128\nds_read_b64_tr_b16 v[66:67], v126 offset:4640\nds_read_b64_tr_b16 v[240:241], v126 offset:4160\n" \
"ds_read_b64_tr_b16 v[242:243], v126 offset:4672\nds_read_b64_tr_b16 v[248:249], v126 offset:4192\nds_read_b64_tr_b16 v[250:251], v126 offset:4704\ns_waitcnt lgkmcnt(0)\nds_write_b128 v1, v[64:67] offset:14336\ns_waitcnt vmcnt(0)\nds_read_b64_tr_b16 v[236:237], v126 offset:8192\nds_read_b64_tr_b16 v[238:239], v126 offset:8704\n" \
"ds_read_b64_tr_b16 v[64:65], v126 offset:8224\nds_read_b64_tr_b16 v[66:67], v126 offset:8736\nds_read_b64_tr_b16 v[244:245], v126 offset:8256\nds_read_b64_tr_b16 v[246:247], v126 offset:8768\nds_read_b64_tr_b16 v[252:253], v126 offset:8288\nds_read_b64_tr_b16 v[254:255], v126 offset:8800\ns_waitcnt lgkmcnt(0)\nds_write_b128 v1, v[64:67] offset:15360\n" \
"s_waitcnt lgkmcnt(0)\ns_barrier\ns_mov_b32 s40, 0\ns_mov_b32 s43, 0\ns_mov_b32 s56, -2147483648\n.p2align 6\n.Ldd_next%=:\ns_cmp_eq_u32 s40, %[b1]\n" \
"s_cselect_b32 s44, 4, 0\nv_xor_b32 v7, s44, v7\nbuffer_load_dword v8, v7, %[rs], 0 offen sc1\ns_lshl_b32 s58, s40, 1\ns_add_u32 s44, s40, 0\ns_lshl_b32 s44, s44, 19\ns_add_u32 s44, s44, %[ho]\ns_lshr_b32 s45, s44, 8\n" \
"s_and_b32 s45, s45, 0x2000\ns_xor_b32 s55, s44, s45\ns_waitcnt vmcnt(0)\nbuffer_store_dword v9, off, %[rs], s56 sc1\ns_mov_b32 s56, -2147483648\n.Ldd_w0%=:\nv_cmp_gt_i32 vcc, s58, v8\ns_cbranch_vccz .Ldd_w0go%=\n" \
"s_cmp_ge_u32 s40, 62\ns_cbranch_scc1 .Ldd_ns671%=\ns_sleep 8\n.Ldd_ns671%=:\ns_sleep 1\nbuffer_load_dword v8, v7, %[rs], 0 offen sc1\ns_waitcnt vmcnt(0)\ns_add_u32 s43, s43, 1\n" \
"s_cmp_lt_u32 s43, 262144\ns_cbranch_scc1 .Ldd_w0%=\ns_branch .Ldd_abort%=\n.Ldd_w0go%=:\ns_add_u32 s60, s55, 0\nbuffer_load_dwordx4 v[48:51], v0, %[rs], s60 offen offset:0 sc1\nbuffer_load_dwordx4 v[52:55], v0, %[rs], s60 offen offset:2048 sc1\ns_add_u32 s60, s55, 4096\n" \
"buffer_load_dwordx4 v[56:59], v0, %[rs], s60 offen offset:0 sc1\nbuffer_load_dwordx4 v[60:63], v0, %[rs], s60 offen offset:2048 sc1\ns_add_u32 s60, s55, 16384\nbuffer_load_dwordx4 v[64:67], v0, %[rs], s60 offen offset:0 sc1\nbuffer_load_dwordx4 v[68:71], v0, %[rs], s60 offen offset:2048 sc1\ns_add_u32 s60, s55, 20480\nbuffer_load_dwordx4 v[72:75], v0, %[rs], s60 offen offset:0 sc1\nbuffer_load_dwordx4 v[76:79], v0, %[rs], s60 offen offset:2048 sc1\n" \
"s_add_u32 s60, s55, 32768\nbuffer_load_dwordx4 v[80:83], v0, %[rs], s60 offen offset:0 sc1\nbuffer_load_dwordx4 v[84:87], v0, %[rs], s60 offen offset:2048 sc1\ns_add_u32 s60, s55, 36864\nbuffer_load_dwordx4 v[88:91], v0, %[rs], s60 offen offset:0 sc1\nbuffer_load_dwordx4 v[92:95], v0, %[rs], s60 offen offset:2048 sc1\ns_add_u32 s60, s55, 49152\nbuffer_load_dwordx4 v[96:99], v0, %[rs], s60 offen offset:0 sc1\n" \
"buffer_load_dwordx4 v[100:103], v0, %[rs], s60 offen offset:2048 sc1\n.Ldd_run%=:\ns_add_u32 s44, s40, 0\ns_lshl_b32 s44, s44, 17\ns_add_u32 s44, s44, %[xn]\ns_lshr_b32 s45, s44, 8\ns_and_b32 s45, s45, 0x2000\ns_xor_b32 s41, s44, s45\n" \
"s_lshl_b32 s44, s40, 16\ns_cmp_eq_u32 %[l], 0\ns_cselect_b32 s42, s41, s44\ns_add_u32 s58, s58, 1\nds_read_b128 v[120:123], v1 offset:0\ns_waitcnt vmcnt(13)\nv_mfma_f32_16x16x32_bf16 v[16:19], a[0:3], v[48:51], 0\nds_read_b128 v[124:127], v1 offset:1024\n" \
"v_mfma_f32_16x16x32_bf16 v[24:27], a[8:11], v[48:51], 0\nbuffer_load_dword v8, v7, %[rs], 0 offen sc1\nv_mfma_f32_16x16x32_bf16 v[32:35], a[16:19], v[48:51], 0\ns_add_u32 s60, s55, 53248\nv_mfma_f32_16x16x32_bf16 v[40:43], a[24:27], v[48:51], 0\ns_waitcnt vmcnt(13)\nbuffer_load_dwordx4 v[48:51], v0, %[rs], s60 offen offset:0 sc1\nv_mfma_f32_16x16x32_bf16 v[16:19], a[4:7], v[52:55], v[16:19]\n" \
"v_mfma_f32_16x16x32_bf16 v[24:27], a[12:15], v[52:55], v[24:27]\nv_mfma_f32_16x16x32_bf16 v[32:35], a[20:23], v[52:55], v[32:35]\nv_mfma_f32_16x16x32_bf16 v[40:43], a[28:31], v[52:55], v[40:43]\ns_waitcnt vmcnt(13)\nbuffer_load_dwordx4 v[52:55], v0, %[rs], s60 offen offset:2048 sc1\nv_mfma_f32_16x16x32_bf16 v[16:19], a[32:35], v[56:59], v[16:19]\ns_waitcnt lgkmcnt(1)\nv_mfma_f32_16x16x32_bf16 v[24:27], v[120:123], v[56:59], v[24:27]\n" \
"v_mfma_f32_16x16x32_bf16 v[32:35], a[40:43], v[56:59], v[32:35]\ns_add_u32 s60, s55, 65536\nv_mfma_f32_16x16x32_bf16 v[40:43], a[48:51], v[56:59], v[40:43]\ns_waitcnt vmcnt(13)\nbuffer_load_dwordx4 v[56:59], v0, %[rs], s60 offen offset:0 sc1\nv_mfma_f32_16x16x32_bf16 v[16:19], a[36:39], v[60:63], v[16:19]\ns_waitcnt lgkmcnt(0)\nds_read_b128 v[120:123], v1 offset:2048\n" \
"v_mfma_f32_16x16x32_bf16 v[24:27], v[124:127], v[60:63], v[24:27]\nv_mfma_f32_16x16x32_bf16 v[32:35], a[44:47], v[60:63], v[32:35]\nv_mfma_f32_16x16x32_bf16 v[40:43], a[52:55], v[60:63], v[40:43]\ns_waitcnt vmcnt(13)\nbuffer_load_dwordx4 v[60:63], v0, %[rs], s60 offen offset:2048 sc1\nv_mfma_f32_16x16x32_bf16 v[16:19], a[56:59], v[64:67], v[16:19]\nds_read_b128 v[124:127], v1 offset:3072\nv_mfma_f32_16x16x32_bf16 v[24:27], a[64:67], v[64:67], v[24:27]\n" \
"v_mfma_f32_16x16x32_bf16 v[32:35], a[72:75], v[64:67], v[32:35]\ns_add_u32 s60, s55, 69632\nv_mfma_f32_16x16x32_bf16 v[40:43], a[80:83], v[64:67], v[40:43]\ns_waitcnt vmcnt(13)\nbuffer_load_dwordx4 v[64:67], v0, %[rs], s60 offen offset:0 sc1\nv_mfma_f32_16x16x32_bf16 v[16:19], a[60:63], v[68:71], v[16:19]\nv_mfma_f32_16x16x32_bf16 v[24:27], a[68:71], v[68:71], v[24:27]\nv_mfma_f32_16x16x32_bf16 v[32:35], a[76:79], v[68:71], v[32:35]\n" \
"v_mfma_f32_16x16x32_bf16 v[40:43], a[84:87], v[68:71], v[40:43]\ns_waitcnt vmcnt(13)\nbuffer_load_dwordx4 v[68:71], v0, %[rs], s60 offen offset:2048 sc1\nv_mfma_f32_16x16x32_bf16 v[16:19], a[88:91], v[72:75], v[16:19]\nv_mfma_f32_16x16x32_bf16 v[24:27], a[96:99], v[72:75], v[24:27]\nv_mfma_f32_16x16x32_bf16 v[32:35], a[104:107], v[72:75], v[32:35]\ns_waitcnt lgkmcnt(1)\ns_add_u32 s60, s55, 81920\n" \
"v_mfma_f32_16x16x32_bf16 v[40:43], v[120:123], v[72:75], v[40:43]\ns_waitcnt vmcnt(13)\nbuffer_load_dwordx4 v[72:75], v0, %[rs], s60 offen offset:0 sc1\nv_mfma_f32_16x16x32_bf16 v[16:19], a[92:95], v[76:79], v[16:19]\nds_read_b128 v[120:123], v1 offset:4096\nv_mfma_f32_16x16x32_bf16 v[24:27], a[100:103], v[76:79], v[24:27]\nv_mfma_f32_16x16x32_bf16 v[32:35], a[108:111], v[76:79], v[32:35]\ns_waitcnt lgkmcnt(1)\n" \
"v_mfma_f32_16x16x32_bf16 v[40:43], v[124:127], v[76:79], v[40:43]\ns_waitcnt vmcnt(13)\nbuffer_load_dwordx4 v[76:79], v0, %[rs], s60 offen offset:2048 sc1\nv_mfma_f32_16x16x32_bf16 v[16:19], a[112:115], v[80:83], v[16:19]\nds_read_b128 v[124:127], v1 offset:5120\nv_mfma_f32_16x16x32_bf16 v[24:27], a[120:123], v[80:83], v[24:27]\nv_mfma_f32_16x16x32_bf16 v[32:35], a[128:131], v[80:83], v[32:35]\ns_add_u32 s60, s55, 86016\n" \
"v_mfma_f32_16x16x32_bf16 v[40:43], a[136:139], v[80:83], v[40:43]\ns_waitcnt vmcnt(13)\nbuffer_load_dwordx4 v[80:83], v0, %[rs], s60 offen offset:0 sc1\nv_mfma_f32_16x16x32_bf16 v[16:19], a[116:119], v[84:87], v[16:19]\nds_read_b128 v[112:115], v1 offset:6144\nv_mfma_f32_16x16x32_bf16 v[24:27], a[124:127], v[84:87], v[24:27]\nv_mfma_f32_16x16x32_bf16 v[32:35], a[132:135], v[84:87], v[32:35]\nv_mfma_f32_16x16x32_bf16 v[40:43], a[140:143], v[84:87], v[40:43]\n" \
"s_waitcnt vmcnt(13)\nbuffer_load_dwordx4 v[84:87], v0, %[rs], s60 offen offset:2048 sc1\nv_mfma_f32_16x16x32_bf16 v[16:19], a[144:147], v[88:91], v[16:19]\ns_waitcnt lgkmcnt(2)\nds_read_b128 v[116:119], v1 offset:7168\nv_mfma_f32_16x16x32_bf16 v[24:27], v[120:123], v[88:91], v[24:27]\nv_mfma_f32_16x16x32_bf16 v[32:35], a[152:155], v[88:91], v[32:35]\ns_add_u32 s60, s55, 98304\n" \
"v_mfma_f32_16x16x32_bf16 v[40:43], a[160:163], v[88:91], v[40:43]\ns_waitcnt vmcnt(13)\nbuffer_load_dwordx4 v[88:91], v0, %[rs], s60 offen offset:0 sc1\nv_mfma_f32_16x16x32_bf16 v[16:19], a[148:151], v[92:95], v[16:19]\ns_waitcnt lgkmcnt(2)\nv_mfma_f32_16x16x32_bf16 v[24:27], v[124:127], v[92:95], v[24:27]\nv_mfma_f32_16x16x32_bf16 v[32:35], a[156:159], v[92:95], v[32:35]\nv_mfma_f32_16x16x32_bf16 v[40:43], a[164:167], v[92:95], v[40:43]\n" \
"s_waitcnt vmcnt(13)\nbuffer_load_dwordx4 v[92:95], v0, %[rs], s60 offen offset:2048 sc1\nv_mfma_f32_16x16x32_bf16 v[16:19], a[168:171], v[96:99], v[16:19]\nv_mfma_f32_16x16x32_bf16 v[24:27], a[176:179], v[96:99], v[24:27]\ns_waitcnt lgkmcnt(1)\nv_mfma_f32_16x16x32_bf16 v[32:35], v[112:115], v[96:99], v[32:35]\ns_add_u32 s60, s55, 102400\nv_mfma_f32_16x16x32_bf16 v[40:43], a[184:187], v[96:99], v[40:43]\n" \
"s_waitcnt vmcnt(13)\nbuffer_load_dwordx4 v[96:99], v0, %[rs], s60 offen offset:0 sc1\nv_mfma_f32_16x16x32_bf16 v[16:19], a[172:175], v[100:103], v[16:19]\nds_read_b128 v[112:115], v1 offset:8192\nv_mfma_f32_16x16x32_bf16 v[24:27], a[180:183], v[100:103], v[24:27]\ns_waitcnt vmcnt(13)\ns_waitcnt lgkmcnt(1)\nv_cmp_gt_i32 vcc, s58, v8\n" \
"v_mfma_f32_16x16x32_bf16 v[32:35], v[116:119], v[100:103], v[32:35]\ns_cmp_eq_u64 vcc, 0\ns_cselect_b32 s61, s55, -2147483648\nv_mfma_f32_16x16x32_bf16 v[40:43], a[188:191], v[100:103], v[40:43]\ns_waitcnt vmcnt(12)\nbuffer_load_dwordx4 v[100:103], v0, %[rs], s60 offen offset:2048 sc1\nv_mfma_f32_16x16x32_bf16 v[16:19], a[192:195], v[48:51], v[16:19]\nds_read_b128 v[116:119], v1 offset:9216\n" \
"v_mfma_f32_16x16x32_bf16 v[24:27], a[200:203], v[48:51], v[24:27]\nv_mfma_f32_16x16x32_bf16 v[32:35], a[208:211], v[48:51], v[32:35]\ns_add_u32 s60, s61, 0\nv_mfma_f32_16x16x32_bf16 v[40:43], a[216:219], v[48:51], v[40:43]\ns_waitcnt vmcnt(12)\nbuffer_load_dwordx4 v[48:51], v0, %[rs], s60 offen offset:1024 sc1\nv_mfma_f32_16x16x32_bf16 v[16:19], a[196:199], v[52:55], v[16:19]\nv_mfma_f32_16x16x32_bf16 v[24:27], a[204:207], v[52:55], v[24:27]\n" \
"v_mfma_f32_16x16x32_bf16 v[32:35], a[212:215], v[52:55], v[32:35]\nv_mfma_f32_16x16x32_bf16 v[40:43], a[220:223], v[52:55], v[40:43]\ns_waitcnt vmcnt(12)\ns_waitcnt lgkmcnt(1)\nbuffer_load_dwordx4 v[52:55], v0, %[rs], s60 offen offset:3072 sc1\nv_mfma_f32_16x16x32_bf16 v[16:19], v[112:115], v[56:59], v[16:19]\nv_mfma_f32_16x16x32_bf16 v[24:27], a[224:227], v[56:59], v[24:27]\nv_mfma_f32_16x16x32_bf16 v[32:35], a[232:235], v[56:59], v[32:35]\n" \
"s_add_u32 s60, s61, 4096\nv_mfma_f32_16x16x32_bf16 v[40:43], a[240:243], v[56:59], v[40:43]\ns_waitcnt vmcnt(12)\ns_waitcnt lgkmcnt(0)\nbuffer_load_dwordx4 v[56:59], v0, %[rs], s60 offen offset:1024 sc1\nv_mfma_f32_16x16x32_bf16 v[16:19], v[116:119], v[60:63], v[16:19]\nds_read_b128 v[112:115], v1 offset:10240\nv_mfma_f32_16x16x32_bf16 v[24:27], a[228:231], v[60:63], v[24:27]\n" \
"v_mfma_f32_16x16x32_bf16 v[32:35], a[236:239], v[60:63], v[32:35]\nv_mfma_f32_16x16x32_bf16 v[40:43], a[244:247], v[60:63], v[40:43]\ns_waitcnt vmcnt(12)\nbuffer_load_dwordx4 v[60:63], v0, %[rs], s60 offen offset:3072 sc1\nv_mfma_f32_16x16x32_bf16 v[16:19], a[248:251], v[64:67], v[16:19]\nds_read_b128 v[116:119], v1 offset:11264\nv_mfma_f32_16x16x32_bf16 v[24:27], v[128:131], v[64:67], v[24:27]\nv_mfma_f32_16x16x32_bf16 v[32:35], v[136:139], v[64:67], v[32:35]\n" \
"s_add_u32 s60, s61, 16384\nv_mfma_f32_16x16x32_bf16 v[40:43], v[144:147], v[64:67], v[40:43]\ns_waitcnt vmcnt(12)\nbuffer_load_dwordx4 v[64:67], v0, %[rs], s60 offen offset:1024 sc1\nv_mfma_f32_16x16x32_bf16 v[16:19], a[252:255], v[68:71], v[16:19]\nv_mfma_f32_16x16x32_bf16 v[24:27], v[132:135], v[68:71], v[24:27]\nv_mfma_f32_16x16x32_bf16 v[32:35], v[140:143], v[68:71], v[32:35]\nv_mfma_f32_16x16x32_bf16 v[40:43], v[148:151], v[68:71], v[40:43]\n" \
"s_waitcnt vmcnt(12)\nbuffer_load_dwordx4 v[68:71], v0, %[rs], s60 offen offset:3072 sc1\nv_mfma_f32_16x16x32_bf16 v[16:19], v[152:155], v[72:75], v[16:19]\nv_mfma_f32_16x16x32_bf16 v[24:27], v[160:163], v[72:75], v[24:27]\ns_waitcnt lgkmcnt(1)\nv_mfma_f32_16x16x32_bf16 v[32:35], v[112:115], v[72:75], v[32:35]\ns_add_u32 s60, s61, 20480\nv_mfma_f32_16x16x32_bf16 v[40:43], v[168:171], v[72:75], v[40:43]\n" \
"s_waitcnt vmcnt(12)\nbuffer_load_dwordx4 v[72:75], v0, %[rs], s60 offen offset:1024 sc1\nv_mfma_f32_16x16x32_bf16 v[16:19], v[156:159], v[76:79], v[16:19]\nds_read_b128 v[112:115], v1 offset:12288\nv_mfma_f32_16x16x32_bf16 v[24:27], v[164:167], v[76:79], v[24:27]\ns_waitcnt lgkmcnt(1)\nv_mfma_f32_16x16x32_bf16 v[32:35], v[116:119], v[76:79], v[32:35]\nv_mfma_f32_16x16x32_bf16 v[40:43], v[172:175], v[76:79], v[40:43]\n" \
"s_waitcnt vmcnt(12)\nbuffer_load_dwordx4 v[76:79], v0, %[rs], s60 offen offset:3072 sc1\nv_mfma_f32_16x16x32_bf16 v[16:19], v[176:179], v[80:83], v[16:19]\nds_read_b128 v[116:119], v1 offset:13312\nv_mfma_f32_16x16x32_bf16 v[24:27], v[184:187], v[80:83], v[24:27]\nv_mfma_f32_16x16x32_bf16 v[32:35], v[192:195], v[80:83], v[32:35]\ns_add_u32 s60, s61, 32768\nv_mfma_f32_16x16x32_bf16 v[40:43], v[200:203], v[80:83], v[40:43]\n" \
"s_waitcnt vmcnt(12)\nbuffer_load_dwordx4 v[80:83], v0, %[rs], s60 offen offset:1024 sc1\nv_mfma_f32_16x16x32_bf16 v[16:19], v[180:183], v[84:87], v[16:19]\nds_read_b128 v[120:123], v1 offset:14336\nv_mfma_f32_16x16x32_bf16 v[24:27], v[188:191], v[84:87], v[24:27]\nv_mfma_f32_16x16x32_bf16 v[32:35], v[196:199], v[84:87], v[32:35]\nv_mfma_f32_16x16x32_bf16 v[40:43], v[204:207], v[84:87], v[40:43]\ns_waitcnt vmcnt(12)\n" \
"s_waitcnt lgkmcnt(2)\nbuffer_load_dwordx4 v[84:87], v0, %[rs], s60 offen offset:3072 sc1\nv_mfma_f32_16x16x32_bf16 v[16:19], v[112:115], v[88:91], v[16:19]\nds_read_b128 v[124:127], v1 offset:15360\nv_mfma_f32_16x16x32_bf16 v[24:27], v[208:211], v[88:91], v[24:27]\nv_mfma_f32_16x16x32_bf16 v[32:35], v[216:219], v[88:91], v[32:35]\ns_add_u32 s60, s61, 36864\nv_mfma_f32_16x16x32_bf16 v[40:43], v[224:227], v[88:91], v[40:43]\n" \
"s_waitcnt vmcnt(12)\ns_waitcnt lgkmcnt(2)\nbuffer_load_dwordx4 v[88:91], v0, %[rs], s60 offen offset:1024 sc1\nv_mfma_f32_16x16x32_bf16 v[16:19], v[116:119], v[92:95], v[16:19]\nv_mfma_f32_16x16x32_bf16 v[24:27], v[212:215], v[92:95], v[24:27]\nv_mfma_f32_16x16x32_bf16 v[32:35], v[220:223], v[92:95], v[32:35]\nv_mfma_f32_16x16x32_bf16 v[40:43], v[228:231], v[92:95], v[40:43]\ns_waitcnt vmcnt(12)\n" \
"buffer_load_dwordx4 v[92:95], v0, %[rs], s60 offen offset:3072 sc1\nv_mfma_f32_16x16x32_bf16 v[16:19], v[232:235], v[96:99], v[16:19]\ns_waitcnt lgkmcnt(1)\nv_mfma_f32_16x16x32_bf16 v[24:27], v[120:123], v[96:99], v[24:27]\nv_mfma_f32_16x16x32_bf16 v[32:35], v[240:243], v[96:99], v[32:35]\ns_add_u32 s60, s61, 49152\nv_mfma_f32_16x16x32_bf16 v[40:43], v[248:251], v[96:99], v[40:43]\ns_waitcnt vmcnt(12)\n" \
"buffer_load_dwordx4 v[96:99], v0, %[rs], s60 offen offset:1024 sc1\nv_mfma_f32_16x16x32_bf16 v[16:19], v[236:239], v[100:103], v[16:19]\ns_waitcnt lgkmcnt(0)\nds_read_b128 v[120:123], v1 offset:0\nv_mfma_f32_16x16x32_bf16 v[24:27], v[124:127], v[100:103], v[24:27]\nv_mfma_f32_16x16x32_bf16 v[32:35], v[244:247], v[100:103], v[32:35]\nv_mfma_f32_16x16x32_bf16 v[40:43], v[252:255], v[100:103], v[40:43]\nbuffer_load_dwordx4 v[100:103], v0, %[rs], s60 offen offset:3072 sc1\n" \
"s_cbranch_vccnz .Ldd_slow%=\n.Ldd_slowr%=:\ns_waitcnt vmcnt(13)\nv_mfma_f32_16x16x32_bf16 v[20:23], a[0:3], v[48:51], 0\nds_read_b128 v[124:127], v1 offset:1024\nv_mfma_f32_16x16x32_bf16 v[28:31], a[8:11], v[48:51], 0\nv_mfma_f32_16x16x32_bf16 v[36:39], a[16:19], v[48:51], 0\ns_add_u32 s60, s55, 53248\n" \
"v_mfma_f32_16x16x32_bf16 v[44:47], a[24:27], v[48:51], 0\ns_waitcnt vmcnt(12)\nbuffer_load_dwordx4 v[48:51], v0, %[rs], s60 offen offset:1024 sc1\nv_mfma_f32_16x16x32_bf16 v[20:23], a[4:7], v[52:55], v[20:23]\nv_mfma_f32_16x16x32_bf16 v[28:31], a[12:15], v[52:55], v[28:31]\nv_mfma_f32_16x16x32_bf16 v[36:39], a[20:23], v[52:55], v[36:39]\nv_mfma_f32_16x16x32_bf16 v[44:47], a[28:31], v[52:55], v[44:47]\ns_waitcnt vmcnt(12)\n" \
"buffer_load_dwordx4 v[52:55], v0, %[rs], s60 offen offset:3072 sc1\nv_mfma_f32_16x16x32_bf16 v[20:23], a[32:35], v[56:59], v[20:23]\ns_waitcnt lgkmcnt(1)\nv_mfma_f32_16x16x32_bf16 v[28:31], v[120:123], v[56:59], v[28:31]\nds_write_b128 v2, v[16:19] offset:0\nv_mfma_f32_16x16x32_bf16 v[36:39], a[40:43], v[56:59], v[36:39]\ns_add_u32 s60, s55, 65536\nv_mfma_f32_16x16x32_bf16 v[44:47], a[48:51], v[56:59], v[44:47]\n" \
"s_waitcnt vmcnt(12)\nbuffer_load_dwordx4 v[56:59], v0, %[rs], s60 offen offset:1024 sc1\nv_mfma_f32_16x16x32_bf16 v[20:23], a[36:39], v[60:63], v[20:23]\ns_waitcnt lgkmcnt(1)\nds_read_b128 v[120:123], v1 offset:2048\nv_mfma_f32_16x16x32_bf16 v[28:31], v[124:127], v[60:63], v[28:31]\nv_mfma_f32_16x16x32_bf16 v[36:39], a[44:47], v[60:63], v[36:39]\nv_mfma_f32_16x16x32_bf16 v[44:47], a[52:55], v[60:63], v[44:47]\n" \
"s_waitcnt vmcnt(12)\nbuffer_load_dwordx4 v[60:63], v0, %[rs], s60 offen offset:3072 sc1\nv_mfma_f32_16x16x32_bf16 v[20:23], a[56:59], v[64:67], v[20:23]\nds_read_b128 v[124:127], v1 offset:3072\nv_mfma_f32_16x16x32_bf16 v[28:31], a[64:67], v[64:67], v[28:31]\nds_write_b128 v2, v[24:27] offset:2048\nv_mfma_f32_16x16x32_bf16 v[36:39], a[72:75], v[64:67], v[36:39]\ns_add_u32 s60, s55, 69632\n" \
"v_mfma_f32_16x16x32_bf16 v[44:47], a[80:83], v[64:67], v[44:47]\ns_waitcnt vmcnt(12)\nbuffer_load_dwordx4 v[64:67], v0, %[rs], s60 offen offset:1024 sc1\nv_mfma_f32_16x16x32_bf16 v[20:23], a[60:63], v[68:71], v[20:23]\nv_mfma_f32_16x16x32_bf16 v[28:31], a[68:71], v[68:71], v[28:31]\nv_mfma_f32_16x16x32_bf16 v[36:39], a[76:79], v[68:71], v[36:39]\nv_mfma_f32_16x16x32_bf16 v[44:47], a[84:87], v[68:71], v[44:47]\ns_waitcnt vmcnt(12)\n" \
"buffer_load_dwordx4 v[68:71], v0, %[rs], s60 offen offset:3072 sc1\nv_mfma_f32_16x16x32_bf16 v[20:23], a[88:91], v[72:75], v[20:23]\nv_mfma_f32_16x16x32_bf16 v[28:31], a[96:99], v[72:75], v[28:31]\nds_write_b128 v2, v[32:35] offset:4096\nv_mfma_f32_16x16x32_bf16 v[36:39], a[104:107], v[72:75], v[36:39]\ns_waitcnt lgkmcnt(3)\ns_add_u32 s60, s55, 81920\nv_mfma_f32_16x16x32_bf16 v[44:47], v[120:123], v[72:75], v[44:47]\n" \
"s_waitcnt vmcnt(12)\nbuffer_load_dwordx4 v[72:75], v0, %[rs], s60 offen offset:1024 sc1\nv_mfma_f32_16x16x32_bf16 v[20:23], a[92:95], v[76:79], v[20:23]\nds_read_b128 v[120:123], v1 offset:4096\nv_mfma_f32_16x16x32_bf16 v[28:31], a[100:103], v[76:79], v[28:31]\nv_mfma_f32_16x16x32_bf16 v[36:39], a[108:111], v[76:79], v[36:39]\ns_waitcnt lgkmcnt(3)\nv_mfma_f32_16x16x32_bf16 v[44:47], v[124:127], v[76:79], v[44:47]\n" \
"s_waitcnt vmcnt(12)\nbuffer_load_dwordx4 v[76:79], v0, %[rs], s60 offen offset:3072 sc1\nv_mfma_f32_16x16x32_bf16 v[20:23], a[112:115], v[80:83], v[20:23]\nds_read_b128 v[124:127], v1 offset:5120\nv_mfma_f32_16x16x32_bf16 v[28:31], a[120:123], v[80:83], v[28:31]\nds_write_b128 v2, v[40:43] offset:6144\nv_mfma_f32_16x16x32_bf16 v[36:39], a[128:131], v[80:83], v[36:39]\ns_add_u32 s60, s55, 86016\n" \
"v_mfma_f32_16x16x32_bf16 v[44:47], a[136:139], v[80:83], v[44:47]\ns_waitcnt vmcnt(12)\nbuffer_load_dwordx4 v[80:83], v0, %[rs], s60 offen offset:1024 sc1\nv_mfma_f32_16x16x32_bf16 v[20:23], a[116:119], v[84:87], v[20:23]\nds_read_b128 v[112:115], v1 offset:6144\nv_mfma_f32_16x16x32_bf16 v[28:31], a[124:127], v[84:87], v[28:31]\nv_mfma_f32_16x16x32_bf16 v[36:39], a[132:135], v[84:87], v[36:39]\nv_mfma_f32_16x16x32_bf16 v[44:47], a[140:143], v[84:87], v[44:47]\n" \
"s_waitcnt vmcnt(12)\nbuffer_load_dwordx4 v[84:87], v0, %[rs], s60 offen offset:3072 sc1\nv_mfma_f32_16x16x32_bf16 v[20:23], a[144:147], v[88:91], v[20:23]\ns_waitcnt lgkmcnt(3)\nds_read_b128 v[116:119], v1 offset:7168\nv_mfma_f32_16x16x32_bf16 v[28:31], v[120:123], v[88:91], v[28:31]\nv_mfma_f32_16x16x32_bf16 v[36:39], a[152:155], v[88:91], v[36:39]\ns_add_u32 s60, s55, 98304\n" \
"v_mfma_f32_16x16x32_bf16 v[44:47], a[160:163], v[88:91], v[44:47]\ns_waitcnt vmcnt(12)\nbuffer_load_dwordx4 v[88:91], v0, %[rs], s60 offen offset:1024 sc1\nv_mfma_f32_16x16x32_bf16 v[20:23], a[148:151], v[92:95], v[20:23]\ns_waitcnt lgkmcnt(3)\nv_mfma_f32_16x16x32_bf16 v[28:31], v[124:127], v[92:95], v[28:31]\nbuffer_load_dwordx4 v[12:15], v4, %[rs], s41 offen sc1\nv_mfma_f32_16x16x32_bf16 v[36:39], a[156:159], v[92:95], v[36:39]\n" \
"v_mfma_f32_16x16x32_bf16 v[44:47], a[164:167], v[92:95], v[44:47]\ns_waitcnt vmcnt(13)\nbuffer_load_dwordx4 v[92:95], v0, %[rs], s60 offen offset:3072 sc1\nv_mfma_f32_16x16x32_bf16 v[20:23], a[168:171], v[96:99], v[20:23]\nv_mfma_f32_16x16x32_bf16 v[28:31], a[176:179], v[96:99], v[28:31]\ns_waitcnt lgkmcnt(1)\nv_mfma_f32_16x16x32_bf16 v[36:39], v[112:115], v[96:99], v[36:39]\ns_add_u32 s60, s55, 102400\n" \
"v_mfma_f32_16x16x32_bf16 v[44:47], a[184:187], v[96:99], v[44:47]\ns_waitcnt vmcnt(13)\nbuffer_load_dwordx4 v[96:99], v0, %[rs], s60 offen offset:1024 sc1\nv_mfma_f32_16x16x32_bf16 v[20:23], a[172:175], v[100:103], v[20:23]\nds_read_b128 v[112:115], v1 offset:8192\nv_mfma_f32_16x16x32_bf16 v[28:31], a[180:183], v[100:103], v[28:31]\ns_waitcnt lgkmcnt(1)\nv_mfma_f32_16x16x32_bf16 v[36:39], v[116:119], v[100:103], v[36:39]\n" \
"v_mfma_f32_16x16x32_bf16 v[44:47], a[188:191], v[100:103], v[44:47]\ns_waitcnt vmcnt(13)\nbuffer_load_dwordx4 v[100:103], v0, %[rs], s60 offen offset:3072 sc1\nv_mfma_f32_16x16x32_bf16 v[20:23], a[192:195], v[48:51], v[20:23]\nds_read_b128 v[116:119], v1 offset:9216\nv_mfma_f32_16x16x32_bf16 v[28:31], a[200:203], v[48:51], v[28:31]\nv_mfma_f32_16x16x32_bf16 v[36:39], a[208:211], v[48:51], v[36:39]\nv_mfma_f32_16x16x32_bf16 v[44:47], a[216:219], v[48:51], v[44:47]\n" \
"s_waitcnt vmcnt(13)\nv_mfma_f32_16x16x32_bf16 v[20:23], a[196:199], v[52:55], v[20:23]\nv_mfma_f32_16x16x32_bf16 v[28:31], a[204:207], v[52:55], v[28:31]\nv_mfma_f32_16x16x32_bf16 v[36:39], a[212:215], v[52:55], v[36:39]\nv_mfma_f32_16x16x32_bf16 v[44:47], a[220:223], v[52:55], v[44:47]\ns_waitcnt vmcnt(12)\ns_waitcnt lgkmcnt(1)\nv_mfma_f32_16x16x32_bf16 v[20:23], v[112:115], v[56:59], v[20:23]\n" \
"v_mfma_f32_16x16x32_bf16 v[28:31], a[224:227], v[56:59], v[28:31]\nv_mfma_f32_16x16x32_bf16 v[36:39], a[232:235], v[56:59], v[36:39]\nv_mfma_f32_16x16x32_bf16 v[44:47], a[240:243], v[56:59], v[44:47]\ns_waitcnt vmcnt(11)\ns_waitcnt lgkmcnt(0)\nv_mfma_f32_16x16x32_bf16 v[20:23], v[116:119], v[60:63], v[20:23]\nds_read_b128 v[112:115], v1 offset:10240\nv_mfma_f32_16x16x32_bf16 v[28:31], a[228:231], v[60:63], v[28:31]\n" \
"v_mfma_f32_16x16x32_bf16 v[36:39], a[236:239], v[60:63], v[36:39]\nv_mfma_f32_16x16x32_bf16 v[44:47], a[244:247], v[60:63], v[44:47]\ns_waitcnt vmcnt(10)\nv_mfma_f32_16x16x32_bf16 v[20:23], a[248:251], v[64:67], v[20:23]\nds_read_b128 v[116:119], v1 offset:11264\nv_mfma_f32_16x16x32_bf16 v[28:31], v[128:131], v[64:67], v[28:31]\nv_mfma_f32_16x16x32_bf16 v[36:39], v[136:139], v[64:67], v[36:39]\nv_mfma_f32_16x16x32_bf16 v[44:47], v[144:147], v[64:67], v[44:47]\n" \
"s_waitcnt vmcnt(9)\nv_mfma_f32_16x16x32_bf16 v[20:23], a[252:255], v[68:71], v[20:23]\nv_mfma_f32_16x16x32_bf16 v[28:31], v[132:135], v[68:71], v[28:31]\nv_mfma_f32_16x16x32_bf16 v[36:39], v[140:143], v[68:71], v[36:39]\nv_mfma_f32_16x16x32_bf16 v[44:47], v[148:151], v[68:71], v[44:47]\ns_waitcnt vmcnt(8)\nv_mfma_f32_16x16x32_bf16 v[20:23], v[152:155], v[72:75], v[20:23]\nv_mfma_f32_16x16x32_bf16 v[28:31], v[160:163], v[72:75], v[28:31]\n" \
"s_waitcnt lgkmcnt(1)\nv_mfma_f32_16x16x32_bf16 v[36:39], v[112:115], v[72:75], v[36:39]\nv_mfma_f32_16x16x32_bf16 v[44:47], v[168:171], v[72:75], v[44:47]\ns_waitcnt vmcnt(7)\nv_mfma_f32_16x16x32_bf16 v[20:23], v[156:159], v[76:79], v[20:23]\nds_read_b128 v[112:115], v1 offset:12288\nv_mfma_f32_16x16x32_bf16 v[28:31], v[164:167], v[76:79], v[28:31]\ns_waitcnt lgkmcnt(1)\n" \
"v_mfma_f32_16x16x32_bf16 v[36:39], v[116:119], v[76:79], v[36:39]\nv_mfma_f32_16x16x32_bf16 v[44:47], v[172:175], v[76:79], v[44:47]\ns_waitcnt vmcnt(6)\nv_mfma_f32_16x16x32_bf16 v[20:23], v[176:179], v[80:83], v[20:23]\nds_read_b128 v[116:119], v1 offset:13312\nv_mfma_f32_16x16x32_bf16 v[28:31], v[184:187], v[80:83], v[28:31]\nv_mfma_f32_16x16x32_bf16 v[36:39], v[192:195], v[80:83], v[36:39]\nv_mfma_f32_16x16x32_bf16 v[44:47], v[200:203], v[80:83], v[44:47]\n" \
"s_waitcnt vmcnt(5)\nv_mfma_f32_16x16x32_bf16 v[20:23], v[180:183], v[84:87], v[20:23]\nds_read_b128 v[120:123], v1 offset:14336\nv_mfma_f32_16x16x32_bf16 v[28:31], v[188:191], v[84:87], v[28:31]\nv_mfma_f32_16x16x32_bf16 v[36:39], v[196:199], v[84:87], v[36:39]\nv_mfma_f32_16x16x32_bf16 v[44:47], v[204:207], v[84:87], v[44:47]\ns_waitcnt vmcnt(4)\ns_waitcnt lgkmcnt(2)\n" \
"v_mfma_f32_16x16x32_bf16 v[20:23], v[112:115], v[88:91], v[20:23]\nds_read_b128 v[124:127], v1 offset:15360\nv_mfma_f32_16x16x32_bf16 v[28:31], v[208:211], v[88:91], v[28:31]\nv_mfma_f32_16x16x32_bf16 v[36:39], v[216:219], v[88:91], v[36:39]\nv_mfma_f32_16x16x32_bf16 v[44:47], v[224:227], v[88:91], v[44:47]\ns_waitcnt vmcnt(2)\ns_waitcnt lgkmcnt(2)\nv_mfma_f32_16x16x32_bf16 v[20:23], v[116:119], v[92:95], v[20:23]\n" \
"v_mfma_f32_16x16x32_bf16 v[28:31], v[212:215], v[92:95], v[28:31]\nv_mfma_f32_16x16x32_bf16 v[36:39], v[220:223], v[92:95], v[36:39]\nv_mfma_f32_16x16x32_bf16 v[44:47], v[228:231], v[92:95], v[44:47]\ns_waitcnt vmcnt(1)\nv_mfma_f32_16x16x32_bf16 v[20:23], v[232:235], v[96:99], v[20:23]\ns_waitcnt lgkmcnt(1)\nv_mfma_f32_16x16x32_bf16 v[28:31], v[120:123], v[96:99], v[28:31]\nv_mfma_f32_16x16x32_bf16 v[36:39], v[240:243], v[96:99], v[36:39]\n" \
"v_mfma_f32_16x16x32_bf16 v[44:47], v[248:251], v[96:99], v[44:47]\ns_waitcnt vmcnt(0)\nv_mfma_f32_16x16x32_bf16 v[20:23], v[236:239], v[100:103], v[20:23]\ns_waitcnt lgkmcnt(0)\nv_lshlrev_b32 v104, 16, v12\nv_and_b32 v105, 0xffff0000, v12\nv_mfma_f32_16x16x32_bf16 v[28:31], v[124:127], v[100:103], v[28:31]\nv_lshlrev_b32 v106, 16, v13\n" \
"v_and_b32 v107, 0xffff0000, v13\nv_mfma_f32_16x16x32_bf16 v[36:39], v[244:247], v[100:103], v[36:39]\nv_lshlrev_b32 v108, 16, v14\nv_and_b32 v109, 0xffff0000, v14\nv_mfma_f32_16x16x32_bf16 v[44:47], v[252:255], v[100:103], v[44:47]\ns_mul_i32 s44, s40, %[l]\nds_write_b128 v2, v[20:23] offset:1024\nds_write_b128 v2, v[28:31] offset:3072\n" \
"s_nop 0\nds_write_b128 v2, v[36:39] offset:5120\ns_nop 2\nds_write_b128 v2, v[44:47] offset:7168\nv_lshlrev_b32 v110, 16, v15\nv_and_b32 v111, 0xffff0000, v15\nv_mov_b32 v9, s40\ns_cmp_eq_u32 s44, 0\n" \
"s_cselect_b32 s56, %[fd], -2147483648\ns_add_u32 s40, s40, 1\ns_waitcnt lgkmcnt(0)\ns_barrier\nds_read_b128 v[16:19], v3 offset:0\nds_read_b128 v[20:23], v3 offset:256\nds_read_b128 v[24:27], v3 offset:8192\nds_read_b128 v[28:31], v3 offset:8448\n" \
"ds_read_b128 v[32:35], v3 offset:16384\nds_read_b128 v[36:39], v3 offset:16640\nds_read_b128 v[40:43], v3 offset:24576\nds_read_b128 v[44:47], v3 offset:24832\ns_waitcnt lgkmcnt(6)\nv_pk_add_f32 v[16:17], v[16:17], v[104:105]\nv_pk_add_f32 v[18:19], v[18:19], v[106:107]\nv_pk_add_f32 v[20:21], v[20:21], v[108:109]\n" \
"v_pk_add_f32 v[22:23], v[22:23], v[110:111]\ns_waitcnt lgkmcnt(4)\nv_pk_add_f32 v[16:17], v[16:17], v[24:25]\nv_pk_add_f32 v[18:19], v[18:19], v[26:27]\nv_pk_add_f32 v[20:21], v[20:21], v[28:29]\nv_pk_add_f32 v[22:23], v[22:23], v[30:31]\ns_waitcnt lgkmcnt(2)\nv_pk_add_f32 v[16:17], v[16:17], v[32:33]\n" \
"v_pk_add_f32 v[18:19], v[18:19], v[34:35]\nv_pk_add_f32 v[20:21], v[20:21], v[36:37]\nv_pk_add_f32 v[22:23], v[22:23], v[38:39]\ns_waitcnt lgkmcnt(0)\nv_pk_add_f32 v[16:17], v[16:17], v[40:41]\nv_pk_add_f32 v[18:19], v[18:19], v[42:43]\nv_pk_add_f32 v[20:21], v[20:21], v[44:45]\nv_pk_add_f32 v[22:23], v[22:23], v[46:47]\n" \
"v_cvt_pk_bf16_f32 v12, v16, v17\nv_cvt_pk_bf16_f32 v13, v18, v19\nv_cvt_pk_bf16_f32 v14, v20, v21\nv_cvt_pk_bf16_f32 v15, v22, v23\nbuffer_store_dwordx4 v[12:15], v5, %[ro], s42 offen sc1\nv_pk_add_f32 v[24:25], v[16:17], v[18:19]\nv_pk_add_f32 v[26:27], v[20:21], v[22:23]\nv_pk_add_f32 v[24:25], v[24:25], v[26:27]\n" \
"v_add_f32 v24, v24, v25\ns_nop 1\nv_add_f32_dpp v25, v24, v24 row_ror:8 row_mask:0xf bank_mask:0xf\nv_add_f32_dpp v26, v24, v24 row_ror:8 row_mask:0xf bank_mask:0xf\ns_nop 1\nv_permlane16_swap_b32 v25, v26\nv_add_f32 v24, v25, v26\nv_add_f32 v25, v25, v26\n" \
"s_nop 1\nv_permlane32_swap_b32 v24, v25\nv_add_f32 v24, v24, v25\nv_mul_f32 v24, 0x3c800000, v24\nv_pk_add_f32 v[16:17], v[16:17], v[24:25] op_sel_hi:[1,0] neg_lo:[0,1] neg_hi:[0,1]\nv_pk_add_f32 v[18:19], v[18:19], v[24:25] op_sel_hi:[1,0] neg_lo:[0,1] neg_hi:[0,1]\nv_pk_add_f32 v[20:21], v[20:21], v[24:25] op_sel_hi:[1,0] neg_lo:[0,1] neg_hi:[0,1]\nv_pk_add_f32 v[22:23], v[22:23], v[24:25] op_sel_hi:[1,0] neg_lo:[0,1] neg_hi:[0,1]\n" \
"v_pk_mul_f32 v[28:29], v[16:17], v[16:17]\nv_pk_mul_f32 v[30:31], v[18:19], v[18:19]\nv_pk_fma_f32 v[28:29], v[20:21], v[20:21], v[28:29]\nv_pk_fma_f32 v[30:31], v[22:23], v[22:23], v[30:31]\nv_pk_add_f32 v[28:29], v[28:29], v[30:31]\nv_add_f32 v28, v28, v29\ns_nop 1\nv_add_f32_dpp v29, v28, v28 row_ror:8 row_mask:0xf bank_mask:0xf\n" \
"v_add_f32_dpp v30, v28, v28 row_ror:8 row_mask:0xf bank_mask:0xf\ns_nop 1\nv_permlane16_swap_b32 v29, v30\nv_add_f32 v28, v29, v30\nv_add_f32 v29, v29, v30\ns_nop 1\nv_permlane32_swap_b32 v28, v29\nv_add_f32 v28, v28, v29\n" \
"v_mov_b32 v25, v28\nbuffer_store_dwordx2 v[24:25], v6, %[rs], s41 offen sc1\nv_xor_b32 v2, 0x8000, v2\nv_xor_b32 v3, 0x8000, v3\ns_cmp_lt_u32 s40, 64\ns_cbranch_scc1 .Ldd_next%=\ns_waitcnt vmcnt(0)\nbuffer_store_dword v9, off, %[rs], s56 sc1\n" \
"s_branch .Ldd_end%=\n.Ldd_slow%=:\n.Ldd_w1%=:\nv_cmp_gt_i32 vcc, s58, v8\ns_cbranch_vccz .Ldd_w1go%=\ns_cmp_ge_u32 s40, 62\ns_cbranch_scc1 .Ldd_ns1228%=\ns_sleep 8\n" \
".Ldd_ns1228%=:\ns_sleep 1\nbuffer_load_dword v8, v7, %[rs], 0 offen sc1\ns_waitcnt vmcnt(0)\ns_add_u32 s43, s43, 1\ns_cmp_lt_u32 s43, 262144\ns_cbranch_scc1 .Ldd_w1%=\ns_branch .Ldd_abort%=\n" \
".Ldd_w1go%=:\ns_add_u32 s60, s55, 0\nbuffer_load_dwordx4 v[48:51], v0, %[rs], s60 offen offset:1024 sc1\nbuffer_load_dwordx4 v[52:55], v0, %[rs], s60 offen offset:3072 sc1\ns_add_u32 s60, s55, 4096\nbuffer_load_dwordx4 v[56:59], v0, %[rs], s60 offen offset:1024 sc1\nbuffer_load_dwordx4 v[60:63], v0, %[rs], s60 offen offset:3072 sc1\ns_add_u32 s60, s55, 16384\n" \
"buffer_load_dwordx4 v[64:67], v0, %[rs], s60 offen offset:1024 sc1\nbuffer_load_dwordx4 v[68:71], v0, %[rs], s60 offen offset:3072 sc1\ns_add_u32 s60, s55, 20480\nbuffer_load_dwordx4 v[72:75], v0, %[rs], s60 offen offset:1024 sc1\nbuffer_load_dwordx4 v[76:79], v0, %[rs], s60 offen offset:3072 sc1\ns_add_u32 s60, s55, 32768\nbuffer_load_dwordx4 v[80:83], v0, %[rs], s60 offen offset:1024 sc1\nbuffer_load_dwordx4 v[84:87], v0, %[rs], s60 offen offset:3072 sc1\n" \
"s_add_u32 s60, s55, 36864\nbuffer_load_dwordx4 v[88:91], v0, %[rs], s60 offen offset:1024 sc1\nbuffer_load_dwordx4 v[92:95], v0, %[rs], s60 offen offset:3072 sc1\ns_add_u32 s60, s55, 49152\nbuffer_load_dwordx4 v[96:99], v0, %[rs], s60 offen offset:1024 sc1\nbuffer_load_dwordx4 v[100:103], v0, %[rs], s60 offen offset:3072 sc1\ns_branch .Ldd_slowr%=\n.Ldd_abort%=:\n" \
"s_mov_b64 exec, -1\nv_mov_b32 v16, 0\ns_movk_i32 s44, 6400\nbuffer_store_dword v16, off, %[rs], s44 sc1\n.Ldd_end%=:\ns_waitcnt vmcnt(0) lgkmcnt(0)\n" \
""
#define GSW 0
#define PX4 1
#define QX4 7
#define G1F 1
#define A1F 0

#define V10(p, x) p #x "0", p #x "1", p #x "2", p #x "3", p #x "4", p #x "5", p #x "6", p #x "7", p #x "8", p #x "9"
#define V256(p) V10(p, ), V10(p, 1), V10(p, 2), V10(p, 3), V10(p, 4), V10(p, 5), V10(p, 6), V10(p, 7), V10(p, 8), V10(p, 9), V10(p, 10), V10(p, 11), V10(p, 12), V10(p, 13), V10(p, 14), V10(p, 15), V10(p, 16), \
    V10(p, 17), V10(p, 18), V10(p, 19), V10(p, 20), V10(p, 21), V10(p, 22), V10(p, 23), V10(p, 24), p "250", p "251", p "252", p "253", p "254", p "255"
#define CLOB "memory", "vcc", "scc", V256("v"), V256("a"), V10("s", 4), V10("s", 5), V10("s", 6), V10("s", 7), V10("s", 8)
template <int N> DEVI void table(lds_char* p, const unsigned (&t)[N]) {
#pragma unroll
    for (int i = 0; i < N; ++i) *(l1*)(p + i * 256) = t[i];
    WL0;
}
DEVI unsigned prof_at(int cu, int w) { return O_PROF + (262144 + cu * 2560 + w * 8) * 4; }
DEVI unsigned sp_idx(int lane) { return (lane & 2) ? 0xEEEEu : 0x4444u; }
DEVI int sp_row(int r, int hf) { return 4 * (r >> 1) + 2 * hf + (r & 1); }

DEVI void qk_role(const Args& a, const int w, lds_char* smem, int l, int h) {
    const int lane = lane_id(), g = lane >> 4, n = lane & 15, b = n & 3, p = n >> 2;
    const unsigned l0 = (unsigned)(size_t)smem, tb = l0 + w * 12288, lw = l0 + 55296 + w * 24576, ls = l * SD, IMG = l0 + 32768, MIM = l0 + 49152;
    const unsigned xw = (w >> 1) * 16384 + (w & 1) * 4096, oc = (h >> 3) * 16384 + ls, tr = (8 * g + p) * 128 + b * 16;
    const unsigned cm[4] = {4 * g > n ? 0xF149F2CAu : 0u, 4 * g + 1 > n ? 0xF149F2CAu : 0u, 4 * g + 2 > n ? 0xF149F2CAu : 0u, 4 * g + 3 > n ? 0xF149F2CAu : 0u};
    const unsigned t[19] = {
        l0 + lane * 16, lw + lane * 16, l0 + (g >> 1) * 1024 + (g & 1) * 512 + n * 16, O_E1 + ls + xw + lane * 16, sp_idx(lane),
        IMG + (w >> 1) * 8192 + ((w & 1) * 8 + 2 * g) * 512 + n * 16,
        MIM + (w * 8 + 2 * p) * 144 + g * 32 + b * 4,
        IMG + g * 512 + (w ? n * 16 : 8192 + (16 * (b >> 1) + 8 * (p >> 1) + 2 * (p & 1) + (b & 1)) * 16),
        MIM + g * 144 + (n & 7) * 16, MIM + 2304 + g * 144 + (2 * p + (b & 1)) * 16,
        w ? O_PT + (h >> 3) * 32768 + ls + lane * 16 + (w == 1 ? 16384 + ((h & 7) >> 1) * 1024 + (h & 1) * 8 : (h & 7) * 1024)
          : (QX4 & 1) && n >> 3 ? OOB : O_PM + oc + ((h >> 1) & 3) * 1024 + ((2 * (h & 1) + (g >> 1)) * 16 + sp_row(n & 7, g & 1)) * 16 + (n >> 3) * 8,
        w ? (QX4 & 4) && g ? OOB : O_PT + (h >> 3) * 32768 + ls + 20480 + ((h >> 2) & 1) * 2048 + (w - 1) * 1024 + (h & 3) * 256 + n * 16 + g * 4
          : n >> 3 || ((QX4 & 2) && g >> 1) ? OOB : O_PM + oc + 4096 + ((h & 7) >> 1) * 256 + sp_row(n & 7, g & 1) * 16 + (h & 1) * 8 + (g >> 1) * 4,
        w == 3 ? (l ? F_X + SD + p * 128 + (X1F ? 0 : b * 4) : F_X0 + b * 4) : cm[0], cm[1], cm[2], cm[3],
        (lane >> 3) * 8192u + (lane & 7) * 16, tb + tr, lw + tr};
    table(smem + w * 12288 + lane * 4, t);
    asm volatile(QK_ASM : : [ws] "s"(a.ws), [sz] "n"(WS_RS), [rw] "s"(mkrs(w < 2 ? a.Wq : a.Wk, 8u << 20)), [wo] "s"(h * 256 + (w & 1) * 128), [tb] "s"(tb), [lw] "s"(lw), [l0] "s"(l0), [w] "s"(w),
        [xn] "s"(O_XN + ls), [xw] "s"(xw), [fs] "s"(l ? 0 : 128), [sg] "s"(F_Y0 + 4096 + ls + h * 128 + 16), [pr] "s"(prof_at(l * 80 + h, w)) : CLOB, "s36", "s37", "s38", "s39");
    __builtin_amdgcn_endpgm();
}
DEVI void vu_role(const Args& a, const int w, lds_char* smem, int l, int h) {
    const int lane = lane_id(), g = lane >> 4, n = lane & 15, b = n & 3, p = n >> 2;
    const unsigned l0 = (unsigned)(size_t)smem, tb = l0 + w * 12288, lw = l0 + 61440 + w * 24576, ls = l * SD;
    const unsigned xw = (w >> 1) * 16384 + (w & 1) * 4096, tv = (8 * g + p) * 128 + b * 16 + (w & 1) * 64, to = (4 * g + (p & 1)) * 128 + (p >> 1) * 32 + b * 8;
    const unsigned t[18] = {
        l0 + lane * 16, lw + lane * 16, l0 + (g >> 1) * 1024 + (g & 1) * 512 + n * 16, O_E1 + ls + xw + lane * 16, sp_idx(lane),
        l0 + 57344 + (2 * w + (n >> 3)) * 256 + (n >> 3) * 128 + g * 32 + (n & 7) * 2,
        O_UM + ls + (4 * w + (g & 1)) * 65536 + (h >> 3) * 16384 + (g >> 1) * 2048 + (h & 7) * 256 + n * 16,
        sd(O_Y + h * 131072 + ls) + w * 32768 + (g >> 1) * 1024 + (g & 1) * 256 + n * 16,
        l ? F_X + SD + p * 128 + (X1F ? 0 : b * 4) : F_X0 + b * 4, l0 + 49152 + (w >> 1) * 2048 + (2 * (w & 1) + (g >> 1)) * 256 + (n ^ (g >> 1) * 8) * 16 + (g & 1) * 8, sp_idx(lane >> 2), l0 + g * 256 + (g & 1) * 128 + (n & 7) * 16,
        (lane >> 3) * 8192u + (lane & 7) * 16, (8 * (lane >> 5) + ((lane >> 3) & 3)) * 2048u + (lane & 7) * 16, tb + tv - (w & 1) * 12288, lw + tv - (w & 1) * 24576, tb + to, lw + to};
    table(smem + w * 12288 + lane * 4, t);
    asm volatile(VU_ASM : : [ws] "s"(a.ws), [sz] "n"(WS_RS), [rw] "s"(mkrs(a.Wv, 8u << 20)), [ro] "s"(mkrs(a.Wo, 8u << 20)), [wo] "s"(h * 256 + (w >> 1) * 128 + (w & 1) * 262144), [w2] "s"(h * 262144 + w * 512), [tb] "s"(tb), [lw] "s"(lw), [w] "s"(w),
        [xn] "s"(O_XN + ls), [xw] "s"(xw), [fs] "s"(l ? 0 : 128), [xt] "s"(w * 16384 - xw), [fy] "s"(F_Y0 + ls + h * 128 + w * 4), [sg] "s"(w == 1 ? F_Y0 + ls + h * 128 + 16 : OOB), [pr] "s"(prof_at(l * 80 + 32 + h, w) - 128) : CLOB, "s36", "s37", "s38", "s39");
    __builtin_amdgcn_endpgm();
}
template <class F> DEVI void rd_role(const Args& a, const int w, lds_char* smem, int l, int c, F&& idle_work) {
    const int lane = lane_id(), g = lane >> 4, n = lane & 15, r = 2 * (n >> 2) + (n & 1), hf = (n >> 1) & 1;
    const rsrc_t rs = mkrs(a.ws, WS_RS);
    const unsigned l0 = (unsigned)(size_t)smem, tb = l0 + 65536 + w * 4096, ls = l * SD, vp = O_E1 + ls + (c >> 3) * 16384 + (c & 7) * 1024 + w * 256 + n * 16;
    {
        const v4u ff = {SENT, SENT, SENT, SENT};
#pragma unroll
        for (int i = 0; i < 8; ++i) st16(ff, rs, O_UM + ls + c * 65536 + w * 16384 + i * 1024 + lane * 16);
#pragma unroll
        for (int i = 0; i < 2; ++i) st16(ff, rs, O_PT + ls + c * 16384 + w * 2048 + i * 1024 + lane * 16);
        st16(ff, rs, c < 8 ? O_PM + ls + w * 16384 + c * 1024 + lane * 16 : OOB);
        st16(ff, rs, g ? vp + g * 32768 : OOB);
        WV0; BAR;
    }
    st4(g & 1 ? *(const unsigned*)(a.mem0 + r * 1024 + c * 64 + w * 16 + 4 * g + 2 * hf) : nosent(*(const unsigned*)(a.mem0 + r * 1024 + c * 64 + w * 16 + 4 * g + 2 * hf)), rs, g & 1 ? vp + g * 4 : OOB, 0);
    WV0;
    st4(nosent(*(const unsigned*)(a.mem0 + r * 1024 + c * 64 + w * 16 + 4 * g + 2 * hf)), rs, g & 1 ? OOB : vp + g * 4, 0);
    idle_work();
    const int gi = lane >> 3, r8 = lane & 7;
    const unsigned t[12] = {
        (unsigned)lane * 16, sp_idx(lane), l0 + w * 4096 + g * 576 + ((n >> 3) * 16 + hf * 4 + 2 * ((n >> 2) & 1) + (n & 1)) * 4,
        l0 + (r >> 1) * 576 + w * 128 + (g >> 1) * 64 + (r & 1) * 32 + hf * 16,
        PX4 && g >> 1 ? OOB : (vp ^ ((g & 1) * 65536)) + (g >> 1) * 8, g & 1 ? SENT : 0u,
        l0 + 16384 + w * 8192 + lane * 16, l0 + 16384 + ((gi >> 1) * 2 + (w >> 1)) * 1024 + ((gi & 1) * 32 + (w & 1) * 8 + r8) * 16,
        (c >> 1) * 16384u + (c & 1) * 4096 + gi * 512 + (w * 8 + r8) * 16,
        gi ? OOB : O_ST - O_XR + c * 256 + ((w & 1) * 8 + r8) * 16 + (w >> 1) * 8,
        F_Y0 + ls + (w * 8 + r8) * 128 + (c >> 2) * 4, F_Y0 + ls + (gi & 1) * 4096 + (w * 8 + r8) * 128 + 16};
    table(smem + 65536 + w * 4096 + lane * 4, t);
    asm volatile(RD_ASM : : [rs] "s"(rs), [tb] "s"(tb), [su] "s"(O_UM + ls + c * 65536 + w * 16384), [sp] "s"(O_PM + ls + w * 16384), [st] "s"(O_PT + ls + w * 32768),
        [sy] "s"(sd(O_Y + w * (1u << 20) + ls) + (c >> 1) * 16384 + (c & 1) * 4096), [at] "s"(O_XR + ls), [du] "s"((c & 2) * 5 / 2), [fa] "s"(A1F && w ? OOB : F_A + ls + c * 128 + w * 4), [pr] "s"(prof_at(l * 80 + 64 + c, w) + 128) : CLOB);
    __builtin_amdgcn_endpgm();
}
DEVI void gg_role(const Args& a, const int w, lds_char* smem, int l, int gi) {
    const int lane = lane_id(), g = lane >> 4, n = lane & 15, b = n & 3, p = n >> 2;
    const unsigned l0 = (unsigned)(size_t)smem, tb = l0 + w * 12288, lw = l0 + 65536 + w * 24576, ls = l * SD, tv = (8 * g + p) * 128 + b * 16 + (w & 1) * 64, fh = (g >> 1) * 1024 + (g & 1) * 512 + n * 16;
    const unsigned lf = GSW ? 0 : ls, fo = w ? OOB : F_H + (gi / 7) * 1024 + (gi % 7) * 128;
    const unsigned t[9] = {l0 + lane * 16, lw + lane * 16, l0 + fh, w ? OOB : F_A + lf + (lane >> 2) * 128 + (A1F ? 0 : (lane & 3) * 4), w * 2048u + lane * 16, O_ST - O_XR + g * 1024 + n * 16, (lane >> 3) * 7168u + (lane & 7) * 16, tb + tv - (w & 1) * 12288, lw + tv - (w & 1) * 24576};
    table(smem + w * 12288 + lane * 4, t);
    asm volatile(GG_ASM : : [rs] "s"(mkrs(a.ws, WS_RS)), [rw] "s"(mkrs(w & 1 ? a.Wu : a.Wg, 3584 * 2048)), [wo] "s"(gi * 256 + (w >> 1) * 128), [tb] "s"(tb), [lw] "s"(lw), [w] "s"(w),
        [xn0] "s"(O_XR + lf), [xw] "s"((w >> 1) * 16384 + (w & 1) * 4096), [ho0] "s"(O_H + lf + gi * 16384), [fh0] "s"(fo + (GSW ? l * 4 : ls)), [fh1] "s"(fo + ls), [b0] "s"(l ? 0 : GSW), [b1] "s"(l && GSW ? GSW : 64), [pr] "s"(prof_at(160 + l * 28 + gi, w) + 128) : CLOB);
    __builtin_amdgcn_endpgm();
}
DEVI void dd_role(const Args& a, const int w, lds_char* smem, int l, int c) {
    const int lane = lane_id(), g = lane >> 4, b = lane & 3, p = (lane >> 2) & 3, gi = lane >> 3, r8 = lane & 7;
    const unsigned l0 = (unsigned)(size_t)smem, tb = l0 + w * 12288, lw = l0 + 65536 + w * 24576, ls = l * SD, tr = (8 * g + p) * 128 + b * 8;
    const unsigned ta = (c >> 1) * 16384u + (c & 1) * 4096 + gi * 512 + (w * 8 + r8) * 16;
    const rsrc_t rs = mkrs(a.ws, WS_RS);
    const unsigned t[11] = {(unsigned)lane * 16, lw + lane * 16, l0 + w * 8192 + lane * 16, l0 + ((gi >> 1) * 2 + (w >> 1)) * 1024 + ((gi & 1) * 32 + (w & 1) * 8 + r8) * 16,
        ta, l ? (w * 8 + r8) * 2048 + c * 128 + gi * 16 : ta, gi || l ? OOB : O_ST - O_XR + 4096 + c * 256 + (w * 8 + r8) * 8,
        F_H + ls + w * 1024 + ((lane >> 2) % 7) * 128 + (GSW && !l ? 4 : 0), gi * 2048u + r8 * 16, tb + tr, lw + tr};
    table(smem + w * 12288 + lane * 4, t);
    asm volatile(DD_ASM : : [rs] "s"(rs), [rw] "s"(mkrs(a.Wd, 3584 * 2048)), [ro] "s"(mkrs(l ? (char*)a.out : a.ws, l ? 2048 * 2048 : WS_RS)), [wo] "s"(w * (896 * 2048) + c * 128), [tb] "s"(tb), [lw] "s"(lw), [l] "s"(l), [b1] "s"(GSW && !l ? GSW : 99),
        [xn] "s"(O_XR + ls), [ho] "s"(O_H + ls + w * 7 * 16384), [fd] "s"(D1F && w ? OOB : F_D + ls + c * 128 + w * 4), [lt] "s"(F_D + ls + c * 128 + w * 4 + 64), [pr] "s"(prof_at(216 + l * 16 + c, w) + 128) : CLOB);
    __builtin_amdgcn_endpgm();
}

#define PFG 0
#define PFT 1000
#define PFX 0
#define CPF 74
#define CPJ 0
#define LNW 0
#define DLQ 150
#define DLS 400
#define SPL 0
#define PLAN 3
#define PFA 0
#define PFB 3
#define PFL 1
#define VUX 1
#define LNS 40
#define LN1 700
#define DLG 1100
#define DLD 1500
#define DL1 1500
#define DLG1 2600
#define DLD1 3000
DEVI Args args(unsigned sh) {
    auto p = (const __attribute__((address_space(4))) Args*)__builtin_amdgcn_kernarg_segment_ptr();
    asm volatile("" : "+s"(p));
    Args a;
    a.x = p->x; a.mem0 = p->mem0; a.Wq = p->Wq; a.Wk = p->Wk; a.Wv = p->Wv; a.Wo = p->Wo; a.Wg = p->Wg; a.Wu = p->Wu; a.Wd = p->Wd; a.ws = p->ws + sh; a.out = p->out;
    return a;
}
DEVI unsigned beat_wait(unsigned x0) {
    const rsrc_t r0 = mkrs(args(0).ws, 2u << 20);
    for (unsigned spins = 0; spins < SPIN_CAP; ++spins) {
        CB;
        const v2u t = __builtin_amdgcn_raw_buffer_load_b64(r0, 0, O_HB, AUX_SC1);
        if (RFL(t[0]) != x0) return RFL(t[1]) & (2u << 20);
        __builtin_amdgcn_s_sleep(8);
    }
    __builtin_amdgcn_endpgm();
}
#define R2W 0
#define MPF 0
DEVI void mpf(rsrc_t rs, unsigned o) {
    o += O_E1 + lane_id() * 128;
    unsigned spins = 0;
    for (int s = 0; s < NSEG && spins < SPIN_CAP; ++spins) {
        CB;
        if (__ballot(ld4(rs, o, (s & 3) * 32768) == SENT) == 0ull) { ++s; for (int i = 0; i < MPF; ++i) __builtin_amdgcn_s_sleep(8); }
    }
}
DEVI void leader(const Args& a, const int w, lds_char* smem, unsigned x0) {
    const int lane = lane_id();
    const rsrc_t r0 = mkrs(a.ws, 4u << 20);
    unsigned t[2] = {~0u, ~0u};
    for (int i = 0; i < 2 * NPRB; ++i) {
        const unsigned t0 = (unsigned)__builtin_amdgcn_s_memtime();
        WL0;
        if (lane == 0) st4(0u, r0, 0, O_PRB + (i & 1) * SD + w * 1024 + (i >> 1) * 128);
        WV0;
        const unsigned d = (unsigned)__builtin_amdgcn_s_memtime() - t0;
        if (i & 1) t[1] = min(t[1], d); else t[0] = min(t[0], d);
    }
    *(l2*)(smem + w * 8) = v2u{t[0], t[1]};
    WL0; BAR;
    const v4u p = *(l4*)smem, q = *(l4*)(smem + 16);
    const unsigned sh = RFL(min(min(p[0], p[2]), min(q[0], q[2])) < min(min(p[1], p[3]), min(q[1], q[3])) ? 2u << 20 : 0u);
    const unsigned o = sh + w * 1024 + lane * 16;
    const v4u ff = {SENT, SENT, SENT, SENT};
#pragma unroll
    for (int i = 0; i < 8; ++i) st16(ff, r0, o + O_E1 + i * 4096);
#pragma unroll
    for (int i = 1; i < 16; ++i) if (i != 2 && i != 11) st16(ff, r0, o + i * 4096);
    WV0; BAR;
    if (w) { if (MPF && w > 1) mpf(r0, sh + SD + (w & 1) * 16384); return; }
    for (unsigned spins = 0; spins < SPIN_CAP; ++spins) {
        if (lane == 0) st8(v2u{++x0, sh}, r0, 0, O_HB);
        if (__ballot((int)ld4(r0, sh + F_D + SD + (lane >> 2) * 128 + (D1F ? 0 : (lane & 3) * 4), 0) < 0) == 0ull) break;
        __builtin_amdgcn_s_sleep(16);
    }
}
__global__ __launch_bounds__(256, 1) void sys_kernel(Args) {
    extern __shared__ __attribute__((aligned(16))) char smem_[];
    lds_char* smem = (lds_char*)smem_;
    const int x = blockIdx.x & 7, j = blockIdx.x >> 3, wv = RFL(threadIdx.x >> 6), l = x >> 2, xx = x & 3;
    const unsigned t0 = (unsigned)__builtin_amdgcn_s_memrealtime();
    auto until = [&](unsigned dl) AI { while ((unsigned)__builtin_amdgcn_s_memrealtime() - t0 < dl) __builtin_amdgcn_s_sleep(8); };
    unsigned x0 = 0;
    if (l || j >= 16) x0 = RFL(ld4(mkrs(args(0).ws, 2u << 20), 0, O_HB));
    if (j < 16) {
        if (l) { until(j ? LN1 + j * LNS : 0); ln0(args(0), wv, j * 4 + xx, [&]() AI { return beat_wait(x0); }); until(DL1); }
        if (DLQ && !l && j < 8) until(DLQ);
        if (j < 8) qk_role(args(0), wv, smem, l, xx * 8 + j); else vu_role(args(0), wv, smem, l, (xx ^ VUX) * 8 + j - 8);
        return;
    }
    if (j >= 16 + CPJ && j < 20 + CPJ) {
        unsigned long long pc; unsigned n;
        asm volatile("s_getpc_b64 %0\n\ts_mov_b32 %1, .-_Z10sys_kernel4Args" : "=s"(pc), "=s"(n));
        const rsrc_t rc = mkrs((const void*)(pc - n), CPF * 1024);
        __builtin_amdgcn_raw_ptr_buffer_load_lds(rc, smem + LDSB - 1024 + wv * 256, 4, lane_id() * 128, ((j - 16 - CPJ) * 4 + wv) * 8192, 0, 0);
    }
    {
        const int lane = lane_id(), q = (j - 16) * 4 + wv, m = SPL && !l ? ((xx & 1) != (SPL & 1)) * 2 + (q >> 5) : q >> 4;
        const Args a = args(0);
        const u16 *W0 = a.Wq, *W1 = a.Wk, *W2 = a.Wv, *W3 = a.Wo;
        asm volatile("" : "+s"(W0), "+s"(W1), "+s"(W2), "+s"(W3));
        const u16* W = m == 0 ? W0 : m == 1 ? W1 : m == 2 ? W2 : W3;
        const rsrc_t rw = mkrs(W, 8u << 20);
        if (l) {
#pragma unroll
            for (int i = 0; i < PFA; ++i) __builtin_amdgcn_raw_ptr_buffer_load_lds(rw, smem + LDSB - 1024 + wv * 256, 4, lane * 256, i * (1u << 20) + (xx * 16 + (q & 15)) * 16384, 0, 0);
        } else {
            const unsigned po = (0x100u - (unsigned)(size_t)W) & 0x300u, hx = SPL ? (xx & 2) + ((q >> 4) & 1) : m < 2 ? xx : xx ^ VUX;
            const unsigned vo = m == 3 ? (lane >> 1) * 1024 + (lane & 1) * 128 : (lane >> 2) * 8192 + ((lane >> 1) & 1) * 1024 + (lane & 1) * 128;
            const unsigned so = po + (m == 3 ? hx * (2u << 20) + (q & 15) * 32768 : hx * 2048 + (q & 15) * 131072);
#pragma unroll
            for (int i = 0; i < PFB; ++i) __builtin_amdgcn_raw_ptr_buffer_load_lds(rw, smem + LDSB - 1024 + wv * 256, 4, vo, so + i * (m == 3 ? 524288 : 2u << 20), 0, 0);
            const unsigned pl = (0x1F00u - (unsigned)(size_t)W) & 0x1F00u;
#pragma unroll
            for (int i = 0; i < PFL; ++i) __builtin_amdgcn_raw_ptr_buffer_load_lds(rw, smem + LDSB - 1024 + wv * 256, 4, (lane >> 1) * 8192 + (lane & 1) * 128, (pl >> 11) == hx && m < 3 ? pl + ((q & 15) + 16 * i) * 262144 : OOB, 0, 0);
        }
    }
    int ro, ix;
    if ((xx & 1) == (PLAN & 1)) { if (j < 24) { ro = 0; ix = (xx >> 1) * 8 + j - 16; } else if (j < 26) { ro = 3; ix = (xx >> 1) * 2 + j - 24; } else { ro = 1; ix = (xx >> 1) * 14 + j - 26; } }
    else if (j < 24) { ro = 1; ix = (xx >> 1) * 14 + j - 10; } else { ro = 2; ix = (xx >> 1) * 8 + j - 24; }
    if (ro == 3 && l) { if (ix == 0) leader(args(0), wv, smem, x0); else if (MPF && wv > 1) mpf(mkrs(args(0).ws, 4u << 20), beat_wait(x0) + SD + (wv & 1) * 16384); return; }
    const unsigned sh = beat_wait(x0);
    auto pfg = [&]() AI {
        if (!PFG || l) return;
        until(PFT);
        const Args a = args(0);
        const int lane = lane_id(), q = (ix & 7) * 4 + wv;
        const rsrc_t rw = mkrs(q & 1 ? a.Wu : a.Wg, 3584 * 2048);
        const unsigned vo = (lane & 15) < ((xx & 1) ? 12 : 16) ? (lane >> 4) * 7168 + (lane & 15) * 128 : OOB, so = ((xx >> 1) * 14 + ((xx & 1) ? 0 : 6)) * 256 + (1024 - 64 * PFG + (q >> 1) * 4) * 7168;
#pragma unroll
        for (int i = 0; i < PFG; ++i) __builtin_amdgcn_raw_ptr_buffer_load_lds(rw, smem + LDSB - 1024 + wv * 256, 4, vo, so + i * 64 * 7168, 0, PFX);
    };
    if (ro == 0) { rd_role(args(sh), wv, smem, l, ix, pfg); return; }
    if (ro == 1) { const Args a = args(sh); until((l ? DLG1 : DLG) - (((((unsigned)(size_t)a.Wg >> 8) + ix) & 3) == 1 || ((((unsigned)(size_t)a.Wu >> 8) + ix) & 3) == 1 ? DLS : 0)); WV0; BAR; gg_role(a, wv, smem, l, ix); return; }
    if (ro == 2) { pfg(); const Args a = args(sh); until((l ? DLD1 : DLD) - (((((unsigned)(size_t)a.Wd >> 8) + (ix >> 1)) & 3) == 1 ? DLS + 200 : 0)); WV0; BAR; dd_role(a, wv, smem, l, ix); return; }
    r_role(args(sh), wv, ix);
}

extern "C" void kernel_launch(void* const* d_in, const int* in_sizes, int n_in, void* d_out, int out_size, void* d_ws, size_t ws_size, hipStream_t stream) {
    (void)in_sizes; (void)n_in; (void)out_size; (void)ws_size;
    Args a;
    a.x = (const u16*)d_in[0]; a.mem0 = (const u16*)d_in[1]; a.Wq = (const u16*)d_in[2]; a.Wk = (const u16*)d_in[3]; a.Wv = (const u16*)d_in[4]; a.Wo = (const u16*)d_in[5];
    a.Wg = (const u16*)d_in[8]; a.Wu = (const u16*)d_in[9]; a.Wd = (const u16*)d_in[10];
    a.ws = (char*)(((uintptr_t)d_ws + (2u << 20) - 1) & ~(uintptr_t)((2u << 20) - 1)); a.out = (u16*)d_out;
    (void)hipFuncSetAttribute((const void*)sys_kernel, hipFuncAttributeMaxDynamicSharedMemorySize, LDSB);
    hipLaunchKernelGGL(sys_kernel, dim3(256), dim3(256), LDSB, stream, a);
}
```

```cpp
#include <hip/hip_runtime.h>
#include <stdint.h>

typedef unsigned short u16;
typedef unsigned long long u64;
typedef __attribute__((ext_vector_type(8))) short bf16x8;
typedef __attribute__((ext_vector_type(4))) short s16x4;
typedef __attribute__((ext_vector_type(4))) float f32x4;
typedef __attribute__((ext_vector_type(16))) float f32x16;
typedef __attribute__((ext_vector_type(2))) float f32x2;
typedef __attribute__((ext_vector_type(4))) unsigned v4u;
typedef __attribute__((ext_vector_type(2))) unsigned v2u;
typedef __amdgpu_buffer_rsrc_t rsrc_t;
typedef __attribute__((address_space(3))) char lds_char;
typedef __attribute__((address_space(3))) bf16x8 l8;
typedef __attribute__((address_space(3))) v4u l4;
typedef __attribute__((address_space(3))) v2u l2;
typedef __attribute__((address_space(3))) unsigned l1;
typedef __attribute__((address_space(3))) f32x4 lf4;
typedef __attribute__((address_space(3))) float lf1;
#define DEVI __device__ __forceinline__
#define AI __attribute__((always_inline))

constexpr int NSEG = 64;
constexpr float ATT_SCALE = 0.08838834764831845f;
constexpr float LOG2E = 1.4426950408889634f;
#define NPRB 4
constexpr unsigned SD = 8192;
DEVI unsigned sd(unsigned o) { return o ^ ((o >> 8) & SD); }
constexpr unsigned F_X0 = 32768;
constexpr unsigned F_X = 4096;
constexpr unsigned F_A = 16384;
constexpr unsigned F_H = F_A + 2048;
constexpr unsigned F_D = F_H + 4096;
constexpr unsigned F_Y0 = 49152;
constexpr unsigned F_ABORT = 6400;
constexpr unsigned O_PM = 65536;
constexpr unsigned O_E1 = 131072;
constexpr unsigned O_PT = 262144;
constexpr unsigned O_UM = 524288;
constexpr unsigned CTRL_END = O_UM + (1u << 20);
constexpr unsigned O_PRB = 0x1F0000, O_HB = 0x1FFF00;
constexpr unsigned O_XN = 4u << 20;
constexpr unsigned O_XR = O_XN + (8u << 20);
constexpr unsigned O_ST = O_XR + (8u << 20);
constexpr unsigned O_H = O_ST + (8u << 20);
constexpr unsigned O_Y = O_H + (36u << 20);
constexpr unsigned WS_NEED = O_Y + (8u << 20);
constexpr unsigned O_PROF = WS_NEED;
constexpr unsigned WS_RS = WS_NEED;
static_assert(CTRL_END <= O_PRB && CTRL_END % 16384 == 0 && O_Y % (8u << 20) == 0, "workspace");
constexpr unsigned OOB = 0x80000000u, OOB2 = 0x90000000u, SENT = 0xFFFFFFFFu;

struct Args { const u16 *x, *mem0, *Wq, *Wk, *Wv, *Wo, *Wg, *Wu, *Wd; char* ws; u16* out; };

typedef __attribute__((ext_vector_type(2))) __bf16 bf16x2_t;
DEVI unsigned pack2(float lo, float hi) { f32x2 v = {lo, hi}; bf16x2_t b = __builtin_convertvector(v, bf16x2_t); return __builtin_bit_cast(unsigned, b); }
DEVI float bflo(unsigned u) { return __uint_as_float(u << 16); }
DEVI float bfhi(unsigned u) { return __uint_as_float(u & 0xffff0000u); }
DEVI float rbf(float x) { return bflo(pack2(x, x)); }
DEVI bf16x8 as8(v4u v) { return __builtin_bit_cast(bf16x8, v); }
DEVI f32x4 mfma16(bf16x8 a, bf16x8 b, f32x4 c) { return __builtin_amdgcn_mfma_f32_16x16x32_bf16(a, b, c, 0, 0, 0); }
DEVI f32x16 mfma32(bf16x8 a, bf16x8 b, f32x16 c) { return __builtin_amdgcn_mfma_f32_32x32x16_bf16(a, b, c, 0, 0, 0); }
constexpr int AUX_SC1 = 16;
DEVI v4u ld16(rsrc_t r, unsigned vo, unsigned so) { return __builtin_amdgcn_raw_buffer_load_b128(r, vo, so, AUX_SC1); }
DEVI unsigned ld4(rsrc_t r, unsigned vo, unsigned so) { return __builtin_amdgcn_raw_buffer_load_b32(r, vo, so, AUX_SC1); }
DEVI void st16(v4u v, rsrc_t r, unsigned vo) { __builtin_amdgcn_raw_buffer_store_b128(v, r, vo, 0, AUX_SC1); }
DEVI void st8(v2u v, rsrc_t r, unsigned vo, unsigned so) { __builtin_amdgcn_raw_buffer_store_b64(v, r, vo, so, AUX_SC1); }
DEVI void st4(unsigned v, rsrc_t r, unsigned vo, unsigned so) { __builtin_amdgcn_raw_buffer_store_b32(v, r, vo, so, AUX_SC1); }
DEVI v2u trd(lds_char* p) { return __builtin_bit_cast(v2u, __builtin_amdgcn_ds_read_tr16_b64_v4i16((__attribute__((address_space(3))) s16x4*)p)); }
template <int O1, int O2> DEVI void tra(v2u& lo, v2u& hi, unsigned a) { asm volatile("ds_read_b64_tr_b16 %0, %2 offset:%3\n\tds_read_b64_tr_b16 %1, %2 offset:%4" : "=&v"(lo), "=&v"(hi) : "v"(a), "n"(O1), "n"(O2) : "memory"); }
DEVI void trw(v2u& a, v2u& b, v2u& c, v2u& d) { asm volatile("s_waitcnt lgkmcnt(0)" : "+v"(a), "+v"(b), "+v"(c), "+v"(d)::"memory"); }
DEVI rsrc_t mkrs(const void* p, unsigned n) { return __builtin_amdgcn_make_buffer_rsrc((void*)p, 0, (int)n, 0x00020000); }
#define CB asm volatile("" ::: "memory")
#define SB __builtin_amdgcn_sched_barrier(0)
#define WL0 asm volatile("s_waitcnt lgkmcnt(0)" ::: "memory")
#define WV0 asm volatile("s_waitcnt vmcnt(0)" ::: "memory")
#define WV(n) asm volatile("s_waitcnt vmcnt(%0)" ::"n"(n) : "memory")
#define BAR do { __builtin_amdgcn_s_barrier(); CB; } while (0)
#define RFL(x) __builtin_amdgcn_readfirstlane(x)
DEVI int lane_id() { int x; asm volatile("v_mbcnt_lo_u32_b32 %0, -1, 0\n\tv_mbcnt_hi_u32_b32 %0, -1, %0" : "=v"(x)); return x; }
template <int I> struct IC { static constexpr int v = I; };
template <int B, int E_, class F> DEVI void sfor(F&& f) { if constexpr (B < E_) { f(IC<B>{}); sfor<B + 1, E_>(f); } }
DEVI float red_x32(float x, bool mx) { unsigned u = __float_as_uint(x); auto r = __builtin_amdgcn_permlane32_swap(u, u, false, false); float a = __uint_as_float(r[0]), b = __uint_as_float(r[1]); return mx ? fmaxf(a, b) : a + b; }
DEVI float red_x16(float x, bool mx) { unsigned u = __float_as_uint(x); auto r = __builtin_amdgcn_permlane16_swap(u, u, false, false); float a = __uint_as_float(r[0]), b = __uint_as_float(r[1]); return mx ? fmaxf(a, b) : a + b; }
DEVI unsigned vmax4(v4u d) { return max(max(d[0], d[1]), max(d[2], d[3])); }
DEVI unsigned nosent(unsigned v) { return min(v, SENT - 1u); }
constexpr unsigned SPIN_CAP = 1u << 18;
DEVI bool spin_bail(unsigned& spins, rsrc_t rs) {
    if (++spins > SPIN_CAP) { st4(0u, rs, 0, F_ABORT); return true; }
    return (spins & 255u) == 0u && RFL(ld4(rs, 0, F_ABORT)) != SENT;
}
#define TSTAMP(idx)
constexpr int LDSB = 160 * 1024;
DEVI void idle(unsigned dl) { const unsigned t0 = (unsigned)__builtin_amdgcn_s_memrealtime(); while ((unsigned)__builtin_amdgcn_s_memrealtime() - t0 < dl) __builtin_amdgcn_s_sleep(8); }
#define RXE 0
#define D1F 0
#define X1F 0

DEVI float red_rows(float s) {
    s += __uint_as_float(__builtin_amdgcn_update_dpp(0, __float_as_uint(s), 0x128, 0xf, 0xf, false));
    s = red_x16(s, false); return red_x32(s, false);
}
DEVI f32x2 up2(unsigned u) { return f32x2{bflo(u), bfhi(u)}; }
DEVI float ln_inv(float var) { return __builtin_amdgcn_rcpf(rbf(__builtin_amdgcn_sqrtf(rbf(rbf(fmaxf(var, 0.f)) + 1e-5f)))); }
template <class F> DEVI void ln0(const Args& a, const int w, int b, F&& base) {
    const int lane = lane_id(), row = w * 8 + (lane & 7);
    const rsrc_t rx = mkrs(a.x, 2048 * 2048);
    v4u u[16];
#pragma unroll
    for (int i = 0; i < 16; ++i) u[i] = __builtin_amdgcn_raw_buffer_load_b128(rx, row * 2048 + (lane >> 3) * 16 + i * 128, b * 65536, 0);
    const float k0 = __shfl(bflo(u[0][0]), lane & 7);
    f32x2 s1 = {0.f, 0.f}, s2 = {0.f, 0.f};
#pragma unroll
    for (int i = 0; i < 16; ++i)
#pragma unroll
        for (int e = 0; e < 4; ++e) { const f32x2 d = up2(u[i][e]) - k0; s1 += d; s2 += d * d; }
    const float s = red_rows(s1[0] + s1[1]) * (1.0f / 1024.0f), q = red_rows(s2[0] + s2[1]) * (1.0f / 1024.0f);
    float inv = ln_inv(q - s * s), c = -rbf(k0 + s) * inv;
    asm volatile("" : "+v"(inv), "+v"(c));
    const rsrc_t rs = mkrs(a.ws + base(), WS_RS);
    const unsigned o = sd(O_XN + b * 131072) + (lane >> 3) * 512 + row * 16;
#pragma unroll
    for (int i = 0; i < 16; ++i) {
        v4u r;
#pragma unroll
        for (int e = 0; e < 4; ++e) { const f32x2 z = up2(u[i][e]) * inv + c; r[e] = pack2(z[0], z[1]); }
        st16(r, rs, o + (i >> 1) * 16384 + (i & 1) * 4096);
    }
#pragma unroll
    for (int i = 0; i < 16; ++i) st16(u[i], rs, o + (O_XR - O_XN) + (i >> 1) * 16384 + (i & 1) * 4096);
    st16(u[0], rs, sd(O_ST + b * 131072) + w * 1024 + lane * 16); st16(u[0], rs, sd(O_ST + b * 131072) + 4096 + w * 1024 + lane * 16); st16(u[0], rs, sd(O_ST + SD + b * 131072) + w * 1024 + lane * 16);
    WV0;
    st4(b, rs, 0, F_X0 + b * 128 + w * 4);
}
#define RSLP 8
DEVI void r_role(const Args& a, const int w, int rq) {
    const int lane = lane_id(), row = (w >> 1) * 16 + (rq & 1) * 8 + (lane & 7), cq = (rq >> 1) * 2 + (w & 1);
    const rsrc_t rs = mkrs(a.ws, WS_RS);
    const unsigned fo = cq * 32768 + (lane >> 3) * 512 + row * 16, so = O_ST - O_XR + 4096 + (lane >> 3) * 512 + row * 8;
    auto io = [](int i) -> unsigned { return (i >> 1) * 16384 + (i & 1) * 4096; };
    const unsigned fd = F_D + (lane & 15) * 128 + (D1F ? 0 : (row >> 3) * 4);
    for (int b = 0; b < NSEG; ++b) {
        unsigned spins = 0;
        for (;;) {
            CB;
            if (__ballot((int)ld4(rs, fd, 0) < b) == 0ull) break;
            if (spin_bail(spins, rs)) return;
            __builtin_amdgcn_s_sleep(RSLP);
        }
        const unsigned sx = sd(O_XR + b * 131072);
        const v2u s0 = __builtin_amdgcn_raw_buffer_load_b64(rs, so, sx, AUX_SC1), s1 = __builtin_amdgcn_raw_buffer_load_b64(rs, so + 256, sx, AUX_SC1);
        v4u u[4];
#pragma unroll
        for (int i = 0; i < 4; ++i) u[i] = ld16(rs, fo + io(i), sx);
        const float m0 = __uint_as_float(s0[0]), m1 = __uint_as_float(s1[0]), mean = red_rows(m0 + m1) * 0.0625f;
        const float var = red_rows(__uint_as_float(s0[1]) + __uint_as_float(s1[1]) + 64.f * ((m0 - mean) * (m0 - mean) + (m1 - mean) * (m1 - mean))) * (1.0f / 1024.0f);
        const float inv = ln_inv(var), c = -rbf(mean) * inv;
#pragma unroll
        for (int i = 0; i < 4; ++i) {
            v4u o;
#pragma unroll
            for (int e = 0; e < 4; ++e) { const f32x2 z = up2(u[i][e]) * inv + c; o[e] = pack2(z[0], z[1]); }
            st16(o, rs, fo + io(i) + ((sx ^ SD) - (O_XR - O_XN)));
        }
        CB;
#pragma unroll
        for (int i = 0; i < 4; ++i) st16(u[i], rs, fo + io(i) + (sx ^ SD));
        WV0;
        if (X1F) BAR;
        st4(b, rs, 0, X1F && w ? OOB : F_X + SD + rq * 128 + w * 4);
        if (rq == 0 && w == 0) TSTAMP(200000 + 64 + b);
    }
}
DEVI void r_role2(const Args& a, const int w, int rq) {
    const int lane = lane_id(), row = w * 16 + (rq & 1) * 8 + (lane & 7), cq = (rq >> 1) * 2;
    const rsrc_t rs = mkrs(a.ws, WS_RS);
    const unsigned fo = cq * 32768 + (lane >> 3) * 512 + row * 16, so = O_ST - O_XR + 4096 + (lane >> 3) * 512 + row * 8;
    auto io = [](int i) -> unsigned { return (i >> 1) * 16384 + (i & 1) * 4096; };
    const unsigned fd = F_D + (lane & 15) * 128 + (D1F ? 0 : (row >> 3) * 4);
    for (int b = 0; b < NSEG; ++b) {
        unsigned spins = 0;
        for (;;) {
            CB;
            if (__ballot((int)ld4(rs, fd, 0) < b) == 0ull) break;
            if (spin_bail(spins, rs)) return;
            __builtin_amdgcn_s_sleep(RSLP);
        }
        const unsigned sx = sd(O_XR + b * 131072);
        const v2u s0 = __builtin_amdgcn_raw_buffer_load_b64(rs, so, sx, AUX_SC1), s1 = __builtin_amdgcn_raw_buffer_load_b64(rs, so + 256, sx, AUX_SC1);
        v4u u[8];
#pragma unroll
        for (int i = 0; i < 8; ++i) u[i] = ld16(rs, fo + io(i), sx);
        const float m0 = __uint_as_float(s0[0]), m1 = __uint_as_float(s1[0]), mean = red_rows(m0 + m1) * 0.0625f;
        const float var = red_rows(__uint_as_float(s0[1]) + __uint_as_float(s1[1]) + 64.f * ((m0 - mean) * (m0 - mean) + (m1 - mean) * (m1 - mean))) * (1.0f / 1024.0f);
        const float inv = ln_inv(var), c = -rbf(mean) * inv;
#pragma unroll
        for (int i = 0; i < 8; ++i) {
            v4u o;
#pragma unroll
            for (int e = 0; e < 4; ++e) { const f32x2 z = up2(u[i][e]) * inv + c; o[e] = pack2(z[0], z[1]); }
            st16(o, rs, fo + io(i) + ((sx ^ SD) - (O_XR - O_XN)));
        }
        CB;
#pragma unroll
        for (int i = 0; i < 8; ++i) st16(u[i], rs, fo + io(i) + (sx ^ SD));
        WV0;
        if (X1F) BAR;
        st8(v2u{(unsigned)b, (unsigned)b}, rs, 0, F_X + SD + rq * 128 + w * 8);
        if (rq == 0 && w == 0) TSTAMP(200000 + 64 + b);
    }
}
#define QK_ASM \
"v_mbcnt_lo_u32_b32 v255, -1, 0\nv_mbcnt_hi_u32_b32 v255, -1, v255\nv_lshlrev_b32 v255, 2, v255\nv_add_u32 v255, %[tb], v255\nds_read_b32 v0, v255 offset:0\nds_read_b32 v1, v255 offset:256\nds_read_b32 v2, v255 offset:512\nds_read_b32 v3, v255 offset:768\n" \
"ds_read_b32 v4, v255 offset:1024\nds_read_b32 v5, v255 offset:1280\nds_read_b32 v6, v255 offset:1536\nds_read_b32 v7, v255 offset:1792\nds_read_b32 v8, v255 offset:2048\nds_read_b32 v9, v255 offset:2304\nds_read_b32 v10, v255 offset:2560\nds_read_b32 v11, v255 offset:2816\n" \
"ds_read_b32 v12, v255 offset:3072\nds_read_b32 v13, v255 offset:3328\nds_read_b32 v14, v255 offset:3584\nds_read_b32 v15, v255 offset:3840\nds_read_b32 v92, v255 offset:4096\nds_read_b32 v93, v255 offset:4352\nds_read_b32 v94, v255 offset:4608\ns_waitcnt lgkmcnt(0)\n" \
"s_mov_b64 s[36:37], %[ws]\ns_mov_b32 s38, %[sz]\ns_mov_b32 s39, 0x20000\ns_mov_b32 s45, 2096896\nbuffer_load_dword v95, off, s[36:39], s45 sc1\ns_add_u32 m0, %[lw], 16384\ns_add_u32 s44, %[wo], 0\nbuffer_load_dwordx4 v92, %[rw], s44 offen nt lds\n" \
"s_add_u32 m0, %[lw], 17408\ns_add_u32 s44, %[wo], 65536\nbuffer_load_dwordx4 v92, %[rw], s44 offen nt lds\ns_add_u32 m0, %[lw], 18432\ns_add_u32 s44, %[wo], 131072\nbuffer_load_dwordx4 v92, %[rw], s44 offen nt lds\ns_add_u32 m0, %[lw], 19456\ns_add_u32 s44, %[wo], 196608\n" \
"buffer_load_dwordx4 v92, %[rw], s44 offen nt lds\ns_add_u32 m0, %[lw], 20480\ns_add_u32 s44, %[wo], 262144\nbuffer_load_dwordx4 v92, %[rw], s44 offen nt lds\ns_add_u32 m0, %[lw], 21504\ns_add_u32 s44, %[wo], 327680\nbuffer_load_dwordx4 v92, %[rw], s44 offen nt lds\ns_add_u32 m0, %[lw], 22528\n" \
"s_add_u32 s44, %[wo], 393216\nbuffer_load_dwordx4 v92, %[rw], s44 offen nt lds\ns_add_u32 m0, %[lw], 23552\ns_add_u32 s44, %[wo], 458752\nbuffer_load_dwordx4 v92, %[rw], s44 offen nt lds\ns_add_u32 m0, %[tb], 0\ns_add_u32 s44, %[wo], 524288\nbuffer_load_dwordx4 v92, %[rw], s44 offen nt lds\n" \
"s_add_u32 m0, %[tb], 1024\ns_add_u32 s44, %[wo], 589824\nbuffer_load_dwordx4 v92, %[rw], s44 offen nt lds\ns_add_u32 m0, %[tb], 2048\ns_add_u32 s44, %[wo], 655360\nbuffer_load_dwordx4 v92, %[rw], s44 offen nt lds\ns_add_u32 m0, %[tb], 3072\ns_add_u32 s44, %[wo], 720896\n" \
"buffer_load_dwordx4 v92, %[rw], s44 offen nt lds\ns_add_u32 m0, %[tb], 4096\ns_add_u32 s44, %[wo], 786432\nbuffer_load_dwordx4 v92, %[rw], s44 offen nt lds\ns_add_u32 m0, %[tb], 5120\ns_add_u32 s44, %[wo], 851968\nbuffer_load_dwordx4 v92, %[rw], s44 offen nt lds\ns_add_u32 m0, %[tb], 6144\n" \
"s_add_u32 s44, %[wo], 917504\nbuffer_load_dwordx4 v92, %[rw], s44 offen nt lds\ns_add_u32 m0, %[tb], 7168\ns_add_u32 s44, %[wo], 983040\nbuffer_load_dwordx4 v92, %[rw], s44 offen nt lds\ns_add_u32 m0, %[tb], 8192\ns_add_u32 s44, %[wo], 1048576\nbuffer_load_dwordx4 v92, %[rw], s44 offen nt lds\n" \
"s_add_u32 m0, %[tb], 9216\ns_add_u32 s44, %[wo], 1114112\nbuffer_load_dwordx4 v92, %[rw], s44 offen nt lds\ns_add_u32 m0, %[tb], 10240\ns_add_u32 s44, %[wo], 1179648\nbuffer_load_dwordx4 v92, %[rw], s44 offen nt lds\ns_add_u32 m0, %[tb], 11264\ns_add_u32 s44, %[wo], 1245184\n" \
"buffer_load_dwordx4 v92, %[rw], s44 offen nt lds\ns_add_u32 m0, %[lw], 0\ns_add_u32 s44, %[wo], 1310720\nbuffer_load_dwordx4 v92, %[rw], s44 offen nt lds\ns_add_u32 m0, %[lw], 1024\ns_add_u32 s44, %[wo], 1376256\nbuffer_load_dwordx4 v92, %[rw], s44 offen nt lds\ns_add_u32 m0, %[lw], 2048\n" \
"s_add_u32 s44, %[wo], 1441792\nbuffer_load_dwordx4 v92, %[rw], s44 offen nt lds\ns_add_u32 m0, %[lw], 3072\ns_add_u32 s44, %[wo], 1507328\nbuffer_load_dwordx4 v92, %[rw], s44 offen nt lds\ns_add_u32 m0, %[lw], 4096\ns_add_u32 s44, %[wo], 1572864\nbuffer_load_dwordx4 v92, %[rw], s44 offen nt lds\n" \
"s_add_u32 m0, %[lw], 5120\ns_add_u32 s44, %[wo], 1638400\nbuffer_load_dwordx4 v92, %[rw], s44 offen nt lds\ns_add_u32 m0, %[lw], 6144\ns_add_u32 s44, %[wo], 1703936\nbuffer_load_dwordx4 v92, %[rw], s44 offen nt lds\ns_add_u32 m0, %[lw], 7168\ns_add_u32 s44, %[wo], 1769472\n" \
"buffer_load_dwordx4 v92, %[rw], s44 offen nt lds\ns_add_u32 m0, %[lw], 8192\ns_add_u32 s44, %[wo], 1835008\nbuffer_load_dwordx4 v92, %[rw], s44 offen nt lds\ns_add_u32 m0, %[lw], 9216\ns_add_u32 s44, %[wo], 1900544\nbuffer_load_dwordx4 v92, %[rw], s44 offen nt lds\ns_add_u32 m0, %[lw], 10240\n" \
"s_add_u32 s44, %[wo], 1966080\nbuffer_load_dwordx4 v92, %[rw], s44 offen nt lds\ns_add_u32 m0, %[lw], 11264\ns_add_u32 s44, %[wo], 2031616\nbuffer_load_dwordx4 v92, %[rw], s44 offen nt lds\ns_add_u32 m0, %[lw], 12288\ns_add_u32 s44, %[wo], 2097152\nbuffer_load_dwordx4 v92, %[rw], s44 offen nt lds\n" \
"s_add_u32 m0, %[lw], 13312\ns_add_u32 s44, %[wo], 2162688\nbuffer_load_dwordx4 v92, %[rw], s44 offen nt lds\ns_add_u32 m0, %[lw], 14336\ns_add_u32 s44, %[wo], 2228224\nbuffer_load_dwordx4 v92, %[rw], s44 offen nt lds\ns_add_u32 m0, %[lw], 15360\ns_add_u32 s44, %[wo], 2293760\n" \
"buffer_load_dwordx4 v92, %[rw], s44 offen nt lds\ns_waitcnt vmcnt(32)\nds_read_b64_tr_b16 a[0:1], v94 offset:16384\nds_read_b64_tr_b16 a[2:3], v94 offset:16896\nds_read_b64_tr_b16 a[8:9], v94 offset:16392\nds_read_b64_tr_b16 a[10:11], v94 offset:16904\nds_read_b64_tr_b16 a[16:17], v94 offset:16448\nds_read_b64_tr_b16 a[18:19], v94 offset:16960\n" \
"ds_read_b64_tr_b16 a[24:25], v94 offset:16456\nds_read_b64_tr_b16 a[26:27], v94 offset:16968\ns_waitcnt lgkmcnt(0)\ns_add_u32 m0, %[lw], 16384\ns_add_u32 s44, %[wo], 2359296\nbuffer_load_dwordx4 v92, %[rw], s44 offen nt lds\ns_add_u32 m0, %[lw], 17408\ns_add_u32 s44, %[wo], 2424832\n" \
"buffer_load_dwordx4 v92, %[rw], s44 offen nt lds\ns_add_u32 m0, %[lw], 18432\ns_add_u32 s44, %[wo], 2490368\nbuffer_load_dwordx4 v92, %[rw], s44 offen nt lds\ns_add_u32 m0, %[lw], 19456\ns_add_u32 s44, %[wo], 2555904\nbuffer_load_dwordx4 v92, %[rw], s44 offen nt lds\ns_waitcnt vmcnt(32)\n" \
"ds_read_b64_tr_b16 a[4:5], v94 offset:20480\nds_read_b64_tr_b16 a[6:7], v94 offset:20992\nds_read_b64_tr_b16 a[12:13], v94 offset:20488\nds_read_b64_tr_b16 a[14:15], v94 offset:21000\nds_read_b64_tr_b16 a[20:21], v94 offset:20544\nds_read_b64_tr_b16 a[22:23], v94 offset:21056\nds_read_b64_tr_b16 a[28:29], v94 offset:20552\nds_read_b64_tr_b16 a[30:31], v94 offset:21064\n" \
"s_waitcnt lgkmcnt(0)\ns_add_u32 m0, %[lw], 20480\ns_add_u32 s44, %[wo], 2621440\nbuffer_load_dwordx4 v92, %[rw], s44 offen nt lds\ns_add_u32 m0, %[lw], 21504\ns_add_u32 s44, %[wo], 2686976\nbuffer_load_dwordx4 v92, %[rw], s44 offen nt lds\ns_add_u32 m0, %[lw], 22528\n" \
"s_add_u32 s44, %[wo], 2752512\nbuffer_load_dwordx4 v92, %[rw], s44 offen nt lds\ns_add_u32 m0, %[lw], 23552\ns_add_u32 s44, %[wo], 2818048\nbuffer_load_dwordx4 v92, %[rw], s44 offen nt lds\ns_waitcnt vmcnt(32)\nds_read_b64_tr_b16 a[32:33], v93 offset:0\nds_read_b64_tr_b16 a[34:35], v93 offset:512\n" \
"ds_read_b64_tr_b16 v[84:85], v93 offset:8\nds_read_b64_tr_b16 v[86:87], v93 offset:520\nds_read_b64_tr_b16 a[40:41], v93 offset:64\nds_read_b64_tr_b16 a[42:43], v93 offset:576\nds_read_b64_tr_b16 a[48:49], v93 offset:72\nds_read_b64_tr_b16 a[50:51], v93 offset:584\ns_waitcnt lgkmcnt(0)\ns_add_u32 m0, %[tb], 0\n" \
"s_add_u32 s44, %[wo], 2883584\nbuffer_load_dwordx4 v92, %[rw], s44 offen nt lds\ns_add_u32 m0, %[tb], 1024\ns_add_u32 s44, %[wo], 2949120\nbuffer_load_dwordx4 v92, %[rw], s44 offen nt lds\ns_add_u32 m0, %[tb], 2048\ns_add_u32 s44, %[wo], 3014656\nbuffer_load_dwordx4 v92, %[rw], s44 offen nt lds\n" \
"s_add_u32 m0, %[tb], 3072\ns_add_u32 s44, %[wo], 3080192\nbuffer_load_dwordx4 v92, %[rw], s44 offen nt lds\ns_waitcnt vmcnt(32)\nds_read_b64_tr_b16 a[36:37], v93 offset:4096\nds_read_b64_tr_b16 a[38:39], v93 offset:4608\nds_read_b64_tr_b16 v[80:81], v93 offset:4104\nds_read_b64_tr_b16 v[82:83], v93 offset:4616\n" \
"ds_read_b64_tr_b16 a[44:45], v93 offset:4160\nds_read_b64_tr_b16 a[46:47], v93 offset:4672\nds_read_b64_tr_b16 a[52:53], v93 offset:4168\nds_read_b64_tr_b16 a[54:55], v93 offset:4680\ns_waitcnt lgkmcnt(0)\ns_add_u32 m0, %[tb], 4096\ns_add_u32 s44, %[wo], 3145728\nbuffer_load_dwordx4 v92, %[rw], s44 offen nt lds\n" \
"s_add_u32 m0, %[tb], 5120\ns_add_u32 s44, %[wo], 3211264\nbuffer_load_dwordx4 v92, %[rw], s44 offen nt lds\ns_add_u32 m0, %[tb], 6144\ns_add_u32 s44, %[wo], 3276800\nbuffer_load_dwordx4 v92, %[rw], s44 offen nt lds\ns_add_u32 m0, %[tb], 7168\ns_add_u32 s44, %[wo], 3342336\n" \
"buffer_load_dwordx4 v92, %[rw], s44 offen nt lds\ns_waitcnt vmcnt(32)\nds_read_b64_tr_b16 a[56:57], v93 offset:8192\nds_read_b64_tr_b16 a[58:59], v93 offset:8704\nds_read_b64_tr_b16 a[64:65], v93 offset:8200\nds_read_b64_tr_b16 a[66:67], v93 offset:8712\nds_read_b64_tr_b16 v[76:77], v93 offset:8256\nds_read_b64_tr_b16 v[78:79], v93 offset:8768\n" \
"ds_read_b64_tr_b16 a[72:73], v93 offset:8264\nds_read_b64_tr_b16 a[74:75], v93 offset:8776\ns_waitcnt lgkmcnt(0)\ns_add_u32 m0, %[tb], 8192\ns_add_u32 s44, %[wo], 3407872\nbuffer_load_dwordx4 v92, %[rw], s44 offen nt lds\ns_add_u32 m0, %[tb], 9216\ns_add_u32 s44, %[wo], 3473408\n" \
"buffer_load_dwordx4 v92, %[rw], s44 offen nt lds\ns_add_u32 m0, %[tb], 10240\ns_add_u32 s44, %[wo], 3538944\nbuffer_load_dwordx4 v92, %[rw], s44 offen nt lds\ns_add_u32 m0, %[tb], 11264\ns_add_u32 s44, %[wo], 3604480\nbuffer_load_dwordx4 v92, %[rw], s44 offen nt lds\ns_waitcnt vmcnt(32)\n" \
"ds_read_b64_tr_b16 a[60:61], v94 offset:0\nds_read_b64_tr_b16 a[62:63], v94 offset:512\nds_read_b64_tr_b16 a[68:69], v94 offset:8\nds_read_b64_tr_b16 a[70:71], v94 offset:520\nds_read_b64_tr_b16 v[72:73], v94 offset:64\nds_read_b64_tr_b16 v[74:75], v94 offset:576\nds_read_b64_tr_b16 a[76:77], v94 offset:72\nds_read_b64_tr_b16 a[78:79], v94 offset:584\n" \
"s_waitcnt lgkmcnt(0)\ns_add_u32 m0, %[lw], 0\ns_add_u32 s44, %[wo], 3670016\nbuffer_load_dwordx4 v92, %[rw], s44 offen nt lds\ns_add_u32 m0, %[lw], 1024\ns_add_u32 s44, %[wo], 3735552\nbuffer_load_dwordx4 v92, %[rw], s44 offen nt lds\ns_add_u32 m0, %[lw], 2048\n" \
"s_add_u32 s44, %[wo], 3801088\nbuffer_load_dwordx4 v92, %[rw], s44 offen nt lds\ns_add_u32 m0, %[lw], 3072\ns_add_u32 s44, %[wo], 3866624\nbuffer_load_dwordx4 v92, %[rw], s44 offen nt lds\ns_waitcnt vmcnt(32)\nds_read_b64_tr_b16 a[80:81], v94 offset:4096\nds_read_b64_tr_b16 a[82:83], v94 offset:4608\n" \
"ds_read_b64_tr_b16 a[88:89], v94 offset:4104\nds_read_b64_tr_b16 a[90:91], v94 offset:4616\nds_read_b64_tr_b16 a[96:97], v94 offset:4160\nds_read_b64_tr_b16 a[98:99], v94 offset:4672\nds_read_b64_tr_b16 v[68:69], v94 offset:4168\nds_read_b64_tr_b16 v[70:71], v94 offset:4680\ns_waitcnt lgkmcnt(0)\ns_add_u32 m0, %[lw], 4096\n" \
"s_add_u32 s44, %[wo], 3932160\nbuffer_load_dwordx4 v92, %[rw], s44 offen nt lds\ns_add_u32 m0, %[lw], 5120\ns_add_u32 s44, %[wo], 3997696\nbuffer_load_dwordx4 v92, %[rw], s44 offen nt lds\ns_add_u32 m0, %[lw], 6144\ns_add_u32 s44, %[wo], 4063232\nbuffer_load_dwordx4 v92, %[rw], s44 offen nt lds\n" \
"s_add_u32 m0, %[lw], 7168\ns_add_u32 s44, %[wo], 4128768\nbuffer_load_dwordx4 v92, %[rw], s44 offen nt lds\ns_waitcnt vmcnt(32)\nds_read_b64_tr_b16 a[84:85], v94 offset:8192\nds_read_b64_tr_b16 a[86:87], v94 offset:8704\nds_read_b64_tr_b16 a[92:93], v94 offset:8200\nds_read_b64_tr_b16 a[94:95], v94 offset:8712\n" \
"ds_read_b64_tr_b16 a[100:101], v94 offset:8256\nds_read_b64_tr_b16 a[102:103], v94 offset:8768\nds_read_b64_tr_b16 v[64:65], v94 offset:8264\nds_read_b64_tr_b16 v[66:67], v94 offset:8776\ns_waitcnt lgkmcnt(0)\ns_add_u32 m0, %[lw], 8192\ns_add_u32 s44, %[wo], 4194304\nbuffer_load_dwordx4 v92, %[rw], s44 offen nt lds\n" \
"s_add_u32 m0, %[lw], 9216\ns_add_u32 s44, %[wo], 4259840\nbuffer_load_dwordx4 v92, %[rw], s44 offen nt lds\ns_add_u32 m0, %[lw], 10240\ns_add_u32 s44, %[wo], 4325376\nbuffer_load_dwordx4 v92, %[rw], s44 offen nt lds\ns_add_u32 m0, %[lw], 11264\ns_add_u32 s44, %[wo], 4390912\n" \
"buffer_load_dwordx4 v92, %[rw], s44 offen nt lds\ns_waitcnt vmcnt(32)\nds_read_b64_tr_b16 a[104:105], v94 offset:12288\nds_read_b64_tr_b16 a[106:107], v94 offset:12800\nds_read_b64_tr_b16 a[112:113], v94 offset:12296\nds_read_b64_tr_b16 a[114:115], v94 offset:12808\nds_read_b64_tr_b16 a[120:121], v94 offset:12352\nds_read_b64_tr_b16 a[122:123], v94 offset:12864\n" \
"ds_read_b64_tr_b16 a[128:129], v94 offset:12360\nds_read_b64_tr_b16 a[130:131], v94 offset:12872\ns_waitcnt lgkmcnt(0)\ns_add_u32 m0, %[lw], 12288\ns_add_u32 s44, %[wo], 4456448\nbuffer_load_dwordx4 v92, %[rw], s44 offen nt lds\ns_add_u32 m0, %[lw], 13312\ns_add_u32 s44, %[wo], 4521984\n" \
"buffer_load_dwordx4 v92, %[rw], s44 offen nt lds\ns_add_u32 m0, %[lw], 14336\ns_add_u32 s44, %[wo], 4587520\nbuffer_load_dwordx4 v92, %[rw], s44 offen nt lds\ns_add_u32 m0, %[lw], 15360\ns_add_u32 s44, %[wo], 4653056\nbuffer_load_dwordx4 v92, %[rw], s44 offen nt lds\ns_waitcnt vmcnt(32)\n" \
"ds_read_b64_tr_b16 a[108:109], v94 offset:16384\nds_read_b64_tr_b16 a[110:111], v94 offset:16896\nds_read_b64_tr_b16 a[116:117], v94 offset:16392\nds_read_b64_tr_b16 a[118:119], v94 offset:16904\nds_read_b64_tr_b16 a[124:125], v94 offset:16448\nds_read_b64_tr_b16 a[126:127], v94 offset:16960\nds_read_b64_tr_b16 a[132:133], v94 offset:16456\nds_read_b64_tr_b16 a[134:135], v94 offset:16968\n" \
"s_waitcnt lgkmcnt(0)\ns_add_u32 m0, %[lw], 16384\ns_add_u32 s44, %[wo], 4718592\nbuffer_load_dwordx4 v92, %[rw], s44 offen nt lds\ns_add_u32 m0, %[lw], 17408\ns_add_u32 s44, %[wo], 4784128\nbuffer_load_dwordx4 v92, %[rw], s44 offen nt lds\ns_add_u32 m0, %[lw], 18432\n" \
"s_add_u32 s44, %[wo], 4849664\nbuffer_load_dwordx4 v92, %[rw], s44 offen nt lds\ns_add_u32 m0, %[lw], 19456\ns_add_u32 s44, %[wo], 4915200\nbuffer_load_dwordx4 v92, %[rw], s44 offen nt lds\ns_waitcnt vmcnt(32)\nds_read_b64_tr_b16 a[136:137], v94 offset:20480\nds_read_b64_tr_b16 a[138:139], v94 offset:20992\n" \
"ds_read_b64_tr_b16 v[60:61], v94 offset:20488\nds_read_b64_tr_b16 v[62:63], v94 offset:21000\nds_read_b64_tr_b16 a[144:145], v94 offset:20544\nds_read_b64_tr_b16 a[146:147], v94 offset:21056\nds_read_b64_tr_b16 a[152:153], v94 offset:20552\nds_read_b64_tr_b16 a[154:155], v94 offset:21064\ns_waitcnt lgkmcnt(0)\ns_add_u32 m0, %[lw], 20480\n" \
"s_add_u32 s44, %[wo], 4980736\nbuffer_load_dwordx4 v92, %[rw], s44 offen nt lds\ns_add_u32 m0, %[lw], 21504\ns_add_u32 s44, %[wo], 5046272\nbuffer_load_dwordx4 v92, %[rw], s44 offen nt lds\ns_add_u32 m0, %[lw], 22528\ns_add_u32 s44, %[wo], 5111808\nbuffer_load_dwordx4 v92, %[rw], s44 offen nt lds\n" \
"s_add_u32 m0, %[lw], 23552\ns_add_u32 s44, %[wo], 5177344\nbuffer_load_dwordx4 v92, %[rw], s44 offen nt lds\ns_waitcnt vmcnt(32)\nds_read_b64_tr_b16 a[140:141], v93 offset:0\nds_read_b64_tr_b16 a[142:143], v93 offset:512\nds_read_b64_tr_b16 v[56:57], v93 offset:8\nds_read_b64_tr_b16 v[58:59], v93 offset:520\n" \
"ds_read_b64_tr_b16 a[148:149], v93 offset:64\nds_read_b64_tr_b16 a[150:151], v93 offset:576\nds_read_b64_tr_b16 a[156:157], v93 offset:72\nds_read_b64_tr_b16 a[158:159], v93 offset:584\ns_waitcnt lgkmcnt(0)\ns_add_u32 m0, %[tb], 0\ns_add_u32 s44, %[wo], 5242880\nbuffer_load_dwordx4 v92, %[rw], s44 offen nt lds\n" \
"s_add_u32 m0, %[tb], 1024\ns_add_u32 s44, %[wo], 5308416\nbuffer_load_dwordx4 v92, %[rw], s44 offen nt lds\ns_add_u32 m0, %[tb], 2048\ns_add_u32 s44, %[wo], 5373952\nbuffer_load_dwordx4 v92, %[rw], s44 offen nt lds\ns_add_u32 m0, %[tb], 3072\ns_add_u32 s44, %[wo], 5439488\n" \
"buffer_load_dwordx4 v92, %[rw], s44 offen nt lds\ns_waitcnt vmcnt(32)\nds_read_b64_tr_b16 a[160:161], v93 offset:4096\nds_read_b64_tr_b16 a[162:163], v93 offset:4608\nds_read_b64_tr_b16 a[168:169], v93 offset:4104\nds_read_b64_tr_b16 a[170:171], v93 offset:4616\nds_read_b64_tr_b16 v[52:53], v93 offset:4160\nds_read_b64_tr_b16 v[54:55], v93 offset:4672\n" \
"ds_read_b64_tr_b16 a[176:177], v93 offset:4168\nds_read_b64_tr_b16 a[178:179], v93 offset:4680\ns_waitcnt lgkmcnt(0)\ns_add_u32 m0, %[tb], 4096\ns_add_u32 s44, %[wo], 5505024\nbuffer_load_dwordx4 v92, %[rw], s44 offen nt lds\ns_add_u32 m0, %[tb], 5120\ns_add_u32 s44, %[wo], 5570560\n" \
"buffer_load_dwordx4 v92, %[rw], s44 offen nt lds\ns_add_u32 m0, %[tb], 6144\ns_add_u32 s44, %[wo], 5636096\nbuffer_load_dwordx4 v92, %[rw], s44 offen nt lds\ns_add_u32 m0, %[tb], 7168\ns_add_u32 s44, %[wo], 5701632\nbuffer_load_dwordx4 v92, %[rw], s44 offen nt lds\ns_waitcnt vmcnt(32)\n" \
"ds_read_b64_tr_b16 a[164:165], v93 offset:8192\nds_read_b64_tr_b16 a[166:167], v93 offset:8704\nds_read_b64_tr_b16 a[172:173], v93 offset:8200\nds_read_b64_tr_b16 a[174:175], v93 offset:8712\nds_read_b64_tr_b16 v[48:49], v93 offset:8256\nds_read_b64_tr_b16 v[50:51], v93 offset:8768\nds_read_b64_tr_b16 a[180:181], v93 offset:8264\nds_read_b64_tr_b16 a[182:183], v93 offset:8776\n" \
"s_waitcnt lgkmcnt(0)\ns_add_u32 m0, %[tb], 8192\ns_add_u32 s44, %[wo], 5767168\nbuffer_load_dwordx4 v92, %[rw], s44 offen nt lds\ns_add_u32 m0, %[tb], 9216\ns_add_u32 s44, %[wo], 5832704\nbuffer_load_dwordx4 v92, %[rw], s44 offen nt lds\ns_add_u32 m0, %[tb], 10240\n" \
"s_add_u32 s44, %[wo], 5898240\nbuffer_load_dwordx4 v92, %[rw], s44 offen nt lds\ns_add_u32 m0, %[tb], 11264\ns_add_u32 s44, %[wo], 5963776\nbuffer_load_dwordx4 v92, %[rw], s44 offen nt lds\ns_waitcnt vmcnt(32)\nds_read_b64_tr_b16 a[184:185], v94 offset:0\nds_read_b64_tr_b16 a[186:187], v94 offset:512\n" \
"ds_read_b64_tr_b16 a[192:193], v94 offset:8\nds_read_b64_tr_b16 a[194:195], v94 offset:520\nds_read_b64_tr_b16 a[200:201], v94 offset:64\nds_read_b64_tr_b16 a[202:203], v94 offset:576\nds_read_b64_tr_b16 v[44:45], v94 offset:72\nds_read_b64_tr_b16 v[46:47], v94 offset:584\ns_waitcnt lgkmcnt(0)\ns_add_u32 m0, %[lw], 0\n" \
"s_add_u32 s44, %[wo], 6029312\nbuffer_load_dwordx4 v92, %[rw], s44 offen nt lds\ns_add_u32 m0, %[lw], 1024\ns_add_u32 s44, %[wo], 6094848\nbuffer_load_dwordx4 v92, %[rw], s44 offen nt lds\ns_add_u32 m0, %[lw], 2048\ns_add_u32 s44, %[wo], 6160384\nbuffer_load_dwordx4 v92, %[rw], s44 offen nt lds\n" \
"s_add_u32 m0, %[lw], 3072\ns_add_u32 s44, %[wo], 6225920\nbuffer_load_dwordx4 v92, %[rw], s44 offen nt lds\ns_waitcnt vmcnt(32)\nds_read_b64_tr_b16 a[188:189], v94 offset:4096\nds_read_b64_tr_b16 a[190:191], v94 offset:4608\nds_read_b64_tr_b16 a[196:197], v94 offset:4104\nds_read_b64_tr_b16 a[198:199], v94 offset:4616\n" \
"ds_read_b64_tr_b16 a[204:205], v94 offset:4160\nds_read_b64_tr_b16 a[206:207], v94 offset:4672\nds_read_b64_tr_b16 v[40:41], v94 offset:4168\nds_read_b64_tr_b16 v[42:43], v94 offset:4680\ns_waitcnt lgkmcnt(0)\ns_add_u32 m0, %[lw], 4096\ns_add_u32 s44, %[wo], 6291456\nbuffer_load_dwordx4 v92, %[rw], s44 offen nt lds\n" \
"s_add_u32 m0, %[lw], 5120\ns_add_u32 s44, %[wo], 6356992\nbuffer_load_dwordx4 v92, %[rw], s44 offen nt lds\ns_add_u32 m0, %[lw], 6144\ns_add_u32 s44, %[wo], 6422528\nbuffer_load_dwordx4 v92, %[rw], s44 offen nt lds\ns_add_u32 m0, %[lw], 7168\ns_add_u32 s44, %[wo], 6488064\n" \
"buffer_load_dwordx4 v92, %[rw], s44 offen nt lds\ns_waitcnt vmcnt(32)\nds_read_b64_tr_b16 a[208:209], v94 offset:8192\nds_read_b64_tr_b16 a[210:211], v94 offset:8704\nds_read_b64_tr_b16 a[216:217], v94 offset:8200\nds_read_b64_tr_b16 a[218:219], v94 offset:8712\nds_read_b64_tr_b16 a[224:225], v94 offset:8256\nds_read_b64_tr_b16 a[226:227], v94 offset:8768\n" \
"ds_read_b64_tr_b16 a[232:233], v94 offset:8264\nds_read_b64_tr_b16 a[234:235], v94 offset:8776\ns_waitcnt lgkmcnt(0)\ns_add_u32 m0, %[lw], 8192\ns_add_u32 s44, %[wo], 6553600\nbuffer_load_dwordx4 v92, %[rw], s44 offen nt lds\ns_add_u32 m0, %[lw], 9216\ns_add_u32 s44, %[wo], 6619136\n" \
"buffer_load_dwordx4 v92, %[rw], s44 offen nt lds\ns_add_u32 m0, %[lw], 10240\ns_add_u32 s44, %[wo], 6684672\nbuffer_load_dwordx4 v92, %[rw], s44 offen nt lds\ns_add_u32 m0, %[lw], 11264\ns_add_u32 s44, %[wo], 6750208\nbuffer_load_dwordx4 v92, %[rw], s44 offen nt lds\ns_waitcnt vmcnt(32)\n" \
"ds_read_b64_tr_b16 a[212:213], v94 offset:12288\nds_read_b64_tr_b16 a[214:215], v94 offset:12800\nds_read_b64_tr_b16 a[220:221], v94 offset:12296\nds_read_b64_tr_b16 a[222:223], v94 offset:12808\nds_read_b64_tr_b16 a[228:229], v94 offset:12352\nds_read_b64_tr_b16 a[230:231], v94 offset:12864\nds_read_b64_tr_b16 a[236:237], v94 offset:12360\nds_read_b64_tr_b16 a[238:239], v94 offset:12872\n" \
"s_waitcnt lgkmcnt(0)\ns_add_u32 m0, %[lw], 12288\ns_add_u32 s44, %[wo], 6815744\nbuffer_load_dwordx4 v92, %[rw], s44 offen nt lds\ns_add_u32 m0, %[lw], 13312\ns_add_u32 s44, %[wo], 6881280\nbuffer_load_dwordx4 v92, %[rw], s44 offen nt lds\ns_add_u32 m0, %[lw], 14336\n" \
"s_add_u32 s44, %[wo], 6946816\nbuffer_load_dwordx4 v92, %[rw], s44 offen nt lds\ns_add_u32 m0, %[lw], 15360\ns_add_u32 s44, %[wo], 7012352\nbuffer_load_dwordx4 v92, %[rw], s44 offen nt lds\ns_waitcnt vmcnt(32)\nds_read_b64_tr_b16 a[240:241], v94 offset:16384\nds_read_b64_tr_b16 a[242:243], v94 offset:16896\n" \
"ds_read_b64_tr_b16 v[36:37], v94 offset:16392\nds_read_b64_tr_b16 v[38:39], v94 offset:16904\nds_read_b64_tr_b16 a[248:249], v94 offset:16448\nds_read_b64_tr_b16 a[250:251], v94 offset:16960\nds_read_b64_tr_b16 v[96:97], v94 offset:16456\nds_read_b64_tr_b16 v[98:99], v94 offset:16968\ns_waitcnt lgkmcnt(0)\ns_add_u32 m0, %[lw], 16384\n" \
"s_add_u32 s44, %[wo], 7077888\nbuffer_load_dwordx4 v92, %[rw], s44 offen nt lds\ns_add_u32 m0, %[lw], 17408\ns_add_u32 s44, %[wo], 7143424\nbuffer_load_dwordx4 v92, %[rw], s44 offen nt lds\ns_add_u32 m0, %[lw], 18432\ns_add_u32 s44, %[wo], 7208960\nbuffer_load_dwordx4 v92, %[rw], s44 offen nt lds\n" \
"s_add_u32 m0, %[lw], 19456\ns_add_u32 s44, %[wo], 7274496\nbuffer_load_dwordx4 v92, %[rw], s44 offen nt lds\ns_waitcnt vmcnt(32)\nds_read_b64_tr_b16 a[244:245], v94 offset:20480\nds_read_b64_tr_b16 a[246:247], v94 offset:20992\nds_read_b64_tr_b16 v[32:33], v94 offset:20488\nds_read_b64_tr_b16 v[34:35], v94 offset:21000\n" \
"ds_read_b64_tr_b16 a[252:253], v94 offset:20544\nds_read_b64_tr_b16 a[254:255], v94 offset:21056\nds_read_b64_tr_b16 v[100:101], v94 offset:20552\nds_read_b64_tr_b16 v[102:103], v94 offset:21064\ns_waitcnt lgkmcnt(0)\ns_add_u32 m0, %[lw], 20480\ns_add_u32 s44, %[wo], 7340032\nbuffer_load_dwordx4 v92, %[rw], s44 offen nt lds\n" \
"s_add_u32 m0, %[lw], 21504\ns_add_u32 s44, %[wo], 7405568\nbuffer_load_dwordx4 v92, %[rw], s44 offen nt lds\ns_add_u32 m0, %[lw], 22528\ns_add_u32 s44, %[wo], 7471104\nbuffer_load_dwordx4 v92, %[rw], s44 offen nt lds\ns_add_u32 m0, %[lw], 23552\ns_add_u32 s44, %[wo], 7536640\n" \
"buffer_load_dwordx4 v92, %[rw], s44 offen nt lds\ns_waitcnt vmcnt(32)\nds_read_b64_tr_b16 v[104:105], v93 offset:0\nds_read_b64_tr_b16 v[106:107], v93 offset:512\nds_read_b64_tr_b16 v[112:113], v93 offset:8\nds_read_b64_tr_b16 v[114:115], v93 offset:520\nds_read_b64_tr_b16 v[28:29], v93 offset:64\nds_read_b64_tr_b16 v[30:31], v93 offset:576\n" \
"ds_read_b64_tr_b16 v[120:121], v93 offset:72\nds_read_b64_tr_b16 v[122:123], v93 offset:584\ns_waitcnt lgkmcnt(0)\ns_add_u32 m0, %[tb], 0\ns_add_u32 s44, %[wo], 7602176\nbuffer_load_dwordx4 v92, %[rw], s44 offen nt lds\ns_add_u32 m0, %[tb], 1024\ns_add_u32 s44, %[wo], 7667712\n" \
"buffer_load_dwordx4 v92, %[rw], s44 offen nt lds\ns_add_u32 m0, %[tb], 2048\ns_add_u32 s44, %[wo], 7733248\nbuffer_load_dwordx4 v92, %[rw], s44 offen nt lds\ns_add_u32 m0, %[tb], 3072\ns_add_u32 s44, %[wo], 7798784\nbuffer_load_dwordx4 v92, %[rw], s44 offen nt lds\ns_waitcnt vmcnt(32)\n" \
"ds_read_b64_tr_b16 v[108:109], v93 offset:4096\nds_read_b64_tr_b16 v[110:111], v93 offset:4608\nds_read_b64_tr_b16 v[116:117], v93 offset:4104\nds_read_b64_tr_b16 v[118:119], v93 offset:4616\nds_read_b64_tr_b16 v[24:25], v93 offset:4160\nds_read_b64_tr_b16 v[26:27], v93 offset:4672\nds_read_b64_tr_b16 v[124:125], v93 offset:4168\nds_read_b64_tr_b16 v[126:127], v93 offset:4680\n" \
"s_waitcnt lgkmcnt(0)\nbuffer_load_dwordx2 v[88:89], off, s[36:39], s45 sc1\ns_add_u32 m0, %[tb], 4096\ns_add_u32 s44, %[wo], 7864320\nbuffer_load_dwordx4 v92, %[rw], s44 offen nt lds\ns_add_u32 m0, %[tb], 5120\ns_add_u32 s44, %[wo], 7929856\nbuffer_load_dwordx4 v92, %[rw], s44 offen nt lds\n" \
"s_add_u32 m0, %[tb], 6144\ns_add_u32 s44, %[wo], 7995392\nbuffer_load_dwordx4 v92, %[rw], s44 offen nt lds\ns_add_u32 m0, %[tb], 7168\ns_add_u32 s44, %[wo], 8060928\nbuffer_load_dwordx4 v92, %[rw], s44 offen nt lds\ns_waitcnt vmcnt(33)\nds_read_b64_tr_b16 v[128:129], v93 offset:8192\n" \
"ds_read_b64_tr_b16 v[130:131], v93 offset:8704\nds_read_b64_tr_b16 v[136:137], v93 offset:8200\nds_read_b64_tr_b16 v[138:139], v93 offset:8712\nds_read_b64_tr_b16 v[144:145], v93 offset:8256\nds_read_b64_tr_b16 v[146:147], v93 offset:8768\nds_read_b64_tr_b16 v[20:21], v93 offset:8264\nds_read_b64_tr_b16 v[22:23], v93 offset:8776\ns_waitcnt lgkmcnt(0)\n" \
"s_add_u32 m0, %[tb], 8192\ns_add_u32 s44, %[wo], 8126464\nbuffer_load_dwordx4 v92, %[rw], s44 offen nt lds\ns_add_u32 m0, %[tb], 9216\ns_add_u32 s44, %[wo], 8192000\nbuffer_load_dwordx4 v92, %[rw], s44 offen nt lds\ns_add_u32 m0, %[tb], 10240\ns_add_u32 s44, %[wo], 8257536\n" \
"buffer_load_dwordx4 v92, %[rw], s44 offen nt lds\ns_add_u32 m0, %[tb], 11264\ns_add_u32 s44, %[wo], 8323072\nbuffer_load_dwordx4 v92, %[rw], s44 offen nt lds\ns_waitcnt vmcnt(33)\nds_read_b64_tr_b16 v[132:133], v94 offset:0\nds_read_b64_tr_b16 v[134:135], v94 offset:512\nds_read_b64_tr_b16 v[140:141], v94 offset:8\n" \
"ds_read_b64_tr_b16 v[142:143], v94 offset:520\nds_read_b64_tr_b16 v[148:149], v94 offset:64\nds_read_b64_tr_b16 v[150:151], v94 offset:576\nds_read_b64_tr_b16 v[16:17], v94 offset:72\nds_read_b64_tr_b16 v[18:19], v94 offset:584\ns_waitcnt lgkmcnt(0)\nds_write_b128 v1, v[84:87] offset:0\nds_write_b128 v1, v[80:83] offset:1024\n" \
"ds_write_b128 v1, v[76:79] offset:2048\nds_write_b128 v1, v[72:75] offset:3072\ns_waitcnt vmcnt(29)\nds_read_b64_tr_b16 v[152:153], v94 offset:4096\nds_read_b64_tr_b16 v[154:155], v94 offset:4608\nds_read_b64_tr_b16 v[160:161], v94 offset:4104\nds_read_b64_tr_b16 v[162:163], v94 offset:4616\nds_read_b64_tr_b16 v[168:169], v94 offset:4160\n" \
"ds_read_b64_tr_b16 v[170:171], v94 offset:4672\nds_read_b64_tr_b16 v[176:177], v94 offset:4168\nds_read_b64_tr_b16 v[178:179], v94 offset:4680\ns_waitcnt lgkmcnt(0)\nds_write_b128 v1, v[68:71] offset:4096\nds_write_b128 v1, v[64:67] offset:5120\nds_write_b128 v1, v[60:63] offset:6144\nds_write_b128 v1, v[56:59] offset:7168\n" \
"s_waitcnt vmcnt(25)\nds_read_b64_tr_b16 v[156:157], v94 offset:8192\nds_read_b64_tr_b16 v[158:159], v94 offset:8704\nds_read_b64_tr_b16 v[164:165], v94 offset:8200\nds_read_b64_tr_b16 v[166:167], v94 offset:8712\nds_read_b64_tr_b16 v[172:173], v94 offset:8256\nds_read_b64_tr_b16 v[174:175], v94 offset:8768\nds_read_b64_tr_b16 v[180:181], v94 offset:8264\n" \
"ds_read_b64_tr_b16 v[182:183], v94 offset:8776\ns_waitcnt lgkmcnt(0)\nds_write_b128 v1, v[52:55] offset:8192\nds_write_b128 v1, v[48:51] offset:9216\nds_write_b128 v1, v[44:47] offset:10240\nds_write_b128 v1, v[40:43] offset:11264\ns_waitcnt vmcnt(21)\nds_read_b64_tr_b16 v[184:185], v94 offset:12288\n" \
"ds_read_b64_tr_b16 v[186:187], v94 offset:12800\nds_read_b64_tr_b16 v[40:41], v94 offset:12296\nds_read_b64_tr_b16 v[42:43], v94 offset:12808\nds_read_b64_tr_b16 v[192:193], v94 offset:12352\nds_read_b64_tr_b16 v[194:195], v94 offset:12864\nds_read_b64_tr_b16 v[200:201], v94 offset:12360\nds_read_b64_tr_b16 v[202:203], v94 offset:12872\ns_waitcnt lgkmcnt(0)\n" \
"ds_write_b128 v1, v[36:39] offset:12288\nds_write_b128 v1, v[32:35] offset:13312\nds_write_b128 v1, v[28:31] offset:14336\nds_write_b128 v1, v[24:27] offset:15360\ns_waitcnt vmcnt(17)\nds_read_b64_tr_b16 v[188:189], v94 offset:16384\nds_read_b64_tr_b16 v[190:191], v94 offset:16896\nds_read_b64_tr_b16 v[24:25], v94 offset:16392\n" \
"ds_read_b64_tr_b16 v[26:27], v94 offset:16904\nds_read_b64_tr_b16 v[196:197], v94 offset:16448\nds_read_b64_tr_b16 v[198:199], v94 offset:16960\nds_read_b64_tr_b16 v[204:205], v94 offset:16456\nds_read_b64_tr_b16 v[206:207], v94 offset:16968\ns_waitcnt lgkmcnt(0)\nds_write_b128 v1, v[20:23] offset:16384\nds_write_b128 v1, v[16:19] offset:17408\n" \
"ds_write_b128 v1, v[40:43] offset:18432\nds_write_b128 v1, v[24:27] offset:19456\ns_waitcnt vmcnt(13)\nds_read_b64_tr_b16 v[208:209], v94 offset:20480\nds_read_b64_tr_b16 v[210:211], v94 offset:20992\nds_read_b64_tr_b16 v[216:217], v94 offset:20488\nds_read_b64_tr_b16 v[218:219], v94 offset:21000\nds_read_b64_tr_b16 v[24:25], v94 offset:20544\n" \
"ds_read_b64_tr_b16 v[26:27], v94 offset:21056\nds_read_b64_tr_b16 v[224:225], v94 offset:20552\nds_read_b64_tr_b16 v[226:227], v94 offset:21064\ns_waitcnt lgkmcnt(0)\nds_write_b128 v1, v[24:27] offset:20480\ns_waitcnt vmcnt(9)\nds_read_b64_tr_b16 v[212:213], v93 offset:0\nds_read_b64_tr_b16 v[214:215], v93 offset:512\n" \
"ds_read_b64_tr_b16 v[220:221], v93 offset:8\nds_read_b64_tr_b16 v[222:223], v93 offset:520\nds_read_b64_tr_b16 v[24:25], v93 offset:64\nds_read_b64_tr_b16 v[26:27], v93 offset:576\nds_read_b64_tr_b16 v[228:229], v93 offset:72\nds_read_b64_tr_b16 v[230:231], v93 offset:584\ns_waitcnt lgkmcnt(0)\nds_write_b128 v1, v[24:27] offset:21504\n" \
"s_waitcnt vmcnt(4)\nds_read_b64_tr_b16 v[232:233], v93 offset:4096\nds_read_b64_tr_b16 v[234:235], v93 offset:4608\nds_read_b64_tr_b16 v[240:241], v93 offset:4104\nds_read_b64_tr_b16 v[242:243], v93 offset:4616\nds_read_b64_tr_b16 v[248:249], v93 offset:4160\nds_read_b64_tr_b16 v[250:251], v93 offset:4672\nds_read_b64_tr_b16 v[24:25], v93 offset:4168\n" \
"ds_read_b64_tr_b16 v[26:27], v93 offset:4680\ns_waitcnt lgkmcnt(0)\nds_write_b128 v1, v[24:27] offset:22528\ns_waitcnt vmcnt(0)\nds_read_b64_tr_b16 v[236:237], v93 offset:8192\nds_read_b64_tr_b16 v[238:239], v93 offset:8704\nds_read_b64_tr_b16 v[244:245], v93 offset:8200\nds_read_b64_tr_b16 v[246:247], v93 offset:8712\n" \
"ds_read_b64_tr_b16 v[252:253], v93 offset:8256\nds_read_b64_tr_b16 v[254:255], v93 offset:8768\nds_read_b64_tr_b16 v[24:25], v93 offset:8264\nds_read_b64_tr_b16 v[26:27], v93 offset:8776\ns_waitcnt lgkmcnt(0)\ns_mov_b32 s43, 0\ns_mov_b32 s86, 110\ns_mov_b32 s89, 0\n" \
".Lqk_hb%=:\nv_cmp_ne_u32 vcc, v95, v88\ns_cbranch_vccnz .Lqk_hb1%=\ns_sleep 4\nbuffer_load_dwordx2 v[88:89], off, s[36:39], s45 sc1\ns_waitcnt vmcnt(0)\ns_add_u32 s43, s43, 1\ns_cmp_lt_u32 s43, 262144\n" \
"s_cbranch_scc1 .Lqk_hb%=\ns_branch .Lqk_abort%=\n.Lqk_hb1%=:\nv_readfirstlane_b32 s44, v89\ns_and_b32 s44, s44, 0x200000\ns_add_u32 s36, s36, s44\ns_addc_u32 s37, s37, 0\nds_write_b128 v1, v[24:27] offset:23552\n" \
"s_waitcnt lgkmcnt(0)\ns_barrier\ns_mov_b32 s40, -1\ns_mov_b32 s43, 0\ns_mov_b32 s61, 0x7fff7fff\ns_mov_b32 s58, 0x3e0293ee\ns_mov_b32 s59, 0xbe0293ee\ns_lshl_b32 s57, %[w], 12\n" \
"s_mov_b32 s62, 0xff00ff00\ns_mov_b32 s63, 0xff00ff00\ns_mov_b32 s56, 0\ns_cmp_eq_u32 %[w], 3\ns_cbranch_scc1 .Lqk_fetch0%=\n.Lqk_pwait%=:\ns_barrier\nds_read_b128 v[80:83], v2 offset:0\n" \
"ds_read_b128 v[84:87], v2 offset:256\nds_read_b128 v[88:91], v2 offset:2048\n.Lqk_proj%=:\ns_waitcnt lgkmcnt(2)\nv_mfma_f32_16x16x32_bf16 v[16:19], a[0:3], v[80:83], 0\nds_read_b128 v[92:95], v2 offset:2304\nv_mfma_f32_16x16x32_bf16 v[20:23], a[8:11], v[80:83], 0\nds_read_b128 v[72:75], v1 offset:0\n" \
"v_mfma_f32_16x16x32_bf16 v[24:27], a[16:19], v[80:83], 0\nv_mfma_f32_16x16x32_bf16 v[28:31], a[24:27], v[80:83], 0\ns_waitcnt lgkmcnt(3)\nv_mfma_f32_16x16x32_bf16 v[32:35], a[0:3], v[84:87], 0\nds_read_b128 v[80:83], v2 offset:4096\nv_mfma_f32_16x16x32_bf16 v[36:39], a[8:11], v[84:87], 0\nv_mfma_f32_16x16x32_bf16 v[40:43], a[16:19], v[84:87], 0\nv_mfma_f32_16x16x32_bf16 v[44:47], a[24:27], v[84:87], 0\n" \
"s_waitcnt lgkmcnt(3)\nv_mfma_f32_16x16x32_bf16 v[16:19], a[4:7], v[88:91], v[16:19]\nds_read_b128 v[84:87], v2 offset:4352\nv_mfma_f32_16x16x32_bf16 v[20:23], a[12:15], v[88:91], v[20:23]\nds_read_b128 v[76:79], v1 offset:1024\nv_mfma_f32_16x16x32_bf16 v[24:27], a[20:23], v[88:91], v[24:27]\nv_mfma_f32_16x16x32_bf16 v[28:31], a[28:31], v[88:91], v[28:31]\ns_waitcnt lgkmcnt(4)\n" \
"v_mfma_f32_16x16x32_bf16 v[32:35], a[4:7], v[92:95], v[32:35]\nds_read_b128 v[88:91], v2 offset:6144\nv_mfma_f32_16x16x32_bf16 v[36:39], a[12:15], v[92:95], v[36:39]\nv_mfma_f32_16x16x32_bf16 v[40:43], a[20:23], v[92:95], v[40:43]\nv_mfma_f32_16x16x32_bf16 v[44:47], a[28:31], v[92:95], v[44:47]\ns_waitcnt lgkmcnt(3)\nv_mfma_f32_16x16x32_bf16 v[16:19], a[32:35], v[80:83], v[16:19]\nds_read_b128 v[92:95], v2 offset:6400\n" \
"v_mfma_f32_16x16x32_bf16 v[20:23], v[72:75], v[80:83], v[20:23]\nds_read_b128 v[64:67], v1 offset:2048\nv_mfma_f32_16x16x32_bf16 v[24:27], a[40:43], v[80:83], v[24:27]\nv_mfma_f32_16x16x32_bf16 v[28:31], a[48:51], v[80:83], v[28:31]\ns_waitcnt lgkmcnt(4)\nv_mfma_f32_16x16x32_bf16 v[32:35], a[32:35], v[84:87], v[32:35]\nds_read_b128 v[80:83], v2 offset:8192\nv_mfma_f32_16x16x32_bf16 v[36:39], v[72:75], v[84:87], v[36:39]\n" \
"v_mfma_f32_16x16x32_bf16 v[40:43], a[40:43], v[84:87], v[40:43]\nv_mfma_f32_16x16x32_bf16 v[44:47], a[48:51], v[84:87], v[44:47]\ns_waitcnt lgkmcnt(3)\nv_mfma_f32_16x16x32_bf16 v[16:19], a[36:39], v[88:91], v[16:19]\nds_read_b128 v[84:87], v2 offset:8448\nv_mfma_f32_16x16x32_bf16 v[20:23], v[76:79], v[88:91], v[20:23]\nds_read_b128 v[68:71], v1 offset:3072\nv_mfma_f32_16x16x32_bf16 v[24:27], a[44:47], v[88:91], v[24:27]\n" \
"v_mfma_f32_16x16x32_bf16 v[28:31], a[52:55], v[88:91], v[28:31]\ns_waitcnt lgkmcnt(4)\nv_mfma_f32_16x16x32_bf16 v[32:35], a[36:39], v[92:95], v[32:35]\nds_read_b128 v[88:91], v2 offset:10240\nv_mfma_f32_16x16x32_bf16 v[36:39], v[76:79], v[92:95], v[36:39]\nv_mfma_f32_16x16x32_bf16 v[40:43], a[44:47], v[92:95], v[40:43]\nv_mfma_f32_16x16x32_bf16 v[44:47], a[52:55], v[92:95], v[44:47]\ns_waitcnt lgkmcnt(3)\n" \
"v_mfma_f32_16x16x32_bf16 v[16:19], a[56:59], v[80:83], v[16:19]\nds_read_b128 v[92:95], v2 offset:10496\nv_mfma_f32_16x16x32_bf16 v[20:23], a[64:67], v[80:83], v[20:23]\nds_read_b128 v[72:75], v1 offset:4096\nv_mfma_f32_16x16x32_bf16 v[24:27], v[64:67], v[80:83], v[24:27]\nv_mfma_f32_16x16x32_bf16 v[28:31], a[72:75], v[80:83], v[28:31]\ns_waitcnt lgkmcnt(4)\nv_mfma_f32_16x16x32_bf16 v[32:35], a[56:59], v[84:87], v[32:35]\n" \
"ds_read_b128 v[80:83], v2 offset:12288\nv_mfma_f32_16x16x32_bf16 v[36:39], a[64:67], v[84:87], v[36:39]\nv_mfma_f32_16x16x32_bf16 v[40:43], v[64:67], v[84:87], v[40:43]\nv_mfma_f32_16x16x32_bf16 v[44:47], a[72:75], v[84:87], v[44:47]\ns_waitcnt lgkmcnt(3)\nv_mfma_f32_16x16x32_bf16 v[16:19], a[60:63], v[88:91], v[16:19]\nds_read_b128 v[84:87], v2 offset:12544\nv_mfma_f32_16x16x32_bf16 v[20:23], a[68:71], v[88:91], v[20:23]\n" \
"ds_read_b128 v[76:79], v1 offset:5120\nv_mfma_f32_16x16x32_bf16 v[24:27], v[68:71], v[88:91], v[24:27]\nv_mfma_f32_16x16x32_bf16 v[28:31], a[76:79], v[88:91], v[28:31]\ns_waitcnt lgkmcnt(4)\nv_mfma_f32_16x16x32_bf16 v[32:35], a[60:63], v[92:95], v[32:35]\nds_read_b128 v[88:91], v2 offset:14336\nv_mfma_f32_16x16x32_bf16 v[36:39], a[68:71], v[92:95], v[36:39]\nv_mfma_f32_16x16x32_bf16 v[40:43], v[68:71], v[92:95], v[40:43]\n" \
"v_mfma_f32_16x16x32_bf16 v[44:47], a[76:79], v[92:95], v[44:47]\n.p2align 6\n.Lqk_pj%=:\ns_waitcnt lgkmcnt(3)\nv_mfma_f32_16x16x32_bf16 v[16:19], a[80:83], v[80:83], v[16:19]\nds_read_b128 v[92:95], v2 offset:14592\nv_mfma_f32_16x16x32_bf16 v[20:23], a[88:91], v[80:83], v[20:23]\ns_add_u32 s40, s40, 1\n" \
"v_mfma_f32_16x16x32_bf16 v[24:27], a[96:99], v[80:83], v[24:27]\ns_lshl_b32 s44, s40, 17\nv_mfma_f32_16x16x32_bf16 v[28:31], v[72:75], v[80:83], v[28:31]\ns_add_u32 s44, s44, %[xn]\ns_waitcnt lgkmcnt(3)\nv_mfma_f32_16x16x32_bf16 v[32:35], a[80:83], v[84:87], v[32:35]\ns_barrier\nds_read_b128 v[80:83], v2 offset:16384\n" \
"v_mfma_f32_16x16x32_bf16 v[36:39], a[88:91], v[84:87], v[36:39]\ns_lshr_b32 s45, s44, 8\nv_mfma_f32_16x16x32_bf16 v[40:43], a[96:99], v[84:87], v[40:43]\ns_and_b32 s45, s45, 0x2000\nv_mfma_f32_16x16x32_bf16 v[44:47], v[72:75], v[84:87], v[44:47]\ns_xor_b32 s41, s44, s45\ns_waitcnt lgkmcnt(2)\nv_mfma_f32_16x16x32_bf16 v[16:19], a[84:87], v[88:91], v[16:19]\n" \
"ds_read_b128 v[84:87], v2 offset:16640\nv_mfma_f32_16x16x32_bf16 v[20:23], a[92:95], v[88:91], v[20:23]\ns_add_u32 s41, s41, %[xw]\ns_and_b32 s42, s40, 3\ns_add_u32 m0, s57, 32768\nv_mfma_f32_16x16x32_bf16 v[24:27], a[100:103], v[88:91], v[24:27]\ns_add_u32 s60, s41, 65536\nbuffer_load_dwordx4 v0, s[36:39], s60 offen offset:0 sc1 lds\n" \
"v_mfma_f32_16x16x32_bf16 v[28:31], v[76:79], v[88:91], v[28:31]\ns_lshl_b32 s42, s42, 15\ns_waitcnt lgkmcnt(2)\nv_mfma_f32_16x16x32_bf16 v[32:35], a[84:87], v[92:95], v[32:35]\nds_read_b128 v[88:91], v2 offset:18432\nv_mfma_f32_16x16x32_bf16 v[36:39], a[92:95], v[92:95], v[36:39]\ns_and_b32 s44, s40, 1\nv_mfma_f32_16x16x32_bf16 v[40:43], a[100:103], v[92:95], v[40:43]\n" \
"s_lshl_b32 s55, s44, 17\nbuffer_load_dwordx4 v0, s[36:39], s60 offen offset:1024 sc1 lds\nv_mfma_f32_16x16x32_bf16 v[44:47], v[76:79], v[92:95], v[44:47]\ns_add_u32 s56, s56, %[fs]\ns_waitcnt lgkmcnt(2)\nv_mfma_f32_16x16x32_bf16 v[16:19], a[104:107], v[80:83], v[16:19]\nds_read_b128 v[92:95], v2 offset:18688\nv_mfma_f32_16x16x32_bf16 v[20:23], a[112:115], v[80:83], v[20:23]\n" \
"ds_read_b128 v[72:75], v1 offset:6144\nv_mfma_f32_16x16x32_bf16 v[24:27], a[120:123], v[80:83], v[24:27]\nbuffer_load_dwordx4 v0, s[36:39], s60 offen offset:2048 sc1 lds\nv_mfma_f32_16x16x32_bf16 v[28:31], a[128:131], v[80:83], v[28:31]\ns_waitcnt lgkmcnt(3)\nv_mfma_f32_16x16x32_bf16 v[32:35], a[104:107], v[84:87], v[32:35]\nds_read_b128 v[80:83], v2 offset:20480\nv_mfma_f32_16x16x32_bf16 v[36:39], a[112:115], v[84:87], v[36:39]\n" \
"v_mfma_f32_16x16x32_bf16 v[40:43], a[120:123], v[84:87], v[40:43]\nbuffer_load_dwordx4 v0, s[36:39], s60 offen offset:3072 sc1 lds\nv_mfma_f32_16x16x32_bf16 v[44:47], a[128:131], v[84:87], v[44:47]\ns_waitcnt lgkmcnt(3)\nv_mfma_f32_16x16x32_bf16 v[16:19], a[108:111], v[88:91], v[16:19]\nds_read_b128 v[84:87], v2 offset:20736\nv_mfma_f32_16x16x32_bf16 v[20:23], a[116:119], v[88:91], v[20:23]\nds_read_b128 v[76:79], v1 offset:7168\n" \
"v_mfma_f32_16x16x32_bf16 v[24:27], a[124:127], v[88:91], v[24:27]\nv_mfma_f32_16x16x32_bf16 v[28:31], a[132:135], v[88:91], v[28:31]\ns_waitcnt lgkmcnt(4)\nv_mfma_f32_16x16x32_bf16 v[32:35], a[108:111], v[92:95], v[32:35]\nds_read_b128 v[88:91], v2 offset:22528\nv_mfma_f32_16x16x32_bf16 v[36:39], a[116:119], v[92:95], v[36:39]\nv_mfma_f32_16x16x32_bf16 v[40:43], a[124:127], v[92:95], v[40:43]\nv_mfma_f32_16x16x32_bf16 v[44:47], a[132:135], v[92:95], v[44:47]\n" \
"s_waitcnt lgkmcnt(3)\nv_mfma_f32_16x16x32_bf16 v[16:19], a[136:139], v[80:83], v[16:19]\nds_read_b128 v[92:95], v2 offset:22784\nv_mfma_f32_16x16x32_bf16 v[20:23], v[72:75], v[80:83], v[20:23]\nds_read_b128 v[64:67], v1 offset:8192\nv_mfma_f32_16x16x32_bf16 v[24:27], a[144:147], v[80:83], v[24:27]\nv_mfma_f32_16x16x32_bf16 v[28:31], a[152:155], v[80:83], v[28:31]\ns_waitcnt lgkmcnt(4)\n" \
"v_mfma_f32_16x16x32_bf16 v[32:35], a[136:139], v[84:87], v[32:35]\nds_read_b128 v[80:83], v2 offset:24576\nv_mfma_f32_16x16x32_bf16 v[36:39], v[72:75], v[84:87], v[36:39]\nv_mfma_f32_16x16x32_bf16 v[40:43], a[144:147], v[84:87], v[40:43]\nv_mfma_f32_16x16x32_bf16 v[44:47], a[152:155], v[84:87], v[44:47]\ns_waitcnt lgkmcnt(3)\nv_mfma_f32_16x16x32_bf16 v[16:19], a[140:143], v[88:91], v[16:19]\nds_read_b128 v[84:87], v2 offset:24832\n" \
"v_mfma_f32_16x16x32_bf16 v[20:23], v[76:79], v[88:91], v[20:23]\nds_read_b128 v[68:71], v1 offset:9216\nv_mfma_f32_16x16x32_bf16 v[24:27], a[148:151], v[88:91], v[24:27]\nv_mfma_f32_16x16x32_bf16 v[28:31], a[156:159], v[88:91], v[28:31]\ns_waitcnt lgkmcnt(4)\nv_mfma_f32_16x16x32_bf16 v[32:35], a[140:143], v[92:95], v[32:35]\nds_read_b128 v[88:91], v2 offset:26624\nv_mfma_f32_16x16x32_bf16 v[36:39], v[76:79], v[92:95], v[36:39]\n" \
"v_mfma_f32_16x16x32_bf16 v[40:43], a[148:151], v[92:95], v[40:43]\nv_mfma_f32_16x16x32_bf16 v[44:47], a[156:159], v[92:95], v[44:47]\ns_waitcnt lgkmcnt(3)\nv_mfma_f32_16x16x32_bf16 v[16:19], a[160:163], v[80:83], v[16:19]\nds_read_b128 v[92:95], v2 offset:26880\nv_mfma_f32_16x16x32_bf16 v[20:23], a[168:171], v[80:83], v[20:23]\nds_read_b128 v[72:75], v1 offset:10240\nv_mfma_f32_16x16x32_bf16 v[24:27], v[64:67], v[80:83], v[24:27]\n" \
"v_mfma_f32_16x16x32_bf16 v[28:31], a[176:179], v[80:83], v[28:31]\ns_waitcnt lgkmcnt(4)\nv_mfma_f32_16x16x32_bf16 v[32:35], a[160:163], v[84:87], v[32:35]\nds_read_b128 v[80:83], v2 offset:28672\nv_mfma_f32_16x16x32_bf16 v[36:39], a[168:171], v[84:87], v[36:39]\nv_mfma_f32_16x16x32_bf16 v[40:43], v[64:67], v[84:87], v[40:43]\nv_mfma_f32_16x16x32_bf16 v[44:47], a[176:179], v[84:87], v[44:47]\ns_waitcnt lgkmcnt(3)\n" \
"v_mfma_f32_16x16x32_bf16 v[16:19], a[164:167], v[88:91], v[16:19]\nds_read_b128 v[84:87], v2 offset:28928\nv_mfma_f32_16x16x32_bf16 v[20:23], a[172:175], v[88:91], v[20:23]\nds_read_b128 v[76:79], v1 offset:11264\nv_mfma_f32_16x16x32_bf16 v[24:27], v[68:71], v[88:91], v[24:27]\nv_mfma_f32_16x16x32_bf16 v[28:31], a[180:183], v[88:91], v[28:31]\ns_waitcnt lgkmcnt(4)\nv_mfma_f32_16x16x32_bf16 v[32:35], a[164:167], v[92:95], v[32:35]\n" \
"ds_read_b128 v[88:91], v2 offset:30720\nv_mfma_f32_16x16x32_bf16 v[36:39], a[172:175], v[92:95], v[36:39]\nv_mfma_f32_16x16x32_bf16 v[40:43], v[68:71], v[92:95], v[40:43]\nv_mfma_f32_16x16x32_bf16 v[44:47], a[180:183], v[92:95], v[44:47]\ns_waitcnt lgkmcnt(3)\nv_mfma_f32_16x16x32_bf16 v[16:19], a[184:187], v[80:83], v[16:19]\nds_read_b128 v[92:95], v2 offset:30976\nv_mfma_f32_16x16x32_bf16 v[20:23], a[192:195], v[80:83], v[20:23]\n" \
"v_mfma_f32_16x16x32_bf16 v[24:27], a[200:203], v[80:83], v[24:27]\nv_mfma_f32_16x16x32_bf16 v[28:31], v[72:75], v[80:83], v[28:31]\ns_waitcnt lgkmcnt(3)\nv_mfma_f32_16x16x32_bf16 v[32:35], a[184:187], v[84:87], v[32:35]\ns_waitcnt vmcnt(0)\ns_barrier\nds_read_b128 v[48:51], v2 offset:32768\nv_mfma_f32_16x16x32_bf16 v[36:39], a[192:195], v[84:87], v[36:39]\n" \
"s_add_u32 m0, s57, 0\nv_mfma_f32_16x16x32_bf16 v[40:43], a[200:203], v[84:87], v[40:43]\ns_add_u32 s60, s41, 98304\nbuffer_load_dwordx4 v0, s[36:39], s60 offen offset:0 sc1 lds\nv_mfma_f32_16x16x32_bf16 v[44:47], v[72:75], v[84:87], v[44:47]\ns_waitcnt lgkmcnt(2)\nv_mfma_f32_16x16x32_bf16 v[16:19], a[188:191], v[88:91], v[16:19]\nds_read_b128 v[52:55], v2 offset:33024\n" \
"v_mfma_f32_16x16x32_bf16 v[20:23], a[196:199], v[88:91], v[20:23]\nv_mfma_f32_16x16x32_bf16 v[24:27], a[204:207], v[88:91], v[24:27]\nbuffer_load_dwordx4 v0, s[36:39], s60 offen offset:1024 sc1 lds\nv_mfma_f32_16x16x32_bf16 v[28:31], v[76:79], v[88:91], v[28:31]\ns_waitcnt lgkmcnt(2)\nv_mfma_f32_16x16x32_bf16 v[32:35], a[188:191], v[92:95], v[32:35]\nds_read_b128 v[56:59], v2 offset:34816\nv_mfma_f32_16x16x32_bf16 v[36:39], a[196:199], v[92:95], v[36:39]\n" \
"v_mfma_f32_16x16x32_bf16 v[40:43], a[204:207], v[92:95], v[40:43]\nbuffer_load_dwordx4 v0, s[36:39], s60 offen offset:2048 sc1 lds\nv_mfma_f32_16x16x32_bf16 v[44:47], v[76:79], v[92:95], v[44:47]\ns_waitcnt lgkmcnt(2)\nv_mfma_f32_16x16x32_bf16 v[16:19], a[208:211], v[48:51], v[16:19]\nds_read_b128 v[60:63], v2 offset:35072\nv_mfma_f32_16x16x32_bf16 v[20:23], a[216:219], v[48:51], v[20:23]\nds_read_b128 v[72:75], v1 offset:12288\n" \
"v_mfma_f32_16x16x32_bf16 v[24:27], a[224:227], v[48:51], v[24:27]\nbuffer_load_dwordx4 v0, s[36:39], s60 offen offset:3072 sc1 lds\nv_mfma_f32_16x16x32_bf16 v[28:31], a[232:235], v[48:51], v[28:31]\ns_waitcnt lgkmcnt(3)\nv_mfma_f32_16x16x32_bf16 v[32:35], a[208:211], v[52:55], v[32:35]\nds_read_b128 v[48:51], v2 offset:36864\nv_mfma_f32_16x16x32_bf16 v[36:39], a[216:219], v[52:55], v[36:39]\nv_mfma_f32_16x16x32_bf16 v[40:43], a[224:227], v[52:55], v[40:43]\n" \
"v_mfma_f32_16x16x32_bf16 v[44:47], a[232:235], v[52:55], v[44:47]\ns_waitcnt lgkmcnt(3)\nv_mfma_f32_16x16x32_bf16 v[16:19], a[212:215], v[56:59], v[16:19]\nds_read_b128 v[52:55], v2 offset:37120\nv_mfma_f32_16x16x32_bf16 v[20:23], a[220:223], v[56:59], v[20:23]\nds_read_b128 v[76:79], v1 offset:13312\nv_mfma_f32_16x16x32_bf16 v[24:27], a[228:231], v[56:59], v[24:27]\nv_mfma_f32_16x16x32_bf16 v[28:31], a[236:239], v[56:59], v[28:31]\n" \
"s_waitcnt lgkmcnt(4)\nv_mfma_f32_16x16x32_bf16 v[32:35], a[212:215], v[60:63], v[32:35]\nds_read_b128 v[56:59], v2 offset:38912\nv_mfma_f32_16x16x32_bf16 v[36:39], a[220:223], v[60:63], v[36:39]\nv_mfma_f32_16x16x32_bf16 v[40:43], a[228:231], v[60:63], v[40:43]\nv_mfma_f32_16x16x32_bf16 v[44:47], a[236:239], v[60:63], v[44:47]\ns_waitcnt lgkmcnt(3)\nv_mfma_f32_16x16x32_bf16 v[16:19], a[240:243], v[48:51], v[16:19]\n" \
"ds_read_b128 v[60:63], v2 offset:39168\nv_mfma_f32_16x16x32_bf16 v[20:23], v[72:75], v[48:51], v[20:23]\nds_read_b128 v[64:67], v1 offset:14336\nv_mfma_f32_16x16x32_bf16 v[24:27], a[248:251], v[48:51], v[24:27]\nv_mfma_f32_16x16x32_bf16 v[28:31], v[96:99], v[48:51], v[28:31]\ns_waitcnt lgkmcnt(4)\nv_mfma_f32_16x16x32_bf16 v[32:35], a[240:243], v[52:55], v[32:35]\nds_read_b128 v[48:51], v2 offset:40960\n" \
"v_mfma_f32_16x16x32_bf16 v[36:39], v[72:75], v[52:55], v[36:39]\nv_mfma_f32_16x16x32_bf16 v[40:43], a[248:251], v[52:55], v[40:43]\nv_mfma_f32_16x16x32_bf16 v[44:47], v[96:99], v[52:55], v[44:47]\ns_waitcnt lgkmcnt(3)\nv_mfma_f32_16x16x32_bf16 v[16:19], a[244:247], v[56:59], v[16:19]\nds_read_b128 v[52:55], v2 offset:41216\nv_mfma_f32_16x16x32_bf16 v[20:23], v[76:79], v[56:59], v[20:23]\nds_read_b128 v[68:71], v1 offset:15360\n" \
"v_mfma_f32_16x16x32_bf16 v[24:27], a[252:255], v[56:59], v[24:27]\nv_mfma_f32_16x16x32_bf16 v[28:31], v[100:103], v[56:59], v[28:31]\ns_waitcnt lgkmcnt(4)\nv_mfma_f32_16x16x32_bf16 v[32:35], a[244:247], v[60:63], v[32:35]\nds_read_b128 v[56:59], v2 offset:43008\nv_mfma_f32_16x16x32_bf16 v[36:39], v[76:79], v[60:63], v[36:39]\nv_mfma_f32_16x16x32_bf16 v[40:43], a[252:255], v[60:63], v[40:43]\nv_mfma_f32_16x16x32_bf16 v[44:47], v[100:103], v[60:63], v[44:47]\n" \
"s_waitcnt lgkmcnt(3)\nv_mfma_f32_16x16x32_bf16 v[16:19], v[104:107], v[48:51], v[16:19]\nds_read_b128 v[60:63], v2 offset:43264\nv_mfma_f32_16x16x32_bf16 v[20:23], v[112:115], v[48:51], v[20:23]\nds_read_b128 v[72:75], v1 offset:16384\nv_mfma_f32_16x16x32_bf16 v[24:27], v[64:67], v[48:51], v[24:27]\nv_mfma_f32_16x16x32_bf16 v[28:31], v[120:123], v[48:51], v[28:31]\ns_waitcnt lgkmcnt(4)\n" \
"v_mfma_f32_16x16x32_bf16 v[32:35], v[104:107], v[52:55], v[32:35]\nds_read_b128 v[48:51], v2 offset:45056\nv_mfma_f32_16x16x32_bf16 v[36:39], v[112:115], v[52:55], v[36:39]\nv_mfma_f32_16x16x32_bf16 v[40:43], v[64:67], v[52:55], v[40:43]\nv_mfma_f32_16x16x32_bf16 v[44:47], v[120:123], v[52:55], v[44:47]\ns_waitcnt lgkmcnt(3)\nv_mfma_f32_16x16x32_bf16 v[16:19], v[108:111], v[56:59], v[16:19]\nds_read_b128 v[52:55], v2 offset:45312\n" \
"v_mfma_f32_16x16x32_bf16 v[20:23], v[116:119], v[56:59], v[20:23]\nds_read_b128 v[76:79], v1 offset:17408\nv_mfma_f32_16x16x32_bf16 v[24:27], v[68:71], v[56:59], v[24:27]\nv_mfma_f32_16x16x32_bf16 v[28:31], v[124:127], v[56:59], v[28:31]\ns_waitcnt lgkmcnt(4)\nv_mfma_f32_16x16x32_bf16 v[32:35], v[108:111], v[60:63], v[32:35]\nds_read_b128 v[56:59], v2 offset:47104\nv_mfma_f32_16x16x32_bf16 v[36:39], v[116:119], v[60:63], v[36:39]\n" \
"v_mfma_f32_16x16x32_bf16 v[40:43], v[68:71], v[60:63], v[40:43]\nv_mfma_f32_16x16x32_bf16 v[44:47], v[124:127], v[60:63], v[44:47]\ns_waitcnt lgkmcnt(3)\nv_mfma_f32_16x16x32_bf16 v[16:19], v[128:131], v[48:51], v[16:19]\nds_read_b128 v[60:63], v2 offset:47360\nv_mfma_f32_16x16x32_bf16 v[20:23], v[136:139], v[48:51], v[20:23]\nv_mfma_f32_16x16x32_bf16 v[24:27], v[144:147], v[48:51], v[24:27]\nv_mfma_f32_16x16x32_bf16 v[28:31], v[72:75], v[48:51], v[28:31]\n" \
"s_waitcnt lgkmcnt(3)\nv_mfma_f32_16x16x32_bf16 v[32:35], v[128:131], v[52:55], v[32:35]\ns_waitcnt lgkmcnt(0)\ns_waitcnt vmcnt(0)\ns_barrier\nds_read_b128 v[48:51], v2 offset:0\nv_mfma_f32_16x16x32_bf16 v[36:39], v[136:139], v[52:55], v[36:39]\nv_mfma_f32_16x16x32_bf16 v[40:43], v[144:147], v[52:55], v[40:43]\n" \
"v_mfma_f32_16x16x32_bf16 v[44:47], v[72:75], v[52:55], v[44:47]\nv_mfma_f32_16x16x32_bf16 v[16:19], v[132:135], v[56:59], v[16:19]\nds_read_b128 v[52:55], v2 offset:256\nv_mfma_f32_16x16x32_bf16 v[20:23], v[140:143], v[56:59], v[20:23]\nv_mfma_f32_16x16x32_bf16 v[24:27], v[148:151], v[56:59], v[24:27]\nv_mfma_f32_16x16x32_bf16 v[28:31], v[76:79], v[56:59], v[28:31]\nv_mfma_f32_16x16x32_bf16 v[32:35], v[132:135], v[60:63], v[32:35]\nds_read_b128 v[56:59], v2 offset:2048\n" \
"v_mfma_f32_16x16x32_bf16 v[36:39], v[140:143], v[60:63], v[36:39]\nv_mfma_f32_16x16x32_bf16 v[40:43], v[148:151], v[60:63], v[40:43]\nv_mfma_f32_16x16x32_bf16 v[44:47], v[76:79], v[60:63], v[44:47]\ns_waitcnt lgkmcnt(2)\nv_mfma_f32_16x16x32_bf16 v[16:19], v[152:155], v[48:51], v[16:19]\nds_read_b128 v[60:63], v2 offset:2304\nv_mfma_f32_16x16x32_bf16 v[20:23], v[160:163], v[48:51], v[20:23]\nds_read_b128 v[72:75], v1 offset:18432\n" \
"v_mfma_f32_16x16x32_bf16 v[24:27], v[168:171], v[48:51], v[24:27]\nv_mfma_f32_16x16x32_bf16 v[28:31], v[176:179], v[48:51], v[28:31]\ns_waitcnt lgkmcnt(3)\nv_mfma_f32_16x16x32_bf16 v[32:35], v[152:155], v[52:55], v[32:35]\nds_read_b128 v[48:51], v2 offset:4096\nv_mfma_f32_16x16x32_bf16 v[36:39], v[160:163], v[52:55], v[36:39]\nv_mfma_f32_16x16x32_bf16 v[40:43], v[168:171], v[52:55], v[40:43]\nv_mfma_f32_16x16x32_bf16 v[44:47], v[176:179], v[52:55], v[44:47]\n" \
"s_waitcnt lgkmcnt(3)\nv_mfma_f32_16x16x32_bf16 v[16:19], v[156:159], v[56:59], v[16:19]\nds_read_b128 v[52:55], v2 offset:4352\nv_mfma_f32_16x16x32_bf16 v[20:23], v[164:167], v[56:59], v[20:23]\nds_read_b128 v[76:79], v1 offset:19456\nv_mfma_f32_16x16x32_bf16 v[24:27], v[172:175], v[56:59], v[24:27]\nv_mfma_f32_16x16x32_bf16 v[28:31], v[180:183], v[56:59], v[28:31]\ns_waitcnt lgkmcnt(4)\n" \
"v_mfma_f32_16x16x32_bf16 v[32:35], v[156:159], v[60:63], v[32:35]\nds_read_b128 v[56:59], v2 offset:6144\nv_mfma_f32_16x16x32_bf16 v[36:39], v[164:167], v[60:63], v[36:39]\nv_mfma_f32_16x16x32_bf16 v[40:43], v[172:175], v[60:63], v[40:43]\nv_mfma_f32_16x16x32_bf16 v[44:47], v[180:183], v[60:63], v[44:47]\ns_waitcnt lgkmcnt(3)\nv_mfma_f32_16x16x32_bf16 v[16:19], v[184:187], v[48:51], v[16:19]\nds_read_b128 v[60:63], v2 offset:6400\n" \
"v_mfma_f32_16x16x32_bf16 v[20:23], v[72:75], v[48:51], v[20:23]\nds_read_b128 v[64:67], v1 offset:20480\nv_mfma_f32_16x16x32_bf16 v[24:27], v[192:195], v[48:51], v[24:27]\nv_mfma_f32_16x16x32_bf16 v[28:31], v[200:203], v[48:51], v[28:31]\ns_waitcnt lgkmcnt(4)\nv_mfma_f32_16x16x32_bf16 v[32:35], v[184:187], v[52:55], v[32:35]\nds_read_b128 v[48:51], v2 offset:8192\nv_mfma_f32_16x16x32_bf16 v[36:39], v[72:75], v[52:55], v[36:39]\n" \
"v_mfma_f32_16x16x32_bf16 v[40:43], v[192:195], v[52:55], v[40:43]\nv_mfma_f32_16x16x32_bf16 v[44:47], v[200:203], v[52:55], v[44:47]\ns_waitcnt lgkmcnt(3)\nv_mfma_f32_16x16x32_bf16 v[16:19], v[188:191], v[56:59], v[16:19]\nds_read_b128 v[52:55], v2 offset:10240\nv_mfma_f32_16x16x32_bf16 v[20:23], v[76:79], v[56:59], v[20:23]\nds_read_b128 v[68:71], v1 offset:21504\nv_mfma_f32_16x16x32_bf16 v[24:27], v[196:199], v[56:59], v[24:27]\n" \
"buffer_load_dwordx4 v[80:83], v3, s[36:39], s42 offen offset:0 sc1\nv_mfma_f32_16x16x32_bf16 v[28:31], v[204:207], v[56:59], v[28:31]\ns_waitcnt lgkmcnt(4)\nv_mfma_f32_16x16x32_bf16 v[32:35], v[188:191], v[60:63], v[32:35]\nds_read_b128 v[56:59], v2 offset:12288\nv_mfma_f32_16x16x32_bf16 v[36:39], v[76:79], v[60:63], v[36:39]\nds_read_b128 v[72:75], v1 offset:22528\nv_mfma_f32_16x16x32_bf16 v[40:43], v[196:199], v[60:63], v[40:43]\n" \
"buffer_load_dwordx4 v[84:87], v3, s[36:39], s42 offen offset:1024 sc1\nv_mfma_f32_16x16x32_bf16 v[44:47], v[204:207], v[60:63], v[44:47]\ns_waitcnt lgkmcnt(4)\nv_mfma_f32_16x16x32_bf16 v[16:19], v[208:211], v[48:51], v[16:19]\nds_read_b128 v[60:63], v2 offset:14336\nv_mfma_f32_16x16x32_bf16 v[20:23], v[216:219], v[48:51], v[20:23]\nds_read_b128 v[76:79], v1 offset:23552\nv_mfma_f32_16x16x32_bf16 v[24:27], v[64:67], v[48:51], v[24:27]\n" \
"buffer_load_dwordx4 v[88:91], v3, s[36:39], s42 offen offset:2048 sc1\nv_mfma_f32_16x16x32_bf16 v[28:31], v[224:227], v[48:51], v[28:31]\ns_waitcnt lgkmcnt(5)\nv_mfma_f32_16x16x32_bf16 v[16:19], v[212:215], v[52:55], v[16:19]\nds_read_b128 v[48:51], v2 offset:8448\nv_mfma_f32_16x16x32_bf16 v[20:23], v[220:223], v[52:55], v[20:23]\ns_waitcnt lgkmcnt(5)\nv_mfma_f32_16x16x32_bf16 v[24:27], v[68:71], v[52:55], v[24:27]\n" \
"buffer_load_dwordx4 v[92:95], v3, s[36:39], s42 offen offset:3072 sc1\nv_mfma_f32_16x16x32_bf16 v[28:31], v[228:231], v[52:55], v[28:31]\ns_waitcnt lgkmcnt(4)\nv_mfma_f32_16x16x32_bf16 v[16:19], v[232:235], v[56:59], v[16:19]\nds_read_b128 v[52:55], v2 offset:10496\nv_mfma_f32_16x16x32_bf16 v[20:23], v[240:243], v[56:59], v[20:23]\nv_mfma_f32_16x16x32_bf16 v[24:27], v[248:251], v[56:59], v[24:27]\ns_waitcnt lgkmcnt(4)\n" \
"v_mfma_f32_16x16x32_bf16 v[28:31], v[72:75], v[56:59], v[28:31]\ns_waitcnt lgkmcnt(3)\nv_mfma_f32_16x16x32_bf16 v[16:19], v[236:239], v[60:63], v[16:19]\nds_read_b128 v[56:59], v2 offset:12544\nv_mfma_f32_16x16x32_bf16 v[20:23], v[244:247], v[60:63], v[20:23]\nv_mfma_f32_16x16x32_bf16 v[24:27], v[252:255], v[60:63], v[24:27]\ns_waitcnt lgkmcnt(3)\nv_mfma_f32_16x16x32_bf16 v[28:31], v[76:79], v[60:63], v[28:31]\n" \
"s_waitcnt lgkmcnt(2)\nv_mfma_f32_16x16x32_bf16 v[32:35], v[208:211], v[48:51], v[32:35]\nds_read_b128 v[60:63], v2 offset:14592\nv_mfma_f32_16x16x32_bf16 v[36:39], v[216:219], v[48:51], v[36:39]\nv_cvt_pk_bf16_f32 v20, v16, v20\nv_cvt_pk_bf16_f32 v21, v17, v21\nv_mfma_f32_16x16x32_bf16 v[40:43], v[64:67], v[48:51], v[40:43]\nv_cvt_pk_bf16_f32 v22, v18, v22\n" \
"v_cvt_pk_bf16_f32 v23, v19, v23\nv_mfma_f32_16x16x32_bf16 v[44:47], v[224:227], v[48:51], v[44:47]\ns_waitcnt lgkmcnt(2)\nds_write_b128 v5, v[20:23] offset:0\nv_mfma_f32_16x16x32_bf16 v[32:35], v[212:215], v[52:55], v[32:35]\nv_cvt_pk_bf16_f32 v28, v24, v28\nv_cvt_pk_bf16_f32 v29, v25, v29\nv_mfma_f32_16x16x32_bf16 v[36:39], v[220:223], v[52:55], v[36:39]\n" \
"v_cvt_pk_bf16_f32 v30, v26, v30\nv_cvt_pk_bf16_f32 v31, v27, v31\nv_mfma_f32_16x16x32_bf16 v[40:43], v[68:71], v[52:55], v[40:43]\nv_mov_b64 v[48:49], 0\nv_mov_b64 v[50:51], 0\nv_mfma_f32_16x16x32_bf16 v[44:47], v[228:231], v[52:55], v[44:47]\ns_cmp_eq_u32 %[w], 1\nds_write_b128 v5, v[28:31] offset:512\n" \
"s_cbranch_scc1 .Lqk_c1%=\ns_cmp_eq_u32 %[w], 2\ns_cbranch_scc1 .Lqk_c2%=\ns_cmp_eq_u32 %[w], 3\ns_cbranch_scc1 .Lqk_c3%=\n.Lqk_c0%=:\ns_waitcnt lgkmcnt(3)\nv_mfma_f32_16x16x32_bf16 v[32:35], v[232:235], v[56:59], v[32:35]\n" \
"v_mov_b64 v[52:53], 0\nv_mov_b64 v[54:55], 0\nv_mfma_f32_16x16x32_bf16 v[36:39], v[240:243], v[56:59], v[36:39]\ns_waitcnt lgkmcnt(2)\nds_read_b128 v[64:67], v1 offset:0\nv_mfma_f32_16x16x32_bf16 v[32:35], v[236:239], v[60:63], v[32:35]\nds_read_b128 v[68:71], v1 offset:1024\nv_mfma_f32_16x16x32_bf16 v[36:39], v[244:247], v[60:63], v[36:39]\n" \
"s_bfe_i32 s54, s40, 0x10000\nv_mfma_f32_16x16x32_bf16 v[40:43], v[248:251], v[56:59], v[40:43]\nv_mfma_f32_16x16x32_bf16 v[44:47], v[72:75], v[56:59], v[44:47]\nv_cvt_pk_bf16_f32 v36, v32, v36\nv_cvt_pk_bf16_f32 v37, v33, v37\nv_mfma_f32_16x16x32_bf16 v[40:43], v[252:255], v[60:63], v[40:43]\nv_cvt_pk_bf16_f32 v38, v34, v38\nv_cvt_pk_bf16_f32 v39, v35, v39\n" \
"v_mfma_f32_16x16x32_bf16 v[44:47], v[76:79], v[60:63], v[44:47]\nds_read_b128 v[72:75], v1 offset:2048\nds_read_b128 v[76:79], v1 offset:3072\nv_mov_b64 v[56:57], 0\nv_mov_b64 v[58:59], 0\nv_mov_b64 v[60:61], 0\nv_mov_b64 v[62:63], 0\n.Lqk_poll0%=:\n" \
"s_waitcnt vmcnt(0)\nv_smfmac_f32_16x16x64_bf16 v[48:51], v[80:83], a[0:7], v4\nds_write_b128 v0, v[80:83] offset:16384\nv_max3_u32 v16, v80, v82, v84\nv_smfmac_f32_16x16x64_bf16 v[52:55], v[80:83], a[8:15], v4\nv_max3_u32 v16, v16, v86, v88\nv_smfmac_f32_16x16x64_bf16 v[56:59], v[80:83], a[16:23], v4\nds_write_b128 v0, v[84:87] offset:17408\n" \
"v_max3_u32 v16, v16, v90, v92\nv_max_u32 v16, v16, v94\nv_smfmac_f32_16x16x64_bf16 v[60:63], v[80:83], a[24:31], v4\nv_cmp_eq_u32 vcc, -1, v16\nv_smfmac_f32_16x16x64_bf16 v[48:51], v[84:87], a[32:39], v4\nds_write_b128 v0, v[88:91] offset:18432\ns_cbranch_vccnz .Lqk_re0%=\ns_waitcnt lgkmcnt(5)\n" \
"v_smfmac_f32_16x16x64_bf16 v[52:55], v[84:87], v[64:71], v4\nv_smfmac_f32_16x16x64_bf16 v[56:59], v[84:87], a[40:47], v4\nds_write_b128 v0, v[92:95] offset:19456\nv_smfmac_f32_16x16x64_bf16 v[60:63], v[84:87], a[48:55], v4\nv_smfmac_f32_16x16x64_bf16 v[48:51], v[88:91], a[56:63], v4\nv_smfmac_f32_16x16x64_bf16 v[52:55], v[88:91], a[64:71], v4\ns_waitcnt lgkmcnt(0)\n.Lqk_jn0%=:\n" \
"s_barrier\ns_memrealtime s[68:69]\nds_read_b128 v[80:83], v0 offset:20480\nds_read_b128 v[64:67], v1 offset:4096\nds_read_b128 v[68:71], v1 offset:5120\nv_mov_b32 v24, s40\nv_smfmac_f32_16x16x64_bf16 v[56:59], v[88:91], v[72:79], v4\nds_read_b128 v[72:75], v1 offset:6144\n" \
"ds_read_b128 v[76:79], v1 offset:7168\nv_smfmac_f32_16x16x64_bf16 v[60:63], v[88:91], a[72:79], v4\nds_read_b128 v[84:87], v0 offset:21504\nds_read_b128 v[88:91], v0 offset:22528\nv_smfmac_f32_16x16x64_bf16 v[48:51], v[92:95], a[80:87], v4\nds_write_b128 v5, v[36:39] offset:256\nv_cvt_pk_bf16_f32 v44, v40, v44\nv_cvt_pk_bf16_f32 v45, v41, v45\n" \
"v_cvt_pk_bf16_f32 v46, v42, v46\nv_cvt_pk_bf16_f32 v47, v43, v47\nv_smfmac_f32_16x16x64_bf16 v[52:55], v[92:95], a[88:95], v4\nv_smfmac_f32_16x16x64_bf16 v[56:59], v[92:95], a[96:103], v4\ns_waitcnt lgkmcnt(5)\nv_smfmac_f32_16x16x64_bf16 v[60:63], v[92:95], v[64:71], v4\ns_sub_u32 s88, s86, s89\nds_read_b128 v[92:95], v0 offset:23552\n" \
"ds_read_b128 v[64:67], v1 offset:8192\nds_read_b128 v[68:71], v1 offset:9216\nv_smfmac_f32_16x16x64_bf16 v[48:51], v[80:83], a[104:111], v4\ns_min_u32 s88, s88, 150\nds_write_b128 v5, v[44:47] offset:768\nv_smfmac_f32_16x16x64_bf16 v[52:55], v[80:83], a[112:119], v4\ns_add_u32 s88, s88, s68\ns_mov_b32 s89, s68\n" \
"v_mov_b32 v25, s88\nv_smfmac_f32_16x16x64_bf16 v[56:59], v[80:83], a[120:127], v4\nbuffer_store_dwordx2 v[24:25], off, s[36:39], %[sg] sc1\nv_smfmac_f32_16x16x64_bf16 v[60:63], v[80:83], a[128:135], v4\ns_waitcnt lgkmcnt(6)\nds_read_b128 v[80:83], v0 offset:24576\nv_smfmac_f32_16x16x64_bf16 v[48:51], v[84:87], a[136:143], v4\nv_smfmac_f32_16x16x64_bf16 v[52:55], v[84:87], v[72:79], v4\n" \
"ds_read_b128 v[72:75], v1 offset:10240\nds_read_b128 v[76:79], v1 offset:11264\nv_smfmac_f32_16x16x64_bf16 v[56:59], v[84:87], a[144:151], v4\nv_smfmac_f32_16x16x64_bf16 v[60:63], v[84:87], a[152:159], v4\ns_waitcnt lgkmcnt(8)\nds_read_b128 v[84:87], v0 offset:25600\nv_smfmac_f32_16x16x64_bf16 v[48:51], v[88:91], a[160:167], v4\ns_waitcnt lgkmcnt(4)\n" \
"s_barrier\nv_smfmac_f32_16x16x64_bf16 v[52:55], v[88:91], a[168:175], v4\nv_smfmac_f32_16x16x64_bf16 v[56:59], v[88:91], v[64:71], v4\nds_read_b128 v[64:67], v1 offset:12288\nds_read_b128 v[68:71], v1 offset:13312\nv_smfmac_f32_16x16x64_bf16 v[60:63], v[88:91], a[176:183], v4\nds_read_b128 v[88:91], v0 offset:26624\nv_smfmac_f32_16x16x64_bf16 v[48:51], v[92:95], a[184:191], v4\n" \
"ds_read_b128 v[16:19], v7 offset:0\nv_smfmac_f32_16x16x64_bf16 v[52:55], v[92:95], a[192:199], v4\nv_smfmac_f32_16x16x64_bf16 v[56:59], v[92:95], a[200:207], v4\ns_waitcnt lgkmcnt(5)\nds_read_b128 v[20:23], v7 offset:64\nv_smfmac_f32_16x16x64_bf16 v[60:63], v[92:95], v[72:79], v4\nds_read_b128 v[92:95], v0 offset:27648\nds_read_b128 v[72:75], v1 offset:14336\n" \
"ds_read_b128 v[76:79], v1 offset:15360\nv_smfmac_f32_16x16x64_bf16 v[48:51], v[80:83], a[208:215], v4\nds_read_b128 v[24:27], v7 offset:2048\nv_smfmac_f32_16x16x64_bf16 v[52:55], v[80:83], a[216:223], v4\nv_smfmac_f32_16x16x64_bf16 v[56:59], v[80:83], a[224:231], v4\nds_read_b128 v[28:31], v7 offset:2112\nv_smfmac_f32_16x16x64_bf16 v[60:63], v[80:83], a[232:239], v4\ns_waitcnt lgkmcnt(10)\n" \
"ds_read_b128 v[80:83], v0 offset:28672\nv_smfmac_f32_16x16x64_bf16 v[48:51], v[84:87], a[240:247], v4\ns_waitcnt lgkmcnt(9)\nds_read_b128 v[32:35], v7 offset:4096\nv_smfmac_f32_16x16x64_bf16 v[52:55], v[84:87], v[64:71], v4\nds_read_b128 v[64:67], v1 offset:16384\nds_read_b128 v[68:71], v1 offset:17408\nv_smfmac_f32_16x16x64_bf16 v[56:59], v[84:87], a[248:255], v4\n" \
"ds_read_b128 v[36:39], v7 offset:4160\nv_smfmac_f32_16x16x64_bf16 v[60:63], v[84:87], v[96:103], v4\ns_waitcnt lgkmcnt(12)\nds_read_b128 v[84:87], v0 offset:29696\nv_smfmac_f32_16x16x64_bf16 v[48:51], v[88:91], v[104:111], v4\nds_read_b128 v[40:43], v7 offset:6144\nv_smfmac_f32_16x16x64_bf16 v[52:55], v[88:91], v[112:119], v4\ns_waitcnt lgkmcnt(9)\n" \
"v_smfmac_f32_16x16x64_bf16 v[56:59], v[88:91], v[72:79], v4\nds_read_b128 v[72:75], v1 offset:18432\nds_read_b128 v[76:79], v1 offset:19456\nv_smfmac_f32_16x16x64_bf16 v[60:63], v[88:91], v[120:127], v4\nds_read_b128 v[88:91], v0 offset:30720\nv_smfmac_f32_16x16x64_bf16 v[48:51], v[92:95], v[128:135], v4\nds_read_b128 v[44:47], v7 offset:6208\nv_smfmac_f32_16x16x64_bf16 v[52:55], v[92:95], v[136:143], v4\n" \
"v_smfmac_f32_16x16x64_bf16 v[56:59], v[92:95], v[144:151], v4\ns_waitcnt lgkmcnt(7)\nv_smfmac_f32_16x16x64_bf16 v[60:63], v[92:95], v[64:71], v4\nds_read_b128 v[92:95], v0 offset:31744\nds_read_b128 v[64:67], v1 offset:20480\nds_read_b128 v[68:71], v1 offset:21504\nv_smfmac_f32_16x16x64_bf16 v[48:51], v[80:83], v[152:159], v4\nv_smfmac_f32_16x16x64_bf16 v[52:55], v[80:83], v[160:167], v4\n" \
"v_smfmac_f32_16x16x64_bf16 v[56:59], v[80:83], v[168:175], v4\nv_smfmac_f32_16x16x64_bf16 v[60:63], v[80:83], v[176:183], v4\ns_waitcnt lgkmcnt(8)\nv_smfmac_f32_16x16x64_bf16 v[48:51], v[84:87], v[184:191], v4\ns_waitcnt lgkmcnt(5)\nv_smfmac_f32_16x16x64_bf16 v[52:55], v[84:87], v[72:79], v4\nds_read_b128 v[72:75], v1 offset:22528\nds_read_b128 v[76:79], v1 offset:23552\n" \
"v_smfmac_f32_16x16x64_bf16 v[56:59], v[84:87], v[192:199], v4\nv_smfmac_f32_16x16x64_bf16 v[60:63], v[84:87], v[200:207], v4\ns_waitcnt lgkmcnt(6)\nv_smfmac_f32_16x16x64_bf16 v[48:51], v[88:91], v[208:215], v4\nv_smfmac_f32_16x16x64_bf16 v[52:55], v[88:91], v[216:223], v4\ns_waitcnt lgkmcnt(4)\nv_smfmac_f32_16x16x64_bf16 v[48:51], v[92:95], v[232:239], v4\nv_smfmac_f32_16x16x64_bf16 v[52:55], v[92:95], v[240:247], v4\n" \
"s_waitcnt lgkmcnt(2)\nv_smfmac_f32_16x16x64_bf16 v[56:59], v[88:91], v[64:71], v4\nv_add_f32 v48, v48, v50\nv_smfmac_f32_16x16x64_bf16 v[60:63], v[88:91], v[224:231], v4\nv_add_f32 v52, v52, v54\nv_add_f32 v49, v49, v51\nv_smfmac_f32_16x16x64_bf16 v[56:59], v[92:95], v[248:255], v4\ns_waitcnt lgkmcnt(0)\n" \
"v_add_f32 v53, v53, v55\nv_cvt_pk_bf16_f32 v50, v48, v52\nv_smfmac_f32_16x16x64_bf16 v[60:63], v[92:95], v[72:79], v4\nv_cvt_pk_bf16_f32 v51, v49, v53\nds_write2_b32 v6, v50, v51 offset0:0 offset1:4\ns_nop 1\nv_add_f32 v56, v56, v58\nv_add_f32 v57, v57, v59\n" \
"s_nop 1\nv_add_f32 v60, v60, v62\nv_add_f32 v61, v61, v63\nv_cvt_pk_bf16_f32 v62, v56, v60\nv_cvt_pk_bf16_f32 v63, v57, v61\nds_write2_b32 v6, v62, v63 offset0:36 offset1:40\nv_mov_b32 v61, s54\ns_waitcnt lgkmcnt(0)\n" \
"s_barrier\nds_read_b128 v[80:83], v8 offset:0\nds_read_b128 v[84:87], v8 offset:576\nds_read_b128 v[88:91], v8 offset:1152\nds_read_b128 v[92:95], v8 offset:1728\nds_read_b128 v[64:67], v9 offset:0\nds_read_b128 v[68:71], v9 offset:576\nds_read_b128 v[72:75], v9 offset:1152\n" \
"ds_read_b128 v[76:79], v9 offset:1728\nds_read_b32 v60, v0 offset:53760\ns_waitcnt lgkmcnt(8)\nv_mfma_f32_16x16x32_bf16 v[52:55], v[16:19], v[80:83], 0\nv_mfma_f32_16x16x32_bf16 v[56:59], v[20:23], v[80:83], 0\ns_waitcnt lgkmcnt(7)\nv_mfma_f32_16x16x32_bf16 v[52:55], v[24:27], v[84:87], v[52:55]\nv_mfma_f32_16x16x32_bf16 v[56:59], v[28:31], v[84:87], v[56:59]\n" \
"s_waitcnt lgkmcnt(6)\nv_mfma_f32_16x16x32_bf16 v[52:55], v[32:35], v[88:91], v[52:55]\nv_mfma_f32_16x16x32_bf16 v[56:59], v[36:39], v[88:91], v[56:59]\ns_waitcnt lgkmcnt(5)\nv_mfma_f32_16x16x32_bf16 v[52:55], v[40:43], v[92:95], v[52:55]\nv_mfma_f32_16x16x32_bf16 v[56:59], v[44:47], v[92:95], v[56:59]\ns_waitcnt lgkmcnt(4)\nv_mfma_f32_16x16x32_bf16 v[48:51], v[64:67], v[80:83], 0\n" \
"s_waitcnt lgkmcnt(3)\nv_mfma_f32_16x16x32_bf16 v[48:51], v[68:71], v[84:87], v[48:51]\ns_waitcnt lgkmcnt(2)\nds_read_b128 v[80:83], v2 offset:0\nv_mfma_f32_16x16x32_bf16 v[48:51], v[72:75], v[88:91], v[48:51]\ns_waitcnt lgkmcnt(2)\nds_read_b128 v[84:87], v2 offset:256\nv_mfma_f32_16x16x32_bf16 v[48:51], v[76:79], v[92:95], v[48:51]\n" \
"ds_read_b128 v[88:91], v2 offset:2048\nv_max3_f32 v62, v52, v53, v54\nv_max3_f32 v63, v55, v56, v57\nv_max3_f32 v62, v62, v58, v59\nv_cndmask_b32 v52, v52, v56, s[62:63]\nv_cndmask_b32 v53, v53, v57, s[62:63]\nv_cndmask_b32 v54, v54, v58, s[62:63]\nv_cndmask_b32 v55, v55, v59, s[62:63]\n" \
"v_max_f32 v62, v62, v63\nv_max3_f32 v63, v62, v48, v49\nv_max3_f32 v62, v62, v48, v49\ns_waitcnt lgkmcnt(2)\nv_mfma_f32_16x16x32_bf16 v[16:19], a[0:3], v[80:83], 0\nds_read_b128 v[92:95], v2 offset:2304\nv_mfma_f32_16x16x32_bf16 v[20:23], a[8:11], v[80:83], 0\nds_read_b128 v[72:75], v1 offset:0\n" \
"v_mfma_f32_16x16x32_bf16 v[24:27], a[16:19], v[80:83], 0\nv_permlane16_swap_b32 v62, v63\nv_mfma_f32_16x16x32_bf16 v[28:31], a[24:27], v[80:83], 0\ns_waitcnt lgkmcnt(3)\nv_max_f32 v50, v62, v63\nv_max_f32 v62, v62, v63\nv_mfma_f32_16x16x32_bf16 v[32:35], a[0:3], v[84:87], 0\nds_read_b128 v[80:83], v2 offset:4096\n" \
"v_mfma_f32_16x16x32_bf16 v[36:39], a[8:11], v[84:87], 0\nv_permlane32_swap_b32 v62, v50\nv_mfma_f32_16x16x32_bf16 v[40:43], a[16:19], v[84:87], 0\nv_max_f32 v62, v62, v50\nv_mul_f32 v62, s59, v62\nv_mfma_f32_16x16x32_bf16 v[44:47], a[24:27], v[84:87], 0\ns_waitcnt lgkmcnt(3)\nv_fma_f32 v52, v52, s58, v62\n" \
"v_fma_f32 v53, v53, s58, v62\nv_mfma_f32_16x16x32_bf16 v[16:19], a[4:7], v[88:91], v[16:19]\nds_read_b128 v[84:87], v2 offset:4352\nv_fma_f32 v54, v54, s58, v62\nv_mfma_f32_16x16x32_bf16 v[20:23], a[12:15], v[88:91], v[20:23]\nds_read_b128 v[76:79], v1 offset:1024\nv_fma_f32 v55, v55, s58, v62\nv_mfma_f32_16x16x32_bf16 v[24:27], a[20:23], v[88:91], v[24:27]\n" \
"v_exp_f32 v52, v52\nv_mfma_f32_16x16x32_bf16 v[28:31], a[28:31], v[88:91], v[28:31]\ns_waitcnt lgkmcnt(4)\nv_exp_f32 v53, v53\nv_mfma_f32_16x16x32_bf16 v[32:35], a[4:7], v[92:95], v[32:35]\nds_read_b128 v[88:91], v2 offset:6144\nv_add_f32 v63, v52, v53\nv_mfma_f32_16x16x32_bf16 v[36:39], a[12:15], v[92:95], v[36:39]\n" \
"v_exp_f32 v54, v54\nv_mfma_f32_16x16x32_bf16 v[40:43], a[20:23], v[92:95], v[40:43]\nv_fma_f32 v48, v48, s58, v62\nv_add_f32 v63, v63, v54\nv_mfma_f32_16x16x32_bf16 v[44:47], a[28:31], v[92:95], v[44:47]\ns_waitcnt lgkmcnt(3)\nv_exp_f32 v55, v55\nv_mfma_f32_16x16x32_bf16 v[16:19], a[32:35], v[80:83], v[16:19]\n" \
"ds_read_b128 v[92:95], v2 offset:6400\nv_fma_f32 v49, v49, s58, v62\nv_mfma_f32_16x16x32_bf16 v[20:23], v[72:75], v[80:83], v[20:23]\nds_read_b128 v[64:67], v1 offset:2048\nv_add_f32 v63, v63, v55\nv_mfma_f32_16x16x32_bf16 v[24:27], a[40:43], v[80:83], v[24:27]\nv_exp_f32 v48, v48\nv_mfma_f32_16x16x32_bf16 v[28:31], a[48:51], v[80:83], v[28:31]\n" \
"s_waitcnt lgkmcnt(4)\nv_exp_f32 v49, v49\nv_mfma_f32_16x16x32_bf16 v[32:35], a[32:35], v[84:87], v[32:35]\nds_read_b128 v[80:83], v2 offset:8192\nv_add_f32 v50, v48, v63\nv_mfma_f32_16x16x32_bf16 v[36:39], v[72:75], v[84:87], v[36:39]\nv_add_f32 v50, v50, v49\nv_mfma_f32_16x16x32_bf16 v[40:43], a[40:43], v[84:87], v[40:43]\n" \
"s_nop 0\nv_add_f32_dpp v62, v63, v50 row_ror:8 row_mask:0xf bank_mask:0xf\nv_add_f32_dpp v50, v63, v50 row_ror:8 row_mask:0xf bank_mask:0xf\nv_mfma_f32_16x16x32_bf16 v[44:47], a[48:51], v[84:87], v[44:47]\ns_waitcnt lgkmcnt(3)\nv_permlane16_swap_b32 v62, v50\nv_mfma_f32_16x16x32_bf16 v[16:19], a[36:39], v[88:91], v[16:19]\nds_read_b128 v[84:87], v2 offset:8448\n" \
"v_add_f32 v63, v62, v50\nv_mfma_f32_16x16x32_bf16 v[20:23], v[76:79], v[88:91], v[20:23]\nds_read_b128 v[68:71], v1 offset:3072\nv_add_f32 v50, v62, v50\nv_mfma_f32_16x16x32_bf16 v[24:27], a[44:47], v[88:91], v[24:27]\ns_nop 0\nv_permlane32_swap_b32 v63, v50\nv_mfma_f32_16x16x32_bf16 v[28:31], a[52:55], v[88:91], v[28:31]\n" \
"s_waitcnt lgkmcnt(4)\nv_add_f32 v63, v63, v50\nv_mfma_f32_16x16x32_bf16 v[32:35], a[36:39], v[92:95], v[32:35]\nds_read_b128 v[88:91], v2 offset:10240\nv_mfma_f32_16x16x32_bf16 v[36:39], v[76:79], v[92:95], v[36:39]\nv_rcp_f32 v63, v63\nv_mfma_f32_16x16x32_bf16 v[40:43], a[44:47], v[92:95], v[40:43]\nv_mul_f32 v52, v52, v63\n" \
"v_mul_f32 v53, v53, v63\nv_mfma_f32_16x16x32_bf16 v[44:47], a[52:55], v[92:95], v[44:47]\ns_waitcnt lgkmcnt(3)\nv_mul_f32 v54, v54, v63\nv_mul_f32 v55, v55, v63\nv_mfma_f32_16x16x32_bf16 v[16:19], a[56:59], v[80:83], v[16:19]\nds_read_b128 v[92:95], v2 offset:10496\nv_mul_f32 v48, v48, v63\n" \
"v_mfma_f32_16x16x32_bf16 v[20:23], a[64:67], v[80:83], v[20:23]\nds_read_b128 v[72:75], v1 offset:4096\nv_mul_f32 v49, v49, v63\nv_mfma_f32_16x16x32_bf16 v[24:27], v[64:67], v[80:83], v[24:27]\nv_cvt_pk_bf16_f32 v52, v52, v53\nv_cvt_pk_bf16_f32 v53, v54, v55\nv_mfma_f32_16x16x32_bf16 v[28:31], a[72:75], v[80:83], v[28:31]\ns_waitcnt lgkmcnt(4)\n" \
"v_cvt_pk_bf16_f32 v48, v48, v49\nv_bfi_b32 v52, s61, v52, v61\nv_mfma_f32_16x16x32_bf16 v[32:35], a[56:59], v[84:87], v[32:35]\nds_read_b128 v[80:83], v2 offset:12288\nv_bfi_b32 v53, s61, v53, v61\nv_mfma_f32_16x16x32_bf16 v[36:39], a[64:67], v[84:87], v[36:39]\nv_bfi_b32 v48, s61, v48, v61\nv_mov_b32_dpp v54, v52 row_ror:8 row_mask:0xf bank_mask:0xf\n" \
"v_mfma_f32_16x16x32_bf16 v[40:43], v[64:67], v[84:87], v[40:43]\nv_mov_b32_dpp v55, v53 row_ror:8 row_mask:0xf bank_mask:0xf\nv_mfma_f32_16x16x32_bf16 v[44:47], a[72:75], v[84:87], v[44:47]\ns_waitcnt lgkmcnt(3)\nbuffer_store_dwordx4 v[52:55], v10, s[36:39], 0 offen sc1\nv_mfma_f32_16x16x32_bf16 v[16:19], a[60:63], v[88:91], v[16:19]\nds_read_b128 v[84:87], v2 offset:12544\nv_mov_b32 v49, v48\n" \
"v_mfma_f32_16x16x32_bf16 v[20:23], a[68:71], v[88:91], v[20:23]\nds_read_b128 v[76:79], v1 offset:5120\nv_mfma_f32_16x16x32_bf16 v[24:27], v[68:71], v[88:91], v[24:27]\nv_permlane32_swap_b32 v48, v49\nv_mfma_f32_16x16x32_bf16 v[28:31], a[76:79], v[88:91], v[28:31]\ns_memrealtime s[86:87]\ns_waitcnt lgkmcnt(4)\nbuffer_store_dwordx2 v[48:49], v11, s[36:39], 0 offen sc1\n" \
"v_mfma_f32_16x16x32_bf16 v[32:35], a[60:63], v[92:95], v[32:35]\nds_read_b128 v[88:91], v2 offset:14336\nv_cmp_ne_u32 vcc, 0, v60\nv_mfma_f32_16x16x32_bf16 v[36:39], a[68:71], v[92:95], v[36:39]\nv_mfma_f32_16x16x32_bf16 v[40:43], v[68:71], v[92:95], v[40:43]\nv_mfma_f32_16x16x32_bf16 v[44:47], a[76:79], v[92:95], v[44:47]\ns_cbranch_vccnz .Lqk_pj%=\ns_cmp_lt_i32 s40, 63\n" \
"s_waitcnt lgkmcnt(0)\ns_cbranch_scc1 .Lqk_pwait%=\ns_branch .Lqk_end%=\n.Lqk_re0%=:\nv_max_u32 v17, v80, v82\nv_cmp_eq_u32 vcc, -1, v17\ns_cbranch_vccz .Lqk_sk1617%=\ns_mov_b64 exec, vcc\n" \
"buffer_load_dwordx4 v[80:83], v3, s[36:39], s42 offen offset:0 sc1\ns_mov_b64 exec, -1\n.Lqk_sk1617%=:\nv_max_u32 v17, v84, v86\nv_cmp_eq_u32 vcc, -1, v17\ns_cbranch_vccz .Lqk_sk1624%=\ns_mov_b64 exec, vcc\nbuffer_load_dwordx4 v[84:87], v3, s[36:39], s42 offen offset:1024 sc1\n" \
"s_mov_b64 exec, -1\n.Lqk_sk1624%=:\nv_max_u32 v17, v88, v90\nv_cmp_eq_u32 vcc, -1, v17\ns_cbranch_vccz .Lqk_sk1631%=\ns_mov_b64 exec, vcc\nbuffer_load_dwordx4 v[88:91], v3, s[36:39], s42 offen offset:2048 sc1\ns_mov_b64 exec, -1\n" \
".Lqk_sk1631%=:\nv_max_u32 v17, v92, v94\nv_cmp_eq_u32 vcc, -1, v17\ns_cbranch_vccz .Lqk_sk1638%=\ns_mov_b64 exec, vcc\nbuffer_load_dwordx4 v[92:95], v3, s[36:39], s42 offen offset:3072 sc1\ns_mov_b64 exec, -1\n.Lqk_sk1638%=:\n" \
"v_mov_b64 v[48:49], 0\nv_mov_b64 v[50:51], 0\nv_mov_b64 v[52:53], 0\nv_mov_b64 v[54:55], 0\nv_mov_b64 v[56:57], 0\nv_mov_b64 v[58:59], 0\nv_mov_b64 v[60:61], 0\nv_mov_b64 v[62:63], 0\n" \
"s_add_u32 s43, s43, 1\ns_cmp_lt_u32 s43, 262144\ns_cbranch_scc1 .Lqk_poll0%=\ns_branch .Lqk_abort%=\n.p2align 6\n.Lqk_c1%=:\ns_waitcnt lgkmcnt(3)\nv_mfma_f32_16x16x32_bf16 v[32:35], v[232:235], v[56:59], v[32:35]\n" \
"v_mov_b64 v[52:53], 0\nv_mov_b64 v[54:55], 0\nv_mfma_f32_16x16x32_bf16 v[36:39], v[240:243], v[56:59], v[36:39]\ns_waitcnt lgkmcnt(2)\nds_read_b128 v[64:67], v1 offset:6144\nv_mfma_f32_16x16x32_bf16 v[32:35], v[236:239], v[60:63], v[32:35]\nds_read_b128 v[68:71], v1 offset:7168\nv_mfma_f32_16x16x32_bf16 v[36:39], v[244:247], v[60:63], v[36:39]\n" \
"s_bfe_i32 s54, s40, 0x10001\nv_mfma_f32_16x16x32_bf16 v[40:43], v[248:251], v[56:59], v[40:43]\nv_mfma_f32_16x16x32_bf16 v[44:47], v[72:75], v[56:59], v[44:47]\nv_cvt_pk_bf16_f32 v36, v32, v36\nv_cvt_pk_bf16_f32 v37, v33, v37\nv_mfma_f32_16x16x32_bf16 v[40:43], v[252:255], v[60:63], v[40:43]\nv_cvt_pk_bf16_f32 v38, v34, v38\nv_cvt_pk_bf16_f32 v39, v35, v39\n" \
"v_mfma_f32_16x16x32_bf16 v[44:47], v[76:79], v[60:63], v[44:47]\nds_read_b128 v[72:75], v1 offset:8192\nds_read_b128 v[76:79], v1 offset:9216\nv_mov_b64 v[56:57], 0\nv_mov_b64 v[58:59], 0\nv_mov_b64 v[60:61], 0\nv_mov_b64 v[62:63], 0\n.Lqk_poll1%=:\n" \
"s_waitcnt vmcnt(0)\nv_smfmac_f32_16x16x64_bf16 v[48:51], v[80:83], a[104:111], v4\nds_write_b128 v0, v[80:83] offset:20480\nv_max3_u32 v16, v80, v82, v84\nv_smfmac_f32_16x16x64_bf16 v[52:55], v[80:83], a[112:119], v4\nv_max3_u32 v16, v16, v86, v88\nv_smfmac_f32_16x16x64_bf16 v[56:59], v[80:83], a[120:127], v4\nds_write_b128 v0, v[84:87] offset:21504\n" \
"v_max3_u32 v16, v16, v90, v92\nv_max_u32 v16, v16, v94\nv_smfmac_f32_16x16x64_bf16 v[60:63], v[80:83], a[128:135], v4\nv_cmp_eq_u32 vcc, -1, v16\nv_smfmac_f32_16x16x64_bf16 v[48:51], v[84:87], a[136:143], v4\nds_write_b128 v0, v[88:91] offset:22528\ns_cbranch_vccnz .Lqk_re1%=\ns_waitcnt lgkmcnt(5)\n" \
"v_smfmac_f32_16x16x64_bf16 v[52:55], v[84:87], v[64:71], v4\nv_smfmac_f32_16x16x64_bf16 v[56:59], v[84:87], a[144:151], v4\nds_write_b128 v0, v[92:95] offset:23552\nv_smfmac_f32_16x16x64_bf16 v[60:63], v[84:87], a[152:159], v4\nv_smfmac_f32_16x16x64_bf16 v[48:51], v[88:91], a[160:167], v4\nv_smfmac_f32_16x16x64_bf16 v[52:55], v[88:91], a[168:175], v4\ns_waitcnt lgkmcnt(0)\n.Lqk_jn1%=:\n" \
"s_barrier\nds_read_b128 v[80:83], v0 offset:24576\nds_read_b128 v[64:67], v1 offset:10240\nds_read_b128 v[68:71], v1 offset:11264\nv_smfmac_f32_16x16x64_bf16 v[56:59], v[88:91], v[72:79], v4\nds_read_b128 v[72:75], v1 offset:12288\nds_read_b128 v[76:79], v1 offset:13312\nv_smfmac_f32_16x16x64_bf16 v[60:63], v[88:91], a[176:183], v4\n" \
"ds_read_b128 v[84:87], v0 offset:25600\nds_read_b128 v[88:91], v0 offset:26624\nv_smfmac_f32_16x16x64_bf16 v[48:51], v[92:95], a[184:191], v4\nds_write_b128 v5, v[36:39] offset:256\nv_cvt_pk_bf16_f32 v44, v40, v44\nv_cvt_pk_bf16_f32 v45, v41, v45\nv_cvt_pk_bf16_f32 v46, v42, v46\nv_cvt_pk_bf16_f32 v47, v43, v47\n" \
"v_smfmac_f32_16x16x64_bf16 v[52:55], v[92:95], a[192:199], v4\nv_smfmac_f32_16x16x64_bf16 v[56:59], v[92:95], a[200:207], v4\ns_waitcnt lgkmcnt(5)\nv_smfmac_f32_16x16x64_bf16 v[60:63], v[92:95], v[64:71], v4\nds_read_b128 v[92:95], v0 offset:27648\nds_read_b128 v[64:67], v1 offset:14336\nds_read_b128 v[68:71], v1 offset:15360\nv_smfmac_f32_16x16x64_bf16 v[48:51], v[80:83], a[208:215], v4\n" \
"ds_write_b128 v5, v[44:47] offset:768\nv_smfmac_f32_16x16x64_bf16 v[52:55], v[80:83], a[216:223], v4\nv_smfmac_f32_16x16x64_bf16 v[56:59], v[80:83], a[224:231], v4\nv_smfmac_f32_16x16x64_bf16 v[60:63], v[80:83], a[232:239], v4\ns_waitcnt lgkmcnt(6)\nds_read_b128 v[80:83], v0 offset:28672\nv_smfmac_f32_16x16x64_bf16 v[48:51], v[84:87], a[240:247], v4\nv_smfmac_f32_16x16x64_bf16 v[52:55], v[84:87], v[72:79], v4\n" \
"ds_read_b128 v[72:75], v1 offset:16384\nds_read_b128 v[76:79], v1 offset:17408\nv_smfmac_f32_16x16x64_bf16 v[56:59], v[84:87], a[248:255], v4\nv_smfmac_f32_16x16x64_bf16 v[60:63], v[84:87], v[96:103], v4\ns_waitcnt lgkmcnt(8)\nds_read_b128 v[84:87], v0 offset:29696\nv_smfmac_f32_16x16x64_bf16 v[48:51], v[88:91], v[104:111], v4\ns_waitcnt lgkmcnt(4)\n" \
"s_barrier\nv_smfmac_f32_16x16x64_bf16 v[52:55], v[88:91], v[112:119], v4\nv_smfmac_f32_16x16x64_bf16 v[56:59], v[88:91], v[64:71], v4\nds_read_b128 v[64:67], v1 offset:18432\nds_read_b128 v[68:71], v1 offset:19456\nv_smfmac_f32_16x16x64_bf16 v[60:63], v[88:91], v[120:127], v4\nds_read_b128 v[88:91], v0 offset:30720\nv_smfmac_f32_16x16x64_bf16 v[48:51], v[92:95], v[128:135], v4\n" \
"ds_read_b128 v[16:19], v7 offset:0\nv_smfmac_f32_16x16x64_bf16 v[52:55], v[92:95], v[136:143], v4\nv_smfmac_f32_16x16x64_bf16 v[56:59], v[92:95], v[144:151], v4\ns_waitcnt lgkmcnt(5)\nds_read_b128 v[20:23], v7 offset:2048\nv_smfmac_f32_16x16x64_bf16 v[60:63], v[92:95], v[72:79], v4\nds_read_b128 v[92:95], v0 offset:31744\nds_read_b128 v[72:75], v1 offset:20480\n" \
"ds_read_b128 v[76:79], v1 offset:21504\nv_smfmac_f32_16x16x64_bf16 v[48:51], v[80:83], v[152:159], v4\nds_read_b128 v[24:27], v7 offset:4096\nv_smfmac_f32_16x16x64_bf16 v[52:55], v[80:83], v[160:167], v4\nv_smfmac_f32_16x16x64_bf16 v[56:59], v[80:83], v[168:175], v4\nds_read_b128 v[28:31], v7 offset:6144\nv_smfmac_f32_16x16x64_bf16 v[60:63], v[80:83], v[176:183], v4\ns_waitcnt lgkmcnt(10)\n" \
"ds_read_b128 v[80:83], v0 offset:16384\nv_smfmac_f32_16x16x64_bf16 v[48:51], v[84:87], v[184:191], v4\ns_waitcnt lgkmcnt(9)\nds_read_b128 v[32:35], v7 offset:8192\nv_smfmac_f32_16x16x64_bf16 v[52:55], v[84:87], v[64:71], v4\nds_read_b128 v[64:67], v1 offset:22528\nds_read_b128 v[68:71], v1 offset:23552\nv_smfmac_f32_16x16x64_bf16 v[56:59], v[84:87], v[192:199], v4\n" \
"ds_read_b128 v[36:39], v7 offset:10240\nv_smfmac_f32_16x16x64_bf16 v[60:63], v[84:87], v[200:207], v4\ns_waitcnt lgkmcnt(12)\nds_read_b128 v[84:87], v0 offset:17408\nv_smfmac_f32_16x16x64_bf16 v[48:51], v[88:91], v[208:215], v4\nds_read_b128 v[40:43], v7 offset:12288\nv_smfmac_f32_16x16x64_bf16 v[52:55], v[88:91], v[216:223], v4\ns_waitcnt lgkmcnt(9)\n" \
"v_smfmac_f32_16x16x64_bf16 v[56:59], v[88:91], v[72:79], v4\nds_read_b128 v[72:75], v1 offset:0\nds_read_b128 v[76:79], v1 offset:1024\nv_smfmac_f32_16x16x64_bf16 v[60:63], v[88:91], v[224:231], v4\nds_read_b128 v[88:91], v0 offset:18432\nv_smfmac_f32_16x16x64_bf16 v[48:51], v[92:95], v[232:239], v4\nds_read_b128 v[44:47], v7 offset:14336\nv_smfmac_f32_16x16x64_bf16 v[52:55], v[92:95], v[240:247], v4\n" \
"v_smfmac_f32_16x16x64_bf16 v[56:59], v[92:95], v[248:255], v4\ns_waitcnt lgkmcnt(7)\nv_smfmac_f32_16x16x64_bf16 v[60:63], v[92:95], v[64:71], v4\nds_read_b128 v[92:95], v0 offset:19456\nds_read_b128 v[64:67], v1 offset:2048\nds_read_b128 v[68:71], v1 offset:3072\nv_smfmac_f32_16x16x64_bf16 v[48:51], v[80:83], a[0:7], v4\nv_smfmac_f32_16x16x64_bf16 v[52:55], v[80:83], a[8:15], v4\n" \
"v_smfmac_f32_16x16x64_bf16 v[56:59], v[80:83], a[16:23], v4\nv_smfmac_f32_16x16x64_bf16 v[60:63], v[80:83], a[24:31], v4\ns_waitcnt lgkmcnt(8)\nv_smfmac_f32_16x16x64_bf16 v[48:51], v[84:87], a[32:39], v4\ns_waitcnt lgkmcnt(5)\nv_smfmac_f32_16x16x64_bf16 v[52:55], v[84:87], v[72:79], v4\nds_read_b128 v[72:75], v1 offset:4096\nds_read_b128 v[76:79], v1 offset:5120\n" \
"v_smfmac_f32_16x16x64_bf16 v[56:59], v[84:87], a[40:47], v4\nv_smfmac_f32_16x16x64_bf16 v[60:63], v[84:87], a[48:55], v4\ns_waitcnt lgkmcnt(6)\nv_smfmac_f32_16x16x64_bf16 v[48:51], v[88:91], a[56:63], v4\nv_smfmac_f32_16x16x64_bf16 v[52:55], v[88:91], a[64:71], v4\ns_waitcnt lgkmcnt(4)\nv_smfmac_f32_16x16x64_bf16 v[48:51], v[92:95], a[80:87], v4\nv_smfmac_f32_16x16x64_bf16 v[52:55], v[92:95], a[88:95], v4\n" \
"s_waitcnt lgkmcnt(2)\nv_smfmac_f32_16x16x64_bf16 v[56:59], v[88:91], v[64:71], v4\nv_add_f32 v48, v48, v50\nv_smfmac_f32_16x16x64_bf16 v[60:63], v[88:91], a[72:79], v4\nv_add_f32 v52, v52, v54\nv_add_f32 v49, v49, v51\nv_smfmac_f32_16x16x64_bf16 v[56:59], v[92:95], a[96:103], v4\ns_waitcnt lgkmcnt(0)\n" \
"v_add_f32 v53, v53, v55\nv_cvt_pk_bf16_f32 v50, v48, v52\nv_smfmac_f32_16x16x64_bf16 v[60:63], v[92:95], v[72:79], v4\nv_cvt_pk_bf16_f32 v51, v49, v53\nds_write2_b32 v6, v50, v51 offset0:0 offset1:4\ns_nop 1\nv_add_f32 v56, v56, v58\nv_add_f32 v57, v57, v59\n" \
"s_nop 1\nv_add_f32 v60, v60, v62\nv_add_f32 v61, v61, v63\nv_cvt_pk_bf16_f32 v62, v56, v60\nv_cvt_pk_bf16_f32 v63, v57, v61\nds_write2_b32 v6, v62, v63 offset0:36 offset1:40\nv_mov_b32 v61, s54\nv_mfma_f32_16x16x32_bf16 v[52:55], v[32:35], v[16:19], v[12:15]\n" \
"v_mfma_f32_16x16x32_bf16 v[52:55], v[36:39], v[20:23], v[52:55]\nv_mfma_f32_16x16x32_bf16 v[52:55], v[40:43], v[24:27], v[52:55]\nv_mfma_f32_16x16x32_bf16 v[52:55], v[44:47], v[28:31], v[52:55]\ns_waitcnt lgkmcnt(0)\ns_barrier\ns_nop 10\nds_read_b128 v[32:35], v9 offset:0\nds_read_b128 v[36:39], v9 offset:576\n" \
"ds_read_b128 v[40:43], v9 offset:1152\nds_read_b128 v[44:47], v9 offset:1728\nds_read_b32 v60, v0 offset:53760\nds_read_b128 v[80:83], v2 offset:0\nds_read_b128 v[84:87], v2 offset:256\nds_read_b128 v[88:91], v2 offset:2048\ns_waitcnt lgkmcnt(7)\nv_mfma_f32_16x16x32_bf16 v[48:51], v[32:35], v[16:19], 0\n" \
"s_waitcnt lgkmcnt(6)\nv_mfma_f32_16x16x32_bf16 v[48:51], v[36:39], v[20:23], v[48:51]\ns_waitcnt lgkmcnt(5)\nv_mfma_f32_16x16x32_bf16 v[48:51], v[40:43], v[24:27], v[48:51]\ns_waitcnt lgkmcnt(4)\nv_mfma_f32_16x16x32_bf16 v[48:51], v[44:47], v[28:31], v[48:51]\ns_waitcnt lgkmcnt(2)\nv_mfma_f32_16x16x32_bf16 v[16:19], a[0:3], v[80:83], 0\n" \
"ds_read_b128 v[92:95], v2 offset:2304\nv_max3_f32 v62, v52, v53, v54\nv_max_f32 v62, v62, v55\nv_mfma_f32_16x16x32_bf16 v[20:23], a[8:11], v[80:83], 0\nds_read_b128 v[72:75], v1 offset:0\nv_max3_f32 v63, v62, v48, v49\nv_max3_f32 v62, v62, v48, v49\nv_mfma_f32_16x16x32_bf16 v[24:27], a[16:19], v[80:83], 0\n" \
"s_nop 0\nv_permlane16_swap_b32 v62, v63\nv_max_f32 v50, v62, v63\nv_mfma_f32_16x16x32_bf16 v[28:31], a[24:27], v[80:83], 0\ns_waitcnt lgkmcnt(3)\nv_max_f32 v62, v62, v63\nv_mfma_f32_16x16x32_bf16 v[32:35], a[0:3], v[84:87], 0\nds_read_b128 v[80:83], v2 offset:4096\n" \
"v_permlane32_swap_b32 v62, v50\nv_mfma_f32_16x16x32_bf16 v[36:39], a[8:11], v[84:87], 0\nv_max_f32 v62, v62, v50\nv_mul_f32 v62, s59, v62\nv_fma_f32 v52, v52, s58, v62\nv_mfma_f32_16x16x32_bf16 v[40:43], a[16:19], v[84:87], 0\nv_fma_f32 v53, v53, s58, v62\nv_exp_f32 v52, v52\n" \
"v_mfma_f32_16x16x32_bf16 v[44:47], a[24:27], v[84:87], 0\ns_waitcnt lgkmcnt(3)\nv_fma_f32 v54, v54, s58, v62\nv_exp_f32 v53, v53\nv_mfma_f32_16x16x32_bf16 v[16:19], a[4:7], v[88:91], v[16:19]\nds_read_b128 v[84:87], v2 offset:4352\nv_fma_f32 v55, v55, s58, v62\nv_add_f32 v63, v52, v53\n" \
"v_mfma_f32_16x16x32_bf16 v[20:23], a[12:15], v[88:91], v[20:23]\nds_read_b128 v[76:79], v1 offset:1024\nv_exp_f32 v54, v54\nv_mfma_f32_16x16x32_bf16 v[24:27], a[20:23], v[88:91], v[24:27]\nv_fma_f32 v48, v48, s58, v62\nv_exp_f32 v55, v55\nv_mfma_f32_16x16x32_bf16 v[28:31], a[28:31], v[88:91], v[28:31]\ns_waitcnt lgkmcnt(4)\n" \
"v_add_f32 v63, v63, v54\nv_fma_f32 v49, v49, s58, v62\nv_add_f32 v63, v63, v55\nv_mfma_f32_16x16x32_bf16 v[32:35], a[4:7], v[92:95], v[32:35]\nds_read_b128 v[88:91], v2 offset:6144\nv_exp_f32 v48, v48\nv_mfma_f32_16x16x32_bf16 v[36:39], a[12:15], v[92:95], v[36:39]\nv_exp_f32 v49, v49\n" \
"v_add_f32 v63, v63, v48\nv_mfma_f32_16x16x32_bf16 v[40:43], a[20:23], v[92:95], v[40:43]\nv_add_f32 v62, v63, v49\nv_add_f32 v50, v63, v49\nv_mfma_f32_16x16x32_bf16 v[44:47], a[28:31], v[92:95], v[44:47]\ns_waitcnt lgkmcnt(3)\nv_permlane16_swap_b32 v62, v50\nv_add_f32 v63, v62, v50\n" \
"v_mfma_f32_16x16x32_bf16 v[16:19], a[32:35], v[80:83], v[16:19]\nds_read_b128 v[92:95], v2 offset:6400\nv_add_f32 v50, v62, v50\nv_mfma_f32_16x16x32_bf16 v[20:23], v[72:75], v[80:83], v[20:23]\nds_read_b128 v[64:67], v1 offset:2048\nv_permlane32_swap_b32 v63, v50\nv_mfma_f32_16x16x32_bf16 v[24:27], a[40:43], v[80:83], v[24:27]\nv_add_f32 v63, v63, v50\n" \
"v_rcp_f32 v63, v63\nv_mfma_f32_16x16x32_bf16 v[28:31], a[48:51], v[80:83], v[28:31]\ns_waitcnt lgkmcnt(4)\nv_mul_f32 v52, v52, v63\nv_mul_f32 v53, v53, v63\nv_mul_f32 v54, v54, v63\nv_mfma_f32_16x16x32_bf16 v[32:35], a[32:35], v[84:87], v[32:35]\nds_read_b128 v[80:83], v2 offset:8192\n" \
"v_mul_f32 v55, v55, v63\nv_mul_f32 v48, v48, v63\nv_mfma_f32_16x16x32_bf16 v[36:39], v[72:75], v[84:87], v[36:39]\nv_mul_f32 v49, v49, v63\nv_cvt_pk_bf16_f32 v52, v52, v53\nv_cvt_pk_bf16_f32 v53, v54, v55\nv_mfma_f32_16x16x32_bf16 v[40:43], a[40:43], v[84:87], v[40:43]\nv_cvt_pk_bf16_f32 v48, v48, v49\n" \
"v_bfi_b32 v52, s61, v52, v61\nv_bfi_b32 v53, s61, v53, v61\nv_mfma_f32_16x16x32_bf16 v[44:47], a[48:51], v[84:87], v[44:47]\ns_waitcnt lgkmcnt(3)\nv_bfi_b32 v48, s61, v48, v61\nv_mfma_f32_16x16x32_bf16 v[16:19], a[36:39], v[88:91], v[16:19]\nds_read_b128 v[84:87], v2 offset:8448\nv_mfma_f32_16x16x32_bf16 v[20:23], v[76:79], v[88:91], v[20:23]\n" \
"ds_read_b128 v[68:71], v1 offset:3072\nv_mfma_f32_16x16x32_bf16 v[24:27], a[44:47], v[88:91], v[24:27]\nbuffer_store_dwordx2 v[52:53], v10, s[36:39], s55 offen sc1\nv_mov_b32 v49, v48\nv_mfma_f32_16x16x32_bf16 v[28:31], a[52:55], v[88:91], v[28:31]\ns_waitcnt lgkmcnt(4)\nv_permlane16_swap_b32 v48, v49\nv_mov_b32 v50, v48\n" \
"v_mfma_f32_16x16x32_bf16 v[32:35], a[36:39], v[92:95], v[32:35]\nds_read_b128 v[88:91], v2 offset:10240\nv_mov_b32 v51, v49\nv_mfma_f32_16x16x32_bf16 v[36:39], v[76:79], v[92:95], v[36:39]\nv_permlane32_swap_b32 v48, v50\nv_mfma_f32_16x16x32_bf16 v[40:43], a[44:47], v[92:95], v[40:43]\nv_permlane32_swap_b32 v49, v51\nv_mfma_f32_16x16x32_bf16 v[44:47], a[52:55], v[92:95], v[44:47]\n" \
"s_waitcnt lgkmcnt(3)\nbuffer_store_dwordx4 v[48:51], v11, s[36:39], s55 offen sc1\nv_cmp_ne_u32 vcc, 0, v60\nv_mfma_f32_16x16x32_bf16 v[16:19], a[56:59], v[80:83], v[16:19]\nds_read_b128 v[92:95], v2 offset:10496\nv_mfma_f32_16x16x32_bf16 v[20:23], a[64:67], v[80:83], v[20:23]\nds_read_b128 v[72:75], v1 offset:4096\nv_mfma_f32_16x16x32_bf16 v[24:27], v[64:67], v[80:83], v[24:27]\n" \
"v_mfma_f32_16x16x32_bf16 v[28:31], a[72:75], v[80:83], v[28:31]\ns_waitcnt lgkmcnt(4)\nv_mfma_f32_16x16x32_bf16 v[32:35], a[56:59], v[84:87], v[32:35]\nds_read_b128 v[80:83], v2 offset:12288\nv_mfma_f32_16x16x32_bf16 v[36:39], a[64:67], v[84:87], v[36:39]\nv_mfma_f32_16x16x32_bf16 v[40:43], v[64:67], v[84:87], v[40:43]\nv_mfma_f32_16x16x32_bf16 v[44:47], a[72:75], v[84:87], v[44:47]\ns_waitcnt lgkmcnt(3)\n" \
"v_mfma_f32_16x16x32_bf16 v[16:19], a[60:63], v[88:91], v[16:19]\nds_read_b128 v[84:87], v2 offset:12544\nv_mfma_f32_16x16x32_bf16 v[20:23], a[68:71], v[88:91], v[20:23]\nds_read_b128 v[76:79], v1 offset:5120\nv_mfma_f32_16x16x32_bf16 v[24:27], v[68:71], v[88:91], v[24:27]\nv_mfma_f32_16x16x32_bf16 v[28:31], a[76:79], v[88:91], v[28:31]\ns_waitcnt lgkmcnt(4)\nv_mfma_f32_16x16x32_bf16 v[32:35], a[60:63], v[92:95], v[32:35]\n" \
"ds_read_b128 v[88:91], v2 offset:14336\nv_mfma_f32_16x16x32_bf16 v[36:39], a[68:71], v[92:95], v[36:39]\nv_mfma_f32_16x16x32_bf16 v[40:43], v[68:71], v[92:95], v[40:43]\nv_mfma_f32_16x16x32_bf16 v[44:47], a[76:79], v[92:95], v[44:47]\ns_cbranch_vccnz .Lqk_pj%=\ns_cmp_lt_i32 s40, 63\ns_waitcnt lgkmcnt(0)\ns_cbranch_scc1 .Lqk_pwait%=\n" \
"s_branch .Lqk_end%=\n.Lqk_re1%=:\nv_max_u32 v17, v80, v82\nv_cmp_eq_u32 vcc, -1, v17\ns_cbranch_vccz .Lqk_sk2003%=\ns_mov_b64 exec, vcc\nbuffer_load_dwordx4 v[80:83], v3, s[36:39], s42 offen offset:0 sc1\ns_mov_b64 exec, -1\n" \
".Lqk_sk2003%=:\nv_max_u32 v17, v84, v86\nv_cmp_eq_u32 vcc, -1, v17\ns_cbranch_vccz .Lqk_sk2010%=\ns_mov_b64 exec, vcc\nbuffer_load_dwordx4 v[84:87], v3, s[36:39], s42 offen offset:1024 sc1\ns_mov_b64 exec, -1\n.Lqk_sk2010%=:\n" \
"v_max_u32 v17, v88, v90\nv_cmp_eq_u32 vcc, -1, v17\ns_cbranch_vccz .Lqk_sk2017%=\ns_mov_b64 exec, vcc\nbuffer_load_dwordx4 v[88:91], v3, s[36:39], s42 offen offset:2048 sc1\ns_mov_b64 exec, -1\n.Lqk_sk2017%=:\nv_max_u32 v17, v92, v94\n" \
"v_cmp_eq_u32 vcc, -1, v17\ns_cbranch_vccz .Lqk_sk2024%=\ns_mov_b64 exec, vcc\nbuffer_load_dwordx4 v[92:95], v3, s[36:39], s42 offen offset:3072 sc1\ns_mov_b64 exec, -1\n.Lqk_sk2024%=:\nv_mov_b64 v[48:49], 0\nv_mov_b64 v[50:51], 0\n" \
"v_mov_b64 v[52:53], 0\nv_mov_b64 v[54:55], 0\nv_mov_b64 v[56:57], 0\nv_mov_b64 v[58:59], 0\nv_mov_b64 v[60:61], 0\nv_mov_b64 v[62:63], 0\ns_add_u32 s43, s43, 1\ns_cmp_lt_u32 s43, 262144\n" \
"s_cbranch_scc1 .Lqk_poll1%=\ns_branch .Lqk_abort%=\n.p2align 6\n.Lqk_c2%=:\ns_waitcnt lgkmcnt(3)\nv_mfma_f32_16x16x32_bf16 v[32:35], v[232:235], v[56:59], v[32:35]\nv_mov_b64 v[52:53], 0\nv_mov_b64 v[54:55], 0\n" \
"v_mfma_f32_16x16x32_bf16 v[36:39], v[240:243], v[56:59], v[36:39]\ns_waitcnt lgkmcnt(2)\nds_read_b128 v[64:67], v1 offset:12288\nv_mfma_f32_16x16x32_bf16 v[32:35], v[236:239], v[60:63], v[32:35]\nds_read_b128 v[68:71], v1 offset:13312\nv_mfma_f32_16x16x32_bf16 v[36:39], v[244:247], v[60:63], v[36:39]\ns_bfe_i32 s54, s40, 0x10001\nv_mfma_f32_16x16x32_bf16 v[40:43], v[248:251], v[56:59], v[40:43]\n" \
"v_mfma_f32_16x16x32_bf16 v[44:47], v[72:75], v[56:59], v[44:47]\nv_cvt_pk_bf16_f32 v36, v32, v36\nv_cvt_pk_bf16_f32 v37, v33, v37\nv_mfma_f32_16x16x32_bf16 v[40:43], v[252:255], v[60:63], v[40:43]\nv_cvt_pk_bf16_f32 v38, v34, v38\nv_cvt_pk_bf16_f32 v39, v35, v39\nv_mfma_f32_16x16x32_bf16 v[44:47], v[76:79], v[60:63], v[44:47]\nds_read_b128 v[72:75], v1 offset:14336\n" \
"ds_read_b128 v[76:79], v1 offset:15360\nv_mov_b64 v[56:57], 0\nv_mov_b64 v[58:59], 0\nv_mov_b64 v[60:61], 0\nv_mov_b64 v[62:63], 0\n.Lqk_poll2%=:\ns_waitcnt vmcnt(0)\nv_smfmac_f32_16x16x64_bf16 v[48:51], v[80:83], a[208:215], v4\n" \
"ds_write_b128 v0, v[80:83] offset:24576\nv_max3_u32 v16, v80, v82, v84\nv_smfmac_f32_16x16x64_bf16 v[52:55], v[80:83], a[216:223], v4\nv_max3_u32 v16, v16, v86, v88\nv_smfmac_f32_16x16x64_bf16 v[56:59], v[80:83], a[224:231], v4\nds_write_b128 v0, v[84:87] offset:25600\nv_max3_u32 v16, v16, v90, v92\nv_max_u32 v16, v16, v94\n" \
"v_smfmac_f32_16x16x64_bf16 v[60:63], v[80:83], a[232:239], v4\nv_cmp_eq_u32 vcc, -1, v16\nv_smfmac_f32_16x16x64_bf16 v[48:51], v[84:87], a[240:247], v4\nds_write_b128 v0, v[88:91] offset:26624\ns_cbranch_vccnz .Lqk_re2%=\ns_waitcnt lgkmcnt(5)\nv_smfmac_f32_16x16x64_bf16 v[52:55], v[84:87], v[64:71], v4\nv_smfmac_f32_16x16x64_bf16 v[56:59], v[84:87], a[248:255], v4\n" \
"ds_write_b128 v0, v[92:95] offset:27648\nv_smfmac_f32_16x16x64_bf16 v[60:63], v[84:87], v[96:103], v4\nv_smfmac_f32_16x16x64_bf16 v[48:51], v[88:91], v[104:111], v4\nv_smfmac_f32_16x16x64_bf16 v[52:55], v[88:91], v[112:119], v4\ns_waitcnt lgkmcnt(0)\n.Lqk_jn2%=:\ns_barrier\nds_read_b128 v[80:83], v0 offset:28672\n" \
"ds_read_b128 v[64:67], v1 offset:16384\nds_read_b128 v[68:71], v1 offset:17408\nv_smfmac_f32_16x16x64_bf16 v[56:59], v[88:91], v[72:79], v4\nds_read_b128 v[72:75], v1 offset:18432\nds_read_b128 v[76:79], v1 offset:19456\nv_smfmac_f32_16x16x64_bf16 v[60:63], v[88:91], v[120:127], v4\nds_read_b128 v[84:87], v0 offset:29696\nds_read_b128 v[88:91], v0 offset:30720\n" \
"v_smfmac_f32_16x16x64_bf16 v[48:51], v[92:95], v[128:135], v4\nds_write_b128 v5, v[36:39] offset:256\nv_cvt_pk_bf16_f32 v44, v40, v44\nv_cvt_pk_bf16_f32 v45, v41, v45\nv_cvt_pk_bf16_f32 v46, v42, v46\nv_cvt_pk_bf16_f32 v47, v43, v47\nv_smfmac_f32_16x16x64_bf16 v[52:55], v[92:95], v[136:143], v4\nv_smfmac_f32_16x16x64_bf16 v[56:59], v[92:95], v[144:151], v4\n" \
"s_waitcnt lgkmcnt(5)\nv_smfmac_f32_16x16x64_bf16 v[60:63], v[92:95], v[64:71], v4\nds_read_b128 v[92:95], v0 offset:31744\nds_read_b128 v[64:67], v1 offset:20480\nds_read_b128 v[68:71], v1 offset:21504\nv_smfmac_f32_16x16x64_bf16 v[48:51], v[80:83], v[152:159], v4\nds_write_b128 v5, v[44:47] offset:768\nv_smfmac_f32_16x16x64_bf16 v[52:55], v[80:83], v[160:167], v4\n" \
"v_smfmac_f32_16x16x64_bf16 v[56:59], v[80:83], v[168:175], v4\nv_smfmac_f32_16x16x64_bf16 v[60:63], v[80:83], v[176:183], v4\ns_waitcnt lgkmcnt(6)\nds_read_b128 v[80:83], v0 offset:16384\nv_smfmac_f32_16x16x64_bf16 v[48:51], v[84:87], v[184:191], v4\nv_smfmac_f32_16x16x64_bf16 v[52:55], v[84:87], v[72:79], v4\nds_read_b128 v[72:75], v1 offset:22528\nds_read_b128 v[76:79], v1 offset:23552\n" \
"v_smfmac_f32_16x16x64_bf16 v[56:59], v[84:87], v[192:199], v4\nv_smfmac_f32_16x16x64_bf16 v[60:63], v[84:87], v[200:207], v4\ns_waitcnt lgkmcnt(8)\nds_read_b128 v[84:87], v0 offset:17408\nv_smfmac_f32_16x16x64_bf16 v[48:51], v[88:91], v[208:215], v4\ns_waitcnt lgkmcnt(4)\ns_barrier\nv_smfmac_f32_16x16x64_bf16 v[52:55], v[88:91], v[216:223], v4\n" \
"v_smfmac_f32_16x16x64_bf16 v[56:59], v[88:91], v[64:71], v4\nds_read_b128 v[64:67], v1 offset:0\nds_read_b128 v[68:71], v1 offset:1024\nv_smfmac_f32_16x16x64_bf16 v[60:63], v[88:91], v[224:231], v4\nds_read_b128 v[88:91], v0 offset:18432\nv_smfmac_f32_16x16x64_bf16 v[48:51], v[92:95], v[232:239], v4\nds_read_b128 v[16:19], v7 offset:256\nv_smfmac_f32_16x16x64_bf16 v[52:55], v[92:95], v[240:247], v4\n" \
"v_smfmac_f32_16x16x64_bf16 v[56:59], v[92:95], v[248:255], v4\ns_waitcnt lgkmcnt(5)\nds_read_b128 v[20:23], v7 offset:2304\nv_smfmac_f32_16x16x64_bf16 v[60:63], v[92:95], v[72:79], v4\nds_read_b128 v[92:95], v0 offset:19456\nds_read_b128 v[72:75], v1 offset:2048\nds_read_b128 v[76:79], v1 offset:3072\nv_smfmac_f32_16x16x64_bf16 v[48:51], v[80:83], a[0:7], v4\n" \
"ds_read_b128 v[24:27], v7 offset:4352\nv_smfmac_f32_16x16x64_bf16 v[52:55], v[80:83], a[8:15], v4\nv_smfmac_f32_16x16x64_bf16 v[56:59], v[80:83], a[16:23], v4\nds_read_b128 v[28:31], v7 offset:6400\nv_smfmac_f32_16x16x64_bf16 v[60:63], v[80:83], a[24:31], v4\ns_waitcnt lgkmcnt(10)\nds_read_b128 v[80:83], v0 offset:20480\nv_smfmac_f32_16x16x64_bf16 v[48:51], v[84:87], a[32:39], v4\n" \
"s_waitcnt lgkmcnt(9)\nds_read_b128 v[32:35], v7 offset:8192\nv_smfmac_f32_16x16x64_bf16 v[52:55], v[84:87], v[64:71], v4\nds_read_b128 v[64:67], v1 offset:4096\nds_read_b128 v[68:71], v1 offset:5120\nv_smfmac_f32_16x16x64_bf16 v[56:59], v[84:87], a[40:47], v4\nds_read_b128 v[36:39], v7 offset:10240\nv_smfmac_f32_16x16x64_bf16 v[60:63], v[84:87], a[48:55], v4\n" \
"s_waitcnt lgkmcnt(12)\nds_read_b128 v[84:87], v0 offset:21504\nv_smfmac_f32_16x16x64_bf16 v[48:51], v[88:91], a[56:63], v4\nds_read_b128 v[40:43], v7 offset:12288\nv_smfmac_f32_16x16x64_bf16 v[52:55], v[88:91], a[64:71], v4\ns_waitcnt lgkmcnt(9)\nv_smfmac_f32_16x16x64_bf16 v[56:59], v[88:91], v[72:79], v4\nds_read_b128 v[72:75], v1 offset:6144\n" \
"ds_read_b128 v[76:79], v1 offset:7168\nv_smfmac_f32_16x16x64_bf16 v[60:63], v[88:91], a[72:79], v4\nds_read_b128 v[88:91], v0 offset:22528\nv_smfmac_f32_16x16x64_bf16 v[48:51], v[92:95], a[80:87], v4\nds_read_b128 v[44:47], v7 offset:14336\nv_smfmac_f32_16x16x64_bf16 v[52:55], v[92:95], a[88:95], v4\nv_smfmac_f32_16x16x64_bf16 v[56:59], v[92:95], a[96:103], v4\ns_waitcnt lgkmcnt(7)\n" \
"v_smfmac_f32_16x16x64_bf16 v[60:63], v[92:95], v[64:71], v4\nds_read_b128 v[92:95], v0 offset:23552\nds_read_b128 v[64:67], v1 offset:8192\nds_read_b128 v[68:71], v1 offset:9216\nv_smfmac_f32_16x16x64_bf16 v[48:51], v[80:83], a[104:111], v4\nv_smfmac_f32_16x16x64_bf16 v[52:55], v[80:83], a[112:119], v4\nv_smfmac_f32_16x16x64_bf16 v[56:59], v[80:83], a[120:127], v4\nv_smfmac_f32_16x16x64_bf16 v[60:63], v[80:83], a[128:135], v4\n" \
"s_waitcnt lgkmcnt(8)\nv_smfmac_f32_16x16x64_bf16 v[48:51], v[84:87], a[136:143], v4\ns_waitcnt lgkmcnt(5)\nds_read_b128 v[80:83], v7 offset:8448\nv_smfmac_f32_16x16x64_bf16 v[52:55], v[84:87], v[72:79], v4\nds_read_b128 v[72:75], v1 offset:10240\nds_read_b128 v[76:79], v1 offset:11264\nv_smfmac_f32_16x16x64_bf16 v[56:59], v[84:87], a[144:151], v4\n" \
"v_smfmac_f32_16x16x64_bf16 v[60:63], v[84:87], a[152:159], v4\ns_waitcnt lgkmcnt(7)\nv_smfmac_f32_16x16x64_bf16 v[48:51], v[88:91], a[160:167], v4\nds_read_b128 v[84:87], v7 offset:10496\nv_smfmac_f32_16x16x64_bf16 v[52:55], v[88:91], a[168:175], v4\ns_waitcnt lgkmcnt(6)\nv_smfmac_f32_16x16x64_bf16 v[48:51], v[92:95], a[184:191], v4\nv_smfmac_f32_16x16x64_bf16 v[52:55], v[92:95], a[192:199], v4\n" \
"s_waitcnt lgkmcnt(4)\nv_smfmac_f32_16x16x64_bf16 v[56:59], v[88:91], v[64:71], v4\nds_read_b128 v[64:67], v7 offset:12544\nv_add_f32 v48, v48, v50\nv_smfmac_f32_16x16x64_bf16 v[60:63], v[88:91], a[176:183], v4\nds_read_b128 v[68:71], v7 offset:14592\nv_add_f32 v52, v52, v54\nv_add_f32 v49, v49, v51\n" \
"v_smfmac_f32_16x16x64_bf16 v[56:59], v[92:95], a[200:207], v4\ns_waitcnt lgkmcnt(3)\nv_add_f32 v53, v53, v55\nv_cvt_pk_bf16_f32 v50, v48, v52\nv_smfmac_f32_16x16x64_bf16 v[60:63], v[92:95], v[72:79], v4\nv_cvt_pk_bf16_f32 v51, v49, v53\nds_write2_b32 v6, v50, v51 offset0:0 offset1:4\ns_nop 1\n" \
"v_add_f32 v56, v56, v58\nv_add_f32 v57, v57, v59\ns_nop 1\nv_add_f32 v60, v60, v62\nv_add_f32 v61, v61, v63\nv_cvt_pk_bf16_f32 v62, v56, v60\nv_cvt_pk_bf16_f32 v63, v57, v61\nds_write2_b32 v6, v62, v63 offset0:36 offset1:40\n" \
"v_mov_b32 v61, s54\nv_mfma_f32_16x16x32_bf16 v[52:55], v[32:35], v[16:19], 0\nv_mfma_f32_16x16x32_bf16 v[52:55], v[36:39], v[20:23], v[52:55]\nv_mfma_f32_16x16x32_bf16 v[52:55], v[40:43], v[24:27], v[52:55]\nv_mfma_f32_16x16x32_bf16 v[52:55], v[44:47], v[28:31], v[52:55]\ns_waitcnt lgkmcnt(0)\ns_barrier\nv_mfma_f32_16x16x32_bf16 v[56:59], v[80:83], v[16:19], v[12:15]\n" \
"v_mfma_f32_16x16x32_bf16 v[56:59], v[84:87], v[20:23], v[56:59]\nds_read_b128 v[32:35], v9 offset:0\nds_read_b128 v[36:39], v9 offset:576\nds_read_b128 v[40:43], v9 offset:1152\nds_read_b128 v[44:47], v9 offset:1728\nds_read_b32 v60, v0 offset:53760\nv_mfma_f32_16x16x32_bf16 v[56:59], v[64:67], v[24:27], v[56:59]\nv_mfma_f32_16x16x32_bf16 v[56:59], v[68:71], v[28:31], v[56:59]\n" \
"s_waitcnt lgkmcnt(4)\nds_read_b128 v[80:83], v2 offset:0\nds_read_b128 v[84:87], v2 offset:256\nds_read_b128 v[88:91], v2 offset:2048\nv_mfma_f32_16x16x32_bf16 v[48:51], v[32:35], v[16:19], 0\ns_waitcnt lgkmcnt(6)\nv_mfma_f32_16x16x32_bf16 v[48:51], v[36:39], v[20:23], v[48:51]\ns_waitcnt lgkmcnt(5)\n" \
"v_mfma_f32_16x16x32_bf16 v[48:51], v[40:43], v[24:27], v[48:51]\ns_waitcnt lgkmcnt(4)\nv_mfma_f32_16x16x32_bf16 v[48:51], v[44:47], v[28:31], v[48:51]\ns_waitcnt lgkmcnt(2)\nv_mfma_f32_16x16x32_bf16 v[16:19], a[0:3], v[80:83], 0\nds_read_b128 v[92:95], v2 offset:2304\nv_max3_f32 v62, v52, v53, v54\nv_max3_f32 v62, v62, v56, v57\n" \
"v_mfma_f32_16x16x32_bf16 v[20:23], a[8:11], v[80:83], 0\nds_read_b128 v[72:75], v1 offset:0\nv_max3_f32 v62, v62, v58, v59\nv_max_f32 v62, v62, v55\nv_mfma_f32_16x16x32_bf16 v[24:27], a[16:19], v[80:83], 0\nv_max3_f32 v63, v62, v48, v49\nv_max3_f32 v62, v62, v48, v49\nv_mfma_f32_16x16x32_bf16 v[28:31], a[24:27], v[80:83], 0\n" \
"s_waitcnt lgkmcnt(3)\nv_permlane16_swap_b32 v62, v63\nv_max_f32 v50, v62, v63\nv_mfma_f32_16x16x32_bf16 v[32:35], a[0:3], v[84:87], 0\nds_read_b128 v[80:83], v2 offset:4096\nv_max_f32 v62, v62, v63\nv_mfma_f32_16x16x32_bf16 v[36:39], a[8:11], v[84:87], 0\ns_nop 0\n" \
"v_permlane32_swap_b32 v62, v50\nv_max_f32 v62, v62, v50\nv_mfma_f32_16x16x32_bf16 v[40:43], a[16:19], v[84:87], 0\nv_mul_f32 v62, s59, v62\nv_fma_f32 v52, v52, s58, v62\nv_fma_f32 v53, v53, s58, v62\nv_mfma_f32_16x16x32_bf16 v[44:47], a[24:27], v[84:87], 0\ns_waitcnt lgkmcnt(3)\n" \
"v_exp_f32 v52, v52\nv_fma_f32 v54, v54, s58, v62\nv_mfma_f32_16x16x32_bf16 v[16:19], a[4:7], v[88:91], v[16:19]\nds_read_b128 v[84:87], v2 offset:4352\nv_exp_f32 v53, v53\nv_mfma_f32_16x16x32_bf16 v[20:23], a[12:15], v[88:91], v[20:23]\nds_read_b128 v[76:79], v1 offset:1024\nv_fma_f32 v55, v55, s58, v62\n" \
"v_add_f32 v63, v52, v53\nv_mfma_f32_16x16x32_bf16 v[24:27], a[20:23], v[88:91], v[24:27]\nv_exp_f32 v54, v54\nv_fma_f32 v56, v56, s58, v62\nv_mfma_f32_16x16x32_bf16 v[28:31], a[28:31], v[88:91], v[28:31]\ns_waitcnt lgkmcnt(4)\nv_exp_f32 v55, v55\nv_add_f32 v63, v63, v54\n" \
"v_mfma_f32_16x16x32_bf16 v[32:35], a[4:7], v[92:95], v[32:35]\nds_read_b128 v[88:91], v2 offset:6144\nv_fma_f32 v57, v57, s58, v62\nv_add_f32 v63, v63, v55\nv_mfma_f32_16x16x32_bf16 v[36:39], a[12:15], v[92:95], v[36:39]\nv_exp_f32 v56, v56\nv_fma_f32 v58, v58, s58, v62\nv_mfma_f32_16x16x32_bf16 v[40:43], a[20:23], v[92:95], v[40:43]\n" \
"v_exp_f32 v57, v57\nv_add_f32 v63, v63, v56\nv_mfma_f32_16x16x32_bf16 v[44:47], a[28:31], v[92:95], v[44:47]\ns_waitcnt lgkmcnt(3)\nv_fma_f32 v59, v59, s58, v62\nv_exp_f32 v58, v58\nv_mfma_f32_16x16x32_bf16 v[16:19], a[32:35], v[80:83], v[16:19]\nds_read_b128 v[92:95], v2 offset:6400\n" \
"v_add_f32 v63, v63, v57\nv_fma_f32 v48, v48, s58, v62\nv_mfma_f32_16x16x32_bf16 v[20:23], v[72:75], v[80:83], v[20:23]\nds_read_b128 v[64:67], v1 offset:2048\nv_exp_f32 v59, v59\nv_mfma_f32_16x16x32_bf16 v[24:27], a[40:43], v[80:83], v[24:27]\nv_add_f32 v63, v63, v58\nv_fma_f32 v49, v49, s58, v62\n" \
"v_add_f32 v63, v63, v59\nv_mfma_f32_16x16x32_bf16 v[28:31], a[48:51], v[80:83], v[28:31]\ns_waitcnt lgkmcnt(4)\nv_exp_f32 v48, v48\nv_mfma_f32_16x16x32_bf16 v[32:35], a[32:35], v[84:87], v[32:35]\nds_read_b128 v[80:83], v2 offset:8192\nv_exp_f32 v49, v49\nv_mfma_f32_16x16x32_bf16 v[36:39], v[72:75], v[84:87], v[36:39]\n" \
"v_add_f32 v63, v63, v48\nv_add_f32 v62, v63, v49\nv_add_f32 v50, v63, v49\nv_mfma_f32_16x16x32_bf16 v[40:43], a[40:43], v[84:87], v[40:43]\ns_nop 0\nv_permlane16_swap_b32 v62, v50\nv_add_f32 v63, v62, v50\nv_mfma_f32_16x16x32_bf16 v[44:47], a[48:51], v[84:87], v[44:47]\n" \
"s_waitcnt lgkmcnt(3)\nv_add_f32 v50, v62, v50\nv_mfma_f32_16x16x32_bf16 v[16:19], a[36:39], v[88:91], v[16:19]\nds_read_b128 v[84:87], v2 offset:8448\nv_permlane32_swap_b32 v63, v50\nv_mfma_f32_16x16x32_bf16 v[20:23], v[76:79], v[88:91], v[20:23]\nds_read_b128 v[68:71], v1 offset:3072\nv_add_f32 v63, v63, v50\n" \
"v_mfma_f32_16x16x32_bf16 v[24:27], a[44:47], v[88:91], v[24:27]\nv_rcp_f32 v63, v63\nv_mfma_f32_16x16x32_bf16 v[28:31], a[52:55], v[88:91], v[28:31]\ns_waitcnt lgkmcnt(4)\nv_mul_f32 v52, v52, v63\nv_mul_f32 v53, v53, v63\nv_mul_f32 v54, v54, v63\nv_mfma_f32_16x16x32_bf16 v[32:35], a[36:39], v[92:95], v[32:35]\n" \
"ds_read_b128 v[88:91], v2 offset:10240\nv_mul_f32 v55, v55, v63\nv_mul_f32 v56, v56, v63\nv_mfma_f32_16x16x32_bf16 v[36:39], v[76:79], v[92:95], v[36:39]\nv_mul_f32 v57, v57, v63\nv_mul_f32 v58, v58, v63\nv_mul_f32 v59, v59, v63\nv_mfma_f32_16x16x32_bf16 v[40:43], a[44:47], v[92:95], v[40:43]\n" \
"v_mul_f32 v48, v48, v63\nv_mul_f32 v49, v49, v63\nv_cvt_pk_bf16_f32 v52, v52, v53\nv_mfma_f32_16x16x32_bf16 v[44:47], a[52:55], v[92:95], v[44:47]\ns_waitcnt lgkmcnt(3)\nv_cvt_pk_bf16_f32 v53, v54, v55\nv_cvt_pk_bf16_f32 v48, v48, v49\nv_cvt_pk_bf16_f32 v54, v56, v57\n" \
"v_mfma_f32_16x16x32_bf16 v[16:19], a[56:59], v[80:83], v[16:19]\nds_read_b128 v[92:95], v2 offset:10496\nv_cvt_pk_bf16_f32 v55, v58, v59\nv_bfi_b32 v52, s61, v52, v61\nv_mfma_f32_16x16x32_bf16 v[20:23], a[64:67], v[80:83], v[20:23]\nds_read_b128 v[72:75], v1 offset:4096\nv_bfi_b32 v53, s61, v53, v61\nv_bfi_b32 v54, s61, v54, v61\n" \
"v_mfma_f32_16x16x32_bf16 v[24:27], v[64:67], v[80:83], v[24:27]\nv_bfi_b32 v55, s61, v55, v61\nv_bfi_b32 v48, s61, v48, v61\nv_mfma_f32_16x16x32_bf16 v[28:31], a[72:75], v[80:83], v[28:31]\ns_waitcnt lgkmcnt(4)\nbuffer_store_dwordx4 v[52:55], v10, s[36:39], s55 offen sc1\nv_mov_b32 v49, v48\nv_mfma_f32_16x16x32_bf16 v[32:35], a[56:59], v[84:87], v[32:35]\n" \
"ds_read_b128 v[80:83], v2 offset:12288\nv_permlane16_swap_b32 v48, v49\nv_mfma_f32_16x16x32_bf16 v[36:39], a[64:67], v[84:87], v[36:39]\nv_mov_b32 v50, v48\nv_mov_b32 v51, v49\nv_mfma_f32_16x16x32_bf16 v[40:43], v[64:67], v[84:87], v[40:43]\nv_permlane32_swap_b32 v48, v50\nv_mfma_f32_16x16x32_bf16 v[44:47], a[72:75], v[84:87], v[44:47]\n" \
"s_waitcnt lgkmcnt(3)\nv_permlane32_swap_b32 v49, v51\nv_mfma_f32_16x16x32_bf16 v[16:19], a[60:63], v[88:91], v[16:19]\nds_read_b128 v[84:87], v2 offset:12544\nv_mfma_f32_16x16x32_bf16 v[20:23], a[68:71], v[88:91], v[20:23]\nds_read_b128 v[76:79], v1 offset:5120\nv_mfma_f32_16x16x32_bf16 v[24:27], v[68:71], v[88:91], v[24:27]\nbuffer_store_dwordx4 v[48:51], v11, s[36:39], s55 offen sc1\n" \
"v_cmp_ne_u32 vcc, 0, v60\nv_mfma_f32_16x16x32_bf16 v[28:31], a[76:79], v[88:91], v[28:31]\ns_waitcnt lgkmcnt(4)\nv_mfma_f32_16x16x32_bf16 v[32:35], a[60:63], v[92:95], v[32:35]\nds_read_b128 v[88:91], v2 offset:14336\nv_mfma_f32_16x16x32_bf16 v[36:39], a[68:71], v[92:95], v[36:39]\nv_mfma_f32_16x16x32_bf16 v[40:43], v[68:71], v[92:95], v[40:43]\nv_mfma_f32_16x16x32_bf16 v[44:47], a[76:79], v[92:95], v[44:47]\n" \
"s_cbranch_vccnz .Lqk_pj%=\ns_cmp_lt_i32 s40, 63\ns_waitcnt lgkmcnt(0)\ns_cbranch_scc1 .Lqk_pwait%=\ns_branch .Lqk_end%=\n.Lqk_re2%=:\nv_max_u32 v17, v80, v82\nv_cmp_eq_u32 vcc, -1, v17\n" \
"s_cbranch_vccz .Lqk_sk2418%=\ns_mov_b64 exec, vcc\nbuffer_load_dwordx4 v[80:83], v3, s[36:39], s42 offen offset:0 sc1\ns_mov_b64 exec, -1\n.Lqk_sk2418%=:\nv_max_u32 v17, v84, v86\nv_cmp_eq_u32 vcc, -1, v17\ns_cbranch_vccz .Lqk_sk2425%=\n" \
"s_mov_b64 exec, vcc\nbuffer_load_dwordx4 v[84:87], v3, s[36:39], s42 offen offset:1024 sc1\ns_mov_b64 exec, -1\n.Lqk_sk2425%=:\nv_max_u32 v17, v88, v90\nv_cmp_eq_u32 vcc, -1, v17\ns_cbranch_vccz .Lqk_sk2432%=\ns_mov_b64 exec, vcc\n" \
"buffer_load_dwordx4 v[88:91], v3, s[36:39], s42 offen offset:2048 sc1\ns_mov_b64 exec, -1\n.Lqk_sk2432%=:\nv_max_u32 v17, v92, v94\nv_cmp_eq_u32 vcc, -1, v17\ns_cbranch_vccz .Lqk_sk2439%=\ns_mov_b64 exec, vcc\nbuffer_load_dwordx4 v[92:95], v3, s[36:39], s42 offen offset:3072 sc1\n" \
"s_mov_b64 exec, -1\n.Lqk_sk2439%=:\nv_mov_b64 v[48:49], 0\nv_mov_b64 v[50:51], 0\nv_mov_b64 v[52:53], 0\nv_mov_b64 v[54:55], 0\nv_mov_b64 v[56:57], 0\nv_mov_b64 v[58:59], 0\n" \
"v_mov_b64 v[60:61], 0\nv_mov_b64 v[62:63], 0\ns_add_u32 s43, s43, 1\ns_cmp_lt_u32 s43, 262144\ns_cbranch_scc1 .Lqk_poll2%=\ns_branch .Lqk_abort%=\n.p2align 6\n.Lqk_c3%=:\n" \
"s_waitcnt lgkmcnt(3)\nv_mfma_f32_16x16x32_bf16 v[32:35], v[232:235], v[56:59], v[32:35]\ns_add_u32 s44, s40, 1\nv_mov_b64 v[52:53], 0\nv_mov_b64 v[54:55], 0\nv_mfma_f32_16x16x32_bf16 v[36:39], v[240:243], v[56:59], v[36:39]\ns_lshl_b32 s44, s44, 17\ns_waitcnt lgkmcnt(2)\n" \
"ds_read_b128 v[64:67], v1 offset:18432\nv_mfma_f32_16x16x32_bf16 v[32:35], v[236:239], v[60:63], v[32:35]\ns_add_u32 s44, s44, %[xn]\ns_lshr_b32 s45, s44, 8\nds_read_b128 v[68:71], v1 offset:19456\nv_mfma_f32_16x16x32_bf16 v[36:39], v[244:247], v[60:63], v[36:39]\ns_and_b32 s45, s45, 0x2000\ns_xor_b32 s41, s44, s45\n" \
"v_mfma_f32_16x16x32_bf16 v[40:43], v[248:251], v[56:59], v[40:43]\nbuffer_load_dword v16, v12, s[36:39], s56 offen sc1\nv_mfma_f32_16x16x32_bf16 v[44:47], v[72:75], v[56:59], v[44:47]\nv_cvt_pk_bf16_f32 v36, v32, v36\nv_cvt_pk_bf16_f32 v37, v33, v37\nv_mfma_f32_16x16x32_bf16 v[40:43], v[252:255], v[60:63], v[40:43]\nv_cvt_pk_bf16_f32 v38, v34, v38\nv_cvt_pk_bf16_f32 v39, v35, v39\n" \
"v_mfma_f32_16x16x32_bf16 v[44:47], v[76:79], v[60:63], v[44:47]\nds_read_b128 v[72:75], v1 offset:20480\nds_read_b128 v[76:79], v1 offset:21504\nv_mov_b64 v[56:57], 0\nv_mov_b64 v[58:59], 0\nv_mov_b64 v[60:61], 0\nv_mov_b64 v[62:63], 0\n.Lqk_poll3%=:\n" \
"s_waitcnt vmcnt(1)\nv_smfmac_f32_16x16x64_bf16 v[48:51], v[80:83], v[152:159], v4\nds_write_b128 v0, v[80:83] offset:28672\nv_max3_u32 v17, v80, v82, v84\nv_smfmac_f32_16x16x64_bf16 v[52:55], v[80:83], v[160:167], v4\nv_max3_u32 v17, v17, v86, v88\nv_smfmac_f32_16x16x64_bf16 v[56:59], v[80:83], v[168:175], v4\nds_write_b128 v0, v[84:87] offset:29696\n" \
"v_max3_u32 v17, v17, v90, v92\nv_max_u32 v17, v17, v94\nv_smfmac_f32_16x16x64_bf16 v[60:63], v[80:83], v[176:183], v4\nv_cmp_eq_u32 vcc, -1, v17\nv_smfmac_f32_16x16x64_bf16 v[48:51], v[84:87], v[184:191], v4\nds_write_b128 v0, v[88:91] offset:30720\ns_cbranch_vccnz .Lqk_re3%=\ns_waitcnt lgkmcnt(5)\n" \
"v_smfmac_f32_16x16x64_bf16 v[52:55], v[84:87], v[64:71], v4\nv_smfmac_f32_16x16x64_bf16 v[56:59], v[84:87], v[192:199], v4\nds_write_b128 v0, v[92:95] offset:31744\nv_smfmac_f32_16x16x64_bf16 v[60:63], v[84:87], v[200:207], v4\nv_smfmac_f32_16x16x64_bf16 v[48:51], v[88:91], v[208:215], v4\nv_smfmac_f32_16x16x64_bf16 v[52:55], v[88:91], v[216:223], v4\ns_waitcnt lgkmcnt(0)\n.Lqk_jn3%=:\n" \
"s_barrier\nds_read_b128 v[80:83], v0 offset:16384\nds_read_b128 v[64:67], v1 offset:22528\nds_read_b128 v[68:71], v1 offset:23552\nv_smfmac_f32_16x16x64_bf16 v[56:59], v[88:91], v[72:79], v4\ns_waitcnt vmcnt(0)\nds_read_b128 v[72:75], v1 offset:0\nds_read_b128 v[76:79], v1 offset:1024\n" \
"v_cmp_ge_i32 vcc, s40, v16\nv_smfmac_f32_16x16x64_bf16 v[60:63], v[88:91], v[224:231], v4\ns_cmp_eq_u64 vcc, 0\ns_cselect_b32 s61, s41, -2147483648\ns_cselect_b32 s44, 1, 0\nds_read_b128 v[84:87], v0 offset:17408\nds_read_b128 v[88:91], v0 offset:18432\nv_smfmac_f32_16x16x64_bf16 v[48:51], v[92:95], v[232:239], v4\n" \
"s_add_u32 m0, %[l0], 0\ns_add_u32 s60, s61, 0\nds_write_b128 v5, v[36:39] offset:256\nbuffer_load_dwordx4 v0, s[36:39], s60 offen offset:0 sc1 lds\nv_cvt_pk_bf16_f32 v44, v40, v44\nv_cvt_pk_bf16_f32 v45, v41, v45\nv_cvt_pk_bf16_f32 v46, v42, v46\nv_cvt_pk_bf16_f32 v47, v43, v47\n" \
"v_smfmac_f32_16x16x64_bf16 v[52:55], v[92:95], v[240:247], v4\nv_mov_b32 v16, s44\nv_smfmac_f32_16x16x64_bf16 v[56:59], v[92:95], v[248:255], v4\ns_waitcnt lgkmcnt(5)\nbuffer_load_dwordx4 v0, s[36:39], s60 offen offset:1024 sc1 lds\nv_smfmac_f32_16x16x64_bf16 v[60:63], v[92:95], v[64:71], v4\nds_read_b128 v[92:95], v0 offset:19456\nds_read_b128 v[64:67], v1 offset:2048\n" \
"ds_read_b128 v[68:71], v1 offset:3072\nds_write_b32 v0, v16 offset:53760\nv_smfmac_f32_16x16x64_bf16 v[48:51], v[80:83], a[0:7], v4\nds_write_b128 v5, v[44:47] offset:768\nbuffer_load_dwordx4 v0, s[36:39], s60 offen offset:2048 sc1 lds\nv_smfmac_f32_16x16x64_bf16 v[52:55], v[80:83], a[8:15], v4\nv_smfmac_f32_16x16x64_bf16 v[56:59], v[80:83], a[16:23], v4\nbuffer_load_dwordx4 v0, s[36:39], s60 offen offset:3072 sc1 lds\n" \
"v_smfmac_f32_16x16x64_bf16 v[60:63], v[80:83], a[24:31], v4\ns_waitcnt lgkmcnt(7)\ns_add_u32 m0, %[l0], 4096\nds_read_b128 v[80:83], v0 offset:20480\nv_smfmac_f32_16x16x64_bf16 v[48:51], v[84:87], a[32:39], v4\ns_add_u32 s60, s61, 4096\nbuffer_load_dwordx4 v0, s[36:39], s60 offen offset:0 sc1 lds\nv_smfmac_f32_16x16x64_bf16 v[52:55], v[84:87], v[72:79], v4\n" \
"ds_read_b128 v[72:75], v1 offset:4096\nds_read_b128 v[76:79], v1 offset:5120\nv_smfmac_f32_16x16x64_bf16 v[56:59], v[84:87], a[40:47], v4\nbuffer_load_dwordx4 v0, s[36:39], s60 offen offset:1024 sc1 lds\nv_smfmac_f32_16x16x64_bf16 v[60:63], v[84:87], a[48:55], v4\ns_waitcnt lgkmcnt(9)\nds_read_b128 v[84:87], v0 offset:21504\nv_smfmac_f32_16x16x64_bf16 v[48:51], v[88:91], a[56:63], v4\n" \
"s_waitcnt lgkmcnt(4)\ns_barrier\nbuffer_load_dwordx4 v0, s[36:39], s60 offen offset:2048 sc1 lds\nv_smfmac_f32_16x16x64_bf16 v[52:55], v[88:91], a[64:71], v4\nv_smfmac_f32_16x16x64_bf16 v[56:59], v[88:91], v[64:71], v4\nds_read_b128 v[64:67], v1 offset:6144\nds_read_b128 v[68:71], v1 offset:7168\nbuffer_load_dwordx4 v0, s[36:39], s60 offen offset:3072 sc1 lds\n" \
"v_smfmac_f32_16x16x64_bf16 v[60:63], v[88:91], a[72:79], v4\ns_add_u32 m0, %[l0], 8192\nds_read_b128 v[88:91], v0 offset:22528\nv_smfmac_f32_16x16x64_bf16 v[48:51], v[92:95], a[80:87], v4\ns_add_u32 s60, s61, 16384\nbuffer_load_dwordx4 v0, s[36:39], s60 offen offset:0 sc1 lds\nv_smfmac_f32_16x16x64_bf16 v[52:55], v[92:95], a[88:95], v4\nv_smfmac_f32_16x16x64_bf16 v[56:59], v[92:95], a[96:103], v4\n" \
"s_waitcnt lgkmcnt(4)\nbuffer_load_dwordx4 v0, s[36:39], s60 offen offset:1024 sc1 lds\nv_smfmac_f32_16x16x64_bf16 v[60:63], v[92:95], v[72:79], v4\nds_read_b128 v[92:95], v0 offset:23552\nds_read_b128 v[72:75], v1 offset:8192\nds_read_b128 v[76:79], v1 offset:9216\nv_smfmac_f32_16x16x64_bf16 v[48:51], v[80:83], a[104:111], v4\nbuffer_load_dwordx4 v0, s[36:39], s60 offen offset:2048 sc1 lds\n" \
"v_smfmac_f32_16x16x64_bf16 v[52:55], v[80:83], a[112:119], v4\nv_smfmac_f32_16x16x64_bf16 v[56:59], v[80:83], a[120:127], v4\nbuffer_load_dwordx4 v0, s[36:39], s60 offen offset:3072 sc1 lds\nv_smfmac_f32_16x16x64_bf16 v[60:63], v[80:83], a[128:135], v4\ns_waitcnt lgkmcnt(6)\ns_add_u32 m0, %[l0], 12288\nds_read_b128 v[80:83], v0 offset:24576\nv_smfmac_f32_16x16x64_bf16 v[48:51], v[84:87], a[136:143], v4\n" \
"s_add_u32 s60, s61, 20480\ns_waitcnt lgkmcnt(5)\nbuffer_load_dwordx4 v0, s[36:39], s60 offen offset:0 sc1 lds\nv_smfmac_f32_16x16x64_bf16 v[52:55], v[84:87], v[64:71], v4\nds_read_b128 v[64:67], v1 offset:10240\nds_read_b128 v[68:71], v1 offset:11264\nv_smfmac_f32_16x16x64_bf16 v[56:59], v[84:87], a[144:151], v4\nbuffer_load_dwordx4 v0, s[36:39], s60 offen offset:1024 sc1 lds\n" \
"v_smfmac_f32_16x16x64_bf16 v[60:63], v[84:87], a[152:159], v4\ns_waitcnt lgkmcnt(6)\nds_read_b128 v[84:87], v0 offset:25600\nv_smfmac_f32_16x16x64_bf16 v[48:51], v[88:91], a[160:167], v4\nbuffer_load_dwordx4 v0, s[36:39], s60 offen offset:2048 sc1 lds\nv_smfmac_f32_16x16x64_bf16 v[52:55], v[88:91], a[168:175], v4\ns_waitcnt lgkmcnt(4)\nv_smfmac_f32_16x16x64_bf16 v[56:59], v[88:91], v[72:79], v4\n" \
"ds_read_b128 v[72:75], v1 offset:12288\nds_read_b128 v[76:79], v1 offset:13312\nbuffer_load_dwordx4 v0, s[36:39], s60 offen offset:3072 sc1 lds\nv_smfmac_f32_16x16x64_bf16 v[60:63], v[88:91], a[176:183], v4\nds_read_b128 v[88:91], v0 offset:26624\nv_smfmac_f32_16x16x64_bf16 v[48:51], v[92:95], a[184:191], v4\nv_smfmac_f32_16x16x64_bf16 v[52:55], v[92:95], a[192:199], v4\nv_smfmac_f32_16x16x64_bf16 v[56:59], v[92:95], a[200:207], v4\n" \
"s_waitcnt lgkmcnt(4)\nv_smfmac_f32_16x16x64_bf16 v[60:63], v[92:95], v[64:71], v4\nds_read_b128 v[92:95], v0 offset:27648\nds_read_b128 v[64:67], v1 offset:14336\nds_read_b128 v[68:71], v1 offset:15360\nv_smfmac_f32_16x16x64_bf16 v[48:51], v[80:83], a[208:215], v4\nv_smfmac_f32_16x16x64_bf16 v[52:55], v[80:83], a[216:223], v4\nv_smfmac_f32_16x16x64_bf16 v[56:59], v[80:83], a[224:231], v4\n" \
"v_smfmac_f32_16x16x64_bf16 v[60:63], v[80:83], a[232:239], v4\ns_waitcnt lgkmcnt(6)\nv_smfmac_f32_16x16x64_bf16 v[48:51], v[84:87], a[240:247], v4\ns_waitcnt lgkmcnt(4)\nv_smfmac_f32_16x16x64_bf16 v[52:55], v[84:87], v[72:79], v4\nds_read_b128 v[72:75], v1 offset:16384\nds_read_b128 v[76:79], v1 offset:17408\nv_smfmac_f32_16x16x64_bf16 v[56:59], v[84:87], a[248:255], v4\n" \
"v_smfmac_f32_16x16x64_bf16 v[60:63], v[84:87], v[96:103], v4\ns_waitcnt lgkmcnt(5)\nv_smfmac_f32_16x16x64_bf16 v[48:51], v[88:91], v[104:111], v4\nv_smfmac_f32_16x16x64_bf16 v[52:55], v[88:91], v[112:119], v4\ns_waitcnt lgkmcnt(4)\nv_smfmac_f32_16x16x64_bf16 v[48:51], v[92:95], v[128:135], v4\nv_smfmac_f32_16x16x64_bf16 v[52:55], v[92:95], v[136:143], v4\ns_waitcnt lgkmcnt(2)\n" \
"v_smfmac_f32_16x16x64_bf16 v[56:59], v[88:91], v[64:71], v4\nv_add_f32 v48, v48, v50\nv_smfmac_f32_16x16x64_bf16 v[60:63], v[88:91], v[120:127], v4\nv_add_f32 v52, v52, v54\nv_add_f32 v49, v49, v51\nv_smfmac_f32_16x16x64_bf16 v[56:59], v[92:95], v[144:151], v4\ns_waitcnt lgkmcnt(0)\nv_add_f32 v53, v53, v55\n" \
"v_cvt_pk_bf16_f32 v50, v48, v52\nv_smfmac_f32_16x16x64_bf16 v[60:63], v[92:95], v[72:79], v4\nv_cvt_pk_bf16_f32 v51, v49, v53\nds_write2_b32 v6, v50, v51 offset0:0 offset1:4\ns_nop 1\nv_add_f32 v56, v56, v58\nv_add_f32 v57, v57, v59\ns_nop 1\n" \
"v_add_f32 v60, v60, v62\nv_add_f32 v61, v61, v63\nv_cvt_pk_bf16_f32 v62, v56, v60\nv_cvt_pk_bf16_f32 v63, v57, v61\nds_write2_b32 v6, v62, v63 offset0:36 offset1:40\ns_waitcnt vmcnt(0)\ns_waitcnt lgkmcnt(0)\ns_barrier\n" \
"s_cbranch_vccnz .Lqk_slow%=\ns_add_u32 m0, %[l0], 16384\ns_add_u32 s60, s41, 32768\nbuffer_load_dwordx4 v0, s[36:39], s60 offen offset:0 sc1 lds\nbuffer_load_dwordx4 v0, s[36:39], s60 offen offset:1024 sc1 lds\nbuffer_load_dwordx4 v0, s[36:39], s60 offen offset:2048 sc1 lds\nbuffer_load_dwordx4 v0, s[36:39], s60 offen offset:3072 sc1 lds\ns_add_u32 m0, %[l0], 20480\n" \
"s_add_u32 s60, s41, 36864\nbuffer_load_dwordx4 v0, s[36:39], s60 offen offset:0 sc1 lds\nbuffer_load_dwordx4 v0, s[36:39], s60 offen offset:1024 sc1 lds\nds_read_b128 v[80:83], v2 offset:0\nds_read_b128 v[84:87], v2 offset:256\nds_read_b128 v[88:91], v2 offset:2048\nbuffer_load_dwordx4 v0, s[36:39], s60 offen offset:2048 sc1 lds\nbuffer_load_dwordx4 v0, s[36:39], s60 offen offset:3072 sc1 lds\n" \
"s_add_u32 m0, %[l0], 24576\ns_add_u32 s60, s41, 49152\nbuffer_load_dwordx4 v0, s[36:39], s60 offen offset:0 sc1 lds\nbuffer_load_dwordx4 v0, s[36:39], s60 offen offset:1024 sc1 lds\nbuffer_load_dwordx4 v0, s[36:39], s60 offen offset:2048 sc1 lds\nbuffer_load_dwordx4 v0, s[36:39], s60 offen offset:3072 sc1 lds\ns_add_u32 m0, %[l0], 28672\ns_add_u32 s60, s41, 53248\n" \
"buffer_load_dwordx4 v0, s[36:39], s60 offen offset:0 sc1 lds\nbuffer_load_dwordx4 v0, s[36:39], s60 offen offset:1024 sc1 lds\nbuffer_load_dwordx4 v0, s[36:39], s60 offen offset:2048 sc1 lds\nbuffer_load_dwordx4 v0, s[36:39], s60 offen offset:3072 sc1 lds\ns_waitcnt lgkmcnt(2)\nv_mfma_f32_16x16x32_bf16 v[16:19], a[0:3], v[80:83], 0\nds_read_b128 v[92:95], v2 offset:2304\nv_mfma_f32_16x16x32_bf16 v[20:23], a[8:11], v[80:83], 0\n" \
"ds_read_b128 v[72:75], v1 offset:0\nv_mfma_f32_16x16x32_bf16 v[24:27], a[16:19], v[80:83], 0\nv_mfma_f32_16x16x32_bf16 v[28:31], a[24:27], v[80:83], 0\ns_waitcnt lgkmcnt(3)\nv_mfma_f32_16x16x32_bf16 v[32:35], a[0:3], v[84:87], 0\nds_read_b128 v[80:83], v2 offset:4096\nv_mfma_f32_16x16x32_bf16 v[36:39], a[8:11], v[84:87], 0\nv_mfma_f32_16x16x32_bf16 v[40:43], a[16:19], v[84:87], 0\n" \
"v_mfma_f32_16x16x32_bf16 v[44:47], a[24:27], v[84:87], 0\ns_waitcnt lgkmcnt(3)\nv_mfma_f32_16x16x32_bf16 v[16:19], a[4:7], v[88:91], v[16:19]\nds_read_b128 v[84:87], v2 offset:4352\nv_mfma_f32_16x16x32_bf16 v[20:23], a[12:15], v[88:91], v[20:23]\nds_read_b128 v[76:79], v1 offset:1024\nv_mfma_f32_16x16x32_bf16 v[24:27], a[20:23], v[88:91], v[24:27]\nv_mfma_f32_16x16x32_bf16 v[28:31], a[28:31], v[88:91], v[28:31]\n" \
"s_waitcnt lgkmcnt(4)\nv_mfma_f32_16x16x32_bf16 v[32:35], a[4:7], v[92:95], v[32:35]\nds_read_b128 v[88:91], v2 offset:6144\nv_mfma_f32_16x16x32_bf16 v[36:39], a[12:15], v[92:95], v[36:39]\nv_mfma_f32_16x16x32_bf16 v[40:43], a[20:23], v[92:95], v[40:43]\nv_mfma_f32_16x16x32_bf16 v[44:47], a[28:31], v[92:95], v[44:47]\ns_waitcnt lgkmcnt(3)\nv_mfma_f32_16x16x32_bf16 v[16:19], a[32:35], v[80:83], v[16:19]\n" \
"ds_read_b128 v[92:95], v2 offset:6400\nv_mfma_f32_16x16x32_bf16 v[20:23], v[72:75], v[80:83], v[20:23]\nds_read_b128 v[64:67], v1 offset:2048\nv_mfma_f32_16x16x32_bf16 v[24:27], a[40:43], v[80:83], v[24:27]\nv_mfma_f32_16x16x32_bf16 v[28:31], a[48:51], v[80:83], v[28:31]\ns_waitcnt lgkmcnt(4)\nv_mfma_f32_16x16x32_bf16 v[32:35], a[32:35], v[84:87], v[32:35]\nds_read_b128 v[80:83], v2 offset:8192\n" \
"v_mfma_f32_16x16x32_bf16 v[36:39], v[72:75], v[84:87], v[36:39]\nv_mfma_f32_16x16x32_bf16 v[40:43], a[40:43], v[84:87], v[40:43]\nv_mfma_f32_16x16x32_bf16 v[44:47], a[48:51], v[84:87], v[44:47]\ns_waitcnt lgkmcnt(3)\nv_mfma_f32_16x16x32_bf16 v[16:19], a[36:39], v[88:91], v[16:19]\nds_read_b128 v[84:87], v2 offset:8448\nv_mfma_f32_16x16x32_bf16 v[20:23], v[76:79], v[88:91], v[20:23]\nds_read_b128 v[68:71], v1 offset:3072\n" \
"v_mfma_f32_16x16x32_bf16 v[24:27], a[44:47], v[88:91], v[24:27]\nv_mfma_f32_16x16x32_bf16 v[28:31], a[52:55], v[88:91], v[28:31]\ns_waitcnt lgkmcnt(4)\nv_mfma_f32_16x16x32_bf16 v[32:35], a[36:39], v[92:95], v[32:35]\nds_read_b128 v[88:91], v2 offset:10240\nv_mfma_f32_16x16x32_bf16 v[36:39], v[76:79], v[92:95], v[36:39]\nv_mfma_f32_16x16x32_bf16 v[40:43], a[44:47], v[92:95], v[40:43]\nv_mfma_f32_16x16x32_bf16 v[44:47], a[52:55], v[92:95], v[44:47]\n" \
"s_waitcnt lgkmcnt(3)\nv_mfma_f32_16x16x32_bf16 v[16:19], a[56:59], v[80:83], v[16:19]\nds_read_b128 v[92:95], v2 offset:10496\nv_mfma_f32_16x16x32_bf16 v[20:23], a[64:67], v[80:83], v[20:23]\nds_read_b128 v[72:75], v1 offset:4096\nv_mfma_f32_16x16x32_bf16 v[24:27], v[64:67], v[80:83], v[24:27]\nv_mfma_f32_16x16x32_bf16 v[28:31], a[72:75], v[80:83], v[28:31]\ns_waitcnt lgkmcnt(4)\n" \
"v_mfma_f32_16x16x32_bf16 v[32:35], a[56:59], v[84:87], v[32:35]\nds_read_b128 v[80:83], v2 offset:12288\nv_mfma_f32_16x16x32_bf16 v[36:39], a[64:67], v[84:87], v[36:39]\nv_mfma_f32_16x16x32_bf16 v[40:43], v[64:67], v[84:87], v[40:43]\nv_mfma_f32_16x16x32_bf16 v[44:47], a[72:75], v[84:87], v[44:47]\ns_waitcnt lgkmcnt(3)\nv_mfma_f32_16x16x32_bf16 v[16:19], a[60:63], v[88:91], v[16:19]\nds_read_b128 v[84:87], v2 offset:12544\n" \
"v_mfma_f32_16x16x32_bf16 v[20:23], a[68:71], v[88:91], v[20:23]\nds_read_b128 v[76:79], v1 offset:5120\nv_mfma_f32_16x16x32_bf16 v[24:27], v[68:71], v[88:91], v[24:27]\nv_mfma_f32_16x16x32_bf16 v[28:31], a[76:79], v[88:91], v[28:31]\ns_waitcnt lgkmcnt(4)\nv_mfma_f32_16x16x32_bf16 v[32:35], a[60:63], v[92:95], v[32:35]\nds_read_b128 v[88:91], v2 offset:14336\nv_mfma_f32_16x16x32_bf16 v[36:39], a[68:71], v[92:95], v[36:39]\n" \
"v_mfma_f32_16x16x32_bf16 v[40:43], v[68:71], v[92:95], v[40:43]\nv_mfma_f32_16x16x32_bf16 v[44:47], a[76:79], v[92:95], v[44:47]\ns_waitcnt vmcnt(0)\ns_branch .Lqk_pj%=\n.Lqk_slow%=:\ns_cmp_ge_i32 s40, 63\ns_cbranch_scc1 .Lqk_end%=\n.Lqk_fetch0%=:\n" \
"buffer_load_dword v16, v12, s[36:39], s56 offen sc1\ns_waitcnt vmcnt(0)\nv_cmp_ge_i32 vcc, s40, v16\ns_cbranch_vccz .Lqk_fok%=\ns_add_u32 s43, s43, 1\ns_cmp_lt_u32 s43, 262144\ns_cbranch_scc1 .Lqk_fetch0%=\ns_branch .Lqk_abort%=\n" \
".Lqk_fok%=:\ns_add_u32 s44, s40, 1\ns_lshl_b32 s44, s44, 17\ns_add_u32 s44, s44, %[xn]\ns_lshr_b32 s45, s44, 8\ns_and_b32 s45, s45, 0x2000\ns_xor_b32 s41, s44, s45\ns_add_u32 m0, %[l0], 0\n" \
"s_add_u32 s60, s41, 0\nbuffer_load_dwordx4 v0, s[36:39], s60 offen offset:0 sc1 lds\nbuffer_load_dwordx4 v0, s[36:39], s60 offen offset:1024 sc1 lds\nbuffer_load_dwordx4 v0, s[36:39], s60 offen offset:2048 sc1 lds\nbuffer_load_dwordx4 v0, s[36:39], s60 offen offset:3072 sc1 lds\ns_add_u32 m0, %[l0], 4096\ns_add_u32 s60, s41, 4096\nbuffer_load_dwordx4 v0, s[36:39], s60 offen offset:0 sc1 lds\n" \
"buffer_load_dwordx4 v0, s[36:39], s60 offen offset:1024 sc1 lds\nbuffer_load_dwordx4 v0, s[36:39], s60 offen offset:2048 sc1 lds\nbuffer_load_dwordx4 v0, s[36:39], s60 offen offset:3072 sc1 lds\ns_add_u32 m0, %[l0], 8192\ns_add_u32 s60, s41, 16384\nbuffer_load_dwordx4 v0, s[36:39], s60 offen offset:0 sc1 lds\nbuffer_load_dwordx4 v0, s[36:39], s60 offen offset:1024 sc1 lds\nbuffer_load_dwordx4 v0, s[36:39], s60 offen offset:2048 sc1 lds\n" \
"buffer_load_dwordx4 v0, s[36:39], s60 offen offset:3072 sc1 lds\ns_add_u32 m0, %[l0], 12288\ns_add_u32 s60, s41, 20480\nbuffer_load_dwordx4 v0, s[36:39], s60 offen offset:0 sc1 lds\nbuffer_load_dwordx4 v0, s[36:39], s60 offen offset:1024 sc1 lds\nbuffer_load_dwordx4 v0, s[36:39], s60 offen offset:2048 sc1 lds\nbuffer_load_dwordx4 v0, s[36:39], s60 offen offset:3072 sc1 lds\ns_add_u32 m0, %[l0], 16384\n" \
"s_add_u32 s60, s41, 32768\nbuffer_load_dwordx4 v0, s[36:39], s60 offen offset:0 sc1 lds\nbuffer_load_dwordx4 v0, s[36:39], s60 offen offset:1024 sc1 lds\nbuffer_load_dwordx4 v0, s[36:39], s60 offen offset:2048 sc1 lds\nbuffer_load_dwordx4 v0, s[36:39], s60 offen offset:3072 sc1 lds\ns_add_u32 m0, %[l0], 20480\ns_add_u32 s60, s41, 36864\nbuffer_load_dwordx4 v0, s[36:39], s60 offen offset:0 sc1 lds\n" \
"buffer_load_dwordx4 v0, s[36:39], s60 offen offset:1024 sc1 lds\nbuffer_load_dwordx4 v0, s[36:39], s60 offen offset:2048 sc1 lds\nbuffer_load_dwordx4 v0, s[36:39], s60 offen offset:3072 sc1 lds\ns_add_u32 m0, %[l0], 24576\ns_add_u32 s60, s41, 49152\nbuffer_load_dwordx4 v0, s[36:39], s60 offen offset:0 sc1 lds\nbuffer_load_dwordx4 v0, s[36:39], s60 offen offset:1024 sc1 lds\nbuffer_load_dwordx4 v0, s[36:39], s60 offen offset:2048 sc1 lds\n" \
"buffer_load_dwordx4 v0, s[36:39], s60 offen offset:3072 sc1 lds\ns_add_u32 m0, %[l0], 28672\ns_add_u32 s60, s41, 53248\nbuffer_load_dwordx4 v0, s[36:39], s60 offen offset:0 sc1 lds\nbuffer_load_dwordx4 v0, s[36:39], s60 offen offset:1024 sc1 lds\nbuffer_load_dwordx4 v0, s[36:39], s60 offen offset:2048 sc1 lds\nbuffer_load_dwordx4 v0, s[36:39], s60 offen offset:3072 sc1 lds\ns_waitcnt vmcnt(0)\n" \
"s_branch .Lqk_pwait%=\n.Lqk_re3%=:\nv_max_u32 v17, v80, v82\nv_cmp_eq_u32 vcc, -1, v17\ns_cbranch_vccz .Lqk_sk2851%=\ns_mov_b64 exec, vcc\nbuffer_load_dwordx4 v[80:83], v3, s[36:39], s42 offen offset:0 sc1\ns_mov_b64 exec, -1\n" \
".Lqk_sk2851%=:\nv_max_u32 v17, v84, v86\nv_cmp_eq_u32 vcc, -1, v17\ns_cbranch_vccz .Lqk_sk2858%=\ns_mov_b64 exec, vcc\nbuffer_load_dwordx4 v[84:87], v3, s[36:39], s42 offen offset:1024 sc1\ns_mov_b64 exec, -1\n.Lqk_sk2858%=:\n" \
"v_max_u32 v17, v88, v90\nv_cmp_eq_u32 vcc, -1, v17\ns_cbranch_vccz .Lqk_sk2865%=\ns_mov_b64 exec, vcc\nbuffer_load_dwordx4 v[88:91], v3, s[36:39], s42 offen offset:2048 sc1\ns_mov_b64 exec, -1\n.Lqk_sk2865%=:\nv_max_u32 v17, v92, v94\n" \
"v_cmp_eq_u32 vcc, -1, v17\ns_cbranch_vccz .Lqk_sk2872%=\ns_mov_b64 exec, vcc\nbuffer_load_dwordx4 v[92:95], v3, s[36:39], s42 offen offset:3072 sc1\ns_mov_b64 exec, -1\n.Lqk_sk2872%=:\nbuffer_load_dword v16, v12, s[36:39], s56 offen sc1\nv_mov_b64 v[48:49], 0\n" \
"v_mov_b64 v[50:51], 0\nv_mov_b64 v[52:53], 0\nv_mov_b64 v[54:55], 0\nv_mov_b64 v[56:57], 0\nv_mov_b64 v[58:59], 0\nv_mov_b64 v[60:61], 0\nv_mov_b64 v[62:63], 0\ns_add_u32 s43, s43, 1\n" \
"s_cmp_lt_u32 s43, 262144\ns_cbranch_scc1 .Lqk_poll3%=\ns_branch .Lqk_abort%=\n.Lqk_abort%=:\ns_mov_b64 exec, -1\nv_mov_b32 v16, 0\ns_movk_i32 s44, 6400\nbuffer_store_dword v16, off, s[36:39], s44 sc1\n" \
".Lqk_end%=:\ns_waitcnt vmcnt(0) lgkmcnt(0)\n" \
""
#define VU_ASM \
"v_mbcnt_lo_u32_b32 v255, -1, 0\nv_mbcnt_hi_u32_b32 v255, -1, v255\nv_lshlrev_b32 v255, 2, v255\nv_add_u32 v255, %[tb], v255\nds_read_b32 v0, v255 offset:0\nds_read_b32 v1, v255 offset:256\nds_read_b32 v2, v255 offset:512\nds_read_b32 v3, v255 offset:768\n" \
"ds_read_b32 v4, v255 offset:1024\nds_read_b32 v5, v255 offset:1280\nds_read_b32 v6, v255 offset:1536\nds_read_b32 v7, v255 offset:1792\nds_read_b32 v8, v255 offset:2048\nds_read_b32 v9, v255 offset:2304\nds_read_b32 v10, v255 offset:2560\nds_read_b32 v11, v255 offset:2816\n" \
"ds_read_b32 v90, v255 offset:3072\nds_read_b32 v91, v255 offset:3328\nds_read_b32 v92, v255 offset:3584\nds_read_b32 v93, v255 offset:3840\nds_read_b32 v94, v255 offset:4096\nds_read_b32 v95, v255 offset:4352\ns_waitcnt lgkmcnt(0)\ns_mov_b64 s[36:37], %[ws]\n" \
"s_mov_b32 s38, %[sz]\ns_mov_b32 s39, 0x20000\ns_mov_b32 s45, 2096896\nbuffer_load_dword v15, off, s[36:39], s45 sc1\ns_add_u32 m0, %[lw], 16384\ns_add_u32 s44, %[wo], 0\nbuffer_load_dwordx4 v90, %[rw], s44 offen nt lds\ns_add_u32 m0, %[lw], 17408\n" \
"s_add_u32 s44, %[wo], 65536\nbuffer_load_dwordx4 v90, %[rw], s44 offen nt lds\ns_add_u32 m0, %[lw], 18432\ns_add_u32 s44, %[wo], 131072\nbuffer_load_dwordx4 v90, %[rw], s44 offen nt lds\ns_add_u32 m0, %[lw], 19456\ns_add_u32 s44, %[wo], 196608\nbuffer_load_dwordx4 v90, %[rw], s44 offen nt lds\n" \
"s_add_u32 m0, %[lw], 20480\ns_add_u32 s44, %[wo], 524288\nbuffer_load_dwordx4 v90, %[rw], s44 offen nt lds\ns_add_u32 m0, %[lw], 21504\ns_add_u32 s44, %[wo], 589824\nbuffer_load_dwordx4 v90, %[rw], s44 offen nt lds\ns_add_u32 m0, %[lw], 22528\ns_add_u32 s44, %[wo], 655360\n" \
"buffer_load_dwordx4 v90, %[rw], s44 offen nt lds\ns_add_u32 m0, %[lw], 23552\ns_add_u32 s44, %[wo], 720896\nbuffer_load_dwordx4 v90, %[rw], s44 offen nt lds\ns_add_u32 m0, %[tb], 0\ns_add_u32 s44, %[wo], 1048576\nbuffer_load_dwordx4 v90, %[rw], s44 offen nt lds\ns_add_u32 m0, %[tb], 1024\n" \
"s_add_u32 s44, %[wo], 1114112\nbuffer_load_dwordx4 v90, %[rw], s44 offen nt lds\ns_add_u32 m0, %[tb], 2048\ns_add_u32 s44, %[wo], 1179648\nbuffer_load_dwordx4 v90, %[rw], s44 offen nt lds\ns_add_u32 m0, %[tb], 3072\ns_add_u32 s44, %[wo], 1245184\nbuffer_load_dwordx4 v90, %[rw], s44 offen nt lds\n" \
"s_add_u32 m0, %[tb], 4096\ns_add_u32 s44, %[wo], 1572864\nbuffer_load_dwordx4 v90, %[rw], s44 offen nt lds\ns_add_u32 m0, %[tb], 5120\ns_add_u32 s44, %[wo], 1638400\nbuffer_load_dwordx4 v90, %[rw], s44 offen nt lds\ns_add_u32 m0, %[tb], 6144\ns_add_u32 s44, %[wo], 1703936\n" \
"buffer_load_dwordx4 v90, %[rw], s44 offen nt lds\ns_add_u32 m0, %[tb], 7168\ns_add_u32 s44, %[wo], 1769472\nbuffer_load_dwordx4 v90, %[rw], s44 offen nt lds\ns_add_u32 m0, %[tb], 8192\ns_add_u32 s44, %[wo], 2097152\nbuffer_load_dwordx4 v90, %[rw], s44 offen nt lds\ns_add_u32 m0, %[tb], 9216\n" \
"s_add_u32 s44, %[wo], 2162688\nbuffer_load_dwordx4 v90, %[rw], s44 offen nt lds\ns_add_u32 m0, %[tb], 10240\ns_add_u32 s44, %[wo], 2228224\nbuffer_load_dwordx4 v90, %[rw], s44 offen nt lds\ns_add_u32 m0, %[tb], 11264\ns_add_u32 s44, %[wo], 2293760\nbuffer_load_dwordx4 v90, %[rw], s44 offen nt lds\n" \
"s_add_u32 m0, %[lw], 0\ns_add_u32 s44, %[wo], 2621440\nbuffer_load_dwordx4 v90, %[rw], s44 offen nt lds\ns_add_u32 m0, %[lw], 1024\ns_add_u32 s44, %[wo], 2686976\nbuffer_load_dwordx4 v90, %[rw], s44 offen nt lds\ns_add_u32 m0, %[lw], 2048\ns_add_u32 s44, %[wo], 2752512\n" \
"buffer_load_dwordx4 v90, %[rw], s44 offen nt lds\ns_add_u32 m0, %[lw], 3072\ns_add_u32 s44, %[wo], 2818048\nbuffer_load_dwordx4 v90, %[rw], s44 offen nt lds\ns_add_u32 m0, %[lw], 4096\ns_add_u32 s44, %[wo], 3145728\nbuffer_load_dwordx4 v90, %[rw], s44 offen nt lds\ns_add_u32 m0, %[lw], 5120\n" \
"s_add_u32 s44, %[wo], 3211264\nbuffer_load_dwordx4 v90, %[rw], s44 offen nt lds\ns_add_u32 m0, %[lw], 6144\ns_add_u32 s44, %[wo], 3276800\nbuffer_load_dwordx4 v90, %[rw], s44 offen nt lds\ns_add_u32 m0, %[lw], 7168\ns_add_u32 s44, %[wo], 3342336\nbuffer_load_dwordx4 v90, %[rw], s44 offen nt lds\n" \
"s_add_u32 m0, %[lw], 8192\ns_add_u32 s44, %[wo], 3670016\nbuffer_load_dwordx4 v90, %[rw], s44 offen nt lds\ns_add_u32 m0, %[lw], 9216\ns_add_u32 s44, %[wo], 3735552\nbuffer_load_dwordx4 v90, %[rw], s44 offen nt lds\ns_add_u32 m0, %[lw], 10240\ns_add_u32 s44, %[wo], 3801088\n" \
"buffer_load_dwordx4 v90, %[rw], s44 offen nt lds\ns_add_u32 m0, %[lw], 11264\ns_add_u32 s44, %[wo], 3866624\nbuffer_load_dwordx4 v90, %[rw], s44 offen nt lds\ns_waitcnt vmcnt(28)\ns_barrier\ns_add_u32 m0, %[lw], 12288\ns_add_u32 s44, %[wo], 4194304\n" \
"buffer_load_dwordx4 v90, %[rw], s44 offen nt lds\ns_add_u32 m0, %[lw], 13312\ns_add_u32 s44, %[wo], 4259840\nbuffer_load_dwordx4 v90, %[rw], s44 offen nt lds\ns_add_u32 m0, %[lw], 14336\ns_add_u32 s44, %[wo], 4325376\nbuffer_load_dwordx4 v90, %[rw], s44 offen nt lds\ns_add_u32 m0, %[lw], 15360\n" \
"s_add_u32 s44, %[wo], 4390912\nbuffer_load_dwordx4 v90, %[rw], s44 offen nt lds\nds_read_b64_tr_b16 a[0:1], v93 offset:16384\nds_read_b64_tr_b16 a[2:3], v93 offset:16896\nds_read_b64_tr_b16 a[16:17], v93 offset:16392\nds_read_b64_tr_b16 a[18:19], v93 offset:16904\nds_read_b64_tr_b16 a[4:5], v93 offset:40960\nds_read_b64_tr_b16 a[6:7], v93 offset:41472\n" \
"ds_read_b64_tr_b16 a[20:21], v93 offset:40968\nds_read_b64_tr_b16 a[22:23], v93 offset:41480\ns_waitcnt lgkmcnt(0)\ns_waitcnt vmcnt(28)\ns_barrier\ns_add_u32 m0, %[lw], 16384\ns_add_u32 s44, %[wo], 4718592\nbuffer_load_dwordx4 v90, %[rw], s44 offen nt lds\n" \
"s_add_u32 m0, %[lw], 17408\ns_add_u32 s44, %[wo], 4784128\nbuffer_load_dwordx4 v90, %[rw], s44 offen nt lds\ns_add_u32 m0, %[lw], 18432\ns_add_u32 s44, %[wo], 4849664\nbuffer_load_dwordx4 v90, %[rw], s44 offen nt lds\ns_add_u32 m0, %[lw], 19456\ns_add_u32 s44, %[wo], 4915200\n" \
"buffer_load_dwordx4 v90, %[rw], s44 offen nt lds\nds_read_b64_tr_b16 a[24:25], v93 offset:20480\nds_read_b64_tr_b16 a[26:27], v93 offset:20992\nds_read_b64_tr_b16 a[40:41], v93 offset:20488\nds_read_b64_tr_b16 a[42:43], v93 offset:21000\nds_read_b64_tr_b16 a[28:29], v93 offset:45056\nds_read_b64_tr_b16 a[30:31], v93 offset:45568\nds_read_b64_tr_b16 a[44:45], v93 offset:45064\n" \
"ds_read_b64_tr_b16 a[46:47], v93 offset:45576\ns_waitcnt lgkmcnt(0)\ns_waitcnt vmcnt(28)\ns_barrier\ns_add_u32 m0, %[lw], 20480\ns_add_u32 s44, %[wo], 5242880\nbuffer_load_dwordx4 v90, %[rw], s44 offen nt lds\ns_add_u32 m0, %[lw], 21504\n" \
"s_add_u32 s44, %[wo], 5308416\nbuffer_load_dwordx4 v90, %[rw], s44 offen nt lds\ns_add_u32 m0, %[lw], 22528\ns_add_u32 s44, %[wo], 5373952\nbuffer_load_dwordx4 v90, %[rw], s44 offen nt lds\ns_add_u32 m0, %[lw], 23552\ns_add_u32 s44, %[wo], 5439488\nbuffer_load_dwordx4 v90, %[rw], s44 offen nt lds\n" \
"ds_read_b64_tr_b16 v[84:85], v92 offset:0\nds_read_b64_tr_b16 v[86:87], v92 offset:512\nds_read_b64_tr_b16 a[64:65], v92 offset:8\nds_read_b64_tr_b16 a[66:67], v92 offset:520\nds_read_b64_tr_b16 v[80:81], v92 offset:12288\nds_read_b64_tr_b16 v[82:83], v92 offset:12800\nds_read_b64_tr_b16 a[68:69], v92 offset:12296\nds_read_b64_tr_b16 a[70:71], v92 offset:12808\n" \
"s_waitcnt lgkmcnt(0)\ns_waitcnt vmcnt(28)\ns_barrier\ns_add_u32 m0, %[tb], 0\ns_add_u32 s44, %[wo], 5767168\nbuffer_load_dwordx4 v90, %[rw], s44 offen nt lds\ns_add_u32 m0, %[tb], 1024\ns_add_u32 s44, %[wo], 5832704\n" \
"buffer_load_dwordx4 v90, %[rw], s44 offen nt lds\ns_add_u32 m0, %[tb], 2048\ns_add_u32 s44, %[wo], 5898240\nbuffer_load_dwordx4 v90, %[rw], s44 offen nt lds\ns_add_u32 m0, %[tb], 3072\ns_add_u32 s44, %[wo], 5963776\nbuffer_load_dwordx4 v90, %[rw], s44 offen nt lds\nds_read_b64_tr_b16 a[80:81], v92 offset:4096\n" \
"ds_read_b64_tr_b16 a[82:83], v92 offset:4608\nds_read_b64_tr_b16 a[88:89], v92 offset:4104\nds_read_b64_tr_b16 a[90:91], v92 offset:4616\nds_read_b64_tr_b16 a[84:85], v92 offset:16384\nds_read_b64_tr_b16 a[86:87], v92 offset:16896\nds_read_b64_tr_b16 a[92:93], v92 offset:16392\nds_read_b64_tr_b16 a[94:95], v92 offset:16904\ns_waitcnt lgkmcnt(0)\n" \
"s_waitcnt vmcnt(28)\ns_barrier\ns_add_u32 m0, %[tb], 4096\ns_add_u32 s44, %[wo], 6291456\nbuffer_load_dwordx4 v90, %[rw], s44 offen nt lds\ns_add_u32 m0, %[tb], 5120\ns_add_u32 s44, %[wo], 6356992\nbuffer_load_dwordx4 v90, %[rw], s44 offen nt lds\n" \
"s_add_u32 m0, %[tb], 6144\ns_add_u32 s44, %[wo], 6422528\nbuffer_load_dwordx4 v90, %[rw], s44 offen nt lds\ns_add_u32 m0, %[tb], 7168\ns_add_u32 s44, %[wo], 6488064\nbuffer_load_dwordx4 v90, %[rw], s44 offen nt lds\nds_read_b64_tr_b16 a[104:105], v92 offset:8192\nds_read_b64_tr_b16 a[106:107], v92 offset:8704\n" \
"ds_read_b64_tr_b16 a[120:121], v92 offset:8200\nds_read_b64_tr_b16 a[122:123], v92 offset:8712\nds_read_b64_tr_b16 a[108:109], v92 offset:20480\nds_read_b64_tr_b16 a[110:111], v92 offset:20992\nds_read_b64_tr_b16 a[124:125], v92 offset:20488\nds_read_b64_tr_b16 a[126:127], v92 offset:21000\ns_waitcnt lgkmcnt(0)\ns_waitcnt vmcnt(28)\n" \
"s_barrier\ns_add_u32 m0, %[tb], 8192\ns_add_u32 s44, %[wo], 6815744\nbuffer_load_dwordx4 v90, %[rw], s44 offen nt lds\ns_add_u32 m0, %[tb], 9216\ns_add_u32 s44, %[wo], 6881280\nbuffer_load_dwordx4 v90, %[rw], s44 offen nt lds\ns_add_u32 m0, %[tb], 10240\n" \
"s_add_u32 s44, %[wo], 6946816\nbuffer_load_dwordx4 v90, %[rw], s44 offen nt lds\ns_add_u32 m0, %[tb], 11264\ns_add_u32 s44, %[wo], 7012352\nbuffer_load_dwordx4 v90, %[rw], s44 offen nt lds\nds_read_b64_tr_b16 a[128:129], v93 offset:0\nds_read_b64_tr_b16 a[130:131], v93 offset:512\nds_read_b64_tr_b16 v[76:77], v93 offset:8\n" \
"ds_read_b64_tr_b16 v[78:79], v93 offset:520\nds_read_b64_tr_b16 a[132:133], v93 offset:24576\nds_read_b64_tr_b16 a[134:135], v93 offset:25088\nds_read_b64_tr_b16 v[72:73], v93 offset:24584\nds_read_b64_tr_b16 v[74:75], v93 offset:25096\ns_waitcnt lgkmcnt(0)\ns_waitcnt vmcnt(28)\ns_barrier\n" \
"s_add_u32 m0, %[lw], 0\ns_add_u32 s44, %[wo], 7340032\nbuffer_load_dwordx4 v90, %[rw], s44 offen nt lds\ns_add_u32 m0, %[lw], 1024\ns_add_u32 s44, %[wo], 7405568\nbuffer_load_dwordx4 v90, %[rw], s44 offen nt lds\ns_add_u32 m0, %[lw], 2048\ns_add_u32 s44, %[wo], 7471104\n" \
"buffer_load_dwordx4 v90, %[rw], s44 offen nt lds\ns_add_u32 m0, %[lw], 3072\ns_add_u32 s44, %[wo], 7536640\nbuffer_load_dwordx4 v90, %[rw], s44 offen nt lds\nds_read_b64_tr_b16 a[152:153], v93 offset:4096\nds_read_b64_tr_b16 a[154:155], v93 offset:4608\nds_read_b64_tr_b16 a[168:169], v93 offset:4104\nds_read_b64_tr_b16 a[170:171], v93 offset:4616\n" \
"ds_read_b64_tr_b16 a[156:157], v93 offset:28672\nds_read_b64_tr_b16 a[158:159], v93 offset:29184\nds_read_b64_tr_b16 a[172:173], v93 offset:28680\nds_read_b64_tr_b16 a[174:175], v93 offset:29192\ns_waitcnt lgkmcnt(0)\ns_waitcnt vmcnt(28)\ns_barrier\ns_add_u32 m0, %[lw], 4096\n" \
"s_add_u32 s44, %[wo], 7864320\nbuffer_load_dwordx4 v90, %[rw], s44 offen nt lds\ns_add_u32 m0, %[lw], 5120\ns_add_u32 s44, %[wo], 7929856\nbuffer_load_dwordx4 v90, %[rw], s44 offen nt lds\ns_add_u32 m0, %[lw], 6144\ns_add_u32 s44, %[wo], 7995392\nbuffer_load_dwordx4 v90, %[rw], s44 offen nt lds\n" \
"s_add_u32 m0, %[lw], 7168\ns_add_u32 s44, %[wo], 8060928\nbuffer_load_dwordx4 v90, %[rw], s44 offen nt lds\nds_read_b64_tr_b16 a[184:185], v93 offset:8192\nds_read_b64_tr_b16 a[186:187], v93 offset:8704\nds_read_b64_tr_b16 a[192:193], v93 offset:8200\nds_read_b64_tr_b16 a[194:195], v93 offset:8712\nds_read_b64_tr_b16 a[188:189], v93 offset:32768\n" \
"ds_read_b64_tr_b16 a[190:191], v93 offset:33280\nds_read_b64_tr_b16 a[196:197], v93 offset:32776\nds_read_b64_tr_b16 a[198:199], v93 offset:33288\ns_waitcnt lgkmcnt(0)\ns_waitcnt vmcnt(28)\ns_barrier\ns_add_u32 m0, %[lw], 8192\ns_add_u32 s44, %[w2], 0\n" \
"buffer_load_dwordx4 v91, %[ro], s44 offen nt lds\ns_add_u32 m0, %[lw], 9216\ns_add_u32 s44, %[w2], 32768\nbuffer_load_dwordx4 v91, %[ro], s44 offen nt lds\ns_add_u32 m0, %[lw], 10240\ns_add_u32 s44, %[w2], 65536\nbuffer_load_dwordx4 v91, %[ro], s44 offen nt lds\ns_add_u32 m0, %[lw], 11264\n" \
"s_add_u32 s44, %[w2], 98304\nbuffer_load_dwordx4 v91, %[ro], s44 offen nt lds\nds_read_b64_tr_b16 v[68:69], v93 offset:12288\nds_read_b64_tr_b16 v[70:71], v93 offset:12800\nds_read_b64_tr_b16 a[216:217], v93 offset:12296\nds_read_b64_tr_b16 a[218:219], v93 offset:12808\nds_read_b64_tr_b16 v[64:65], v93 offset:36864\nds_read_b64_tr_b16 v[66:67], v93 offset:37376\n" \
"ds_read_b64_tr_b16 a[220:221], v93 offset:36872\nds_read_b64_tr_b16 a[222:223], v93 offset:37384\ns_waitcnt lgkmcnt(0)\ns_waitcnt vmcnt(28)\ns_barrier\ns_add_u32 m0, %[lw], 12288\ns_add_u32 s44, %[w2], 128\nbuffer_load_dwordx4 v91, %[ro], s44 offen nt lds\n" \
"s_add_u32 m0, %[lw], 13312\ns_add_u32 s44, %[w2], 32896\nbuffer_load_dwordx4 v91, %[ro], s44 offen nt lds\ns_add_u32 m0, %[lw], 14336\ns_add_u32 s44, %[w2], 65664\nbuffer_load_dwordx4 v91, %[ro], s44 offen nt lds\ns_add_u32 m0, %[lw], 15360\ns_add_u32 s44, %[w2], 98432\n" \
"buffer_load_dwordx4 v91, %[ro], s44 offen nt lds\nds_read_b64_tr_b16 a[224:225], v93 offset:16384\nds_read_b64_tr_b16 a[226:227], v93 offset:16896\nds_read_b64_tr_b16 a[240:241], v93 offset:16392\nds_read_b64_tr_b16 a[242:243], v93 offset:16904\nds_read_b64_tr_b16 a[228:229], v93 offset:40960\nds_read_b64_tr_b16 a[230:231], v93 offset:41472\nds_read_b64_tr_b16 a[244:245], v93 offset:40968\n" \
"ds_read_b64_tr_b16 a[246:247], v93 offset:41480\ns_waitcnt lgkmcnt(0)\ns_waitcnt vmcnt(28)\ns_barrier\ns_add_u32 m0, %[lw], 16384\ns_add_u32 s44, %[w2], 256\nbuffer_load_dwordx4 v91, %[ro], s44 offen nt lds\ns_add_u32 m0, %[lw], 17408\n" \
"s_add_u32 s44, %[w2], 33024\nbuffer_load_dwordx4 v91, %[ro], s44 offen nt lds\ns_add_u32 m0, %[lw], 18432\ns_add_u32 s44, %[w2], 65792\nbuffer_load_dwordx4 v91, %[ro], s44 offen nt lds\ns_add_u32 m0, %[lw], 19456\ns_add_u32 s44, %[w2], 98560\nbuffer_load_dwordx4 v91, %[ro], s44 offen nt lds\n" \
"ds_read_b64_tr_b16 v[96:97], v93 offset:20480\nds_read_b64_tr_b16 v[98:99], v93 offset:20992\nds_read_b64_tr_b16 v[112:113], v93 offset:20488\nds_read_b64_tr_b16 v[114:115], v93 offset:21000\nds_read_b64_tr_b16 v[100:101], v93 offset:45056\nds_read_b64_tr_b16 v[102:103], v93 offset:45568\nds_read_b64_tr_b16 v[116:117], v93 offset:45064\nds_read_b64_tr_b16 v[118:119], v93 offset:45576\n" \
"s_waitcnt lgkmcnt(0)\ns_waitcnt vmcnt(28)\ns_barrier\ns_add_u32 m0, %[lw], 20480\ns_add_u32 s44, %[w2], 384\nbuffer_load_dwordx4 v91, %[ro], s44 offen nt lds\ns_add_u32 m0, %[lw], 21504\ns_add_u32 s44, %[w2], 33152\n" \
"buffer_load_dwordx4 v91, %[ro], s44 offen nt lds\ns_add_u32 m0, %[lw], 22528\ns_add_u32 s44, %[w2], 65920\nbuffer_load_dwordx4 v91, %[ro], s44 offen nt lds\ns_add_u32 m0, %[lw], 23552\ns_add_u32 s44, %[w2], 98688\nbuffer_load_dwordx4 v91, %[ro], s44 offen nt lds\nds_read_b64_tr_b16 v[128:129], v92 offset:0\n" \
"ds_read_b64_tr_b16 v[130:131], v92 offset:512\nds_read_b64_tr_b16 v[60:61], v92 offset:8\nds_read_b64_tr_b16 v[62:63], v92 offset:520\nds_read_b64_tr_b16 v[132:133], v92 offset:12288\nds_read_b64_tr_b16 v[134:135], v92 offset:12800\nds_read_b64_tr_b16 v[56:57], v92 offset:12296\nds_read_b64_tr_b16 v[58:59], v92 offset:12808\ns_waitcnt lgkmcnt(0)\n" \
"s_waitcnt vmcnt(28)\ns_barrier\ns_add_u32 m0, %[tb], 0\ns_add_u32 s44, %[w2], 139264\nbuffer_load_dwordx4 v91, %[ro], s44 offen nt lds\ns_add_u32 m0, %[tb], 1024\ns_add_u32 s44, %[w2], 172032\nbuffer_load_dwordx4 v91, %[ro], s44 offen nt lds\n" \
"s_add_u32 m0, %[tb], 2048\ns_add_u32 s44, %[w2], 204800\nbuffer_load_dwordx4 v91, %[ro], s44 offen nt lds\ns_add_u32 m0, %[tb], 3072\ns_add_u32 s44, %[w2], 237568\nbuffer_load_dwordx4 v91, %[ro], s44 offen nt lds\nds_read_b64_tr_b16 v[144:145], v92 offset:4096\nds_read_b64_tr_b16 v[146:147], v92 offset:4608\n" \
"ds_read_b64_tr_b16 v[160:161], v92 offset:4104\nds_read_b64_tr_b16 v[162:163], v92 offset:4616\nds_read_b64_tr_b16 v[148:149], v92 offset:16384\nds_read_b64_tr_b16 v[150:151], v92 offset:16896\nds_read_b64_tr_b16 v[164:165], v92 offset:16392\nds_read_b64_tr_b16 v[166:167], v92 offset:16904\ns_waitcnt lgkmcnt(0)\ns_waitcnt vmcnt(28)\n" \
"s_barrier\ns_add_u32 m0, %[tb], 4096\ns_add_u32 s44, %[w2], 139392\nbuffer_load_dwordx4 v91, %[ro], s44 offen nt lds\ns_add_u32 m0, %[tb], 5120\ns_add_u32 s44, %[w2], 172160\nbuffer_load_dwordx4 v91, %[ro], s44 offen nt lds\ns_add_u32 m0, %[tb], 6144\n" \
"s_add_u32 s44, %[w2], 204928\nbuffer_load_dwordx4 v91, %[ro], s44 offen nt lds\ns_add_u32 m0, %[tb], 7168\ns_add_u32 s44, %[w2], 237696\nbuffer_load_dwordx4 v91, %[ro], s44 offen nt lds\nds_read_b64_tr_b16 v[168:169], v92 offset:8192\nds_read_b64_tr_b16 v[170:171], v92 offset:8704\nds_read_b64_tr_b16 v[184:185], v92 offset:8200\n" \
"ds_read_b64_tr_b16 v[186:187], v92 offset:8712\nds_read_b64_tr_b16 v[172:173], v92 offset:20480\nds_read_b64_tr_b16 v[174:175], v92 offset:20992\nds_read_b64_tr_b16 v[188:189], v92 offset:20488\nds_read_b64_tr_b16 v[190:191], v92 offset:21000\ns_waitcnt lgkmcnt(0)\ns_waitcnt vmcnt(28)\ns_barrier\n" \
"s_add_u32 m0, %[tb], 8192\ns_add_u32 s44, %[w2], 139520\nbuffer_load_dwordx4 v91, %[ro], s44 offen nt lds\ns_add_u32 m0, %[tb], 9216\ns_add_u32 s44, %[w2], 172288\nbuffer_load_dwordx4 v91, %[ro], s44 offen nt lds\ns_add_u32 m0, %[tb], 10240\ns_add_u32 s44, %[w2], 205056\n" \
"buffer_load_dwordx4 v91, %[ro], s44 offen nt lds\ns_add_u32 m0, %[tb], 11264\ns_add_u32 s44, %[w2], 237824\nbuffer_load_dwordx4 v91, %[ro], s44 offen nt lds\nds_read_b64_tr_b16 v[200:201], v93 offset:0\nds_read_b64_tr_b16 v[202:203], v93 offset:512\nds_read_b64_tr_b16 v[216:217], v93 offset:8\nds_read_b64_tr_b16 v[218:219], v93 offset:520\n" \
"ds_read_b64_tr_b16 v[204:205], v93 offset:24576\nds_read_b64_tr_b16 v[206:207], v93 offset:25088\nds_read_b64_tr_b16 v[220:221], v93 offset:24584\nds_read_b64_tr_b16 v[222:223], v93 offset:25096\ns_waitcnt lgkmcnt(0)\ns_waitcnt vmcnt(28)\ns_barrier\ns_add_u32 m0, %[lw], 0\n" \
"s_add_u32 s44, %[w2], 139648\nbuffer_load_dwordx4 v91, %[ro], s44 offen nt lds\ns_add_u32 m0, %[lw], 1024\ns_add_u32 s44, %[w2], 172416\nbuffer_load_dwordx4 v91, %[ro], s44 offen nt lds\ns_add_u32 m0, %[lw], 2048\ns_add_u32 s44, %[w2], 205184\nbuffer_load_dwordx4 v91, %[ro], s44 offen nt lds\n" \
"s_add_u32 m0, %[lw], 3072\ns_add_u32 s44, %[w2], 237952\nbuffer_load_dwordx4 v91, %[ro], s44 offen nt lds\nds_read_b64_tr_b16 v[232:233], v93 offset:4096\nds_read_b64_tr_b16 v[234:235], v93 offset:4608\nds_read_b64_tr_b16 v[240:241], v93 offset:4104\nds_read_b64_tr_b16 v[242:243], v93 offset:4616\nds_read_b64_tr_b16 v[236:237], v93 offset:28672\n" \
"ds_read_b64_tr_b16 v[238:239], v93 offset:29184\nds_read_b64_tr_b16 v[244:245], v93 offset:28680\nds_read_b64_tr_b16 v[246:247], v93 offset:29192\ns_waitcnt lgkmcnt(0)\ns_waitcnt vmcnt(28)\ns_barrier\ns_add_u32 m0, %[lw], 4096\ns_add_u32 s44, %[w2], 131072\n" \
"buffer_load_dwordx4 v91, %[ro], s44 offen nt lds\ns_add_u32 m0, %[lw], 5120\ns_add_u32 s44, %[w2], 163840\nbuffer_load_dwordx4 v91, %[ro], s44 offen nt lds\ns_add_u32 m0, %[lw], 6144\ns_add_u32 s44, %[w2], 196608\nbuffer_load_dwordx4 v91, %[ro], s44 offen nt lds\ns_add_u32 m0, %[lw], 7168\n" \
"s_add_u32 s44, %[w2], 229376\nbuffer_load_dwordx4 v91, %[ro], s44 offen nt lds\nds_read_b64_tr_b16 a[8:9], v95 offset:8192\nds_read_b64_tr_b16 a[10:11], v95 offset:8448\nds_read_b64_tr_b16 a[12:13], v95 offset:10240\nds_read_b64_tr_b16 a[14:15], v95 offset:10496\nds_read_b64_tr_b16 a[208:209], v95 offset:8256\nds_read_b64_tr_b16 a[210:211], v95 offset:8512\n" \
"ds_read_b64_tr_b16 a[212:213], v95 offset:10304\nds_read_b64_tr_b16 a[214:215], v95 offset:10560\ns_waitcnt lgkmcnt(0)\ns_waitcnt vmcnt(28)\ns_barrier\ns_add_u32 m0, %[lw], 8192\ns_add_u32 s44, %[w2], 131200\nbuffer_load_dwordx4 v91, %[ro], s44 offen nt lds\n" \
"s_add_u32 m0, %[lw], 9216\ns_add_u32 s44, %[w2], 163968\nbuffer_load_dwordx4 v91, %[ro], s44 offen nt lds\ns_add_u32 m0, %[lw], 10240\ns_add_u32 s44, %[w2], 196736\nbuffer_load_dwordx4 v91, %[ro], s44 offen nt lds\ns_add_u32 m0, %[lw], 11264\ns_add_u32 s44, %[w2], 229504\n" \
"buffer_load_dwordx4 v91, %[ro], s44 offen nt lds\nds_read_b64_tr_b16 a[56:57], v95 offset:12288\nds_read_b64_tr_b16 a[58:59], v95 offset:12544\nds_read_b64_tr_b16 a[60:61], v95 offset:14336\nds_read_b64_tr_b16 a[62:63], v95 offset:14592\nds_read_b64_tr_b16 v[104:105], v95 offset:12352\nds_read_b64_tr_b16 v[106:107], v95 offset:12608\nds_read_b64_tr_b16 v[108:109], v95 offset:14400\n" \
"ds_read_b64_tr_b16 v[110:111], v95 offset:14656\ns_waitcnt lgkmcnt(0)\ns_waitcnt vmcnt(28)\ns_barrier\ns_add_u32 m0, %[lw], 12288\ns_add_u32 s44, %[w2], 131328\nbuffer_load_dwordx4 v91, %[ro], s44 offen nt lds\ns_add_u32 m0, %[lw], 13312\n" \
"s_add_u32 s44, %[w2], 164096\nbuffer_load_dwordx4 v91, %[ro], s44 offen nt lds\ns_add_u32 m0, %[lw], 14336\ns_add_u32 s44, %[w2], 196864\nbuffer_load_dwordx4 v91, %[ro], s44 offen nt lds\ns_add_u32 m0, %[lw], 15360\ns_add_u32 s44, %[w2], 229632\nbuffer_load_dwordx4 v91, %[ro], s44 offen nt lds\n" \
"ds_read_b64_tr_b16 a[112:113], v95 offset:16384\nds_read_b64_tr_b16 a[114:115], v95 offset:16640\nds_read_b64_tr_b16 a[116:117], v95 offset:18432\nds_read_b64_tr_b16 a[118:119], v95 offset:18688\nds_read_b64_tr_b16 v[152:153], v95 offset:16448\nds_read_b64_tr_b16 v[154:155], v95 offset:16704\nds_read_b64_tr_b16 v[156:157], v95 offset:18496\nds_read_b64_tr_b16 v[158:159], v95 offset:18752\n" \
"s_waitcnt lgkmcnt(0)\ns_waitcnt vmcnt(28)\ns_barrier\ns_add_u32 m0, %[lw], 16384\ns_add_u32 s44, %[w2], 131456\nbuffer_load_dwordx4 v91, %[ro], s44 offen nt lds\ns_add_u32 m0, %[lw], 17408\ns_add_u32 s44, %[w2], 164224\n" \
"buffer_load_dwordx4 v91, %[ro], s44 offen nt lds\ns_add_u32 m0, %[lw], 18432\ns_add_u32 s44, %[w2], 196992\nbuffer_load_dwordx4 v91, %[ro], s44 offen nt lds\ns_add_u32 m0, %[lw], 19456\ns_add_u32 s44, %[w2], 229760\nbuffer_load_dwordx4 v91, %[ro], s44 offen nt lds\nds_read_b64_tr_b16 a[160:161], v95 offset:20480\n" \
"ds_read_b64_tr_b16 a[162:163], v95 offset:20736\nds_read_b64_tr_b16 a[164:165], v95 offset:22528\nds_read_b64_tr_b16 a[166:167], v95 offset:22784\nds_read_b64_tr_b16 v[208:209], v95 offset:20544\nds_read_b64_tr_b16 v[210:211], v95 offset:20800\nds_read_b64_tr_b16 v[212:213], v95 offset:22592\nds_read_b64_tr_b16 v[214:215], v95 offset:22848\ns_waitcnt lgkmcnt(0)\n" \
"s_waitcnt vmcnt(28)\ns_barrier\ns_add_u32 m0, %[lw], 20480\ns_add_u32 s44, %[w2], 8192\nbuffer_load_dwordx4 v91, %[ro], s44 offen nt lds\ns_add_u32 m0, %[lw], 21504\ns_add_u32 s44, %[w2], 40960\nbuffer_load_dwordx4 v91, %[ro], s44 offen nt lds\n" \
"s_add_u32 m0, %[lw], 22528\ns_add_u32 s44, %[w2], 73728\nbuffer_load_dwordx4 v91, %[ro], s44 offen nt lds\ns_add_u32 m0, %[lw], 23552\ns_add_u32 s44, %[w2], 106496\nbuffer_load_dwordx4 v91, %[ro], s44 offen nt lds\nds_read_b64_tr_b16 a[48:49], v94 offset:0\nds_read_b64_tr_b16 a[50:51], v94 offset:256\n" \
"ds_read_b64_tr_b16 a[52:53], v94 offset:2048\nds_read_b64_tr_b16 a[54:55], v94 offset:2304\nds_read_b64_tr_b16 a[248:249], v94 offset:64\nds_read_b64_tr_b16 a[250:251], v94 offset:320\nds_read_b64_tr_b16 a[252:253], v94 offset:2112\nds_read_b64_tr_b16 a[254:255], v94 offset:2368\ns_waitcnt lgkmcnt(0)\ns_waitcnt vmcnt(28)\n" \
"s_barrier\ns_add_u32 m0, %[tb], 0\ns_add_u32 s44, %[w2], 8320\nbuffer_load_dwordx4 v91, %[ro], s44 offen nt lds\ns_add_u32 m0, %[tb], 1024\ns_add_u32 s44, %[w2], 41088\nbuffer_load_dwordx4 v91, %[ro], s44 offen nt lds\ns_add_u32 m0, %[tb], 2048\n" \
"s_add_u32 s44, %[w2], 73856\nbuffer_load_dwordx4 v91, %[ro], s44 offen nt lds\ns_add_u32 m0, %[tb], 3072\ns_add_u32 s44, %[w2], 106624\nbuffer_load_dwordx4 v91, %[ro], s44 offen nt lds\nds_read_b64_tr_b16 a[96:97], v94 offset:4096\nds_read_b64_tr_b16 a[98:99], v94 offset:4352\nds_read_b64_tr_b16 a[100:101], v94 offset:6144\n" \
"ds_read_b64_tr_b16 a[102:103], v94 offset:6400\nds_read_b64_tr_b16 v[136:137], v94 offset:4160\nds_read_b64_tr_b16 v[138:139], v94 offset:4416\nds_read_b64_tr_b16 v[140:141], v94 offset:6208\nds_read_b64_tr_b16 v[142:143], v94 offset:6464\ns_waitcnt lgkmcnt(0)\nbuffer_load_dwordx2 v[12:13], off, s[36:39], s45 sc1\ns_waitcnt vmcnt(29)\n" \
"s_barrier\ns_add_u32 m0, %[tb], 4096\ns_add_u32 s44, %[w2], 8448\nbuffer_load_dwordx4 v91, %[ro], s44 offen nt lds\ns_add_u32 m0, %[tb], 5120\ns_add_u32 s44, %[w2], 41216\nbuffer_load_dwordx4 v91, %[ro], s44 offen nt lds\ns_add_u32 m0, %[tb], 6144\n" \
"s_add_u32 s44, %[w2], 73984\nbuffer_load_dwordx4 v91, %[ro], s44 offen nt lds\ns_add_u32 m0, %[tb], 7168\ns_add_u32 s44, %[w2], 106752\nbuffer_load_dwordx4 v91, %[ro], s44 offen nt lds\nds_read_b64_tr_b16 a[144:145], v94 offset:8192\nds_read_b64_tr_b16 a[146:147], v94 offset:8448\nds_read_b64_tr_b16 a[148:149], v94 offset:10240\n" \
"ds_read_b64_tr_b16 a[150:151], v94 offset:10496\nds_read_b64_tr_b16 v[192:193], v94 offset:8256\nds_read_b64_tr_b16 v[194:195], v94 offset:8512\nds_read_b64_tr_b16 v[196:197], v94 offset:10304\nds_read_b64_tr_b16 v[198:199], v94 offset:10560\ns_waitcnt lgkmcnt(0)\ns_waitcnt vmcnt(29)\ns_barrier\n" \
"s_add_u32 m0, %[tb], 8192\ns_add_u32 s44, %[w2], 8576\nbuffer_load_dwordx4 v91, %[ro], s44 offen nt lds\ns_add_u32 m0, %[tb], 9216\ns_add_u32 s44, %[w2], 41344\nbuffer_load_dwordx4 v91, %[ro], s44 offen nt lds\ns_add_u32 m0, %[tb], 10240\ns_add_u32 s44, %[w2], 74112\n" \
"buffer_load_dwordx4 v91, %[ro], s44 offen nt lds\ns_add_u32 m0, %[tb], 11264\ns_add_u32 s44, %[w2], 106880\nbuffer_load_dwordx4 v91, %[ro], s44 offen nt lds\nds_read_b64_tr_b16 a[200:201], v95 offset:0\nds_read_b64_tr_b16 a[202:203], v95 offset:256\nds_read_b64_tr_b16 a[204:205], v95 offset:2048\nds_read_b64_tr_b16 a[206:207], v95 offset:2304\n" \
"ds_read_b64_tr_b16 v[248:249], v95 offset:64\nds_read_b64_tr_b16 v[250:251], v95 offset:320\nds_read_b64_tr_b16 v[252:253], v95 offset:2112\nds_read_b64_tr_b16 v[254:255], v95 offset:2368\ns_waitcnt lgkmcnt(0)\ns_waitcnt vmcnt(29)\ns_barrier\nds_read_b64_tr_b16 v[52:53], v95 offset:4096\n" \
"ds_read_b64_tr_b16 v[54:55], v95 offset:4352\nds_read_b64_tr_b16 v[48:49], v95 offset:6144\nds_read_b64_tr_b16 v[50:51], v95 offset:6400\nds_read_b64_tr_b16 v[44:45], v95 offset:4160\nds_read_b64_tr_b16 v[46:47], v95 offset:4416\nds_read_b64_tr_b16 v[40:41], v95 offset:6208\nds_read_b64_tr_b16 v[42:43], v95 offset:6464\ns_waitcnt lgkmcnt(0)\n" \
"ds_write_b128 v1, v[84:87] offset:0\nds_write_b128 v1, v[80:83] offset:1024\nds_write_b128 v1, v[76:79] offset:2048\nds_write_b128 v1, v[72:75] offset:3072\ns_waitcnt vmcnt(25)\ns_barrier\nds_read_b64_tr_b16 a[72:73], v95 offset:8192\nds_read_b64_tr_b16 a[74:75], v95 offset:8448\n" \
"ds_read_b64_tr_b16 a[76:77], v95 offset:10240\nds_read_b64_tr_b16 a[78:79], v95 offset:10496\nds_read_b64_tr_b16 v[120:121], v95 offset:8256\nds_read_b64_tr_b16 v[122:123], v95 offset:8512\nds_read_b64_tr_b16 v[124:125], v95 offset:10304\nds_read_b64_tr_b16 v[126:127], v95 offset:10560\ns_waitcnt lgkmcnt(0)\nds_write_b128 v1, v[68:71] offset:4096\n" \
"ds_write_b128 v1, v[64:67] offset:5120\nds_write_b128 v1, v[60:63] offset:6144\nds_write_b128 v1, v[56:59] offset:7168\ns_waitcnt vmcnt(21)\ns_barrier\nds_read_b64_tr_b16 v[56:57], v95 offset:12288\nds_read_b64_tr_b16 v[58:59], v95 offset:12544\nds_read_b64_tr_b16 v[60:61], v95 offset:14336\n" \
"ds_read_b64_tr_b16 v[62:63], v95 offset:14592\nds_read_b64_tr_b16 v[64:65], v95 offset:12352\nds_read_b64_tr_b16 v[66:67], v95 offset:12608\nds_read_b64_tr_b16 v[68:69], v95 offset:14400\nds_read_b64_tr_b16 v[70:71], v95 offset:14656\ns_waitcnt lgkmcnt(0)\nds_write_b128 v1, v[52:55] offset:8192\nds_write_b128 v1, v[48:51] offset:9216\n" \
"ds_write_b128 v1, v[44:47] offset:10240\nds_write_b128 v1, v[40:43] offset:11264\ns_waitcnt vmcnt(17)\ns_barrier\nds_read_b64_tr_b16 a[176:177], v95 offset:16384\nds_read_b64_tr_b16 a[178:179], v95 offset:16640\nds_read_b64_tr_b16 a[180:181], v95 offset:18432\nds_read_b64_tr_b16 a[182:183], v95 offset:18688\n" \
"ds_read_b64_tr_b16 v[224:225], v95 offset:16448\nds_read_b64_tr_b16 v[226:227], v95 offset:16704\nds_read_b64_tr_b16 v[228:229], v95 offset:18496\nds_read_b64_tr_b16 v[230:231], v95 offset:18752\ns_waitcnt lgkmcnt(0)\nds_write_b128 v1, v[56:59] offset:12288\nds_write_b128 v1, v[60:63] offset:13312\nds_write_b128 v1, v[64:67] offset:14336\n" \
"ds_write_b128 v1, v[68:71] offset:15360\ns_waitcnt vmcnt(13)\ns_barrier\nds_read_b64_tr_b16 a[32:33], v95 offset:20480\nds_read_b64_tr_b16 a[34:35], v95 offset:20736\nds_read_b64_tr_b16 a[36:37], v95 offset:22528\nds_read_b64_tr_b16 a[38:39], v95 offset:22784\nds_read_b64_tr_b16 a[232:233], v95 offset:20544\n" \
"ds_read_b64_tr_b16 a[234:235], v95 offset:20800\nds_read_b64_tr_b16 a[236:237], v95 offset:22592\nds_read_b64_tr_b16 a[238:239], v95 offset:22848\ns_waitcnt lgkmcnt(0)\ns_waitcnt vmcnt(9)\ns_barrier\nds_read_b64_tr_b16 v[68:69], v94 offset:0\nds_read_b64_tr_b16 v[70:71], v94 offset:256\n" \
"ds_read_b64_tr_b16 v[64:65], v94 offset:2048\nds_read_b64_tr_b16 v[66:67], v94 offset:2304\nds_read_b64_tr_b16 v[60:61], v94 offset:64\nds_read_b64_tr_b16 v[62:63], v94 offset:320\nds_read_b64_tr_b16 v[56:57], v94 offset:2112\nds_read_b64_tr_b16 v[58:59], v94 offset:2368\ns_waitcnt lgkmcnt(0)\nds_write_b128 v1, v[68:71] offset:16384\n" \
"ds_write_b128 v1, v[64:67] offset:17408\nds_write_b128 v1, v[60:63] offset:18432\nds_write_b128 v1, v[56:59] offset:19456\ns_waitcnt vmcnt(4)\ns_barrier\nds_read_b64_tr_b16 a[136:137], v94 offset:4096\nds_read_b64_tr_b16 a[138:139], v94 offset:4352\nds_read_b64_tr_b16 a[140:141], v94 offset:6144\n" \
"ds_read_b64_tr_b16 a[142:143], v94 offset:6400\nds_read_b64_tr_b16 v[176:177], v94 offset:4160\nds_read_b64_tr_b16 v[178:179], v94 offset:4416\nds_read_b64_tr_b16 v[180:181], v94 offset:6208\nds_read_b64_tr_b16 v[182:183], v94 offset:6464\ns_waitcnt lgkmcnt(0)\ns_waitcnt vmcnt(0)\ns_barrier\n" \
"ds_read_b64_tr_b16 v[56:57], v94 offset:8192\nds_read_b64_tr_b16 v[58:59], v94 offset:8448\nds_read_b64_tr_b16 v[60:61], v94 offset:10240\nds_read_b64_tr_b16 v[62:63], v94 offset:10496\nds_read_b64_tr_b16 v[64:65], v94 offset:8256\nds_read_b64_tr_b16 v[66:67], v94 offset:8512\nds_read_b64_tr_b16 v[68:69], v94 offset:10304\nds_read_b64_tr_b16 v[70:71], v94 offset:10560\n" \
"s_waitcnt lgkmcnt(0)\ns_mov_b32 s43, 0\ns_mov_b32 s86, 110\ns_mov_b32 s89, 0\n.Lvu_hb%=:\nv_cmp_ne_u32 vcc, v15, v12\ns_cbranch_vccnz .Lvu_hb1%=\ns_sleep 4\n" \
"buffer_load_dwordx2 v[12:13], off, s[36:39], s45 sc1\ns_waitcnt vmcnt(0)\ns_add_u32 s43, s43, 1\ns_cmp_lt_u32 s43, 262144\ns_cbranch_scc1 .Lvu_hb%=\ns_branch .Lvu_abort%=\n.Lvu_hb1%=:\nv_readfirstlane_b32 s44, v13\n" \
"s_and_b32 s44, s44, 0x200000\ns_add_u32 s36, s36, s44\ns_addc_u32 s37, s37, 0\nds_write_b128 v1, v[56:59] offset:20480\nds_write_b128 v1, v[60:63] offset:21504\nds_write_b128 v1, v[64:67] offset:22528\nds_write_b128 v1, v[68:71] offset:23552\ns_waitcnt lgkmcnt(0)\n" \
"s_barrier\ns_mov_b32 s40, -1\ns_mov_b32 s43, 0\ns_lshl_b32 s57, %[w], 12\ns_mov_b32 s56, 0\ns_mov_b32 s42, 0\ns_mov_b32 s60, -2147483648\ns_mov_b32 s54, -2147483648\n" \
"s_add_u32 s44, s40, 1\ns_lshl_b32 s44, s44, 17\ns_add_u32 s44, s44, %[xn]\ns_lshr_b32 s45, s44, 8\ns_and_b32 s45, s45, 0x2000\ns_xor_b32 s41, s44, s45\ns_add_u32 s41, s41, %[xw]\n.Lvu_f0%=:\n" \
"buffer_load_dword v14, v8, s[36:39], s56 offen sc1\ns_waitcnt vmcnt(0)\nv_cmp_ge_i32 vcc, s40, v14\ns_cbranch_vccz .Lvu_f0fok%=\ns_add_u32 s43, s43, 1\ns_cmp_lt_u32 s43, 262144\ns_cbranch_scc1 .Lvu_f0%=\ns_branch .Lvu_abort%=\n" \
".Lvu_f0fok%=:\ns_add_u32 m0, s57, 0\ns_add_u32 s58, s41, 0\nbuffer_load_dwordx4 v0, s[36:39], s58 offen offset:0 sc1 lds\nbuffer_load_dwordx4 v0, s[36:39], s58 offen offset:1024 sc1 lds\nbuffer_load_dwordx4 v0, s[36:39], s58 offen offset:2048 sc1 lds\nbuffer_load_dwordx4 v0, s[36:39], s58 offen offset:3072 sc1 lds\ns_add_u32 m0, s57, 32768\n" \
"s_add_u32 s58, s41, 32768\nbuffer_load_dwordx4 v0, s[36:39], s58 offen offset:0 sc1 lds\nbuffer_load_dwordx4 v0, s[36:39], s58 offen offset:1024 sc1 lds\nbuffer_load_dwordx4 v0, s[36:39], s58 offen offset:2048 sc1 lds\nbuffer_load_dwordx4 v0, s[36:39], s58 offen offset:3072 sc1 lds\ns_waitcnt vmcnt(0)\ns_barrier\nds_read_b128 v[32:35], v2 offset:0\n" \
"ds_read_b128 v[36:39], v2 offset:256\nds_read_b128 v[40:43], v2 offset:2048\nds_read_b128 v[44:47], v2 offset:2304\n.p2align 6\n.Lvu_tok%=:\nds_read_b128 v[64:67], v2 offset:4096\nds_read_b128 v[68:71], v2 offset:4352\ns_waitcnt lgkmcnt(5)\n" \
"v_mfma_f32_16x16x32_bf16 v[16:19], a[0:3], v[32:35], 0\nv_cvt_pk_bf16_f32 v92, v80, v81\nv_cvt_pk_bf16_f32 v93, v82, v83\nv_mfma_f32_16x16x32_bf16 v[20:23], a[16:19], v[32:35], 0\ns_waitcnt lgkmcnt(4)\nds_read_b128 v[32:35], v2 offset:6144\nv_cvt_pk_bf16_f32 v94, v84, v85\nv_cvt_pk_bf16_f32 v95, v86, v87\n" \
"v_mfma_f32_16x16x32_bf16 v[24:27], a[0:3], v[36:39], 0\nv_and_or_b32 v92, v92, -2, v13\nv_and_or_b32 v94, v94, -2, v13\nv_mfma_f32_16x16x32_bf16 v[28:31], a[16:19], v[36:39], 0\ns_waitcnt lgkmcnt(4)\nbuffer_store_dwordx4 v[92:95], v7, s[36:39], s60 offen offset:2560 sc1\nds_read_b128 v[36:39], v2 offset:6400\nv_mfma_f32_16x16x32_bf16 v[16:19], a[4:7], v[40:43], v[16:19]\n" \
"ds_read_b128 v[56:59], v1 offset:0\nds_read_b128 v[60:63], v1 offset:1024\nv_mfma_f32_16x16x32_bf16 v[20:23], a[20:23], v[40:43], v[20:23]\ns_waitcnt lgkmcnt(6)\ns_add_u32 m0, s57, 16384\nds_read_b128 v[40:43], v2 offset:8192\nv_mfma_f32_16x16x32_bf16 v[24:27], a[4:7], v[44:47], v[24:27]\ns_add_u32 s60, s41, 65536\n" \
"buffer_load_dwordx4 v0, s[36:39], s60 offen offset:0 sc1 lds\nv_mfma_f32_16x16x32_bf16 v[28:31], a[20:23], v[44:47], v[28:31]\ns_waitcnt lgkmcnt(6)\nds_read_b128 v[44:47], v2 offset:8448\nv_mfma_f32_16x16x32_bf16 v[16:19], a[24:27], v[64:67], v[16:19]\nv_mfma_f32_16x16x32_bf16 v[20:23], a[40:43], v[64:67], v[20:23]\ns_waitcnt lgkmcnt(6)\nds_read_b128 v[64:67], v2 offset:10240\n" \
"v_mfma_f32_16x16x32_bf16 v[24:27], a[24:27], v[68:71], v[24:27]\nbuffer_load_dwordx4 v0, s[36:39], s60 offen offset:1024 sc1 lds\nv_mfma_f32_16x16x32_bf16 v[28:31], a[40:43], v[68:71], v[28:31]\ns_waitcnt lgkmcnt(6)\nds_read_b128 v[68:71], v2 offset:10496\nv_mfma_f32_16x16x32_bf16 v[16:19], a[28:31], v[32:35], v[16:19]\nv_mfma_f32_16x16x32_bf16 v[20:23], a[44:47], v[32:35], v[20:23]\ns_waitcnt lgkmcnt(6)\n" \
"ds_read_b128 v[32:35], v2 offset:12288\nv_mfma_f32_16x16x32_bf16 v[24:27], a[28:31], v[36:39], v[24:27]\nbuffer_load_dwordx4 v0, s[36:39], s60 offen offset:2048 sc1 lds\nv_mfma_f32_16x16x32_bf16 v[28:31], a[44:47], v[36:39], v[28:31]\ns_waitcnt lgkmcnt(4)\nds_read_b128 v[36:39], v2 offset:12544\nv_mfma_f32_16x16x32_bf16 v[16:19], v[56:59], v[40:43], v[16:19]\nv_mfma_f32_16x16x32_bf16 v[20:23], a[64:67], v[40:43], v[20:23]\n" \
"s_waitcnt lgkmcnt(4)\nds_read_b128 v[40:43], v2 offset:14336\nv_mfma_f32_16x16x32_bf16 v[24:27], v[56:59], v[44:47], v[24:27]\nbuffer_load_dwordx4 v0, s[36:39], s60 offen offset:3072 sc1 lds\nv_mfma_f32_16x16x32_bf16 v[28:31], a[64:67], v[44:47], v[28:31]\ns_waitcnt lgkmcnt(4)\nds_read_b128 v[44:47], v2 offset:14592\nv_mfma_f32_16x16x32_bf16 v[16:19], v[60:63], v[64:67], v[16:19]\n" \
"s_add_u32 s56, s56, %[fs]\ns_add_u32 s62, s40, 1\ns_lshr_b32 m0, s57, 4\nv_mfma_f32_16x16x32_bf16 v[20:23], a[68:71], v[64:67], v[20:23]\ns_add_u32 m0, m0, 159744\ns_waitcnt lgkmcnt(4)\nds_read_b128 v[64:67], v2 offset:32768\nv_mfma_f32_16x16x32_bf16 v[24:27], v[60:63], v[68:71], v[24:27]\n" \
"buffer_load_dword v8, s[36:39], s56 offen sc1 lds\nv_lshrrev_b32 v14, 2, v0\nv_mfma_f32_16x16x32_bf16 v[28:31], a[68:71], v[68:71], v[28:31]\ns_waitcnt lgkmcnt(4)\nds_read_b128 v[68:71], v2 offset:33024\nv_mfma_f32_16x16x32_bf16 v[16:19], a[80:83], v[32:35], v[16:19]\nv_add_u32 v14, 159744, v14\nv_mfma_f32_16x16x32_bf16 v[20:23], a[88:91], v[32:35], v[20:23]\n" \
"s_waitcnt lgkmcnt(4)\nds_read_b128 v[32:35], v2 offset:34816\nv_mfma_f32_16x16x32_bf16 v[24:27], a[80:83], v[36:39], v[24:27]\nds_read_b128 v[56:59], v1 offset:2048\nds_read_b128 v[60:63], v1 offset:3072\nv_mfma_f32_16x16x32_bf16 v[28:31], a[88:91], v[36:39], v[28:31]\ns_waitcnt lgkmcnt(6)\nds_read_b128 v[36:39], v2 offset:35072\n" \
"v_mfma_f32_16x16x32_bf16 v[16:19], a[84:87], v[40:43], v[16:19]\nv_mfma_f32_16x16x32_bf16 v[20:23], a[92:95], v[40:43], v[20:23]\ns_waitcnt lgkmcnt(6)\nds_read_b128 v[40:43], v2 offset:36864\nv_mfma_f32_16x16x32_bf16 v[24:27], a[84:87], v[44:47], v[24:27]\nv_mfma_f32_16x16x32_bf16 v[28:31], a[92:95], v[44:47], v[28:31]\ns_waitcnt lgkmcnt(6)\nds_read_b128 v[44:47], v2 offset:37120\n" \
"v_mfma_f32_16x16x32_bf16 v[16:19], a[104:107], v[64:67], v[16:19]\nv_mfma_f32_16x16x32_bf16 v[20:23], a[120:123], v[64:67], v[20:23]\ns_waitcnt lgkmcnt(6)\nds_read_b128 v[64:67], v2 offset:38912\nv_mfma_f32_16x16x32_bf16 v[24:27], a[104:107], v[68:71], v[24:27]\ns_barrier\nv_mfma_f32_16x16x32_bf16 v[28:31], a[120:123], v[68:71], v[28:31]\ns_waitcnt lgkmcnt(6)\n" \
"s_add_u32 m0, s57, 0\nds_read_b128 v[68:71], v2 offset:39168\nv_mfma_f32_16x16x32_bf16 v[16:19], a[108:111], v[32:35], v[16:19]\ns_add_u32 s60, s41, 98304\nbuffer_load_dwordx4 v0, s[36:39], s60 offen offset:0 sc1 lds\nv_mfma_f32_16x16x32_bf16 v[20:23], a[124:127], v[32:35], v[20:23]\ns_waitcnt lgkmcnt(4)\nds_read_b128 v[32:35], v2 offset:40960\n" \
"v_mfma_f32_16x16x32_bf16 v[24:27], a[108:111], v[36:39], v[24:27]\nv_mfma_f32_16x16x32_bf16 v[28:31], a[124:127], v[36:39], v[28:31]\ns_waitcnt lgkmcnt(4)\nds_read_b128 v[36:39], v2 offset:41216\nv_mfma_f32_16x16x32_bf16 v[16:19], a[128:131], v[40:43], v[16:19]\nbuffer_load_dwordx4 v0, s[36:39], s60 offen offset:1024 sc1 lds\nv_mfma_f32_16x16x32_bf16 v[20:23], v[56:59], v[40:43], v[20:23]\ns_waitcnt lgkmcnt(4)\n" \
"ds_read_b128 v[40:43], v2 offset:43008\nv_mfma_f32_16x16x32_bf16 v[24:27], a[128:131], v[44:47], v[24:27]\nv_mfma_f32_16x16x32_bf16 v[28:31], v[56:59], v[44:47], v[28:31]\ns_waitcnt lgkmcnt(4)\nds_read_b128 v[44:47], v2 offset:43264\nv_mfma_f32_16x16x32_bf16 v[16:19], a[132:135], v[64:67], v[16:19]\nbuffer_load_dwordx4 v0, s[36:39], s60 offen offset:2048 sc1 lds\nv_mfma_f32_16x16x32_bf16 v[20:23], v[60:63], v[64:67], v[20:23]\n" \
"s_waitcnt lgkmcnt(4)\nds_read_b128 v[64:67], v2 offset:45056\nv_mfma_f32_16x16x32_bf16 v[24:27], a[132:135], v[68:71], v[24:27]\nv_mfma_f32_16x16x32_bf16 v[28:31], v[60:63], v[68:71], v[28:31]\ns_waitcnt lgkmcnt(4)\nds_read_b128 v[68:71], v2 offset:45312\nv_mfma_f32_16x16x32_bf16 v[16:19], a[152:155], v[32:35], v[16:19]\nbuffer_load_dwordx4 v0, s[36:39], s60 offen offset:3072 sc1 lds\n" \
"v_mfma_f32_16x16x32_bf16 v[20:23], a[168:171], v[32:35], v[20:23]\ns_waitcnt lgkmcnt(4)\ns_add_u32 s44, s40, 2\nds_read_b128 v[32:35], v2 offset:47104\nv_mfma_f32_16x16x32_bf16 v[24:27], a[152:155], v[36:39], v[24:27]\ns_lshl_b32 s44, s44, 17\nds_read_b128 v[56:59], v1 offset:4096\nds_read_b128 v[60:63], v1 offset:5120\n" \
"v_mfma_f32_16x16x32_bf16 v[28:31], a[168:171], v[36:39], v[28:31]\ns_waitcnt lgkmcnt(6)\nds_read_b128 v[36:39], v2 offset:47360\nv_mfma_f32_16x16x32_bf16 v[16:19], a[156:159], v[40:43], v[16:19]\ns_add_u32 s44, s44, %[xn]\ns_lshr_b32 s45, s44, 8\nv_mfma_f32_16x16x32_bf16 v[20:23], a[172:175], v[40:43], v[20:23]\ns_waitcnt lgkmcnt(6)\n" \
"v_mfma_f32_16x16x32_bf16 v[24:27], a[156:159], v[44:47], v[24:27]\ns_and_b32 s45, s45, 0x2000\ns_xor_b32 s41, s44, s45\nv_mfma_f32_16x16x32_bf16 v[28:31], a[172:175], v[44:47], v[28:31]\ns_waitcnt lgkmcnt(0)\ns_waitcnt vmcnt(4)\ns_barrier\nds_read_b128 v[40:43], v2 offset:16384\n" \
"ds_read_b128 v[44:47], v2 offset:16640\nv_mfma_f32_16x16x32_bf16 v[16:19], a[184:187], v[64:67], v[16:19]\nds_read_b32 v14, v14\nv_mfma_f32_16x16x32_bf16 v[20:23], a[192:195], v[64:67], v[20:23]\nds_read_b128 v[64:67], v2 offset:18432\nv_mfma_f32_16x16x32_bf16 v[24:27], a[184:187], v[68:71], v[24:27]\ns_add_u32 s41, s41, %[xw]\nv_mfma_f32_16x16x32_bf16 v[28:31], a[192:195], v[68:71], v[28:31]\n" \
"ds_read_b128 v[68:71], v2 offset:18688\nv_mfma_f32_16x16x32_bf16 v[16:19], a[188:191], v[32:35], v[16:19]\nv_mfma_f32_16x16x32_bf16 v[20:23], a[196:199], v[32:35], v[20:23]\nds_read_b128 v[32:35], v2 offset:20480\nv_mfma_f32_16x16x32_bf16 v[24:27], a[188:191], v[36:39], v[24:27]\ns_waitcnt lgkmcnt(3)\nv_cmp_ge_i32 s[64:65], s62, v14\nv_mfma_f32_16x16x32_bf16 v[28:31], a[196:199], v[36:39], v[28:31]\n" \
"s_cmp_eq_u64 s[64:65], 0\ns_cselect_b32 s61, s41, -2147483648\nds_read_b128 v[36:39], v2 offset:20736\nv_mfma_f32_16x16x32_bf16 v[16:19], v[56:59], v[40:43], v[16:19]\ns_waitcnt vmcnt(4)\nbuffer_store_dword v15, off, s[36:39], s54 sc1\nv_mfma_f32_16x16x32_bf16 v[20:23], a[216:219], v[40:43], v[20:23]\ns_add_u32 m0, s57, 32768\n" \
"ds_read_b128 v[40:43], v2 offset:22528\nv_mfma_f32_16x16x32_bf16 v[24:27], v[56:59], v[44:47], v[24:27]\ns_add_u32 s58, s61, 32768\nbuffer_load_dwordx4 v0, s[36:39], s58 offen offset:0 sc1 lds\nv_mfma_f32_16x16x32_bf16 v[28:31], a[216:219], v[44:47], v[28:31]\ns_waitcnt lgkmcnt(4)\nds_read_b128 v[44:47], v2 offset:22784\nv_mfma_f32_16x16x32_bf16 v[16:19], v[60:63], v[64:67], v[16:19]\n" \
"v_mfma_f32_16x16x32_bf16 v[20:23], a[220:223], v[64:67], v[20:23]\ns_waitcnt lgkmcnt(4)\nds_read_b128 v[64:67], v2 offset:24576\nv_mfma_f32_16x16x32_bf16 v[24:27], v[60:63], v[68:71], v[24:27]\nbuffer_load_dwordx4 v0, s[36:39], s58 offen offset:1024 sc1 lds\nv_mfma_f32_16x16x32_bf16 v[28:31], a[220:223], v[68:71], v[28:31]\ns_waitcnt lgkmcnt(4)\nds_read_b128 v[68:71], v2 offset:24832\n" \
"v_mfma_f32_16x16x32_bf16 v[16:19], a[224:227], v[32:35], v[16:19]\nds_read_b128 v[56:59], v1 offset:6144\nds_read_b128 v[60:63], v1 offset:7168\nv_mfma_f32_16x16x32_bf16 v[20:23], a[240:243], v[32:35], v[20:23]\ns_waitcnt lgkmcnt(6)\nds_read_b128 v[32:35], v2 offset:26624\nv_mfma_f32_16x16x32_bf16 v[24:27], a[224:227], v[36:39], v[24:27]\nbuffer_load_dwordx4 v0, s[36:39], s58 offen offset:2048 sc1 lds\n" \
"v_mfma_f32_16x16x32_bf16 v[28:31], a[240:243], v[36:39], v[28:31]\ns_waitcnt lgkmcnt(6)\nds_read_b128 v[36:39], v2 offset:26880\nv_mfma_f32_16x16x32_bf16 v[16:19], a[228:231], v[40:43], v[16:19]\nv_mfma_f32_16x16x32_bf16 v[20:23], a[244:247], v[40:43], v[20:23]\ns_waitcnt lgkmcnt(6)\nds_read_b128 v[40:43], v2 offset:28672\nv_mfma_f32_16x16x32_bf16 v[24:27], a[228:231], v[44:47], v[24:27]\n" \
"buffer_load_dwordx4 v0, s[36:39], s58 offen offset:3072 sc1 lds\nv_mfma_f32_16x16x32_bf16 v[28:31], a[244:247], v[44:47], v[28:31]\ns_waitcnt lgkmcnt(6)\nds_read_b128 v[44:47], v2 offset:28928\nv_mfma_f32_16x16x32_bf16 v[16:19], v[96:99], v[64:67], v[16:19]\nv_mfma_f32_16x16x32_bf16 v[20:23], v[112:115], v[64:67], v[20:23]\ns_waitcnt lgkmcnt(6)\nds_read_b128 v[64:67], v2 offset:30720\n" \
"v_mfma_f32_16x16x32_bf16 v[24:27], v[96:99], v[68:71], v[24:27]\nv_mfma_f32_16x16x32_bf16 v[28:31], v[112:115], v[68:71], v[28:31]\ns_waitcnt lgkmcnt(4)\nds_read_b128 v[68:71], v2 offset:30976\nv_mfma_f32_16x16x32_bf16 v[16:19], v[100:103], v[32:35], v[16:19]\nv_mfma_f32_16x16x32_bf16 v[20:23], v[116:119], v[32:35], v[20:23]\ns_waitcnt lgkmcnt(4)\nv_mfma_f32_16x16x32_bf16 v[24:27], v[100:103], v[36:39], v[24:27]\n" \
"v_mfma_f32_16x16x32_bf16 v[28:31], v[116:119], v[36:39], v[28:31]\ns_waitcnt lgkmcnt(0)\ns_waitcnt vmcnt(5)\ns_barrier\nds_read_b128 v[32:35], v2 offset:0\nds_read_b128 v[36:39], v2 offset:2048\nv_mfma_f32_16x16x32_bf16 v[16:19], v[128:131], v[40:43], v[16:19]\ns_add_u32 m0, s57, 16384\n" \
"s_nop 0\nbuffer_load_dwordx4 v3, s[36:39], s42 offen offset:0 sc1 lds\nv_mfma_f32_16x16x32_bf16 v[20:23], v[56:59], v[40:43], v[20:23]\nds_read_b128 v[40:43], v2 offset:4096\nv_mfma_f32_16x16x32_bf16 v[24:27], v[128:131], v[44:47], v[24:27]\nv_mfma_f32_16x16x32_bf16 v[28:31], v[56:59], v[44:47], v[28:31]\nds_read_b128 v[44:47], v2 offset:6144\nv_mfma_f32_16x16x32_bf16 v[16:19], v[132:135], v[64:67], v[16:19]\n" \
"buffer_load_dwordx4 v3, s[36:39], s42 offen offset:1024 sc1 lds\nv_mfma_f32_16x16x32_bf16 v[20:23], v[60:63], v[64:67], v[20:23]\nds_read_b128 v[64:67], v2 offset:8192\nv_mfma_f32_16x16x32_bf16 v[24:27], v[132:135], v[68:71], v[24:27]\nv_mfma_f32_16x16x32_bf16 v[28:31], v[60:63], v[68:71], v[28:31]\ns_waitcnt lgkmcnt(4)\nds_read_b128 v[68:71], v2 offset:10240\nv_mfma_f32_16x16x32_bf16 v[16:19], v[144:147], v[32:35], v[16:19]\n" \
"buffer_load_dwordx4 v3, s[36:39], s42 offen offset:2048 sc1 lds\nv_mfma_f32_16x16x32_bf16 v[20:23], v[160:163], v[32:35], v[20:23]\ns_waitcnt lgkmcnt(4)\nds_read_b128 v[32:35], v2 offset:12288\nv_mfma_f32_16x16x32_bf16 v[16:19], v[148:151], v[36:39], v[16:19]\nv_mfma_f32_16x16x32_bf16 v[20:23], v[164:167], v[36:39], v[20:23]\ns_waitcnt lgkmcnt(4)\nds_read_b128 v[36:39], v2 offset:14336\n" \
"v_mfma_f32_16x16x32_bf16 v[16:19], v[168:171], v[40:43], v[16:19]\nbuffer_load_dwordx4 v3, s[36:39], s42 offen offset:3072 sc1 lds\nv_add_u32 v14, s57, v0\nv_mfma_f32_16x16x32_bf16 v[20:23], v[184:187], v[40:43], v[20:23]\ns_waitcnt lgkmcnt(4)\nds_read_b128 v[40:43], v2 offset:256\nv_mfma_f32_16x16x32_bf16 v[16:19], v[172:175], v[44:47], v[16:19]\nv_mov_b64 v[48:49], 0\n" \
"v_mfma_f32_16x16x32_bf16 v[20:23], v[188:191], v[44:47], v[20:23]\ns_waitcnt lgkmcnt(4)\nds_read_b128 v[44:47], v2 offset:2304\nv_mov_b64 v[50:51], 0\nv_mfma_f32_16x16x32_bf16 v[16:19], v[200:203], v[64:67], v[16:19]\nv_mov_b64 v[52:53], 0\nv_mfma_f32_16x16x32_bf16 v[20:23], v[216:219], v[64:67], v[20:23]\ns_waitcnt lgkmcnt(4)\n" \
"ds_read_b128 v[64:67], v2 offset:4352\nv_mov_b64 v[54:55], 0\nv_mfma_f32_16x16x32_bf16 v[16:19], v[204:207], v[68:71], v[16:19]\nds_read_b128 v[56:59], v1 offset:0\nds_read_b128 v[60:63], v1 offset:1024\nv_mfma_f32_16x16x32_bf16 v[20:23], v[220:223], v[68:71], v[20:23]\ns_waitcnt lgkmcnt(6)\nds_read_b128 v[68:71], v2 offset:6400\n" \
"v_mfma_f32_16x16x32_bf16 v[16:19], v[232:235], v[32:35], v[16:19]\ns_waitcnt vmcnt(0)\nds_read_b128 v[80:83], v14 offset:16384\nds_read_b128 v[84:87], v14 offset:17408\nv_mfma_f32_16x16x32_bf16 v[20:23], v[240:243], v[32:35], v[20:23]\ns_waitcnt lgkmcnt(8)\nds_read_b128 v[32:35], v2 offset:8448\nv_mfma_f32_16x16x32_bf16 v[16:19], v[236:239], v[36:39], v[16:19]\n" \
"ds_read_b128 v[88:91], v14 offset:18432\nds_read_b128 v[92:95], v14 offset:19456\nv_mfma_f32_16x16x32_bf16 v[20:23], v[244:247], v[36:39], v[20:23]\ns_waitcnt lgkmcnt(10)\nds_read_b128 v[36:39], v2 offset:10496\nv_mfma_f32_16x16x32_bf16 v[24:27], v[144:147], v[40:43], v[24:27]\nv_mfma_f32_16x16x32_bf16 v[28:31], v[160:163], v[40:43], v[28:31]\ns_waitcnt lgkmcnt(10)\n" \
"ds_read_b128 v[40:43], v2 offset:12544\nv_cvt_pk_bf16_f32 v72, v16, v17\nv_mfma_f32_16x16x32_bf16 v[24:27], v[148:151], v[44:47], v[24:27]\nv_cvt_pk_bf16_f32 v73, v18, v19\nv_mfma_f32_16x16x32_bf16 v[28:31], v[164:167], v[44:47], v[28:31]\ns_waitcnt lgkmcnt(10)\nds_read_b128 v[44:47], v2 offset:14592\nv_cvt_pk_bf16_f32 v74, v20, v21\n" \
"v_mfma_f32_16x16x32_bf16 v[24:27], v[168:171], v[64:67], v[24:27]\ns_waitcnt lgkmcnt(6)\nv_max3_u32 v76, v80, v82, v84\nv_cvt_pk_bf16_f32 v75, v22, v23\nv_mfma_f32_16x16x32_bf16 v[28:31], v[184:187], v[64:67], v[28:31]\ns_waitcnt lgkmcnt(4)\nds_write2st64_b64 v9, v[72:73], v[74:75] offset0:0 offset1:8\nv_max3_u32 v76, v76, v86, v88\n" \
"v_mfma_f32_16x16x32_bf16 v[24:27], v[172:175], v[68:71], v[24:27]\ns_waitcnt lgkmcnt(4)\nv_max3_u32 v76, v76, v90, v92\nv_max_u32 v76, v76, v94\nv_mfma_f32_16x16x32_bf16 v[28:31], v[188:191], v[68:71], v[28:31]\nv_cmp_eq_u32 vcc, -1, v76\nv_mfma_f32_16x16x32_bf16 v[24:27], v[200:203], v[32:35], v[24:27]\ns_cbranch_vccnz .Lvu_re%=\n" \
".Lvu_rer%=:\ns_barrier\ns_memrealtime s[68:69]\nds_read_b128 v[80:83], v0 offset:16384\nv_mfma_f32_16x16x32_bf16 v[28:31], v[216:219], v[32:35], v[28:31]\ns_waitcnt lgkmcnt(4)\nds_read_b128 v[84:87], v0 offset:17408\nv_mfma_f32_16x16x32_bf16 v[24:27], v[204:207], v[36:39], v[24:27]\n" \
"ds_read_b128 v[88:91], v0 offset:18432\nv_mfma_f32_16x16x32_bf16 v[28:31], v[220:223], v[36:39], v[28:31]\ns_waitcnt lgkmcnt(5)\nds_read_b128 v[92:95], v0 offset:19456\nv_mfma_f32_16x16x32_bf16 v[24:27], v[232:235], v[40:43], v[24:27]\nds_read_b128 v[16:19], v0 offset:20480\nv_mfma_f32_16x16x32_bf16 v[28:31], v[240:243], v[40:43], v[28:31]\ns_waitcnt lgkmcnt(6)\n" \
"ds_read_b128 v[20:23], v0 offset:21504\nv_mfma_f32_16x16x32_bf16 v[24:27], v[236:239], v[44:47], v[24:27]\nds_read_b128 v[32:35], v0 offset:22528\nv_mfma_f32_16x16x32_bf16 v[28:31], v[244:247], v[44:47], v[28:31]\ns_cmp_lt_i32 s40, 0\nds_read_b128 v[36:39], v0 offset:23552\ns_cbranch_scc1 .Lvu_first%=\n.Lvu_crit%=:\n" \
"s_waitcnt lgkmcnt(7)\nv_smfmac_f32_16x16x64_bf16 v[48:51], v[80:83], a[0:7], v4\ns_waitcnt lgkmcnt(6)\nds_read_b128 v[64:67], v1 offset:4096\nds_read_b128 v[40:43], v0 offset:24576\nv_mov_b32 v14, s40\nv_smfmac_f32_16x16x64_bf16 v[48:51], v[84:87], a[24:31], v4\ns_waitcnt lgkmcnt(7)\n" \
"ds_read_b128 v[68:71], v1 offset:5120\nds_read_b128 v[44:47], v0 offset:25600\nv_cvt_pk_bf16_f32 v76, v24, v25\nv_smfmac_f32_16x16x64_bf16 v[48:51], v[88:91], v[56:63], v4\ns_sub_u32 s88, s86, s89\ns_min_u32 s88, s88, 150\ns_waitcnt lgkmcnt(8)\nds_read_b128 v[80:83], v0 offset:28672\n" \
"v_cvt_pk_bf16_f32 v77, v26, v27\nv_smfmac_f32_16x16x64_bf16 v[48:51], v[92:95], a[80:87], v4\ns_waitcnt lgkmcnt(8)\nds_read_b128 v[84:87], v0 offset:29696\nv_cvt_pk_bf16_f32 v78, v28, v29\nv_smfmac_f32_16x16x64_bf16 v[48:51], v[16:19], a[104:111], v4\ns_add_u32 s88, s88, s68\ns_mov_b32 s89, s68\n" \
"s_waitcnt lgkmcnt(8)\nds_read_b128 v[88:91], v0 offset:30720\nv_cvt_pk_bf16_f32 v79, v30, v31\nv_smfmac_f32_16x16x64_bf16 v[48:51], v[20:23], a[128:135], v4\ns_waitcnt lgkmcnt(8)\nds_read_b128 v[92:95], v0 offset:31744\nv_mov_b32 v15, s88\nv_smfmac_f32_16x16x64_bf16 v[48:51], v[32:35], a[152:159], v4\n" \
"s_waitcnt lgkmcnt(8)\nds_read_b128 v[24:27], v0 offset:26624\nbuffer_store_dwordx2 v[14:15], off, s[36:39], %[sg] sc1\nv_smfmac_f32_16x16x64_bf16 v[48:51], v[36:39], a[184:191], v4\ns_waitcnt lgkmcnt(7)\ns_waitcnt lgkmcnt(6)\nds_read_b128 v[28:31], v0 offset:27648\nv_smfmac_f32_16x16x64_bf16 v[48:51], v[40:43], v[64:71], v4\n" \
"s_waitcnt lgkmcnt(6)\nds_read_b128 v[56:59], v1 offset:2048\nv_smfmac_f32_16x16x64_bf16 v[48:51], v[44:47], a[224:231], v4\ns_waitcnt lgkmcnt(6)\nds_read_b128 v[60:63], v1 offset:3072\nv_smfmac_f32_16x16x64_bf16 v[48:51], v[80:83], v[144:151], v4\ns_waitcnt lgkmcnt(6)\nds_write2st64_b64 v9, v[76:77], v[78:79] offset0:2 offset1:10\n" \
"v_smfmac_f32_16x16x64_bf16 v[48:51], v[84:87], v[168:175], v4\ns_waitcnt lgkmcnt(6)\nv_smfmac_f32_16x16x64_bf16 v[48:51], v[88:91], v[200:207], v4\ns_waitcnt lgkmcnt(5)\nv_smfmac_f32_16x16x64_bf16 v[48:51], v[92:95], v[232:239], v4\ns_waitcnt lgkmcnt(4)\nv_smfmac_f32_16x16x64_bf16 v[48:51], v[24:27], v[96:103], v4\ns_waitcnt lgkmcnt(3)\n" \
"ds_read_b128 v[64:67], v1 offset:6144\nv_smfmac_f32_16x16x64_bf16 v[48:51], v[28:31], v[128:135], v4\nds_read_b128 v[68:71], v1 offset:7168\nv_smfmac_f32_16x16x64_bf16 v[52:55], v[80:83], v[160:167], v4\nv_smfmac_f32_16x16x64_bf16 v[52:55], v[84:87], v[184:191], v4\nv_add_f32 v48, v48, v50\nv_smfmac_f32_16x16x64_bf16 v[52:55], v[88:91], v[216:223], v4\nds_read_b128 v[80:83], v0 offset:16384\n" \
"v_add_f32 v49, v49, v51\nv_smfmac_f32_16x16x64_bf16 v[52:55], v[92:95], v[240:247], v4\nds_read_b128 v[84:87], v0 offset:17408\nv_cvt_pk_bf16_f32 v14, v48, v49\nv_smfmac_f32_16x16x64_bf16 v[52:55], v[16:19], a[120:127], v4\ns_waitcnt lgkmcnt(5)\nds_write_b16 v5, v14 offset:0\nds_write_b16_d16_hi v5, v14 offset:16\n" \
"v_smfmac_f32_16x16x64_bf16 v[52:55], v[20:23], v[56:63], v4\nds_read_b128 v[88:91], v0 offset:18432\nv_mov_b64 v[16:17], 0\nv_smfmac_f32_16x16x64_bf16 v[52:55], v[24:27], v[112:119], v4\ns_waitcnt lgkmcnt(5)\nds_read_b128 v[92:95], v0 offset:19456\nv_mov_b64 v[18:19], 0\nv_smfmac_f32_16x16x64_bf16 v[52:55], v[28:31], v[64:71], v4\n" \
"ds_read_b128 v[56:59], v1 offset:8192\nv_mov_b64 v[20:21], 0\nv_smfmac_f32_16x16x64_bf16 v[52:55], v[32:35], a[168:175], v4\ns_waitcnt lgkmcnt(3)\nds_read_b128 v[60:63], v1 offset:9216\ns_barrier\nds_read_b128 v[72:75], v11 offset:57344\nv_smfmac_f32_16x16x64_bf16 v[52:55], v[36:39], a[192:199], v4\n" \
"ds_read_b128 v[76:79], v11 offset:58368\nv_mov_b64 v[22:23], 0\nv_smfmac_f32_16x16x64_bf16 v[52:55], v[40:43], a[216:223], v4\nv_mov_b64 v[24:25], 0\nv_smfmac_f32_16x16x64_bf16 v[52:55], v[44:47], a[240:247], v4\nds_read_b128 v[64:67], v1 offset:12288\nv_mov_b64 v[26:27], 0\nv_smfmac_f32_16x16x64_bf16 v[52:55], v[80:83], a[16:23], v4\n" \
"ds_read_b128 v[68:71], v1 offset:13312\nv_mov_b64 v[28:29], 0\nv_smfmac_f32_16x16x64_bf16 v[52:55], v[84:87], a[40:47], v4\ns_waitcnt lgkmcnt(7)\nv_mov_b64 v[30:31], 0\nv_smfmac_f32_16x16x64_bf16 v[52:55], v[88:91], a[64:71], v4\ns_waitcnt lgkmcnt(6)\nv_mov_b64 v[32:33], 0\n" \
"v_smfmac_f32_16x16x64_bf16 v[52:55], v[92:95], a[88:95], v4\ns_waitcnt lgkmcnt(3)\nv_mov_b64 v[34:35], 0\nv_smfmac_f32_16x16x64_bf16 v[16:19], v[72:75], a[8:15], v10\ns_waitcnt lgkmcnt(2)\nv_mov_b64 v[36:37], 0\nv_smfmac_f32_16x16x64_bf16 v[16:19], v[76:79], v[56:63], v10\nds_read_b128 v[56:59], v1 offset:10240\n" \
"v_add_f32 v52, v52, v54\nv_smfmac_f32_16x16x64_bf16 v[20:23], v[72:75], a[56:63], v10\nds_read_b128 v[60:63], v1 offset:11264\nv_add_f32 v53, v53, v55\nv_smfmac_f32_16x16x64_bf16 v[20:23], v[76:79], a[72:79], v10\nv_cvt_pk_bf16_f32 v14, v52, v53\nv_smfmac_f32_16x16x64_bf16 v[24:27], v[72:75], a[112:119], v10\ns_waitcnt lgkmcnt(2)\n" \
"ds_write_b16 v5, v14 offset:2048\nds_write_b16_d16_hi v5, v14 offset:2064\nv_smfmac_f32_16x16x64_bf16 v[24:27], v[76:79], v[64:71], v10\nds_read_b128 v[64:67], v1 offset:14336\nv_mov_b64 v[38:39], 0\nv_smfmac_f32_16x16x64_bf16 v[28:31], v[72:75], a[160:167], v10\nds_read_b128 v[68:71], v1 offset:15360\nv_mov_b64 v[40:41], 0\n" \
"v_smfmac_f32_16x16x64_bf16 v[28:31], v[76:79], a[176:183], v10\nv_mov_b64 v[42:43], 0\nv_smfmac_f32_16x16x64_bf16 v[32:35], v[72:75], a[208:215], v10\ns_waitcnt lgkmcnt(2)\ns_barrier\ns_mov_b32 s59, 0\nds_read_b128 v[80:83], v11 offset:59392\nv_smfmac_f32_16x16x64_bf16 v[32:35], v[76:79], v[56:63], v10\n" \
"s_add_u32 m0, s57, 0\ns_add_u32 s58, s61, 0\nds_read_b128 v[56:59], v1 offset:16384\nbuffer_load_dwordx4 v0, s[36:39], s58 offen offset:0 sc1 lds\nds_read_b128 v[84:87], v11 offset:60416\nv_smfmac_f32_16x16x64_bf16 v[36:39], v[72:75], v[104:111], v10\nds_read_b128 v[60:63], v1 offset:17408\nv_mov_b64 v[44:45], 0\n" \
"v_smfmac_f32_16x16x64_bf16 v[36:39], v[76:79], v[120:127], v10\nv_mov_b64 v[46:47], 0\nv_smfmac_f32_16x16x64_bf16 v[40:43], v[72:75], v[152:159], v10\ns_waitcnt lgkmcnt(4)\nbuffer_load_dwordx4 v0, s[36:39], s58 offen offset:1024 sc1 lds\nv_smfmac_f32_16x16x64_bf16 v[40:43], v[76:79], v[64:71], v10\nds_read_b128 v[64:67], v1 offset:20480\nv_mov_b64 v[48:49], 0\n" \
"v_smfmac_f32_16x16x64_bf16 v[44:47], v[72:75], v[208:215], v10\nds_read_b128 v[68:71], v1 offset:21504\nv_mov_b64 v[50:51], 0\nv_smfmac_f32_16x16x64_bf16 v[44:47], v[76:79], v[224:231], v10\ns_waitcnt lgkmcnt(5)\nbuffer_load_dwordx4 v0, s[36:39], s58 offen offset:2048 sc1 lds\nv_smfmac_f32_16x16x64_bf16 v[16:19], v[80:83], a[32:39], v10\ns_waitcnt lgkmcnt(3)\n" \
"ds_read_b128 v[72:75], v11 offset:49152\nv_mov_b64 v[52:53], 0\nv_smfmac_f32_16x16x64_bf16 v[16:19], v[84:87], a[48:55], v10\ns_waitcnt lgkmcnt(3)\nds_read_b128 v[76:79], v11 offset:50176\nv_mov_b64 v[54:55], 0\nv_smfmac_f32_16x16x64_bf16 v[20:23], v[80:83], v[56:63], v10\nds_read_b128 v[56:59], v1 offset:18432\n" \
"buffer_load_dwordx4 v0, s[36:39], s58 offen offset:3072 sc1 lds\nv_smfmac_f32_16x16x64_bf16 v[20:23], v[84:87], a[96:103], v10\nds_read_b128 v[60:63], v1 offset:19456\nv_cvt_pk_bf16_f32 v88, v16, v17\nv_smfmac_f32_16x16x64_bf16 v[24:27], v[80:83], a[136:143], v10\nv_and_or_b32 v88, v88, -2, v12\nv_cvt_pk_bf16_f32 v89, v18, v19\nv_smfmac_f32_16x16x64_bf16 v[24:27], v[84:87], a[144:151], v10\n" \
"s_waitcnt lgkmcnt(4)\nv_cvt_pk_bf16_f32 v90, v20, v21\nv_cvt_pk_bf16_f32 v91, v22, v23\nv_and_or_b32 v90, v90, -2, v12\nv_smfmac_f32_16x16x64_bf16 v[28:31], v[80:83], v[64:71], v10\nds_read_b128 v[64:67], v1 offset:22528\nv_permlane16_swap_b32 v89, v91\nv_smfmac_f32_16x16x64_bf16 v[28:31], v[84:87], a[200:207], v10\n" \
"ds_read_b128 v[68:71], v1 offset:23552\nv_permlane16_swap_b32 v88, v90\nv_smfmac_f32_16x16x64_bf16 v[32:35], v[80:83], a[232:239], v10\ns_add_u32 s63, s59, 0\nds_read_b128 v[16:19], v11 offset:51200\nv_cvt_pk_bf16_f32 v92, v24, v25\nv_and_or_b32 v92, v92, -2, v12\nv_smfmac_f32_16x16x64_bf16 v[32:35], v[84:87], a[248:255], v10\n" \
"s_waitcnt lgkmcnt(3)\nds_read_b128 v[20:23], v11 offset:52224\nbuffer_store_dwordx4 v[88:91], v6, s[36:39], s63 offen sc1\nv_cvt_pk_bf16_f32 v93, v26, v27\nv_cvt_pk_bf16_f32 v94, v28, v29\nv_cvt_pk_bf16_f32 v95, v30, v31\nv_and_or_b32 v94, v94, -2, v12\nv_smfmac_f32_16x16x64_bf16 v[36:39], v[80:83], v[56:63], v10\n" \
"ds_read_b128 v[56:59], v1 offset:8192\nv_permlane16_swap_b32 v93, v95\nv_smfmac_f32_16x16x64_bf16 v[36:39], v[84:87], v[136:143], v10\nds_read_b128 v[60:63], v1 offset:9216\nv_permlane16_swap_b32 v92, v94\nv_smfmac_f32_16x16x64_bf16 v[40:43], v[80:83], v[176:183], v10\ns_add_u32 s63, s59, 131072\nds_read_b128 v[24:27], v11 offset:53248\n" \
"v_cvt_pk_bf16_f32 v88, v32, v33\nv_and_or_b32 v88, v88, -2, v12\nv_smfmac_f32_16x16x64_bf16 v[40:43], v[84:87], v[192:199], v10\ns_waitcnt lgkmcnt(5)\nds_read_b128 v[28:31], v11 offset:54272\nbuffer_store_dwordx4 v[92:95], v6, s[36:39], s63 offen sc1\nv_cvt_pk_bf16_f32 v89, v34, v35\nv_cvt_pk_bf16_f32 v90, v36, v37\n" \
"v_cvt_pk_bf16_f32 v91, v38, v39\nv_and_or_b32 v90, v90, -2, v12\nv_smfmac_f32_16x16x64_bf16 v[44:47], v[80:83], v[64:71], v10\nds_read_b128 v[64:67], v1 offset:16384\nv_permlane16_swap_b32 v89, v91\nv_smfmac_f32_16x16x64_bf16 v[44:47], v[84:87], v[248:255], v10\ns_cmp_ge_i32 s40, 63\nds_read_b128 v[68:71], v1 offset:17408\n" \
"v_permlane16_swap_b32 v88, v90\ns_cbranch_scc1 .Lvu_last%=\n.Lvu_y%=:\nv_smfmac_f32_16x16x64_bf16 v[48:51], v[72:75], a[8:15], v10\ns_add_u32 s63, s59, 4096\nds_read_b128 v[32:35], v11 offset:55296\nds_read_b128 v[36:39], v11 offset:56320\nv_cvt_pk_bf16_f32 v92, v40, v41\n" \
"v_and_or_b32 v92, v92, -2, v12\nv_smfmac_f32_16x16x64_bf16 v[52:55], v[76:79], a[8:15], v10\ns_waitcnt lgkmcnt(9)\ns_waitcnt lgkmcnt(6)\nbuffer_store_dwordx4 v[88:91], v6, s[36:39], s63 offen sc1\nv_cvt_pk_bf16_f32 v93, v42, v43\nv_cvt_pk_bf16_f32 v94, v44, v45\nv_cvt_pk_bf16_f32 v95, v46, v47\n" \
"v_and_or_b32 v94, v94, -2, v12\nv_smfmac_f32_16x16x64_bf16 v[48:51], v[16:19], v[56:63], v10\ns_add_u32 s40, s40, 1\ns_and_b32 s44, s40, 1\nv_permlane16_swap_b32 v93, v95\nv_smfmac_f32_16x16x64_bf16 v[52:55], v[20:23], v[56:63], v10\ns_lshl_b32 s55, s44, 22\ns_waitcnt lgkmcnt(5)\n" \
"v_permlane16_swap_b32 v92, v94\nv_smfmac_f32_16x16x64_bf16 v[48:51], v[24:27], a[32:39], v10\ns_waitcnt lgkmcnt(4)\ns_add_u32 s63, s59, 135168\nds_read_b128 v[56:59], v1 offset:12288\nv_smfmac_f32_16x16x64_bf16 v[52:55], v[28:31], a[32:39], v10\ns_waitcnt lgkmcnt(2)\nds_read_b128 v[60:63], v1 offset:13312\n" \
"buffer_store_dwordx4 v[92:95], v6, s[36:39], s63 offen sc1\nv_mov_b64 v[80:81], 0\nv_mov_b64 v[82:83], 0\nv_mov_b64 v[84:85], 0\nv_mov_b64 v[86:87], 0\nv_smfmac_f32_16x16x64_bf16 v[48:51], v[32:35], a[48:55], v10\ns_memrealtime s[86:87]\ns_bfe_u32 s45, s40, 0x10001\n" \
"s_waitcnt lgkmcnt(2)\nv_smfmac_f32_16x16x64_bf16 v[52:55], v[36:39], a[48:55], v10\nv_mov_b32 v13, s45\nv_smfmac_f32_16x16x64_bf16 v[80:83], v[72:75], a[56:63], v10\nv_cvt_pk_bf16_f32 v88, v48, v49\nv_smfmac_f32_16x16x64_bf16 v[84:87], v[76:79], a[56:63], v10\nv_cvt_pk_bf16_f32 v89, v50, v51\nv_smfmac_f32_16x16x64_bf16 v[80:83], v[16:19], a[72:79], v10\n" \
"v_cvt_pk_bf16_f32 v90, v52, v53\nv_smfmac_f32_16x16x64_bf16 v[84:87], v[20:23], a[72:79], v10\ns_add_u32 s60, s55, 0\nv_cvt_pk_bf16_f32 v91, v54, v55\nv_smfmac_f32_16x16x64_bf16 v[80:83], v[24:27], v[64:71], v10\nv_and_or_b32 v88, v88, -2, v13\nv_smfmac_f32_16x16x64_bf16 v[84:87], v[28:31], v[64:71], v10\ns_and_b32 s42, s40, 3\n" \
"s_lshl_b32 s42, s42, 15\nv_and_or_b32 v90, v90, -2, v13\nv_smfmac_f32_16x16x64_bf16 v[80:83], v[32:35], a[96:103], v10\nbuffer_store_dwordx4 v[88:91], v7, s[36:39], s60 offen offset:0 sc1\nv_mov_b64 v[48:49], 0\nv_mov_b64 v[50:51], 0\nv_mov_b64 v[52:53], 0\nv_mov_b32 v12, s44\n" \
"v_smfmac_f32_16x16x64_bf16 v[84:87], v[36:39], a[96:103], v10\nds_read_b128 v[64:67], v1 offset:20480\nv_mov_b64 v[54:55], 0\nv_smfmac_f32_16x16x64_bf16 v[48:51], v[72:75], a[112:119], v10\nds_read_b128 v[68:71], v1 offset:21504\nv_cvt_pk_bf16_f32 v92, v80, v81\nv_smfmac_f32_16x16x64_bf16 v[52:55], v[76:79], a[112:119], v10\ns_waitcnt lgkmcnt(2)\n" \
"v_cvt_pk_bf16_f32 v93, v82, v83\nv_smfmac_f32_16x16x64_bf16 v[48:51], v[16:19], v[56:63], v10\nv_cvt_pk_bf16_f32 v94, v84, v85\nv_smfmac_f32_16x16x64_bf16 v[52:55], v[20:23], v[56:63], v10\ns_add_u32 s60, s55, 4096\nv_cvt_pk_bf16_f32 v95, v86, v87\nv_smfmac_f32_16x16x64_bf16 v[48:51], v[24:27], a[136:143], v10\nds_read_b128 v[56:59], v1 offset:10240\n" \
"v_and_or_b32 v92, v92, -2, v13\nv_smfmac_f32_16x16x64_bf16 v[52:55], v[28:31], a[136:143], v10\nds_read_b128 v[60:63], v1 offset:11264\nv_and_or_b32 v94, v94, -2, v13\nv_smfmac_f32_16x16x64_bf16 v[48:51], v[32:35], a[144:151], v10\ns_waitcnt vmcnt(5)\nbuffer_store_dwordx4 v[92:95], v7, s[36:39], s60 offen offset:0 sc1\nv_mov_b64 v[80:81], 0\n" \
"v_mov_b64 v[82:83], 0\nv_mov_b64 v[84:85], 0\ns_barrier\nv_smfmac_f32_16x16x64_bf16 v[52:55], v[36:39], a[144:151], v10\nv_mov_b64 v[86:87], 0\nv_smfmac_f32_16x16x64_bf16 v[80:83], v[72:75], a[160:167], v10\nv_cvt_pk_bf16_f32 v88, v48, v49\nv_smfmac_f32_16x16x64_bf16 v[84:87], v[76:79], a[160:167], v10\n" \
"v_cvt_pk_bf16_f32 v89, v50, v51\nv_smfmac_f32_16x16x64_bf16 v[80:83], v[16:19], a[176:183], v10\ns_cmp_lt_u32 s40, 2\ns_cselect_b32 s54, %[fy], -2147483648\nv_cvt_pk_bf16_f32 v90, v52, v53\nv_smfmac_f32_16x16x64_bf16 v[84:87], v[20:23], a[176:183], v10\ns_add_u32 s60, s55, 16384\ns_waitcnt lgkmcnt(2)\n" \
"v_cvt_pk_bf16_f32 v91, v54, v55\nv_smfmac_f32_16x16x64_bf16 v[80:83], v[24:27], v[64:71], v10\nv_and_or_b32 v88, v88, -2, v13\nv_smfmac_f32_16x16x64_bf16 v[84:87], v[28:31], v[64:71], v10\nv_and_or_b32 v90, v90, -2, v13\nv_smfmac_f32_16x16x64_bf16 v[80:83], v[32:35], a[200:207], v10\nbuffer_store_dwordx4 v[88:91], v7, s[36:39], s60 offen offset:0 sc1\nv_mov_b64 v[48:49], 0\n" \
"v_mov_b64 v[50:51], 0\nv_mov_b64 v[52:53], 0\nv_smfmac_f32_16x16x64_bf16 v[84:87], v[36:39], a[200:207], v10\nds_read_b128 v[64:67], v1 offset:18432\nv_mov_b64 v[54:55], 0\nv_smfmac_f32_16x16x64_bf16 v[48:51], v[72:75], a[208:215], v10\nds_read_b128 v[68:71], v1 offset:19456\nv_cvt_pk_bf16_f32 v92, v80, v81\n" \
"v_smfmac_f32_16x16x64_bf16 v[52:55], v[76:79], a[208:215], v10\ns_waitcnt lgkmcnt(2)\nv_cvt_pk_bf16_f32 v93, v82, v83\nv_smfmac_f32_16x16x64_bf16 v[48:51], v[16:19], v[56:63], v10\nv_cvt_pk_bf16_f32 v94, v84, v85\nv_smfmac_f32_16x16x64_bf16 v[52:55], v[20:23], v[56:63], v10\ns_add_u32 s60, s55, 20480\nv_cvt_pk_bf16_f32 v95, v86, v87\n" \
"v_smfmac_f32_16x16x64_bf16 v[48:51], v[24:27], a[232:239], v10\nds_read_b128 v[56:59], v1 offset:14336\nv_and_or_b32 v92, v92, -2, v13\nv_smfmac_f32_16x16x64_bf16 v[52:55], v[28:31], a[232:239], v10\nds_read_b128 v[60:63], v1 offset:15360\nv_and_or_b32 v94, v94, -2, v13\nv_smfmac_f32_16x16x64_bf16 v[48:51], v[32:35], a[248:255], v10\nbuffer_store_dwordx4 v[92:95], v7, s[36:39], s60 offen offset:0 sc1\n" \
"v_mov_b64 v[80:81], 0\nv_mov_b64 v[82:83], 0\nv_mov_b64 v[84:85], 0\nv_smfmac_f32_16x16x64_bf16 v[52:55], v[36:39], a[248:255], v10\nv_mov_b64 v[86:87], 0\nv_smfmac_f32_16x16x64_bf16 v[80:83], v[72:75], v[104:111], v10\nv_cvt_pk_bf16_f32 v88, v48, v49\nv_smfmac_f32_16x16x64_bf16 v[84:87], v[76:79], v[104:111], v10\n" \
"v_cvt_pk_bf16_f32 v89, v50, v51\nv_smfmac_f32_16x16x64_bf16 v[80:83], v[16:19], v[120:127], v10\nv_cvt_pk_bf16_f32 v90, v52, v53\nv_smfmac_f32_16x16x64_bf16 v[84:87], v[20:23], v[120:127], v10\ns_add_u32 s60, s55, 0\ns_waitcnt lgkmcnt(2)\nv_cvt_pk_bf16_f32 v91, v54, v55\nv_smfmac_f32_16x16x64_bf16 v[80:83], v[24:27], v[64:71], v10\n" \
"v_and_or_b32 v88, v88, -2, v13\nv_smfmac_f32_16x16x64_bf16 v[84:87], v[28:31], v[64:71], v10\nv_and_or_b32 v90, v90, -2, v13\nv_smfmac_f32_16x16x64_bf16 v[80:83], v[32:35], v[136:143], v10\nbuffer_store_dwordx4 v[88:91], v7, s[36:39], s60 offen offset:2048 sc1\nv_xor_b32 v11, 128, v11\nv_mov_b64 v[48:49], 0\nv_mov_b64 v[50:51], 0\n" \
"v_mov_b64 v[52:53], 0\nv_smfmac_f32_16x16x64_bf16 v[84:87], v[36:39], v[136:143], v10\nds_read_b128 v[64:67], v1 offset:22528\nv_mov_b64 v[54:55], 0\nv_smfmac_f32_16x16x64_bf16 v[48:51], v[72:75], v[152:159], v10\nds_read_b128 v[68:71], v1 offset:23552\nv_cvt_pk_bf16_f32 v92, v80, v81\nv_smfmac_f32_16x16x64_bf16 v[52:55], v[76:79], v[152:159], v10\n" \
"s_waitcnt lgkmcnt(2)\nds_read_b128 v[40:43], v11 offset:49152\nv_cvt_pk_bf16_f32 v93, v82, v83\nv_smfmac_f32_16x16x64_bf16 v[48:51], v[16:19], v[56:63], v10\nds_read_b128 v[44:47], v11 offset:50176\nv_cvt_pk_bf16_f32 v94, v84, v85\nv_smfmac_f32_16x16x64_bf16 v[52:55], v[20:23], v[56:63], v10\ns_add_u32 s60, s55, 4096\n" \
"v_cvt_pk_bf16_f32 v95, v86, v87\nv_smfmac_f32_16x16x64_bf16 v[48:51], v[24:27], v[176:183], v10\nds_read_b128 v[56:59], v1 offset:8192\nv_and_or_b32 v92, v92, -2, v13\nv_smfmac_f32_16x16x64_bf16 v[52:55], v[28:31], v[176:183], v10\nds_read_b128 v[60:63], v1 offset:9216\nv_and_or_b32 v94, v94, -2, v13\nv_smfmac_f32_16x16x64_bf16 v[48:51], v[32:35], v[192:199], v10\n" \
"buffer_store_dwordx4 v[92:95], v7, s[36:39], s60 offen offset:2048 sc1\nv_mov_b64 v[80:81], 0\nv_mov_b64 v[82:83], 0\nv_mov_b64 v[84:85], 0\nv_smfmac_f32_16x16x64_bf16 v[52:55], v[36:39], v[192:199], v10\nv_mov_b64 v[86:87], 0\nv_smfmac_f32_16x16x64_bf16 v[80:83], v[72:75], v[208:215], v10\nds_read_b128 v[72:75], v11 offset:51200\n" \
"v_cvt_pk_bf16_f32 v88, v48, v49\nv_smfmac_f32_16x16x64_bf16 v[84:87], v[76:79], v[208:215], v10\nds_read_b128 v[76:79], v11 offset:52224\nv_cvt_pk_bf16_f32 v89, v50, v51\nv_smfmac_f32_16x16x64_bf16 v[80:83], v[16:19], v[224:231], v10\ns_add_u32 s60, s55, 16384\nds_read_b128 v[16:19], v11 offset:53248\nv_cvt_pk_bf16_f32 v90, v52, v53\n" \
"v_smfmac_f32_16x16x64_bf16 v[84:87], v[20:23], v[224:231], v10\ns_waitcnt lgkmcnt(7)\nds_read_b128 v[20:23], v11 offset:54272\nv_cvt_pk_bf16_f32 v91, v54, v55\nv_smfmac_f32_16x16x64_bf16 v[80:83], v[24:27], v[64:71], v10\nds_read_b128 v[24:27], v11 offset:55296\nv_and_or_b32 v88, v88, -2, v13\nv_smfmac_f32_16x16x64_bf16 v[84:87], v[28:31], v[64:71], v10\n" \
"ds_read_b128 v[28:31], v11 offset:56320\nv_and_or_b32 v90, v90, -2, v13\nv_smfmac_f32_16x16x64_bf16 v[80:83], v[32:35], v[248:255], v10\nbuffer_store_dwordx4 v[88:91], v7, s[36:39], s60 offen offset:2048 sc1\nv_mov_b64 v[48:49], 0\nv_mov_b64 v[50:51], 0\nv_mov_b64 v[52:53], 0\nv_smfmac_f32_16x16x64_bf16 v[84:87], v[36:39], v[248:255], v10\n" \
"s_waitcnt lgkmcnt(9)\nds_read_b128 v[64:67], v1 offset:16384\nv_mov_b64 v[54:55], 0\nv_smfmac_f32_16x16x64_bf16 v[48:51], v[40:43], a[8:15], v10\ns_waitcnt lgkmcnt(9)\nds_read_b128 v[68:71], v1 offset:17408\nv_cvt_pk_bf16_f32 v92, v80, v81\nv_smfmac_f32_16x16x64_bf16 v[52:55], v[44:47], a[8:15], v10\n" \
"s_waitcnt lgkmcnt(7)\nv_cvt_pk_bf16_f32 v93, v82, v83\nv_smfmac_f32_16x16x64_bf16 v[48:51], v[72:75], v[56:63], v10\ns_waitcnt lgkmcnt(6)\nv_cvt_pk_bf16_f32 v94, v84, v85\nv_smfmac_f32_16x16x64_bf16 v[52:55], v[76:79], v[56:63], v10\ns_add_u32 s60, s55, 20480\ns_waitcnt lgkmcnt(5)\n" \
"v_cvt_pk_bf16_f32 v95, v86, v87\nv_smfmac_f32_16x16x64_bf16 v[48:51], v[16:19], a[32:39], v10\ns_waitcnt lgkmcnt(4)\nds_read_b128 v[56:59], v1 offset:12288\nv_and_or_b32 v92, v92, -2, v13\nv_smfmac_f32_16x16x64_bf16 v[52:55], v[20:23], a[32:39], v10\ns_waitcnt lgkmcnt(4)\nds_read_b128 v[60:63], v1 offset:13312\n" \
"v_and_or_b32 v94, v94, -2, v13\nv_smfmac_f32_16x16x64_bf16 v[48:51], v[24:27], a[48:55], v10\ns_waitcnt lgkmcnt(4)\nbuffer_store_dwordx4 v[92:95], v7, s[36:39], s60 offen offset:2048 sc1\nv_xor_b32 v11, 128, v11\nv_mov_b64 v[80:81], 0\nv_mov_b64 v[82:83], 0\nv_mov_b64 v[84:85], 0\n" \
"v_smfmac_f32_16x16x64_bf16 v[52:55], v[28:31], a[48:55], v10\nv_mov_b64 v[86:87], 0\nv_smfmac_f32_16x16x64_bf16 v[80:83], v[40:43], a[56:63], v10\nv_cvt_pk_bf16_f32 v88, v48, v49\nv_smfmac_f32_16x16x64_bf16 v[84:87], v[44:47], a[56:63], v10\nv_cvt_pk_bf16_f32 v89, v50, v51\nv_smfmac_f32_16x16x64_bf16 v[80:83], v[72:75], a[72:79], v10\nv_cvt_pk_bf16_f32 v90, v52, v53\n" \
"v_smfmac_f32_16x16x64_bf16 v[84:87], v[76:79], a[72:79], v10\ns_add_u32 s60, s55, 0\ns_waitcnt lgkmcnt(2)\nv_cvt_pk_bf16_f32 v91, v54, v55\nv_smfmac_f32_16x16x64_bf16 v[80:83], v[16:19], v[64:71], v10\nv_and_or_b32 v88, v88, -2, v13\nv_smfmac_f32_16x16x64_bf16 v[84:87], v[20:23], v[64:71], v10\nv_and_or_b32 v90, v90, -2, v13\n" \
"v_smfmac_f32_16x16x64_bf16 v[80:83], v[24:27], a[96:103], v10\nbuffer_store_dwordx4 v[88:91], v7, s[36:39], s60 offen offset:512 sc1\nv_mov_b64 v[48:49], 0\nv_mov_b64 v[50:51], 0\nv_mov_b64 v[52:53], 0\nv_smfmac_f32_16x16x64_bf16 v[84:87], v[28:31], a[96:103], v10\nds_read_b128 v[64:67], v1 offset:20480\nv_mov_b64 v[54:55], 0\n" \
"v_smfmac_f32_16x16x64_bf16 v[48:51], v[40:43], a[112:119], v10\nds_read_b128 v[68:71], v1 offset:21504\nv_cvt_pk_bf16_f32 v92, v80, v81\nv_smfmac_f32_16x16x64_bf16 v[52:55], v[44:47], a[112:119], v10\ns_waitcnt lgkmcnt(2)\nv_cvt_pk_bf16_f32 v93, v82, v83\nv_smfmac_f32_16x16x64_bf16 v[48:51], v[72:75], v[56:63], v10\nv_cvt_pk_bf16_f32 v94, v84, v85\n" \
"v_smfmac_f32_16x16x64_bf16 v[52:55], v[76:79], v[56:63], v10\ns_add_u32 s60, s55, 4096\nv_cvt_pk_bf16_f32 v95, v86, v87\nv_smfmac_f32_16x16x64_bf16 v[48:51], v[16:19], a[136:143], v10\nds_read_b128 v[56:59], v1 offset:10240\nv_and_or_b32 v92, v92, -2, v13\nv_smfmac_f32_16x16x64_bf16 v[52:55], v[20:23], a[136:143], v10\nds_read_b128 v[60:63], v1 offset:11264\n" \
"v_and_or_b32 v94, v94, -2, v13\nv_smfmac_f32_16x16x64_bf16 v[48:51], v[24:27], a[144:151], v10\nbuffer_store_dwordx4 v[92:95], v7, s[36:39], s60 offen offset:512 sc1\nv_mov_b64 v[80:81], 0\nv_mov_b64 v[82:83], 0\nv_mov_b64 v[84:85], 0\nv_smfmac_f32_16x16x64_bf16 v[52:55], v[28:31], a[144:151], v10\nv_mov_b64 v[86:87], 0\n" \
"v_smfmac_f32_16x16x64_bf16 v[80:83], v[40:43], a[160:167], v10\nv_cvt_pk_bf16_f32 v88, v48, v49\nv_smfmac_f32_16x16x64_bf16 v[84:87], v[44:47], a[160:167], v10\nv_cvt_pk_bf16_f32 v89, v50, v51\nv_smfmac_f32_16x16x64_bf16 v[80:83], v[72:75], a[176:183], v10\nv_cvt_pk_bf16_f32 v90, v52, v53\nv_smfmac_f32_16x16x64_bf16 v[84:87], v[76:79], a[176:183], v10\ns_add_u32 s60, s55, 16384\n" \
"s_waitcnt lgkmcnt(2)\nv_cvt_pk_bf16_f32 v91, v54, v55\nv_smfmac_f32_16x16x64_bf16 v[80:83], v[16:19], v[64:71], v10\nv_and_or_b32 v88, v88, -2, v13\nv_smfmac_f32_16x16x64_bf16 v[84:87], v[20:23], v[64:71], v10\nv_and_or_b32 v90, v90, -2, v13\nv_smfmac_f32_16x16x64_bf16 v[80:83], v[24:27], a[200:207], v10\nbuffer_store_dwordx4 v[88:91], v7, s[36:39], s60 offen offset:512 sc1\n" \
"v_mov_b64 v[48:49], 0\nv_mov_b64 v[50:51], 0\nv_mov_b64 v[52:53], 0\nv_smfmac_f32_16x16x64_bf16 v[84:87], v[28:31], a[200:207], v10\nds_read_b128 v[64:67], v1 offset:18432\nv_mov_b64 v[54:55], 0\nv_smfmac_f32_16x16x64_bf16 v[48:51], v[40:43], a[208:215], v10\nds_read_b128 v[68:71], v1 offset:19456\n" \
"v_cvt_pk_bf16_f32 v92, v80, v81\nv_smfmac_f32_16x16x64_bf16 v[52:55], v[44:47], a[208:215], v10\ns_waitcnt lgkmcnt(2)\nv_cvt_pk_bf16_f32 v93, v82, v83\nv_smfmac_f32_16x16x64_bf16 v[48:51], v[72:75], v[56:63], v10\nv_cvt_pk_bf16_f32 v94, v84, v85\nv_smfmac_f32_16x16x64_bf16 v[52:55], v[76:79], v[56:63], v10\ns_add_u32 s60, s55, 20480\n" \
"v_cvt_pk_bf16_f32 v95, v86, v87\nv_smfmac_f32_16x16x64_bf16 v[48:51], v[16:19], a[232:239], v10\nds_read_b128 v[56:59], v1 offset:14336\nv_and_or_b32 v92, v92, -2, v13\nv_smfmac_f32_16x16x64_bf16 v[52:55], v[20:23], a[232:239], v10\nds_read_b128 v[60:63], v1 offset:15360\nv_and_or_b32 v94, v94, -2, v13\nv_smfmac_f32_16x16x64_bf16 v[48:51], v[24:27], a[248:255], v10\n" \
"buffer_store_dwordx4 v[92:95], v7, s[36:39], s60 offen offset:512 sc1\nv_mov_b64 v[80:81], 0\nv_mov_b64 v[82:83], 0\nv_mov_b64 v[84:85], 0\nv_smfmac_f32_16x16x64_bf16 v[52:55], v[28:31], a[248:255], v10\nv_mov_b64 v[86:87], 0\nv_smfmac_f32_16x16x64_bf16 v[80:83], v[40:43], v[104:111], v10\nv_cvt_pk_bf16_f32 v88, v48, v49\n" \
"v_smfmac_f32_16x16x64_bf16 v[84:87], v[44:47], v[104:111], v10\nv_cvt_pk_bf16_f32 v89, v50, v51\nv_smfmac_f32_16x16x64_bf16 v[80:83], v[72:75], v[120:127], v10\nv_cvt_pk_bf16_f32 v90, v52, v53\nv_smfmac_f32_16x16x64_bf16 v[84:87], v[76:79], v[120:127], v10\ns_add_u32 s60, s55, 0\ns_waitcnt lgkmcnt(2)\nv_cvt_pk_bf16_f32 v91, v54, v55\n" \
"v_smfmac_f32_16x16x64_bf16 v[80:83], v[16:19], v[64:71], v10\nv_and_or_b32 v88, v88, -2, v13\nv_smfmac_f32_16x16x64_bf16 v[84:87], v[20:23], v[64:71], v10\nv_and_or_b32 v90, v90, -2, v13\nv_smfmac_f32_16x16x64_bf16 v[80:83], v[24:27], v[136:143], v10\nbuffer_store_dwordx4 v[88:91], v7, s[36:39], s60 offen offset:2560 sc1\nv_mov_b64 v[48:49], 0\nv_mov_b64 v[50:51], 0\n" \
"v_mov_b64 v[52:53], 0\nv_smfmac_f32_16x16x64_bf16 v[84:87], v[28:31], v[136:143], v10\nds_read_b128 v[64:67], v1 offset:22528\nv_mov_b64 v[54:55], 0\nv_smfmac_f32_16x16x64_bf16 v[48:51], v[40:43], v[152:159], v10\nds_read_b128 v[68:71], v1 offset:23552\nv_cvt_pk_bf16_f32 v92, v80, v81\nv_smfmac_f32_16x16x64_bf16 v[52:55], v[44:47], v[152:159], v10\n" \
"s_waitcnt lgkmcnt(2)\nv_cvt_pk_bf16_f32 v93, v82, v83\nv_smfmac_f32_16x16x64_bf16 v[48:51], v[72:75], v[56:63], v10\nv_cvt_pk_bf16_f32 v94, v84, v85\nv_smfmac_f32_16x16x64_bf16 v[52:55], v[76:79], v[56:63], v10\ns_add_u32 s60, s55, 4096\nv_cvt_pk_bf16_f32 v95, v86, v87\nv_smfmac_f32_16x16x64_bf16 v[48:51], v[16:19], v[176:183], v10\n" \
"v_and_or_b32 v92, v92, -2, v13\nv_smfmac_f32_16x16x64_bf16 v[52:55], v[20:23], v[176:183], v10\nv_and_or_b32 v94, v94, -2, v13\nv_smfmac_f32_16x16x64_bf16 v[48:51], v[24:27], v[192:199], v10\nbuffer_store_dwordx4 v[92:95], v7, s[36:39], s60 offen offset:2560 sc1\nv_mov_b64 v[80:81], 0\nv_mov_b64 v[82:83], 0\nv_mov_b64 v[84:85], 0\n" \
"v_smfmac_f32_16x16x64_bf16 v[52:55], v[28:31], v[192:199], v10\nds_read_b128 v[32:35], v2 offset:0\nv_mov_b64 v[86:87], 0\nv_smfmac_f32_16x16x64_bf16 v[80:83], v[40:43], v[208:215], v10\nds_read_b128 v[36:39], v2 offset:256\nv_cvt_pk_bf16_f32 v88, v48, v49\nv_smfmac_f32_16x16x64_bf16 v[84:87], v[44:47], v[208:215], v10\nv_cvt_pk_bf16_f32 v89, v50, v51\n" \
"v_smfmac_f32_16x16x64_bf16 v[80:83], v[72:75], v[224:231], v10\ns_add_u32 s60, s55, 16384\nds_read_b128 v[40:43], v2 offset:2048\nv_cvt_pk_bf16_f32 v90, v52, v53\nv_smfmac_f32_16x16x64_bf16 v[84:87], v[76:79], v[224:231], v10\ns_waitcnt lgkmcnt(3)\nds_read_b128 v[44:47], v2 offset:2304\nv_cvt_pk_bf16_f32 v91, v54, v55\n" \
"v_smfmac_f32_16x16x64_bf16 v[80:83], v[16:19], v[64:71], v10\nv_and_or_b32 v88, v88, -2, v13\nv_smfmac_f32_16x16x64_bf16 v[84:87], v[20:23], v[64:71], v10\nv_and_or_b32 v90, v90, -2, v13\nv_smfmac_f32_16x16x64_bf16 v[80:83], v[24:27], v[248:255], v10\nbuffer_store_dwordx4 v[88:91], v7, s[36:39], s60 offen offset:2560 sc1\nv_mov_b64 v[48:49], 0\nv_mov_b64 v[50:51], 0\n" \
"v_mov_b64 v[52:53], 0\nv_mov_b32 v15, s40\nv_smfmac_f32_16x16x64_bf16 v[84:87], v[28:31], v[248:255], v10\ns_add_u32 s60, s55, 20480\ns_cmp_eq_u64 s[64:65], 0\nv_mov_b64 v[54:55], 0\ns_cbranch_scc1 .Lvu_tok%=\nv_cvt_pk_bf16_f32 v92, v80, v81\n" \
"v_cvt_pk_bf16_f32 v93, v82, v83\ns_nop 1\nv_cvt_pk_bf16_f32 v94, v84, v85\nv_cvt_pk_bf16_f32 v95, v86, v87\nv_and_or_b32 v92, v92, -2, v13\nv_and_or_b32 v94, v94, -2, v13\nbuffer_store_dwordx4 v[92:95], v7, s[36:39], s60 offen offset:2560 sc1\ns_mov_b32 s60, -2147483648\n" \
"s_cmp_lt_i32 s40, 63\ns_cbranch_scc1 .Lvu_f0%=\ns_mov_b32 s61, -2147483648\nv_add_u32 v14, s57, v0\ns_waitcnt lgkmcnt(0)\n.Lvu_nov%=:\ns_add_u32 m0, s57, 16384\ns_nop 0\n" \
"buffer_load_dwordx4 v3, s[36:39], s42 offen offset:0 sc1 lds\nbuffer_load_dwordx4 v3, s[36:39], s42 offen offset:1024 sc1 lds\nbuffer_load_dwordx4 v3, s[36:39], s42 offen offset:2048 sc1 lds\nbuffer_load_dwordx4 v3, s[36:39], s42 offen offset:3072 sc1 lds\ns_waitcnt vmcnt(0)\nds_read_b128 v[80:83], v14 offset:16384\nds_read_b128 v[84:87], v14 offset:17408\nds_read_b128 v[88:91], v14 offset:18432\n" \
"ds_read_b128 v[92:95], v14 offset:19456\ns_waitcnt lgkmcnt(2)\nv_max3_u32 v76, v80, v82, v84\ns_waitcnt lgkmcnt(1)\nv_max3_u32 v76, v76, v86, v88\ns_waitcnt lgkmcnt(0)\nv_max3_u32 v76, v76, v90, v92\nv_max_u32 v76, v76, v94\n" \
"v_cmp_eq_u32 vcc, -1, v76\ns_cbranch_vccz .Lvu_novfok%=\ns_add_u32 s43, s43, 1\ns_cmp_lt_u32 s43, 262144\ns_cbranch_scc1 .Lvu_nov%=\ns_branch .Lvu_abort%=\n.Lvu_novfok%=:\ns_waitcnt lgkmcnt(0)\n" \
"s_barrier\ns_memrealtime s[68:69]\nds_read_b128 v[56:59], v1 offset:0\nds_read_b128 v[60:63], v1 offset:1024\nds_read_b128 v[80:83], v0 offset:16384\nds_read_b128 v[84:87], v0 offset:17408\nds_read_b128 v[88:91], v0 offset:18432\nds_read_b128 v[92:95], v0 offset:19456\n" \
"ds_read_b128 v[16:19], v0 offset:20480\nds_read_b128 v[20:23], v0 offset:21504\nds_read_b128 v[32:35], v0 offset:22528\nds_read_b128 v[36:39], v0 offset:23552\ns_waitcnt lgkmcnt(0)\ns_branch .Lvu_crit%=\n.Lvu_re%=:\n.Lvu_re2%=:\n" \
"s_add_u32 m0, s57, 16384\nv_max_u32 v76, v80, v82\nv_cmp_eq_u32 vcc, -1, v76\ns_cbranch_vccz .Lvu_sk1893%=\ns_mov_b64 exec, vcc\nbuffer_load_dwordx4 v3, s[36:39], s42 offen offset:0 sc1 lds\ns_mov_b64 exec, -1\n.Lvu_sk1893%=:\n" \
"v_max_u32 v76, v84, v86\nv_cmp_eq_u32 vcc, -1, v76\ns_cbranch_vccz .Lvu_sk1900%=\ns_mov_b64 exec, vcc\nbuffer_load_dwordx4 v3, s[36:39], s42 offen offset:1024 sc1 lds\ns_mov_b64 exec, -1\n.Lvu_sk1900%=:\nv_max_u32 v76, v88, v90\n" \
"v_cmp_eq_u32 vcc, -1, v76\ns_cbranch_vccz .Lvu_sk1907%=\ns_mov_b64 exec, vcc\nbuffer_load_dwordx4 v3, s[36:39], s42 offen offset:2048 sc1 lds\ns_mov_b64 exec, -1\n.Lvu_sk1907%=:\nv_max_u32 v76, v92, v94\nv_cmp_eq_u32 vcc, -1, v76\n" \
"s_cbranch_vccz .Lvu_sk1914%=\ns_mov_b64 exec, vcc\nbuffer_load_dwordx4 v3, s[36:39], s42 offen offset:3072 sc1 lds\ns_mov_b64 exec, -1\n.Lvu_sk1914%=:\n.Lvu_skf%=:\ns_waitcnt vmcnt(0)\nds_read_b128 v[80:83], v14 offset:16384\n" \
"ds_read_b128 v[84:87], v14 offset:17408\nds_read_b128 v[88:91], v14 offset:18432\nds_read_b128 v[92:95], v14 offset:19456\ns_waitcnt lgkmcnt(2)\nv_max3_u32 v76, v80, v82, v84\ns_waitcnt lgkmcnt(1)\nv_max3_u32 v76, v76, v86, v88\ns_waitcnt lgkmcnt(0)\n" \
"v_max3_u32 v76, v76, v90, v92\nv_max_u32 v76, v76, v94\nv_cmp_eq_u32 vcc, -1, v76\ns_cbranch_vccz .Lvu_rer%=\ns_add_u32 s43, s43, 1\ns_cmp_lt_u32 s43, 262144\ns_cbranch_scc1 .Lvu_re2%=\ns_branch .Lvu_abort%=\n" \
".Lvu_rew%=:\nds_read_b128 v[80:83], v14 offset:16384\nds_read_b128 v[84:87], v14 offset:17408\nds_read_b128 v[88:91], v14 offset:18432\nds_read_b128 v[92:95], v14 offset:19456\ns_waitcnt lgkmcnt(0)\ns_branch .Lvu_rer%=\n.Lvu_first%=:\n" \
"s_nop 7\nv_cvt_pk_bf16_f32 v76, v24, v25\nv_cvt_pk_bf16_f32 v77, v26, v27\nv_cvt_pk_bf16_f32 v78, v28, v29\nv_cvt_pk_bf16_f32 v79, v30, v31\nds_write2st64_b64 v9, v[76:77], v[78:79] offset0:2 offset1:10\ns_waitcnt vmcnt(0) lgkmcnt(0)\ns_barrier\n" \
"ds_read_b128 v[56:59], v1 offset:8192\nds_read_b128 v[60:63], v1 offset:9216\nds_read_b128 v[64:67], v1 offset:16384\nds_read_b128 v[68:71], v1 offset:17408\nds_read_b128 v[72:75], v11 offset:49152\nds_read_b128 v[76:79], v11 offset:50176\nds_read_b128 v[16:19], v11 offset:51200\nds_read_b128 v[20:23], v11 offset:52224\n" \
"ds_read_b128 v[24:27], v11 offset:53248\nds_read_b128 v[28:31], v11 offset:54272\nds_read_b128 v[32:35], v11 offset:55296\nds_read_b128 v[36:39], v11 offset:56320\ns_waitcnt lgkmcnt(0)\ns_add_u32 m0, s57, 0\ns_add_u32 s58, s61, 0\nbuffer_load_dwordx4 v0, s[36:39], s58 offen offset:0 sc1 lds\n" \
"buffer_load_dwordx4 v0, s[36:39], s58 offen offset:1024 sc1 lds\nbuffer_load_dwordx4 v0, s[36:39], s58 offen offset:2048 sc1 lds\nbuffer_load_dwordx4 v0, s[36:39], s58 offen offset:3072 sc1 lds\ns_mov_b32 s59, -2147483648\nbuffer_store_dword v14, off, s[36:39], s59\nbuffer_store_dword v14, off, s[36:39], s59\nbuffer_store_dword v14, off, s[36:39], s59\ns_branch .Lvu_y%=\n" \
".Lvu_last%=:\ns_nop 7\nv_cvt_pk_bf16_f32 v92, v40, v41\nv_and_or_b32 v92, v92, -2, v12\ns_add_u32 s63, s59, 4096\nbuffer_store_dwordx4 v[88:91], v6, s[36:39], s63 offen sc1\nv_cvt_pk_bf16_f32 v93, v42, v43\nv_cvt_pk_bf16_f32 v94, v44, v45\n" \
"v_cvt_pk_bf16_f32 v95, v46, v47\nv_and_or_b32 v94, v94, -2, v12\ns_nop 0\nv_permlane16_swap_b32 v93, v95\nv_permlane16_swap_b32 v92, v94\ns_add_u32 s63, s59, 135168\nbuffer_store_dwordx4 v[92:95], v6, s[36:39], s63 offen sc1\ns_branch .Lvu_end%=\n" \
".Lvu_abort%=:\ns_mov_b64 exec, -1\nv_mov_b32 v16, 0\ns_movk_i32 s44, 6400\nbuffer_store_dword v16, off, s[36:39], s44 sc1\n.Lvu_end%=:\ns_waitcnt vmcnt(0) lgkmcnt(0)\n" \
""
#define RD_ASM \
"v_mbcnt_lo_u32_b32 v255, -1, 0\nv_mbcnt_hi_u32_b32 v255, -1, v255\nv_lshlrev_b32 v255, 2, v255\nv_add_u32 v255, %[tb], v255\nds_read_b32 v196, v255 offset:0\nds_read_b32 v197, v255 offset:256\nds_read_b32 v198, v255 offset:512\nds_read_b32 v199, v255 offset:768\n" \
"ds_read_b32 v200, v255 offset:1024\nds_read_b32 v201, v255 offset:1280\nds_read_b32 v202, v255 offset:1536\nds_read_b32 v203, v255 offset:1792\nds_read_b32 v204, v255 offset:2048\nds_read_b32 v205, v255 offset:2304\nds_read_b32 v206, v255 offset:2560\nds_read_b32 v207, v255 offset:2816\n" \
"s_waitcnt lgkmcnt(0)\nv_accvgpr_write_b32 a32, v206\nv_accvgpr_write_b32 a33, v207\ns_mov_b32 s40, 0\ns_mov_b32 s43, 0\ns_add_u32 s57, %[su], 4096\ns_add_u32 s56, %[sp], 4096\ns_mov_b32 s41, %[sy]\n" \
"s_mov_b32 s59, -2147483648\n.Lrd_yflag%=:\nv_accvgpr_read_b32 v206, a32\nbuffer_load_dword v208, v206, %[rs], 0 offen sc1\ns_waitcnt vmcnt(0)\nv_cmp_gt_i32 vcc, s40, v208\ns_cbranch_vccz .Lrd_yld%=\ns_add_u32 s43, s43, 1\n" \
"s_cmp_lt_u32 s43, 262144\ns_cbranch_scc1 .Lrd_yflag%=\ns_branch .Lrd_abort%=\n.Lrd_yld%=:\ns_add_u32 s60, s41, 0\nbuffer_load_dwordx4 v[0:3], v196, %[rs], s60 offen offset:0 sc1\ns_add_u32 s60, s41, 0\nbuffer_load_dwordx4 v[32:35], v196, %[rs], s60 offen offset:1024 sc1\n" \
"s_add_u32 s60, s41, 0\nbuffer_load_dwordx4 v[64:67], v196, %[rs], s60 offen offset:2048 sc1\ns_add_u32 s60, s41, 131072\nbuffer_load_dwordx4 v[4:7], v196, %[rs], s60 offen offset:0 sc1\ns_add_u32 s60, s41, 131072\nbuffer_load_dwordx4 v[36:39], v196, %[rs], s60 offen offset:1024 sc1\ns_add_u32 s60, s41, 131072\nbuffer_load_dwordx4 v[68:71], v196, %[rs], s60 offen offset:2048 sc1\n" \
"s_add_u32 s60, s41, 262144\nbuffer_load_dwordx4 v[8:11], v196, %[rs], s60 offen offset:0 sc1\ns_add_u32 s60, s41, 262144\nbuffer_load_dwordx4 v[40:43], v196, %[rs], s60 offen offset:1024 sc1\ns_add_u32 s60, s41, 262144\nbuffer_load_dwordx4 v[72:75], v196, %[rs], s60 offen offset:2048 sc1\ns_add_u32 s60, s41, 393216\nbuffer_load_dwordx4 v[12:15], v196, %[rs], s60 offen offset:0 sc1\n" \
"s_add_u32 s60, s41, 393216\nbuffer_load_dwordx4 v[44:47], v196, %[rs], s60 offen offset:1024 sc1\ns_add_u32 s60, s41, 393216\nbuffer_load_dwordx4 v[76:79], v196, %[rs], s60 offen offset:2048 sc1\ns_add_u32 s60, s41, 524288\nbuffer_load_dwordx4 v[16:19], v196, %[rs], s60 offen offset:0 sc1\ns_add_u32 s60, s41, 524288\nbuffer_load_dwordx4 v[48:51], v196, %[rs], s60 offen offset:1024 sc1\n" \
"s_add_u32 s60, s41, 524288\nbuffer_load_dwordx4 v[80:83], v196, %[rs], s60 offen offset:2048 sc1\ns_add_u32 s60, s41, 655360\nbuffer_load_dwordx4 v[20:23], v196, %[rs], s60 offen offset:0 sc1\ns_add_u32 s60, s41, 655360\nbuffer_load_dwordx4 v[52:55], v196, %[rs], s60 offen offset:1024 sc1\ns_add_u32 s60, s41, 655360\nbuffer_load_dwordx4 v[84:87], v196, %[rs], s60 offen offset:2048 sc1\n" \
"s_add_u32 s60, s41, 786432\nbuffer_load_dwordx4 v[24:27], v196, %[rs], s60 offen offset:0 sc1\ns_add_u32 s60, s41, 786432\nbuffer_load_dwordx4 v[56:59], v196, %[rs], s60 offen offset:1024 sc1\ns_add_u32 s60, s41, 786432\nbuffer_load_dwordx4 v[88:91], v196, %[rs], s60 offen offset:2048 sc1\ns_add_u32 s60, s41, 917504\nbuffer_load_dwordx4 v[28:31], v196, %[rs], s60 offen offset:0 sc1\n" \
"s_add_u32 s60, s41, 917504\nbuffer_load_dwordx4 v[60:63], v196, %[rs], s60 offen offset:1024 sc1\ns_add_u32 s60, s41, 917504\nbuffer_load_dwordx4 v[92:95], v196, %[rs], s60 offen offset:2048 sc1\ns_add_u32 s60, s41, 0\nbuffer_load_dwordx4 v[96:99], v196, %[rs], s60 offen offset:3072 sc1\ns_add_u32 s60, s41, 131072\nbuffer_load_dwordx4 v[100:103], v196, %[rs], s60 offen offset:3072 sc1\n" \
"s_add_u32 s60, s41, 262144\nbuffer_load_dwordx4 v[104:107], v196, %[rs], s60 offen offset:3072 sc1\ns_add_u32 s60, s41, 393216\nbuffer_load_dwordx4 v[108:111], v196, %[rs], s60 offen offset:3072 sc1\ns_add_u32 s60, s41, 524288\nbuffer_load_dwordx4 v[112:115], v196, %[rs], s60 offen offset:3072 sc1\ns_add_u32 s60, s41, 655360\nbuffer_load_dwordx4 v[116:119], v196, %[rs], s60 offen offset:3072 sc1\n" \
"s_add_u32 s60, s41, 786432\nbuffer_load_dwordx4 v[120:123], v196, %[rs], s60 offen offset:3072 sc1\ns_add_u32 s60, s41, 917504\nbuffer_load_dwordx4 v[124:127], v196, %[rs], s60 offen offset:3072 sc1\ns_mov_b64 exec, -1\nv_accvgpr_read_b32 v212, a33\nbuffer_load_dwordx2 v[214:215], v212, %[rs], 0 offen sc1\ns_waitcnt vmcnt(0)\n" \
"s_branch .Lrd_ychk%=\n.Lrd_u2%=:\nbuffer_load_dwordx4 v[136:139], v196, %[rs], %[su] offen offset:2048 sc1\nbuffer_load_dwordx4 v[140:143], v196, %[rs], %[su] offen offset:3072 sc1\ns_add_u32 s43, s43, 1\ns_cmp_ge_u32 s43, 262144\ns_cbranch_scc1 .Lrd_abort%=\ns_waitcnt vmcnt(0)\n" \
"v_bitop3_b32 v208, v136, v138, s54 bitop3:0x7e\nv_bitop3_b32 v209, v140, v142, s54 bitop3:0x7e\nv_bitop3_b32 v208, v208, 1, v209 bitop3:0xc8\nv_cmp_ne_u32 vcc, 0, v208\ns_cbranch_vccnz .Lrd_u2%=\n.Lrd_u3%=:\nbuffer_load_dwordx4 v[128:131], v196, %[rs], %[su] offen offset:0 sc1\nbuffer_load_dwordx4 v[132:135], v196, %[rs], %[su] offen offset:1024 sc1\n" \
"buffer_load_dwordx4 v[144:147], v196, %[rs], s57 offen offset:0 sc1\nbuffer_load_dwordx4 v[148:151], v196, %[rs], s57 offen offset:1024 sc1\nbuffer_load_dwordx4 v[152:155], v196, %[rs], s57 offen offset:2048 sc1\nbuffer_load_dwordx4 v[156:159], v196, %[rs], s57 offen offset:3072 sc1\nbuffer_load_dwordx4 v[160:163], v196, %[rs], %[sp] offen offset:0 sc1\nbuffer_load_dwordx4 v[164:167], v196, %[rs], %[sp] offen offset:1024 sc1\nbuffer_load_dwordx4 v[168:171], v196, %[rs], %[sp] offen offset:2048 sc1\nbuffer_load_dwordx4 v[172:175], v196, %[rs], %[sp] offen offset:3072 sc1\n" \
"buffer_load_dwordx4 v[176:179], v196, %[rs], s56 offen offset:0 sc1\n.p2align 6\n.Lrd_ra%=:\ns_waitcnt vmcnt(10)\nv_bitop3_b32 v208, v128, v130, s54 bitop3:0x7e\nv_bitop3_b32 v209, v136, v138, s54 bitop3:0x7e\nv_bitop3_b32 v208, v208, 1, v209 bitop3:0xc8\nv_cmp_ne_u32 s[86:87], 0, v208\n" \
"v_bitop3_b32 v208, v144, v146, s54 bitop3:0x7e\nv_bitop3_b32 v209, v152, v154, s54 bitop3:0x7e\nv_bitop3_b32 v208, v208, 1, v209 bitop3:0xc8\nv_cmp_ne_u32 s[88:89], 0, v208\nv_bitop3_b32 v208, v132, v134, s54 bitop3:0x7e\nv_bitop3_b32 v209, v140, v142, s54 bitop3:0x7e\nv_bitop3_b32 v208, v208, 1, v209 bitop3:0xc8\nv_cmp_ne_u32 s[68:69], 0, v208\n" \
"v_bitop3_b32 v208, v148, v150, s54 bitop3:0x7e\nv_bitop3_b32 v209, v156, v158, s54 bitop3:0x7e\nv_bitop3_b32 v208, v208, 1, v209 bitop3:0xc8\nv_cmp_ne_u32 s[62:63], 0, v208\ns_or_b64 vcc, s[86:87], s[88:89]\ns_or_b64 vcc, vcc, s[68:69]\ns_or_b64 vcc, vcc, s[62:63]\ns_waitcnt vmcnt(9)\n" \
"v_smfmac_f32_16x16x64_bf16 v[180:183], v[160:163], v[0:7], v197\nv_bitop3_b32 v208, v160, v162, s54 bitop3:0x7e\nv_smfmac_f32_16x16x64_bf16 v[184:187], v[160:163], v[32:39], v197\nv_cmp_gt_i32 s[46:47], 0, v208\nv_smfmac_f32_16x16x64_bf16 v[188:191], v[160:163], v[64:71], v197\nv_smfmac_f32_16x16x64_bf16 v[192:195], v[160:163], v[96:103], v197\ns_or_b64 vcc, vcc, s[46:47]\ns_waitcnt vmcnt(8)\n" \
"v_smfmac_f32_16x16x64_bf16 v[180:183], v[164:167], v[8:15], v197\nv_bitop3_b32 v208, v164, v166, s54 bitop3:0x7e\nv_smfmac_f32_16x16x64_bf16 v[184:187], v[164:167], v[40:47], v197\nv_cmp_gt_i32 s[48:49], 0, v208\nv_smfmac_f32_16x16x64_bf16 v[188:191], v[164:167], v[72:79], v197\nv_smfmac_f32_16x16x64_bf16 v[192:195], v[164:167], v[104:111], v197\ns_or_b64 vcc, vcc, s[48:49]\ns_waitcnt vmcnt(7)\n" \
"v_smfmac_f32_16x16x64_bf16 v[180:183], v[168:171], v[16:23], v197\nv_bitop3_b32 v208, v168, v170, s54 bitop3:0x7e\nv_smfmac_f32_16x16x64_bf16 v[184:187], v[168:171], v[48:55], v197\nv_cmp_gt_i32 s[50:51], 0, v208\nv_smfmac_f32_16x16x64_bf16 v[188:191], v[168:171], v[80:87], v197\nv_smfmac_f32_16x16x64_bf16 v[192:195], v[168:171], v[112:119], v197\ns_or_b64 vcc, vcc, s[50:51]\ns_waitcnt vmcnt(6)\n" \
"v_smfmac_f32_16x16x64_bf16 v[180:183], v[172:175], v[24:31], v197\nv_bitop3_b32 v208, v172, v174, s54 bitop3:0x7e\nv_smfmac_f32_16x16x64_bf16 v[184:187], v[172:175], v[56:63], v197\nv_cmp_gt_i32 s[52:53], 0, v208\nv_smfmac_f32_16x16x64_bf16 v[188:191], v[172:175], v[88:95], v197\nv_smfmac_f32_16x16x64_bf16 v[192:195], v[172:175], v[120:127], v197\ns_or_b64 vcc, vcc, s[52:53]\ns_waitcnt vmcnt(5)\n" \
"v_smfmac_f32_16x16x64_bf16 v[180:183], v[176:179], v[128:135], v197\nv_bitop3_b32 v208, v176, v178, s54 bitop3:0x7e\nv_smfmac_f32_16x16x64_bf16 v[184:187], v[176:179], v[136:143], v197\nv_cmp_gt_i32 s[64:65], 0, v208\nv_smfmac_f32_16x16x64_bf16 v[188:191], v[176:179], v[144:151], v197\ns_or_b64 vcc, vcc, s[64:65]\ns_cbranch_scc1 .Lrd_rb%=\nv_smfmac_f32_16x16x64_bf16 v[192:195], v[176:179], v[152:159], v197\n" \
".Lrd_rd0%=:\nv_add_f32 v180, v180, v182\nv_add_f32 v181, v181, v183\nds_write2_b32 v198, v180, v181 offset0:0 offset1:8\nv_add_f32 v184, v184, v186\nv_add_f32 v185, v185, v187\nv_add_f32 v188, v188, v190\nv_add_f32 v189, v189, v191\n" \
"ds_write2_b32 v198, v184, v185 offset0:32 offset1:40\nv_add_f32 v192, v192, v194\nv_add_f32 v193, v193, v195\nds_write2_b32 v198, v188, v189 offset0:64 offset1:72\nds_write2_b32 v198, v192, v193 offset0:96 offset1:104\ns_add_u32 s60, s58, 0\nbuffer_load_dwordx4 v[232:235], v196, %[rs], s60 offen offset:3072 sc1\ns_add_u32 s60, s58, 16384\n" \
"buffer_load_dwordx4 v[236:239], v196, %[rs], s60 offen offset:2048 sc1\ns_waitcnt lgkmcnt(0)\ns_barrier\nds_read_b128 v[180:183], v199 offset:0\nds_read_b128 v[184:187], v199 offset:4096\nds_read_b128 v[188:191], v199 offset:8192\nds_read_b128 v[192:195], v199 offset:12288\ns_and_b32 s44, s54, 0x80008000\n" \
"s_add_u32 s60, s58, 4096\nbuffer_load_dwordx4 v[240:243], v196, %[rs], s60 offen offset:0 sc1\ns_add_u32 s60, s58, 4096\nbuffer_load_dwordx4 v[244:247], v196, %[rs], s60 offen offset:1024 sc1\ns_waitcnt lgkmcnt(2)\nv_pk_add_f32 v[182:183], v[182:183], v[186:187]\nv_pk_add_f32 v[180:181], v[180:181], v[184:185]\ns_waitcnt lgkmcnt(0)\n" \
"v_pk_add_f32 v[186:187], v[188:189], v[192:193]\nv_pk_add_f32 v[184:185], v[190:191], v[194:195]\nv_pk_add_f32 v[180:181], v[180:181], v[186:187]\nv_pk_add_f32 v[182:183], v[182:183], v[184:185]\nv_cvt_pk_bf16_f32 v180, v180, v181\nv_xor_b32 v180, s44, v180\nv_cvt_pk_bf16_f32 v181, v182, v183\nv_min_u32 v180, -2, v180\n" \
"v_xor_b32 v209, s44, v181\nv_or_b32 v208, v180, v201\nv_mov_b32 v211, v209\nv_or_b32 v210, v180, v201\ns_nop 0\nv_permlane32_swap_b32 v209, v211\nv_permlane32_swap_b32 v208, v210\nbuffer_store_dwordx4 v[208:211], v206, %[rs], 0 offen sc1\n" \
"s_add_u32 s60, s58, 16384\nbuffer_load_dwordx4 v[248:251], v196, %[rs], s60 offen offset:3072 sc1\ns_add_u32 s60, s58, 20480\nbuffer_load_dwordx4 v[252:255], v196, %[rs], s60 offen offset:0 sc1\ns_add_u32 s60, s58, 20480\nbuffer_load_dwordx4 v[160:163], v196, %[rs], s60 offen offset:2048 sc1\ns_xor_b32 s41, s41, 4194304\nv_mov_b32 v207, s40\n" \
"s_waitcnt vmcnt(12)\nv_bitop3_b32 v209, v212, v214, s61 bitop3:0x7e\nv_cmp_gt_i32 vcc, 0, v209\ns_cbranch_vccnz .Lrd_tre0%=\n.Lrd_tl0%=:\nv_mfma_f32_16x16x16_bf16 a[0:3], v[0:1], v[212:213], 0\ns_add_u32 s60, s58, 4096\nbuffer_load_dwordx4 v[164:167], v196, %[rs], s60 offen offset:2048 sc1\n" \
"v_mfma_f32_16x16x16_bf16 a[8:11], v[32:33], v[212:213], 0\nv_mfma_f32_16x16x16_bf16 a[16:19], v[64:65], v[212:213], 0\nv_mfma_f32_16x16x16_bf16 a[24:27], v[96:97], v[212:213], 0\ns_waitcnt vmcnt(12)\nv_mfma_f32_16x16x16_bf16 a[0:3], v[4:5], v[214:215], a[0:3]\ns_add_u32 s60, s58, 4096\nbuffer_load_dwordx4 v[168:171], v196, %[rs], s60 offen offset:3072 sc1\nv_bitop3_b32 v209, v216, v218, s61 bitop3:0x7e\n" \
"v_mfma_f32_16x16x16_bf16 a[8:11], v[36:37], v[214:215], a[8:11]\ns_lshl_b32 s42, s40, 17\ns_add_u32 s42, s42, %[at]\nv_mfma_f32_16x16x16_bf16 a[16:19], v[68:69], v[214:215], a[16:19]\nv_cmp_gt_i32 vcc, 0, v209\nv_mfma_f32_16x16x16_bf16 a[24:27], v[100:101], v[214:215], a[24:27]\ns_cbranch_vccnz .Lrd_tre1%=\n.Lrd_tl1%=:\n" \
"s_waitcnt vmcnt(12)\nv_mfma_f32_16x16x32_bf16 a[4:7], v[0:3], v[216:219], 0\ns_add_u32 s60, s58, 20480\nbuffer_load_dwordx4 v[172:175], v196, %[rs], s60 offen offset:1024 sc1\nv_bitop3_b32 v209, v220, v222, s61 bitop3:0x7e\nv_mfma_f32_16x16x32_bf16 a[12:15], v[32:35], v[216:219], 0\ns_lshr_b32 s45, s42, 8\ns_and_b32 s45, s45, 0x2000\n" \
"v_mfma_f32_16x16x32_bf16 a[20:23], v[64:67], v[216:219], 0\nv_cmp_gt_i32 vcc, 0, v209\nv_mfma_f32_16x16x32_bf16 a[28:31], v[96:99], v[216:219], 0\ns_cbranch_vccnz .Lrd_tre2%=\n.Lrd_tl2%=:\ns_waitcnt vmcnt(12)\nv_mfma_f32_16x16x32_bf16 a[4:7], v[4:7], v[220:223], a[4:7]\ns_add_u32 s60, s58, 20480\n" \
"buffer_load_dwordx4 v[176:179], v196, %[rs], s60 offen offset:3072 sc1\nv_bitop3_b32 v209, v224, v226, s61 bitop3:0x7e\nv_mfma_f32_16x16x32_bf16 a[12:15], v[36:39], v[220:223], a[12:15]\ns_xor_b32 s42, s42, s45\ns_add_u32 s40, s40, 1\nv_mfma_f32_16x16x32_bf16 a[20:23], v[68:71], v[220:223], a[20:23]\nv_cmp_gt_i32 vcc, 0, v209\nv_mfma_f32_16x16x32_bf16 a[28:31], v[100:103], v[220:223], a[28:31]\n" \
"s_cbranch_vccnz .Lrd_tre3%=\n.Lrd_tl3%=:\nv_mfma_f32_16x16x16_bf16 a[0:3], v[8:9], v[224:225], a[0:3]\ns_cmp_eq_u32 s40, 1\ns_cbranch_scc1 .Lrd_y1%=\n.Lrd_y1r%=:\ns_add_u32 s60, s41, 0\nbuffer_load_dwordx4 v[0:3], v196, %[rs], s60 offen offset:0 sc1\n" \
"v_mfma_f32_16x16x16_bf16 a[8:11], v[40:41], v[224:225], a[8:11]\nv_mfma_f32_16x16x16_bf16 a[16:19], v[72:73], v[224:225], a[16:19]\nbuffer_load_dwordx4 v[212:215], v204, %[rs], s42 offen sc1\nv_mfma_f32_16x16x16_bf16 a[24:27], v[104:105], v[224:225], a[24:27]\ns_waitcnt vmcnt(14)\nv_mfma_f32_16x16x16_bf16 a[0:3], v[12:13], v[226:227], a[0:3]\ns_add_u32 s60, s41, 0\nbuffer_load_dwordx4 v[32:35], v196, %[rs], s60 offen offset:1024 sc1\n" \
"v_bitop3_b32 v209, v228, v230, s61 bitop3:0x7e\nv_mfma_f32_16x16x16_bf16 a[8:11], v[44:45], v[226:227], a[8:11]\ns_lshl_b32 s62, s55, 31\ns_or_b32 s62, s62, 1.0\ns_mov_b32 s63, s62\nv_mfma_f32_16x16x16_bf16 a[16:19], v[76:77], v[226:227], a[16:19]\nv_cmp_gt_i32 vcc, 0, v209\nv_mfma_f32_16x16x16_bf16 a[24:27], v[108:109], v[226:227], a[24:27]\n" \
"s_cbranch_vccnz .Lrd_tre4%=\n.Lrd_tl4%=:\ns_waitcnt vmcnt(14)\nv_mfma_f32_16x16x32_bf16 a[4:7], v[8:11], v[228:231], a[4:7]\ns_add_u32 s60, s41, 0\nbuffer_load_dwordx4 v[64:67], v196, %[rs], s60 offen offset:2048 sc1\nv_bitop3_b32 v209, v232, v234, s61 bitop3:0x7e\nv_mfma_f32_16x16x32_bf16 a[12:15], v[40:43], v[228:231], a[12:15]\n" \
"v_mfma_f32_16x16x32_bf16 a[20:23], v[72:75], v[228:231], a[20:23]\nv_cmp_gt_i32 vcc, 0, v209\nv_mfma_f32_16x16x32_bf16 a[28:31], v[104:107], v[228:231], a[28:31]\ns_cbranch_vccnz .Lrd_tre5%=\n.Lrd_tl5%=:\ns_waitcnt vmcnt(14)\nv_mfma_f32_16x16x32_bf16 a[4:7], v[12:15], v[232:235], a[4:7]\ns_add_u32 s60, s41, 131072\n" \
"buffer_load_dwordx4 v[4:7], v196, %[rs], s60 offen offset:0 sc1\nv_bitop3_b32 v209, v236, v238, s61 bitop3:0x7e\nv_mfma_f32_16x16x32_bf16 a[12:15], v[44:47], v[232:235], a[12:15]\nv_mfma_f32_16x16x32_bf16 a[20:23], v[76:79], v[232:235], a[20:23]\nv_cmp_gt_i32 vcc, 0, v209\nv_mfma_f32_16x16x32_bf16 a[28:31], v[108:111], v[232:235], a[28:31]\ns_cbranch_vccnz .Lrd_tre6%=\n.Lrd_tl6%=:\n" \
"v_mfma_f32_16x16x16_bf16 a[0:3], v[16:17], v[236:237], a[0:3]\ns_add_u32 s60, s41, 131072\nbuffer_load_dwordx4 v[36:39], v196, %[rs], s60 offen offset:1024 sc1\nv_mfma_f32_16x16x16_bf16 a[8:11], v[48:49], v[236:237], a[8:11]\nv_mfma_f32_16x16x16_bf16 a[16:19], v[80:81], v[236:237], a[16:19]\nv_mfma_f32_16x16x16_bf16 a[24:27], v[112:113], v[236:237], a[24:27]\ns_waitcnt vmcnt(15)\nv_mfma_f32_16x16x16_bf16 a[0:3], v[20:21], v[238:239], a[0:3]\n" \
"s_add_u32 s60, s41, 131072\nbuffer_load_dwordx4 v[68:71], v196, %[rs], s60 offen offset:2048 sc1\nv_bitop3_b32 v209, v240, v242, s61 bitop3:0x7e\nv_mfma_f32_16x16x16_bf16 a[8:11], v[52:53], v[238:239], a[8:11]\nv_mfma_f32_16x16x16_bf16 a[16:19], v[84:85], v[238:239], a[16:19]\nv_cmp_gt_i32 vcc, 0, v209\nv_mfma_f32_16x16x16_bf16 a[24:27], v[116:117], v[238:239], a[24:27]\ns_cbranch_vccnz .Lrd_tre7%=\n" \
".Lrd_tl7%=:\ns_waitcnt vmcnt(15)\nv_mfma_f32_16x16x32_bf16 a[4:7], v[16:19], v[240:243], a[4:7]\ns_add_u32 s60, s41, 262144\nbuffer_load_dwordx4 v[8:11], v196, %[rs], s60 offen offset:0 sc1\nv_bitop3_b32 v209, v244, v246, s61 bitop3:0x7e\nv_mfma_f32_16x16x32_bf16 a[12:15], v[48:51], v[240:243], a[12:15]\nv_mfma_f32_16x16x32_bf16 a[20:23], v[80:83], v[240:243], a[20:23]\n" \
"v_cmp_gt_i32 vcc, 0, v209\nv_mfma_f32_16x16x32_bf16 a[28:31], v[112:115], v[240:243], a[28:31]\ns_cbranch_vccnz .Lrd_tre8%=\n.Lrd_tl8%=:\ns_waitcnt vmcnt(14)\nv_mfma_f32_16x16x32_bf16 a[4:7], v[20:23], v[244:247], a[4:7]\ns_add_u32 s60, s41, 262144\nbuffer_load_dwordx4 v[40:43], v196, %[rs], s60 offen offset:1024 sc1\n" \
"v_bitop3_b32 v209, v248, v250, s61 bitop3:0x7e\nv_mfma_f32_16x16x32_bf16 a[12:15], v[52:55], v[244:247], a[12:15]\nv_mfma_f32_16x16x32_bf16 a[20:23], v[84:87], v[244:247], a[20:23]\nv_cmp_gt_i32 vcc, 0, v209\nv_mfma_f32_16x16x32_bf16 a[28:31], v[116:119], v[244:247], a[28:31]\ns_cbranch_vccnz .Lrd_tre9%=\n.Lrd_tl9%=:\nv_mfma_f32_16x16x16_bf16 a[0:3], v[24:25], v[248:249], a[0:3]\n" \
"s_add_u32 s60, s41, 262144\nbuffer_load_dwordx4 v[72:75], v196, %[rs], s60 offen offset:2048 sc1\nv_mfma_f32_16x16x16_bf16 a[8:11], v[56:57], v[248:249], a[8:11]\nv_mfma_f32_16x16x16_bf16 a[16:19], v[88:89], v[248:249], a[16:19]\nv_mfma_f32_16x16x16_bf16 a[24:27], v[120:121], v[248:249], a[24:27]\ns_waitcnt vmcnt(15)\nv_mfma_f32_16x16x16_bf16 a[0:3], v[28:29], v[250:251], a[0:3]\ns_add_u32 s60, s41, 393216\n" \
"buffer_load_dwordx4 v[12:15], v196, %[rs], s60 offen offset:0 sc1\nv_bitop3_b32 v209, v252, v254, s61 bitop3:0x7e\nv_mfma_f32_16x16x16_bf16 a[8:11], v[60:61], v[250:251], a[8:11]\nv_mfma_f32_16x16x16_bf16 a[16:19], v[92:93], v[250:251], a[16:19]\nv_cmp_gt_i32 vcc, 0, v209\nv_mfma_f32_16x16x16_bf16 a[24:27], v[124:125], v[250:251], a[24:27]\ns_cbranch_vccnz .Lrd_tre10%=\n.Lrd_tl10%=:\n" \
"s_waitcnt vmcnt(15)\nv_mfma_f32_16x16x32_bf16 a[0:3], v[128:131], v[252:255], a[0:3]\ns_add_u32 s60, s41, 393216\nbuffer_load_dwordx4 v[44:47], v196, %[rs], s60 offen offset:1024 sc1\nv_bitop3_b32 v209, v160, v162, s61 bitop3:0x7e\nv_mfma_f32_16x16x32_bf16 a[8:11], v[136:139], v[252:255], a[8:11]\nv_mfma_f32_16x16x32_bf16 a[16:19], v[144:147], v[252:255], a[16:19]\nv_cmp_gt_i32 vcc, 0, v209\n" \
"v_mfma_f32_16x16x32_bf16 a[24:27], v[152:155], v[252:255], a[24:27]\ns_cbranch_vccnz .Lrd_tre11%=\n.Lrd_tl11%=:\ns_waitcnt vmcnt(15)\nv_mfma_f32_16x16x32_bf16 a[0:3], v[132:135], v[160:163], a[0:3]\ns_add_u32 s60, s41, 393216\nbuffer_load_dwordx4 v[76:79], v196, %[rs], s60 offen offset:2048 sc1\nv_bitop3_b32 v209, v164, v166, s61 bitop3:0x7e\n" \
"v_mfma_f32_16x16x32_bf16 a[8:11], v[140:143], v[160:163], a[8:11]\nv_mfma_f32_16x16x32_bf16 a[16:19], v[148:151], v[160:163], a[16:19]\nv_cmp_gt_i32 vcc, 0, v209\nv_mfma_f32_16x16x32_bf16 a[24:27], v[156:159], v[160:163], a[24:27]\ns_cbranch_vccnz .Lrd_tre12%=\n.Lrd_tl12%=:\ns_waitcnt vmcnt(15)\nv_mfma_f32_16x16x32_bf16 a[4:7], v[24:27], v[164:167], a[4:7]\n" \
"s_add_u32 s60, s41, 524288\nbuffer_load_dwordx4 v[16:19], v196, %[rs], s60 offen offset:0 sc1\nv_bitop3_b32 v209, v168, v170, s61 bitop3:0x7e\nv_mfma_f32_16x16x32_bf16 a[12:15], v[56:59], v[164:167], a[12:15]\nv_mfma_f32_16x16x32_bf16 a[20:23], v[88:91], v[164:167], a[20:23]\nds_write_b128 v202, a[0:3] offset:0\nv_cmp_gt_i32 vcc, 0, v209\nv_mfma_f32_16x16x32_bf16 a[28:31], v[120:123], v[164:167], a[28:31]\n" \
"s_cbranch_vccnz .Lrd_tre13%=\n.Lrd_tl13%=:\ns_waitcnt vmcnt(15)\nv_mfma_f32_16x16x32_bf16 a[4:7], v[28:31], v[168:171], a[4:7]\ns_add_u32 s60, s41, 524288\nbuffer_load_dwordx4 v[48:51], v196, %[rs], s60 offen offset:1024 sc1\nv_bitop3_b32 v209, v172, v174, s61 bitop3:0x7e\nv_mfma_f32_16x16x32_bf16 a[12:15], v[60:63], v[168:171], a[12:15]\n" \
"v_mfma_f32_16x16x32_bf16 a[20:23], v[92:95], v[168:171], a[20:23]\nds_write_b128 v202, a[8:11] offset:2048\nv_cmp_gt_i32 vcc, 0, v209\nv_mfma_f32_16x16x32_bf16 a[28:31], v[124:127], v[168:171], a[28:31]\ns_cbranch_vccnz .Lrd_tre14%=\n.Lrd_tl14%=:\ns_waitcnt vmcnt(15)\nv_mfma_f32_16x16x32_bf16 a[4:7], v[128:131], v[172:175], a[4:7]\n" \
"s_add_u32 s60, s41, 524288\nbuffer_load_dwordx4 v[80:83], v196, %[rs], s60 offen offset:2048 sc1\nv_bitop3_b32 v209, v176, v178, s61 bitop3:0x7e\nv_mfma_f32_16x16x32_bf16 a[12:15], v[136:139], v[172:175], a[12:15]\nv_mfma_f32_16x16x32_bf16 a[20:23], v[144:147], v[172:175], a[20:23]\nds_write_b128 v202, a[16:19] offset:4096\nv_cmp_gt_i32 vcc, 0, v209\nv_mfma_f32_16x16x32_bf16 a[28:31], v[152:155], v[172:175], a[28:31]\n" \
"s_cbranch_vccnz .Lrd_tre15%=\n.Lrd_tl15%=:\nv_mfma_f32_16x16x32_bf16 a[4:7], v[132:135], v[176:179], a[4:7]\ns_add_u32 s60, s41, 655360\ns_waitcnt vmcnt(14)\nbuffer_load_dwordx4 v[20:23], v196, %[rs], s60 offen offset:0 sc1\nv_lshlrev_b32 v216, 16, v212\nv_and_b32 v217, 0xffff0000, v212\n" \
"v_mfma_f32_16x16x32_bf16 a[12:15], v[140:143], v[176:179], a[12:15]\nv_lshlrev_b32 v218, 16, v213\nv_and_b32 v219, 0xffff0000, v213\nv_mfma_f32_16x16x32_bf16 a[20:23], v[148:151], v[176:179], a[20:23]\nds_write_b128 v202, a[24:27] offset:6144\nv_lshlrev_b32 v220, 16, v214\nv_and_b32 v221, 0xffff0000, v214\nv_mfma_f32_16x16x32_bf16 a[28:31], v[156:159], v[176:179], a[28:31]\n" \
"s_add_u32 s60, s41, 655360\nbuffer_load_dwordx4 v[52:55], v196, %[rs], s60 offen offset:1024 sc1\ns_add_u32 s60, s41, 655360\nds_write_b128 v202, a[4:7] offset:1024\nbuffer_load_dwordx4 v[84:87], v196, %[rs], s60 offen offset:2048 sc1\nds_write_b128 v202, a[12:15] offset:3072\nv_lshlrev_b32 v222, 16, v215\nv_and_b32 v223, 0xffff0000, v215\n" \
"s_add_u32 s60, s41, 786432\nbuffer_load_dwordx4 v[24:27], v196, %[rs], s60 offen offset:0 sc1\nds_write_b128 v202, a[20:23] offset:5120\ns_add_u32 s60, s41, 786432\nbuffer_load_dwordx4 v[56:59], v196, %[rs], s60 offen offset:1024 sc1\nds_write_b128 v202, a[28:31] offset:7168\ns_waitcnt lgkmcnt(0)\ns_barrier\n" \
"ds_read_b128 v[224:227], v203 offset:0\nds_read_b128 v[228:231], v203 offset:256\nds_read_b128 v[232:235], v203 offset:8192\nds_read_b128 v[236:239], v203 offset:8448\nds_read_b128 v[240:243], v203 offset:16384\nds_read_b128 v[244:247], v203 offset:16640\nds_read_b128 v[248:251], v203 offset:24576\nds_read_b128 v[252:255], v203 offset:24832\n" \
"s_add_u32 s60, s41, 786432\nbuffer_load_dwordx4 v[88:91], v196, %[rs], s60 offen offset:2048 sc1\ns_waitcnt lgkmcnt(4)\nv_pk_add_f32 v[224:225], v[224:225], v[232:233]\nv_pk_add_f32 v[226:227], v[226:227], v[234:235]\nv_pk_add_f32 v[228:229], v[228:229], v[236:237]\nv_pk_add_f32 v[230:231], v[230:231], v[238:239]\ns_add_u32 s60, s41, 917504\n" \
"buffer_load_dwordx4 v[28:31], v196, %[rs], s60 offen offset:0 sc1\ns_waitcnt lgkmcnt(2)\nv_pk_add_f32 v[224:225], v[224:225], v[240:241]\nv_pk_add_f32 v[226:227], v[226:227], v[242:243]\nv_pk_add_f32 v[228:229], v[228:229], v[244:245]\nv_pk_add_f32 v[230:231], v[230:231], v[246:247]\ns_add_u32 s60, s41, 917504\nbuffer_load_dwordx4 v[60:63], v196, %[rs], s60 offen offset:1024 sc1\n" \
"s_waitcnt lgkmcnt(0)\nv_pk_add_f32 v[224:225], v[224:225], v[248:249]\nv_pk_add_f32 v[226:227], v[226:227], v[250:251]\nv_pk_add_f32 v[228:229], v[228:229], v[252:253]\nv_pk_add_f32 v[230:231], v[230:231], v[254:255]\nv_pk_fma_f32 v[224:225], v[224:225], s[62:63], v[216:217]\nv_pk_fma_f32 v[226:227], v[226:227], s[62:63], v[218:219]\nv_pk_fma_f32 v[228:229], v[228:229], s[62:63], v[220:221]\n" \
"v_pk_fma_f32 v[230:231], v[230:231], s[62:63], v[222:223]\nv_cvt_pk_bf16_f32 v208, v224, v225\nv_cvt_pk_bf16_f32 v209, v226, v227\nv_cvt_pk_bf16_f32 v210, v228, v229\nv_cvt_pk_bf16_f32 v211, v230, v231\nbuffer_store_dwordx4 v[208:211], v204, %[rs], s42 offen sc1\nv_pk_add_f32 v[232:233], v[224:225], v[226:227]\nv_pk_add_f32 v[234:235], v[228:229], v[230:231]\n" \
"v_pk_add_f32 v[232:233], v[232:233], v[234:235]\ns_add_u32 s60, s41, 917504\nbuffer_load_dwordx4 v[92:95], v196, %[rs], s60 offen offset:2048 sc1\ns_add_u32 s60, s41, 0\nbuffer_load_dwordx4 v[96:99], v196, %[rs], s60 offen offset:3072 sc1\nv_add_f32 v232, v232, v233\ns_nop 1\nv_add_f32_dpp v233, v232, v232 row_ror:8 row_mask:0xf bank_mask:0xf\n" \
"v_add_f32_dpp v234, v232, v232 row_ror:8 row_mask:0xf bank_mask:0xf\ns_nop 1\nv_permlane16_swap_b32 v233, v234\nv_add_f32 v232, v233, v234\nv_add_f32 v233, v233, v234\ns_nop 1\nv_permlane32_swap_b32 v232, v233\nv_add_f32 v232, v232, v233\n" \
"v_mul_f32 v232, 0x3c800000, v232\nv_pk_add_f32 v[224:225], v[224:225], v[232:233] op_sel_hi:[1,0] neg_lo:[0,1] neg_hi:[0,1]\nv_pk_add_f32 v[226:227], v[226:227], v[232:233] op_sel_hi:[1,0] neg_lo:[0,1] neg_hi:[0,1]\nv_pk_add_f32 v[228:229], v[228:229], v[232:233] op_sel_hi:[1,0] neg_lo:[0,1] neg_hi:[0,1]\nv_pk_add_f32 v[230:231], v[230:231], v[232:233] op_sel_hi:[1,0] neg_lo:[0,1] neg_hi:[0,1]\ns_add_u32 s60, s41, 131072\nbuffer_load_dwordx4 v[100:103], v196, %[rs], s60 offen offset:3072 sc1\nv_accvgpr_read_b32 v212, a33\n" \
"buffer_load_dwordx2 v[214:215], v212, %[rs], 0 offen sc1\nv_pk_mul_f32 v[236:237], v[224:225], v[224:225]\nv_pk_mul_f32 v[238:239], v[226:227], v[226:227]\nv_pk_fma_f32 v[236:237], v[228:229], v[228:229], v[236:237]\nv_pk_fma_f32 v[238:239], v[230:231], v[230:231], v[238:239]\nv_pk_add_f32 v[236:237], v[236:237], v[238:239]\nv_add_f32 v236, v236, v237\ns_nop 1\n" \
"v_add_f32_dpp v237, v236, v236 row_ror:8 row_mask:0xf bank_mask:0xf\nv_add_f32_dpp v238, v236, v236 row_ror:8 row_mask:0xf bank_mask:0xf\ns_nop 1\nv_permlane16_swap_b32 v237, v238\nv_add_f32 v236, v237, v238\nv_add_f32 v237, v237, v238\ns_nop 1\nv_permlane32_swap_b32 v236, v237\n" \
"v_add_f32 v236, v236, v237\nv_mov_b32 v233, v236\nbuffer_store_dwordx2 v[232:233], v205, %[rs], s42 offen sc1\ns_mov_b32 s59, %[fa]\ns_add_u32 s60, s41, 262144\nbuffer_load_dwordx4 v[104:107], v196, %[rs], s60 offen offset:3072 sc1\ns_add_u32 s60, s41, 393216\nbuffer_load_dwordx4 v[108:111], v196, %[rs], s60 offen offset:3072 sc1\n" \
"s_add_u32 s60, s41, 524288\nbuffer_load_dwordx4 v[112:115], v196, %[rs], s60 offen offset:3072 sc1\ns_add_u32 s60, s41, 655360\nbuffer_load_dwordx4 v[116:119], v196, %[rs], s60 offen offset:3072 sc1\ns_add_u32 s60, s41, 786432\nbuffer_load_dwordx4 v[120:123], v196, %[rs], s60 offen offset:3072 sc1\ns_add_u32 s60, s41, 917504\nbuffer_load_dwordx4 v[124:127], v196, %[rs], s60 offen offset:3072 sc1\n" \
"s_cmp_ge_u32 s40, 64\ns_cbranch_scc1 .Lrd_fin%=\n.Lrd_ychk%=:\ns_and_b32 s44, s40, 1\ns_bfe_u32 s55, s40, 0x10001\ns_mul_i32 s54, s44, 0x80008001\ns_mul_i32 s61, s55, 0x80008000\ns_lshl_b32 s58, s44, 17\n" \
"s_add_u32 s58, s58, %[st]\ns_add_u32 s44, s40, 1\ns_and_b32 s44, s44, 3\ns_lshl_b32 s44, s44, 15\nv_xor_b32 v206, s44, v200\nv_mov_b64 v[180:181], 0\nv_mov_b64 v[182:183], 0\nv_mov_b64 v[184:185], 0\n" \
"v_mov_b64 v[186:187], 0\nv_mov_b64 v[188:189], 0\nv_mov_b64 v[190:191], 0\nv_mov_b64 v[192:193], 0\nv_mov_b64 v[194:195], 0\ns_waitcnt vmcnt(7)\n.Lrd_sg0%=:\ns_memrealtime s[68:69]\n" \
"v_cmp_ne_u32 vcc, s40, v214\ns_cbranch_vccnz .Lrd_sg1%=\nv_max_u32_dpp v215, v215, v215 quad_perm:[1,0,3,2] row_mask:0xf bank_mask:0xf\ns_nop 1\nv_max_u32_dpp v215, v215, v215 quad_perm:[2,3,0,1] row_mask:0xf bank_mask:0xf\ns_nop 1\nv_max_u32_dpp v215, v215, v215 row_half_mirror row_mask:0xf bank_mask:0xf\ns_nop 0\n" \
"v_readlane_b32 s86, v215, 0\nv_readlane_b32 s87, v215, 8\ns_add_u32 s86, s86, %[du]\n.Lrd_tw608%=:\ns_waitcnt lgkmcnt(0)\ns_sub_u32 s44, s86, s68\ns_add_u32 s44, s44, 11\ns_cmp_lt_u32 s44, 250\n" \
"s_cbranch_scc1 .Lrd_tw608x%=\nbuffer_load_dwordx4 v[128:131], v196, %[rs], %[su] offen offset:0 sc1\nbuffer_load_dwordx4 v[132:135], v196, %[rs], %[su] offen offset:1024 sc1\ns_memrealtime s[68:69]\nbuffer_load_dwordx4 v[136:139], v196, %[rs], %[su] offen offset:2048 sc1\nbuffer_load_dwordx4 v[140:143], v196, %[rs], %[su] offen offset:3072 sc1\n.Lrd_tw619%=:\ns_waitcnt lgkmcnt(0)\n" \
"s_sub_u32 s44, s86, s68\ns_add_u32 s44, s44, 21\ns_cmp_lt_u32 s44, 250\ns_cbranch_scc1 .Lrd_tw619x%=\nbuffer_load_dwordx4 v[144:147], v196, %[rs], s57 offen offset:0 sc1\nbuffer_load_dwordx4 v[148:151], v196, %[rs], s57 offen offset:1024 sc1\ns_memrealtime s[68:69]\nbuffer_load_dwordx4 v[152:155], v196, %[rs], s57 offen offset:2048 sc1\n" \
"buffer_load_dwordx4 v[156:159], v196, %[rs], s57 offen offset:3072 sc1\ns_waitcnt vmcnt(14)\nbuffer_store_dword v207, off, %[rs], s59 sc1\ns_mov_b32 s59, -2147483648\n.Lrd_tw633%=:\ns_waitcnt lgkmcnt(0)\ns_sub_u32 s44, s87, s68\ns_add_u32 s44, s44, 24\n" \
"s_cmp_lt_u32 s44, 250\ns_cbranch_scc1 .Lrd_tw633x%=\nbuffer_load_dwordx4 v[160:163], v196, %[rs], %[sp] offen offset:0 sc1\nbuffer_load_dwordx4 v[164:167], v196, %[rs], %[sp] offen offset:1024 sc1\nbuffer_load_dwordx4 v[168:171], v196, %[rs], %[sp] offen offset:2048 sc1\nbuffer_load_dwordx4 v[172:175], v196, %[rs], %[sp] offen offset:3072 sc1\nbuffer_load_dwordx4 v[176:179], v196, %[rs], s56 offen offset:0 sc1\ns_add_u32 s60, s58, 16384\n" \
"buffer_load_dwordx4 v[212:215], v196, %[rs], s60 offen offset:0 sc1\nv_bitop3_b32 v209, v0, v2, s55 bitop3:0x7e\nv_bitop3_b32 v210, v32, v34, s55 bitop3:0x7e\nv_or3_b32 v208, v209, v210, 0\nv_bitop3_b32 v209, v64, v66, s55 bitop3:0x7e\nv_bitop3_b32 v210, v4, v6, s55 bitop3:0x7e\nv_or3_b32 v208, v209, v210, v208\ns_add_u32 s60, s58, 0\n" \
"buffer_load_dwordx4 v[216:219], v196, %[rs], s60 offen offset:0 sc1\nv_bitop3_b32 v209, v36, v38, s55 bitop3:0x7e\nv_bitop3_b32 v210, v68, v70, s55 bitop3:0x7e\nv_or3_b32 v208, v209, v210, v208\nv_bitop3_b32 v209, v8, v10, s55 bitop3:0x7e\nv_bitop3_b32 v210, v40, v42, s55 bitop3:0x7e\nv_or3_b32 v208, v209, v210, v208\ns_add_u32 s60, s58, 0\n" \
"buffer_load_dwordx4 v[220:223], v196, %[rs], s60 offen offset:1024 sc1\nv_bitop3_b32 v209, v72, v74, s55 bitop3:0x7e\nv_bitop3_b32 v210, v12, v14, s55 bitop3:0x7e\nv_or3_b32 v208, v209, v210, v208\nv_bitop3_b32 v209, v44, v46, s55 bitop3:0x7e\nv_bitop3_b32 v210, v76, v78, s55 bitop3:0x7e\nv_or3_b32 v208, v209, v210, v208\ns_add_u32 s60, s58, 16384\n" \
"buffer_load_dwordx4 v[224:227], v196, %[rs], s60 offen offset:1024 sc1\nv_bitop3_b32 v209, v16, v18, s55 bitop3:0x7e\nv_bitop3_b32 v210, v48, v50, s55 bitop3:0x7e\nv_or3_b32 v208, v209, v210, v208\nv_bitop3_b32 v209, v80, v82, s55 bitop3:0x7e\nv_bitop3_b32 v210, v20, v22, s55 bitop3:0x7e\nv_or3_b32 v208, v209, v210, v208\ns_add_u32 s60, s58, 0\n" \
"buffer_load_dwordx4 v[228:231], v196, %[rs], s60 offen offset:2048 sc1\nv_bitop3_b32 v209, v52, v54, s55 bitop3:0x7e\nv_bitop3_b32 v210, v84, v86, s55 bitop3:0x7e\nv_or3_b32 v208, v209, v210, v208\nv_bitop3_b32 v209, v24, v26, s55 bitop3:0x7e\nv_bitop3_b32 v210, v56, v58, s55 bitop3:0x7e\nv_or3_b32 v208, v209, v210, v208\nv_bitop3_b32 v209, v88, v90, s55 bitop3:0x7e\n" \
"v_bitop3_b32 v210, v28, v30, s55 bitop3:0x7e\nv_or3_b32 v208, v209, v210, v208\nv_bitop3_b32 v209, v60, v62, s55 bitop3:0x7e\nv_bitop3_b32 v210, v92, v94, s55 bitop3:0x7e\nv_or3_b32 v208, v209, v210, v208\ns_waitcnt vmcnt(23)\nv_bitop3_b32 v209, v96, v98, s55 bitop3:0x7e\nv_bitop3_b32 v210, v100, v102, s55 bitop3:0x7e\n" \
"v_or3_b32 v208, v209, v210, v208\nv_bitop3_b32 v209, v104, v106, s55 bitop3:0x7e\nv_bitop3_b32 v210, v108, v110, s55 bitop3:0x7e\nv_or3_b32 v208, v209, v210, v208\ns_waitcnt vmcnt(19)\nv_bitop3_b32 v209, v112, v114, s55 bitop3:0x7e\nv_bitop3_b32 v210, v116, v118, s55 bitop3:0x7e\nv_or3_b32 v208, v209, v210, v208\n" \
"v_bitop3_b32 v209, v120, v122, s55 bitop3:0x7e\nv_bitop3_b32 v210, v124, v126, s55 bitop3:0x7e\nv_or3_b32 v208, v209, v210, v208\nv_and_b32 v208, 1, v208\nv_cmp_ne_u32 vcc, 0, v208\ns_cbranch_vccnz .Lrd_yre%=\ns_branch .Lrd_ra%=\n.Lrd_sg1%=:\n" \
"buffer_load_dwordx2 v[214:215], v212, %[rs], 0 offen sc1\ns_waitcnt vmcnt(0)\nbuffer_store_dword v207, off, %[rs], s59 sc1\ns_mov_b32 s59, -2147483648\ns_add_u32 s43, s43, 1\ns_cmp_lt_u32 s43, 262144\ns_cbranch_scc1 .Lrd_sg0%=\ns_branch .Lrd_abort%=\n" \
".Lrd_tw608x%=:\ns_memrealtime s[68:69]\ns_branch .Lrd_tw608%=\n.Lrd_tw619x%=:\ns_memrealtime s[68:69]\ns_branch .Lrd_tw619%=\n.Lrd_tw633x%=:\ns_memrealtime s[68:69]\n" \
"s_branch .Lrd_tw633%=\n.Lrd_fin%=:\ns_waitcnt vmcnt(0)\nbuffer_store_dword v207, off, %[rs], s59 sc1\ns_mov_b32 s59, -2147483648\ns_branch .Lrd_end%=\n.Lrd_yre%=:\ns_mov_b64 exec, vcc\n" \
"s_waitcnt vmcnt(0)\ns_add_u32 s43, s43, 1\ns_cmp_lt_u32 s43, 262144\ns_cbranch_scc1 .Lrd_yld%=\ns_branch .Lrd_abort%=\n.Lrd_rb%=:\ns_mov_b64 exec, s[46:47]\ns_cbranch_execz .Lrd_rs0%=\n" \
"buffer_load_dwordx4 v[160:163], v196, %[rs], %[sp] offen offset:0 sc1\n.Lrd_rs0%=:\ns_mov_b64 exec, s[48:49]\ns_cbranch_execz .Lrd_rs1%=\nbuffer_load_dwordx4 v[164:167], v196, %[rs], %[sp] offen offset:1024 sc1\n.Lrd_rs1%=:\ns_mov_b64 exec, s[50:51]\ns_cbranch_execz .Lrd_rs2%=\n" \
"buffer_load_dwordx4 v[168:171], v196, %[rs], %[sp] offen offset:2048 sc1\n.Lrd_rs2%=:\ns_mov_b64 exec, s[52:53]\ns_cbranch_execz .Lrd_rs3%=\nbuffer_load_dwordx4 v[172:175], v196, %[rs], %[sp] offen offset:3072 sc1\n.Lrd_rs3%=:\ns_mov_b64 exec, s[64:65]\ns_cbranch_execz .Lrd_rs4%=\n" \
"buffer_load_dwordx4 v[176:179], v196, %[rs], s56 offen offset:0 sc1\n.Lrd_rs4%=:\ns_mov_b64 exec, s[86:87]\ns_cbranch_execz .Lrd_rs5%=\nbuffer_load_dwordx4 v[128:131], v196, %[rs], %[su] offen offset:0 sc1\nbuffer_load_dwordx4 v[136:139], v196, %[rs], %[su] offen offset:2048 sc1\n.Lrd_rs5%=:\ns_mov_b64 exec, s[88:89]\n" \
"s_cbranch_execz .Lrd_rs6%=\nbuffer_load_dwordx4 v[144:147], v196, %[rs], s57 offen offset:0 sc1\nbuffer_load_dwordx4 v[152:155], v196, %[rs], s57 offen offset:2048 sc1\n.Lrd_rs6%=:\ns_mov_b64 exec, s[68:69]\ns_cbranch_execz .Lrd_rs7%=\nbuffer_load_dwordx4 v[132:135], v196, %[rs], %[su] offen offset:1024 sc1\nbuffer_load_dwordx4 v[140:143], v196, %[rs], %[su] offen offset:3072 sc1\n" \
".Lrd_rs7%=:\ns_mov_b64 exec, s[62:63]\ns_cbranch_execz .Lrd_rs8%=\nbuffer_load_dwordx4 v[148:151], v196, %[rs], s57 offen offset:1024 sc1\nbuffer_load_dwordx4 v[156:159], v196, %[rs], s57 offen offset:3072 sc1\n.Lrd_rs8%=:\ns_mov_b64 exec, -1\nv_mov_b64 v[180:181], 0\n" \
"v_mov_b64 v[182:183], 0\nv_mov_b64 v[184:185], 0\nv_mov_b64 v[186:187], 0\nv_mov_b64 v[188:189], 0\nv_mov_b64 v[190:191], 0\nv_mov_b64 v[192:193], 0\nv_mov_b64 v[194:195], 0\ns_waitcnt vmcnt(0)\n" \
"s_add_u32 s43, s43, 1\ns_cmp_lt_u32 s43, 262144\ns_cbranch_scc1 .Lrd_ra%=\ns_branch .Lrd_abort%=\n.Lrd_y1%=:\nv_accvgpr_read_b32 v206, a32\nbuffer_load_dword v208, v206, %[rs], 0 offen sc1\ns_waitcnt vmcnt(0)\n" \
"v_cmp_gt_i32 vcc, 1, v208\ns_cbranch_vccz .Lrd_y1r%=\ns_add_u32 s43, s43, 1\ns_cmp_lt_u32 s43, 262144\ns_cbranch_scc1 .Lrd_y1%=\ns_branch .Lrd_abort%=\n.Lrd_tre0%=:\ns_add_u32 s60, s58, 16384\n" \
"buffer_load_dwordx4 v[212:215], v196, %[rs], s60 offen offset:0 sc1\ns_waitcnt vmcnt(0)\nv_bitop3_b32 v209, v212, v214, s61 bitop3:0x7e\nv_cmp_gt_i32 vcc, 0, v209\ns_cbranch_vccz .Lrd_tl0%=\ns_add_u32 s43, s43, 1\ns_cmp_lt_u32 s43, 262144\ns_cbranch_scc1 .Lrd_tre0%=\n" \
"s_branch .Lrd_abort%=\n.Lrd_tre1%=:\ns_add_u32 s60, s58, 0\nbuffer_load_dwordx4 v[216:219], v196, %[rs], s60 offen offset:0 sc1\ns_waitcnt vmcnt(0)\nv_bitop3_b32 v209, v216, v218, s61 bitop3:0x7e\nv_cmp_gt_i32 vcc, 0, v209\ns_cbranch_vccz .Lrd_tl1%=\n" \
"s_add_u32 s43, s43, 1\ns_cmp_lt_u32 s43, 262144\ns_cbranch_scc1 .Lrd_tre1%=\ns_branch .Lrd_abort%=\n.Lrd_tre2%=:\ns_add_u32 s60, s58, 0\nbuffer_load_dwordx4 v[220:223], v196, %[rs], s60 offen offset:1024 sc1\ns_waitcnt vmcnt(0)\n" \
"v_bitop3_b32 v209, v220, v222, s61 bitop3:0x7e\nv_cmp_gt_i32 vcc, 0, v209\ns_cbranch_vccz .Lrd_tl2%=\ns_add_u32 s43, s43, 1\ns_cmp_lt_u32 s43, 262144\ns_cbranch_scc1 .Lrd_tre2%=\ns_branch .Lrd_abort%=\n.Lrd_tre3%=:\n" \
"s_add_u32 s60, s58, 16384\nbuffer_load_dwordx4 v[224:227], v196, %[rs], s60 offen offset:1024 sc1\ns_waitcnt vmcnt(0)\nv_bitop3_b32 v209, v224, v226, s61 bitop3:0x7e\nv_cmp_gt_i32 vcc, 0, v209\ns_cbranch_vccz .Lrd_tl3%=\ns_add_u32 s43, s43, 1\ns_cmp_lt_u32 s43, 262144\n" \
"s_cbranch_scc1 .Lrd_tre3%=\ns_branch .Lrd_abort%=\n.Lrd_tre4%=:\ns_add_u32 s60, s58, 0\nbuffer_load_dwordx4 v[228:231], v196, %[rs], s60 offen offset:2048 sc1\ns_waitcnt vmcnt(0)\nv_bitop3_b32 v209, v228, v230, s61 bitop3:0x7e\nv_cmp_gt_i32 vcc, 0, v209\n" \
"s_cbranch_vccz .Lrd_tl4%=\ns_add_u32 s43, s43, 1\ns_cmp_lt_u32 s43, 262144\ns_cbranch_scc1 .Lrd_tre4%=\ns_branch .Lrd_abort%=\n.Lrd_tre5%=:\ns_add_u32 s60, s58, 0\nbuffer_load_dwordx4 v[232:235], v196, %[rs], s60 offen offset:3072 sc1\n" \
"s_waitcnt vmcnt(0)\nv_bitop3_b32 v209, v232, v234, s61 bitop3:0x7e\nv_cmp_gt_i32 vcc, 0, v209\ns_cbranch_vccz .Lrd_tl5%=\ns_add_u32 s43, s43, 1\ns_cmp_lt_u32 s43, 262144\ns_cbranch_scc1 .Lrd_tre5%=\ns_branch .Lrd_abort%=\n" \
".Lrd_tre6%=:\ns_add_u32 s60, s58, 16384\nbuffer_load_dwordx4 v[236:239], v196, %[rs], s60 offen offset:2048 sc1\ns_waitcnt vmcnt(0)\nv_bitop3_b32 v209, v236, v238, s61 bitop3:0x7e\nv_cmp_gt_i32 vcc, 0, v209\ns_cbranch_vccz .Lrd_tl6%=\ns_add_u32 s43, s43, 1\n" \
"s_cmp_lt_u32 s43, 262144\ns_cbranch_scc1 .Lrd_tre6%=\ns_branch .Lrd_abort%=\n.Lrd_tre7%=:\ns_add_u32 s60, s58, 4096\nbuffer_load_dwordx4 v[240:243], v196, %[rs], s60 offen offset:0 sc1\ns_waitcnt vmcnt(0)\nv_bitop3_b32 v209, v240, v242, s61 bitop3:0x7e\n" \
"v_cmp_gt_i32 vcc, 0, v209\ns_cbranch_vccz .Lrd_tl7%=\ns_add_u32 s43, s43, 1\ns_cmp_lt_u32 s43, 262144\ns_cbranch_scc1 .Lrd_tre7%=\ns_branch .Lrd_abort%=\n.Lrd_tre8%=:\ns_add_u32 s60, s58, 4096\n" \
"buffer_load_dwordx4 v[244:247], v196, %[rs], s60 offen offset:1024 sc1\ns_waitcnt vmcnt(0)\nv_bitop3_b32 v209, v244, v246, s61 bitop3:0x7e\nv_cmp_gt_i32 vcc, 0, v209\ns_cbranch_vccz .Lrd_tl8%=\ns_add_u32 s43, s43, 1\ns_cmp_lt_u32 s43, 262144\ns_cbranch_scc1 .Lrd_tre8%=\n" \
"s_branch .Lrd_abort%=\n.Lrd_tre9%=:\ns_add_u32 s60, s58, 16384\nbuffer_load_dwordx4 v[248:251], v196, %[rs], s60 offen offset:3072 sc1\ns_waitcnt vmcnt(0)\nv_bitop3_b32 v209, v248, v250, s61 bitop3:0x7e\nv_cmp_gt_i32 vcc, 0, v209\ns_cbranch_vccz .Lrd_tl9%=\n" \
"s_add_u32 s43, s43, 1\ns_cmp_lt_u32 s43, 262144\ns_cbranch_scc1 .Lrd_tre9%=\ns_branch .Lrd_abort%=\n.Lrd_tre10%=:\ns_add_u32 s60, s58, 20480\nbuffer_load_dwordx4 v[252:255], v196, %[rs], s60 offen offset:0 sc1\ns_waitcnt vmcnt(0)\n" \
"v_bitop3_b32 v209, v252, v254, s61 bitop3:0x7e\nv_cmp_gt_i32 vcc, 0, v209\ns_cbranch_vccz .Lrd_tl10%=\ns_add_u32 s43, s43, 1\ns_cmp_lt_u32 s43, 262144\ns_cbranch_scc1 .Lrd_tre10%=\ns_branch .Lrd_abort%=\n.Lrd_tre11%=:\n" \
"s_add_u32 s60, s58, 20480\nbuffer_load_dwordx4 v[160:163], v196, %[rs], s60 offen offset:2048 sc1\ns_waitcnt vmcnt(0)\nv_bitop3_b32 v209, v160, v162, s61 bitop3:0x7e\nv_cmp_gt_i32 vcc, 0, v209\ns_cbranch_vccz .Lrd_tl11%=\ns_add_u32 s43, s43, 1\ns_cmp_lt_u32 s43, 262144\n" \
"s_cbranch_scc1 .Lrd_tre11%=\ns_branch .Lrd_abort%=\n.Lrd_tre12%=:\ns_add_u32 s60, s58, 4096\nbuffer_load_dwordx4 v[164:167], v196, %[rs], s60 offen offset:2048 sc1\ns_waitcnt vmcnt(0)\nv_bitop3_b32 v209, v164, v166, s61 bitop3:0x7e\nv_cmp_gt_i32 vcc, 0, v209\n" \
"s_cbranch_vccz .Lrd_tl12%=\ns_add_u32 s43, s43, 1\ns_cmp_lt_u32 s43, 262144\ns_cbranch_scc1 .Lrd_tre12%=\ns_branch .Lrd_abort%=\n.Lrd_tre13%=:\ns_add_u32 s60, s58, 4096\nbuffer_load_dwordx4 v[168:171], v196, %[rs], s60 offen offset:3072 sc1\n" \
"s_waitcnt vmcnt(0)\nv_bitop3_b32 v209, v168, v170, s61 bitop3:0x7e\nv_cmp_gt_i32 vcc, 0, v209\ns_cbranch_vccz .Lrd_tl13%=\ns_add_u32 s43, s43, 1\ns_cmp_lt_u32 s43, 262144\ns_cbranch_scc1 .Lrd_tre13%=\ns_branch .Lrd_abort%=\n" \
".Lrd_tre14%=:\ns_add_u32 s60, s58, 20480\nbuffer_load_dwordx4 v[172:175], v196, %[rs], s60 offen offset:1024 sc1\ns_waitcnt vmcnt(0)\nv_bitop3_b32 v209, v172, v174, s61 bitop3:0x7e\nv_cmp_gt_i32 vcc, 0, v209\ns_cbranch_vccz .Lrd_tl14%=\ns_add_u32 s43, s43, 1\n" \
"s_cmp_lt_u32 s43, 262144\ns_cbranch_scc1 .Lrd_tre14%=\ns_branch .Lrd_abort%=\n.Lrd_tre15%=:\ns_add_u32 s60, s58, 20480\nbuffer_load_dwordx4 v[176:179], v196, %[rs], s60 offen offset:3072 sc1\ns_waitcnt vmcnt(0)\nv_bitop3_b32 v209, v176, v178, s61 bitop3:0x7e\n" \
"v_cmp_gt_i32 vcc, 0, v209\ns_cbranch_vccz .Lrd_tl15%=\ns_add_u32 s43, s43, 1\ns_cmp_lt_u32 s43, 262144\ns_cbranch_scc1 .Lrd_tre15%=\ns_branch .Lrd_abort%=\n.Lrd_abort%=:\ns_mov_b64 exec, -1\n" \
"v_mov_b32 v16, 0\ns_movk_i32 s44, 6400\nbuffer_store_dword v16, off, %[rs], s44 sc1\n.Lrd_end%=:\ns_waitcnt vmcnt(0) lgkmcnt(0)\n" \
""
#define GG_ASM \
"v_mbcnt_lo_u32_b32 v255, -1, 0\nv_mbcnt_hi_u32_b32 v255, -1, v255\nv_lshlrev_b32 v255, 2, v255\nv_add_u32 v255, %[tb], v255\nds_read_b32 v0, v255 offset:0\nds_read_b32 v1, v255 offset:256\nds_read_b32 v2, v255 offset:512\nds_read_b32 v3, v255 offset:768\n" \
"ds_read_b32 v4, v255 offset:1024\nds_read_b32 v5, v255 offset:1280\nds_read_b32 v92, v255 offset:1536\nds_read_b32 v93, v255 offset:1792\nds_read_b32 v94, v255 offset:2048\ns_waitcnt lgkmcnt(0)\nv_mov_b32 v6, 0x3f803f80\nv_mov_b32 v7, 0x3f803f80\n" \
"s_add_u32 m0, %[lw], 16384\ns_add_u32 s44, %[wo], 0\nbuffer_load_dwordx4 v92, %[rw], s44 offen nt lds\ns_add_u32 m0, %[lw], 17408\ns_add_u32 s44, %[wo], 57344\nbuffer_load_dwordx4 v92, %[rw], s44 offen nt lds\ns_add_u32 m0, %[lw], 18432\ns_add_u32 s44, %[wo], 114688\n" \
"buffer_load_dwordx4 v92, %[rw], s44 offen nt lds\ns_add_u32 m0, %[lw], 19456\ns_add_u32 s44, %[wo], 172032\nbuffer_load_dwordx4 v92, %[rw], s44 offen nt lds\ns_add_u32 m0, %[lw], 20480\ns_add_u32 s44, %[wo], 229376\nbuffer_load_dwordx4 v92, %[rw], s44 offen nt lds\ns_add_u32 m0, %[lw], 21504\n" \
"s_add_u32 s44, %[wo], 286720\nbuffer_load_dwordx4 v92, %[rw], s44 offen nt lds\ns_add_u32 m0, %[lw], 22528\ns_add_u32 s44, %[wo], 344064\nbuffer_load_dwordx4 v92, %[rw], s44 offen nt lds\ns_add_u32 m0, %[lw], 23552\ns_add_u32 s44, %[wo], 401408\nbuffer_load_dwordx4 v92, %[rw], s44 offen nt lds\n" \
"s_add_u32 m0, %[tb], 0\ns_add_u32 s44, %[wo], 1835008\nbuffer_load_dwordx4 v92, %[rw], s44 offen nt lds\ns_add_u32 m0, %[tb], 1024\ns_add_u32 s44, %[wo], 1892352\nbuffer_load_dwordx4 v92, %[rw], s44 offen nt lds\ns_add_u32 m0, %[tb], 2048\ns_add_u32 s44, %[wo], 1949696\n" \
"buffer_load_dwordx4 v92, %[rw], s44 offen nt lds\ns_add_u32 m0, %[tb], 3072\ns_add_u32 s44, %[wo], 2007040\nbuffer_load_dwordx4 v92, %[rw], s44 offen nt lds\ns_add_u32 m0, %[tb], 4096\ns_add_u32 s44, %[wo], 2064384\nbuffer_load_dwordx4 v92, %[rw], s44 offen nt lds\ns_add_u32 m0, %[tb], 5120\n" \
"s_add_u32 s44, %[wo], 2121728\nbuffer_load_dwordx4 v92, %[rw], s44 offen nt lds\ns_add_u32 m0, %[tb], 6144\ns_add_u32 s44, %[wo], 2179072\nbuffer_load_dwordx4 v92, %[rw], s44 offen nt lds\ns_add_u32 m0, %[tb], 7168\ns_add_u32 s44, %[wo], 2236416\nbuffer_load_dwordx4 v92, %[rw], s44 offen nt lds\n" \
"s_add_u32 m0, %[tb], 8192\ns_add_u32 s44, %[wo], 3670016\nbuffer_load_dwordx4 v92, %[rw], s44 offen nt lds\ns_add_u32 m0, %[tb], 9216\ns_add_u32 s44, %[wo], 3727360\nbuffer_load_dwordx4 v92, %[rw], s44 offen nt lds\ns_add_u32 m0, %[tb], 10240\ns_add_u32 s44, %[wo], 3784704\n" \
"buffer_load_dwordx4 v92, %[rw], s44 offen nt lds\ns_add_u32 m0, %[tb], 11264\ns_add_u32 s44, %[wo], 3842048\nbuffer_load_dwordx4 v92, %[rw], s44 offen nt lds\ns_add_u32 m0, %[lw], 0\ns_add_u32 s44, %[wo], 3899392\nbuffer_load_dwordx4 v92, %[rw], s44 offen nt lds\ns_add_u32 m0, %[lw], 1024\n" \
"s_add_u32 s44, %[wo], 3956736\nbuffer_load_dwordx4 v92, %[rw], s44 offen nt lds\ns_add_u32 m0, %[lw], 2048\ns_add_u32 s44, %[wo], 4014080\nbuffer_load_dwordx4 v92, %[rw], s44 offen nt lds\ns_add_u32 m0, %[lw], 3072\ns_add_u32 s44, %[wo], 4071424\nbuffer_load_dwordx4 v92, %[rw], s44 offen nt lds\n" \
"s_add_u32 m0, %[lw], 4096\ns_add_u32 s44, %[wo], 5505024\nbuffer_load_dwordx4 v92, %[rw], s44 offen nt lds\ns_add_u32 m0, %[lw], 5120\ns_add_u32 s44, %[wo], 5562368\nbuffer_load_dwordx4 v92, %[rw], s44 offen nt lds\ns_add_u32 m0, %[lw], 6144\ns_add_u32 s44, %[wo], 5619712\n" \
"buffer_load_dwordx4 v92, %[rw], s44 offen nt lds\ns_add_u32 m0, %[lw], 7168\ns_add_u32 s44, %[wo], 5677056\nbuffer_load_dwordx4 v92, %[rw], s44 offen nt lds\ns_add_u32 m0, %[lw], 8192\ns_add_u32 s44, %[wo], 5734400\nbuffer_load_dwordx4 v92, %[rw], s44 offen nt lds\ns_add_u32 m0, %[lw], 9216\n" \
"s_add_u32 s44, %[wo], 5791744\nbuffer_load_dwordx4 v92, %[rw], s44 offen nt lds\ns_add_u32 m0, %[lw], 10240\ns_add_u32 s44, %[wo], 5849088\nbuffer_load_dwordx4 v92, %[rw], s44 offen nt lds\ns_add_u32 m0, %[lw], 11264\ns_add_u32 s44, %[wo], 5906432\nbuffer_load_dwordx4 v92, %[rw], s44 offen nt lds\n" \
"s_waitcnt vmcnt(28)\ns_barrier\ns_add_u32 m0, %[lw], 12288\ns_add_u32 s44, %[wo], 458752\nbuffer_load_dwordx4 v92, %[rw], s44 offen nt lds\ns_add_u32 m0, %[lw], 13312\ns_add_u32 s44, %[wo], 516096\nbuffer_load_dwordx4 v92, %[rw], s44 offen nt lds\n" \
"s_add_u32 m0, %[lw], 14336\ns_add_u32 s44, %[wo], 573440\nbuffer_load_dwordx4 v92, %[rw], s44 offen nt lds\ns_add_u32 m0, %[lw], 15360\ns_add_u32 s44, %[wo], 630784\nbuffer_load_dwordx4 v92, %[rw], s44 offen nt lds\nds_read_b64_tr_b16 a[0:1], v94 offset:16384\nds_read_b64_tr_b16 a[2:3], v94 offset:16896\n" \
"ds_read_b64_tr_b16 a[8:9], v94 offset:16392\nds_read_b64_tr_b16 a[10:11], v94 offset:16904\nds_read_b64_tr_b16 a[16:17], v94 offset:40960\nds_read_b64_tr_b16 a[18:19], v94 offset:41472\nds_read_b64_tr_b16 a[24:25], v94 offset:40968\nds_read_b64_tr_b16 a[26:27], v94 offset:41480\ns_waitcnt lgkmcnt(0)\nv_mfma_f32_16x16x16_bf16 v[8:11], a[0:1], v[6:7], 0\n" \
"v_mfma_f32_16x16x16_bf16 v[12:15], a[8:9], v[6:7], 0\nv_mfma_f32_16x16x16_bf16 v[16:19], a[16:17], v[6:7], 0\nv_mfma_f32_16x16x16_bf16 v[20:23], a[24:25], v[6:7], 0\nv_mfma_f32_16x16x16_bf16 v[8:11], a[2:3], v[6:7], v[8:11]\nv_mfma_f32_16x16x16_bf16 v[12:15], a[10:11], v[6:7], v[12:15]\nv_mfma_f32_16x16x16_bf16 v[16:19], a[18:19], v[6:7], v[16:19]\nv_mfma_f32_16x16x16_bf16 v[20:23], a[26:27], v[6:7], v[20:23]\ns_waitcnt vmcnt(28)\n" \
"s_barrier\ns_add_u32 m0, %[lw], 16384\ns_add_u32 s44, %[wo], 688128\nbuffer_load_dwordx4 v92, %[rw], s44 offen nt lds\ns_add_u32 m0, %[lw], 17408\ns_add_u32 s44, %[wo], 745472\nbuffer_load_dwordx4 v92, %[rw], s44 offen nt lds\ns_add_u32 m0, %[lw], 18432\n" \
"s_add_u32 s44, %[wo], 802816\nbuffer_load_dwordx4 v92, %[rw], s44 offen nt lds\ns_add_u32 m0, %[lw], 19456\ns_add_u32 s44, %[wo], 860160\nbuffer_load_dwordx4 v92, %[rw], s44 offen nt lds\nds_read_b64_tr_b16 a[4:5], v94 offset:20480\nds_read_b64_tr_b16 a[6:7], v94 offset:20992\nds_read_b64_tr_b16 a[12:13], v94 offset:20488\n" \
"ds_read_b64_tr_b16 a[14:15], v94 offset:21000\nds_read_b64_tr_b16 a[20:21], v94 offset:45056\nds_read_b64_tr_b16 a[22:23], v94 offset:45568\nds_read_b64_tr_b16 a[28:29], v94 offset:45064\nds_read_b64_tr_b16 a[30:31], v94 offset:45576\ns_waitcnt lgkmcnt(0)\nv_mfma_f32_16x16x16_bf16 v[8:11], a[4:5], v[6:7], v[8:11]\nv_mfma_f32_16x16x16_bf16 v[12:15], a[12:13], v[6:7], v[12:15]\n" \
"v_mfma_f32_16x16x16_bf16 v[16:19], a[20:21], v[6:7], v[16:19]\nv_mfma_f32_16x16x16_bf16 v[20:23], a[28:29], v[6:7], v[20:23]\nv_mfma_f32_16x16x16_bf16 v[8:11], a[6:7], v[6:7], v[8:11]\nv_mfma_f32_16x16x16_bf16 v[12:15], a[14:15], v[6:7], v[12:15]\nv_mfma_f32_16x16x16_bf16 v[16:19], a[22:23], v[6:7], v[16:19]\nv_mfma_f32_16x16x16_bf16 v[20:23], a[30:31], v[6:7], v[20:23]\ns_waitcnt vmcnt(28)\ns_barrier\n" \
"s_add_u32 m0, %[lw], 20480\ns_add_u32 s44, %[wo], 917504\nbuffer_load_dwordx4 v92, %[rw], s44 offen nt lds\ns_add_u32 m0, %[lw], 21504\ns_add_u32 s44, %[wo], 974848\nbuffer_load_dwordx4 v92, %[rw], s44 offen nt lds\ns_add_u32 m0, %[lw], 22528\ns_add_u32 s44, %[wo], 1032192\n" \
"buffer_load_dwordx4 v92, %[rw], s44 offen nt lds\ns_add_u32 m0, %[lw], 23552\ns_add_u32 s44, %[wo], 1089536\nbuffer_load_dwordx4 v92, %[rw], s44 offen nt lds\nds_read_b64_tr_b16 a[104:105], v93 offset:0\nds_read_b64_tr_b16 a[106:107], v93 offset:512\nds_read_b64_tr_b16 a[112:113], v93 offset:8\nds_read_b64_tr_b16 a[114:115], v93 offset:520\n" \
"ds_read_b64_tr_b16 a[120:121], v93 offset:12288\nds_read_b64_tr_b16 a[122:123], v93 offset:12800\nds_read_b64_tr_b16 a[128:129], v93 offset:12296\nds_read_b64_tr_b16 a[130:131], v93 offset:12808\ns_waitcnt lgkmcnt(0)\nv_mfma_f32_16x16x16_bf16 v[8:11], a[104:105], v[6:7], v[8:11]\nv_mfma_f32_16x16x16_bf16 v[12:15], a[112:113], v[6:7], v[12:15]\nv_mfma_f32_16x16x16_bf16 v[16:19], a[120:121], v[6:7], v[16:19]\n" \
"v_mfma_f32_16x16x16_bf16 v[20:23], a[128:129], v[6:7], v[20:23]\nv_mfma_f32_16x16x16_bf16 v[8:11], a[106:107], v[6:7], v[8:11]\nv_mfma_f32_16x16x16_bf16 v[12:15], a[114:115], v[6:7], v[12:15]\nv_mfma_f32_16x16x16_bf16 v[16:19], a[122:123], v[6:7], v[16:19]\nv_mfma_f32_16x16x16_bf16 v[20:23], a[130:131], v[6:7], v[20:23]\ns_waitcnt vmcnt(28)\ns_barrier\ns_add_u32 m0, %[tb], 0\n" \
"s_add_u32 s44, %[wo], 1146880\nbuffer_load_dwordx4 v92, %[rw], s44 offen nt lds\ns_add_u32 m0, %[tb], 1024\ns_add_u32 s44, %[wo], 1204224\nbuffer_load_dwordx4 v92, %[rw], s44 offen nt lds\ns_add_u32 m0, %[tb], 2048\ns_add_u32 s44, %[wo], 1261568\nbuffer_load_dwordx4 v92, %[rw], s44 offen nt lds\n" \
"s_add_u32 m0, %[tb], 3072\ns_add_u32 s44, %[wo], 1318912\nbuffer_load_dwordx4 v92, %[rw], s44 offen nt lds\nds_read_b64_tr_b16 a[108:109], v93 offset:4096\nds_read_b64_tr_b16 a[110:111], v93 offset:4608\nds_read_b64_tr_b16 a[116:117], v93 offset:4104\nds_read_b64_tr_b16 a[118:119], v93 offset:4616\nds_read_b64_tr_b16 a[124:125], v93 offset:16384\n" \
"ds_read_b64_tr_b16 a[126:127], v93 offset:16896\nds_read_b64_tr_b16 a[132:133], v93 offset:16392\nds_read_b64_tr_b16 a[134:135], v93 offset:16904\ns_waitcnt lgkmcnt(0)\nv_mfma_f32_16x16x16_bf16 v[8:11], a[108:109], v[6:7], v[8:11]\nv_mfma_f32_16x16x16_bf16 v[12:15], a[116:117], v[6:7], v[12:15]\nv_mfma_f32_16x16x16_bf16 v[16:19], a[124:125], v[6:7], v[16:19]\nv_mfma_f32_16x16x16_bf16 v[20:23], a[132:133], v[6:7], v[20:23]\n" \
"v_mfma_f32_16x16x16_bf16 v[8:11], a[110:111], v[6:7], v[8:11]\nv_mfma_f32_16x16x16_bf16 v[12:15], a[118:119], v[6:7], v[12:15]\nv_mfma_f32_16x16x16_bf16 v[16:19], a[126:127], v[6:7], v[16:19]\nv_mfma_f32_16x16x16_bf16 v[20:23], a[134:135], v[6:7], v[20:23]\ns_waitcnt vmcnt(28)\ns_barrier\ns_add_u32 m0, %[tb], 4096\ns_add_u32 s44, %[wo], 1376256\n" \
"buffer_load_dwordx4 v92, %[rw], s44 offen nt lds\ns_add_u32 m0, %[tb], 5120\ns_add_u32 s44, %[wo], 1433600\nbuffer_load_dwordx4 v92, %[rw], s44 offen nt lds\ns_add_u32 m0, %[tb], 6144\ns_add_u32 s44, %[wo], 1490944\nbuffer_load_dwordx4 v92, %[rw], s44 offen nt lds\ns_add_u32 m0, %[tb], 7168\n" \
"s_add_u32 s44, %[wo], 1548288\nbuffer_load_dwordx4 v92, %[rw], s44 offen nt lds\nds_read_b64_tr_b16 a[208:209], v93 offset:8192\nds_read_b64_tr_b16 a[210:211], v93 offset:8704\nds_read_b64_tr_b16 a[216:217], v93 offset:8200\nds_read_b64_tr_b16 a[218:219], v93 offset:8712\nds_read_b64_tr_b16 a[224:225], v93 offset:20480\nds_read_b64_tr_b16 a[226:227], v93 offset:20992\n" \
"ds_read_b64_tr_b16 a[232:233], v93 offset:20488\nds_read_b64_tr_b16 a[234:235], v93 offset:21000\ns_waitcnt lgkmcnt(0)\nv_mfma_f32_16x16x16_bf16 v[8:11], a[208:209], v[6:7], v[8:11]\nv_mfma_f32_16x16x16_bf16 v[12:15], a[216:217], v[6:7], v[12:15]\nv_mfma_f32_16x16x16_bf16 v[16:19], a[224:225], v[6:7], v[16:19]\nv_mfma_f32_16x16x16_bf16 v[20:23], a[232:233], v[6:7], v[20:23]\nv_mfma_f32_16x16x16_bf16 v[8:11], a[210:211], v[6:7], v[8:11]\n" \
"v_mfma_f32_16x16x16_bf16 v[12:15], a[218:219], v[6:7], v[12:15]\nv_mfma_f32_16x16x16_bf16 v[16:19], a[226:227], v[6:7], v[16:19]\nv_mfma_f32_16x16x16_bf16 v[20:23], a[234:235], v[6:7], v[20:23]\ns_waitcnt vmcnt(28)\ns_barrier\ns_add_u32 m0, %[tb], 8192\ns_add_u32 s44, %[wo], 1605632\nbuffer_load_dwordx4 v92, %[rw], s44 offen nt lds\n" \
"s_add_u32 m0, %[tb], 9216\ns_add_u32 s44, %[wo], 1662976\nbuffer_load_dwordx4 v92, %[rw], s44 offen nt lds\ns_add_u32 m0, %[tb], 10240\ns_add_u32 s44, %[wo], 1720320\nbuffer_load_dwordx4 v92, %[rw], s44 offen nt lds\ns_add_u32 m0, %[tb], 11264\ns_add_u32 s44, %[wo], 1777664\n" \
"buffer_load_dwordx4 v92, %[rw], s44 offen nt lds\nds_read_b64_tr_b16 a[212:213], v94 offset:0\nds_read_b64_tr_b16 a[214:215], v94 offset:512\nds_read_b64_tr_b16 a[220:221], v94 offset:8\nds_read_b64_tr_b16 a[222:223], v94 offset:520\nds_read_b64_tr_b16 a[228:229], v94 offset:24576\nds_read_b64_tr_b16 a[230:231], v94 offset:25088\nds_read_b64_tr_b16 a[236:237], v94 offset:24584\n" \
"ds_read_b64_tr_b16 a[238:239], v94 offset:25096\ns_waitcnt lgkmcnt(0)\nv_mfma_f32_16x16x16_bf16 v[8:11], a[212:213], v[6:7], v[8:11]\nv_mfma_f32_16x16x16_bf16 v[12:15], a[220:221], v[6:7], v[12:15]\nv_mfma_f32_16x16x16_bf16 v[16:19], a[228:229], v[6:7], v[16:19]\nv_mfma_f32_16x16x16_bf16 v[20:23], a[236:237], v[6:7], v[20:23]\nv_mfma_f32_16x16x16_bf16 v[8:11], a[214:215], v[6:7], v[8:11]\nv_mfma_f32_16x16x16_bf16 v[12:15], a[222:223], v[6:7], v[12:15]\n" \
"v_mfma_f32_16x16x16_bf16 v[16:19], a[230:231], v[6:7], v[16:19]\nv_mfma_f32_16x16x16_bf16 v[20:23], a[238:239], v[6:7], v[20:23]\ns_waitcnt vmcnt(28)\ns_barrier\ns_add_u32 m0, %[lw], 0\ns_add_u32 s44, %[wo], 2293760\nbuffer_load_dwordx4 v92, %[rw], s44 offen nt lds\ns_add_u32 m0, %[lw], 1024\n" \
"s_add_u32 s44, %[wo], 2351104\nbuffer_load_dwordx4 v92, %[rw], s44 offen nt lds\ns_add_u32 m0, %[lw], 2048\ns_add_u32 s44, %[wo], 2408448\nbuffer_load_dwordx4 v92, %[rw], s44 offen nt lds\ns_add_u32 m0, %[lw], 3072\ns_add_u32 s44, %[wo], 2465792\nbuffer_load_dwordx4 v92, %[rw], s44 offen nt lds\n" \
"ds_read_b64_tr_b16 v[152:153], v94 offset:4096\nds_read_b64_tr_b16 v[154:155], v94 offset:4608\nds_read_b64_tr_b16 v[160:161], v94 offset:4104\nds_read_b64_tr_b16 v[162:163], v94 offset:4616\nds_read_b64_tr_b16 v[168:169], v94 offset:28672\nds_read_b64_tr_b16 v[170:171], v94 offset:29184\nds_read_b64_tr_b16 v[176:177], v94 offset:28680\nds_read_b64_tr_b16 v[178:179], v94 offset:29192\n" \
"s_waitcnt lgkmcnt(0)\nv_mfma_f32_16x16x16_bf16 v[8:11], v[152:153], v[6:7], v[8:11]\nv_mfma_f32_16x16x16_bf16 v[12:15], v[160:161], v[6:7], v[12:15]\nv_mfma_f32_16x16x16_bf16 v[16:19], v[168:169], v[6:7], v[16:19]\nv_mfma_f32_16x16x16_bf16 v[20:23], v[176:177], v[6:7], v[20:23]\nv_mfma_f32_16x16x16_bf16 v[8:11], v[154:155], v[6:7], v[8:11]\nv_mfma_f32_16x16x16_bf16 v[12:15], v[162:163], v[6:7], v[12:15]\nv_mfma_f32_16x16x16_bf16 v[16:19], v[170:171], v[6:7], v[16:19]\n" \
"v_mfma_f32_16x16x16_bf16 v[20:23], v[178:179], v[6:7], v[20:23]\ns_waitcnt vmcnt(28)\ns_barrier\ns_add_u32 m0, %[lw], 4096\ns_add_u32 s44, %[wo], 2523136\nbuffer_load_dwordx4 v92, %[rw], s44 offen nt lds\ns_add_u32 m0, %[lw], 5120\ns_add_u32 s44, %[wo], 2580480\n" \
"buffer_load_dwordx4 v92, %[rw], s44 offen nt lds\ns_add_u32 m0, %[lw], 6144\ns_add_u32 s44, %[wo], 2637824\nbuffer_load_dwordx4 v92, %[rw], s44 offen nt lds\ns_add_u32 m0, %[lw], 7168\ns_add_u32 s44, %[wo], 2695168\nbuffer_load_dwordx4 v92, %[rw], s44 offen nt lds\nds_read_b64_tr_b16 v[156:157], v94 offset:8192\n" \
"ds_read_b64_tr_b16 v[158:159], v94 offset:8704\nds_read_b64_tr_b16 v[164:165], v94 offset:8200\nds_read_b64_tr_b16 v[166:167], v94 offset:8712\nds_read_b64_tr_b16 v[172:173], v94 offset:32768\nds_read_b64_tr_b16 v[174:175], v94 offset:33280\nds_read_b64_tr_b16 v[180:181], v94 offset:32776\nds_read_b64_tr_b16 v[182:183], v94 offset:33288\ns_waitcnt lgkmcnt(0)\n" \
"v_mfma_f32_16x16x16_bf16 v[8:11], v[156:157], v[6:7], v[8:11]\nv_mfma_f32_16x16x16_bf16 v[12:15], v[164:165], v[6:7], v[12:15]\nv_mfma_f32_16x16x16_bf16 v[16:19], v[172:173], v[6:7], v[16:19]\nv_mfma_f32_16x16x16_bf16 v[20:23], v[180:181], v[6:7], v[20:23]\nv_mfma_f32_16x16x16_bf16 v[8:11], v[158:159], v[6:7], v[8:11]\nv_mfma_f32_16x16x16_bf16 v[12:15], v[166:167], v[6:7], v[12:15]\nv_mfma_f32_16x16x16_bf16 v[16:19], v[174:175], v[6:7], v[16:19]\nv_mfma_f32_16x16x16_bf16 v[20:23], v[182:183], v[6:7], v[20:23]\n" \
"s_waitcnt vmcnt(28)\ns_barrier\ns_add_u32 m0, %[lw], 8192\ns_add_u32 s44, %[wo], 2752512\nbuffer_load_dwordx4 v92, %[rw], s44 offen nt lds\ns_add_u32 m0, %[lw], 9216\ns_add_u32 s44, %[wo], 2809856\nbuffer_load_dwordx4 v92, %[rw], s44 offen nt lds\n" \
"s_add_u32 m0, %[lw], 10240\ns_add_u32 s44, %[wo], 2867200\nbuffer_load_dwordx4 v92, %[rw], s44 offen nt lds\ns_add_u32 m0, %[lw], 11264\ns_add_u32 s44, %[wo], 2924544\nbuffer_load_dwordx4 v92, %[rw], s44 offen nt lds\nds_read_b64_tr_b16 a[32:33], v94 offset:12288\nds_read_b64_tr_b16 a[34:35], v94 offset:12800\n" \
"ds_read_b64_tr_b16 v[88:89], v94 offset:12296\nds_read_b64_tr_b16 v[90:91], v94 offset:12808\nds_read_b64_tr_b16 a[40:41], v94 offset:36864\nds_read_b64_tr_b16 a[42:43], v94 offset:37376\nds_read_b64_tr_b16 a[48:49], v94 offset:36872\nds_read_b64_tr_b16 a[50:51], v94 offset:37384\ns_waitcnt lgkmcnt(0)\nv_mfma_f32_16x16x16_bf16 v[8:11], a[32:33], v[6:7], v[8:11]\n" \
"v_mfma_f32_16x16x16_bf16 v[12:15], v[88:89], v[6:7], v[12:15]\nv_mfma_f32_16x16x16_bf16 v[16:19], a[40:41], v[6:7], v[16:19]\nv_mfma_f32_16x16x16_bf16 v[20:23], a[48:49], v[6:7], v[20:23]\nv_mfma_f32_16x16x16_bf16 v[8:11], a[34:35], v[6:7], v[8:11]\nv_mfma_f32_16x16x16_bf16 v[12:15], v[90:91], v[6:7], v[12:15]\nv_mfma_f32_16x16x16_bf16 v[16:19], a[42:43], v[6:7], v[16:19]\nv_mfma_f32_16x16x16_bf16 v[20:23], a[50:51], v[6:7], v[20:23]\ns_waitcnt vmcnt(28)\n" \
"s_barrier\ns_add_u32 m0, %[lw], 12288\ns_add_u32 s44, %[wo], 2981888\nbuffer_load_dwordx4 v92, %[rw], s44 offen nt lds\ns_add_u32 m0, %[lw], 13312\ns_add_u32 s44, %[wo], 3039232\nbuffer_load_dwordx4 v92, %[rw], s44 offen nt lds\ns_add_u32 m0, %[lw], 14336\n" \
"s_add_u32 s44, %[wo], 3096576\nbuffer_load_dwordx4 v92, %[rw], s44 offen nt lds\ns_add_u32 m0, %[lw], 15360\ns_add_u32 s44, %[wo], 3153920\nbuffer_load_dwordx4 v92, %[rw], s44 offen nt lds\nds_read_b64_tr_b16 a[36:37], v94 offset:16384\nds_read_b64_tr_b16 a[38:39], v94 offset:16896\nds_read_b64_tr_b16 v[84:85], v94 offset:16392\n" \
"ds_read_b64_tr_b16 v[86:87], v94 offset:16904\nds_read_b64_tr_b16 a[44:45], v94 offset:40960\nds_read_b64_tr_b16 a[46:47], v94 offset:41472\nds_read_b64_tr_b16 a[52:53], v94 offset:40968\nds_read_b64_tr_b16 a[54:55], v94 offset:41480\ns_waitcnt lgkmcnt(0)\nv_mfma_f32_16x16x16_bf16 v[8:11], a[36:37], v[6:7], v[8:11]\nv_mfma_f32_16x16x16_bf16 v[12:15], v[84:85], v[6:7], v[12:15]\n" \
"v_mfma_f32_16x16x16_bf16 v[16:19], a[44:45], v[6:7], v[16:19]\nv_mfma_f32_16x16x16_bf16 v[20:23], a[52:53], v[6:7], v[20:23]\nv_mfma_f32_16x16x16_bf16 v[8:11], a[38:39], v[6:7], v[8:11]\nv_mfma_f32_16x16x16_bf16 v[12:15], v[86:87], v[6:7], v[12:15]\nv_mfma_f32_16x16x16_bf16 v[16:19], a[46:47], v[6:7], v[16:19]\nv_mfma_f32_16x16x16_bf16 v[20:23], a[54:55], v[6:7], v[20:23]\ns_waitcnt vmcnt(28)\ns_barrier\n" \
"s_add_u32 m0, %[lw], 16384\ns_add_u32 s44, %[wo], 3211264\nbuffer_load_dwordx4 v92, %[rw], s44 offen nt lds\ns_add_u32 m0, %[lw], 17408\ns_add_u32 s44, %[wo], 3268608\nbuffer_load_dwordx4 v92, %[rw], s44 offen nt lds\ns_add_u32 m0, %[lw], 18432\ns_add_u32 s44, %[wo], 3325952\n" \
"buffer_load_dwordx4 v92, %[rw], s44 offen nt lds\ns_add_u32 m0, %[lw], 19456\ns_add_u32 s44, %[wo], 3383296\nbuffer_load_dwordx4 v92, %[rw], s44 offen nt lds\nds_read_b64_tr_b16 a[56:57], v94 offset:20480\nds_read_b64_tr_b16 a[58:59], v94 offset:20992\nds_read_b64_tr_b16 a[64:65], v94 offset:20488\nds_read_b64_tr_b16 a[66:67], v94 offset:21000\n" \
"ds_read_b64_tr_b16 v[80:81], v94 offset:45056\nds_read_b64_tr_b16 v[82:83], v94 offset:45568\nds_read_b64_tr_b16 a[72:73], v94 offset:45064\nds_read_b64_tr_b16 a[74:75], v94 offset:45576\ns_waitcnt lgkmcnt(0)\nv_mfma_f32_16x16x16_bf16 v[8:11], a[56:57], v[6:7], v[8:11]\nv_mfma_f32_16x16x16_bf16 v[12:15], a[64:65], v[6:7], v[12:15]\nv_mfma_f32_16x16x16_bf16 v[16:19], v[80:81], v[6:7], v[16:19]\n" \
"v_mfma_f32_16x16x16_bf16 v[20:23], a[72:73], v[6:7], v[20:23]\nv_mfma_f32_16x16x16_bf16 v[8:11], a[58:59], v[6:7], v[8:11]\nv_mfma_f32_16x16x16_bf16 v[12:15], a[66:67], v[6:7], v[12:15]\nv_mfma_f32_16x16x16_bf16 v[16:19], v[82:83], v[6:7], v[16:19]\nv_mfma_f32_16x16x16_bf16 v[20:23], a[74:75], v[6:7], v[20:23]\ns_waitcnt vmcnt(28)\ns_barrier\ns_add_u32 m0, %[lw], 20480\n" \
"s_add_u32 s44, %[wo], 3440640\nbuffer_load_dwordx4 v92, %[rw], s44 offen nt lds\ns_add_u32 m0, %[lw], 21504\ns_add_u32 s44, %[wo], 3497984\nbuffer_load_dwordx4 v92, %[rw], s44 offen nt lds\ns_add_u32 m0, %[lw], 22528\ns_add_u32 s44, %[wo], 3555328\nbuffer_load_dwordx4 v92, %[rw], s44 offen nt lds\n" \
"s_add_u32 m0, %[lw], 23552\ns_add_u32 s44, %[wo], 3612672\nbuffer_load_dwordx4 v92, %[rw], s44 offen nt lds\nds_read_b64_tr_b16 a[60:61], v93 offset:0\nds_read_b64_tr_b16 a[62:63], v93 offset:512\nds_read_b64_tr_b16 a[68:69], v93 offset:8\nds_read_b64_tr_b16 a[70:71], v93 offset:520\nds_read_b64_tr_b16 v[76:77], v93 offset:12288\n" \
"ds_read_b64_tr_b16 v[78:79], v93 offset:12800\nds_read_b64_tr_b16 a[76:77], v93 offset:12296\nds_read_b64_tr_b16 a[78:79], v93 offset:12808\ns_waitcnt lgkmcnt(0)\nv_mfma_f32_16x16x16_bf16 v[8:11], a[60:61], v[6:7], v[8:11]\nv_mfma_f32_16x16x16_bf16 v[12:15], a[68:69], v[6:7], v[12:15]\nv_mfma_f32_16x16x16_bf16 v[16:19], v[76:77], v[6:7], v[16:19]\nv_mfma_f32_16x16x16_bf16 v[20:23], a[76:77], v[6:7], v[20:23]\n" \
"v_mfma_f32_16x16x16_bf16 v[8:11], a[62:63], v[6:7], v[8:11]\nv_mfma_f32_16x16x16_bf16 v[12:15], a[70:71], v[6:7], v[12:15]\nv_mfma_f32_16x16x16_bf16 v[16:19], v[78:79], v[6:7], v[16:19]\nv_mfma_f32_16x16x16_bf16 v[20:23], a[78:79], v[6:7], v[20:23]\ns_waitcnt vmcnt(28)\ns_barrier\ns_add_u32 m0, %[tb], 0\ns_add_u32 s44, %[wo], 4128768\n" \
"buffer_load_dwordx4 v92, %[rw], s44 offen nt lds\ns_add_u32 m0, %[tb], 1024\ns_add_u32 s44, %[wo], 4186112\nbuffer_load_dwordx4 v92, %[rw], s44 offen nt lds\ns_add_u32 m0, %[tb], 2048\ns_add_u32 s44, %[wo], 4243456\nbuffer_load_dwordx4 v92, %[rw], s44 offen nt lds\ns_add_u32 m0, %[tb], 3072\n" \
"s_add_u32 s44, %[wo], 4300800\nbuffer_load_dwordx4 v92, %[rw], s44 offen nt lds\nds_read_b64_tr_b16 a[80:81], v93 offset:4096\nds_read_b64_tr_b16 a[82:83], v93 offset:4608\nds_read_b64_tr_b16 a[88:89], v93 offset:4104\nds_read_b64_tr_b16 a[90:91], v93 offset:4616\nds_read_b64_tr_b16 a[96:97], v93 offset:16384\nds_read_b64_tr_b16 a[98:99], v93 offset:16896\n" \
"ds_read_b64_tr_b16 v[72:73], v93 offset:16392\nds_read_b64_tr_b16 v[74:75], v93 offset:16904\ns_waitcnt lgkmcnt(0)\nv_mfma_f32_16x16x16_bf16 v[8:11], a[80:81], v[6:7], v[8:11]\nv_mfma_f32_16x16x16_bf16 v[12:15], a[88:89], v[6:7], v[12:15]\nv_mfma_f32_16x16x16_bf16 v[16:19], a[96:97], v[6:7], v[16:19]\nv_mfma_f32_16x16x16_bf16 v[20:23], v[72:73], v[6:7], v[20:23]\nv_mfma_f32_16x16x16_bf16 v[8:11], a[82:83], v[6:7], v[8:11]\n" \
"v_mfma_f32_16x16x16_bf16 v[12:15], a[90:91], v[6:7], v[12:15]\nv_mfma_f32_16x16x16_bf16 v[16:19], a[98:99], v[6:7], v[16:19]\nv_mfma_f32_16x16x16_bf16 v[20:23], v[74:75], v[6:7], v[20:23]\ns_waitcnt vmcnt(28)\ns_barrier\ns_add_u32 m0, %[tb], 4096\ns_add_u32 s44, %[wo], 4358144\nbuffer_load_dwordx4 v92, %[rw], s44 offen nt lds\n" \
"s_add_u32 m0, %[tb], 5120\ns_add_u32 s44, %[wo], 4415488\nbuffer_load_dwordx4 v92, %[rw], s44 offen nt lds\ns_add_u32 m0, %[tb], 6144\ns_add_u32 s44, %[wo], 4472832\nbuffer_load_dwordx4 v92, %[rw], s44 offen nt lds\ns_add_u32 m0, %[tb], 7168\ns_add_u32 s44, %[wo], 4530176\n" \
"buffer_load_dwordx4 v92, %[rw], s44 offen nt lds\nds_read_b64_tr_b16 a[84:85], v93 offset:8192\nds_read_b64_tr_b16 a[86:87], v93 offset:8704\nds_read_b64_tr_b16 a[92:93], v93 offset:8200\nds_read_b64_tr_b16 a[94:95], v93 offset:8712\nds_read_b64_tr_b16 a[100:101], v93 offset:20480\nds_read_b64_tr_b16 a[102:103], v93 offset:20992\nds_read_b64_tr_b16 v[68:69], v93 offset:20488\n" \
"ds_read_b64_tr_b16 v[70:71], v93 offset:21000\ns_waitcnt lgkmcnt(0)\nv_mfma_f32_16x16x16_bf16 v[8:11], a[84:85], v[6:7], v[8:11]\nv_mfma_f32_16x16x16_bf16 v[12:15], a[92:93], v[6:7], v[12:15]\nv_mfma_f32_16x16x16_bf16 v[16:19], a[100:101], v[6:7], v[16:19]\nv_mfma_f32_16x16x16_bf16 v[20:23], v[68:69], v[6:7], v[20:23]\nv_mfma_f32_16x16x16_bf16 v[8:11], a[86:87], v[6:7], v[8:11]\nv_mfma_f32_16x16x16_bf16 v[12:15], a[94:95], v[6:7], v[12:15]\n" \
"v_mfma_f32_16x16x16_bf16 v[16:19], a[102:103], v[6:7], v[16:19]\nv_mfma_f32_16x16x16_bf16 v[20:23], v[70:71], v[6:7], v[20:23]\ns_waitcnt vmcnt(28)\ns_barrier\ns_add_u32 m0, %[tb], 8192\ns_add_u32 s44, %[wo], 4587520\nbuffer_load_dwordx4 v92, %[rw], s44 offen nt lds\ns_add_u32 m0, %[tb], 9216\n" \
"s_add_u32 s44, %[wo], 4644864\nbuffer_load_dwordx4 v92, %[rw], s44 offen nt lds\ns_add_u32 m0, %[tb], 10240\ns_add_u32 s44, %[wo], 4702208\nbuffer_load_dwordx4 v92, %[rw], s44 offen nt lds\ns_add_u32 m0, %[tb], 11264\ns_add_u32 s44, %[wo], 4759552\nbuffer_load_dwordx4 v92, %[rw], s44 offen nt lds\n" \
"ds_read_b64_tr_b16 a[136:137], v94 offset:0\nds_read_b64_tr_b16 a[138:139], v94 offset:512\nds_read_b64_tr_b16 v[64:65], v94 offset:8\nds_read_b64_tr_b16 v[66:67], v94 offset:520\nds_read_b64_tr_b16 a[144:145], v94 offset:24576\nds_read_b64_tr_b16 a[146:147], v94 offset:25088\nds_read_b64_tr_b16 a[152:153], v94 offset:24584\nds_read_b64_tr_b16 a[154:155], v94 offset:25096\n" \
"s_waitcnt lgkmcnt(0)\nv_mfma_f32_16x16x16_bf16 v[8:11], a[136:137], v[6:7], v[8:11]\nv_mfma_f32_16x16x16_bf16 v[12:15], v[64:65], v[6:7], v[12:15]\nv_mfma_f32_16x16x16_bf16 v[16:19], a[144:145], v[6:7], v[16:19]\nv_mfma_f32_16x16x16_bf16 v[20:23], a[152:153], v[6:7], v[20:23]\nv_mfma_f32_16x16x16_bf16 v[8:11], a[138:139], v[6:7], v[8:11]\nv_mfma_f32_16x16x16_bf16 v[12:15], v[66:67], v[6:7], v[12:15]\nv_mfma_f32_16x16x16_bf16 v[16:19], a[146:147], v[6:7], v[16:19]\n" \
"v_mfma_f32_16x16x16_bf16 v[20:23], a[154:155], v[6:7], v[20:23]\ns_waitcnt vmcnt(28)\ns_barrier\ns_add_u32 m0, %[lw], 0\ns_add_u32 s44, %[wo], 4816896\nbuffer_load_dwordx4 v92, %[rw], s44 offen nt lds\ns_add_u32 m0, %[lw], 1024\ns_add_u32 s44, %[wo], 4874240\n" \
"buffer_load_dwordx4 v92, %[rw], s44 offen nt lds\ns_add_u32 m0, %[lw], 2048\ns_add_u32 s44, %[wo], 4931584\nbuffer_load_dwordx4 v92, %[rw], s44 offen nt lds\ns_add_u32 m0, %[lw], 3072\ns_add_u32 s44, %[wo], 4988928\nbuffer_load_dwordx4 v92, %[rw], s44 offen nt lds\nds_read_b64_tr_b16 a[140:141], v94 offset:4096\n" \
"ds_read_b64_tr_b16 a[142:143], v94 offset:4608\nds_read_b64_tr_b16 v[60:61], v94 offset:4104\nds_read_b64_tr_b16 v[62:63], v94 offset:4616\nds_read_b64_tr_b16 a[148:149], v94 offset:28672\nds_read_b64_tr_b16 a[150:151], v94 offset:29184\nds_read_b64_tr_b16 a[156:157], v94 offset:28680\nds_read_b64_tr_b16 a[158:159], v94 offset:29192\ns_waitcnt lgkmcnt(0)\n" \
"v_mfma_f32_16x16x16_bf16 v[8:11], a[140:141], v[6:7], v[8:11]\nv_mfma_f32_16x16x16_bf16 v[12:15], v[60:61], v[6:7], v[12:15]\nv_mfma_f32_16x16x16_bf16 v[16:19], a[148:149], v[6:7], v[16:19]\nv_mfma_f32_16x16x16_bf16 v[20:23], a[156:157], v[6:7], v[20:23]\nv_mfma_f32_16x16x16_bf16 v[8:11], a[142:143], v[6:7], v[8:11]\nv_mfma_f32_16x16x16_bf16 v[12:15], v[62:63], v[6:7], v[12:15]\nv_mfma_f32_16x16x16_bf16 v[16:19], a[150:151], v[6:7], v[16:19]\nv_mfma_f32_16x16x16_bf16 v[20:23], a[158:159], v[6:7], v[20:23]\n" \
"s_waitcnt vmcnt(28)\ns_barrier\ns_add_u32 m0, %[lw], 4096\ns_add_u32 s44, %[wo], 5046272\nbuffer_load_dwordx4 v92, %[rw], s44 offen nt lds\ns_add_u32 m0, %[lw], 5120\ns_add_u32 s44, %[wo], 5103616\nbuffer_load_dwordx4 v92, %[rw], s44 offen nt lds\n" \
"s_add_u32 m0, %[lw], 6144\ns_add_u32 s44, %[wo], 5160960\nbuffer_load_dwordx4 v92, %[rw], s44 offen nt lds\ns_add_u32 m0, %[lw], 7168\ns_add_u32 s44, %[wo], 5218304\nbuffer_load_dwordx4 v92, %[rw], s44 offen nt lds\nds_read_b64_tr_b16 a[160:161], v94 offset:8192\nds_read_b64_tr_b16 a[162:163], v94 offset:8704\n" \
"ds_read_b64_tr_b16 a[168:169], v94 offset:8200\nds_read_b64_tr_b16 a[170:171], v94 offset:8712\nds_read_b64_tr_b16 v[56:57], v94 offset:32768\nds_read_b64_tr_b16 v[58:59], v94 offset:33280\nds_read_b64_tr_b16 a[176:177], v94 offset:32776\nds_read_b64_tr_b16 a[178:179], v94 offset:33288\ns_waitcnt lgkmcnt(0)\nv_mfma_f32_16x16x16_bf16 v[8:11], a[160:161], v[6:7], v[8:11]\n" \
"v_mfma_f32_16x16x16_bf16 v[12:15], a[168:169], v[6:7], v[12:15]\nv_mfma_f32_16x16x16_bf16 v[16:19], v[56:57], v[6:7], v[16:19]\nv_mfma_f32_16x16x16_bf16 v[20:23], a[176:177], v[6:7], v[20:23]\nv_mfma_f32_16x16x16_bf16 v[8:11], a[162:163], v[6:7], v[8:11]\nv_mfma_f32_16x16x16_bf16 v[12:15], a[170:171], v[6:7], v[12:15]\nv_mfma_f32_16x16x16_bf16 v[16:19], v[58:59], v[6:7], v[16:19]\nv_mfma_f32_16x16x16_bf16 v[20:23], a[178:179], v[6:7], v[20:23]\ns_waitcnt vmcnt(28)\n" \
"s_barrier\ns_add_u32 m0, %[lw], 8192\ns_add_u32 s44, %[wo], 5275648\nbuffer_load_dwordx4 v92, %[rw], s44 offen nt lds\ns_add_u32 m0, %[lw], 9216\ns_add_u32 s44, %[wo], 5332992\nbuffer_load_dwordx4 v92, %[rw], s44 offen nt lds\ns_add_u32 m0, %[lw], 10240\n" \
"s_add_u32 s44, %[wo], 5390336\nbuffer_load_dwordx4 v92, %[rw], s44 offen nt lds\ns_add_u32 m0, %[lw], 11264\ns_add_u32 s44, %[wo], 5447680\nbuffer_load_dwordx4 v92, %[rw], s44 offen nt lds\nds_read_b64_tr_b16 a[164:165], v94 offset:12288\nds_read_b64_tr_b16 a[166:167], v94 offset:12800\nds_read_b64_tr_b16 a[172:173], v94 offset:12296\n" \
"ds_read_b64_tr_b16 a[174:175], v94 offset:12808\nds_read_b64_tr_b16 v[52:53], v94 offset:36864\nds_read_b64_tr_b16 v[54:55], v94 offset:37376\nds_read_b64_tr_b16 a[180:181], v94 offset:36872\nds_read_b64_tr_b16 a[182:183], v94 offset:37384\ns_waitcnt lgkmcnt(0)\nv_mfma_f32_16x16x16_bf16 v[8:11], a[164:165], v[6:7], v[8:11]\nv_mfma_f32_16x16x16_bf16 v[12:15], a[172:173], v[6:7], v[12:15]\n" \
"v_mfma_f32_16x16x16_bf16 v[16:19], v[52:53], v[6:7], v[16:19]\nv_mfma_f32_16x16x16_bf16 v[20:23], a[180:181], v[6:7], v[20:23]\nv_mfma_f32_16x16x16_bf16 v[8:11], a[166:167], v[6:7], v[8:11]\nv_mfma_f32_16x16x16_bf16 v[12:15], a[174:175], v[6:7], v[12:15]\nv_mfma_f32_16x16x16_bf16 v[16:19], v[54:55], v[6:7], v[16:19]\nv_mfma_f32_16x16x16_bf16 v[20:23], a[182:183], v[6:7], v[20:23]\ns_waitcnt vmcnt(28)\ns_barrier\n" \
"s_add_u32 m0, %[lw], 12288\ns_add_u32 s44, %[wo], 5963776\nbuffer_load_dwordx4 v92, %[rw], s44 offen nt lds\ns_add_u32 m0, %[lw], 13312\ns_add_u32 s44, %[wo], 6021120\nbuffer_load_dwordx4 v92, %[rw], s44 offen nt lds\ns_add_u32 m0, %[lw], 14336\ns_add_u32 s44, %[wo], 6078464\n" \
"buffer_load_dwordx4 v92, %[rw], s44 offen nt lds\ns_add_u32 m0, %[lw], 15360\ns_add_u32 s44, %[wo], 6135808\nbuffer_load_dwordx4 v92, %[rw], s44 offen nt lds\nds_read_b64_tr_b16 a[184:185], v94 offset:16384\nds_read_b64_tr_b16 a[186:187], v94 offset:16896\nds_read_b64_tr_b16 a[192:193], v94 offset:16392\nds_read_b64_tr_b16 a[194:195], v94 offset:16904\n" \
"ds_read_b64_tr_b16 a[200:201], v94 offset:40960\nds_read_b64_tr_b16 a[202:203], v94 offset:41472\nds_read_b64_tr_b16 v[48:49], v94 offset:40968\nds_read_b64_tr_b16 v[50:51], v94 offset:41480\ns_waitcnt lgkmcnt(0)\nv_mfma_f32_16x16x16_bf16 v[8:11], a[184:185], v[6:7], v[8:11]\nv_mfma_f32_16x16x16_bf16 v[12:15], a[192:193], v[6:7], v[12:15]\nv_mfma_f32_16x16x16_bf16 v[16:19], a[200:201], v[6:7], v[16:19]\n" \
"v_mfma_f32_16x16x16_bf16 v[20:23], v[48:49], v[6:7], v[20:23]\nv_mfma_f32_16x16x16_bf16 v[8:11], a[186:187], v[6:7], v[8:11]\nv_mfma_f32_16x16x16_bf16 v[12:15], a[194:195], v[6:7], v[12:15]\nv_mfma_f32_16x16x16_bf16 v[16:19], a[202:203], v[6:7], v[16:19]\nv_mfma_f32_16x16x16_bf16 v[20:23], v[50:51], v[6:7], v[20:23]\ns_waitcnt vmcnt(28)\ns_barrier\ns_add_u32 m0, %[lw], 16384\n" \
"s_add_u32 s44, %[wo], 6193152\nbuffer_load_dwordx4 v92, %[rw], s44 offen nt lds\ns_add_u32 m0, %[lw], 17408\ns_add_u32 s44, %[wo], 6250496\nbuffer_load_dwordx4 v92, %[rw], s44 offen nt lds\ns_add_u32 m0, %[lw], 18432\ns_add_u32 s44, %[wo], 6307840\nbuffer_load_dwordx4 v92, %[rw], s44 offen nt lds\n" \
"s_add_u32 m0, %[lw], 19456\ns_add_u32 s44, %[wo], 6365184\nbuffer_load_dwordx4 v92, %[rw], s44 offen nt lds\nds_read_b64_tr_b16 a[188:189], v94 offset:20480\nds_read_b64_tr_b16 a[190:191], v94 offset:20992\nds_read_b64_tr_b16 a[196:197], v94 offset:20488\nds_read_b64_tr_b16 a[198:199], v94 offset:21000\nds_read_b64_tr_b16 a[204:205], v94 offset:45056\n" \
"ds_read_b64_tr_b16 a[206:207], v94 offset:45568\nds_read_b64_tr_b16 v[44:45], v94 offset:45064\nds_read_b64_tr_b16 v[46:47], v94 offset:45576\ns_waitcnt lgkmcnt(0)\nv_mfma_f32_16x16x16_bf16 v[8:11], a[188:189], v[6:7], v[8:11]\nv_mfma_f32_16x16x16_bf16 v[12:15], a[196:197], v[6:7], v[12:15]\nv_mfma_f32_16x16x16_bf16 v[16:19], a[204:205], v[6:7], v[16:19]\nv_mfma_f32_16x16x16_bf16 v[20:23], v[44:45], v[6:7], v[20:23]\n" \
"v_mfma_f32_16x16x16_bf16 v[8:11], a[190:191], v[6:7], v[8:11]\nv_mfma_f32_16x16x16_bf16 v[12:15], a[198:199], v[6:7], v[12:15]\nv_mfma_f32_16x16x16_bf16 v[16:19], a[206:207], v[6:7], v[16:19]\nv_mfma_f32_16x16x16_bf16 v[20:23], v[46:47], v[6:7], v[20:23]\ns_waitcnt vmcnt(28)\ns_barrier\ns_add_u32 m0, %[lw], 20480\ns_add_u32 s44, %[wo], 6422528\n" \
"buffer_load_dwordx4 v92, %[rw], s44 offen nt lds\ns_add_u32 m0, %[lw], 21504\ns_add_u32 s44, %[wo], 6479872\nbuffer_load_dwordx4 v92, %[rw], s44 offen nt lds\ns_add_u32 m0, %[lw], 22528\ns_add_u32 s44, %[wo], 6537216\nbuffer_load_dwordx4 v92, %[rw], s44 offen nt lds\ns_add_u32 m0, %[lw], 23552\n" \
"s_add_u32 s44, %[wo], 6594560\nbuffer_load_dwordx4 v92, %[rw], s44 offen nt lds\nds_read_b64_tr_b16 a[240:241], v93 offset:0\nds_read_b64_tr_b16 a[242:243], v93 offset:512\nds_read_b64_tr_b16 v[40:41], v93 offset:8\nds_read_b64_tr_b16 v[42:43], v93 offset:520\nds_read_b64_tr_b16 a[248:249], v93 offset:12288\nds_read_b64_tr_b16 a[250:251], v93 offset:12800\n" \
"ds_read_b64_tr_b16 v[96:97], v93 offset:12296\nds_read_b64_tr_b16 v[98:99], v93 offset:12808\ns_waitcnt lgkmcnt(0)\nv_mfma_f32_16x16x16_bf16 v[8:11], a[240:241], v[6:7], v[8:11]\nv_mfma_f32_16x16x16_bf16 v[12:15], v[40:41], v[6:7], v[12:15]\nv_mfma_f32_16x16x16_bf16 v[16:19], a[248:249], v[6:7], v[16:19]\nv_mfma_f32_16x16x16_bf16 v[20:23], v[96:97], v[6:7], v[20:23]\nv_mfma_f32_16x16x16_bf16 v[8:11], a[242:243], v[6:7], v[8:11]\n" \
"v_mfma_f32_16x16x16_bf16 v[12:15], v[42:43], v[6:7], v[12:15]\nv_mfma_f32_16x16x16_bf16 v[16:19], a[250:251], v[6:7], v[16:19]\nv_mfma_f32_16x16x16_bf16 v[20:23], v[98:99], v[6:7], v[20:23]\ns_waitcnt vmcnt(28)\ns_barrier\ns_add_u32 m0, %[tb], 0\ns_add_u32 s44, %[wo], 6651904\nbuffer_load_dwordx4 v92, %[rw], s44 offen nt lds\n" \
"s_add_u32 m0, %[tb], 1024\ns_add_u32 s44, %[wo], 6709248\nbuffer_load_dwordx4 v92, %[rw], s44 offen nt lds\ns_add_u32 m0, %[tb], 2048\ns_add_u32 s44, %[wo], 6766592\nbuffer_load_dwordx4 v92, %[rw], s44 offen nt lds\ns_add_u32 m0, %[tb], 3072\ns_add_u32 s44, %[wo], 6823936\n" \
"buffer_load_dwordx4 v92, %[rw], s44 offen nt lds\nds_read_b64_tr_b16 a[244:245], v93 offset:4096\nds_read_b64_tr_b16 a[246:247], v93 offset:4608\nds_read_b64_tr_b16 v[36:37], v93 offset:4104\nds_read_b64_tr_b16 v[38:39], v93 offset:4616\nds_read_b64_tr_b16 a[252:253], v93 offset:16384\nds_read_b64_tr_b16 a[254:255], v93 offset:16896\nds_read_b64_tr_b16 v[100:101], v93 offset:16392\n" \
"ds_read_b64_tr_b16 v[102:103], v93 offset:16904\ns_waitcnt lgkmcnt(0)\nv_mfma_f32_16x16x16_bf16 v[8:11], a[244:245], v[6:7], v[8:11]\nv_mfma_f32_16x16x16_bf16 v[12:15], v[36:37], v[6:7], v[12:15]\nv_mfma_f32_16x16x16_bf16 v[16:19], a[252:253], v[6:7], v[16:19]\nv_mfma_f32_16x16x16_bf16 v[20:23], v[100:101], v[6:7], v[20:23]\nv_mfma_f32_16x16x16_bf16 v[8:11], a[246:247], v[6:7], v[8:11]\nv_mfma_f32_16x16x16_bf16 v[12:15], v[38:39], v[6:7], v[12:15]\n" \
"v_mfma_f32_16x16x16_bf16 v[16:19], a[254:255], v[6:7], v[16:19]\nv_mfma_f32_16x16x16_bf16 v[20:23], v[102:103], v[6:7], v[20:23]\ns_waitcnt vmcnt(28)\ns_barrier\ns_add_u32 m0, %[tb], 4096\ns_add_u32 s44, %[wo], 6881280\nbuffer_load_dwordx4 v92, %[rw], s44 offen nt lds\ns_add_u32 m0, %[tb], 5120\n" \
"s_add_u32 s44, %[wo], 6938624\nbuffer_load_dwordx4 v92, %[rw], s44 offen nt lds\ns_add_u32 m0, %[tb], 6144\ns_add_u32 s44, %[wo], 6995968\nbuffer_load_dwordx4 v92, %[rw], s44 offen nt lds\ns_add_u32 m0, %[tb], 7168\ns_add_u32 s44, %[wo], 7053312\nbuffer_load_dwordx4 v92, %[rw], s44 offen nt lds\n" \
"ds_read_b64_tr_b16 v[104:105], v93 offset:8192\nds_read_b64_tr_b16 v[106:107], v93 offset:8704\nds_read_b64_tr_b16 v[112:113], v93 offset:8200\nds_read_b64_tr_b16 v[114:115], v93 offset:8712\nds_read_b64_tr_b16 v[32:33], v93 offset:20480\nds_read_b64_tr_b16 v[34:35], v93 offset:20992\nds_read_b64_tr_b16 v[120:121], v93 offset:20488\nds_read_b64_tr_b16 v[122:123], v93 offset:21000\n" \
"s_waitcnt lgkmcnt(0)\nv_mfma_f32_16x16x16_bf16 v[8:11], v[104:105], v[6:7], v[8:11]\nv_mfma_f32_16x16x16_bf16 v[12:15], v[112:113], v[6:7], v[12:15]\nv_mfma_f32_16x16x16_bf16 v[16:19], v[32:33], v[6:7], v[16:19]\nv_mfma_f32_16x16x16_bf16 v[20:23], v[120:121], v[6:7], v[20:23]\nv_mfma_f32_16x16x16_bf16 v[8:11], v[106:107], v[6:7], v[8:11]\nv_mfma_f32_16x16x16_bf16 v[12:15], v[114:115], v[6:7], v[12:15]\nv_mfma_f32_16x16x16_bf16 v[16:19], v[34:35], v[6:7], v[16:19]\n" \
"v_mfma_f32_16x16x16_bf16 v[20:23], v[122:123], v[6:7], v[20:23]\ns_waitcnt vmcnt(28)\ns_barrier\ns_add_u32 m0, %[tb], 8192\ns_add_u32 s44, %[wo], 7110656\nbuffer_load_dwordx4 v92, %[rw], s44 offen nt lds\ns_add_u32 m0, %[tb], 9216\ns_add_u32 s44, %[wo], 7168000\n" \
"buffer_load_dwordx4 v92, %[rw], s44 offen nt lds\ns_add_u32 m0, %[tb], 10240\ns_add_u32 s44, %[wo], 7225344\nbuffer_load_dwordx4 v92, %[rw], s44 offen nt lds\ns_add_u32 m0, %[tb], 11264\ns_add_u32 s44, %[wo], 7282688\nbuffer_load_dwordx4 v92, %[rw], s44 offen nt lds\nds_read_b64_tr_b16 v[108:109], v94 offset:0\n" \
"ds_read_b64_tr_b16 v[110:111], v94 offset:512\nds_read_b64_tr_b16 v[116:117], v94 offset:8\nds_read_b64_tr_b16 v[118:119], v94 offset:520\nds_read_b64_tr_b16 v[28:29], v94 offset:24576\nds_read_b64_tr_b16 v[30:31], v94 offset:25088\nds_read_b64_tr_b16 v[124:125], v94 offset:24584\nds_read_b64_tr_b16 v[126:127], v94 offset:25096\ns_waitcnt lgkmcnt(0)\n" \
"v_mfma_f32_16x16x16_bf16 v[8:11], v[108:109], v[6:7], v[8:11]\nv_mfma_f32_16x16x16_bf16 v[12:15], v[116:117], v[6:7], v[12:15]\nv_mfma_f32_16x16x16_bf16 v[16:19], v[28:29], v[6:7], v[16:19]\nv_mfma_f32_16x16x16_bf16 v[20:23], v[124:125], v[6:7], v[20:23]\nv_mfma_f32_16x16x16_bf16 v[8:11], v[110:111], v[6:7], v[8:11]\nv_mfma_f32_16x16x16_bf16 v[12:15], v[118:119], v[6:7], v[12:15]\nv_mfma_f32_16x16x16_bf16 v[16:19], v[30:31], v[6:7], v[16:19]\nv_mfma_f32_16x16x16_bf16 v[20:23], v[126:127], v[6:7], v[20:23]\n" \
"s_waitcnt vmcnt(28)\ns_barrier\nds_read_b64_tr_b16 v[128:129], v94 offset:4096\nds_read_b64_tr_b16 v[130:131], v94 offset:4608\nds_read_b64_tr_b16 v[136:137], v94 offset:4104\nds_read_b64_tr_b16 v[138:139], v94 offset:4616\nds_read_b64_tr_b16 v[144:145], v94 offset:28672\nds_read_b64_tr_b16 v[146:147], v94 offset:29184\n" \
"ds_read_b64_tr_b16 v[24:25], v94 offset:28680\nds_read_b64_tr_b16 v[26:27], v94 offset:29192\ns_waitcnt lgkmcnt(0)\nv_mfma_f32_16x16x16_bf16 v[8:11], v[128:129], v[6:7], v[8:11]\nv_mfma_f32_16x16x16_bf16 v[12:15], v[136:137], v[6:7], v[12:15]\nv_mfma_f32_16x16x16_bf16 v[16:19], v[144:145], v[6:7], v[16:19]\nv_mfma_f32_16x16x16_bf16 v[20:23], v[24:25], v[6:7], v[20:23]\nv_mfma_f32_16x16x16_bf16 v[8:11], v[130:131], v[6:7], v[8:11]\n" \
"v_mfma_f32_16x16x16_bf16 v[12:15], v[138:139], v[6:7], v[12:15]\nv_mfma_f32_16x16x16_bf16 v[16:19], v[146:147], v[6:7], v[16:19]\nv_mfma_f32_16x16x16_bf16 v[20:23], v[26:27], v[6:7], v[20:23]\ns_waitcnt vmcnt(24)\nds_write_b128 v1, v[88:91] offset:0\nds_write_b128 v1, v[84:87] offset:1024\nds_write_b128 v1, v[80:83] offset:2048\nds_write_b128 v1, v[76:79] offset:3072\n" \
"s_barrier\nds_read_b64_tr_b16 v[132:133], v94 offset:8192\nds_read_b64_tr_b16 v[134:135], v94 offset:8704\nds_read_b64_tr_b16 v[140:141], v94 offset:8200\nds_read_b64_tr_b16 v[142:143], v94 offset:8712\nds_read_b64_tr_b16 v[148:149], v94 offset:32768\nds_read_b64_tr_b16 v[150:151], v94 offset:33280\nds_read_b64_tr_b16 v[76:77], v94 offset:32776\n" \
"ds_read_b64_tr_b16 v[78:79], v94 offset:33288\ns_waitcnt lgkmcnt(0)\nv_mfma_f32_16x16x16_bf16 v[8:11], v[132:133], v[6:7], v[8:11]\nv_mfma_f32_16x16x16_bf16 v[12:15], v[140:141], v[6:7], v[12:15]\nv_mfma_f32_16x16x16_bf16 v[16:19], v[148:149], v[6:7], v[16:19]\nv_mfma_f32_16x16x16_bf16 v[20:23], v[76:77], v[6:7], v[20:23]\nv_mfma_f32_16x16x16_bf16 v[8:11], v[134:135], v[6:7], v[8:11]\nv_mfma_f32_16x16x16_bf16 v[12:15], v[142:143], v[6:7], v[12:15]\n" \
"v_mfma_f32_16x16x16_bf16 v[16:19], v[150:151], v[6:7], v[16:19]\nv_mfma_f32_16x16x16_bf16 v[20:23], v[78:79], v[6:7], v[20:23]\ns_waitcnt vmcnt(20)\nds_write_b128 v1, v[72:75] offset:4096\nds_write_b128 v1, v[68:71] offset:5120\nds_write_b128 v1, v[64:67] offset:6144\nds_write_b128 v1, v[60:63] offset:7168\ns_barrier\n" \
"ds_read_b64_tr_b16 v[184:185], v94 offset:12288\nds_read_b64_tr_b16 v[186:187], v94 offset:12800\nds_read_b64_tr_b16 v[60:61], v94 offset:12296\nds_read_b64_tr_b16 v[62:63], v94 offset:12808\nds_read_b64_tr_b16 v[192:193], v94 offset:36864\nds_read_b64_tr_b16 v[194:195], v94 offset:37376\nds_read_b64_tr_b16 v[200:201], v94 offset:36872\nds_read_b64_tr_b16 v[202:203], v94 offset:37384\n" \
"s_waitcnt lgkmcnt(0)\nv_mfma_f32_16x16x16_bf16 v[8:11], v[184:185], v[6:7], v[8:11]\nv_mfma_f32_16x16x16_bf16 v[12:15], v[60:61], v[6:7], v[12:15]\nv_mfma_f32_16x16x16_bf16 v[16:19], v[192:193], v[6:7], v[16:19]\nv_mfma_f32_16x16x16_bf16 v[20:23], v[200:201], v[6:7], v[20:23]\nv_mfma_f32_16x16x16_bf16 v[8:11], v[186:187], v[6:7], v[8:11]\nv_mfma_f32_16x16x16_bf16 v[12:15], v[62:63], v[6:7], v[12:15]\nv_mfma_f32_16x16x16_bf16 v[16:19], v[194:195], v[6:7], v[16:19]\n" \
"v_mfma_f32_16x16x16_bf16 v[20:23], v[202:203], v[6:7], v[20:23]\ns_waitcnt vmcnt(16)\nds_write_b128 v1, v[56:59] offset:8192\nds_write_b128 v1, v[52:55] offset:9216\nds_write_b128 v1, v[48:51] offset:10240\nds_write_b128 v1, v[44:47] offset:11264\ns_barrier\nds_read_b64_tr_b16 v[188:189], v94 offset:16384\n" \
"ds_read_b64_tr_b16 v[190:191], v94 offset:16896\nds_read_b64_tr_b16 v[44:45], v94 offset:16392\nds_read_b64_tr_b16 v[46:47], v94 offset:16904\nds_read_b64_tr_b16 v[196:197], v94 offset:40960\nds_read_b64_tr_b16 v[198:199], v94 offset:41472\nds_read_b64_tr_b16 v[204:205], v94 offset:40968\nds_read_b64_tr_b16 v[206:207], v94 offset:41480\ns_waitcnt lgkmcnt(0)\n" \
"v_mfma_f32_16x16x16_bf16 v[8:11], v[188:189], v[6:7], v[8:11]\nv_mfma_f32_16x16x16_bf16 v[12:15], v[44:45], v[6:7], v[12:15]\nv_mfma_f32_16x16x16_bf16 v[16:19], v[196:197], v[6:7], v[16:19]\nv_mfma_f32_16x16x16_bf16 v[20:23], v[204:205], v[6:7], v[20:23]\nv_mfma_f32_16x16x16_bf16 v[8:11], v[190:191], v[6:7], v[8:11]\nv_mfma_f32_16x16x16_bf16 v[12:15], v[46:47], v[6:7], v[12:15]\nv_mfma_f32_16x16x16_bf16 v[16:19], v[198:199], v[6:7], v[16:19]\nv_mfma_f32_16x16x16_bf16 v[20:23], v[206:207], v[6:7], v[20:23]\n" \
"s_waitcnt vmcnt(12)\nds_write_b128 v1, v[40:43] offset:12288\nds_write_b128 v1, v[36:39] offset:13312\nds_write_b128 v1, v[32:35] offset:14336\nds_write_b128 v1, v[28:31] offset:15360\ns_barrier\nds_read_b64_tr_b16 v[208:209], v94 offset:20480\nds_read_b64_tr_b16 v[210:211], v94 offset:20992\n" \
"ds_read_b64_tr_b16 v[216:217], v94 offset:20488\nds_read_b64_tr_b16 v[218:219], v94 offset:21000\nds_read_b64_tr_b16 v[28:29], v94 offset:45056\nds_read_b64_tr_b16 v[30:31], v94 offset:45568\nds_read_b64_tr_b16 v[224:225], v94 offset:45064\nds_read_b64_tr_b16 v[226:227], v94 offset:45576\ns_waitcnt lgkmcnt(0)\nv_mfma_f32_16x16x16_bf16 v[8:11], v[208:209], v[6:7], v[8:11]\n" \
"v_mfma_f32_16x16x16_bf16 v[12:15], v[216:217], v[6:7], v[12:15]\nv_mfma_f32_16x16x16_bf16 v[16:19], v[28:29], v[6:7], v[16:19]\nv_mfma_f32_16x16x16_bf16 v[20:23], v[224:225], v[6:7], v[20:23]\nv_mfma_f32_16x16x16_bf16 v[8:11], v[210:211], v[6:7], v[8:11]\nv_mfma_f32_16x16x16_bf16 v[12:15], v[218:219], v[6:7], v[12:15]\nv_mfma_f32_16x16x16_bf16 v[16:19], v[30:31], v[6:7], v[16:19]\nv_mfma_f32_16x16x16_bf16 v[20:23], v[226:227], v[6:7], v[20:23]\ns_waitcnt vmcnt(8)\n" \
"ds_write_b128 v1, v[24:27] offset:16384\nds_write_b128 v1, v[76:79] offset:17408\nds_write_b128 v1, v[60:63] offset:18432\nds_write_b128 v1, v[44:47] offset:19456\ns_barrier\nds_read_b64_tr_b16 v[212:213], v93 offset:0\nds_read_b64_tr_b16 v[214:215], v93 offset:512\nds_read_b64_tr_b16 v[220:221], v93 offset:8\n" \
"ds_read_b64_tr_b16 v[222:223], v93 offset:520\nds_read_b64_tr_b16 v[44:45], v93 offset:12288\nds_read_b64_tr_b16 v[46:47], v93 offset:12800\nds_read_b64_tr_b16 v[228:229], v93 offset:12296\nds_read_b64_tr_b16 v[230:231], v93 offset:12808\ns_waitcnt lgkmcnt(0)\nv_mfma_f32_16x16x16_bf16 v[8:11], v[212:213], v[6:7], v[8:11]\nv_mfma_f32_16x16x16_bf16 v[12:15], v[220:221], v[6:7], v[12:15]\n" \
"v_mfma_f32_16x16x16_bf16 v[16:19], v[44:45], v[6:7], v[16:19]\nv_mfma_f32_16x16x16_bf16 v[20:23], v[228:229], v[6:7], v[20:23]\nv_mfma_f32_16x16x16_bf16 v[8:11], v[214:215], v[6:7], v[8:11]\nv_mfma_f32_16x16x16_bf16 v[12:15], v[222:223], v[6:7], v[12:15]\nv_mfma_f32_16x16x16_bf16 v[16:19], v[46:47], v[6:7], v[16:19]\nv_mfma_f32_16x16x16_bf16 v[20:23], v[230:231], v[6:7], v[20:23]\ns_waitcnt vmcnt(4)\nds_write_b128 v1, v[28:31] offset:20480\n" \
"ds_write_b128 v1, v[44:47] offset:21504\ns_barrier\nds_read_b64_tr_b16 v[232:233], v93 offset:4096\nds_read_b64_tr_b16 v[234:235], v93 offset:4608\nds_read_b64_tr_b16 v[240:241], v93 offset:4104\nds_read_b64_tr_b16 v[242:243], v93 offset:4616\nds_read_b64_tr_b16 v[248:249], v93 offset:16384\nds_read_b64_tr_b16 v[250:251], v93 offset:16896\n" \
"ds_read_b64_tr_b16 v[44:45], v93 offset:16392\nds_read_b64_tr_b16 v[46:47], v93 offset:16904\ns_waitcnt lgkmcnt(0)\nv_mfma_f32_16x16x16_bf16 v[8:11], v[232:233], v[6:7], v[8:11]\nv_mfma_f32_16x16x16_bf16 v[12:15], v[240:241], v[6:7], v[12:15]\nv_mfma_f32_16x16x16_bf16 v[16:19], v[248:249], v[6:7], v[16:19]\nv_mfma_f32_16x16x16_bf16 v[20:23], v[44:45], v[6:7], v[20:23]\nv_mfma_f32_16x16x16_bf16 v[8:11], v[234:235], v[6:7], v[8:11]\n" \
"v_mfma_f32_16x16x16_bf16 v[12:15], v[242:243], v[6:7], v[12:15]\nv_mfma_f32_16x16x16_bf16 v[16:19], v[250:251], v[6:7], v[16:19]\nv_mfma_f32_16x16x16_bf16 v[20:23], v[46:47], v[6:7], v[20:23]\ns_waitcnt vmcnt(0)\nds_write_b128 v1, v[44:47] offset:22528\ns_barrier\nds_read_b64_tr_b16 v[236:237], v93 offset:8192\nds_read_b64_tr_b16 v[238:239], v93 offset:8704\n" \
"ds_read_b64_tr_b16 v[244:245], v93 offset:8200\nds_read_b64_tr_b16 v[246:247], v93 offset:8712\nds_read_b64_tr_b16 v[252:253], v93 offset:20480\nds_read_b64_tr_b16 v[254:255], v93 offset:20992\nds_read_b64_tr_b16 v[44:45], v93 offset:20488\nds_read_b64_tr_b16 v[46:47], v93 offset:21000\ns_waitcnt lgkmcnt(0)\nv_mfma_f32_16x16x16_bf16 v[8:11], v[236:237], v[6:7], v[8:11]\n" \
"v_mfma_f32_16x16x16_bf16 v[12:15], v[244:245], v[6:7], v[12:15]\nv_mfma_f32_16x16x16_bf16 v[16:19], v[252:253], v[6:7], v[16:19]\nv_mfma_f32_16x16x16_bf16 v[20:23], v[44:45], v[6:7], v[20:23]\nv_mfma_f32_16x16x16_bf16 v[8:11], v[238:239], v[6:7], v[8:11]\nv_mfma_f32_16x16x16_bf16 v[12:15], v[246:247], v[6:7], v[12:15]\nv_mfma_f32_16x16x16_bf16 v[16:19], v[254:255], v[6:7], v[16:19]\nv_mfma_f32_16x16x16_bf16 v[20:23], v[46:47], v[6:7], v[20:23]\nds_write_b128 v1, v[44:47] offset:23552\n" \
"s_waitcnt lgkmcnt(0)\ns_barrier\ns_mov_b32 s86, %[xn0]\ns_mov_b32 s87, %[ho0]\ns_mov_b32 s88, %[fh0]\ns_mov_b32 s89, %[b1]\ns_mov_b32 s40, %[b0]\ns_mov_b32 s43, 0\n" \
"s_lshl_b32 s57, %[w], 12\ns_mov_b32 s58, 0xbeeff558\ns_mov_b32 s59, 0x409d30fc\ns_mov_b32 s56, -2147483648\n.p2align 6\n.Lgg_next%=:\ns_cmp_lg_u32 %[w], 0\ns_cbranch_scc1 .Lgg_w1%=\n" \
"buffer_load_dword v56, v3, %[rs], 0 offen sc1\ns_waitcnt vmcnt(0)\ns_barrier\nbuffer_store_dword v90, off, %[rs], s56 sc1\ns_mov_b32 s56, -2147483648\n.Lgg_poll%=:\nv_cmp_gt_i32 vcc, s40, v56\ns_cbranch_vccz .Lgg_go%=\n" \
"s_cmp_ge_u32 s40, 62\ns_cbranch_scc1 .Lgg_ns1055%=\ns_sleep 6\n.Lgg_ns1055%=:\ns_sleep 1\nbuffer_load_dword v56, v3, %[rs], 0 offen sc1\ns_waitcnt vmcnt(0)\ns_add_u32 s43, s43, 1\n" \
"s_cmp_lt_u32 s43, 262144\ns_cbranch_scc1 .Lgg_poll%=\ns_branch .Lgg_abort%=\n.p2align 6\n.Lgg_w1%=:\ns_waitcnt vmcnt(0)\ns_barrier\n.p2align 6\n" \
".Lgg_go%=:\ns_add_u32 s44, s40, 0\ns_lshl_b32 s44, s44, 17\ns_add_u32 s44, s44, s86\ns_lshr_b32 s45, s44, 8\ns_and_b32 s45, s45, 0x2000\ns_xor_b32 s41, s44, s45\ns_add_u32 s61, s41, %[xw]\n" \
"s_barrier\ns_add_u32 m0, s57, 0\ns_add_u32 s60, s61, 0\nbuffer_load_dwordx4 v0, %[rs], s60 offen offset:0 sc1 lds\nbuffer_load_dwordx4 v0, %[rs], s60 offen offset:1024 sc1 lds\nbuffer_load_dwordx4 v0, %[rs], s60 offen offset:2048 sc1 lds\nbuffer_load_dwordx4 v0, %[rs], s60 offen offset:3072 sc1 lds\ns_add_u32 m0, s57, 16384\n" \
"s_add_u32 s60, s61, 32768\nbuffer_load_dwordx4 v0, %[rs], s60 offen offset:0 sc1 lds\nbuffer_load_dwordx4 v0, %[rs], s60 offen offset:1024 sc1 lds\nbuffer_load_dwordx4 v0, %[rs], s60 offen offset:2048 sc1 lds\nbuffer_load_dwordx4 v0, %[rs], s60 offen offset:3072 sc1 lds\nbuffer_load_dwordx4 v[40:43], v5, %[rs], s41 offen offset:0 sc1\nbuffer_load_dwordx4 v[44:47], v5, %[rs], s41 offen offset:256 sc1\ns_lshl_b32 s44, s40, 19\n" \
"s_add_u32 s44, s44, s87\ns_lshr_b32 s45, s44, 8\ns_and_b32 s45, s45, 0x2000\ns_xor_b32 s55, s44, s45\ns_waitcnt vmcnt(6)\ns_barrier\nds_read_b128 v[56:59], v2 offset:0\nds_read_b128 v[60:63], v2 offset:2048\n" \
"ds_read_b128 v[64:67], v2 offset:4096\ns_waitcnt lgkmcnt(2)\nv_mfma_f32_16x16x32_bf16 v[24:27], a[0:3], v[56:59], 0\nds_read_b128 v[68:71], v2 offset:6144\nv_mfma_f32_16x16x32_bf16 v[28:31], a[8:11], v[56:59], 0\nds_read_b128 v[72:75], v1 offset:0\nbuffer_load_dwordx4 v[48:51], v5, %[rs], s41 offen offset:512 sc1\nv_mfma_f32_16x16x32_bf16 v[32:35], a[16:19], v[56:59], 0\n" \
"ds_read_b128 v[76:79], v1 offset:1024\nv_mfma_f32_16x16x32_bf16 v[36:39], a[24:27], v[56:59], 0\ns_waitcnt lgkmcnt(4)\nv_mfma_f32_16x16x32_bf16 v[24:27], a[4:7], v[60:63], v[24:27]\nds_read_b128 v[56:59], v2 offset:8192\nv_mfma_f32_16x16x32_bf16 v[28:31], a[12:15], v[60:63], v[28:31]\nds_read_b128 v[80:83], v1 offset:2048\nbuffer_load_dwordx4 v[52:55], v5, %[rs], s41 offen offset:768 sc1\n" \
"v_mfma_f32_16x16x32_bf16 v[32:35], a[20:23], v[60:63], v[32:35]\nv_mfma_f32_16x16x32_bf16 v[36:39], a[28:31], v[60:63], v[36:39]\ns_waitcnt lgkmcnt(5)\nv_mfma_f32_16x16x32_bf16 v[24:27], a[32:35], v[64:67], v[24:27]\ns_waitcnt lgkmcnt(3)\ns_add_u32 m0, s57, 32768\nds_read_b128 v[60:63], v2 offset:10240\nv_mfma_f32_16x16x32_bf16 v[28:31], v[72:75], v[64:67], v[28:31]\n" \
"s_add_u32 s60, s61, 65536\nds_read_b128 v[72:75], v1 offset:3072\nbuffer_load_dwordx4 v0, %[rs], s60 offen offset:0 sc1 lds\nv_mfma_f32_16x16x32_bf16 v[32:35], a[40:43], v[64:67], v[32:35]\nv_mfma_f32_16x16x32_bf16 v[36:39], a[48:51], v[64:67], v[36:39]\nv_mfma_f32_16x16x32_bf16 v[24:27], a[36:39], v[68:71], v[24:27]\ns_waitcnt lgkmcnt(4)\nds_read_b128 v[64:67], v2 offset:12288\n" \
"v_mfma_f32_16x16x32_bf16 v[28:31], v[76:79], v[68:71], v[28:31]\nbuffer_load_dwordx4 v0, %[rs], s60 offen offset:1024 sc1 lds\nv_mfma_f32_16x16x32_bf16 v[32:35], a[44:47], v[68:71], v[32:35]\nv_mfma_f32_16x16x32_bf16 v[36:39], a[52:55], v[68:71], v[36:39]\ns_waitcnt lgkmcnt(4)\nds_read_b128 v[76:79], v1 offset:4096\nv_mfma_f32_16x16x32_bf16 v[24:27], a[56:59], v[56:59], v[24:27]\nds_read_b128 v[68:71], v2 offset:14336\n" \
"v_mfma_f32_16x16x32_bf16 v[28:31], a[64:67], v[56:59], v[28:31]\ns_waitcnt lgkmcnt(5)\nbuffer_load_dwordx4 v0, %[rs], s60 offen offset:2048 sc1 lds\nv_mfma_f32_16x16x32_bf16 v[32:35], v[80:83], v[56:59], v[32:35]\nv_mfma_f32_16x16x32_bf16 v[36:39], a[72:75], v[56:59], v[36:39]\ns_waitcnt lgkmcnt(4)\nds_read_b128 v[80:83], v1 offset:5120\nv_mfma_f32_16x16x32_bf16 v[24:27], a[60:63], v[60:63], v[24:27]\n" \
"s_waitcnt vmcnt(7)\ns_barrier\nds_read_b128 v[56:59], v2 offset:16384\nv_mfma_f32_16x16x32_bf16 v[28:31], a[68:71], v[60:63], v[28:31]\ns_waitcnt lgkmcnt(5)\nbuffer_load_dwordx4 v0, %[rs], s60 offen offset:3072 sc1 lds\nv_mfma_f32_16x16x32_bf16 v[32:35], v[72:75], v[60:63], v[32:35]\nv_mfma_f32_16x16x32_bf16 v[36:39], a[76:79], v[60:63], v[36:39]\n" \
"s_waitcnt lgkmcnt(4)\nv_mfma_f32_16x16x32_bf16 v[24:27], a[80:83], v[64:67], v[24:27]\ns_add_u32 m0, s57, 49152\nds_read_b128 v[60:63], v2 offset:18432\nv_mfma_f32_16x16x32_bf16 v[28:31], a[88:91], v[64:67], v[28:31]\ns_add_u32 s60, s61, 98304\nbuffer_load_dwordx4 v0, %[rs], s60 offen offset:0 sc1 lds\nv_mfma_f32_16x16x32_bf16 v[32:35], a[96:99], v[64:67], v[32:35]\n" \
"s_waitcnt lgkmcnt(4)\nv_mfma_f32_16x16x32_bf16 v[36:39], v[76:79], v[64:67], v[36:39]\ns_waitcnt lgkmcnt(3)\nv_mfma_f32_16x16x32_bf16 v[24:27], a[84:87], v[68:71], v[24:27]\nds_read_b128 v[64:67], v2 offset:20480\nv_mfma_f32_16x16x32_bf16 v[28:31], a[92:95], v[68:71], v[28:31]\nds_read_b128 v[72:75], v1 offset:6144\nbuffer_load_dwordx4 v0, %[rs], s60 offen offset:1024 sc1 lds\n" \
"v_mfma_f32_16x16x32_bf16 v[32:35], a[100:103], v[68:71], v[32:35]\ns_waitcnt lgkmcnt(4)\nv_mfma_f32_16x16x32_bf16 v[36:39], v[80:83], v[68:71], v[36:39]\ns_waitcnt lgkmcnt(3)\nv_mfma_f32_16x16x32_bf16 v[24:27], a[104:107], v[56:59], v[24:27]\nds_read_b128 v[68:71], v2 offset:22528\nv_mfma_f32_16x16x32_bf16 v[28:31], a[112:115], v[56:59], v[28:31]\nds_read_b128 v[76:79], v1 offset:7168\n" \
"buffer_load_dwordx4 v0, %[rs], s60 offen offset:2048 sc1 lds\nv_mfma_f32_16x16x32_bf16 v[32:35], a[120:123], v[56:59], v[32:35]\nv_mfma_f32_16x16x32_bf16 v[36:39], a[128:131], v[56:59], v[36:39]\ns_waitcnt lgkmcnt(4)\nv_mfma_f32_16x16x32_bf16 v[24:27], a[108:111], v[60:63], v[24:27]\nds_read_b128 v[56:59], v2 offset:24576\nv_mfma_f32_16x16x32_bf16 v[28:31], a[116:119], v[60:63], v[28:31]\nds_read_b128 v[80:83], v1 offset:8192\n" \
"buffer_load_dwordx4 v0, %[rs], s60 offen offset:3072 sc1 lds\nv_mfma_f32_16x16x32_bf16 v[32:35], a[124:127], v[60:63], v[32:35]\nv_mfma_f32_16x16x32_bf16 v[36:39], a[132:135], v[60:63], v[36:39]\ns_waitcnt lgkmcnt(5)\nv_mfma_f32_16x16x32_bf16 v[24:27], a[136:139], v[64:67], v[24:27]\ns_waitcnt vmcnt(8)\ns_waitcnt lgkmcnt(4)\nds_read_b128 v[60:63], v2 offset:26624\n" \
"v_add_f32 v84, v40, v44\nv_mfma_f32_16x16x32_bf16 v[28:31], v[72:75], v[64:67], v[28:31]\nds_read_b128 v[72:75], v1 offset:9216\nv_add_f32 v85, v48, v52\nv_mfma_f32_16x16x32_bf16 v[32:35], a[144:147], v[64:67], v[32:35]\nv_add_f32 v84, v84, v85\nv_mov_b32 v85, v84\nv_mfma_f32_16x16x32_bf16 v[36:39], a[152:155], v[64:67], v[36:39]\n" \
"s_waitcnt lgkmcnt(5)\nv_permlane16_swap_b32 v84, v85\nv_mfma_f32_16x16x32_bf16 v[24:27], a[140:143], v[68:71], v[24:27]\ns_waitcnt lgkmcnt(4)\nds_read_b128 v[64:67], v2 offset:28672\nv_add_f32 v84, v84, v85\nv_mfma_f32_16x16x32_bf16 v[28:31], v[76:79], v[68:71], v[28:31]\nv_mov_b32 v85, v84\n" \
"v_mfma_f32_16x16x32_bf16 v[32:35], a[148:151], v[68:71], v[32:35]\ns_nop 0\nv_permlane32_swap_b32 v84, v85\nv_mfma_f32_16x16x32_bf16 v[36:39], a[156:159], v[68:71], v[36:39]\ns_waitcnt lgkmcnt(4)\nds_read_b128 v[76:79], v1 offset:10240\nv_add_f32 v84, v84, v85\nv_mfma_f32_16x16x32_bf16 v[24:27], a[160:163], v[56:59], v[24:27]\n" \
"ds_read_b128 v[68:71], v2 offset:30720\nv_mul_f32 v84, 0xbd800000, v84\nv_mfma_f32_16x16x32_bf16 v[28:31], a[168:171], v[56:59], v[28:31]\ns_waitcnt lgkmcnt(5)\nv_add_f32 v40, v40, v84\nv_mul_f32 v40, 0x41000000, v40\nv_mfma_f32_16x16x32_bf16 v[32:35], v[80:83], v[56:59], v[32:35]\nv_fma_f32 v41, v40, v40, v41\n" \
"v_add_f32 v44, v44, v84\nv_mfma_f32_16x16x32_bf16 v[36:39], a[176:179], v[56:59], v[36:39]\ns_waitcnt lgkmcnt(4)\nds_read_b128 v[80:83], v1 offset:11264\nv_mul_f32 v44, 0x41000000, v44\nv_mfma_f32_16x16x32_bf16 v[24:27], a[164:167], v[60:63], v[24:27]\ns_waitcnt vmcnt(4)\ns_barrier\n" \
"ds_read_b128 v[56:59], v2 offset:32768\nv_mfma_f32_16x16x32_bf16 v[28:31], a[172:175], v[60:63], v[28:31]\ns_waitcnt lgkmcnt(5)\nv_fma_f32 v45, v44, v44, v45\nv_add_f32 v48, v48, v84\nv_mfma_f32_16x16x32_bf16 v[32:35], v[72:75], v[60:63], v[32:35]\nv_mul_f32 v48, 0x41000000, v48\nv_fma_f32 v49, v48, v48, v49\n" \
"v_mfma_f32_16x16x32_bf16 v[36:39], a[180:183], v[60:63], v[36:39]\ns_waitcnt lgkmcnt(4)\nv_add_f32 v52, v52, v84\nv_mul_f32 v52, 0x41000000, v52\nv_mfma_f32_16x16x32_bf16 v[24:27], a[184:187], v[64:67], v[24:27]\nds_read_b128 v[60:63], v2 offset:34816\nv_fma_f32 v53, v52, v52, v53\nv_mfma_f32_16x16x32_bf16 v[28:31], a[192:195], v[64:67], v[28:31]\n" \
"v_add_f32 v85, v41, v45\nv_add_f32 v86, v49, v53\nv_mfma_f32_16x16x32_bf16 v[32:35], a[200:203], v[64:67], v[32:35]\ns_waitcnt lgkmcnt(4)\nv_add_f32 v85, v85, v86\nv_mov_b32 v86, v85\nv_mfma_f32_16x16x32_bf16 v[36:39], v[76:79], v[64:67], v[36:39]\ns_waitcnt lgkmcnt(3)\n" \
"v_permlane16_swap_b32 v85, v86\nv_mfma_f32_16x16x32_bf16 v[24:27], a[188:191], v[68:71], v[24:27]\nds_read_b128 v[64:67], v2 offset:36864\nv_add_f32 v85, v85, v86\nv_mfma_f32_16x16x32_bf16 v[28:31], a[196:199], v[68:71], v[28:31]\nds_read_b128 v[72:75], v1 offset:12288\nv_mov_b32 v86, v85\nv_mfma_f32_16x16x32_bf16 v[32:35], a[204:207], v[68:71], v[32:35]\n" \
"s_waitcnt lgkmcnt(4)\nv_permlane32_swap_b32 v85, v86\nv_mfma_f32_16x16x32_bf16 v[36:39], v[80:83], v[68:71], v[36:39]\ns_waitcnt lgkmcnt(3)\nv_add_f32 v85, v85, v86\nv_mul_f32 v85, 0x3a800000, v85\nv_mfma_f32_16x16x32_bf16 v[24:27], a[208:211], v[56:59], v[24:27]\nds_read_b128 v[68:71], v2 offset:38912\n" \
"v_cvt_pk_bf16_f32 v85, v85, v85\nv_mfma_f32_16x16x32_bf16 v[28:31], a[216:219], v[56:59], v[28:31]\nds_read_b128 v[76:79], v1 offset:13312\nv_and_b32 v85, 0xffff0000, v85\nv_mfma_f32_16x16x32_bf16 v[32:35], a[224:227], v[56:59], v[32:35]\nv_add_f32 v85, 0x3727c5ac, v85\nv_cvt_pk_bf16_f32 v85, v85, v85\nv_mfma_f32_16x16x32_bf16 v[36:39], a[232:235], v[56:59], v[36:39]\n" \
"s_waitcnt lgkmcnt(4)\nv_and_b32 v85, 0xffff0000, v85\nv_cvt_pk_bf16_f32 v84, v84, v84\nv_mfma_f32_16x16x32_bf16 v[24:27], a[212:215], v[60:63], v[24:27]\nds_read_b128 v[56:59], v2 offset:40960\nv_and_b32 v84, 0xffff0000, v84\nv_mfma_f32_16x16x32_bf16 v[28:31], a[220:223], v[60:63], v[28:31]\nds_read_b128 v[80:83], v1 offset:14336\n" \
"v_mfma_f32_16x16x32_bf16 v[32:35], a[228:231], v[60:63], v[32:35]\nv_sqrt_f32 v85, v85\nv_mfma_f32_16x16x32_bf16 v[36:39], a[236:239], v[60:63], v[36:39]\ns_waitcnt lgkmcnt(5)\nv_cvt_pk_bf16_f32 v85, v85, v85\nv_and_b32 v85, 0xffff0000, v85\nv_mfma_f32_16x16x32_bf16 v[24:27], a[240:243], v[64:67], v[24:27]\ns_waitcnt lgkmcnt(4)\n" \
"ds_read_b128 v[60:63], v2 offset:43008\nv_mul_f32 v86, s58, v85\nv_mfma_f32_16x16x32_bf16 v[28:31], v[72:75], v[64:67], v[28:31]\nds_read_b128 v[72:75], v1 offset:15360\nv_add_f32 v87, v42, v46\nv_mfma_f32_16x16x32_bf16 v[32:35], a[248:251], v[64:67], v[32:35]\nv_rcp_f32 v85, v85\nv_mfma_f32_16x16x32_bf16 v[36:39], v[96:99], v[64:67], v[36:39]\n" \
"s_waitcnt lgkmcnt(5)\nv_mul_f32 v85, s58, v85\nv_add_f32 v88, v50, v54\nv_mfma_f32_16x16x32_bf16 v[24:27], a[244:247], v[68:71], v[24:27]\ns_waitcnt lgkmcnt(4)\nds_read_b128 v[64:67], v2 offset:45056\nv_add_f32 v87, v87, v88\nv_mfma_f32_16x16x32_bf16 v[28:31], v[76:79], v[68:71], v[28:31]\n" \
"v_mov_b32 v88, v87\nv_mfma_f32_16x16x32_bf16 v[32:35], a[252:255], v[68:71], v[32:35]\ns_nop 0\nv_permlane16_swap_b32 v87, v88\nv_mfma_f32_16x16x32_bf16 v[36:39], v[100:103], v[68:71], v[36:39]\ns_waitcnt lgkmcnt(4)\nds_read_b128 v[76:79], v1 offset:16384\nv_add_f32 v87, v87, v88\n" \
"v_mfma_f32_16x16x32_bf16 v[24:27], v[104:107], v[56:59], v[24:27]\nds_read_b128 v[68:71], v2 offset:47104\nv_mov_b32 v88, v87\nv_mfma_f32_16x16x32_bf16 v[28:31], v[112:115], v[56:59], v[28:31]\ns_waitcnt lgkmcnt(5)\nv_permlane32_swap_b32 v87, v88\nv_mfma_f32_16x16x32_bf16 v[32:35], v[80:83], v[56:59], v[32:35]\nv_add_f32 v87, v87, v88\n" \
"v_mul_f32 v87, 0xbd800000, v87\nv_mfma_f32_16x16x32_bf16 v[36:39], v[120:123], v[56:59], v[36:39]\ns_waitcnt lgkmcnt(4)\nds_read_b128 v[80:83], v1 offset:17408\nv_add_f32 v42, v42, v87\nv_mfma_f32_16x16x32_bf16 v[24:27], v[108:111], v[60:63], v[24:27]\ns_waitcnt vmcnt(0)\ns_barrier\n" \
"ds_read_b128 v[56:59], v2 offset:49152\nv_mfma_f32_16x16x32_bf16 v[28:31], v[116:119], v[60:63], v[28:31]\ns_waitcnt lgkmcnt(5)\nv_mul_f32 v42, 0x41000000, v42\nv_fma_f32 v43, v42, v42, v43\nv_mfma_f32_16x16x32_bf16 v[32:35], v[72:75], v[60:63], v[32:35]\nv_add_f32 v46, v46, v87\nv_mul_f32 v46, 0x41000000, v46\n" \
"v_mfma_f32_16x16x32_bf16 v[36:39], v[124:127], v[60:63], v[36:39]\ns_waitcnt lgkmcnt(4)\nv_fma_f32 v47, v46, v46, v47\nv_add_f32 v50, v50, v87\nv_mfma_f32_16x16x32_bf16 v[24:27], v[128:131], v[64:67], v[24:27]\nds_read_b128 v[60:63], v2 offset:51200\nv_mul_f32 v50, 0x41000000, v50\nv_mfma_f32_16x16x32_bf16 v[28:31], v[136:139], v[64:67], v[28:31]\n" \
"v_fma_f32 v51, v50, v50, v51\nv_add_f32 v54, v54, v87\nv_mfma_f32_16x16x32_bf16 v[32:35], v[144:147], v[64:67], v[32:35]\ns_waitcnt lgkmcnt(4)\nv_mul_f32 v54, 0x41000000, v54\nv_fma_f32 v55, v54, v54, v55\nv_mfma_f32_16x16x32_bf16 v[36:39], v[76:79], v[64:67], v[36:39]\ns_waitcnt lgkmcnt(3)\n" \
"v_add_f32 v88, v43, v47\nv_add_f32 v89, v51, v55\nv_mfma_f32_16x16x32_bf16 v[24:27], v[132:135], v[68:71], v[24:27]\nds_read_b128 v[64:67], v2 offset:53248\nv_add_f32 v88, v88, v89\nv_mfma_f32_16x16x32_bf16 v[28:31], v[140:143], v[68:71], v[28:31]\nds_read_b128 v[72:75], v1 offset:18432\nv_mov_b32 v89, v88\n" \
"v_mfma_f32_16x16x32_bf16 v[32:35], v[148:151], v[68:71], v[32:35]\ns_waitcnt lgkmcnt(4)\nv_permlane16_swap_b32 v88, v89\nv_mfma_f32_16x16x32_bf16 v[36:39], v[80:83], v[68:71], v[36:39]\ns_waitcnt lgkmcnt(3)\nv_add_f32 v88, v88, v89\nv_mov_b32 v89, v88\nv_mfma_f32_16x16x32_bf16 v[24:27], v[152:155], v[56:59], v[24:27]\n" \
"ds_read_b128 v[68:71], v2 offset:55296\nv_mfma_f32_16x16x32_bf16 v[28:31], v[160:163], v[56:59], v[28:31]\nds_read_b128 v[76:79], v1 offset:19456\nv_mfma_f32_16x16x32_bf16 v[32:35], v[168:171], v[56:59], v[32:35]\nv_permlane32_swap_b32 v88, v89\nv_mfma_f32_16x16x32_bf16 v[36:39], v[176:179], v[56:59], v[36:39]\ns_waitcnt lgkmcnt(4)\nv_add_f32 v88, v88, v89\n" \
"v_mul_f32 v88, 0x3a800000, v88\nv_mfma_f32_16x16x32_bf16 v[24:27], v[156:159], v[60:63], v[24:27]\nds_read_b128 v[56:59], v2 offset:57344\nv_cvt_pk_bf16_f32 v88, v88, v88\nv_mfma_f32_16x16x32_bf16 v[28:31], v[164:167], v[60:63], v[28:31]\nds_read_b128 v[80:83], v1 offset:20480\nv_and_b32 v88, 0xffff0000, v88\nv_mfma_f32_16x16x32_bf16 v[32:35], v[172:175], v[60:63], v[32:35]\n" \
"v_add_f32 v88, 0x3727c5ac, v88\nv_cvt_pk_bf16_f32 v88, v88, v88\nv_mfma_f32_16x16x32_bf16 v[36:39], v[180:183], v[60:63], v[36:39]\ns_waitcnt lgkmcnt(5)\nv_and_b32 v88, 0xffff0000, v88\nv_cvt_pk_bf16_f32 v87, v87, v87\nv_mfma_f32_16x16x32_bf16 v[24:27], v[184:187], v[64:67], v[24:27]\ns_waitcnt lgkmcnt(4)\n" \
"ds_read_b128 v[60:63], v2 offset:59392\nv_and_b32 v87, 0xffff0000, v87\nv_mfma_f32_16x16x32_bf16 v[28:31], v[72:75], v[64:67], v[28:31]\nds_read_b128 v[72:75], v1 offset:21504\nv_mfma_f32_16x16x32_bf16 v[32:35], v[192:195], v[64:67], v[32:35]\nv_sqrt_f32 v88, v88\nv_mfma_f32_16x16x32_bf16 v[36:39], v[200:203], v[64:67], v[36:39]\ns_waitcnt lgkmcnt(5)\n" \
"v_cvt_pk_bf16_f32 v88, v88, v88\nv_and_b32 v88, 0xffff0000, v88\nv_mfma_f32_16x16x32_bf16 v[24:27], v[188:191], v[68:71], v[24:27]\ns_waitcnt lgkmcnt(4)\nds_read_b128 v[64:67], v2 offset:61440\nv_mul_f32 v89, s58, v88\nv_mfma_f32_16x16x32_bf16 v[28:31], v[76:79], v[68:71], v[28:31]\nv_rcp_f32 v88, v88\n" \
"v_mfma_f32_16x16x32_bf16 v[32:35], v[196:199], v[68:71], v[32:35]\nv_mul_f32 v88, s58, v88\nv_mfma_f32_16x16x32_bf16 v[36:39], v[204:207], v[68:71], v[36:39]\ns_waitcnt lgkmcnt(4)\nds_read_b128 v[76:79], v1 offset:22528\nv_mfma_f32_16x16x32_bf16 v[24:27], v[208:211], v[56:59], v[24:27]\nds_read_b128 v[68:71], v2 offset:63488\nv_mfma_f32_16x16x32_bf16 v[28:31], v[216:219], v[56:59], v[28:31]\n" \
"s_waitcnt lgkmcnt(5)\nv_mfma_f32_16x16x32_bf16 v[32:35], v[80:83], v[56:59], v[32:35]\nv_mfma_f32_16x16x32_bf16 v[36:39], v[224:227], v[56:59], v[36:39]\ns_waitcnt lgkmcnt(4)\nds_read_b128 v[80:83], v1 offset:23552\nv_mfma_f32_16x16x32_bf16 v[24:27], v[212:215], v[60:63], v[24:27]\nds_read_b128 v[56:59], v2 offset:256\nv_mfma_f32_16x16x32_bf16 v[28:31], v[220:223], v[60:63], v[28:31]\n" \
"s_waitcnt lgkmcnt(5)\nv_mfma_f32_16x16x32_bf16 v[32:35], v[72:75], v[60:63], v[32:35]\nv_mfma_f32_16x16x32_bf16 v[36:39], v[228:231], v[60:63], v[36:39]\ns_waitcnt lgkmcnt(4)\nv_mfma_f32_16x16x32_bf16 v[24:27], v[232:235], v[64:67], v[24:27]\nds_read_b128 v[60:63], v2 offset:2304\nv_mfma_f32_16x16x32_bf16 v[28:31], v[240:243], v[64:67], v[28:31]\nv_mfma_f32_16x16x32_bf16 v[32:35], v[248:251], v[64:67], v[32:35]\n" \
"s_waitcnt lgkmcnt(4)\nv_mfma_f32_16x16x32_bf16 v[36:39], v[76:79], v[64:67], v[36:39]\ns_waitcnt lgkmcnt(3)\nv_mfma_f32_16x16x32_bf16 v[24:27], v[236:239], v[68:71], v[24:27]\nds_read_b128 v[64:67], v2 offset:4352\nv_mfma_f32_16x16x32_bf16 v[28:31], v[244:247], v[68:71], v[28:31]\nds_read_b128 v[72:75], v1 offset:0\nv_mfma_f32_16x16x32_bf16 v[32:35], v[252:255], v[68:71], v[32:35]\n" \
"s_waitcnt lgkmcnt(4)\nv_mfma_f32_16x16x32_bf16 v[36:39], v[80:83], v[68:71], v[36:39]\ns_waitcnt lgkmcnt(3)\nv_mfma_f32_16x16x32_bf16 v[40:43], a[0:3], v[56:59], 0\nds_read_b128 v[68:71], v2 offset:6400\nv_mfma_f32_16x16x32_bf16 v[44:47], a[8:11], v[56:59], 0\ns_lshl_b32 s44, s40, 1\nds_read_b128 v[76:79], v1 offset:1024\n" \
"v_mov_b32 v90, s44\nv_mfma_f32_16x16x32_bf16 v[48:51], a[16:19], v[56:59], 0\nv_mfma_f32_16x16x32_bf16 v[52:55], a[24:27], v[56:59], 0\ns_waitcnt lgkmcnt(4)\nv_fma_f32 v24, v8, v84, v24\nv_fma_f32 v25, v9, v84, v25\nv_mfma_f32_16x16x32_bf16 v[40:43], a[4:7], v[60:63], v[40:43]\nds_read_b128 v[56:59], v2 offset:8448\n" \
"v_fma_f32 v32, v16, v84, v32\nv_mfma_f32_16x16x32_bf16 v[44:47], a[12:15], v[60:63], v[44:47]\nds_read_b128 v[80:83], v1 offset:2048\nv_fma_f32 v33, v17, v84, v33\nv_mfma_f32_16x16x32_bf16 v[48:51], a[20:23], v[60:63], v[48:51]\nv_mul_f32 v24, v24, v85\nv_mul_f32 v25, v25, v85\nv_mfma_f32_16x16x32_bf16 v[52:55], a[28:31], v[60:63], v[52:55]\n" \
"s_waitcnt lgkmcnt(5)\nv_fma_f32 v91, v24, v24, s59\nv_fma_f32 v92, v25, v25, s59\nv_mfma_f32_16x16x32_bf16 v[40:43], a[32:35], v[64:67], v[40:43]\ns_waitcnt lgkmcnt(4)\nds_read_b128 v[60:63], v2 offset:10496\nv_mul_f32 v91, v91, v24\nv_mfma_f32_16x16x32_bf16 v[44:47], v[72:75], v[64:67], v[44:47]\n" \
"ds_read_b128 v[72:75], v1 offset:3072\nv_mul_f32 v92, v92, v25\nv_mfma_f32_16x16x32_bf16 v[48:51], a[40:43], v[64:67], v[48:51]\nv_exp_f32 v91, v91\nv_mfma_f32_16x16x32_bf16 v[52:55], a[48:51], v[64:67], v[52:55]\ns_waitcnt lgkmcnt(5)\nv_exp_f32 v92, v92\nv_mfma_f32_16x16x32_bf16 v[40:43], a[36:39], v[68:71], v[40:43]\n" \
"s_waitcnt lgkmcnt(4)\nds_read_b128 v[64:67], v2 offset:12544\nv_mul_f32 v24, v24, v32\nv_mfma_f32_16x16x32_bf16 v[44:47], v[76:79], v[68:71], v[44:47]\nv_mul_f32 v25, v25, v33\nv_fma_f32 v91, v91, v86, v86\nv_mfma_f32_16x16x32_bf16 v[48:51], a[44:47], v[68:71], v[48:51]\nv_fma_f32 v92, v92, v86, v86\n" \
"v_fma_f32 v26, v10, v84, v26\nv_mfma_f32_16x16x32_bf16 v[52:55], a[52:55], v[68:71], v[52:55]\ns_waitcnt lgkmcnt(4)\nds_read_b128 v[76:79], v1 offset:4096\nv_fma_f32 v27, v11, v84, v27\nv_mfma_f32_16x16x32_bf16 v[40:43], a[56:59], v[56:59], v[40:43]\nds_read_b128 v[68:71], v2 offset:14592\nv_fma_f32 v34, v18, v84, v34\n" \
"v_mfma_f32_16x16x32_bf16 v[44:47], a[64:67], v[56:59], v[44:47]\ns_waitcnt lgkmcnt(5)\nv_rcp_f32 v91, v91\nv_mfma_f32_16x16x32_bf16 v[48:51], v[80:83], v[56:59], v[48:51]\nv_rcp_f32 v92, v92\nv_mfma_f32_16x16x32_bf16 v[52:55], a[72:75], v[56:59], v[52:55]\ns_waitcnt lgkmcnt(4)\nds_read_b128 v[80:83], v1 offset:5120\n" \
"v_mul_f32 v24, v24, v91\nv_mfma_f32_16x16x32_bf16 v[40:43], a[60:63], v[60:63], v[40:43]\nds_read_b128 v[56:59], v2 offset:16640\nv_mul_f32 v25, v25, v92\nv_mfma_f32_16x16x32_bf16 v[44:47], a[68:71], v[60:63], v[44:47]\ns_waitcnt lgkmcnt(5)\nv_cvt_pk_bf16_f32 v32, v24, v25\nv_fma_f32 v35, v19, v84, v35\n" \
"v_mfma_f32_16x16x32_bf16 v[48:51], v[72:75], v[60:63], v[48:51]\nv_mul_f32 v26, v26, v85\nv_mul_f32 v27, v27, v85\nv_mfma_f32_16x16x32_bf16 v[52:55], a[76:79], v[60:63], v[52:55]\ns_waitcnt lgkmcnt(4)\nv_fma_f32 v91, v26, v26, s59\nv_fma_f32 v92, v27, v27, s59\nv_mfma_f32_16x16x32_bf16 v[40:43], a[80:83], v[64:67], v[40:43]\n" \
"ds_read_b128 v[60:63], v2 offset:18688\nv_mul_f32 v91, v91, v26\nv_mfma_f32_16x16x32_bf16 v[44:47], a[88:91], v[64:67], v[44:47]\nv_mul_f32 v92, v92, v27\nv_mul_f32 v26, v26, v34\nv_mfma_f32_16x16x32_bf16 v[48:51], a[96:99], v[64:67], v[48:51]\ns_waitcnt lgkmcnt(4)\nv_exp_f32 v91, v91\n" \
"v_mfma_f32_16x16x32_bf16 v[52:55], v[76:79], v[64:67], v[52:55]\ns_waitcnt lgkmcnt(3)\nv_exp_f32 v92, v92\nv_mfma_f32_16x16x32_bf16 v[40:43], a[84:87], v[68:71], v[40:43]\nds_read_b128 v[64:67], v2 offset:20736\nv_mul_f32 v27, v27, v35\nv_mfma_f32_16x16x32_bf16 v[44:47], a[92:95], v[68:71], v[44:47]\nds_read_b128 v[72:75], v1 offset:6144\n" \
"v_fma_f32 v91, v91, v86, v86\nv_mfma_f32_16x16x32_bf16 v[48:51], a[100:103], v[68:71], v[48:51]\ns_waitcnt lgkmcnt(4)\nv_fma_f32 v92, v92, v86, v86\nv_fma_f32 v28, v12, v84, v28\nv_mfma_f32_16x16x32_bf16 v[52:55], v[80:83], v[68:71], v[52:55]\ns_waitcnt lgkmcnt(3)\nv_rcp_f32 v91, v91\n" \
"v_mfma_f32_16x16x32_bf16 v[40:43], a[104:107], v[56:59], v[40:43]\nds_read_b128 v[68:71], v2 offset:22784\nv_mul_f32 v26, v26, v91\nv_mfma_f32_16x16x32_bf16 v[44:47], a[112:115], v[56:59], v[44:47]\nds_read_b128 v[76:79], v1 offset:7168\nv_fma_f32 v29, v13, v84, v29\nv_mfma_f32_16x16x32_bf16 v[48:51], a[120:123], v[56:59], v[48:51]\nv_rcp_f32 v92, v92\n" \
"v_mfma_f32_16x16x32_bf16 v[52:55], a[128:131], v[56:59], v[52:55]\ns_waitcnt lgkmcnt(4)\nv_mul_f32 v27, v27, v92\nv_cvt_pk_bf16_f32 v33, v26, v27\nv_mfma_f32_16x16x32_bf16 v[40:43], a[108:111], v[60:63], v[40:43]\nds_read_b128 v[56:59], v2 offset:24832\nv_fma_f32 v36, v20, v84, v36\nv_mfma_f32_16x16x32_bf16 v[44:47], a[116:119], v[60:63], v[44:47]\n" \
"ds_read_b128 v[80:83], v1 offset:8192\nv_fma_f32 v37, v21, v84, v37\nv_mfma_f32_16x16x32_bf16 v[48:51], a[124:127], v[60:63], v[48:51]\nv_mul_f32 v28, v28, v85\nv_mul_f32 v29, v29, v85\nv_mfma_f32_16x16x32_bf16 v[52:55], a[132:135], v[60:63], v[52:55]\ns_waitcnt lgkmcnt(5)\nv_fma_f32 v91, v28, v28, s59\n" \
"v_fma_f32 v92, v29, v29, s59\nv_mfma_f32_16x16x32_bf16 v[40:43], a[136:139], v[64:67], v[40:43]\ns_waitcnt lgkmcnt(4)\nds_read_b128 v[60:63], v2 offset:26880\nv_mul_f32 v91, v91, v28\nv_mfma_f32_16x16x32_bf16 v[44:47], v[72:75], v[64:67], v[44:47]\nds_read_b128 v[72:75], v1 offset:9216\nv_mul_f32 v92, v92, v29\n" \
"v_mfma_f32_16x16x32_bf16 v[48:51], a[144:147], v[64:67], v[48:51]\nv_exp_f32 v91, v91\nv_mfma_f32_16x16x32_bf16 v[52:55], a[152:155], v[64:67], v[52:55]\ns_waitcnt lgkmcnt(5)\nv_exp_f32 v92, v92\nv_mfma_f32_16x16x32_bf16 v[40:43], a[140:143], v[68:71], v[40:43]\ns_waitcnt lgkmcnt(4)\nds_read_b128 v[64:67], v2 offset:28928\n" \
"v_mul_f32 v28, v28, v36\nv_mfma_f32_16x16x32_bf16 v[44:47], v[76:79], v[68:71], v[44:47]\nv_mul_f32 v29, v29, v37\nv_fma_f32 v91, v91, v86, v86\nv_mfma_f32_16x16x32_bf16 v[48:51], a[148:151], v[68:71], v[48:51]\nv_fma_f32 v92, v92, v86, v86\nv_fma_f32 v30, v14, v84, v30\nv_mfma_f32_16x16x32_bf16 v[52:55], a[156:159], v[68:71], v[52:55]\n" \
"s_waitcnt lgkmcnt(4)\nds_read_b128 v[76:79], v1 offset:10240\nv_fma_f32 v31, v15, v84, v31\nv_mfma_f32_16x16x32_bf16 v[40:43], a[160:163], v[56:59], v[40:43]\nds_read_b128 v[68:71], v2 offset:30976\nv_fma_f32 v38, v22, v84, v38\nv_mfma_f32_16x16x32_bf16 v[44:47], a[168:171], v[56:59], v[44:47]\ns_waitcnt lgkmcnt(5)\n" \
"v_rcp_f32 v91, v91\nv_mfma_f32_16x16x32_bf16 v[48:51], v[80:83], v[56:59], v[48:51]\nv_rcp_f32 v92, v92\nv_mfma_f32_16x16x32_bf16 v[52:55], a[176:179], v[56:59], v[52:55]\ns_waitcnt lgkmcnt(4)\nds_read_b128 v[80:83], v1 offset:11264\nv_mul_f32 v28, v28, v91\nv_mfma_f32_16x16x32_bf16 v[40:43], a[164:167], v[60:63], v[40:43]\n" \
"ds_read_b128 v[56:59], v2 offset:33024\nv_mul_f32 v29, v29, v92\nv_mfma_f32_16x16x32_bf16 v[44:47], a[172:175], v[60:63], v[44:47]\ns_waitcnt lgkmcnt(5)\nv_cvt_pk_bf16_f32 v34, v28, v29\nv_fma_f32 v39, v23, v84, v39\nv_mfma_f32_16x16x32_bf16 v[48:51], v[72:75], v[60:63], v[48:51]\nv_mul_f32 v30, v30, v85\n" \
"v_mul_f32 v31, v31, v85\nv_mfma_f32_16x16x32_bf16 v[52:55], a[180:183], v[60:63], v[52:55]\ns_waitcnt lgkmcnt(4)\nv_fma_f32 v91, v30, v30, s59\nv_fma_f32 v92, v31, v31, s59\nv_mfma_f32_16x16x32_bf16 v[40:43], a[184:187], v[64:67], v[40:43]\nds_read_b128 v[60:63], v2 offset:35072\nv_mul_f32 v91, v91, v30\n" \
"v_mfma_f32_16x16x32_bf16 v[44:47], a[192:195], v[64:67], v[44:47]\nv_mul_f32 v92, v92, v31\nv_mul_f32 v30, v30, v38\nv_mfma_f32_16x16x32_bf16 v[48:51], a[200:203], v[64:67], v[48:51]\ns_waitcnt lgkmcnt(4)\nv_exp_f32 v91, v91\nv_mfma_f32_16x16x32_bf16 v[52:55], v[76:79], v[64:67], v[52:55]\ns_waitcnt lgkmcnt(3)\n" \
"v_exp_f32 v92, v92\nv_mfma_f32_16x16x32_bf16 v[40:43], a[188:191], v[68:71], v[40:43]\nds_read_b128 v[64:67], v2 offset:37120\nv_mul_f32 v31, v31, v39\nv_mfma_f32_16x16x32_bf16 v[44:47], a[196:199], v[68:71], v[44:47]\nds_read_b128 v[72:75], v1 offset:12288\nv_fma_f32 v91, v91, v86, v86\nv_mfma_f32_16x16x32_bf16 v[48:51], a[204:207], v[68:71], v[48:51]\n" \
"s_waitcnt lgkmcnt(4)\nv_fma_f32 v92, v92, v86, v86\nv_mfma_f32_16x16x32_bf16 v[52:55], v[80:83], v[68:71], v[52:55]\ns_waitcnt lgkmcnt(3)\nv_rcp_f32 v91, v91\nv_mfma_f32_16x16x32_bf16 v[40:43], a[208:211], v[56:59], v[40:43]\nds_read_b128 v[68:71], v2 offset:39168\nv_mul_f32 v30, v30, v91\n" \
"v_mfma_f32_16x16x32_bf16 v[44:47], a[216:219], v[56:59], v[44:47]\nds_read_b128 v[76:79], v1 offset:13312\nv_mfma_f32_16x16x32_bf16 v[48:51], a[224:227], v[56:59], v[48:51]\nv_rcp_f32 v92, v92\nv_mfma_f32_16x16x32_bf16 v[52:55], a[232:235], v[56:59], v[52:55]\ns_waitcnt lgkmcnt(4)\nv_mul_f32 v31, v31, v92\nv_cvt_pk_bf16_f32 v35, v30, v31\n" \
"v_mfma_f32_16x16x32_bf16 v[40:43], a[212:215], v[60:63], v[40:43]\nds_read_b128 v[56:59], v2 offset:41216\nv_mfma_f32_16x16x32_bf16 v[44:47], a[220:223], v[60:63], v[44:47]\nds_read_b128 v[80:83], v1 offset:14336\nv_mfma_f32_16x16x32_bf16 v[48:51], a[228:231], v[60:63], v[48:51]\nbuffer_store_dwordx4 v[32:35], v4, %[rs], s55 offen offset:0 sc1\nv_mfma_f32_16x16x32_bf16 v[52:55], a[236:239], v[60:63], v[52:55]\ns_waitcnt lgkmcnt(5)\n" \
"v_mfma_f32_16x16x32_bf16 v[40:43], a[240:243], v[64:67], v[40:43]\ns_waitcnt lgkmcnt(4)\nds_read_b128 v[60:63], v2 offset:43264\nv_mfma_f32_16x16x32_bf16 v[44:47], v[72:75], v[64:67], v[44:47]\nds_read_b128 v[72:75], v1 offset:15360\nv_mfma_f32_16x16x32_bf16 v[48:51], a[248:251], v[64:67], v[48:51]\nv_mfma_f32_16x16x32_bf16 v[52:55], v[96:99], v[64:67], v[52:55]\ns_waitcnt lgkmcnt(5)\n" \
"v_mfma_f32_16x16x32_bf16 v[40:43], a[244:247], v[68:71], v[40:43]\ns_waitcnt lgkmcnt(4)\nds_read_b128 v[64:67], v2 offset:45312\nv_mfma_f32_16x16x32_bf16 v[44:47], v[76:79], v[68:71], v[44:47]\nds_read_b128 v[24:27], v2 offset:47360\nv_mfma_f32_16x16x32_bf16 v[48:51], a[252:255], v[68:71], v[48:51]\nds_read_b128 v[28:31], v2 offset:49408\nv_mfma_f32_16x16x32_bf16 v[52:55], v[100:103], v[68:71], v[52:55]\n" \
"s_waitcnt lgkmcnt(6)\nv_mfma_f32_16x16x32_bf16 v[40:43], v[104:107], v[56:59], v[40:43]\ns_waitcnt lgkmcnt(5)\nds_read_b128 v[68:71], v2 offset:51456\nv_mfma_f32_16x16x32_bf16 v[48:51], v[80:83], v[56:59], v[48:51]\ns_waitcnt lgkmcnt(5)\nv_mfma_f32_16x16x32_bf16 v[40:43], v[108:111], v[60:63], v[40:43]\ns_waitcnt lgkmcnt(4)\n" \
"ds_read_b128 v[56:59], v2 offset:53504\nv_mfma_f32_16x16x32_bf16 v[48:51], v[72:75], v[60:63], v[48:51]\ns_waitcnt lgkmcnt(4)\nv_mfma_f32_16x16x32_bf16 v[40:43], v[128:131], v[64:67], v[40:43]\nds_read_b128 v[60:63], v2 offset:55552\nv_mfma_f32_16x16x32_bf16 v[48:51], v[144:147], v[64:67], v[48:51]\ns_waitcnt lgkmcnt(4)\nds_read_b128 v[76:79], v1 offset:20480\n" \
"v_mfma_f32_16x16x32_bf16 v[40:43], v[132:135], v[24:27], v[40:43]\nds_read_b128 v[64:67], v2 offset:57600\nv_mfma_f32_16x16x32_bf16 v[48:51], v[148:151], v[24:27], v[48:51]\ns_waitcnt lgkmcnt(5)\nds_read_b128 v[80:83], v1 offset:21504\nv_mfma_f32_16x16x32_bf16 v[40:43], v[152:155], v[28:31], v[40:43]\nds_read_b128 v[24:27], v2 offset:59648\nv_mfma_f32_16x16x32_bf16 v[48:51], v[168:171], v[28:31], v[48:51]\n" \
"s_waitcnt lgkmcnt(6)\nv_mfma_f32_16x16x32_bf16 v[40:43], v[156:159], v[68:71], v[40:43]\nds_read_b128 v[28:31], v2 offset:61696\nv_mfma_f32_16x16x32_bf16 v[48:51], v[172:175], v[68:71], v[48:51]\ns_waitcnt lgkmcnt(6)\nv_mfma_f32_16x16x32_bf16 v[40:43], v[184:187], v[56:59], v[40:43]\nds_read_b128 v[68:71], v2 offset:63744\nv_mfma_f32_16x16x32_bf16 v[48:51], v[192:195], v[56:59], v[48:51]\n" \
"s_waitcnt lgkmcnt(6)\nv_mfma_f32_16x16x32_bf16 v[40:43], v[188:191], v[60:63], v[40:43]\nds_read_b128 v[56:59], v2 offset:41216\nv_mfma_f32_16x16x32_bf16 v[48:51], v[196:199], v[60:63], v[48:51]\ns_waitcnt lgkmcnt(5)\nv_mfma_f32_16x16x32_bf16 v[40:43], v[208:211], v[64:67], v[40:43]\nds_read_b128 v[60:63], v2 offset:43264\nv_mfma_f32_16x16x32_bf16 v[48:51], v[76:79], v[64:67], v[48:51]\n" \
"s_waitcnt lgkmcnt(4)\nds_read_b128 v[72:75], v1 offset:16384\nv_mfma_f32_16x16x32_bf16 v[40:43], v[212:215], v[24:27], v[40:43]\nds_read_b128 v[64:67], v2 offset:45312\nv_mfma_f32_16x16x32_bf16 v[48:51], v[80:83], v[24:27], v[48:51]\ns_waitcnt lgkmcnt(5)\nds_read_b128 v[76:79], v1 offset:17408\nv_mfma_f32_16x16x32_bf16 v[40:43], v[232:235], v[28:31], v[40:43]\n" \
"ds_read_b128 v[24:27], v2 offset:47360\nv_mfma_f32_16x16x32_bf16 v[48:51], v[248:251], v[28:31], v[48:51]\ns_waitcnt vmcnt(0)\ns_barrier\ns_waitcnt lgkmcnt(6)\nbuffer_store_dword v90, off, %[rs], s88 sc1\nv_mfma_f32_16x16x32_bf16 v[40:43], v[236:239], v[68:71], v[40:43]\nds_read_b128 v[28:31], v2 offset:49408\n" \
"v_mfma_f32_16x16x32_bf16 v[48:51], v[252:255], v[68:71], v[48:51]\ns_waitcnt lgkmcnt(6)\nds_read_b128 v[80:83], v1 offset:18432\nv_mfma_f32_16x16x32_bf16 v[44:47], v[112:115], v[56:59], v[44:47]\nds_read_b128 v[68:71], v2 offset:51456\nv_mfma_f32_16x16x32_bf16 v[52:55], v[120:123], v[56:59], v[52:55]\ns_waitcnt lgkmcnt(7)\nv_fma_f32 v40, v8, v87, v40\n" \
"v_fma_f32 v41, v9, v87, v41\nv_mfma_f32_16x16x32_bf16 v[44:47], v[116:119], v[60:63], v[44:47]\nds_read_b128 v[56:59], v2 offset:53504\nv_fma_f32 v48, v16, v87, v48\nv_mfma_f32_16x16x32_bf16 v[52:55], v[124:127], v[60:63], v[52:55]\ns_waitcnt lgkmcnt(6)\nv_fma_f32 v49, v17, v87, v49\nv_mul_f32 v40, v40, v88\n" \
"v_mfma_f32_16x16x32_bf16 v[44:47], v[136:139], v[64:67], v[44:47]\nds_read_b128 v[60:63], v2 offset:55552\nv_mul_f32 v41, v41, v88\nv_mfma_f32_16x16x32_bf16 v[52:55], v[72:75], v[64:67], v[52:55]\ns_waitcnt lgkmcnt(5)\nds_read_b128 v[72:75], v1 offset:19456\nv_fma_f32 v91, v40, v40, s59\nv_mfma_f32_16x16x32_bf16 v[44:47], v[140:143], v[24:27], v[44:47]\n" \
"ds_read_b128 v[64:67], v2 offset:57600\nv_fma_f32 v92, v41, v41, s59\nv_mfma_f32_16x16x32_bf16 v[52:55], v[76:79], v[24:27], v[52:55]\ns_waitcnt lgkmcnt(6)\nv_mul_f32 v91, v91, v40\nv_mul_f32 v92, v92, v41\nv_mfma_f32_16x16x32_bf16 v[44:47], v[160:163], v[28:31], v[44:47]\nds_read_b128 v[24:27], v2 offset:59648\n" \
"v_mul_f32 v40, v40, v48\nv_mfma_f32_16x16x32_bf16 v[52:55], v[176:179], v[28:31], v[52:55]\ns_waitcnt lgkmcnt(5)\nds_read_b128 v[76:79], v1 offset:22528\nv_mul_f32 v41, v41, v49\nv_mfma_f32_16x16x32_bf16 v[44:47], v[164:167], v[68:71], v[44:47]\nds_read_b128 v[28:31], v2 offset:61696\nv_mfma_f32_16x16x32_bf16 v[52:55], v[180:183], v[68:71], v[52:55]\n" \
"s_waitcnt lgkmcnt(6)\nv_exp_f32 v91, v91\nv_mfma_f32_16x16x32_bf16 v[44:47], v[80:83], v[56:59], v[44:47]\nds_read_b128 v[68:71], v2 offset:63744\nds_read_b128 v[80:83], v1 offset:23552\nv_mfma_f32_16x16x32_bf16 v[52:55], v[200:203], v[56:59], v[52:55]\ns_waitcnt lgkmcnt(6)\nv_exp_f32 v92, v92\n" \
"v_mfma_f32_16x16x32_bf16 v[44:47], v[72:75], v[60:63], v[44:47]\nv_fma_f32 v91, v91, v89, v89\nv_fma_f32 v92, v92, v89, v89\nv_mfma_f32_16x16x32_bf16 v[52:55], v[204:207], v[60:63], v[52:55]\ns_waitcnt lgkmcnt(5)\nv_rcp_f32 v91, v91\nv_mfma_f32_16x16x32_bf16 v[44:47], v[216:219], v[64:67], v[44:47]\nv_rcp_f32 v92, v92\n" \
"v_mfma_f32_16x16x32_bf16 v[52:55], v[224:227], v[64:67], v[52:55]\ns_waitcnt lgkmcnt(4)\nv_mul_f32 v40, v40, v91\nv_mul_f32 v41, v41, v92\nv_mfma_f32_16x16x32_bf16 v[44:47], v[220:223], v[24:27], v[44:47]\nv_cvt_pk_bf16_f32 v48, v40, v41\nv_fma_f32 v42, v10, v87, v42\nv_mfma_f32_16x16x32_bf16 v[52:55], v[228:231], v[24:27], v[52:55]\n" \
"s_waitcnt lgkmcnt(2)\nv_fma_f32 v43, v11, v87, v43\nv_fma_f32 v50, v18, v87, v50\nv_mfma_f32_16x16x32_bf16 v[44:47], v[240:243], v[28:31], v[44:47]\nv_fma_f32 v51, v19, v87, v51\nv_mul_f32 v42, v42, v88\nv_mfma_f32_16x16x32_bf16 v[52:55], v[76:79], v[28:31], v[52:55]\ns_waitcnt lgkmcnt(1)\n" \
"v_mul_f32 v43, v43, v88\nv_fma_f32 v91, v42, v42, s59\nv_mfma_f32_16x16x32_bf16 v[44:47], v[244:247], v[68:71], v[44:47]\ns_waitcnt lgkmcnt(0)\nv_fma_f32 v92, v43, v43, s59\nv_mul_f32 v91, v91, v42\nv_mfma_f32_16x16x32_bf16 v[52:55], v[80:83], v[68:71], v[52:55]\nv_mul_f32 v92, v92, v43\n" \
"v_mul_f32 v42, v42, v50\nv_exp_f32 v91, v91\nv_exp_f32 v92, v92\nv_mul_f32 v43, v43, v51\nv_fma_f32 v91, v91, v89, v89\nv_fma_f32 v92, v92, v89, v89\nv_rcp_f32 v91, v91\nv_rcp_f32 v92, v92\n" \
"v_mul_f32 v42, v42, v91\nv_mul_f32 v43, v43, v92\nv_cvt_pk_bf16_f32 v49, v42, v43\nv_fma_f32 v44, v12, v87, v44\nv_fma_f32 v45, v13, v87, v45\nv_fma_f32 v52, v20, v87, v52\nv_fma_f32 v53, v21, v87, v53\nv_mul_f32 v44, v44, v88\n" \
"v_mul_f32 v45, v45, v88\nv_fma_f32 v91, v44, v44, s59\nv_fma_f32 v92, v45, v45, s59\nv_mul_f32 v91, v91, v44\nv_mul_f32 v92, v92, v45\nv_exp_f32 v91, v91\nv_exp_f32 v92, v92\nv_mul_f32 v44, v44, v52\n" \
"v_mul_f32 v45, v45, v53\nv_fma_f32 v91, v91, v89, v89\nv_fma_f32 v92, v92, v89, v89\nv_rcp_f32 v91, v91\nv_rcp_f32 v92, v92\nv_mul_f32 v44, v44, v91\nv_mul_f32 v45, v45, v92\nv_cvt_pk_bf16_f32 v50, v44, v45\n" \
"v_fma_f32 v46, v14, v87, v46\nv_fma_f32 v47, v15, v87, v47\nv_fma_f32 v54, v22, v87, v54\nv_fma_f32 v55, v23, v87, v55\nv_mul_f32 v46, v46, v88\nv_mul_f32 v47, v47, v88\nv_fma_f32 v91, v46, v46, s59\nv_fma_f32 v92, v47, v47, s59\n" \
"v_mul_f32 v91, v91, v46\nv_mul_f32 v92, v92, v47\nv_exp_f32 v91, v91\nv_exp_f32 v92, v92\nv_mul_f32 v46, v46, v54\nv_mul_f32 v47, v47, v55\nv_fma_f32 v91, v91, v89, v89\nv_fma_f32 v92, v92, v89, v89\n" \
"v_rcp_f32 v91, v91\nv_rcp_f32 v92, v92\nv_mul_f32 v46, v46, v91\nv_mul_f32 v47, v47, v92\nv_cvt_pk_bf16_f32 v51, v46, v47\nbuffer_store_dwordx4 v[48:51], v4, %[rs], s55 offen offset:1024 sc1\nv_add_u32 v90, 1, v90\ns_mov_b32 s56, s88\n" \
"s_add_u32 s40, s40, 1\ns_cmp_lt_u32 s40, s89\ns_cbranch_scc1 .Lgg_next%=\ns_cmp_eq_u32 s89, 64\ns_cbranch_scc1 .Lgg_fin%=\ns_mov_b32 s89, 64\ns_mov_b32 s40, 0\ns_xor_b32 s86, s86, 0x2000\n" \
"s_xor_b32 s87, s87, 0x2000\ns_mov_b32 s88, %[fh1]\nv_xor_b32 v3, 0x2000, v3\ns_branch .Lgg_next%=\n.Lgg_fin%=:\ns_waitcnt vmcnt(0)\ns_barrier\nbuffer_store_dword v90, off, %[rs], s56 sc1\n" \
"s_branch .Lgg_end%=\n.Lgg_abort%=:\ns_mov_b64 exec, -1\nv_mov_b32 v16, 0\ns_movk_i32 s44, 6400\nbuffer_store_dword v16, off, %[rs], s44 sc1\n.Lgg_end%=:\ns_waitcnt vmcnt(0) lgkmcnt(0)\n" \
""
#define DD_ASM \
"v_mbcnt_lo_u32_b32 v255, -1, 0\nv_mbcnt_hi_u32_b32 v255, -1, v255\nv_lshlrev_b32 v255, 2, v255\nv_add_u32 v255, %[tb], v255\nds_read_b32 v0, v255 offset:0\nds_read_b32 v1, v255 offset:256\nds_read_b32 v2, v255 offset:512\nds_read_b32 v3, v255 offset:768\n" \
"ds_read_b32 v4, v255 offset:1024\nds_read_b32 v5, v255 offset:1280\nds_read_b32 v6, v255 offset:1536\nds_read_b32 v7, v255 offset:1792\nds_read_b32 v125, v255 offset:2048\nds_read_b32 v126, v255 offset:2304\nds_read_b32 v127, v255 offset:2560\ns_waitcnt lgkmcnt(0)\n" \
"s_add_u32 m0, %[tb], 8192\ns_add_u32 s44, %[wo], 0\nbuffer_load_dwordx4 v125, %[rw], s44 offen nt lds\ns_add_u32 m0, %[tb], 9216\ns_add_u32 s44, %[wo], 16384\nbuffer_load_dwordx4 v125, %[rw], s44 offen nt lds\ns_add_u32 m0, %[tb], 10240\ns_add_u32 s44, %[wo], 32768\n" \
"buffer_load_dwordx4 v125, %[rw], s44 offen nt lds\ns_add_u32 m0, %[tb], 11264\ns_add_u32 s44, %[wo], 49152\nbuffer_load_dwordx4 v125, %[rw], s44 offen nt lds\ns_add_u32 m0, %[lw], 0\ns_add_u32 s44, %[wo], 65536\nbuffer_load_dwordx4 v125, %[rw], s44 offen nt lds\ns_add_u32 m0, %[lw], 1024\n" \
"s_add_u32 s44, %[wo], 81920\nbuffer_load_dwordx4 v125, %[rw], s44 offen nt lds\ns_add_u32 m0, %[lw], 2048\ns_add_u32 s44, %[wo], 98304\nbuffer_load_dwordx4 v125, %[rw], s44 offen nt lds\ns_add_u32 m0, %[lw], 3072\ns_add_u32 s44, %[wo], 114688\nbuffer_load_dwordx4 v125, %[rw], s44 offen nt lds\n" \
"s_add_u32 m0, %[lw], 4096\ns_add_u32 s44, %[wo], 131072\nbuffer_load_dwordx4 v125, %[rw], s44 offen nt lds\ns_add_u32 m0, %[lw], 5120\ns_add_u32 s44, %[wo], 147456\nbuffer_load_dwordx4 v125, %[rw], s44 offen nt lds\ns_add_u32 m0, %[lw], 6144\ns_add_u32 s44, %[wo], 163840\n" \
"buffer_load_dwordx4 v125, %[rw], s44 offen nt lds\ns_add_u32 m0, %[lw], 7168\ns_add_u32 s44, %[wo], 180224\nbuffer_load_dwordx4 v125, %[rw], s44 offen nt lds\ns_add_u32 m0, %[lw], 8192\ns_add_u32 s44, %[wo], 196608\nbuffer_load_dwordx4 v125, %[rw], s44 offen nt lds\ns_add_u32 m0, %[lw], 9216\n" \
"s_add_u32 s44, %[wo], 212992\nbuffer_load_dwordx4 v125, %[rw], s44 offen nt lds\ns_add_u32 m0, %[lw], 10240\ns_add_u32 s44, %[wo], 229376\nbuffer_load_dwordx4 v125, %[rw], s44 offen nt lds\ns_add_u32 m0, %[lw], 11264\ns_add_u32 s44, %[wo], 245760\nbuffer_load_dwordx4 v125, %[rw], s44 offen nt lds\n" \
"s_add_u32 m0, %[lw], 12288\ns_add_u32 s44, %[wo], 262144\nbuffer_load_dwordx4 v125, %[rw], s44 offen nt lds\ns_add_u32 m0, %[lw], 13312\ns_add_u32 s44, %[wo], 278528\nbuffer_load_dwordx4 v125, %[rw], s44 offen nt lds\ns_add_u32 m0, %[lw], 14336\ns_add_u32 s44, %[wo], 294912\n" \
"buffer_load_dwordx4 v125, %[rw], s44 offen nt lds\ns_add_u32 m0, %[lw], 15360\ns_add_u32 s44, %[wo], 311296\nbuffer_load_dwordx4 v125, %[rw], s44 offen nt lds\ns_add_u32 m0, %[lw], 16384\ns_add_u32 s44, %[wo], 327680\nbuffer_load_dwordx4 v125, %[rw], s44 offen nt lds\ns_add_u32 m0, %[lw], 17408\n" \
"s_add_u32 s44, %[wo], 344064\nbuffer_load_dwordx4 v125, %[rw], s44 offen nt lds\ns_add_u32 m0, %[lw], 18432\ns_add_u32 s44, %[wo], 360448\nbuffer_load_dwordx4 v125, %[rw], s44 offen nt lds\ns_add_u32 m0, %[lw], 19456\ns_add_u32 s44, %[wo], 376832\nbuffer_load_dwordx4 v125, %[rw], s44 offen nt lds\n" \
"s_add_u32 m0, %[lw], 20480\ns_add_u32 s44, %[wo], 393216\nbuffer_load_dwordx4 v125, %[rw], s44 offen nt lds\ns_add_u32 m0, %[lw], 21504\ns_add_u32 s44, %[wo], 409600\nbuffer_load_dwordx4 v125, %[rw], s44 offen nt lds\ns_add_u32 m0, %[lw], 22528\ns_add_u32 s44, %[wo], 425984\n" \
"buffer_load_dwordx4 v125, %[rw], s44 offen nt lds\ns_add_u32 m0, %[lw], 23552\ns_add_u32 s44, %[wo], 442368\nbuffer_load_dwordx4 v125, %[rw], s44 offen nt lds\ns_add_u32 m0, %[tb], 0\ns_add_u32 s44, %[wo], 458752\nbuffer_load_dwordx4 v125, %[rw], s44 offen nt lds\ns_add_u32 m0, %[tb], 1024\n" \
"s_add_u32 s44, %[wo], 475136\nbuffer_load_dwordx4 v125, %[rw], s44 offen nt lds\ns_add_u32 m0, %[tb], 2048\ns_add_u32 s44, %[wo], 491520\nbuffer_load_dwordx4 v125, %[rw], s44 offen nt lds\ns_add_u32 m0, %[tb], 3072\ns_add_u32 s44, %[wo], 507904\nbuffer_load_dwordx4 v125, %[rw], s44 offen nt lds\n" \
"s_add_u32 m0, %[tb], 4096\ns_add_u32 s44, %[wo], 524288\nbuffer_load_dwordx4 v125, %[rw], s44 offen nt lds\ns_add_u32 m0, %[tb], 5120\ns_add_u32 s44, %[wo], 540672\nbuffer_load_dwordx4 v125, %[rw], s44 offen nt lds\ns_add_u32 m0, %[tb], 6144\ns_add_u32 s44, %[wo], 557056\n" \
"buffer_load_dwordx4 v125, %[rw], s44 offen nt lds\ns_add_u32 m0, %[tb], 7168\ns_add_u32 s44, %[wo], 573440\nbuffer_load_dwordx4 v125, %[rw], s44 offen nt lds\ns_waitcnt vmcnt(32)\nds_read_b64_tr_b16 a[0:1], v126 offset:8192\nds_read_b64_tr_b16 a[2:3], v126 offset:8704\nds_read_b64_tr_b16 a[8:9], v126 offset:8224\n" \
"ds_read_b64_tr_b16 a[10:11], v126 offset:8736\nds_read_b64_tr_b16 a[16:17], v126 offset:8256\nds_read_b64_tr_b16 a[18:19], v126 offset:8768\nds_read_b64_tr_b16 a[24:25], v126 offset:8288\nds_read_b64_tr_b16 a[26:27], v126 offset:8800\ns_waitcnt lgkmcnt(0)\ns_add_u32 m0, %[tb], 8192\ns_add_u32 s44, %[wo], 589824\n" \
"buffer_load_dwordx4 v125, %[rw], s44 offen nt lds\ns_add_u32 m0, %[tb], 9216\ns_add_u32 s44, %[wo], 606208\nbuffer_load_dwordx4 v125, %[rw], s44 offen nt lds\ns_add_u32 m0, %[tb], 10240\ns_add_u32 s44, %[wo], 622592\nbuffer_load_dwordx4 v125, %[rw], s44 offen nt lds\ns_add_u32 m0, %[tb], 11264\n" \
"s_add_u32 s44, %[wo], 638976\nbuffer_load_dwordx4 v125, %[rw], s44 offen nt lds\ns_waitcnt vmcnt(32)\nds_read_b64_tr_b16 a[4:5], v127 offset:0\nds_read_b64_tr_b16 a[6:7], v127 offset:512\nds_read_b64_tr_b16 a[12:13], v127 offset:32\nds_read_b64_tr_b16 a[14:15], v127 offset:544\nds_read_b64_tr_b16 a[20:21], v127 offset:64\n" \
"ds_read_b64_tr_b16 a[22:23], v127 offset:576\nds_read_b64_tr_b16 a[28:29], v127 offset:96\nds_read_b64_tr_b16 a[30:31], v127 offset:608\ns_waitcnt lgkmcnt(0)\ns_add_u32 m0, %[lw], 0\ns_add_u32 s44, %[wo], 655360\nbuffer_load_dwordx4 v125, %[rw], s44 offen nt lds\ns_add_u32 m0, %[lw], 1024\n" \
"s_add_u32 s44, %[wo], 671744\nbuffer_load_dwordx4 v125, %[rw], s44 offen nt lds\ns_add_u32 m0, %[lw], 2048\ns_add_u32 s44, %[wo], 688128\nbuffer_load_dwordx4 v125, %[rw], s44 offen nt lds\ns_add_u32 m0, %[lw], 3072\ns_add_u32 s44, %[wo], 704512\nbuffer_load_dwordx4 v125, %[rw], s44 offen nt lds\n" \
"s_waitcnt vmcnt(32)\nds_read_b64_tr_b16 a[32:33], v127 offset:4096\nds_read_b64_tr_b16 a[34:35], v127 offset:4608\nds_read_b64_tr_b16 v[92:93], v127 offset:4128\nds_read_b64_tr_b16 v[94:95], v127 offset:4640\nds_read_b64_tr_b16 a[40:41], v127 offset:4160\nds_read_b64_tr_b16 a[42:43], v127 offset:4672\nds_read_b64_tr_b16 a[48:49], v127 offset:4192\n" \
"ds_read_b64_tr_b16 a[50:51], v127 offset:4704\ns_waitcnt lgkmcnt(0)\ns_add_u32 m0, %[lw], 4096\ns_add_u32 s44, %[wo], 720896\nbuffer_load_dwordx4 v125, %[rw], s44 offen nt lds\ns_add_u32 m0, %[lw], 5120\ns_add_u32 s44, %[wo], 737280\nbuffer_load_dwordx4 v125, %[rw], s44 offen nt lds\n" \
"s_add_u32 m0, %[lw], 6144\ns_add_u32 s44, %[wo], 753664\nbuffer_load_dwordx4 v125, %[rw], s44 offen nt lds\ns_add_u32 m0, %[lw], 7168\ns_add_u32 s44, %[wo], 770048\nbuffer_load_dwordx4 v125, %[rw], s44 offen nt lds\ns_waitcnt vmcnt(32)\nds_read_b64_tr_b16 a[36:37], v127 offset:8192\n" \
"ds_read_b64_tr_b16 a[38:39], v127 offset:8704\nds_read_b64_tr_b16 v[88:89], v127 offset:8224\nds_read_b64_tr_b16 v[90:91], v127 offset:8736\nds_read_b64_tr_b16 a[44:45], v127 offset:8256\nds_read_b64_tr_b16 a[46:47], v127 offset:8768\nds_read_b64_tr_b16 a[52:53], v127 offset:8288\nds_read_b64_tr_b16 a[54:55], v127 offset:8800\ns_waitcnt lgkmcnt(0)\n" \
"s_add_u32 m0, %[lw], 8192\ns_add_u32 s44, %[wo], 786432\nbuffer_load_dwordx4 v125, %[rw], s44 offen nt lds\ns_add_u32 m0, %[lw], 9216\ns_add_u32 s44, %[wo], 802816\nbuffer_load_dwordx4 v125, %[rw], s44 offen nt lds\ns_add_u32 m0, %[lw], 10240\ns_add_u32 s44, %[wo], 819200\n" \
"buffer_load_dwordx4 v125, %[rw], s44 offen nt lds\ns_add_u32 m0, %[lw], 11264\ns_add_u32 s44, %[wo], 835584\nbuffer_load_dwordx4 v125, %[rw], s44 offen nt lds\ns_waitcnt vmcnt(32)\nds_read_b64_tr_b16 a[56:57], v127 offset:12288\nds_read_b64_tr_b16 a[58:59], v127 offset:12800\nds_read_b64_tr_b16 a[64:65], v127 offset:12320\n" \
"ds_read_b64_tr_b16 a[66:67], v127 offset:12832\nds_read_b64_tr_b16 a[72:73], v127 offset:12352\nds_read_b64_tr_b16 a[74:75], v127 offset:12864\nds_read_b64_tr_b16 a[80:81], v127 offset:12384\nds_read_b64_tr_b16 a[82:83], v127 offset:12896\ns_waitcnt lgkmcnt(0)\ns_add_u32 m0, %[lw], 12288\ns_add_u32 s44, %[wo], 851968\n" \
"buffer_load_dwordx4 v125, %[rw], s44 offen nt lds\ns_add_u32 m0, %[lw], 13312\ns_add_u32 s44, %[wo], 868352\nbuffer_load_dwordx4 v125, %[rw], s44 offen nt lds\ns_add_u32 m0, %[lw], 14336\ns_add_u32 s44, %[wo], 884736\nbuffer_load_dwordx4 v125, %[rw], s44 offen nt lds\ns_add_u32 m0, %[lw], 15360\n" \
"s_add_u32 s44, %[wo], 901120\nbuffer_load_dwordx4 v125, %[rw], s44 offen nt lds\ns_waitcnt vmcnt(32)\nds_read_b64_tr_b16 a[60:61], v127 offset:16384\nds_read_b64_tr_b16 a[62:63], v127 offset:16896\nds_read_b64_tr_b16 a[68:69], v127 offset:16416\nds_read_b64_tr_b16 a[70:71], v127 offset:16928\nds_read_b64_tr_b16 a[76:77], v127 offset:16448\n" \
"ds_read_b64_tr_b16 a[78:79], v127 offset:16960\nds_read_b64_tr_b16 a[84:85], v127 offset:16480\nds_read_b64_tr_b16 a[86:87], v127 offset:16992\ns_waitcnt lgkmcnt(0)\ns_add_u32 m0, %[lw], 16384\ns_add_u32 s44, %[wo], 917504\nbuffer_load_dwordx4 v125, %[rw], s44 offen nt lds\ns_add_u32 m0, %[lw], 17408\n" \
"s_add_u32 s44, %[wo], 933888\nbuffer_load_dwordx4 v125, %[rw], s44 offen nt lds\ns_add_u32 m0, %[lw], 18432\ns_add_u32 s44, %[wo], 950272\nbuffer_load_dwordx4 v125, %[rw], s44 offen nt lds\ns_add_u32 m0, %[lw], 19456\ns_add_u32 s44, %[wo], 966656\nbuffer_load_dwordx4 v125, %[rw], s44 offen nt lds\n" \
"s_waitcnt vmcnt(32)\nds_read_b64_tr_b16 a[88:89], v127 offset:20480\nds_read_b64_tr_b16 a[90:91], v127 offset:20992\nds_read_b64_tr_b16 a[96:97], v127 offset:20512\nds_read_b64_tr_b16 a[98:99], v127 offset:21024\nds_read_b64_tr_b16 a[104:105], v127 offset:20544\nds_read_b64_tr_b16 a[106:107], v127 offset:21056\nds_read_b64_tr_b16 v[84:85], v127 offset:20576\n" \
"ds_read_b64_tr_b16 v[86:87], v127 offset:21088\ns_waitcnt lgkmcnt(0)\ns_add_u32 m0, %[lw], 20480\ns_add_u32 s44, %[wo], 983040\nbuffer_load_dwordx4 v125, %[rw], s44 offen nt lds\ns_add_u32 m0, %[lw], 21504\ns_add_u32 s44, %[wo], 999424\nbuffer_load_dwordx4 v125, %[rw], s44 offen nt lds\n" \
"s_add_u32 m0, %[lw], 22528\ns_add_u32 s44, %[wo], 1015808\nbuffer_load_dwordx4 v125, %[rw], s44 offen nt lds\ns_add_u32 m0, %[lw], 23552\ns_add_u32 s44, %[wo], 1032192\nbuffer_load_dwordx4 v125, %[rw], s44 offen nt lds\ns_waitcnt vmcnt(32)\nds_read_b64_tr_b16 a[92:93], v126 offset:0\n" \
"ds_read_b64_tr_b16 a[94:95], v126 offset:512\nds_read_b64_tr_b16 a[100:101], v126 offset:32\nds_read_b64_tr_b16 a[102:103], v126 offset:544\nds_read_b64_tr_b16 a[108:109], v126 offset:64\nds_read_b64_tr_b16 a[110:111], v126 offset:576\nds_read_b64_tr_b16 v[80:81], v126 offset:96\nds_read_b64_tr_b16 v[82:83], v126 offset:608\ns_waitcnt lgkmcnt(0)\n" \
"s_add_u32 m0, %[tb], 0\ns_add_u32 s44, %[wo], 1048576\nbuffer_load_dwordx4 v125, %[rw], s44 offen nt lds\ns_add_u32 m0, %[tb], 1024\ns_add_u32 s44, %[wo], 1064960\nbuffer_load_dwordx4 v125, %[rw], s44 offen nt lds\ns_add_u32 m0, %[tb], 2048\ns_add_u32 s44, %[wo], 1081344\n" \
"buffer_load_dwordx4 v125, %[rw], s44 offen nt lds\ns_add_u32 m0, %[tb], 3072\ns_add_u32 s44, %[wo], 1097728\nbuffer_load_dwordx4 v125, %[rw], s44 offen nt lds\ns_waitcnt vmcnt(32)\nds_read_b64_tr_b16 a[112:113], v126 offset:4096\nds_read_b64_tr_b16 a[114:115], v126 offset:4608\nds_read_b64_tr_b16 a[120:121], v126 offset:4128\n" \
"ds_read_b64_tr_b16 a[122:123], v126 offset:4640\nds_read_b64_tr_b16 a[128:129], v126 offset:4160\nds_read_b64_tr_b16 a[130:131], v126 offset:4672\nds_read_b64_tr_b16 a[136:137], v126 offset:4192\nds_read_b64_tr_b16 a[138:139], v126 offset:4704\ns_waitcnt lgkmcnt(0)\ns_add_u32 m0, %[tb], 4096\ns_add_u32 s44, %[wo], 1114112\n" \
"buffer_load_dwordx4 v125, %[rw], s44 offen nt lds\ns_add_u32 m0, %[tb], 5120\ns_add_u32 s44, %[wo], 1130496\nbuffer_load_dwordx4 v125, %[rw], s44 offen nt lds\ns_add_u32 m0, %[tb], 6144\ns_add_u32 s44, %[wo], 1146880\nbuffer_load_dwordx4 v125, %[rw], s44 offen nt lds\ns_add_u32 m0, %[tb], 7168\n" \
"s_add_u32 s44, %[wo], 1163264\nbuffer_load_dwordx4 v125, %[rw], s44 offen nt lds\ns_waitcnt vmcnt(32)\nds_read_b64_tr_b16 a[116:117], v126 offset:8192\nds_read_b64_tr_b16 a[118:119], v126 offset:8704\nds_read_b64_tr_b16 a[124:125], v126 offset:8224\nds_read_b64_tr_b16 a[126:127], v126 offset:8736\nds_read_b64_tr_b16 a[132:133], v126 offset:8256\n" \
"ds_read_b64_tr_b16 a[134:135], v126 offset:8768\nds_read_b64_tr_b16 a[140:141], v126 offset:8288\nds_read_b64_tr_b16 a[142:143], v126 offset:8800\ns_waitcnt lgkmcnt(0)\ns_add_u32 m0, %[tb], 8192\ns_add_u32 s44, %[wo], 1179648\nbuffer_load_dwordx4 v125, %[rw], s44 offen nt lds\ns_add_u32 m0, %[tb], 9216\n" \
"s_add_u32 s44, %[wo], 1196032\nbuffer_load_dwordx4 v125, %[rw], s44 offen nt lds\ns_add_u32 m0, %[tb], 10240\ns_add_u32 s44, %[wo], 1212416\nbuffer_load_dwordx4 v125, %[rw], s44 offen nt lds\ns_add_u32 m0, %[tb], 11264\ns_add_u32 s44, %[wo], 1228800\nbuffer_load_dwordx4 v125, %[rw], s44 offen nt lds\n" \
"s_waitcnt vmcnt(32)\nds_read_b64_tr_b16 a[144:145], v127 offset:0\nds_read_b64_tr_b16 a[146:147], v127 offset:512\nds_read_b64_tr_b16 v[76:77], v127 offset:32\nds_read_b64_tr_b16 v[78:79], v127 offset:544\nds_read_b64_tr_b16 a[152:153], v127 offset:64\nds_read_b64_tr_b16 a[154:155], v127 offset:576\nds_read_b64_tr_b16 a[160:161], v127 offset:96\n" \
"ds_read_b64_tr_b16 a[162:163], v127 offset:608\ns_waitcnt lgkmcnt(0)\ns_add_u32 m0, %[lw], 0\ns_add_u32 s44, %[wo], 1245184\nbuffer_load_dwordx4 v125, %[rw], s44 offen nt lds\ns_add_u32 m0, %[lw], 1024\ns_add_u32 s44, %[wo], 1261568\nbuffer_load_dwordx4 v125, %[rw], s44 offen nt lds\n" \
"s_add_u32 m0, %[lw], 2048\ns_add_u32 s44, %[wo], 1277952\nbuffer_load_dwordx4 v125, %[rw], s44 offen nt lds\ns_add_u32 m0, %[lw], 3072\ns_add_u32 s44, %[wo], 1294336\nbuffer_load_dwordx4 v125, %[rw], s44 offen nt lds\ns_waitcnt vmcnt(32)\nds_read_b64_tr_b16 a[148:149], v127 offset:4096\n" \
"ds_read_b64_tr_b16 a[150:151], v127 offset:4608\nds_read_b64_tr_b16 v[72:73], v127 offset:4128\nds_read_b64_tr_b16 v[74:75], v127 offset:4640\nds_read_b64_tr_b16 a[156:157], v127 offset:4160\nds_read_b64_tr_b16 a[158:159], v127 offset:4672\nds_read_b64_tr_b16 a[164:165], v127 offset:4192\nds_read_b64_tr_b16 a[166:167], v127 offset:4704\ns_waitcnt lgkmcnt(0)\n" \
"s_add_u32 m0, %[lw], 4096\ns_add_u32 s44, %[wo], 1310720\nbuffer_load_dwordx4 v125, %[rw], s44 offen nt lds\ns_add_u32 m0, %[lw], 5120\ns_add_u32 s44, %[wo], 1327104\nbuffer_load_dwordx4 v125, %[rw], s44 offen nt lds\ns_add_u32 m0, %[lw], 6144\ns_add_u32 s44, %[wo], 1343488\n" \
"buffer_load_dwordx4 v125, %[rw], s44 offen nt lds\ns_add_u32 m0, %[lw], 7168\ns_add_u32 s44, %[wo], 1359872\nbuffer_load_dwordx4 v125, %[rw], s44 offen nt lds\ns_waitcnt vmcnt(32)\nds_read_b64_tr_b16 a[168:169], v127 offset:8192\nds_read_b64_tr_b16 a[170:171], v127 offset:8704\nds_read_b64_tr_b16 a[176:177], v127 offset:8224\n" \
"ds_read_b64_tr_b16 a[178:179], v127 offset:8736\nds_read_b64_tr_b16 v[68:69], v127 offset:8256\nds_read_b64_tr_b16 v[70:71], v127 offset:8768\nds_read_b64_tr_b16 a[184:185], v127 offset:8288\nds_read_b64_tr_b16 a[186:187], v127 offset:8800\ns_waitcnt lgkmcnt(0)\ns_add_u32 m0, %[lw], 8192\ns_add_u32 s44, %[wo], 1376256\n" \
"buffer_load_dwordx4 v125, %[rw], s44 offen nt lds\ns_add_u32 m0, %[lw], 9216\ns_add_u32 s44, %[wo], 1392640\nbuffer_load_dwordx4 v125, %[rw], s44 offen nt lds\ns_add_u32 m0, %[lw], 10240\ns_add_u32 s44, %[wo], 1409024\nbuffer_load_dwordx4 v125, %[rw], s44 offen nt lds\ns_add_u32 m0, %[lw], 11264\n" \
"s_add_u32 s44, %[wo], 1425408\nbuffer_load_dwordx4 v125, %[rw], s44 offen nt lds\ns_waitcnt vmcnt(32)\nds_read_b64_tr_b16 a[172:173], v127 offset:12288\nds_read_b64_tr_b16 a[174:175], v127 offset:12800\nds_read_b64_tr_b16 a[180:181], v127 offset:12320\nds_read_b64_tr_b16 a[182:183], v127 offset:12832\nds_read_b64_tr_b16 v[64:65], v127 offset:12352\n" \
"ds_read_b64_tr_b16 v[66:67], v127 offset:12864\nds_read_b64_tr_b16 a[188:189], v127 offset:12384\nds_read_b64_tr_b16 a[190:191], v127 offset:12896\ns_waitcnt lgkmcnt(0)\ns_add_u32 m0, %[lw], 12288\ns_add_u32 s44, %[wo], 1441792\nbuffer_load_dwordx4 v125, %[rw], s44 offen nt lds\ns_add_u32 m0, %[lw], 13312\n" \
"s_add_u32 s44, %[wo], 1458176\nbuffer_load_dwordx4 v125, %[rw], s44 offen nt lds\ns_add_u32 m0, %[lw], 14336\ns_add_u32 s44, %[wo], 1474560\nbuffer_load_dwordx4 v125, %[rw], s44 offen nt lds\ns_add_u32 m0, %[lw], 15360\ns_add_u32 s44, %[wo], 1490944\nbuffer_load_dwordx4 v125, %[rw], s44 offen nt lds\n" \
"s_waitcnt vmcnt(32)\nds_read_b64_tr_b16 a[192:193], v127 offset:16384\nds_read_b64_tr_b16 a[194:195], v127 offset:16896\nds_read_b64_tr_b16 a[200:201], v127 offset:16416\nds_read_b64_tr_b16 a[202:203], v127 offset:16928\nds_read_b64_tr_b16 a[208:209], v127 offset:16448\nds_read_b64_tr_b16 a[210:211], v127 offset:16960\nds_read_b64_tr_b16 a[216:217], v127 offset:16480\n" \
"ds_read_b64_tr_b16 a[218:219], v127 offset:16992\ns_waitcnt lgkmcnt(0)\ns_add_u32 m0, %[lw], 16384\ns_add_u32 s44, %[wo], 1507328\nbuffer_load_dwordx4 v125, %[rw], s44 offen nt lds\ns_add_u32 m0, %[lw], 17408\ns_add_u32 s44, %[wo], 1523712\nbuffer_load_dwordx4 v125, %[rw], s44 offen nt lds\n" \
"s_add_u32 m0, %[lw], 18432\ns_add_u32 s44, %[wo], 1540096\nbuffer_load_dwordx4 v125, %[rw], s44 offen nt lds\ns_add_u32 m0, %[lw], 19456\ns_add_u32 s44, %[wo], 1556480\nbuffer_load_dwordx4 v125, %[rw], s44 offen nt lds\ns_waitcnt vmcnt(32)\nds_read_b64_tr_b16 a[196:197], v127 offset:20480\n" \
"ds_read_b64_tr_b16 a[198:199], v127 offset:20992\nds_read_b64_tr_b16 a[204:205], v127 offset:20512\nds_read_b64_tr_b16 a[206:207], v127 offset:21024\nds_read_b64_tr_b16 a[212:213], v127 offset:20544\nds_read_b64_tr_b16 a[214:215], v127 offset:21056\nds_read_b64_tr_b16 a[220:221], v127 offset:20576\nds_read_b64_tr_b16 a[222:223], v127 offset:21088\ns_waitcnt lgkmcnt(0)\n" \
"s_add_u32 m0, %[lw], 20480\ns_add_u32 s44, %[wo], 1572864\nbuffer_load_dwordx4 v125, %[rw], s44 offen nt lds\ns_add_u32 m0, %[lw], 21504\ns_add_u32 s44, %[wo], 1589248\nbuffer_load_dwordx4 v125, %[rw], s44 offen nt lds\ns_add_u32 m0, %[lw], 22528\ns_add_u32 s44, %[wo], 1605632\n" \
"buffer_load_dwordx4 v125, %[rw], s44 offen nt lds\ns_add_u32 m0, %[lw], 23552\ns_add_u32 s44, %[wo], 1622016\nbuffer_load_dwordx4 v125, %[rw], s44 offen nt lds\ns_waitcnt vmcnt(32)\nds_read_b64_tr_b16 v[60:61], v126 offset:0\nds_read_b64_tr_b16 v[62:63], v126 offset:512\nds_read_b64_tr_b16 a[224:225], v126 offset:32\n" \
"ds_read_b64_tr_b16 a[226:227], v126 offset:544\nds_read_b64_tr_b16 a[232:233], v126 offset:64\nds_read_b64_tr_b16 a[234:235], v126 offset:576\nds_read_b64_tr_b16 a[240:241], v126 offset:96\nds_read_b64_tr_b16 a[242:243], v126 offset:608\ns_waitcnt lgkmcnt(0)\ns_add_u32 m0, %[tb], 0\ns_add_u32 s44, %[wo], 1638400\n" \
"buffer_load_dwordx4 v125, %[rw], s44 offen nt lds\ns_add_u32 m0, %[tb], 1024\ns_add_u32 s44, %[wo], 1654784\nbuffer_load_dwordx4 v125, %[rw], s44 offen nt lds\ns_add_u32 m0, %[tb], 2048\ns_add_u32 s44, %[wo], 1671168\nbuffer_load_dwordx4 v125, %[rw], s44 offen nt lds\ns_add_u32 m0, %[tb], 3072\n" \
"s_add_u32 s44, %[wo], 1687552\nbuffer_load_dwordx4 v125, %[rw], s44 offen nt lds\ns_waitcnt vmcnt(32)\nds_read_b64_tr_b16 v[56:57], v126 offset:4096\nds_read_b64_tr_b16 v[58:59], v126 offset:4608\nds_read_b64_tr_b16 a[228:229], v126 offset:4128\nds_read_b64_tr_b16 a[230:231], v126 offset:4640\nds_read_b64_tr_b16 a[236:237], v126 offset:4160\n" \
"ds_read_b64_tr_b16 a[238:239], v126 offset:4672\nds_read_b64_tr_b16 a[244:245], v126 offset:4192\nds_read_b64_tr_b16 a[246:247], v126 offset:4704\ns_waitcnt lgkmcnt(0)\ns_add_u32 m0, %[tb], 4096\ns_add_u32 s44, %[wo], 1703936\nbuffer_load_dwordx4 v125, %[rw], s44 offen nt lds\ns_add_u32 m0, %[tb], 5120\n" \
"s_add_u32 s44, %[wo], 1720320\nbuffer_load_dwordx4 v125, %[rw], s44 offen nt lds\ns_add_u32 m0, %[tb], 6144\ns_add_u32 s44, %[wo], 1736704\nbuffer_load_dwordx4 v125, %[rw], s44 offen nt lds\ns_add_u32 m0, %[tb], 7168\ns_add_u32 s44, %[wo], 1753088\nbuffer_load_dwordx4 v125, %[rw], s44 offen nt lds\n" \
"s_waitcnt vmcnt(32)\nds_read_b64_tr_b16 a[248:249], v126 offset:8192\nds_read_b64_tr_b16 a[250:251], v126 offset:8704\nds_read_b64_tr_b16 v[128:129], v126 offset:8224\nds_read_b64_tr_b16 v[130:131], v126 offset:8736\nds_read_b64_tr_b16 v[136:137], v126 offset:8256\nds_read_b64_tr_b16 v[138:139], v126 offset:8768\nds_read_b64_tr_b16 v[144:145], v126 offset:8288\n" \
"ds_read_b64_tr_b16 v[146:147], v126 offset:8800\ns_waitcnt lgkmcnt(0)\ns_add_u32 m0, %[tb], 8192\ns_add_u32 s44, %[wo], 1769472\nbuffer_load_dwordx4 v125, %[rw], s44 offen nt lds\ns_add_u32 m0, %[tb], 9216\ns_add_u32 s44, %[wo], 1785856\nbuffer_load_dwordx4 v125, %[rw], s44 offen nt lds\n" \
"s_add_u32 m0, %[tb], 10240\ns_add_u32 s44, %[wo], 1802240\nbuffer_load_dwordx4 v125, %[rw], s44 offen nt lds\ns_add_u32 m0, %[tb], 11264\ns_add_u32 s44, %[wo], 1818624\nbuffer_load_dwordx4 v125, %[rw], s44 offen nt lds\ns_waitcnt vmcnt(32)\nds_read_b64_tr_b16 a[252:253], v127 offset:0\n" \
"ds_read_b64_tr_b16 a[254:255], v127 offset:512\nds_read_b64_tr_b16 v[132:133], v127 offset:32\nds_read_b64_tr_b16 v[134:135], v127 offset:544\nds_read_b64_tr_b16 v[140:141], v127 offset:64\nds_read_b64_tr_b16 v[142:143], v127 offset:576\nds_read_b64_tr_b16 v[148:149], v127 offset:96\nds_read_b64_tr_b16 v[150:151], v127 offset:608\ns_waitcnt lgkmcnt(0)\n" \
"ds_write_b128 v1, v[92:95] offset:0\nds_write_b128 v1, v[88:91] offset:1024\nds_write_b128 v1, v[84:87] offset:2048\nds_write_b128 v1, v[80:83] offset:3072\ns_waitcnt vmcnt(28)\nds_read_b64_tr_b16 v[152:153], v127 offset:4096\nds_read_b64_tr_b16 v[154:155], v127 offset:4608\nds_read_b64_tr_b16 v[160:161], v127 offset:4128\n" \
"ds_read_b64_tr_b16 v[162:163], v127 offset:4640\nds_read_b64_tr_b16 v[80:81], v127 offset:4160\nds_read_b64_tr_b16 v[82:83], v127 offset:4672\nds_read_b64_tr_b16 v[168:169], v127 offset:4192\nds_read_b64_tr_b16 v[170:171], v127 offset:4704\ns_waitcnt lgkmcnt(0)\nds_write_b128 v1, v[76:79] offset:4096\nds_write_b128 v1, v[72:75] offset:5120\n" \
"ds_write_b128 v1, v[68:71] offset:6144\nds_write_b128 v1, v[64:67] offset:7168\ns_waitcnt vmcnt(24)\nds_read_b64_tr_b16 v[156:157], v127 offset:8192\nds_read_b64_tr_b16 v[158:159], v127 offset:8704\nds_read_b64_tr_b16 v[164:165], v127 offset:8224\nds_read_b64_tr_b16 v[166:167], v127 offset:8736\nds_read_b64_tr_b16 v[64:65], v127 offset:8256\n" \
"ds_read_b64_tr_b16 v[66:67], v127 offset:8768\nds_read_b64_tr_b16 v[172:173], v127 offset:8288\nds_read_b64_tr_b16 v[174:175], v127 offset:8800\ns_waitcnt lgkmcnt(0)\nds_write_b128 v1, v[60:63] offset:8192\nds_write_b128 v1, v[56:59] offset:9216\nds_write_b128 v1, v[80:83] offset:10240\nds_write_b128 v1, v[64:67] offset:11264\n" \
"s_waitcnt vmcnt(20)\nds_read_b64_tr_b16 v[176:177], v127 offset:12288\nds_read_b64_tr_b16 v[178:179], v127 offset:12800\nds_read_b64_tr_b16 v[184:185], v127 offset:12320\nds_read_b64_tr_b16 v[186:187], v127 offset:12832\nds_read_b64_tr_b16 v[192:193], v127 offset:12352\nds_read_b64_tr_b16 v[194:195], v127 offset:12864\nds_read_b64_tr_b16 v[200:201], v127 offset:12384\n" \
"ds_read_b64_tr_b16 v[202:203], v127 offset:12896\ns_waitcnt lgkmcnt(0)\ns_waitcnt vmcnt(16)\nds_read_b64_tr_b16 v[180:181], v127 offset:16384\nds_read_b64_tr_b16 v[182:183], v127 offset:16896\nds_read_b64_tr_b16 v[188:189], v127 offset:16416\nds_read_b64_tr_b16 v[190:191], v127 offset:16928\nds_read_b64_tr_b16 v[196:197], v127 offset:16448\n" \
"ds_read_b64_tr_b16 v[198:199], v127 offset:16960\nds_read_b64_tr_b16 v[204:205], v127 offset:16480\nds_read_b64_tr_b16 v[206:207], v127 offset:16992\ns_waitcnt lgkmcnt(0)\ns_waitcnt vmcnt(12)\nds_read_b64_tr_b16 v[64:65], v127 offset:20480\nds_read_b64_tr_b16 v[66:67], v127 offset:20992\nds_read_b64_tr_b16 v[208:209], v127 offset:20512\n" \
"ds_read_b64_tr_b16 v[210:211], v127 offset:21024\nds_read_b64_tr_b16 v[216:217], v127 offset:20544\nds_read_b64_tr_b16 v[218:219], v127 offset:21056\nds_read_b64_tr_b16 v[224:225], v127 offset:20576\nds_read_b64_tr_b16 v[226:227], v127 offset:21088\ns_waitcnt lgkmcnt(0)\nds_write_b128 v1, v[64:67] offset:12288\ns_waitcnt vmcnt(8)\n" \
"ds_read_b64_tr_b16 v[64:65], v126 offset:0\nds_read_b64_tr_b16 v[66:67], v126 offset:512\nds_read_b64_tr_b16 v[212:213], v126 offset:32\nds_read_b64_tr_b16 v[214:215], v126 offset:544\nds_read_b64_tr_b16 v[220:221], v126 offset:64\nds_read_b64_tr_b16 v[222:223], v126 offset:576\nds_read_b64_tr_b16 v[228:229], v126 offset:96\nds_read_b64_tr_b16 v[230:231], v126 offset:608\n" \
"s_waitcnt lgkmcnt(0)\nds_write_b128 v1, v[64:67] offset:13312\ns_waitcnt vmcnt(4)\nds_read_b64_tr_b16 v[232:233], v126 offset:4096\nds_read_b64_tr_b16 v[234:235], v126 offset:4608\nds_read_b64_tr_b16 v[64:65], v126 offset:4128\nds_read_b64_tr_b16 v[66:67], v126 offset:4640\nds_read_b64_tr_b16 v[240:241], v126 offset:4160\n" \
"ds_read_b64_tr_b16 v[242:243], v126 offset:4672\nds_read_b64_tr_b16 v[248:249], v126 offset:4192\nds_read_b64_tr_b16 v[250:251], v126 offset:4704\ns_waitcnt lgkmcnt(0)\nds_write_b128 v1, v[64:67] offset:14336\ns_waitcnt vmcnt(0)\nds_read_b64_tr_b16 v[236:237], v126 offset:8192\nds_read_b64_tr_b16 v[238:239], v126 offset:8704\n" \
"ds_read_b64_tr_b16 v[64:65], v126 offset:8224\nds_read_b64_tr_b16 v[66:67], v126 offset:8736\nds_read_b64_tr_b16 v[244:245], v126 offset:8256\nds_read_b64_tr_b16 v[246:247], v126 offset:8768\nds_read_b64_tr_b16 v[252:253], v126 offset:8288\nds_read_b64_tr_b16 v[254:255], v126 offset:8800\ns_waitcnt lgkmcnt(0)\nds_write_b128 v1, v[64:67] offset:15360\n" \
"s_waitcnt lgkmcnt(0)\ns_barrier\ns_mov_b32 s40, 0\ns_mov_b32 s43, 0\ns_mov_b32 s56, -2147483648\n.p2align 6\n.Ldd_next%=:\ns_cmp_eq_u32 s40, %[b1]\n" \
"s_cselect_b32 s44, 4, 0\nv_xor_b32 v7, s44, v7\nbuffer_load_dword v8, v7, %[rs], 0 offen sc1\ns_lshl_b32 s58, s40, 1\ns_add_u32 s44, s40, 0\ns_lshl_b32 s44, s44, 19\ns_add_u32 s44, s44, %[ho]\ns_lshr_b32 s45, s44, 8\n" \
"s_and_b32 s45, s45, 0x2000\ns_xor_b32 s55, s44, s45\ns_waitcnt vmcnt(0)\nbuffer_store_dword v9, off, %[rs], s56 sc1\ns_mov_b32 s56, -2147483648\n.Ldd_w0%=:\nv_cmp_gt_i32 vcc, s58, v8\ns_cbranch_vccz .Ldd_w0go%=\n" \
"s_cmp_ge_u32 s40, 62\ns_cbranch_scc1 .Ldd_ns671%=\ns_sleep 8\n.Ldd_ns671%=:\ns_sleep 1\nbuffer_load_dword v8, v7, %[rs], 0 offen sc1\ns_waitcnt vmcnt(0)\ns_add_u32 s43, s43, 1\n" \
"s_cmp_lt_u32 s43, 262144\ns_cbranch_scc1 .Ldd_w0%=\ns_branch .Ldd_abort%=\n.Ldd_w0go%=:\ns_add_u32 s60, s55, 0\nbuffer_load_dwordx4 v[48:51], v0, %[rs], s60 offen offset:0 sc1\nbuffer_load_dwordx4 v[52:55], v0, %[rs], s60 offen offset:2048 sc1\ns_add_u32 s60, s55, 4096\n" \
"buffer_load_dwordx4 v[56:59], v0, %[rs], s60 offen offset:0 sc1\nbuffer_load_dwordx4 v[60:63], v0, %[rs], s60 offen offset:2048 sc1\ns_add_u32 s60, s55, 16384\nbuffer_load_dwordx4 v[64:67], v0, %[rs], s60 offen offset:0 sc1\nbuffer_load_dwordx4 v[68:71], v0, %[rs], s60 offen offset:2048 sc1\ns_add_u32 s60, s55, 20480\nbuffer_load_dwordx4 v[72:75], v0, %[rs], s60 offen offset:0 sc1\nbuffer_load_dwordx4 v[76:79], v0, %[rs], s60 offen offset:2048 sc1\n" \
"s_add_u32 s60, s55, 32768\nbuffer_load_dwordx4 v[80:83], v0, %[rs], s60 offen offset:0 sc1\nbuffer_load_dwordx4 v[84:87], v0, %[rs], s60 offen offset:2048 sc1\ns_add_u32 s60, s55, 36864\nbuffer_load_dwordx4 v[88:91], v0, %[rs], s60 offen offset:0 sc1\nbuffer_load_dwordx4 v[92:95], v0, %[rs], s60 offen offset:2048 sc1\ns_add_u32 s60, s55, 49152\nbuffer_load_dwordx4 v[96:99], v0, %[rs], s60 offen offset:0 sc1\n" \
"buffer_load_dwordx4 v[100:103], v0, %[rs], s60 offen offset:2048 sc1\n.Ldd_run%=:\ns_add_u32 s44, s40, 0\ns_lshl_b32 s44, s44, 17\ns_add_u32 s44, s44, %[xn]\ns_lshr_b32 s45, s44, 8\ns_and_b32 s45, s45, 0x2000\ns_xor_b32 s41, s44, s45\n" \
"s_lshl_b32 s44, s40, 16\ns_cmp_eq_u32 %[l], 0\ns_cselect_b32 s42, s41, s44\ns_add_u32 s58, s58, 1\nds_read_b128 v[120:123], v1 offset:0\ns_waitcnt vmcnt(13)\nv_mfma_f32_16x16x32_bf16 v[16:19], a[0:3], v[48:51], 0\nds_read_b128 v[124:127], v1 offset:1024\n" \
"v_mfma_f32_16x16x32_bf16 v[24:27], a[8:11], v[48:51], 0\nbuffer_load_dword v8, v7, %[rs], 0 offen sc1\nv_mfma_f32_16x16x32_bf16 v[32:35], a[16:19], v[48:51], 0\ns_add_u32 s60, s55, 53248\nv_mfma_f32_16x16x32_bf16 v[40:43], a[24:27], v[48:51], 0\ns_waitcnt vmcnt(13)\nbuffer_load_dwordx4 v[48:51], v0, %[rs], s60 offen offset:0 sc1\nv_mfma_f32_16x16x32_bf16 v[16:19], a[4:7], v[52:55], v[16:19]\n" \
"v_mfma_f32_16x16x32_bf16 v[24:27], a[12:15], v[52:55], v[24:27]\nv_mfma_f32_16x16x32_bf16 v[32:35], a[20:23], v[52:55], v[32:35]\nv_mfma_f32_16x16x32_bf16 v[40:43], a[28:31], v[52:55], v[40:43]\ns_waitcnt vmcnt(13)\nbuffer_load_dwordx4 v[52:55], v0, %[rs], s60 offen offset:2048 sc1\nv_mfma_f32_16x16x32_bf16 v[16:19], a[32:35], v[56:59], v[16:19]\ns_waitcnt lgkmcnt(1)\nv_mfma_f32_16x16x32_bf16 v[24:27], v[120:123], v[56:59], v[24:27]\n" \
"v_mfma_f32_16x16x32_bf16 v[32:35], a[40:43], v[56:59], v[32:35]\ns_add_u32 s60, s55, 65536\nv_mfma_f32_16x16x32_bf16 v[40:43], a[48:51], v[56:59], v[40:43]\ns_waitcnt vmcnt(13)\nbuffer_load_dwordx4 v[56:59], v0, %[rs], s60 offen offset:0 sc1\nv_mfma_f32_16x16x32_bf16 v[16:19], a[36:39], v[60:63], v[16:19]\ns_waitcnt lgkmcnt(0)\nds_read_b128 v[120:123], v1 offset:2048\n" \
"v_mfma_f32_16x16x32_bf16 v[24:27], v[124:127], v[60:63], v[24:27]\nv_mfma_f32_16x16x32_bf16 v[32:35], a[44:47], v[60:63], v[32:35]\nv_mfma_f32_16x16x32_bf16 v[40:43], a[52:55], v[60:63], v[40:43]\ns_waitcnt vmcnt(13)\nbuffer_load_dwordx4 v[60:63], v0, %[rs], s60 offen offset:2048 sc1\nv_mfma_f32_16x16x32_bf16 v[16:19], a[56:59], v[64:67], v[16:19]\nds_read_b128 v[124:127], v1 offset:3072\nv_mfma_f32_16x16x32_bf16 v[24:27], a[64:67], v[64:67], v[24:27]\n" \
"v_mfma_f32_16x16x32_bf16 v[32:35], a[72:75], v[64:67], v[32:35]\ns_add_u32 s60, s55, 69632\nv_mfma_f32_16x16x32_bf16 v[40:43], a[80:83], v[64:67], v[40:43]\ns_waitcnt vmcnt(13)\nbuffer_load_dwordx4 v[64:67], v0, %[rs], s60 offen offset:0 sc1\nv_mfma_f32_16x16x32_bf16 v[16:19], a[60:63], v[68:71], v[16:19]\nv_mfma_f32_16x16x32_bf16 v[24:27], a[68:71], v[68:71], v[24:27]\nv_mfma_f32_16x16x32_bf16 v[32:35], a[76:79], v[68:71], v[32:35]\n" \
"v_mfma_f32_16x16x32_bf16 v[40:43], a[84:87], v[68:71], v[40:43]\ns_waitcnt vmcnt(13)\nbuffer_load_dwordx4 v[68:71], v0, %[rs], s60 offen offset:2048 sc1\nv_mfma_f32_16x16x32_bf16 v[16:19], a[88:91], v[72:75], v[16:19]\nv_mfma_f32_16x16x32_bf16 v[24:27], a[96:99], v[72:75], v[24:27]\nv_mfma_f32_16x16x32_bf16 v[32:35], a[104:107], v[72:75], v[32:35]\ns_waitcnt lgkmcnt(1)\ns_add_u32 s60, s55, 81920\n" \
"v_mfma_f32_16x16x32_bf16 v[40:43], v[120:123], v[72:75], v[40:43]\ns_waitcnt vmcnt(13)\nbuffer_load_dwordx4 v[72:75], v0, %[rs], s60 offen offset:0 sc1\nv_mfma_f32_16x16x32_bf16 v[16:19], a[92:95], v[76:79], v[16:19]\nds_read_b128 v[120:123], v1 offset:4096\nv_mfma_f32_16x16x32_bf16 v[24:27], a[100:103], v[76:79], v[24:27]\nv_mfma_f32_16x16x32_bf16 v[32:35], a[108:111], v[76:79], v[32:35]\ns_waitcnt lgkmcnt(1)\n" \
"v_mfma_f32_16x16x32_bf16 v[40:43], v[124:127], v[76:79], v[40:43]\ns_waitcnt vmcnt(13)\nbuffer_load_dwordx4 v[76:79], v0, %[rs], s60 offen offset:2048 sc1\nv_mfma_f32_16x16x32_bf16 v[16:19], a[112:115], v[80:83], v[16:19]\nds_read_b128 v[124:127], v1 offset:5120\nv_mfma_f32_16x16x32_bf16 v[24:27], a[120:123], v[80:83], v[24:27]\nv_mfma_f32_16x16x32_bf16 v[32:35], a[128:131], v[80:83], v[32:35]\ns_add_u32 s60, s55, 86016\n" \
"v_mfma_f32_16x16x32_bf16 v[40:43], a[136:139], v[80:83], v[40:43]\ns_waitcnt vmcnt(13)\nbuffer_load_dwordx4 v[80:83], v0, %[rs], s60 offen offset:0 sc1\nv_mfma_f32_16x16x32_bf16 v[16:19], a[116:119], v[84:87], v[16:19]\nds_read_b128 v[112:115], v1 offset:6144\nv_mfma_f32_16x16x32_bf16 v[24:27], a[124:127], v[84:87], v[24:27]\nv_mfma_f32_16x16x32_bf16 v[32:35], a[132:135], v[84:87], v[32:35]\nv_mfma_f32_16x16x32_bf16 v[40:43], a[140:143], v[84:87], v[40:43]\n" \
"s_waitcnt vmcnt(13)\nbuffer_load_dwordx4 v[84:87], v0, %[rs], s60 offen offset:2048 sc1\nv_mfma_f32_16x16x32_bf16 v[16:19], a[144:147], v[88:91], v[16:19]\ns_waitcnt lgkmcnt(2)\nds_read_b128 v[116:119], v1 offset:7168\nv_mfma_f32_16x16x32_bf16 v[24:27], v[120:123], v[88:91], v[24:27]\nv_mfma_f32_16x16x32_bf16 v[32:35], a[152:155], v[88:91], v[32:35]\ns_add_u32 s60, s55, 98304\n" \
"v_mfma_f32_16x16x32_bf16 v[40:43], a[160:163], v[88:91], v[40:43]\ns_waitcnt vmcnt(13)\nbuffer_load_dwordx4 v[88:91], v0, %[rs], s60 offen offset:0 sc1\nv_mfma_f32_16x16x32_bf16 v[16:19], a[148:151], v[92:95], v[16:19]\ns_waitcnt lgkmcnt(2)\nv_mfma_f32_16x16x32_bf16 v[24:27], v[124:127], v[92:95], v[24:27]\nv_mfma_f32_16x16x32_bf16 v[32:35], a[156:159], v[92:95], v[32:35]\nv_mfma_f32_16x16x32_bf16 v[40:43], a[164:167], v[92:95], v[40:43]\n" \
"s_waitcnt vmcnt(13)\nbuffer_load_dwordx4 v[92:95], v0, %[rs], s60 offen offset:2048 sc1\nv_mfma_f32_16x16x32_bf16 v[16:19], a[168:171], v[96:99], v[16:19]\nv_mfma_f32_16x16x32_bf16 v[24:27], a[176:179], v[96:99], v[24:27]\ns_waitcnt lgkmcnt(1)\nv_mfma_f32_16x16x32_bf16 v[32:35], v[112:115], v[96:99], v[32:35]\ns_add_u32 s60, s55, 102400\nv_mfma_f32_16x16x32_bf16 v[40:43], a[184:187], v[96:99], v[40:43]\n" \
"s_waitcnt vmcnt(13)\nbuffer_load_dwordx4 v[96:99], v0, %[rs], s60 offen offset:0 sc1\nv_mfma_f32_16x16x32_bf16 v[16:19], a[172:175], v[100:103], v[16:19]\nds_read_b128 v[112:115], v1 offset:8192\nv_mfma_f32_16x16x32_bf16 v[24:27], a[180:183], v[100:103], v[24:27]\ns_waitcnt vmcnt(13)\ns_waitcnt lgkmcnt(1)\nv_cmp_gt_i32 vcc, s58, v8\n" \
"v_mfma_f32_16x16x32_bf16 v[32:35], v[116:119], v[100:103], v[32:35]\ns_cmp_eq_u64 vcc, 0\ns_cselect_b32 s61, s55, -2147483648\nv_mfma_f32_16x16x32_bf16 v[40:43], a[188:191], v[100:103], v[40:43]\ns_waitcnt vmcnt(12)\nbuffer_load_dwordx4 v[100:103], v0, %[rs], s60 offen offset:2048 sc1\nv_mfma_f32_16x16x32_bf16 v[16:19], a[192:195], v[48:51], v[16:19]\nds_read_b128 v[116:119], v1 offset:9216\n" \
"v_mfma_f32_16x16x32_bf16 v[24:27], a[200:203], v[48:51], v[24:27]\nv_mfma_f32_16x16x32_bf16 v[32:35], a[208:211], v[48:51], v[32:35]\ns_add_u32 s60, s61, 0\nv_mfma_f32_16x16x32_bf16 v[40:43], a[216:219], v[48:51], v[40:43]\ns_waitcnt vmcnt(12)\nbuffer_load_dwordx4 v[48:51], v0, %[rs], s60 offen offset:1024 sc1\nv_mfma_f32_16x16x32_bf16 v[16:19], a[196:199], v[52:55], v[16:19]\nv_mfma_f32_16x16x32_bf16 v[24:27], a[204:207], v[52:55], v[24:27]\n" \
"v_mfma_f32_16x16x32_bf16 v[32:35], a[212:215], v[52:55], v[32:35]\nv_mfma_f32_16x16x32_bf16 v[40:43], a[220:223], v[52:55], v[40:43]\ns_waitcnt vmcnt(12)\ns_waitcnt lgkmcnt(1)\nbuffer_load_dwordx4 v[52:55], v0, %[rs], s60 offen offset:3072 sc1\nv_mfma_f32_16x16x32_bf16 v[16:19], v[112:115], v[56:59], v[16:19]\nv_mfma_f32_16x16x32_bf16 v[24:27], a[224:227], v[56:59], v[24:27]\nv_mfma_f32_16x16x32_bf16 v[32:35], a[232:235], v[56:59], v[32:35]\n" \
"s_add_u32 s60, s61, 4096\nv_mfma_f32_16x16x32_bf16 v[40:43], a[240:243], v[56:59], v[40:43]\ns_waitcnt vmcnt(12)\ns_waitcnt lgkmcnt(0)\nbuffer_load_dwordx4 v[56:59], v0, %[rs], s60 offen offset:1024 sc1\nv_mfma_f32_16x16x32_bf16 v[16:19], v[116:119], v[60:63], v[16:19]\nds_read_b128 v[112:115], v1 offset:10240\nv_mfma_f32_16x16x32_bf16 v[24:27], a[228:231], v[60:63], v[24:27]\n" \
"v_mfma_f32_16x16x32_bf16 v[32:35], a[236:239], v[60:63], v[32:35]\nv_mfma_f32_16x16x32_bf16 v[40:43], a[244:247], v[60:63], v[40:43]\ns_waitcnt vmcnt(12)\nbuffer_load_dwordx4 v[60:63], v0, %[rs], s60 offen offset:3072 sc1\nv_mfma_f32_16x16x32_bf16 v[16:19], a[248:251], v[64:67], v[16:19]\nds_read_b128 v[116:119], v1 offset:11264\nv_mfma_f32_16x16x32_bf16 v[24:27], v[128:131], v[64:67], v[24:27]\nv_mfma_f32_16x16x32_bf16 v[32:35], v[136:139], v[64:67], v[32:35]\n" \
"s_add_u32 s60, s61, 16384\nv_mfma_f32_16x16x32_bf16 v[40:43], v[144:147], v[64:67], v[40:43]\ns_waitcnt vmcnt(12)\nbuffer_load_dwordx4 v[64:67], v0, %[rs], s60 offen offset:1024 sc1\nv_mfma_f32_16x16x32_bf16 v[16:19], a[252:255], v[68:71], v[16:19]\nv_mfma_f32_16x16x32_bf16 v[24:27], v[132:135], v[68:71], v[24:27]\nv_mfma_f32_16x16x32_bf16 v[32:35], v[140:143], v[68:71], v[32:35]\nv_mfma_f32_16x16x32_bf16 v[40:43], v[148:151], v[68:71], v[40:43]\n" \
"s_waitcnt vmcnt(12)\nbuffer_load_dwordx4 v[68:71], v0, %[rs], s60 offen offset:3072 sc1\nv_mfma_f32_16x16x32_bf16 v[16:19], v[152:155], v[72:75], v[16:19]\nv_mfma_f32_16x16x32_bf16 v[24:27], v[160:163], v[72:75], v[24:27]\ns_waitcnt lgkmcnt(1)\nv_mfma_f32_16x16x32_bf16 v[32:35], v[112:115], v[72:75], v[32:35]\ns_add_u32 s60, s61, 20480\nv_mfma_f32_16x16x32_bf16 v[40:43], v[168:171], v[72:75], v[40:43]\n" \
"s_waitcnt vmcnt(12)\nbuffer_load_dwordx4 v[72:75], v0, %[rs], s60 offen offset:1024 sc1\nv_mfma_f32_16x16x32_bf16 v[16:19], v[156:159], v[76:79], v[16:19]\nds_read_b128 v[112:115], v1 offset:12288\nv_mfma_f32_16x16x32_bf16 v[24:27], v[164:167], v[76:79], v[24:27]\ns_waitcnt lgkmcnt(1)\nv_mfma_f32_16x16x32_bf16 v[32:35], v[116:119], v[76:79], v[32:35]\nv_mfma_f32_16x16x32_bf16 v[40:43], v[172:175], v[76:79], v[40:43]\n" \
"s_waitcnt vmcnt(12)\nbuffer_load_dwordx4 v[76:79], v0, %[rs], s60 offen offset:3072 sc1\nv_mfma_f32_16x16x32_bf16 v[16:19], v[176:179], v[80:83], v[16:19]\nds_read_b128 v[116:119], v1 offset:13312\nv_mfma_f32_16x16x32_bf16 v[24:27], v[184:187], v[80:83], v[24:27]\nv_mfma_f32_16x16x32_bf16 v[32:35], v[192:195], v[80:83], v[32:35]\ns_add_u32 s60, s61, 32768\nv_mfma_f32_16x16x32_bf16 v[40:43], v[200:203], v[80:83], v[40:43]\n" \
"s_waitcnt vmcnt(12)\nbuffer_load_dwordx4 v[80:83], v0, %[rs], s60 offen offset:1024 sc1\nv_mfma_f32_16x16x32_bf16 v[16:19], v[180:183], v[84:87], v[16:19]\nds_read_b128 v[120:123], v1 offset:14336\nv_mfma_f32_16x16x32_bf16 v[24:27], v[188:191], v[84:87], v[24:27]\nv_mfma_f32_16x16x32_bf16 v[32:35], v[196:199], v[84:87], v[32:35]\nv_mfma_f32_16x16x32_bf16 v[40:43], v[204:207], v[84:87], v[40:43]\ns_waitcnt vmcnt(12)\n" \
"s_waitcnt lgkmcnt(2)\nbuffer_load_dwordx4 v[84:87], v0, %[rs], s60 offen offset:3072 sc1\nv_mfma_f32_16x16x32_bf16 v[16:19], v[112:115], v[88:91], v[16:19]\nds_read_b128 v[124:127], v1 offset:15360\nv_mfma_f32_16x16x32_bf16 v[24:27], v[208:211], v[88:91], v[24:27]\nv_mfma_f32_16x16x32_bf16 v[32:35], v[216:219], v[88:91], v[32:35]\ns_add_u32 s60, s61, 36864\nv_mfma_f32_16x16x32_bf16 v[40:43], v[224:227], v[88:91], v[40:43]\n" \
"s_waitcnt vmcnt(12)\ns_waitcnt lgkmcnt(2)\nbuffer_load_dwordx4 v[88:91], v0, %[rs], s60 offen offset:1024 sc1\nv_mfma_f32_16x16x32_bf16 v[16:19], v[116:119], v[92:95], v[16:19]\nv_mfma_f32_16x16x32_bf16 v[24:27], v[212:215], v[92:95], v[24:27]\nv_mfma_f32_16x16x32_bf16 v[32:35], v[220:223], v[92:95], v[32:35]\nv_mfma_f32_16x16x32_bf16 v[40:43], v[228:231], v[92:95], v[40:43]\ns_waitcnt vmcnt(12)\n" \
"buffer_load_dwordx4 v[92:95], v0, %[rs], s60 offen offset:3072 sc1\nv_mfma_f32_16x16x32_bf16 v[16:19], v[232:235], v[96:99], v[16:19]\ns_waitcnt lgkmcnt(1)\nv_mfma_f32_16x16x32_bf16 v[24:27], v[120:123], v[96:99], v[24:27]\nv_mfma_f32_16x16x32_bf16 v[32:35], v[240:243], v[96:99], v[32:35]\ns_add_u32 s60, s61, 49152\nv_mfma_f32_16x16x32_bf16 v[40:43], v[248:251], v[96:99], v[40:43]\ns_waitcnt vmcnt(12)\n" \
"buffer_load_dwordx4 v[96:99], v0, %[rs], s60 offen offset:1024 sc1\nv_mfma_f32_16x16x32_bf16 v[16:19], v[236:239], v[100:103], v[16:19]\ns_waitcnt lgkmcnt(0)\nds_read_b128 v[120:123], v1 offset:0\nv_mfma_f32_16x16x32_bf16 v[24:27], v[124:127], v[100:103], v[24:27]\nv_mfma_f32_16x16x32_bf16 v[32:35], v[244:247], v[100:103], v[32:35]\nv_mfma_f32_16x16x32_bf16 v[40:43], v[252:255], v[100:103], v[40:43]\nbuffer_load_dwordx4 v[100:103], v0, %[rs], s60 offen offset:3072 sc1\n" \
"s_cbranch_vccnz .Ldd_slow%=\n.Ldd_slowr%=:\ns_waitcnt vmcnt(13)\nv_mfma_f32_16x16x32_bf16 v[20:23], a[0:3], v[48:51], 0\nds_read_b128 v[124:127], v1 offset:1024\nv_mfma_f32_16x16x32_bf16 v[28:31], a[8:11], v[48:51], 0\nv_mfma_f32_16x16x32_bf16 v[36:39], a[16:19], v[48:51], 0\ns_add_u32 s60, s55, 53248\n" \
"v_mfma_f32_16x16x32_bf16 v[44:47], a[24:27], v[48:51], 0\ns_waitcnt vmcnt(12)\nbuffer_load_dwordx4 v[48:51], v0, %[rs], s60 offen offset:1024 sc1\nv_mfma_f32_16x16x32_bf16 v[20:23], a[4:7], v[52:55], v[20:23]\nv_mfma_f32_16x16x32_bf16 v[28:31], a[12:15], v[52:55], v[28:31]\nv_mfma_f32_16x16x32_bf16 v[36:39], a[20:23], v[52:55], v[36:39]\nv_mfma_f32_16x16x32_bf16 v[44:47], a[28:31], v[52:55], v[44:47]\ns_waitcnt vmcnt(12)\n" \
"buffer_load_dwordx4 v[52:55], v0, %[rs], s60 offen offset:3072 sc1\nv_mfma_f32_16x16x32_bf16 v[20:23], a[32:35], v[56:59], v[20:23]\ns_waitcnt lgkmcnt(1)\nv_mfma_f32_16x16x32_bf16 v[28:31], v[120:123], v[56:59], v[28:31]\nds_write_b128 v2, v[16:19] offset:0\nv_mfma_f32_16x16x32_bf16 v[36:39], a[40:43], v[56:59], v[36:39]\ns_add_u32 s60, s55, 65536\nv_mfma_f32_16x16x32_bf16 v[44:47], a[48:51], v[56:59], v[44:47]\n" \
"s_waitcnt vmcnt(12)\nbuffer_load_dwordx4 v[56:59], v0, %[rs], s60 offen offset:1024 sc1\nv_mfma_f32_16x16x32_bf16 v[20:23], a[36:39], v[60:63], v[20:23]\ns_waitcnt lgkmcnt(1)\nds_read_b128 v[120:123], v1 offset:2048\nv_mfma_f32_16x16x32_bf16 v[28:31], v[124:127], v[60:63], v[28:31]\nv_mfma_f32_16x16x32_bf16 v[36:39], a[44:47], v[60:63], v[36:39]\nv_mfma_f32_16x16x32_bf16 v[44:47], a[52:55], v[60:63], v[44:47]\n" \
"s_waitcnt vmcnt(12)\nbuffer_load_dwordx4 v[60:63], v0, %[rs], s60 offen offset:3072 sc1\nv_mfma_f32_16x16x32_bf16 v[20:23], a[56:59], v[64:67], v[20:23]\nds_read_b128 v[124:127], v1 offset:3072\nv_mfma_f32_16x16x32_bf16 v[28:31], a[64:67], v[64:67], v[28:31]\nds_write_b128 v2, v[24:27] offset:2048\nv_mfma_f32_16x16x32_bf16 v[36:39], a[72:75], v[64:67], v[36:39]\ns_add_u32 s60, s55, 69632\n" \
"v_mfma_f32_16x16x32_bf16 v[44:47], a[80:83], v[64:67], v[44:47]\ns_waitcnt vmcnt(12)\nbuffer_load_dwordx4 v[64:67], v0, %[rs], s60 offen offset:1024 sc1\nv_mfma_f32_16x16x32_bf16 v[20:23], a[60:63], v[68:71], v[20:23]\nv_mfma_f32_16x16x32_bf16 v[28:31], a[68:71], v[68:71], v[28:31]\nv_mfma_f32_16x16x32_bf16 v[36:39], a[76:79], v[68:71], v[36:39]\nv_mfma_f32_16x16x32_bf16 v[44:47], a[84:87], v[68:71], v[44:47]\ns_waitcnt vmcnt(12)\n" \
"buffer_load_dwordx4 v[68:71], v0, %[rs], s60 offen offset:3072 sc1\nv_mfma_f32_16x16x32_bf16 v[20:23], a[88:91], v[72:75], v[20:23]\nv_mfma_f32_16x16x32_bf16 v[28:31], a[96:99], v[72:75], v[28:31]\nds_write_b128 v2, v[32:35] offset:4096\nv_mfma_f32_16x16x32_bf16 v[36:39], a[104:107], v[72:75], v[36:39]\ns_waitcnt lgkmcnt(3)\ns_add_u32 s60, s55, 81920\nv_mfma_f32_16x16x32_bf16 v[44:47], v[120:123], v[72:75], v[44:47]\n" \
"s_waitcnt vmcnt(12)\nbuffer_load_dwordx4 v[72:75], v0, %[rs], s60 offen offset:1024 sc1\nv_mfma_f32_16x16x32_bf16 v[20:23], a[92:95], v[76:79], v[20:23]\nds_read_b128 v[120:123], v1 offset:4096\nv_mfma_f32_16x16x32_bf16 v[28:31], a[100:103], v[76:79], v[28:31]\nv_mfma_f32_16x16x32_bf16 v[36:39], a[108:111], v[76:79], v[36:39]\ns_waitcnt lgkmcnt(3)\nv_mfma_f32_16x16x32_bf16 v[44:47], v[124:127], v[76:79], v[44:47]\n" \
"s_waitcnt vmcnt(12)\nbuffer_load_dwordx4 v[76:79], v0, %[rs], s60 offen offset:3072 sc1\nv_mfma_f32_16x16x32_bf16 v[20:23], a[112:115], v[80:83], v[20:23]\nds_read_b128 v[124:127], v1 offset:5120\nv_mfma_f32_16x16x32_bf16 v[28:31], a[120:123], v[80:83], v[28:31]\nds_write_b128 v2, v[40:43] offset:6144\nv_mfma_f32_16x16x32_bf16 v[36:39], a[128:131], v[80:83], v[36:39]\ns_add_u32 s60, s55, 86016\n" \
"v_mfma_f32_16x16x32_bf16 v[44:47], a[136:139], v[80:83], v[44:47]\ns_waitcnt vmcnt(12)\nbuffer_load_dwordx4 v[80:83], v0, %[rs], s60 offen offset:1024 sc1\nv_mfma_f32_16x16x32_bf16 v[20:23], a[116:119], v[84:87], v[20:23]\nds_read_b128 v[112:115], v1 offset:6144\nv_mfma_f32_16x16x32_bf16 v[28:31], a[124:127], v[84:87], v[28:31]\nv_mfma_f32_16x16x32_bf16 v[36:39], a[132:135], v[84:87], v[36:39]\nv_mfma_f32_16x16x32_bf16 v[44:47], a[140:143], v[84:87], v[44:47]\n" \
"s_waitcnt vmcnt(12)\nbuffer_load_dwordx4 v[84:87], v0, %[rs], s60 offen offset:3072 sc1\nv_mfma_f32_16x16x32_bf16 v[20:23], a[144:147], v[88:91], v[20:23]\ns_waitcnt lgkmcnt(3)\nds_read_b128 v[116:119], v1 offset:7168\nv_mfma_f32_16x16x32_bf16 v[28:31], v[120:123], v[88:91], v[28:31]\nv_mfma_f32_16x16x32_bf16 v[36:39], a[152:155], v[88:91], v[36:39]\ns_add_u32 s60, s55, 98304\n" \
"v_mfma_f32_16x16x32_bf16 v[44:47], a[160:163], v[88:91], v[44:47]\ns_waitcnt vmcnt(12)\nbuffer_load_dwordx4 v[88:91], v0, %[rs], s60 offen offset:1024 sc1\nv_mfma_f32_16x16x32_bf16 v[20:23], a[148:151], v[92:95], v[20:23]\ns_waitcnt lgkmcnt(3)\nv_mfma_f32_16x16x32_bf16 v[28:31], v[124:127], v[92:95], v[28:31]\nbuffer_load_dwordx4 v[12:15], v4, %[rs], s41 offen sc1\nv_mfma_f32_16x16x32_bf16 v[36:39], a[156:159], v[92:95], v[36:39]\n" \
"v_mfma_f32_16x16x32_bf16 v[44:47], a[164:167], v[92:95], v[44:47]\ns_waitcnt vmcnt(13)\nbuffer_load_dwordx4 v[92:95], v0, %[rs], s60 offen offset:3072 sc1\nv_mfma_f32_16x16x32_bf16 v[20:23], a[168:171], v[96:99], v[20:23]\nv_mfma_f32_16x16x32_bf16 v[28:31], a[176:179], v[96:99], v[28:31]\ns_waitcnt lgkmcnt(1)\nv_mfma_f32_16x16x32_bf16 v[36:39], v[112:115], v[96:99], v[36:39]\ns_add_u32 s60, s55, 102400\n" \
"v_mfma_f32_16x16x32_bf16 v[44:47], a[184:187], v[96:99], v[44:47]\ns_waitcnt vmcnt(13)\nbuffer_load_dwordx4 v[96:99], v0, %[rs], s60 offen offset:1024 sc1\nv_mfma_f32_16x16x32_bf16 v[20:23], a[172:175], v[100:103], v[20:23]\nds_read_b128 v[112:115], v1 offset:8192\nv_mfma_f32_16x16x32_bf16 v[28:31], a[180:183], v[100:103], v[28:31]\ns_waitcnt lgkmcnt(1)\nv_mfma_f32_16x16x32_bf16 v[36:39], v[116:119], v[100:103], v[36:39]\n" \
"v_mfma_f32_16x16x32_bf16 v[44:47], a[188:191], v[100:103], v[44:47]\ns_waitcnt vmcnt(13)\nbuffer_load_dwordx4 v[100:103], v0, %[rs], s60 offen offset:3072 sc1\nv_mfma_f32_16x16x32_bf16 v[20:23], a[192:195], v[48:51], v[20:23]\nds_read_b128 v[116:119], v1 offset:9216\nv_mfma_f32_16x16x32_bf16 v[28:31], a[200:203], v[48:51], v[28:31]\nv_mfma_f32_16x16x32_bf16 v[36:39], a[208:211], v[48:51], v[36:39]\nv_mfma_f32_16x16x32_bf16 v[44:47], a[216:219], v[48:51], v[44:47]\n" \
"s_waitcnt vmcnt(13)\nv_mfma_f32_16x16x32_bf16 v[20:23], a[196:199], v[52:55], v[20:23]\nv_mfma_f32_16x16x32_bf16 v[28:31], a[204:207], v[52:55], v[28:31]\nv_mfma_f32_16x16x32_bf16 v[36:39], a[212:215], v[52:55], v[36:39]\nv_mfma_f32_16x16x32_bf16 v[44:47], a[220:223], v[52:55], v[44:47]\ns_waitcnt vmcnt(12)\ns_waitcnt lgkmcnt(1)\nv_mfma_f32_16x16x32_bf16 v[20:23], v[112:115], v[56:59], v[20:23]\n" \
"v_mfma_f32_16x16x32_bf16 v[28:31], a[224:227], v[56:59], v[28:31]\nv_mfma_f32_16x16x32_bf16 v[36:39], a[232:235], v[56:59], v[36:39]\nv_mfma_f32_16x16x32_bf16 v[44:47], a[240:243], v[56:59], v[44:47]\ns_waitcnt vmcnt(11)\ns_waitcnt lgkmcnt(0)\nv_mfma_f32_16x16x32_bf16 v[20:23], v[116:119], v[60:63], v[20:23]\nds_read_b128 v[112:115], v1 offset:10240\nv_mfma_f32_16x16x32_bf16 v[28:31], a[228:231], v[60:63], v[28:31]\n" \
"v_mfma_f32_16x16x32_bf16 v[36:39], a[236:239], v[60:63], v[36:39]\nv_mfma_f32_16x16x32_bf16 v[44:47], a[244:247], v[60:63], v[44:47]\ns_waitcnt vmcnt(10)\nv_mfma_f32_16x16x32_bf16 v[20:23], a[248:251], v[64:67], v[20:23]\nds_read_b128 v[116:119], v1 offset:11264\nv_mfma_f32_16x16x32_bf16 v[28:31], v[128:131], v[64:67], v[28:31]\nv_mfma_f32_16x16x32_bf16 v[36:39], v[136:139], v[64:67], v[36:39]\nv_mfma_f32_16x16x32_bf16 v[44:47], v[144:147], v[64:67], v[44:47]\n" \
"s_waitcnt vmcnt(9)\nv_mfma_f32_16x16x32_bf16 v[20:23], a[252:255], v[68:71], v[20:23]\nv_mfma_f32_16x16x32_bf16 v[28:31], v[132:135], v[68:71], v[28:31]\nv_mfma_f32_16x16x32_bf16 v[36:39], v[140:143], v[68:71], v[36:39]\nv_mfma_f32_16x16x32_bf16 v[44:47], v[148:151], v[68:71], v[44:47]\ns_waitcnt vmcnt(8)\nv_mfma_f32_16x16x32_bf16 v[20:23], v[152:155], v[72:75], v[20:23]\nv_mfma_f32_16x16x32_bf16 v[28:31], v[160:163], v[72:75], v[28:31]\n" \
"s_waitcnt lgkmcnt(1)\nv_mfma_f32_16x16x32_bf16 v[36:39], v[112:115], v[72:75], v[36:39]\nv_mfma_f32_16x16x32_bf16 v[44:47], v[168:171], v[72:75], v[44:47]\ns_waitcnt vmcnt(7)\nv_mfma_f32_16x16x32_bf16 v[20:23], v[156:159], v[76:79], v[20:23]\nds_read_b128 v[112:115], v1 offset:12288\nv_mfma_f32_16x16x32_bf16 v[28:31], v[164:167], v[76:79], v[28:31]\ns_waitcnt lgkmcnt(1)\n" \
"v_mfma_f32_16x16x32_bf16 v[36:39], v[116:119], v[76:79], v[36:39]\nv_mfma_f32_16x16x32_bf16 v[44:47], v[172:175], v[76:79], v[44:47]\ns_waitcnt vmcnt(6)\nv_mfma_f32_16x16x32_bf16 v[20:23], v[176:179], v[80:83], v[20:23]\nds_read_b128 v[116:119], v1 offset:13312\nv_mfma_f32_16x16x32_bf16 v[28:31], v[184:187], v[80:83], v[28:31]\nv_mfma_f32_16x16x32_bf16 v[36:39], v[192:195], v[80:83], v[36:39]\nv_mfma_f32_16x16x32_bf16 v[44:47], v[200:203], v[80:83], v[44:47]\n" \
"s_waitcnt vmcnt(5)\nv_mfma_f32_16x16x32_bf16 v[20:23], v[180:183], v[84:87], v[20:23]\nds_read_b128 v[120:123], v1 offset:14336\nv_mfma_f32_16x16x32_bf16 v[28:31], v[188:191], v[84:87], v[28:31]\nv_mfma_f32_16x16x32_bf16 v[36:39], v[196:199], v[84:87], v[36:39]\nv_mfma_f32_16x16x32_bf16 v[44:47], v[204:207], v[84:87], v[44:47]\ns_waitcnt vmcnt(4)\ns_waitcnt lgkmcnt(2)\n" \
"v_mfma_f32_16x16x32_bf16 v[20:23], v[112:115], v[88:91], v[20:23]\nds_read_b128 v[124:127], v1 offset:15360\nv_mfma_f32_16x16x32_bf16 v[28:31], v[208:211], v[88:91], v[28:31]\nv_mfma_f32_16x16x32_bf16 v[36:39], v[216:219], v[88:91], v[36:39]\nv_mfma_f32_16x16x32_bf16 v[44:47], v[224:227], v[88:91], v[44:47]\ns_waitcnt vmcnt(2)\ns_waitcnt lgkmcnt(2)\nv_mfma_f32_16x16x32_bf16 v[20:23], v[116:119], v[92:95], v[20:23]\n" \
"v_mfma_f32_16x16x32_bf16 v[28:31], v[212:215], v[92:95], v[28:31]\nv_mfma_f32_16x16x32_bf16 v[36:39], v[220:223], v[92:95], v[36:39]\nv_mfma_f32_16x16x32_bf16 v[44:47], v[228:231], v[92:95], v[44:47]\ns_waitcnt vmcnt(1)\nv_mfma_f32_16x16x32_bf16 v[20:23], v[232:235], v[96:99], v[20:23]\ns_waitcnt lgkmcnt(1)\nv_mfma_f32_16x16x32_bf16 v[28:31], v[120:123], v[96:99], v[28:31]\nv_mfma_f32_16x16x32_bf16 v[36:39], v[240:243], v[96:99], v[36:39]\n" \
"v_mfma_f32_16x16x32_bf16 v[44:47], v[248:251], v[96:99], v[44:47]\ns_waitcnt vmcnt(0)\nv_mfma_f32_16x16x32_bf16 v[20:23], v[236:239], v[100:103], v[20:23]\ns_waitcnt lgkmcnt(0)\nv_lshlrev_b32 v104, 16, v12\nv_and_b32 v105, 0xffff0000, v12\nv_mfma_f32_16x16x32_bf16 v[28:31], v[124:127], v[100:103], v[28:31]\nv_lshlrev_b32 v106, 16, v13\n" \
"v_and_b32 v107, 0xffff0000, v13\nv_mfma_f32_16x16x32_bf16 v[36:39], v[244:247], v[100:103], v[36:39]\nv_lshlrev_b32 v108, 16, v14\nv_and_b32 v109, 0xffff0000, v14\nv_mfma_f32_16x16x32_bf16 v[44:47], v[252:255], v[100:103], v[44:47]\ns_mul_i32 s44, s40, %[l]\nds_write_b128 v2, v[20:23] offset:1024\nds_write_b128 v2, v[28:31] offset:3072\n" \
"s_nop 0\nds_write_b128 v2, v[36:39] offset:5120\ns_nop 2\nds_write_b128 v2, v[44:47] offset:7168\nv_lshlrev_b32 v110, 16, v15\nv_and_b32 v111, 0xffff0000, v15\nv_mov_b32 v9, s40\ns_cmp_eq_u32 s44, 0\n" \
"s_cselect_b32 s56, %[fd], -2147483648\ns_add_u32 s40, s40, 1\ns_waitcnt lgkmcnt(0)\ns_barrier\nds_read_b128 v[16:19], v3 offset:0\nds_read_b128 v[20:23], v3 offset:256\nds_read_b128 v[24:27], v3 offset:8192\nds_read_b128 v[28:31], v3 offset:8448\n" \
"ds_read_b128 v[32:35], v3 offset:16384\nds_read_b128 v[36:39], v3 offset:16640\nds_read_b128 v[40:43], v3 offset:24576\nds_read_b128 v[44:47], v3 offset:24832\ns_waitcnt lgkmcnt(6)\nv_pk_add_f32 v[16:17], v[16:17], v[104:105]\nv_pk_add_f32 v[18:19], v[18:19], v[106:107]\nv_pk_add_f32 v[20:21], v[20:21], v[108:109]\n" \
"v_pk_add_f32 v[22:23], v[22:23], v[110:111]\ns_waitcnt lgkmcnt(4)\nv_pk_add_f32 v[16:17], v[16:17], v[24:25]\nv_pk_add_f32 v[18:19], v[18:19], v[26:27]\nv_pk_add_f32 v[20:21], v[20:21], v[28:29]\nv_pk_add_f32 v[22:23], v[22:23], v[30:31]\ns_waitcnt lgkmcnt(2)\nv_pk_add_f32 v[16:17], v[16:17], v[32:33]\n" \
"v_pk_add_f32 v[18:19], v[18:19], v[34:35]\nv_pk_add_f32 v[20:21], v[20:21], v[36:37]\nv_pk_add_f32 v[22:23], v[22:23], v[38:39]\ns_waitcnt lgkmcnt(0)\nv_pk_add_f32 v[16:17], v[16:17], v[40:41]\nv_pk_add_f32 v[18:19], v[18:19], v[42:43]\nv_pk_add_f32 v[20:21], v[20:21], v[44:45]\nv_pk_add_f32 v[22:23], v[22:23], v[46:47]\n" \
"v_cvt_pk_bf16_f32 v12, v16, v17\nv_cvt_pk_bf16_f32 v13, v18, v19\nv_cvt_pk_bf16_f32 v14, v20, v21\nv_cvt_pk_bf16_f32 v15, v22, v23\nbuffer_store_dwordx4 v[12:15], v5, %[ro], s42 offen sc1\nv_pk_add_f32 v[24:25], v[16:17], v[18:19]\nv_pk_add_f32 v[26:27], v[20:21], v[22:23]\nv_pk_add_f32 v[24:25], v[24:25], v[26:27]\n" \
"v_add_f32 v24, v24, v25\ns_nop 1\nv_add_f32_dpp v25, v24, v24 row_ror:8 row_mask:0xf bank_mask:0xf\nv_add_f32_dpp v26, v24, v24 row_ror:8 row_mask:0xf bank_mask:0xf\ns_nop 1\nv_permlane16_swap_b32 v25, v26\nv_add_f32 v24, v25, v26\nv_add_f32 v25, v25, v26\n" \
"s_nop 1\nv_permlane32_swap_b32 v24, v25\nv_add_f32 v24, v24, v25\nv_mul_f32 v24, 0x3c800000, v24\nv_pk_add_f32 v[16:17], v[16:17], v[24:25] op_sel_hi:[1,0] neg_lo:[0,1] neg_hi:[0,1]\nv_pk_add_f32 v[18:19], v[18:19], v[24:25] op_sel_hi:[1,0] neg_lo:[0,1] neg_hi:[0,1]\nv_pk_add_f32 v[20:21], v[20:21], v[24:25] op_sel_hi:[1,0] neg_lo:[0,1] neg_hi:[0,1]\nv_pk_add_f32 v[22:23], v[22:23], v[24:25] op_sel_hi:[1,0] neg_lo:[0,1] neg_hi:[0,1]\n" \
"v_pk_mul_f32 v[28:29], v[16:17], v[16:17]\nv_pk_mul_f32 v[30:31], v[18:19], v[18:19]\nv_pk_fma_f32 v[28:29], v[20:21], v[20:21], v[28:29]\nv_pk_fma_f32 v[30:31], v[22:23], v[22:23], v[30:31]\nv_pk_add_f32 v[28:29], v[28:29], v[30:31]\nv_add_f32 v28, v28, v29\ns_nop 1\nv_add_f32_dpp v29, v28, v28 row_ror:8 row_mask:0xf bank_mask:0xf\n" \
"v_add_f32_dpp v30, v28, v28 row_ror:8 row_mask:0xf bank_mask:0xf\ns_nop 1\nv_permlane16_swap_b32 v29, v30\nv_add_f32 v28, v29, v30\nv_add_f32 v29, v29, v30\ns_nop 1\nv_permlane32_swap_b32 v28, v29\nv_add_f32 v28, v28, v29\n" \
"v_mov_b32 v25, v28\nbuffer_store_dwordx2 v[24:25], v6, %[rs], s41 offen sc1\nv_xor_b32 v2, 0x8000, v2\nv_xor_b32 v3, 0x8000, v3\ns_cmp_lt_u32 s40, 64\ns_cbranch_scc1 .Ldd_next%=\ns_waitcnt vmcnt(0)\nbuffer_store_dword v9, off, %[rs], s56 sc1\n" \
"s_branch .Ldd_end%=\n.Ldd_slow%=:\n.Ldd_w1%=:\nv_cmp_gt_i32 vcc, s58, v8\ns_cbranch_vccz .Ldd_w1go%=\ns_cmp_ge_u32 s40, 62\ns_cbranch_scc1 .Ldd_ns1228%=\ns_sleep 8\n" \
".Ldd_ns1228%=:\ns_sleep 1\nbuffer_load_dword v8, v7, %[rs], 0 offen sc1\ns_waitcnt vmcnt(0)\ns_add_u32 s43, s43, 1\ns_cmp_lt_u32 s43, 262144\ns_cbranch_scc1 .Ldd_w1%=\ns_branch .Ldd_abort%=\n" \
".Ldd_w1go%=:\ns_add_u32 s60, s55, 0\nbuffer_load_dwordx4 v[48:51], v0, %[rs], s60 offen offset:1024 sc1\nbuffer_load_dwordx4 v[52:55], v0, %[rs], s60 offen offset:3072 sc1\ns_add_u32 s60, s55, 4096\nbuffer_load_dwordx4 v[56:59], v0, %[rs], s60 offen offset:1024 sc1\nbuffer_load_dwordx4 v[60:63], v0, %[rs], s60 offen offset:3072 sc1\ns_add_u32 s60, s55, 16384\n" \
"buffer_load_dwordx4 v[64:67], v0, %[rs], s60 offen offset:1024 sc1\nbuffer_load_dwordx4 v[68:71], v0, %[rs], s60 offen offset:3072 sc1\ns_add_u32 s60, s55, 20480\nbuffer_load_dwordx4 v[72:75], v0, %[rs], s60 offen offset:1024 sc1\nbuffer_load_dwordx4 v[76:79], v0, %[rs], s60 offen offset:3072 sc1\ns_add_u32 s60, s55, 32768\nbuffer_load_dwordx4 v[80:83], v0, %[rs], s60 offen offset:1024 sc1\nbuffer_load_dwordx4 v[84:87], v0, %[rs], s60 offen offset:3072 sc1\n" \
"s_add_u32 s60, s55, 36864\nbuffer_load_dwordx4 v[88:91], v0, %[rs], s60 offen offset:1024 sc1\nbuffer_load_dwordx4 v[92:95], v0, %[rs], s60 offen offset:3072 sc1\ns_add_u32 s60, s55, 49152\nbuffer_load_dwordx4 v[96:99], v0, %[rs], s60 offen offset:1024 sc1\nbuffer_load_dwordx4 v[100:103], v0, %[rs], s60 offen offset:3072 sc1\ns_branch .Ldd_slowr%=\n.Ldd_abort%=:\n" \
"s_mov_b64 exec, -1\nv_mov_b32 v16, 0\ns_movk_i32 s44, 6400\nbuffer_store_dword v16, off, %[rs], s44 sc1\n.Ldd_end%=:\ns_waitcnt vmcnt(0) lgkmcnt(0)\n" \
""
#define GSW 0
#define PX4 1
#define QX4 7
#define G1F 1
#define A1F 0

#define V10(p, x) p #x "0", p #x "1", p #x "2", p #x "3", p #x "4", p #x "5", p #x "6", p #x "7", p #x "8", p #x "9"
#define V256(p) V10(p, ), V10(p, 1), V10(p, 2), V10(p, 3), V10(p, 4), V10(p, 5), V10(p, 6), V10(p, 7), V10(p, 8), V10(p, 9), V10(p, 10), V10(p, 11), V10(p, 12), V10(p, 13), V10(p, 14), V10(p, 15), V10(p, 16), \
    V10(p, 17), V10(p, 18), V10(p, 19), V10(p, 20), V10(p, 21), V10(p, 22), V10(p, 23), V10(p, 24), p "250", p "251", p "252", p "253", p "254", p "255"
#define CLOB "memory", "vcc", "scc", V256("v"), V256("a"), V10("s", 4), V10("s", 5), V10("s", 6), V10("s", 7), V10("s", 8)
template <int N> DEVI void table(lds_char* p, const unsigned (&t)[N]) {
#pragma unroll
    for (int i = 0; i < N; ++i) *(l1*)(p + i * 256) = t[i];
    WL0;
}
DEVI unsigned prof_at(int cu, int w) { return O_PROF + (262144 + cu * 2560 + w * 8) * 4; }
DEVI unsigned sp_idx(int lane) { return (lane & 2) ? 0xEEEEu : 0x4444u; }
DEVI int sp_row(int r, int hf) { return 4 * (r >> 1) + 2 * hf + (r & 1); }

DEVI void qk_role(const Args& a, const int w, lds_char* smem, int l, int h) {
    const int lane = lane_id(), g = lane >> 4, n = lane & 15, b = n & 3, p = n >> 2;
    const unsigned l0 = (unsigned)(size_t)smem, tb = l0 + w * 12288, lw = l0 + 55296 + w * 24576, ls = l * SD, IMG = l0 + 32768, MIM = l0 + 49152;
    const unsigned xw = (w >> 1) * 16384 + (w & 1) * 4096, oc = (h >> 3) * 16384 + ls, tr = (8 * g + p) * 128 + b * 16;
    const unsigned cm[4] = {4 * g > n ? 0xF149F2CAu : 0u, 4 * g + 1 > n ? 0xF149F2CAu : 0u, 4 * g + 2 > n ? 0xF149F2CAu : 0u, 4 * g + 3 > n ? 0xF149F2CAu : 0u};
    const unsigned t[19] = {
        l0 + lane * 16, lw + lane * 16, l0 + (g >> 1) * 1024 + (g & 1) * 512 + n * 16, O_E1 + ls + xw + lane * 16, sp_idx(lane),
        IMG + (w >> 1) * 8192 + ((w & 1) * 8 + 2 * g) * 512 + n * 16,
        MIM + (w * 8 + 2 * p) * 144 + g * 32 + b * 4,
        IMG + g * 512 + (w ? n * 16 : 8192 + (16 * (b >> 1) + 8 * (p >> 1) + 2 * (p & 1) + (b & 1)) * 16),
        MIM + g * 144 + (n & 7) * 16, MIM + 2304 + g * 144 + (2 * p + (b & 1)) * 16,
        w ? O_PT + (h >> 3) * 32768 + ls + lane * 16 + (w == 1 ? 16384 + ((h & 7) >> 1) * 1024 + (h & 1) * 8 : (h & 7) * 1024)
          : (QX4 & 1) && n >> 3 ? OOB : O_PM + oc + ((h >> 1) & 3) * 1024 + ((2 * (h & 1) + (g >> 1)) * 16 + sp_row(n & 7, g & 1)) * 16 + (n >> 3) * 8,
        w ? (QX4 & 4) && g ? OOB : O_PT + (h >> 3) * 32768 + ls + 20480 + ((h >> 2) & 1) * 2048 + (w - 1) * 1024 + (h & 3) * 256 + n * 16 + g * 4
          : n >> 3 || ((QX4 & 2) && g >> 1) ? OOB : O_PM + oc + 4096 + ((h & 7) >> 1) * 256 + sp_row(n & 7, g & 1) * 16 + (h & 1) * 8 + (g >> 1) * 4,
        w == 3 ? (l ? F_X + SD + p * 128 + (X1F ? 0 : b * 4) : F_X0 + b * 4) : cm[0], cm[1], cm[2], cm[3],
        (lane >> 3) * 8192u + (lane & 7) * 16, tb + tr, lw + tr};
    table(smem + w * 12288 + lane * 4, t);
    asm volatile(QK_ASM : : [ws] "s"(a.ws), [sz] "n"(WS_RS), [rw] "s"(mkrs(w < 2 ? a.Wq : a.Wk, 8u << 20)), [wo] "s"(h * 256 + (w & 1) * 128), [tb] "s"(tb), [lw] "s"(lw), [l0] "s"(l0), [w] "s"(w),
        [xn] "s"(O_XN + ls), [xw] "s"(xw), [fs] "s"(l ? 0 : 128), [sg] "s"(F_Y0 + 4096 + ls + h * 128 + 16), [pr] "s"(prof_at(l * 80 + h, w)) : CLOB, "s36", "s37", "s38", "s39");
    __builtin_amdgcn_endpgm();
}
DEVI void vu_role(const Args& a, const int w, lds_char* smem, int l, int h) {
    const int lane = lane_id(), g = lane >> 4, n = lane & 15, b = n & 3, p = n >> 2;
    const unsigned l0 = (unsigned)(size_t)smem, tb = l0 + w * 12288, lw = l0 + 61440 + w * 24576, ls = l * SD;
    const unsigned xw = (w >> 1) * 16384 + (w & 1) * 4096, tv = (8 * g + p) * 128 + b * 16 + (w & 1) * 64, to = (4 * g + (p & 1)) * 128 + (p >> 1) * 32 + b * 8;
    const unsigned t[18] = {
        l0 + lane * 16, lw + lane * 16, l0 + (g >> 1) * 1024 + (g & 1) * 512 + n * 16, O_E1 + ls + xw + lane * 16, sp_idx(lane),
        l0 + 57344 + (2 * w + (n >> 3)) * 256 + (n >> 3) * 128 + g * 32 + (n & 7) * 2,
        O_UM + ls + (4 * w + (g & 1)) * 65536 + (h >> 3) * 16384 + (g >> 1) * 2048 + (h & 7) * 256 + n * 16,
        sd(O_Y + h * 131072 + ls) + w * 32768 + (g >> 1) * 1024 + (g & 1) * 256 + n * 16,
        l ? F_X + SD + p * 128 + (X1F ? 0 : b * 4) : F_X0 + b * 4, l0 + 49152 + (w >> 1) * 2048 + (2 * (w & 1) + (g >> 1)) * 256 + (n ^ (g >> 1) * 8) * 16 + (g & 1) * 8, sp_idx(lane >> 2), l0 + g * 256 + (g & 1) * 128 + (n & 7) * 16,
        (lane >> 3) * 8192u + (lane & 7) * 16, (8 * (lane >> 5) + ((lane >> 3) & 3)) * 2048u + (lane & 7) * 16, tb + tv - (w & 1) * 12288, lw + tv - (w & 1) * 24576, tb + to, lw + to};
    table(smem + w * 12288 + lane * 4, t);
    asm volatile(VU_ASM : : [ws] "s"(a.ws), [sz] "n"(WS_RS), [rw] "s"(mkrs(a.Wv, 8u << 20)), [ro] "s"(mkrs(a.Wo, 8u << 20)), [wo] "s"(h * 256 + (w >> 1) * 128 + (w & 1) * 262144), [w2] "s"(h * 262144 + w * 512), [tb] "s"(tb), [lw] "s"(lw), [w] "s"(w),
        [xn] "s"(O_XN + ls), [xw] "s"(xw), [fs] "s"(l ? 0 : 128), [xt] "s"(w * 16384 - xw), [fy] "s"(F_Y0 + ls + h * 128 + w * 4), [sg] "s"(w == 1 ? F_Y0 + ls + h * 128 + 16 : OOB), [pr] "s"(prof_at(l * 80 + 32 + h, w) - 128) : CLOB, "s36", "s37", "s38", "s39");
    __builtin_amdgcn_endpgm();
}
template <class F> DEVI void rd_role(const Args& a, const int w, lds_char* smem, int l, int c, F&& idle_work) {
    const int lane = lane_id(), g = lane >> 4, n = lane & 15, r = 2 * (n >> 2) + (n & 1), hf = (n >> 1) & 1;
    const rsrc_t rs = mkrs(a.ws, WS_RS);
    const unsigned l0 = (unsigned)(size_t)smem, tb = l0 + 65536 + w * 4096, ls = l * SD, vp = O_E1 + ls + (c >> 3) * 16384 + (c & 7) * 1024 + w * 256 + n * 16;
    {
        const v4u ff = {SENT, SENT, SENT, SENT};
#pragma unroll
        for (int i = 0; i < 8; ++i) st16(ff, rs, O_UM + ls + c * 65536 + w * 16384 + i * 1024 + lane * 16);
#pragma unroll
        for (int i = 0; i < 2; ++i) st16(ff, rs, O_PT + ls + c * 16384 + w * 2048 + i * 1024 + lane * 16);
        st16(ff, rs, c < 8 ? O_PM + ls + w * 16384 + c * 1024 + lane * 16 : OOB);
        st16(ff, rs, g ? vp + g * 32768 : OOB);
        WV0; BAR;
    }
    st4(g & 1 ? *(const unsigned*)(a.mem0 + r * 1024 + c * 64 + w * 16 + 4 * g + 2 * hf) : nosent(*(const unsigned*)(a.mem0 + r * 1024 + c * 64 + w * 16 + 4 * g + 2 * hf)), rs, g & 1 ? vp + g * 4 : OOB, 0);
    WV0;
    st4(nosent(*(const unsigned*)(a.mem0 + r * 1024 + c * 64 + w * 16 + 4 * g + 2 * hf)), rs, g & 1 ? OOB : vp + g * 4, 0);
    idle_work();
    const int gi = lane >> 3, r8 = lane & 7;
    const unsigned t[12] = {
        (unsigned)lane * 16, sp_idx(lane), l0 + w * 4096 + g * 576 + ((n >> 3) * 16 + hf * 4 + 2 * ((n >> 2) & 1) + (n & 1)) * 4,
        l0 + (r >> 1) * 576 + w * 128 + (g >> 1) * 64 + (r & 1) * 32 + hf * 16,
        PX4 && g >> 1 ? OOB : (vp ^ ((g & 1) * 65536)) + (g >> 1) * 8, g & 1 ? SENT : 0u,
        l0 + 16384 + w * 8192 + lane * 16, l0 + 16384 + ((gi >> 1) * 2 + (w >> 1)) * 1024 + ((gi & 1) * 32 + (w & 1) * 8 + r8) * 16,
        (c >> 1) * 16384u + (c & 1) * 4096 + gi * 512 + (w * 8 + r8) * 16,
        gi ? OOB : O_ST - O_XR + c * 256 + ((w & 1) * 8 + r8) * 16 + (w >> 1) * 8,
        F_Y0 + ls + (w * 8 + r8) * 128 + (c >> 2) * 4, F_Y0 + ls + (gi & 1) * 4096 + (w * 8 + r8) * 128 + 16};
    table(smem + 65536 + w * 4096 + lane * 4, t);
    asm volatile(RD_ASM : : [rs] "s"(rs), [tb] "s"(tb), [su] "s"(O_UM + ls + c * 65536 + w * 16384), [sp] "s"(O_PM + ls + w * 16384), [st] "s"(O_PT + ls + w * 32768),
        [sy] "s"(sd(O_Y + w * (1u << 20) + ls) + (c >> 1) * 16384 + (c & 1) * 4096), [at] "s"(O_XR + ls), [du] "s"((c & 2) * 5 / 2), [fa] "s"(A1F && w ? OOB : F_A + ls + c * 128 + w * 4), [pr] "s"(prof_at(l * 80 + 64 + c, w) + 128) : CLOB);
    __builtin_amdgcn_endpgm();
}
DEVI void gg_role(const Args& a, const int w, lds_char* smem, int l, int gi) {
    const int lane = lane_id(), g = lane >> 4, n = lane & 15, b = n & 3, p = n >> 2;
    const unsigned l0 = (unsigned)(size_t)smem, tb = l0 + w * 12288, lw = l0 + 65536 + w * 24576, ls = l * SD, tv = (8 * g + p) * 128 + b * 16 + (w & 1) * 64, fh = (g >> 1) * 1024 + (g & 1) * 512 + n * 16;
    const unsigned lf = GSW ? 0 : ls, fo = w ? OOB : F_H + (gi / 7) * 1024 + (gi % 7) * 128;
    const unsigned t[9] = {l0 + lane * 16, lw + lane * 16, l0 + fh, w ? OOB : F_A + lf + (lane >> 2) * 128 + (A1F ? 0 : (lane & 3) * 4), w * 2048u + lane * 16, O_ST - O_XR + g * 1024 + n * 16, (lane >> 3) * 7168u + (lane & 7) * 16, tb + tv - (w & 1) * 12288, lw + tv - (w & 1) * 24576};
    table(smem + w * 12288 + lane * 4, t);
    asm volatile(GG_ASM : : [rs] "s"(mkrs(a.ws, WS_RS)), [rw] "s"(mkrs(w & 1 ? a.Wu : a.Wg, 3584 * 2048)), [wo] "s"(gi * 256 + (w >> 1) * 128), [tb] "s"(tb), [lw] "s"(lw), [w] "s"(w),
        [xn0] "s"(O_XR + lf), [xw] "s"((w >> 1) * 16384 + (w & 1) * 4096), [ho0] "s"(O_H + lf + gi * 16384), [fh0] "s"(fo + (GSW ? l * 4 : ls)), [fh1] "s"(fo + ls), [b0] "s"(l ? 0 : GSW), [b1] "s"(l && GSW ? GSW : 64), [pr] "s"(prof_at(160 + l * 28 + gi, w) + 128) : CLOB);
    __builtin_amdgcn_endpgm();
}
DEVI void dd_role(const Args& a, const int w, lds_char* smem, int l, int c) {
    const int lane = lane_id(), g = lane >> 4, b = lane & 3, p = (lane >> 2) & 3, gi = lane >> 3, r8 = lane & 7;
    const unsigned l0 = (unsigned)(size_t)smem, tb = l0 + w * 12288, lw = l0 + 65536 + w * 24576, ls = l * SD, tr = (8 * g + p) * 128 + b * 8;
    const unsigned ta = (c >> 1) * 16384u + (c & 1) * 4096 + gi * 512 + (w * 8 + r8) * 16;
    const rsrc_t rs = mkrs(a.ws, WS_RS);
    const unsigned t[11] = {(unsigned)lane * 16, lw + lane * 16, l0 + w * 8192 + lane * 16, l0 + ((gi >> 1) * 2 + (w >> 1)) * 1024 + ((gi & 1) * 32 + (w & 1) * 8 + r8) * 16,
        ta, l ? (w * 8 + r8) * 2048 + c * 128 + gi * 16 : ta, gi || l ? OOB : O_ST - O_XR + 4096 + c * 256 + (w * 8 + r8) * 8,
        F_H + ls + w * 1024 + ((lane >> 2) % 7) * 128 + (GSW && !l ? 4 : 0), gi * 2048u + r8 * 16, tb + tr, lw + tr};
    table(smem + w * 12288 + lane * 4, t);
    asm volatile(DD_ASM : : [rs] "s"(rs), [rw] "s"(mkrs(a.Wd, 3584 * 2048)), [ro] "s"(mkrs(l ? (char*)a.out : a.ws, l ? 2048 * 2048 : WS_RS)), [wo] "s"(w * (896 * 2048) + c * 128), [tb] "s"(tb), [lw] "s"(lw), [l] "s"(l), [b1] "s"(GSW && !l ? GSW : 99),
        [xn] "s"(O_XR + ls), [ho] "s"(O_H + ls + w * 7 * 16384), [fd] "s"(D1F && w ? OOB : F_D + ls + c * 128 + w * 4), [lt] "s"(F_D + ls + c * 128 + w * 4 + 64), [pr] "s"(prof_at(216 + l * 16 + c, w) + 128) : CLOB);
    __builtin_amdgcn_endpgm();
}

#define PFG 0
#define PFT 1000
#define PFX 0
#define CPF 74
#define CPJ 0
#define LNW 0
#define DLQ 150
#define DLS 400
#define SPL 0
#define PLAN 3
#define PFA 0
#define PFB 3
#define PFL 1
#define VUX 1
#define LNS 40
#define LN1 700
#define DLG 1100
#define DLD 1500
#define DL1 1500
#define DLG1 2600
#define DLD1 3000
DEVI Args args(unsigned sh) {
    auto p = (const __attribute__((address_space(4))) Args*)__builtin_amdgcn_kernarg_segment_ptr();
    asm volatile("" : "+s"(p));
    Args a;
    a.x = p->x; a.mem0 = p->mem0; a.Wq = p->Wq; a.Wk = p->Wk; a.Wv = p->Wv; a.Wo = p->Wo; a.Wg = p->Wg; a.Wu = p->Wu; a.Wd = p->Wd; a.ws = p->ws + sh; a.out = p->out;
    return a;
}
DEVI unsigned beat_wait(unsigned x0) {
    const rsrc_t r0 = mkrs(args(0).ws, 2u << 20);
    for (unsigned spins = 0; spins < SPIN_CAP; ++spins) {
        CB;
        const v2u t = __builtin_amdgcn_raw_buffer_load_b64(r0, 0, O_HB, AUX_SC1);
        if (RFL(t[0]) != x0) return RFL(t[1]) & (2u << 20);
        __builtin_amdgcn_s_sleep(8);
    }
    __builtin_amdgcn_endpgm();
}
#define R2W 0
#define MPF 0
DEVI void mpf(rsrc_t rs, unsigned o) {
    o += O_E1 + lane_id() * 128;
    unsigned spins = 0;
    for (int s = 0; s < NSEG && spins < SPIN_CAP; ++spins) {
        CB;
        if (__ballot(ld4(rs, o, (s & 3) * 32768) == SENT) == 0ull) { ++s; for (int i = 0; i < MPF; ++i) __builtin_amdgcn_s_sleep(8); }
    }
}
DEVI void leader(const Args& a, const int w, lds_char* smem, unsigned x0) {
    const int lane = lane_id();
    const rsrc_t r0 = mkrs(a.ws, 4u << 20);
    unsigned t[2] = {~0u, ~0u};
    for (int i = 0; i < 2 * NPRB; ++i) {
        const unsigned t0 = (unsigned)__builtin_amdgcn_s_memtime();
        WL0;
        if (lane == 0) st4(0u, r0, 0, O_PRB + (i & 1) * SD + w * 1024 + (i >> 1) * 128);
        WV0;
        const unsigned d = (unsigned)__builtin_amdgcn_s_memtime() - t0;
        if (i & 1) t[1] = min(t[1], d); else t[0] = min(t[0], d);
    }
    *(l2*)(smem + w * 8) = v2u{t[0], t[1]};
    WL0; BAR;
    const v4u p = *(l4*)smem, q = *(l4*)(smem + 16);
    const unsigned sh = RFL(min(min(p[0], p[2]), min(q[0], q[2])) < min(min(p[1], p[3]), min(q[1], q[3])) ? 2u << 20 : 0u);
    const unsigned o = sh + w * 1024 + lane * 16;
    const v4u ff = {SENT, SENT, SENT, SENT};
#pragma unroll
    for (int i = 0; i < 8; ++i) st16(ff, r0, o + O_E1 + i * 4096);
#pragma unroll
    for (int i = 1; i < 16; ++i) if (i != 2 && i != 11) st16(ff, r0, o + i * 4096);
    WV0; BAR;
    if (w) { if (MPF && w > 1) mpf(r0, sh + SD + (w & 1) * 16384); return; }
    for (unsigned spins = 0; spins < SPIN_CAP; ++spins) {
        if (lane == 0) st8(v2u{++x0, sh}, r0, 0, O_HB);
        if (__ballot((int)ld4(r0, sh + F_D + SD + (lane >> 2) * 128 + (D1F ? 0 : (lane & 3) * 4), 0) < 0) == 0ull) break;
        __builtin_amdgcn_s_sleep(16);
    }
}
__global__ __launch_bounds__(256, 1) void sys_kernel(Args) {
    extern __shared__ __attribute__((aligned(16))) char smem_[];
    lds_char* smem = (lds_char*)smem_;
    const int x = blockIdx.x & 7, j = blockIdx.x >> 3, wv = RFL(threadIdx.x >> 6), l = x >> 2, xx = x & 3;
    const unsigned t0 = (unsigned)__builtin_amdgcn_s_memrealtime();
    auto until = [&](unsigned dl) AI { while ((unsigned)__builtin_amdgcn_s_memrealtime() - t0 < dl) __builtin_amdgcn_s_sleep(8); };
    unsigned x0 = 0;
    if (l || j >= 16) x0 = RFL(ld4(mkrs(args(0).ws, 2u << 20), 0, O_HB));
    if (j < 16) {
        if (l) { until(j ? LN1 + j * LNS : 0); ln0(args(0), wv, j * 4 + xx, [&]() AI { return beat_wait(x0); }); until(DL1); }
        if (DLQ && !l && j < 8) until(DLQ);
        if (j < 8) qk_role(args(0), wv, smem, l, xx * 8 + j); else vu_role(args(0), wv, smem, l, (xx ^ VUX) * 8 + j - 8);
        return;
    }
    if (j >= 16 + CPJ && j < 20 + CPJ) {
        unsigned long long pc; unsigned n;
        asm volatile("s_getpc_b64 %0\n\ts_mov_b32 %1, .-_Z10sys_kernel4Args" : "=s"(pc), "=s"(n));
        const rsrc_t rc = mkrs((const void*)(pc - n), CPF * 1024);
        __builtin_amdgcn_raw_ptr_buffer_load_lds(rc, smem + LDSB - 1024 + wv * 256, 4, lane_id() * 128, ((j - 16 - CPJ) * 4 + wv) * 8192, 0, 0);
    }
    {
        const int lane = lane_id(), q = (j - 16) * 4 + wv, m = SPL && !l ? ((xx & 1) != (SPL & 1)) * 2 + (q >> 5) : q >> 4;
        const Args a = args(0);
        const u16 *W0 = a.Wq, *W1 = a.Wk, *W2 = a.Wv, *W3 = a.Wo;
        asm volatile("" : "+s"(W0), "+s"(W1), "+s"(W2), "+s"(W3));
        const u16* W = m == 0 ? W0 : m == 1 ? W1 : m == 2 ? W2 : W3;
        const rsrc_t rw = mkrs(W, 8u << 20);
        if (l) {
#pragma unroll
            for (int i = 0; i < PFA; ++i) __builtin_amdgcn_raw_ptr_buffer_load_lds(rw, smem + LDSB - 1024 + wv * 256, 4, lane * 256, i * (1u << 20) + (xx * 16 + (q & 15)) * 16384, 0, 0);
        } else {
            const unsigned po = (0x100u - (unsigned)(size_t)W) & 0x300u, hx = SPL ? (xx & 2) + ((q >> 4) & 1) : m < 2 ? xx : xx ^ VUX;
            const unsigned vo = m == 3 ? (lane >> 1) * 1024 + (lane & 1) * 128 : (lane >> 2) * 8192 + ((lane >> 1) & 1) * 1024 + (lane & 1) * 128;
            const unsigned so = po + (m == 3 ? hx * (2u << 20) + (q & 15) * 32768 : hx * 2048 + (q & 15) * 131072);
#pragma unroll
            for (int i = 0; i < PFB; ++i) __builtin_amdgcn_raw_ptr_buffer_load_lds(rw, smem + LDSB - 1024 + wv * 256, 4, vo, so + i * (m == 3 ? 524288 : 2u << 20), 0, 0);
            const unsigned pl = (0x1F00u - (unsigned)(size_t)W) & 0x1F00u;
#pragma unroll
            for (int i = 0; i < PFL; ++i) __builtin_amdgcn_raw_ptr_buffer_load_lds(rw, smem + LDSB - 1024 + wv * 256, 4, (lane >> 1) * 8192 + (lane & 1) * 128, (pl >> 11) == hx && m < 3 ? pl + ((q & 15) + 16 * i) * 262144 : OOB, 0, 0);
        }
    }
    int ro, ix;
    if ((xx & 1) == (PLAN & 1)) { if (j < 24) { ro = 0; ix = (xx >> 1) * 8 + j - 16; } else if (j < 26) { ro = 3; ix = (xx >> 1) * 2 + j - 24; } else { ro = 1; ix = (xx >> 1) * 14 + j - 26; } }
    else if (j < 24) { ro = 1; ix = (xx >> 1) * 14 + j - 10; } else { ro = 2; ix = (xx >> 1) * 8 + j - 24; }
    if (ro == 3 && l) { if (ix == 0) leader(args(0), wv, smem, x0); else if (MPF && wv > 1) mpf(mkrs(args(0).ws, 4u << 20), beat_wait(x0) + SD + (wv & 1) * 16384); return; }
    const unsigned sh = beat_wait(x0);
    auto pfg = [&]() AI {
        if (!PFG || l) return;
        until(PFT);
        const Args a = args(0);
        const int lane = lane_id(), q = (ix & 7) * 4 + wv;
        const rsrc_t rw = mkrs(q & 1 ? a.Wu : a.Wg, 3584 * 2048);
        const unsigned vo = (lane & 15) < ((xx & 1) ? 12 : 16) ? (lane >> 4) * 7168 + (lane & 15) * 128 : OOB, so = ((xx >> 1) * 14 + ((xx & 1) ? 0 : 6)) * 256 + (1024 - 64 * PFG + (q >> 1) * 4) * 7168;
#pragma unroll
        for (int i = 0; i < PFG; ++i) __builtin_amdgcn_raw_ptr_buffer_load_lds(rw, smem + LDSB - 1024 + wv * 256, 4, vo, so + i * 64 * 7168, 0, PFX);
    };
    if (ro == 0) { rd_role(args(sh), wv, smem, l, ix, pfg); return; }
    if (ro == 1) { const Args a = args(sh); until((l ? DLG1 : DLG) - (((((unsigned)(size_t)a.Wg >> 8) + ix) & 3) == 1 || ((((unsigned)(size_t)a.Wu >> 8) + ix) & 3) == 1 ? DLS : 0)); WV0; BAR; gg_role(a, wv, smem, l, ix); return; }
    if (ro == 2) { pfg(); const Args a = args(sh); until((l ? DLD1 : DLD) - (((((unsigned)(size_t)a.Wd >> 8) + (ix >> 1)) & 3) == 1 ? DLS + 200 : 0)); WV0; BAR; dd_role(a, wv, smem, l, ix); return; }
    r_role(args(sh), wv, ix);
}

extern "C" void kernel_launch(void* const* d_in, const int* in_sizes, int n_in, void* d_out, int out_size, void* d_ws, size_t ws_size, hipStream_t stream) {
    (void)in_sizes; (void)n_in; (void)out_size; (void)ws_size;
    Args a;
    a.x = (const u16*)d_in[0]; a.mem0 = (const u16*)d_in[1]; a.Wq = (const u16*)d_in[2]; a.Wk = (const u16*)d_in[3]; a.Wv = (const u16*)d_in[4]; a.Wo = (const u16*)d_in[5];
    a.Wg = (const u16*)d_in[8]; a.Wu = (const u16*)d_in[9]; a.Wd = (const u16*)d_in[10];
    a.ws = (char*)(((uintptr_t)d_ws + (2u << 20) - 1) & ~(uintptr_t)((2u << 20) - 1)); a.out = (u16*)d_out;
    (void)hipFuncSetAttribute((const void*)sys_kernel, hipFuncAttributeMaxDynamicSharedMemorySize, LDSB);
    hipLaunchKernelGGL(sys_kernel, dim3(256), dim3(256), LDSB, stream, a);
}
```

```cpp
#include <hip/hip_runtime.h>
#include <stdint.h>

typedef unsigned short u16;
typedef unsigned long long u64;
typedef __attribute__((ext_vector_type(8))) short bf16x8;
typedef __attribute__((ext_vector_type(4))) short s16x4;
typedef __attribute__((ext_vector_type(4))) float f32x4;
typedef __attribute__((ext_vector_type(16))) float f32x16;
typedef __attribute__((ext_vector_type(2))) float f32x2;
typedef __attribute__((ext_vector_type(4))) unsigned v4u;
typedef __attribute__((ext_vector_type(2))) unsigned v2u;
typedef __amdgpu_buffer_rsrc_t rsrc_t;
typedef __attribute__((address_space(3))) char lds_char;
typedef __attribute__((address_space(3))) bf16x8 l8;
typedef __attribute__((address_space(3))) v4u l4;
typedef __attribute__((address_space(3))) v2u l2;
typedef __attribute__((address_space(3))) unsigned l1;
typedef __attribute__((address_space(3))) f32x4 lf4;
typedef __attribute__((address_space(3))) float lf1;
#define DEVI __device__ __forceinline__
#define AI __attribute__((always_inline))

constexpr int NSEG = 64;
constexpr float ATT_SCALE = 0.08838834764831845f;
constexpr float LOG2E = 1.4426950408889634f;
#define NPRB 4
constexpr unsigned SD = 8192;
DEVI unsigned sd(unsigned o) { return o ^ ((o >> 8) & SD); }
constexpr unsigned F_X0 = 32768;
constexpr unsigned F_X = 4096;
constexpr unsigned F_A = 16384;
constexpr unsigned F_H = F_A + 2048;
constexpr unsigned F_D = F_H + 4096;
constexpr unsigned F_Y0 = 49152;
constexpr unsigned F_ABORT = 6400;
constexpr unsigned O_PM = 65536;
constexpr unsigned O_E1 = 131072;
constexpr unsigned O_PT = 262144;
constexpr unsigned O_UM = 524288;
constexpr unsigned CTRL_END = O_UM + (1u << 20);
constexpr unsigned O_PRB = 0x1F0000, O_HB = 0x1FFF00;
constexpr unsigned O_XN = 4u << 20;
constexpr unsigned O_XR = O_XN + (8u << 20);
constexpr unsigned O_ST = O_XR + (8u << 20);
constexpr unsigned O_H = O_ST + (8u << 20);
constexpr unsigned O_Y = O_H + (36u << 20);
constexpr unsigned WS_NEED = O_Y + (8u << 20);
constexpr unsigned O_PROF = WS_NEED;
constexpr unsigned WS_RS = WS_NEED;
static_assert(CTRL_END <= O_PRB && CTRL_END % 16384 == 0 && O_Y % (8u << 20) == 0, "workspace");
constexpr unsigned OOB = 0x80000000u, OOB2 = 0x90000000u, SENT = 0xFFFFFFFFu;

struct Args { const u16 *x, *mem0, *Wq, *Wk, *Wv, *Wo, *Wg, *Wu, *Wd; char* ws; u16* out; };

typedef __attribute__((ext_vector_type(2))) __bf16 bf16x2_t;
DEVI unsigned pack2(float lo, float hi) { f32x2 v = {lo, hi}; bf16x2_t b = __builtin_convertvector(v, bf16x2_t); return __builtin_bit_cast(unsigned, b); }
DEVI float bflo(unsigned u) { return __uint_as_float(u << 16); }
DEVI float bfhi(unsigned u) { return __uint_as_float(u & 0xffff0000u); }
DEVI float rbf(float x) { return bflo(pack2(x, x)); }
DEVI bf16x8 as8(v4u v) { return __builtin_bit_cast(bf16x8, v); }
DEVI f32x4 mfma16(bf16x8 a, bf16x8 b, f32x4 c) { return __builtin_amdgcn_mfma_f32_16x16x32_bf16(a, b, c, 0, 0, 0); }
DEVI f32x16 mfma32(bf16x8 a, bf16x8 b, f32x16 c) { return __builtin_amdgcn_mfma_f32_32x32x16_bf16(a, b, c, 0, 0, 0); }
constexpr int AUX_SC1 = 16;
DEVI v4u ld16(rsrc_t r, unsigned vo, unsigned so) { return __builtin_amdgcn_raw_buffer_load_b128(r, vo, so, AUX_SC1); }
DEVI unsigned ld4(rsrc_t r, unsigned vo, unsigned so) { return __builtin_amdgcn_raw_buffer_load_b32(r, vo, so, AUX_SC1); }
DEVI void st16(v4u v, rsrc_t r, unsigned vo) { __builtin_amdgcn_raw_buffer_store_b128(v, r, vo, 0, AUX_SC1); }
DEVI void st8(v2u v, rsrc_t r, unsigned vo, unsigned so) { __builtin_amdgcn_raw_buffer_store_b64(v, r, vo, so, AUX_SC1); }
DEVI void st4(unsigned v, rsrc_t r, unsigned vo, unsigned so) { __builtin_amdgcn_raw_buffer_store_b32(v, r, vo, so, AUX_SC1); }
DEVI v2u trd(lds_char* p) { return __builtin_bit_cast(v2u, __builtin_amdgcn_ds_read_tr16_b64_v4i16((__attribute__((address_space(3))) s16x4*)p)); }
template <int O1, int O2> DEVI void tra(v2u& lo, v2u& hi, unsigned a) { asm volatile("ds_read_b64_tr_b16 %0, %2 offset:%3\n\tds_read_b64_tr_b16 %1, %2 offset:%4" : "=&v"(lo), "=&v"(hi) : "v"(a), "n"(O1), "n"(O2) : "memory"); }
DEVI void trw(v2u& a, v2u& b, v2u& c, v2u& d) { asm volatile("s_waitcnt lgkmcnt(0)" : "+v"(a), "+v"(b), "+v"(c), "+v"(d)::"memory"); }
DEVI rsrc_t mkrs(const void* p, unsigned n) { return __builtin_amdgcn_make_buffer_rsrc((void*)p, 0, (int)n, 0x00020000); }
#define CB asm volatile("" ::: "memory")
#define SB __builtin_amdgcn_sched_barrier(0)
#define WL0 asm volatile("s_waitcnt lgkmcnt(0)" ::: "memory")
#define WV0 asm volatile("s_waitcnt vmcnt(0)" ::: "memory")
#define WV(n) asm volatile("s_waitcnt vmcnt(%0)" ::"n"(n) : "memory")
#define BAR do { __builtin_amdgcn_s_barrier(); CB; } while (0)
#define RFL(x) __builtin_amdgcn_readfirstlane(x)
DEVI int lane_id() { int x; asm volatile("v_mbcnt_lo_u32_b32 %0, -1, 0\n\tv_mbcnt_hi_u32_b32 %0, -1, %0" : "=v"(x)); return x; }
template <int I> struct IC { static constexpr int v = I; };
template <int B, int E_, class F> DEVI void sfor(F&& f) { if constexpr (B < E_) { f(IC<B>{}); sfor<B + 1, E_>(f); } }
DEVI float red_x32(float x, bool mx) { unsigned u = __float_as_uint(x); auto r = __builtin_amdgcn_permlane32_swap(u, u, false, false); float a = __uint_as_float(r[0]), b = __uint_as_float(r[1]); return mx ? fmaxf(a, b) : a + b; }
DEVI float red_x16(float x, bool mx) { unsigned u = __float_as_uint(x); auto r = __builtin_amdgcn_permlane16_swap(u, u, false, false); float a = __uint_as_float(r[0]), b = __uint_as_float(r[1]); return mx ? fmaxf(a, b) : a + b; }
DEVI unsigned vmax4(v4u d) { return max(max(d[0], d[1]), max(d[2], d[3])); }
DEVI unsigned nosent(unsigned v) { return min(v, SENT - 1u); }
constexpr unsigned SPIN_CAP = 1u << 18;
DEVI bool spin_bail(unsigned& spins, rsrc_t rs) {
    if (++spins > SPIN_CAP) { st4(0u, rs, 0, F_ABORT); return true; }
    return (spins & 255u) == 0u && RFL(ld4(rs, 0, F_ABORT)) != SENT;
}
#define TSTAMP(idx)
constexpr int LDSB = 160 * 1024;
DEVI void idle(unsigned dl) { const unsigned t0 = (unsigned)__builtin_amdgcn_s_memrealtime(); while ((unsigned)__builtin_amdgcn_s_memrealtime() - t0 < dl) __builtin_amdgcn_s_sleep(8); }
#define RXE 0
#define D1F 0
#define X1F 0

DEVI float red_rows(float s) {
    s += __uint_as_float(__builtin_amdgcn_update_dpp(0, __float_as_uint(s), 0x128, 0xf, 0xf, false));
    s = red_x16(s, false); return red_x32(s, false);
}
DEVI f32x2 up2(unsigned u) { return f32x2{bflo(u), bfhi(u)}; }
DEVI float ln_inv(float var) { return __builtin_amdgcn_rcpf(rbf(__builtin_amdgcn_sqrtf(rbf(rbf(fmaxf(var, 0.f)) + 1e-5f)))); }
template <class F> DEVI void ln0(const Args& a, const int w, int b, F&& base) {
    const int lane = lane_id(), row = w * 8 + (lane & 7);
    const rsrc_t rx = mkrs(a.x, 2048 * 2048);
    v4u u[16];
#pragma unroll
    for (int i = 0; i < 16; ++i) u[i] = __builtin_amdgcn_raw_buffer_load_b128(rx, row * 2048 + (lane >> 3) * 16 + i * 128, b * 65536, 0);
    const float k0 = __shfl(bflo(u[0][0]), lane & 7);
    f32x2 s1 = {0.f, 0.f}, s2 = {0.f, 0.f};
#pragma unroll
    for (int i = 0; i < 16; ++i)
#pragma unroll
        for (int e = 0; e < 4; ++e) { const f32x2 d = up2(u[i][e]) - k0; s1 += d; s2 += d * d; }
    const float s = red_rows(s1[0] + s1[1]) * (1.0f / 1024.0f), q = red_rows(s2[0] + s2[1]) * (1.0f / 1024.0f);
    float inv = ln_inv(q - s * s), c = -rbf(k0 + s) * inv;
    asm volatile("" : "+v"(inv), "+v"(c));
    const rsrc_t rs = mkrs(a.ws + base(), WS_RS);
    const unsigned o = sd(O_XN + b * 131072) + (lane >> 3) * 512 + row * 16;
#pragma unroll
    for (int i = 0; i < 16; ++i) {
        v4u r;
#pragma unroll
        for (int e = 0; e < 4; ++e) { const f32x2 z = up2(u[i][e]) * inv + c; r[e] = pack2(z[0], z[1]); }
        st16(r, rs, o + (i >> 1) * 16384 + (i & 1) * 4096);
    }
#pragma unroll
    for (int i = 0; i < 16; ++i) st16(u[i], rs, o + (O_XR - O_XN) + (i >> 1) * 16384 + (i & 1) * 4096);
    st16(u[0], rs, sd(O_ST + b * 131072) + w * 1024 + lane * 16); st16(u[0], rs, sd(O_ST + b * 131072) + 4096 + w * 1024 + lane * 16); st16(u[0], rs, sd(O_ST + SD + b * 131072) + w * 1024 + lane * 16);
    WV0;
    st4(b, rs, 0, F_X0 + b * 128 + w * 4);
}
#define RSLP 8
DEVI void r_role(const Args& a, const int w, int rq) {
    const int lane = lane_id(), row = (w >> 1) * 16 + (rq & 1) * 8 + (lane & 7), cq = (rq >> 1) * 2 + (w & 1);
    const rsrc_t rs = mkrs(a.ws, WS_RS);
    const unsigned fo = cq * 32768 + (lane >> 3) * 512 + row * 16, so = O_ST - O_XR + 4096 + (lane >> 3) * 512 + row * 8;
    auto io = [](int i) -> unsigned { return (i >> 1) * 16384 + (i & 1) * 4096; };
    const unsigned fd = F_D + (lane & 15) * 128 + (D1F ? 0 : (row >> 3) * 4);
    for (int b = 0; b < NSEG; ++b) {
        unsigned spins = 0;
        for (;;) {
            CB;
            if (__ballot((int)ld4(rs, fd, 0) < b) == 0ull) break;
            if (spin_bail(spins, rs)) return;
            __builtin_amdgcn_s_sleep(RSLP);
        }
        const unsigned sx = sd(O_XR + b * 131072);
        const v2u s0 = __builtin_amdgcn_raw_buffer_load_b64(rs, so, sx, AUX_SC1), s1 = __builtin_amdgcn_raw_buffer_load_b64(rs, so + 256, sx, AUX_SC1);
        v4u u[4];
#pragma unroll
        for (int i = 0; i < 4; ++i) u[i] = ld16(rs, fo + io(i), sx);
        const float m0 = __uint_as_float(s0[0]), m1 = __uint_as_float(s1[0]), mean = red_rows(m0 + m1) * 0.0625f;
        const float var = red_rows(__uint_as_float(s0[1]) + __uint_as_float(s1[1]) + 64.f * ((m0 - mean) * (m0 - mean) + (m1 - mean) * (m1 - mean))) * (1.0f / 1024.0f);
        const float inv = ln_inv(var), c = -rbf(mean) * inv;
#pragma unroll
        for (int i = 0; i < 4; ++i) {
            v4u o;
#pragma unroll
            for (int e = 0; e < 4; ++e) { const f32x2 z = up2(u[i][e]) * inv + c; o[e] = pack2(z[0], z[1]); }
            st16(o, rs, fo + io(i) + ((sx ^ SD) - (O_XR - O_XN)));
        }
        CB;
#pragma unroll
        for (int i = 0; i < 4; ++i) st16(u[i], rs, fo + io(i) + (sx ^ SD));
        WV0;
        if (X1F) BAR;
        st4(b, rs, 0, X1F && w ? OOB : F_X + SD + rq * 128 + w * 4);
        if (rq == 0 && w == 0) TSTAMP(200000 + 64 + b);
    }
}
DEVI void r_role2(const Args& a, const int w, int rq) {
    const int lane = lane_id(), row = w * 16 + (rq & 1) * 8 + (lane & 7), cq = (rq >> 1) * 2;
    const rsrc_t rs = mkrs(a.ws, WS_RS);
    const unsigned fo = cq * 32768 + (lane >> 3) * 512 + row * 16, so = O_ST - O_XR + 4096 + (lane >> 3) * 512 + row * 8;
    auto io = [](int i) -> unsigned { return (i >> 1) * 16384 + (i & 1) * 4096; };
    const unsigned fd = F_D + (lane & 15) * 128 + (D1F ? 0 : (row >> 3) * 4);
    for (int b = 0; b < NSEG; ++b) {
        unsigned spins = 0;
        for (;;) {
            CB;
            if (__ballot((int)ld4(rs, fd, 0) < b) == 0ull) break;
            if (spin_bail(spins, rs)) return;
            __builtin_amdgcn_s_sleep(RSLP);
        }
        const unsigned sx = sd(O_XR + b * 131072);
        const v2u s0 = __builtin_amdgcn_raw_buffer_load_b64(rs, so, sx, AUX_SC1), s1 = __builtin_amdgcn_raw_buffer_load_b64(rs, so + 256, sx, AUX_SC1);
        v4u u[8];
#pragma unroll
        for (int i = 0; i < 8; ++i) u[i] = ld16(rs, fo + io(i), sx);
        const float m0 = __uint_as_float(s0[0]), m1 = __uint_as_float(s1[0]), mean = red_rows(m0 + m1) * 0.0625f;
        const float var = red_rows(__uint_as_float(s0[1]) + __uint_as_float(s1[1]) + 64.f * ((m0 - mean) * (m0 - mean) + (m1 - mean) * (m1 - mean))) * (1.0f / 1024.0f);
        const float inv = ln_inv(var), c = -rbf(mean) * inv;
#pragma unroll
        for (int i = 0; i < 8; ++i) {
            v4u o;
#pragma unroll
            for (int e = 0; e < 4; ++e) { const f32x2 z = up2(u[i][e]) * inv + c; o[e] = pack2(z[0], z[1]); }
            st16(o, rs, fo + io(i) + ((sx ^ SD) - (O_XR - O_XN)));
        }
        CB;
#pragma unroll
        for (int i = 0; i < 8; ++i) st16(u[i], rs, fo + io(i) + (sx ^ SD));
        WV0;
        if (X1F) BAR;
        st8(v2u{(unsigned)b, (unsigned)b}, rs, 0, F_X + SD + rq * 128 + w * 8);
        if (rq == 0 && w == 0) TSTAMP(200000 + 64 + b);
    }
}
#define QK_ASM \
"v_mbcnt_lo_u32_b32 v255, -1, 0\nv_mbcnt_hi_u32_b32 v255, -1, v255\nv_lshlrev_b32 v255, 2, v255\nv_add_u32 v255, %[tb], v255\nds_read_b32 v0, v255 offset:0\nds_read_b32 v1, v255 offset:256\nds_read_b32 v2, v255 offset:512\nds_read_b32 v3, v255 offset:768\n" \
"ds_read_b32 v4, v255 offset:1024\nds_read_b32 v5, v255 offset:1280\nds_read_b32 v6, v255 offset:1536\nds_read_b32 v7, v255 offset:1792\nds_read_b32 v8, v255 offset:2048\nds_read_b32 v9, v255 offset:2304\nds_read_b32 v10, v255 offset:2560\nds_read_b32 v11, v255 offset:2816\n" \
"ds_read_b32 v12, v255 offset:3072\nds_read_b32 v13, v255 offset:3328\nds_read_b32 v14, v255 offset:3584\nds_read_b32 v15, v255 offset:3840\nds_read_b32 v92, v255 offset:4096\nds_read_b32 v93, v255 offset:4352\nds_read_b32 v94, v255 offset:4608\ns_waitcnt lgkmcnt(0)\n" \
"s_mov_b64 s[36:37], %[ws]\ns_mov_b32 s38, %[sz]\ns_mov_b32 s39, 0x20000\ns_mov_b32 s45, 2096896\nbuffer_load_dword v95, off, s[36:39], s45 sc1\ns_add_u32 m0, %[lw], 16384\ns_add_u32 s44, %[wo], 0\nbuffer_load_dwordx4 v92, %[rw], s44 offen nt lds\n" \
"s_add_u32 m0, %[lw], 17408\ns_add_u32 s44, %[wo], 65536\nbuffer_load_dwordx4 v92, %[rw], s44 offen nt lds\ns_add_u32 m0, %[lw], 18432\ns_add_u32 s44, %[wo], 131072\nbuffer_load_dwordx4 v92, %[rw], s44 offen nt lds\ns_add_u32 m0, %[lw], 19456\ns_add_u32 s44, %[wo], 196608\n" \
"buffer_load_dwordx4 v92, %[rw], s44 offen nt lds\ns_add_u32 m0, %[lw], 20480\ns_add_u32 s44, %[wo], 262144\nbuffer_load_dwordx4 v92, %[rw], s44 offen nt lds\ns_add_u32 m0, %[lw], 21504\ns_add_u32 s44, %[wo], 327680\nbuffer_load_dwordx4 v92, %[rw], s44 offen nt lds\ns_add_u32 m0, %[lw], 22528\n" \
"s_add_u32 s44, %[wo], 393216\nbuffer_load_dwordx4 v92, %[rw], s44 offen nt lds\ns_add_u32 m0, %[lw], 23552\ns_add_u32 s44, %[wo], 458752\nbuffer_load_dwordx4 v92, %[rw], s44 offen nt lds\ns_add_u32 m0, %[tb], 0\ns_add_u32 s44, %[wo], 524288\nbuffer_load_dwordx4 v92, %[rw], s44 offen nt lds\n" \
"s_add_u32 m0, %[tb], 1024\ns_add_u32 s44, %[wo], 589824\nbuffer_load_dwordx4 v92, %[rw], s44 offen nt lds\ns_add_u32 m0, %[tb], 2048\ns_add_u32 s44, %[wo], 655360\nbuffer_load_dwordx4 v92, %[rw], s44 offen nt lds\ns_add_u32 m0, %[tb], 3072\ns_add_u32 s44, %[wo], 720896\n" \
"buffer_load_dwordx4 v92, %[rw], s44 offen nt lds\ns_add_u32 m0, %[tb], 4096\ns_add_u32 s44, %[wo], 786432\nbuffer_load_dwordx4 v92, %[rw], s44 offen nt lds\ns_add_u32 m0, %[tb], 5120\ns_add_u32 s44, %[wo], 851968\nbuffer_load_dwordx4 v92, %[rw], s44 offen nt lds\ns_add_u32 m0, %[tb], 6144\n" \
"s_add_u32 s44, %[wo], 917504\nbuffer_load_dwordx4 v92, %[rw], s44 offen nt lds\ns_add_u32 m0, %[tb], 7168\ns_add_u32 s44, %[wo], 983040\nbuffer_load_dwordx4 v92, %[rw], s44 offen nt lds\ns_add_u32 m0, %[tb], 8192\ns_add_u32 s44, %[wo], 1048576\nbuffer_load_dwordx4 v92, %[rw], s44 offen nt lds\n" \
"s_add_u32 m0, %[tb], 9216\ns_add_u32 s44, %[wo], 1114112\nbuffer_load_dwordx4 v92, %[rw], s44 offen nt lds\ns_add_u32 m0, %[tb], 10240\ns_add_u32 s44, %[wo], 1179648\nbuffer_load_dwordx4 v92, %[rw], s44 offen nt lds\ns_add_u32 m0, %[tb], 11264\ns_add_u32 s44, %[wo], 1245184\n" \
"buffer_load_dwordx4 v92, %[rw], s44 offen nt lds\ns_add_u32 m0, %[lw], 0\ns_add_u32 s44, %[wo], 1310720\nbuffer_load_dwordx4 v92, %[rw], s44 offen nt lds\ns_add_u32 m0, %[lw], 1024\ns_add_u32 s44, %[wo], 1376256\nbuffer_load_dwordx4 v92, %[rw], s44 offen nt lds\ns_add_u32 m0, %[lw], 2048\n" \
"s_add_u32 s44, %[wo], 1441792\nbuffer_load_dwordx4 v92, %[rw], s44 offen nt lds\ns_add_u32 m0, %[lw], 3072\ns_add_u32 s44, %[wo], 1507328\nbuffer_load_dwordx4 v92, %[rw], s44 offen nt lds\ns_add_u32 m0, %[lw], 4096\ns_add_u32 s44, %[wo], 1572864\nbuffer_load_dwordx4 v92, %[rw], s44 offen nt lds\n" \
"s_add_u32 m0, %[lw], 5120\ns_add_u32 s44, %[wo], 1638400\nbuffer_load_dwordx4 v92, %[rw], s44 offen nt lds\ns_add_u32 m0, %[lw], 6144\ns_add_u32 s44, %[wo], 1703936\nbuffer_load_dwordx4 v92, %[rw], s44 offen nt lds\ns_add_u32 m0, %[lw], 7168\ns_add_u32 s44, %[wo], 1769472\n" \
"buffer_load_dwordx4 v92, %[rw], s44 offen nt lds\ns_add_u32 m0, %[lw], 8192\ns_add_u32 s44, %[wo], 1835008\nbuffer_load_dwordx4 v92, %[rw], s44 offen nt lds\ns_add_u32 m0, %[lw], 9216\ns_add_u32 s44, %[wo], 1900544\nbuffer_load_dwordx4 v92, %[rw], s44 offen nt lds\ns_add_u32 m0, %[lw], 10240\n" \
"s_add_u32 s44, %[wo], 1966080\nbuffer_load_dwordx4 v92, %[rw], s44 offen nt lds\ns_add_u32 m0, %[lw], 11264\ns_add_u32 s44, %[wo], 2031616\nbuffer_load_dwordx4 v92, %[rw], s44 offen nt lds\ns_add_u32 m0, %[lw], 12288\ns_add_u32 s44, %[wo], 2097152\nbuffer_load_dwordx4 v92, %[rw], s44 offen nt lds\n" \
"s_add_u32 m0, %[lw], 13312\ns_add_u32 s44, %[wo], 2162688\nbuffer_load_dwordx4 v92, %[rw], s44 offen nt lds\ns_add_u32 m0, %[lw], 14336\ns_add_u32 s44, %[wo], 2228224\nbuffer_load_dwordx4 v92, %[rw], s44 offen nt lds\ns_add_u32 m0, %[lw], 15360\ns_add_u32 s44, %[wo], 2293760\n" \
"buffer_load_dwordx4 v92, %[rw], s44 offen nt lds\ns_waitcnt vmcnt(32)\nds_read_b64_tr_b16 a[0:1], v94 offset:16384\nds_read_b64_tr_b16 a[2:3], v94 offset:16896\nds_read_b64_tr_b16 a[8:9], v94 offset:16392\nds_read_b64_tr_b16 a[10:11], v94 offset:16904\nds_read_b64_tr_b16 a[16:17], v94 offset:16448\nds_read_b64_tr_b16 a[18:19], v94 offset:16960\n" \
"ds_read_b64_tr_b16 a[24:25], v94 offset:16456\nds_read_b64_tr_b16 a[26:27], v94 offset:16968\ns_waitcnt lgkmcnt(0)\ns_add_u32 m0, %[lw], 16384\ns_add_u32 s44, %[wo], 2359296\nbuffer_load_dwordx4 v92, %[rw], s44 offen nt lds\ns_add_u32 m0, %[lw], 17408\ns_add_u32 s44, %[wo], 2424832\n" \
"buffer_load_dwordx4 v92, %[rw], s44 offen nt lds\ns_add_u32 m0, %[lw], 18432\ns_add_u32 s44, %[wo], 2490368\nbuffer_load_dwordx4 v92, %[rw], s44 offen nt lds\ns_add_u32 m0, %[lw], 19456\ns_add_u32 s44, %[wo], 2555904\nbuffer_load_dwordx4 v92, %[rw], s44 offen nt lds\ns_waitcnt vmcnt(32)\n" \
"ds_read_b64_tr_b16 a[4:5], v94 offset:20480\nds_read_b64_tr_b16 a[6:7], v94 offset:20992\nds_read_b64_tr_b16 a[12:13], v94 offset:20488\nds_read_b64_tr_b16 a[14:15], v94 offset:21000\nds_read_b64_tr_b16 a[20:21], v94 offset:20544\nds_read_b64_tr_b16 a[22:23], v94 offset:21056\nds_read_b64_tr_b16 a[28:29], v94 offset:20552\nds_read_b64_tr_b16 a[30:31], v94 offset:21064\n" \
"s_waitcnt lgkmcnt(0)\ns_add_u32 m0, %[lw], 20480\ns_add_u32 s44, %[wo], 2621440\nbuffer_load_dwordx4 v92, %[rw], s44 offen nt lds\ns_add_u32 m0, %[lw], 21504\ns_add_u32 s44, %[wo], 2686976\nbuffer_load_dwordx4 v92, %[rw], s44 offen nt lds\ns_add_u32 m0, %[lw], 22528\n" \
"s_add_u32 s44, %[wo], 2752512\nbuffer_load_dwordx4 v92, %[rw], s44 offen nt lds\ns_add_u32 m0, %[lw], 23552\ns_add_u32 s44, %[wo], 2818048\nbuffer_load_dwordx4 v92, %[rw], s44 offen nt lds\ns_waitcnt vmcnt(32)\nds_read_b64_tr_b16 a[32:33], v93 offset:0\nds_read_b64_tr_b16 a[34:35], v93 offset:512\n" \
"ds_read_b64_tr_b16 v[84:85], v93 offset:8\nds_read_b64_tr_b16 v[86:87], v93 offset:520\nds_read_b64_tr_b16 a[40:41], v93 offset:64\nds_read_b64_tr_b16 a[42:43], v93 offset:576\nds_read_b64_tr_b16 a[48:49], v93 offset:72\nds_read_b64_tr_b16 a[50:51], v93 offset:584\ns_waitcnt lgkmcnt(0)\ns_add_u32 m0, %[tb], 0\n" \
"s_add_u32 s44, %[wo], 2883584\nbuffer_load_dwordx4 v92, %[rw], s44 offen nt lds\ns_add_u32 m0, %[tb], 1024\ns_add_u32 s44, %[wo], 2949120\nbuffer_load_dwordx4 v92, %[rw], s44 offen nt lds\ns_add_u32 m0, %[tb], 2048\ns_add_u32 s44, %[wo], 3014656\nbuffer_load_dwordx4 v92, %[rw], s44 offen nt lds\n" \
"s_add_u32 m0, %[tb], 3072\ns_add_u32 s44, %[wo], 3080192\nbuffer_load_dwordx4 v92, %[rw], s44 offen nt lds\ns_waitcnt vmcnt(32)\nds_read_b64_tr_b16 a[36:37], v93 offset:4096\nds_read_b64_tr_b16 a[38:39], v93 offset:4608\nds_read_b64_tr_b16 v[80:81], v93 offset:4104\nds_read_b64_tr_b16 v[82:83], v93 offset:4616\n" \
"ds_read_b64_tr_b16 a[44:45], v93 offset:4160\nds_read_b64_tr_b16 a[46:47], v93 offset:4672\nds_read_b64_tr_b16 a[52:53], v93 offset:4168\nds_read_b64_tr_b16 a[54:55], v93 offset:4680\ns_waitcnt lgkmcnt(0)\ns_add_u32 m0, %[tb], 4096\ns_add_u32 s44, %[wo], 3145728\nbuffer_load_dwordx4 v92, %[rw], s44 offen nt lds\n" \
"s_add_u32 m0, %[tb], 5120\ns_add_u32 s44, %[wo], 3211264\nbuffer_load_dwordx4 v92, %[rw], s44 offen nt lds\ns_add_u32 m0, %[tb], 6144\ns_add_u32 s44, %[wo], 3276800\nbuffer_load_dwordx4 v92, %[rw], s44 offen nt lds\ns_add_u32 m0, %[tb], 7168\ns_add_u32 s44, %[wo], 3342336\n" \
"buffer_load_dwordx4 v92, %[rw], s44 offen nt lds\ns_waitcnt vmcnt(32)\nds_read_b64_tr_b16 a[56:57], v93 offset:8192\nds_read_b64_tr_b16 a[58:59], v93 offset:8704\nds_read_b64_tr_b16 a[64:65], v93 offset:8200\nds_read_b64_tr_b16 a[66:67], v93 offset:8712\nds_read_b64_tr_b16 v[76:77], v93 offset:8256\nds_read_b64_tr_b16 v[78:79], v93 offset:8768\n" \
"ds_read_b64_tr_b16 a[72:73], v93 offset:8264\nds_read_b64_tr_b16 a[74:75], v93 offset:8776\ns_waitcnt lgkmcnt(0)\ns_add_u32 m0, %[tb], 8192\ns_add_u32 s44, %[wo], 3407872\nbuffer_load_dwordx4 v92, %[rw], s44 offen nt lds\ns_add_u32 m0, %[tb], 9216\ns_add_u32 s44, %[wo], 3473408\n" \
"buffer_load_dwordx4 v92, %[rw], s44 offen nt lds\ns_add_u32 m0, %[tb], 10240\ns_add_u32 s44, %[wo], 3538944\nbuffer_load_dwordx4 v92, %[rw], s44 offen nt lds\ns_add_u32 m0, %[tb], 11264\ns_add_u32 s44, %[wo], 3604480\nbuffer_load_dwordx4 v92, %[rw], s44 offen nt lds\ns_waitcnt vmcnt(32)\n" \
"ds_read_b64_tr_b16 a[60:61], v94 offset:0\nds_read_b64_tr_b16 a[62:63], v94 offset:512\nds_read_b64_tr_b16 a[68:69], v94 offset:8\nds_read_b64_tr_b16 a[70:71], v94 offset:520\nds_read_b64_tr_b16 v[72:73], v94 offset:64\nds_read_b64_tr_b16 v[74:75], v94 offset:576\nds_read_b64_tr_b16 a[76:77], v94 offset:72\nds_read_b64_tr_b16 a[78:79], v94 offset:584\n" \
"s_waitcnt lgkmcnt(0)\ns_add_u32 m0, %[lw], 0\ns_add_u32 s44, %[wo], 3670016\nbuffer_load_dwordx4 v92, %[rw], s44 offen nt lds\ns_add_u32 m0, %[lw], 1024\ns_add_u32 s44, %[wo], 3735552\nbuffer_load_dwordx4 v92, %[rw], s44 offen nt lds\ns_add_u32 m0, %[lw], 2048\n" \
"s_add_u32 s44, %[wo], 3801088\nbuffer_load_dwordx4 v92, %[rw], s44 offen nt lds\ns_add_u32 m0, %[lw], 3072\ns_add_u32 s44, %[wo], 3866624\nbuffer_load_dwordx4 v92, %[rw], s44 offen nt lds\ns_waitcnt vmcnt(32)\nds_read_b64_tr_b16 a[80:81], v94 offset:4096\nds_read_b64_tr_b16 a[82:83], v94 offset:4608\n" \
"ds_read_b64_tr_b16 a[88:89], v94 offset:4104\nds_read_b64_tr_b16 a[90:91], v94 offset:4616\nds_read_b64_tr_b16 a[96:97], v94 offset:4160\nds_read_b64_tr_b16 a[98:99], v94 offset:4672\nds_read_b64_tr_b16 v[68:69], v94 offset:4168\nds_read_b64_tr_b16 v[70:71], v94 offset:4680\ns_waitcnt lgkmcnt(0)\ns_add_u32 m0, %[lw], 4096\n" \
"s_add_u32 s44, %[wo], 3932160\nbuffer_load_dwordx4 v92, %[rw], s44 offen nt lds\ns_add_u32 m0, %[lw], 5120\ns_add_u32 s44, %[wo], 3997696\nbuffer_load_dwordx4 v92, %[rw], s44 offen nt lds\ns_add_u32 m0, %[lw], 6144\ns_add_u32 s44, %[wo], 4063232\nbuffer_load_dwordx4 v92, %[rw], s44 offen nt lds\n" \
"s_add_u32 m0, %[lw], 7168\ns_add_u32 s44, %[wo], 4128768\nbuffer_load_dwordx4 v92, %[rw], s44 offen nt lds\ns_waitcnt vmcnt(32)\nds_read_b64_tr_b16 a[84:85], v94 offset:8192\nds_read_b64_tr_b16 a[86:87], v94 offset:8704\nds_read_b64_tr_b16 a[92:93], v94 offset:8200\nds_read_b64_tr_b16 a[94:95], v94 offset:8712\n" \
"ds_read_b64_tr_b16 a[100:101], v94 offset:8256\nds_read_b64_tr_b16 a[102:103], v94 offset:8768\nds_read_b64_tr_b16 v[64:65], v94 offset:8264\nds_read_b64_tr_b16 v[66:67], v94 offset:8776\ns_waitcnt lgkmcnt(0)\ns_add_u32 m0, %[lw], 8192\ns_add_u32 s44, %[wo], 4194304\nbuffer_load_dwordx4 v92, %[rw], s44 offen nt lds\n" \
"s_add_u32 m0, %[lw], 9216\ns_add_u32 s44, %[wo], 4259840\nbuffer_load_dwordx4 v92, %[rw], s44 offen nt lds\ns_add_u32 m0, %[lw], 10240\ns_add_u32 s44, %[wo], 4325376\nbuffer_load_dwordx4 v92, %[rw], s44 offen nt lds\ns_add_u32 m0, %[lw], 11264\ns_add_u32 s44, %[wo], 4390912\n" \
"buffer_load_dwordx4 v92, %[rw], s44 offen nt lds\ns_waitcnt vmcnt(32)\nds_read_b64_tr_b16 a[104:105], v94 offset:12288\nds_read_b64_tr_b16 a[106:107], v94 offset:12800\nds_read_b64_tr_b16 a[112:113], v94 offset:12296\nds_read_b64_tr_b16 a[114:115], v94 offset:12808\nds_read_b64_tr_b16 a[120:121], v94 offset:12352\nds_read_b64_tr_b16 a[122:123], v94 offset:12864\n" \
"ds_read_b64_tr_b16 a[128:129], v94 offset:12360\nds_read_b64_tr_b16 a[130:131], v94 offset:12872\ns_waitcnt lgkmcnt(0)\ns_add_u32 m0, %[lw], 12288\ns_add_u32 s44, %[wo], 4456448\nbuffer_load_dwordx4 v92, %[rw], s44 offen nt lds\ns_add_u32 m0, %[lw], 13312\ns_add_u32 s44, %[wo], 4521984\n" \
"buffer_load_dwordx4 v92, %[rw], s44 offen nt lds\ns_add_u32 m0, %[lw], 14336\ns_add_u32 s44, %[wo], 4587520\nbuffer_load_dwordx4 v92, %[rw], s44 offen nt lds\ns_add_u32 m0, %[lw], 15360\ns_add_u32 s44, %[wo], 4653056\nbuffer_load_dwordx4 v92, %[rw], s44 offen nt lds\ns_waitcnt vmcnt(32)\n" \
"ds_read_b64_tr_b16 a[108:109], v94 offset:16384\nds_read_b64_tr_b16 a[110:111], v94 offset:16896\nds_read_b64_tr_b16 a[116:117], v94 offset:16392\nds_read_b64_tr_b16 a[118:119], v94 offset:16904\nds_read_b64_tr_b16 a[124:125], v94 offset:16448\nds_read_b64_tr_b16 a[126:127], v94 offset:16960\nds_read_b64_tr_b16 a[132:133], v94 offset:16456\nds_read_b64_tr_b16 a[134:135], v94 offset:16968\n" \
"s_waitcnt lgkmcnt(0)\ns_add_u32 m0, %[lw], 16384\ns_add_u32 s44, %[wo], 4718592\nbuffer_load_dwordx4 v92, %[rw], s44 offen nt lds\ns_add_u32 m0, %[lw], 17408\ns_add_u32 s44, %[wo], 4784128\nbuffer_load_dwordx4 v92, %[rw], s44 offen nt lds\ns_add_u32 m0, %[lw], 18432\n" \
"s_add_u32 s44, %[wo], 4849664\nbuffer_load_dwordx4 v92, %[rw], s44 offen nt lds\ns_add_u32 m0, %[lw], 19456\ns_add_u32 s44, %[wo], 4915200\nbuffer_load_dwordx4 v92, %[rw], s44 offen nt lds\ns_waitcnt vmcnt(32)\nds_read_b64_tr_b16 a[136:137], v94 offset:20480\nds_read_b64_tr_b16 a[138:139], v94 offset:20992\n" \
"ds_read_b64_tr_b16 v[60:61], v94 offset:20488\nds_read_b64_tr_b16 v[62:63], v94 offset:21000\nds_read_b64_tr_b16 a[144:145], v94 offset:20544\nds_read_b64_tr_b16 a[146:147], v94 offset:21056\nds_read_b64_tr_b16 a[152:153], v94 offset:20552\nds_read_b64_tr_b16 a[154:155], v94 offset:21064\ns_waitcnt lgkmcnt(0)\ns_add_u32 m0, %[lw], 20480\n" \
"s_add_u32 s44, %[wo], 4980736\nbuffer_load_dwordx4 v92, %[rw], s44 offen nt lds\ns_add_u32 m0, %[lw], 21504\ns_add_u32 s44, %[wo], 5046272\nbuffer_load_dwordx4 v92, %[rw], s44 offen nt lds\ns_add_u32 m0, %[lw], 22528\ns_add_u32 s44, %[wo], 5111808\nbuffer_load_dwordx4 v92, %[rw], s44 offen nt lds\n" \
"s_add_u32 m0, %[lw], 23552\ns_add_u32 s44, %[wo], 5177344\nbuffer_load_dwordx4 v92, %[rw], s44 offen nt lds\ns_waitcnt vmcnt(32)\nds_read_b64_tr_b16 a[140:141], v93 offset:0\nds_read_b64_tr_b16 a[142:143], v93 offset:512\nds_read_b64_tr_b16 v[56:57], v93 offset:8\nds_read_b64_tr_b16 v[58:59], v93 offset:520\n" \
"ds_read_b64_tr_b16 a[148:149], v93 offset:64\nds_read_b64_tr_b16 a[150:151], v93 offset:576\nds_read_b64_tr_b16 a[156:157], v93 offset:72\nds_read_b64_tr_b16 a[158:159], v93 offset:584\ns_waitcnt lgkmcnt(0)\ns_add_u32 m0, %[tb], 0\ns_add_u32 s44, %[wo], 5242880\nbuffer_load_dwordx4 v92, %[rw], s44 offen nt lds\n" \
"s_add_u32 m0, %[tb], 1024\ns_add_u32 s44, %[wo], 5308416\nbuffer_load_dwordx4 v92, %[rw], s44 offen nt lds\ns_add_u32 m0, %[tb], 2048\ns_add_u32 s44, %[wo], 5373952\nbuffer_load_dwordx4 v92, %[rw], s44 offen nt lds\ns_add_u32 m0, %[tb], 3072\ns_add_u32 s44, %[wo], 5439488\n" \
"buffer_load_dwordx4 v92, %[rw], s44 offen nt lds\ns_waitcnt vmcnt(32)\nds_read_b64_tr_b16 a[160:161], v93 offset:4096\nds_read_b64_tr_b16 a[162:163], v93 offset:4608\nds_read_b64_tr_b16 a[168:169], v93 offset:4104\nds_read_b64_tr_b16 a[170:171], v93 offset:4616\nds_read_b64_tr_b16 v[52:53], v93 offset:4160\nds_read_b64_tr_b16 v[54:55], v93 offset:4672\n" \
"ds_read_b64_tr_b16 a[176:177], v93 offset:4168\nds_read_b64_tr_b16 a[178:179], v93 offset:4680\ns_waitcnt lgkmcnt(0)\ns_add_u32 m0, %[tb], 4096\ns_add_u32 s44, %[wo], 5505024\nbuffer_load_dwordx4 v92, %[rw], s44 offen nt lds\ns_add_u32 m0, %[tb], 5120\ns_add_u32 s44, %[wo], 5570560\n" \
"buffer_load_dwordx4 v92, %[rw], s44 offen nt lds\ns_add_u32 m0, %[tb], 6144\ns_add_u32 s44, %[wo], 5636096\nbuffer_load_dwordx4 v92, %[rw], s44 offen nt lds\ns_add_u32 m0, %[tb], 7168\ns_add_u32 s44, %[wo], 5701632\nbuffer_load_dwordx4 v92, %[rw], s44 offen nt lds\ns_waitcnt vmcnt(32)\n" \
"ds_read_b64_tr_b16 a[164:165], v93 offset:8192\nds_read_b64_tr_b16 a[166:167], v93 offset:8704\nds_read_b64_tr_b16 a[172:173], v93 offset:8200\nds_read_b64_tr_b16 a[174:175], v93 offset:8712\nds_read_b64_tr_b16 v[48:49], v93 offset:8256\nds_read_b64_tr_b16 v[50:51], v93 offset:8768\nds_read_b64_tr_b16 a[180:181], v93 offset:8264\nds_read_b64_tr_b16 a[182:183], v93 offset:8776\n" \
"s_waitcnt lgkmcnt(0)\ns_add_u32 m0, %[tb], 8192\ns_add_u32 s44, %[wo], 5767168\nbuffer_load_dwordx4 v92, %[rw], s44 offen nt lds\ns_add_u32 m0, %[tb], 9216\ns_add_u32 s44, %[wo], 5832704\nbuffer_load_dwordx4 v92, %[rw], s44 offen nt lds\ns_add_u32 m0, %[tb], 10240\n" \
"s_add_u32 s44, %[wo], 5898240\nbuffer_load_dwordx4 v92, %[rw], s44 offen nt lds\ns_add_u32 m0, %[tb], 11264\ns_add_u32 s44, %[wo], 5963776\nbuffer_load_dwordx4 v92, %[rw], s44 offen nt lds\ns_waitcnt vmcnt(32)\nds_read_b64_tr_b16 a[184:185], v94 offset:0\nds_read_b64_tr_b16 a[186:187], v94 offset:512\n" \
"ds_read_b64_tr_b16 a[192:193], v94 offset:8\nds_read_b64_tr_b16 a[194:195], v94 offset:520\nds_read_b64_tr_b16 a[200:201], v94 offset:64\nds_read_b64_tr_b16 a[202:203], v94 offset:576\nds_read_b64_tr_b16 v[44:45], v94 offset:72\nds_read_b64_tr_b16 v[46:47], v94 offset:584\ns_waitcnt lgkmcnt(0)\ns_add_u32 m0, %[lw], 0\n" \
"s_add_u32 s44, %[wo], 6029312\nbuffer_load_dwordx4 v92, %[rw], s44 offen nt lds\ns_add_u32 m0, %[lw], 1024\ns_add_u32 s44, %[wo], 6094848\nbuffer_load_dwordx4 v92, %[rw], s44 offen nt lds\ns_add_u32 m0, %[lw], 2048\ns_add_u32 s44, %[wo], 6160384\nbuffer_load_dwordx4 v92, %[rw], s44 offen nt lds\n" \
"s_add_u32 m0, %[lw], 3072\ns_add_u32 s44, %[wo], 6225920\nbuffer_load_dwordx4 v92, %[rw], s44 offen nt lds\ns_waitcnt vmcnt(32)\nds_read_b64_tr_b16 a[188:189], v94 offset:4096\nds_read_b64_tr_b16 a[190:191], v94 offset:4608\nds_read_b64_tr_b16 a[196:197], v94 offset:4104\nds_read_b64_tr_b16 a[198:199], v94 offset:4616\n" \
"ds_read_b64_tr_b16 a[204:205], v94 offset:4160\nds_read_b64_tr_b16 a[206:207], v94 offset:4672\nds_read_b64_tr_b16 v[40:41], v94 offset:4168\nds_read_b64_tr_b16 v[42:43], v94 offset:4680\ns_waitcnt lgkmcnt(0)\ns_add_u32 m0, %[lw], 4096\ns_add_u32 s44, %[wo], 6291456\nbuffer_load_dwordx4 v92, %[rw], s44 offen nt lds\n" \
"s_add_u32 m0, %[lw], 5120\ns_add_u32 s44, %[wo], 6356992\nbuffer_load_dwordx4 v92, %[rw], s44 offen nt lds\ns_add_u32 m0, %[lw], 6144\ns_add_u32 s44, %[wo], 6422528\nbuffer_load_dwordx4 v92, %[rw], s44 offen nt lds\ns_add_u32 m0, %[lw], 7168\ns_add_u32 s44, %[wo], 6488064\n" \
"buffer_load_dwordx4 v92, %[rw], s44 offen nt lds\ns_waitcnt vmcnt(32)\nds_read_b64_tr_b16 a[208:209], v94 offset:8192\nds_read_b64_tr_b16 a[210:211], v94 offset:8704\nds_read_b64_tr_b16 a[216:217], v94 offset:8200\nds_read_b64_tr_b16 a[218:219], v94 offset:8712\nds_read_b64_tr_b16 a[224:225], v94 offset:8256\nds_read_b64_tr_b16 a[226:227], v94 offset:8768\n" \
"ds_read_b64_tr_b16 a[232:233], v94 offset:8264\nds_read_b64_tr_b16 a[234:235], v94 offset:8776\ns_waitcnt lgkmcnt(0)\ns_add_u32 m0, %[lw], 8192\ns_add_u32 s44, %[wo], 6553600\nbuffer_load_dwordx4 v92, %[rw], s44 offen nt lds\ns_add_u32 m0, %[lw], 9216\ns_add_u32 s44, %[wo], 6619136\n" \
"buffer_load_dwordx4 v92, %[rw], s44 offen nt lds\ns_add_u32 m0, %[lw], 10240\ns_add_u32 s44, %[wo], 6684672\nbuffer_load_dwordx4 v92, %[rw], s44 offen nt lds\ns_add_u32 m0, %[lw], 11264\ns_add_u32 s44, %[wo], 6750208\nbuffer_load_dwordx4 v92, %[rw], s44 offen nt lds\ns_waitcnt vmcnt(32)\n" \
"ds_read_b64_tr_b16 a[212:213], v94 offset:12288\nds_read_b64_tr_b16 a[214:215], v94 offset:12800\nds_read_b64_tr_b16 a[220:221], v94 offset:12296\nds_read_b64_tr_b16 a[222:223], v94 offset:12808\nds_read_b64_tr_b16 a[228:229], v94 offset:12352\nds_read_b64_tr_b16 a[230:231], v94 offset:12864\nds_read_b64_tr_b16 a[236:237], v94 offset:12360\nds_read_b64_tr_b16 a[238:239], v94 offset:12872\n" \
"s_waitcnt lgkmcnt(0)\ns_add_u32 m0, %[lw], 12288\ns_add_u32 s44, %[wo], 6815744\nbuffer_load_dwordx4 v92, %[rw], s44 offen nt lds\ns_add_u32 m0, %[lw], 13312\ns_add_u32 s44, %[wo], 6881280\nbuffer_load_dwordx4 v92, %[rw], s44 offen nt lds\ns_add_u32 m0, %[lw], 14336\n" \
"s_add_u32 s44, %[wo], 6946816\nbuffer_load_dwordx4 v92, %[rw], s44 offen nt lds\ns_add_u32 m0, %[lw], 15360\ns_add_u32 s44, %[wo], 7012352\nbuffer_load_dwordx4 v92, %[rw], s44 offen nt lds\ns_waitcnt vmcnt(32)\nds_read_b64_tr_b16 a[240:241], v94 offset:16384\nds_read_b64_tr_b16 a[242:243], v94 offset:16896\n" \
"ds_read_b64_tr_b16 v[36:37], v94 offset:16392\nds_read_b64_tr_b16 v[38:39], v94 offset:16904\nds_read_b64_tr_b16 a[248:249], v94 offset:16448\nds_read_b64_tr_b16 a[250:251], v94 offset:16960\nds_read_b64_tr_b16 v[96:97], v94 offset:16456\nds_read_b64_tr_b16 v[98:99], v94 offset:16968\ns_waitcnt lgkmcnt(0)\ns_add_u32 m0, %[lw], 16384\n" \
"s_add_u32 s44, %[wo], 7077888\nbuffer_load_dwordx4 v92, %[rw], s44 offen nt lds\ns_add_u32 m0, %[lw], 17408\ns_add_u32 s44, %[wo], 7143424\nbuffer_load_dwordx4 v92, %[rw], s44 offen nt lds\ns_add_u32 m0, %[lw], 18432\ns_add_u32 s44, %[wo], 7208960\nbuffer_load_dwordx4 v92, %[rw], s44 offen nt lds\n" \
"s_add_u32 m0, %[lw], 19456\ns_add_u32 s44, %[wo], 7274496\nbuffer_load_dwordx4 v92, %[rw], s44 offen nt lds\ns_waitcnt vmcnt(32)\nds_read_b64_tr_b16 a[244:245], v94 offset:20480\nds_read_b64_tr_b16 a[246:247], v94 offset:20992\nds_read_b64_tr_b16 v[32:33], v94 offset:20488\nds_read_b64_tr_b16 v[34:35], v94 offset:21000\n" \
"ds_read_b64_tr_b16 a[252:253], v94 offset:20544\nds_read_b64_tr_b16 a[254:255], v94 offset:21056\nds_read_b64_tr_b16 v[100:101], v94 offset:20552\nds_read_b64_tr_b16 v[102:103], v94 offset:21064\ns_waitcnt lgkmcnt(0)\ns_add_u32 m0, %[lw], 20480\ns_add_u32 s44, %[wo], 7340032\nbuffer_load_dwordx4 v92, %[rw], s44 offen nt lds\n" \
"s_add_u32 m0, %[lw], 21504\ns_add_u32 s44, %[wo], 7405568\nbuffer_load_dwordx4 v92, %[rw], s44 offen nt lds\ns_add_u32 m0, %[lw], 22528\ns_add_u32 s44, %[wo], 7471104\nbuffer_load_dwordx4 v92, %[rw], s44 offen nt lds\ns_add_u32 m0, %[lw], 23552\ns_add_u32 s44, %[wo], 7536640\n" \
"buffer_load_dwordx4 v92, %[rw], s44 offen nt lds\ns_waitcnt vmcnt(32)\nds_read_b64_tr_b16 v[104:105], v93 offset:0\nds_read_b64_tr_b16 v[106:107], v93 offset:512\nds_read_b64_tr_b16 v[112:113], v93 offset:8\nds_read_b64_tr_b16 v[114:115], v93 offset:520\nds_read_b64_tr_b16 v[28:29], v93 offset:64\nds_read_b64_tr_b16 v[30:31], v93 offset:576\n" \
"ds_read_b64_tr_b16 v[120:121], v93 offset:72\nds_read_b64_tr_b16 v[122:123], v93 offset:584\ns_waitcnt lgkmcnt(0)\ns_add_u32 m0, %[tb], 0\ns_add_u32 s44, %[wo], 7602176\nbuffer_load_dwordx4 v92, %[rw], s44 offen nt lds\ns_add_u32 m0, %[tb], 1024\ns_add_u32 s44, %[wo], 7667712\n" \
"buffer_load_dwordx4 v92, %[rw], s44 offen nt lds\ns_add_u32 m0, %[tb], 2048\ns_add_u32 s44, %[wo], 7733248\nbuffer_load_dwordx4 v92, %[rw], s44 offen nt lds\ns_add_u32 m0, %[tb], 3072\ns_add_u32 s44, %[wo], 7798784\nbuffer_load_dwordx4 v92, %[rw], s44 offen nt lds\ns_waitcnt vmcnt(32)\n" \
"ds_read_b64_tr_b16 v[108:109], v93 offset:4096\nds_read_b64_tr_b16 v[110:111], v93 offset:4608\nds_read_b64_tr_b16 v[116:117], v93 offset:4104\nds_read_b64_tr_b16 v[118:119], v93 offset:4616\nds_read_b64_tr_b16 v[24:25], v93 offset:4160\nds_read_b64_tr_b16 v[26:27], v93 offset:4672\nds_read_b64_tr_b16 v[124:125], v93 offset:4168\nds_read_b64_tr_b16 v[126:127], v93 offset:4680\n" \
"s_waitcnt lgkmcnt(0)\nbuffer_load_dwordx2 v[88:89], off, s[36:39], s45 sc1\ns_add_u32 m0, %[tb], 4096\ns_add_u32 s44, %[wo], 7864320\nbuffer_load_dwordx4 v92, %[rw], s44 offen nt lds\ns_add_u32 m0, %[tb], 5120\ns_add_u32 s44, %[wo], 7929856\nbuffer_load_dwordx4 v92, %[rw], s44 offen nt lds\n" \
"s_add_u32 m0, %[tb], 6144\ns_add_u32 s44, %[wo], 7995392\nbuffer_load_dwordx4 v92, %[rw], s44 offen nt lds\ns_add_u32 m0, %[tb], 7168\ns_add_u32 s44, %[wo], 8060928\nbuffer_load_dwordx4 v92, %[rw], s44 offen nt lds\ns_waitcnt vmcnt(33)\nds_read_b64_tr_b16 v[128:129], v93 offset:8192\n" \
"ds_read_b64_tr_b16 v[130:131], v93 offset:8704\nds_read_b64_tr_b16 v[136:137], v93 offset:8200\nds_read_b64_tr_b16 v[138:139], v93 offset:8712\nds_read_b64_tr_b16 v[144:145], v93 offset:8256\nds_read_b64_tr_b16 v[146:147], v93 offset:8768\nds_read_b64_tr_b16 v[20:21], v93 offset:8264\nds_read_b64_tr_b16 v[22:23], v93 offset:8776\ns_waitcnt lgkmcnt(0)\n" \
"s_add_u32 m0, %[tb], 8192\ns_add_u32 s44, %[wo], 8126464\nbuffer_load_dwordx4 v92, %[rw], s44 offen nt lds\ns_add_u32 m0, %[tb], 9216\ns_add_u32 s44, %[wo], 8192000\nbuffer_load_dwordx4 v92, %[rw], s44 offen nt lds\ns_add_u32 m0, %[tb], 10240\ns_add_u32 s44, %[wo], 8257536\n" \
"buffer_load_dwordx4 v92, %[rw], s44 offen nt lds\ns_add_u32 m0, %[tb], 11264\ns_add_u32 s44, %[wo], 8323072\nbuffer_load_dwordx4 v92, %[rw], s44 offen nt lds\ns_waitcnt vmcnt(33)\nds_read_b64_tr_b16 v[132:133], v94 offset:0\nds_read_b64_tr_b16 v[134:135], v94 offset:512\nds_read_b64_tr_b16 v[140:141], v94 offset:8\n" \
"ds_read_b64_tr_b16 v[142:143], v94 offset:520\nds_read_b64_tr_b16 v[148:149], v94 offset:64\nds_read_b64_tr_b16 v[150:151], v94 offset:576\nds_read_b64_tr_b16 v[16:17], v94 offset:72\nds_read_b64_tr_b16 v[18:19], v94 offset:584\ns_waitcnt lgkmcnt(0)\nds_write_b128 v1, v[84:87] offset:0\nds_write_b128 v1, v[80:83] offset:1024\n" \
"ds_write_b128 v1, v[76:79] offset:2048\nds_write_b128 v1, v[72:75] offset:3072\ns_waitcnt vmcnt(29)\nds_read_b64_tr_b16 v[152:153], v94 offset:4096\nds_read_b64_tr_b16 v[154:155], v94 offset:4608\nds_read_b64_tr_b16 v[160:161], v94 offset:4104\nds_read_b64_tr_b16 v[162:163], v94 offset:4616\nds_read_b64_tr_b16 v[168:169], v94 offset:4160\n" \
"ds_read_b64_tr_b16 v[170:171], v94 offset:4672\nds_read_b64_tr_b16 v[176:177], v94 offset:4168\nds_read_b64_tr_b16 v[178:179], v94 offset:4680\ns_waitcnt lgkmcnt(0)\nds_write_b128 v1, v[68:71] offset:4096\nds_write_b128 v1, v[64:67] offset:5120\nds_write_b128 v1, v[60:63] offset:6144\nds_write_b128 v1, v[56:59] offset:7168\n" \
"s_waitcnt vmcnt(25)\nds_read_b64_tr_b16 v[156:157], v94 offset:8192\nds_read_b64_tr_b16 v[158:159], v94 offset:8704\nds_read_b64_tr_b16 v[164:165], v94 offset:8200\nds_read_b64_tr_b16 v[166:167], v94 offset:8712\nds_read_b64_tr_b16 v[172:173], v94 offset:8256\nds_read_b64_tr_b16 v[174:175], v94 offset:8768\nds_read_b64_tr_b16 v[180:181], v94 offset:8264\n" \
"ds_read_b64_tr_b16 v[182:183], v94 offset:8776\ns_waitcnt lgkmcnt(0)\nds_write_b128 v1, v[52:55] offset:8192\nds_write_b128 v1, v[48:51] offset:9216\nds_write_b128 v1, v[44:47] offset:10240\nds_write_b128 v1, v[40:43] offset:11264\ns_waitcnt vmcnt(21)\nds_read_b64_tr_b16 v[184:185], v94 offset:12288\n" \
"ds_read_b64_tr_b16 v[186:187], v94 offset:12800\nds_read_b64_tr_b16 v[40:41], v94 offset:12296\nds_read_b64_tr_b16 v[42:43], v94 offset:12808\nds_read_b64_tr_b16 v[192:193], v94 offset:12352\nds_read_b64_tr_b16 v[194:195], v94 offset:12864\nds_read_b64_tr_b16 v[200:201], v94 offset:12360\nds_read_b64_tr_b16 v[202:203], v94 offset:12872\ns_waitcnt lgkmcnt(0)\n" \
"ds_write_b128 v1, v[36:39] offset:12288\nds_write_b128 v1, v[32:35] offset:13312\nds_write_b128 v1, v[28:31] offset:14336\nds_write_b128 v1, v[24:27] offset:15360\ns_waitcnt vmcnt(17)\nds_read_b64_tr_b16 v[188:189], v94 offset:16384\nds_read_b64_tr_b16 v[190:191], v94 offset:16896\nds_read_b64_tr_b16 v[24:25], v94 offset:16392\n" \
"ds_read_b64_tr_b16 v[26:27], v94 offset:16904\nds_read_b64_tr_b16 v[196:197], v94 offset:16448\nds_read_b64_tr_b16 v[198:199], v94 offset:16960\nds_read_b64_tr_b16 v[204:205], v94 offset:16456\nds_read_b64_tr_b16 v[206:207], v94 offset:16968\ns_waitcnt lgkmcnt(0)\nds_write_b128 v1, v[20:23] offset:16384\nds_write_b128 v1, v[16:19] offset:17408\n" \
"ds_write_b128 v1, v[40:43] offset:18432\nds_write_b128 v1, v[24:27] offset:19456\ns_waitcnt vmcnt(13)\nds_read_b64_tr_b16 v[208:209], v94 offset:20480\nds_read_b64_tr_b16 v[210:211], v94 offset:20992\nds_read_b64_tr_b16 v[216:217], v94 offset:20488\nds_read_b64_tr_b16 v[218:219], v94 offset:21000\nds_read_b64_tr_b16 v[24:25], v94 offset:20544\n" \
"ds_read_b64_tr_b16 v[26:27], v94 offset:21056\nds_read_b64_tr_b16 v[224:225], v94 offset:20552\nds_read_b64_tr_b16 v[226:227], v94 offset:21064\ns_waitcnt lgkmcnt(0)\nds_write_b128 v1, v[24:27] offset:20480\ns_waitcnt vmcnt(9)\nds_read_b64_tr_b16 v[212:213], v93 offset:0\nds_read_b64_tr_b16 v[214:215], v93 offset:512\n" \
"ds_read_b64_tr_b16 v[220:221], v93 offset:8\nds_read_b64_tr_b16 v[222:223], v93 offset:520\nds_read_b64_tr_b16 v[24:25], v93 offset:64\nds_read_b64_tr_b16 v[26:27], v93 offset:576\nds_read_b64_tr_b16 v[228:229], v93 offset:72\nds_read_b64_tr_b16 v[230:231], v93 offset:584\ns_waitcnt lgkmcnt(0)\nds_write_b128 v1, v[24:27] offset:21504\n" \
"s_waitcnt vmcnt(4)\nds_read_b64_tr_b16 v[232:233], v93 offset:4096\nds_read_b64_tr_b16 v[234:235], v93 offset:4608\nds_read_b64_tr_b16 v[240:241], v93 offset:4104\nds_read_b64_tr_b16 v[242:243], v93 offset:4616\nds_read_b64_tr_b16 v[248:249], v93 offset:4160\nds_read_b64_tr_b16 v[250:251], v93 offset:4672\nds_read_b64_tr_b16 v[24:25], v93 offset:4168\n" \
"ds_read_b64_tr_b16 v[26:27], v93 offset:4680\ns_waitcnt lgkmcnt(0)\nds_write_b128 v1, v[24:27] offset:22528\ns_waitcnt vmcnt(0)\nds_read_b64_tr_b16 v[236:237], v93 offset:8192\nds_read_b64_tr_b16 v[238:239], v93 offset:8704\nds_read_b64_tr_b16 v[244:245], v93 offset:8200\nds_read_b64_tr_b16 v[246:247], v93 offset:8712\n" \
"ds_read_b64_tr_b16 v[252:253], v93 offset:8256\nds_read_b64_tr_b16 v[254:255], v93 offset:8768\nds_read_b64_tr_b16 v[24:25], v93 offset:8264\nds_read_b64_tr_b16 v[26:27], v93 offset:8776\ns_waitcnt lgkmcnt(0)\ns_mov_b32 s43, 0\ns_mov_b32 s86, 110\ns_mov_b32 s89, 0\n" \
".Lqk_hb%=:\nv_cmp_ne_u32 vcc, v95, v88\ns_cbranch_vccnz .Lqk_hb1%=\ns_sleep 4\nbuffer_load_dwordx2 v[88:89], off, s[36:39], s45 sc1\ns_waitcnt vmcnt(0)\ns_add_u32 s43, s43, 1\ns_cmp_lt_u32 s43, 262144\n" \
"s_cbranch_scc1 .Lqk_hb%=\ns_branch .Lqk_abort%=\n.Lqk_hb1%=:\nv_readfirstlane_b32 s44, v89\ns_and_b32 s44, s44, 0x200000\ns_add_u32 s36, s36, s44\ns_addc_u32 s37, s37, 0\nds_write_b128 v1, v[24:27] offset:23552\n" \
"s_waitcnt lgkmcnt(0)\ns_barrier\ns_mov_b32 s40, -1\ns_mov_b32 s43, 0\ns_mov_b32 s61, 0x7fff7fff\ns_mov_b32 s58, 0x3e0293ee\ns_mov_b32 s59, 0xbe0293ee\ns_lshl_b32 s57, %[w], 12\n" \
"s_mov_b32 s62, 0xff00ff00\ns_mov_b32 s63, 0xff00ff00\ns_mov_b32 s56, 0\ns_cmp_eq_u32 %[w], 3\ns_cbranch_scc1 .Lqk_fetch0%=\n.Lqk_pwait%=:\ns_barrier\nds_read_b128 v[80:83], v2 offset:0\n" \
"ds_read_b128 v[84:87], v2 offset:2048\n.Lqk_proj%=:\ns_waitcnt lgkmcnt(1)\nv_mfma_f32_16x16x32_bf16 v[16:19], a[0:3], v[80:83], 0\ns_waitcnt lgkmcnt(0)\nds_read_b128 v[88:91], v2 offset:256\nv_mfma_f32_16x16x32_bf16 v[16:19], a[4:7], v[84:87], v[16:19]\nds_read_b128 v[92:95], v2 offset:2304\n" \
"v_mfma_f32_16x16x32_bf16 v[20:23], a[8:11], v[80:83], 0\nv_mfma_f32_16x16x32_bf16 v[20:23], a[12:15], v[84:87], v[20:23]\nv_mfma_f32_16x16x32_bf16 v[24:27], a[16:19], v[80:83], 0\nds_read_b128 v[72:75], v1 offset:0\nv_mfma_f32_16x16x32_bf16 v[24:27], a[20:23], v[84:87], v[24:27]\nds_read_b128 v[76:79], v1 offset:1024\nv_mfma_f32_16x16x32_bf16 v[28:31], a[24:27], v[80:83], 0\nv_mfma_f32_16x16x32_bf16 v[28:31], a[28:31], v[84:87], v[28:31]\n" \
"s_waitcnt lgkmcnt(3)\nv_mfma_f32_16x16x32_bf16 v[32:35], a[0:3], v[88:91], 0\ns_waitcnt lgkmcnt(2)\nds_read_b128 v[80:83], v2 offset:4096\nv_mfma_f32_16x16x32_bf16 v[32:35], a[4:7], v[92:95], v[32:35]\nds_read_b128 v[84:87], v2 offset:6144\nv_mfma_f32_16x16x32_bf16 v[36:39], a[8:11], v[88:91], 0\nv_mfma_f32_16x16x32_bf16 v[36:39], a[12:15], v[92:95], v[36:39]\n" \
"v_mfma_f32_16x16x32_bf16 v[40:43], a[16:19], v[88:91], 0\nv_mfma_f32_16x16x32_bf16 v[40:43], a[20:23], v[92:95], v[40:43]\nv_mfma_f32_16x16x32_bf16 v[44:47], a[24:27], v[88:91], 0\nv_mfma_f32_16x16x32_bf16 v[44:47], a[28:31], v[92:95], v[44:47]\ns_waitcnt lgkmcnt(1)\nv_mfma_f32_16x16x32_bf16 v[16:19], a[32:35], v[80:83], v[16:19]\ns_waitcnt lgkmcnt(0)\nds_read_b128 v[88:91], v2 offset:4352\n" \
"v_mfma_f32_16x16x32_bf16 v[16:19], a[36:39], v[84:87], v[16:19]\nds_read_b128 v[92:95], v2 offset:6400\nv_mfma_f32_16x16x32_bf16 v[20:23], v[72:75], v[80:83], v[20:23]\nv_mfma_f32_16x16x32_bf16 v[20:23], v[76:79], v[84:87], v[20:23]\nv_mfma_f32_16x16x32_bf16 v[24:27], a[40:43], v[80:83], v[24:27]\nds_read_b128 v[64:67], v1 offset:2048\nv_mfma_f32_16x16x32_bf16 v[24:27], a[44:47], v[84:87], v[24:27]\nds_read_b128 v[68:71], v1 offset:3072\n" \
"v_mfma_f32_16x16x32_bf16 v[28:31], a[48:51], v[80:83], v[28:31]\nv_mfma_f32_16x16x32_bf16 v[28:31], a[52:55], v[84:87], v[28:31]\ns_waitcnt lgkmcnt(3)\nv_mfma_f32_16x16x32_bf16 v[32:35], a[32:35], v[88:91], v[32:35]\ns_waitcnt lgkmcnt(2)\nds_read_b128 v[80:83], v2 offset:8192\nv_mfma_f32_16x16x32_bf16 v[32:35], a[36:39], v[92:95], v[32:35]\nds_read_b128 v[84:87], v2 offset:10240\n" \
"v_mfma_f32_16x16x32_bf16 v[36:39], v[72:75], v[88:91], v[36:39]\nv_mfma_f32_16x16x32_bf16 v[36:39], v[76:79], v[92:95], v[36:39]\nv_mfma_f32_16x16x32_bf16 v[40:43], a[40:43], v[88:91], v[40:43]\nv_mfma_f32_16x16x32_bf16 v[40:43], a[44:47], v[92:95], v[40:43]\nv_mfma_f32_16x16x32_bf16 v[44:47], a[48:51], v[88:91], v[44:47]\nv_mfma_f32_16x16x32_bf16 v[44:47], a[52:55], v[92:95], v[44:47]\ns_waitcnt lgkmcnt(1)\nv_mfma_f32_16x16x32_bf16 v[16:19], a[56:59], v[80:83], v[16:19]\n" \
"s_waitcnt lgkmcnt(0)\nds_read_b128 v[88:91], v2 offset:8448\nv_mfma_f32_16x16x32_bf16 v[16:19], a[60:63], v[84:87], v[16:19]\nds_read_b128 v[92:95], v2 offset:10496\nv_mfma_f32_16x16x32_bf16 v[20:23], a[64:67], v[80:83], v[20:23]\nv_mfma_f32_16x16x32_bf16 v[20:23], a[68:71], v[84:87], v[20:23]\nv_mfma_f32_16x16x32_bf16 v[24:27], v[64:67], v[80:83], v[24:27]\nds_read_b128 v[72:75], v1 offset:4096\n" \
"v_mfma_f32_16x16x32_bf16 v[24:27], v[68:71], v[84:87], v[24:27]\nds_read_b128 v[76:79], v1 offset:5120\nv_mfma_f32_16x16x32_bf16 v[28:31], a[72:75], v[80:83], v[28:31]\nv_mfma_f32_16x16x32_bf16 v[28:31], a[76:79], v[84:87], v[28:31]\ns_waitcnt lgkmcnt(3)\nv_mfma_f32_16x16x32_bf16 v[32:35], a[56:59], v[88:91], v[32:35]\ns_waitcnt lgkmcnt(2)\nds_read_b128 v[80:83], v2 offset:12288\n" \
"v_mfma_f32_16x16x32_bf16 v[32:35], a[60:63], v[92:95], v[32:35]\nds_read_b128 v[84:87], v2 offset:14336\nv_mfma_f32_16x16x32_bf16 v[36:39], a[64:67], v[88:91], v[36:39]\nv_mfma_f32_16x16x32_bf16 v[36:39], a[68:71], v[92:95], v[36:39]\nv_mfma_f32_16x16x32_bf16 v[40:43], v[64:67], v[88:91], v[40:43]\nv_mfma_f32_16x16x32_bf16 v[40:43], v[68:71], v[92:95], v[40:43]\nv_mfma_f32_16x16x32_bf16 v[44:47], a[72:75], v[88:91], v[44:47]\nv_mfma_f32_16x16x32_bf16 v[44:47], a[76:79], v[92:95], v[44:47]\n" \
".p2align 6\n.Lqk_pj%=:\ns_waitcnt lgkmcnt(1)\nv_mfma_f32_16x16x32_bf16 v[16:19], a[80:83], v[80:83], v[16:19]\ns_waitcnt lgkmcnt(0)\nds_read_b128 v[88:91], v2 offset:12544\nv_mfma_f32_16x16x32_bf16 v[16:19], a[84:87], v[84:87], v[16:19]\ns_add_u32 s40, s40, 1\n" \
"ds_read_b128 v[92:95], v2 offset:14592\nv_mfma_f32_16x16x32_bf16 v[20:23], a[88:91], v[80:83], v[20:23]\ns_lshl_b32 s44, s40, 17\nv_mfma_f32_16x16x32_bf16 v[20:23], a[92:95], v[84:87], v[20:23]\ns_add_u32 s44, s44, %[xn]\nv_mfma_f32_16x16x32_bf16 v[24:27], a[96:99], v[80:83], v[24:27]\nv_mfma_f32_16x16x32_bf16 v[24:27], a[100:103], v[84:87], v[24:27]\ns_lshr_b32 s45, s44, 8\n" \
"v_mfma_f32_16x16x32_bf16 v[28:31], v[72:75], v[80:83], v[28:31]\ns_and_b32 s45, s45, 0x2000\nv_mfma_f32_16x16x32_bf16 v[28:31], v[76:79], v[84:87], v[28:31]\ns_xor_b32 s41, s44, s45\ns_waitcnt lgkmcnt(1)\nv_mfma_f32_16x16x32_bf16 v[32:35], a[80:83], v[88:91], v[32:35]\ns_barrier\ns_waitcnt lgkmcnt(0)\n" \
"ds_read_b128 v[80:83], v2 offset:16384\nv_mfma_f32_16x16x32_bf16 v[32:35], a[84:87], v[92:95], v[32:35]\ns_add_u32 s41, s41, %[xw]\ns_and_b32 s42, s40, 3\nds_read_b128 v[84:87], v2 offset:18432\nv_mfma_f32_16x16x32_bf16 v[36:39], a[88:91], v[88:91], v[36:39]\ns_add_u32 m0, s57, 32768\ns_add_u32 s60, s41, 65536\n" \
"buffer_load_dwordx4 v0, s[36:39], s60 offen offset:0 sc1 lds\nv_mfma_f32_16x16x32_bf16 v[36:39], a[92:95], v[92:95], v[36:39]\ns_lshl_b32 s42, s42, 15\nv_mfma_f32_16x16x32_bf16 v[40:43], a[96:99], v[88:91], v[40:43]\nv_mfma_f32_16x16x32_bf16 v[40:43], a[100:103], v[92:95], v[40:43]\ns_and_b32 s44, s40, 1\nv_mfma_f32_16x16x32_bf16 v[44:47], v[72:75], v[88:91], v[44:47]\ns_lshl_b32 s55, s44, 17\n" \
"buffer_load_dwordx4 v0, s[36:39], s60 offen offset:1024 sc1 lds\nv_mfma_f32_16x16x32_bf16 v[44:47], v[76:79], v[92:95], v[44:47]\ns_add_u32 s56, s56, %[fs]\ns_waitcnt lgkmcnt(1)\nv_mfma_f32_16x16x32_bf16 v[16:19], a[104:107], v[80:83], v[16:19]\ns_waitcnt lgkmcnt(0)\nds_read_b128 v[88:91], v2 offset:16640\nv_mfma_f32_16x16x32_bf16 v[16:19], a[108:111], v[84:87], v[16:19]\n" \
"ds_read_b128 v[92:95], v2 offset:18688\nv_mfma_f32_16x16x32_bf16 v[20:23], a[112:115], v[80:83], v[20:23]\nbuffer_load_dwordx4 v0, s[36:39], s60 offen offset:2048 sc1 lds\nv_mfma_f32_16x16x32_bf16 v[20:23], a[116:119], v[84:87], v[20:23]\nv_mfma_f32_16x16x32_bf16 v[24:27], a[120:123], v[80:83], v[24:27]\nds_read_b128 v[72:75], v1 offset:6144\nv_mfma_f32_16x16x32_bf16 v[24:27], a[124:127], v[84:87], v[24:27]\nds_read_b128 v[76:79], v1 offset:7168\n" \
"v_mfma_f32_16x16x32_bf16 v[28:31], a[128:131], v[80:83], v[28:31]\nbuffer_load_dwordx4 v0, s[36:39], s60 offen offset:3072 sc1 lds\nv_mfma_f32_16x16x32_bf16 v[28:31], a[132:135], v[84:87], v[28:31]\ns_waitcnt lgkmcnt(3)\nv_mfma_f32_16x16x32_bf16 v[32:35], a[104:107], v[88:91], v[32:35]\ns_waitcnt lgkmcnt(2)\nds_read_b128 v[80:83], v2 offset:20480\nv_mfma_f32_16x16x32_bf16 v[32:35], a[108:111], v[92:95], v[32:35]\n" \
"ds_read_b128 v[84:87], v2 offset:22528\nv_mfma_f32_16x16x32_bf16 v[36:39], a[112:115], v[88:91], v[36:39]\nv_mfma_f32_16x16x32_bf16 v[36:39], a[116:119], v[92:95], v[36:39]\nv_mfma_f32_16x16x32_bf16 v[40:43], a[120:123], v[88:91], v[40:43]\nv_mfma_f32_16x16x32_bf16 v[40:43], a[124:127], v[92:95], v[40:43]\nv_mfma_f32_16x16x32_bf16 v[44:47], a[128:131], v[88:91], v[44:47]\nv_mfma_f32_16x16x32_bf16 v[44:47], a[132:135], v[92:95], v[44:47]\ns_waitcnt lgkmcnt(1)\n" \
"v_mfma_f32_16x16x32_bf16 v[16:19], a[136:139], v[80:83], v[16:19]\ns_waitcnt lgkmcnt(0)\nds_read_b128 v[88:91], v2 offset:20736\nv_mfma_f32_16x16x32_bf16 v[16:19], a[140:143], v[84:87], v[16:19]\nds_read_b128 v[92:95], v2 offset:22784\nv_mfma_f32_16x16x32_bf16 v[20:23], v[72:75], v[80:83], v[20:23]\nv_mfma_f32_16x16x32_bf16 v[20:23], v[76:79], v[84:87], v[20:23]\nv_mfma_f32_16x16x32_bf16 v[24:27], a[144:147], v[80:83], v[24:27]\n" \
"ds_read_b128 v[64:67], v1 offset:8192\nv_mfma_f32_16x16x32_bf16 v[24:27], a[148:151], v[84:87], v[24:27]\nds_read_b128 v[68:71], v1 offset:9216\nv_mfma_f32_16x16x32_bf16 v[28:31], a[152:155], v[80:83], v[28:31]\nv_mfma_f32_16x16x32_bf16 v[28:31], a[156:159], v[84:87], v[28:31]\ns_waitcnt lgkmcnt(3)\nv_mfma_f32_16x16x32_bf16 v[32:35], a[136:139], v[88:91], v[32:35]\ns_waitcnt lgkmcnt(2)\n" \
"ds_read_b128 v[80:83], v2 offset:24576\nv_mfma_f32_16x16x32_bf16 v[32:35], a[140:143], v[92:95], v[32:35]\nds_read_b128 v[84:87], v2 offset:26624\nv_mfma_f32_16x16x32_bf16 v[36:39], v[72:75], v[88:91], v[36:39]\nv_mfma_f32_16x16x32_bf16 v[36:39], v[76:79], v[92:95], v[36:39]\nv_mfma_f32_16x16x32_bf16 v[40:43], a[144:147], v[88:91], v[40:43]\nv_mfma_f32_16x16x32_bf16 v[40:43], a[148:151], v[92:95], v[40:43]\nv_mfma_f32_16x16x32_bf16 v[44:47], a[152:155], v[88:91], v[44:47]\n" \
"v_mfma_f32_16x16x32_bf16 v[44:47], a[156:159], v[92:95], v[44:47]\ns_waitcnt lgkmcnt(1)\nv_mfma_f32_16x16x32_bf16 v[16:19], a[160:163], v[80:83], v[16:19]\ns_waitcnt lgkmcnt(0)\nds_read_b128 v[88:91], v2 offset:24832\nv_mfma_f32_16x16x32_bf16 v[16:19], a[164:167], v[84:87], v[16:19]\nds_read_b128 v[92:95], v2 offset:26880\nv_mfma_f32_16x16x32_bf16 v[20:23], a[168:171], v[80:83], v[20:23]\n" \
"v_mfma_f32_16x16x32_bf16 v[20:23], a[172:175], v[84:87], v[20:23]\nv_mfma_f32_16x16x32_bf16 v[24:27], v[64:67], v[80:83], v[24:27]\nds_read_b128 v[72:75], v1 offset:10240\nv_mfma_f32_16x16x32_bf16 v[24:27], v[68:71], v[84:87], v[24:27]\nds_read_b128 v[76:79], v1 offset:11264\nv_mfma_f32_16x16x32_bf16 v[28:31], a[176:179], v[80:83], v[28:31]\nv_mfma_f32_16x16x32_bf16 v[28:31], a[180:183], v[84:87], v[28:31]\ns_waitcnt lgkmcnt(3)\n" \
"v_mfma_f32_16x16x32_bf16 v[32:35], a[160:163], v[88:91], v[32:35]\ns_waitcnt lgkmcnt(2)\nds_read_b128 v[80:83], v2 offset:28672\nv_mfma_f32_16x16x32_bf16 v[32:35], a[164:167], v[92:95], v[32:35]\nds_read_b128 v[84:87], v2 offset:30720\nv_mfma_f32_16x16x32_bf16 v[36:39], a[168:171], v[88:91], v[36:39]\nv_mfma_f32_16x16x32_bf16 v[36:39], a[172:175], v[92:95], v[36:39]\nv_mfma_f32_16x16x32_bf16 v[40:43], v[64:67], v[88:91], v[40:43]\n" \
"v_mfma_f32_16x16x32_bf16 v[40:43], v[68:71], v[92:95], v[40:43]\nv_mfma_f32_16x16x32_bf16 v[44:47], a[176:179], v[88:91], v[44:47]\nv_mfma_f32_16x16x32_bf16 v[44:47], a[180:183], v[92:95], v[44:47]\ns_waitcnt lgkmcnt(1)\nv_mfma_f32_16x16x32_bf16 v[16:19], a[184:187], v[80:83], v[16:19]\ns_waitcnt lgkmcnt(0)\nds_read_b128 v[88:91], v2 offset:28928\nv_mfma_f32_16x16x32_bf16 v[16:19], a[188:191], v[84:87], v[16:19]\n" \
"ds_read_b128 v[92:95], v2 offset:30976\nv_mfma_f32_16x16x32_bf16 v[20:23], a[192:195], v[80:83], v[20:23]\nv_mfma_f32_16x16x32_bf16 v[20:23], a[196:199], v[84:87], v[20:23]\nv_mfma_f32_16x16x32_bf16 v[24:27], a[200:203], v[80:83], v[24:27]\nv_mfma_f32_16x16x32_bf16 v[24:27], a[204:207], v[84:87], v[24:27]\nv_mfma_f32_16x16x32_bf16 v[28:31], v[72:75], v[80:83], v[28:31]\nv_mfma_f32_16x16x32_bf16 v[28:31], v[76:79], v[84:87], v[28:31]\ns_waitcnt lgkmcnt(1)\n" \
"v_mfma_f32_16x16x32_bf16 v[32:35], a[184:187], v[88:91], v[32:35]\ns_waitcnt vmcnt(0)\ns_barrier\ns_waitcnt lgkmcnt(0)\nds_read_b128 v[48:51], v2 offset:32768\nv_mfma_f32_16x16x32_bf16 v[32:35], a[188:191], v[92:95], v[32:35]\ns_add_u32 m0, s57, 0\nds_read_b128 v[52:55], v2 offset:34816\n" \
"v_mfma_f32_16x16x32_bf16 v[36:39], a[192:195], v[88:91], v[36:39]\ns_add_u32 s60, s41, 98304\nbuffer_load_dwordx4 v0, s[36:39], s60 offen offset:0 sc1 lds\nv_mfma_f32_16x16x32_bf16 v[36:39], a[196:199], v[92:95], v[36:39]\nv_mfma_f32_16x16x32_bf16 v[40:43], a[200:203], v[88:91], v[40:43]\nv_mfma_f32_16x16x32_bf16 v[40:43], a[204:207], v[92:95], v[40:43]\nv_mfma_f32_16x16x32_bf16 v[44:47], v[72:75], v[88:91], v[44:47]\nbuffer_load_dwordx4 v0, s[36:39], s60 offen offset:1024 sc1 lds\n" \
"v_mfma_f32_16x16x32_bf16 v[44:47], v[76:79], v[92:95], v[44:47]\ns_waitcnt lgkmcnt(1)\nv_mfma_f32_16x16x32_bf16 v[16:19], a[208:211], v[48:51], v[16:19]\ns_waitcnt lgkmcnt(0)\nds_read_b128 v[56:59], v2 offset:33024\nv_mfma_f32_16x16x32_bf16 v[16:19], a[212:215], v[52:55], v[16:19]\nds_read_b128 v[60:63], v2 offset:35072\nv_mfma_f32_16x16x32_bf16 v[20:23], a[216:219], v[48:51], v[20:23]\n" \
"buffer_load_dwordx4 v0, s[36:39], s60 offen offset:2048 sc1 lds\nv_mfma_f32_16x16x32_bf16 v[20:23], a[220:223], v[52:55], v[20:23]\nv_mfma_f32_16x16x32_bf16 v[24:27], a[224:227], v[48:51], v[24:27]\nds_read_b128 v[72:75], v1 offset:12288\nv_mfma_f32_16x16x32_bf16 v[24:27], a[228:231], v[52:55], v[24:27]\nds_read_b128 v[76:79], v1 offset:13312\nv_mfma_f32_16x16x32_bf16 v[28:31], a[232:235], v[48:51], v[28:31]\nbuffer_load_dwordx4 v0, s[36:39], s60 offen offset:3072 sc1 lds\n" \
"v_mfma_f32_16x16x32_bf16 v[28:31], a[236:239], v[52:55], v[28:31]\ns_waitcnt lgkmcnt(3)\nv_mfma_f32_16x16x32_bf16 v[32:35], a[208:211], v[56:59], v[32:35]\ns_waitcnt lgkmcnt(2)\nds_read_b128 v[48:51], v2 offset:36864\nv_mfma_f32_16x16x32_bf16 v[32:35], a[212:215], v[60:63], v[32:35]\nds_read_b128 v[52:55], v2 offset:38912\nv_mfma_f32_16x16x32_bf16 v[36:39], a[216:219], v[56:59], v[36:39]\n" \
"v_mfma_f32_16x16x32_bf16 v[36:39], a[220:223], v[60:63], v[36:39]\nv_mfma_f32_16x16x32_bf16 v[40:43], a[224:227], v[56:59], v[40:43]\nv_mfma_f32_16x16x32_bf16 v[40:43], a[228:231], v[60:63], v[40:43]\nv_mfma_f32_16x16x32_bf16 v[44:47], a[232:235], v[56:59], v[44:47]\nv_mfma_f32_16x16x32_bf16 v[44:47], a[236:239], v[60:63], v[44:47]\ns_waitcnt lgkmcnt(1)\nv_mfma_f32_16x16x32_bf16 v[16:19], a[240:243], v[48:51], v[16:19]\ns_waitcnt lgkmcnt(0)\n" \
"ds_read_b128 v[56:59], v2 offset:37120\nv_mfma_f32_16x16x32_bf16 v[16:19], a[244:247], v[52:55], v[16:19]\nds_read_b128 v[60:63], v2 offset:39168\nv_mfma_f32_16x16x32_bf16 v[20:23], v[72:75], v[48:51], v[20:23]\nv_mfma_f32_16x16x32_bf16 v[20:23], v[76:79], v[52:55], v[20:23]\nv_mfma_f32_16x16x32_bf16 v[24:27], a[248:251], v[48:51], v[24:27]\nds_read_b128 v[64:67], v1 offset:14336\nv_mfma_f32_16x16x32_bf16 v[24:27], a[252:255], v[52:55], v[24:27]\n" \
"ds_read_b128 v[68:71], v1 offset:15360\nv_mfma_f32_16x16x32_bf16 v[28:31], v[96:99], v[48:51], v[28:31]\nv_mfma_f32_16x16x32_bf16 v[28:31], v[100:103], v[52:55], v[28:31]\ns_waitcnt lgkmcnt(3)\nv_mfma_f32_16x16x32_bf16 v[32:35], a[240:243], v[56:59], v[32:35]\ns_waitcnt lgkmcnt(2)\nds_read_b128 v[48:51], v2 offset:40960\nv_mfma_f32_16x16x32_bf16 v[32:35], a[244:247], v[60:63], v[32:35]\n" \
"ds_read_b128 v[52:55], v2 offset:43008\nv_mfma_f32_16x16x32_bf16 v[36:39], v[72:75], v[56:59], v[36:39]\nv_mfma_f32_16x16x32_bf16 v[36:39], v[76:79], v[60:63], v[36:39]\nv_mfma_f32_16x16x32_bf16 v[40:43], a[248:251], v[56:59], v[40:43]\nv_mfma_f32_16x16x32_bf16 v[40:43], a[252:255], v[60:63], v[40:43]\nv_mfma_f32_16x16x32_bf16 v[44:47], v[96:99], v[56:59], v[44:47]\nv_mfma_f32_16x16x32_bf16 v[44:47], v[100:103], v[60:63], v[44:47]\ns_waitcnt lgkmcnt(1)\n" \
"v_mfma_f32_16x16x32_bf16 v[16:19], v[104:107], v[48:51], v[16:19]\ns_waitcnt lgkmcnt(0)\nds_read_b128 v[56:59], v2 offset:41216\nv_mfma_f32_16x16x32_bf16 v[16:19], v[108:111], v[52:55], v[16:19]\nds_read_b128 v[60:63], v2 offset:43264\nv_mfma_f32_16x16x32_bf16 v[20:23], v[112:115], v[48:51], v[20:23]\nv_mfma_f32_16x16x32_bf16 v[20:23], v[116:119], v[52:55], v[20:23]\nv_mfma_f32_16x16x32_bf16 v[24:27], v[64:67], v[48:51], v[24:27]\n" \
"ds_read_b128 v[72:75], v1 offset:16384\nv_mfma_f32_16x16x32_bf16 v[24:27], v[68:71], v[52:55], v[24:27]\nds_read_b128 v[76:79], v1 offset:17408\nv_mfma_f32_16x16x32_bf16 v[28:31], v[120:123], v[48:51], v[28:31]\nv_mfma_f32_16x16x32_bf16 v[28:31], v[124:127], v[52:55], v[28:31]\ns_waitcnt lgkmcnt(3)\nv_mfma_f32_16x16x32_bf16 v[32:35], v[104:107], v[56:59], v[32:35]\ns_waitcnt lgkmcnt(2)\n" \
"ds_read_b128 v[48:51], v2 offset:45056\nv_mfma_f32_16x16x32_bf16 v[32:35], v[108:111], v[60:63], v[32:35]\nds_read_b128 v[52:55], v2 offset:47104\nv_mfma_f32_16x16x32_bf16 v[36:39], v[112:115], v[56:59], v[36:39]\nv_mfma_f32_16x16x32_bf16 v[36:39], v[116:119], v[60:63], v[36:39]\nv_mfma_f32_16x16x32_bf16 v[40:43], v[64:67], v[56:59], v[40:43]\nv_mfma_f32_16x16x32_bf16 v[40:43], v[68:71], v[60:63], v[40:43]\nv_mfma_f32_16x16x32_bf16 v[44:47], v[120:123], v[56:59], v[44:47]\n" \
"v_mfma_f32_16x16x32_bf16 v[44:47], v[124:127], v[60:63], v[44:47]\ns_waitcnt lgkmcnt(1)\nv_mfma_f32_16x16x32_bf16 v[16:19], v[128:131], v[48:51], v[16:19]\ns_waitcnt lgkmcnt(0)\nds_read_b128 v[56:59], v2 offset:45312\nv_mfma_f32_16x16x32_bf16 v[16:19], v[132:135], v[52:55], v[16:19]\nds_read_b128 v[60:63], v2 offset:47360\nv_mfma_f32_16x16x32_bf16 v[20:23], v[136:139], v[48:51], v[20:23]\n" \
"v_mfma_f32_16x16x32_bf16 v[20:23], v[140:143], v[52:55], v[20:23]\nv_mfma_f32_16x16x32_bf16 v[24:27], v[144:147], v[48:51], v[24:27]\nv_mfma_f32_16x16x32_bf16 v[24:27], v[148:151], v[52:55], v[24:27]\nv_mfma_f32_16x16x32_bf16 v[28:31], v[72:75], v[48:51], v[28:31]\nv_mfma_f32_16x16x32_bf16 v[28:31], v[76:79], v[52:55], v[28:31]\ns_waitcnt lgkmcnt(1)\nv_mfma_f32_16x16x32_bf16 v[32:35], v[128:131], v[56:59], v[32:35]\ns_waitcnt lgkmcnt(0)\n" \
"s_waitcnt vmcnt(0)\ns_barrier\nds_read_b128 v[48:51], v2 offset:0\nv_mfma_f32_16x16x32_bf16 v[32:35], v[132:135], v[60:63], v[32:35]\nds_read_b128 v[52:55], v2 offset:2048\nv_mfma_f32_16x16x32_bf16 v[36:39], v[136:139], v[56:59], v[36:39]\nv_mfma_f32_16x16x32_bf16 v[36:39], v[140:143], v[60:63], v[36:39]\nv_mfma_f32_16x16x32_bf16 v[40:43], v[144:147], v[56:59], v[40:43]\n" \
"v_mfma_f32_16x16x32_bf16 v[40:43], v[148:151], v[60:63], v[40:43]\nv_mfma_f32_16x16x32_bf16 v[44:47], v[72:75], v[56:59], v[44:47]\nv_mfma_f32_16x16x32_bf16 v[44:47], v[76:79], v[60:63], v[44:47]\ns_waitcnt lgkmcnt(1)\nv_mfma_f32_16x16x32_bf16 v[16:19], v[152:155], v[48:51], v[16:19]\ns_waitcnt lgkmcnt(0)\nds_read_b128 v[56:59], v2 offset:256\nv_mfma_f32_16x16x32_bf16 v[16:19], v[156:159], v[52:55], v[16:19]\n" \
"ds_read_b128 v[60:63], v2 offset:2304\nv_mfma_f32_16x16x32_bf16 v[20:23], v[160:163], v[48:51], v[20:23]\nv_mfma_f32_16x16x32_bf16 v[20:23], v[164:167], v[52:55], v[20:23]\nv_mfma_f32_16x16x32_bf16 v[24:27], v[168:171], v[48:51], v[24:27]\nds_read_b128 v[72:75], v1 offset:18432\nv_mfma_f32_16x16x32_bf16 v[24:27], v[172:175], v[52:55], v[24:27]\nds_read_b128 v[76:79], v1 offset:19456\nv_mfma_f32_16x16x32_bf16 v[28:31], v[176:179], v[48:51], v[28:31]\n" \
"v_mfma_f32_16x16x32_bf16 v[28:31], v[180:183], v[52:55], v[28:31]\ns_waitcnt lgkmcnt(3)\nv_mfma_f32_16x16x32_bf16 v[32:35], v[152:155], v[56:59], v[32:35]\ns_waitcnt lgkmcnt(2)\nds_read_b128 v[48:51], v2 offset:4096\nv_mfma_f32_16x16x32_bf16 v[32:35], v[156:159], v[60:63], v[32:35]\nds_read_b128 v[52:55], v2 offset:6144\nv_mfma_f32_16x16x32_bf16 v[36:39], v[160:163], v[56:59], v[36:39]\n" \
"v_mfma_f32_16x16x32_bf16 v[36:39], v[164:167], v[60:63], v[36:39]\nv_mfma_f32_16x16x32_bf16 v[40:43], v[168:171], v[56:59], v[40:43]\nv_mfma_f32_16x16x32_bf16 v[40:43], v[172:175], v[60:63], v[40:43]\nv_mfma_f32_16x16x32_bf16 v[44:47], v[176:179], v[56:59], v[44:47]\nv_mfma_f32_16x16x32_bf16 v[44:47], v[180:183], v[60:63], v[44:47]\ns_waitcnt lgkmcnt(1)\nv_mfma_f32_16x16x32_bf16 v[16:19], v[184:187], v[48:51], v[16:19]\ns_waitcnt lgkmcnt(0)\n" \
"ds_read_b128 v[56:59], v2 offset:4352\nv_mfma_f32_16x16x32_bf16 v[16:19], v[188:191], v[52:55], v[16:19]\nds_read_b128 v[60:63], v2 offset:6400\nv_mfma_f32_16x16x32_bf16 v[20:23], v[72:75], v[48:51], v[20:23]\nv_mfma_f32_16x16x32_bf16 v[20:23], v[76:79], v[52:55], v[20:23]\nv_mfma_f32_16x16x32_bf16 v[24:27], v[192:195], v[48:51], v[24:27]\nds_read_b128 v[64:67], v1 offset:20480\nv_mfma_f32_16x16x32_bf16 v[24:27], v[196:199], v[52:55], v[24:27]\n" \
"ds_read_b128 v[68:71], v1 offset:21504\nv_mfma_f32_16x16x32_bf16 v[28:31], v[200:203], v[48:51], v[28:31]\nv_mfma_f32_16x16x32_bf16 v[28:31], v[204:207], v[52:55], v[28:31]\ns_waitcnt lgkmcnt(3)\nv_mfma_f32_16x16x32_bf16 v[32:35], v[184:187], v[56:59], v[32:35]\ns_waitcnt lgkmcnt(2)\nds_read_b128 v[48:51], v2 offset:8192\nv_mfma_f32_16x16x32_bf16 v[32:35], v[188:191], v[60:63], v[32:35]\n" \
"ds_read_b128 v[52:55], v2 offset:10240\nv_mfma_f32_16x16x32_bf16 v[36:39], v[72:75], v[56:59], v[36:39]\nv_mfma_f32_16x16x32_bf16 v[36:39], v[76:79], v[60:63], v[36:39]\nv_mfma_f32_16x16x32_bf16 v[40:43], v[192:195], v[56:59], v[40:43]\nv_mfma_f32_16x16x32_bf16 v[40:43], v[196:199], v[60:63], v[40:43]\nv_mfma_f32_16x16x32_bf16 v[44:47], v[200:203], v[56:59], v[44:47]\nbuffer_load_dwordx4 v[80:83], v3, s[36:39], s42 offen offset:0 sc1\nv_mfma_f32_16x16x32_bf16 v[44:47], v[204:207], v[60:63], v[44:47]\n" \
"s_waitcnt lgkmcnt(1)\nv_mfma_f32_16x16x32_bf16 v[16:19], v[208:211], v[48:51], v[16:19]\ns_waitcnt lgkmcnt(0)\nds_read_b128 v[56:59], v2 offset:12288\nv_mfma_f32_16x16x32_bf16 v[16:19], v[212:215], v[52:55], v[16:19]\nds_read_b128 v[60:63], v2 offset:14336\nv_mfma_f32_16x16x32_bf16 v[20:23], v[216:219], v[48:51], v[20:23]\nbuffer_load_dwordx4 v[84:87], v3, s[36:39], s42 offen offset:1024 sc1\n" \
"v_mfma_f32_16x16x32_bf16 v[20:23], v[220:223], v[52:55], v[20:23]\nv_mfma_f32_16x16x32_bf16 v[24:27], v[64:67], v[48:51], v[24:27]\nds_read_b128 v[72:75], v1 offset:22528\nv_mfma_f32_16x16x32_bf16 v[24:27], v[68:71], v[52:55], v[24:27]\nds_read_b128 v[76:79], v1 offset:23552\nv_mfma_f32_16x16x32_bf16 v[28:31], v[224:227], v[48:51], v[28:31]\nbuffer_load_dwordx4 v[88:91], v3, s[36:39], s42 offen offset:2048 sc1\nv_mfma_f32_16x16x32_bf16 v[28:31], v[228:231], v[52:55], v[28:31]\n" \
"s_waitcnt lgkmcnt(3)\nv_mfma_f32_16x16x32_bf16 v[16:19], v[232:235], v[56:59], v[16:19]\ns_waitcnt lgkmcnt(2)\nds_read_b128 v[48:51], v2 offset:8448\nv_mfma_f32_16x16x32_bf16 v[16:19], v[236:239], v[60:63], v[16:19]\nds_read_b128 v[52:55], v2 offset:10496\nv_mfma_f32_16x16x32_bf16 v[20:23], v[240:243], v[56:59], v[20:23]\nbuffer_load_dwordx4 v[92:95], v3, s[36:39], s42 offen offset:3072 sc1\n" \
"v_mfma_f32_16x16x32_bf16 v[20:23], v[244:247], v[60:63], v[20:23]\nv_mfma_f32_16x16x32_bf16 v[24:27], v[248:251], v[56:59], v[24:27]\nv_mfma_f32_16x16x32_bf16 v[24:27], v[252:255], v[60:63], v[24:27]\ns_waitcnt lgkmcnt(3)\nv_mfma_f32_16x16x32_bf16 v[28:31], v[72:75], v[56:59], v[28:31]\ns_waitcnt lgkmcnt(2)\nv_cvt_pk_bf16_f32 v20, v16, v20\nv_cvt_pk_bf16_f32 v21, v17, v21\n" \
"v_mfma_f32_16x16x32_bf16 v[28:31], v[76:79], v[60:63], v[28:31]\ns_waitcnt lgkmcnt(1)\nv_cvt_pk_bf16_f32 v22, v18, v22\nv_cvt_pk_bf16_f32 v23, v19, v23\nv_mfma_f32_16x16x32_bf16 v[32:35], v[208:211], v[48:51], v[32:35]\ns_waitcnt lgkmcnt(0)\nds_read_b128 v[56:59], v2 offset:12544\nv_mfma_f32_16x16x32_bf16 v[32:35], v[212:215], v[52:55], v[32:35]\n" \
"ds_read_b128 v[60:63], v2 offset:14592\nv_mfma_f32_16x16x32_bf16 v[36:39], v[216:219], v[48:51], v[36:39]\nds_write_b128 v5, v[20:23] offset:0\nv_mfma_f32_16x16x32_bf16 v[36:39], v[220:223], v[52:55], v[36:39]\nv_cvt_pk_bf16_f32 v28, v24, v28\nv_cvt_pk_bf16_f32 v29, v25, v29\nv_mfma_f32_16x16x32_bf16 v[40:43], v[64:67], v[48:51], v[40:43]\nv_cvt_pk_bf16_f32 v30, v26, v30\n" \
"v_cvt_pk_bf16_f32 v31, v27, v31\nv_mfma_f32_16x16x32_bf16 v[40:43], v[68:71], v[52:55], v[40:43]\nv_mfma_f32_16x16x32_bf16 v[44:47], v[224:227], v[48:51], v[44:47]\ns_cmp_eq_u32 %[w], 1\nds_write_b128 v5, v[28:31] offset:512\nv_mfma_f32_16x16x32_bf16 v[44:47], v[228:231], v[52:55], v[44:47]\ns_cbranch_scc1 .Lqk_c1%=\ns_cmp_eq_u32 %[w], 2\n" \
"s_cbranch_scc1 .Lqk_c2%=\ns_cmp_eq_u32 %[w], 3\ns_cbranch_scc1 .Lqk_c3%=\n.Lqk_c0%=:\ns_waitcnt lgkmcnt(3)\nv_mfma_f32_16x16x32_bf16 v[32:35], v[232:235], v[56:59], v[32:35]\ns_waitcnt lgkmcnt(2)\nv_mov_b64 v[48:49], 0\n" \
"v_mov_b64 v[50:51], 0\nv_mfma_f32_16x16x32_bf16 v[32:35], v[236:239], v[60:63], v[32:35]\nds_read_b128 v[64:67], v1 offset:0\nv_mfma_f32_16x16x32_bf16 v[36:39], v[240:243], v[56:59], v[36:39]\nds_read_b128 v[68:71], v1 offset:1024\nv_mfma_f32_16x16x32_bf16 v[36:39], v[244:247], v[60:63], v[36:39]\ns_bfe_i32 s54, s40, 0x10000\nv_mov_b64 v[52:53], 0\n" \
"v_mov_b64 v[54:55], 0\nv_mfma_f32_16x16x32_bf16 v[40:43], v[248:251], v[56:59], v[40:43]\nv_mfma_f32_16x16x32_bf16 v[40:43], v[252:255], v[60:63], v[40:43]\nv_cvt_pk_bf16_f32 v36, v32, v36\nv_cvt_pk_bf16_f32 v37, v33, v37\nv_mfma_f32_16x16x32_bf16 v[44:47], v[72:75], v[56:59], v[44:47]\nv_cvt_pk_bf16_f32 v38, v34, v38\nv_cvt_pk_bf16_f32 v39, v35, v39\n" \
"v_mfma_f32_16x16x32_bf16 v[44:47], v[76:79], v[60:63], v[44:47]\nds_read_b128 v[72:75], v1 offset:2048\nds_read_b128 v[76:79], v1 offset:3072\nv_mov_b64 v[56:57], 0\nv_mov_b64 v[58:59], 0\nv_mov_b64 v[60:61], 0\nv_mov_b64 v[62:63], 0\n.Lqk_poll0%=:\n" \
"s_waitcnt vmcnt(0)\nv_smfmac_f32_16x16x64_bf16 v[48:51], v[80:83], a[0:7], v4\nds_write_b128 v0, v[80:83] offset:16384\nv_max3_u32 v16, v80, v82, v84\nv_smfmac_f32_16x16x64_bf16 v[48:51], v[84:87], a[32:39], v4\nv_max3_u32 v16, v16, v86, v88\nv_smfmac_f32_16x16x64_bf16 v[52:55], v[80:83], a[8:15], v4\ns_waitcnt lgkmcnt(3)\n" \
"ds_write_b128 v0, v[84:87] offset:17408\nv_max3_u32 v16, v16, v90, v92\nv_max_u32 v16, v16, v94\nv_smfmac_f32_16x16x64_bf16 v[52:55], v[84:87], v[64:71], v4\nv_cmp_eq_u32 vcc, -1, v16\nv_smfmac_f32_16x16x64_bf16 v[56:59], v[80:83], a[16:23], v4\nds_write_b128 v0, v[88:91] offset:18432\ns_cbranch_vccnz .Lqk_re0%=\n" \
"v_smfmac_f32_16x16x64_bf16 v[56:59], v[84:87], a[40:47], v4\nv_smfmac_f32_16x16x64_bf16 v[60:63], v[80:83], a[24:31], v4\nds_write_b128 v0, v[92:95] offset:19456\nv_smfmac_f32_16x16x64_bf16 v[60:63], v[84:87], a[48:55], v4\nv_smfmac_f32_16x16x64_bf16 v[48:51], v[88:91], a[56:63], v4\nv_smfmac_f32_16x16x64_bf16 v[48:51], v[92:95], a[80:87], v4\ns_waitcnt lgkmcnt(0)\n.Lqk_jn0%=:\n" \
"s_barrier\ns_memrealtime s[68:69]\nds_read_b128 v[80:83], v0 offset:20480\nds_read_b128 v[64:67], v1 offset:4096\nds_read_b128 v[68:71], v1 offset:5120\nv_mov_b32 v24, s40\nv_smfmac_f32_16x16x64_bf16 v[52:55], v[88:91], a[64:71], v4\nv_smfmac_f32_16x16x64_bf16 v[52:55], v[92:95], a[88:95], v4\n" \
"ds_read_b128 v[84:87], v0 offset:21504\nv_smfmac_f32_16x16x64_bf16 v[56:59], v[88:91], v[72:79], v4\nds_read_b128 v[72:75], v1 offset:6144\nds_read_b128 v[76:79], v1 offset:7168\nv_smfmac_f32_16x16x64_bf16 v[56:59], v[92:95], a[96:103], v4\nv_smfmac_f32_16x16x64_bf16 v[60:63], v[88:91], a[72:79], v4\ns_waitcnt lgkmcnt(3)\nds_read_b128 v[88:91], v0 offset:22528\n" \
"v_smfmac_f32_16x16x64_bf16 v[60:63], v[92:95], v[64:71], v4\ns_sub_u32 s88, s86, s89\nds_read_b128 v[92:95], v0 offset:23552\nds_read_b128 v[64:67], v1 offset:8192\nds_read_b128 v[68:71], v1 offset:9216\nv_smfmac_f32_16x16x64_bf16 v[48:51], v[80:83], a[104:111], v4\ns_min_u32 s88, s88, 150\ns_waitcnt lgkmcnt(6)\n" \
"ds_write_b128 v5, v[36:39] offset:256\nv_cvt_pk_bf16_f32 v44, v40, v44\nv_cvt_pk_bf16_f32 v45, v41, v45\nv_cvt_pk_bf16_f32 v46, v42, v46\nv_cvt_pk_bf16_f32 v47, v43, v47\nv_smfmac_f32_16x16x64_bf16 v[48:51], v[84:87], a[136:143], v4\ns_add_u32 s88, s88, s68\ns_mov_b32 s89, s68\n" \
"v_mov_b32 v25, s88\nv_smfmac_f32_16x16x64_bf16 v[52:55], v[80:83], a[112:119], v4\ns_waitcnt lgkmcnt(5)\nbuffer_store_dwordx2 v[24:25], off, s[36:39], %[sg] sc1\nv_smfmac_f32_16x16x64_bf16 v[52:55], v[84:87], v[72:79], v4\nds_read_b128 v[72:75], v1 offset:10240\nds_read_b128 v[76:79], v1 offset:11264\nv_smfmac_f32_16x16x64_bf16 v[56:59], v[80:83], a[120:127], v4\n" \
"v_smfmac_f32_16x16x64_bf16 v[56:59], v[84:87], a[144:151], v4\nv_smfmac_f32_16x16x64_bf16 v[60:63], v[80:83], a[128:135], v4\nds_read_b128 v[80:83], v0 offset:24576\nv_smfmac_f32_16x16x64_bf16 v[60:63], v[84:87], a[152:159], v4\ns_waitcnt lgkmcnt(7)\nds_read_b128 v[84:87], v0 offset:25600\nv_smfmac_f32_16x16x64_bf16 v[48:51], v[88:91], a[160:167], v4\ns_waitcnt lgkmcnt(7)\n" \
"ds_write_b128 v5, v[44:47] offset:768\nv_smfmac_f32_16x16x64_bf16 v[48:51], v[92:95], a[184:191], v4\nv_smfmac_f32_16x16x64_bf16 v[52:55], v[88:91], a[168:175], v4\nv_smfmac_f32_16x16x64_bf16 v[52:55], v[92:95], a[192:199], v4\ns_waitcnt lgkmcnt(6)\nv_smfmac_f32_16x16x64_bf16 v[56:59], v[88:91], v[64:71], v4\nds_read_b128 v[64:67], v1 offset:12288\nds_read_b128 v[68:71], v1 offset:13312\n" \
"v_smfmac_f32_16x16x64_bf16 v[56:59], v[92:95], a[200:207], v4\ns_waitcnt lgkmcnt(2)\ns_barrier\nv_smfmac_f32_16x16x64_bf16 v[60:63], v[88:91], a[176:183], v4\nds_read_b128 v[88:91], v0 offset:26624\nds_read_b128 v[16:19], v7 offset:0\nv_smfmac_f32_16x16x64_bf16 v[60:63], v[92:95], v[72:79], v4\nds_read_b128 v[92:95], v0 offset:27648\n" \
"ds_read_b128 v[72:75], v1 offset:14336\nds_read_b128 v[76:79], v1 offset:15360\nv_smfmac_f32_16x16x64_bf16 v[48:51], v[80:83], a[208:215], v4\nds_read_b128 v[20:23], v7 offset:64\nv_smfmac_f32_16x16x64_bf16 v[48:51], v[84:87], a[240:247], v4\nv_smfmac_f32_16x16x64_bf16 v[52:55], v[80:83], a[216:223], v4\ns_waitcnt lgkmcnt(6)\nds_read_b128 v[24:27], v7 offset:2048\n" \
"v_smfmac_f32_16x16x64_bf16 v[52:55], v[84:87], v[64:71], v4\nds_read_b128 v[64:67], v1 offset:16384\nds_read_b128 v[68:71], v1 offset:17408\nv_smfmac_f32_16x16x64_bf16 v[56:59], v[80:83], a[224:231], v4\nds_read_b128 v[28:31], v7 offset:2112\nv_smfmac_f32_16x16x64_bf16 v[56:59], v[84:87], a[248:255], v4\nv_smfmac_f32_16x16x64_bf16 v[60:63], v[80:83], a[232:239], v4\nds_read_b128 v[80:83], v0 offset:28672\n" \
"ds_read_b128 v[32:35], v7 offset:4096\nv_smfmac_f32_16x16x64_bf16 v[60:63], v[84:87], v[96:103], v4\ns_waitcnt lgkmcnt(11)\nds_read_b128 v[84:87], v0 offset:29696\nv_smfmac_f32_16x16x64_bf16 v[48:51], v[88:91], v[104:111], v4\ns_waitcnt lgkmcnt(10)\nds_read_b128 v[36:39], v7 offset:4160\nv_smfmac_f32_16x16x64_bf16 v[48:51], v[92:95], v[128:135], v4\n" \
"v_smfmac_f32_16x16x64_bf16 v[52:55], v[88:91], v[112:119], v4\nds_read_b128 v[40:43], v7 offset:6144\nv_smfmac_f32_16x16x64_bf16 v[52:55], v[92:95], v[136:143], v4\ns_waitcnt lgkmcnt(10)\nv_smfmac_f32_16x16x64_bf16 v[56:59], v[88:91], v[72:79], v4\nds_read_b128 v[72:75], v1 offset:18432\nds_read_b128 v[76:79], v1 offset:19456\nv_smfmac_f32_16x16x64_bf16 v[56:59], v[92:95], v[144:151], v4\n" \
"v_smfmac_f32_16x16x64_bf16 v[60:63], v[88:91], v[120:127], v4\ns_waitcnt lgkmcnt(8)\nds_read_b128 v[88:91], v0 offset:30720\nds_read_b128 v[44:47], v7 offset:6208\nv_smfmac_f32_16x16x64_bf16 v[60:63], v[92:95], v[64:71], v4\ns_waitcnt lgkmcnt(8)\nds_read_b128 v[92:95], v0 offset:31744\nds_read_b128 v[64:67], v1 offset:20480\n" \
"ds_read_b128 v[68:71], v1 offset:21504\nv_smfmac_f32_16x16x64_bf16 v[48:51], v[80:83], v[152:159], v4\ns_waitcnt lgkmcnt(9)\nv_smfmac_f32_16x16x64_bf16 v[48:51], v[84:87], v[184:191], v4\nv_smfmac_f32_16x16x64_bf16 v[52:55], v[80:83], v[160:167], v4\ns_waitcnt lgkmcnt(5)\nv_smfmac_f32_16x16x64_bf16 v[52:55], v[84:87], v[72:79], v4\nds_read_b128 v[72:75], v1 offset:22528\n" \
"ds_read_b128 v[76:79], v1 offset:23552\nv_smfmac_f32_16x16x64_bf16 v[56:59], v[80:83], v[168:175], v4\nv_smfmac_f32_16x16x64_bf16 v[56:59], v[84:87], v[192:199], v4\nv_smfmac_f32_16x16x64_bf16 v[60:63], v[80:83], v[176:183], v4\nv_smfmac_f32_16x16x64_bf16 v[60:63], v[84:87], v[200:207], v4\ns_waitcnt lgkmcnt(6)\nv_smfmac_f32_16x16x64_bf16 v[48:51], v[88:91], v[208:215], v4\ns_waitcnt lgkmcnt(4)\n" \
"v_smfmac_f32_16x16x64_bf16 v[48:51], v[92:95], v[232:239], v4\nv_smfmac_f32_16x16x64_bf16 v[52:55], v[88:91], v[216:223], v4\nv_smfmac_f32_16x16x64_bf16 v[52:55], v[92:95], v[240:247], v4\ns_waitcnt lgkmcnt(2)\nv_smfmac_f32_16x16x64_bf16 v[56:59], v[88:91], v[64:71], v4\nv_add_f32 v48, v48, v50\nv_smfmac_f32_16x16x64_bf16 v[56:59], v[92:95], v[248:255], v4\nv_add_f32 v52, v52, v54\n" \
"v_add_f32 v49, v49, v51\nv_smfmac_f32_16x16x64_bf16 v[60:63], v[88:91], v[224:231], v4\ns_waitcnt lgkmcnt(0)\nv_add_f32 v53, v53, v55\nv_cvt_pk_bf16_f32 v50, v48, v52\nv_smfmac_f32_16x16x64_bf16 v[60:63], v[92:95], v[72:79], v4\nv_cvt_pk_bf16_f32 v51, v49, v53\nds_write2_b32 v6, v50, v51 offset0:0 offset1:4\n" \
"v_add_f32 v56, v56, v58\nv_add_f32 v57, v57, v59\ns_nop 3\nv_add_f32 v60, v60, v62\nv_add_f32 v61, v61, v63\nv_cvt_pk_bf16_f32 v62, v56, v60\nv_cvt_pk_bf16_f32 v63, v57, v61\nds_write2_b32 v6, v62, v63 offset0:36 offset1:40\n" \
"v_mov_b32 v61, s54\ns_waitcnt lgkmcnt(0)\ns_barrier\nds_read_b128 v[80:83], v8 offset:0\nds_read_b128 v[84:87], v8 offset:576\nds_read_b128 v[88:91], v8 offset:1152\nds_read_b128 v[92:95], v8 offset:1728\nds_read_b128 v[64:67], v9 offset:0\n" \
"ds_read_b128 v[68:71], v9 offset:576\nds_read_b128 v[72:75], v9 offset:1152\nds_read_b128 v[76:79], v9 offset:1728\nds_read_b32 v60, v0 offset:53760\ns_waitcnt lgkmcnt(8)\nv_mfma_f32_16x16x32_bf16 v[52:55], v[16:19], v[80:83], 0\nv_mfma_f32_16x16x32_bf16 v[56:59], v[20:23], v[80:83], 0\ns_waitcnt lgkmcnt(7)\n" \
"v_mfma_f32_16x16x32_bf16 v[52:55], v[24:27], v[84:87], v[52:55]\nv_mfma_f32_16x16x32_bf16 v[56:59], v[28:31], v[84:87], v[56:59]\ns_waitcnt lgkmcnt(6)\nv_mfma_f32_16x16x32_bf16 v[52:55], v[32:35], v[88:91], v[52:55]\nv_mfma_f32_16x16x32_bf16 v[56:59], v[36:39], v[88:91], v[56:59]\ns_waitcnt lgkmcnt(5)\nv_mfma_f32_16x16x32_bf16 v[52:55], v[40:43], v[92:95], v[52:55]\nv_mfma_f32_16x16x32_bf16 v[56:59], v[44:47], v[92:95], v[56:59]\n" \
"s_waitcnt lgkmcnt(4)\nv_mfma_f32_16x16x32_bf16 v[48:51], v[64:67], v[80:83], 0\ns_waitcnt lgkmcnt(3)\nv_mfma_f32_16x16x32_bf16 v[48:51], v[68:71], v[84:87], v[48:51]\ns_waitcnt lgkmcnt(2)\nds_read_b128 v[80:83], v2 offset:0\nv_mfma_f32_16x16x32_bf16 v[48:51], v[72:75], v[88:91], v[48:51]\ns_waitcnt lgkmcnt(2)\n" \
"ds_read_b128 v[84:87], v2 offset:2048\nv_mfma_f32_16x16x32_bf16 v[48:51], v[76:79], v[92:95], v[48:51]\nv_max3_f32 v62, v52, v53, v54\nv_max3_f32 v63, v55, v56, v57\nv_max3_f32 v62, v62, v58, v59\nv_cndmask_b32 v52, v52, v56, s[62:63]\nv_cndmask_b32 v53, v53, v57, s[62:63]\nv_cndmask_b32 v54, v54, v58, s[62:63]\n" \
"v_cndmask_b32 v55, v55, v59, s[62:63]\nv_max_f32 v62, v62, v63\nv_max3_f32 v63, v62, v48, v49\nv_max3_f32 v62, v62, v48, v49\ns_waitcnt lgkmcnt(1)\nv_mfma_f32_16x16x32_bf16 v[16:19], a[0:3], v[80:83], 0\ns_waitcnt lgkmcnt(0)\nds_read_b128 v[88:91], v2 offset:256\n" \
"v_mfma_f32_16x16x32_bf16 v[16:19], a[4:7], v[84:87], v[16:19]\nds_read_b128 v[92:95], v2 offset:2304\nv_mfma_f32_16x16x32_bf16 v[20:23], a[8:11], v[80:83], 0\nv_permlane16_swap_b32 v62, v63\nv_mfma_f32_16x16x32_bf16 v[20:23], a[12:15], v[84:87], v[20:23]\nv_max_f32 v50, v62, v63\nv_max_f32 v62, v62, v63\nv_mfma_f32_16x16x32_bf16 v[24:27], a[16:19], v[80:83], 0\n" \
"ds_read_b128 v[72:75], v1 offset:0\nv_mfma_f32_16x16x32_bf16 v[24:27], a[20:23], v[84:87], v[24:27]\nds_read_b128 v[76:79], v1 offset:1024\nv_mfma_f32_16x16x32_bf16 v[28:31], a[24:27], v[80:83], 0\nv_permlane32_swap_b32 v62, v50\nv_mfma_f32_16x16x32_bf16 v[28:31], a[28:31], v[84:87], v[28:31]\ns_waitcnt lgkmcnt(3)\nv_max_f32 v62, v62, v50\n" \
"v_mul_f32 v62, s59, v62\nv_mfma_f32_16x16x32_bf16 v[32:35], a[0:3], v[88:91], 0\ns_waitcnt lgkmcnt(2)\nds_read_b128 v[80:83], v2 offset:4096\nv_fma_f32 v52, v52, s58, v62\nv_mfma_f32_16x16x32_bf16 v[32:35], a[4:7], v[92:95], v[32:35]\nds_read_b128 v[84:87], v2 offset:6144\nv_fma_f32 v53, v53, s58, v62\n" \
"v_mfma_f32_16x16x32_bf16 v[36:39], a[8:11], v[88:91], 0\nv_exp_f32 v52, v52\nv_mfma_f32_16x16x32_bf16 v[36:39], a[12:15], v[92:95], v[36:39]\nv_fma_f32 v54, v54, s58, v62\nv_fma_f32 v55, v55, s58, v62\nv_mfma_f32_16x16x32_bf16 v[40:43], a[16:19], v[88:91], 0\nv_exp_f32 v53, v53\nv_mfma_f32_16x16x32_bf16 v[40:43], a[20:23], v[92:95], v[40:43]\n" \
"v_exp_f32 v54, v54\nv_mfma_f32_16x16x32_bf16 v[44:47], a[24:27], v[88:91], 0\nv_add_f32 v63, v52, v53\nv_fma_f32 v48, v48, s58, v62\nv_mfma_f32_16x16x32_bf16 v[44:47], a[28:31], v[92:95], v[44:47]\ns_waitcnt lgkmcnt(1)\nv_exp_f32 v55, v55\nv_mfma_f32_16x16x32_bf16 v[16:19], a[32:35], v[80:83], v[16:19]\n" \
"s_waitcnt lgkmcnt(0)\nds_read_b128 v[88:91], v2 offset:4352\nv_add_f32 v63, v63, v54\nv_mfma_f32_16x16x32_bf16 v[16:19], a[36:39], v[84:87], v[16:19]\nds_read_b128 v[92:95], v2 offset:6400\nv_fma_f32 v49, v49, s58, v62\nv_mfma_f32_16x16x32_bf16 v[20:23], v[72:75], v[80:83], v[20:23]\nv_exp_f32 v48, v48\n" \
"v_mfma_f32_16x16x32_bf16 v[20:23], v[76:79], v[84:87], v[20:23]\nv_add_f32 v63, v63, v55\nv_add_f32 v50, v48, v63\nv_mfma_f32_16x16x32_bf16 v[24:27], a[40:43], v[80:83], v[24:27]\nds_read_b128 v[64:67], v1 offset:2048\nv_mfma_f32_16x16x32_bf16 v[24:27], a[44:47], v[84:87], v[24:27]\nds_read_b128 v[68:71], v1 offset:3072\nv_mfma_f32_16x16x32_bf16 v[28:31], a[48:51], v[80:83], v[28:31]\n" \
"v_exp_f32 v49, v49\nv_mfma_f32_16x16x32_bf16 v[28:31], a[52:55], v[84:87], v[28:31]\ns_waitcnt lgkmcnt(3)\nv_add_f32 v50, v50, v49\nv_mfma_f32_16x16x32_bf16 v[32:35], a[32:35], v[88:91], v[32:35]\ns_waitcnt lgkmcnt(2)\nds_read_b128 v[80:83], v2 offset:8192\nv_add_f32_dpp v62, v63, v50 row_ror:8 row_mask:0xf bank_mask:0xf\n" \
"v_mfma_f32_16x16x32_bf16 v[32:35], a[36:39], v[92:95], v[32:35]\nds_read_b128 v[84:87], v2 offset:10240\nv_add_f32_dpp v50, v63, v50 row_ror:8 row_mask:0xf bank_mask:0xf\nv_mfma_f32_16x16x32_bf16 v[36:39], v[72:75], v[88:91], v[36:39]\ns_nop 0\nv_permlane16_swap_b32 v62, v50\nv_mfma_f32_16x16x32_bf16 v[36:39], v[76:79], v[92:95], v[36:39]\nv_add_f32 v63, v62, v50\n" \
"v_add_f32 v50, v62, v50\nv_mfma_f32_16x16x32_bf16 v[40:43], a[40:43], v[88:91], v[40:43]\ns_nop 0\nv_permlane32_swap_b32 v63, v50\nv_mfma_f32_16x16x32_bf16 v[40:43], a[44:47], v[92:95], v[40:43]\nv_add_f32 v63, v63, v50\nv_mfma_f32_16x16x32_bf16 v[44:47], a[48:51], v[88:91], v[44:47]\nv_rcp_f32 v63, v63\n" \
"v_mfma_f32_16x16x32_bf16 v[44:47], a[52:55], v[92:95], v[44:47]\ns_waitcnt lgkmcnt(1)\nv_mul_f32 v52, v52, v63\nv_mul_f32 v53, v53, v63\nv_mfma_f32_16x16x32_bf16 v[16:19], a[56:59], v[80:83], v[16:19]\ns_waitcnt lgkmcnt(0)\nds_read_b128 v[88:91], v2 offset:8448\nv_mul_f32 v54, v54, v63\n" \
"v_mfma_f32_16x16x32_bf16 v[16:19], a[60:63], v[84:87], v[16:19]\nds_read_b128 v[92:95], v2 offset:10496\nv_mul_f32 v55, v55, v63\nv_mfma_f32_16x16x32_bf16 v[20:23], a[64:67], v[80:83], v[20:23]\nv_mul_f32 v48, v48, v63\nv_mul_f32 v49, v49, v63\nv_mfma_f32_16x16x32_bf16 v[20:23], a[68:71], v[84:87], v[20:23]\nv_cvt_pk_bf16_f32 v52, v52, v53\n" \
"v_cvt_pk_bf16_f32 v53, v54, v55\nv_mfma_f32_16x16x32_bf16 v[24:27], v[64:67], v[80:83], v[24:27]\nds_read_b128 v[72:75], v1 offset:4096\nv_cvt_pk_bf16_f32 v48, v48, v49\nv_mfma_f32_16x16x32_bf16 v[24:27], v[68:71], v[84:87], v[24:27]\nds_read_b128 v[76:79], v1 offset:5120\nv_bfi_b32 v52, s61, v52, v61\nv_mfma_f32_16x16x32_bf16 v[28:31], a[72:75], v[80:83], v[28:31]\n" \
"v_bfi_b32 v53, s61, v53, v61\nv_bfi_b32 v48, s61, v48, v61\nv_mfma_f32_16x16x32_bf16 v[28:31], a[76:79], v[84:87], v[28:31]\ns_waitcnt lgkmcnt(3)\nv_mov_b32_dpp v54, v52 row_ror:8 row_mask:0xf bank_mask:0xf\nv_mov_b32_dpp v55, v53 row_ror:8 row_mask:0xf bank_mask:0xf\nv_mfma_f32_16x16x32_bf16 v[32:35], a[56:59], v[88:91], v[32:35]\ns_waitcnt lgkmcnt(2)\n" \
"ds_read_b128 v[80:83], v2 offset:12288\nv_mfma_f32_16x16x32_bf16 v[32:35], a[60:63], v[92:95], v[32:35]\nds_read_b128 v[84:87], v2 offset:14336\nv_mfma_f32_16x16x32_bf16 v[36:39], a[64:67], v[88:91], v[36:39]\nbuffer_store_dwordx4 v[52:55], v10, s[36:39], 0 offen sc1\nv_mfma_f32_16x16x32_bf16 v[36:39], a[68:71], v[92:95], v[36:39]\nv_mov_b32 v49, v48\nv_mfma_f32_16x16x32_bf16 v[40:43], v[64:67], v[88:91], v[40:43]\n" \
"s_nop 0\nv_permlane32_swap_b32 v48, v49\nv_mfma_f32_16x16x32_bf16 v[40:43], v[68:71], v[92:95], v[40:43]\ns_memrealtime s[86:87]\nbuffer_store_dwordx2 v[48:49], v11, s[36:39], 0 offen sc1\nv_mfma_f32_16x16x32_bf16 v[44:47], a[72:75], v[88:91], v[44:47]\nv_cmp_ne_u32 vcc, 0, v60\nv_mfma_f32_16x16x32_bf16 v[44:47], a[76:79], v[92:95], v[44:47]\n" \
"s_cbranch_vccnz .Lqk_pj%=\ns_cmp_lt_i32 s40, 63\ns_waitcnt lgkmcnt(0)\ns_cbranch_scc1 .Lqk_pwait%=\ns_branch .Lqk_end%=\n.Lqk_re0%=:\nv_max_u32 v17, v80, v82\nv_cmp_eq_u32 vcc, -1, v17\n" \
"s_cbranch_vccz .Lqk_sk1619%=\ns_mov_b64 exec, vcc\nbuffer_load_dwordx4 v[80:83], v3, s[36:39], s42 offen offset:0 sc1\ns_mov_b64 exec, -1\n.Lqk_sk1619%=:\nv_max_u32 v17, v84, v86\nv_cmp_eq_u32 vcc, -1, v17\ns_cbranch_vccz .Lqk_sk1626%=\n" \
"s_mov_b64 exec, vcc\nbuffer_load_dwordx4 v[84:87], v3, s[36:39], s42 offen offset:1024 sc1\ns_mov_b64 exec, -1\n.Lqk_sk1626%=:\nv_max_u32 v17, v88, v90\nv_cmp_eq_u32 vcc, -1, v17\ns_cbranch_vccz .Lqk_sk1633%=\ns_mov_b64 exec, vcc\n" \
"buffer_load_dwordx4 v[88:91], v3, s[36:39], s42 offen offset:2048 sc1\ns_mov_b64 exec, -1\n.Lqk_sk1633%=:\nv_max_u32 v17, v92, v94\nv_cmp_eq_u32 vcc, -1, v17\ns_cbranch_vccz .Lqk_sk1640%=\ns_mov_b64 exec, vcc\nbuffer_load_dwordx4 v[92:95], v3, s[36:39], s42 offen offset:3072 sc1\n" \
"s_mov_b64 exec, -1\n.Lqk_sk1640%=:\nv_mov_b64 v[48:49], 0\nv_mov_b64 v[50:51], 0\nv_mov_b64 v[52:53], 0\nv_mov_b64 v[54:55], 0\nv_mov_b64 v[56:57], 0\nv_mov_b64 v[58:59], 0\n" \
"v_mov_b64 v[60:61], 0\nv_mov_b64 v[62:63], 0\ns_add_u32 s43, s43, 1\ns_cmp_lt_u32 s43, 262144\ns_cbranch_scc1 .Lqk_poll0%=\ns_branch .Lqk_abort%=\n.p2align 6\n.Lqk_c1%=:\n" \
"s_waitcnt lgkmcnt(3)\nv_mfma_f32_16x16x32_bf16 v[32:35], v[232:235], v[56:59], v[32:35]\ns_waitcnt lgkmcnt(2)\nv_mov_b64 v[48:49], 0\nv_mov_b64 v[50:51], 0\nv_mfma_f32_16x16x32_bf16 v[32:35], v[236:239], v[60:63], v[32:35]\nds_read_b128 v[64:67], v1 offset:6144\nv_mfma_f32_16x16x32_bf16 v[36:39], v[240:243], v[56:59], v[36:39]\n" \
"ds_read_b128 v[68:71], v1 offset:7168\nv_mfma_f32_16x16x32_bf16 v[36:39], v[244:247], v[60:63], v[36:39]\ns_bfe_i32 s54, s40, 0x10001\nv_mov_b64 v[52:53], 0\nv_mov_b64 v[54:55], 0\nv_mfma_f32_16x16x32_bf16 v[40:43], v[248:251], v[56:59], v[40:43]\nv_mfma_f32_16x16x32_bf16 v[40:43], v[252:255], v[60:63], v[40:43]\nv_cvt_pk_bf16_f32 v36, v32, v36\n" \
"v_cvt_pk_bf16_f32 v37, v33, v37\nv_mfma_f32_16x16x32_bf16 v[44:47], v[72:75], v[56:59], v[44:47]\nv_cvt_pk_bf16_f32 v38, v34, v38\nv_cvt_pk_bf16_f32 v39, v35, v39\nv_mfma_f32_16x16x32_bf16 v[44:47], v[76:79], v[60:63], v[44:47]\nds_read_b128 v[72:75], v1 offset:8192\nds_read_b128 v[76:79], v1 offset:9216\nv_mov_b64 v[56:57], 0\n" \
"v_mov_b64 v[58:59], 0\nv_mov_b64 v[60:61], 0\nv_mov_b64 v[62:63], 0\n.Lqk_poll1%=:\ns_waitcnt vmcnt(0)\nv_smfmac_f32_16x16x64_bf16 v[48:51], v[80:83], a[104:111], v4\nds_write_b128 v0, v[80:83] offset:20480\nv_max3_u32 v16, v80, v82, v84\n" \
"v_smfmac_f32_16x16x64_bf16 v[48:51], v[84:87], a[136:143], v4\nv_max3_u32 v16, v16, v86, v88\nv_smfmac_f32_16x16x64_bf16 v[52:55], v[80:83], a[112:119], v4\ns_waitcnt lgkmcnt(3)\nds_write_b128 v0, v[84:87] offset:21504\nv_max3_u32 v16, v16, v90, v92\nv_max_u32 v16, v16, v94\nv_smfmac_f32_16x16x64_bf16 v[52:55], v[84:87], v[64:71], v4\n" \
"v_cmp_eq_u32 vcc, -1, v16\nv_smfmac_f32_16x16x64_bf16 v[56:59], v[80:83], a[120:127], v4\nds_write_b128 v0, v[88:91] offset:22528\ns_cbranch_vccnz .Lqk_re1%=\nv_smfmac_f32_16x16x64_bf16 v[56:59], v[84:87], a[144:151], v4\nv_smfmac_f32_16x16x64_bf16 v[60:63], v[80:83], a[128:135], v4\nds_write_b128 v0, v[92:95] offset:23552\nv_smfmac_f32_16x16x64_bf16 v[60:63], v[84:87], a[152:159], v4\n" \
"v_smfmac_f32_16x16x64_bf16 v[48:51], v[88:91], a[160:167], v4\nv_smfmac_f32_16x16x64_bf16 v[48:51], v[92:95], a[184:191], v4\ns_waitcnt lgkmcnt(0)\n.Lqk_jn1%=:\ns_barrier\nds_read_b128 v[80:83], v0 offset:24576\nds_read_b128 v[64:67], v1 offset:10240\nds_read_b128 v[68:71], v1 offset:11264\n" \
"v_smfmac_f32_16x16x64_bf16 v[52:55], v[88:91], a[168:175], v4\nv_smfmac_f32_16x16x64_bf16 v[52:55], v[92:95], a[192:199], v4\nds_read_b128 v[84:87], v0 offset:25600\nv_smfmac_f32_16x16x64_bf16 v[56:59], v[88:91], v[72:79], v4\nds_read_b128 v[72:75], v1 offset:12288\nds_read_b128 v[76:79], v1 offset:13312\nv_smfmac_f32_16x16x64_bf16 v[56:59], v[92:95], a[200:207], v4\nv_smfmac_f32_16x16x64_bf16 v[60:63], v[88:91], a[176:183], v4\n" \
"s_waitcnt lgkmcnt(3)\nds_read_b128 v[88:91], v0 offset:26624\nv_smfmac_f32_16x16x64_bf16 v[60:63], v[92:95], v[64:71], v4\nds_read_b128 v[92:95], v0 offset:27648\nds_read_b128 v[64:67], v1 offset:14336\nds_read_b128 v[68:71], v1 offset:15360\nv_smfmac_f32_16x16x64_bf16 v[48:51], v[80:83], a[208:215], v4\ns_waitcnt lgkmcnt(6)\n" \
"ds_write_b128 v5, v[36:39] offset:256\nv_cvt_pk_bf16_f32 v44, v40, v44\nv_cvt_pk_bf16_f32 v45, v41, v45\nv_cvt_pk_bf16_f32 v46, v42, v46\nv_cvt_pk_bf16_f32 v47, v43, v47\nv_smfmac_f32_16x16x64_bf16 v[48:51], v[84:87], a[240:247], v4\nv_smfmac_f32_16x16x64_bf16 v[52:55], v[80:83], a[216:223], v4\ns_waitcnt lgkmcnt(5)\n" \
"v_smfmac_f32_16x16x64_bf16 v[52:55], v[84:87], v[72:79], v4\nds_read_b128 v[72:75], v1 offset:16384\nds_read_b128 v[76:79], v1 offset:17408\nv_smfmac_f32_16x16x64_bf16 v[56:59], v[80:83], a[224:231], v4\nv_smfmac_f32_16x16x64_bf16 v[56:59], v[84:87], a[248:255], v4\nv_smfmac_f32_16x16x64_bf16 v[60:63], v[80:83], a[232:239], v4\nds_read_b128 v[80:83], v0 offset:28672\nv_smfmac_f32_16x16x64_bf16 v[60:63], v[84:87], v[96:103], v4\n" \
"s_waitcnt lgkmcnt(7)\nds_read_b128 v[84:87], v0 offset:29696\nv_smfmac_f32_16x16x64_bf16 v[48:51], v[88:91], v[104:111], v4\ns_waitcnt lgkmcnt(7)\nds_write_b128 v5, v[44:47] offset:768\nv_smfmac_f32_16x16x64_bf16 v[48:51], v[92:95], v[128:135], v4\nv_smfmac_f32_16x16x64_bf16 v[52:55], v[88:91], v[112:119], v4\nv_smfmac_f32_16x16x64_bf16 v[52:55], v[92:95], v[136:143], v4\n" \
"s_waitcnt lgkmcnt(6)\nv_smfmac_f32_16x16x64_bf16 v[56:59], v[88:91], v[64:71], v4\nds_read_b128 v[64:67], v1 offset:18432\nds_read_b128 v[68:71], v1 offset:19456\nv_smfmac_f32_16x16x64_bf16 v[56:59], v[92:95], v[144:151], v4\ns_waitcnt lgkmcnt(2)\ns_barrier\nv_smfmac_f32_16x16x64_bf16 v[60:63], v[88:91], v[120:127], v4\n" \
"ds_read_b128 v[88:91], v0 offset:30720\nds_read_b128 v[16:19], v7 offset:0\nv_smfmac_f32_16x16x64_bf16 v[60:63], v[92:95], v[72:79], v4\nds_read_b128 v[92:95], v0 offset:31744\nds_read_b128 v[72:75], v1 offset:20480\nds_read_b128 v[76:79], v1 offset:21504\nv_smfmac_f32_16x16x64_bf16 v[48:51], v[80:83], v[152:159], v4\nds_read_b128 v[20:23], v7 offset:2048\n" \
"v_smfmac_f32_16x16x64_bf16 v[48:51], v[84:87], v[184:191], v4\nv_smfmac_f32_16x16x64_bf16 v[52:55], v[80:83], v[160:167], v4\ns_waitcnt lgkmcnt(6)\nds_read_b128 v[24:27], v7 offset:4096\nv_smfmac_f32_16x16x64_bf16 v[52:55], v[84:87], v[64:71], v4\nds_read_b128 v[64:67], v1 offset:22528\nds_read_b128 v[68:71], v1 offset:23552\nv_smfmac_f32_16x16x64_bf16 v[56:59], v[80:83], v[168:175], v4\n" \
"ds_read_b128 v[28:31], v7 offset:6144\nv_smfmac_f32_16x16x64_bf16 v[56:59], v[84:87], v[192:199], v4\nv_smfmac_f32_16x16x64_bf16 v[60:63], v[80:83], v[176:183], v4\nds_read_b128 v[80:83], v0 offset:16384\nds_read_b128 v[32:35], v7 offset:8192\nv_smfmac_f32_16x16x64_bf16 v[60:63], v[84:87], v[200:207], v4\ns_waitcnt lgkmcnt(11)\nds_read_b128 v[84:87], v0 offset:17408\n" \
"v_smfmac_f32_16x16x64_bf16 v[48:51], v[88:91], v[208:215], v4\ns_waitcnt lgkmcnt(10)\nds_read_b128 v[36:39], v7 offset:10240\nv_smfmac_f32_16x16x64_bf16 v[48:51], v[92:95], v[232:239], v4\nv_smfmac_f32_16x16x64_bf16 v[52:55], v[88:91], v[216:223], v4\nds_read_b128 v[40:43], v7 offset:12288\nv_smfmac_f32_16x16x64_bf16 v[52:55], v[92:95], v[240:247], v4\ns_waitcnt lgkmcnt(10)\n" \
"v_smfmac_f32_16x16x64_bf16 v[56:59], v[88:91], v[72:79], v4\nds_read_b128 v[72:75], v1 offset:0\nds_read_b128 v[76:79], v1 offset:1024\nv_smfmac_f32_16x16x64_bf16 v[56:59], v[92:95], v[248:255], v4\nv_smfmac_f32_16x16x64_bf16 v[60:63], v[88:91], v[224:231], v4\ns_waitcnt lgkmcnt(8)\nds_read_b128 v[88:91], v0 offset:18432\nds_read_b128 v[44:47], v7 offset:14336\n" \
"v_smfmac_f32_16x16x64_bf16 v[60:63], v[92:95], v[64:71], v4\ns_waitcnt lgkmcnt(8)\nds_read_b128 v[92:95], v0 offset:19456\nds_read_b128 v[64:67], v1 offset:2048\nds_read_b128 v[68:71], v1 offset:3072\nv_smfmac_f32_16x16x64_bf16 v[48:51], v[80:83], a[0:7], v4\ns_waitcnt lgkmcnt(9)\nv_smfmac_f32_16x16x64_bf16 v[48:51], v[84:87], a[32:39], v4\n" \
"v_smfmac_f32_16x16x64_bf16 v[52:55], v[80:83], a[8:15], v4\ns_waitcnt lgkmcnt(5)\nv_smfmac_f32_16x16x64_bf16 v[52:55], v[84:87], v[72:79], v4\nds_read_b128 v[72:75], v1 offset:4096\nds_read_b128 v[76:79], v1 offset:5120\nv_smfmac_f32_16x16x64_bf16 v[56:59], v[80:83], a[16:23], v4\nv_smfmac_f32_16x16x64_bf16 v[56:59], v[84:87], a[40:47], v4\nv_smfmac_f32_16x16x64_bf16 v[60:63], v[80:83], a[24:31], v4\n" \
"v_smfmac_f32_16x16x64_bf16 v[60:63], v[84:87], a[48:55], v4\ns_waitcnt lgkmcnt(6)\nv_smfmac_f32_16x16x64_bf16 v[48:51], v[88:91], a[56:63], v4\ns_waitcnt lgkmcnt(4)\nv_smfmac_f32_16x16x64_bf16 v[48:51], v[92:95], a[80:87], v4\nv_smfmac_f32_16x16x64_bf16 v[52:55], v[88:91], a[64:71], v4\nv_smfmac_f32_16x16x64_bf16 v[52:55], v[92:95], a[88:95], v4\ns_waitcnt lgkmcnt(2)\n" \
"v_smfmac_f32_16x16x64_bf16 v[56:59], v[88:91], v[64:71], v4\nv_add_f32 v48, v48, v50\nv_smfmac_f32_16x16x64_bf16 v[56:59], v[92:95], a[96:103], v4\nv_add_f32 v52, v52, v54\nv_add_f32 v49, v49, v51\nv_smfmac_f32_16x16x64_bf16 v[60:63], v[88:91], a[72:79], v4\ns_waitcnt lgkmcnt(0)\nv_add_f32 v53, v53, v55\n" \
"v_cvt_pk_bf16_f32 v50, v48, v52\nv_smfmac_f32_16x16x64_bf16 v[60:63], v[92:95], v[72:79], v4\nv_cvt_pk_bf16_f32 v51, v49, v53\nds_write2_b32 v6, v50, v51 offset0:0 offset1:4\nv_add_f32 v56, v56, v58\nv_add_f32 v57, v57, v59\ns_nop 3\nv_add_f32 v60, v60, v62\n" \
"v_add_f32 v61, v61, v63\nv_cvt_pk_bf16_f32 v62, v56, v60\nv_cvt_pk_bf16_f32 v63, v57, v61\nds_write2_b32 v6, v62, v63 offset0:36 offset1:40\nv_mov_b32 v61, s54\nv_mfma_f32_16x16x32_bf16 v[52:55], v[32:35], v[16:19], v[12:15]\nv_mfma_f32_16x16x32_bf16 v[52:55], v[36:39], v[20:23], v[52:55]\nv_mfma_f32_16x16x32_bf16 v[52:55], v[40:43], v[24:27], v[52:55]\n" \
"v_mfma_f32_16x16x32_bf16 v[52:55], v[44:47], v[28:31], v[52:55]\ns_waitcnt lgkmcnt(0)\ns_barrier\ns_nop 10\nds_read_b128 v[32:35], v9 offset:0\nds_read_b128 v[36:39], v9 offset:576\nds_read_b128 v[40:43], v9 offset:1152\nds_read_b128 v[44:47], v9 offset:1728\n" \
"ds_read_b32 v60, v0 offset:53760\nds_read_b128 v[80:83], v2 offset:0\nds_read_b128 v[84:87], v2 offset:2048\ns_waitcnt lgkmcnt(6)\nv_mfma_f32_16x16x32_bf16 v[48:51], v[32:35], v[16:19], 0\ns_waitcnt lgkmcnt(5)\nv_mfma_f32_16x16x32_bf16 v[48:51], v[36:39], v[20:23], v[48:51]\ns_waitcnt lgkmcnt(4)\n" \
"v_mfma_f32_16x16x32_bf16 v[48:51], v[40:43], v[24:27], v[48:51]\ns_waitcnt lgkmcnt(3)\nv_mfma_f32_16x16x32_bf16 v[48:51], v[44:47], v[28:31], v[48:51]\ns_waitcnt lgkmcnt(1)\nv_mfma_f32_16x16x32_bf16 v[16:19], a[0:3], v[80:83], 0\ns_waitcnt lgkmcnt(0)\nds_read_b128 v[88:91], v2 offset:256\nv_max3_f32 v62, v52, v53, v54\n" \
"v_max_f32 v62, v62, v55\nv_mfma_f32_16x16x32_bf16 v[16:19], a[4:7], v[84:87], v[16:19]\nds_read_b128 v[92:95], v2 offset:2304\nv_max3_f32 v63, v62, v48, v49\nv_max3_f32 v62, v62, v48, v49\nv_mfma_f32_16x16x32_bf16 v[20:23], a[8:11], v[80:83], 0\ns_nop 0\nv_permlane16_swap_b32 v62, v63\n" \
"v_max_f32 v50, v62, v63\nv_mfma_f32_16x16x32_bf16 v[20:23], a[12:15], v[84:87], v[20:23]\nv_max_f32 v62, v62, v63\nv_mfma_f32_16x16x32_bf16 v[24:27], a[16:19], v[80:83], 0\nds_read_b128 v[72:75], v1 offset:0\nv_permlane32_swap_b32 v62, v50\nv_mfma_f32_16x16x32_bf16 v[24:27], a[20:23], v[84:87], v[24:27]\nds_read_b128 v[76:79], v1 offset:1024\n" \
"v_max_f32 v62, v62, v50\nv_mul_f32 v62, s59, v62\nv_mfma_f32_16x16x32_bf16 v[28:31], a[24:27], v[80:83], 0\nv_fma_f32 v52, v52, s58, v62\nv_fma_f32 v53, v53, s58, v62\nv_fma_f32 v54, v54, s58, v62\nv_mfma_f32_16x16x32_bf16 v[28:31], a[28:31], v[84:87], v[28:31]\ns_waitcnt lgkmcnt(3)\n" \
"v_exp_f32 v52, v52\nv_fma_f32 v55, v55, s58, v62\nv_mfma_f32_16x16x32_bf16 v[32:35], a[0:3], v[88:91], 0\ns_waitcnt lgkmcnt(2)\nds_read_b128 v[80:83], v2 offset:4096\nv_exp_f32 v53, v53\nv_mfma_f32_16x16x32_bf16 v[32:35], a[4:7], v[92:95], v[32:35]\nds_read_b128 v[84:87], v2 offset:6144\n" \
"v_exp_f32 v54, v54\nv_mfma_f32_16x16x32_bf16 v[36:39], a[8:11], v[88:91], 0\nv_add_f32 v63, v52, v53\nv_fma_f32 v48, v48, s58, v62\nv_add_f32 v63, v63, v54\nv_mfma_f32_16x16x32_bf16 v[36:39], a[12:15], v[92:95], v[36:39]\nv_exp_f32 v55, v55\nv_fma_f32 v49, v49, s58, v62\n" \
"v_mfma_f32_16x16x32_bf16 v[40:43], a[16:19], v[88:91], 0\nv_exp_f32 v48, v48\nv_add_f32 v63, v63, v55\nv_mfma_f32_16x16x32_bf16 v[40:43], a[20:23], v[92:95], v[40:43]\nv_exp_f32 v49, v49\nv_add_f32 v63, v63, v48\nv_mfma_f32_16x16x32_bf16 v[44:47], a[24:27], v[88:91], 0\nv_add_f32 v62, v63, v49\n" \
"v_add_f32 v50, v63, v49\nv_mfma_f32_16x16x32_bf16 v[44:47], a[28:31], v[92:95], v[44:47]\ns_waitcnt lgkmcnt(1)\nv_permlane16_swap_b32 v62, v50\nv_add_f32 v63, v62, v50\nv_mfma_f32_16x16x32_bf16 v[16:19], a[32:35], v[80:83], v[16:19]\ns_waitcnt lgkmcnt(0)\nds_read_b128 v[88:91], v2 offset:4352\n" \
"v_add_f32 v50, v62, v50\nv_mfma_f32_16x16x32_bf16 v[16:19], a[36:39], v[84:87], v[16:19]\nds_read_b128 v[92:95], v2 offset:6400\nv_permlane32_swap_b32 v63, v50\nv_mfma_f32_16x16x32_bf16 v[20:23], v[72:75], v[80:83], v[20:23]\nv_add_f32 v63, v63, v50\nv_rcp_f32 v63, v63\nv_mfma_f32_16x16x32_bf16 v[20:23], v[76:79], v[84:87], v[20:23]\n" \
"v_mul_f32 v52, v52, v63\nv_mul_f32 v53, v53, v63\nv_mul_f32 v54, v54, v63\nv_mfma_f32_16x16x32_bf16 v[24:27], a[40:43], v[80:83], v[24:27]\nds_read_b128 v[64:67], v1 offset:2048\nv_mul_f32 v55, v55, v63\nv_mul_f32 v48, v48, v63\nv_mfma_f32_16x16x32_bf16 v[24:27], a[44:47], v[84:87], v[24:27]\n" \
"ds_read_b128 v[68:71], v1 offset:3072\nv_mul_f32 v49, v49, v63\nv_cvt_pk_bf16_f32 v52, v52, v53\nv_mfma_f32_16x16x32_bf16 v[28:31], a[48:51], v[80:83], v[28:31]\nv_cvt_pk_bf16_f32 v53, v54, v55\nv_cvt_pk_bf16_f32 v48, v48, v49\nv_bfi_b32 v52, s61, v52, v61\nv_mfma_f32_16x16x32_bf16 v[28:31], a[52:55], v[84:87], v[28:31]\n" \
"s_waitcnt lgkmcnt(3)\nv_bfi_b32 v53, s61, v53, v61\nv_bfi_b32 v48, s61, v48, v61\nv_mfma_f32_16x16x32_bf16 v[32:35], a[32:35], v[88:91], v[32:35]\ns_waitcnt lgkmcnt(2)\nds_read_b128 v[80:83], v2 offset:8192\nv_mfma_f32_16x16x32_bf16 v[32:35], a[36:39], v[92:95], v[32:35]\nds_read_b128 v[84:87], v2 offset:10240\n" \
"v_mfma_f32_16x16x32_bf16 v[36:39], v[72:75], v[88:91], v[36:39]\nbuffer_store_dwordx2 v[52:53], v10, s[36:39], s55 offen sc1\nv_mov_b32 v49, v48\nv_mfma_f32_16x16x32_bf16 v[36:39], v[76:79], v[92:95], v[36:39]\ns_nop 0\nv_permlane16_swap_b32 v48, v49\nv_mov_b32 v50, v48\nv_mfma_f32_16x16x32_bf16 v[40:43], a[40:43], v[88:91], v[40:43]\n" \
"v_mov_b32 v51, v49\nv_permlane32_swap_b32 v48, v50\nv_mfma_f32_16x16x32_bf16 v[40:43], a[44:47], v[92:95], v[40:43]\nv_permlane32_swap_b32 v49, v51\nv_mfma_f32_16x16x32_bf16 v[44:47], a[48:51], v[88:91], v[44:47]\nbuffer_store_dwordx4 v[48:51], v11, s[36:39], s55 offen sc1\nv_cmp_ne_u32 vcc, 0, v60\nv_mfma_f32_16x16x32_bf16 v[44:47], a[52:55], v[92:95], v[44:47]\n" \
"s_waitcnt lgkmcnt(1)\nv_mfma_f32_16x16x32_bf16 v[16:19], a[56:59], v[80:83], v[16:19]\ns_waitcnt lgkmcnt(0)\nds_read_b128 v[88:91], v2 offset:8448\nv_mfma_f32_16x16x32_bf16 v[16:19], a[60:63], v[84:87], v[16:19]\nds_read_b128 v[92:95], v2 offset:10496\nv_mfma_f32_16x16x32_bf16 v[20:23], a[64:67], v[80:83], v[20:23]\nv_mfma_f32_16x16x32_bf16 v[20:23], a[68:71], v[84:87], v[20:23]\n" \
"v_mfma_f32_16x16x32_bf16 v[24:27], v[64:67], v[80:83], v[24:27]\nds_read_b128 v[72:75], v1 offset:4096\nv_mfma_f32_16x16x32_bf16 v[24:27], v[68:71], v[84:87], v[24:27]\nds_read_b128 v[76:79], v1 offset:5120\nv_mfma_f32_16x16x32_bf16 v[28:31], a[72:75], v[80:83], v[28:31]\nv_mfma_f32_16x16x32_bf16 v[28:31], a[76:79], v[84:87], v[28:31]\ns_waitcnt lgkmcnt(3)\nv_mfma_f32_16x16x32_bf16 v[32:35], a[56:59], v[88:91], v[32:35]\n" \
"s_waitcnt lgkmcnt(2)\nds_read_b128 v[80:83], v2 offset:12288\nv_mfma_f32_16x16x32_bf16 v[32:35], a[60:63], v[92:95], v[32:35]\nds_read_b128 v[84:87], v2 offset:14336\nv_mfma_f32_16x16x32_bf16 v[36:39], a[64:67], v[88:91], v[36:39]\nv_mfma_f32_16x16x32_bf16 v[36:39], a[68:71], v[92:95], v[36:39]\nv_mfma_f32_16x16x32_bf16 v[40:43], v[64:67], v[88:91], v[40:43]\nv_mfma_f32_16x16x32_bf16 v[40:43], v[68:71], v[92:95], v[40:43]\n" \
"v_mfma_f32_16x16x32_bf16 v[44:47], a[72:75], v[88:91], v[44:47]\nv_mfma_f32_16x16x32_bf16 v[44:47], a[76:79], v[92:95], v[44:47]\ns_cbranch_vccnz .Lqk_pj%=\ns_cmp_lt_i32 s40, 63\ns_waitcnt lgkmcnt(0)\ns_cbranch_scc1 .Lqk_pwait%=\ns_branch .Lqk_end%=\n.Lqk_re1%=:\n" \
"v_max_u32 v17, v80, v82\nv_cmp_eq_u32 vcc, -1, v17\ns_cbranch_vccz .Lqk_sk2009%=\ns_mov_b64 exec, vcc\nbuffer_load_dwordx4 v[80:83], v3, s[36:39], s42 offen offset:0 sc1\ns_mov_b64 exec, -1\n.Lqk_sk2009%=:\nv_max_u32 v17, v84, v86\n" \
"v_cmp_eq_u32 vcc, -1, v17\ns_cbranch_vccz .Lqk_sk2016%=\ns_mov_b64 exec, vcc\nbuffer_load_dwordx4 v[84:87], v3, s[36:39], s42 offen offset:1024 sc1\ns_mov_b64 exec, -1\n.Lqk_sk2016%=:\nv_max_u32 v17, v88, v90\nv_cmp_eq_u32 vcc, -1, v17\n" \
"s_cbranch_vccz .Lqk_sk2023%=\ns_mov_b64 exec, vcc\nbuffer_load_dwordx4 v[88:91], v3, s[36:39], s42 offen offset:2048 sc1\ns_mov_b64 exec, -1\n.Lqk_sk2023%=:\nv_max_u32 v17, v92, v94\nv_cmp_eq_u32 vcc, -1, v17\ns_cbranch_vccz .Lqk_sk2030%=\n" \
"s_mov_b64 exec, vcc\nbuffer_load_dwordx4 v[92:95], v3, s[36:39], s42 offen offset:3072 sc1\ns_mov_b64 exec, -1\n.Lqk_sk2030%=:\nv_mov_b64 v[48:49], 0\nv_mov_b64 v[50:51], 0\nv_mov_b64 v[52:53], 0\nv_mov_b64 v[54:55], 0\n" \
"v_mov_b64 v[56:57], 0\nv_mov_b64 v[58:59], 0\nv_mov_b64 v[60:61], 0\nv_mov_b64 v[62:63], 0\ns_add_u32 s43, s43, 1\ns_cmp_lt_u32 s43, 262144\ns_cbranch_scc1 .Lqk_poll1%=\ns_branch .Lqk_abort%=\n" \
".p2align 6\n.Lqk_c2%=:\ns_waitcnt lgkmcnt(3)\nv_mfma_f32_16x16x32_bf16 v[32:35], v[232:235], v[56:59], v[32:35]\ns_waitcnt lgkmcnt(2)\nv_mov_b64 v[48:49], 0\nv_mov_b64 v[50:51], 0\nv_mfma_f32_16x16x32_bf16 v[32:35], v[236:239], v[60:63], v[32:35]\n" \
"ds_read_b128 v[64:67], v1 offset:12288\nv_mfma_f32_16x16x32_bf16 v[36:39], v[240:243], v[56:59], v[36:39]\nds_read_b128 v[68:71], v1 offset:13312\nv_mfma_f32_16x16x32_bf16 v[36:39], v[244:247], v[60:63], v[36:39]\ns_bfe_i32 s54, s40, 0x10001\nv_mov_b64 v[52:53], 0\nv_mov_b64 v[54:55], 0\nv_mfma_f32_16x16x32_bf16 v[40:43], v[248:251], v[56:59], v[40:43]\n" \
"v_mfma_f32_16x16x32_bf16 v[40:43], v[252:255], v[60:63], v[40:43]\nv_cvt_pk_bf16_f32 v36, v32, v36\nv_cvt_pk_bf16_f32 v37, v33, v37\nv_mfma_f32_16x16x32_bf16 v[44:47], v[72:75], v[56:59], v[44:47]\nv_cvt_pk_bf16_f32 v38, v34, v38\nv_cvt_pk_bf16_f32 v39, v35, v39\nv_mfma_f32_16x16x32_bf16 v[44:47], v[76:79], v[60:63], v[44:47]\nds_read_b128 v[72:75], v1 offset:14336\n" \
"ds_read_b128 v[76:79], v1 offset:15360\nv_mov_b64 v[56:57], 0\nv_mov_b64 v[58:59], 0\nv_mov_b64 v[60:61], 0\nv_mov_b64 v[62:63], 0\n.Lqk_poll2%=:\ns_waitcnt vmcnt(0)\nv_smfmac_f32_16x16x64_bf16 v[48:51], v[80:83], a[208:215], v4\n" \
"ds_write_b128 v0, v[80:83] offset:24576\nv_max3_u32 v16, v80, v82, v84\nv_smfmac_f32_16x16x64_bf16 v[48:51], v[84:87], a[240:247], v4\nv_max3_u32 v16, v16, v86, v88\nv_smfmac_f32_16x16x64_bf16 v[52:55], v[80:83], a[216:223], v4\ns_waitcnt lgkmcnt(3)\nds_write_b128 v0, v[84:87] offset:25600\nv_max3_u32 v16, v16, v90, v92\n" \
"v_max_u32 v16, v16, v94\nv_smfmac_f32_16x16x64_bf16 v[52:55], v[84:87], v[64:71], v4\nv_cmp_eq_u32 vcc, -1, v16\nv_smfmac_f32_16x16x64_bf16 v[56:59], v[80:83], a[224:231], v4\nds_write_b128 v0, v[88:91] offset:26624\ns_cbranch_vccnz .Lqk_re2%=\nv_smfmac_f32_16x16x64_bf16 v[56:59], v[84:87], a[248:255], v4\nv_smfmac_f32_16x16x64_bf16 v[60:63], v[80:83], a[232:239], v4\n" \
"ds_write_b128 v0, v[92:95] offset:27648\nv_smfmac_f32_16x16x64_bf16 v[60:63], v[84:87], v[96:103], v4\nv_smfmac_f32_16x16x64_bf16 v[48:51], v[88:91], v[104:111], v4\nv_smfmac_f32_16x16x64_bf16 v[48:51], v[92:95], v[128:135], v4\ns_waitcnt lgkmcnt(0)\n.Lqk_jn2%=:\ns_barrier\nds_read_b128 v[80:83], v0 offset:28672\n" \
"ds_read_b128 v[64:67], v1 offset:16384\nds_read_b128 v[68:71], v1 offset:17408\nv_smfmac_f32_16x16x64_bf16 v[52:55], v[88:91], v[112:119], v4\nv_smfmac_f32_16x16x64_bf16 v[52:55], v[92:95], v[136:143], v4\nds_read_b128 v[84:87], v0 offset:29696\nv_smfmac_f32_16x16x64_bf16 v[56:59], v[88:91], v[72:79], v4\nds_read_b128 v[72:75], v1 offset:18432\nds_read_b128 v[76:79], v1 offset:19456\n" \
"v_smfmac_f32_16x16x64_bf16 v[56:59], v[92:95], v[144:151], v4\nv_smfmac_f32_16x16x64_bf16 v[60:63], v[88:91], v[120:127], v4\ns_waitcnt lgkmcnt(3)\nds_read_b128 v[88:91], v0 offset:30720\nv_smfmac_f32_16x16x64_bf16 v[60:63], v[92:95], v[64:71], v4\nds_read_b128 v[92:95], v0 offset:31744\nds_read_b128 v[64:67], v1 offset:20480\nds_read_b128 v[68:71], v1 offset:21504\n" \
"v_smfmac_f32_16x16x64_bf16 v[48:51], v[80:83], v[152:159], v4\ns_waitcnt lgkmcnt(6)\nds_write_b128 v5, v[36:39] offset:256\nv_cvt_pk_bf16_f32 v44, v40, v44\nv_cvt_pk_bf16_f32 v45, v41, v45\nv_cvt_pk_bf16_f32 v46, v42, v46\nv_cvt_pk_bf16_f32 v47, v43, v47\nv_smfmac_f32_16x16x64_bf16 v[48:51], v[84:87], v[184:191], v4\n" \
"v_smfmac_f32_16x16x64_bf16 v[52:55], v[80:83], v[160:167], v4\ns_waitcnt lgkmcnt(5)\nv_smfmac_f32_16x16x64_bf16 v[52:55], v[84:87], v[72:79], v4\nds_read_b128 v[72:75], v1 offset:22528\nds_read_b128 v[76:79], v1 offset:23552\nv_smfmac_f32_16x16x64_bf16 v[56:59], v[80:83], v[168:175], v4\nv_smfmac_f32_16x16x64_bf16 v[56:59], v[84:87], v[192:199], v4\nv_smfmac_f32_16x16x64_bf16 v[60:63], v[80:83], v[176:183], v4\n" \
"ds_read_b128 v[80:83], v0 offset:16384\nv_smfmac_f32_16x16x64_bf16 v[60:63], v[84:87], v[200:207], v4\ns_waitcnt lgkmcnt(7)\nds_read_b128 v[84:87], v0 offset:17408\nv_smfmac_f32_16x16x64_bf16 v[48:51], v[88:91], v[208:215], v4\ns_waitcnt lgkmcnt(7)\nds_write_b128 v5, v[44:47] offset:768\nv_smfmac_f32_16x16x64_bf16 v[48:51], v[92:95], v[232:239], v4\n" \
"v_smfmac_f32_16x16x64_bf16 v[52:55], v[88:91], v[216:223], v4\nv_smfmac_f32_16x16x64_bf16 v[52:55], v[92:95], v[240:247], v4\ns_waitcnt lgkmcnt(6)\nv_smfmac_f32_16x16x64_bf16 v[56:59], v[88:91], v[64:71], v4\nds_read_b128 v[64:67], v1 offset:0\nds_read_b128 v[68:71], v1 offset:1024\nv_smfmac_f32_16x16x64_bf16 v[56:59], v[92:95], v[248:255], v4\ns_waitcnt lgkmcnt(2)\n" \
"s_barrier\nv_smfmac_f32_16x16x64_bf16 v[60:63], v[88:91], v[224:231], v4\nds_read_b128 v[88:91], v0 offset:18432\nds_read_b128 v[16:19], v7 offset:256\nv_smfmac_f32_16x16x64_bf16 v[60:63], v[92:95], v[72:79], v4\nds_read_b128 v[92:95], v0 offset:19456\nds_read_b128 v[72:75], v1 offset:2048\nds_read_b128 v[76:79], v1 offset:3072\n" \
"v_smfmac_f32_16x16x64_bf16 v[48:51], v[80:83], a[0:7], v4\nds_read_b128 v[20:23], v7 offset:2304\nv_smfmac_f32_16x16x64_bf16 v[48:51], v[84:87], a[32:39], v4\nv_smfmac_f32_16x16x64_bf16 v[52:55], v[80:83], a[8:15], v4\ns_waitcnt lgkmcnt(6)\nds_read_b128 v[24:27], v7 offset:4352\nv_smfmac_f32_16x16x64_bf16 v[52:55], v[84:87], v[64:71], v4\nds_read_b128 v[64:67], v1 offset:4096\n" \
"ds_read_b128 v[68:71], v1 offset:5120\nv_smfmac_f32_16x16x64_bf16 v[56:59], v[80:83], a[16:23], v4\nds_read_b128 v[28:31], v7 offset:6400\nv_smfmac_f32_16x16x64_bf16 v[56:59], v[84:87], a[40:47], v4\nv_smfmac_f32_16x16x64_bf16 v[60:63], v[80:83], a[24:31], v4\nds_read_b128 v[80:83], v0 offset:20480\nds_read_b128 v[32:35], v7 offset:8192\nv_smfmac_f32_16x16x64_bf16 v[60:63], v[84:87], a[48:55], v4\n" \
"s_waitcnt lgkmcnt(11)\nds_read_b128 v[84:87], v0 offset:21504\nv_smfmac_f32_16x16x64_bf16 v[48:51], v[88:91], a[56:63], v4\ns_waitcnt lgkmcnt(10)\nds_read_b128 v[36:39], v7 offset:10240\nv_smfmac_f32_16x16x64_bf16 v[48:51], v[92:95], a[80:87], v4\nv_smfmac_f32_16x16x64_bf16 v[52:55], v[88:91], a[64:71], v4\nds_read_b128 v[40:43], v7 offset:12288\n" \
"v_smfmac_f32_16x16x64_bf16 v[52:55], v[92:95], a[88:95], v4\ns_waitcnt lgkmcnt(10)\nv_smfmac_f32_16x16x64_bf16 v[56:59], v[88:91], v[72:79], v4\nds_read_b128 v[72:75], v1 offset:6144\nds_read_b128 v[76:79], v1 offset:7168\nv_smfmac_f32_16x16x64_bf16 v[56:59], v[92:95], a[96:103], v4\nv_smfmac_f32_16x16x64_bf16 v[60:63], v[88:91], a[72:79], v4\ns_waitcnt lgkmcnt(8)\n" \
"ds_read_b128 v[88:91], v0 offset:22528\nds_read_b128 v[44:47], v7 offset:14336\nv_smfmac_f32_16x16x64_bf16 v[60:63], v[92:95], v[64:71], v4\ns_waitcnt lgkmcnt(8)\nds_read_b128 v[92:95], v0 offset:23552\nds_read_b128 v[64:67], v1 offset:8192\nds_read_b128 v[68:71], v1 offset:9216\nv_smfmac_f32_16x16x64_bf16 v[48:51], v[80:83], a[104:111], v4\n" \
"s_waitcnt lgkmcnt(9)\nv_smfmac_f32_16x16x64_bf16 v[48:51], v[84:87], a[136:143], v4\nv_smfmac_f32_16x16x64_bf16 v[52:55], v[80:83], a[112:119], v4\ns_waitcnt lgkmcnt(5)\nv_smfmac_f32_16x16x64_bf16 v[52:55], v[84:87], v[72:79], v4\nds_read_b128 v[72:75], v1 offset:10240\nds_read_b128 v[76:79], v1 offset:11264\nv_smfmac_f32_16x16x64_bf16 v[56:59], v[80:83], a[120:127], v4\n" \
"v_smfmac_f32_16x16x64_bf16 v[56:59], v[84:87], a[144:151], v4\nv_smfmac_f32_16x16x64_bf16 v[60:63], v[80:83], a[128:135], v4\nv_smfmac_f32_16x16x64_bf16 v[60:63], v[84:87], a[152:159], v4\ns_waitcnt lgkmcnt(6)\nv_smfmac_f32_16x16x64_bf16 v[48:51], v[88:91], a[160:167], v4\ns_waitcnt lgkmcnt(4)\nds_read_b128 v[80:83], v7 offset:8448\nv_smfmac_f32_16x16x64_bf16 v[48:51], v[92:95], a[184:191], v4\n" \
"ds_read_b128 v[84:87], v7 offset:10496\nv_smfmac_f32_16x16x64_bf16 v[52:55], v[88:91], a[168:175], v4\nv_smfmac_f32_16x16x64_bf16 v[52:55], v[92:95], a[192:199], v4\ns_waitcnt lgkmcnt(4)\nv_smfmac_f32_16x16x64_bf16 v[56:59], v[88:91], v[64:71], v4\nds_read_b128 v[64:67], v7 offset:12544\nv_add_f32 v48, v48, v50\nv_smfmac_f32_16x16x64_bf16 v[56:59], v[92:95], a[200:207], v4\n" \
"ds_read_b128 v[68:71], v7 offset:14592\nv_add_f32 v52, v52, v54\nv_add_f32 v49, v49, v51\nv_smfmac_f32_16x16x64_bf16 v[60:63], v[88:91], a[176:183], v4\ns_waitcnt lgkmcnt(4)\nv_add_f32 v53, v53, v55\nv_cvt_pk_bf16_f32 v50, v48, v52\nv_smfmac_f32_16x16x64_bf16 v[60:63], v[92:95], v[72:79], v4\n" \
"v_cvt_pk_bf16_f32 v51, v49, v53\nds_write2_b32 v6, v50, v51 offset0:0 offset1:4\nv_add_f32 v56, v56, v58\nv_add_f32 v57, v57, v59\ns_nop 3\nv_add_f32 v60, v60, v62\nv_add_f32 v61, v61, v63\nv_cvt_pk_bf16_f32 v62, v56, v60\n" \
"v_cvt_pk_bf16_f32 v63, v57, v61\nds_write2_b32 v6, v62, v63 offset0:36 offset1:40\nv_mov_b32 v61, s54\nv_mfma_f32_16x16x32_bf16 v[52:55], v[32:35], v[16:19], 0\nv_mfma_f32_16x16x32_bf16 v[52:55], v[36:39], v[20:23], v[52:55]\nv_mfma_f32_16x16x32_bf16 v[52:55], v[40:43], v[24:27], v[52:55]\nv_mfma_f32_16x16x32_bf16 v[52:55], v[44:47], v[28:31], v[52:55]\ns_waitcnt lgkmcnt(0)\n" \
"s_barrier\nv_mfma_f32_16x16x32_bf16 v[56:59], v[80:83], v[16:19], v[12:15]\nv_mfma_f32_16x16x32_bf16 v[56:59], v[84:87], v[20:23], v[56:59]\nds_read_b128 v[32:35], v9 offset:0\nds_read_b128 v[36:39], v9 offset:576\nds_read_b128 v[40:43], v9 offset:1152\nds_read_b128 v[44:47], v9 offset:1728\nds_read_b32 v60, v0 offset:53760\n" \
"v_mfma_f32_16x16x32_bf16 v[56:59], v[64:67], v[24:27], v[56:59]\nv_mfma_f32_16x16x32_bf16 v[56:59], v[68:71], v[28:31], v[56:59]\ns_waitcnt lgkmcnt(4)\nds_read_b128 v[80:83], v2 offset:0\nds_read_b128 v[84:87], v2 offset:2048\nv_mfma_f32_16x16x32_bf16 v[48:51], v[32:35], v[16:19], 0\ns_waitcnt lgkmcnt(5)\nv_mfma_f32_16x16x32_bf16 v[48:51], v[36:39], v[20:23], v[48:51]\n" \
"s_waitcnt lgkmcnt(4)\nv_mfma_f32_16x16x32_bf16 v[48:51], v[40:43], v[24:27], v[48:51]\ns_waitcnt lgkmcnt(3)\nv_mfma_f32_16x16x32_bf16 v[48:51], v[44:47], v[28:31], v[48:51]\ns_waitcnt lgkmcnt(1)\nv_mfma_f32_16x16x32_bf16 v[16:19], a[0:3], v[80:83], 0\ns_waitcnt lgkmcnt(0)\nds_read_b128 v[88:91], v2 offset:256\n" \
"v_max3_f32 v62, v52, v53, v54\nv_max3_f32 v62, v62, v56, v57\nv_mfma_f32_16x16x32_bf16 v[16:19], a[4:7], v[84:87], v[16:19]\nds_read_b128 v[92:95], v2 offset:2304\nv_max3_f32 v62, v62, v58, v59\nv_max_f32 v62, v62, v55\nv_mfma_f32_16x16x32_bf16 v[20:23], a[8:11], v[80:83], 0\nv_max3_f32 v63, v62, v48, v49\n" \
"v_max3_f32 v62, v62, v48, v49\nv_mfma_f32_16x16x32_bf16 v[20:23], a[12:15], v[84:87], v[20:23]\ns_nop 0\nv_permlane16_swap_b32 v62, v63\nv_max_f32 v50, v62, v63\nv_mfma_f32_16x16x32_bf16 v[24:27], a[16:19], v[80:83], 0\nds_read_b128 v[72:75], v1 offset:0\nv_max_f32 v62, v62, v63\n" \
"v_mfma_f32_16x16x32_bf16 v[24:27], a[20:23], v[84:87], v[24:27]\nds_read_b128 v[76:79], v1 offset:1024\nv_permlane32_swap_b32 v62, v50\nv_mfma_f32_16x16x32_bf16 v[28:31], a[24:27], v[80:83], 0\nv_max_f32 v62, v62, v50\nv_mul_f32 v62, s59, v62\nv_fma_f32 v52, v52, s58, v62\nv_mfma_f32_16x16x32_bf16 v[28:31], a[28:31], v[84:87], v[28:31]\n" \
"s_waitcnt lgkmcnt(3)\nv_fma_f32 v53, v53, s58, v62\nv_exp_f32 v52, v52\nv_mfma_f32_16x16x32_bf16 v[32:35], a[0:3], v[88:91], 0\ns_waitcnt lgkmcnt(2)\nds_read_b128 v[80:83], v2 offset:4096\nv_fma_f32 v54, v54, s58, v62\nv_fma_f32 v55, v55, s58, v62\n" \
"v_mfma_f32_16x16x32_bf16 v[32:35], a[4:7], v[92:95], v[32:35]\nds_read_b128 v[84:87], v2 offset:6144\nv_exp_f32 v53, v53\nv_mfma_f32_16x16x32_bf16 v[36:39], a[8:11], v[88:91], 0\nv_exp_f32 v54, v54\nv_add_f32 v63, v52, v53\nv_mfma_f32_16x16x32_bf16 v[36:39], a[12:15], v[92:95], v[36:39]\nv_fma_f32 v56, v56, s58, v62\n" \
"v_exp_f32 v55, v55\nv_mfma_f32_16x16x32_bf16 v[40:43], a[16:19], v[88:91], 0\nv_add_f32 v63, v63, v54\nv_fma_f32 v57, v57, s58, v62\nv_add_f32 v63, v63, v55\nv_mfma_f32_16x16x32_bf16 v[40:43], a[20:23], v[92:95], v[40:43]\nv_exp_f32 v56, v56\nv_fma_f32 v58, v58, s58, v62\n" \
"v_mfma_f32_16x16x32_bf16 v[44:47], a[24:27], v[88:91], 0\nv_exp_f32 v57, v57\nv_add_f32 v63, v63, v56\nv_mfma_f32_16x16x32_bf16 v[44:47], a[28:31], v[92:95], v[44:47]\ns_waitcnt lgkmcnt(1)\nv_fma_f32 v59, v59, s58, v62\nv_exp_f32 v58, v58\nv_mfma_f32_16x16x32_bf16 v[16:19], a[32:35], v[80:83], v[16:19]\n" \
"s_waitcnt lgkmcnt(0)\nds_read_b128 v[88:91], v2 offset:4352\nv_add_f32 v63, v63, v57\nv_fma_f32 v48, v48, s58, v62\nv_mfma_f32_16x16x32_bf16 v[16:19], a[36:39], v[84:87], v[16:19]\nds_read_b128 v[92:95], v2 offset:6400\nv_exp_f32 v59, v59\nv_mfma_f32_16x16x32_bf16 v[20:23], v[72:75], v[80:83], v[20:23]\n" \
"v_add_f32 v63, v63, v58\nv_fma_f32 v49, v49, s58, v62\nv_add_f32 v63, v63, v59\nv_mfma_f32_16x16x32_bf16 v[20:23], v[76:79], v[84:87], v[20:23]\nv_exp_f32 v48, v48\nv_mfma_f32_16x16x32_bf16 v[24:27], a[40:43], v[80:83], v[24:27]\nds_read_b128 v[64:67], v1 offset:2048\nv_exp_f32 v49, v49\n" \
"v_mfma_f32_16x16x32_bf16 v[24:27], a[44:47], v[84:87], v[24:27]\nds_read_b128 v[68:71], v1 offset:3072\nv_add_f32 v63, v63, v48\nv_add_f32 v62, v63, v49\nv_mfma_f32_16x16x32_bf16 v[28:31], a[48:51], v[80:83], v[28:31]\nv_add_f32 v50, v63, v49\nv_mfma_f32_16x16x32_bf16 v[28:31], a[52:55], v[84:87], v[28:31]\ns_waitcnt lgkmcnt(3)\n" \
"v_permlane16_swap_b32 v62, v50\nv_add_f32 v63, v62, v50\nv_mfma_f32_16x16x32_bf16 v[32:35], a[32:35], v[88:91], v[32:35]\ns_waitcnt lgkmcnt(2)\nds_read_b128 v[80:83], v2 offset:8192\nv_add_f32 v50, v62, v50\nv_mfma_f32_16x16x32_bf16 v[32:35], a[36:39], v[92:95], v[32:35]\nds_read_b128 v[84:87], v2 offset:10240\n" \
"v_permlane32_swap_b32 v63, v50\nv_mfma_f32_16x16x32_bf16 v[36:39], v[72:75], v[88:91], v[36:39]\nv_add_f32 v63, v63, v50\nv_rcp_f32 v63, v63\nv_mfma_f32_16x16x32_bf16 v[36:39], v[76:79], v[92:95], v[36:39]\nv_mul_f32 v52, v52, v63\nv_mul_f32 v53, v53, v63\nv_mul_f32 v54, v54, v63\n" \
"v_mfma_f32_16x16x32_bf16 v[40:43], a[40:43], v[88:91], v[40:43]\nv_mul_f32 v55, v55, v63\nv_mul_f32 v56, v56, v63\nv_mul_f32 v57, v57, v63\nv_mfma_f32_16x16x32_bf16 v[40:43], a[44:47], v[92:95], v[40:43]\nv_mul_f32 v58, v58, v63\nv_mul_f32 v59, v59, v63\nv_mul_f32 v48, v48, v63\n" \
"v_mfma_f32_16x16x32_bf16 v[44:47], a[48:51], v[88:91], v[44:47]\nv_mul_f32 v49, v49, v63\nv_cvt_pk_bf16_f32 v52, v52, v53\nv_cvt_pk_bf16_f32 v53, v54, v55\nv_mfma_f32_16x16x32_bf16 v[44:47], a[52:55], v[92:95], v[44:47]\ns_waitcnt lgkmcnt(1)\nv_cvt_pk_bf16_f32 v48, v48, v49\nv_cvt_pk_bf16_f32 v54, v56, v57\n" \
"v_cvt_pk_bf16_f32 v55, v58, v59\nv_mfma_f32_16x16x32_bf16 v[16:19], a[56:59], v[80:83], v[16:19]\ns_waitcnt lgkmcnt(0)\nds_read_b128 v[88:91], v2 offset:8448\nv_bfi_b32 v52, s61, v52, v61\nv_bfi_b32 v53, s61, v53, v61\nv_mfma_f32_16x16x32_bf16 v[16:19], a[60:63], v[84:87], v[16:19]\nds_read_b128 v[92:95], v2 offset:10496\n" \
"v_bfi_b32 v54, s61, v54, v61\nv_bfi_b32 v55, s61, v55, v61\nv_mfma_f32_16x16x32_bf16 v[20:23], a[64:67], v[80:83], v[20:23]\nv_bfi_b32 v48, s61, v48, v61\nv_mfma_f32_16x16x32_bf16 v[20:23], a[68:71], v[84:87], v[20:23]\nbuffer_store_dwordx4 v[52:55], v10, s[36:39], s55 offen sc1\nv_mov_b32 v49, v48\nv_mfma_f32_16x16x32_bf16 v[24:27], v[64:67], v[80:83], v[24:27]\n" \
"ds_read_b128 v[72:75], v1 offset:4096\nv_permlane16_swap_b32 v48, v49\nv_mfma_f32_16x16x32_bf16 v[24:27], v[68:71], v[84:87], v[24:27]\nds_read_b128 v[76:79], v1 offset:5120\nv_mov_b32 v50, v48\nv_mov_b32 v51, v49\nv_mfma_f32_16x16x32_bf16 v[28:31], a[72:75], v[80:83], v[28:31]\nv_permlane32_swap_b32 v48, v50\n" \
"v_mfma_f32_16x16x32_bf16 v[28:31], a[76:79], v[84:87], v[28:31]\ns_waitcnt lgkmcnt(3)\nv_permlane32_swap_b32 v49, v51\nv_mfma_f32_16x16x32_bf16 v[32:35], a[56:59], v[88:91], v[32:35]\ns_waitcnt lgkmcnt(2)\nds_read_b128 v[80:83], v2 offset:12288\nv_mfma_f32_16x16x32_bf16 v[32:35], a[60:63], v[92:95], v[32:35]\nds_read_b128 v[84:87], v2 offset:14336\n" \
"v_mfma_f32_16x16x32_bf16 v[36:39], a[64:67], v[88:91], v[36:39]\nbuffer_store_dwordx4 v[48:51], v11, s[36:39], s55 offen sc1\nv_cmp_ne_u32 vcc, 0, v60\nv_mfma_f32_16x16x32_bf16 v[36:39], a[68:71], v[92:95], v[36:39]\nv_mfma_f32_16x16x32_bf16 v[40:43], v[64:67], v[88:91], v[40:43]\nv_mfma_f32_16x16x32_bf16 v[40:43], v[68:71], v[92:95], v[40:43]\nv_mfma_f32_16x16x32_bf16 v[44:47], a[72:75], v[88:91], v[44:47]\nv_mfma_f32_16x16x32_bf16 v[44:47], a[76:79], v[92:95], v[44:47]\n" \
"s_cbranch_vccnz .Lqk_pj%=\ns_cmp_lt_i32 s40, 63\ns_waitcnt lgkmcnt(0)\ns_cbranch_scc1 .Lqk_pwait%=\ns_branch .Lqk_end%=\n.Lqk_re2%=:\nv_max_u32 v17, v80, v82\nv_cmp_eq_u32 vcc, -1, v17\n" \
"s_cbranch_vccz .Lqk_sk2428%=\ns_mov_b64 exec, vcc\nbuffer_load_dwordx4 v[80:83], v3, s[36:39], s42 offen offset:0 sc1\ns_mov_b64 exec, -1\n.Lqk_sk2428%=:\nv_max_u32 v17, v84, v86\nv_cmp_eq_u32 vcc, -1, v17\ns_cbranch_vccz .Lqk_sk2435%=\n" \
"s_mov_b64 exec, vcc\nbuffer_load_dwordx4 v[84:87], v3, s[36:39], s42 offen offset:1024 sc1\ns_mov_b64 exec, -1\n.Lqk_sk2435%=:\nv_max_u32 v17, v88, v90\nv_cmp_eq_u32 vcc, -1, v17\ns_cbranch_vccz .Lqk_sk2442%=\ns_mov_b64 exec, vcc\n" \
"buffer_load_dwordx4 v[88:91], v3, s[36:39], s42 offen offset:2048 sc1\ns_mov_b64 exec, -1\n.Lqk_sk2442%=:\nv_max_u32 v17, v92, v94\nv_cmp_eq_u32 vcc, -1, v17\ns_cbranch_vccz .Lqk_sk2449%=\ns_mov_b64 exec, vcc\nbuffer_load_dwordx4 v[92:95], v3, s[36:39], s42 offen offset:3072 sc1\n" \
"s_mov_b64 exec, -1\n.Lqk_sk2449%=:\nv_mov_b64 v[48:49], 0\nv_mov_b64 v[50:51], 0\nv_mov_b64 v[52:53], 0\nv_mov_b64 v[54:55], 0\nv_mov_b64 v[56:57], 0\nv_mov_b64 v[58:59], 0\n" \
"v_mov_b64 v[60:61], 0\nv_mov_b64 v[62:63], 0\ns_add_u32 s43, s43, 1\ns_cmp_lt_u32 s43, 262144\ns_cbranch_scc1 .Lqk_poll2%=\ns_branch .Lqk_abort%=\n.p2align 6\n.Lqk_c3%=:\n" \
"s_waitcnt lgkmcnt(3)\nv_mfma_f32_16x16x32_bf16 v[32:35], v[232:235], v[56:59], v[32:35]\ns_waitcnt lgkmcnt(2)\nv_mov_b64 v[48:49], 0\nv_mov_b64 v[50:51], 0\nv_mfma_f32_16x16x32_bf16 v[32:35], v[236:239], v[60:63], v[32:35]\ns_add_u32 s44, s40, 1\ns_lshl_b32 s44, s44, 17\n" \
"ds_read_b128 v[64:67], v1 offset:18432\nv_mfma_f32_16x16x32_bf16 v[36:39], v[240:243], v[56:59], v[36:39]\ns_add_u32 s44, s44, %[xn]\ns_lshr_b32 s45, s44, 8\nds_read_b128 v[68:71], v1 offset:19456\nv_mfma_f32_16x16x32_bf16 v[36:39], v[244:247], v[60:63], v[36:39]\ns_and_b32 s45, s45, 0x2000\ns_xor_b32 s41, s44, s45\n" \
"v_mov_b64 v[52:53], 0\nv_mov_b64 v[54:55], 0\nv_mfma_f32_16x16x32_bf16 v[40:43], v[248:251], v[56:59], v[40:43]\nbuffer_load_dword v16, v12, s[36:39], s56 offen sc1\nv_mfma_f32_16x16x32_bf16 v[40:43], v[252:255], v[60:63], v[40:43]\nv_cvt_pk_bf16_f32 v36, v32, v36\nv_cvt_pk_bf16_f32 v37, v33, v37\nv_mfma_f32_16x16x32_bf16 v[44:47], v[72:75], v[56:59], v[44:47]\n" \
"v_cvt_pk_bf16_f32 v38, v34, v38\nv_cvt_pk_bf16_f32 v39, v35, v39\nv_mfma_f32_16x16x32_bf16 v[44:47], v[76:79], v[60:63], v[44:47]\nds_read_b128 v[72:75], v1 offset:20480\nds_read_b128 v[76:79], v1 offset:21504\nv_mov_b64 v[56:57], 0\nv_mov_b64 v[58:59], 0\nv_mov_b64 v[60:61], 0\n" \
"v_mov_b64 v[62:63], 0\n.Lqk_poll3%=:\ns_waitcnt vmcnt(1)\nv_smfmac_f32_16x16x64_bf16 v[48:51], v[80:83], v[152:159], v4\nds_write_b128 v0, v[80:83] offset:28672\nv_max3_u32 v17, v80, v82, v84\nv_smfmac_f32_16x16x64_bf16 v[48:51], v[84:87], v[184:191], v4\nv_max3_u32 v17, v17, v86, v88\n" \
"v_smfmac_f32_16x16x64_bf16 v[52:55], v[80:83], v[160:167], v4\ns_waitcnt lgkmcnt(3)\nds_write_b128 v0, v[84:87] offset:29696\nv_max3_u32 v17, v17, v90, v92\nv_max_u32 v17, v17, v94\nv_smfmac_f32_16x16x64_bf16 v[52:55], v[84:87], v[64:71], v4\nv_cmp_eq_u32 vcc, -1, v17\nv_smfmac_f32_16x16x64_bf16 v[56:59], v[80:83], v[168:175], v4\n" \
"ds_write_b128 v0, v[88:91] offset:30720\ns_cbranch_vccnz .Lqk_re3%=\nv_smfmac_f32_16x16x64_bf16 v[56:59], v[84:87], v[192:199], v4\nv_smfmac_f32_16x16x64_bf16 v[60:63], v[80:83], v[176:183], v4\nds_write_b128 v0, v[92:95] offset:31744\nv_smfmac_f32_16x16x64_bf16 v[60:63], v[84:87], v[200:207], v4\nv_smfmac_f32_16x16x64_bf16 v[48:51], v[88:91], v[208:215], v4\nv_smfmac_f32_16x16x64_bf16 v[48:51], v[92:95], v[232:239], v4\n" \
"s_waitcnt lgkmcnt(0)\n.Lqk_jn3%=:\ns_barrier\nds_read_b128 v[80:83], v0 offset:16384\nds_read_b128 v[64:67], v1 offset:22528\nds_read_b128 v[68:71], v1 offset:23552\nv_smfmac_f32_16x16x64_bf16 v[52:55], v[88:91], v[216:223], v4\ns_waitcnt vmcnt(0)\n" \
"v_cmp_ge_i32 vcc, s40, v16\nv_smfmac_f32_16x16x64_bf16 v[52:55], v[92:95], v[240:247], v4\ns_cmp_eq_u64 vcc, 0\ns_cselect_b32 s61, s41, -2147483648\ns_cselect_b32 s44, 1, 0\nds_read_b128 v[84:87], v0 offset:17408\nv_smfmac_f32_16x16x64_bf16 v[56:59], v[88:91], v[72:79], v4\ns_add_u32 m0, %[l0], 0\n" \
"s_add_u32 s60, s61, 0\nds_read_b128 v[72:75], v1 offset:0\nds_read_b128 v[76:79], v1 offset:1024\nbuffer_load_dwordx4 v0, s[36:39], s60 offen offset:0 sc1 lds\nv_smfmac_f32_16x16x64_bf16 v[56:59], v[92:95], v[248:255], v4\nv_mov_b32 v16, s44\nv_smfmac_f32_16x16x64_bf16 v[60:63], v[88:91], v[224:231], v4\ns_waitcnt lgkmcnt(3)\n" \
"ds_read_b128 v[88:91], v0 offset:18432\nbuffer_load_dwordx4 v0, s[36:39], s60 offen offset:1024 sc1 lds\nv_smfmac_f32_16x16x64_bf16 v[60:63], v[92:95], v[64:71], v4\nds_read_b128 v[92:95], v0 offset:19456\nds_read_b128 v[64:67], v1 offset:2048\nds_read_b128 v[68:71], v1 offset:3072\nds_write_b32 v0, v16 offset:53760\nv_smfmac_f32_16x16x64_bf16 v[48:51], v[80:83], a[0:7], v4\n" \
"s_waitcnt lgkmcnt(7)\nds_write_b128 v5, v[36:39] offset:256\nbuffer_load_dwordx4 v0, s[36:39], s60 offen offset:2048 sc1 lds\nv_cvt_pk_bf16_f32 v44, v40, v44\nv_cvt_pk_bf16_f32 v45, v41, v45\nv_cvt_pk_bf16_f32 v46, v42, v46\nv_cvt_pk_bf16_f32 v47, v43, v47\nv_smfmac_f32_16x16x64_bf16 v[48:51], v[84:87], a[32:39], v4\n" \
"v_smfmac_f32_16x16x64_bf16 v[52:55], v[80:83], a[8:15], v4\ns_waitcnt lgkmcnt(6)\nbuffer_load_dwordx4 v0, s[36:39], s60 offen offset:3072 sc1 lds\nv_smfmac_f32_16x16x64_bf16 v[52:55], v[84:87], v[72:79], v4\ns_add_u32 m0, %[l0], 4096\nds_read_b128 v[72:75], v1 offset:4096\nds_read_b128 v[76:79], v1 offset:5120\nv_smfmac_f32_16x16x64_bf16 v[56:59], v[80:83], a[16:23], v4\n" \
"s_add_u32 s60, s61, 4096\nbuffer_load_dwordx4 v0, s[36:39], s60 offen offset:0 sc1 lds\nv_smfmac_f32_16x16x64_bf16 v[56:59], v[84:87], a[40:47], v4\nv_smfmac_f32_16x16x64_bf16 v[60:63], v[80:83], a[24:31], v4\nds_read_b128 v[80:83], v0 offset:20480\nbuffer_load_dwordx4 v0, s[36:39], s60 offen offset:1024 sc1 lds\nv_smfmac_f32_16x16x64_bf16 v[60:63], v[84:87], a[48:55], v4\ns_waitcnt lgkmcnt(8)\n" \
"ds_read_b128 v[84:87], v0 offset:21504\nv_smfmac_f32_16x16x64_bf16 v[48:51], v[88:91], a[56:63], v4\ns_waitcnt lgkmcnt(8)\nds_write_b128 v5, v[44:47] offset:768\nbuffer_load_dwordx4 v0, s[36:39], s60 offen offset:2048 sc1 lds\nv_smfmac_f32_16x16x64_bf16 v[48:51], v[92:95], a[80:87], v4\nv_smfmac_f32_16x16x64_bf16 v[52:55], v[88:91], a[64:71], v4\nbuffer_load_dwordx4 v0, s[36:39], s60 offen offset:3072 sc1 lds\n" \
"v_smfmac_f32_16x16x64_bf16 v[52:55], v[92:95], a[88:95], v4\ns_waitcnt lgkmcnt(7)\ns_add_u32 m0, %[l0], 8192\nv_smfmac_f32_16x16x64_bf16 v[56:59], v[88:91], v[64:71], v4\ns_add_u32 s60, s61, 16384\nds_read_b128 v[64:67], v1 offset:6144\nds_read_b128 v[68:71], v1 offset:7168\nbuffer_load_dwordx4 v0, s[36:39], s60 offen offset:0 sc1 lds\n" \
"v_smfmac_f32_16x16x64_bf16 v[56:59], v[92:95], a[96:103], v4\ns_waitcnt lgkmcnt(2)\ns_barrier\nv_smfmac_f32_16x16x64_bf16 v[60:63], v[88:91], a[72:79], v4\nds_read_b128 v[88:91], v0 offset:22528\nbuffer_load_dwordx4 v0, s[36:39], s60 offen offset:1024 sc1 lds\nv_smfmac_f32_16x16x64_bf16 v[60:63], v[92:95], v[72:79], v4\nds_read_b128 v[92:95], v0 offset:23552\n" \
"ds_read_b128 v[72:75], v1 offset:8192\nds_read_b128 v[76:79], v1 offset:9216\nv_smfmac_f32_16x16x64_bf16 v[48:51], v[80:83], a[104:111], v4\nbuffer_load_dwordx4 v0, s[36:39], s60 offen offset:2048 sc1 lds\nv_smfmac_f32_16x16x64_bf16 v[48:51], v[84:87], a[136:143], v4\nv_smfmac_f32_16x16x64_bf16 v[52:55], v[80:83], a[112:119], v4\ns_waitcnt lgkmcnt(4)\nbuffer_load_dwordx4 v0, s[36:39], s60 offen offset:3072 sc1 lds\n" \
"v_smfmac_f32_16x16x64_bf16 v[52:55], v[84:87], v[64:71], v4\ns_add_u32 m0, %[l0], 12288\nds_read_b128 v[64:67], v1 offset:10240\nds_read_b128 v[68:71], v1 offset:11264\nv_smfmac_f32_16x16x64_bf16 v[56:59], v[80:83], a[120:127], v4\ns_add_u32 s60, s61, 20480\nbuffer_load_dwordx4 v0, s[36:39], s60 offen offset:0 sc1 lds\nv_smfmac_f32_16x16x64_bf16 v[56:59], v[84:87], a[144:151], v4\n" \
"v_smfmac_f32_16x16x64_bf16 v[60:63], v[80:83], a[128:135], v4\nds_read_b128 v[80:83], v0 offset:24576\nbuffer_load_dwordx4 v0, s[36:39], s60 offen offset:1024 sc1 lds\nv_smfmac_f32_16x16x64_bf16 v[60:63], v[84:87], a[152:159], v4\ns_waitcnt lgkmcnt(6)\nds_read_b128 v[84:87], v0 offset:25600\nv_smfmac_f32_16x16x64_bf16 v[48:51], v[88:91], a[160:167], v4\ns_waitcnt lgkmcnt(6)\n" \
"buffer_load_dwordx4 v0, s[36:39], s60 offen offset:2048 sc1 lds\nv_smfmac_f32_16x16x64_bf16 v[48:51], v[92:95], a[184:191], v4\nv_smfmac_f32_16x16x64_bf16 v[52:55], v[88:91], a[168:175], v4\nbuffer_load_dwordx4 v0, s[36:39], s60 offen offset:3072 sc1 lds\nv_smfmac_f32_16x16x64_bf16 v[52:55], v[92:95], a[192:199], v4\ns_waitcnt lgkmcnt(4)\nv_smfmac_f32_16x16x64_bf16 v[56:59], v[88:91], v[72:79], v4\nds_read_b128 v[72:75], v1 offset:12288\n" \
"ds_read_b128 v[76:79], v1 offset:13312\nv_smfmac_f32_16x16x64_bf16 v[56:59], v[92:95], a[200:207], v4\nv_smfmac_f32_16x16x64_bf16 v[60:63], v[88:91], a[176:183], v4\ns_waitcnt lgkmcnt(4)\nds_read_b128 v[88:91], v0 offset:26624\nv_smfmac_f32_16x16x64_bf16 v[60:63], v[92:95], v[64:71], v4\ns_waitcnt lgkmcnt(4)\nds_read_b128 v[92:95], v0 offset:27648\n" \
"ds_read_b128 v[64:67], v1 offset:14336\nds_read_b128 v[68:71], v1 offset:15360\nv_smfmac_f32_16x16x64_bf16 v[48:51], v[80:83], a[208:215], v4\ns_waitcnt lgkmcnt(6)\nv_smfmac_f32_16x16x64_bf16 v[48:51], v[84:87], a[240:247], v4\nv_smfmac_f32_16x16x64_bf16 v[52:55], v[80:83], a[216:223], v4\ns_waitcnt lgkmcnt(4)\nv_smfmac_f32_16x16x64_bf16 v[52:55], v[84:87], v[72:79], v4\n" \
"ds_read_b128 v[72:75], v1 offset:16384\nds_read_b128 v[76:79], v1 offset:17408\nv_smfmac_f32_16x16x64_bf16 v[56:59], v[80:83], a[224:231], v4\nv_smfmac_f32_16x16x64_bf16 v[56:59], v[84:87], a[248:255], v4\nv_smfmac_f32_16x16x64_bf16 v[60:63], v[80:83], a[232:239], v4\nv_smfmac_f32_16x16x64_bf16 v[60:63], v[84:87], v[96:103], v4\ns_waitcnt lgkmcnt(5)\nv_smfmac_f32_16x16x64_bf16 v[48:51], v[88:91], v[104:111], v4\n" \
"s_waitcnt lgkmcnt(4)\nv_smfmac_f32_16x16x64_bf16 v[48:51], v[92:95], v[128:135], v4\nv_smfmac_f32_16x16x64_bf16 v[52:55], v[88:91], v[112:119], v4\nv_smfmac_f32_16x16x64_bf16 v[52:55], v[92:95], v[136:143], v4\ns_waitcnt lgkmcnt(2)\nv_smfmac_f32_16x16x64_bf16 v[56:59], v[88:91], v[64:71], v4\nv_add_f32 v48, v48, v50\nv_smfmac_f32_16x16x64_bf16 v[56:59], v[92:95], v[144:151], v4\n" \
"v_add_f32 v52, v52, v54\nv_add_f32 v49, v49, v51\nv_smfmac_f32_16x16x64_bf16 v[60:63], v[88:91], v[120:127], v4\ns_waitcnt lgkmcnt(0)\nv_add_f32 v53, v53, v55\nv_cvt_pk_bf16_f32 v50, v48, v52\nv_smfmac_f32_16x16x64_bf16 v[60:63], v[92:95], v[72:79], v4\nv_cvt_pk_bf16_f32 v51, v49, v53\n" \
"ds_write2_b32 v6, v50, v51 offset0:0 offset1:4\nv_add_f32 v56, v56, v58\nv_add_f32 v57, v57, v59\ns_nop 3\nv_add_f32 v60, v60, v62\nv_add_f32 v61, v61, v63\nv_cvt_pk_bf16_f32 v62, v56, v60\nv_cvt_pk_bf16_f32 v63, v57, v61\n" \
"ds_write2_b32 v6, v62, v63 offset0:36 offset1:40\ns_waitcnt vmcnt(0)\ns_waitcnt lgkmcnt(0)\ns_barrier\ns_cbranch_vccnz .Lqk_slow%=\ns_add_u32 m0, %[l0], 16384\ns_add_u32 s60, s41, 32768\nbuffer_load_dwordx4 v0, s[36:39], s60 offen offset:0 sc1 lds\n" \
"buffer_load_dwordx4 v0, s[36:39], s60 offen offset:1024 sc1 lds\nbuffer_load_dwordx4 v0, s[36:39], s60 offen offset:2048 sc1 lds\nbuffer_load_dwordx4 v0, s[36:39], s60 offen offset:3072 sc1 lds\ns_add_u32 m0, %[l0], 20480\ns_add_u32 s60, s41, 36864\nbuffer_load_dwordx4 v0, s[36:39], s60 offen offset:0 sc1 lds\nbuffer_load_dwordx4 v0, s[36:39], s60 offen offset:1024 sc1 lds\nds_read_b128 v[80:83], v2 offset:0\n" \
"ds_read_b128 v[84:87], v2 offset:2048\nbuffer_load_dwordx4 v0, s[36:39], s60 offen offset:2048 sc1 lds\nbuffer_load_dwordx4 v0, s[36:39], s60 offen offset:3072 sc1 lds\ns_add_u32 m0, %[l0], 24576\ns_add_u32 s60, s41, 49152\nbuffer_load_dwordx4 v0, s[36:39], s60 offen offset:0 sc1 lds\nbuffer_load_dwordx4 v0, s[36:39], s60 offen offset:1024 sc1 lds\nbuffer_load_dwordx4 v0, s[36:39], s60 offen offset:2048 sc1 lds\n" \
"buffer_load_dwordx4 v0, s[36:39], s60 offen offset:3072 sc1 lds\ns_add_u32 m0, %[l0], 28672\ns_add_u32 s60, s41, 53248\nbuffer_load_dwordx4 v0, s[36:39], s60 offen offset:0 sc1 lds\nbuffer_load_dwordx4 v0, s[36:39], s60 offen offset:1024 sc1 lds\nbuffer_load_dwordx4 v0, s[36:39], s60 offen offset:2048 sc1 lds\nbuffer_load_dwordx4 v0, s[36:39], s60 offen offset:3072 sc1 lds\ns_waitcnt lgkmcnt(1)\n" \
"v_mfma_f32_16x16x32_bf16 v[16:19], a[0:3], v[80:83], 0\ns_waitcnt lgkmcnt(0)\nds_read_b128 v[88:91], v2 offset:256\nv_mfma_f32_16x16x32_bf16 v[16:19], a[4:7], v[84:87], v[16:19]\nds_read_b128 v[92:95], v2 offset:2304\nv_mfma_f32_16x16x32_bf16 v[20:23], a[8:11], v[80:83], 0\nv_mfma_f32_16x16x32_bf16 v[20:23], a[12:15], v[84:87], v[20:23]\nv_mfma_f32_16x16x32_bf16 v[24:27], a[16:19], v[80:83], 0\n" \
"ds_read_b128 v[72:75], v1 offset:0\nv_mfma_f32_16x16x32_bf16 v[24:27], a[20:23], v[84:87], v[24:27]\nds_read_b128 v[76:79], v1 offset:1024\nv_mfma_f32_16x16x32_bf16 v[28:31], a[24:27], v[80:83], 0\nv_mfma_f32_16x16x32_bf16 v[28:31], a[28:31], v[84:87], v[28:31]\ns_waitcnt lgkmcnt(3)\nv_mfma_f32_16x16x32_bf16 v[32:35], a[0:3], v[88:91], 0\ns_waitcnt lgkmcnt(2)\n" \
"ds_read_b128 v[80:83], v2 offset:4096\nv_mfma_f32_16x16x32_bf16 v[32:35], a[4:7], v[92:95], v[32:35]\nds_read_b128 v[84:87], v2 offset:6144\nv_mfma_f32_16x16x32_bf16 v[36:39], a[8:11], v[88:91], 0\nv_mfma_f32_16x16x32_bf16 v[36:39], a[12:15], v[92:95], v[36:39]\nv_mfma_f32_16x16x32_bf16 v[40:43], a[16:19], v[88:91], 0\nv_mfma_f32_16x16x32_bf16 v[40:43], a[20:23], v[92:95], v[40:43]\nv_mfma_f32_16x16x32_bf16 v[44:47], a[24:27], v[88:91], 0\n" \
"v_mfma_f32_16x16x32_bf16 v[44:47], a[28:31], v[92:95], v[44:47]\ns_waitcnt lgkmcnt(1)\nv_mfma_f32_16x16x32_bf16 v[16:19], a[32:35], v[80:83], v[16:19]\ns_waitcnt lgkmcnt(0)\nds_read_b128 v[88:91], v2 offset:4352\nv_mfma_f32_16x16x32_bf16 v[16:19], a[36:39], v[84:87], v[16:19]\nds_read_b128 v[92:95], v2 offset:6400\nv_mfma_f32_16x16x32_bf16 v[20:23], v[72:75], v[80:83], v[20:23]\n" \
"v_mfma_f32_16x16x32_bf16 v[20:23], v[76:79], v[84:87], v[20:23]\nv_mfma_f32_16x16x32_bf16 v[24:27], a[40:43], v[80:83], v[24:27]\nds_read_b128 v[64:67], v1 offset:2048\nv_mfma_f32_16x16x32_bf16 v[24:27], a[44:47], v[84:87], v[24:27]\nds_read_b128 v[68:71], v1 offset:3072\nv_mfma_f32_16x16x32_bf16 v[28:31], a[48:51], v[80:83], v[28:31]\nv_mfma_f32_16x16x32_bf16 v[28:31], a[52:55], v[84:87], v[28:31]\ns_waitcnt lgkmcnt(3)\n" \
"v_mfma_f32_16x16x32_bf16 v[32:35], a[32:35], v[88:91], v[32:35]\ns_waitcnt lgkmcnt(2)\nds_read_b128 v[80:83], v2 offset:8192\nv_mfma_f32_16x16x32_bf16 v[32:35], a[36:39], v[92:95], v[32:35]\nds_read_b128 v[84:87], v2 offset:10240\nv_mfma_f32_16x16x32_bf16 v[36:39], v[72:75], v[88:91], v[36:39]\nv_mfma_f32_16x16x32_bf16 v[36:39], v[76:79], v[92:95], v[36:39]\nv_mfma_f32_16x16x32_bf16 v[40:43], a[40:43], v[88:91], v[40:43]\n" \
"v_mfma_f32_16x16x32_bf16 v[40:43], a[44:47], v[92:95], v[40:43]\nv_mfma_f32_16x16x32_bf16 v[44:47], a[48:51], v[88:91], v[44:47]\nv_mfma_f32_16x16x32_bf16 v[44:47], a[52:55], v[92:95], v[44:47]\ns_waitcnt lgkmcnt(1)\nv_mfma_f32_16x16x32_bf16 v[16:19], a[56:59], v[80:83], v[16:19]\ns_waitcnt lgkmcnt(0)\nds_read_b128 v[88:91], v2 offset:8448\nv_mfma_f32_16x16x32_bf16 v[16:19], a[60:63], v[84:87], v[16:19]\n" \
"ds_read_b128 v[92:95], v2 offset:10496\nv_mfma_f32_16x16x32_bf16 v[20:23], a[64:67], v[80:83], v[20:23]\nv_mfma_f32_16x16x32_bf16 v[20:23], a[68:71], v[84:87], v[20:23]\nv_mfma_f32_16x16x32_bf16 v[24:27], v[64:67], v[80:83], v[24:27]\nds_read_b128 v[72:75], v1 offset:4096\nv_mfma_f32_16x16x32_bf16 v[24:27], v[68:71], v[84:87], v[24:27]\nds_read_b128 v[76:79], v1 offset:5120\nv_mfma_f32_16x16x32_bf16 v[28:31], a[72:75], v[80:83], v[28:31]\n" \
"v_mfma_f32_16x16x32_bf16 v[28:31], a[76:79], v[84:87], v[28:31]\ns_waitcnt lgkmcnt(3)\nv_mfma_f32_16x16x32_bf16 v[32:35], a[56:59], v[88:91], v[32:35]\ns_waitcnt lgkmcnt(2)\nds_read_b128 v[80:83], v2 offset:12288\nv_mfma_f32_16x16x32_bf16 v[32:35], a[60:63], v[92:95], v[32:35]\nds_read_b128 v[84:87], v2 offset:14336\nv_mfma_f32_16x16x32_bf16 v[36:39], a[64:67], v[88:91], v[36:39]\n" \
"v_mfma_f32_16x16x32_bf16 v[36:39], a[68:71], v[92:95], v[36:39]\nv_mfma_f32_16x16x32_bf16 v[40:43], v[64:67], v[88:91], v[40:43]\nv_mfma_f32_16x16x32_bf16 v[40:43], v[68:71], v[92:95], v[40:43]\nv_mfma_f32_16x16x32_bf16 v[44:47], a[72:75], v[88:91], v[44:47]\nv_mfma_f32_16x16x32_bf16 v[44:47], a[76:79], v[92:95], v[44:47]\ns_waitcnt vmcnt(0)\ns_branch .Lqk_pj%=\n.Lqk_slow%=:\n" \
"s_cmp_ge_i32 s40, 63\ns_cbranch_scc1 .Lqk_end%=\n.Lqk_fetch0%=:\nbuffer_load_dword v16, v12, s[36:39], s56 offen sc1\ns_waitcnt vmcnt(0)\nv_cmp_ge_i32 vcc, s40, v16\ns_cbranch_vccz .Lqk_fok%=\ns_add_u32 s43, s43, 1\n" \
"s_cmp_lt_u32 s43, 262144\ns_cbranch_scc1 .Lqk_fetch0%=\ns_branch .Lqk_abort%=\n.Lqk_fok%=:\ns_add_u32 s44, s40, 1\ns_lshl_b32 s44, s44, 17\ns_add_u32 s44, s44, %[xn]\ns_lshr_b32 s45, s44, 8\n" \
"s_and_b32 s45, s45, 0x2000\ns_xor_b32 s41, s44, s45\ns_add_u32 m0, %[l0], 0\ns_add_u32 s60, s41, 0\nbuffer_load_dwordx4 v0, s[36:39], s60 offen offset:0 sc1 lds\nbuffer_load_dwordx4 v0, s[36:39], s60 offen offset:1024 sc1 lds\nbuffer_load_dwordx4 v0, s[36:39], s60 offen offset:2048 sc1 lds\nbuffer_load_dwordx4 v0, s[36:39], s60 offen offset:3072 sc1 lds\n" \
"s_add_u32 m0, %[l0], 4096\ns_add_u32 s60, s41, 4096\nbuffer_load_dwordx4 v0, s[36:39], s60 offen offset:0 sc1 lds\nbuffer_load_dwordx4 v0, s[36:39], s60 offen offset:1024 sc1 lds\nbuffer_load_dwordx4 v0, s[36:39], s60 offen offset:2048 sc1 lds\nbuffer_load_dwordx4 v0, s[36:39], s60 offen offset:3072 sc1 lds\ns_add_u32 m0, %[l0], 8192\ns_add_u32 s60, s41, 16384\n" \
"buffer_load_dwordx4 v0, s[36:39], s60 offen offset:0 sc1 lds\nbuffer_load_dwordx4 v0, s[36:39], s60 offen offset:1024 sc1 lds\nbuffer_load_dwordx4 v0, s[36:39], s60 offen offset:2048 sc1 lds\nbuffer_load_dwordx4 v0, s[36:39], s60 offen offset:3072 sc1 lds\ns_add_u32 m0, %[l0], 12288\ns_add_u32 s60, s41, 20480\nbuffer_load_dwordx4 v0, s[36:39], s60 offen offset:0 sc1 lds\nbuffer_load_dwordx4 v0, s[36:39], s60 offen offset:1024 sc1 lds\n" \
"buffer_load_dwordx4 v0, s[36:39], s60 offen offset:2048 sc1 lds\nbuffer_load_dwordx4 v0, s[36:39], s60 offen offset:3072 sc1 lds\ns_add_u32 m0, %[l0], 16384\ns_add_u32 s60, s41, 32768\nbuffer_load_dwordx4 v0, s[36:39], s60 offen offset:0 sc1 lds\nbuffer_load_dwordx4 v0, s[36:39], s60 offen offset:1024 sc1 lds\nbuffer_load_dwordx4 v0, s[36:39], s60 offen offset:2048 sc1 lds\nbuffer_load_dwordx4 v0, s[36:39], s60 offen offset:3072 sc1 lds\n" \
"s_add_u32 m0, %[l0], 20480\ns_add_u32 s60, s41, 36864\nbuffer_load_dwordx4 v0, s[36:39], s60 offen offset:0 sc1 lds\nbuffer_load_dwordx4 v0, s[36:39], s60 offen offset:1024 sc1 lds\nbuffer_load_dwordx4 v0, s[36:39], s60 offen offset:2048 sc1 lds\nbuffer_load_dwordx4 v0, s[36:39], s60 offen offset:3072 sc1 lds\ns_add_u32 m0, %[l0], 24576\ns_add_u32 s60, s41, 49152\n" \
"buffer_load_dwordx4 v0, s[36:39], s60 offen offset:0 sc1 lds\nbuffer_load_dwordx4 v0, s[36:39], s60 offen offset:1024 sc1 lds\nbuffer_load_dwordx4 v0, s[36:39], s60 offen offset:2048 sc1 lds\nbuffer_load_dwordx4 v0, s[36:39], s60 offen offset:3072 sc1 lds\ns_add_u32 m0, %[l0], 28672\ns_add_u32 s60, s41, 53248\nbuffer_load_dwordx4 v0, s[36:39], s60 offen offset:0 sc1 lds\nbuffer_load_dwordx4 v0, s[36:39], s60 offen offset:1024 sc1 lds\n" \
"buffer_load_dwordx4 v0, s[36:39], s60 offen offset:2048 sc1 lds\nbuffer_load_dwordx4 v0, s[36:39], s60 offen offset:3072 sc1 lds\ns_waitcnt vmcnt(0)\ns_branch .Lqk_pwait%=\n.Lqk_re3%=:\nv_max_u32 v17, v80, v82\nv_cmp_eq_u32 vcc, -1, v17\ns_cbranch_vccz .Lqk_sk2865%=\n" \
"s_mov_b64 exec, vcc\nbuffer_load_dwordx4 v[80:83], v3, s[36:39], s42 offen offset:0 sc1\ns_mov_b64 exec, -1\n.Lqk_sk2865%=:\nv_max_u32 v17, v84, v86\nv_cmp_eq_u32 vcc, -1, v17\ns_cbranch_vccz .Lqk_sk2872%=\ns_mov_b64 exec, vcc\n" \
"buffer_load_dwordx4 v[84:87], v3, s[36:39], s42 offen offset:1024 sc1\ns_mov_b64 exec, -1\n.Lqk_sk2872%=:\nv_max_u32 v17, v88, v90\nv_cmp_eq_u32 vcc, -1, v17\ns_cbranch_vccz .Lqk_sk2879%=\ns_mov_b64 exec, vcc\nbuffer_load_dwordx4 v[88:91], v3, s[36:39], s42 offen offset:2048 sc1\n" \
"s_mov_b64 exec, -1\n.Lqk_sk2879%=:\nv_max_u32 v17, v92, v94\nv_cmp_eq_u32 vcc, -1, v17\ns_cbranch_vccz .Lqk_sk2886%=\ns_mov_b64 exec, vcc\nbuffer_load_dwordx4 v[92:95], v3, s[36:39], s42 offen offset:3072 sc1\ns_mov_b64 exec, -1\n" \
".Lqk_sk2886%=:\nbuffer_load_dword v16, v12, s[36:39], s56 offen sc1\nv_mov_b64 v[48:49], 0\nv_mov_b64 v[50:51], 0\nv_mov_b64 v[52:53], 0\nv_mov_b64 v[54:55], 0\nv_mov_b64 v[56:57], 0\nv_mov_b64 v[58:59], 0\n" \
"v_mov_b64 v[60:61], 0\nv_mov_b64 v[62:63], 0\ns_add_u32 s43, s43, 1\ns_cmp_lt_u32 s43, 262144\ns_cbranch_scc1 .Lqk_poll3%=\ns_branch .Lqk_abort%=\n.Lqk_abort%=:\ns_mov_b64 exec, -1\n" \
"v_mov_b32 v16, 0\ns_movk_i32 s44, 6400\nbuffer_store_dword v16, off, s[36:39], s44 sc1\n.Lqk_end%=:\ns_waitcnt vmcnt(0) lgkmcnt(0)\n" \
""
#define VU_ASM \
"v_mbcnt_lo_u32_b32 v255, -1, 0\nv_mbcnt_hi_u32_b32 v255, -1, v255\nv_lshlrev_b32 v255, 2, v255\nv_add_u32 v255, %[tb], v255\nds_read_b32 v0, v255 offset:0\nds_read_b32 v1, v255 offset:256\nds_read_b32 v2, v255 offset:512\nds_read_b32 v3, v255 offset:768\n" \
"ds_read_b32 v4, v255 offset:1024\nds_read_b32 v5, v255 offset:1280\nds_read_b32 v6, v255 offset:1536\nds_read_b32 v7, v255 offset:1792\nds_read_b32 v8, v255 offset:2048\nds_read_b32 v9, v255 offset:2304\nds_read_b32 v10, v255 offset:2560\nds_read_b32 v11, v255 offset:2816\n" \
"ds_read_b32 v90, v255 offset:3072\nds_read_b32 v91, v255 offset:3328\nds_read_b32 v92, v255 offset:3584\nds_read_b32 v93, v255 offset:3840\nds_read_b32 v94, v255 offset:4096\nds_read_b32 v95, v255 offset:4352\ns_waitcnt lgkmcnt(0)\ns_mov_b64 s[36:37], %[ws]\n" \
"s_mov_b32 s38, %[sz]\ns_mov_b32 s39, 0x20000\ns_mov_b32 s45, 2096896\nbuffer_load_dword v15, off, s[36:39], s45 sc1\ns_add_u32 m0, %[lw], 16384\ns_add_u32 s44, %[wo], 0\nbuffer_load_dwordx4 v90, %[rw], s44 offen nt lds\ns_add_u32 m0, %[lw], 17408\n" \
"s_add_u32 s44, %[wo], 65536\nbuffer_load_dwordx4 v90, %[rw], s44 offen nt lds\ns_add_u32 m0, %[lw], 18432\ns_add_u32 s44, %[wo], 131072\nbuffer_load_dwordx4 v90, %[rw], s44 offen nt lds\ns_add_u32 m0, %[lw], 19456\ns_add_u32 s44, %[wo], 196608\nbuffer_load_dwordx4 v90, %[rw], s44 offen nt lds\n" \
"s_add_u32 m0, %[lw], 20480\ns_add_u32 s44, %[wo], 524288\nbuffer_load_dwordx4 v90, %[rw], s44 offen nt lds\ns_add_u32 m0, %[lw], 21504\ns_add_u32 s44, %[wo], 589824\nbuffer_load_dwordx4 v90, %[rw], s44 offen nt lds\ns_add_u32 m0, %[lw], 22528\ns_add_u32 s44, %[wo], 655360\n" \
"buffer_load_dwordx4 v90, %[rw], s44 offen nt lds\ns_add_u32 m0, %[lw], 23552\ns_add_u32 s44, %[wo], 720896\nbuffer_load_dwordx4 v90, %[rw], s44 offen nt lds\ns_add_u32 m0, %[tb], 0\ns_add_u32 s44, %[wo], 1048576\nbuffer_load_dwordx4 v90, %[rw], s44 offen nt lds\ns_add_u32 m0, %[tb], 1024\n" \
"s_add_u32 s44, %[wo], 1114112\nbuffer_load_dwordx4 v90, %[rw], s44 offen nt lds\ns_add_u32 m0, %[tb], 2048\ns_add_u32 s44, %[wo], 1179648\nbuffer_load_dwordx4 v90, %[rw], s44 offen nt lds\ns_add_u32 m0, %[tb], 3072\ns_add_u32 s44, %[wo], 1245184\nbuffer_load_dwordx4 v90, %[rw], s44 offen nt lds\n" \
"s_add_u32 m0, %[tb], 4096\ns_add_u32 s44, %[wo], 1572864\nbuffer_load_dwordx4 v90, %[rw], s44 offen nt lds\ns_add_u32 m0, %[tb], 5120\ns_add_u32 s44, %[wo], 1638400\nbuffer_load_dwordx4 v90, %[rw], s44 offen nt lds\ns_add_u32 m0, %[tb], 6144\ns_add_u32 s44, %[wo], 1703936\n" \
"buffer_load_dwordx4 v90, %[rw], s44 offen nt lds\ns_add_u32 m0, %[tb], 7168\ns_add_u32 s44, %[wo], 1769472\nbuffer_load_dwordx4 v90, %[rw], s44 offen nt lds\ns_add_u32 m0, %[tb], 8192\ns_add_u32 s44, %[wo], 2097152\nbuffer_load_dwordx4 v90, %[rw], s44 offen nt lds\ns_add_u32 m0, %[tb], 9216\n" \
"s_add_u32 s44, %[wo], 2162688\nbuffer_load_dwordx4 v90, %[rw], s44 offen nt lds\ns_add_u32 m0, %[tb], 10240\ns_add_u32 s44, %[wo], 2228224\nbuffer_load_dwordx4 v90, %[rw], s44 offen nt lds\ns_add_u32 m0, %[tb], 11264\ns_add_u32 s44, %[wo], 2293760\nbuffer_load_dwordx4 v90, %[rw], s44 offen nt lds\n" \
"s_add_u32 m0, %[lw], 0\ns_add_u32 s44, %[wo], 2621440\nbuffer_load_dwordx4 v90, %[rw], s44 offen nt lds\ns_add_u32 m0, %[lw], 1024\ns_add_u32 s44, %[wo], 2686976\nbuffer_load_dwordx4 v90, %[rw], s44 offen nt lds\ns_add_u32 m0, %[lw], 2048\ns_add_u32 s44, %[wo], 2752512\n" \
"buffer_load_dwordx4 v90, %[rw], s44 offen nt lds\ns_add_u32 m0, %[lw], 3072\ns_add_u32 s44, %[wo], 2818048\nbuffer_load_dwordx4 v90, %[rw], s44 offen nt lds\ns_add_u32 m0, %[lw], 4096\ns_add_u32 s44, %[wo], 3145728\nbuffer_load_dwordx4 v90, %[rw], s44 offen nt lds\ns_add_u32 m0, %[lw], 5120\n" \
"s_add_u32 s44, %[wo], 3211264\nbuffer_load_dwordx4 v90, %[rw], s44 offen nt lds\ns_add_u32 m0, %[lw], 6144\ns_add_u32 s44, %[wo], 3276800\nbuffer_load_dwordx4 v90, %[rw], s44 offen nt lds\ns_add_u32 m0, %[lw], 7168\ns_add_u32 s44, %[wo], 3342336\nbuffer_load_dwordx4 v90, %[rw], s44 offen nt lds\n" \
"s_add_u32 m0, %[lw], 8192\ns_add_u32 s44, %[wo], 3670016\nbuffer_load_dwordx4 v90, %[rw], s44 offen nt lds\ns_add_u32 m0, %[lw], 9216\ns_add_u32 s44, %[wo], 3735552\nbuffer_load_dwordx4 v90, %[rw], s44 offen nt lds\ns_add_u32 m0, %[lw], 10240\ns_add_u32 s44, %[wo], 3801088\n" \
"buffer_load_dwordx4 v90, %[rw], s44 offen nt lds\ns_add_u32 m0, %[lw], 11264\ns_add_u32 s44, %[wo], 3866624\nbuffer_load_dwordx4 v90, %[rw], s44 offen nt lds\ns_waitcnt vmcnt(28)\ns_barrier\ns_add_u32 m0, %[lw], 12288\ns_add_u32 s44, %[wo], 4194304\n" \
"buffer_load_dwordx4 v90, %[rw], s44 offen nt lds\ns_add_u32 m0, %[lw], 13312\ns_add_u32 s44, %[wo], 4259840\nbuffer_load_dwordx4 v90, %[rw], s44 offen nt lds\ns_add_u32 m0, %[lw], 14336\ns_add_u32 s44, %[wo], 4325376\nbuffer_load_dwordx4 v90, %[rw], s44 offen nt lds\ns_add_u32 m0, %[lw], 15360\n" \
"s_add_u32 s44, %[wo], 4390912\nbuffer_load_dwordx4 v90, %[rw], s44 offen nt lds\nds_read_b64_tr_b16 a[0:1], v93 offset:16384\nds_read_b64_tr_b16 a[2:3], v93 offset:16896\nds_read_b64_tr_b16 a[16:17], v93 offset:16392\nds_read_b64_tr_b16 a[18:19], v93 offset:16904\nds_read_b64_tr_b16 a[4:5], v93 offset:40960\nds_read_b64_tr_b16 a[6:7], v93 offset:41472\n" \
"ds_read_b64_tr_b16 a[20:21], v93 offset:40968\nds_read_b64_tr_b16 a[22:23], v93 offset:41480\ns_waitcnt lgkmcnt(0)\ns_waitcnt vmcnt(28)\ns_barrier\ns_add_u32 m0, %[lw], 16384\ns_add_u32 s44, %[wo], 4718592\nbuffer_load_dwordx4 v90, %[rw], s44 offen nt lds\n" \
"s_add_u32 m0, %[lw], 17408\ns_add_u32 s44, %[wo], 4784128\nbuffer_load_dwordx4 v90, %[rw], s44 offen nt lds\ns_add_u32 m0, %[lw], 18432\ns_add_u32 s44, %[wo], 4849664\nbuffer_load_dwordx4 v90, %[rw], s44 offen nt lds\ns_add_u32 m0, %[lw], 19456\ns_add_u32 s44, %[wo], 4915200\n" \
"buffer_load_dwordx4 v90, %[rw], s44 offen nt lds\nds_read_b64_tr_b16 a[24:25], v93 offset:20480\nds_read_b64_tr_b16 a[26:27], v93 offset:20992\nds_read_b64_tr_b16 a[40:41], v93 offset:20488\nds_read_b64_tr_b16 a[42:43], v93 offset:21000\nds_read_b64_tr_b16 a[28:29], v93 offset:45056\nds_read_b64_tr_b16 a[30:31], v93 offset:45568\nds_read_b64_tr_b16 a[44:45], v93 offset:45064\n" \
"ds_read_b64_tr_b16 a[46:47], v93 offset:45576\ns_waitcnt lgkmcnt(0)\ns_waitcnt vmcnt(28)\ns_barrier\ns_add_u32 m0, %[lw], 20480\ns_add_u32 s44, %[wo], 5242880\nbuffer_load_dwordx4 v90, %[rw], s44 offen nt lds\ns_add_u32 m0, %[lw], 21504\n" \
"s_add_u32 s44, %[wo], 5308416\nbuffer_load_dwordx4 v90, %[rw], s44 offen nt lds\ns_add_u32 m0, %[lw], 22528\ns_add_u32 s44, %[wo], 5373952\nbuffer_load_dwordx4 v90, %[rw], s44 offen nt lds\ns_add_u32 m0, %[lw], 23552\ns_add_u32 s44, %[wo], 5439488\nbuffer_load_dwordx4 v90, %[rw], s44 offen nt lds\n" \
"ds_read_b64_tr_b16 v[84:85], v92 offset:0\nds_read_b64_tr_b16 v[86:87], v92 offset:512\nds_read_b64_tr_b16 a[64:65], v92 offset:8\nds_read_b64_tr_b16 a[66:67], v92 offset:520\nds_read_b64_tr_b16 v[80:81], v92 offset:12288\nds_read_b64_tr_b16 v[82:83], v92 offset:12800\nds_read_b64_tr_b16 a[68:69], v92 offset:12296\nds_read_b64_tr_b16 a[70:71], v92 offset:12808\n" \
"s_waitcnt lgkmcnt(0)\ns_waitcnt vmcnt(28)\ns_barrier\ns_add_u32 m0, %[tb], 0\ns_add_u32 s44, %[wo], 5767168\nbuffer_load_dwordx4 v90, %[rw], s44 offen nt lds\ns_add_u32 m0, %[tb], 1024\ns_add_u32 s44, %[wo], 5832704\n" \
"buffer_load_dwordx4 v90, %[rw], s44 offen nt lds\ns_add_u32 m0, %[tb], 2048\ns_add_u32 s44, %[wo], 5898240\nbuffer_load_dwordx4 v90, %[rw], s44 offen nt lds\ns_add_u32 m0, %[tb], 3072\ns_add_u32 s44, %[wo], 5963776\nbuffer_load_dwordx4 v90, %[rw], s44 offen nt lds\nds_read_b64_tr_b16 a[80:81], v92 offset:4096\n" \
"ds_read_b64_tr_b16 a[82:83], v92 offset:4608\nds_read_b64_tr_b16 a[88:89], v92 offset:4104\nds_read_b64_tr_b16 a[90:91], v92 offset:4616\nds_read_b64_tr_b16 a[84:85], v92 offset:16384\nds_read_b64_tr_b16 a[86:87], v92 offset:16896\nds_read_b64_tr_b16 a[92:93], v92 offset:16392\nds_read_b64_tr_b16 a[94:95], v92 offset:16904\ns_waitcnt lgkmcnt(0)\n" \
"s_waitcnt vmcnt(28)\ns_barrier\ns_add_u32 m0, %[tb], 4096\ns_add_u32 s44, %[wo], 6291456\nbuffer_load_dwordx4 v90, %[rw], s44 offen nt lds\ns_add_u32 m0, %[tb], 5120\ns_add_u32 s44, %[wo], 6356992\nbuffer_load_dwordx4 v90, %[rw], s44 offen nt lds\n" \
"s_add_u32 m0, %[tb], 6144\ns_add_u32 s44, %[wo], 6422528\nbuffer_load_dwordx4 v90, %[rw], s44 offen nt lds\ns_add_u32 m0, %[tb], 7168\ns_add_u32 s44, %[wo], 6488064\nbuffer_load_dwordx4 v90, %[rw], s44 offen nt lds\nds_read_b64_tr_b16 a[104:105], v92 offset:8192\nds_read_b64_tr_b16 a[106:107], v92 offset:8704\n" \
"ds_read_b64_tr_b16 a[120:121], v92 offset:8200\nds_read_b64_tr_b16 a[122:123], v92 offset:8712\nds_read_b64_tr_b16 a[108:109], v92 offset:20480\nds_read_b64_tr_b16 a[110:111], v92 offset:20992\nds_read_b64_tr_b16 a[124:125], v92 offset:20488\nds_read_b64_tr_b16 a[126:127], v92 offset:21000\ns_waitcnt lgkmcnt(0)\ns_waitcnt vmcnt(28)\n" \
"s_barrier\ns_add_u32 m0, %[tb], 8192\ns_add_u32 s44, %[wo], 6815744\nbuffer_load_dwordx4 v90, %[rw], s44 offen nt lds\ns_add_u32 m0, %[tb], 9216\ns_add_u32 s44, %[wo], 6881280\nbuffer_load_dwordx4 v90, %[rw], s44 offen nt lds\ns_add_u32 m0, %[tb], 10240\n" \
"s_add_u32 s44, %[wo], 6946816\nbuffer_load_dwordx4 v90, %[rw], s44 offen nt lds\ns_add_u32 m0, %[tb], 11264\ns_add_u32 s44, %[wo], 7012352\nbuffer_load_dwordx4 v90, %[rw], s44 offen nt lds\nds_read_b64_tr_b16 a[128:129], v93 offset:0\nds_read_b64_tr_b16 a[130:131], v93 offset:512\nds_read_b64_tr_b16 v[76:77], v93 offset:8\n" \
"ds_read_b64_tr_b16 v[78:79], v93 offset:520\nds_read_b64_tr_b16 a[132:133], v93 offset:24576\nds_read_b64_tr_b16 a[134:135], v93 offset:25088\nds_read_b64_tr_b16 v[72:73], v93 offset:24584\nds_read_b64_tr_b16 v[74:75], v93 offset:25096\ns_waitcnt lgkmcnt(0)\ns_waitcnt vmcnt(28)\ns_barrier\n" \
"s_add_u32 m0, %[lw], 0\ns_add_u32 s44, %[wo], 7340032\nbuffer_load_dwordx4 v90, %[rw], s44 offen nt lds\ns_add_u32 m0, %[lw], 1024\ns_add_u32 s44, %[wo], 7405568\nbuffer_load_dwordx4 v90, %[rw], s44 offen nt lds\ns_add_u32 m0, %[lw], 2048\ns_add_u32 s44, %[wo], 7471104\n" \
"buffer_load_dwordx4 v90, %[rw], s44 offen nt lds\ns_add_u32 m0, %[lw], 3072\ns_add_u32 s44, %[wo], 7536640\nbuffer_load_dwordx4 v90, %[rw], s44 offen nt lds\nds_read_b64_tr_b16 a[152:153], v93 offset:4096\nds_read_b64_tr_b16 a[154:155], v93 offset:4608\nds_read_b64_tr_b16 a[168:169], v93 offset:4104\nds_read_b64_tr_b16 a[170:171], v93 offset:4616\n" \
"ds_read_b64_tr_b16 a[156:157], v93 offset:28672\nds_read_b64_tr_b16 a[158:159], v93 offset:29184\nds_read_b64_tr_b16 a[172:173], v93 offset:28680\nds_read_b64_tr_b16 a[174:175], v93 offset:29192\ns_waitcnt lgkmcnt(0)\ns_waitcnt vmcnt(28)\ns_barrier\ns_add_u32 m0, %[lw], 4096\n" \
"s_add_u32 s44, %[wo], 7864320\nbuffer_load_dwordx4 v90, %[rw], s44 offen nt lds\ns_add_u32 m0, %[lw], 5120\ns_add_u32 s44, %[wo], 7929856\nbuffer_load_dwordx4 v90, %[rw], s44 offen nt lds\ns_add_u32 m0, %[lw], 6144\ns_add_u32 s44, %[wo], 7995392\nbuffer_load_dwordx4 v90, %[rw], s44 offen nt lds\n" \
"s_add_u32 m0, %[lw], 7168\ns_add_u32 s44, %[wo], 8060928\nbuffer_load_dwordx4 v90, %[rw], s44 offen nt lds\nds_read_b64_tr_b16 a[184:185], v93 offset:8192\nds_read_b64_tr_b16 a[186:187], v93 offset:8704\nds_read_b64_tr_b16 a[192:193], v93 offset:8200\nds_read_b64_tr_b16 a[194:195], v93 offset:8712\nds_read_b64_tr_b16 a[188:189], v93 offset:32768\n" \
"ds_read_b64_tr_b16 a[190:191], v93 offset:33280\nds_read_b64_tr_b16 a[196:197], v93 offset:32776\nds_read_b64_tr_b16 a[198:199], v93 offset:33288\ns_waitcnt lgkmcnt(0)\ns_waitcnt vmcnt(28)\ns_barrier\ns_add_u32 m0, %[lw], 8192\ns_add_u32 s44, %[w2], 0\n" \
"buffer_load_dwordx4 v91, %[ro], s44 offen nt lds\ns_add_u32 m0, %[lw], 9216\ns_add_u32 s44, %[w2], 32768\nbuffer_load_dwordx4 v91, %[ro], s44 offen nt lds\ns_add_u32 m0, %[lw], 10240\ns_add_u32 s44, %[w2], 65536\nbuffer_load_dwordx4 v91, %[ro], s44 offen nt lds\ns_add_u32 m0, %[lw], 11264\n" \
"s_add_u32 s44, %[w2], 98304\nbuffer_load_dwordx4 v91, %[ro], s44 offen nt lds\nds_read_b64_tr_b16 v[68:69], v93 offset:12288\nds_read_b64_tr_b16 v[70:71], v93 offset:12800\nds_read_b64_tr_b16 a[216:217], v93 offset:12296\nds_read_b64_tr_b16 a[218:219], v93 offset:12808\nds_read_b64_tr_b16 v[64:65], v93 offset:36864\nds_read_b64_tr_b16 v[66:67], v93 offset:37376\n" \
"ds_read_b64_tr_b16 a[220:221], v93 offset:36872\nds_read_b64_tr_b16 a[222:223], v93 offset:37384\ns_waitcnt lgkmcnt(0)\ns_waitcnt vmcnt(28)\ns_barrier\ns_add_u32 m0, %[lw], 12288\ns_add_u32 s44, %[w2], 128\nbuffer_load_dwordx4 v91, %[ro], s44 offen nt lds\n" \
"s_add_u32 m0, %[lw], 13312\ns_add_u32 s44, %[w2], 32896\nbuffer_load_dwordx4 v91, %[ro], s44 offen nt lds\ns_add_u32 m0, %[lw], 14336\ns_add_u32 s44, %[w2], 65664\nbuffer_load_dwordx4 v91, %[ro], s44 offen nt lds\ns_add_u32 m0, %[lw], 15360\ns_add_u32 s44, %[w2], 98432\n" \
"buffer_load_dwordx4 v91, %[ro], s44 offen nt lds\nds_read_b64_tr_b16 a[224:225], v93 offset:16384\nds_read_b64_tr_b16 a[226:227], v93 offset:16896\nds_read_b64_tr_b16 a[240:241], v93 offset:16392\nds_read_b64_tr_b16 a[242:243], v93 offset:16904\nds_read_b64_tr_b16 a[228:229], v93 offset:40960\nds_read_b64_tr_b16 a[230:231], v93 offset:41472\nds_read_b64_tr_b16 a[244:245], v93 offset:40968\n" \
"ds_read_b64_tr_b16 a[246:247], v93 offset:41480\ns_waitcnt lgkmcnt(0)\ns_waitcnt vmcnt(28)\ns_barrier\ns_add_u32 m0, %[lw], 16384\ns_add_u32 s44, %[w2], 256\nbuffer_load_dwordx4 v91, %[ro], s44 offen nt lds\ns_add_u32 m0, %[lw], 17408\n" \
"s_add_u32 s44, %[w2], 33024\nbuffer_load_dwordx4 v91, %[ro], s44 offen nt lds\ns_add_u32 m0, %[lw], 18432\ns_add_u32 s44, %[w2], 65792\nbuffer_load_dwordx4 v91, %[ro], s44 offen nt lds\ns_add_u32 m0, %[lw], 19456\ns_add_u32 s44, %[w2], 98560\nbuffer_load_dwordx4 v91, %[ro], s44 offen nt lds\n" \
"ds_read_b64_tr_b16 v[96:97], v93 offset:20480\nds_read_b64_tr_b16 v[98:99], v93 offset:20992\nds_read_b64_tr_b16 v[112:113], v93 offset:20488\nds_read_b64_tr_b16 v[114:115], v93 offset:21000\nds_read_b64_tr_b16 v[100:101], v93 offset:45056\nds_read_b64_tr_b16 v[102:103], v93 offset:45568\nds_read_b64_tr_b16 v[116:117], v93 offset:45064\nds_read_b64_tr_b16 v[118:119], v93 offset:45576\n" \
"s_waitcnt lgkmcnt(0)\ns_waitcnt vmcnt(28)\ns_barrier\ns_add_u32 m0, %[lw], 20480\ns_add_u32 s44, %[w2], 384\nbuffer_load_dwordx4 v91, %[ro], s44 offen nt lds\ns_add_u32 m0, %[lw], 21504\ns_add_u32 s44, %[w2], 33152\n" \
"buffer_load_dwordx4 v91, %[ro], s44 offen nt lds\ns_add_u32 m0, %[lw], 22528\ns_add_u32 s44, %[w2], 65920\nbuffer_load_dwordx4 v91, %[ro], s44 offen nt lds\ns_add_u32 m0, %[lw], 23552\ns_add_u32 s44, %[w2], 98688\nbuffer_load_dwordx4 v91, %[ro], s44 offen nt lds\nds_read_b64_tr_b16 v[128:129], v92 offset:0\n" \
"ds_read_b64_tr_b16 v[130:131], v92 offset:512\nds_read_b64_tr_b16 v[60:61], v92 offset:8\nds_read_b64_tr_b16 v[62:63], v92 offset:520\nds_read_b64_tr_b16 v[132:133], v92 offset:12288\nds_read_b64_tr_b16 v[134:135], v92 offset:12800\nds_read_b64_tr_b16 v[56:57], v92 offset:12296\nds_read_b64_tr_b16 v[58:59], v92 offset:12808\ns_waitcnt lgkmcnt(0)\n" \
"s_waitcnt vmcnt(28)\ns_barrier\ns_add_u32 m0, %[tb], 0\ns_add_u32 s44, %[w2], 139264\nbuffer_load_dwordx4 v91, %[ro], s44 offen nt lds\ns_add_u32 m0, %[tb], 1024\ns_add_u32 s44, %[w2], 172032\nbuffer_load_dwordx4 v91, %[ro], s44 offen nt lds\n" \
"s_add_u32 m0, %[tb], 2048\ns_add_u32 s44, %[w2], 204800\nbuffer_load_dwordx4 v91, %[ro], s44 offen nt lds\ns_add_u32 m0, %[tb], 3072\ns_add_u32 s44, %[w2], 237568\nbuffer_load_dwordx4 v91, %[ro], s44 offen nt lds\nds_read_b64_tr_b16 v[144:145], v92 offset:4096\nds_read_b64_tr_b16 v[146:147], v92 offset:4608\n" \
"ds_read_b64_tr_b16 v[160:161], v92 offset:4104\nds_read_b64_tr_b16 v[162:163], v92 offset:4616\nds_read_b64_tr_b16 v[148:149], v92 offset:16384\nds_read_b64_tr_b16 v[150:151], v92 offset:16896\nds_read_b64_tr_b16 v[164:165], v92 offset:16392\nds_read_b64_tr_b16 v[166:167], v92 offset:16904\ns_waitcnt lgkmcnt(0)\ns_waitcnt vmcnt(28)\n" \
"s_barrier\ns_add_u32 m0, %[tb], 4096\ns_add_u32 s44, %[w2], 139392\nbuffer_load_dwordx4 v91, %[ro], s44 offen nt lds\ns_add_u32 m0, %[tb], 5120\ns_add_u32 s44, %[w2], 172160\nbuffer_load_dwordx4 v91, %[ro], s44 offen nt lds\ns_add_u32 m0, %[tb], 6144\n" \
"s_add_u32 s44, %[w2], 204928\nbuffer_load_dwordx4 v91, %[ro], s44 offen nt lds\ns_add_u32 m0, %[tb], 7168\ns_add_u32 s44, %[w2], 237696\nbuffer_load_dwordx4 v91, %[ro], s44 offen nt lds\nds_read_b64_tr_b16 v[168:169], v92 offset:8192\nds_read_b64_tr_b16 v[170:171], v92 offset:8704\nds_read_b64_tr_b16 v[184:185], v92 offset:8200\n" \
"ds_read_b64_tr_b16 v[186:187], v92 offset:8712\nds_read_b64_tr_b16 v[172:173], v92 offset:20480\nds_read_b64_tr_b16 v[174:175], v92 offset:20992\nds_read_b64_tr_b16 v[188:189], v92 offset:20488\nds_read_b64_tr_b16 v[190:191], v92 offset:21000\ns_waitcnt lgkmcnt(0)\ns_waitcnt vmcnt(28)\ns_barrier\n" \
"s_add_u32 m0, %[tb], 8192\ns_add_u32 s44, %[w2], 139520\nbuffer_load_dwordx4 v91, %[ro], s44 offen nt lds\ns_add_u32 m0, %[tb], 9216\ns_add_u32 s44, %[w2], 172288\nbuffer_load_dwordx4 v91, %[ro], s44 offen nt lds\ns_add_u32 m0, %[tb], 10240\ns_add_u32 s44, %[w2], 205056\n" \
"buffer_load_dwordx4 v91, %[ro], s44 offen nt lds\ns_add_u32 m0, %[tb], 11264\ns_add_u32 s44, %[w2], 237824\nbuffer_load_dwordx4 v91, %[ro], s44 offen nt lds\nds_read_b64_tr_b16 v[200:201], v93 offset:0\nds_read_b64_tr_b16 v[202:203], v93 offset:512\nds_read_b64_tr_b16 v[216:217], v93 offset:8\nds_read_b64_tr_b16 v[218:219], v93 offset:520\n" \
"ds_read_b64_tr_b16 v[204:205], v93 offset:24576\nds_read_b64_tr_b16 v[206:207], v93 offset:25088\nds_read_b64_tr_b16 v[220:221], v93 offset:24584\nds_read_b64_tr_b16 v[222:223], v93 offset:25096\ns_waitcnt lgkmcnt(0)\ns_waitcnt vmcnt(28)\ns_barrier\ns_add_u32 m0, %[lw], 0\n" \
"s_add_u32 s44, %[w2], 139648\nbuffer_load_dwordx4 v91, %[ro], s44 offen nt lds\ns_add_u32 m0, %[lw], 1024\ns_add_u32 s44, %[w2], 172416\nbuffer_load_dwordx4 v91, %[ro], s44 offen nt lds\ns_add_u32 m0, %[lw], 2048\ns_add_u32 s44, %[w2], 205184\nbuffer_load_dwordx4 v91, %[ro], s44 offen nt lds\n" \
"s_add_u32 m0, %[lw], 3072\ns_add_u32 s44, %[w2], 237952\nbuffer_load_dwordx4 v91, %[ro], s44 offen nt lds\nds_read_b64_tr_b16 v[232:233], v93 offset:4096\nds_read_b64_tr_b16 v[234:235], v93 offset:4608\nds_read_b64_tr_b16 v[240:241], v93 offset:4104\nds_read_b64_tr_b16 v[242:243], v93 offset:4616\nds_read_b64_tr_b16 v[236:237], v93 offset:28672\n" \
"ds_read_b64_tr_b16 v[238:239], v93 offset:29184\nds_read_b64_tr_b16 v[244:245], v93 offset:28680\nds_read_b64_tr_b16 v[246:247], v93 offset:29192\ns_waitcnt lgkmcnt(0)\ns_waitcnt vmcnt(28)\ns_barrier\ns_add_u32 m0, %[lw], 4096\ns_add_u32 s44, %[w2], 131072\n" \
"buffer_load_dwordx4 v91, %[ro], s44 offen nt lds\ns_add_u32 m0, %[lw], 5120\ns_add_u32 s44, %[w2], 163840\nbuffer_load_dwordx4 v91, %[ro], s44 offen nt lds\ns_add_u32 m0, %[lw], 6144\ns_add_u32 s44, %[w2], 196608\nbuffer_load_dwordx4 v91, %[ro], s44 offen nt lds\ns_add_u32 m0, %[lw], 7168\n" \
"s_add_u32 s44, %[w2], 229376\nbuffer_load_dwordx4 v91, %[ro], s44 offen nt lds\nds_read_b64_tr_b16 a[8:9], v95 offset:8192\nds_read_b64_tr_b16 a[10:11], v95 offset:8448\nds_read_b64_tr_b16 a[12:13], v95 offset:10240\nds_read_b64_tr_b16 a[14:15], v95 offset:10496\nds_read_b64_tr_b16 a[208:209], v95 offset:8256\nds_read_b64_tr_b16 a[210:211], v95 offset:8512\n" \
"ds_read_b64_tr_b16 a[212:213], v95 offset:10304\nds_read_b64_tr_b16 a[214:215], v95 offset:10560\ns_waitcnt lgkmcnt(0)\ns_waitcnt vmcnt(28)\ns_barrier\ns_add_u32 m0, %[lw], 8192\ns_add_u32 s44, %[w2], 131200\nbuffer_load_dwordx4 v91, %[ro], s44 offen nt lds\n" \
"s_add_u32 m0, %[lw], 9216\ns_add_u32 s44, %[w2], 163968\nbuffer_load_dwordx4 v91, %[ro], s44 offen nt lds\ns_add_u32 m0, %[lw], 10240\ns_add_u32 s44, %[w2], 196736\nbuffer_load_dwordx4 v91, %[ro], s44 offen nt lds\ns_add_u32 m0, %[lw], 11264\ns_add_u32 s44, %[w2], 229504\n" \
"buffer_load_dwordx4 v91, %[ro], s44 offen nt lds\nds_read_b64_tr_b16 a[56:57], v95 offset:12288\nds_read_b64_tr_b16 a[58:59], v95 offset:12544\nds_read_b64_tr_b16 a[60:61], v95 offset:14336\nds_read_b64_tr_b16 a[62:63], v95 offset:14592\nds_read_b64_tr_b16 v[104:105], v95 offset:12352\nds_read_b64_tr_b16 v[106:107], v95 offset:12608\nds_read_b64_tr_b16 v[108:109], v95 offset:14400\n" \
"ds_read_b64_tr_b16 v[110:111], v95 offset:14656\ns_waitcnt lgkmcnt(0)\ns_waitcnt vmcnt(28)\ns_barrier\ns_add_u32 m0, %[lw], 12288\ns_add_u32 s44, %[w2], 131328\nbuffer_load_dwordx4 v91, %[ro], s44 offen nt lds\ns_add_u32 m0, %[lw], 13312\n" \
"s_add_u32 s44, %[w2], 164096\nbuffer_load_dwordx4 v91, %[ro], s44 offen nt lds\ns_add_u32 m0, %[lw], 14336\ns_add_u32 s44, %[w2], 196864\nbuffer_load_dwordx4 v91, %[ro], s44 offen nt lds\ns_add_u32 m0, %[lw], 15360\ns_add_u32 s44, %[w2], 229632\nbuffer_load_dwordx4 v91, %[ro], s44 offen nt lds\n" \
"ds_read_b64_tr_b16 a[112:113], v95 offset:16384\nds_read_b64_tr_b16 a[114:115], v95 offset:16640\nds_read_b64_tr_b16 a[116:117], v95 offset:18432\nds_read_b64_tr_b16 a[118:119], v95 offset:18688\nds_read_b64_tr_b16 v[152:153], v95 offset:16448\nds_read_b64_tr_b16 v[154:155], v95 offset:16704\nds_read_b64_tr_b16 v[156:157], v95 offset:18496\nds_read_b64_tr_b16 v[158:159], v95 offset:18752\n" \
"s_waitcnt lgkmcnt(0)\ns_waitcnt vmcnt(28)\ns_barrier\ns_add_u32 m0, %[lw], 16384\ns_add_u32 s44, %[w2], 131456\nbuffer_load_dwordx4 v91, %[ro], s44 offen nt lds\ns_add_u32 m0, %[lw], 17408\ns_add_u32 s44, %[w2], 164224\n" \
"buffer_load_dwordx4 v91, %[ro], s44 offen nt lds\ns_add_u32 m0, %[lw], 18432\ns_add_u32 s44, %[w2], 196992\nbuffer_load_dwordx4 v91, %[ro], s44 offen nt lds\ns_add_u32 m0, %[lw], 19456\ns_add_u32 s44, %[w2], 229760\nbuffer_load_dwordx4 v91, %[ro], s44 offen nt lds\nds_read_b64_tr_b16 a[160:161], v95 offset:20480\n" \
"ds_read_b64_tr_b16 a[162:163], v95 offset:20736\nds_read_b64_tr_b16 a[164:165], v95 offset:22528\nds_read_b64_tr_b16 a[166:167], v95 offset:22784\nds_read_b64_tr_b16 v[208:209], v95 offset:20544\nds_read_b64_tr_b16 v[210:211], v95 offset:20800\nds_read_b64_tr_b16 v[212:213], v95 offset:22592\nds_read_b64_tr_b16 v[214:215], v95 offset:22848\ns_waitcnt lgkmcnt(0)\n" \
"s_waitcnt vmcnt(28)\ns_barrier\ns_add_u32 m0, %[lw], 20480\ns_add_u32 s44, %[w2], 8192\nbuffer_load_dwordx4 v91, %[ro], s44 offen nt lds\ns_add_u32 m0, %[lw], 21504\ns_add_u32 s44, %[w2], 40960\nbuffer_load_dwordx4 v91, %[ro], s44 offen nt lds\n" \
"s_add_u32 m0, %[lw], 22528\ns_add_u32 s44, %[w2], 73728\nbuffer_load_dwordx4 v91, %[ro], s44 offen nt lds\ns_add_u32 m0, %[lw], 23552\ns_add_u32 s44, %[w2], 106496\nbuffer_load_dwordx4 v91, %[ro], s44 offen nt lds\nds_read_b64_tr_b16 a[48:49], v94 offset:0\nds_read_b64_tr_b16 a[50:51], v94 offset:256\n" \
"ds_read_b64_tr_b16 a[52:53], v94 offset:2048\nds_read_b64_tr_b16 a[54:55], v94 offset:2304\nds_read_b64_tr_b16 a[248:249], v94 offset:64\nds_read_b64_tr_b16 a[250:251], v94 offset:320\nds_read_b64_tr_b16 a[252:253], v94 offset:2112\nds_read_b64_tr_b16 a[254:255], v94 offset:2368\ns_waitcnt lgkmcnt(0)\ns_waitcnt vmcnt(28)\n" \
"s_barrier\ns_add_u32 m0, %[tb], 0\ns_add_u32 s44, %[w2], 8320\nbuffer_load_dwordx4 v91, %[ro], s44 offen nt lds\ns_add_u32 m0, %[tb], 1024\ns_add_u32 s44, %[w2], 41088\nbuffer_load_dwordx4 v91, %[ro], s44 offen nt lds\ns_add_u32 m0, %[tb], 2048\n" \
"s_add_u32 s44, %[w2], 73856\nbuffer_load_dwordx4 v91, %[ro], s44 offen nt lds\ns_add_u32 m0, %[tb], 3072\ns_add_u32 s44, %[w2], 106624\nbuffer_load_dwordx4 v91, %[ro], s44 offen nt lds\nds_read_b64_tr_b16 a[96:97], v94 offset:4096\nds_read_b64_tr_b16 a[98:99], v94 offset:4352\nds_read_b64_tr_b16 a[100:101], v94 offset:6144\n" \
"ds_read_b64_tr_b16 a[102:103], v94 offset:6400\nds_read_b64_tr_b16 v[136:137], v94 offset:4160\nds_read_b64_tr_b16 v[138:139], v94 offset:4416\nds_read_b64_tr_b16 v[140:141], v94 offset:6208\nds_read_b64_tr_b16 v[142:143], v94 offset:6464\ns_waitcnt lgkmcnt(0)\nbuffer_load_dwordx2 v[12:13], off, s[36:39], s45 sc1\ns_waitcnt vmcnt(29)\n" \
"s_barrier\ns_add_u32 m0, %[tb], 4096\ns_add_u32 s44, %[w2], 8448\nbuffer_load_dwordx4 v91, %[ro], s44 offen nt lds\ns_add_u32 m0, %[tb], 5120\ns_add_u32 s44, %[w2], 41216\nbuffer_load_dwordx4 v91, %[ro], s44 offen nt lds\ns_add_u32 m0, %[tb], 6144\n" \
"s_add_u32 s44, %[w2], 73984\nbuffer_load_dwordx4 v91, %[ro], s44 offen nt lds\ns_add_u32 m0, %[tb], 7168\ns_add_u32 s44, %[w2], 106752\nbuffer_load_dwordx4 v91, %[ro], s44 offen nt lds\nds_read_b64_tr_b16 a[144:145], v94 offset:8192\nds_read_b64_tr_b16 a[146:147], v94 offset:8448\nds_read_b64_tr_b16 a[148:149], v94 offset:10240\n" \
"ds_read_b64_tr_b16 a[150:151], v94 offset:10496\nds_read_b64_tr_b16 v[192:193], v94 offset:8256\nds_read_b64_tr_b16 v[194:195], v94 offset:8512\nds_read_b64_tr_b16 v[196:197], v94 offset:10304\nds_read_b64_tr_b16 v[198:199], v94 offset:10560\ns_waitcnt lgkmcnt(0)\ns_waitcnt vmcnt(29)\ns_barrier\n" \
"s_add_u32 m0, %[tb], 8192\ns_add_u32 s44, %[w2], 8576\nbuffer_load_dwordx4 v91, %[ro], s44 offen nt lds\ns_add_u32 m0, %[tb], 9216\ns_add_u32 s44, %[w2], 41344\nbuffer_load_dwordx4 v91, %[ro], s44 offen nt lds\ns_add_u32 m0, %[tb], 10240\ns_add_u32 s44, %[w2], 74112\n" \
"buffer_load_dwordx4 v91, %[ro], s44 offen nt lds\ns_add_u32 m0, %[tb], 11264\ns_add_u32 s44, %[w2], 106880\nbuffer_load_dwordx4 v91, %[ro], s44 offen nt lds\nds_read_b64_tr_b16 a[200:201], v95 offset:0\nds_read_b64_tr_b16 a[202:203], v95 offset:256\nds_read_b64_tr_b16 a[204:205], v95 offset:2048\nds_read_b64_tr_b16 a[206:207], v95 offset:2304\n" \
"ds_read_b64_tr_b16 v[248:249], v95 offset:64\nds_read_b64_tr_b16 v[250:251], v95 offset:320\nds_read_b64_tr_b16 v[252:253], v95 offset:2112\nds_read_b64_tr_b16 v[254:255], v95 offset:2368\ns_waitcnt lgkmcnt(0)\ns_waitcnt vmcnt(29)\ns_barrier\nds_read_b64_tr_b16 v[52:53], v95 offset:4096\n" \
"ds_read_b64_tr_b16 v[54:55], v95 offset:4352\nds_read_b64_tr_b16 v[48:49], v95 offset:6144\nds_read_b64_tr_b16 v[50:51], v95 offset:6400\nds_read_b64_tr_b16 v[44:45], v95 offset:4160\nds_read_b64_tr_b16 v[46:47], v95 offset:4416\nds_read_b64_tr_b16 v[40:41], v95 offset:6208\nds_read_b64_tr_b16 v[42:43], v95 offset:6464\ns_waitcnt lgkmcnt(0)\n" \
"ds_write_b128 v1, v[84:87] offset:0\nds_write_b128 v1, v[80:83] offset:1024\nds_write_b128 v1, v[76:79] offset:2048\nds_write_b128 v1, v[72:75] offset:3072\ns_waitcnt vmcnt(25)\ns_barrier\nds_read_b64_tr_b16 a[72:73], v95 offset:8192\nds_read_b64_tr_b16 a[74:75], v95 offset:8448\n" \
"ds_read_b64_tr_b16 a[76:77], v95 offset:10240\nds_read_b64_tr_b16 a[78:79], v95 offset:10496\nds_read_b64_tr_b16 v[120:121], v95 offset:8256\nds_read_b64_tr_b16 v[122:123], v95 offset:8512\nds_read_b64_tr_b16 v[124:125], v95 offset:10304\nds_read_b64_tr_b16 v[126:127], v95 offset:10560\ns_waitcnt lgkmcnt(0)\nds_write_b128 v1, v[68:71] offset:4096\n" \
"ds_write_b128 v1, v[64:67] offset:5120\nds_write_b128 v1, v[60:63] offset:6144\nds_write_b128 v1, v[56:59] offset:7168\ns_waitcnt vmcnt(21)\ns_barrier\nds_read_b64_tr_b16 v[56:57], v95 offset:12288\nds_read_b64_tr_b16 v[58:59], v95 offset:12544\nds_read_b64_tr_b16 v[60:61], v95 offset:14336\n" \
"ds_read_b64_tr_b16 v[62:63], v95 offset:14592\nds_read_b64_tr_b16 v[64:65], v95 offset:12352\nds_read_b64_tr_b16 v[66:67], v95 offset:12608\nds_read_b64_tr_b16 v[68:69], v95 offset:14400\nds_read_b64_tr_b16 v[70:71], v95 offset:14656\ns_waitcnt lgkmcnt(0)\nds_write_b128 v1, v[52:55] offset:8192\nds_write_b128 v1, v[48:51] offset:9216\n" \
"ds_write_b128 v1, v[44:47] offset:10240\nds_write_b128 v1, v[40:43] offset:11264\ns_waitcnt vmcnt(17)\ns_barrier\nds_read_b64_tr_b16 a[176:177], v95 offset:16384\nds_read_b64_tr_b16 a[178:179], v95 offset:16640\nds_read_b64_tr_b16 a[180:181], v95 offset:18432\nds_read_b64_tr_b16 a[182:183], v95 offset:18688\n" \
"ds_read_b64_tr_b16 v[224:225], v95 offset:16448\nds_read_b64_tr_b16 v[226:227], v95 offset:16704\nds_read_b64_tr_b16 v[228:229], v95 offset:18496\nds_read_b64_tr_b16 v[230:231], v95 offset:18752\ns_waitcnt lgkmcnt(0)\nds_write_b128 v1, v[56:59] offset:12288\nds_write_b128 v1, v[60:63] offset:13312\nds_write_b128 v1, v[64:67] offset:14336\n" \
"ds_write_b128 v1, v[68:71] offset:15360\ns_waitcnt vmcnt(13)\ns_barrier\nds_read_b64_tr_b16 a[32:33], v95 offset:20480\nds_read_b64_tr_b16 a[34:35], v95 offset:20736\nds_read_b64_tr_b16 a[36:37], v95 offset:22528\nds_read_b64_tr_b16 a[38:39], v95 offset:22784\nds_read_b64_tr_b16 a[232:233], v95 offset:20544\n" \
"ds_read_b64_tr_b16 a[234:235], v95 offset:20800\nds_read_b64_tr_b16 a[236:237], v95 offset:22592\nds_read_b64_tr_b16 a[238:239], v95 offset:22848\ns_waitcnt lgkmcnt(0)\ns_waitcnt vmcnt(9)\ns_barrier\nds_read_b64_tr_b16 v[68:69], v94 offset:0\nds_read_b64_tr_b16 v[70:71], v94 offset:256\n" \
"ds_read_b64_tr_b16 v[64:65], v94 offset:2048\nds_read_b64_tr_b16 v[66:67], v94 offset:2304\nds_read_b64_tr_b16 v[60:61], v94 offset:64\nds_read_b64_tr_b16 v[62:63], v94 offset:320\nds_read_b64_tr_b16 v[56:57], v94 offset:2112\nds_read_b64_tr_b16 v[58:59], v94 offset:2368\ns_waitcnt lgkmcnt(0)\nds_write_b128 v1, v[68:71] offset:16384\n" \
"ds_write_b128 v1, v[64:67] offset:17408\nds_write_b128 v1, v[60:63] offset:18432\nds_write_b128 v1, v[56:59] offset:19456\ns_waitcnt vmcnt(4)\ns_barrier\nds_read_b64_tr_b16 a[136:137], v94 offset:4096\nds_read_b64_tr_b16 a[138:139], v94 offset:4352\nds_read_b64_tr_b16 a[140:141], v94 offset:6144\n" \
"ds_read_b64_tr_b16 a[142:143], v94 offset:6400\nds_read_b64_tr_b16 v[176:177], v94 offset:4160\nds_read_b64_tr_b16 v[178:179], v94 offset:4416\nds_read_b64_tr_b16 v[180:181], v94 offset:6208\nds_read_b64_tr_b16 v[182:183], v94 offset:6464\ns_waitcnt lgkmcnt(0)\ns_waitcnt vmcnt(0)\ns_barrier\n" \
"ds_read_b64_tr_b16 v[56:57], v94 offset:8192\nds_read_b64_tr_b16 v[58:59], v94 offset:8448\nds_read_b64_tr_b16 v[60:61], v94 offset:10240\nds_read_b64_tr_b16 v[62:63], v94 offset:10496\nds_read_b64_tr_b16 v[64:65], v94 offset:8256\nds_read_b64_tr_b16 v[66:67], v94 offset:8512\nds_read_b64_tr_b16 v[68:69], v94 offset:10304\nds_read_b64_tr_b16 v[70:71], v94 offset:10560\n" \
"s_waitcnt lgkmcnt(0)\ns_mov_b32 s43, 0\ns_mov_b32 s86, 110\ns_mov_b32 s89, 0\n.Lvu_hb%=:\nv_cmp_ne_u32 vcc, v15, v12\ns_cbranch_vccnz .Lvu_hb1%=\ns_sleep 4\n" \
"buffer_load_dwordx2 v[12:13], off, s[36:39], s45 sc1\ns_waitcnt vmcnt(0)\ns_add_u32 s43, s43, 1\ns_cmp_lt_u32 s43, 262144\ns_cbranch_scc1 .Lvu_hb%=\ns_branch .Lvu_abort%=\n.Lvu_hb1%=:\nv_readfirstlane_b32 s44, v13\n" \
"s_and_b32 s44, s44, 0x200000\ns_add_u32 s36, s36, s44\ns_addc_u32 s37, s37, 0\nds_write_b128 v1, v[56:59] offset:20480\nds_write_b128 v1, v[60:63] offset:21504\nds_write_b128 v1, v[64:67] offset:22528\nds_write_b128 v1, v[68:71] offset:23552\ns_waitcnt lgkmcnt(0)\n" \
"s_barrier\ns_mov_b32 s40, -1\ns_mov_b32 s43, 0\ns_lshl_b32 s57, %[w], 12\ns_mov_b32 s56, 0\ns_mov_b32 s42, 0\ns_mov_b32 s60, -2147483648\ns_mov_b32 s54, -2147483648\n" \
"s_add_u32 s44, s40, 1\ns_lshl_b32 s44, s44, 17\ns_add_u32 s44, s44, %[xn]\ns_lshr_b32 s45, s44, 8\ns_and_b32 s45, s45, 0x2000\ns_xor_b32 s41, s44, s45\ns_add_u32 s41, s41, %[xw]\n.Lvu_f0%=:\n" \
"buffer_load_dword v14, v8, s[36:39], s56 offen sc1\ns_waitcnt vmcnt(0)\nv_cmp_ge_i32 vcc, s40, v14\ns_cbranch_vccz .Lvu_f0fok%=\ns_add_u32 s43, s43, 1\ns_cmp_lt_u32 s43, 262144\ns_cbranch_scc1 .Lvu_f0%=\ns_branch .Lvu_abort%=\n" \
".Lvu_f0fok%=:\ns_add_u32 m0, s57, 0\ns_add_u32 s58, s41, 0\nbuffer_load_dwordx4 v0, s[36:39], s58 offen offset:0 sc1 lds\nbuffer_load_dwordx4 v0, s[36:39], s58 offen offset:1024 sc1 lds\nbuffer_load_dwordx4 v0, s[36:39], s58 offen offset:2048 sc1 lds\nbuffer_load_dwordx4 v0, s[36:39], s58 offen offset:3072 sc1 lds\ns_add_u32 m0, s57, 32768\n" \
"s_add_u32 s58, s41, 32768\nbuffer_load_dwordx4 v0, s[36:39], s58 offen offset:0 sc1 lds\nbuffer_load_dwordx4 v0, s[36:39], s58 offen offset:1024 sc1 lds\nbuffer_load_dwordx4 v0, s[36:39], s58 offen offset:2048 sc1 lds\nbuffer_load_dwordx4 v0, s[36:39], s58 offen offset:3072 sc1 lds\ns_waitcnt vmcnt(0)\ns_barrier\nds_read_b128 v[32:35], v2 offset:0\n" \
"ds_read_b128 v[36:39], v2 offset:2048\nds_read_b128 v[40:43], v2 offset:256\nds_read_b128 v[44:47], v2 offset:2304\n.p2align 6\n.Lvu_tok%=:\nds_read_b128 v[64:67], v2 offset:4096\nds_read_b128 v[68:71], v2 offset:6144\nds_read_b128 v[72:75], v2 offset:4352\n" \
"s_waitcnt lgkmcnt(6)\nv_mfma_f32_16x16x32_bf16 v[16:19], a[0:3], v[32:35], 0\ns_waitcnt lgkmcnt(5)\nv_cvt_pk_bf16_f32 v92, v80, v81\nv_cvt_pk_bf16_f32 v93, v82, v83\nv_mfma_f32_16x16x32_bf16 v[16:19], a[4:7], v[36:39], v[16:19]\nds_read_b128 v[76:79], v2 offset:6400\nv_cvt_pk_bf16_f32 v94, v84, v85\n" \
"v_cvt_pk_bf16_f32 v95, v86, v87\nv_mfma_f32_16x16x32_bf16 v[20:23], a[16:19], v[32:35], 0\nv_and_or_b32 v92, v92, -2, v13\nv_and_or_b32 v94, v94, -2, v13\nv_mfma_f32_16x16x32_bf16 v[20:23], a[20:23], v[36:39], v[20:23]\ns_waitcnt lgkmcnt(5)\nbuffer_store_dwordx4 v[92:95], v7, s[36:39], s60 offen offset:2560 sc1\nds_read_b128 v[32:35], v2 offset:8192\n" \
"v_mfma_f32_16x16x32_bf16 v[24:27], a[0:3], v[40:43], 0\ns_waitcnt lgkmcnt(5)\nds_read_b128 v[56:59], v1 offset:0\nds_read_b128 v[60:63], v1 offset:1024\nv_mfma_f32_16x16x32_bf16 v[24:27], a[4:7], v[44:47], v[24:27]\ns_add_u32 m0, s57, 16384\nds_read_b128 v[36:39], v2 offset:10240\nv_mfma_f32_16x16x32_bf16 v[28:31], a[16:19], v[40:43], 0\n" \
"s_add_u32 s60, s41, 65536\nbuffer_load_dwordx4 v0, s[36:39], s60 offen offset:0 sc1 lds\nv_mfma_f32_16x16x32_bf16 v[28:31], a[20:23], v[44:47], v[28:31]\ns_waitcnt lgkmcnt(7)\nds_read_b128 v[40:43], v2 offset:8448\nv_mfma_f32_16x16x32_bf16 v[16:19], a[24:27], v[64:67], v[16:19]\ns_waitcnt lgkmcnt(7)\nv_mfma_f32_16x16x32_bf16 v[16:19], a[28:31], v[68:71], v[16:19]\n" \
"ds_read_b128 v[44:47], v2 offset:10496\nv_mfma_f32_16x16x32_bf16 v[20:23], a[40:43], v[64:67], v[20:23]\nbuffer_load_dwordx4 v0, s[36:39], s60 offen offset:1024 sc1 lds\nv_mfma_f32_16x16x32_bf16 v[20:23], a[44:47], v[68:71], v[20:23]\ns_waitcnt lgkmcnt(7)\nds_read_b128 v[64:67], v2 offset:12288\nv_mfma_f32_16x16x32_bf16 v[24:27], a[24:27], v[72:75], v[24:27]\ns_waitcnt lgkmcnt(7)\n" \
"v_mfma_f32_16x16x32_bf16 v[24:27], a[28:31], v[76:79], v[24:27]\nds_read_b128 v[68:71], v2 offset:14336\nv_mfma_f32_16x16x32_bf16 v[28:31], a[40:43], v[72:75], v[28:31]\nbuffer_load_dwordx4 v0, s[36:39], s60 offen offset:2048 sc1 lds\nv_mfma_f32_16x16x32_bf16 v[28:31], a[44:47], v[76:79], v[28:31]\ns_waitcnt lgkmcnt(5)\nds_read_b128 v[72:75], v2 offset:12544\nv_mfma_f32_16x16x32_bf16 v[16:19], v[56:59], v[32:35], v[16:19]\n" \
"s_waitcnt lgkmcnt(5)\nv_mfma_f32_16x16x32_bf16 v[16:19], v[60:63], v[36:39], v[16:19]\nds_read_b128 v[76:79], v2 offset:14592\nv_mfma_f32_16x16x32_bf16 v[20:23], a[64:67], v[32:35], v[20:23]\nbuffer_load_dwordx4 v0, s[36:39], s60 offen offset:3072 sc1 lds\nv_mfma_f32_16x16x32_bf16 v[20:23], a[68:71], v[36:39], v[20:23]\ns_waitcnt lgkmcnt(5)\nds_read_b128 v[32:35], v2 offset:32768\n" \
"v_mfma_f32_16x16x32_bf16 v[24:27], v[56:59], v[40:43], v[24:27]\ns_add_u32 s56, s56, %[fs]\ns_add_u32 s62, s40, 1\ns_waitcnt lgkmcnt(5)\nv_mfma_f32_16x16x32_bf16 v[24:27], v[60:63], v[44:47], v[24:27]\ns_lshr_b32 m0, s57, 4\ns_add_u32 m0, m0, 159744\nds_read_b128 v[36:39], v2 offset:34816\n" \
"v_mfma_f32_16x16x32_bf16 v[28:31], a[64:67], v[40:43], v[28:31]\nbuffer_load_dword v8, s[36:39], s56 offen sc1 lds\nv_lshrrev_b32 v14, 2, v0\nv_mfma_f32_16x16x32_bf16 v[28:31], a[68:71], v[44:47], v[28:31]\ns_waitcnt lgkmcnt(5)\nds_read_b128 v[40:43], v2 offset:33024\nv_mfma_f32_16x16x32_bf16 v[16:19], a[80:83], v[64:67], v[16:19]\ns_waitcnt lgkmcnt(5)\n" \
"v_add_u32 v14, 159744, v14\nv_mfma_f32_16x16x32_bf16 v[16:19], a[84:87], v[68:71], v[16:19]\nds_read_b128 v[44:47], v2 offset:35072\nv_mfma_f32_16x16x32_bf16 v[20:23], a[88:91], v[64:67], v[20:23]\nds_read_b128 v[56:59], v1 offset:2048\nds_read_b128 v[60:63], v1 offset:3072\nv_mfma_f32_16x16x32_bf16 v[20:23], a[92:95], v[68:71], v[20:23]\ns_waitcnt lgkmcnt(7)\n" \
"ds_read_b128 v[64:67], v2 offset:36864\nv_mfma_f32_16x16x32_bf16 v[24:27], a[80:83], v[72:75], v[24:27]\ns_waitcnt lgkmcnt(7)\nv_mfma_f32_16x16x32_bf16 v[24:27], a[84:87], v[76:79], v[24:27]\nds_read_b128 v[68:71], v2 offset:38912\nv_mfma_f32_16x16x32_bf16 v[28:31], a[88:91], v[72:75], v[28:31]\nv_mfma_f32_16x16x32_bf16 v[28:31], a[92:95], v[76:79], v[28:31]\ns_waitcnt lgkmcnt(7)\n" \
"ds_read_b128 v[72:75], v2 offset:37120\nv_mfma_f32_16x16x32_bf16 v[16:19], a[104:107], v[32:35], v[16:19]\ns_waitcnt lgkmcnt(7)\nv_mfma_f32_16x16x32_bf16 v[16:19], a[108:111], v[36:39], v[16:19]\nds_read_b128 v[76:79], v2 offset:39168\nv_mfma_f32_16x16x32_bf16 v[20:23], a[120:123], v[32:35], v[20:23]\ns_barrier\nv_mfma_f32_16x16x32_bf16 v[20:23], a[124:127], v[36:39], v[20:23]\n" \
"s_waitcnt lgkmcnt(7)\ns_add_u32 m0, s57, 0\nds_read_b128 v[32:35], v2 offset:40960\nv_mfma_f32_16x16x32_bf16 v[24:27], a[104:107], v[40:43], v[24:27]\ns_add_u32 s60, s41, 98304\ns_waitcnt lgkmcnt(7)\nbuffer_load_dwordx4 v0, s[36:39], s60 offen offset:0 sc1 lds\nv_mfma_f32_16x16x32_bf16 v[24:27], a[108:111], v[44:47], v[24:27]\n" \
"ds_read_b128 v[36:39], v2 offset:43008\nv_mfma_f32_16x16x32_bf16 v[28:31], a[120:123], v[40:43], v[28:31]\nv_mfma_f32_16x16x32_bf16 v[28:31], a[124:127], v[44:47], v[28:31]\ns_waitcnt lgkmcnt(5)\nds_read_b128 v[40:43], v2 offset:41216\nv_mfma_f32_16x16x32_bf16 v[16:19], a[128:131], v[64:67], v[16:19]\ns_waitcnt lgkmcnt(5)\nbuffer_load_dwordx4 v0, s[36:39], s60 offen offset:1024 sc1 lds\n" \
"v_mfma_f32_16x16x32_bf16 v[16:19], a[132:135], v[68:71], v[16:19]\nds_read_b128 v[44:47], v2 offset:43264\nv_mfma_f32_16x16x32_bf16 v[20:23], v[56:59], v[64:67], v[20:23]\nv_mfma_f32_16x16x32_bf16 v[20:23], v[60:63], v[68:71], v[20:23]\ns_waitcnt lgkmcnt(5)\nds_read_b128 v[64:67], v2 offset:45056\nv_mfma_f32_16x16x32_bf16 v[24:27], a[128:131], v[72:75], v[24:27]\ns_waitcnt lgkmcnt(5)\n" \
"buffer_load_dwordx4 v0, s[36:39], s60 offen offset:2048 sc1 lds\nv_mfma_f32_16x16x32_bf16 v[24:27], a[132:135], v[76:79], v[24:27]\nds_read_b128 v[68:71], v2 offset:47104\nv_mfma_f32_16x16x32_bf16 v[28:31], v[56:59], v[72:75], v[28:31]\nv_mfma_f32_16x16x32_bf16 v[28:31], v[60:63], v[76:79], v[28:31]\ns_waitcnt lgkmcnt(5)\nds_read_b128 v[72:75], v2 offset:45312\nv_mfma_f32_16x16x32_bf16 v[16:19], a[152:155], v[32:35], v[16:19]\n" \
"s_waitcnt lgkmcnt(5)\nbuffer_load_dwordx4 v0, s[36:39], s60 offen offset:3072 sc1 lds\nv_mfma_f32_16x16x32_bf16 v[16:19], a[156:159], v[36:39], v[16:19]\ns_add_u32 s44, s40, 2\nds_read_b128 v[76:79], v2 offset:47360\nv_mfma_f32_16x16x32_bf16 v[20:23], a[168:171], v[32:35], v[20:23]\ns_lshl_b32 s44, s44, 17\nds_read_b128 v[56:59], v1 offset:4096\n" \
"ds_read_b128 v[60:63], v1 offset:5120\nv_mfma_f32_16x16x32_bf16 v[20:23], a[172:175], v[36:39], v[20:23]\ns_waitcnt lgkmcnt(7)\nv_mfma_f32_16x16x32_bf16 v[24:27], a[152:155], v[40:43], v[24:27]\ns_add_u32 s44, s44, %[xn]\ns_lshr_b32 s45, s44, 8\ns_waitcnt lgkmcnt(6)\nv_mfma_f32_16x16x32_bf16 v[24:27], a[156:159], v[44:47], v[24:27]\n" \
"v_mfma_f32_16x16x32_bf16 v[28:31], a[168:171], v[40:43], v[28:31]\ns_and_b32 s45, s45, 0x2000\ns_xor_b32 s41, s44, s45\nv_mfma_f32_16x16x32_bf16 v[28:31], a[172:175], v[44:47], v[28:31]\ns_waitcnt lgkmcnt(5)\nv_mfma_f32_16x16x32_bf16 v[16:19], a[184:187], v[64:67], v[16:19]\ns_waitcnt lgkmcnt(4)\nv_mfma_f32_16x16x32_bf16 v[16:19], a[188:191], v[68:71], v[16:19]\n" \
"v_mfma_f32_16x16x32_bf16 v[20:23], a[192:195], v[64:67], v[20:23]\ns_waitcnt lgkmcnt(2)\ns_waitcnt vmcnt(4)\ns_barrier\ns_add_u32 s41, s41, %[xw]\nds_read_b128 v[32:35], v2 offset:16384\nv_mfma_f32_16x16x32_bf16 v[20:23], a[196:199], v[68:71], v[20:23]\nds_read_b128 v[36:39], v2 offset:18432\n" \
"v_mfma_f32_16x16x32_bf16 v[24:27], a[184:187], v[72:75], v[24:27]\nds_read_b128 v[40:43], v2 offset:16640\nds_read_b32 v14, v14\nv_mfma_f32_16x16x32_bf16 v[24:27], a[188:191], v[76:79], v[24:27]\nds_read_b128 v[44:47], v2 offset:18688\nv_mfma_f32_16x16x32_bf16 v[28:31], a[192:195], v[72:75], v[28:31]\ns_waitcnt vmcnt(4)\nbuffer_store_dword v15, off, s[36:39], s54 sc1\n" \
"ds_read_b128 v[64:67], v2 offset:20480\nv_mfma_f32_16x16x32_bf16 v[28:31], a[196:199], v[76:79], v[28:31]\ns_waitcnt lgkmcnt(5)\nds_read_b128 v[68:71], v2 offset:22528\nv_mfma_f32_16x16x32_bf16 v[16:19], v[56:59], v[32:35], v[16:19]\ns_waitcnt lgkmcnt(3)\nds_read_b128 v[72:75], v2 offset:20736\nv_cmp_ge_i32 s[64:65], s62, v14\n" \
"v_mfma_f32_16x16x32_bf16 v[16:19], v[60:63], v[36:39], v[16:19]\ns_cmp_eq_u64 s[64:65], 0\ns_cselect_b32 s61, s41, -2147483648\nds_read_b128 v[76:79], v2 offset:22784\nv_mfma_f32_16x16x32_bf16 v[20:23], a[216:219], v[32:35], v[20:23]\ns_add_u32 m0, s57, 32768\ns_add_u32 s58, s61, 32768\nbuffer_load_dwordx4 v0, s[36:39], s58 offen offset:0 sc1 lds\n" \
"v_mfma_f32_16x16x32_bf16 v[20:23], a[220:223], v[36:39], v[20:23]\nds_read_b128 v[32:35], v2 offset:24576\nv_mfma_f32_16x16x32_bf16 v[24:27], v[56:59], v[40:43], v[24:27]\ns_waitcnt lgkmcnt(5)\nv_mfma_f32_16x16x32_bf16 v[24:27], v[60:63], v[44:47], v[24:27]\nds_read_b128 v[36:39], v2 offset:26624\nv_mfma_f32_16x16x32_bf16 v[28:31], a[216:219], v[40:43], v[28:31]\nbuffer_load_dwordx4 v0, s[36:39], s58 offen offset:1024 sc1 lds\n" \
"v_mfma_f32_16x16x32_bf16 v[28:31], a[220:223], v[44:47], v[28:31]\ns_waitcnt lgkmcnt(5)\nds_read_b128 v[40:43], v2 offset:24832\nv_mfma_f32_16x16x32_bf16 v[16:19], a[224:227], v[64:67], v[16:19]\ns_waitcnt lgkmcnt(5)\nds_read_b128 v[56:59], v1 offset:6144\nds_read_b128 v[60:63], v1 offset:7168\nv_mfma_f32_16x16x32_bf16 v[16:19], a[228:231], v[68:71], v[16:19]\n" \
"ds_read_b128 v[44:47], v2 offset:26880\nv_mfma_f32_16x16x32_bf16 v[20:23], a[240:243], v[64:67], v[20:23]\nbuffer_load_dwordx4 v0, s[36:39], s58 offen offset:2048 sc1 lds\nv_mfma_f32_16x16x32_bf16 v[20:23], a[244:247], v[68:71], v[20:23]\ns_waitcnt lgkmcnt(7)\nds_read_b128 v[64:67], v2 offset:28672\nv_mfma_f32_16x16x32_bf16 v[24:27], a[224:227], v[72:75], v[24:27]\ns_waitcnt lgkmcnt(7)\n" \
"v_mfma_f32_16x16x32_bf16 v[24:27], a[228:231], v[76:79], v[24:27]\nds_read_b128 v[68:71], v2 offset:30720\nv_mfma_f32_16x16x32_bf16 v[28:31], a[240:243], v[72:75], v[28:31]\nbuffer_load_dwordx4 v0, s[36:39], s58 offen offset:3072 sc1 lds\nv_mfma_f32_16x16x32_bf16 v[28:31], a[244:247], v[76:79], v[28:31]\ns_waitcnt lgkmcnt(7)\nds_read_b128 v[72:75], v2 offset:28928\nv_mfma_f32_16x16x32_bf16 v[16:19], v[96:99], v[32:35], v[16:19]\n" \
"s_waitcnt lgkmcnt(7)\nv_mfma_f32_16x16x32_bf16 v[16:19], v[100:103], v[36:39], v[16:19]\nds_read_b128 v[76:79], v2 offset:30976\nv_mfma_f32_16x16x32_bf16 v[20:23], v[112:115], v[32:35], v[20:23]\nv_mfma_f32_16x16x32_bf16 v[20:23], v[116:119], v[36:39], v[20:23]\ns_waitcnt lgkmcnt(7)\nv_mfma_f32_16x16x32_bf16 v[24:27], v[96:99], v[40:43], v[24:27]\ns_waitcnt lgkmcnt(4)\n" \
"v_mfma_f32_16x16x32_bf16 v[24:27], v[100:103], v[44:47], v[24:27]\nv_mfma_f32_16x16x32_bf16 v[28:31], v[112:115], v[40:43], v[28:31]\ns_waitcnt lgkmcnt(0)\ns_waitcnt vmcnt(5)\ns_barrier\ns_add_u32 m0, s57, 16384\nds_read_b128 v[32:35], v2 offset:0\nbuffer_load_dwordx4 v3, s[36:39], s42 offen offset:0 sc1 lds\n" \
"v_mfma_f32_16x16x32_bf16 v[28:31], v[116:119], v[44:47], v[28:31]\nds_read_b128 v[36:39], v2 offset:2048\nv_mfma_f32_16x16x32_bf16 v[16:19], v[128:131], v[64:67], v[16:19]\nds_read_b128 v[40:43], v2 offset:4096\nv_mfma_f32_16x16x32_bf16 v[16:19], v[132:135], v[68:71], v[16:19]\nds_read_b128 v[44:47], v2 offset:6144\nv_mfma_f32_16x16x32_bf16 v[20:23], v[56:59], v[64:67], v[20:23]\nbuffer_load_dwordx4 v3, s[36:39], s42 offen offset:1024 sc1 lds\n" \
"v_mfma_f32_16x16x32_bf16 v[20:23], v[60:63], v[68:71], v[20:23]\nds_read_b128 v[64:67], v2 offset:8192\nv_mfma_f32_16x16x32_bf16 v[24:27], v[128:131], v[72:75], v[24:27]\nv_mfma_f32_16x16x32_bf16 v[24:27], v[132:135], v[76:79], v[24:27]\nds_read_b128 v[68:71], v2 offset:10240\nv_mfma_f32_16x16x32_bf16 v[28:31], v[56:59], v[72:75], v[28:31]\nbuffer_load_dwordx4 v3, s[36:39], s42 offen offset:2048 sc1 lds\nv_mfma_f32_16x16x32_bf16 v[28:31], v[60:63], v[76:79], v[28:31]\n" \
"s_waitcnt lgkmcnt(5)\nds_read_b128 v[72:75], v2 offset:12288\nv_mfma_f32_16x16x32_bf16 v[16:19], v[144:147], v[32:35], v[16:19]\ns_waitcnt lgkmcnt(5)\nv_mfma_f32_16x16x32_bf16 v[16:19], v[148:151], v[36:39], v[16:19]\nds_read_b128 v[76:79], v2 offset:14336\nv_mfma_f32_16x16x32_bf16 v[20:23], v[160:163], v[32:35], v[20:23]\nbuffer_load_dwordx4 v3, s[36:39], s42 offen offset:3072 sc1 lds\n" \
"v_mfma_f32_16x16x32_bf16 v[20:23], v[164:167], v[36:39], v[20:23]\ns_waitcnt lgkmcnt(5)\nds_read_b128 v[32:35], v2 offset:256\nv_mfma_f32_16x16x32_bf16 v[16:19], v[168:171], v[40:43], v[16:19]\ns_waitcnt lgkmcnt(5)\nv_add_u32 v14, s57, v0\nv_mfma_f32_16x16x32_bf16 v[16:19], v[172:175], v[44:47], v[16:19]\nds_read_b128 v[36:39], v2 offset:2304\n" \
"v_mfma_f32_16x16x32_bf16 v[20:23], v[184:187], v[40:43], v[20:23]\nv_mov_b64 v[48:49], 0\nv_mfma_f32_16x16x32_bf16 v[20:23], v[188:191], v[44:47], v[20:23]\ns_waitcnt lgkmcnt(5)\nds_read_b128 v[40:43], v2 offset:4352\nv_mov_b64 v[50:51], 0\nv_mfma_f32_16x16x32_bf16 v[16:19], v[200:203], v[64:67], v[16:19]\ns_waitcnt lgkmcnt(5)\n" \
"v_mov_b64 v[52:53], 0\nv_mfma_f32_16x16x32_bf16 v[16:19], v[204:207], v[68:71], v[16:19]\nds_read_b128 v[44:47], v2 offset:6400\nv_mov_b64 v[54:55], 0\nv_mfma_f32_16x16x32_bf16 v[20:23], v[216:219], v[64:67], v[20:23]\nds_read_b128 v[56:59], v1 offset:0\nds_read_b128 v[60:63], v1 offset:1024\nv_mfma_f32_16x16x32_bf16 v[20:23], v[220:223], v[68:71], v[20:23]\n" \
"s_waitcnt lgkmcnt(7)\nds_read_b128 v[64:67], v2 offset:8448\nv_mfma_f32_16x16x32_bf16 v[16:19], v[232:235], v[72:75], v[16:19]\ns_waitcnt lgkmcnt(7)\nv_mfma_f32_16x16x32_bf16 v[16:19], v[236:239], v[76:79], v[16:19]\nds_read_b128 v[68:71], v2 offset:10496\nv_mfma_f32_16x16x32_bf16 v[20:23], v[240:243], v[72:75], v[20:23]\nv_mfma_f32_16x16x32_bf16 v[20:23], v[244:247], v[76:79], v[20:23]\n" \
"s_waitcnt lgkmcnt(7)\nv_mfma_f32_16x16x32_bf16 v[24:27], v[144:147], v[32:35], v[24:27]\ns_waitcnt vmcnt(0)\ns_waitcnt lgkmcnt(6)\nds_read_b128 v[80:83], v14 offset:16384\nds_read_b128 v[84:87], v14 offset:17408\nv_mfma_f32_16x16x32_bf16 v[24:27], v[148:151], v[36:39], v[24:27]\nv_cvt_pk_bf16_f32 v72, v16, v17\n" \
"v_mfma_f32_16x16x32_bf16 v[28:31], v[160:163], v[32:35], v[28:31]\nds_read_b128 v[88:91], v14 offset:18432\nds_read_b128 v[92:95], v14 offset:19456\nv_cvt_pk_bf16_f32 v73, v18, v19\nv_mfma_f32_16x16x32_bf16 v[28:31], v[164:167], v[36:39], v[28:31]\ns_waitcnt lgkmcnt(9)\nds_read_b128 v[32:35], v2 offset:12544\nv_cvt_pk_bf16_f32 v74, v20, v21\n" \
"v_mfma_f32_16x16x32_bf16 v[24:27], v[168:171], v[40:43], v[24:27]\ns_waitcnt lgkmcnt(3)\nv_max3_u32 v76, v80, v82, v84\nv_cvt_pk_bf16_f32 v75, v22, v23\nv_mfma_f32_16x16x32_bf16 v[24:27], v[172:175], v[44:47], v[24:27]\ns_waitcnt lgkmcnt(2)\nds_read_b128 v[36:39], v2 offset:14592\nv_max3_u32 v76, v76, v86, v88\n" \
"v_mfma_f32_16x16x32_bf16 v[28:31], v[184:187], v[40:43], v[28:31]\ns_waitcnt lgkmcnt(2)\nds_write2st64_b64 v9, v[72:73], v[74:75] offset0:0 offset1:8\nv_max3_u32 v76, v76, v90, v92\nv_max_u32 v76, v76, v94\nv_mfma_f32_16x16x32_bf16 v[28:31], v[188:191], v[44:47], v[28:31]\nv_cmp_eq_u32 vcc, -1, v76\nv_mfma_f32_16x16x32_bf16 v[24:27], v[200:203], v[64:67], v[24:27]\n" \
"s_cbranch_vccnz .Lvu_re%=\n.Lvu_rer%=:\ns_barrier\ns_memrealtime s[68:69]\nds_read_b128 v[80:83], v0 offset:16384\nv_mfma_f32_16x16x32_bf16 v[24:27], v[204:207], v[68:71], v[24:27]\nds_read_b128 v[84:87], v0 offset:17408\nv_mfma_f32_16x16x32_bf16 v[28:31], v[216:219], v[64:67], v[28:31]\n" \
"ds_read_b128 v[88:91], v0 offset:18432\nv_mfma_f32_16x16x32_bf16 v[28:31], v[220:223], v[68:71], v[28:31]\ns_waitcnt lgkmcnt(5)\nds_read_b128 v[92:95], v0 offset:19456\nv_mfma_f32_16x16x32_bf16 v[24:27], v[232:235], v[32:35], v[24:27]\ns_waitcnt lgkmcnt(5)\nds_read_b128 v[16:19], v0 offset:20480\nv_mfma_f32_16x16x32_bf16 v[24:27], v[236:239], v[36:39], v[24:27]\n" \
"ds_read_b128 v[20:23], v0 offset:21504\nv_mfma_f32_16x16x32_bf16 v[28:31], v[240:243], v[32:35], v[28:31]\nds_read_b128 v[40:43], v0 offset:22528\nv_mfma_f32_16x16x32_bf16 v[28:31], v[244:247], v[36:39], v[28:31]\ns_cmp_lt_i32 s40, 0\nds_read_b128 v[44:47], v0 offset:23552\ns_cbranch_scc1 .Lvu_first%=\n.Lvu_crit%=:\n" \
"s_waitcnt lgkmcnt(7)\nv_smfmac_f32_16x16x64_bf16 v[48:51], v[80:83], a[0:7], v4\ns_waitcnt lgkmcnt(6)\nds_read_b128 v[64:67], v1 offset:4096\nds_read_b128 v[32:35], v0 offset:24576\nv_mov_b32 v14, s40\nv_smfmac_f32_16x16x64_bf16 v[48:51], v[84:87], a[24:31], v4\ns_waitcnt lgkmcnt(7)\n" \
"ds_read_b128 v[68:71], v1 offset:5120\nds_read_b128 v[36:39], v0 offset:25600\nv_cvt_pk_bf16_f32 v76, v24, v25\nv_smfmac_f32_16x16x64_bf16 v[48:51], v[88:91], v[56:63], v4\ns_sub_u32 s88, s86, s89\ns_min_u32 s88, s88, 150\ns_waitcnt lgkmcnt(8)\nds_read_b128 v[80:83], v0 offset:28672\n" \
"v_cvt_pk_bf16_f32 v77, v26, v27\nv_smfmac_f32_16x16x64_bf16 v[48:51], v[92:95], a[80:87], v4\ns_waitcnt lgkmcnt(8)\nds_read_b128 v[84:87], v0 offset:29696\nv_cvt_pk_bf16_f32 v78, v28, v29\nv_smfmac_f32_16x16x64_bf16 v[48:51], v[16:19], a[104:111], v4\ns_add_u32 s88, s88, s68\ns_mov_b32 s89, s68\n" \
"s_waitcnt lgkmcnt(8)\nds_read_b128 v[88:91], v0 offset:30720\nv_cvt_pk_bf16_f32 v79, v30, v31\nv_smfmac_f32_16x16x64_bf16 v[48:51], v[20:23], a[128:135], v4\ns_waitcnt lgkmcnt(8)\nds_read_b128 v[92:95], v0 offset:31744\nv_mov_b32 v15, s88\nv_smfmac_f32_16x16x64_bf16 v[48:51], v[40:43], a[152:159], v4\n" \
"s_waitcnt lgkmcnt(8)\nds_read_b128 v[24:27], v0 offset:26624\nbuffer_store_dwordx2 v[14:15], off, s[36:39], %[sg] sc1\nv_smfmac_f32_16x16x64_bf16 v[48:51], v[44:47], a[184:191], v4\ns_waitcnt lgkmcnt(7)\ns_waitcnt lgkmcnt(6)\nds_read_b128 v[28:31], v0 offset:27648\nv_smfmac_f32_16x16x64_bf16 v[48:51], v[32:35], v[64:71], v4\n" \
"s_waitcnt lgkmcnt(6)\nds_read_b128 v[56:59], v1 offset:2048\nv_smfmac_f32_16x16x64_bf16 v[48:51], v[36:39], a[224:231], v4\ns_waitcnt lgkmcnt(6)\nds_read_b128 v[60:63], v1 offset:3072\nv_smfmac_f32_16x16x64_bf16 v[48:51], v[80:83], v[144:151], v4\ns_waitcnt lgkmcnt(6)\nds_write2st64_b64 v9, v[76:77], v[78:79] offset0:2 offset1:10\n" \
"v_smfmac_f32_16x16x64_bf16 v[48:51], v[84:87], v[168:175], v4\ns_waitcnt lgkmcnt(6)\nv_smfmac_f32_16x16x64_bf16 v[48:51], v[88:91], v[200:207], v4\ns_waitcnt lgkmcnt(5)\nv_smfmac_f32_16x16x64_bf16 v[48:51], v[92:95], v[232:239], v4\ns_waitcnt lgkmcnt(4)\nv_smfmac_f32_16x16x64_bf16 v[48:51], v[24:27], v[96:103], v4\ns_waitcnt lgkmcnt(3)\n" \
"ds_read_b128 v[64:67], v1 offset:6144\nv_smfmac_f32_16x16x64_bf16 v[48:51], v[28:31], v[128:135], v4\nds_read_b128 v[68:71], v1 offset:7168\nv_smfmac_f32_16x16x64_bf16 v[52:55], v[80:83], v[160:167], v4\nv_smfmac_f32_16x16x64_bf16 v[52:55], v[84:87], v[184:191], v4\nv_add_f32 v48, v48, v50\nv_smfmac_f32_16x16x64_bf16 v[52:55], v[88:91], v[216:223], v4\nds_read_b128 v[80:83], v0 offset:16384\n" \
"v_add_f32 v49, v49, v51\nv_smfmac_f32_16x16x64_bf16 v[52:55], v[92:95], v[240:247], v4\nds_read_b128 v[84:87], v0 offset:17408\nv_cvt_pk_bf16_f32 v14, v48, v49\nv_smfmac_f32_16x16x64_bf16 v[52:55], v[16:19], a[120:127], v4\ns_waitcnt lgkmcnt(5)\nds_write_b16 v5, v14 offset:0\nds_write_b16_d16_hi v5, v14 offset:16\n" \
"v_smfmac_f32_16x16x64_bf16 v[52:55], v[20:23], v[56:63], v4\nds_read_b128 v[88:91], v0 offset:18432\nv_mov_b64 v[16:17], 0\nv_smfmac_f32_16x16x64_bf16 v[52:55], v[24:27], v[112:119], v4\ns_waitcnt lgkmcnt(5)\nds_read_b128 v[92:95], v0 offset:19456\nv_mov_b64 v[18:19], 0\nv_smfmac_f32_16x16x64_bf16 v[52:55], v[28:31], v[64:71], v4\n" \
"ds_read_b128 v[56:59], v1 offset:8192\nv_mov_b64 v[20:21], 0\nv_smfmac_f32_16x16x64_bf16 v[52:55], v[32:35], a[216:223], v4\ns_waitcnt lgkmcnt(3)\nds_read_b128 v[60:63], v1 offset:9216\ns_barrier\nds_read_b128 v[72:75], v11 offset:57344\nv_smfmac_f32_16x16x64_bf16 v[52:55], v[36:39], a[240:247], v4\n" \
"ds_read_b128 v[76:79], v11 offset:58368\nv_mov_b64 v[22:23], 0\nv_smfmac_f32_16x16x64_bf16 v[52:55], v[40:43], a[168:175], v4\nv_mov_b64 v[24:25], 0\nv_smfmac_f32_16x16x64_bf16 v[52:55], v[44:47], a[192:199], v4\nds_read_b128 v[64:67], v1 offset:12288\nv_mov_b64 v[26:27], 0\nv_smfmac_f32_16x16x64_bf16 v[52:55], v[80:83], a[16:23], v4\n" \
"ds_read_b128 v[68:71], v1 offset:13312\nv_mov_b64 v[28:29], 0\nv_smfmac_f32_16x16x64_bf16 v[52:55], v[84:87], a[40:47], v4\ns_waitcnt lgkmcnt(7)\nv_mov_b64 v[30:31], 0\nv_smfmac_f32_16x16x64_bf16 v[52:55], v[88:91], a[64:71], v4\ns_waitcnt lgkmcnt(6)\nv_mov_b64 v[32:33], 0\n" \
"v_smfmac_f32_16x16x64_bf16 v[52:55], v[92:95], a[88:95], v4\ns_waitcnt lgkmcnt(3)\nv_mov_b64 v[34:35], 0\nv_smfmac_f32_16x16x64_bf16 v[16:19], v[72:75], a[8:15], v10\ns_waitcnt lgkmcnt(2)\nv_mov_b64 v[36:37], 0\nv_smfmac_f32_16x16x64_bf16 v[16:19], v[76:79], v[56:63], v10\nds_read_b128 v[56:59], v1 offset:10240\n" \
"v_add_f32 v52, v52, v54\nv_smfmac_f32_16x16x64_bf16 v[20:23], v[72:75], a[56:63], v10\nds_read_b128 v[60:63], v1 offset:11264\nv_add_f32 v53, v53, v55\nv_smfmac_f32_16x16x64_bf16 v[20:23], v[76:79], a[72:79], v10\nv_cvt_pk_bf16_f32 v14, v52, v53\nv_smfmac_f32_16x16x64_bf16 v[24:27], v[72:75], a[112:119], v10\ns_waitcnt lgkmcnt(2)\n" \
"ds_write_b16 v5, v14 offset:2048\nds_write_b16_d16_hi v5, v14 offset:2064\nv_smfmac_f32_16x16x64_bf16 v[24:27], v[76:79], v[64:71], v10\nds_read_b128 v[64:67], v1 offset:14336\nv_mov_b64 v[38:39], 0\nv_smfmac_f32_16x16x64_bf16 v[28:31], v[72:75], a[160:167], v10\nds_read_b128 v[68:71], v1 offset:15360\nv_mov_b64 v[40:41], 0\n" \
"v_smfmac_f32_16x16x64_bf16 v[28:31], v[76:79], a[176:183], v10\nv_mov_b64 v[42:43], 0\nv_smfmac_f32_16x16x64_bf16 v[32:35], v[72:75], a[208:215], v10\ns_waitcnt lgkmcnt(2)\ns_barrier\ns_mov_b32 s59, 0\nds_read_b128 v[80:83], v11 offset:59392\nv_smfmac_f32_16x16x64_bf16 v[32:35], v[76:79], v[56:63], v10\n" \
"s_add_u32 m0, s57, 0\ns_add_u32 s58, s61, 0\nds_read_b128 v[56:59], v1 offset:16384\nbuffer_load_dwordx4 v0, s[36:39], s58 offen offset:0 sc1 lds\nds_read_b128 v[84:87], v11 offset:60416\nv_smfmac_f32_16x16x64_bf16 v[36:39], v[72:75], v[104:111], v10\nds_read_b128 v[60:63], v1 offset:17408\nv_mov_b64 v[44:45], 0\n" \
"v_smfmac_f32_16x16x64_bf16 v[36:39], v[76:79], v[120:127], v10\nv_mov_b64 v[46:47], 0\nv_smfmac_f32_16x16x64_bf16 v[40:43], v[72:75], v[152:159], v10\ns_waitcnt lgkmcnt(4)\nbuffer_load_dwordx4 v0, s[36:39], s58 offen offset:1024 sc1 lds\nv_smfmac_f32_16x16x64_bf16 v[40:43], v[76:79], v[64:71], v10\nds_read_b128 v[64:67], v1 offset:20480\nv_mov_b64 v[48:49], 0\n" \
"v_smfmac_f32_16x16x64_bf16 v[44:47], v[72:75], v[208:215], v10\nds_read_b128 v[68:71], v1 offset:21504\nv_mov_b64 v[50:51], 0\nv_smfmac_f32_16x16x64_bf16 v[44:47], v[76:79], v[224:231], v10\ns_waitcnt lgkmcnt(5)\nbuffer_load_dwordx4 v0, s[36:39], s58 offen offset:2048 sc1 lds\nv_smfmac_f32_16x16x64_bf16 v[16:19], v[80:83], a[32:39], v10\ns_waitcnt lgkmcnt(3)\n" \
"ds_read_b128 v[72:75], v11 offset:49152\nv_mov_b64 v[52:53], 0\nv_smfmac_f32_16x16x64_bf16 v[16:19], v[84:87], a[48:55], v10\ns_waitcnt lgkmcnt(3)\nds_read_b128 v[76:79], v11 offset:51200\nv_mov_b64 v[54:55], 0\nv_smfmac_f32_16x16x64_bf16 v[20:23], v[80:83], v[56:63], v10\nds_read_b128 v[56:59], v1 offset:18432\n" \
"buffer_load_dwordx4 v0, s[36:39], s58 offen offset:3072 sc1 lds\nv_smfmac_f32_16x16x64_bf16 v[20:23], v[84:87], a[96:103], v10\nds_read_b128 v[60:63], v1 offset:19456\nv_cvt_pk_bf16_f32 v88, v16, v17\nv_smfmac_f32_16x16x64_bf16 v[24:27], v[80:83], a[136:143], v10\nv_and_or_b32 v88, v88, -2, v12\nv_cvt_pk_bf16_f32 v89, v18, v19\nv_smfmac_f32_16x16x64_bf16 v[24:27], v[84:87], a[144:151], v10\n" \
"s_waitcnt lgkmcnt(4)\nv_cvt_pk_bf16_f32 v90, v20, v21\nv_cvt_pk_bf16_f32 v91, v22, v23\nv_and_or_b32 v90, v90, -2, v12\nv_smfmac_f32_16x16x64_bf16 v[28:31], v[80:83], v[64:71], v10\nds_read_b128 v[64:67], v1 offset:22528\nv_permlane16_swap_b32 v89, v91\nv_smfmac_f32_16x16x64_bf16 v[28:31], v[84:87], a[200:207], v10\n" \
"ds_read_b128 v[68:71], v1 offset:23552\nv_permlane16_swap_b32 v88, v90\nv_smfmac_f32_16x16x64_bf16 v[32:35], v[80:83], a[232:239], v10\ns_add_u32 s63, s59, 0\nds_read_b128 v[16:19], v11 offset:53248\nv_cvt_pk_bf16_f32 v92, v24, v25\nv_and_or_b32 v92, v92, -2, v12\nv_smfmac_f32_16x16x64_bf16 v[32:35], v[84:87], a[248:255], v10\n" \
"s_waitcnt lgkmcnt(3)\nds_read_b128 v[20:23], v11 offset:55296\nbuffer_store_dwordx4 v[88:91], v6, s[36:39], s63 offen sc1\nv_cvt_pk_bf16_f32 v93, v26, v27\nv_cvt_pk_bf16_f32 v94, v28, v29\nv_cvt_pk_bf16_f32 v95, v30, v31\nv_and_or_b32 v94, v94, -2, v12\nv_smfmac_f32_16x16x64_bf16 v[36:39], v[80:83], v[56:63], v10\n" \
"ds_read_b128 v[56:59], v1 offset:8192\nv_permlane16_swap_b32 v93, v95\nv_smfmac_f32_16x16x64_bf16 v[36:39], v[84:87], v[136:143], v10\nds_read_b128 v[60:63], v1 offset:9216\nv_permlane16_swap_b32 v92, v94\nv_smfmac_f32_16x16x64_bf16 v[40:43], v[80:83], v[176:183], v10\ns_add_u32 s63, s59, 131072\nds_read_b128 v[24:27], v11 offset:50176\n" \
"v_cvt_pk_bf16_f32 v88, v32, v33\nv_and_or_b32 v88, v88, -2, v12\nv_smfmac_f32_16x16x64_bf16 v[40:43], v[84:87], v[192:199], v10\ns_waitcnt lgkmcnt(5)\nds_read_b128 v[28:31], v11 offset:52224\nbuffer_store_dwordx4 v[92:95], v6, s[36:39], s63 offen sc1\nv_cvt_pk_bf16_f32 v89, v34, v35\nv_cvt_pk_bf16_f32 v90, v36, v37\n" \
"v_cvt_pk_bf16_f32 v91, v38, v39\nv_and_or_b32 v90, v90, -2, v12\nv_smfmac_f32_16x16x64_bf16 v[44:47], v[80:83], v[64:71], v10\nds_read_b128 v[64:67], v1 offset:16384\nv_permlane16_swap_b32 v89, v91\nv_smfmac_f32_16x16x64_bf16 v[44:47], v[84:87], v[248:255], v10\ns_cmp_ge_i32 s40, 63\nds_read_b128 v[68:71], v1 offset:17408\n" \
"v_permlane16_swap_b32 v88, v90\ns_cbranch_scc1 .Lvu_last%=\n.Lvu_y%=:\nv_smfmac_f32_16x16x64_bf16 v[48:51], v[72:75], a[8:15], v10\ns_waitcnt lgkmcnt(4)\nds_read_b128 v[32:35], v11 offset:54272\nds_read_b128 v[36:39], v11 offset:56320\nv_cvt_pk_bf16_f32 v92, v40, v41\n" \
"v_and_or_b32 v92, v92, -2, v12\nv_smfmac_f32_16x16x64_bf16 v[48:51], v[76:79], v[56:63], v10\ns_add_u32 s63, s59, 4096\nbuffer_store_dwordx4 v[88:91], v6, s[36:39], s63 offen sc1\nv_cvt_pk_bf16_f32 v93, v42, v43\nv_cvt_pk_bf16_f32 v94, v44, v45\nv_cvt_pk_bf16_f32 v95, v46, v47\nv_and_or_b32 v94, v94, -2, v12\n" \
"v_smfmac_f32_16x16x64_bf16 v[48:51], v[16:19], a[32:39], v10\ns_add_u32 s40, s40, 1\ns_and_b32 s44, s40, 1\nv_permlane16_swap_b32 v93, v95\nv_smfmac_f32_16x16x64_bf16 v[48:51], v[20:23], a[48:55], v10\ns_lshl_b32 s55, s44, 22\ns_waitcnt lgkmcnt(5)\nv_permlane16_swap_b32 v92, v94\n" \
"v_smfmac_f32_16x16x64_bf16 v[52:55], v[24:27], a[8:15], v10\ns_waitcnt lgkmcnt(4)\ns_add_u32 s63, s59, 135168\nv_smfmac_f32_16x16x64_bf16 v[52:55], v[28:31], v[56:63], v10\ns_waitcnt lgkmcnt(1)\nbuffer_store_dwordx4 v[92:95], v6, s[36:39], s63 offen sc1\nv_mov_b64 v[80:81], 0\nv_mov_b64 v[82:83], 0\n" \
"v_mov_b64 v[84:85], 0\nv_mov_b64 v[86:87], 0\nv_smfmac_f32_16x16x64_bf16 v[52:55], v[32:35], a[32:39], v10\ns_memrealtime s[86:87]\ns_bfe_u32 s45, s40, 0x10001\ns_waitcnt lgkmcnt(0)\nv_smfmac_f32_16x16x64_bf16 v[52:55], v[36:39], a[48:55], v10\nds_read_b128 v[56:59], v1 offset:12288\n" \
"v_mov_b32 v13, s45\nv_smfmac_f32_16x16x64_bf16 v[80:83], v[72:75], a[56:63], v10\nds_read_b128 v[60:63], v1 offset:13312\nv_cvt_pk_bf16_f32 v88, v48, v49\nv_smfmac_f32_16x16x64_bf16 v[80:83], v[76:79], a[72:79], v10\nv_cvt_pk_bf16_f32 v89, v50, v51\nv_smfmac_f32_16x16x64_bf16 v[80:83], v[16:19], v[64:71], v10\nv_cvt_pk_bf16_f32 v90, v52, v53\n" \
"v_smfmac_f32_16x16x64_bf16 v[80:83], v[20:23], a[96:103], v10\ns_add_u32 s60, s55, 0\nv_cvt_pk_bf16_f32 v91, v54, v55\nv_smfmac_f32_16x16x64_bf16 v[84:87], v[24:27], a[56:63], v10\nv_and_or_b32 v88, v88, -2, v13\nv_smfmac_f32_16x16x64_bf16 v[84:87], v[28:31], a[72:79], v10\ns_and_b32 s42, s40, 3\ns_lshl_b32 s42, s42, 15\n" \
"v_and_or_b32 v90, v90, -2, v13\nv_smfmac_f32_16x16x64_bf16 v[84:87], v[32:35], v[64:71], v10\nbuffer_store_dwordx4 v[88:91], v7, s[36:39], s60 offen offset:0 sc1\nv_mov_b64 v[48:49], 0\nv_mov_b64 v[50:51], 0\nv_mov_b64 v[52:53], 0\nv_mov_b32 v12, s44\nv_smfmac_f32_16x16x64_bf16 v[84:87], v[36:39], a[96:103], v10\n" \
"ds_read_b128 v[64:67], v1 offset:20480\nv_mov_b64 v[54:55], 0\nv_smfmac_f32_16x16x64_bf16 v[48:51], v[72:75], a[112:119], v10\ns_waitcnt lgkmcnt(1)\nds_read_b128 v[68:71], v1 offset:21504\nv_cvt_pk_bf16_f32 v92, v80, v81\nv_smfmac_f32_16x16x64_bf16 v[48:51], v[76:79], v[56:63], v10\nv_cvt_pk_bf16_f32 v93, v82, v83\n" \
"v_smfmac_f32_16x16x64_bf16 v[48:51], v[16:19], a[136:143], v10\nv_cvt_pk_bf16_f32 v94, v84, v85\nv_smfmac_f32_16x16x64_bf16 v[48:51], v[20:23], a[144:151], v10\ns_add_u32 s60, s55, 4096\nv_cvt_pk_bf16_f32 v95, v86, v87\nv_smfmac_f32_16x16x64_bf16 v[52:55], v[24:27], a[112:119], v10\nv_and_or_b32 v92, v92, -2, v13\nv_smfmac_f32_16x16x64_bf16 v[52:55], v[28:31], v[56:63], v10\n" \
"v_and_or_b32 v94, v94, -2, v13\nv_smfmac_f32_16x16x64_bf16 v[52:55], v[32:35], a[136:143], v10\ns_waitcnt vmcnt(5)\nbuffer_store_dwordx4 v[92:95], v7, s[36:39], s60 offen offset:0 sc1\nv_mov_b64 v[80:81], 0\nv_mov_b64 v[82:83], 0\nv_mov_b64 v[84:85], 0\ns_barrier\n" \
"v_smfmac_f32_16x16x64_bf16 v[52:55], v[36:39], a[144:151], v10\nds_read_b128 v[56:59], v1 offset:10240\nv_mov_b64 v[86:87], 0\nv_smfmac_f32_16x16x64_bf16 v[80:83], v[72:75], a[160:167], v10\nds_read_b128 v[60:63], v1 offset:11264\nv_cvt_pk_bf16_f32 v88, v48, v49\nv_smfmac_f32_16x16x64_bf16 v[80:83], v[76:79], a[176:183], v10\ns_waitcnt lgkmcnt(2)\n" \
"v_cvt_pk_bf16_f32 v89, v50, v51\nv_smfmac_f32_16x16x64_bf16 v[80:83], v[16:19], v[64:71], v10\ns_cmp_lt_u32 s40, 2\ns_cselect_b32 s54, %[fy], -2147483648\nv_cvt_pk_bf16_f32 v90, v52, v53\nv_smfmac_f32_16x16x64_bf16 v[80:83], v[20:23], a[200:207], v10\ns_add_u32 s60, s55, 16384\nv_cvt_pk_bf16_f32 v91, v54, v55\n" \
"v_smfmac_f32_16x16x64_bf16 v[84:87], v[24:27], a[160:167], v10\nv_and_or_b32 v88, v88, -2, v13\nv_smfmac_f32_16x16x64_bf16 v[84:87], v[28:31], a[176:183], v10\nv_and_or_b32 v90, v90, -2, v13\nv_smfmac_f32_16x16x64_bf16 v[84:87], v[32:35], v[64:71], v10\nbuffer_store_dwordx4 v[88:91], v7, s[36:39], s60 offen offset:0 sc1\nv_mov_b64 v[48:49], 0\nv_mov_b64 v[50:51], 0\n" \
"v_mov_b64 v[52:53], 0\nv_smfmac_f32_16x16x64_bf16 v[84:87], v[36:39], a[200:207], v10\nds_read_b128 v[64:67], v1 offset:18432\nv_mov_b64 v[54:55], 0\nv_smfmac_f32_16x16x64_bf16 v[48:51], v[72:75], a[208:215], v10\ns_waitcnt lgkmcnt(1)\nds_read_b128 v[68:71], v1 offset:19456\nv_cvt_pk_bf16_f32 v92, v80, v81\n" \
"v_smfmac_f32_16x16x64_bf16 v[48:51], v[76:79], v[56:63], v10\nv_cvt_pk_bf16_f32 v93, v82, v83\nv_smfmac_f32_16x16x64_bf16 v[48:51], v[16:19], a[232:239], v10\nv_cvt_pk_bf16_f32 v94, v84, v85\nv_smfmac_f32_16x16x64_bf16 v[48:51], v[20:23], a[248:255], v10\ns_add_u32 s60, s55, 20480\nv_cvt_pk_bf16_f32 v95, v86, v87\nv_smfmac_f32_16x16x64_bf16 v[52:55], v[24:27], a[208:215], v10\n" \
"v_and_or_b32 v92, v92, -2, v13\nv_smfmac_f32_16x16x64_bf16 v[52:55], v[28:31], v[56:63], v10\nv_and_or_b32 v94, v94, -2, v13\nv_smfmac_f32_16x16x64_bf16 v[52:55], v[32:35], a[232:239], v10\nbuffer_store_dwordx4 v[92:95], v7, s[36:39], s60 offen offset:0 sc1\nv_mov_b64 v[80:81], 0\nv_mov_b64 v[82:83], 0\nv_mov_b64 v[84:85], 0\n" \
"v_smfmac_f32_16x16x64_bf16 v[52:55], v[36:39], a[248:255], v10\nds_read_b128 v[56:59], v1 offset:14336\nv_mov_b64 v[86:87], 0\nv_smfmac_f32_16x16x64_bf16 v[80:83], v[72:75], v[104:111], v10\nds_read_b128 v[60:63], v1 offset:15360\nv_cvt_pk_bf16_f32 v88, v48, v49\nv_smfmac_f32_16x16x64_bf16 v[80:83], v[76:79], v[120:127], v10\ns_waitcnt lgkmcnt(2)\n" \
"v_cvt_pk_bf16_f32 v89, v50, v51\nv_smfmac_f32_16x16x64_bf16 v[80:83], v[16:19], v[64:71], v10\nv_cvt_pk_bf16_f32 v90, v52, v53\nv_smfmac_f32_16x16x64_bf16 v[80:83], v[20:23], v[136:143], v10\ns_add_u32 s60, s55, 0\nv_cvt_pk_bf16_f32 v91, v54, v55\nv_smfmac_f32_16x16x64_bf16 v[84:87], v[24:27], v[104:111], v10\nv_and_or_b32 v88, v88, -2, v13\n" \
"v_smfmac_f32_16x16x64_bf16 v[84:87], v[28:31], v[120:127], v10\nv_and_or_b32 v90, v90, -2, v13\nv_smfmac_f32_16x16x64_bf16 v[84:87], v[32:35], v[64:71], v10\nbuffer_store_dwordx4 v[88:91], v7, s[36:39], s60 offen offset:2048 sc1\nv_xor_b32 v11, 128, v11\nv_mov_b64 v[48:49], 0\nv_mov_b64 v[50:51], 0\nv_mov_b64 v[52:53], 0\n" \
"v_smfmac_f32_16x16x64_bf16 v[84:87], v[36:39], v[136:143], v10\nds_read_b128 v[64:67], v1 offset:22528\nv_mov_b64 v[54:55], 0\nv_smfmac_f32_16x16x64_bf16 v[48:51], v[72:75], v[152:159], v10\ns_waitcnt lgkmcnt(1)\nds_read_b128 v[68:71], v1 offset:23552\nv_cvt_pk_bf16_f32 v92, v80, v81\nv_smfmac_f32_16x16x64_bf16 v[48:51], v[76:79], v[56:63], v10\n" \
"ds_read_b128 v[40:43], v11 offset:49152\nv_cvt_pk_bf16_f32 v93, v82, v83\nv_smfmac_f32_16x16x64_bf16 v[48:51], v[16:19], v[176:183], v10\nds_read_b128 v[44:47], v11 offset:51200\nv_cvt_pk_bf16_f32 v94, v84, v85\nv_smfmac_f32_16x16x64_bf16 v[48:51], v[20:23], v[192:199], v10\ns_add_u32 s60, s55, 4096\nv_cvt_pk_bf16_f32 v95, v86, v87\n" \
"v_smfmac_f32_16x16x64_bf16 v[52:55], v[24:27], v[152:159], v10\nv_and_or_b32 v92, v92, -2, v13\nv_smfmac_f32_16x16x64_bf16 v[52:55], v[28:31], v[56:63], v10\nv_and_or_b32 v94, v94, -2, v13\nv_smfmac_f32_16x16x64_bf16 v[52:55], v[32:35], v[176:183], v10\nbuffer_store_dwordx4 v[92:95], v7, s[36:39], s60 offen offset:2048 sc1\nv_mov_b64 v[80:81], 0\nv_mov_b64 v[82:83], 0\n" \
"v_mov_b64 v[84:85], 0\nv_smfmac_f32_16x16x64_bf16 v[52:55], v[36:39], v[192:199], v10\nds_read_b128 v[56:59], v1 offset:8192\nv_mov_b64 v[86:87], 0\nv_smfmac_f32_16x16x64_bf16 v[80:83], v[72:75], v[208:215], v10\nds_read_b128 v[60:63], v1 offset:9216\nds_read_b128 v[72:75], v11 offset:53248\nv_cvt_pk_bf16_f32 v88, v48, v49\n" \
"v_smfmac_f32_16x16x64_bf16 v[80:83], v[76:79], v[224:231], v10\ns_waitcnt lgkmcnt(5)\nds_read_b128 v[76:79], v11 offset:55296\nv_cvt_pk_bf16_f32 v89, v50, v51\nv_smfmac_f32_16x16x64_bf16 v[80:83], v[16:19], v[64:71], v10\nds_read_b128 v[16:19], v11 offset:50176\nv_cvt_pk_bf16_f32 v90, v52, v53\nv_smfmac_f32_16x16x64_bf16 v[80:83], v[20:23], v[248:255], v10\n" \
"s_add_u32 s60, s55, 16384\nds_read_b128 v[20:23], v11 offset:52224\nv_cvt_pk_bf16_f32 v91, v54, v55\nv_smfmac_f32_16x16x64_bf16 v[84:87], v[24:27], v[208:215], v10\nds_read_b128 v[24:27], v11 offset:54272\nv_and_or_b32 v88, v88, -2, v13\nv_smfmac_f32_16x16x64_bf16 v[84:87], v[28:31], v[224:231], v10\nds_read_b128 v[28:31], v11 offset:56320\n" \
"v_and_or_b32 v90, v90, -2, v13\nv_smfmac_f32_16x16x64_bf16 v[84:87], v[32:35], v[64:71], v10\nbuffer_store_dwordx4 v[88:91], v7, s[36:39], s60 offen offset:2048 sc1\nv_mov_b64 v[48:49], 0\nv_mov_b64 v[50:51], 0\nv_mov_b64 v[52:53], 0\nv_smfmac_f32_16x16x64_bf16 v[84:87], v[36:39], v[248:255], v10\ns_waitcnt lgkmcnt(9)\n" \
"ds_read_b128 v[64:67], v1 offset:16384\nv_mov_b64 v[54:55], 0\nv_smfmac_f32_16x16x64_bf16 v[48:51], v[40:43], a[8:15], v10\ns_waitcnt lgkmcnt(9)\ns_waitcnt lgkmcnt(7)\nds_read_b128 v[68:71], v1 offset:17408\nv_cvt_pk_bf16_f32 v92, v80, v81\nv_smfmac_f32_16x16x64_bf16 v[48:51], v[44:47], v[56:63], v10\n" \
"s_waitcnt lgkmcnt(7)\nv_cvt_pk_bf16_f32 v93, v82, v83\nv_smfmac_f32_16x16x64_bf16 v[48:51], v[72:75], a[32:39], v10\ns_waitcnt lgkmcnt(6)\nv_cvt_pk_bf16_f32 v94, v84, v85\nv_smfmac_f32_16x16x64_bf16 v[48:51], v[76:79], a[48:55], v10\ns_add_u32 s60, s55, 20480\ns_waitcnt lgkmcnt(5)\n" \
"v_cvt_pk_bf16_f32 v95, v86, v87\nv_smfmac_f32_16x16x64_bf16 v[52:55], v[16:19], a[8:15], v10\ns_waitcnt lgkmcnt(4)\nv_and_or_b32 v92, v92, -2, v13\nv_smfmac_f32_16x16x64_bf16 v[52:55], v[20:23], v[56:63], v10\ns_waitcnt lgkmcnt(3)\nv_and_or_b32 v94, v94, -2, v13\nv_smfmac_f32_16x16x64_bf16 v[52:55], v[24:27], a[32:39], v10\n" \
"s_waitcnt lgkmcnt(2)\nbuffer_store_dwordx4 v[92:95], v7, s[36:39], s60 offen offset:2048 sc1\nv_xor_b32 v11, 128, v11\nv_mov_b64 v[80:81], 0\nv_mov_b64 v[82:83], 0\nv_mov_b64 v[84:85], 0\nv_smfmac_f32_16x16x64_bf16 v[52:55], v[28:31], a[48:55], v10\nds_read_b128 v[56:59], v1 offset:12288\n" \
"v_mov_b64 v[86:87], 0\nv_smfmac_f32_16x16x64_bf16 v[80:83], v[40:43], a[56:63], v10\nds_read_b128 v[60:63], v1 offset:13312\nv_cvt_pk_bf16_f32 v88, v48, v49\nv_smfmac_f32_16x16x64_bf16 v[80:83], v[44:47], a[72:79], v10\ns_waitcnt lgkmcnt(2)\nv_cvt_pk_bf16_f32 v89, v50, v51\nv_smfmac_f32_16x16x64_bf16 v[80:83], v[72:75], v[64:71], v10\n" \
"v_cvt_pk_bf16_f32 v90, v52, v53\nv_smfmac_f32_16x16x64_bf16 v[80:83], v[76:79], a[96:103], v10\ns_add_u32 s60, s55, 0\nv_cvt_pk_bf16_f32 v91, v54, v55\nv_smfmac_f32_16x16x64_bf16 v[84:87], v[16:19], a[56:63], v10\nv_and_or_b32 v88, v88, -2, v13\nv_smfmac_f32_16x16x64_bf16 v[84:87], v[20:23], a[72:79], v10\nv_and_or_b32 v90, v90, -2, v13\n" \
"v_smfmac_f32_16x16x64_bf16 v[84:87], v[24:27], v[64:71], v10\nbuffer_store_dwordx4 v[88:91], v7, s[36:39], s60 offen offset:512 sc1\nv_mov_b64 v[48:49], 0\nv_mov_b64 v[50:51], 0\nv_mov_b64 v[52:53], 0\nv_smfmac_f32_16x16x64_bf16 v[84:87], v[28:31], a[96:103], v10\nds_read_b128 v[64:67], v1 offset:20480\nv_mov_b64 v[54:55], 0\n" \
"v_smfmac_f32_16x16x64_bf16 v[48:51], v[40:43], a[112:119], v10\ns_waitcnt lgkmcnt(1)\nds_read_b128 v[68:71], v1 offset:21504\nv_cvt_pk_bf16_f32 v92, v80, v81\nv_smfmac_f32_16x16x64_bf16 v[48:51], v[44:47], v[56:63], v10\nv_cvt_pk_bf16_f32 v93, v82, v83\nv_smfmac_f32_16x16x64_bf16 v[48:51], v[72:75], a[136:143], v10\nv_cvt_pk_bf16_f32 v94, v84, v85\n" \
"v_smfmac_f32_16x16x64_bf16 v[48:51], v[76:79], a[144:151], v10\ns_add_u32 s60, s55, 4096\nv_cvt_pk_bf16_f32 v95, v86, v87\nv_smfmac_f32_16x16x64_bf16 v[52:55], v[16:19], a[112:119], v10\nv_and_or_b32 v92, v92, -2, v13\nv_smfmac_f32_16x16x64_bf16 v[52:55], v[20:23], v[56:63], v10\nv_and_or_b32 v94, v94, -2, v13\nv_smfmac_f32_16x16x64_bf16 v[52:55], v[24:27], a[136:143], v10\n" \
"buffer_store_dwordx4 v[92:95], v7, s[36:39], s60 offen offset:512 sc1\nv_mov_b64 v[80:81], 0\nv_mov_b64 v[82:83], 0\nv_mov_b64 v[84:85], 0\nv_smfmac_f32_16x16x64_bf16 v[52:55], v[28:31], a[144:151], v10\nds_read_b128 v[56:59], v1 offset:10240\nv_mov_b64 v[86:87], 0\nv_smfmac_f32_16x16x64_bf16 v[80:83], v[40:43], a[160:167], v10\n" \
"ds_read_b128 v[60:63], v1 offset:11264\nv_cvt_pk_bf16_f32 v88, v48, v49\nv_smfmac_f32_16x16x64_bf16 v[80:83], v[44:47], a[176:183], v10\ns_waitcnt lgkmcnt(2)\nv_cvt_pk_bf16_f32 v89, v50, v51\nv_smfmac_f32_16x16x64_bf16 v[80:83], v[72:75], v[64:71], v10\nv_cvt_pk_bf16_f32 v90, v52, v53\nv_smfmac_f32_16x16x64_bf16 v[80:83], v[76:79], a[200:207], v10\n" \
"s_add_u32 s60, s55, 16384\nv_cvt_pk_bf16_f32 v91, v54, v55\nv_smfmac_f32_16x16x64_bf16 v[84:87], v[16:19], a[160:167], v10\nv_and_or_b32 v88, v88, -2, v13\nv_smfmac_f32_16x16x64_bf16 v[84:87], v[20:23], a[176:183], v10\nv_and_or_b32 v90, v90, -2, v13\nv_smfmac_f32_16x16x64_bf16 v[84:87], v[24:27], v[64:71], v10\nbuffer_store_dwordx4 v[88:91], v7, s[36:39], s60 offen offset:512 sc1\n" \
"v_mov_b64 v[48:49], 0\nv_mov_b64 v[50:51], 0\nv_mov_b64 v[52:53], 0\nv_smfmac_f32_16x16x64_bf16 v[84:87], v[28:31], a[200:207], v10\nds_read_b128 v[64:67], v1 offset:18432\nv_mov_b64 v[54:55], 0\nv_smfmac_f32_16x16x64_bf16 v[48:51], v[40:43], a[208:215], v10\ns_waitcnt lgkmcnt(1)\n" \
"ds_read_b128 v[68:71], v1 offset:19456\nv_cvt_pk_bf16_f32 v92, v80, v81\nv_smfmac_f32_16x16x64_bf16 v[48:51], v[44:47], v[56:63], v10\nv_cvt_pk_bf16_f32 v93, v82, v83\nv_smfmac_f32_16x16x64_bf16 v[48:51], v[72:75], a[232:239], v10\nv_cvt_pk_bf16_f32 v94, v84, v85\nv_smfmac_f32_16x16x64_bf16 v[48:51], v[76:79], a[248:255], v10\ns_add_u32 s60, s55, 20480\n" \
"v_cvt_pk_bf16_f32 v95, v86, v87\nv_smfmac_f32_16x16x64_bf16 v[52:55], v[16:19], a[208:215], v10\nv_and_or_b32 v92, v92, -2, v13\nv_smfmac_f32_16x16x64_bf16 v[52:55], v[20:23], v[56:63], v10\nv_and_or_b32 v94, v94, -2, v13\nv_smfmac_f32_16x16x64_bf16 v[52:55], v[24:27], a[232:239], v10\nbuffer_store_dwordx4 v[92:95], v7, s[36:39], s60 offen offset:512 sc1\nv_mov_b64 v[80:81], 0\n" \
"v_mov_b64 v[82:83], 0\nv_mov_b64 v[84:85], 0\nv_smfmac_f32_16x16x64_bf16 v[52:55], v[28:31], a[248:255], v10\nds_read_b128 v[56:59], v1 offset:14336\nv_mov_b64 v[86:87], 0\nv_smfmac_f32_16x16x64_bf16 v[80:83], v[40:43], v[104:111], v10\nds_read_b128 v[60:63], v1 offset:15360\nv_cvt_pk_bf16_f32 v88, v48, v49\n" \
"v_smfmac_f32_16x16x64_bf16 v[80:83], v[44:47], v[120:127], v10\ns_waitcnt lgkmcnt(2)\nv_cvt_pk_bf16_f32 v89, v50, v51\nv_smfmac_f32_16x16x64_bf16 v[80:83], v[72:75], v[64:71], v10\nv_cvt_pk_bf16_f32 v90, v52, v53\nv_smfmac_f32_16x16x64_bf16 v[80:83], v[76:79], v[136:143], v10\ns_add_u32 s60, s55, 0\nv_cvt_pk_bf16_f32 v91, v54, v55\n" \
"v_smfmac_f32_16x16x64_bf16 v[84:87], v[16:19], v[104:111], v10\nv_and_or_b32 v88, v88, -2, v13\nv_smfmac_f32_16x16x64_bf16 v[84:87], v[20:23], v[120:127], v10\nv_and_or_b32 v90, v90, -2, v13\nv_smfmac_f32_16x16x64_bf16 v[84:87], v[24:27], v[64:71], v10\nbuffer_store_dwordx4 v[88:91], v7, s[36:39], s60 offen offset:2560 sc1\nv_mov_b64 v[48:49], 0\nv_mov_b64 v[50:51], 0\n" \
"v_mov_b64 v[52:53], 0\nv_smfmac_f32_16x16x64_bf16 v[84:87], v[28:31], v[136:143], v10\nds_read_b128 v[64:67], v1 offset:22528\nv_mov_b64 v[54:55], 0\nv_smfmac_f32_16x16x64_bf16 v[48:51], v[40:43], v[152:159], v10\ns_waitcnt lgkmcnt(1)\nds_read_b128 v[68:71], v1 offset:23552\nv_cvt_pk_bf16_f32 v92, v80, v81\n" \
"v_smfmac_f32_16x16x64_bf16 v[48:51], v[44:47], v[56:63], v10\nv_cvt_pk_bf16_f32 v93, v82, v83\nv_smfmac_f32_16x16x64_bf16 v[48:51], v[72:75], v[176:183], v10\nv_cvt_pk_bf16_f32 v94, v84, v85\nv_smfmac_f32_16x16x64_bf16 v[48:51], v[76:79], v[192:199], v10\ns_add_u32 s60, s55, 4096\nv_cvt_pk_bf16_f32 v95, v86, v87\nv_smfmac_f32_16x16x64_bf16 v[52:55], v[16:19], v[152:159], v10\n" \
"v_and_or_b32 v92, v92, -2, v13\nv_smfmac_f32_16x16x64_bf16 v[52:55], v[20:23], v[56:63], v10\nv_and_or_b32 v94, v94, -2, v13\nv_smfmac_f32_16x16x64_bf16 v[52:55], v[24:27], v[176:183], v10\nbuffer_store_dwordx4 v[92:95], v7, s[36:39], s60 offen offset:2560 sc1\nv_mov_b64 v[80:81], 0\nv_mov_b64 v[82:83], 0\nv_mov_b64 v[84:85], 0\n" \
"v_smfmac_f32_16x16x64_bf16 v[52:55], v[28:31], v[192:199], v10\nds_read_b128 v[32:35], v2 offset:0\nv_mov_b64 v[86:87], 0\nv_smfmac_f32_16x16x64_bf16 v[80:83], v[40:43], v[208:215], v10\nds_read_b128 v[36:39], v2 offset:2048\nv_cvt_pk_bf16_f32 v88, v48, v49\nv_smfmac_f32_16x16x64_bf16 v[80:83], v[44:47], v[224:231], v10\ns_waitcnt lgkmcnt(2)\n" \
"v_cvt_pk_bf16_f32 v89, v50, v51\nv_smfmac_f32_16x16x64_bf16 v[80:83], v[72:75], v[64:71], v10\nds_read_b128 v[40:43], v2 offset:256\nv_cvt_pk_bf16_f32 v90, v52, v53\nv_smfmac_f32_16x16x64_bf16 v[80:83], v[76:79], v[248:255], v10\ns_add_u32 s60, s55, 16384\nds_read_b128 v[44:47], v2 offset:2304\nv_cvt_pk_bf16_f32 v91, v54, v55\n" \
"v_smfmac_f32_16x16x64_bf16 v[84:87], v[16:19], v[208:215], v10\nv_and_or_b32 v88, v88, -2, v13\nv_smfmac_f32_16x16x64_bf16 v[84:87], v[20:23], v[224:231], v10\nv_and_or_b32 v90, v90, -2, v13\nv_smfmac_f32_16x16x64_bf16 v[84:87], v[24:27], v[64:71], v10\nbuffer_store_dwordx4 v[88:91], v7, s[36:39], s60 offen offset:2560 sc1\nv_mov_b64 v[48:49], 0\nv_mov_b64 v[50:51], 0\n" \
"v_mov_b64 v[52:53], 0\nv_mov_b32 v15, s40\nv_smfmac_f32_16x16x64_bf16 v[84:87], v[28:31], v[248:255], v10\ns_add_u32 s60, s55, 20480\ns_cmp_eq_u64 s[64:65], 0\nv_mov_b64 v[54:55], 0\ns_cbranch_scc1 .Lvu_tok%=\nv_cvt_pk_bf16_f32 v92, v80, v81\n" \
"v_cvt_pk_bf16_f32 v93, v82, v83\ns_nop 1\nv_cvt_pk_bf16_f32 v94, v84, v85\nv_cvt_pk_bf16_f32 v95, v86, v87\nv_and_or_b32 v92, v92, -2, v13\nv_and_or_b32 v94, v94, -2, v13\nbuffer_store_dwordx4 v[92:95], v7, s[36:39], s60 offen offset:2560 sc1\ns_mov_b32 s60, -2147483648\n" \
"s_cmp_lt_i32 s40, 63\ns_cbranch_scc1 .Lvu_f0%=\ns_mov_b32 s61, -2147483648\nv_add_u32 v14, s57, v0\ns_waitcnt lgkmcnt(0)\n.Lvu_nov%=:\ns_add_u32 m0, s57, 16384\ns_nop 0\n" \
"buffer_load_dwordx4 v3, s[36:39], s42 offen offset:0 sc1 lds\nbuffer_load_dwordx4 v3, s[36:39], s42 offen offset:1024 sc1 lds\nbuffer_load_dwordx4 v3, s[36:39], s42 offen offset:2048 sc1 lds\nbuffer_load_dwordx4 v3, s[36:39], s42 offen offset:3072 sc1 lds\ns_waitcnt vmcnt(0)\nds_read_b128 v[80:83], v14 offset:16384\nds_read_b128 v[84:87], v14 offset:17408\nds_read_b128 v[88:91], v14 offset:18432\n" \
"ds_read_b128 v[92:95], v14 offset:19456\ns_waitcnt lgkmcnt(2)\nv_max3_u32 v76, v80, v82, v84\ns_waitcnt lgkmcnt(1)\nv_max3_u32 v76, v76, v86, v88\ns_waitcnt lgkmcnt(0)\nv_max3_u32 v76, v76, v90, v92\nv_max_u32 v76, v76, v94\n" \
"v_cmp_eq_u32 vcc, -1, v76\ns_cbranch_vccz .Lvu_novfok%=\ns_add_u32 s43, s43, 1\ns_cmp_lt_u32 s43, 262144\ns_cbranch_scc1 .Lvu_nov%=\ns_branch .Lvu_abort%=\n.Lvu_novfok%=:\ns_waitcnt lgkmcnt(0)\n" \
"s_barrier\ns_memrealtime s[68:69]\nds_read_b128 v[56:59], v1 offset:0\nds_read_b128 v[60:63], v1 offset:1024\nds_read_b128 v[80:83], v0 offset:16384\nds_read_b128 v[84:87], v0 offset:17408\nds_read_b128 v[88:91], v0 offset:18432\nds_read_b128 v[92:95], v0 offset:19456\n" \
"ds_read_b128 v[16:19], v0 offset:20480\nds_read_b128 v[20:23], v0 offset:21504\nds_read_b128 v[40:43], v0 offset:22528\nds_read_b128 v[44:47], v0 offset:23552\ns_waitcnt lgkmcnt(0)\ns_branch .Lvu_crit%=\n.Lvu_re%=:\n.Lvu_re2%=:\n" \
"s_add_u32 m0, s57, 16384\nv_max_u32 v76, v80, v82\nv_cmp_eq_u32 vcc, -1, v76\ns_cbranch_vccz .Lvu_sk1894%=\ns_mov_b64 exec, vcc\nbuffer_load_dwordx4 v3, s[36:39], s42 offen offset:0 sc1 lds\ns_mov_b64 exec, -1\n.Lvu_sk1894%=:\n" \
"v_max_u32 v76, v84, v86\nv_cmp_eq_u32 vcc, -1, v76\ns_cbranch_vccz .Lvu_sk1901%=\ns_mov_b64 exec, vcc\nbuffer_load_dwordx4 v3, s[36:39], s42 offen offset:1024 sc1 lds\ns_mov_b64 exec, -1\n.Lvu_sk1901%=:\nv_max_u32 v76, v88, v90\n" \
"v_cmp_eq_u32 vcc, -1, v76\ns_cbranch_vccz .Lvu_sk1908%=\ns_mov_b64 exec, vcc\nbuffer_load_dwordx4 v3, s[36:39], s42 offen offset:2048 sc1 lds\ns_mov_b64 exec, -1\n.Lvu_sk1908%=:\nv_max_u32 v76, v92, v94\nv_cmp_eq_u32 vcc, -1, v76\n" \
"s_cbranch_vccz .Lvu_sk1915%=\ns_mov_b64 exec, vcc\nbuffer_load_dwordx4 v3, s[36:39], s42 offen offset:3072 sc1 lds\ns_mov_b64 exec, -1\n.Lvu_sk1915%=:\n.Lvu_skf%=:\ns_waitcnt vmcnt(0)\nds_read_b128 v[80:83], v14 offset:16384\n" \
"ds_read_b128 v[84:87], v14 offset:17408\nds_read_b128 v[88:91], v14 offset:18432\nds_read_b128 v[92:95], v14 offset:19456\ns_waitcnt lgkmcnt(2)\nv_max3_u32 v76, v80, v82, v84\ns_waitcnt lgkmcnt(1)\nv_max3_u32 v76, v76, v86, v88\ns_waitcnt lgkmcnt(0)\n" \
"v_max3_u32 v76, v76, v90, v92\nv_max_u32 v76, v76, v94\nv_cmp_eq_u32 vcc, -1, v76\ns_cbranch_vccz .Lvu_rer%=\ns_add_u32 s43, s43, 1\ns_cmp_lt_u32 s43, 262144\ns_cbranch_scc1 .Lvu_re2%=\ns_branch .Lvu_abort%=\n" \
".Lvu_rew%=:\nds_read_b128 v[80:83], v14 offset:16384\nds_read_b128 v[84:87], v14 offset:17408\nds_read_b128 v[88:91], v14 offset:18432\nds_read_b128 v[92:95], v14 offset:19456\ns_waitcnt lgkmcnt(0)\ns_branch .Lvu_rer%=\n.Lvu_first%=:\n" \
"s_nop 7\nv_cvt_pk_bf16_f32 v76, v24, v25\nv_cvt_pk_bf16_f32 v77, v26, v27\nv_cvt_pk_bf16_f32 v78, v28, v29\nv_cvt_pk_bf16_f32 v79, v30, v31\nds_write2st64_b64 v9, v[76:77], v[78:79] offset0:2 offset1:10\ns_waitcnt vmcnt(0) lgkmcnt(0)\ns_barrier\n" \
"ds_read_b128 v[56:59], v1 offset:8192\nds_read_b128 v[60:63], v1 offset:9216\nds_read_b128 v[64:67], v1 offset:16384\nds_read_b128 v[68:71], v1 offset:17408\nds_read_b128 v[72:75], v11 offset:49152\nds_read_b128 v[76:79], v11 offset:51200\nds_read_b128 v[16:19], v11 offset:53248\nds_read_b128 v[20:23], v11 offset:55296\n" \
"ds_read_b128 v[24:27], v11 offset:50176\nds_read_b128 v[28:31], v11 offset:52224\nds_read_b128 v[32:35], v11 offset:54272\nds_read_b128 v[36:39], v11 offset:56320\ns_waitcnt lgkmcnt(0)\ns_add_u32 m0, s57, 0\ns_add_u32 s58, s61, 0\nbuffer_load_dwordx4 v0, s[36:39], s58 offen offset:0 sc1 lds\n" \
"buffer_load_dwordx4 v0, s[36:39], s58 offen offset:1024 sc1 lds\nbuffer_load_dwordx4 v0, s[36:39], s58 offen offset:2048 sc1 lds\nbuffer_load_dwordx4 v0, s[36:39], s58 offen offset:3072 sc1 lds\ns_mov_b32 s59, -2147483648\nbuffer_store_dword v14, off, s[36:39], s59\nbuffer_store_dword v14, off, s[36:39], s59\nbuffer_store_dword v14, off, s[36:39], s59\ns_branch .Lvu_y%=\n" \
".Lvu_last%=:\ns_nop 7\nv_cvt_pk_bf16_f32 v92, v40, v41\nv_and_or_b32 v92, v92, -2, v12\ns_add_u32 s63, s59, 4096\nbuffer_store_dwordx4 v[88:91], v6, s[36:39], s63 offen sc1\nv_cvt_pk_bf16_f32 v93, v42, v43\nv_cvt_pk_bf16_f32 v94, v44, v45\n" \
"v_cvt_pk_bf16_f32 v95, v46, v47\nv_and_or_b32 v94, v94, -2, v12\ns_nop 0\nv_permlane16_swap_b32 v93, v95\nv_permlane16_swap_b32 v92, v94\ns_add_u32 s63, s59, 135168\nbuffer_store_dwordx4 v[92:95], v6, s[36:39], s63 offen sc1\ns_branch .Lvu_end%=\n" \
".Lvu_abort%=:\ns_mov_b64 exec, -1\nv_mov_b32 v16, 0\ns_movk_i32 s44, 6400\nbuffer_store_dword v16, off, s[36:39], s44 sc1\n.Lvu_end%=:\ns_waitcnt vmcnt(0) lgkmcnt(0)\n" \
""
#define RD_ASM \
"v_mbcnt_lo_u32_b32 v255, -1, 0\nv_mbcnt_hi_u32_b32 v255, -1, v255\nv_lshlrev_b32 v255, 2, v255\nv_add_u32 v255, %[tb], v255\nds_read_b32 v196, v255 offset:0\nds_read_b32 v197, v255 offset:256\nds_read_b32 v198, v255 offset:512\nds_read_b32 v199, v255 offset:768\n" \
"ds_read_b32 v200, v255 offset:1024\nds_read_b32 v201, v255 offset:1280\nds_read_b32 v202, v255 offset:1536\nds_read_b32 v203, v255 offset:1792\nds_read_b32 v204, v255 offset:2048\nds_read_b32 v205, v255 offset:2304\nds_read_b32 v206, v255 offset:2560\nds_read_b32 v207, v255 offset:2816\n" \
"s_waitcnt lgkmcnt(0)\nv_accvgpr_write_b32 a32, v206\nv_accvgpr_write_b32 a33, v207\ns_mov_b32 s40, 0\ns_mov_b32 s43, 0\ns_add_u32 s57, %[su], 4096\ns_add_u32 s56, %[sp], 4096\ns_mov_b32 s41, %[sy]\n" \
"s_mov_b32 s59, -2147483648\n.Lrd_yflag%=:\nv_accvgpr_read_b32 v206, a32\nbuffer_load_dword v208, v206, %[rs], 0 offen sc1\ns_waitcnt vmcnt(0)\nv_cmp_gt_i32 vcc, s40, v208\ns_cbranch_vccz .Lrd_yld%=\ns_add_u32 s43, s43, 1\n" \
"s_cmp_lt_u32 s43, 262144\ns_cbranch_scc1 .Lrd_yflag%=\ns_branch .Lrd_abort%=\n.Lrd_yld%=:\ns_add_u32 s60, s41, 0\nbuffer_load_dwordx4 v[0:3], v196, %[rs], s60 offen offset:0 sc1\ns_add_u32 s60, s41, 0\nbuffer_load_dwordx4 v[32:35], v196, %[rs], s60 offen offset:1024 sc1\n" \
"s_add_u32 s60, s41, 0\nbuffer_load_dwordx4 v[64:67], v196, %[rs], s60 offen offset:2048 sc1\ns_add_u32 s60, s41, 131072\nbuffer_load_dwordx4 v[4:7], v196, %[rs], s60 offen offset:0 sc1\ns_add_u32 s60, s41, 131072\nbuffer_load_dwordx4 v[36:39], v196, %[rs], s60 offen offset:1024 sc1\ns_add_u32 s60, s41, 131072\nbuffer_load_dwordx4 v[68:71], v196, %[rs], s60 offen offset:2048 sc1\n" \
"s_add_u32 s60, s41, 262144\nbuffer_load_dwordx4 v[8:11], v196, %[rs], s60 offen offset:0 sc1\ns_add_u32 s60, s41, 262144\nbuffer_load_dwordx4 v[40:43], v196, %[rs], s60 offen offset:1024 sc1\ns_add_u32 s60, s41, 262144\nbuffer_load_dwordx4 v[72:75], v196, %[rs], s60 offen offset:2048 sc1\ns_add_u32 s60, s41, 393216\nbuffer_load_dwordx4 v[12:15], v196, %[rs], s60 offen offset:0 sc1\n" \
"s_add_u32 s60, s41, 393216\nbuffer_load_dwordx4 v[44:47], v196, %[rs], s60 offen offset:1024 sc1\ns_add_u32 s60, s41, 393216\nbuffer_load_dwordx4 v[76:79], v196, %[rs], s60 offen offset:2048 sc1\ns_add_u32 s60, s41, 524288\nbuffer_load_dwordx4 v[16:19], v196, %[rs], s60 offen offset:0 sc1\ns_add_u32 s60, s41, 524288\nbuffer_load_dwordx4 v[48:51], v196, %[rs], s60 offen offset:1024 sc1\n" \
"s_add_u32 s60, s41, 524288\nbuffer_load_dwordx4 v[80:83], v196, %[rs], s60 offen offset:2048 sc1\ns_add_u32 s60, s41, 655360\nbuffer_load_dwordx4 v[20:23], v196, %[rs], s60 offen offset:0 sc1\ns_add_u32 s60, s41, 655360\nbuffer_load_dwordx4 v[52:55], v196, %[rs], s60 offen offset:1024 sc1\ns_add_u32 s60, s41, 655360\nbuffer_load_dwordx4 v[84:87], v196, %[rs], s60 offen offset:2048 sc1\n" \
"s_add_u32 s60, s41, 786432\nbuffer_load_dwordx4 v[24:27], v196, %[rs], s60 offen offset:0 sc1\ns_add_u32 s60, s41, 786432\nbuffer_load_dwordx4 v[56:59], v196, %[rs], s60 offen offset:1024 sc1\ns_add_u32 s60, s41, 786432\nbuffer_load_dwordx4 v[88:91], v196, %[rs], s60 offen offset:2048 sc1\ns_add_u32 s60, s41, 917504\nbuffer_load_dwordx4 v[28:31], v196, %[rs], s60 offen offset:0 sc1\n" \
"s_add_u32 s60, s41, 917504\nbuffer_load_dwordx4 v[60:63], v196, %[rs], s60 offen offset:1024 sc1\ns_add_u32 s60, s41, 917504\nbuffer_load_dwordx4 v[92:95], v196, %[rs], s60 offen offset:2048 sc1\ns_add_u32 s60, s41, 0\nbuffer_load_dwordx4 v[96:99], v196, %[rs], s60 offen offset:3072 sc1\ns_add_u32 s60, s41, 131072\nbuffer_load_dwordx4 v[100:103], v196, %[rs], s60 offen offset:3072 sc1\n" \
"s_add_u32 s60, s41, 262144\nbuffer_load_dwordx4 v[104:107], v196, %[rs], s60 offen offset:3072 sc1\ns_add_u32 s60, s41, 393216\nbuffer_load_dwordx4 v[108:111], v196, %[rs], s60 offen offset:3072 sc1\ns_add_u32 s60, s41, 524288\nbuffer_load_dwordx4 v[112:115], v196, %[rs], s60 offen offset:3072 sc1\ns_add_u32 s60, s41, 655360\nbuffer_load_dwordx4 v[116:119], v196, %[rs], s60 offen offset:3072 sc1\n" \
"s_add_u32 s60, s41, 786432\nbuffer_load_dwordx4 v[120:123], v196, %[rs], s60 offen offset:3072 sc1\ns_add_u32 s60, s41, 917504\nbuffer_load_dwordx4 v[124:127], v196, %[rs], s60 offen offset:3072 sc1\ns_mov_b64 exec, -1\nv_accvgpr_read_b32 v212, a33\nbuffer_load_dwordx2 v[214:215], v212, %[rs], 0 offen sc1\ns_waitcnt vmcnt(0)\n" \
"s_branch .Lrd_ychk%=\n.Lrd_u2%=:\nbuffer_load_dwordx4 v[136:139], v196, %[rs], %[su] offen offset:2048 sc1\nbuffer_load_dwordx4 v[140:143], v196, %[rs], %[su] offen offset:3072 sc1\ns_add_u32 s43, s43, 1\ns_cmp_ge_u32 s43, 262144\ns_cbranch_scc1 .Lrd_abort%=\ns_waitcnt vmcnt(0)\n" \
"v_bitop3_b32 v208, v136, v138, s54 bitop3:0x7e\nv_bitop3_b32 v209, v140, v142, s54 bitop3:0x7e\nv_bitop3_b32 v208, v208, 1, v209 bitop3:0xc8\nv_cmp_ne_u32 vcc, 0, v208\ns_cbranch_vccnz .Lrd_u2%=\n.Lrd_u3%=:\nbuffer_load_dwordx4 v[128:131], v196, %[rs], %[su] offen offset:0 sc1\nbuffer_load_dwordx4 v[132:135], v196, %[rs], %[su] offen offset:1024 sc1\n" \
"buffer_load_dwordx4 v[144:147], v196, %[rs], s57 offen offset:0 sc1\nbuffer_load_dwordx4 v[148:151], v196, %[rs], s57 offen offset:1024 sc1\nbuffer_load_dwordx4 v[152:155], v196, %[rs], s57 offen offset:2048 sc1\nbuffer_load_dwordx4 v[156:159], v196, %[rs], s57 offen offset:3072 sc1\nbuffer_load_dwordx4 v[160:163], v196, %[rs], %[sp] offen offset:0 sc1\nbuffer_load_dwordx4 v[164:167], v196, %[rs], %[sp] offen offset:1024 sc1\nbuffer_load_dwordx4 v[168:171], v196, %[rs], %[sp] offen offset:2048 sc1\nbuffer_load_dwordx4 v[172:175], v196, %[rs], %[sp] offen offset:3072 sc1\n" \
"buffer_load_dwordx4 v[176:179], v196, %[rs], s56 offen offset:0 sc1\n.p2align 6\n.Lrd_ra%=:\ns_waitcnt vmcnt(10)\nv_bitop3_b32 v208, v128, v130, s54 bitop3:0x7e\nv_bitop3_b32 v209, v136, v138, s54 bitop3:0x7e\nv_bitop3_b32 v208, v208, 1, v209 bitop3:0xc8\nv_cmp_ne_u32 s[86:87], 0, v208\n" \
"v_bitop3_b32 v208, v144, v146, s54 bitop3:0x7e\nv_bitop3_b32 v209, v152, v154, s54 bitop3:0x7e\nv_bitop3_b32 v208, v208, 1, v209 bitop3:0xc8\nv_cmp_ne_u32 s[88:89], 0, v208\nv_bitop3_b32 v208, v132, v134, s54 bitop3:0x7e\nv_bitop3_b32 v209, v140, v142, s54 bitop3:0x7e\nv_bitop3_b32 v208, v208, 1, v209 bitop3:0xc8\nv_cmp_ne_u32 s[68:69], 0, v208\n" \
"v_bitop3_b32 v208, v148, v150, s54 bitop3:0x7e\nv_bitop3_b32 v209, v156, v158, s54 bitop3:0x7e\nv_bitop3_b32 v208, v208, 1, v209 bitop3:0xc8\nv_cmp_ne_u32 s[62:63], 0, v208\ns_or_b64 vcc, s[86:87], s[88:89]\ns_or_b64 vcc, vcc, s[68:69]\ns_or_b64 vcc, vcc, s[62:63]\ns_waitcnt vmcnt(9)\n" \
"v_smfmac_f32_16x16x64_bf16 v[180:183], v[160:163], v[0:7], v197\nv_bitop3_b32 v208, v160, v162, s54 bitop3:0x7e\nv_smfmac_f32_16x16x64_bf16 v[184:187], v[160:163], v[32:39], v197\nv_cmp_gt_i32 s[46:47], 0, v208\nv_smfmac_f32_16x16x64_bf16 v[188:191], v[160:163], v[64:71], v197\nv_smfmac_f32_16x16x64_bf16 v[192:195], v[160:163], v[96:103], v197\ns_or_b64 vcc, vcc, s[46:47]\ns_waitcnt vmcnt(8)\n" \
"v_smfmac_f32_16x16x64_bf16 v[180:183], v[164:167], v[8:15], v197\nv_bitop3_b32 v208, v164, v166, s54 bitop3:0x7e\nv_smfmac_f32_16x16x64_bf16 v[184:187], v[164:167], v[40:47], v197\nv_cmp_gt_i32 s[48:49], 0, v208\nv_smfmac_f32_16x16x64_bf16 v[188:191], v[164:167], v[72:79], v197\nv_smfmac_f32_16x16x64_bf16 v[192:195], v[164:167], v[104:111], v197\ns_or_b64 vcc, vcc, s[48:49]\ns_waitcnt vmcnt(7)\n" \
"v_smfmac_f32_16x16x64_bf16 v[180:183], v[168:171], v[16:23], v197\nv_bitop3_b32 v208, v168, v170, s54 bitop3:0x7e\nv_smfmac_f32_16x16x64_bf16 v[184:187], v[168:171], v[48:55], v197\nv_cmp_gt_i32 s[50:51], 0, v208\nv_smfmac_f32_16x16x64_bf16 v[188:191], v[168:171], v[80:87], v197\nv_smfmac_f32_16x16x64_bf16 v[192:195], v[168:171], v[112:119], v197\ns_or_b64 vcc, vcc, s[50:51]\ns_waitcnt vmcnt(6)\n" \
"v_smfmac_f32_16x16x64_bf16 v[180:183], v[172:175], v[24:31], v197\nv_bitop3_b32 v208, v172, v174, s54 bitop3:0x7e\nv_smfmac_f32_16x16x64_bf16 v[184:187], v[172:175], v[56:63], v197\nv_cmp_gt_i32 s[52:53], 0, v208\nv_smfmac_f32_16x16x64_bf16 v[188:191], v[172:175], v[88:95], v197\nv_smfmac_f32_16x16x64_bf16 v[192:195], v[172:175], v[120:127], v197\ns_or_b64 vcc, vcc, s[52:53]\ns_waitcnt vmcnt(5)\n" \
"v_smfmac_f32_16x16x64_bf16 v[180:183], v[176:179], v[128:135], v197\nv_bitop3_b32 v208, v176, v178, s54 bitop3:0x7e\nv_smfmac_f32_16x16x64_bf16 v[184:187], v[176:179], v[136:143], v197\nv_cmp_gt_i32 s[64:65], 0, v208\nv_smfmac_f32_16x16x64_bf16 v[188:191], v[176:179], v[144:151], v197\ns_or_b64 vcc, vcc, s[64:65]\ns_cbranch_scc1 .Lrd_rb%=\nv_smfmac_f32_16x16x64_bf16 v[192:195], v[176:179], v[152:159], v197\n" \
".Lrd_rd0%=:\nv_add_f32 v180, v180, v182\nv_add_f32 v181, v181, v183\nds_write2_b32 v198, v180, v181 offset0:0 offset1:8\nv_add_f32 v184, v184, v186\nv_add_f32 v185, v185, v187\nv_add_f32 v188, v188, v190\nv_add_f32 v189, v189, v191\n" \
"ds_write2_b32 v198, v184, v185 offset0:32 offset1:40\nv_add_f32 v192, v192, v194\nv_add_f32 v193, v193, v195\nds_write2_b32 v198, v188, v189 offset0:64 offset1:72\nds_write2_b32 v198, v192, v193 offset0:96 offset1:104\ns_add_u32 s60, s58, 0\nbuffer_load_dwordx4 v[232:235], v196, %[rs], s60 offen offset:3072 sc1\ns_add_u32 s60, s58, 16384\n" \
"buffer_load_dwordx4 v[236:239], v196, %[rs], s60 offen offset:2048 sc1\ns_waitcnt lgkmcnt(0)\ns_barrier\nds_read_b128 v[180:183], v199 offset:0\nds_read_b128 v[184:187], v199 offset:4096\nds_read_b128 v[188:191], v199 offset:8192\nds_read_b128 v[192:195], v199 offset:12288\ns_and_b32 s44, s54, 0x80008000\n" \
"s_add_u32 s60, s58, 4096\nbuffer_load_dwordx4 v[240:243], v196, %[rs], s60 offen offset:0 sc1\ns_add_u32 s60, s58, 4096\nbuffer_load_dwordx4 v[244:247], v196, %[rs], s60 offen offset:1024 sc1\ns_waitcnt lgkmcnt(2)\nv_pk_add_f32 v[182:183], v[182:183], v[186:187]\nv_pk_add_f32 v[180:181], v[180:181], v[184:185]\ns_waitcnt lgkmcnt(0)\n" \
"v_pk_add_f32 v[186:187], v[188:189], v[192:193]\nv_pk_add_f32 v[184:185], v[190:191], v[194:195]\nv_pk_add_f32 v[180:181], v[180:181], v[186:187]\nv_pk_add_f32 v[182:183], v[182:183], v[184:185]\nv_cvt_pk_bf16_f32 v180, v180, v181\nv_xor_b32 v180, s44, v180\nv_cvt_pk_bf16_f32 v181, v182, v183\nv_min_u32 v180, -2, v180\n" \
"v_xor_b32 v209, s44, v181\nv_or_b32 v208, v180, v201\nv_mov_b32 v211, v209\nv_or_b32 v210, v180, v201\ns_nop 0\nv_permlane32_swap_b32 v209, v211\nv_permlane32_swap_b32 v208, v210\nbuffer_store_dwordx4 v[208:211], v206, %[rs], 0 offen sc1\n" \
"s_add_u32 s60, s58, 16384\nbuffer_load_dwordx4 v[248:251], v196, %[rs], s60 offen offset:3072 sc1\ns_add_u32 s60, s58, 20480\nbuffer_load_dwordx4 v[252:255], v196, %[rs], s60 offen offset:0 sc1\ns_add_u32 s60, s58, 20480\nbuffer_load_dwordx4 v[160:163], v196, %[rs], s60 offen offset:2048 sc1\ns_xor_b32 s41, s41, 4194304\nv_mov_b32 v207, s40\n" \
"s_waitcnt vmcnt(12)\nv_bitop3_b32 v209, v212, v214, s61 bitop3:0x7e\nv_cmp_gt_i32 vcc, 0, v209\ns_cbranch_vccnz .Lrd_tre0%=\n.Lrd_tl0%=:\nv_mfma_f32_16x16x16_bf16 a[0:3], v[0:1], v[212:213], 0\ns_add_u32 s60, s58, 4096\nbuffer_load_dwordx4 v[164:167], v196, %[rs], s60 offen offset:2048 sc1\n" \
"v_mfma_f32_16x16x16_bf16 a[8:11], v[32:33], v[212:213], 0\nv_mfma_f32_16x16x16_bf16 a[16:19], v[64:65], v[212:213], 0\nv_mfma_f32_16x16x16_bf16 a[24:27], v[96:97], v[212:213], 0\ns_waitcnt vmcnt(12)\nv_mfma_f32_16x16x16_bf16 a[0:3], v[4:5], v[214:215], a[0:3]\ns_add_u32 s60, s58, 4096\nbuffer_load_dwordx4 v[168:171], v196, %[rs], s60 offen offset:3072 sc1\nv_bitop3_b32 v209, v216, v218, s61 bitop3:0x7e\n" \
"v_mfma_f32_16x16x16_bf16 a[8:11], v[36:37], v[214:215], a[8:11]\ns_lshl_b32 s42, s40, 17\ns_add_u32 s42, s42, %[at]\nv_mfma_f32_16x16x16_bf16 a[16:19], v[68:69], v[214:215], a[16:19]\nv_cmp_gt_i32 vcc, 0, v209\nv_mfma_f32_16x16x16_bf16 a[24:27], v[100:101], v[214:215], a[24:27]\ns_cbranch_vccnz .Lrd_tre1%=\n.Lrd_tl1%=:\n" \
"s_waitcnt vmcnt(12)\nv_mfma_f32_16x16x32_bf16 a[4:7], v[0:3], v[216:219], 0\ns_add_u32 s60, s58, 20480\nbuffer_load_dwordx4 v[172:175], v196, %[rs], s60 offen offset:1024 sc1\nv_bitop3_b32 v209, v220, v222, s61 bitop3:0x7e\nv_mfma_f32_16x16x32_bf16 a[12:15], v[32:35], v[216:219], 0\ns_lshr_b32 s45, s42, 8\ns_and_b32 s45, s45, 0x2000\n" \
"v_mfma_f32_16x16x32_bf16 a[20:23], v[64:67], v[216:219], 0\nv_cmp_gt_i32 vcc, 0, v209\nv_mfma_f32_16x16x32_bf16 a[28:31], v[96:99], v[216:219], 0\ns_cbranch_vccnz .Lrd_tre2%=\n.Lrd_tl2%=:\ns_waitcnt vmcnt(12)\nv_mfma_f32_16x16x32_bf16 a[4:7], v[4:7], v[220:223], a[4:7]\ns_add_u32 s60, s58, 20480\n" \
"buffer_load_dwordx4 v[176:179], v196, %[rs], s60 offen offset:3072 sc1\nv_bitop3_b32 v209, v224, v226, s61 bitop3:0x7e\nv_mfma_f32_16x16x32_bf16 a[12:15], v[36:39], v[220:223], a[12:15]\ns_xor_b32 s42, s42, s45\ns_add_u32 s40, s40, 1\nv_mfma_f32_16x16x32_bf16 a[20:23], v[68:71], v[220:223], a[20:23]\nv_cmp_gt_i32 vcc, 0, v209\nv_mfma_f32_16x16x32_bf16 a[28:31], v[100:103], v[220:223], a[28:31]\n" \
"s_cbranch_vccnz .Lrd_tre3%=\n.Lrd_tl3%=:\nv_mfma_f32_16x16x16_bf16 a[0:3], v[8:9], v[224:225], a[0:3]\ns_cmp_eq_u32 s40, 1\ns_cbranch_scc1 .Lrd_y1%=\n.Lrd_y1r%=:\ns_add_u32 s60, s41, 0\nbuffer_load_dwordx4 v[0:3], v196, %[rs], s60 offen offset:0 sc1\n" \
"v_mfma_f32_16x16x16_bf16 a[8:11], v[40:41], v[224:225], a[8:11]\nv_mfma_f32_16x16x16_bf16 a[16:19], v[72:73], v[224:225], a[16:19]\nbuffer_load_dwordx4 v[212:215], v204, %[rs], s42 offen sc1\nv_mfma_f32_16x16x16_bf16 a[24:27], v[104:105], v[224:225], a[24:27]\ns_waitcnt vmcnt(14)\nv_mfma_f32_16x16x16_bf16 a[0:3], v[12:13], v[226:227], a[0:3]\ns_add_u32 s60, s41, 0\nbuffer_load_dwordx4 v[32:35], v196, %[rs], s60 offen offset:1024 sc1\n" \
"v_bitop3_b32 v209, v228, v230, s61 bitop3:0x7e\nv_mfma_f32_16x16x16_bf16 a[8:11], v[44:45], v[226:227], a[8:11]\ns_lshl_b32 s62, s55, 31\ns_or_b32 s62, s62, 1.0\ns_mov_b32 s63, s62\nv_mfma_f32_16x16x16_bf16 a[16:19], v[76:77], v[226:227], a[16:19]\nv_cmp_gt_i32 vcc, 0, v209\nv_mfma_f32_16x16x16_bf16 a[24:27], v[108:109], v[226:227], a[24:27]\n" \
"s_cbranch_vccnz .Lrd_tre4%=\n.Lrd_tl4%=:\ns_waitcnt vmcnt(14)\nv_mfma_f32_16x16x32_bf16 a[4:7], v[8:11], v[228:231], a[4:7]\ns_add_u32 s60, s41, 0\nbuffer_load_dwordx4 v[64:67], v196, %[rs], s60 offen offset:2048 sc1\nv_bitop3_b32 v209, v232, v234, s61 bitop3:0x7e\nv_mfma_f32_16x16x32_bf16 a[12:15], v[40:43], v[228:231], a[12:15]\n" \
"v_mfma_f32_16x16x32_bf16 a[20:23], v[72:75], v[228:231], a[20:23]\nv_cmp_gt_i32 vcc, 0, v209\nv_mfma_f32_16x16x32_bf16 a[28:31], v[104:107], v[228:231], a[28:31]\ns_cbranch_vccnz .Lrd_tre5%=\n.Lrd_tl5%=:\ns_waitcnt vmcnt(14)\nv_mfma_f32_16x16x32_bf16 a[4:7], v[12:15], v[232:235], a[4:7]\ns_add_u32 s60, s41, 131072\n" \
"buffer_load_dwordx4 v[4:7], v196, %[rs], s60 offen offset:0 sc1\nv_bitop3_b32 v209, v236, v238, s61 bitop3:0x7e\nv_mfma_f32_16x16x32_bf16 a[12:15], v[44:47], v[232:235], a[12:15]\nv_mfma_f32_16x16x32_bf16 a[20:23], v[76:79], v[232:235], a[20:23]\nv_cmp_gt_i32 vcc, 0, v209\nv_mfma_f32_16x16x32_bf16 a[28:31], v[108:111], v[232:235], a[28:31]\ns_cbranch_vccnz .Lrd_tre6%=\n.Lrd_tl6%=:\n" \
"v_mfma_f32_16x16x16_bf16 a[0:3], v[16:17], v[236:237], a[0:3]\ns_add_u32 s60, s41, 131072\nbuffer_load_dwordx4 v[36:39], v196, %[rs], s60 offen offset:1024 sc1\nv_mfma_f32_16x16x16_bf16 a[8:11], v[48:49], v[236:237], a[8:11]\nv_mfma_f32_16x16x16_bf16 a[16:19], v[80:81], v[236:237], a[16:19]\nv_mfma_f32_16x16x16_bf16 a[24:27], v[112:113], v[236:237], a[24:27]\ns_waitcnt vmcnt(15)\nv_mfma_f32_16x16x16_bf16 a[0:3], v[20:21], v[238:239], a[0:3]\n" \
"s_add_u32 s60, s41, 131072\nbuffer_load_dwordx4 v[68:71], v196, %[rs], s60 offen offset:2048 sc1\nv_bitop3_b32 v209, v240, v242, s61 bitop3:0x7e\nv_mfma_f32_16x16x16_bf16 a[8:11], v[52:53], v[238:239], a[8:11]\nv_mfma_f32_16x16x16_bf16 a[16:19], v[84:85], v[238:239], a[16:19]\nv_cmp_gt_i32 vcc, 0, v209\nv_mfma_f32_16x16x16_bf16 a[24:27], v[116:117], v[238:239], a[24:27]\ns_cbranch_vccnz .Lrd_tre7%=\n" \
".Lrd_tl7%=:\ns_waitcnt vmcnt(15)\nv_mfma_f32_16x16x32_bf16 a[4:7], v[16:19], v[240:243], a[4:7]\ns_add_u32 s60, s41, 262144\nbuffer_load_dwordx4 v[8:11], v196, %[rs], s60 offen offset:0 sc1\nv_bitop3_b32 v209, v244, v246, s61 bitop3:0x7e\nv_mfma_f32_16x16x32_bf16 a[12:15], v[48:51], v[240:243], a[12:15]\nv_mfma_f32_16x16x32_bf16 a[20:23], v[80:83], v[240:243], a[20:23]\n" \
"v_cmp_gt_i32 vcc, 0, v209\nv_mfma_f32_16x16x32_bf16 a[28:31], v[112:115], v[240:243], a[28:31]\ns_cbranch_vccnz .Lrd_tre8%=\n.Lrd_tl8%=:\ns_waitcnt vmcnt(14)\nv_mfma_f32_16x16x32_bf16 a[4:7], v[20:23], v[244:247], a[4:7]\ns_add_u32 s60, s41, 262144\nbuffer_load_dwordx4 v[40:43], v196, %[rs], s60 offen offset:1024 sc1\n" \
"v_bitop3_b32 v209, v248, v250, s61 bitop3:0x7e\nv_mfma_f32_16x16x32_bf16 a[12:15], v[52:55], v[244:247], a[12:15]\nv_mfma_f32_16x16x32_bf16 a[20:23], v[84:87], v[244:247], a[20:23]\nv_cmp_gt_i32 vcc, 0, v209\nv_mfma_f32_16x16x32_bf16 a[28:31], v[116:119], v[244:247], a[28:31]\ns_cbranch_vccnz .Lrd_tre9%=\n.Lrd_tl9%=:\nv_mfma_f32_16x16x16_bf16 a[0:3], v[24:25], v[248:249], a[0:3]\n" \
"s_add_u32 s60, s41, 262144\nbuffer_load_dwordx4 v[72:75], v196, %[rs], s60 offen offset:2048 sc1\nv_mfma_f32_16x16x16_bf16 a[8:11], v[56:57], v[248:249], a[8:11]\nv_mfma_f32_16x16x16_bf16 a[16:19], v[88:89], v[248:249], a[16:19]\nv_mfma_f32_16x16x16_bf16 a[24:27], v[120:121], v[248:249], a[24:27]\ns_waitcnt vmcnt(15)\nv_mfma_f32_16x16x16_bf16 a[0:3], v[28:29], v[250:251], a[0:3]\ns_add_u32 s60, s41, 393216\n" \
"buffer_load_dwordx4 v[12:15], v196, %[rs], s60 offen offset:0 sc1\nv_bitop3_b32 v209, v252, v254, s61 bitop3:0x7e\nv_mfma_f32_16x16x16_bf16 a[8:11], v[60:61], v[250:251], a[8:11]\nv_mfma_f32_16x16x16_bf16 a[16:19], v[92:93], v[250:251], a[16:19]\nv_cmp_gt_i32 vcc, 0, v209\nv_mfma_f32_16x16x16_bf16 a[24:27], v[124:125], v[250:251], a[24:27]\ns_cbranch_vccnz .Lrd_tre10%=\n.Lrd_tl10%=:\n" \
"s_waitcnt vmcnt(15)\nv_mfma_f32_16x16x32_bf16 a[0:3], v[128:131], v[252:255], a[0:3]\ns_add_u32 s60, s41, 393216\nbuffer_load_dwordx4 v[44:47], v196, %[rs], s60 offen offset:1024 sc1\nv_bitop3_b32 v209, v160, v162, s61 bitop3:0x7e\nv_mfma_f32_16x16x32_bf16 a[8:11], v[136:139], v[252:255], a[8:11]\nv_mfma_f32_16x16x32_bf16 a[16:19], v[144:147], v[252:255], a[16:19]\nv_cmp_gt_i32 vcc, 0, v209\n" \
"v_mfma_f32_16x16x32_bf16 a[24:27], v[152:155], v[252:255], a[24:27]\ns_cbranch_vccnz .Lrd_tre11%=\n.Lrd_tl11%=:\ns_waitcnt vmcnt(15)\nv_mfma_f32_16x16x32_bf16 a[0:3], v[132:135], v[160:163], a[0:3]\ns_add_u32 s60, s41, 393216\nbuffer_load_dwordx4 v[76:79], v196, %[rs], s60 offen offset:2048 sc1\nv_bitop3_b32 v209, v164, v166, s61 bitop3:0x7e\n" \
"v_mfma_f32_16x16x32_bf16 a[8:11], v[140:143], v[160:163], a[8:11]\nv_mfma_f32_16x16x32_bf16 a[16:19], v[148:151], v[160:163], a[16:19]\nv_cmp_gt_i32 vcc, 0, v209\nv_mfma_f32_16x16x32_bf16 a[24:27], v[156:159], v[160:163], a[24:27]\ns_cbranch_vccnz .Lrd_tre12%=\n.Lrd_tl12%=:\ns_waitcnt vmcnt(15)\nv_mfma_f32_16x16x32_bf16 a[4:7], v[24:27], v[164:167], a[4:7]\n" \
"s_add_u32 s60, s41, 524288\nbuffer_load_dwordx4 v[16:19], v196, %[rs], s60 offen offset:0 sc1\nv_bitop3_b32 v209, v168, v170, s61 bitop3:0x7e\nv_mfma_f32_16x16x32_bf16 a[12:15], v[56:59], v[164:167], a[12:15]\nv_mfma_f32_16x16x32_bf16 a[20:23], v[88:91], v[164:167], a[20:23]\nds_write_b128 v202, a[0:3] offset:0\nv_cmp_gt_i32 vcc, 0, v209\nv_mfma_f32_16x16x32_bf16 a[28:31], v[120:123], v[164:167], a[28:31]\n" \
"s_cbranch_vccnz .Lrd_tre13%=\n.Lrd_tl13%=:\ns_waitcnt vmcnt(15)\nv_mfma_f32_16x16x32_bf16 a[4:7], v[28:31], v[168:171], a[4:7]\ns_add_u32 s60, s41, 524288\nbuffer_load_dwordx4 v[48:51], v196, %[rs], s60 offen offset:1024 sc1\nv_bitop3_b32 v209, v172, v174, s61 bitop3:0x7e\nv_mfma_f32_16x16x32_bf16 a[12:15], v[60:63], v[168:171], a[12:15]\n" \
"v_mfma_f32_16x16x32_bf16 a[20:23], v[92:95], v[168:171], a[20:23]\nds_write_b128 v202, a[8:11] offset:2048\nv_cmp_gt_i32 vcc, 0, v209\nv_mfma_f32_16x16x32_bf16 a[28:31], v[124:127], v[168:171], a[28:31]\ns_cbranch_vccnz .Lrd_tre14%=\n.Lrd_tl14%=:\ns_waitcnt vmcnt(15)\nv_mfma_f32_16x16x32_bf16 a[4:7], v[128:131], v[172:175], a[4:7]\n" \
"s_add_u32 s60, s41, 524288\nbuffer_load_dwordx4 v[80:83], v196, %[rs], s60 offen offset:2048 sc1\nv_bitop3_b32 v209, v176, v178, s61 bitop3:0x7e\nv_mfma_f32_16x16x32_bf16 a[12:15], v[136:139], v[172:175], a[12:15]\nv_mfma_f32_16x16x32_bf16 a[20:23], v[144:147], v[172:175], a[20:23]\nds_write_b128 v202, a[16:19] offset:4096\nv_cmp_gt_i32 vcc, 0, v209\nv_mfma_f32_16x16x32_bf16 a[28:31], v[152:155], v[172:175], a[28:31]\n" \
"s_cbranch_vccnz .Lrd_tre15%=\n.Lrd_tl15%=:\nv_mfma_f32_16x16x32_bf16 a[4:7], v[132:135], v[176:179], a[4:7]\ns_add_u32 s60, s41, 655360\ns_waitcnt vmcnt(14)\nbuffer_load_dwordx4 v[20:23], v196, %[rs], s60 offen offset:0 sc1\nv_lshlrev_b32 v216, 16, v212\nv_and_b32 v217, 0xffff0000, v212\n" \
"v_mfma_f32_16x16x32_bf16 a[12:15], v[140:143], v[176:179], a[12:15]\nv_lshlrev_b32 v218, 16, v213\nv_and_b32 v219, 0xffff0000, v213\nv_mfma_f32_16x16x32_bf16 a[20:23], v[148:151], v[176:179], a[20:23]\nds_write_b128 v202, a[24:27] offset:6144\nv_lshlrev_b32 v220, 16, v214\nv_and_b32 v221, 0xffff0000, v214\nv_mfma_f32_16x16x32_bf16 a[28:31], v[156:159], v[176:179], a[28:31]\n" \
"s_add_u32 s60, s41, 655360\nbuffer_load_dwordx4 v[52:55], v196, %[rs], s60 offen offset:1024 sc1\ns_add_u32 s60, s41, 655360\nds_write_b128 v202, a[4:7] offset:1024\nbuffer_load_dwordx4 v[84:87], v196, %[rs], s60 offen offset:2048 sc1\nds_write_b128 v202, a[12:15] offset:3072\nv_lshlrev_b32 v222, 16, v215\nv_and_b32 v223, 0xffff0000, v215\n" \
"s_add_u32 s60, s41, 786432\nbuffer_load_dwordx4 v[24:27], v196, %[rs], s60 offen offset:0 sc1\nds_write_b128 v202, a[20:23] offset:5120\ns_add_u32 s60, s41, 786432\nbuffer_load_dwordx4 v[56:59], v196, %[rs], s60 offen offset:1024 sc1\nds_write_b128 v202, a[28:31] offset:7168\ns_waitcnt lgkmcnt(0)\ns_barrier\n" \
"ds_read_b128 v[224:227], v203 offset:0\nds_read_b128 v[228:231], v203 offset:256\nds_read_b128 v[232:235], v203 offset:8192\nds_read_b128 v[236:239], v203 offset:8448\nds_read_b128 v[240:243], v203 offset:16384\nds_read_b128 v[244:247], v203 offset:16640\nds_read_b128 v[248:251], v203 offset:24576\nds_read_b128 v[252:255], v203 offset:24832\n" \
"s_add_u32 s60, s41, 786432\nbuffer_load_dwordx4 v[88:91], v196, %[rs], s60 offen offset:2048 sc1\ns_waitcnt lgkmcnt(4)\nv_pk_add_f32 v[224:225], v[224:225], v[232:233]\nv_pk_add_f32 v[226:227], v[226:227], v[234:235]\nv_pk_add_f32 v[228:229], v[228:229], v[236:237]\nv_pk_add_f32 v[230:231], v[230:231], v[238:239]\ns_add_u32 s60, s41, 917504\n" \
"buffer_load_dwordx4 v[28:31], v196, %[rs], s60 offen offset:0 sc1\ns_waitcnt lgkmcnt(2)\nv_pk_add_f32 v[224:225], v[224:225], v[240:241]\nv_pk_add_f32 v[226:227], v[226:227], v[242:243]\nv_pk_add_f32 v[228:229], v[228:229], v[244:245]\nv_pk_add_f32 v[230:231], v[230:231], v[246:247]\ns_add_u32 s60, s41, 917504\nbuffer_load_dwordx4 v[60:63], v196, %[rs], s60 offen offset:1024 sc1\n" \
"s_waitcnt lgkmcnt(0)\nv_pk_add_f32 v[224:225], v[224:225], v[248:249]\nv_pk_add_f32 v[226:227], v[226:227], v[250:251]\nv_pk_add_f32 v[228:229], v[228:229], v[252:253]\nv_pk_add_f32 v[230:231], v[230:231], v[254:255]\nv_pk_fma_f32 v[224:225], v[224:225], s[62:63], v[216:217]\nv_pk_fma_f32 v[226:227], v[226:227], s[62:63], v[218:219]\nv_pk_fma_f32 v[228:229], v[228:229], s[62:63], v[220:221]\n" \
"v_pk_fma_f32 v[230:231], v[230:231], s[62:63], v[222:223]\nv_cvt_pk_bf16_f32 v208, v224, v225\nv_cvt_pk_bf16_f32 v209, v226, v227\nv_cvt_pk_bf16_f32 v210, v228, v229\nv_cvt_pk_bf16_f32 v211, v230, v231\nbuffer_store_dwordx4 v[208:211], v204, %[rs], s42 offen sc1\nv_pk_add_f32 v[232:233], v[224:225], v[226:227]\nv_pk_add_f32 v[234:235], v[228:229], v[230:231]\n" \
"v_pk_add_f32 v[232:233], v[232:233], v[234:235]\ns_add_u32 s60, s41, 917504\nbuffer_load_dwordx4 v[92:95], v196, %[rs], s60 offen offset:2048 sc1\ns_add_u32 s60, s41, 0\nbuffer_load_dwordx4 v[96:99], v196, %[rs], s60 offen offset:3072 sc1\nv_add_f32 v232, v232, v233\ns_nop 1\nv_add_f32_dpp v233, v232, v232 row_ror:8 row_mask:0xf bank_mask:0xf\n" \
"v_add_f32_dpp v234, v232, v232 row_ror:8 row_mask:0xf bank_mask:0xf\ns_nop 1\nv_permlane16_swap_b32 v233, v234\nv_add_f32 v232, v233, v234\nv_add_f32 v233, v233, v234\ns_nop 1\nv_permlane32_swap_b32 v232, v233\nv_add_f32 v232, v232, v233\n" \
"v_mul_f32 v232, 0x3c800000, v232\nv_pk_add_f32 v[224:225], v[224:225], v[232:233] op_sel_hi:[1,0] neg_lo:[0,1] neg_hi:[0,1]\nv_pk_add_f32 v[226:227], v[226:227], v[232:233] op_sel_hi:[1,0] neg_lo:[0,1] neg_hi:[0,1]\nv_pk_add_f32 v[228:229], v[228:229], v[232:233] op_sel_hi:[1,0] neg_lo:[0,1] neg_hi:[0,1]\nv_pk_add_f32 v[230:231], v[230:231], v[232:233] op_sel_hi:[1,0] neg_lo:[0,1] neg_hi:[0,1]\ns_add_u32 s60, s41, 131072\nbuffer_load_dwordx4 v[100:103], v196, %[rs], s60 offen offset:3072 sc1\nv_accvgpr_read_b32 v212, a33\n" \
"buffer_load_dwordx2 v[214:215], v212, %[rs], 0 offen sc1\nv_pk_mul_f32 v[236:237], v[224:225], v[224:225]\nv_pk_mul_f32 v[238:239], v[226:227], v[226:227]\nv_pk_fma_f32 v[236:237], v[228:229], v[228:229], v[236:237]\nv_pk_fma_f32 v[238:239], v[230:231], v[230:231], v[238:239]\nv_pk_add_f32 v[236:237], v[236:237], v[238:239]\nv_add_f32 v236, v236, v237\ns_nop 1\n" \
"v_add_f32_dpp v237, v236, v236 row_ror:8 row_mask:0xf bank_mask:0xf\nv_add_f32_dpp v238, v236, v236 row_ror:8 row_mask:0xf bank_mask:0xf\ns_nop 1\nv_permlane16_swap_b32 v237, v238\nv_add_f32 v236, v237, v238\nv_add_f32 v237, v237, v238\ns_nop 1\nv_permlane32_swap_b32 v236, v237\n" \
"v_add_f32 v236, v236, v237\nv_mov_b32 v233, v236\nbuffer_store_dwordx2 v[232:233], v205, %[rs], s42 offen sc1\ns_mov_b32 s59, %[fa]\ns_add_u32 s60, s41, 262144\nbuffer_load_dwordx4 v[104:107], v196, %[rs], s60 offen offset:3072 sc1\ns_add_u32 s60, s41, 393216\nbuffer_load_dwordx4 v[108:111], v196, %[rs], s60 offen offset:3072 sc1\n" \
"s_add_u32 s60, s41, 524288\nbuffer_load_dwordx4 v[112:115], v196, %[rs], s60 offen offset:3072 sc1\ns_add_u32 s60, s41, 655360\nbuffer_load_dwordx4 v[116:119], v196, %[rs], s60 offen offset:3072 sc1\ns_add_u32 s60, s41, 786432\nbuffer_load_dwordx4 v[120:123], v196, %[rs], s60 offen offset:3072 sc1\ns_add_u32 s60, s41, 917504\nbuffer_load_dwordx4 v[124:127], v196, %[rs], s60 offen offset:3072 sc1\n" \
"s_cmp_ge_u32 s40, 64\ns_cbranch_scc1 .Lrd_fin%=\n.Lrd_ychk%=:\ns_and_b32 s44, s40, 1\ns_bfe_u32 s55, s40, 0x10001\ns_mul_i32 s54, s44, 0x80008001\ns_mul_i32 s61, s55, 0x80008000\ns_lshl_b32 s58, s44, 17\n" \
"s_add_u32 s58, s58, %[st]\ns_add_u32 s44, s40, 1\ns_and_b32 s44, s44, 3\ns_lshl_b32 s44, s44, 15\nv_xor_b32 v206, s44, v200\nv_mov_b64 v[180:181], 0\nv_mov_b64 v[182:183], 0\nv_mov_b64 v[184:185], 0\n" \
"v_mov_b64 v[186:187], 0\nv_mov_b64 v[188:189], 0\nv_mov_b64 v[190:191], 0\nv_mov_b64 v[192:193], 0\nv_mov_b64 v[194:195], 0\ns_waitcnt vmcnt(7)\n.Lrd_sg0%=:\ns_memrealtime s[68:69]\n" \
"v_cmp_ne_u32 vcc, s40, v214\ns_cbranch_vccnz .Lrd_sg1%=\nv_max_u32_dpp v215, v215, v215 quad_perm:[1,0,3,2] row_mask:0xf bank_mask:0xf\ns_nop 1\nv_max_u32_dpp v215, v215, v215 quad_perm:[2,3,0,1] row_mask:0xf bank_mask:0xf\ns_nop 1\nv_max_u32_dpp v215, v215, v215 row_half_mirror row_mask:0xf bank_mask:0xf\ns_nop 0\n" \
"v_readlane_b32 s86, v215, 0\nv_readlane_b32 s87, v215, 8\ns_add_u32 s86, s86, %[du]\n.Lrd_tw608%=:\ns_waitcnt lgkmcnt(0)\ns_sub_u32 s44, s86, s68\ns_add_u32 s44, s44, 11\ns_cmp_lt_u32 s44, 250\n" \
"s_cbranch_scc1 .Lrd_tw608x%=\nbuffer_load_dwordx4 v[128:131], v196, %[rs], %[su] offen offset:0 sc1\nbuffer_load_dwordx4 v[132:135], v196, %[rs], %[su] offen offset:1024 sc1\ns_memrealtime s[68:69]\nbuffer_load_dwordx4 v[136:139], v196, %[rs], %[su] offen offset:2048 sc1\nbuffer_load_dwordx4 v[140:143], v196, %[rs], %[su] offen offset:3072 sc1\n.Lrd_tw619%=:\ns_waitcnt lgkmcnt(0)\n" \
"s_sub_u32 s44, s86, s68\ns_add_u32 s44, s44, 21\ns_cmp_lt_u32 s44, 250\ns_cbranch_scc1 .Lrd_tw619x%=\nbuffer_load_dwordx4 v[144:147], v196, %[rs], s57 offen offset:0 sc1\nbuffer_load_dwordx4 v[148:151], v196, %[rs], s57 offen offset:1024 sc1\ns_memrealtime s[68:69]\nbuffer_load_dwordx4 v[152:155], v196, %[rs], s57 offen offset:2048 sc1\n" \
"buffer_load_dwordx4 v[156:159], v196, %[rs], s57 offen offset:3072 sc1\ns_waitcnt vmcnt(14)\nbuffer_store_dword v207, off, %[rs], s59 sc1\ns_mov_b32 s59, -2147483648\n.Lrd_tw633%=:\ns_waitcnt lgkmcnt(0)\ns_sub_u32 s44, s87, s68\ns_add_u32 s44, s44, 24\n" \
"s_cmp_lt_u32 s44, 250\ns_cbranch_scc1 .Lrd_tw633x%=\nbuffer_load_dwordx4 v[160:163], v196, %[rs], %[sp] offen offset:0 sc1\nbuffer_load_dwordx4 v[164:167], v196, %[rs], %[sp] offen offset:1024 sc1\nbuffer_load_dwordx4 v[168:171], v196, %[rs], %[sp] offen offset:2048 sc1\nbuffer_load_dwordx4 v[172:175], v196, %[rs], %[sp] offen offset:3072 sc1\nbuffer_load_dwordx4 v[176:179], v196, %[rs], s56 offen offset:0 sc1\ns_add_u32 s60, s58, 16384\n" \
"buffer_load_dwordx4 v[212:215], v196, %[rs], s60 offen offset:0 sc1\nv_bitop3_b32 v209, v0, v2, s55 bitop3:0x7e\nv_bitop3_b32 v210, v32, v34, s55 bitop3:0x7e\nv_or3_b32 v208, v209, v210, 0\nv_bitop3_b32 v209, v64, v66, s55 bitop3:0x7e\nv_bitop3_b32 v210, v4, v6, s55 bitop3:0x7e\nv_or3_b32 v208, v209, v210, v208\ns_add_u32 s60, s58, 0\n" \
"buffer_load_dwordx4 v[216:219], v196, %[rs], s60 offen offset:0 sc1\nv_bitop3_b32 v209, v36, v38, s55 bitop3:0x7e\nv_bitop3_b32 v210, v68, v70, s55 bitop3:0x7e\nv_or3_b32 v208, v209, v210, v208\nv_bitop3_b32 v209, v8, v10, s55 bitop3:0x7e\nv_bitop3_b32 v210, v40, v42, s55 bitop3:0x7e\nv_or3_b32 v208, v209, v210, v208\ns_add_u32 s60, s58, 0\n" \
"buffer_load_dwordx4 v[220:223], v196, %[rs], s60 offen offset:1024 sc1\nv_bitop3_b32 v209, v72, v74, s55 bitop3:0x7e\nv_bitop3_b32 v210, v12, v14, s55 bitop3:0x7e\nv_or3_b32 v208, v209, v210, v208\nv_bitop3_b32 v209, v44, v46, s55 bitop3:0x7e\nv_bitop3_b32 v210, v76, v78, s55 bitop3:0x7e\nv_or3_b32 v208, v209, v210, v208\ns_add_u32 s60, s58, 16384\n" \
"buffer_load_dwordx4 v[224:227], v196, %[rs], s60 offen offset:1024 sc1\nv_bitop3_b32 v209, v16, v18, s55 bitop3:0x7e\nv_bitop3_b32 v210, v48, v50, s55 bitop3:0x7e\nv_or3_b32 v208, v209, v210, v208\nv_bitop3_b32 v209, v80, v82, s55 bitop3:0x7e\nv_bitop3_b32 v210, v20, v22, s55 bitop3:0x7e\nv_or3_b32 v208, v209, v210, v208\ns_add_u32 s60, s58, 0\n" \
"buffer_load_dwordx4 v[228:231], v196, %[rs], s60 offen offset:2048 sc1\nv_bitop3_b32 v209, v52, v54, s55 bitop3:0x7e\nv_bitop3_b32 v210, v84, v86, s55 bitop3:0x7e\nv_or3_b32 v208, v209, v210, v208\nv_bitop3_b32 v209, v24, v26, s55 bitop3:0x7e\nv_bitop3_b32 v210, v56, v58, s55 bitop3:0x7e\nv_or3_b32 v208, v209, v210, v208\nv_bitop3_b32 v209, v88, v90, s55 bitop3:0x7e\n" \
"v_bitop3_b32 v210, v28, v30, s55 bitop3:0x7e\nv_or3_b32 v208, v209, v210, v208\nv_bitop3_b32 v209, v60, v62, s55 bitop3:0x7e\nv_bitop3_b32 v210, v92, v94, s55 bitop3:0x7e\nv_or3_b32 v208, v209, v210, v208\ns_waitcnt vmcnt(23)\nv_bitop3_b32 v209, v96, v98, s55 bitop3:0x7e\nv_bitop3_b32 v210, v100, v102, s55 bitop3:0x7e\n" \
"v_or3_b32 v208, v209, v210, v208\nv_bitop3_b32 v209, v104, v106, s55 bitop3:0x7e\nv_bitop3_b32 v210, v108, v110, s55 bitop3:0x7e\nv_or3_b32 v208, v209, v210, v208\ns_waitcnt vmcnt(19)\nv_bitop3_b32 v209, v112, v114, s55 bitop3:0x7e\nv_bitop3_b32 v210, v116, v118, s55 bitop3:0x7e\nv_or3_b32 v208, v209, v210, v208\n" \
"v_bitop3_b32 v209, v120, v122, s55 bitop3:0x7e\nv_bitop3_b32 v210, v124, v126, s55 bitop3:0x7e\nv_or3_b32 v208, v209, v210, v208\nv_and_b32 v208, 1, v208\nv_cmp_ne_u32 vcc, 0, v208\ns_cbranch_vccnz .Lrd_yre%=\ns_branch .Lrd_ra%=\n.Lrd_sg1%=:\n" \
"buffer_load_dwordx2 v[214:215], v212, %[rs], 0 offen sc1\ns_waitcnt vmcnt(0)\nbuffer_store_dword v207, off, %[rs], s59 sc1\ns_mov_b32 s59, -2147483648\ns_add_u32 s43, s43, 1\ns_cmp_lt_u32 s43, 262144\ns_cbranch_scc1 .Lrd_sg0%=\ns_branch .Lrd_abort%=\n" \
".Lrd_tw608x%=:\ns_memrealtime s[68:69]\ns_branch .Lrd_tw608%=\n.Lrd_tw619x%=:\ns_memrealtime s[68:69]\ns_branch .Lrd_tw619%=\n.Lrd_tw633x%=:\ns_memrealtime s[68:69]\n" \
"s_branch .Lrd_tw633%=\n.Lrd_fin%=:\ns_waitcnt vmcnt(0)\nbuffer_store_dword v207, off, %[rs], s59 sc1\ns_mov_b32 s59, -2147483648\ns_branch .Lrd_end%=\n.Lrd_yre%=:\ns_mov_b64 exec, vcc\n" \
"s_waitcnt vmcnt(0)\ns_add_u32 s43, s43, 1\ns_cmp_lt_u32 s43, 262144\ns_cbranch_scc1 .Lrd_yld%=\ns_branch .Lrd_abort%=\n.Lrd_rb%=:\ns_mov_b64 exec, s[46:47]\ns_cbranch_execz .Lrd_rs0%=\n" \
"buffer_load_dwordx4 v[160:163], v196, %[rs], %[sp] offen offset:0 sc1\n.Lrd_rs0%=:\ns_mov_b64 exec, s[48:49]\ns_cbranch_execz .Lrd_rs1%=\nbuffer_load_dwordx4 v[164:167], v196, %[rs], %[sp] offen offset:1024 sc1\n.Lrd_rs1%=:\ns_mov_b64 exec, s[50:51]\ns_cbranch_execz .Lrd_rs2%=\n" \
"buffer_load_dwordx4 v[168:171], v196, %[rs], %[sp] offen offset:2048 sc1\n.Lrd_rs2%=:\ns_mov_b64 exec, s[52:53]\ns_cbranch_execz .Lrd_rs3%=\nbuffer_load_dwordx4 v[172:175], v196, %[rs], %[sp] offen offset:3072 sc1\n.Lrd_rs3%=:\ns_mov_b64 exec, s[64:65]\ns_cbranch_execz .Lrd_rs4%=\n" \
"buffer_load_dwordx4 v[176:179], v196, %[rs], s56 offen offset:0 sc1\n.Lrd_rs4%=:\ns_mov_b64 exec, s[86:87]\ns_cbranch_execz .Lrd_rs5%=\nbuffer_load_dwordx4 v[128:131], v196, %[rs], %[su] offen offset:0 sc1\nbuffer_load_dwordx4 v[136:139], v196, %[rs], %[su] offen offset:2048 sc1\n.Lrd_rs5%=:\ns_mov_b64 exec, s[88:89]\n" \
"s_cbranch_execz .Lrd_rs6%=\nbuffer_load_dwordx4 v[144:147], v196, %[rs], s57 offen offset:0 sc1\nbuffer_load_dwordx4 v[152:155], v196, %[rs], s57 offen offset:2048 sc1\n.Lrd_rs6%=:\ns_mov_b64 exec, s[68:69]\ns_cbranch_execz .Lrd_rs7%=\nbuffer_load_dwordx4 v[132:135], v196, %[rs], %[su] offen offset:1024 sc1\nbuffer_load_dwordx4 v[140:143], v196, %[rs], %[su] offen offset:3072 sc1\n" \
".Lrd_rs7%=:\ns_mov_b64 exec, s[62:63]\ns_cbranch_execz .Lrd_rs8%=\nbuffer_load_dwordx4 v[148:151], v196, %[rs], s57 offen offset:1024 sc1\nbuffer_load_dwordx4 v[156:159], v196, %[rs], s57 offen offset:3072 sc1\n.Lrd_rs8%=:\ns_mov_b64 exec, -1\nv_mov_b64 v[180:181], 0\n" \
"v_mov_b64 v[182:183], 0\nv_mov_b64 v[184:185], 0\nv_mov_b64 v[186:187], 0\nv_mov_b64 v[188:189], 0\nv_mov_b64 v[190:191], 0\nv_mov_b64 v[192:193], 0\nv_mov_b64 v[194:195], 0\ns_waitcnt vmcnt(0)\n" \
"s_add_u32 s43, s43, 1\ns_cmp_lt_u32 s43, 262144\ns_cbranch_scc1 .Lrd_ra%=\ns_branch .Lrd_abort%=\n.Lrd_y1%=:\nv_accvgpr_read_b32 v206, a32\nbuffer_load_dword v208, v206, %[rs], 0 offen sc1\ns_waitcnt vmcnt(0)\n" \
"v_cmp_gt_i32 vcc, 1, v208\ns_cbranch_vccz .Lrd_y1r%=\ns_add_u32 s43, s43, 1\ns_cmp_lt_u32 s43, 262144\ns_cbranch_scc1 .Lrd_y1%=\ns_branch .Lrd_abort%=\n.Lrd_tre0%=:\ns_add_u32 s60, s58, 16384\n" \
"buffer_load_dwordx4 v[212:215], v196, %[rs], s60 offen offset:0 sc1\ns_waitcnt vmcnt(0)\nv_bitop3_b32 v209, v212, v214, s61 bitop3:0x7e\nv_cmp_gt_i32 vcc, 0, v209\ns_cbranch_vccz .Lrd_tl0%=\ns_add_u32 s43, s43, 1\ns_cmp_lt_u32 s43, 262144\ns_cbranch_scc1 .Lrd_tre0%=\n" \
"s_branch .Lrd_abort%=\n.Lrd_tre1%=:\ns_add_u32 s60, s58, 0\nbuffer_load_dwordx4 v[216:219], v196, %[rs], s60 offen offset:0 sc1\ns_waitcnt vmcnt(0)\nv_bitop3_b32 v209, v216, v218, s61 bitop3:0x7e\nv_cmp_gt_i32 vcc, 0, v209\ns_cbranch_vccz .Lrd_tl1%=\n" \
"s_add_u32 s43, s43, 1\ns_cmp_lt_u32 s43, 262144\ns_cbranch_scc1 .Lrd_tre1%=\ns_branch .Lrd_abort%=\n.Lrd_tre2%=:\ns_add_u32 s60, s58, 0\nbuffer_load_dwordx4 v[220:223], v196, %[rs], s60 offen offset:1024 sc1\ns_waitcnt vmcnt(0)\n" \
"v_bitop3_b32 v209, v220, v222, s61 bitop3:0x7e\nv_cmp_gt_i32 vcc, 0, v209\ns_cbranch_vccz .Lrd_tl2%=\ns_add_u32 s43, s43, 1\ns_cmp_lt_u32 s43, 262144\ns_cbranch_scc1 .Lrd_tre2%=\ns_branch .Lrd_abort%=\n.Lrd_tre3%=:\n" \
"s_add_u32 s60, s58, 16384\nbuffer_load_dwordx4 v[224:227], v196, %[rs], s60 offen offset:1024 sc1\ns_waitcnt vmcnt(0)\nv_bitop3_b32 v209, v224, v226, s61 bitop3:0x7e\nv_cmp_gt_i32 vcc, 0, v209\ns_cbranch_vccz .Lrd_tl3%=\ns_add_u32 s43, s43, 1\ns_cmp_lt_u32 s43, 262144\n" \
"s_cbranch_scc1 .Lrd_tre3%=\ns_branch .Lrd_abort%=\n.Lrd_tre4%=:\ns_add_u32 s60, s58, 0\nbuffer_load_dwordx4 v[228:231], v196, %[rs], s60 offen offset:2048 sc1\ns_waitcnt vmcnt(0)\nv_bitop3_b32 v209, v228, v230, s61 bitop3:0x7e\nv_cmp_gt_i32 vcc, 0, v209\n" \
"s_cbranch_vccz .Lrd_tl4%=\ns_add_u32 s43, s43, 1\ns_cmp_lt_u32 s43, 262144\ns_cbranch_scc1 .Lrd_tre4%=\ns_branch .Lrd_abort%=\n.Lrd_tre5%=:\ns_add_u32 s60, s58, 0\nbuffer_load_dwordx4 v[232:235], v196, %[rs], s60 offen offset:3072 sc1\n" \
"s_waitcnt vmcnt(0)\nv_bitop3_b32 v209, v232, v234, s61 bitop3:0x7e\nv_cmp_gt_i32 vcc, 0, v209\ns_cbranch_vccz .Lrd_tl5%=\ns_add_u32 s43, s43, 1\ns_cmp_lt_u32 s43, 262144\ns_cbranch_scc1 .Lrd_tre5%=\ns_branch .Lrd_abort%=\n" \
".Lrd_tre6%=:\ns_add_u32 s60, s58, 16384\nbuffer_load_dwordx4 v[236:239], v196, %[rs], s60 offen offset:2048 sc1\ns_waitcnt vmcnt(0)\nv_bitop3_b32 v209, v236, v238, s61 bitop3:0x7e\nv_cmp_gt_i32 vcc, 0, v209\ns_cbranch_vccz .Lrd_tl6%=\ns_add_u32 s43, s43, 1\n" \
"s_cmp_lt_u32 s43, 262144\ns_cbranch_scc1 .Lrd_tre6%=\ns_branch .Lrd_abort%=\n.Lrd_tre7%=:\ns_add_u32 s60, s58, 4096\nbuffer_load_dwordx4 v[240:243], v196, %[rs], s60 offen offset:0 sc1\ns_waitcnt vmcnt(0)\nv_bitop3_b32 v209, v240, v242, s61 bitop3:0x7e\n" \
"v_cmp_gt_i32 vcc, 0, v209\ns_cbranch_vccz .Lrd_tl7%=\ns_add_u32 s43, s43, 1\ns_cmp_lt_u32 s43, 262144\ns_cbranch_scc1 .Lrd_tre7%=\ns_branch .Lrd_abort%=\n.Lrd_tre8%=:\ns_add_u32 s60, s58, 4096\n" \
"buffer_load_dwordx4 v[244:247], v196, %[rs], s60 offen offset:1024 sc1\ns_waitcnt vmcnt(0)\nv_bitop3_b32 v209, v244, v246, s61 bitop3:0x7e\nv_cmp_gt_i32 vcc, 0, v209\ns_cbranch_vccz .Lrd_tl8%=\ns_add_u32 s43, s43, 1\ns_cmp_lt_u32 s43, 262144\ns_cbranch_scc1 .Lrd_tre8%=\n" \
"s_branch .Lrd_abort%=\n.Lrd_tre9%=:\ns_add_u32 s60, s58, 16384\nbuffer_load_dwordx4 v[248:251], v196, %[rs], s60 offen offset:3072 sc1\ns_waitcnt vmcnt(0)\nv_bitop3_b32 v209, v248, v250, s61 bitop3:0x7e\nv_cmp_gt_i32 vcc, 0, v209\ns_cbranch_vccz .Lrd_tl9%=\n" \
"s_add_u32 s43, s43, 1\ns_cmp_lt_u32 s43, 262144\ns_cbranch_scc1 .Lrd_tre9%=\ns_branch .Lrd_abort%=\n.Lrd_tre10%=:\ns_add_u32 s60, s58, 20480\nbuffer_load_dwordx4 v[252:255], v196, %[rs], s60 offen offset:0 sc1\ns_waitcnt vmcnt(0)\n" \
"v_bitop3_b32 v209, v252, v254, s61 bitop3:0x7e\nv_cmp_gt_i32 vcc, 0, v209\ns_cbranch_vccz .Lrd_tl10%=\ns_add_u32 s43, s43, 1\ns_cmp_lt_u32 s43, 262144\ns_cbranch_scc1 .Lrd_tre10%=\ns_branch .Lrd_abort%=\n.Lrd_tre11%=:\n" \
"s_add_u32 s60, s58, 20480\nbuffer_load_dwordx4 v[160:163], v196, %[rs], s60 offen offset:2048 sc1\ns_waitcnt vmcnt(0)\nv_bitop3_b32 v209, v160, v162, s61 bitop3:0x7e\nv_cmp_gt_i32 vcc, 0, v209\ns_cbranch_vccz .Lrd_tl11%=\ns_add_u32 s43, s43, 1\ns_cmp_lt_u32 s43, 262144\n" \
"s_cbranch_scc1 .Lrd_tre11%=\ns_branch .Lrd_abort%=\n.Lrd_tre12%=:\ns_add_u32 s60, s58, 4096\nbuffer_load_dwordx4 v[164:167], v196, %[rs], s60 offen offset:2048 sc1\ns_waitcnt vmcnt(0)\nv_bitop3_b32 v209, v164, v166, s61 bitop3:0x7e\nv_cmp_gt_i32 vcc, 0, v209\n" \
"s_cbranch_vccz .Lrd_tl12%=\ns_add_u32 s43, s43, 1\ns_cmp_lt_u32 s43, 262144\ns_cbranch_scc1 .Lrd_tre12%=\ns_branch .Lrd_abort%=\n.Lrd_tre13%=:\ns_add_u32 s60, s58, 4096\nbuffer_load_dwordx4 v[168:171], v196, %[rs], s60 offen offset:3072 sc1\n" \
"s_waitcnt vmcnt(0)\nv_bitop3_b32 v209, v168, v170, s61 bitop3:0x7e\nv_cmp_gt_i32 vcc, 0, v209\ns_cbranch_vccz .Lrd_tl13%=\ns_add_u32 s43, s43, 1\ns_cmp_lt_u32 s43, 262144\ns_cbranch_scc1 .Lrd_tre13%=\ns_branch .Lrd_abort%=\n" \
".Lrd_tre14%=:\ns_add_u32 s60, s58, 20480\nbuffer_load_dwordx4 v[172:175], v196, %[rs], s60 offen offset:1024 sc1\ns_waitcnt vmcnt(0)\nv_bitop3_b32 v209, v172, v174, s61 bitop3:0x7e\nv_cmp_gt_i32 vcc, 0, v209\ns_cbranch_vccz .Lrd_tl14%=\ns_add_u32 s43, s43, 1\n" \
"s_cmp_lt_u32 s43, 262144\ns_cbranch_scc1 .Lrd_tre14%=\ns_branch .Lrd_abort%=\n.Lrd_tre15%=:\ns_add_u32 s60, s58, 20480\nbuffer_load_dwordx4 v[176:179], v196, %[rs], s60 offen offset:3072 sc1\ns_waitcnt vmcnt(0)\nv_bitop3_b32 v209, v176, v178, s61 bitop3:0x7e\n" \
"v_cmp_gt_i32 vcc, 0, v209\ns_cbranch_vccz .Lrd_tl15%=\ns_add_u32 s43, s43, 1\ns_cmp_lt_u32 s43, 262144\ns_cbranch_scc1 .Lrd_tre15%=\ns_branch .Lrd_abort%=\n.Lrd_abort%=:\ns_mov_b64 exec, -1\n" \
"v_mov_b32 v16, 0\ns_movk_i32 s44, 6400\nbuffer_store_dword v16, off, %[rs], s44 sc1\n.Lrd_end%=:\ns_waitcnt vmcnt(0) lgkmcnt(0)\n" \
""
#define GG_ASM \
"v_mbcnt_lo_u32_b32 v255, -1, 0\nv_mbcnt_hi_u32_b32 v255, -1, v255\nv_lshlrev_b32 v255, 2, v255\nv_add_u32 v255, %[tb], v255\nds_read_b32 v0, v255 offset:0\nds_read_b32 v1, v255 offset:256\nds_read_b32 v2, v255 offset:512\nds_read_b32 v3, v255 offset:768\n" \
"ds_read_b32 v4, v255 offset:1024\nds_read_b32 v5, v255 offset:1280\nds_read_b32 v92, v255 offset:1536\nds_read_b32 v93, v255 offset:1792\nds_read_b32 v94, v255 offset:2048\ns_waitcnt lgkmcnt(0)\nv_mov_b32 v6, 0x3f803f80\nv_mov_b32 v7, 0x3f803f80\n" \
"s_add_u32 m0, %[lw], 16384\ns_add_u32 s44, %[wo], 0\nbuffer_load_dwordx4 v92, %[rw], s44 offen nt lds\ns_add_u32 m0, %[lw], 17408\ns_add_u32 s44, %[wo], 57344\nbuffer_load_dwordx4 v92, %[rw], s44 offen nt lds\ns_add_u32 m0, %[lw], 18432\ns_add_u32 s44, %[wo], 114688\n" \
"buffer_load_dwordx4 v92, %[rw], s44 offen nt lds\ns_add_u32 m0, %[lw], 19456\ns_add_u32 s44, %[wo], 172032\nbuffer_load_dwordx4 v92, %[rw], s44 offen nt lds\ns_add_u32 m0, %[lw], 20480\ns_add_u32 s44, %[wo], 229376\nbuffer_load_dwordx4 v92, %[rw], s44 offen nt lds\ns_add_u32 m0, %[lw], 21504\n" \
"s_add_u32 s44, %[wo], 286720\nbuffer_load_dwordx4 v92, %[rw], s44 offen nt lds\ns_add_u32 m0, %[lw], 22528\ns_add_u32 s44, %[wo], 344064\nbuffer_load_dwordx4 v92, %[rw], s44 offen nt lds\ns_add_u32 m0, %[lw], 23552\ns_add_u32 s44, %[wo], 401408\nbuffer_load_dwordx4 v92, %[rw], s44 offen nt lds\n" \
"s_add_u32 m0, %[tb], 0\ns_add_u32 s44, %[wo], 1835008\nbuffer_load_dwordx4 v92, %[rw], s44 offen nt lds\ns_add_u32 m0, %[tb], 1024\ns_add_u32 s44, %[wo], 1892352\nbuffer_load_dwordx4 v92, %[rw], s44 offen nt lds\ns_add_u32 m0, %[tb], 2048\ns_add_u32 s44, %[wo], 1949696\n" \
"buffer_load_dwordx4 v92, %[rw], s44 offen nt lds\ns_add_u32 m0, %[tb], 3072\ns_add_u32 s44, %[wo], 2007040\nbuffer_load_dwordx4 v92, %[rw], s44 offen nt lds\ns_add_u32 m0, %[tb], 4096\ns_add_u32 s44, %[wo], 2064384\nbuffer_load_dwordx4 v92, %[rw], s44 offen nt lds\ns_add_u32 m0, %[tb], 5120\n" \
"s_add_u32 s44, %[wo], 2121728\nbuffer_load_dwordx4 v92, %[rw], s44 offen nt lds\ns_add_u32 m0, %[tb], 6144\ns_add_u32 s44, %[wo], 2179072\nbuffer_load_dwordx4 v92, %[rw], s44 offen nt lds\ns_add_u32 m0, %[tb], 7168\ns_add_u32 s44, %[wo], 2236416\nbuffer_load_dwordx4 v92, %[rw], s44 offen nt lds\n" \
"s_add_u32 m0, %[tb], 8192\ns_add_u32 s44, %[wo], 3670016\nbuffer_load_dwordx4 v92, %[rw], s44 offen nt lds\ns_add_u32 m0, %[tb], 9216\ns_add_u32 s44, %[wo], 3727360\nbuffer_load_dwordx4 v92, %[rw], s44 offen nt lds\ns_add_u32 m0, %[tb], 10240\ns_add_u32 s44, %[wo], 3784704\n" \
"buffer_load_dwordx4 v92, %[rw], s44 offen nt lds\ns_add_u32 m0, %[tb], 11264\ns_add_u32 s44, %[wo], 3842048\nbuffer_load_dwordx4 v92, %[rw], s44 offen nt lds\ns_add_u32 m0, %[lw], 0\ns_add_u32 s44, %[wo], 3899392\nbuffer_load_dwordx4 v92, %[rw], s44 offen nt lds\ns_add_u32 m0, %[lw], 1024\n" \
"s_add_u32 s44, %[wo], 3956736\nbuffer_load_dwordx4 v92, %[rw], s44 offen nt lds\ns_add_u32 m0, %[lw], 2048\ns_add_u32 s44, %[wo], 4014080\nbuffer_load_dwordx4 v92, %[rw], s44 offen nt lds\ns_add_u32 m0, %[lw], 3072\ns_add_u32 s44, %[wo], 4071424\nbuffer_load_dwordx4 v92, %[rw], s44 offen nt lds\n" \
"s_add_u32 m0, %[lw], 4096\ns_add_u32 s44, %[wo], 5505024\nbuffer_load_dwordx4 v92, %[rw], s44 offen nt lds\ns_add_u32 m0, %[lw], 5120\ns_add_u32 s44, %[wo], 5562368\nbuffer_load_dwordx4 v92, %[rw], s44 offen nt lds\ns_add_u32 m0, %[lw], 6144\ns_add_u32 s44, %[wo], 5619712\n" \
"buffer_load_dwordx4 v92, %[rw], s44 offen nt lds\ns_add_u32 m0, %[lw], 7168\ns_add_u32 s44, %[wo], 5677056\nbuffer_load_dwordx4 v92, %[rw], s44 offen nt lds\ns_add_u32 m0, %[lw], 8192\ns_add_u32 s44, %[wo], 5734400\nbuffer_load_dwordx4 v92, %[rw], s44 offen nt lds\ns_add_u32 m0, %[lw], 9216\n" \
"s_add_u32 s44, %[wo], 5791744\nbuffer_load_dwordx4 v92, %[rw], s44 offen nt lds\ns_add_u32 m0, %[lw], 10240\ns_add_u32 s44, %[wo], 5849088\nbuffer_load_dwordx4 v92, %[rw], s44 offen nt lds\ns_add_u32 m0, %[lw], 11264\ns_add_u32 s44, %[wo], 5906432\nbuffer_load_dwordx4 v92, %[rw], s44 offen nt lds\n" \
"s_waitcnt vmcnt(28)\ns_barrier\ns_add_u32 m0, %[lw], 12288\ns_add_u32 s44, %[wo], 458752\nbuffer_load_dwordx4 v92, %[rw], s44 offen nt lds\ns_add_u32 m0, %[lw], 13312\ns_add_u32 s44, %[wo], 516096\nbuffer_load_dwordx4 v92, %[rw], s44 offen nt lds\n" \
"s_add_u32 m0, %[lw], 14336\ns_add_u32 s44, %[wo], 573440\nbuffer_load_dwordx4 v92, %[rw], s44 offen nt lds\ns_add_u32 m0, %[lw], 15360\ns_add_u32 s44, %[wo], 630784\nbuffer_load_dwordx4 v92, %[rw], s44 offen nt lds\nds_read_b64_tr_b16 a[0:1], v94 offset:16384\nds_read_b64_tr_b16 a[2:3], v94 offset:16896\n" \
"ds_read_b64_tr_b16 a[8:9], v94 offset:16392\nds_read_b64_tr_b16 a[10:11], v94 offset:16904\nds_read_b64_tr_b16 a[16:17], v94 offset:40960\nds_read_b64_tr_b16 a[18:19], v94 offset:41472\nds_read_b64_tr_b16 a[24:25], v94 offset:40968\nds_read_b64_tr_b16 a[26:27], v94 offset:41480\ns_waitcnt lgkmcnt(0)\nv_mfma_f32_16x16x16_bf16 v[8:11], a[0:1], v[6:7], 0\n" \
"v_mfma_f32_16x16x16_bf16 v[12:15], a[8:9], v[6:7], 0\nv_mfma_f32_16x16x16_bf16 v[16:19], a[16:17], v[6:7], 0\nv_mfma_f32_16x16x16_bf16 v[20:23], a[24:25], v[6:7], 0\nv_mfma_f32_16x16x16_bf16 v[8:11], a[2:3], v[6:7], v[8:11]\nv_mfma_f32_16x16x16_bf16 v[12:15], a[10:11], v[6:7], v[12:15]\nv_mfma_f32_16x16x16_bf16 v[16:19], a[18:19], v[6:7], v[16:19]\nv_mfma_f32_16x16x16_bf16 v[20:23], a[26:27], v[6:7], v[20:23]\ns_waitcnt vmcnt(28)\n" \
"s_barrier\ns_add_u32 m0, %[lw], 16384\ns_add_u32 s44, %[wo], 688128\nbuffer_load_dwordx4 v92, %[rw], s44 offen nt lds\ns_add_u32 m0, %[lw], 17408\ns_add_u32 s44, %[wo], 745472\nbuffer_load_dwordx4 v92, %[rw], s44 offen nt lds\ns_add_u32 m0, %[lw], 18432\n" \
"s_add_u32 s44, %[wo], 802816\nbuffer_load_dwordx4 v92, %[rw], s44 offen nt lds\ns_add_u32 m0, %[lw], 19456\ns_add_u32 s44, %[wo], 860160\nbuffer_load_dwordx4 v92, %[rw], s44 offen nt lds\nds_read_b64_tr_b16 a[4:5], v94 offset:20480\nds_read_b64_tr_b16 a[6:7], v94 offset:20992\nds_read_b64_tr_b16 a[12:13], v94 offset:20488\n" \
"ds_read_b64_tr_b16 a[14:15], v94 offset:21000\nds_read_b64_tr_b16 a[20:21], v94 offset:45056\nds_read_b64_tr_b16 a[22:23], v94 offset:45568\nds_read_b64_tr_b16 a[28:29], v94 offset:45064\nds_read_b64_tr_b16 a[30:31], v94 offset:45576\ns_waitcnt lgkmcnt(0)\nv_mfma_f32_16x16x16_bf16 v[8:11], a[4:5], v[6:7], v[8:11]\nv_mfma_f32_16x16x16_bf16 v[12:15], a[12:13], v[6:7], v[12:15]\n" \
"v_mfma_f32_16x16x16_bf16 v[16:19], a[20:21], v[6:7], v[16:19]\nv_mfma_f32_16x16x16_bf16 v[20:23], a[28:29], v[6:7], v[20:23]\nv_mfma_f32_16x16x16_bf16 v[8:11], a[6:7], v[6:7], v[8:11]\nv_mfma_f32_16x16x16_bf16 v[12:15], a[14:15], v[6:7], v[12:15]\nv_mfma_f32_16x16x16_bf16 v[16:19], a[22:23], v[6:7], v[16:19]\nv_mfma_f32_16x16x16_bf16 v[20:23], a[30:31], v[6:7], v[20:23]\ns_waitcnt vmcnt(28)\ns_barrier\n" \
"s_add_u32 m0, %[lw], 20480\ns_add_u32 s44, %[wo], 917504\nbuffer_load_dwordx4 v92, %[rw], s44 offen nt lds\ns_add_u32 m0, %[lw], 21504\ns_add_u32 s44, %[wo], 974848\nbuffer_load_dwordx4 v92, %[rw], s44 offen nt lds\ns_add_u32 m0, %[lw], 22528\ns_add_u32 s44, %[wo], 1032192\n" \
"buffer_load_dwordx4 v92, %[rw], s44 offen nt lds\ns_add_u32 m0, %[lw], 23552\ns_add_u32 s44, %[wo], 1089536\nbuffer_load_dwordx4 v92, %[rw], s44 offen nt lds\nds_read_b64_tr_b16 a[104:105], v93 offset:0\nds_read_b64_tr_b16 a[106:107], v93 offset:512\nds_read_b64_tr_b16 a[112:113], v93 offset:8\nds_read_b64_tr_b16 a[114:115], v93 offset:520\n" \
"ds_read_b64_tr_b16 a[120:121], v93 offset:12288\nds_read_b64_tr_b16 a[122:123], v93 offset:12800\nds_read_b64_tr_b16 a[128:129], v93 offset:12296\nds_read_b64_tr_b16 a[130:131], v93 offset:12808\ns_waitcnt lgkmcnt(0)\nv_mfma_f32_16x16x16_bf16 v[8:11], a[104:105], v[6:7], v[8:11]\nv_mfma_f32_16x16x16_bf16 v[12:15], a[112:113], v[6:7], v[12:15]\nv_mfma_f32_16x16x16_bf16 v[16:19], a[120:121], v[6:7], v[16:19]\n" \
"v_mfma_f32_16x16x16_bf16 v[20:23], a[128:129], v[6:7], v[20:23]\nv_mfma_f32_16x16x16_bf16 v[8:11], a[106:107], v[6:7], v[8:11]\nv_mfma_f32_16x16x16_bf16 v[12:15], a[114:115], v[6:7], v[12:15]\nv_mfma_f32_16x16x16_bf16 v[16:19], a[122:123], v[6:7], v[16:19]\nv_mfma_f32_16x16x16_bf16 v[20:23], a[130:131], v[6:7], v[20:23]\ns_waitcnt vmcnt(28)\ns_barrier\ns_add_u32 m0, %[tb], 0\n" \
"s_add_u32 s44, %[wo], 1146880\nbuffer_load_dwordx4 v92, %[rw], s44 offen nt lds\ns_add_u32 m0, %[tb], 1024\ns_add_u32 s44, %[wo], 1204224\nbuffer_load_dwordx4 v92, %[rw], s44 offen nt lds\ns_add_u32 m0, %[tb], 2048\ns_add_u32 s44, %[wo], 1261568\nbuffer_load_dwordx4 v92, %[rw], s44 offen nt lds\n" \
"s_add_u32 m0, %[tb], 3072\ns_add_u32 s44, %[wo], 1318912\nbuffer_load_dwordx4 v92, %[rw], s44 offen nt lds\nds_read_b64_tr_b16 a[108:109], v93 offset:4096\nds_read_b64_tr_b16 a[110:111], v93 offset:4608\nds_read_b64_tr_b16 a[116:117], v93 offset:4104\nds_read_b64_tr_b16 a[118:119], v93 offset:4616\nds_read_b64_tr_b16 a[124:125], v93 offset:16384\n" \
"ds_read_b64_tr_b16 a[126:127], v93 offset:16896\nds_read_b64_tr_b16 a[132:133], v93 offset:16392\nds_read_b64_tr_b16 a[134:135], v93 offset:16904\ns_waitcnt lgkmcnt(0)\nv_mfma_f32_16x16x16_bf16 v[8:11], a[108:109], v[6:7], v[8:11]\nv_mfma_f32_16x16x16_bf16 v[12:15], a[116:117], v[6:7], v[12:15]\nv_mfma_f32_16x16x16_bf16 v[16:19], a[124:125], v[6:7], v[16:19]\nv_mfma_f32_16x16x16_bf16 v[20:23], a[132:133], v[6:7], v[20:23]\n" \
"v_mfma_f32_16x16x16_bf16 v[8:11], a[110:111], v[6:7], v[8:11]\nv_mfma_f32_16x16x16_bf16 v[12:15], a[118:119], v[6:7], v[12:15]\nv_mfma_f32_16x16x16_bf16 v[16:19], a[126:127], v[6:7], v[16:19]\nv_mfma_f32_16x16x16_bf16 v[20:23], a[134:135], v[6:7], v[20:23]\ns_waitcnt vmcnt(28)\ns_barrier\ns_add_u32 m0, %[tb], 4096\ns_add_u32 s44, %[wo], 1376256\n" \
"buffer_load_dwordx4 v92, %[rw], s44 offen nt lds\ns_add_u32 m0, %[tb], 5120\ns_add_u32 s44, %[wo], 1433600\nbuffer_load_dwordx4 v92, %[rw], s44 offen nt lds\ns_add_u32 m0, %[tb], 6144\ns_add_u32 s44, %[wo], 1490944\nbuffer_load_dwordx4 v92, %[rw], s44 offen nt lds\ns_add_u32 m0, %[tb], 7168\n" \
"s_add_u32 s44, %[wo], 1548288\nbuffer_load_dwordx4 v92, %[rw], s44 offen nt lds\nds_read_b64_tr_b16 a[208:209], v93 offset:8192\nds_read_b64_tr_b16 a[210:211], v93 offset:8704\nds_read_b64_tr_b16 a[216:217], v93 offset:8200\nds_read_b64_tr_b16 a[218:219], v93 offset:8712\nds_read_b64_tr_b16 a[224:225], v93 offset:20480\nds_read_b64_tr_b16 a[226:227], v93 offset:20992\n" \
"ds_read_b64_tr_b16 a[232:233], v93 offset:20488\nds_read_b64_tr_b16 a[234:235], v93 offset:21000\ns_waitcnt lgkmcnt(0)\nv_mfma_f32_16x16x16_bf16 v[8:11], a[208:209], v[6:7], v[8:11]\nv_mfma_f32_16x16x16_bf16 v[12:15], a[216:217], v[6:7], v[12:15]\nv_mfma_f32_16x16x16_bf16 v[16:19], a[224:225], v[6:7], v[16:19]\nv_mfma_f32_16x16x16_bf16 v[20:23], a[232:233], v[6:7], v[20:23]\nv_mfma_f32_16x16x16_bf16 v[8:11], a[210:211], v[6:7], v[8:11]\n" \
"v_mfma_f32_16x16x16_bf16 v[12:15], a[218:219], v[6:7], v[12:15]\nv_mfma_f32_16x16x16_bf16 v[16:19], a[226:227], v[6:7], v[16:19]\nv_mfma_f32_16x16x16_bf16 v[20:23], a[234:235], v[6:7], v[20:23]\ns_waitcnt vmcnt(28)\ns_barrier\ns_add_u32 m0, %[tb], 8192\ns_add_u32 s44, %[wo], 1605632\nbuffer_load_dwordx4 v92, %[rw], s44 offen nt lds\n" \
"s_add_u32 m0, %[tb], 9216\ns_add_u32 s44, %[wo], 1662976\nbuffer_load_dwordx4 v92, %[rw], s44 offen nt lds\ns_add_u32 m0, %[tb], 10240\ns_add_u32 s44, %[wo], 1720320\nbuffer_load_dwordx4 v92, %[rw], s44 offen nt lds\ns_add_u32 m0, %[tb], 11264\ns_add_u32 s44, %[wo], 1777664\n" \
"buffer_load_dwordx4 v92, %[rw], s44 offen nt lds\nds_read_b64_tr_b16 a[212:213], v94 offset:0\nds_read_b64_tr_b16 a[214:215], v94 offset:512\nds_read_b64_tr_b16 a[220:221], v94 offset:8\nds_read_b64_tr_b16 a[222:223], v94 offset:520\nds_read_b64_tr_b16 a[228:229], v94 offset:24576\nds_read_b64_tr_b16 a[230:231], v94 offset:25088\nds_read_b64_tr_b16 a[236:237], v94 offset:24584\n" \
"ds_read_b64_tr_b16 a[238:239], v94 offset:25096\ns_waitcnt lgkmcnt(0)\nv_mfma_f32_16x16x16_bf16 v[8:11], a[212:213], v[6:7], v[8:11]\nv_mfma_f32_16x16x16_bf16 v[12:15], a[220:221], v[6:7], v[12:15]\nv_mfma_f32_16x16x16_bf16 v[16:19], a[228:229], v[6:7], v[16:19]\nv_mfma_f32_16x16x16_bf16 v[20:23], a[236:237], v[6:7], v[20:23]\nv_mfma_f32_16x16x16_bf16 v[8:11], a[214:215], v[6:7], v[8:11]\nv_mfma_f32_16x16x16_bf16 v[12:15], a[222:223], v[6:7], v[12:15]\n" \
"v_mfma_f32_16x16x16_bf16 v[16:19], a[230:231], v[6:7], v[16:19]\nv_mfma_f32_16x16x16_bf16 v[20:23], a[238:239], v[6:7], v[20:23]\ns_waitcnt vmcnt(28)\ns_barrier\ns_add_u32 m0, %[lw], 0\ns_add_u32 s44, %[wo], 2293760\nbuffer_load_dwordx4 v92, %[rw], s44 offen nt lds\ns_add_u32 m0, %[lw], 1024\n" \
"s_add_u32 s44, %[wo], 2351104\nbuffer_load_dwordx4 v92, %[rw], s44 offen nt lds\ns_add_u32 m0, %[lw], 2048\ns_add_u32 s44, %[wo], 2408448\nbuffer_load_dwordx4 v92, %[rw], s44 offen nt lds\ns_add_u32 m0, %[lw], 3072\ns_add_u32 s44, %[wo], 2465792\nbuffer_load_dwordx4 v92, %[rw], s44 offen nt lds\n" \
"ds_read_b64_tr_b16 v[152:153], v94 offset:4096\nds_read_b64_tr_b16 v[154:155], v94 offset:4608\nds_read_b64_tr_b16 v[160:161], v94 offset:4104\nds_read_b64_tr_b16 v[162:163], v94 offset:4616\nds_read_b64_tr_b16 v[168:169], v94 offset:28672\nds_read_b64_tr_b16 v[170:171], v94 offset:29184\nds_read_b64_tr_b16 v[176:177], v94 offset:28680\nds_read_b64_tr_b16 v[178:179], v94 offset:29192\n" \
"s_waitcnt lgkmcnt(0)\nv_mfma_f32_16x16x16_bf16 v[8:11], v[152:153], v[6:7], v[8:11]\nv_mfma_f32_16x16x16_bf16 v[12:15], v[160:161], v[6:7], v[12:15]\nv_mfma_f32_16x16x16_bf16 v[16:19], v[168:169], v[6:7], v[16:19]\nv_mfma_f32_16x16x16_bf16 v[20:23], v[176:177], v[6:7], v[20:23]\nv_mfma_f32_16x16x16_bf16 v[8:11], v[154:155], v[6:7], v[8:11]\nv_mfma_f32_16x16x16_bf16 v[12:15], v[162:163], v[6:7], v[12:15]\nv_mfma_f32_16x16x16_bf16 v[16:19], v[170:171], v[6:7], v[16:19]\n" \
"v_mfma_f32_16x16x16_bf16 v[20:23], v[178:179], v[6:7], v[20:23]\ns_waitcnt vmcnt(28)\ns_barrier\ns_add_u32 m0, %[lw], 4096\ns_add_u32 s44, %[wo], 2523136\nbuffer_load_dwordx4 v92, %[rw], s44 offen nt lds\ns_add_u32 m0, %[lw], 5120\ns_add_u32 s44, %[wo], 2580480\n" \
"buffer_load_dwordx4 v92, %[rw], s44 offen nt lds\ns_add_u32 m0, %[lw], 6144\ns_add_u32 s44, %[wo], 2637824\nbuffer_load_dwordx4 v92, %[rw], s44 offen nt lds\ns_add_u32 m0, %[lw], 7168\ns_add_u32 s44, %[wo], 2695168\nbuffer_load_dwordx4 v92, %[rw], s44 offen nt lds\nds_read_b64_tr_b16 v[156:157], v94 offset:8192\n" \
"ds_read_b64_tr_b16 v[158:159], v94 offset:8704\nds_read_b64_tr_b16 v[164:165], v94 offset:8200\nds_read_b64_tr_b16 v[166:167], v94 offset:8712\nds_read_b64_tr_b16 v[172:173], v94 offset:32768\nds_read_b64_tr_b16 v[174:175], v94 offset:33280\nds_read_b64_tr_b16 v[180:181], v94 offset:32776\nds_read_b64_tr_b16 v[182:183], v94 offset:33288\ns_waitcnt lgkmcnt(0)\n" \
"v_mfma_f32_16x16x16_bf16 v[8:11], v[156:157], v[6:7], v[8:11]\nv_mfma_f32_16x16x16_bf16 v[12:15], v[164:165], v[6:7], v[12:15]\nv_mfma_f32_16x16x16_bf16 v[16:19], v[172:173], v[6:7], v[16:19]\nv_mfma_f32_16x16x16_bf16 v[20:23], v[180:181], v[6:7], v[20:23]\nv_mfma_f32_16x16x16_bf16 v[8:11], v[158:159], v[6:7], v[8:11]\nv_mfma_f32_16x16x16_bf16 v[12:15], v[166:167], v[6:7], v[12:15]\nv_mfma_f32_16x16x16_bf16 v[16:19], v[174:175], v[6:7], v[16:19]\nv_mfma_f32_16x16x16_bf16 v[20:23], v[182:183], v[6:7], v[20:23]\n" \
"s_waitcnt vmcnt(28)\ns_barrier\ns_add_u32 m0, %[lw], 8192\ns_add_u32 s44, %[wo], 2752512\nbuffer_load_dwordx4 v92, %[rw], s44 offen nt lds\ns_add_u32 m0, %[lw], 9216\ns_add_u32 s44, %[wo], 2809856\nbuffer_load_dwordx4 v92, %[rw], s44 offen nt lds\n" \
"s_add_u32 m0, %[lw], 10240\ns_add_u32 s44, %[wo], 2867200\nbuffer_load_dwordx4 v92, %[rw], s44 offen nt lds\ns_add_u32 m0, %[lw], 11264\ns_add_u32 s44, %[wo], 2924544\nbuffer_load_dwordx4 v92, %[rw], s44 offen nt lds\nds_read_b64_tr_b16 a[32:33], v94 offset:12288\nds_read_b64_tr_b16 a[34:35], v94 offset:12800\n" \
"ds_read_b64_tr_b16 v[88:89], v94 offset:12296\nds_read_b64_tr_b16 v[90:91], v94 offset:12808\nds_read_b64_tr_b16 a[40:41], v94 offset:36864\nds_read_b64_tr_b16 a[42:43], v94 offset:37376\nds_read_b64_tr_b16 a[48:49], v94 offset:36872\nds_read_b64_tr_b16 a[50:51], v94 offset:37384\ns_waitcnt lgkmcnt(0)\nv_mfma_f32_16x16x16_bf16 v[8:11], a[32:33], v[6:7], v[8:11]\n" \
"v_mfma_f32_16x16x16_bf16 v[12:15], v[88:89], v[6:7], v[12:15]\nv_mfma_f32_16x16x16_bf16 v[16:19], a[40:41], v[6:7], v[16:19]\nv_mfma_f32_16x16x16_bf16 v[20:23], a[48:49], v[6:7], v[20:23]\nv_mfma_f32_16x16x16_bf16 v[8:11], a[34:35], v[6:7], v[8:11]\nv_mfma_f32_16x16x16_bf16 v[12:15], v[90:91], v[6:7], v[12:15]\nv_mfma_f32_16x16x16_bf16 v[16:19], a[42:43], v[6:7], v[16:19]\nv_mfma_f32_16x16x16_bf16 v[20:23], a[50:51], v[6:7], v[20:23]\ns_waitcnt vmcnt(28)\n" \
"s_barrier\ns_add_u32 m0, %[lw], 12288\ns_add_u32 s44, %[wo], 2981888\nbuffer_load_dwordx4 v92, %[rw], s44 offen nt lds\ns_add_u32 m0, %[lw], 13312\ns_add_u32 s44, %[wo], 3039232\nbuffer_load_dwordx4 v92, %[rw], s44 offen nt lds\ns_add_u32 m0, %[lw], 14336\n" \
"s_add_u32 s44, %[wo], 3096576\nbuffer_load_dwordx4 v92, %[rw], s44 offen nt lds\ns_add_u32 m0, %[lw], 15360\ns_add_u32 s44, %[wo], 3153920\nbuffer_load_dwordx4 v92, %[rw], s44 offen nt lds\nds_read_b64_tr_b16 a[36:37], v94 offset:16384\nds_read_b64_tr_b16 a[38:39], v94 offset:16896\nds_read_b64_tr_b16 v[84:85], v94 offset:16392\n" \
"ds_read_b64_tr_b16 v[86:87], v94 offset:16904\nds_read_b64_tr_b16 a[44:45], v94 offset:40960\nds_read_b64_tr_b16 a[46:47], v94 offset:41472\nds_read_b64_tr_b16 a[52:53], v94 offset:40968\nds_read_b64_tr_b16 a[54:55], v94 offset:41480\ns_waitcnt lgkmcnt(0)\nv_mfma_f32_16x16x16_bf16 v[8:11], a[36:37], v[6:7], v[8:11]\nv_mfma_f32_16x16x16_bf16 v[12:15], v[84:85], v[6:7], v[12:15]\n" \
"v_mfma_f32_16x16x16_bf16 v[16:19], a[44:45], v[6:7], v[16:19]\nv_mfma_f32_16x16x16_bf16 v[20:23], a[52:53], v[6:7], v[20:23]\nv_mfma_f32_16x16x16_bf16 v[8:11], a[38:39], v[6:7], v[8:11]\nv_mfma_f32_16x16x16_bf16 v[12:15], v[86:87], v[6:7], v[12:15]\nv_mfma_f32_16x16x16_bf16 v[16:19], a[46:47], v[6:7], v[16:19]\nv_mfma_f32_16x16x16_bf16 v[20:23], a[54:55], v[6:7], v[20:23]\ns_waitcnt vmcnt(28)\ns_barrier\n" \
"s_add_u32 m0, %[lw], 16384\ns_add_u32 s44, %[wo], 3211264\nbuffer_load_dwordx4 v92, %[rw], s44 offen nt lds\ns_add_u32 m0, %[lw], 17408\ns_add_u32 s44, %[wo], 3268608\nbuffer_load_dwordx4 v92, %[rw], s44 offen nt lds\ns_add_u32 m0, %[lw], 18432\ns_add_u32 s44, %[wo], 3325952\n" \
"buffer_load_dwordx4 v92, %[rw], s44 offen nt lds\ns_add_u32 m0, %[lw], 19456\ns_add_u32 s44, %[wo], 3383296\nbuffer_load_dwordx4 v92, %[rw], s44 offen nt lds\nds_read_b64_tr_b16 a[56:57], v94 offset:20480\nds_read_b64_tr_b16 a[58:59], v94 offset:20992\nds_read_b64_tr_b16 a[64:65], v94 offset:20488\nds_read_b64_tr_b16 a[66:67], v94 offset:21000\n" \
"ds_read_b64_tr_b16 v[80:81], v94 offset:45056\nds_read_b64_tr_b16 v[82:83], v94 offset:45568\nds_read_b64_tr_b16 a[72:73], v94 offset:45064\nds_read_b64_tr_b16 a[74:75], v94 offset:45576\ns_waitcnt lgkmcnt(0)\nv_mfma_f32_16x16x16_bf16 v[8:11], a[56:57], v[6:7], v[8:11]\nv_mfma_f32_16x16x16_bf16 v[12:15], a[64:65], v[6:7], v[12:15]\nv_mfma_f32_16x16x16_bf16 v[16:19], v[80:81], v[6:7], v[16:19]\n" \
"v_mfma_f32_16x16x16_bf16 v[20:23], a[72:73], v[6:7], v[20:23]\nv_mfma_f32_16x16x16_bf16 v[8:11], a[58:59], v[6:7], v[8:11]\nv_mfma_f32_16x16x16_bf16 v[12:15], a[66:67], v[6:7], v[12:15]\nv_mfma_f32_16x16x16_bf16 v[16:19], v[82:83], v[6:7], v[16:19]\nv_mfma_f32_16x16x16_bf16 v[20:23], a[74:75], v[6:7], v[20:23]\ns_waitcnt vmcnt(28)\ns_barrier\ns_add_u32 m0, %[lw], 20480\n" \
"s_add_u32 s44, %[wo], 3440640\nbuffer_load_dwordx4 v92, %[rw], s44 offen nt lds\ns_add_u32 m0, %[lw], 21504\ns_add_u32 s44, %[wo], 3497984\nbuffer_load_dwordx4 v92, %[rw], s44 offen nt lds\ns_add_u32 m0, %[lw], 22528\ns_add_u32 s44, %[wo], 3555328\nbuffer_load_dwordx4 v92, %[rw], s44 offen nt lds\n" \
"s_add_u32 m0, %[lw], 23552\ns_add_u32 s44, %[wo], 3612672\nbuffer_load_dwordx4 v92, %[rw], s44 offen nt lds\nds_read_b64_tr_b16 a[60:61], v93 offset:0\nds_read_b64_tr_b16 a[62:63], v93 offset:512\nds_read_b64_tr_b16 a[68:69], v93 offset:8\nds_read_b64_tr_b16 a[70:71], v93 offset:520\nds_read_b64_tr_b16 v[76:77], v93 offset:12288\n" \
"ds_read_b64_tr_b16 v[78:79], v93 offset:12800\nds_read_b64_tr_b16 a[76:77], v93 offset:12296\nds_read_b64_tr_b16 a[78:79], v93 offset:12808\ns_waitcnt lgkmcnt(0)\nv_mfma_f32_16x16x16_bf16 v[8:11], a[60:61], v[6:7], v[8:11]\nv_mfma_f32_16x16x16_bf16 v[12:15], a[68:69], v[6:7], v[12:15]\nv_mfma_f32_16x16x16_bf16 v[16:19], v[76:77], v[6:7], v[16:19]\nv_mfma_f32_16x16x16_bf16 v[20:23], a[76:77], v[6:7], v[20:23]\n" \
"v_mfma_f32_16x16x16_bf16 v[8:11], a[62:63], v[6:7], v[8:11]\nv_mfma_f32_16x16x16_bf16 v[12:15], a[70:71], v[6:7], v[12:15]\nv_mfma_f32_16x16x16_bf16 v[16:19], v[78:79], v[6:7], v[16:19]\nv_mfma_f32_16x16x16_bf16 v[20:23], a[78:79], v[6:7], v[20:23]\ns_waitcnt vmcnt(28)\ns_barrier\ns_add_u32 m0, %[tb], 0\ns_add_u32 s44, %[wo], 4128768\n" \
"buffer_load_dwordx4 v92, %[rw], s44 offen nt lds\ns_add_u32 m0, %[tb], 1024\ns_add_u32 s44, %[wo], 4186112\nbuffer_load_dwordx4 v92, %[rw], s44 offen nt lds\ns_add_u32 m0, %[tb], 2048\ns_add_u32 s44, %[wo], 4243456\nbuffer_load_dwordx4 v92, %[rw], s44 offen nt lds\ns_add_u32 m0, %[tb], 3072\n" \
"s_add_u32 s44, %[wo], 4300800\nbuffer_load_dwordx4 v92, %[rw], s44 offen nt lds\nds_read_b64_tr_b16 a[80:81], v93 offset:4096\nds_read_b64_tr_b16 a[82:83], v93 offset:4608\nds_read_b64_tr_b16 a[88:89], v93 offset:4104\nds_read_b64_tr_b16 a[90:91], v93 offset:4616\nds_read_b64_tr_b16 a[96:97], v93 offset:16384\nds_read_b64_tr_b16 a[98:99], v93 offset:16896\n" \
"ds_read_b64_tr_b16 v[72:73], v93 offset:16392\nds_read_b64_tr_b16 v[74:75], v93 offset:16904\ns_waitcnt lgkmcnt(0)\nv_mfma_f32_16x16x16_bf16 v[8:11], a[80:81], v[6:7], v[8:11]\nv_mfma_f32_16x16x16_bf16 v[12:15], a[88:89], v[6:7], v[12:15]\nv_mfma_f32_16x16x16_bf16 v[16:19], a[96:97], v[6:7], v[16:19]\nv_mfma_f32_16x16x16_bf16 v[20:23], v[72:73], v[6:7], v[20:23]\nv_mfma_f32_16x16x16_bf16 v[8:11], a[82:83], v[6:7], v[8:11]\n" \
"v_mfma_f32_16x16x16_bf16 v[12:15], a[90:91], v[6:7], v[12:15]\nv_mfma_f32_16x16x16_bf16 v[16:19], a[98:99], v[6:7], v[16:19]\nv_mfma_f32_16x16x16_bf16 v[20:23], v[74:75], v[6:7], v[20:23]\ns_waitcnt vmcnt(28)\ns_barrier\ns_add_u32 m0, %[tb], 4096\ns_add_u32 s44, %[wo], 4358144\nbuffer_load_dwordx4 v92, %[rw], s44 offen nt lds\n" \
"s_add_u32 m0, %[tb], 5120\ns_add_u32 s44, %[wo], 4415488\nbuffer_load_dwordx4 v92, %[rw], s44 offen nt lds\ns_add_u32 m0, %[tb], 6144\ns_add_u32 s44, %[wo], 4472832\nbuffer_load_dwordx4 v92, %[rw], s44 offen nt lds\ns_add_u32 m0, %[tb], 7168\ns_add_u32 s44, %[wo], 4530176\n" \
"buffer_load_dwordx4 v92, %[rw], s44 offen nt lds\nds_read_b64_tr_b16 a[84:85], v93 offset:8192\nds_read_b64_tr_b16 a[86:87], v93 offset:8704\nds_read_b64_tr_b16 a[92:93], v93 offset:8200\nds_read_b64_tr_b16 a[94:95], v93 offset:8712\nds_read_b64_tr_b16 a[100:101], v93 offset:20480\nds_read_b64_tr_b16 a[102:103], v93 offset:20992\nds_read_b64_tr_b16 v[68:69], v93 offset:20488\n" \
"ds_read_b64_tr_b16 v[70:71], v93 offset:21000\ns_waitcnt lgkmcnt(0)\nv_mfma_f32_16x16x16_bf16 v[8:11], a[84:85], v[6:7], v[8:11]\nv_mfma_f32_16x16x16_bf16 v[12:15], a[92:93], v[6:7], v[12:15]\nv_mfma_f32_16x16x16_bf16 v[16:19], a[100:101], v[6:7], v[16:19]\nv_mfma_f32_16x16x16_bf16 v[20:23], v[68:69], v[6:7], v[20:23]\nv_mfma_f32_16x16x16_bf16 v[8:11], a[86:87], v[6:7], v[8:11]\nv_mfma_f32_16x16x16_bf16 v[12:15], a[94:95], v[6:7], v[12:15]\n" \
"v_mfma_f32_16x16x16_bf16 v[16:19], a[102:103], v[6:7], v[16:19]\nv_mfma_f32_16x16x16_bf16 v[20:23], v[70:71], v[6:7], v[20:23]\ns_waitcnt vmcnt(28)\ns_barrier\ns_add_u32 m0, %[tb], 8192\ns_add_u32 s44, %[wo], 4587520\nbuffer_load_dwordx4 v92, %[rw], s44 offen nt lds\ns_add_u32 m0, %[tb], 9216\n" \
"s_add_u32 s44, %[wo], 4644864\nbuffer_load_dwordx4 v92, %[rw], s44 offen nt lds\ns_add_u32 m0, %[tb], 10240\ns_add_u32 s44, %[wo], 4702208\nbuffer_load_dwordx4 v92, %[rw], s44 offen nt lds\ns_add_u32 m0, %[tb], 11264\ns_add_u32 s44, %[wo], 4759552\nbuffer_load_dwordx4 v92, %[rw], s44 offen nt lds\n" \
"ds_read_b64_tr_b16 a[136:137], v94 offset:0\nds_read_b64_tr_b16 a[138:139], v94 offset:512\nds_read_b64_tr_b16 v[64:65], v94 offset:8\nds_read_b64_tr_b16 v[66:67], v94 offset:520\nds_read_b64_tr_b16 a[144:145], v94 offset:24576\nds_read_b64_tr_b16 a[146:147], v94 offset:25088\nds_read_b64_tr_b16 a[152:153], v94 offset:24584\nds_read_b64_tr_b16 a[154:155], v94 offset:25096\n" \
"s_waitcnt lgkmcnt(0)\nv_mfma_f32_16x16x16_bf16 v[8:11], a[136:137], v[6:7], v[8:11]\nv_mfma_f32_16x16x16_bf16 v[12:15], v[64:65], v[6:7], v[12:15]\nv_mfma_f32_16x16x16_bf16 v[16:19], a[144:145], v[6:7], v[16:19]\nv_mfma_f32_16x16x16_bf16 v[20:23], a[152:153], v[6:7], v[20:23]\nv_mfma_f32_16x16x16_bf16 v[8:11], a[138:139], v[6:7], v[8:11]\nv_mfma_f32_16x16x16_bf16 v[12:15], v[66:67], v[6:7], v[12:15]\nv_mfma_f32_16x16x16_bf16 v[16:19], a[146:147], v[6:7], v[16:19]\n" \
"v_mfma_f32_16x16x16_bf16 v[20:23], a[154:155], v[6:7], v[20:23]\ns_waitcnt vmcnt(28)\ns_barrier\ns_add_u32 m0, %[lw], 0\ns_add_u32 s44, %[wo], 4816896\nbuffer_load_dwordx4 v92, %[rw], s44 offen nt lds\ns_add_u32 m0, %[lw], 1024\ns_add_u32 s44, %[wo], 4874240\n" \
"buffer_load_dwordx4 v92, %[rw], s44 offen nt lds\ns_add_u32 m0, %[lw], 2048\ns_add_u32 s44, %[wo], 4931584\nbuffer_load_dwordx4 v92, %[rw], s44 offen nt lds\ns_add_u32 m0, %[lw], 3072\ns_add_u32 s44, %[wo], 4988928\nbuffer_load_dwordx4 v92, %[rw], s44 offen nt lds\nds_read_b64_tr_b16 a[140:141], v94 offset:4096\n" \
"ds_read_b64_tr_b16 a[142:143], v94 offset:4608\nds_read_b64_tr_b16 v[60:61], v94 offset:4104\nds_read_b64_tr_b16 v[62:63], v94 offset:4616\nds_read_b64_tr_b16 a[148:149], v94 offset:28672\nds_read_b64_tr_b16 a[150:151], v94 offset:29184\nds_read_b64_tr_b16 a[156:157], v94 offset:28680\nds_read_b64_tr_b16 a[158:159], v94 offset:29192\ns_waitcnt lgkmcnt(0)\n" \
"v_mfma_f32_16x16x16_bf16 v[8:11], a[140:141], v[6:7], v[8:11]\nv_mfma_f32_16x16x16_bf16 v[12:15], v[60:61], v[6:7], v[12:15]\nv_mfma_f32_16x16x16_bf16 v[16:19], a[148:149], v[6:7], v[16:19]\nv_mfma_f32_16x16x16_bf16 v[20:23], a[156:157], v[6:7], v[20:23]\nv_mfma_f32_16x16x16_bf16 v[8:11], a[142:143], v[6:7], v[8:11]\nv_mfma_f32_16x16x16_bf16 v[12:15], v[62:63], v[6:7], v[12:15]\nv_mfma_f32_16x16x16_bf16 v[16:19], a[150:151], v[6:7], v[16:19]\nv_mfma_f32_16x16x16_bf16 v[20:23], a[158:159], v[6:7], v[20:23]\n" \
"s_waitcnt vmcnt(28)\ns_barrier\ns_add_u32 m0, %[lw], 4096\ns_add_u32 s44, %[wo], 5046272\nbuffer_load_dwordx4 v92, %[rw], s44 offen nt lds\ns_add_u32 m0, %[lw], 5120\ns_add_u32 s44, %[wo], 5103616\nbuffer_load_dwordx4 v92, %[rw], s44 offen nt lds\n" \
"s_add_u32 m0, %[lw], 6144\ns_add_u32 s44, %[wo], 5160960\nbuffer_load_dwordx4 v92, %[rw], s44 offen nt lds\ns_add_u32 m0, %[lw], 7168\ns_add_u32 s44, %[wo], 5218304\nbuffer_load_dwordx4 v92, %[rw], s44 offen nt lds\nds_read_b64_tr_b16 a[160:161], v94 offset:8192\nds_read_b64_tr_b16 a[162:163], v94 offset:8704\n" \
"ds_read_b64_tr_b16 a[168:169], v94 offset:8200\nds_read_b64_tr_b16 a[170:171], v94 offset:8712\nds_read_b64_tr_b16 v[56:57], v94 offset:32768\nds_read_b64_tr_b16 v[58:59], v94 offset:33280\nds_read_b64_tr_b16 a[176:177], v94 offset:32776\nds_read_b64_tr_b16 a[178:179], v94 offset:33288\ns_waitcnt lgkmcnt(0)\nv_mfma_f32_16x16x16_bf16 v[8:11], a[160:161], v[6:7], v[8:11]\n" \
"v_mfma_f32_16x16x16_bf16 v[12:15], a[168:169], v[6:7], v[12:15]\nv_mfma_f32_16x16x16_bf16 v[16:19], v[56:57], v[6:7], v[16:19]\nv_mfma_f32_16x16x16_bf16 v[20:23], a[176:177], v[6:7], v[20:23]\nv_mfma_f32_16x16x16_bf16 v[8:11], a[162:163], v[6:7], v[8:11]\nv_mfma_f32_16x16x16_bf16 v[12:15], a[170:171], v[6:7], v[12:15]\nv_mfma_f32_16x16x16_bf16 v[16:19], v[58:59], v[6:7], v[16:19]\nv_mfma_f32_16x16x16_bf16 v[20:23], a[178:179], v[6:7], v[20:23]\ns_waitcnt vmcnt(28)\n" \
"s_barrier\ns_add_u32 m0, %[lw], 8192\ns_add_u32 s44, %[wo], 5275648\nbuffer_load_dwordx4 v92, %[rw], s44 offen nt lds\ns_add_u32 m0, %[lw], 9216\ns_add_u32 s44, %[wo], 5332992\nbuffer_load_dwordx4 v92, %[rw], s44 offen nt lds\ns_add_u32 m0, %[lw], 10240\n" \
"s_add_u32 s44, %[wo], 5390336\nbuffer_load_dwordx4 v92, %[rw], s44 offen nt lds\ns_add_u32 m0, %[lw], 11264\ns_add_u32 s44, %[wo], 5447680\nbuffer_load_dwordx4 v92, %[rw], s44 offen nt lds\nds_read_b64_tr_b16 a[164:165], v94 offset:12288\nds_read_b64_tr_b16 a[166:167], v94 offset:12800\nds_read_b64_tr_b16 a[172:173], v94 offset:12296\n" \
"ds_read_b64_tr_b16 a[174:175], v94 offset:12808\nds_read_b64_tr_b16 v[52:53], v94 offset:36864\nds_read_b64_tr_b16 v[54:55], v94 offset:37376\nds_read_b64_tr_b16 a[180:181], v94 offset:36872\nds_read_b64_tr_b16 a[182:183], v94 offset:37384\ns_waitcnt lgkmcnt(0)\nv_mfma_f32_16x16x16_bf16 v[8:11], a[164:165], v[6:7], v[8:11]\nv_mfma_f32_16x16x16_bf16 v[12:15], a[172:173], v[6:7], v[12:15]\n" \
"v_mfma_f32_16x16x16_bf16 v[16:19], v[52:53], v[6:7], v[16:19]\nv_mfma_f32_16x16x16_bf16 v[20:23], a[180:181], v[6:7], v[20:23]\nv_mfma_f32_16x16x16_bf16 v[8:11], a[166:167], v[6:7], v[8:11]\nv_mfma_f32_16x16x16_bf16 v[12:15], a[174:175], v[6:7], v[12:15]\nv_mfma_f32_16x16x16_bf16 v[16:19], v[54:55], v[6:7], v[16:19]\nv_mfma_f32_16x16x16_bf16 v[20:23], a[182:183], v[6:7], v[20:23]\ns_waitcnt vmcnt(28)\ns_barrier\n" \
"s_add_u32 m0, %[lw], 12288\ns_add_u32 s44, %[wo], 5963776\nbuffer_load_dwordx4 v92, %[rw], s44 offen nt lds\ns_add_u32 m0, %[lw], 13312\ns_add_u32 s44, %[wo], 6021120\nbuffer_load_dwordx4 v92, %[rw], s44 offen nt lds\ns_add_u32 m0, %[lw], 14336\ns_add_u32 s44, %[wo], 6078464\n" \
"buffer_load_dwordx4 v92, %[rw], s44 offen nt lds\ns_add_u32 m0, %[lw], 15360\ns_add_u32 s44, %[wo], 6135808\nbuffer_load_dwordx4 v92, %[rw], s44 offen nt lds\nds_read_b64_tr_b16 a[184:185], v94 offset:16384\nds_read_b64_tr_b16 a[186:187], v94 offset:16896\nds_read_b64_tr_b16 a[192:193], v94 offset:16392\nds_read_b64_tr_b16 a[194:195], v94 offset:16904\n" \
"ds_read_b64_tr_b16 a[200:201], v94 offset:40960\nds_read_b64_tr_b16 a[202:203], v94 offset:41472\nds_read_b64_tr_b16 v[48:49], v94 offset:40968\nds_read_b64_tr_b16 v[50:51], v94 offset:41480\ns_waitcnt lgkmcnt(0)\nv_mfma_f32_16x16x16_bf16 v[8:11], a[184:185], v[6:7], v[8:11]\nv_mfma_f32_16x16x16_bf16 v[12:15], a[192:193], v[6:7], v[12:15]\nv_mfma_f32_16x16x16_bf16 v[16:19], a[200:201], v[6:7], v[16:19]\n" \
"v_mfma_f32_16x16x16_bf16 v[20:23], v[48:49], v[6:7], v[20:23]\nv_mfma_f32_16x16x16_bf16 v[8:11], a[186:187], v[6:7], v[8:11]\nv_mfma_f32_16x16x16_bf16 v[12:15], a[194:195], v[6:7], v[12:15]\nv_mfma_f32_16x16x16_bf16 v[16:19], a[202:203], v[6:7], v[16:19]\nv_mfma_f32_16x16x16_bf16 v[20:23], v[50:51], v[6:7], v[20:23]\ns_waitcnt vmcnt(28)\ns_barrier\ns_add_u32 m0, %[lw], 16384\n" \
"s_add_u32 s44, %[wo], 6193152\nbuffer_load_dwordx4 v92, %[rw], s44 offen nt lds\ns_add_u32 m0, %[lw], 17408\ns_add_u32 s44, %[wo], 6250496\nbuffer_load_dwordx4 v92, %[rw], s44 offen nt lds\ns_add_u32 m0, %[lw], 18432\ns_add_u32 s44, %[wo], 6307840\nbuffer_load_dwordx4 v92, %[rw], s44 offen nt lds\n" \
"s_add_u32 m0, %[lw], 19456\ns_add_u32 s44, %[wo], 6365184\nbuffer_load_dwordx4 v92, %[rw], s44 offen nt lds\nds_read_b64_tr_b16 a[188:189], v94 offset:20480\nds_read_b64_tr_b16 a[190:191], v94 offset:20992\nds_read_b64_tr_b16 a[196:197], v94 offset:20488\nds_read_b64_tr_b16 a[198:199], v94 offset:21000\nds_read_b64_tr_b16 a[204:205], v94 offset:45056\n" \
"ds_read_b64_tr_b16 a[206:207], v94 offset:45568\nds_read_b64_tr_b16 v[44:45], v94 offset:45064\nds_read_b64_tr_b16 v[46:47], v94 offset:45576\ns_waitcnt lgkmcnt(0)\nv_mfma_f32_16x16x16_bf16 v[8:11], a[188:189], v[6:7], v[8:11]\nv_mfma_f32_16x16x16_bf16 v[12:15], a[196:197], v[6:7], v[12:15]\nv_mfma_f32_16x16x16_bf16 v[16:19], a[204:205], v[6:7], v[16:19]\nv_mfma_f32_16x16x16_bf16 v[20:23], v[44:45], v[6:7], v[20:23]\n" \
"v_mfma_f32_16x16x16_bf16 v[8:11], a[190:191], v[6:7], v[8:11]\nv_mfma_f32_16x16x16_bf16 v[12:15], a[198:199], v[6:7], v[12:15]\nv_mfma_f32_16x16x16_bf16 v[16:19], a[206:207], v[6:7], v[16:19]\nv_mfma_f32_16x16x16_bf16 v[20:23], v[46:47], v[6:7], v[20:23]\ns_waitcnt vmcnt(28)\ns_barrier\ns_add_u32 m0, %[lw], 20480\ns_add_u32 s44, %[wo], 6422528\n" \
"buffer_load_dwordx4 v92, %[rw], s44 offen nt lds\ns_add_u32 m0, %[lw], 21504\ns_add_u32 s44, %[wo], 6479872\nbuffer_load_dwordx4 v92, %[rw], s44 offen nt lds\ns_add_u32 m0, %[lw], 22528\ns_add_u32 s44, %[wo], 6537216\nbuffer_load_dwordx4 v92, %[rw], s44 offen nt lds\ns_add_u32 m0, %[lw], 23552\n" \
"s_add_u32 s44, %[wo], 6594560\nbuffer_load_dwordx4 v92, %[rw], s44 offen nt lds\nds_read_b64_tr_b16 a[240:241], v93 offset:0\nds_read_b64_tr_b16 a[242:243], v93 offset:512\nds_read_b64_tr_b16 v[40:41], v93 offset:8\nds_read_b64_tr_b16 v[42:43], v93 offset:520\nds_read_b64_tr_b16 a[248:249], v93 offset:12288\nds_read_b64_tr_b16 a[250:251], v93 offset:12800\n" \
"ds_read_b64_tr_b16 v[96:97], v93 offset:12296\nds_read_b64_tr_b16 v[98:99], v93 offset:12808\ns_waitcnt lgkmcnt(0)\nv_mfma_f32_16x16x16_bf16 v[8:11], a[240:241], v[6:7], v[8:11]\nv_mfma_f32_16x16x16_bf16 v[12:15], v[40:41], v[6:7], v[12:15]\nv_mfma_f32_16x16x16_bf16 v[16:19], a[248:249], v[6:7], v[16:19]\nv_mfma_f32_16x16x16_bf16 v[20:23], v[96:97], v[6:7], v[20:23]\nv_mfma_f32_16x16x16_bf16 v[8:11], a[242:243], v[6:7], v[8:11]\n" \
"v_mfma_f32_16x16x16_bf16 v[12:15], v[42:43], v[6:7], v[12:15]\nv_mfma_f32_16x16x16_bf16 v[16:19], a[250:251], v[6:7], v[16:19]\nv_mfma_f32_16x16x16_bf16 v[20:23], v[98:99], v[6:7], v[20:23]\ns_waitcnt vmcnt(28)\ns_barrier\ns_add_u32 m0, %[tb], 0\ns_add_u32 s44, %[wo], 6651904\nbuffer_load_dwordx4 v92, %[rw], s44 offen nt lds\n" \
"s_add_u32 m0, %[tb], 1024\ns_add_u32 s44, %[wo], 6709248\nbuffer_load_dwordx4 v92, %[rw], s44 offen nt lds\ns_add_u32 m0, %[tb], 2048\ns_add_u32 s44, %[wo], 6766592\nbuffer_load_dwordx4 v92, %[rw], s44 offen nt lds\ns_add_u32 m0, %[tb], 3072\ns_add_u32 s44, %[wo], 6823936\n" \
"buffer_load_dwordx4 v92, %[rw], s44 offen nt lds\nds_read_b64_tr_b16 a[244:245], v93 offset:4096\nds_read_b64_tr_b16 a[246:247], v93 offset:4608\nds_read_b64_tr_b16 v[36:37], v93 offset:4104\nds_read_b64_tr_b16 v[38:39], v93 offset:4616\nds_read_b64_tr_b16 a[252:253], v93 offset:16384\nds_read_b64_tr_b16 a[254:255], v93 offset:16896\nds_read_b64_tr_b16 v[100:101], v93 offset:16392\n" \
"ds_read_b64_tr_b16 v[102:103], v93 offset:16904\ns_waitcnt lgkmcnt(0)\nv_mfma_f32_16x16x16_bf16 v[8:11], a[244:245], v[6:7], v[8:11]\nv_mfma_f32_16x16x16_bf16 v[12:15], v[36:37], v[6:7], v[12:15]\nv_mfma_f32_16x16x16_bf16 v[16:19], a[252:253], v[6:7], v[16:19]\nv_mfma_f32_16x16x16_bf16 v[20:23], v[100:101], v[6:7], v[20:23]\nv_mfma_f32_16x16x16_bf16 v[8:11], a[246:247], v[6:7], v[8:11]\nv_mfma_f32_16x16x16_bf16 v[12:15], v[38:39], v[6:7], v[12:15]\n" \
"v_mfma_f32_16x16x16_bf16 v[16:19], a[254:255], v[6:7], v[16:19]\nv_mfma_f32_16x16x16_bf16 v[20:23], v[102:103], v[6:7], v[20:23]\ns_waitcnt vmcnt(28)\ns_barrier\ns_add_u32 m0, %[tb], 4096\ns_add_u32 s44, %[wo], 6881280\nbuffer_load_dwordx4 v92, %[rw], s44 offen nt lds\ns_add_u32 m0, %[tb], 5120\n" \
"s_add_u32 s44, %[wo], 6938624\nbuffer_load_dwordx4 v92, %[rw], s44 offen nt lds\ns_add_u32 m0, %[tb], 6144\ns_add_u32 s44, %[wo], 6995968\nbuffer_load_dwordx4 v92, %[rw], s44 offen nt lds\ns_add_u32 m0, %[tb], 7168\ns_add_u32 s44, %[wo], 7053312\nbuffer_load_dwordx4 v92, %[rw], s44 offen nt lds\n" \
"ds_read_b64_tr_b16 v[104:105], v93 offset:8192\nds_read_b64_tr_b16 v[106:107], v93 offset:8704\nds_read_b64_tr_b16 v[112:113], v93 offset:8200\nds_read_b64_tr_b16 v[114:115], v93 offset:8712\nds_read_b64_tr_b16 v[32:33], v93 offset:20480\nds_read_b64_tr_b16 v[34:35], v93 offset:20992\nds_read_b64_tr_b16 v[120:121], v93 offset:20488\nds_read_b64_tr_b16 v[122:123], v93 offset:21000\n" \
"s_waitcnt lgkmcnt(0)\nv_mfma_f32_16x16x16_bf16 v[8:11], v[104:105], v[6:7], v[8:11]\nv_mfma_f32_16x16x16_bf16 v[12:15], v[112:113], v[6:7], v[12:15]\nv_mfma_f32_16x16x16_bf16 v[16:19], v[32:33], v[6:7], v[16:19]\nv_mfma_f32_16x16x16_bf16 v[20:23], v[120:121], v[6:7], v[20:23]\nv_mfma_f32_16x16x16_bf16 v[8:11], v[106:107], v[6:7], v[8:11]\nv_mfma_f32_16x16x16_bf16 v[12:15], v[114:115], v[6:7], v[12:15]\nv_mfma_f32_16x16x16_bf16 v[16:19], v[34:35], v[6:7], v[16:19]\n" \
"v_mfma_f32_16x16x16_bf16 v[20:23], v[122:123], v[6:7], v[20:23]\ns_waitcnt vmcnt(28)\ns_barrier\ns_add_u32 m0, %[tb], 8192\ns_add_u32 s44, %[wo], 7110656\nbuffer_load_dwordx4 v92, %[rw], s44 offen nt lds\ns_add_u32 m0, %[tb], 9216\ns_add_u32 s44, %[wo], 7168000\n" \
"buffer_load_dwordx4 v92, %[rw], s44 offen nt lds\ns_add_u32 m0, %[tb], 10240\ns_add_u32 s44, %[wo], 7225344\nbuffer_load_dwordx4 v92, %[rw], s44 offen nt lds\ns_add_u32 m0, %[tb], 11264\ns_add_u32 s44, %[wo], 7282688\nbuffer_load_dwordx4 v92, %[rw], s44 offen nt lds\nds_read_b64_tr_b16 v[108:109], v94 offset:0\n" \
"ds_read_b64_tr_b16 v[110:111], v94 offset:512\nds_read_b64_tr_b16 v[116:117], v94 offset:8\nds_read_b64_tr_b16 v[118:119], v94 offset:520\nds_read_b64_tr_b16 v[28:29], v94 offset:24576\nds_read_b64_tr_b16 v[30:31], v94 offset:25088\nds_read_b64_tr_b16 v[124:125], v94 offset:24584\nds_read_b64_tr_b16 v[126:127], v94 offset:25096\ns_waitcnt lgkmcnt(0)\n" \
"v_mfma_f32_16x16x16_bf16 v[8:11], v[108:109], v[6:7], v[8:11]\nv_mfma_f32_16x16x16_bf16 v[12:15], v[116:117], v[6:7], v[12:15]\nv_mfma_f32_16x16x16_bf16 v[16:19], v[28:29], v[6:7], v[16:19]\nv_mfma_f32_16x16x16_bf16 v[20:23], v[124:125], v[6:7], v[20:23]\nv_mfma_f32_16x16x16_bf16 v[8:11], v[110:111], v[6:7], v[8:11]\nv_mfma_f32_16x16x16_bf16 v[12:15], v[118:119], v[6:7], v[12:15]\nv_mfma_f32_16x16x16_bf16 v[16:19], v[30:31], v[6:7], v[16:19]\nv_mfma_f32_16x16x16_bf16 v[20:23], v[126:127], v[6:7], v[20:23]\n" \
"s_waitcnt vmcnt(28)\ns_barrier\nds_read_b64_tr_b16 v[128:129], v94 offset:4096\nds_read_b64_tr_b16 v[130:131], v94 offset:4608\nds_read_b64_tr_b16 v[136:137], v94 offset:4104\nds_read_b64_tr_b16 v[138:139], v94 offset:4616\nds_read_b64_tr_b16 v[144:145], v94 offset:28672\nds_read_b64_tr_b16 v[146:147], v94 offset:29184\n" \
"ds_read_b64_tr_b16 v[24:25], v94 offset:28680\nds_read_b64_tr_b16 v[26:27], v94 offset:29192\ns_waitcnt lgkmcnt(0)\nv_mfma_f32_16x16x16_bf16 v[8:11], v[128:129], v[6:7], v[8:11]\nv_mfma_f32_16x16x16_bf16 v[12:15], v[136:137], v[6:7], v[12:15]\nv_mfma_f32_16x16x16_bf16 v[16:19], v[144:145], v[6:7], v[16:19]\nv_mfma_f32_16x16x16_bf16 v[20:23], v[24:25], v[6:7], v[20:23]\nv_mfma_f32_16x16x16_bf16 v[8:11], v[130:131], v[6:7], v[8:11]\n" \
"v_mfma_f32_16x16x16_bf16 v[12:15], v[138:139], v[6:7], v[12:15]\nv_mfma_f32_16x16x16_bf16 v[16:19], v[146:147], v[6:7], v[16:19]\nv_mfma_f32_16x16x16_bf16 v[20:23], v[26:27], v[6:7], v[20:23]\ns_waitcnt vmcnt(24)\nds_write_b128 v1, v[88:91] offset:0\nds_write_b128 v1, v[84:87] offset:1024\nds_write_b128 v1, v[80:83] offset:2048\nds_write_b128 v1, v[76:79] offset:3072\n" \
"s_barrier\nds_read_b64_tr_b16 v[132:133], v94 offset:8192\nds_read_b64_tr_b16 v[134:135], v94 offset:8704\nds_read_b64_tr_b16 v[140:141], v94 offset:8200\nds_read_b64_tr_b16 v[142:143], v94 offset:8712\nds_read_b64_tr_b16 v[148:149], v94 offset:32768\nds_read_b64_tr_b16 v[150:151], v94 offset:33280\nds_read_b64_tr_b16 v[76:77], v94 offset:32776\n" \
"ds_read_b64_tr_b16 v[78:79], v94 offset:33288\ns_waitcnt lgkmcnt(0)\nv_mfma_f32_16x16x16_bf16 v[8:11], v[132:133], v[6:7], v[8:11]\nv_mfma_f32_16x16x16_bf16 v[12:15], v[140:141], v[6:7], v[12:15]\nv_mfma_f32_16x16x16_bf16 v[16:19], v[148:149], v[6:7], v[16:19]\nv_mfma_f32_16x16x16_bf16 v[20:23], v[76:77], v[6:7], v[20:23]\nv_mfma_f32_16x16x16_bf16 v[8:11], v[134:135], v[6:7], v[8:11]\nv_mfma_f32_16x16x16_bf16 v[12:15], v[142:143], v[6:7], v[12:15]\n" \
"v_mfma_f32_16x16x16_bf16 v[16:19], v[150:151], v[6:7], v[16:19]\nv_mfma_f32_16x16x16_bf16 v[20:23], v[78:79], v[6:7], v[20:23]\ns_waitcnt vmcnt(20)\nds_write_b128 v1, v[72:75] offset:4096\nds_write_b128 v1, v[68:71] offset:5120\nds_write_b128 v1, v[64:67] offset:6144\nds_write_b128 v1, v[60:63] offset:7168\ns_barrier\n" \
"ds_read_b64_tr_b16 v[184:185], v94 offset:12288\nds_read_b64_tr_b16 v[186:187], v94 offset:12800\nds_read_b64_tr_b16 v[60:61], v94 offset:12296\nds_read_b64_tr_b16 v[62:63], v94 offset:12808\nds_read_b64_tr_b16 v[192:193], v94 offset:36864\nds_read_b64_tr_b16 v[194:195], v94 offset:37376\nds_read_b64_tr_b16 v[200:201], v94 offset:36872\nds_read_b64_tr_b16 v[202:203], v94 offset:37384\n" \
"s_waitcnt lgkmcnt(0)\nv_mfma_f32_16x16x16_bf16 v[8:11], v[184:185], v[6:7], v[8:11]\nv_mfma_f32_16x16x16_bf16 v[12:15], v[60:61], v[6:7], v[12:15]\nv_mfma_f32_16x16x16_bf16 v[16:19], v[192:193], v[6:7], v[16:19]\nv_mfma_f32_16x16x16_bf16 v[20:23], v[200:201], v[6:7], v[20:23]\nv_mfma_f32_16x16x16_bf16 v[8:11], v[186:187], v[6:7], v[8:11]\nv_mfma_f32_16x16x16_bf16 v[12:15], v[62:63], v[6:7], v[12:15]\nv_mfma_f32_16x16x16_bf16 v[16:19], v[194:195], v[6:7], v[16:19]\n" \
"v_mfma_f32_16x16x16_bf16 v[20:23], v[202:203], v[6:7], v[20:23]\ns_waitcnt vmcnt(16)\nds_write_b128 v1, v[56:59] offset:8192\nds_write_b128 v1, v[52:55] offset:9216\nds_write_b128 v1, v[48:51] offset:10240\nds_write_b128 v1, v[44:47] offset:11264\ns_barrier\nds_read_b64_tr_b16 v[188:189], v94 offset:16384\n" \
"ds_read_b64_tr_b16 v[190:191], v94 offset:16896\nds_read_b64_tr_b16 v[44:45], v94 offset:16392\nds_read_b64_tr_b16 v[46:47], v94 offset:16904\nds_read_b64_tr_b16 v[196:197], v94 offset:40960\nds_read_b64_tr_b16 v[198:199], v94 offset:41472\nds_read_b64_tr_b16 v[204:205], v94 offset:40968\nds_read_b64_tr_b16 v[206:207], v94 offset:41480\ns_waitcnt lgkmcnt(0)\n" \
"v_mfma_f32_16x16x16_bf16 v[8:11], v[188:189], v[6:7], v[8:11]\nv_mfma_f32_16x16x16_bf16 v[12:15], v[44:45], v[6:7], v[12:15]\nv_mfma_f32_16x16x16_bf16 v[16:19], v[196:197], v[6:7], v[16:19]\nv_mfma_f32_16x16x16_bf16 v[20:23], v[204:205], v[6:7], v[20:23]\nv_mfma_f32_16x16x16_bf16 v[8:11], v[190:191], v[6:7], v[8:11]\nv_mfma_f32_16x16x16_bf16 v[12:15], v[46:47], v[6:7], v[12:15]\nv_mfma_f32_16x16x16_bf16 v[16:19], v[198:199], v[6:7], v[16:19]\nv_mfma_f32_16x16x16_bf16 v[20:23], v[206:207], v[6:7], v[20:23]\n" \
"s_waitcnt vmcnt(12)\nds_write_b128 v1, v[40:43] offset:12288\nds_write_b128 v1, v[36:39] offset:13312\nds_write_b128 v1, v[32:35] offset:14336\nds_write_b128 v1, v[28:31] offset:15360\ns_barrier\nds_read_b64_tr_b16 v[208:209], v94 offset:20480\nds_read_b64_tr_b16 v[210:211], v94 offset:20992\n" \
"ds_read_b64_tr_b16 v[216:217], v94 offset:20488\nds_read_b64_tr_b16 v[218:219], v94 offset:21000\nds_read_b64_tr_b16 v[28:29], v94 offset:45056\nds_read_b64_tr_b16 v[30:31], v94 offset:45568\nds_read_b64_tr_b16 v[224:225], v94 offset:45064\nds_read_b64_tr_b16 v[226:227], v94 offset:45576\ns_waitcnt lgkmcnt(0)\nv_mfma_f32_16x16x16_bf16 v[8:11], v[208:209], v[6:7], v[8:11]\n" \
"v_mfma_f32_16x16x16_bf16 v[12:15], v[216:217], v[6:7], v[12:15]\nv_mfma_f32_16x16x16_bf16 v[16:19], v[28:29], v[6:7], v[16:19]\nv_mfma_f32_16x16x16_bf16 v[20:23], v[224:225], v[6:7], v[20:23]\nv_mfma_f32_16x16x16_bf16 v[8:11], v[210:211], v[6:7], v[8:11]\nv_mfma_f32_16x16x16_bf16 v[12:15], v[218:219], v[6:7], v[12:15]\nv_mfma_f32_16x16x16_bf16 v[16:19], v[30:31], v[6:7], v[16:19]\nv_mfma_f32_16x16x16_bf16 v[20:23], v[226:227], v[6:7], v[20:23]\ns_waitcnt vmcnt(8)\n" \
"ds_write_b128 v1, v[24:27] offset:16384\nds_write_b128 v1, v[76:79] offset:17408\nds_write_b128 v1, v[60:63] offset:18432\nds_write_b128 v1, v[44:47] offset:19456\ns_barrier\nds_read_b64_tr_b16 v[212:213], v93 offset:0\nds_read_b64_tr_b16 v[214:215], v93 offset:512\nds_read_b64_tr_b16 v[220:221], v93 offset:8\n" \
"ds_read_b64_tr_b16 v[222:223], v93 offset:520\nds_read_b64_tr_b16 v[44:45], v93 offset:12288\nds_read_b64_tr_b16 v[46:47], v93 offset:12800\nds_read_b64_tr_b16 v[228:229], v93 offset:12296\nds_read_b64_tr_b16 v[230:231], v93 offset:12808\ns_waitcnt lgkmcnt(0)\nv_mfma_f32_16x16x16_bf16 v[8:11], v[212:213], v[6:7], v[8:11]\nv_mfma_f32_16x16x16_bf16 v[12:15], v[220:221], v[6:7], v[12:15]\n" \
"v_mfma_f32_16x16x16_bf16 v[16:19], v[44:45], v[6:7], v[16:19]\nv_mfma_f32_16x16x16_bf16 v[20:23], v[228:229], v[6:7], v[20:23]\nv_mfma_f32_16x16x16_bf16 v[8:11], v[214:215], v[6:7], v[8:11]\nv_mfma_f32_16x16x16_bf16 v[12:15], v[222:223], v[6:7], v[12:15]\nv_mfma_f32_16x16x16_bf16 v[16:19], v[46:47], v[6:7], v[16:19]\nv_mfma_f32_16x16x16_bf16 v[20:23], v[230:231], v[6:7], v[20:23]\ns_waitcnt vmcnt(4)\nds_write_b128 v1, v[28:31] offset:20480\n" \
"ds_write_b128 v1, v[44:47] offset:21504\ns_barrier\nds_read_b64_tr_b16 v[232:233], v93 offset:4096\nds_read_b64_tr_b16 v[234:235], v93 offset:4608\nds_read_b64_tr_b16 v[240:241], v93 offset:4104\nds_read_b64_tr_b16 v[242:243], v93 offset:4616\nds_read_b64_tr_b16 v[248:249], v93 offset:16384\nds_read_b64_tr_b16 v[250:251], v93 offset:16896\n" \
"ds_read_b64_tr_b16 v[44:45], v93 offset:16392\nds_read_b64_tr_b16 v[46:47], v93 offset:16904\ns_waitcnt lgkmcnt(0)\nv_mfma_f32_16x16x16_bf16 v[8:11], v[232:233], v[6:7], v[8:11]\nv_mfma_f32_16x16x16_bf16 v[12:15], v[240:241], v[6:7], v[12:15]\nv_mfma_f32_16x16x16_bf16 v[16:19], v[248:249], v[6:7], v[16:19]\nv_mfma_f32_16x16x16_bf16 v[20:23], v[44:45], v[6:7], v[20:23]\nv_mfma_f32_16x16x16_bf16 v[8:11], v[234:235], v[6:7], v[8:11]\n" \
"v_mfma_f32_16x16x16_bf16 v[12:15], v[242:243], v[6:7], v[12:15]\nv_mfma_f32_16x16x16_bf16 v[16:19], v[250:251], v[6:7], v[16:19]\nv_mfma_f32_16x16x16_bf16 v[20:23], v[46:47], v[6:7], v[20:23]\ns_waitcnt vmcnt(0)\nds_write_b128 v1, v[44:47] offset:22528\ns_barrier\nds_read_b64_tr_b16 v[236:237], v93 offset:8192\nds_read_b64_tr_b16 v[238:239], v93 offset:8704\n" \
"ds_read_b64_tr_b16 v[244:245], v93 offset:8200\nds_read_b64_tr_b16 v[246:247], v93 offset:8712\nds_read_b64_tr_b16 v[252:253], v93 offset:20480\nds_read_b64_tr_b16 v[254:255], v93 offset:20992\nds_read_b64_tr_b16 v[44:45], v93 offset:20488\nds_read_b64_tr_b16 v[46:47], v93 offset:21000\ns_waitcnt lgkmcnt(0)\nv_mfma_f32_16x16x16_bf16 v[8:11], v[236:237], v[6:7], v[8:11]\n" \
"v_mfma_f32_16x16x16_bf16 v[12:15], v[244:245], v[6:7], v[12:15]\nv_mfma_f32_16x16x16_bf16 v[16:19], v[252:253], v[6:7], v[16:19]\nv_mfma_f32_16x16x16_bf16 v[20:23], v[44:45], v[6:7], v[20:23]\nv_mfma_f32_16x16x16_bf16 v[8:11], v[238:239], v[6:7], v[8:11]\nv_mfma_f32_16x16x16_bf16 v[12:15], v[246:247], v[6:7], v[12:15]\nv_mfma_f32_16x16x16_bf16 v[16:19], v[254:255], v[6:7], v[16:19]\nv_mfma_f32_16x16x16_bf16 v[20:23], v[46:47], v[6:7], v[20:23]\nds_write_b128 v1, v[44:47] offset:23552\n" \
"s_waitcnt lgkmcnt(0)\ns_barrier\ns_mov_b32 s86, %[xn0]\ns_mov_b32 s87, %[ho0]\ns_mov_b32 s88, %[fh0]\ns_mov_b32 s89, %[b1]\ns_mov_b32 s40, %[b0]\ns_mov_b32 s43, 0\n" \
"s_lshl_b32 s57, %[w], 12\ns_mov_b32 s58, 0xbeeff558\ns_mov_b32 s59, 0x409d30fc\ns_mov_b32 s56, -2147483648\n.p2align 6\n.Lgg_next%=:\ns_cmp_lg_u32 %[w], 0\ns_cbranch_scc1 .Lgg_w1%=\n" \
"buffer_load_dword v56, v3, %[rs], 0 offen sc1\ns_waitcnt vmcnt(0)\ns_barrier\nbuffer_store_dword v90, off, %[rs], s56 sc1\ns_mov_b32 s56, -2147483648\n.Lgg_poll%=:\nv_cmp_gt_i32 vcc, s40, v56\ns_cbranch_vccz .Lgg_go%=\n" \
"s_cmp_ge_u32 s40, 62\ns_cbranch_scc1 .Lgg_ns1055%=\ns_sleep 6\n.Lgg_ns1055%=:\ns_sleep 1\nbuffer_load_dword v56, v3, %[rs], 0 offen sc1\ns_waitcnt vmcnt(0)\ns_add_u32 s43, s43, 1\n" \
"s_cmp_lt_u32 s43, 262144\ns_cbranch_scc1 .Lgg_poll%=\ns_branch .Lgg_abort%=\n.p2align 6\n.Lgg_w1%=:\ns_waitcnt vmcnt(0)\ns_barrier\n.p2align 6\n" \
".Lgg_go%=:\ns_add_u32 s44, s40, 0\ns_lshl_b32 s44, s44, 17\ns_add_u32 s44, s44, s86\ns_lshr_b32 s45, s44, 8\ns_and_b32 s45, s45, 0x2000\ns_xor_b32 s41, s44, s45\ns_add_u32 s61, s41, %[xw]\n" \
"s_barrier\ns_add_u32 m0, s57, 0\ns_add_u32 s60, s61, 0\nbuffer_load_dwordx4 v0, %[rs], s60 offen offset:0 sc1 lds\nbuffer_load_dwordx4 v0, %[rs], s60 offen offset:1024 sc1 lds\nbuffer_load_dwordx4 v0, %[rs], s60 offen offset:2048 sc1 lds\nbuffer_load_dwordx4 v0, %[rs], s60 offen offset:3072 sc1 lds\ns_add_u32 m0, s57, 16384\n" \
"s_add_u32 s60, s61, 32768\nbuffer_load_dwordx4 v0, %[rs], s60 offen offset:0 sc1 lds\nbuffer_load_dwordx4 v0, %[rs], s60 offen offset:1024 sc1 lds\nbuffer_load_dwordx4 v0, %[rs], s60 offen offset:2048 sc1 lds\nbuffer_load_dwordx4 v0, %[rs], s60 offen offset:3072 sc1 lds\nbuffer_load_dwordx4 v[40:43], v5, %[rs], s41 offen offset:0 sc1\nbuffer_load_dwordx4 v[44:47], v5, %[rs], s41 offen offset:256 sc1\ns_lshl_b32 s44, s40, 19\n" \
"s_add_u32 s44, s44, s87\ns_lshr_b32 s45, s44, 8\ns_and_b32 s45, s45, 0x2000\ns_xor_b32 s55, s44, s45\ns_waitcnt vmcnt(6)\ns_barrier\nds_read_b128 v[56:59], v2 offset:0\nds_read_b128 v[60:63], v2 offset:2048\n" \
"s_waitcnt lgkmcnt(1)\nv_mfma_f32_16x16x32_bf16 v[24:27], a[0:3], v[56:59], 0\ns_waitcnt lgkmcnt(0)\nds_read_b128 v[64:67], v2 offset:4096\nv_mfma_f32_16x16x32_bf16 v[24:27], a[4:7], v[60:63], v[24:27]\nds_read_b128 v[68:71], v2 offset:6144\nds_read_b128 v[72:75], v1 offset:0\nv_mfma_f32_16x16x32_bf16 v[28:31], a[8:11], v[56:59], 0\n" \
"ds_read_b128 v[76:79], v1 offset:1024\nbuffer_load_dwordx4 v[48:51], v5, %[rs], s41 offen offset:512 sc1\nv_mfma_f32_16x16x32_bf16 v[28:31], a[12:15], v[60:63], v[28:31]\nv_mfma_f32_16x16x32_bf16 v[32:35], a[16:19], v[56:59], 0\nv_mfma_f32_16x16x32_bf16 v[32:35], a[20:23], v[60:63], v[32:35]\nv_mfma_f32_16x16x32_bf16 v[36:39], a[24:27], v[56:59], 0\nbuffer_load_dwordx4 v[52:55], v5, %[rs], s41 offen offset:768 sc1\nv_mfma_f32_16x16x32_bf16 v[36:39], a[28:31], v[60:63], v[36:39]\n" \
"s_waitcnt lgkmcnt(3)\nds_read_b128 v[80:83], v1 offset:2048\nv_mfma_f32_16x16x32_bf16 v[24:27], a[32:35], v[64:67], v[24:27]\ns_waitcnt lgkmcnt(3)\nds_read_b128 v[56:59], v2 offset:8192\nv_mfma_f32_16x16x32_bf16 v[24:27], a[36:39], v[68:71], v[24:27]\ns_waitcnt lgkmcnt(3)\ns_add_u32 m0, s57, 32768\n" \
"ds_read_b128 v[60:63], v2 offset:10240\nv_mfma_f32_16x16x32_bf16 v[28:31], v[72:75], v[64:67], v[28:31]\ns_add_u32 s60, s61, 65536\ns_waitcnt lgkmcnt(3)\nds_read_b128 v[72:75], v1 offset:3072\nbuffer_load_dwordx4 v0, %[rs], s60 offen offset:0 sc1 lds\nv_mfma_f32_16x16x32_bf16 v[28:31], v[76:79], v[68:71], v[28:31]\nv_mfma_f32_16x16x32_bf16 v[32:35], a[40:43], v[64:67], v[32:35]\n" \
"v_mfma_f32_16x16x32_bf16 v[32:35], a[44:47], v[68:71], v[32:35]\nv_mfma_f32_16x16x32_bf16 v[36:39], a[48:51], v[64:67], v[36:39]\nbuffer_load_dwordx4 v0, %[rs], s60 offen offset:1024 sc1 lds\nv_mfma_f32_16x16x32_bf16 v[36:39], a[52:55], v[68:71], v[36:39]\ns_waitcnt lgkmcnt(2)\nv_mfma_f32_16x16x32_bf16 v[24:27], a[56:59], v[56:59], v[24:27]\ns_waitcnt lgkmcnt(1)\nds_read_b128 v[64:67], v2 offset:12288\n" \
"v_mfma_f32_16x16x32_bf16 v[24:27], a[60:63], v[60:63], v[24:27]\nds_read_b128 v[68:71], v2 offset:14336\nds_read_b128 v[76:79], v1 offset:4096\nv_mfma_f32_16x16x32_bf16 v[28:31], a[64:67], v[56:59], v[28:31]\nbuffer_load_dwordx4 v0, %[rs], s60 offen offset:2048 sc1 lds\nv_mfma_f32_16x16x32_bf16 v[28:31], a[68:71], v[60:63], v[28:31]\nv_mfma_f32_16x16x32_bf16 v[32:35], v[80:83], v[56:59], v[32:35]\ns_waitcnt lgkmcnt(3)\n" \
"ds_read_b128 v[80:83], v1 offset:5120\nv_mfma_f32_16x16x32_bf16 v[32:35], v[72:75], v[60:63], v[32:35]\nv_mfma_f32_16x16x32_bf16 v[36:39], a[72:75], v[56:59], v[36:39]\nbuffer_load_dwordx4 v0, %[rs], s60 offen offset:3072 sc1 lds\nv_mfma_f32_16x16x32_bf16 v[36:39], a[76:79], v[60:63], v[36:39]\ns_waitcnt lgkmcnt(3)\nv_mfma_f32_16x16x32_bf16 v[24:27], a[80:83], v[64:67], v[24:27]\ns_waitcnt vmcnt(8)\n" \
"s_barrier\ns_waitcnt lgkmcnt(2)\nds_read_b128 v[56:59], v2 offset:16384\nv_mfma_f32_16x16x32_bf16 v[24:27], a[84:87], v[68:71], v[24:27]\ns_add_u32 m0, s57, 49152\nds_read_b128 v[60:63], v2 offset:18432\nv_mfma_f32_16x16x32_bf16 v[28:31], a[88:91], v[64:67], v[28:31]\ns_add_u32 s60, s61, 98304\n" \
"buffer_load_dwordx4 v0, %[rs], s60 offen offset:0 sc1 lds\nv_mfma_f32_16x16x32_bf16 v[28:31], a[92:95], v[68:71], v[28:31]\nv_mfma_f32_16x16x32_bf16 v[32:35], a[96:99], v[64:67], v[32:35]\nv_mfma_f32_16x16x32_bf16 v[32:35], a[100:103], v[68:71], v[32:35]\ns_waitcnt lgkmcnt(3)\nds_read_b128 v[72:75], v1 offset:6144\nv_mfma_f32_16x16x32_bf16 v[36:39], v[76:79], v[64:67], v[36:39]\ns_waitcnt lgkmcnt(3)\n" \
"buffer_load_dwordx4 v0, %[rs], s60 offen offset:1024 sc1 lds\nv_mfma_f32_16x16x32_bf16 v[36:39], v[80:83], v[68:71], v[36:39]\ns_waitcnt lgkmcnt(2)\nds_read_b128 v[76:79], v1 offset:7168\nv_mfma_f32_16x16x32_bf16 v[24:27], a[104:107], v[56:59], v[24:27]\ns_waitcnt lgkmcnt(2)\nds_read_b128 v[64:67], v2 offset:20480\nv_mfma_f32_16x16x32_bf16 v[24:27], a[108:111], v[60:63], v[24:27]\n" \
"ds_read_b128 v[68:71], v2 offset:22528\nv_mfma_f32_16x16x32_bf16 v[28:31], a[112:115], v[56:59], v[28:31]\nbuffer_load_dwordx4 v0, %[rs], s60 offen offset:2048 sc1 lds\nv_mfma_f32_16x16x32_bf16 v[28:31], a[116:119], v[60:63], v[28:31]\nv_mfma_f32_16x16x32_bf16 v[32:35], a[120:123], v[56:59], v[32:35]\nv_mfma_f32_16x16x32_bf16 v[32:35], a[124:127], v[60:63], v[32:35]\nv_mfma_f32_16x16x32_bf16 v[36:39], a[128:131], v[56:59], v[36:39]\nbuffer_load_dwordx4 v0, %[rs], s60 offen offset:3072 sc1 lds\n" \
"v_mfma_f32_16x16x32_bf16 v[36:39], a[132:135], v[60:63], v[36:39]\ns_waitcnt lgkmcnt(1)\nds_read_b128 v[80:83], v1 offset:8192\nv_mfma_f32_16x16x32_bf16 v[24:27], a[136:139], v[64:67], v[24:27]\ns_waitcnt vmcnt(8)\ns_waitcnt lgkmcnt(1)\nds_read_b128 v[56:59], v2 offset:24576\nv_add_f32 v84, v40, v44\n" \
"v_mfma_f32_16x16x32_bf16 v[24:27], a[140:143], v[68:71], v[24:27]\nds_read_b128 v[60:63], v2 offset:26624\nv_add_f32 v85, v48, v52\nv_mfma_f32_16x16x32_bf16 v[28:31], v[72:75], v[64:67], v[28:31]\nds_read_b128 v[72:75], v1 offset:9216\nv_add_f32 v84, v84, v85\nv_mfma_f32_16x16x32_bf16 v[28:31], v[76:79], v[68:71], v[28:31]\nv_mov_b32 v85, v84\n" \
"v_mfma_f32_16x16x32_bf16 v[32:35], a[144:147], v[64:67], v[32:35]\ns_nop 0\nv_permlane16_swap_b32 v84, v85\nv_mfma_f32_16x16x32_bf16 v[32:35], a[148:151], v[68:71], v[32:35]\nv_add_f32 v84, v84, v85\nv_mov_b32 v85, v84\nv_mfma_f32_16x16x32_bf16 v[36:39], a[152:155], v[64:67], v[36:39]\ns_nop 0\n" \
"v_permlane32_swap_b32 v84, v85\nv_mfma_f32_16x16x32_bf16 v[36:39], a[156:159], v[68:71], v[36:39]\ns_waitcnt lgkmcnt(2)\nv_add_f32 v84, v84, v85\nv_mul_f32 v84, 0xbd800000, v84\nv_mfma_f32_16x16x32_bf16 v[24:27], a[160:163], v[56:59], v[24:27]\ns_waitcnt lgkmcnt(1)\nds_read_b128 v[64:67], v2 offset:28672\n" \
"v_add_f32 v40, v40, v84\nv_mfma_f32_16x16x32_bf16 v[24:27], a[164:167], v[60:63], v[24:27]\nds_read_b128 v[68:71], v2 offset:30720\nds_read_b128 v[76:79], v1 offset:10240\nv_mfma_f32_16x16x32_bf16 v[28:31], a[168:171], v[56:59], v[28:31]\nv_mul_f32 v40, 0x41000000, v40\nv_fma_f32 v41, v40, v40, v41\nv_mfma_f32_16x16x32_bf16 v[28:31], a[172:175], v[60:63], v[28:31]\n" \
"v_add_f32 v44, v44, v84\nv_mul_f32 v44, 0x41000000, v44\nv_mfma_f32_16x16x32_bf16 v[32:35], v[80:83], v[56:59], v[32:35]\ns_waitcnt lgkmcnt(3)\nds_read_b128 v[80:83], v1 offset:11264\nv_fma_f32 v45, v44, v44, v45\nv_mfma_f32_16x16x32_bf16 v[32:35], v[72:75], v[60:63], v[32:35]\nv_add_f32 v48, v48, v84\n" \
"v_mul_f32 v48, 0x41000000, v48\nv_mfma_f32_16x16x32_bf16 v[36:39], a[176:179], v[56:59], v[36:39]\nv_fma_f32 v49, v48, v48, v49\nv_add_f32 v52, v52, v84\nv_mfma_f32_16x16x32_bf16 v[36:39], a[180:183], v[60:63], v[36:39]\ns_waitcnt lgkmcnt(3)\nv_mul_f32 v52, 0x41000000, v52\nv_fma_f32 v53, v52, v52, v53\n" \
"v_mfma_f32_16x16x32_bf16 v[24:27], a[184:187], v[64:67], v[24:27]\ns_waitcnt vmcnt(4)\ns_barrier\ns_waitcnt lgkmcnt(2)\nds_read_b128 v[56:59], v2 offset:32768\nv_mfma_f32_16x16x32_bf16 v[24:27], a[188:191], v[68:71], v[24:27]\nds_read_b128 v[60:63], v2 offset:34816\nv_add_f32 v85, v41, v45\n" \
"v_mfma_f32_16x16x32_bf16 v[28:31], a[192:195], v[64:67], v[28:31]\nv_add_f32 v86, v49, v53\nv_add_f32 v85, v85, v86\nv_mfma_f32_16x16x32_bf16 v[28:31], a[196:199], v[68:71], v[28:31]\nv_mov_b32 v86, v85\nv_mfma_f32_16x16x32_bf16 v[32:35], a[200:203], v[64:67], v[32:35]\ns_nop 0\nv_permlane16_swap_b32 v85, v86\n" \
"v_mfma_f32_16x16x32_bf16 v[32:35], a[204:207], v[68:71], v[32:35]\ns_waitcnt lgkmcnt(3)\nds_read_b128 v[72:75], v1 offset:12288\nv_add_f32 v85, v85, v86\nv_mfma_f32_16x16x32_bf16 v[36:39], v[76:79], v[64:67], v[36:39]\ns_waitcnt lgkmcnt(3)\nv_mov_b32 v86, v85\nv_mfma_f32_16x16x32_bf16 v[36:39], v[80:83], v[68:71], v[36:39]\n" \
"s_waitcnt lgkmcnt(2)\nds_read_b128 v[76:79], v1 offset:13312\nv_mfma_f32_16x16x32_bf16 v[24:27], a[208:211], v[56:59], v[24:27]\ns_waitcnt lgkmcnt(2)\nds_read_b128 v[64:67], v2 offset:36864\nv_mfma_f32_16x16x32_bf16 v[24:27], a[212:215], v[60:63], v[24:27]\nds_read_b128 v[68:71], v2 offset:38912\nv_mfma_f32_16x16x32_bf16 v[28:31], a[216:219], v[56:59], v[28:31]\n" \
"v_permlane32_swap_b32 v85, v86\nv_mfma_f32_16x16x32_bf16 v[28:31], a[220:223], v[60:63], v[28:31]\nv_add_f32 v85, v85, v86\nv_mul_f32 v85, 0x3a800000, v85\nv_mfma_f32_16x16x32_bf16 v[32:35], a[224:227], v[56:59], v[32:35]\nv_cvt_pk_bf16_f32 v85, v85, v85\nv_and_b32 v85, 0xffff0000, v85\nv_mfma_f32_16x16x32_bf16 v[32:35], a[228:231], v[60:63], v[32:35]\n" \
"v_add_f32 v85, 0x3727c5ac, v85\nv_cvt_pk_bf16_f32 v85, v85, v85\nv_mfma_f32_16x16x32_bf16 v[36:39], a[232:235], v[56:59], v[36:39]\nv_and_b32 v85, 0xffff0000, v85\nv_cvt_pk_bf16_f32 v84, v84, v84\nv_mfma_f32_16x16x32_bf16 v[36:39], a[236:239], v[60:63], v[36:39]\ns_waitcnt lgkmcnt(1)\nds_read_b128 v[80:83], v1 offset:14336\n" \
"v_and_b32 v84, 0xffff0000, v84\nv_mfma_f32_16x16x32_bf16 v[24:27], a[240:243], v[64:67], v[24:27]\ns_waitcnt lgkmcnt(1)\nds_read_b128 v[56:59], v2 offset:40960\nv_mfma_f32_16x16x32_bf16 v[24:27], a[244:247], v[68:71], v[24:27]\nds_read_b128 v[60:63], v2 offset:43008\nv_mfma_f32_16x16x32_bf16 v[28:31], v[72:75], v[64:67], v[28:31]\nds_read_b128 v[72:75], v1 offset:15360\n" \
"v_mfma_f32_16x16x32_bf16 v[28:31], v[76:79], v[68:71], v[28:31]\nv_sqrt_f32 v85, v85\nv_mfma_f32_16x16x32_bf16 v[32:35], a[248:251], v[64:67], v[32:35]\nv_cvt_pk_bf16_f32 v85, v85, v85\nv_and_b32 v85, 0xffff0000, v85\nv_mfma_f32_16x16x32_bf16 v[32:35], a[252:255], v[68:71], v[32:35]\nv_mul_f32 v86, s58, v85\nv_add_f32 v87, v42, v46\n" \
"v_mfma_f32_16x16x32_bf16 v[36:39], v[96:99], v[64:67], v[36:39]\nv_rcp_f32 v85, v85\nv_mfma_f32_16x16x32_bf16 v[36:39], v[100:103], v[68:71], v[36:39]\ns_waitcnt lgkmcnt(2)\nv_mul_f32 v85, s58, v85\nv_add_f32 v88, v50, v54\nv_mfma_f32_16x16x32_bf16 v[24:27], v[104:107], v[56:59], v[24:27]\ns_waitcnt lgkmcnt(1)\n" \
"ds_read_b128 v[64:67], v2 offset:45056\nv_add_f32 v87, v87, v88\nv_mfma_f32_16x16x32_bf16 v[24:27], v[108:111], v[60:63], v[24:27]\nds_read_b128 v[68:71], v2 offset:47104\nds_read_b128 v[76:79], v1 offset:16384\nv_mfma_f32_16x16x32_bf16 v[28:31], v[112:115], v[56:59], v[28:31]\nv_mov_b32 v88, v87\nv_mfma_f32_16x16x32_bf16 v[28:31], v[116:119], v[60:63], v[28:31]\n" \
"s_nop 0\nv_permlane16_swap_b32 v87, v88\nv_mfma_f32_16x16x32_bf16 v[32:35], v[80:83], v[56:59], v[32:35]\ns_waitcnt lgkmcnt(3)\nds_read_b128 v[80:83], v1 offset:17408\nv_add_f32 v87, v87, v88\nv_mfma_f32_16x16x32_bf16 v[32:35], v[72:75], v[60:63], v[32:35]\nv_mov_b32 v88, v87\n" \
"v_mfma_f32_16x16x32_bf16 v[36:39], v[120:123], v[56:59], v[36:39]\ns_nop 0\nv_permlane32_swap_b32 v87, v88\nv_mfma_f32_16x16x32_bf16 v[36:39], v[124:127], v[60:63], v[36:39]\ns_waitcnt lgkmcnt(3)\nv_add_f32 v87, v87, v88\nv_mul_f32 v87, 0xbd800000, v87\nv_mfma_f32_16x16x32_bf16 v[24:27], v[128:131], v[64:67], v[24:27]\n" \
"s_waitcnt vmcnt(0)\ns_barrier\ns_waitcnt lgkmcnt(2)\nds_read_b128 v[56:59], v2 offset:49152\nv_mfma_f32_16x16x32_bf16 v[24:27], v[132:135], v[68:71], v[24:27]\nds_read_b128 v[60:63], v2 offset:51200\nv_add_f32 v42, v42, v87\nv_mfma_f32_16x16x32_bf16 v[28:31], v[136:139], v[64:67], v[28:31]\n" \
"v_mul_f32 v42, 0x41000000, v42\nv_fma_f32 v43, v42, v42, v43\nv_mfma_f32_16x16x32_bf16 v[28:31], v[140:143], v[68:71], v[28:31]\nv_add_f32 v46, v46, v87\nv_mul_f32 v46, 0x41000000, v46\nv_mfma_f32_16x16x32_bf16 v[32:35], v[144:147], v[64:67], v[32:35]\nv_fma_f32 v47, v46, v46, v47\nv_add_f32 v50, v50, v87\n" \
"v_mfma_f32_16x16x32_bf16 v[32:35], v[148:151], v[68:71], v[32:35]\ns_waitcnt lgkmcnt(3)\nds_read_b128 v[72:75], v1 offset:18432\nv_mul_f32 v50, 0x41000000, v50\nv_mfma_f32_16x16x32_bf16 v[36:39], v[76:79], v[64:67], v[36:39]\ns_waitcnt lgkmcnt(3)\nv_fma_f32 v51, v50, v50, v51\nv_add_f32 v54, v54, v87\n" \
"v_mfma_f32_16x16x32_bf16 v[36:39], v[80:83], v[68:71], v[36:39]\ns_waitcnt lgkmcnt(2)\nds_read_b128 v[76:79], v1 offset:19456\nv_mul_f32 v54, 0x41000000, v54\nv_mfma_f32_16x16x32_bf16 v[24:27], v[152:155], v[56:59], v[24:27]\ns_waitcnt lgkmcnt(2)\nds_read_b128 v[64:67], v2 offset:53248\nv_fma_f32 v55, v54, v54, v55\n" \
"v_mfma_f32_16x16x32_bf16 v[24:27], v[156:159], v[60:63], v[24:27]\nds_read_b128 v[68:71], v2 offset:55296\nv_add_f32 v88, v43, v47\nv_mfma_f32_16x16x32_bf16 v[28:31], v[160:163], v[56:59], v[28:31]\nv_add_f32 v89, v51, v55\nv_add_f32 v88, v88, v89\nv_mfma_f32_16x16x32_bf16 v[28:31], v[164:167], v[60:63], v[28:31]\nv_mov_b32 v89, v88\n" \
"v_mfma_f32_16x16x32_bf16 v[32:35], v[168:171], v[56:59], v[32:35]\ns_nop 0\nv_permlane16_swap_b32 v88, v89\nv_mfma_f32_16x16x32_bf16 v[32:35], v[172:175], v[60:63], v[32:35]\nv_add_f32 v88, v88, v89\nv_mov_b32 v89, v88\nv_mfma_f32_16x16x32_bf16 v[36:39], v[176:179], v[56:59], v[36:39]\ns_nop 0\n" \
"v_permlane32_swap_b32 v88, v89\nv_mfma_f32_16x16x32_bf16 v[36:39], v[180:183], v[60:63], v[36:39]\ns_waitcnt lgkmcnt(1)\nds_read_b128 v[80:83], v1 offset:20480\nv_add_f32 v88, v88, v89\nv_mfma_f32_16x16x32_bf16 v[24:27], v[184:187], v[64:67], v[24:27]\ns_waitcnt lgkmcnt(1)\nds_read_b128 v[56:59], v2 offset:57344\n" \
"v_mul_f32 v88, 0x3a800000, v88\nv_mfma_f32_16x16x32_bf16 v[24:27], v[188:191], v[68:71], v[24:27]\nds_read_b128 v[60:63], v2 offset:59392\nv_cvt_pk_bf16_f32 v88, v88, v88\nv_mfma_f32_16x16x32_bf16 v[28:31], v[72:75], v[64:67], v[28:31]\nds_read_b128 v[72:75], v1 offset:21504\nv_and_b32 v88, 0xffff0000, v88\nv_mfma_f32_16x16x32_bf16 v[28:31], v[76:79], v[68:71], v[28:31]\n" \
"v_add_f32 v88, 0x3727c5ac, v88\nv_cvt_pk_bf16_f32 v88, v88, v88\nv_mfma_f32_16x16x32_bf16 v[32:35], v[192:195], v[64:67], v[32:35]\nv_and_b32 v88, 0xffff0000, v88\nv_cvt_pk_bf16_f32 v87, v87, v87\nv_mfma_f32_16x16x32_bf16 v[32:35], v[196:199], v[68:71], v[32:35]\nv_sqrt_f32 v88, v88\nv_mfma_f32_16x16x32_bf16 v[36:39], v[200:203], v[64:67], v[36:39]\n" \
"v_and_b32 v87, 0xffff0000, v87\nv_cvt_pk_bf16_f32 v88, v88, v88\nv_mfma_f32_16x16x32_bf16 v[36:39], v[204:207], v[68:71], v[36:39]\ns_waitcnt lgkmcnt(2)\nv_and_b32 v88, 0xffff0000, v88\nv_mul_f32 v89, s58, v88\nv_mfma_f32_16x16x32_bf16 v[24:27], v[208:211], v[56:59], v[24:27]\ns_waitcnt lgkmcnt(1)\n" \
"ds_read_b128 v[64:67], v2 offset:61440\nv_mfma_f32_16x16x32_bf16 v[24:27], v[212:215], v[60:63], v[24:27]\nds_read_b128 v[68:71], v2 offset:63488\nds_read_b128 v[76:79], v1 offset:22528\nv_mfma_f32_16x16x32_bf16 v[28:31], v[216:219], v[56:59], v[28:31]\nv_rcp_f32 v88, v88\nv_mfma_f32_16x16x32_bf16 v[28:31], v[220:223], v[60:63], v[28:31]\nv_mul_f32 v88, s58, v88\n" \
"v_mfma_f32_16x16x32_bf16 v[32:35], v[80:83], v[56:59], v[32:35]\ns_waitcnt lgkmcnt(3)\nds_read_b128 v[80:83], v1 offset:23552\nv_mfma_f32_16x16x32_bf16 v[32:35], v[72:75], v[60:63], v[32:35]\nv_mfma_f32_16x16x32_bf16 v[36:39], v[224:227], v[56:59], v[36:39]\nv_mfma_f32_16x16x32_bf16 v[36:39], v[228:231], v[60:63], v[36:39]\ns_waitcnt lgkmcnt(3)\nv_mfma_f32_16x16x32_bf16 v[24:27], v[232:235], v[64:67], v[24:27]\n" \
"s_waitcnt lgkmcnt(2)\nds_read_b128 v[56:59], v2 offset:256\nv_mfma_f32_16x16x32_bf16 v[24:27], v[236:239], v[68:71], v[24:27]\nds_read_b128 v[60:63], v2 offset:2304\nv_mfma_f32_16x16x32_bf16 v[28:31], v[240:243], v[64:67], v[28:31]\nv_mfma_f32_16x16x32_bf16 v[28:31], v[244:247], v[68:71], v[28:31]\nv_mfma_f32_16x16x32_bf16 v[32:35], v[248:251], v[64:67], v[32:35]\nv_mfma_f32_16x16x32_bf16 v[32:35], v[252:255], v[68:71], v[32:35]\n" \
"s_waitcnt lgkmcnt(3)\nds_read_b128 v[72:75], v1 offset:0\nv_mfma_f32_16x16x32_bf16 v[36:39], v[76:79], v[64:67], v[36:39]\ns_waitcnt lgkmcnt(3)\nv_mfma_f32_16x16x32_bf16 v[36:39], v[80:83], v[68:71], v[36:39]\ns_waitcnt lgkmcnt(2)\nds_read_b128 v[76:79], v1 offset:1024\nv_mfma_f32_16x16x32_bf16 v[40:43], a[0:3], v[56:59], 0\n" \
"s_waitcnt lgkmcnt(2)\nds_read_b128 v[64:67], v2 offset:4352\nv_mfma_f32_16x16x32_bf16 v[40:43], a[4:7], v[60:63], v[40:43]\ns_lshl_b32 s44, s40, 1\nds_read_b128 v[68:71], v2 offset:6400\nv_mov_b32 v90, s44\nv_mfma_f32_16x16x32_bf16 v[44:47], a[8:11], v[56:59], 0\nv_mfma_f32_16x16x32_bf16 v[44:47], a[12:15], v[60:63], v[44:47]\n" \
"v_fma_f32 v24, v8, v84, v24\nv_fma_f32 v25, v9, v84, v25\nv_mfma_f32_16x16x32_bf16 v[48:51], a[16:19], v[56:59], 0\nv_fma_f32 v32, v16, v84, v32\nv_fma_f32 v33, v17, v84, v33\nv_mfma_f32_16x16x32_bf16 v[48:51], a[20:23], v[60:63], v[48:51]\nv_mul_f32 v24, v24, v85\nv_mul_f32 v25, v25, v85\n" \
"v_mfma_f32_16x16x32_bf16 v[52:55], a[24:27], v[56:59], 0\nv_fma_f32 v91, v24, v24, s59\nv_fma_f32 v92, v25, v25, s59\nv_mfma_f32_16x16x32_bf16 v[52:55], a[28:31], v[60:63], v[52:55]\ns_waitcnt lgkmcnt(1)\nds_read_b128 v[80:83], v1 offset:2048\nv_mul_f32 v91, v91, v24\nv_mfma_f32_16x16x32_bf16 v[40:43], a[32:35], v[64:67], v[40:43]\n" \
"s_waitcnt lgkmcnt(1)\nds_read_b128 v[56:59], v2 offset:8448\nv_mul_f32 v92, v92, v25\nv_mfma_f32_16x16x32_bf16 v[40:43], a[36:39], v[68:71], v[40:43]\nds_read_b128 v[60:63], v2 offset:10496\nv_mul_f32 v24, v24, v32\nv_mfma_f32_16x16x32_bf16 v[44:47], v[72:75], v[64:67], v[44:47]\nds_read_b128 v[72:75], v1 offset:3072\n" \
"v_mul_f32 v25, v25, v33\nv_mfma_f32_16x16x32_bf16 v[44:47], v[76:79], v[68:71], v[44:47]\nv_exp_f32 v91, v91\nv_mfma_f32_16x16x32_bf16 v[48:51], a[40:43], v[64:67], v[48:51]\nv_exp_f32 v92, v92\nv_mfma_f32_16x16x32_bf16 v[48:51], a[44:47], v[68:71], v[48:51]\nv_fma_f32 v91, v91, v86, v86\nv_fma_f32 v92, v92, v86, v86\n" \
"v_mfma_f32_16x16x32_bf16 v[52:55], a[48:51], v[64:67], v[52:55]\nv_rcp_f32 v91, v91\nv_mfma_f32_16x16x32_bf16 v[52:55], a[52:55], v[68:71], v[52:55]\ns_waitcnt lgkmcnt(2)\nv_rcp_f32 v92, v92\nv_mfma_f32_16x16x32_bf16 v[40:43], a[56:59], v[56:59], v[40:43]\ns_waitcnt lgkmcnt(1)\nds_read_b128 v[64:67], v2 offset:12544\n" \
"v_mul_f32 v24, v24, v91\nv_mfma_f32_16x16x32_bf16 v[40:43], a[60:63], v[60:63], v[40:43]\nds_read_b128 v[68:71], v2 offset:14592\nds_read_b128 v[76:79], v1 offset:4096\nv_mfma_f32_16x16x32_bf16 v[44:47], a[64:67], v[56:59], v[44:47]\nv_mul_f32 v25, v25, v92\nv_cvt_pk_bf16_f32 v32, v24, v25\nv_mfma_f32_16x16x32_bf16 v[44:47], a[68:71], v[60:63], v[44:47]\n" \
"v_fma_f32 v26, v10, v84, v26\nv_fma_f32 v27, v11, v84, v27\nv_mfma_f32_16x16x32_bf16 v[48:51], v[80:83], v[56:59], v[48:51]\ns_waitcnt lgkmcnt(3)\nds_read_b128 v[80:83], v1 offset:5120\nv_fma_f32 v34, v18, v84, v34\nv_mfma_f32_16x16x32_bf16 v[48:51], v[72:75], v[60:63], v[48:51]\nv_fma_f32 v35, v19, v84, v35\n" \
"v_mul_f32 v26, v26, v85\nv_mfma_f32_16x16x32_bf16 v[52:55], a[72:75], v[56:59], v[52:55]\nv_mul_f32 v27, v27, v85\nv_fma_f32 v91, v26, v26, s59\nv_mfma_f32_16x16x32_bf16 v[52:55], a[76:79], v[60:63], v[52:55]\ns_waitcnt lgkmcnt(3)\nv_fma_f32 v92, v27, v27, s59\nv_mul_f32 v91, v91, v26\n" \
"v_mfma_f32_16x16x32_bf16 v[40:43], a[80:83], v[64:67], v[40:43]\ns_waitcnt lgkmcnt(2)\nds_read_b128 v[56:59], v2 offset:16640\nv_mul_f32 v92, v92, v27\nv_mfma_f32_16x16x32_bf16 v[40:43], a[84:87], v[68:71], v[40:43]\nds_read_b128 v[60:63], v2 offset:18688\nv_mul_f32 v26, v26, v34\nv_mfma_f32_16x16x32_bf16 v[44:47], a[88:91], v[64:67], v[44:47]\n" \
"v_exp_f32 v91, v91\nv_mfma_f32_16x16x32_bf16 v[44:47], a[92:95], v[68:71], v[44:47]\nv_exp_f32 v92, v92\nv_mfma_f32_16x16x32_bf16 v[48:51], a[96:99], v[64:67], v[48:51]\nv_mul_f32 v27, v27, v35\nv_fma_f32 v91, v91, v86, v86\nv_mfma_f32_16x16x32_bf16 v[48:51], a[100:103], v[68:71], v[48:51]\ns_waitcnt lgkmcnt(3)\n" \
"ds_read_b128 v[72:75], v1 offset:6144\nv_fma_f32 v92, v92, v86, v86\nv_mfma_f32_16x16x32_bf16 v[52:55], v[76:79], v[64:67], v[52:55]\ns_waitcnt lgkmcnt(3)\nv_rcp_f32 v91, v91\nv_mfma_f32_16x16x32_bf16 v[52:55], v[80:83], v[68:71], v[52:55]\ns_waitcnt lgkmcnt(2)\nds_read_b128 v[76:79], v1 offset:7168\n" \
"v_mul_f32 v26, v26, v91\nv_mfma_f32_16x16x32_bf16 v[40:43], a[104:107], v[56:59], v[40:43]\ns_waitcnt lgkmcnt(2)\nds_read_b128 v[64:67], v2 offset:20736\nv_fma_f32 v28, v12, v84, v28\nv_mfma_f32_16x16x32_bf16 v[40:43], a[108:111], v[60:63], v[40:43]\nds_read_b128 v[68:71], v2 offset:22784\nv_fma_f32 v29, v13, v84, v29\n" \
"v_mfma_f32_16x16x32_bf16 v[44:47], a[112:115], v[56:59], v[44:47]\nv_rcp_f32 v92, v92\nv_mfma_f32_16x16x32_bf16 v[44:47], a[116:119], v[60:63], v[44:47]\nv_mul_f32 v27, v27, v92\nv_cvt_pk_bf16_f32 v33, v26, v27\nv_mfma_f32_16x16x32_bf16 v[48:51], a[120:123], v[56:59], v[48:51]\nv_fma_f32 v36, v20, v84, v36\nv_fma_f32 v37, v21, v84, v37\n" \
"v_mfma_f32_16x16x32_bf16 v[48:51], a[124:127], v[60:63], v[48:51]\nv_mul_f32 v28, v28, v85\nv_mul_f32 v29, v29, v85\nv_mfma_f32_16x16x32_bf16 v[52:55], a[128:131], v[56:59], v[52:55]\nv_fma_f32 v91, v28, v28, s59\nv_fma_f32 v92, v29, v29, s59\nv_mfma_f32_16x16x32_bf16 v[52:55], a[132:135], v[60:63], v[52:55]\ns_waitcnt lgkmcnt(1)\n" \
"ds_read_b128 v[80:83], v1 offset:8192\nv_mul_f32 v91, v91, v28\nv_mfma_f32_16x16x32_bf16 v[40:43], a[136:139], v[64:67], v[40:43]\ns_waitcnt lgkmcnt(1)\nds_read_b128 v[56:59], v2 offset:24832\nv_mul_f32 v92, v92, v29\nv_mfma_f32_16x16x32_bf16 v[40:43], a[140:143], v[68:71], v[40:43]\nds_read_b128 v[60:63], v2 offset:26880\n" \
"v_mul_f32 v28, v28, v36\nv_mfma_f32_16x16x32_bf16 v[44:47], v[72:75], v[64:67], v[44:47]\nds_read_b128 v[72:75], v1 offset:9216\nv_mul_f32 v29, v29, v37\nv_mfma_f32_16x16x32_bf16 v[44:47], v[76:79], v[68:71], v[44:47]\nv_exp_f32 v91, v91\nv_mfma_f32_16x16x32_bf16 v[48:51], a[144:147], v[64:67], v[48:51]\nv_exp_f32 v92, v92\n" \
"v_mfma_f32_16x16x32_bf16 v[48:51], a[148:151], v[68:71], v[48:51]\nv_fma_f32 v91, v91, v86, v86\nv_fma_f32 v92, v92, v86, v86\nv_mfma_f32_16x16x32_bf16 v[52:55], a[152:155], v[64:67], v[52:55]\nv_rcp_f32 v91, v91\nv_mfma_f32_16x16x32_bf16 v[52:55], a[156:159], v[68:71], v[52:55]\ns_waitcnt lgkmcnt(2)\nv_rcp_f32 v92, v92\n" \
"v_mfma_f32_16x16x32_bf16 v[40:43], a[160:163], v[56:59], v[40:43]\ns_waitcnt lgkmcnt(1)\nds_read_b128 v[64:67], v2 offset:28928\nv_mul_f32 v28, v28, v91\nv_mfma_f32_16x16x32_bf16 v[40:43], a[164:167], v[60:63], v[40:43]\nds_read_b128 v[68:71], v2 offset:30976\nds_read_b128 v[76:79], v1 offset:10240\nv_mfma_f32_16x16x32_bf16 v[44:47], a[168:171], v[56:59], v[44:47]\n" \
"v_mul_f32 v29, v29, v92\nv_cvt_pk_bf16_f32 v34, v28, v29\nv_mfma_f32_16x16x32_bf16 v[44:47], a[172:175], v[60:63], v[44:47]\nv_fma_f32 v30, v14, v84, v30\nv_fma_f32 v31, v15, v84, v31\nv_mfma_f32_16x16x32_bf16 v[48:51], v[80:83], v[56:59], v[48:51]\ns_waitcnt lgkmcnt(3)\nds_read_b128 v[80:83], v1 offset:11264\n" \
"v_fma_f32 v38, v22, v84, v38\nv_mfma_f32_16x16x32_bf16 v[48:51], v[72:75], v[60:63], v[48:51]\nv_fma_f32 v39, v23, v84, v39\nv_mul_f32 v30, v30, v85\nv_mfma_f32_16x16x32_bf16 v[52:55], a[176:179], v[56:59], v[52:55]\nv_mul_f32 v31, v31, v85\nv_fma_f32 v91, v30, v30, s59\nv_mfma_f32_16x16x32_bf16 v[52:55], a[180:183], v[60:63], v[52:55]\n" \
"s_waitcnt lgkmcnt(3)\nv_fma_f32 v92, v31, v31, s59\nv_mul_f32 v91, v91, v30\nv_mfma_f32_16x16x32_bf16 v[40:43], a[184:187], v[64:67], v[40:43]\ns_waitcnt lgkmcnt(2)\nds_read_b128 v[56:59], v2 offset:33024\nv_mul_f32 v92, v92, v31\nv_mfma_f32_16x16x32_bf16 v[40:43], a[188:191], v[68:71], v[40:43]\n" \
"ds_read_b128 v[60:63], v2 offset:35072\nv_mul_f32 v30, v30, v38\nv_mfma_f32_16x16x32_bf16 v[44:47], a[192:195], v[64:67], v[44:47]\nv_exp_f32 v91, v91\nv_mfma_f32_16x16x32_bf16 v[44:47], a[196:199], v[68:71], v[44:47]\nv_exp_f32 v92, v92\nv_mfma_f32_16x16x32_bf16 v[48:51], a[200:203], v[64:67], v[48:51]\nv_mul_f32 v31, v31, v39\n" \
"v_fma_f32 v91, v91, v86, v86\nv_mfma_f32_16x16x32_bf16 v[48:51], a[204:207], v[68:71], v[48:51]\ns_waitcnt lgkmcnt(3)\nds_read_b128 v[72:75], v1 offset:12288\nv_fma_f32 v92, v92, v86, v86\nv_mfma_f32_16x16x32_bf16 v[52:55], v[76:79], v[64:67], v[52:55]\ns_waitcnt lgkmcnt(3)\nv_rcp_f32 v91, v91\n" \
"v_mfma_f32_16x16x32_bf16 v[52:55], v[80:83], v[68:71], v[52:55]\ns_waitcnt lgkmcnt(2)\nds_read_b128 v[76:79], v1 offset:13312\nv_mul_f32 v30, v30, v91\nv_mfma_f32_16x16x32_bf16 v[40:43], a[208:211], v[56:59], v[40:43]\ns_waitcnt lgkmcnt(2)\nds_read_b128 v[64:67], v2 offset:37120\nv_mfma_f32_16x16x32_bf16 v[40:43], a[212:215], v[60:63], v[40:43]\n" \
"ds_read_b128 v[68:71], v2 offset:39168\nv_mfma_f32_16x16x32_bf16 v[44:47], a[216:219], v[56:59], v[44:47]\nv_rcp_f32 v92, v92\nv_mfma_f32_16x16x32_bf16 v[44:47], a[220:223], v[60:63], v[44:47]\nv_mul_f32 v31, v31, v92\nv_cvt_pk_bf16_f32 v35, v30, v31\nv_mfma_f32_16x16x32_bf16 v[48:51], a[224:227], v[56:59], v[48:51]\nbuffer_store_dwordx4 v[32:35], v4, %[rs], s55 offen offset:0 sc1\n" \
"v_mfma_f32_16x16x32_bf16 v[48:51], a[228:231], v[60:63], v[48:51]\nds_read_b128 v[80:83], v1 offset:14336\nv_mfma_f32_16x16x32_bf16 v[52:55], a[232:235], v[56:59], v[52:55]\nv_mfma_f32_16x16x32_bf16 v[52:55], a[236:239], v[60:63], v[52:55]\ns_waitcnt lgkmcnt(2)\nv_mfma_f32_16x16x32_bf16 v[40:43], a[240:243], v[64:67], v[40:43]\ns_waitcnt lgkmcnt(1)\nds_read_b128 v[56:59], v2 offset:41216\n" \
"v_mfma_f32_16x16x32_bf16 v[40:43], a[244:247], v[68:71], v[40:43]\nds_read_b128 v[60:63], v2 offset:43264\nv_mfma_f32_16x16x32_bf16 v[44:47], v[72:75], v[64:67], v[44:47]\nds_read_b128 v[72:75], v1 offset:15360\nv_mfma_f32_16x16x32_bf16 v[44:47], v[76:79], v[68:71], v[44:47]\nv_mfma_f32_16x16x32_bf16 v[48:51], a[248:251], v[64:67], v[48:51]\nds_read_b128 v[24:27], v2 offset:45312\nv_mfma_f32_16x16x32_bf16 v[48:51], a[252:255], v[68:71], v[48:51]\n" \
"ds_read_b128 v[28:31], v2 offset:47360\nv_mfma_f32_16x16x32_bf16 v[52:55], v[96:99], v[64:67], v[52:55]\nv_mfma_f32_16x16x32_bf16 v[52:55], v[100:103], v[68:71], v[52:55]\ns_waitcnt lgkmcnt(4)\nv_mfma_f32_16x16x32_bf16 v[40:43], v[104:107], v[56:59], v[40:43]\ns_waitcnt lgkmcnt(3)\nds_read_b128 v[64:67], v2 offset:49408\nv_mfma_f32_16x16x32_bf16 v[40:43], v[108:111], v[60:63], v[40:43]\n" \
"ds_read_b128 v[68:71], v2 offset:51456\nv_mfma_f32_16x16x32_bf16 v[48:51], v[80:83], v[56:59], v[48:51]\ns_waitcnt lgkmcnt(4)\nv_mfma_f32_16x16x32_bf16 v[48:51], v[72:75], v[60:63], v[48:51]\ns_waitcnt lgkmcnt(3)\nv_mfma_f32_16x16x32_bf16 v[40:43], v[128:131], v[24:27], v[40:43]\ns_waitcnt lgkmcnt(2)\nds_read_b128 v[56:59], v2 offset:53504\n" \
"v_mfma_f32_16x16x32_bf16 v[40:43], v[132:135], v[28:31], v[40:43]\nds_read_b128 v[60:63], v2 offset:55552\nds_read_b128 v[76:79], v1 offset:20480\nv_mfma_f32_16x16x32_bf16 v[48:51], v[144:147], v[24:27], v[48:51]\nv_mfma_f32_16x16x32_bf16 v[48:51], v[148:151], v[28:31], v[48:51]\ns_waitcnt lgkmcnt(4)\nds_read_b128 v[80:83], v1 offset:21504\nv_mfma_f32_16x16x32_bf16 v[40:43], v[152:155], v[64:67], v[40:43]\n" \
"s_waitcnt lgkmcnt(4)\nds_read_b128 v[24:27], v2 offset:57600\nv_mfma_f32_16x16x32_bf16 v[40:43], v[156:159], v[68:71], v[40:43]\nds_read_b128 v[28:31], v2 offset:59648\nv_mfma_f32_16x16x32_bf16 v[48:51], v[168:171], v[64:67], v[48:51]\nv_mfma_f32_16x16x32_bf16 v[48:51], v[172:175], v[68:71], v[48:51]\ns_waitcnt lgkmcnt(5)\nv_mfma_f32_16x16x32_bf16 v[40:43], v[184:187], v[56:59], v[40:43]\n" \
"s_waitcnt lgkmcnt(4)\nds_read_b128 v[64:67], v2 offset:61696\nv_mfma_f32_16x16x32_bf16 v[40:43], v[188:191], v[60:63], v[40:43]\nds_read_b128 v[68:71], v2 offset:63744\nv_mfma_f32_16x16x32_bf16 v[48:51], v[192:195], v[56:59], v[48:51]\nv_mfma_f32_16x16x32_bf16 v[48:51], v[196:199], v[60:63], v[48:51]\ns_waitcnt lgkmcnt(3)\nv_mfma_f32_16x16x32_bf16 v[40:43], v[208:211], v[24:27], v[40:43]\n" \
"s_waitcnt lgkmcnt(2)\nds_read_b128 v[56:59], v2 offset:41216\nv_mfma_f32_16x16x32_bf16 v[40:43], v[212:215], v[28:31], v[40:43]\nds_read_b128 v[60:63], v2 offset:43264\nds_read_b128 v[72:75], v1 offset:16384\nv_mfma_f32_16x16x32_bf16 v[48:51], v[76:79], v[24:27], v[48:51]\nv_mfma_f32_16x16x32_bf16 v[48:51], v[80:83], v[28:31], v[48:51]\ns_waitcnt lgkmcnt(4)\n" \
"ds_read_b128 v[76:79], v1 offset:17408\nv_mfma_f32_16x16x32_bf16 v[40:43], v[232:235], v[64:67], v[40:43]\ns_waitcnt lgkmcnt(4)\nds_read_b128 v[24:27], v2 offset:45312\nv_mfma_f32_16x16x32_bf16 v[40:43], v[236:239], v[68:71], v[40:43]\ns_waitcnt vmcnt(0)\nds_read_b128 v[28:31], v2 offset:47360\ns_barrier\n" \
"buffer_store_dword v90, off, %[rs], s88 sc1\nv_mfma_f32_16x16x32_bf16 v[48:51], v[248:251], v[64:67], v[48:51]\nv_mfma_f32_16x16x32_bf16 v[48:51], v[252:255], v[68:71], v[48:51]\ns_waitcnt lgkmcnt(5)\nds_read_b128 v[80:83], v1 offset:18432\nv_mfma_f32_16x16x32_bf16 v[44:47], v[112:115], v[56:59], v[44:47]\ns_waitcnt lgkmcnt(5)\nds_read_b128 v[64:67], v2 offset:49408\n" \
"v_mfma_f32_16x16x32_bf16 v[44:47], v[116:119], v[60:63], v[44:47]\nds_read_b128 v[68:71], v2 offset:51456\nv_fma_f32 v40, v8, v87, v40\nv_mfma_f32_16x16x32_bf16 v[52:55], v[120:123], v[56:59], v[52:55]\nv_fma_f32 v41, v9, v87, v41\nv_fma_f32 v48, v16, v87, v48\nv_mfma_f32_16x16x32_bf16 v[52:55], v[124:127], v[60:63], v[52:55]\ns_waitcnt lgkmcnt(4)\n" \
"v_fma_f32 v49, v17, v87, v49\nv_mul_f32 v40, v40, v88\nv_mfma_f32_16x16x32_bf16 v[44:47], v[136:139], v[24:27], v[44:47]\ns_waitcnt lgkmcnt(3)\nds_read_b128 v[56:59], v2 offset:53504\nv_mul_f32 v41, v41, v88\nv_mfma_f32_16x16x32_bf16 v[44:47], v[140:143], v[28:31], v[44:47]\nds_read_b128 v[60:63], v2 offset:55552\n" \
"v_fma_f32 v91, v40, v40, s59\nv_mfma_f32_16x16x32_bf16 v[52:55], v[72:75], v[24:27], v[52:55]\nds_read_b128 v[72:75], v1 offset:19456\nv_fma_f32 v92, v41, v41, s59\nv_mfma_f32_16x16x32_bf16 v[52:55], v[76:79], v[28:31], v[52:55]\ns_waitcnt lgkmcnt(4)\nv_mul_f32 v91, v91, v40\nv_mul_f32 v92, v92, v41\n" \
"v_mfma_f32_16x16x32_bf16 v[44:47], v[160:163], v[64:67], v[44:47]\ns_waitcnt lgkmcnt(3)\nds_read_b128 v[24:27], v2 offset:57600\nv_mul_f32 v40, v40, v48\nv_mfma_f32_16x16x32_bf16 v[44:47], v[164:167], v[68:71], v[44:47]\nds_read_b128 v[28:31], v2 offset:59648\nds_read_b128 v[76:79], v1 offset:22528\nv_mfma_f32_16x16x32_bf16 v[52:55], v[176:179], v[64:67], v[52:55]\n" \
"v_exp_f32 v91, v91\nv_mfma_f32_16x16x32_bf16 v[52:55], v[180:183], v[68:71], v[52:55]\ns_waitcnt lgkmcnt(5)\nv_exp_f32 v92, v92\nv_mfma_f32_16x16x32_bf16 v[44:47], v[80:83], v[56:59], v[44:47]\ns_waitcnt lgkmcnt(3)\nds_read_b128 v[64:67], v2 offset:61696\nds_read_b128 v[80:83], v1 offset:23552\n" \
"v_mfma_f32_16x16x32_bf16 v[44:47], v[72:75], v[60:63], v[44:47]\nds_read_b128 v[68:71], v2 offset:63744\nv_mul_f32 v41, v41, v49\nv_mfma_f32_16x16x32_bf16 v[52:55], v[200:203], v[56:59], v[52:55]\nv_fma_f32 v91, v91, v89, v89\nv_fma_f32 v92, v92, v89, v89\nv_mfma_f32_16x16x32_bf16 v[52:55], v[204:207], v[60:63], v[52:55]\ns_waitcnt lgkmcnt(5)\n" \
"v_rcp_f32 v91, v91\nv_mfma_f32_16x16x32_bf16 v[44:47], v[216:219], v[24:27], v[44:47]\ns_waitcnt lgkmcnt(4)\nv_rcp_f32 v92, v92\nv_mfma_f32_16x16x32_bf16 v[44:47], v[220:223], v[28:31], v[44:47]\nv_mul_f32 v40, v40, v91\nv_mul_f32 v41, v41, v92\nv_mfma_f32_16x16x32_bf16 v[52:55], v[224:227], v[24:27], v[52:55]\n" \
"v_cvt_pk_bf16_f32 v48, v40, v41\nv_fma_f32 v42, v10, v87, v42\nv_mfma_f32_16x16x32_bf16 v[52:55], v[228:231], v[28:31], v[52:55]\ns_waitcnt lgkmcnt(2)\nv_fma_f32 v43, v11, v87, v43\nv_fma_f32 v50, v18, v87, v50\nv_mfma_f32_16x16x32_bf16 v[44:47], v[240:243], v[64:67], v[44:47]\ns_waitcnt lgkmcnt(0)\n" \
"v_fma_f32 v51, v19, v87, v51\nv_mul_f32 v42, v42, v88\nv_mfma_f32_16x16x32_bf16 v[44:47], v[244:247], v[68:71], v[44:47]\nv_mul_f32 v43, v43, v88\nv_fma_f32 v91, v42, v42, s59\nv_mfma_f32_16x16x32_bf16 v[52:55], v[76:79], v[64:67], v[52:55]\nv_fma_f32 v92, v43, v43, s59\nv_mul_f32 v91, v91, v42\n" \
"v_mfma_f32_16x16x32_bf16 v[52:55], v[80:83], v[68:71], v[52:55]\nv_mul_f32 v92, v92, v43\nv_mul_f32 v42, v42, v50\nv_exp_f32 v91, v91\nv_exp_f32 v92, v92\nv_mul_f32 v43, v43, v51\nv_fma_f32 v91, v91, v89, v89\nv_fma_f32 v92, v92, v89, v89\n" \
"v_rcp_f32 v91, v91\nv_rcp_f32 v92, v92\nv_mul_f32 v42, v42, v91\nv_mul_f32 v43, v43, v92\nv_cvt_pk_bf16_f32 v49, v42, v43\nv_fma_f32 v44, v12, v87, v44\nv_fma_f32 v45, v13, v87, v45\nv_fma_f32 v52, v20, v87, v52\n" \
"v_fma_f32 v53, v21, v87, v53\nv_mul_f32 v44, v44, v88\nv_mul_f32 v45, v45, v88\nv_fma_f32 v91, v44, v44, s59\nv_fma_f32 v92, v45, v45, s59\nv_mul_f32 v91, v91, v44\nv_mul_f32 v92, v92, v45\nv_exp_f32 v91, v91\n" \
"v_exp_f32 v92, v92\nv_mul_f32 v44, v44, v52\nv_mul_f32 v45, v45, v53\nv_fma_f32 v91, v91, v89, v89\nv_fma_f32 v92, v92, v89, v89\nv_rcp_f32 v91, v91\nv_rcp_f32 v92, v92\nv_mul_f32 v44, v44, v91\n" \
"v_mul_f32 v45, v45, v92\nv_cvt_pk_bf16_f32 v50, v44, v45\nv_fma_f32 v46, v14, v87, v46\nv_fma_f32 v47, v15, v87, v47\nv_fma_f32 v54, v22, v87, v54\nv_fma_f32 v55, v23, v87, v55\nv_mul_f32 v46, v46, v88\nv_mul_f32 v47, v47, v88\n" \
"v_fma_f32 v91, v46, v46, s59\nv_fma_f32 v92, v47, v47, s59\nv_mul_f32 v91, v91, v46\nv_mul_f32 v92, v92, v47\nv_exp_f32 v91, v91\nv_exp_f32 v92, v92\nv_mul_f32 v46, v46, v54\nv_mul_f32 v47, v47, v55\n" \
"v_fma_f32 v91, v91, v89, v89\nv_fma_f32 v92, v92, v89, v89\nv_rcp_f32 v91, v91\nv_rcp_f32 v92, v92\nv_mul_f32 v46, v46, v91\nv_mul_f32 v47, v47, v92\nv_cvt_pk_bf16_f32 v51, v46, v47\nbuffer_store_dwordx4 v[48:51], v4, %[rs], s55 offen offset:1024 sc1\n" \
"v_add_u32 v90, 1, v90\ns_mov_b32 s56, s88\ns_add_u32 s40, s40, 1\ns_cmp_lt_u32 s40, s89\ns_cbranch_scc1 .Lgg_next%=\ns_cmp_eq_u32 s89, 64\ns_cbranch_scc1 .Lgg_fin%=\ns_mov_b32 s89, 64\n" \
"s_mov_b32 s40, 0\ns_xor_b32 s86, s86, 0x2000\ns_xor_b32 s87, s87, 0x2000\ns_mov_b32 s88, %[fh1]\nv_xor_b32 v3, 0x2000, v3\ns_branch .Lgg_next%=\n.Lgg_fin%=:\ns_waitcnt vmcnt(0)\n" \
"s_barrier\nbuffer_store_dword v90, off, %[rs], s56 sc1\ns_branch .Lgg_end%=\n.Lgg_abort%=:\ns_mov_b64 exec, -1\nv_mov_b32 v16, 0\ns_movk_i32 s44, 6400\nbuffer_store_dword v16, off, %[rs], s44 sc1\n" \
".Lgg_end%=:\ns_waitcnt vmcnt(0) lgkmcnt(0)\n" \
""
#define DD_ASM \
"v_mbcnt_lo_u32_b32 v255, -1, 0\nv_mbcnt_hi_u32_b32 v255, -1, v255\nv_lshlrev_b32 v255, 2, v255\nv_add_u32 v255, %[tb], v255\nds_read_b32 v0, v255 offset:0\nds_read_b32 v1, v255 offset:256\nds_read_b32 v2, v255 offset:512\nds_read_b32 v3, v255 offset:768\n" \
"ds_read_b32 v4, v255 offset:1024\nds_read_b32 v5, v255 offset:1280\nds_read_b32 v6, v255 offset:1536\nds_read_b32 v7, v255 offset:1792\nds_read_b32 v125, v255 offset:2048\nds_read_b32 v126, v255 offset:2304\nds_read_b32 v127, v255 offset:2560\ns_waitcnt lgkmcnt(0)\n" \
"s_add_u32 m0, %[tb], 8192\ns_add_u32 s44, %[wo], 0\nbuffer_load_dwordx4 v125, %[rw], s44 offen nt lds\ns_add_u32 m0, %[tb], 9216\ns_add_u32 s44, %[wo], 16384\nbuffer_load_dwordx4 v125, %[rw], s44 offen nt lds\ns_add_u32 m0, %[tb], 10240\ns_add_u32 s44, %[wo], 32768\n" \
"buffer_load_dwordx4 v125, %[rw], s44 offen nt lds\ns_add_u32 m0, %[tb], 11264\ns_add_u32 s44, %[wo], 49152\nbuffer_load_dwordx4 v125, %[rw], s44 offen nt lds\ns_add_u32 m0, %[lw], 0\ns_add_u32 s44, %[wo], 65536\nbuffer_load_dwordx4 v125, %[rw], s44 offen nt lds\ns_add_u32 m0, %[lw], 1024\n" \
"s_add_u32 s44, %[wo], 81920\nbuffer_load_dwordx4 v125, %[rw], s44 offen nt lds\ns_add_u32 m0, %[lw], 2048\ns_add_u32 s44, %[wo], 98304\nbuffer_load_dwordx4 v125, %[rw], s44 offen nt lds\ns_add_u32 m0, %[lw], 3072\ns_add_u32 s44, %[wo], 114688\nbuffer_load_dwordx4 v125, %[rw], s44 offen nt lds\n" \
"s_add_u32 m0, %[lw], 4096\ns_add_u32 s44, %[wo], 131072\nbuffer_load_dwordx4 v125, %[rw], s44 offen nt lds\ns_add_u32 m0, %[lw], 5120\ns_add_u32 s44, %[wo], 147456\nbuffer_load_dwordx4 v125, %[rw], s44 offen nt lds\ns_add_u32 m0, %[lw], 6144\ns_add_u32 s44, %[wo], 163840\n" \
"buffer_load_dwordx4 v125, %[rw], s44 offen nt lds\ns_add_u32 m0, %[lw], 7168\ns_add_u32 s44, %[wo], 180224\nbuffer_load_dwordx4 v125, %[rw], s44 offen nt lds\ns_add_u32 m0, %[lw], 8192\ns_add_u32 s44, %[wo], 196608\nbuffer_load_dwordx4 v125, %[rw], s44 offen nt lds\ns_add_u32 m0, %[lw], 9216\n" \
"s_add_u32 s44, %[wo], 212992\nbuffer_load_dwordx4 v125, %[rw], s44 offen nt lds\ns_add_u32 m0, %[lw], 10240\ns_add_u32 s44, %[wo], 229376\nbuffer_load_dwordx4 v125, %[rw], s44 offen nt lds\ns_add_u32 m0, %[lw], 11264\ns_add_u32 s44, %[wo], 245760\nbuffer_load_dwordx4 v125, %[rw], s44 offen nt lds\n" \
"s_add_u32 m0, %[lw], 12288\ns_add_u32 s44, %[wo], 262144\nbuffer_load_dwordx4 v125, %[rw], s44 offen nt lds\ns_add_u32 m0, %[lw], 13312\ns_add_u32 s44, %[wo], 278528\nbuffer_load_dwordx4 v125, %[rw], s44 offen nt lds\ns_add_u32 m0, %[lw], 14336\ns_add_u32 s44, %[wo], 294912\n" \
"buffer_load_dwordx4 v125, %[rw], s44 offen nt lds\ns_add_u32 m0, %[lw], 15360\ns_add_u32 s44, %[wo], 311296\nbuffer_load_dwordx4 v125, %[rw], s44 offen nt lds\ns_add_u32 m0, %[lw], 16384\ns_add_u32 s44, %[wo], 327680\nbuffer_load_dwordx4 v125, %[rw], s44 offen nt lds\ns_add_u32 m0, %[lw], 17408\n" \
"s_add_u32 s44, %[wo], 344064\nbuffer_load_dwordx4 v125, %[rw], s44 offen nt lds\ns_add_u32 m0, %[lw], 18432\ns_add_u32 s44, %[wo], 360448\nbuffer_load_dwordx4 v125, %[rw], s44 offen nt lds\ns_add_u32 m0, %[lw], 19456\ns_add_u32 s44, %[wo], 376832\nbuffer_load_dwordx4 v125, %[rw], s44 offen nt lds\n" \
"s_add_u32 m0, %[lw], 20480\ns_add_u32 s44, %[wo], 393216\nbuffer_load_dwordx4 v125, %[rw], s44 offen nt lds\ns_add_u32 m0, %[lw], 21504\ns_add_u32 s44, %[wo], 409600\nbuffer_load_dwordx4 v125, %[rw], s44 offen nt lds\ns_add_u32 m0, %[lw], 22528\ns_add_u32 s44, %[wo], 425984\n" \
"buffer_load_dwordx4 v125, %[rw], s44 offen nt lds\ns_add_u32 m0, %[lw], 23552\ns_add_u32 s44, %[wo], 442368\nbuffer_load_dwordx4 v125, %[rw], s44 offen nt lds\ns_add_u32 m0, %[tb], 0\ns_add_u32 s44, %[wo], 458752\nbuffer_load_dwordx4 v125, %[rw], s44 offen nt lds\ns_add_u32 m0, %[tb], 1024\n" \
"s_add_u32 s44, %[wo], 475136\nbuffer_load_dwordx4 v125, %[rw], s44 offen nt lds\ns_add_u32 m0, %[tb], 2048\ns_add_u32 s44, %[wo], 491520\nbuffer_load_dwordx4 v125, %[rw], s44 offen nt lds\ns_add_u32 m0, %[tb], 3072\ns_add_u32 s44, %[wo], 507904\nbuffer_load_dwordx4 v125, %[rw], s44 offen nt lds\n" \
"s_add_u32 m0, %[tb], 4096\ns_add_u32 s44, %[wo], 524288\nbuffer_load_dwordx4 v125, %[rw], s44 offen nt lds\ns_add_u32 m0, %[tb], 5120\ns_add_u32 s44, %[wo], 540672\nbuffer_load_dwordx4 v125, %[rw], s44 offen nt lds\ns_add_u32 m0, %[tb], 6144\ns_add_u32 s44, %[wo], 557056\n" \
"buffer_load_dwordx4 v125, %[rw], s44 offen nt lds\ns_add_u32 m0, %[tb], 7168\ns_add_u32 s44, %[wo], 573440\nbuffer_load_dwordx4 v125, %[rw], s44 offen nt lds\ns_waitcnt vmcnt(32)\nds_read_b64_tr_b16 a[0:1], v126 offset:8192\nds_read_b64_tr_b16 a[2:3], v126 offset:8704\nds_read_b64_tr_b16 a[8:9], v126 offset:8224\n" \
"ds_read_b64_tr_b16 a[10:11], v126 offset:8736\nds_read_b64_tr_b16 a[16:17], v126 offset:8256\nds_read_b64_tr_b16 a[18:19], v126 offset:8768\nds_read_b64_tr_b16 a[24:25], v126 offset:8288\nds_read_b64_tr_b16 a[26:27], v126 offset:8800\ns_waitcnt lgkmcnt(0)\ns_add_u32 m0, %[tb], 8192\ns_add_u32 s44, %[wo], 589824\n" \
"buffer_load_dwordx4 v125, %[rw], s44 offen nt lds\ns_add_u32 m0, %[tb], 9216\ns_add_u32 s44, %[wo], 606208\nbuffer_load_dwordx4 v125, %[rw], s44 offen nt lds\ns_add_u32 m0, %[tb], 10240\ns_add_u32 s44, %[wo], 622592\nbuffer_load_dwordx4 v125, %[rw], s44 offen nt lds\ns_add_u32 m0, %[tb], 11264\n" \
"s_add_u32 s44, %[wo], 638976\nbuffer_load_dwordx4 v125, %[rw], s44 offen nt lds\ns_waitcnt vmcnt(32)\nds_read_b64_tr_b16 a[4:5], v127 offset:0\nds_read_b64_tr_b16 a[6:7], v127 offset:512\nds_read_b64_tr_b16 a[12:13], v127 offset:32\nds_read_b64_tr_b16 a[14:15], v127 offset:544\nds_read_b64_tr_b16 a[20:21], v127 offset:64\n" \
"ds_read_b64_tr_b16 a[22:23], v127 offset:576\nds_read_b64_tr_b16 a[28:29], v127 offset:96\nds_read_b64_tr_b16 a[30:31], v127 offset:608\ns_waitcnt lgkmcnt(0)\ns_add_u32 m0, %[lw], 0\ns_add_u32 s44, %[wo], 655360\nbuffer_load_dwordx4 v125, %[rw], s44 offen nt lds\ns_add_u32 m0, %[lw], 1024\n" \
"s_add_u32 s44, %[wo], 671744\nbuffer_load_dwordx4 v125, %[rw], s44 offen nt lds\ns_add_u32 m0, %[lw], 2048\ns_add_u32 s44, %[wo], 688128\nbuffer_load_dwordx4 v125, %[rw], s44 offen nt lds\ns_add_u32 m0, %[lw], 3072\ns_add_u32 s44, %[wo], 704512\nbuffer_load_dwordx4 v125, %[rw], s44 offen nt lds\n" \
"s_waitcnt vmcnt(32)\nds_read_b64_tr_b16 a[32:33], v127 offset:4096\nds_read_b64_tr_b16 a[34:35], v127 offset:4608\nds_read_b64_tr_b16 v[92:93], v127 offset:4128\nds_read_b64_tr_b16 v[94:95], v127 offset:4640\nds_read_b64_tr_b16 a[40:41], v127 offset:4160\nds_read_b64_tr_b16 a[42:43], v127 offset:4672\nds_read_b64_tr_b16 a[48:49], v127 offset:4192\n" \
"ds_read_b64_tr_b16 a[50:51], v127 offset:4704\ns_waitcnt lgkmcnt(0)\ns_add_u32 m0, %[lw], 4096\ns_add_u32 s44, %[wo], 720896\nbuffer_load_dwordx4 v125, %[rw], s44 offen nt lds\ns_add_u32 m0, %[lw], 5120\ns_add_u32 s44, %[wo], 737280\nbuffer_load_dwordx4 v125, %[rw], s44 offen nt lds\n" \
"s_add_u32 m0, %[lw], 6144\ns_add_u32 s44, %[wo], 753664\nbuffer_load_dwordx4 v125, %[rw], s44 offen nt lds\ns_add_u32 m0, %[lw], 7168\ns_add_u32 s44, %[wo], 770048\nbuffer_load_dwordx4 v125, %[rw], s44 offen nt lds\ns_waitcnt vmcnt(32)\nds_read_b64_tr_b16 a[36:37], v127 offset:8192\n" \
"ds_read_b64_tr_b16 a[38:39], v127 offset:8704\nds_read_b64_tr_b16 v[88:89], v127 offset:8224\nds_read_b64_tr_b16 v[90:91], v127 offset:8736\nds_read_b64_tr_b16 a[44:45], v127 offset:8256\nds_read_b64_tr_b16 a[46:47], v127 offset:8768\nds_read_b64_tr_b16 a[52:53], v127 offset:8288\nds_read_b64_tr_b16 a[54:55], v127 offset:8800\ns_waitcnt lgkmcnt(0)\n" \
"s_add_u32 m0, %[lw], 8192\ns_add_u32 s44, %[wo], 786432\nbuffer_load_dwordx4 v125, %[rw], s44 offen nt lds\ns_add_u32 m0, %[lw], 9216\ns_add_u32 s44, %[wo], 802816\nbuffer_load_dwordx4 v125, %[rw], s44 offen nt lds\ns_add_u32 m0, %[lw], 10240\ns_add_u32 s44, %[wo], 819200\n" \
"buffer_load_dwordx4 v125, %[rw], s44 offen nt lds\ns_add_u32 m0, %[lw], 11264\ns_add_u32 s44, %[wo], 835584\nbuffer_load_dwordx4 v125, %[rw], s44 offen nt lds\ns_waitcnt vmcnt(32)\nds_read_b64_tr_b16 a[56:57], v127 offset:12288\nds_read_b64_tr_b16 a[58:59], v127 offset:12800\nds_read_b64_tr_b16 a[64:65], v127 offset:12320\n" \
"ds_read_b64_tr_b16 a[66:67], v127 offset:12832\nds_read_b64_tr_b16 a[72:73], v127 offset:12352\nds_read_b64_tr_b16 a[74:75], v127 offset:12864\nds_read_b64_tr_b16 a[80:81], v127 offset:12384\nds_read_b64_tr_b16 a[82:83], v127 offset:12896\ns_waitcnt lgkmcnt(0)\ns_add_u32 m0, %[lw], 12288\ns_add_u32 s44, %[wo], 851968\n" \
"buffer_load_dwordx4 v125, %[rw], s44 offen nt lds\ns_add_u32 m0, %[lw], 13312\ns_add_u32 s44, %[wo], 868352\nbuffer_load_dwordx4 v125, %[rw], s44 offen nt lds\ns_add_u32 m0, %[lw], 14336\ns_add_u32 s44, %[wo], 884736\nbuffer_load_dwordx4 v125, %[rw], s44 offen nt lds\ns_add_u32 m0, %[lw], 15360\n" \
"s_add_u32 s44, %[wo], 901120\nbuffer_load_dwordx4 v125, %[rw], s44 offen nt lds\ns_waitcnt vmcnt(32)\nds_read_b64_tr_b16 a[60:61], v127 offset:16384\nds_read_b64_tr_b16 a[62:63], v127 offset:16896\nds_read_b64_tr_b16 a[68:69], v127 offset:16416\nds_read_b64_tr_b16 a[70:71], v127 offset:16928\nds_read_b64_tr_b16 a[76:77], v127 offset:16448\n" \
"ds_read_b64_tr_b16 a[78:79], v127 offset:16960\nds_read_b64_tr_b16 a[84:85], v127 offset:16480\nds_read_b64_tr_b16 a[86:87], v127 offset:16992\ns_waitcnt lgkmcnt(0)\ns_add_u32 m0, %[lw], 16384\ns_add_u32 s44, %[wo], 917504\nbuffer_load_dwordx4 v125, %[rw], s44 offen nt lds\ns_add_u32 m0, %[lw], 17408\n" \
"s_add_u32 s44, %[wo], 933888\nbuffer_load_dwordx4 v125, %[rw], s44 offen nt lds\ns_add_u32 m0, %[lw], 18432\ns_add_u32 s44, %[wo], 950272\nbuffer_load_dwordx4 v125, %[rw], s44 offen nt lds\ns_add_u32 m0, %[lw], 19456\ns_add_u32 s44, %[wo], 966656\nbuffer_load_dwordx4 v125, %[rw], s44 offen nt lds\n" \
"s_waitcnt vmcnt(32)\nds_read_b64_tr_b16 a[88:89], v127 offset:20480\nds_read_b64_tr_b16 a[90:91], v127 offset:20992\nds_read_b64_tr_b16 a[96:97], v127 offset:20512\nds_read_b64_tr_b16 a[98:99], v127 offset:21024\nds_read_b64_tr_b16 a[104:105], v127 offset:20544\nds_read_b64_tr_b16 a[106:107], v127 offset:21056\nds_read_b64_tr_b16 v[84:85], v127 offset:20576\n" \
"ds_read_b64_tr_b16 v[86:87], v127 offset:21088\ns_waitcnt lgkmcnt(0)\ns_add_u32 m0, %[lw], 20480\ns_add_u32 s44, %[wo], 983040\nbuffer_load_dwordx4 v125, %[rw], s44 offen nt lds\ns_add_u32 m0, %[lw], 21504\ns_add_u32 s44, %[wo], 999424\nbuffer_load_dwordx4 v125, %[rw], s44 offen nt lds\n" \
"s_add_u32 m0, %[lw], 22528\ns_add_u32 s44, %[wo], 1015808\nbuffer_load_dwordx4 v125, %[rw], s44 offen nt lds\ns_add_u32 m0, %[lw], 23552\ns_add_u32 s44, %[wo], 1032192\nbuffer_load_dwordx4 v125, %[rw], s44 offen nt lds\ns_waitcnt vmcnt(32)\nds_read_b64_tr_b16 a[92:93], v126 offset:0\n" \
"ds_read_b64_tr_b16 a[94:95], v126 offset:512\nds_read_b64_tr_b16 a[100:101], v126 offset:32\nds_read_b64_tr_b16 a[102:103], v126 offset:544\nds_read_b64_tr_b16 a[108:109], v126 offset:64\nds_read_b64_tr_b16 a[110:111], v126 offset:576\nds_read_b64_tr_b16 v[80:81], v126 offset:96\nds_read_b64_tr_b16 v[82:83], v126 offset:608\ns_waitcnt lgkmcnt(0)\n" \
"s_add_u32 m0, %[tb], 0\ns_add_u32 s44, %[wo], 1048576\nbuffer_load_dwordx4 v125, %[rw], s44 offen nt lds\ns_add_u32 m0, %[tb], 1024\ns_add_u32 s44, %[wo], 1064960\nbuffer_load_dwordx4 v125, %[rw], s44 offen nt lds\ns_add_u32 m0, %[tb], 2048\ns_add_u32 s44, %[wo], 1081344\n" \
"buffer_load_dwordx4 v125, %[rw], s44 offen nt lds\ns_add_u32 m0, %[tb], 3072\ns_add_u32 s44, %[wo], 1097728\nbuffer_load_dwordx4 v125, %[rw], s44 offen nt lds\ns_waitcnt vmcnt(32)\nds_read_b64_tr_b16 a[112:113], v126 offset:4096\nds_read_b64_tr_b16 a[114:115], v126 offset:4608\nds_read_b64_tr_b16 a[120:121], v126 offset:4128\n" \
"ds_read_b64_tr_b16 a[122:123], v126 offset:4640\nds_read_b64_tr_b16 a[128:129], v126 offset:4160\nds_read_b64_tr_b16 a[130:131], v126 offset:4672\nds_read_b64_tr_b16 a[136:137], v126 offset:4192\nds_read_b64_tr_b16 a[138:139], v126 offset:4704\ns_waitcnt lgkmcnt(0)\ns_add_u32 m0, %[tb], 4096\ns_add_u32 s44, %[wo], 1114112\n" \
"buffer_load_dwordx4 v125, %[rw], s44 offen nt lds\ns_add_u32 m0, %[tb], 5120\ns_add_u32 s44, %[wo], 1130496\nbuffer_load_dwordx4 v125, %[rw], s44 offen nt lds\ns_add_u32 m0, %[tb], 6144\ns_add_u32 s44, %[wo], 1146880\nbuffer_load_dwordx4 v125, %[rw], s44 offen nt lds\ns_add_u32 m0, %[tb], 7168\n" \
"s_add_u32 s44, %[wo], 1163264\nbuffer_load_dwordx4 v125, %[rw], s44 offen nt lds\ns_waitcnt vmcnt(32)\nds_read_b64_tr_b16 a[116:117], v126 offset:8192\nds_read_b64_tr_b16 a[118:119], v126 offset:8704\nds_read_b64_tr_b16 a[124:125], v126 offset:8224\nds_read_b64_tr_b16 a[126:127], v126 offset:8736\nds_read_b64_tr_b16 a[132:133], v126 offset:8256\n" \
"ds_read_b64_tr_b16 a[134:135], v126 offset:8768\nds_read_b64_tr_b16 a[140:141], v126 offset:8288\nds_read_b64_tr_b16 a[142:143], v126 offset:8800\ns_waitcnt lgkmcnt(0)\ns_add_u32 m0, %[tb], 8192\ns_add_u32 s44, %[wo], 1179648\nbuffer_load_dwordx4 v125, %[rw], s44 offen nt lds\ns_add_u32 m0, %[tb], 9216\n" \
"s_add_u32 s44, %[wo], 1196032\nbuffer_load_dwordx4 v125, %[rw], s44 offen nt lds\ns_add_u32 m0, %[tb], 10240\ns_add_u32 s44, %[wo], 1212416\nbuffer_load_dwordx4 v125, %[rw], s44 offen nt lds\ns_add_u32 m0, %[tb], 11264\ns_add_u32 s44, %[wo], 1228800\nbuffer_load_dwordx4 v125, %[rw], s44 offen nt lds\n" \
"s_waitcnt vmcnt(32)\nds_read_b64_tr_b16 a[144:145], v127 offset:0\nds_read_b64_tr_b16 a[146:147], v127 offset:512\nds_read_b64_tr_b16 v[76:77], v127 offset:32\nds_read_b64_tr_b16 v[78:79], v127 offset:544\nds_read_b64_tr_b16 a[152:153], v127 offset:64\nds_read_b64_tr_b16 a[154:155], v127 offset:576\nds_read_b64_tr_b16 a[160:161], v127 offset:96\n" \
"ds_read_b64_tr_b16 a[162:163], v127 offset:608\ns_waitcnt lgkmcnt(0)\ns_add_u32 m0, %[lw], 0\ns_add_u32 s44, %[wo], 1245184\nbuffer_load_dwordx4 v125, %[rw], s44 offen nt lds\ns_add_u32 m0, %[lw], 1024\ns_add_u32 s44, %[wo], 1261568\nbuffer_load_dwordx4 v125, %[rw], s44 offen nt lds\n" \
"s_add_u32 m0, %[lw], 2048\ns_add_u32 s44, %[wo], 1277952\nbuffer_load_dwordx4 v125, %[rw], s44 offen nt lds\ns_add_u32 m0, %[lw], 3072\ns_add_u32 s44, %[wo], 1294336\nbuffer_load_dwordx4 v125, %[rw], s44 offen nt lds\ns_waitcnt vmcnt(32)\nds_read_b64_tr_b16 a[148:149], v127 offset:4096\n" \
"ds_read_b64_tr_b16 a[150:151], v127 offset:4608\nds_read_b64_tr_b16 v[72:73], v127 offset:4128\nds_read_b64_tr_b16 v[74:75], v127 offset:4640\nds_read_b64_tr_b16 a[156:157], v127 offset:4160\nds_read_b64_tr_b16 a[158:159], v127 offset:4672\nds_read_b64_tr_b16 a[164:165], v127 offset:4192\nds_read_b64_tr_b16 a[166:167], v127 offset:4704\ns_waitcnt lgkmcnt(0)\n" \
"s_add_u32 m0, %[lw], 4096\ns_add_u32 s44, %[wo], 1310720\nbuffer_load_dwordx4 v125, %[rw], s44 offen nt lds\ns_add_u32 m0, %[lw], 5120\ns_add_u32 s44, %[wo], 1327104\nbuffer_load_dwordx4 v125, %[rw], s44 offen nt lds\ns_add_u32 m0, %[lw], 6144\ns_add_u32 s44, %[wo], 1343488\n" \
"buffer_load_dwordx4 v125, %[rw], s44 offen nt lds\ns_add_u32 m0, %[lw], 7168\ns_add_u32 s44, %[wo], 1359872\nbuffer_load_dwordx4 v125, %[rw], s44 offen nt lds\ns_waitcnt vmcnt(32)\nds_read_b64_tr_b16 a[168:169], v127 offset:8192\nds_read_b64_tr_b16 a[170:171], v127 offset:8704\nds_read_b64_tr_b16 a[176:177], v127 offset:8224\n" \
"ds_read_b64_tr_b16 a[178:179], v127 offset:8736\nds_read_b64_tr_b16 v[68:69], v127 offset:8256\nds_read_b64_tr_b16 v[70:71], v127 offset:8768\nds_read_b64_tr_b16 a[184:185], v127 offset:8288\nds_read_b64_tr_b16 a[186:187], v127 offset:8800\ns_waitcnt lgkmcnt(0)\ns_add_u32 m0, %[lw], 8192\ns_add_u32 s44, %[wo], 1376256\n" \
"buffer_load_dwordx4 v125, %[rw], s44 offen nt lds\ns_add_u32 m0, %[lw], 9216\ns_add_u32 s44, %[wo], 1392640\nbuffer_load_dwordx4 v125, %[rw], s44 offen nt lds\ns_add_u32 m0, %[lw], 10240\ns_add_u32 s44, %[wo], 1409024\nbuffer_load_dwordx4 v125, %[rw], s44 offen nt lds\ns_add_u32 m0, %[lw], 11264\n" \
"s_add_u32 s44, %[wo], 1425408\nbuffer_load_dwordx4 v125, %[rw], s44 offen nt lds\ns_waitcnt vmcnt(32)\nds_read_b64_tr_b16 a[172:173], v127 offset:12288\nds_read_b64_tr_b16 a[174:175], v127 offset:12800\nds_read_b64_tr_b16 a[180:181], v127 offset:12320\nds_read_b64_tr_b16 a[182:183], v127 offset:12832\nds_read_b64_tr_b16 v[64:65], v127 offset:12352\n" \
"ds_read_b64_tr_b16 v[66:67], v127 offset:12864\nds_read_b64_tr_b16 a[188:189], v127 offset:12384\nds_read_b64_tr_b16 a[190:191], v127 offset:12896\ns_waitcnt lgkmcnt(0)\ns_add_u32 m0, %[lw], 12288\ns_add_u32 s44, %[wo], 1441792\nbuffer_load_dwordx4 v125, %[rw], s44 offen nt lds\ns_add_u32 m0, %[lw], 13312\n" \
"s_add_u32 s44, %[wo], 1458176\nbuffer_load_dwordx4 v125, %[rw], s44 offen nt lds\ns_add_u32 m0, %[lw], 14336\ns_add_u32 s44, %[wo], 1474560\nbuffer_load_dwordx4 v125, %[rw], s44 offen nt lds\ns_add_u32 m0, %[lw], 15360\ns_add_u32 s44, %[wo], 1490944\nbuffer_load_dwordx4 v125, %[rw], s44 offen nt lds\n" \
"s_waitcnt vmcnt(32)\nds_read_b64_tr_b16 a[192:193], v127 offset:16384\nds_read_b64_tr_b16 a[194:195], v127 offset:16896\nds_read_b64_tr_b16 a[200:201], v127 offset:16416\nds_read_b64_tr_b16 a[202:203], v127 offset:16928\nds_read_b64_tr_b16 a[208:209], v127 offset:16448\nds_read_b64_tr_b16 a[210:211], v127 offset:16960\nds_read_b64_tr_b16 a[216:217], v127 offset:16480\n" \
"ds_read_b64_tr_b16 a[218:219], v127 offset:16992\ns_waitcnt lgkmcnt(0)\ns_add_u32 m0, %[lw], 16384\ns_add_u32 s44, %[wo], 1507328\nbuffer_load_dwordx4 v125, %[rw], s44 offen nt lds\ns_add_u32 m0, %[lw], 17408\ns_add_u32 s44, %[wo], 1523712\nbuffer_load_dwordx4 v125, %[rw], s44 offen nt lds\n" \
"s_add_u32 m0, %[lw], 18432\ns_add_u32 s44, %[wo], 1540096\nbuffer_load_dwordx4 v125, %[rw], s44 offen nt lds\ns_add_u32 m0, %[lw], 19456\ns_add_u32 s44, %[wo], 1556480\nbuffer_load_dwordx4 v125, %[rw], s44 offen nt lds\ns_waitcnt vmcnt(32)\nds_read_b64_tr_b16 a[196:197], v127 offset:20480\n" \
"ds_read_b64_tr_b16 a[198:199], v127 offset:20992\nds_read_b64_tr_b16 a[204:205], v127 offset:20512\nds_read_b64_tr_b16 a[206:207], v127 offset:21024\nds_read_b64_tr_b16 a[212:213], v127 offset:20544\nds_read_b64_tr_b16 a[214:215], v127 offset:21056\nds_read_b64_tr_b16 a[220:221], v127 offset:20576\nds_read_b64_tr_b16 a[222:223], v127 offset:21088\ns_waitcnt lgkmcnt(0)\n" \
"s_add_u32 m0, %[lw], 20480\ns_add_u32 s44, %[wo], 1572864\nbuffer_load_dwordx4 v125, %[rw], s44 offen nt lds\ns_add_u32 m0, %[lw], 21504\ns_add_u32 s44, %[wo], 1589248\nbuffer_load_dwordx4 v125, %[rw], s44 offen nt lds\ns_add_u32 m0, %[lw], 22528\ns_add_u32 s44, %[wo], 1605632\n" \
"buffer_load_dwordx4 v125, %[rw], s44 offen nt lds\ns_add_u32 m0, %[lw], 23552\ns_add_u32 s44, %[wo], 1622016\nbuffer_load_dwordx4 v125, %[rw], s44 offen nt lds\ns_waitcnt vmcnt(32)\nds_read_b64_tr_b16 v[60:61], v126 offset:0\nds_read_b64_tr_b16 v[62:63], v126 offset:512\nds_read_b64_tr_b16 a[224:225], v126 offset:32\n" \
"ds_read_b64_tr_b16 a[226:227], v126 offset:544\nds_read_b64_tr_b16 a[232:233], v126 offset:64\nds_read_b64_tr_b16 a[234:235], v126 offset:576\nds_read_b64_tr_b16 a[240:241], v126 offset:96\nds_read_b64_tr_b16 a[242:243], v126 offset:608\ns_waitcnt lgkmcnt(0)\ns_add_u32 m0, %[tb], 0\ns_add_u32 s44, %[wo], 1638400\n" \
"buffer_load_dwordx4 v125, %[rw], s44 offen nt lds\ns_add_u32 m0, %[tb], 1024\ns_add_u32 s44, %[wo], 1654784\nbuffer_load_dwordx4 v125, %[rw], s44 offen nt lds\ns_add_u32 m0, %[tb], 2048\ns_add_u32 s44, %[wo], 1671168\nbuffer_load_dwordx4 v125, %[rw], s44 offen nt lds\ns_add_u32 m0, %[tb], 3072\n" \
"s_add_u32 s44, %[wo], 1687552\nbuffer_load_dwordx4 v125, %[rw], s44 offen nt lds\ns_waitcnt vmcnt(32)\nds_read_b64_tr_b16 v[56:57], v126 offset:4096\nds_read_b64_tr_b16 v[58:59], v126 offset:4608\nds_read_b64_tr_b16 a[228:229], v126 offset:4128\nds_read_b64_tr_b16 a[230:231], v126 offset:4640\nds_read_b64_tr_b16 a[236:237], v126 offset:4160\n" \
"ds_read_b64_tr_b16 a[238:239], v126 offset:4672\nds_read_b64_tr_b16 a[244:245], v126 offset:4192\nds_read_b64_tr_b16 a[246:247], v126 offset:4704\ns_waitcnt lgkmcnt(0)\ns_add_u32 m0, %[tb], 4096\ns_add_u32 s44, %[wo], 1703936\nbuffer_load_dwordx4 v125, %[rw], s44 offen nt lds\ns_add_u32 m0, %[tb], 5120\n" \
"s_add_u32 s44, %[wo], 1720320\nbuffer_load_dwordx4 v125, %[rw], s44 offen nt lds\ns_add_u32 m0, %[tb], 6144\ns_add_u32 s44, %[wo], 1736704\nbuffer_load_dwordx4 v125, %[rw], s44 offen nt lds\ns_add_u32 m0, %[tb], 7168\ns_add_u32 s44, %[wo], 1753088\nbuffer_load_dwordx4 v125, %[rw], s44 offen nt lds\n" \
"s_waitcnt vmcnt(32)\nds_read_b64_tr_b16 a[248:249], v126 offset:8192\nds_read_b64_tr_b16 a[250:251], v126 offset:8704\nds_read_b64_tr_b16 v[128:129], v126 offset:8224\nds_read_b64_tr_b16 v[130:131], v126 offset:8736\nds_read_b64_tr_b16 v[136:137], v126 offset:8256\nds_read_b64_tr_b16 v[138:139], v126 offset:8768\nds_read_b64_tr_b16 v[144:145], v126 offset:8288\n" \
"ds_read_b64_tr_b16 v[146:147], v126 offset:8800\ns_waitcnt lgkmcnt(0)\ns_add_u32 m0, %[tb], 8192\ns_add_u32 s44, %[wo], 1769472\nbuffer_load_dwordx4 v125, %[rw], s44 offen nt lds\ns_add_u32 m0, %[tb], 9216\ns_add_u32 s44, %[wo], 1785856\nbuffer_load_dwordx4 v125, %[rw], s44 offen nt lds\n" \
"s_add_u32 m0, %[tb], 10240\ns_add_u32 s44, %[wo], 1802240\nbuffer_load_dwordx4 v125, %[rw], s44 offen nt lds\ns_add_u32 m0, %[tb], 11264\ns_add_u32 s44, %[wo], 1818624\nbuffer_load_dwordx4 v125, %[rw], s44 offen nt lds\ns_waitcnt vmcnt(32)\nds_read_b64_tr_b16 a[252:253], v127 offset:0\n" \
"ds_read_b64_tr_b16 a[254:255], v127 offset:512\nds_read_b64_tr_b16 v[132:133], v127 offset:32\nds_read_b64_tr_b16 v[134:135], v127 offset:544\nds_read_b64_tr_b16 v[140:141], v127 offset:64\nds_read_b64_tr_b16 v[142:143], v127 offset:576\nds_read_b64_tr_b16 v[148:149], v127 offset:96\nds_read_b64_tr_b16 v[150:151], v127 offset:608\ns_waitcnt lgkmcnt(0)\n" \
"ds_write_b128 v1, v[92:95] offset:0\nds_write_b128 v1, v[88:91] offset:1024\nds_write_b128 v1, v[84:87] offset:2048\nds_write_b128 v1, v[80:83] offset:3072\ns_waitcnt vmcnt(28)\nds_read_b64_tr_b16 v[152:153], v127 offset:4096\nds_read_b64_tr_b16 v[154:155], v127 offset:4608\nds_read_b64_tr_b16 v[160:161], v127 offset:4128\n" \
"ds_read_b64_tr_b16 v[162:163], v127 offset:4640\nds_read_b64_tr_b16 v[80:81], v127 offset:4160\nds_read_b64_tr_b16 v[82:83], v127 offset:4672\nds_read_b64_tr_b16 v[168:169], v127 offset:4192\nds_read_b64_tr_b16 v[170:171], v127 offset:4704\ns_waitcnt lgkmcnt(0)\nds_write_b128 v1, v[76:79] offset:4096\nds_write_b128 v1, v[72:75] offset:5120\n" \
"ds_write_b128 v1, v[68:71] offset:6144\nds_write_b128 v1, v[64:67] offset:7168\ns_waitcnt vmcnt(24)\nds_read_b64_tr_b16 v[156:157], v127 offset:8192\nds_read_b64_tr_b16 v[158:159], v127 offset:8704\nds_read_b64_tr_b16 v[164:165], v127 offset:8224\nds_read_b64_tr_b16 v[166:167], v127 offset:8736\nds_read_b64_tr_b16 v[64:65], v127 offset:8256\n" \
"ds_read_b64_tr_b16 v[66:67], v127 offset:8768\nds_read_b64_tr_b16 v[172:173], v127 offset:8288\nds_read_b64_tr_b16 v[174:175], v127 offset:8800\ns_waitcnt lgkmcnt(0)\nds_write_b128 v1, v[60:63] offset:8192\nds_write_b128 v1, v[56:59] offset:9216\nds_write_b128 v1, v[80:83] offset:10240\nds_write_b128 v1, v[64:67] offset:11264\n" \
"s_waitcnt vmcnt(20)\nds_read_b64_tr_b16 v[176:177], v127 offset:12288\nds_read_b64_tr_b16 v[178:179], v127 offset:12800\nds_read_b64_tr_b16 v[184:185], v127 offset:12320\nds_read_b64_tr_b16 v[186:187], v127 offset:12832\nds_read_b64_tr_b16 v[192:193], v127 offset:12352\nds_read_b64_tr_b16 v[194:195], v127 offset:12864\nds_read_b64_tr_b16 v[200:201], v127 offset:12384\n" \
"ds_read_b64_tr_b16 v[202:203], v127 offset:12896\ns_waitcnt lgkmcnt(0)\ns_waitcnt vmcnt(16)\nds_read_b64_tr_b16 v[180:181], v127 offset:16384\nds_read_b64_tr_b16 v[182:183], v127 offset:16896\nds_read_b64_tr_b16 v[188:189], v127 offset:16416\nds_read_b64_tr_b16 v[190:191], v127 offset:16928\nds_read_b64_tr_b16 v[196:197], v127 offset:16448\n" \
"ds_read_b64_tr_b16 v[198:199], v127 offset:16960\nds_read_b64_tr_b16 v[204:205], v127 offset:16480\nds_read_b64_tr_b16 v[206:207], v127 offset:16992\ns_waitcnt lgkmcnt(0)\ns_waitcnt vmcnt(12)\nds_read_b64_tr_b16 v[64:65], v127 offset:20480\nds_read_b64_tr_b16 v[66:67], v127 offset:20992\nds_read_b64_tr_b16 v[208:209], v127 offset:20512\n" \
"ds_read_b64_tr_b16 v[210:211], v127 offset:21024\nds_read_b64_tr_b16 v[216:217], v127 offset:20544\nds_read_b64_tr_b16 v[218:219], v127 offset:21056\nds_read_b64_tr_b16 v[224:225], v127 offset:20576\nds_read_b64_tr_b16 v[226:227], v127 offset:21088\ns_waitcnt lgkmcnt(0)\nds_write_b128 v1, v[64:67] offset:12288\ns_waitcnt vmcnt(8)\n" \
"ds_read_b64_tr_b16 v[64:65], v126 offset:0\nds_read_b64_tr_b16 v[66:67], v126 offset:512\nds_read_b64_tr_b16 v[212:213], v126 offset:32\nds_read_b64_tr_b16 v[214:215], v126 offset:544\nds_read_b64_tr_b16 v[220:221], v126 offset:64\nds_read_b64_tr_b16 v[222:223], v126 offset:576\nds_read_b64_tr_b16 v[228:229], v126 offset:96\nds_read_b64_tr_b16 v[230:231], v126 offset:608\n" \
"s_waitcnt lgkmcnt(0)\nds_write_b128 v1, v[64:67] offset:13312\ns_waitcnt vmcnt(4)\nds_read_b64_tr_b16 v[232:233], v126 offset:4096\nds_read_b64_tr_b16 v[234:235], v126 offset:4608\nds_read_b64_tr_b16 v[64:65], v126 offset:4128\nds_read_b64_tr_b16 v[66:67], v126 offset:4640\nds_read_b64_tr_b16 v[240:241], v126 offset:4160\n" \
"ds_read_b64_tr_b16 v[242:243], v126 offset:4672\nds_read_b64_tr_b16 v[248:249], v126 offset:4192\nds_read_b64_tr_b16 v[250:251], v126 offset:4704\ns_waitcnt lgkmcnt(0)\nds_write_b128 v1, v[64:67] offset:14336\ns_waitcnt vmcnt(0)\nds_read_b64_tr_b16 v[236:237], v126 offset:8192\nds_read_b64_tr_b16 v[238:239], v126 offset:8704\n" \
"ds_read_b64_tr_b16 v[64:65], v126 offset:8224\nds_read_b64_tr_b16 v[66:67], v126 offset:8736\nds_read_b64_tr_b16 v[244:245], v126 offset:8256\nds_read_b64_tr_b16 v[246:247], v126 offset:8768\nds_read_b64_tr_b16 v[252:253], v126 offset:8288\nds_read_b64_tr_b16 v[254:255], v126 offset:8800\ns_waitcnt lgkmcnt(0)\nds_write_b128 v1, v[64:67] offset:15360\n" \
"s_waitcnt lgkmcnt(0)\ns_barrier\ns_mov_b32 s40, 0\ns_mov_b32 s43, 0\ns_mov_b32 s56, -2147483648\n.p2align 6\n.Ldd_next%=:\ns_cmp_eq_u32 s40, %[b1]\n" \
"s_cselect_b32 s44, 4, 0\nv_xor_b32 v7, s44, v7\nbuffer_load_dword v8, v7, %[rs], 0 offen sc1\ns_lshl_b32 s58, s40, 1\ns_add_u32 s44, s40, 0\ns_lshl_b32 s44, s44, 19\ns_add_u32 s44, s44, %[ho]\ns_lshr_b32 s45, s44, 8\n" \
"s_and_b32 s45, s45, 0x2000\ns_xor_b32 s55, s44, s45\ns_waitcnt vmcnt(0)\nbuffer_store_dword v9, off, %[rs], s56 sc1\ns_mov_b32 s56, -2147483648\n.Ldd_w0%=:\nv_cmp_gt_i32 vcc, s58, v8\ns_cbranch_vccz .Ldd_w0go%=\n" \
"s_cmp_ge_u32 s40, 62\ns_cbranch_scc1 .Ldd_ns671%=\ns_sleep 8\n.Ldd_ns671%=:\ns_sleep 1\nbuffer_load_dword v8, v7, %[rs], 0 offen sc1\ns_waitcnt vmcnt(0)\ns_add_u32 s43, s43, 1\n" \
"s_cmp_lt_u32 s43, 262144\ns_cbranch_scc1 .Ldd_w0%=\ns_branch .Ldd_abort%=\n.Ldd_w0go%=:\ns_add_u32 s60, s55, 0\nbuffer_load_dwordx4 v[48:51], v0, %[rs], s60 offen offset:0 sc1\nbuffer_load_dwordx4 v[52:55], v0, %[rs], s60 offen offset:2048 sc1\ns_add_u32 s60, s55, 4096\n" \
"buffer_load_dwordx4 v[56:59], v0, %[rs], s60 offen offset:0 sc1\nbuffer_load_dwordx4 v[60:63], v0, %[rs], s60 offen offset:2048 sc1\ns_add_u32 s60, s55, 16384\nbuffer_load_dwordx4 v[64:67], v0, %[rs], s60 offen offset:0 sc1\nbuffer_load_dwordx4 v[68:71], v0, %[rs], s60 offen offset:2048 sc1\ns_add_u32 s60, s55, 20480\nbuffer_load_dwordx4 v[72:75], v0, %[rs], s60 offen offset:0 sc1\nbuffer_load_dwordx4 v[76:79], v0, %[rs], s60 offen offset:2048 sc1\n" \
"s_add_u32 s60, s55, 32768\nbuffer_load_dwordx4 v[80:83], v0, %[rs], s60 offen offset:0 sc1\nbuffer_load_dwordx4 v[84:87], v0, %[rs], s60 offen offset:2048 sc1\ns_add_u32 s60, s55, 36864\nbuffer_load_dwordx4 v[88:91], v0, %[rs], s60 offen offset:0 sc1\nbuffer_load_dwordx4 v[92:95], v0, %[rs], s60 offen offset:2048 sc1\ns_add_u32 s60, s55, 49152\nbuffer_load_dwordx4 v[96:99], v0, %[rs], s60 offen offset:0 sc1\n" \
"buffer_load_dwordx4 v[100:103], v0, %[rs], s60 offen offset:2048 sc1\ns_add_u32 s60, s55, 53248\nbuffer_load_dwordx4 v[104:107], v0, %[rs], s60 offen offset:0 sc1\n.Ldd_run%=:\ns_add_u32 s44, s40, 0\ns_lshl_b32 s44, s44, 17\ns_add_u32 s44, s44, %[xn]\ns_lshr_b32 s45, s44, 8\n" \
"s_and_b32 s45, s45, 0x2000\ns_xor_b32 s41, s44, s45\ns_lshl_b32 s44, s40, 16\ns_cmp_eq_u32 %[l], 0\ns_cselect_b32 s42, s41, s44\ns_add_u32 s58, s58, 1\ns_waitcnt vmcnt(14)\nv_mfma_f32_16x16x32_bf16 v[16:19], a[0:3], v[48:51], 0\n" \
"s_waitcnt vmcnt(13)\nds_read_b128 v[120:123], v1 offset:0\nv_mfma_f32_16x16x32_bf16 v[16:19], a[4:7], v[52:55], v[16:19]\nbuffer_load_dword v8, v7, %[rs], 0 offen sc1\nv_mfma_f32_16x16x32_bf16 v[24:27], a[8:11], v[48:51], 0\nv_mfma_f32_16x16x32_bf16 v[24:27], a[12:15], v[52:55], v[24:27]\nbuffer_load_dwordx4 v[108:111], v0, %[rs], s60 offen offset:2048 sc1\nv_mfma_f32_16x16x32_bf16 v[32:35], a[16:19], v[48:51], 0\n" \
"ds_read_b128 v[124:127], v1 offset:1024\nv_mfma_f32_16x16x32_bf16 v[32:35], a[20:23], v[52:55], v[32:35]\nv_mfma_f32_16x16x32_bf16 v[40:43], a[24:27], v[48:51], 0\ns_add_u32 s60, s55, 65536\nv_mfma_f32_16x16x32_bf16 v[40:43], a[28:31], v[52:55], v[40:43]\ns_waitcnt vmcnt(14)\nbuffer_load_dwordx4 v[48:51], v0, %[rs], s60 offen offset:0 sc1\nv_mfma_f32_16x16x32_bf16 v[16:19], a[32:35], v[56:59], v[16:19]\n" \
"s_waitcnt vmcnt(14)\nv_mfma_f32_16x16x32_bf16 v[16:19], a[36:39], v[60:63], v[16:19]\ns_waitcnt lgkmcnt(1)\nv_mfma_f32_16x16x32_bf16 v[24:27], v[120:123], v[56:59], v[24:27]\ns_waitcnt lgkmcnt(0)\nv_mfma_f32_16x16x32_bf16 v[24:27], v[124:127], v[60:63], v[24:27]\nbuffer_load_dwordx4 v[52:55], v0, %[rs], s60 offen offset:2048 sc1\nv_mfma_f32_16x16x32_bf16 v[32:35], a[40:43], v[56:59], v[32:35]\n" \
"v_mfma_f32_16x16x32_bf16 v[32:35], a[44:47], v[60:63], v[32:35]\nv_mfma_f32_16x16x32_bf16 v[40:43], a[48:51], v[56:59], v[40:43]\ns_add_u32 s60, s55, 69632\nv_mfma_f32_16x16x32_bf16 v[40:43], a[52:55], v[60:63], v[40:43]\ns_waitcnt vmcnt(14)\nbuffer_load_dwordx4 v[56:59], v0, %[rs], s60 offen offset:0 sc1\nv_mfma_f32_16x16x32_bf16 v[16:19], a[56:59], v[64:67], v[16:19]\ns_waitcnt vmcnt(14)\n" \
"ds_read_b128 v[120:123], v1 offset:2048\nv_mfma_f32_16x16x32_bf16 v[16:19], a[60:63], v[68:71], v[16:19]\nv_mfma_f32_16x16x32_bf16 v[24:27], a[64:67], v[64:67], v[24:27]\nv_mfma_f32_16x16x32_bf16 v[24:27], a[68:71], v[68:71], v[24:27]\nbuffer_load_dwordx4 v[60:63], v0, %[rs], s60 offen offset:2048 sc1\nv_mfma_f32_16x16x32_bf16 v[32:35], a[72:75], v[64:67], v[32:35]\nds_read_b128 v[124:127], v1 offset:3072\nv_mfma_f32_16x16x32_bf16 v[32:35], a[76:79], v[68:71], v[32:35]\n" \
"v_mfma_f32_16x16x32_bf16 v[40:43], a[80:83], v[64:67], v[40:43]\ns_add_u32 s60, s55, 81920\nv_mfma_f32_16x16x32_bf16 v[40:43], a[84:87], v[68:71], v[40:43]\ns_waitcnt vmcnt(14)\nbuffer_load_dwordx4 v[64:67], v0, %[rs], s60 offen offset:0 sc1\nv_mfma_f32_16x16x32_bf16 v[16:19], a[88:91], v[72:75], v[16:19]\ns_waitcnt vmcnt(14)\nv_mfma_f32_16x16x32_bf16 v[16:19], a[92:95], v[76:79], v[16:19]\n" \
"v_mfma_f32_16x16x32_bf16 v[24:27], a[96:99], v[72:75], v[24:27]\nv_mfma_f32_16x16x32_bf16 v[24:27], a[100:103], v[76:79], v[24:27]\nbuffer_load_dwordx4 v[68:71], v0, %[rs], s60 offen offset:2048 sc1\nv_mfma_f32_16x16x32_bf16 v[32:35], a[104:107], v[72:75], v[32:35]\nv_mfma_f32_16x16x32_bf16 v[32:35], a[108:111], v[76:79], v[32:35]\ns_waitcnt lgkmcnt(1)\nv_mfma_f32_16x16x32_bf16 v[40:43], v[120:123], v[72:75], v[40:43]\ns_waitcnt lgkmcnt(0)\n" \
"s_add_u32 s60, s55, 86016\nv_mfma_f32_16x16x32_bf16 v[40:43], v[124:127], v[76:79], v[40:43]\ns_waitcnt vmcnt(14)\nbuffer_load_dwordx4 v[72:75], v0, %[rs], s60 offen offset:0 sc1\nv_mfma_f32_16x16x32_bf16 v[16:19], a[112:115], v[80:83], v[16:19]\ns_waitcnt vmcnt(14)\nds_read_b128 v[120:123], v1 offset:4096\nv_mfma_f32_16x16x32_bf16 v[16:19], a[116:119], v[84:87], v[16:19]\n" \
"v_mfma_f32_16x16x32_bf16 v[24:27], a[120:123], v[80:83], v[24:27]\nv_mfma_f32_16x16x32_bf16 v[24:27], a[124:127], v[84:87], v[24:27]\nbuffer_load_dwordx4 v[76:79], v0, %[rs], s60 offen offset:2048 sc1\nv_mfma_f32_16x16x32_bf16 v[32:35], a[128:131], v[80:83], v[32:35]\nds_read_b128 v[124:127], v1 offset:5120\nv_mfma_f32_16x16x32_bf16 v[32:35], a[132:135], v[84:87], v[32:35]\nv_mfma_f32_16x16x32_bf16 v[40:43], a[136:139], v[80:83], v[40:43]\ns_add_u32 s60, s55, 98304\n" \
"v_mfma_f32_16x16x32_bf16 v[40:43], a[140:143], v[84:87], v[40:43]\ns_waitcnt vmcnt(14)\nbuffer_load_dwordx4 v[80:83], v0, %[rs], s60 offen offset:0 sc1\nv_mfma_f32_16x16x32_bf16 v[16:19], a[144:147], v[88:91], v[16:19]\ns_waitcnt vmcnt(14)\nds_read_b128 v[112:115], v1 offset:6144\nv_mfma_f32_16x16x32_bf16 v[16:19], a[148:151], v[92:95], v[16:19]\ns_waitcnt lgkmcnt(2)\n" \
"v_mfma_f32_16x16x32_bf16 v[24:27], v[120:123], v[88:91], v[24:27]\ns_waitcnt lgkmcnt(1)\nv_mfma_f32_16x16x32_bf16 v[24:27], v[124:127], v[92:95], v[24:27]\nbuffer_load_dwordx4 v[84:87], v0, %[rs], s60 offen offset:2048 sc1\nv_mfma_f32_16x16x32_bf16 v[32:35], a[152:155], v[88:91], v[32:35]\nds_read_b128 v[116:119], v1 offset:7168\nv_mfma_f32_16x16x32_bf16 v[32:35], a[156:159], v[92:95], v[32:35]\ns_waitcnt vmcnt(11)\n" \
"v_cmp_gt_i32 vcc, s58, v8\nv_mfma_f32_16x16x32_bf16 v[40:43], a[160:163], v[88:91], v[40:43]\ns_cmp_eq_u64 vcc, 0\ns_cselect_b32 s61, s55, -2147483648\nv_mfma_f32_16x16x32_bf16 v[40:43], a[164:167], v[92:95], v[40:43]\ns_add_u32 s60, s55, 102400\nbuffer_load_dwordx4 v[88:91], v0, %[rs], s60 offen offset:0 sc1\nv_mfma_f32_16x16x32_bf16 v[16:19], a[168:171], v[96:99], v[16:19]\n" \
"v_mfma_f32_16x16x32_bf16 v[16:19], a[172:175], v[100:103], v[16:19]\nv_mfma_f32_16x16x32_bf16 v[24:27], a[176:179], v[96:99], v[24:27]\nv_mfma_f32_16x16x32_bf16 v[24:27], a[180:183], v[100:103], v[24:27]\ns_waitcnt lgkmcnt(1)\nbuffer_load_dwordx4 v[92:95], v0, %[rs], s60 offen offset:2048 sc1\nv_mfma_f32_16x16x32_bf16 v[32:35], v[112:115], v[96:99], v[32:35]\ns_waitcnt lgkmcnt(0)\nv_mfma_f32_16x16x32_bf16 v[32:35], v[116:119], v[100:103], v[32:35]\n" \
"v_mfma_f32_16x16x32_bf16 v[40:43], a[184:187], v[96:99], v[40:43]\nv_mfma_f32_16x16x32_bf16 v[40:43], a[188:191], v[100:103], v[40:43]\ns_add_u32 s60, s61, 0\nbuffer_load_dwordx4 v[96:99], v0, %[rs], s60 offen offset:1024 sc1\nv_mfma_f32_16x16x32_bf16 v[16:19], a[192:195], v[104:107], v[16:19]\ns_waitcnt vmcnt(13)\nds_read_b128 v[112:115], v1 offset:8192\nv_mfma_f32_16x16x32_bf16 v[16:19], a[196:199], v[108:111], v[16:19]\n" \
"v_mfma_f32_16x16x32_bf16 v[24:27], a[200:203], v[104:107], v[24:27]\nv_mfma_f32_16x16x32_bf16 v[24:27], a[204:207], v[108:111], v[24:27]\nbuffer_load_dwordx4 v[100:103], v0, %[rs], s60 offen offset:3072 sc1\nv_mfma_f32_16x16x32_bf16 v[32:35], a[208:211], v[104:107], v[32:35]\nds_read_b128 v[116:119], v1 offset:9216\nv_mfma_f32_16x16x32_bf16 v[32:35], a[212:215], v[108:111], v[32:35]\nv_mfma_f32_16x16x32_bf16 v[40:43], a[216:219], v[104:107], v[40:43]\ns_add_u32 s60, s61, 4096\n" \
"v_mfma_f32_16x16x32_bf16 v[40:43], a[220:223], v[108:111], v[40:43]\ns_waitcnt vmcnt(13)\ns_waitcnt lgkmcnt(1)\nbuffer_load_dwordx4 v[104:107], v0, %[rs], s60 offen offset:1024 sc1\nv_mfma_f32_16x16x32_bf16 v[16:19], v[112:115], v[48:51], v[16:19]\ns_waitcnt vmcnt(13)\ns_waitcnt lgkmcnt(0)\nv_mfma_f32_16x16x32_bf16 v[16:19], v[116:119], v[52:55], v[16:19]\n" \
"v_mfma_f32_16x16x32_bf16 v[24:27], a[224:227], v[48:51], v[24:27]\nv_mfma_f32_16x16x32_bf16 v[24:27], a[228:231], v[52:55], v[24:27]\nbuffer_load_dwordx4 v[108:111], v0, %[rs], s60 offen offset:3072 sc1\nv_mfma_f32_16x16x32_bf16 v[32:35], a[232:235], v[48:51], v[32:35]\nv_mfma_f32_16x16x32_bf16 v[32:35], a[236:239], v[52:55], v[32:35]\nv_mfma_f32_16x16x32_bf16 v[40:43], a[240:243], v[48:51], v[40:43]\ns_add_u32 s60, s61, 16384\nv_mfma_f32_16x16x32_bf16 v[40:43], a[244:247], v[52:55], v[40:43]\n" \
"s_waitcnt vmcnt(13)\nbuffer_load_dwordx4 v[48:51], v0, %[rs], s60 offen offset:1024 sc1\nv_mfma_f32_16x16x32_bf16 v[16:19], a[248:251], v[56:59], v[16:19]\ns_waitcnt vmcnt(13)\nds_read_b128 v[112:115], v1 offset:10240\nv_mfma_f32_16x16x32_bf16 v[16:19], a[252:255], v[60:63], v[16:19]\nv_mfma_f32_16x16x32_bf16 v[24:27], v[128:131], v[56:59], v[24:27]\nv_mfma_f32_16x16x32_bf16 v[24:27], v[132:135], v[60:63], v[24:27]\n" \
"buffer_load_dwordx4 v[52:55], v0, %[rs], s60 offen offset:3072 sc1\nv_mfma_f32_16x16x32_bf16 v[32:35], v[136:139], v[56:59], v[32:35]\nds_read_b128 v[116:119], v1 offset:11264\nv_mfma_f32_16x16x32_bf16 v[32:35], v[140:143], v[60:63], v[32:35]\nv_mfma_f32_16x16x32_bf16 v[40:43], v[144:147], v[56:59], v[40:43]\ns_add_u32 s60, s61, 20480\nv_mfma_f32_16x16x32_bf16 v[40:43], v[148:151], v[60:63], v[40:43]\ns_waitcnt vmcnt(13)\n" \
"buffer_load_dwordx4 v[56:59], v0, %[rs], s60 offen offset:1024 sc1\nv_mfma_f32_16x16x32_bf16 v[16:19], v[152:155], v[64:67], v[16:19]\ns_waitcnt vmcnt(13)\nv_mfma_f32_16x16x32_bf16 v[16:19], v[156:159], v[68:71], v[16:19]\nv_mfma_f32_16x16x32_bf16 v[24:27], v[160:163], v[64:67], v[24:27]\nv_mfma_f32_16x16x32_bf16 v[24:27], v[164:167], v[68:71], v[24:27]\ns_waitcnt lgkmcnt(1)\nbuffer_load_dwordx4 v[60:63], v0, %[rs], s60 offen offset:3072 sc1\n" \
"v_mfma_f32_16x16x32_bf16 v[32:35], v[112:115], v[64:67], v[32:35]\ns_waitcnt lgkmcnt(0)\nv_mfma_f32_16x16x32_bf16 v[32:35], v[116:119], v[68:71], v[32:35]\nv_mfma_f32_16x16x32_bf16 v[40:43], v[168:171], v[64:67], v[40:43]\ns_add_u32 s60, s61, 32768\nv_mfma_f32_16x16x32_bf16 v[40:43], v[172:175], v[68:71], v[40:43]\ns_waitcnt vmcnt(13)\nbuffer_load_dwordx4 v[64:67], v0, %[rs], s60 offen offset:1024 sc1\n" \
"v_mfma_f32_16x16x32_bf16 v[16:19], v[176:179], v[72:75], v[16:19]\ns_waitcnt vmcnt(13)\nds_read_b128 v[112:115], v1 offset:12288\nv_mfma_f32_16x16x32_bf16 v[16:19], v[180:183], v[76:79], v[16:19]\nv_mfma_f32_16x16x32_bf16 v[24:27], v[184:187], v[72:75], v[24:27]\nv_mfma_f32_16x16x32_bf16 v[24:27], v[188:191], v[76:79], v[24:27]\nbuffer_load_dwordx4 v[68:71], v0, %[rs], s60 offen offset:3072 sc1\nv_mfma_f32_16x16x32_bf16 v[32:35], v[192:195], v[72:75], v[32:35]\n" \
"ds_read_b128 v[116:119], v1 offset:13312\nv_mfma_f32_16x16x32_bf16 v[32:35], v[196:199], v[76:79], v[32:35]\nv_mfma_f32_16x16x32_bf16 v[40:43], v[200:203], v[72:75], v[40:43]\ns_add_u32 s60, s61, 36864\nv_mfma_f32_16x16x32_bf16 v[40:43], v[204:207], v[76:79], v[40:43]\ns_waitcnt vmcnt(13)\ns_waitcnt lgkmcnt(1)\nbuffer_load_dwordx4 v[72:75], v0, %[rs], s60 offen offset:1024 sc1\n" \
"v_mfma_f32_16x16x32_bf16 v[16:19], v[112:115], v[80:83], v[16:19]\ns_waitcnt vmcnt(13)\ns_waitcnt lgkmcnt(0)\nds_read_b128 v[120:123], v1 offset:14336\nv_mfma_f32_16x16x32_bf16 v[16:19], v[116:119], v[84:87], v[16:19]\nv_mfma_f32_16x16x32_bf16 v[24:27], v[208:211], v[80:83], v[24:27]\nv_mfma_f32_16x16x32_bf16 v[24:27], v[212:215], v[84:87], v[24:27]\nbuffer_load_dwordx4 v[76:79], v0, %[rs], s60 offen offset:3072 sc1\n" \
"v_mfma_f32_16x16x32_bf16 v[32:35], v[216:219], v[80:83], v[32:35]\nds_read_b128 v[124:127], v1 offset:15360\nv_mfma_f32_16x16x32_bf16 v[32:35], v[220:223], v[84:87], v[32:35]\nv_mfma_f32_16x16x32_bf16 v[40:43], v[224:227], v[80:83], v[40:43]\ns_add_u32 s60, s61, 49152\nv_mfma_f32_16x16x32_bf16 v[40:43], v[228:231], v[84:87], v[40:43]\ns_waitcnt vmcnt(13)\nbuffer_load_dwordx4 v[80:83], v0, %[rs], s60 offen offset:1024 sc1\n" \
"v_mfma_f32_16x16x32_bf16 v[16:19], v[232:235], v[88:91], v[16:19]\ns_waitcnt vmcnt(13)\nv_mfma_f32_16x16x32_bf16 v[16:19], v[236:239], v[92:95], v[16:19]\ns_waitcnt lgkmcnt(1)\nv_mfma_f32_16x16x32_bf16 v[24:27], v[120:123], v[88:91], v[24:27]\ns_waitcnt lgkmcnt(0)\nv_mfma_f32_16x16x32_bf16 v[24:27], v[124:127], v[92:95], v[24:27]\nbuffer_load_dwordx4 v[84:87], v0, %[rs], s60 offen offset:3072 sc1\n" \
"v_mfma_f32_16x16x32_bf16 v[32:35], v[240:243], v[88:91], v[32:35]\nv_mfma_f32_16x16x32_bf16 v[32:35], v[244:247], v[92:95], v[32:35]\nv_mfma_f32_16x16x32_bf16 v[40:43], v[248:251], v[88:91], v[40:43]\nv_mfma_f32_16x16x32_bf16 v[40:43], v[252:255], v[92:95], v[40:43]\ns_add_u32 s60, s61, 53248\nbuffer_load_dwordx4 v[88:91], v0, %[rs], s60 offen offset:1024 sc1\ns_cbranch_vccnz .Ldd_slow%=\n.Ldd_slowr%=:\n" \
"s_waitcnt vmcnt(14)\nv_mfma_f32_16x16x32_bf16 v[20:23], a[0:3], v[96:99], 0\ns_waitcnt vmcnt(13)\nds_read_b128 v[120:123], v1 offset:0\nv_mfma_f32_16x16x32_bf16 v[20:23], a[4:7], v[100:103], v[20:23]\nv_mfma_f32_16x16x32_bf16 v[28:31], a[8:11], v[96:99], 0\nv_mfma_f32_16x16x32_bf16 v[28:31], a[12:15], v[100:103], v[28:31]\nbuffer_load_dwordx4 v[92:95], v0, %[rs], s60 offen offset:3072 sc1\n" \
"v_mfma_f32_16x16x32_bf16 v[36:39], a[16:19], v[96:99], 0\nds_read_b128 v[124:127], v1 offset:1024\nv_mfma_f32_16x16x32_bf16 v[36:39], a[20:23], v[100:103], v[36:39]\nv_mfma_f32_16x16x32_bf16 v[44:47], a[24:27], v[96:99], 0\ns_add_u32 s60, s55, 65536\nv_mfma_f32_16x16x32_bf16 v[44:47], a[28:31], v[100:103], v[44:47]\ns_waitcnt vmcnt(13)\nbuffer_load_dwordx4 v[96:99], v0, %[rs], s60 offen offset:1024 sc1\n" \
"v_mfma_f32_16x16x32_bf16 v[20:23], a[32:35], v[104:107], v[20:23]\ns_waitcnt vmcnt(13)\nv_mfma_f32_16x16x32_bf16 v[20:23], a[36:39], v[108:111], v[20:23]\ns_waitcnt lgkmcnt(1)\nds_write_b128 v2, v[16:19] offset:0\nv_mfma_f32_16x16x32_bf16 v[28:31], v[120:123], v[104:107], v[28:31]\ns_waitcnt lgkmcnt(1)\nv_mfma_f32_16x16x32_bf16 v[28:31], v[124:127], v[108:111], v[28:31]\n" \
"buffer_load_dwordx4 v[100:103], v0, %[rs], s60 offen offset:3072 sc1\nv_mfma_f32_16x16x32_bf16 v[36:39], a[40:43], v[104:107], v[36:39]\nv_mfma_f32_16x16x32_bf16 v[36:39], a[44:47], v[108:111], v[36:39]\nv_mfma_f32_16x16x32_bf16 v[44:47], a[48:51], v[104:107], v[44:47]\ns_add_u32 s60, s55, 69632\nv_mfma_f32_16x16x32_bf16 v[44:47], a[52:55], v[108:111], v[44:47]\ns_waitcnt vmcnt(13)\nbuffer_load_dwordx4 v[104:107], v0, %[rs], s60 offen offset:1024 sc1\n" \
"v_mfma_f32_16x16x32_bf16 v[20:23], a[56:59], v[48:51], v[20:23]\ns_waitcnt vmcnt(13)\nds_read_b128 v[120:123], v1 offset:2048\nv_mfma_f32_16x16x32_bf16 v[20:23], a[60:63], v[52:55], v[20:23]\nds_write_b128 v2, v[24:27] offset:2048\nv_mfma_f32_16x16x32_bf16 v[28:31], a[64:67], v[48:51], v[28:31]\nv_mfma_f32_16x16x32_bf16 v[28:31], a[68:71], v[52:55], v[28:31]\nbuffer_load_dwordx4 v[108:111], v0, %[rs], s60 offen offset:3072 sc1\n" \
"v_mfma_f32_16x16x32_bf16 v[36:39], a[72:75], v[48:51], v[36:39]\nds_read_b128 v[124:127], v1 offset:3072\nv_mfma_f32_16x16x32_bf16 v[36:39], a[76:79], v[52:55], v[36:39]\nv_mfma_f32_16x16x32_bf16 v[44:47], a[80:83], v[48:51], v[44:47]\ns_add_u32 s60, s55, 81920\nv_mfma_f32_16x16x32_bf16 v[44:47], a[84:87], v[52:55], v[44:47]\ns_waitcnt vmcnt(13)\nbuffer_load_dwordx4 v[48:51], v0, %[rs], s60 offen offset:1024 sc1\n" \
"v_mfma_f32_16x16x32_bf16 v[20:23], a[88:91], v[56:59], v[20:23]\ns_waitcnt vmcnt(13)\nv_mfma_f32_16x16x32_bf16 v[20:23], a[92:95], v[60:63], v[20:23]\nds_write_b128 v2, v[32:35] offset:4096\nv_mfma_f32_16x16x32_bf16 v[28:31], a[96:99], v[56:59], v[28:31]\nv_mfma_f32_16x16x32_bf16 v[28:31], a[100:103], v[60:63], v[28:31]\nbuffer_load_dwordx4 v[52:55], v0, %[rs], s60 offen offset:3072 sc1\nv_mfma_f32_16x16x32_bf16 v[36:39], a[104:107], v[56:59], v[36:39]\n" \
"v_mfma_f32_16x16x32_bf16 v[36:39], a[108:111], v[60:63], v[36:39]\ns_waitcnt lgkmcnt(3)\nv_mfma_f32_16x16x32_bf16 v[44:47], v[120:123], v[56:59], v[44:47]\ns_waitcnt lgkmcnt(1)\ns_add_u32 s60, s55, 86016\nv_mfma_f32_16x16x32_bf16 v[44:47], v[124:127], v[60:63], v[44:47]\ns_waitcnt vmcnt(13)\nbuffer_load_dwordx4 v[56:59], v0, %[rs], s60 offen offset:1024 sc1\n" \
"v_mfma_f32_16x16x32_bf16 v[20:23], a[112:115], v[64:67], v[20:23]\ns_waitcnt vmcnt(13)\nds_read_b128 v[120:123], v1 offset:4096\nv_mfma_f32_16x16x32_bf16 v[20:23], a[116:119], v[68:71], v[20:23]\nds_write_b128 v2, v[40:43] offset:6144\nv_mfma_f32_16x16x32_bf16 v[28:31], a[120:123], v[64:67], v[28:31]\nv_mfma_f32_16x16x32_bf16 v[28:31], a[124:127], v[68:71], v[28:31]\nbuffer_load_dwordx4 v[60:63], v0, %[rs], s60 offen offset:3072 sc1\n" \
"v_mfma_f32_16x16x32_bf16 v[36:39], a[128:131], v[64:67], v[36:39]\nds_read_b128 v[124:127], v1 offset:5120\nv_mfma_f32_16x16x32_bf16 v[36:39], a[132:135], v[68:71], v[36:39]\nv_mfma_f32_16x16x32_bf16 v[44:47], a[136:139], v[64:67], v[44:47]\ns_add_u32 s60, s55, 98304\nv_mfma_f32_16x16x32_bf16 v[44:47], a[140:143], v[68:71], v[44:47]\ns_waitcnt vmcnt(13)\nbuffer_load_dwordx4 v[64:67], v0, %[rs], s60 offen offset:1024 sc1\n" \
"v_mfma_f32_16x16x32_bf16 v[20:23], a[144:147], v[72:75], v[20:23]\ns_waitcnt vmcnt(13)\nds_read_b128 v[112:115], v1 offset:6144\nv_mfma_f32_16x16x32_bf16 v[20:23], a[148:151], v[76:79], v[20:23]\ns_waitcnt lgkmcnt(3)\nv_mfma_f32_16x16x32_bf16 v[28:31], v[120:123], v[72:75], v[28:31]\ns_waitcnt lgkmcnt(1)\nv_mfma_f32_16x16x32_bf16 v[28:31], v[124:127], v[76:79], v[28:31]\n" \
"buffer_load_dwordx4 v[68:71], v0, %[rs], s60 offen offset:3072 sc1\nv_mfma_f32_16x16x32_bf16 v[36:39], a[152:155], v[72:75], v[36:39]\nds_read_b128 v[116:119], v1 offset:7168\nv_mfma_f32_16x16x32_bf16 v[36:39], a[156:159], v[76:79], v[36:39]\nbuffer_load_dwordx4 v[12:15], v4, %[rs], s41 offen sc1\nv_mfma_f32_16x16x32_bf16 v[44:47], a[160:163], v[72:75], v[44:47]\ns_add_u32 s60, s55, 102400\nv_mfma_f32_16x16x32_bf16 v[44:47], a[164:167], v[76:79], v[44:47]\n" \
"s_waitcnt vmcnt(14)\nbuffer_load_dwordx4 v[72:75], v0, %[rs], s60 offen offset:1024 sc1\nv_mfma_f32_16x16x32_bf16 v[20:23], a[168:171], v[80:83], v[20:23]\ns_waitcnt vmcnt(14)\nv_mfma_f32_16x16x32_bf16 v[20:23], a[172:175], v[84:87], v[20:23]\nv_mfma_f32_16x16x32_bf16 v[28:31], a[176:179], v[80:83], v[28:31]\nv_mfma_f32_16x16x32_bf16 v[28:31], a[180:183], v[84:87], v[28:31]\ns_waitcnt lgkmcnt(1)\n" \
"buffer_load_dwordx4 v[76:79], v0, %[rs], s60 offen offset:3072 sc1\nv_mfma_f32_16x16x32_bf16 v[36:39], v[112:115], v[80:83], v[36:39]\ns_waitcnt lgkmcnt(0)\nv_mfma_f32_16x16x32_bf16 v[36:39], v[116:119], v[84:87], v[36:39]\nv_mfma_f32_16x16x32_bf16 v[44:47], a[184:187], v[80:83], v[44:47]\nv_mfma_f32_16x16x32_bf16 v[44:47], a[188:191], v[84:87], v[44:47]\ns_waitcnt vmcnt(14)\nv_mfma_f32_16x16x32_bf16 v[20:23], a[192:195], v[88:91], v[20:23]\n" \
"s_waitcnt vmcnt(13)\nds_read_b128 v[112:115], v1 offset:8192\nv_mfma_f32_16x16x32_bf16 v[20:23], a[196:199], v[92:95], v[20:23]\nv_mfma_f32_16x16x32_bf16 v[28:31], a[200:203], v[88:91], v[28:31]\nv_mfma_f32_16x16x32_bf16 v[28:31], a[204:207], v[92:95], v[28:31]\nv_mfma_f32_16x16x32_bf16 v[36:39], a[208:211], v[88:91], v[36:39]\nds_read_b128 v[116:119], v1 offset:9216\nv_mfma_f32_16x16x32_bf16 v[36:39], a[212:215], v[92:95], v[36:39]\n" \
"v_mfma_f32_16x16x32_bf16 v[44:47], a[216:219], v[88:91], v[44:47]\nv_mfma_f32_16x16x32_bf16 v[44:47], a[220:223], v[92:95], v[44:47]\ns_waitcnt vmcnt(12)\ns_waitcnt lgkmcnt(1)\nv_mfma_f32_16x16x32_bf16 v[20:23], v[112:115], v[96:99], v[20:23]\ns_waitcnt vmcnt(11)\ns_waitcnt lgkmcnt(0)\nv_mfma_f32_16x16x32_bf16 v[20:23], v[116:119], v[100:103], v[20:23]\n" \
"v_mfma_f32_16x16x32_bf16 v[28:31], a[224:227], v[96:99], v[28:31]\nv_mfma_f32_16x16x32_bf16 v[28:31], a[228:231], v[100:103], v[28:31]\nv_mfma_f32_16x16x32_bf16 v[36:39], a[232:235], v[96:99], v[36:39]\nv_mfma_f32_16x16x32_bf16 v[36:39], a[236:239], v[100:103], v[36:39]\nv_mfma_f32_16x16x32_bf16 v[44:47], a[240:243], v[96:99], v[44:47]\nv_mfma_f32_16x16x32_bf16 v[44:47], a[244:247], v[100:103], v[44:47]\ns_waitcnt vmcnt(10)\nv_mfma_f32_16x16x32_bf16 v[20:23], a[248:251], v[104:107], v[20:23]\n" \
"s_waitcnt vmcnt(9)\nds_read_b128 v[112:115], v1 offset:10240\nv_mfma_f32_16x16x32_bf16 v[20:23], a[252:255], v[108:111], v[20:23]\nv_mfma_f32_16x16x32_bf16 v[28:31], v[128:131], v[104:107], v[28:31]\nv_mfma_f32_16x16x32_bf16 v[28:31], v[132:135], v[108:111], v[28:31]\nv_mfma_f32_16x16x32_bf16 v[36:39], v[136:139], v[104:107], v[36:39]\nds_read_b128 v[116:119], v1 offset:11264\nv_mfma_f32_16x16x32_bf16 v[36:39], v[140:143], v[108:111], v[36:39]\n" \
"v_mfma_f32_16x16x32_bf16 v[44:47], v[144:147], v[104:107], v[44:47]\nv_mfma_f32_16x16x32_bf16 v[44:47], v[148:151], v[108:111], v[44:47]\ns_waitcnt vmcnt(8)\nv_mfma_f32_16x16x32_bf16 v[20:23], v[152:155], v[48:51], v[20:23]\ns_waitcnt vmcnt(7)\nv_mfma_f32_16x16x32_bf16 v[20:23], v[156:159], v[52:55], v[20:23]\nv_mfma_f32_16x16x32_bf16 v[28:31], v[160:163], v[48:51], v[28:31]\nv_mfma_f32_16x16x32_bf16 v[28:31], v[164:167], v[52:55], v[28:31]\n" \
"s_waitcnt lgkmcnt(1)\nv_mfma_f32_16x16x32_bf16 v[36:39], v[112:115], v[48:51], v[36:39]\ns_waitcnt lgkmcnt(0)\nv_mfma_f32_16x16x32_bf16 v[36:39], v[116:119], v[52:55], v[36:39]\nv_mfma_f32_16x16x32_bf16 v[44:47], v[168:171], v[48:51], v[44:47]\nv_mfma_f32_16x16x32_bf16 v[44:47], v[172:175], v[52:55], v[44:47]\ns_waitcnt vmcnt(6)\nv_mfma_f32_16x16x32_bf16 v[20:23], v[176:179], v[56:59], v[20:23]\n" \
"s_waitcnt vmcnt(5)\nds_read_b128 v[112:115], v1 offset:12288\nv_mfma_f32_16x16x32_bf16 v[20:23], v[180:183], v[60:63], v[20:23]\nv_mfma_f32_16x16x32_bf16 v[28:31], v[184:187], v[56:59], v[28:31]\nv_mfma_f32_16x16x32_bf16 v[28:31], v[188:191], v[60:63], v[28:31]\nv_mfma_f32_16x16x32_bf16 v[36:39], v[192:195], v[56:59], v[36:39]\nds_read_b128 v[116:119], v1 offset:13312\nv_mfma_f32_16x16x32_bf16 v[36:39], v[196:199], v[60:63], v[36:39]\n" \
"v_mfma_f32_16x16x32_bf16 v[44:47], v[200:203], v[56:59], v[44:47]\nv_mfma_f32_16x16x32_bf16 v[44:47], v[204:207], v[60:63], v[44:47]\ns_waitcnt vmcnt(4)\ns_waitcnt lgkmcnt(1)\nv_mfma_f32_16x16x32_bf16 v[20:23], v[112:115], v[64:67], v[20:23]\ns_waitcnt vmcnt(3)\ns_waitcnt lgkmcnt(0)\nds_read_b128 v[120:123], v1 offset:14336\n" \
"v_mfma_f32_16x16x32_bf16 v[20:23], v[116:119], v[68:71], v[20:23]\nv_mfma_f32_16x16x32_bf16 v[28:31], v[208:211], v[64:67], v[28:31]\nv_mfma_f32_16x16x32_bf16 v[28:31], v[212:215], v[68:71], v[28:31]\nv_mfma_f32_16x16x32_bf16 v[36:39], v[216:219], v[64:67], v[36:39]\nds_read_b128 v[124:127], v1 offset:15360\nv_mfma_f32_16x16x32_bf16 v[36:39], v[220:223], v[68:71], v[36:39]\nv_mfma_f32_16x16x32_bf16 v[44:47], v[224:227], v[64:67], v[44:47]\nv_mfma_f32_16x16x32_bf16 v[44:47], v[228:231], v[68:71], v[44:47]\n" \
"s_waitcnt vmcnt(1)\nv_mfma_f32_16x16x32_bf16 v[20:23], v[232:235], v[72:75], v[20:23]\ns_waitcnt vmcnt(0)\nv_mfma_f32_16x16x32_bf16 v[20:23], v[236:239], v[76:79], v[20:23]\ns_waitcnt lgkmcnt(1)\nv_mfma_f32_16x16x32_bf16 v[28:31], v[120:123], v[72:75], v[28:31]\ns_waitcnt lgkmcnt(0)\nv_mfma_f32_16x16x32_bf16 v[28:31], v[124:127], v[76:79], v[28:31]\n" \
"v_mfma_f32_16x16x32_bf16 v[36:39], v[240:243], v[72:75], v[36:39]\nv_lshlrev_b32 v80, 16, v12\nv_and_b32 v81, 0xffff0000, v12\nv_mfma_f32_16x16x32_bf16 v[36:39], v[244:247], v[76:79], v[36:39]\nv_lshlrev_b32 v82, 16, v13\nv_and_b32 v83, 0xffff0000, v13\nv_mfma_f32_16x16x32_bf16 v[44:47], v[248:251], v[72:75], v[44:47]\nv_lshlrev_b32 v84, 16, v14\n" \
"v_and_b32 v85, 0xffff0000, v14\nv_mfma_f32_16x16x32_bf16 v[44:47], v[252:255], v[76:79], v[44:47]\ns_mul_i32 s44, s40, %[l]\nds_write_b128 v2, v[20:23] offset:1024\nds_write_b128 v2, v[28:31] offset:3072\nds_write_b128 v2, v[36:39] offset:5120\ns_nop 3\nds_write_b128 v2, v[44:47] offset:7168\n" \
"v_lshlrev_b32 v86, 16, v15\nv_and_b32 v87, 0xffff0000, v15\nv_mov_b32 v9, s40\ns_cmp_eq_u32 s44, 0\ns_cselect_b32 s56, %[fd], -2147483648\ns_add_u32 s40, s40, 1\ns_waitcnt lgkmcnt(0)\ns_barrier\n" \
"ds_read_b128 v[16:19], v3 offset:0\nds_read_b128 v[20:23], v3 offset:256\nds_read_b128 v[24:27], v3 offset:8192\nds_read_b128 v[28:31], v3 offset:8448\nds_read_b128 v[32:35], v3 offset:16384\nds_read_b128 v[36:39], v3 offset:16640\nds_read_b128 v[40:43], v3 offset:24576\nds_read_b128 v[44:47], v3 offset:24832\n" \
"s_waitcnt lgkmcnt(6)\nv_pk_add_f32 v[16:17], v[16:17], v[80:81]\nv_pk_add_f32 v[18:19], v[18:19], v[82:83]\nv_pk_add_f32 v[20:21], v[20:21], v[84:85]\nv_pk_add_f32 v[22:23], v[22:23], v[86:87]\ns_waitcnt lgkmcnt(4)\nv_pk_add_f32 v[16:17], v[16:17], v[24:25]\nv_pk_add_f32 v[18:19], v[18:19], v[26:27]\n" \
"v_pk_add_f32 v[20:21], v[20:21], v[28:29]\nv_pk_add_f32 v[22:23], v[22:23], v[30:31]\ns_waitcnt lgkmcnt(2)\nv_pk_add_f32 v[16:17], v[16:17], v[32:33]\nv_pk_add_f32 v[18:19], v[18:19], v[34:35]\nv_pk_add_f32 v[20:21], v[20:21], v[36:37]\nv_pk_add_f32 v[22:23], v[22:23], v[38:39]\ns_waitcnt lgkmcnt(0)\n" \
"v_pk_add_f32 v[16:17], v[16:17], v[40:41]\nv_pk_add_f32 v[18:19], v[18:19], v[42:43]\nv_pk_add_f32 v[20:21], v[20:21], v[44:45]\nv_pk_add_f32 v[22:23], v[22:23], v[46:47]\nv_cvt_pk_bf16_f32 v12, v16, v17\nv_cvt_pk_bf16_f32 v13, v18, v19\nv_cvt_pk_bf16_f32 v14, v20, v21\nv_cvt_pk_bf16_f32 v15, v22, v23\n" \
"buffer_store_dwordx4 v[12:15], v5, %[ro], s42 offen sc1\nv_pk_add_f32 v[24:25], v[16:17], v[18:19]\nv_pk_add_f32 v[26:27], v[20:21], v[22:23]\nv_pk_add_f32 v[24:25], v[24:25], v[26:27]\nv_add_f32 v24, v24, v25\ns_nop 1\nv_add_f32_dpp v25, v24, v24 row_ror:8 row_mask:0xf bank_mask:0xf\nv_add_f32_dpp v26, v24, v24 row_ror:8 row_mask:0xf bank_mask:0xf\n" \
"s_nop 1\nv_permlane16_swap_b32 v25, v26\nv_add_f32 v24, v25, v26\nv_add_f32 v25, v25, v26\ns_nop 1\nv_permlane32_swap_b32 v24, v25\nv_add_f32 v24, v24, v25\nv_mul_f32 v24, 0x3c800000, v24\n" \
"v_pk_add_f32 v[16:17], v[16:17], v[24:25] op_sel_hi:[1,0] neg_lo:[0,1] neg_hi:[0,1]\nv_pk_add_f32 v[18:19], v[18:19], v[24:25] op_sel_hi:[1,0] neg_lo:[0,1] neg_hi:[0,1]\nv_pk_add_f32 v[20:21], v[20:21], v[24:25] op_sel_hi:[1,0] neg_lo:[0,1] neg_hi:[0,1]\nv_pk_add_f32 v[22:23], v[22:23], v[24:25] op_sel_hi:[1,0] neg_lo:[0,1] neg_hi:[0,1]\nv_pk_mul_f32 v[28:29], v[16:17], v[16:17]\nv_pk_mul_f32 v[30:31], v[18:19], v[18:19]\nv_pk_fma_f32 v[28:29], v[20:21], v[20:21], v[28:29]\nv_pk_fma_f32 v[30:31], v[22:23], v[22:23], v[30:31]\n" \
"v_pk_add_f32 v[28:29], v[28:29], v[30:31]\nv_add_f32 v28, v28, v29\ns_nop 1\nv_add_f32_dpp v29, v28, v28 row_ror:8 row_mask:0xf bank_mask:0xf\nv_add_f32_dpp v30, v28, v28 row_ror:8 row_mask:0xf bank_mask:0xf\ns_nop 1\nv_permlane16_swap_b32 v29, v30\nv_add_f32 v28, v29, v30\n" \
"v_add_f32 v29, v29, v30\ns_nop 1\nv_permlane32_swap_b32 v28, v29\nv_add_f32 v28, v28, v29\nv_mov_b32 v25, v28\nbuffer_store_dwordx2 v[24:25], v6, %[rs], s41 offen sc1\nv_xor_b32 v2, 0x8000, v2\nv_xor_b32 v3, 0x8000, v3\n" \
"s_cmp_lt_u32 s40, 64\ns_cbranch_scc1 .Ldd_next%=\ns_waitcnt vmcnt(0)\nbuffer_store_dword v9, off, %[rs], s56 sc1\ns_branch .Ldd_end%=\n.Ldd_slow%=:\n.Ldd_w1%=:\nv_cmp_gt_i32 vcc, s58, v8\n" \
"s_cbranch_vccz .Ldd_w1go%=\ns_cmp_ge_u32 s40, 62\ns_cbranch_scc1 .Ldd_ns1225%=\ns_sleep 8\n.Ldd_ns1225%=:\ns_sleep 1\nbuffer_load_dword v8, v7, %[rs], 0 offen sc1\ns_waitcnt vmcnt(0)\n" \
"s_add_u32 s43, s43, 1\ns_cmp_lt_u32 s43, 262144\ns_cbranch_scc1 .Ldd_w1%=\ns_branch .Ldd_abort%=\n.Ldd_w1go%=:\ns_add_u32 s60, s55, 0\nbuffer_load_dwordx4 v[96:99], v0, %[rs], s60 offen offset:1024 sc1\nbuffer_load_dwordx4 v[100:103], v0, %[rs], s60 offen offset:3072 sc1\n" \
"s_add_u32 s60, s55, 4096\nbuffer_load_dwordx4 v[104:107], v0, %[rs], s60 offen offset:1024 sc1\nbuffer_load_dwordx4 v[108:111], v0, %[rs], s60 offen offset:3072 sc1\ns_add_u32 s60, s55, 16384\nbuffer_load_dwordx4 v[48:51], v0, %[rs], s60 offen offset:1024 sc1\nbuffer_load_dwordx4 v[52:55], v0, %[rs], s60 offen offset:3072 sc1\ns_add_u32 s60, s55, 20480\nbuffer_load_dwordx4 v[56:59], v0, %[rs], s60 offen offset:1024 sc1\n" \
"buffer_load_dwordx4 v[60:63], v0, %[rs], s60 offen offset:3072 sc1\ns_add_u32 s60, s55, 32768\nbuffer_load_dwordx4 v[64:67], v0, %[rs], s60 offen offset:1024 sc1\nbuffer_load_dwordx4 v[68:71], v0, %[rs], s60 offen offset:3072 sc1\ns_add_u32 s60, s55, 36864\nbuffer_load_dwordx4 v[72:75], v0, %[rs], s60 offen offset:1024 sc1\nbuffer_load_dwordx4 v[76:79], v0, %[rs], s60 offen offset:3072 sc1\ns_add_u32 s60, s55, 49152\n" \
"buffer_load_dwordx4 v[80:83], v0, %[rs], s60 offen offset:1024 sc1\nbuffer_load_dwordx4 v[84:87], v0, %[rs], s60 offen offset:3072 sc1\ns_add_u32 s60, s55, 53248\nbuffer_load_dwordx4 v[88:91], v0, %[rs], s60 offen offset:1024 sc1\ns_branch .Ldd_slowr%=\n.Ldd_abort%=:\ns_mov_b64 exec, -1\nv_mov_b32 v16, 0\n" \
"s_movk_i32 s44, 6400\nbuffer_store_dword v16, off, %[rs], s44 sc1\n.Ldd_end%=:\ns_waitcnt vmcnt(0) lgkmcnt(0)\n" \
""
#define GSW 0
#define PX4 1
#define QX4 7
#define G1F 1
#define A1F 0

#define V10(p, x) p #x "0", p #x "1", p #x "2", p #x "3", p #x "4", p #x "5", p #x "6", p #x "7", p #x "8", p #x "9"
#define V256(p) V10(p, ), V10(p, 1), V10(p, 2), V10(p, 3), V10(p, 4), V10(p, 5), V10(p, 6), V10(p, 7), V10(p, 8), V10(p, 9), V10(p, 10), V10(p, 11), V10(p, 12), V10(p, 13), V10(p, 14), V10(p, 15), V10(p, 16), \
    V10(p, 17), V10(p, 18), V10(p, 19), V10(p, 20), V10(p, 21), V10(p, 22), V10(p, 23), V10(p, 24), p "250", p "251", p "252", p "253", p "254", p "255"
#define CLOB "memory", "vcc", "scc", V256("v"), V256("a"), V10("s", 4), V10("s", 5), V10("s", 6), V10("s", 7), V10("s", 8)
template <int N> DEVI void table(lds_char* p, const unsigned (&t)[N]) {
#pragma unroll
    for (int i = 0; i < N; ++i) *(l1*)(p + i * 256) = t[i];
    WL0;
}
DEVI unsigned prof_at(int cu, int w) { return O_PROF + (262144 + cu * 2560 + w * 8) * 4; }
DEVI unsigned sp_idx(int lane) { return (lane & 2) ? 0xEEEEu : 0x4444u; }
DEVI int sp_row(int r, int hf) { return 4 * (r >> 1) + 2 * hf + (r & 1); }

DEVI void qk_role(const Args& a, const int w, lds_char* smem, int l, int h) {
    const int lane = lane_id(), g = lane >> 4, n = lane & 15, b = n & 3, p = n >> 2;
    const unsigned l0 = (unsigned)(size_t)smem, tb = l0 + w * 12288, lw = l0 + 55296 + w * 24576, ls = l * SD, IMG = l0 + 32768, MIM = l0 + 49152;
    const unsigned xw = (w >> 1) * 16384 + (w & 1) * 4096, oc = (h >> 3) * 16384 + ls, tr = (8 * g + p) * 128 + b * 16;
    const unsigned cm[4] = {4 * g > n ? 0xF149F2CAu : 0u, 4 * g + 1 > n ? 0xF149F2CAu : 0u, 4 * g + 2 > n ? 0xF149F2CAu : 0u, 4 * g + 3 > n ? 0xF149F2CAu : 0u};
    const unsigned t[19] = {
        l0 + lane * 16, lw + lane * 16, l0 + (g >> 1) * 1024 + (g & 1) * 512 + n * 16, O_E1 + ls + xw + lane * 16, sp_idx(lane),
        IMG + (w >> 1) * 8192 + ((w & 1) * 8 + 2 * g) * 512 + n * 16,
        MIM + (w * 8 + 2 * p) * 144 + g * 32 + b * 4,
        IMG + g * 512 + (w ? n * 16 : 8192 + (16 * (b >> 1) + 8 * (p >> 1) + 2 * (p & 1) + (b & 1)) * 16),
        MIM + g * 144 + (n & 7) * 16, MIM + 2304 + g * 144 + (2 * p + (b & 1)) * 16,
        w ? O_PT + (h >> 3) * 32768 + ls + lane * 16 + (w == 1 ? 16384 + ((h & 7) >> 1) * 1024 + (h & 1) * 8 : (h & 7) * 1024)
          : (QX4 & 1) && n >> 3 ? OOB : O_PM + oc + ((h >> 1) & 3) * 1024 + ((2 * (h & 1) + (g >> 1)) * 16 + sp_row(n & 7, g & 1)) * 16 + (n >> 3) * 8,
        w ? (QX4 & 4) && g ? OOB : O_PT + (h >> 3) * 32768 + ls + 20480 + ((h >> 2) & 1) * 2048 + (w - 1) * 1024 + (h & 3) * 256 + n * 16 + g * 4
          : n >> 3 || ((QX4 & 2) && g >> 1) ? OOB : O_PM + oc + 4096 + ((h & 7) >> 1) * 256 + sp_row(n & 7, g & 1) * 16 + (h & 1) * 8 + (g >> 1) * 4,
        w == 3 ? (l ? F_X + SD + p * 128 + (X1F ? 0 : b * 4) : F_X0 + b * 4) : cm[0], cm[1], cm[2], cm[3],
        (lane >> 3) * 8192u + (lane & 7) * 16, tb + tr, lw + tr};
    table(smem + w * 12288 + lane * 4, t);
    asm volatile(QK_ASM : : [ws] "s"(a.ws), [sz] "n"(WS_RS), [rw] "s"(mkrs(w < 2 ? a.Wq : a.Wk, 8u << 20)), [wo] "s"(h * 256 + (w & 1) * 128), [tb] "s"(tb), [lw] "s"(lw), [l0] "s"(l0), [w] "s"(w),
        [xn] "s"(O_XN + ls), [xw] "s"(xw), [fs] "s"(l ? 0 : 128), [sg] "s"(F_Y0 + 4096 + ls + h * 128 + 16), [pr] "s"(prof_at(l * 80 + h, w)) : CLOB, "s36", "s37", "s38", "s39");
    __builtin_amdgcn_endpgm();
}
DEVI void vu_role(const Args& a, const int w, lds_char* smem, int l, int h) {
    const int lane = lane_id(), g = lane >> 4, n = lane & 15, b = n & 3, p = n >> 2;
    const unsigned l0 = (unsigned)(size_t)smem, tb = l0 + w * 12288, lw = l0 + 61440 + w * 24576, ls = l * SD;
    const unsigned xw = (w >> 1) * 16384 + (w & 1) * 4096, tv = (8 * g + p) * 128 + b * 16 + (w & 1) * 64, to = (4 * g + (p & 1)) * 128 + (p >> 1) * 32 + b * 8;
    const unsigned t[18] = {
        l0 + lane * 16, lw + lane * 16, l0 + (g >> 1) * 1024 + (g & 1) * 512 + n * 16, O_E1 + ls + xw + lane * 16, sp_idx(lane),
        l0 + 57344 + (2 * w + (n >> 3)) * 256 + (n >> 3) * 128 + g * 32 + (n & 7) * 2,
        O_UM + ls + (4 * w + (g & 1)) * 65536 + (h >> 3) * 16384 + (g >> 1) * 2048 + (h & 7) * 256 + n * 16,
        sd(O_Y + h * 131072 + ls) + w * 32768 + (g >> 1) * 1024 + (g & 1) * 256 + n * 16,
        l ? F_X + SD + p * 128 + (X1F ? 0 : b * 4) : F_X0 + b * 4, l0 + 49152 + (w >> 1) * 2048 + (2 * (w & 1) + (g >> 1)) * 256 + (n ^ (g >> 1) * 8) * 16 + (g & 1) * 8, sp_idx(lane >> 2), l0 + g * 256 + (g & 1) * 128 + (n & 7) * 16,
        (lane >> 3) * 8192u + (lane & 7) * 16, (8 * (lane >> 5) + ((lane >> 3) & 3)) * 2048u + (lane & 7) * 16, tb + tv - (w & 1) * 12288, lw + tv - (w & 1) * 24576, tb + to, lw + to};
    table(smem + w * 12288 + lane * 4, t);
    asm volatile(VU_ASM : : [ws] "s"(a.ws), [sz] "n"(WS_RS), [rw] "s"(mkrs(a.Wv, 8u << 20)), [ro] "s"(mkrs(a.Wo, 8u << 20)), [wo] "s"(h * 256 + (w >> 1) * 128 + (w & 1) * 262144), [w2] "s"(h * 262144 + w * 512), [tb] "s"(tb), [lw] "s"(lw), [w] "s"(w),
        [xn] "s"(O_XN + ls), [xw] "s"(xw), [fs] "s"(l ? 0 : 128), [xt] "s"(w * 16384 - xw), [fy] "s"(F_Y0 + ls + h * 128 + w * 4), [sg] "s"(w == 1 ? F_Y0 + ls + h * 128 + 16 : OOB), [pr] "s"(prof_at(l * 80 + 32 + h, w) - 128) : CLOB, "s36", "s37", "s38", "s39");
    __builtin_amdgcn_endpgm();
}
template <class F> DEVI void rd_role(const Args& a, const int w, lds_char* smem, int l, int c, F&& idle_work) {
    const int lane = lane_id(), g = lane >> 4, n = lane & 15, r = 2 * (n >> 2) + (n & 1), hf = (n >> 1) & 1;
    const rsrc_t rs = mkrs(a.ws, WS_RS);
    const unsigned l0 = (unsigned)(size_t)smem, tb = l0 + 65536 + w * 4096, ls = l * SD, vp = O_E1 + ls + (c >> 3) * 16384 + (c & 7) * 1024 + w * 256 + n * 16;
    {
        const v4u ff = {SENT, SENT, SENT, SENT};
#pragma unroll
        for (int i = 0; i < 8; ++i) st16(ff, rs, O_UM + ls + c * 65536 + w * 16384 + i * 1024 + lane * 16);
#pragma unroll
        for (int i = 0; i < 2; ++i) st16(ff, rs, O_PT + ls + c * 16384 + w * 2048 + i * 1024 + lane * 16);
        st16(ff, rs, c < 8 ? O_PM + ls + w * 16384 + c * 1024 + lane * 16 : OOB);
        st16(ff, rs, g ? vp + g * 32768 : OOB);
        WV0; BAR;
    }
    st4(g & 1 ? *(const unsigned*)(a.mem0 + r * 1024 + c * 64 + w * 16 + 4 * g + 2 * hf) : nosent(*(const unsigned*)(a.mem0 + r * 1024 + c * 64 + w * 16 + 4 * g + 2 * hf)), rs, g & 1 ? vp + g * 4 : OOB, 0);
    WV0;
    st4(nosent(*(const unsigned*)(a.mem0 + r * 1024 + c * 64 + w * 16 + 4 * g + 2 * hf)), rs, g & 1 ? OOB : vp + g * 4, 0);
    idle_work();
    const int gi = lane >> 3, r8 = lane & 7;
    const unsigned t[12] = {
        (unsigned)lane * 16, sp_idx(lane), l0 + w * 4096 + g * 576 + ((n >> 3) * 16 + hf * 4 + 2 * ((n >> 2) & 1) + (n & 1)) * 4,
        l0 + (r >> 1) * 576 + w * 128 + (g >> 1) * 64 + (r & 1) * 32 + hf * 16,
        PX4 && g >> 1 ? OOB : (vp ^ ((g & 1) * 65536)) + (g >> 1) * 8, g & 1 ? SENT : 0u,
        l0 + 16384 + w * 8192 + lane * 16, l0 + 16384 + ((gi >> 1) * 2 + (w >> 1)) * 1024 + ((gi & 1) * 32 + (w & 1) * 8 + r8) * 16,
        (c >> 1) * 16384u + (c & 1) * 4096 + gi * 512 + (w * 8 + r8) * 16,
        gi ? OOB : O_ST - O_XR + c * 256 + ((w & 1) * 8 + r8) * 16 + (w >> 1) * 8,
        F_Y0 + ls + (w * 8 + r8) * 128 + (c >> 2) * 4, F_Y0 + ls + (gi & 1) * 4096 + (w * 8 + r8) * 128 + 16};
    table(smem + 65536 + w * 4096 + lane * 4, t);
    asm volatile(RD_ASM : : [rs] "s"(rs), [tb] "s"(tb), [su] "s"(O_UM + ls + c * 65536 + w * 16384), [sp] "s"(O_PM + ls + w * 16384), [st] "s"(O_PT + ls + w * 32768),
        [sy] "s"(sd(O_Y + w * (1u << 20) + ls) + (c >> 1) * 16384 + (c & 1) * 4096), [at] "s"(O_XR + ls), [du] "s"((c & 2) * 5 / 2), [fa] "s"(A1F && w ? OOB : F_A + ls + c * 128 + w * 4), [pr] "s"(prof_at(l * 80 + 64 + c, w) + 128) : CLOB);
    __builtin_amdgcn_endpgm();
}
DEVI void gg_role(const Args& a, const int w, lds_char* smem, int l, int gi) {
    const int lane = lane_id(), g = lane >> 4, n = lane & 15, b = n & 3, p = n >> 2;
    const unsigned l0 = (unsigned)(size_t)smem, tb = l0 + w * 12288, lw = l0 + 65536 + w * 24576, ls = l * SD, tv = (8 * g + p) * 128 + b * 16 + (w & 1) * 64, fh = (g >> 1) * 1024 + (g & 1) * 512 + n * 16;
    const unsigned lf = GSW ? 0 : ls, fo = w ? OOB : F_H + (gi / 7) * 1024 + (gi % 7) * 128;
    const unsigned t[9] = {l0 + lane * 16, lw + lane * 16, l0 + fh, w ? OOB : F_A + lf + (lane >> 2) * 128 + (A1F ? 0 : (lane & 3) * 4), w * 2048u + lane * 16, O_ST - O_XR + g * 1024 + n * 16, (lane >> 3) * 7168u + (lane & 7) * 16, tb + tv - (w & 1) * 12288, lw + tv - (w & 1) * 24576};
    table(smem + w * 12288 + lane * 4, t);
    asm volatile(GG_ASM : : [rs] "s"(mkrs(a.ws, WS_RS)), [rw] "s"(mkrs(w & 1 ? a.Wu : a.Wg, 3584 * 2048)), [wo] "s"(gi * 256 + (w >> 1) * 128), [tb] "s"(tb), [lw] "s"(lw), [w] "s"(w),
        [xn0] "s"(O_XR + lf), [xw] "s"((w >> 1) * 16384 + (w & 1) * 4096), [ho0] "s"(O_H + lf + gi * 16384), [fh0] "s"(fo + (GSW ? l * 4 : ls)), [fh1] "s"(fo + ls), [b0] "s"(l ? 0 : GSW), [b1] "s"(l && GSW ? GSW : 64), [pr] "s"(prof_at(160 + l * 28 + gi, w) + 128) : CLOB);
    __builtin_amdgcn_endpgm();
}
DEVI void dd_role(const Args& a, const int w, lds_char* smem, int l, int c) {
    const int lane = lane_id(), g = lane >> 4, b = lane & 3, p = (lane >> 2) & 3, gi = lane >> 3, r8 = lane & 7;
    const unsigned l0 = (unsigned)(size_t)smem, tb = l0 + w * 12288, lw = l0 + 65536 + w * 24576, ls = l * SD, tr = (8 * g + p) * 128 + b * 8;
    const unsigned ta = (c >> 1) * 16384u + (c & 1) * 4096 + gi * 512 + (w * 8 + r8) * 16;
    const rsrc_t rs = mkrs(a.ws, WS_RS);
    const unsigned t[11] = {(unsigned)lane * 16, lw + lane * 16, l0 + w * 8192 + lane * 16, l0 + ((gi >> 1) * 2 + (w >> 1)) * 1024 + ((gi & 1) * 32 + (w & 1) * 8 + r8) * 16,
        ta, l ? (w * 8 + r8) * 2048 + c * 128 + gi * 16 : ta, gi || l ? OOB : O_ST - O_XR + 4096 + c * 256 + (w * 8 + r8) * 8,
        F_H + ls + w * 1024 + ((lane >> 2) % 7) * 128 + (GSW && !l ? 4 : 0), gi * 2048u + r8 * 16, tb + tr, lw + tr};
    table(smem + w * 12288 + lane * 4, t);
    asm volatile(DD_ASM : : [rs] "s"(rs), [rw] "s"(mkrs(a.Wd, 3584 * 2048)), [ro] "s"(mkrs(l ? (char*)a.out : a.ws, l ? 2048 * 2048 : WS_RS)), [wo] "s"(w * (896 * 2048) + c * 128), [tb] "s"(tb), [lw] "s"(lw), [l] "s"(l), [b1] "s"(GSW && !l ? GSW : 99),
        [xn] "s"(O_XR + ls), [ho] "s"(O_H + ls + w * 7 * 16384), [fd] "s"(D1F && w ? OOB : F_D + ls + c * 128 + w * 4), [lt] "s"(F_D + ls + c * 128 + w * 4 + 64), [pr] "s"(prof_at(216 + l * 16 + c, w) + 128) : CLOB);
    __builtin_amdgcn_endpgm();
}

#define PFG 0
#define PFT 1000
#define PFX 0
#define CPF 74
#define CPJ 0
#define LNW 0
#define DLQ 150
#define DLS 400
#define SPL 0
#define PLAN 3
#define PFA 0
#define PFB 3
#define PFL 1
#define VUX 1
#define LNS 40
#define LN1 700
#define DLG 1100
#define DLD 1500
#define DL1 1500
#define DLG1 2600
#define DLD1 3000
DEVI Args args(unsigned sh) {
    auto p = (const __attribute__((address_space(4))) Args*)__builtin_amdgcn_kernarg_segment_ptr();
    asm volatile("" : "+s"(p));
    Args a;
    a.x = p->x; a.mem0 = p->mem0; a.Wq = p->Wq; a.Wk = p->Wk; a.Wv = p->Wv; a.Wo = p->Wo; a.Wg = p->Wg; a.Wu = p->Wu; a.Wd = p->Wd; a.ws = p->ws + sh; a.out = p->out;
    return a;
}
DEVI unsigned beat_wait(unsigned x0) {
    const rsrc_t r0 = mkrs(args(0).ws, 2u << 20);
    for (unsigned spins = 0; spins < SPIN_CAP; ++spins) {
        CB;
        const v2u t = __builtin_amdgcn_raw_buffer_load_b64(r0, 0, O_HB, AUX_SC1);
        if (RFL(t[0]) != x0) return RFL(t[1]) & (2u << 20);
        __builtin_amdgcn_s_sleep(8);
    }
    __builtin_amdgcn_endpgm();
}
#define R2W 0
#define MPF 0
DEVI void mpf(rsrc_t rs, unsigned o) {
    o += O_E1 + lane_id() * 128;
    unsigned spins = 0;
    for (int s = 0; s < NSEG && spins < SPIN_CAP; ++spins) {
        CB;
        if (__ballot(ld4(rs, o, (s & 3) * 32768) == SENT) == 0ull) { ++s; for (int i = 0; i < MPF; ++i) __builtin_amdgcn_s_sleep(8); }
    }
}
DEVI void leader(const Args& a, const int w, lds_char* smem, unsigned x0) {
    const int lane = lane_id();
    const rsrc_t r0 = mkrs(a.ws, 4u << 20);
    unsigned t[2] = {~0u, ~0u};
    for (int i = 0; i < 2 * NPRB; ++i) {
        const unsigned t0 = (unsigned)__builtin_amdgcn_s_memtime();
        WL0;
        if (lane == 0) st4(0u, r0, 0, O_PRB + (i & 1) * SD + w * 1024 + (i >> 1) * 128);
        WV0;
        const unsigned d = (unsigned)__builtin_amdgcn_s_memtime() - t0;
        if (i & 1) t[1] = min(t[1], d); else t[0] = min(t[0], d);
    }
    *(l2*)(smem + w * 8) = v2u{t[0], t[1]};
    WL0; BAR;
    const v4u p = *(l4*)smem, q = *(l4*)(smem + 16);
    const unsigned sh = RFL(min(min(p[0], p[2]), min(q[0], q[2])) < min(min(p[1], p[3]), min(q[1], q[3])) ? 2u << 20 : 0u);
    const unsigned o = sh + w * 1024 + lane * 16;
    const v4u ff = {SENT, SENT, SENT, SENT};
#pragma unroll
    for (int i = 0; i < 8; ++i) st16(ff, r0, o + O_E1 + i * 4096);
#pragma unroll
    for (int i = 1; i < 16; ++i) if (i != 2 && i != 11) st16(ff, r0, o + i * 4096);
    WV0; BAR;
    if (w) { if (MPF && w > 1) mpf(r0, sh + SD + (w & 1) * 16384); return; }
    for (unsigned spins = 0; spins < SPIN_CAP; ++spins) {
        if (lane == 0) st8(v2u{++x0, sh}, r0, 0, O_HB);
        if (__ballot((int)ld4(r0, sh + F_D + SD + (lane >> 2) * 128 + (D1F ? 0 : (lane & 3) * 4), 0) < 0) == 0ull) break;
        __builtin_amdgcn_s_sleep(16);
    }
}
__global__ __launch_bounds__(256, 1) void sys_kernel(Args) {
    extern __shared__ __attribute__((aligned(16))) char smem_[];
    lds_char* smem = (lds_char*)smem_;
    const int x = blockIdx.x & 7, j = blockIdx.x >> 3, wv = RFL(threadIdx.x >> 6), l = x >> 2, xx = x & 3;
    const unsigned t0 = (unsigned)__builtin_amdgcn_s_memrealtime();
    auto until = [&](unsigned dl) AI { while ((unsigned)__builtin_amdgcn_s_memrealtime() - t0 < dl) __builtin_amdgcn_s_sleep(8); };
    unsigned x0 = 0;
    if (l || j >= 16) x0 = RFL(ld4(mkrs(args(0).ws, 2u << 20), 0, O_HB));
    if (j < 16) {
        if (l) { until(j ? LN1 + j * LNS : 0); ln0(args(0), wv, j * 4 + xx, [&]() AI { return beat_wait(x0); }); until(DL1); }
        if (DLQ && !l && j < 8) until(DLQ);
        if (j < 8) qk_role(args(0), wv, smem, l, xx * 8 + j); else vu_role(args(0), wv, smem, l, (xx ^ VUX) * 8 + j - 8);
        return;
    }
    if (j >= 16 + CPJ && j < 20 + CPJ) {
        unsigned long long pc; unsigned n;
        asm volatile("s_getpc_b64 %0\n\ts_mov_b32 %1, .-_Z10sys_kernel4Args" : "=s"(pc), "=s"(n));
        const rsrc_t rc = mkrs((const void*)(pc - n), CPF * 1024);
        __builtin_amdgcn_raw_ptr_buffer_load_lds(rc, smem + LDSB - 1024 + wv * 256, 4, lane_id() * 128, ((j - 16 - CPJ) * 4 + wv) * 8192, 0, 0);
    }
    {
        const int lane = lane_id(), q = (j - 16) * 4 + wv, m = SPL && !l ? ((xx & 1) != (SPL & 1)) * 2 + (q >> 5) : q >> 4;
        const Args a = args(0);
        const u16 *W0 = a.Wq, *W1 = a.Wk, *W2 = a.Wv, *W3 = a.Wo;
        asm volatile("" : "+s"(W0), "+s"(W1), "+s"(W2), "+s"(W3));
        const u16* W = m == 0 ? W0 : m == 1 ? W1 : m == 2 ? W2 : W3;
        const rsrc_t rw = mkrs(W, 8u << 20);
        if (l) {
#pragma unroll
            for (int i = 0; i < PFA; ++i) __builtin_amdgcn_raw_ptr_buffer_load_lds(rw, smem + LDSB - 1024 + wv * 256, 4, lane * 256, i * (1u << 20) + (xx * 16 + (q & 15)) * 16384, 0, 0);
        } else {
            const unsigned po = (0x100u - (unsigned)(size_t)W) & 0x300u, hx = SPL ? (xx & 2) + ((q >> 4) & 1) : m < 2 ? xx : xx ^ VUX;
            const unsigned vo = m == 3 ? (lane >> 1) * 1024 + (lane & 1) * 128 : (lane >> 2) * 8192 + ((lane >> 1) & 1) * 1024 + (lane & 1) * 128;
            const unsigned so = po + (m == 3 ? hx * (2u << 20) + (q & 15) * 32768 : hx * 2048 + (q & 15) * 131072);
#pragma unroll
            for (int i = 0; i < PFB; ++i) __builtin_amdgcn_raw_ptr_buffer_load_lds(rw, smem + LDSB - 1024 + wv * 256, 4, vo, so + i * (m == 3 ? 524288 : 2u << 20), 0, 0);
            const unsigned pl = (0x1F00u - (unsigned)(size_t)W) & 0x1F00u;
#pragma unroll
            for (int i = 0; i < PFL; ++i) __builtin_amdgcn_raw_ptr_buffer_load_lds(rw, smem + LDSB - 1024 + wv * 256, 4, (lane >> 1) * 8192 + (lane & 1) * 128, (pl >> 11) == hx && m < 3 ? pl + ((q & 15) + 16 * i) * 262144 : OOB, 0, 0);
        }
    }
    int ro, ix;
    if ((xx & 1) == (PLAN & 1)) { if (j < 24) { ro = 0; ix = (xx >> 1) * 8 + j - 16; } else if (j < 26) { ro = 3; ix = (xx >> 1) * 2 + j - 24; } else { ro = 1; ix = (xx >> 1) * 14 + j - 26; } }
    else if (j < 24) { ro = 1; ix = (xx >> 1) * 14 + j - 10; } else { ro = 2; ix = (xx >> 1) * 8 + j - 24; }
    if (ro == 3 && l) { if (ix == 0) leader(args(0), wv, smem, x0); else if (MPF && wv > 1) mpf(mkrs(args(0).ws, 4u << 20), beat_wait(x0) + SD + (wv & 1) * 16384); return; }
    const unsigned sh = beat_wait(x0);
    auto pfg = [&]() AI {
        if (!PFG || l) return;
        until(PFT);
        const Args a = args(0);
        const int lane = lane_id(), q = (ix & 7) * 4 + wv;
        const rsrc_t rw = mkrs(q & 1 ? a.Wu : a.Wg, 3584 * 2048);
        const unsigned vo = (lane & 15) < ((xx & 1) ? 12 : 16) ? (lane >> 4) * 7168 + (lane & 15) * 128 : OOB, so = ((xx >> 1) * 14 + ((xx & 1) ? 0 : 6)) * 256 + (1024 - 64 * PFG + (q >> 1) * 4) * 7168;
#pragma unroll
        for (int i = 0; i < PFG; ++i) __builtin_amdgcn_raw_ptr_buffer_load_lds(rw, smem + LDSB - 1024 + wv * 256, 4, vo, so + i * 64 * 7168, 0, PFX);
    };
    if (ro == 0) { rd_role(args(sh), wv, smem, l, ix, pfg); return; }
    if (ro == 1) { const Args a = args(sh); until((l ? DLG1 : DLG) - (((((unsigned)(size_t)a.Wg >> 8) + ix) & 3) == 1 || ((((unsigned)(size_t)a.Wu >> 8) + ix) & 3) == 1 ? DLS : 0)); WV0; BAR; gg_role(a, wv, smem, l, ix); return; }
    if (ro == 2) { pfg(); const Args a = args(sh); until((l ? DLD1 : DLD) - (((((unsigned)(size_t)a.Wd >> 8) + (ix >> 1)) & 3) == 1 ? DLS + 200 : 0)); WV0; BAR; dd_role(a, wv, smem, l, ix); return; }
    r_role(args(sh), wv, ix);
}

extern "C" void kernel_launch(void* const* d_in, const int* in_sizes, int n_in, void* d_out, int out_size, void* d_ws, size_t ws_size, hipStream_t stream) {
    (void)in_sizes; (void)n_in; (void)out_size; (void)ws_size;
    Args a;
    a.x = (const u16*)d_in[0]; a.mem0 = (const u16*)d_in[1]; a.Wq = (const u16*)d_in[2]; a.Wk = (const u16*)d_in[3]; a.Wv = (const u16*)d_in[4]; a.Wo = (const u16*)d_in[5];
    a.Wg = (const u16*)d_in[8]; a.Wu = (const u16*)d_in[9]; a.Wd = (const u16*)d_in[10];
    a.ws = (char*)(((uintptr_t)d_ws + (2u << 20) - 1) & ~(uintptr_t)((2u << 20) - 1)); a.out = (u16*)d_out;
    (void)hipFuncSetAttribute((const void*)sys_kernel, hipFuncAttributeMaxDynamicSharedMemorySize, LDSB);
    hipLaunchKernelGGL(sys_kernel, dim3(256), dim3(256), LDSB, stream, a);
}
```

```cpp
#include <hip/hip_runtime.h>
#include <stdint.h>

typedef unsigned short u16;
typedef unsigned long long u64;
typedef __attribute__((ext_vector_type(8))) short bf16x8;
typedef __attribute__((ext_vector_type(4))) short s16x4;
typedef __attribute__((ext_vector_type(4))) float f32x4;
typedef __attribute__((ext_vector_type(16))) float f32x16;
typedef __attribute__((ext_vector_type(2))) float f32x2;
typedef __attribute__((ext_vector_type(4))) unsigned v4u;
typedef __attribute__((ext_vector_type(2))) unsigned v2u;
typedef __amdgpu_buffer_rsrc_t rsrc_t;
typedef __attribute__((address_space(3))) char lds_char;
typedef __attribute__((address_space(3))) bf16x8 l8;
typedef __attribute__((address_space(3))) v4u l4;
typedef __attribute__((address_space(3))) v2u l2;
typedef __attribute__((address_space(3))) unsigned l1;
typedef __attribute__((address_space(3))) f32x4 lf4;
typedef __attribute__((address_space(3))) float lf1;
#define DEVI __device__ __forceinline__
#define AI __attribute__((always_inline))

constexpr int NSEG = 64;
constexpr float ATT_SCALE = 0.08838834764831845f;
constexpr float LOG2E = 1.4426950408889634f;
#define NPRB 4
constexpr unsigned SD = 8192;
DEVI unsigned sd(unsigned o) { return o ^ ((o >> 8) & SD); }
constexpr unsigned F_X0 = 32768;
constexpr unsigned F_X = 4096;
constexpr unsigned F_A = 16384;
constexpr unsigned F_H = F_A + 2048;
constexpr unsigned F_D = F_H + 4096;
constexpr unsigned F_Y0 = 49152;
constexpr unsigned F_ABORT = 6400;
constexpr unsigned O_PM = 65536;
constexpr unsigned O_E1 = 131072;
constexpr unsigned O_PT = 262144;
constexpr unsigned O_UM = 524288;
constexpr unsigned CTRL_END = O_UM + (1u << 20);
constexpr unsigned O_PRB = 0x1F0000, O_HB = 0x1FFF00;
constexpr unsigned O_XN = 4u << 20;
constexpr unsigned O_XR = O_XN + (8u << 20);
constexpr unsigned O_ST = O_XR + (8u << 20);
constexpr unsigned O_H = O_ST + (8u << 20);
constexpr unsigned O_Y = O_H + (36u << 20);
constexpr unsigned WS_NEED = O_Y + (8u << 20);
constexpr unsigned O_PROF = WS_NEED;
constexpr unsigned WS_RS = WS_NEED;
static_assert(CTRL_END <= O_PRB && CTRL_END % 16384 == 0 && O_Y % (8u << 20) == 0, "workspace");
constexpr unsigned OOB = 0x80000000u, OOB2 = 0x90000000u, SENT = 0xFFFFFFFFu;

struct Args { const u16 *x, *mem0, *Wq, *Wk, *Wv, *Wo, *Wg, *Wu, *Wd; char* ws; u16* out; };

typedef __attribute__((ext_vector_type(2))) __bf16 bf16x2_t;
DEVI unsigned pack2(float lo, float hi) { f32x2 v = {lo, hi}; bf16x2_t b = __builtin_convertvector(v, bf16x2_t); return __builtin_bit_cast(unsigned, b); }
DEVI float bflo(unsigned u) { return __uint_as_float(u << 16); }
DEVI float bfhi(unsigned u) { return __uint_as_float(u & 0xffff0000u); }
DEVI float rbf(float x) { return bflo(pack2(x, x)); }
DEVI bf16x8 as8(v4u v) { return __builtin_bit_cast(bf16x8, v); }
DEVI f32x4 mfma16(bf16x8 a, bf16x8 b, f32x4 c) { return __builtin_amdgcn_mfma_f32_16x16x32_bf16(a, b, c, 0, 0, 0); }
DEVI f32x16 mfma32(bf16x8 a, bf16x8 b, f32x16 c) { return __builtin_amdgcn_mfma_f32_32x32x16_bf16(a, b, c, 0, 0, 0); }
constexpr int AUX_SC1 = 16;
DEVI v4u ld16(rsrc_t r, unsigned vo, unsigned so) { return __builtin_amdgcn_raw_buffer_load_b128(r, vo, so, AUX_SC1); }
DEVI unsigned ld4(rsrc_t r, unsigned vo, unsigned so) { return __builtin_amdgcn_raw_buffer_load_b32(r, vo, so, AUX_SC1); }
DEVI void st16(v4u v, rsrc_t r, unsigned vo) { __builtin_amdgcn_raw_buffer_store_b128(v, r, vo, 0, AUX_SC1); }
DEVI void st8(v2u v, rsrc_t r, unsigned vo, unsigned so) { __builtin_amdgcn_raw_buffer_store_b64(v, r, vo, so, AUX_SC1); }
DEVI void st4(unsigned v, rsrc_t r, unsigned vo, unsigned so) { __builtin_amdgcn_raw_buffer_store_b32(v, r, vo, so, AUX_SC1); }
DEVI v2u trd(lds_char* p) { return __builtin_bit_cast(v2u, __builtin_amdgcn_ds_read_tr16_b64_v4i16((__attribute__((address_space(3))) s16x4*)p)); }
template <int O1, int O2> DEVI void tra(v2u& lo, v2u& hi, unsigned a) { asm volatile("ds_read_b64_tr_b16 %0, %2 offset:%3\n\tds_read_b64_tr_b16 %1, %2 offset:%4" : "=&v"(lo), "=&v"(hi) : "v"(a), "n"(O1), "n"(O2) : "memory"); }
DEVI void trw(v2u& a, v2u& b, v2u& c, v2u& d) { asm volatile("s_waitcnt lgkmcnt(0)" : "+v"(a), "+v"(b), "+v"(c), "+v"(d)::"memory"); }
DEVI rsrc_t mkrs(const void* p, unsigned n) { return __builtin_amdgcn_make_buffer_rsrc((void*)p, 0, (int)n, 0x00020000); }
#define CB asm volatile("" ::: "memory")
#define SB __builtin_amdgcn_sched_barrier(0)
#define WL0 asm volatile("s_waitcnt lgkmcnt(0)" ::: "memory")
#define WV0 asm volatile("s_waitcnt vmcnt(0)" ::: "memory")
#define WV(n) asm volatile("s_waitcnt vmcnt(%0)" ::"n"(n) : "memory")
#define BAR do { __builtin_amdgcn_s_barrier(); CB; } while (0)
#define RFL(x) __builtin_amdgcn_readfirstlane(x)
DEVI int lane_id() { int x; asm volatile("v_mbcnt_lo_u32_b32 %0, -1, 0\n\tv_mbcnt_hi_u32_b32 %0, -1, %0" : "=v"(x)); return x; }
template <int I> struct IC { static constexpr int v = I; };
template <int B, int E_, class F> DEVI void sfor(F&& f) { if constexpr (B < E_) { f(IC<B>{}); sfor<B + 1, E_>(f); } }
DEVI float red_x32(float x, bool mx) { unsigned u = __float_as_uint(x); auto r = __builtin_amdgcn_permlane32_swap(u, u, false, false); float a = __uint_as_float(r[0]), b = __uint_as_float(r[1]); return mx ? fmaxf(a, b) : a + b; }
DEVI float red_x16(float x, bool mx) { unsigned u = __float_as_uint(x); auto r = __builtin_amdgcn_permlane16_swap(u, u, false, false); float a = __uint_as_float(r[0]), b = __uint_as_float(r[1]); return mx ? fmaxf(a, b) : a + b; }
DEVI unsigned vmax4(v4u d) { return max(max(d[0], d[1]), max(d[2], d[3])); }
DEVI unsigned nosent(unsigned v) { return min(v, SENT - 1u); }
constexpr unsigned SPIN_CAP = 1u << 18;
DEVI bool spin_bail(unsigned& spins, rsrc_t rs) {
    if (++spins > SPIN_CAP) { st4(0u, rs, 0, F_ABORT); return true; }
    return (spins & 255u) == 0u && RFL(ld4(rs, 0, F_ABORT)) != SENT;
}
#define TSTAMP(idx)
constexpr int LDSB = 160 * 1024;
DEVI void idle(unsigned dl) { const unsigned t0 = (unsigned)__builtin_amdgcn_s_memrealtime(); while ((unsigned)__builtin_amdgcn_s_memrealtime() - t0 < dl) __builtin_amdgcn_s_sleep(8); }
#define RXE 0
#define D1F 0
#define X1F 0

DEVI float red_rows(float s) {
    s += __uint_as_float(__builtin_amdgcn_update_dpp(0, __float_as_uint(s), 0x128, 0xf, 0xf, false));
    s = red_x16(s, false); return red_x32(s, false);
}
DEVI f32x2 up2(unsigned u) { return f32x2{bflo(u), bfhi(u)}; }
DEVI float ln_inv(float var) { return __builtin_amdgcn_rcpf(rbf(__builtin_amdgcn_sqrtf(rbf(rbf(fmaxf(var, 0.f)) + 1e-5f)))); }
template <class F> DEVI void ln0(const Args& a, const int w, int b, F&& base) {
    const int lane = lane_id(), row = w * 8 + (lane & 7);
    const rsrc_t rx = mkrs(a.x, 2048 * 2048);
    v4u u[16];
#pragma unroll
    for (int i = 0; i < 16; ++i) u[i] = __builtin_amdgcn_raw_buffer_load_b128(rx, row * 2048 + (lane >> 3) * 16 + i * 128, b * 65536, 0);
    const float k0 = __shfl(bflo(u[0][0]), lane & 7);
    f32x2 s1 = {0.f, 0.f}, s2 = {0.f, 0.f};
#pragma unroll
    for (int i = 0; i < 16; ++i)
#pragma unroll
        for (int e = 0; e < 4; ++e) { const f32x2 d = up2(u[i][e]) - k0; s1 += d; s2 += d * d; }
    const float s = red_rows(s1[0] + s1[1]) * (1.0f / 1024.0f), q = red_rows(s2[0] + s2[1]) * (1.0f / 1024.0f);
    float inv = ln_inv(q - s * s), c = -rbf(k0 + s) * inv;
    asm volatile("" : "+v"(inv), "+v"(c));
    const rsrc_t rs = mkrs(a.ws + base(), WS_RS);
    const unsigned o = sd(O_XN + b * 131072) + (lane >> 3) * 512 + row * 16;
#pragma unroll
    for (int i = 0; i < 16; ++i) {
        v4u r;
#pragma unroll
        for (int e = 0; e < 4; ++e) { const f32x2 z = up2(u[i][e]) * inv + c; r[e] = pack2(z[0], z[1]); }
        st16(r, rs, o + (i >> 1) * 16384 + (i & 1) * 4096);
    }
#pragma unroll
    for (int i = 0; i < 16; ++i) st16(u[i], rs, o + (O_XR - O_XN) + (i >> 1) * 16384 + (i & 1) * 4096);
    st16(u[0], rs, sd(O_ST + b * 131072) + w * 1024 + lane * 16); st16(u[0], rs, sd(O_ST + b * 131072) + 4096 + w * 1024 + lane * 16); st16(u[0], rs, sd(O_ST + SD + b * 131072) + w * 1024 + lane * 16);
    WV0;
    st4(b, rs, 0, F_X0 + b * 128 + w * 4);
}
#define RSLP 8
DEVI void r_role(const Args& a, const int w, int rq) {
    const int lane = lane_id(), row = (w >> 1) * 16 + (rq & 1) * 8 + (lane & 7), cq = (rq >> 1) * 2 + (w & 1);
    const rsrc_t rs = mkrs(a.ws, WS_RS);
    const unsigned fo = cq * 32768 + (lane >> 3) * 512 + row * 16, so = O_ST - O_XR + 4096 + (lane >> 3) * 512 + row * 8;
    auto io = [](int i) -> unsigned { return (i >> 1) * 16384 + (i & 1) * 4096; };
    const unsigned fd = F_D + (lane & 15) * 128 + (D1F ? 0 : (row >> 3) * 4);
    for (int b = 0; b < NSEG; ++b) {
        unsigned spins = 0;
        for (;;) {
            CB;
            if (__ballot((int)ld4(rs, fd, 0) < b) == 0ull) break;
            if (spin_bail(spins, rs)) return;
            __builtin_amdgcn_s_sleep(RSLP);
        }
        const unsigned sx = sd(O_XR + b * 131072);
        const v2u s0 = __builtin_amdgcn_raw_buffer_load_b64(rs, so, sx, AUX_SC1), s1 = __builtin_amdgcn_raw_buffer_load_b64(rs, so + 256, sx, AUX_SC1);
        v4u u[4];
#pragma unroll
        for (int i = 0; i < 4; ++i) u[i] = ld16(rs, fo + io(i), sx);
        const float m0 = __uint_as_float(s0[0]), m1 = __uint_as_float(s1[0]), mean = red_rows(m0 + m1) * 0.0625f;
        const float var = red_rows(__uint_as_float(s0[1]) + __uint_as_float(s1[1]) + 64.f * ((m0 - mean) * (m0 - mean) + (m1 - mean) * (m1 - mean))) * (1.0f / 1024.0f);
        const float inv = ln_inv(var), c = -rbf(mean) * inv;
#pragma unroll
        for (int i = 0; i < 4; ++i) {
            v4u o;
#pragma unroll
            for (int e = 0; e < 4; ++e) { const f32x2 z = up2(u[i][e]) * inv + c; o[e] = pack2(z[0], z[1]); }
            st16(o, rs, fo + io(i) + ((sx ^ SD) - (O_XR - O_XN)));
        }
        CB;
#pragma unroll
        for (int i = 0; i < 4; ++i) st16(u[i], rs, fo + io(i) + (sx ^ SD));
        WV0;
        if (X1F) BAR;
        st4(b, rs, 0, X1F && w ? OOB : F_X + SD + rq * 128 + w * 4);
        if (rq == 0 && w == 0) TSTAMP(200000 + 64 + b);
    }
}
DEVI void r_role2(const Args& a, const int w, int rq) {
    const int lane = lane_id(), row = w * 16 + (rq & 1) * 8 + (lane & 7), cq = (rq >> 1) * 2;
    const rsrc_t rs = mkrs(a.ws, WS_RS);
    const unsigned fo = cq * 32768 + (lane >> 3) * 512 + row * 16, so = O_ST - O_XR + 4096 + (lane >> 3) * 512 + row * 8;
    auto io = [](int i) -> unsigned { return (i >> 1) * 16384 + (i & 1) * 4096; };
    const unsigned fd = F_D + (lane & 15) * 128 + (D1F ? 0 : (row >> 3) * 4);
    for (int b = 0; b < NSEG; ++b) {
        unsigned spins = 0;
        for (;;) {
            CB;
            if (__ballot((int)ld4(rs, fd, 0) < b) == 0ull) break;
            if (spin_bail(spins, rs)) return;
            __builtin_amdgcn_s_sleep(RSLP);
        }
        const unsigned sx = sd(O_XR + b * 131072);
        const v2u s0 = __builtin_amdgcn_raw_buffer_load_b64(rs, so, sx, AUX_SC1), s1 = __builtin_amdgcn_raw_buffer_load_b64(rs, so + 256, sx, AUX_SC1);
        v4u u[8];
#pragma unroll
        for (int i = 0; i < 8; ++i) u[i] = ld16(rs, fo + io(i), sx);
        const float m0 = __uint_as_float(s0[0]), m1 = __uint_as_float(s1[0]), mean = red_rows(m0 + m1) * 0.0625f;
        const float var = red_rows(__uint_as_float(s0[1]) + __uint_as_float(s1[1]) + 64.f * ((m0 - mean) * (m0 - mean) + (m1 - mean) * (m1 - mean))) * (1.0f / 1024.0f);
        const float inv = ln_inv(var), c = -rbf(mean) * inv;
#pragma unroll
        for (int i = 0; i < 8; ++i) {
            v4u o;
#pragma unroll
            for (int e = 0; e < 4; ++e) { const f32x2 z = up2(u[i][e]) * inv + c; o[e] = pack2(z[0], z[1]); }
            st16(o, rs, fo + io(i) + ((sx ^ SD) - (O_XR - O_XN)));
        }
        CB;
#pragma unroll
        for (int i = 0; i < 8; ++i) st16(u[i], rs, fo + io(i) + (sx ^ SD));
        WV0;
        if (X1F) BAR;
        st8(v2u{(unsigned)b, (unsigned)b}, rs, 0, F_X + SD + rq * 128 + w * 8);
        if (rq == 0 && w == 0) TSTAMP(200000 + 64 + b);
    }
}
#define QK_ASM \
"v_mbcnt_lo_u32_b32 v255, -1, 0\nv_mbcnt_hi_u32_b32 v255, -1, v255\nv_lshlrev_b32 v255, 2, v255\nv_add_u32 v255, %[tb], v255\nds_read_b32 v0, v255 offset:0\nds_read_b32 v1, v255 offset:256\nds_read_b32 v2, v255 offset:512\nds_read_b32 v3, v255 offset:768\n" \
"ds_read_b32 v4, v255 offset:1024\nds_read_b32 v5, v255 offset:1280\nds_read_b32 v6, v255 offset:1536\nds_read_b32 v7, v255 offset:1792\nds_read_b32 v8, v255 offset:2048\nds_read_b32 v9, v255 offset:2304\nds_read_b32 v10, v255 offset:2560\nds_read_b32 v11, v255 offset:2816\n" \
"ds_read_b32 v12, v255 offset:3072\nds_read_b32 v13, v255 offset:3328\nds_read_b32 v14, v255 offset:3584\nds_read_b32 v15, v255 offset:3840\nds_read_b32 v92, v255 offset:4096\nds_read_b32 v93, v255 offset:4352\nds_read_b32 v94, v255 offset:4608\ns_waitcnt lgkmcnt(0)\n" \
"s_mov_b64 s[36:37], %[ws]\ns_mov_b32 s38, %[sz]\ns_mov_b32 s39, 0x20000\ns_mov_b32 s45, 2096896\nbuffer_load_dword v95, off, s[36:39], s45 sc1\ns_add_u32 m0, %[lw], 16384\ns_add_u32 s44, %[wo], 0\nbuffer_load_dwordx4 v92, %[rw], s44 offen nt lds\n" \
"s_add_u32 m0, %[lw], 17408\ns_add_u32 s44, %[wo], 65536\nbuffer_load_dwordx4 v92, %[rw], s44 offen nt lds\ns_add_u32 m0, %[lw], 18432\ns_add_u32 s44, %[wo], 131072\nbuffer_load_dwordx4 v92, %[rw], s44 offen nt lds\ns_add_u32 m0, %[lw], 19456\ns_add_u32 s44, %[wo], 196608\n" \
"buffer_load_dwordx4 v92, %[rw], s44 offen nt lds\ns_add_u32 m0, %[lw], 20480\ns_add_u32 s44, %[wo], 262144\nbuffer_load_dwordx4 v92, %[rw], s44 offen nt lds\ns_add_u32 m0, %[lw], 21504\ns_add_u32 s44, %[wo], 327680\nbuffer_load_dwordx4 v92, %[rw], s44 offen nt lds\ns_add_u32 m0, %[lw], 22528\n" \
"s_add_u32 s44, %[wo], 393216\nbuffer_load_dwordx4 v92, %[rw], s44 offen nt lds\ns_add_u32 m0, %[lw], 23552\ns_add_u32 s44, %[wo], 458752\nbuffer_load_dwordx4 v92, %[rw], s44 offen nt lds\ns_add_u32 m0, %[tb], 0\ns_add_u32 s44, %[wo], 524288\nbuffer_load_dwordx4 v92, %[rw], s44 offen nt lds\n" \
"s_add_u32 m0, %[tb], 1024\ns_add_u32 s44, %[wo], 589824\nbuffer_load_dwordx4 v92, %[rw], s44 offen nt lds\ns_add_u32 m0, %[tb], 2048\ns_add_u32 s44, %[wo], 655360\nbuffer_load_dwordx4 v92, %[rw], s44 offen nt lds\ns_add_u32 m0, %[tb], 3072\ns_add_u32 s44, %[wo], 720896\n" \
"buffer_load_dwordx4 v92, %[rw], s44 offen nt lds\ns_add_u32 m0, %[tb], 4096\ns_add_u32 s44, %[wo], 786432\nbuffer_load_dwordx4 v92, %[rw], s44 offen nt lds\ns_add_u32 m0, %[tb], 5120\ns_add_u32 s44, %[wo], 851968\nbuffer_load_dwordx4 v92, %[rw], s44 offen nt lds\ns_add_u32 m0, %[tb], 6144\n" \
"s_add_u32 s44, %[wo], 917504\nbuffer_load_dwordx4 v92, %[rw], s44 offen nt lds\ns_add_u32 m0, %[tb], 7168\ns_add_u32 s44, %[wo], 983040\nbuffer_load_dwordx4 v92, %[rw], s44 offen nt lds\ns_add_u32 m0, %[tb], 8192\ns_add_u32 s44, %[wo], 1048576\nbuffer_load_dwordx4 v92, %[rw], s44 offen nt lds\n" \
"s_add_u32 m0, %[tb], 9216\ns_add_u32 s44, %[wo], 1114112\nbuffer_load_dwordx4 v92, %[rw], s44 offen nt lds\ns_add_u32 m0, %[tb], 10240\ns_add_u32 s44, %[wo], 1179648\nbuffer_load_dwordx4 v92, %[rw], s44 offen nt lds\ns_add_u32 m0, %[tb], 11264\ns_add_u32 s44, %[wo], 1245184\n" \
"buffer_load_dwordx4 v92, %[rw], s44 offen nt lds\ns_add_u32 m0, %[lw], 0\ns_add_u32 s44, %[wo], 1310720\nbuffer_load_dwordx4 v92, %[rw], s44 offen nt lds\ns_add_u32 m0, %[lw], 1024\ns_add_u32 s44, %[wo], 1376256\nbuffer_load_dwordx4 v92, %[rw], s44 offen nt lds\ns_add_u32 m0, %[lw], 2048\n" \
"s_add_u32 s44, %[wo], 1441792\nbuffer_load_dwordx4 v92, %[rw], s44 offen nt lds\ns_add_u32 m0, %[lw], 3072\ns_add_u32 s44, %[wo], 1507328\nbuffer_load_dwordx4 v92, %[rw], s44 offen nt lds\ns_add_u32 m0, %[lw], 4096\ns_add_u32 s44, %[wo], 1572864\nbuffer_load_dwordx4 v92, %[rw], s44 offen nt lds\n" \
"s_add_u32 m0, %[lw], 5120\ns_add_u32 s44, %[wo], 1638400\nbuffer_load_dwordx4 v92, %[rw], s44 offen nt lds\ns_add_u32 m0, %[lw], 6144\ns_add_u32 s44, %[wo], 1703936\nbuffer_load_dwordx4 v92, %[rw], s44 offen nt lds\ns_add_u32 m0, %[lw], 7168\ns_add_u32 s44, %[wo], 1769472\n" \
"buffer_load_dwordx4 v92, %[rw], s44 offen nt lds\ns_add_u32 m0, %[lw], 8192\ns_add_u32 s44, %[wo], 1835008\nbuffer_load_dwordx4 v92, %[rw], s44 offen nt lds\ns_add_u32 m0, %[lw], 9216\ns_add_u32 s44, %[wo], 1900544\nbuffer_load_dwordx4 v92, %[rw], s44 offen nt lds\ns_add_u32 m0, %[lw], 10240\n" \
"s_add_u32 s44, %[wo], 1966080\nbuffer_load_dwordx4 v92, %[rw], s44 offen nt lds\ns_add_u32 m0, %[lw], 11264\ns_add_u32 s44, %[wo], 2031616\nbuffer_load_dwordx4 v92, %[rw], s44 offen nt lds\ns_add_u32 m0, %[lw], 12288\ns_add_u32 s44, %[wo], 2097152\nbuffer_load_dwordx4 v92, %[rw], s44 offen nt lds\n" \
"s_add_u32 m0, %[lw], 13312\ns_add_u32 s44, %[wo], 2162688\nbuffer_load_dwordx4 v92, %[rw], s44 offen nt lds\ns_add_u32 m0, %[lw], 14336\ns_add_u32 s44, %[wo], 2228224\nbuffer_load_dwordx4 v92, %[rw], s44 offen nt lds\ns_add_u32 m0, %[lw], 15360\ns_add_u32 s44, %[wo], 2293760\n" \
"buffer_load_dwordx4 v92, %[rw], s44 offen nt lds\ns_waitcnt vmcnt(32)\nds_read_b64_tr_b16 a[0:1], v94 offset:16384\nds_read_b64_tr_b16 a[2:3], v94 offset:16896\nds_read_b64_tr_b16 a[8:9], v94 offset:16392\nds_read_b64_tr_b16 a[10:11], v94 offset:16904\nds_read_b64_tr_b16 a[16:17], v94 offset:16448\nds_read_b64_tr_b16 a[18:19], v94 offset:16960\n" \
"ds_read_b64_tr_b16 a[24:25], v94 offset:16456\nds_read_b64_tr_b16 a[26:27], v94 offset:16968\ns_waitcnt lgkmcnt(0)\ns_add_u32 m0, %[lw], 16384\ns_add_u32 s44, %[wo], 2359296\nbuffer_load_dwordx4 v92, %[rw], s44 offen nt lds\ns_add_u32 m0, %[lw], 17408\ns_add_u32 s44, %[wo], 2424832\n" \
"buffer_load_dwordx4 v92, %[rw], s44 offen nt lds\ns_add_u32 m0, %[lw], 18432\ns_add_u32 s44, %[wo], 2490368\nbuffer_load_dwordx4 v92, %[rw], s44 offen nt lds\ns_add_u32 m0, %[lw], 19456\ns_add_u32 s44, %[wo], 2555904\nbuffer_load_dwordx4 v92, %[rw], s44 offen nt lds\ns_waitcnt vmcnt(32)\n" \
"ds_read_b64_tr_b16 a[4:5], v94 offset:20480\nds_read_b64_tr_b16 a[6:7], v94 offset:20992\nds_read_b64_tr_b16 a[12:13], v94 offset:20488\nds_read_b64_tr_b16 a[14:15], v94 offset:21000\nds_read_b64_tr_b16 a[20:21], v94 offset:20544\nds_read_b64_tr_b16 a[22:23], v94 offset:21056\nds_read_b64_tr_b16 a[28:29], v94 offset:20552\nds_read_b64_tr_b16 a[30:31], v94 offset:21064\n" \
"s_waitcnt lgkmcnt(0)\ns_add_u32 m0, %[lw], 20480\ns_add_u32 s44, %[wo], 2621440\nbuffer_load_dwordx4 v92, %[rw], s44 offen nt lds\ns_add_u32 m0, %[lw], 21504\ns_add_u32 s44, %[wo], 2686976\nbuffer_load_dwordx4 v92, %[rw], s44 offen nt lds\ns_add_u32 m0, %[lw], 22528\n" \
"s_add_u32 s44, %[wo], 2752512\nbuffer_load_dwordx4 v92, %[rw], s44 offen nt lds\ns_add_u32 m0, %[lw], 23552\ns_add_u32 s44, %[wo], 2818048\nbuffer_load_dwordx4 v92, %[rw], s44 offen nt lds\ns_waitcnt vmcnt(32)\nds_read_b64_tr_b16 a[32:33], v93 offset:0\nds_read_b64_tr_b16 a[34:35], v93 offset:512\n" \
"ds_read_b64_tr_b16 v[84:85], v93 offset:8\nds_read_b64_tr_b16 v[86:87], v93 offset:520\nds_read_b64_tr_b16 a[40:41], v93 offset:64\nds_read_b64_tr_b16 a[42:43], v93 offset:576\nds_read_b64_tr_b16 a[48:49], v93 offset:72\nds_read_b64_tr_b16 a[50:51], v93 offset:584\ns_waitcnt lgkmcnt(0)\ns_add_u32 m0, %[tb], 0\n" \
"s_add_u32 s44, %[wo], 2883584\nbuffer_load_dwordx4 v92, %[rw], s44 offen nt lds\ns_add_u32 m0, %[tb], 1024\ns_add_u32 s44, %[wo], 2949120\nbuffer_load_dwordx4 v92, %[rw], s44 offen nt lds\ns_add_u32 m0, %[tb], 2048\ns_add_u32 s44, %[wo], 3014656\nbuffer_load_dwordx4 v92, %[rw], s44 offen nt lds\n" \
"s_add_u32 m0, %[tb], 3072\ns_add_u32 s44, %[wo], 3080192\nbuffer_load_dwordx4 v92, %[rw], s44 offen nt lds\ns_waitcnt vmcnt(32)\nds_read_b64_tr_b16 a[36:37], v93 offset:4096\nds_read_b64_tr_b16 a[38:39], v93 offset:4608\nds_read_b64_tr_b16 v[80:81], v93 offset:4104\nds_read_b64_tr_b16 v[82:83], v93 offset:4616\n" \
"ds_read_b64_tr_b16 a[44:45], v93 offset:4160\nds_read_b64_tr_b16 a[46:47], v93 offset:4672\nds_read_b64_tr_b16 a[52:53], v93 offset:4168\nds_read_b64_tr_b16 a[54:55], v93 offset:4680\ns_waitcnt lgkmcnt(0)\ns_add_u32 m0, %[tb], 4096\ns_add_u32 s44, %[wo], 3145728\nbuffer_load_dwordx4 v92, %[rw], s44 offen nt lds\n" \
"s_add_u32 m0, %[tb], 5120\ns_add_u32 s44, %[wo], 3211264\nbuffer_load_dwordx4 v92, %[rw], s44 offen nt lds\ns_add_u32 m0, %[tb], 6144\ns_add_u32 s44, %[wo], 3276800\nbuffer_load_dwordx4 v92, %[rw], s44 offen nt lds\ns_add_u32 m0, %[tb], 7168\ns_add_u32 s44, %[wo], 3342336\n" \
"buffer_load_dwordx4 v92, %[rw], s44 offen nt lds\ns_waitcnt vmcnt(32)\nds_read_b64_tr_b16 a[56:57], v93 offset:8192\nds_read_b64_tr_b16 a[58:59], v93 offset:8704\nds_read_b64_tr_b16 a[64:65], v93 offset:8200\nds_read_b64_tr_b16 a[66:67], v93 offset:8712\nds_read_b64_tr_b16 v[76:77], v93 offset:8256\nds_read_b64_tr_b16 v[78:79], v93 offset:8768\n" \
"ds_read_b64_tr_b16 a[72:73], v93 offset:8264\nds_read_b64_tr_b16 a[74:75], v93 offset:8776\ns_waitcnt lgkmcnt(0)\ns_add_u32 m0, %[tb], 8192\ns_add_u32 s44, %[wo], 3407872\nbuffer_load_dwordx4 v92, %[rw], s44 offen nt lds\ns_add_u32 m0, %[tb], 9216\ns_add_u32 s44, %[wo], 3473408\n" \
"buffer_load_dwordx4 v92, %[rw], s44 offen nt lds\ns_add_u32 m0, %[tb], 10240\ns_add_u32 s44, %[wo], 3538944\nbuffer_load_dwordx4 v92, %[rw], s44 offen nt lds\ns_add_u32 m0, %[tb], 11264\ns_add_u32 s44, %[wo], 3604480\nbuffer_load_dwordx4 v92, %[rw], s44 offen nt lds\ns_waitcnt vmcnt(32)\n" \
"ds_read_b64_tr_b16 a[60:61], v94 offset:0\nds_read_b64_tr_b16 a[62:63], v94 offset:512\nds_read_b64_tr_b16 a[68:69], v94 offset:8\nds_read_b64_tr_b16 a[70:71], v94 offset:520\nds_read_b64_tr_b16 v[72:73], v94 offset:64\nds_read_b64_tr_b16 v[74:75], v94 offset:576\nds_read_b64_tr_b16 a[76:77], v94 offset:72\nds_read_b64_tr_b16 a[78:79], v94 offset:584\n" \
"s_waitcnt lgkmcnt(0)\ns_add_u32 m0, %[lw], 0\ns_add_u32 s44, %[wo], 3670016\nbuffer_load_dwordx4 v92, %[rw], s44 offen nt lds\ns_add_u32 m0, %[lw], 1024\ns_add_u32 s44, %[wo], 3735552\nbuffer_load_dwordx4 v92, %[rw], s44 offen nt lds\ns_add_u32 m0, %[lw], 2048\n" \
"s_add_u32 s44, %[wo], 3801088\nbuffer_load_dwordx4 v92, %[rw], s44 offen nt lds\ns_add_u32 m0, %[lw], 3072\ns_add_u32 s44, %[wo], 3866624\nbuffer_load_dwordx4 v92, %[rw], s44 offen nt lds\ns_waitcnt vmcnt(32)\nds_read_b64_tr_b16 a[80:81], v94 offset:4096\nds_read_b64_tr_b16 a[82:83], v94 offset:4608\n" \
"ds_read_b64_tr_b16 a[88:89], v94 offset:4104\nds_read_b64_tr_b16 a[90:91], v94 offset:4616\nds_read_b64_tr_b16 a[96:97], v94 offset:4160\nds_read_b64_tr_b16 a[98:99], v94 offset:4672\nds_read_b64_tr_b16 v[68:69], v94 offset:4168\nds_read_b64_tr_b16 v[70:71], v94 offset:4680\ns_waitcnt lgkmcnt(0)\ns_add_u32 m0, %[lw], 4096\n" \
"s_add_u32 s44, %[wo], 3932160\nbuffer_load_dwordx4 v92, %[rw], s44 offen nt lds\ns_add_u32 m0, %[lw], 5120\ns_add_u32 s44, %[wo], 3997696\nbuffer_load_dwordx4 v92, %[rw], s44 offen nt lds\ns_add_u32 m0, %[lw], 6144\ns_add_u32 s44, %[wo], 4063232\nbuffer_load_dwordx4 v92, %[rw], s44 offen nt lds\n" \
"s_add_u32 m0, %[lw], 7168\ns_add_u32 s44, %[wo], 4128768\nbuffer_load_dwordx4 v92, %[rw], s44 offen nt lds\ns_waitcnt vmcnt(32)\nds_read_b64_tr_b16 a[84:85], v94 offset:8192\nds_read_b64_tr_b16 a[86:87], v94 offset:8704\nds_read_b64_tr_b16 a[92:93], v94 offset:8200\nds_read_b64_tr_b16 a[94:95], v94 offset:8712\n" \
"ds_read_b64_tr_b16 a[100:101], v94 offset:8256\nds_read_b64_tr_b16 a[102:103], v94 offset:8768\nds_read_b64_tr_b16 v[64:65], v94 offset:8264\nds_read_b64_tr_b16 v[66:67], v94 offset:8776\ns_waitcnt lgkmcnt(0)\ns_add_u32 m0, %[lw], 8192\ns_add_u32 s44, %[wo], 4194304\nbuffer_load_dwordx4 v92, %[rw], s44 offen nt lds\n" \
"s_add_u32 m0, %[lw], 9216\ns_add_u32 s44, %[wo], 4259840\nbuffer_load_dwordx4 v92, %[rw], s44 offen nt lds\ns_add_u32 m0, %[lw], 10240\ns_add_u32 s44, %[wo], 4325376\nbuffer_load_dwordx4 v92, %[rw], s44 offen nt lds\ns_add_u32 m0, %[lw], 11264\ns_add_u32 s44, %[wo], 4390912\n" \
"buffer_load_dwordx4 v92, %[rw], s44 offen nt lds\ns_waitcnt vmcnt(32)\nds_read_b64_tr_b16 a[104:105], v94 offset:12288\nds_read_b64_tr_b16 a[106:107], v94 offset:12800\nds_read_b64_tr_b16 a[112:113], v94 offset:12296\nds_read_b64_tr_b16 a[114:115], v94 offset:12808\nds_read_b64_tr_b16 a[120:121], v94 offset:12352\nds_read_b64_tr_b16 a[122:123], v94 offset:12864\n" \
"ds_read_b64_tr_b16 a[128:129], v94 offset:12360\nds_read_b64_tr_b16 a[130:131], v94 offset:12872\ns_waitcnt lgkmcnt(0)\ns_add_u32 m0, %[lw], 12288\ns_add_u32 s44, %[wo], 4456448\nbuffer_load_dwordx4 v92, %[rw], s44 offen nt lds\ns_add_u32 m0, %[lw], 13312\ns_add_u32 s44, %[wo], 4521984\n" \
"buffer_load_dwordx4 v92, %[rw], s44 offen nt lds\ns_add_u32 m0, %[lw], 14336\ns_add_u32 s44, %[wo], 4587520\nbuffer_load_dwordx4 v92, %[rw], s44 offen nt lds\ns_add_u32 m0, %[lw], 15360\ns_add_u32 s44, %[wo], 4653056\nbuffer_load_dwordx4 v92, %[rw], s44 offen nt lds\ns_waitcnt vmcnt(32)\n" \
"ds_read_b64_tr_b16 a[108:109], v94 offset:16384\nds_read_b64_tr_b16 a[110:111], v94 offset:16896\nds_read_b64_tr_b16 a[116:117], v94 offset:16392\nds_read_b64_tr_b16 a[118:119], v94 offset:16904\nds_read_b64_tr_b16 a[124:125], v94 offset:16448\nds_read_b64_tr_b16 a[126:127], v94 offset:16960\nds_read_b64_tr_b16 a[132:133], v94 offset:16456\nds_read_b64_tr_b16 a[134:135], v94 offset:16968\n" \
"s_waitcnt lgkmcnt(0)\ns_add_u32 m0, %[lw], 16384\ns_add_u32 s44, %[wo], 4718592\nbuffer_load_dwordx4 v92, %[rw], s44 offen nt lds\ns_add_u32 m0, %[lw], 17408\ns_add_u32 s44, %[wo], 4784128\nbuffer_load_dwordx4 v92, %[rw], s44 offen nt lds\ns_add_u32 m0, %[lw], 18432\n" \
"s_add_u32 s44, %[wo], 4849664\nbuffer_load_dwordx4 v92, %[rw], s44 offen nt lds\ns_add_u32 m0, %[lw], 19456\ns_add_u32 s44, %[wo], 4915200\nbuffer_load_dwordx4 v92, %[rw], s44 offen nt lds\ns_waitcnt vmcnt(32)\nds_read_b64_tr_b16 a[136:137], v94 offset:20480\nds_read_b64_tr_b16 a[138:139], v94 offset:20992\n" \
"ds_read_b64_tr_b16 v[60:61], v94 offset:20488\nds_read_b64_tr_b16 v[62:63], v94 offset:21000\nds_read_b64_tr_b16 a[144:145], v94 offset:20544\nds_read_b64_tr_b16 a[146:147], v94 offset:21056\nds_read_b64_tr_b16 a[152:153], v94 offset:20552\nds_read_b64_tr_b16 a[154:155], v94 offset:21064\ns_waitcnt lgkmcnt(0)\ns_add_u32 m0, %[lw], 20480\n" \
"s_add_u32 s44, %[wo], 4980736\nbuffer_load_dwordx4 v92, %[rw], s44 offen nt lds\ns_add_u32 m0, %[lw], 21504\ns_add_u32 s44, %[wo], 5046272\nbuffer_load_dwordx4 v92, %[rw], s44 offen nt lds\ns_add_u32 m0, %[lw], 22528\ns_add_u32 s44, %[wo], 5111808\nbuffer_load_dwordx4 v92, %[rw], s44 offen nt lds\n" \
"s_add_u32 m0, %[lw], 23552\ns_add_u32 s44, %[wo], 5177344\nbuffer_load_dwordx4 v92, %[rw], s44 offen nt lds\ns_waitcnt vmcnt(32)\nds_read_b64_tr_b16 a[140:141], v93 offset:0\nds_read_b64_tr_b16 a[142:143], v93 offset:512\nds_read_b64_tr_b16 v[56:57], v93 offset:8\nds_read_b64_tr_b16 v[58:59], v93 offset:520\n" \
"ds_read_b64_tr_b16 a[148:149], v93 offset:64\nds_read_b64_tr_b16 a[150:151], v93 offset:576\nds_read_b64_tr_b16 a[156:157], v93 offset:72\nds_read_b64_tr_b16 a[158:159], v93 offset:584\ns_waitcnt lgkmcnt(0)\ns_add_u32 m0, %[tb], 0\ns_add_u32 s44, %[wo], 5242880\nbuffer_load_dwordx4 v92, %[rw], s44 offen nt lds\n" \
"s_add_u32 m0, %[tb], 1024\ns_add_u32 s44, %[wo], 5308416\nbuffer_load_dwordx4 v92, %[rw], s44 offen nt lds\ns_add_u32 m0, %[tb], 2048\ns_add_u32 s44, %[wo], 5373952\nbuffer_load_dwordx4 v92, %[rw], s44 offen nt lds\ns_add_u32 m0, %[tb], 3072\ns_add_u32 s44, %[wo], 5439488\n" \
"buffer_load_dwordx4 v92, %[rw], s44 offen nt lds\ns_waitcnt vmcnt(32)\nds_read_b64_tr_b16 a[160:161], v93 offset:4096\nds_read_b64_tr_b16 a[162:163], v93 offset:4608\nds_read_b64_tr_b16 a[168:169], v93 offset:4104\nds_read_b64_tr_b16 a[170:171], v93 offset:4616\nds_read_b64_tr_b16 v[52:53], v93 offset:4160\nds_read_b64_tr_b16 v[54:55], v93 offset:4672\n" \
"ds_read_b64_tr_b16 a[176:177], v93 offset:4168\nds_read_b64_tr_b16 a[178:179], v93 offset:4680\ns_waitcnt lgkmcnt(0)\ns_add_u32 m0, %[tb], 4096\ns_add_u32 s44, %[wo], 5505024\nbuffer_load_dwordx4 v92, %[rw], s44 offen nt lds\ns_add_u32 m0, %[tb], 5120\ns_add_u32 s44, %[wo], 5570560\n" \
"buffer_load_dwordx4 v92, %[rw], s44 offen nt lds\ns_add_u32 m0, %[tb], 6144\ns_add_u32 s44, %[wo], 5636096\nbuffer_load_dwordx4 v92, %[rw], s44 offen nt lds\ns_add_u32 m0, %[tb], 7168\ns_add_u32 s44, %[wo], 5701632\nbuffer_load_dwordx4 v92, %[rw], s44 offen nt lds\ns_waitcnt vmcnt(32)\n" \
"ds_read_b64_tr_b16 a[164:165], v93 offset:8192\nds_read_b64_tr_b16 a[166:167], v93 offset:8704\nds_read_b64_tr_b16 a[172:173], v93 offset:8200\nds_read_b64_tr_b16 a[174:175], v93 offset:8712\nds_read_b64_tr_b16 v[48:49], v93 offset:8256\nds_read_b64_tr_b16 v[50:51], v93 offset:8768\nds_read_b64_tr_b16 a[180:181], v93 offset:8264\nds_read_b64_tr_b16 a[182:183], v93 offset:8776\n" \
"s_waitcnt lgkmcnt(0)\ns_add_u32 m0, %[tb], 8192\ns_add_u32 s44, %[wo], 5767168\nbuffer_load_dwordx4 v92, %[rw], s44 offen nt lds\ns_add_u32 m0, %[tb], 9216\ns_add_u32 s44, %[wo], 5832704\nbuffer_load_dwordx4 v92, %[rw], s44 offen nt lds\ns_add_u32 m0, %[tb], 10240\n" \
"s_add_u32 s44, %[wo], 5898240\nbuffer_load_dwordx4 v92, %[rw], s44 offen nt lds\ns_add_u32 m0, %[tb], 11264\ns_add_u32 s44, %[wo], 5963776\nbuffer_load_dwordx4 v92, %[rw], s44 offen nt lds\ns_waitcnt vmcnt(32)\nds_read_b64_tr_b16 a[184:185], v94 offset:0\nds_read_b64_tr_b16 a[186:187], v94 offset:512\n" \
"ds_read_b64_tr_b16 a[192:193], v94 offset:8\nds_read_b64_tr_b16 a[194:195], v94 offset:520\nds_read_b64_tr_b16 a[200:201], v94 offset:64\nds_read_b64_tr_b16 a[202:203], v94 offset:576\nds_read_b64_tr_b16 v[44:45], v94 offset:72\nds_read_b64_tr_b16 v[46:47], v94 offset:584\ns_waitcnt lgkmcnt(0)\ns_add_u32 m0, %[lw], 0\n" \
"s_add_u32 s44, %[wo], 6029312\nbuffer_load_dwordx4 v92, %[rw], s44 offen nt lds\ns_add_u32 m0, %[lw], 1024\ns_add_u32 s44, %[wo], 6094848\nbuffer_load_dwordx4 v92, %[rw], s44 offen nt lds\ns_add_u32 m0, %[lw], 2048\ns_add_u32 s44, %[wo], 6160384\nbuffer_load_dwordx4 v92, %[rw], s44 offen nt lds\n" \
"s_add_u32 m0, %[lw], 3072\ns_add_u32 s44, %[wo], 6225920\nbuffer_load_dwordx4 v92, %[rw], s44 offen nt lds\ns_waitcnt vmcnt(32)\nds_read_b64_tr_b16 a[188:189], v94 offset:4096\nds_read_b64_tr_b16 a[190:191], v94 offset:4608\nds_read_b64_tr_b16 a[196:197], v94 offset:4104\nds_read_b64_tr_b16 a[198:199], v94 offset:4616\n" \
"ds_read_b64_tr_b16 a[204:205], v94 offset:4160\nds_read_b64_tr_b16 a[206:207], v94 offset:4672\nds_read_b64_tr_b16 v[40:41], v94 offset:4168\nds_read_b64_tr_b16 v[42:43], v94 offset:4680\ns_waitcnt lgkmcnt(0)\ns_add_u32 m0, %[lw], 4096\ns_add_u32 s44, %[wo], 6291456\nbuffer_load_dwordx4 v92, %[rw], s44 offen nt lds\n" \
"s_add_u32 m0, %[lw], 5120\ns_add_u32 s44, %[wo], 6356992\nbuffer_load_dwordx4 v92, %[rw], s44 offen nt lds\ns_add_u32 m0, %[lw], 6144\ns_add_u32 s44, %[wo], 6422528\nbuffer_load_dwordx4 v92, %[rw], s44 offen nt lds\ns_add_u32 m0, %[lw], 7168\ns_add_u32 s44, %[wo], 6488064\n" \
"buffer_load_dwordx4 v92, %[rw], s44 offen nt lds\ns_waitcnt vmcnt(32)\nds_read_b64_tr_b16 a[208:209], v94 offset:8192\nds_read_b64_tr_b16 a[210:211], v94 offset:8704\nds_read_b64_tr_b16 a[216:217], v94 offset:8200\nds_read_b64_tr_b16 a[218:219], v94 offset:8712\nds_read_b64_tr_b16 a[224:225], v94 offset:8256\nds_read_b64_tr_b16 a[226:227], v94 offset:8768\n" \
"ds_read_b64_tr_b16 a[232:233], v94 offset:8264\nds_read_b64_tr_b16 a[234:235], v94 offset:8776\ns_waitcnt lgkmcnt(0)\ns_add_u32 m0, %[lw], 8192\ns_add_u32 s44, %[wo], 6553600\nbuffer_load_dwordx4 v92, %[rw], s44 offen nt lds\ns_add_u32 m0, %[lw], 9216\ns_add_u32 s44, %[wo], 6619136\n" \
"buffer_load_dwordx4 v92, %[rw], s44 offen nt lds\ns_add_u32 m0, %[lw], 10240\ns_add_u32 s44, %[wo], 6684672\nbuffer_load_dwordx4 v92, %[rw], s44 offen nt lds\ns_add_u32 m0, %[lw], 11264\ns_add_u32 s44, %[wo], 6750208\nbuffer_load_dwordx4 v92, %[rw], s44 offen nt lds\ns_waitcnt vmcnt(32)\n" \
"ds_read_b64_tr_b16 a[212:213], v94 offset:12288\nds_read_b64_tr_b16 a[214:215], v94 offset:12800\nds_read_b64_tr_b16 a[220:221], v94 offset:12296\nds_read_b64_tr_b16 a[222:223], v94 offset:12808\nds_read_b64_tr_b16 a[228:229], v94 offset:12352\nds_read_b64_tr_b16 a[230:231], v94 offset:12864\nds_read_b64_tr_b16 a[236:237], v94 offset:12360\nds_read_b64_tr_b16 a[238:239], v94 offset:12872\n" \
"s_waitcnt lgkmcnt(0)\ns_add_u32 m0, %[lw], 12288\ns_add_u32 s44, %[wo], 6815744\nbuffer_load_dwordx4 v92, %[rw], s44 offen nt lds\ns_add_u32 m0, %[lw], 13312\ns_add_u32 s44, %[wo], 6881280\nbuffer_load_dwordx4 v92, %[rw], s44 offen nt lds\ns_add_u32 m0, %[lw], 14336\n" \
"s_add_u32 s44, %[wo], 6946816\nbuffer_load_dwordx4 v92, %[rw], s44 offen nt lds\ns_add_u32 m0, %[lw], 15360\ns_add_u32 s44, %[wo], 7012352\nbuffer_load_dwordx4 v92, %[rw], s44 offen nt lds\ns_waitcnt vmcnt(32)\nds_read_b64_tr_b16 a[240:241], v94 offset:16384\nds_read_b64_tr_b16 a[242:243], v94 offset:16896\n" \
"ds_read_b64_tr_b16 v[36:37], v94 offset:16392\nds_read_b64_tr_b16 v[38:39], v94 offset:16904\nds_read_b64_tr_b16 a[248:249], v94 offset:16448\nds_read_b64_tr_b16 a[250:251], v94 offset:16960\nds_read_b64_tr_b16 v[96:97], v94 offset:16456\nds_read_b64_tr_b16 v[98:99], v94 offset:16968\ns_waitcnt lgkmcnt(0)\ns_add_u32 m0, %[lw], 16384\n" \
"s_add_u32 s44, %[wo], 7077888\nbuffer_load_dwordx4 v92, %[rw], s44 offen nt lds\ns_add_u32 m0, %[lw], 17408\ns_add_u32 s44, %[wo], 7143424\nbuffer_load_dwordx4 v92, %[rw], s44 offen nt lds\ns_add_u32 m0, %[lw], 18432\ns_add_u32 s44, %[wo], 7208960\nbuffer_load_dwordx4 v92, %[rw], s44 offen nt lds\n" \
"s_add_u32 m0, %[lw], 19456\ns_add_u32 s44, %[wo], 7274496\nbuffer_load_dwordx4 v92, %[rw], s44 offen nt lds\ns_waitcnt vmcnt(32)\nds_read_b64_tr_b16 a[244:245], v94 offset:20480\nds_read_b64_tr_b16 a[246:247], v94 offset:20992\nds_read_b64_tr_b16 v[32:33], v94 offset:20488\nds_read_b64_tr_b16 v[34:35], v94 offset:21000\n" \
"ds_read_b64_tr_b16 a[252:253], v94 offset:20544\nds_read_b64_tr_b16 a[254:255], v94 offset:21056\nds_read_b64_tr_b16 v[100:101], v94 offset:20552\nds_read_b64_tr_b16 v[102:103], v94 offset:21064\ns_waitcnt lgkmcnt(0)\ns_add_u32 m0, %[lw], 20480\ns_add_u32 s44, %[wo], 7340032\nbuffer_load_dwordx4 v92, %[rw], s44 offen nt lds\n" \
"s_add_u32 m0, %[lw], 21504\ns_add_u32 s44, %[wo], 7405568\nbuffer_load_dwordx4 v92, %[rw], s44 offen nt lds\ns_add_u32 m0, %[lw], 22528\ns_add_u32 s44, %[wo], 7471104\nbuffer_load_dwordx4 v92, %[rw], s44 offen nt lds\ns_add_u32 m0, %[lw], 23552\ns_add_u32 s44, %[wo], 7536640\n" \
"buffer_load_dwordx4 v92, %[rw], s44 offen nt lds\ns_waitcnt vmcnt(32)\nds_read_b64_tr_b16 v[104:105], v93 offset:0\nds_read_b64_tr_b16 v[106:107], v93 offset:512\nds_read_b64_tr_b16 v[112:113], v93 offset:8\nds_read_b64_tr_b16 v[114:115], v93 offset:520\nds_read_b64_tr_b16 v[28:29], v93 offset:64\nds_read_b64_tr_b16 v[30:31], v93 offset:576\n" \
"ds_read_b64_tr_b16 v[120:121], v93 offset:72\nds_read_b64_tr_b16 v[122:123], v93 offset:584\ns_waitcnt lgkmcnt(0)\ns_add_u32 m0, %[tb], 0\ns_add_u32 s44, %[wo], 7602176\nbuffer_load_dwordx4 v92, %[rw], s44 offen nt lds\ns_add_u32 m0, %[tb], 1024\ns_add_u32 s44, %[wo], 7667712\n" \
"buffer_load_dwordx4 v92, %[rw], s44 offen nt lds\ns_add_u32 m0, %[tb], 2048\ns_add_u32 s44, %[wo], 7733248\nbuffer_load_dwordx4 v92, %[rw], s44 offen nt lds\ns_add_u32 m0, %[tb], 3072\ns_add_u32 s44, %[wo], 7798784\nbuffer_load_dwordx4 v92, %[rw], s44 offen nt lds\ns_waitcnt vmcnt(32)\n" \
"ds_read_b64_tr_b16 v[108:109], v93 offset:4096\nds_read_b64_tr_b16 v[110:111], v93 offset:4608\nds_read_b64_tr_b16 v[116:117], v93 offset:4104\nds_read_b64_tr_b16 v[118:119], v93 offset:4616\nds_read_b64_tr_b16 v[24:25], v93 offset:4160\nds_read_b64_tr_b16 v[26:27], v93 offset:4672\nds_read_b64_tr_b16 v[124:125], v93 offset:4168\nds_read_b64_tr_b16 v[126:127], v93 offset:4680\n" \
"s_waitcnt lgkmcnt(0)\nbuffer_load_dwordx2 v[88:89], off, s[36:39], s45 sc1\ns_add_u32 m0, %[tb], 4096\ns_add_u32 s44, %[wo], 7864320\nbuffer_load_dwordx4 v92, %[rw], s44 offen nt lds\ns_add_u32 m0, %[tb], 5120\ns_add_u32 s44, %[wo], 7929856\nbuffer_load_dwordx4 v92, %[rw], s44 offen nt lds\n" \
"s_add_u32 m0, %[tb], 6144\ns_add_u32 s44, %[wo], 7995392\nbuffer_load_dwordx4 v92, %[rw], s44 offen nt lds\ns_add_u32 m0, %[tb], 7168\ns_add_u32 s44, %[wo], 8060928\nbuffer_load_dwordx4 v92, %[rw], s44 offen nt lds\ns_waitcnt vmcnt(33)\nds_read_b64_tr_b16 v[128:129], v93 offset:8192\n" \
"ds_read_b64_tr_b16 v[130:131], v93 offset:8704\nds_read_b64_tr_b16 v[136:137], v93 offset:8200\nds_read_b64_tr_b16 v[138:139], v93 offset:8712\nds_read_b64_tr_b16 v[144:145], v93 offset:8256\nds_read_b64_tr_b16 v[146:147], v93 offset:8768\nds_read_b64_tr_b16 v[20:21], v93 offset:8264\nds_read_b64_tr_b16 v[22:23], v93 offset:8776\ns_waitcnt lgkmcnt(0)\n" \
"s_add_u32 m0, %[tb], 8192\ns_add_u32 s44, %[wo], 8126464\nbuffer_load_dwordx4 v92, %[rw], s44 offen nt lds\ns_add_u32 m0, %[tb], 9216\ns_add_u32 s44, %[wo], 8192000\nbuffer_load_dwordx4 v92, %[rw], s44 offen nt lds\ns_add_u32 m0, %[tb], 10240\ns_add_u32 s44, %[wo], 8257536\n" \
"buffer_load_dwordx4 v92, %[rw], s44 offen nt lds\ns_add_u32 m0, %[tb], 11264\ns_add_u32 s44, %[wo], 8323072\nbuffer_load_dwordx4 v92, %[rw], s44 offen nt lds\ns_waitcnt vmcnt(33)\nds_read_b64_tr_b16 v[132:133], v94 offset:0\nds_read_b64_tr_b16 v[134:135], v94 offset:512\nds_read_b64_tr_b16 v[140:141], v94 offset:8\n" \
"ds_read_b64_tr_b16 v[142:143], v94 offset:520\nds_read_b64_tr_b16 v[148:149], v94 offset:64\nds_read_b64_tr_b16 v[150:151], v94 offset:576\nds_read_b64_tr_b16 v[16:17], v94 offset:72\nds_read_b64_tr_b16 v[18:19], v94 offset:584\ns_waitcnt lgkmcnt(0)\nds_write_b128 v1, v[84:87] offset:0\nds_write_b128 v1, v[80:83] offset:1024\n" \
"ds_write_b128 v1, v[76:79] offset:2048\nds_write_b128 v1, v[72:75] offset:3072\ns_waitcnt vmcnt(29)\nds_read_b64_tr_b16 v[152:153], v94 offset:4096\nds_read_b64_tr_b16 v[154:155], v94 offset:4608\nds_read_b64_tr_b16 v[160:161], v94 offset:4104\nds_read_b64_tr_b16 v[162:163], v94 offset:4616\nds_read_b64_tr_b16 v[168:169], v94 offset:4160\n" \
"ds_read_b64_tr_b16 v[170:171], v94 offset:4672\nds_read_b64_tr_b16 v[176:177], v94 offset:4168\nds_read_b64_tr_b16 v[178:179], v94 offset:4680\ns_waitcnt lgkmcnt(0)\nds_write_b128 v1, v[68:71] offset:4096\nds_write_b128 v1, v[64:67] offset:5120\nds_write_b128 v1, v[60:63] offset:6144\nds_write_b128 v1, v[56:59] offset:7168\n" \
"s_waitcnt vmcnt(25)\nds_read_b64_tr_b16 v[156:157], v94 offset:8192\nds_read_b64_tr_b16 v[158:159], v94 offset:8704\nds_read_b64_tr_b16 v[164:165], v94 offset:8200\nds_read_b64_tr_b16 v[166:167], v94 offset:8712\nds_read_b64_tr_b16 v[172:173], v94 offset:8256\nds_read_b64_tr_b16 v[174:175], v94 offset:8768\nds_read_b64_tr_b16 v[180:181], v94 offset:8264\n" \
"ds_read_b64_tr_b16 v[182:183], v94 offset:8776\ns_waitcnt lgkmcnt(0)\nds_write_b128 v1, v[52:55] offset:8192\nds_write_b128 v1, v[48:51] offset:9216\nds_write_b128 v1, v[44:47] offset:10240\nds_write_b128 v1, v[40:43] offset:11264\ns_waitcnt vmcnt(21)\nds_read_b64_tr_b16 v[184:185], v94 offset:12288\n" \
"ds_read_b64_tr_b16 v[186:187], v94 offset:12800\nds_read_b64_tr_b16 v[40:41], v94 offset:12296\nds_read_b64_tr_b16 v[42:43], v94 offset:12808\nds_read_b64_tr_b16 v[192:193], v94 offset:12352\nds_read_b64_tr_b16 v[194:195], v94 offset:12864\nds_read_b64_tr_b16 v[200:201], v94 offset:12360\nds_read_b64_tr_b16 v[202:203], v94 offset:12872\ns_waitcnt lgkmcnt(0)\n" \
"ds_write_b128 v1, v[36:39] offset:12288\nds_write_b128 v1, v[32:35] offset:13312\nds_write_b128 v1, v[28:31] offset:14336\nds_write_b128 v1, v[24:27] offset:15360\ns_waitcnt vmcnt(17)\nds_read_b64_tr_b16 v[188:189], v94 offset:16384\nds_read_b64_tr_b16 v[190:191], v94 offset:16896\nds_read_b64_tr_b16 v[24:25], v94 offset:16392\n" \
"ds_read_b64_tr_b16 v[26:27], v94 offset:16904\nds_read_b64_tr_b16 v[196:197], v94 offset:16448\nds_read_b64_tr_b16 v[198:199], v94 offset:16960\nds_read_b64_tr_b16 v[204:205], v94 offset:16456\nds_read_b64_tr_b16 v[206:207], v94 offset:16968\ns_waitcnt lgkmcnt(0)\nds_write_b128 v1, v[20:23] offset:16384\nds_write_b128 v1, v[16:19] offset:17408\n" \
"ds_write_b128 v1, v[40:43] offset:18432\nds_write_b128 v1, v[24:27] offset:19456\ns_waitcnt vmcnt(13)\nds_read_b64_tr_b16 v[208:209], v94 offset:20480\nds_read_b64_tr_b16 v[210:211], v94 offset:20992\nds_read_b64_tr_b16 v[216:217], v94 offset:20488\nds_read_b64_tr_b16 v[218:219], v94 offset:21000\nds_read_b64_tr_b16 v[24:25], v94 offset:20544\n" \
"ds_read_b64_tr_b16 v[26:27], v94 offset:21056\nds_read_b64_tr_b16 v[224:225], v94 offset:20552\nds_read_b64_tr_b16 v[226:227], v94 offset:21064\ns_waitcnt lgkmcnt(0)\nds_write_b128 v1, v[24:27] offset:20480\ns_waitcnt vmcnt(9)\nds_read_b64_tr_b16 v[212:213], v93 offset:0\nds_read_b64_tr_b16 v[214:215], v93 offset:512\n" \
"ds_read_b64_tr_b16 v[220:221], v93 offset:8\nds_read_b64_tr_b16 v[222:223], v93 offset:520\nds_read_b64_tr_b16 v[24:25], v93 offset:64\nds_read_b64_tr_b16 v[26:27], v93 offset:576\nds_read_b64_tr_b16 v[228:229], v93 offset:72\nds_read_b64_tr_b16 v[230:231], v93 offset:584\ns_waitcnt lgkmcnt(0)\nds_write_b128 v1, v[24:27] offset:21504\n" \
"s_waitcnt vmcnt(4)\nds_read_b64_tr_b16 v[232:233], v93 offset:4096\nds_read_b64_tr_b16 v[234:235], v93 offset:4608\nds_read_b64_tr_b16 v[240:241], v93 offset:4104\nds_read_b64_tr_b16 v[242:243], v93 offset:4616\nds_read_b64_tr_b16 v[248:249], v93 offset:4160\nds_read_b64_tr_b16 v[250:251], v93 offset:4672\nds_read_b64_tr_b16 v[24:25], v93 offset:4168\n" \
"ds_read_b64_tr_b16 v[26:27], v93 offset:4680\ns_waitcnt lgkmcnt(0)\nds_write_b128 v1, v[24:27] offset:22528\ns_waitcnt vmcnt(0)\nds_read_b64_tr_b16 v[236:237], v93 offset:8192\nds_read_b64_tr_b16 v[238:239], v93 offset:8704\nds_read_b64_tr_b16 v[244:245], v93 offset:8200\nds_read_b64_tr_b16 v[246:247], v93 offset:8712\n" \
"ds_read_b64_tr_b16 v[252:253], v93 offset:8256\nds_read_b64_tr_b16 v[254:255], v93 offset:8768\nds_read_b64_tr_b16 v[24:25], v93 offset:8264\nds_read_b64_tr_b16 v[26:27], v93 offset:8776\ns_waitcnt lgkmcnt(0)\ns_mov_b32 s43, 0\ns_mov_b32 s86, 110\ns_mov_b32 s89, 0\n" \
".Lqk_hb%=:\nv_cmp_ne_u32 vcc, v95, v88\ns_cbranch_vccnz .Lqk_hb1%=\ns_sleep 4\nbuffer_load_dwordx2 v[88:89], off, s[36:39], s45 sc1\ns_waitcnt vmcnt(0)\ns_add_u32 s43, s43, 1\ns_cmp_lt_u32 s43, 262144\n" \
"s_cbranch_scc1 .Lqk_hb%=\ns_branch .Lqk_abort%=\n.Lqk_hb1%=:\nv_readfirstlane_b32 s44, v89\ns_and_b32 s44, s44, 0x200000\ns_add_u32 s36, s36, s44\ns_addc_u32 s37, s37, 0\nds_write_b128 v1, v[24:27] offset:23552\n" \
"s_waitcnt lgkmcnt(0)\ns_barrier\ns_mov_b32 s40, -1\ns_mov_b32 s43, 0\ns_mov_b32 s61, 0x7fff7fff\ns_mov_b32 s58, 0x3e0293ee\ns_mov_b32 s59, 0xbe0293ee\ns_lshl_b32 s57, %[w], 12\n" \
"s_mov_b32 s62, 0xff00ff00\ns_mov_b32 s63, 0xff00ff00\ns_mov_b32 s56, 0\ns_cmp_eq_u32 %[w], 3\ns_cbranch_scc1 .Lqk_fetch0%=\n.Lqk_pwait%=:\ns_barrier\nds_read_b128 v[80:83], v2 offset:0\n" \
"ds_read_b128 v[84:87], v2 offset:2048\n.Lqk_proj%=:\ns_waitcnt lgkmcnt(1)\nv_mfma_f32_16x16x32_bf16 v[16:19], a[0:3], v[80:83], 0\ns_waitcnt lgkmcnt(0)\nds_read_b128 v[88:91], v2 offset:256\nv_mfma_f32_16x16x32_bf16 v[16:19], a[4:7], v[84:87], v[16:19]\nds_read_b128 v[92:95], v2 offset:2304\n" \
"v_mfma_f32_16x16x32_bf16 v[20:23], a[8:11], v[80:83], 0\nv_mfma_f32_16x16x32_bf16 v[20:23], a[12:15], v[84:87], v[20:23]\nv_mfma_f32_16x16x32_bf16 v[24:27], a[16:19], v[80:83], 0\nds_read_b128 v[72:75], v1 offset:0\nv_mfma_f32_16x16x32_bf16 v[24:27], a[20:23], v[84:87], v[24:27]\nds_read_b128 v[76:79], v1 offset:1024\nv_mfma_f32_16x16x32_bf16 v[28:31], a[24:27], v[80:83], 0\nv_mfma_f32_16x16x32_bf16 v[28:31], a[28:31], v[84:87], v[28:31]\n" \
"s_waitcnt lgkmcnt(3)\nv_mfma_f32_16x16x32_bf16 v[32:35], a[0:3], v[88:91], 0\ns_waitcnt lgkmcnt(2)\nds_read_b128 v[80:83], v2 offset:4096\nv_mfma_f32_16x16x32_bf16 v[32:35], a[4:7], v[92:95], v[32:35]\nds_read_b128 v[84:87], v2 offset:6144\nv_mfma_f32_16x16x32_bf16 v[36:39], a[8:11], v[88:91], 0\nv_mfma_f32_16x16x32_bf16 v[36:39], a[12:15], v[92:95], v[36:39]\n" \
"v_mfma_f32_16x16x32_bf16 v[40:43], a[16:19], v[88:91], 0\nv_mfma_f32_16x16x32_bf16 v[40:43], a[20:23], v[92:95], v[40:43]\nv_mfma_f32_16x16x32_bf16 v[44:47], a[24:27], v[88:91], 0\nv_mfma_f32_16x16x32_bf16 v[44:47], a[28:31], v[92:95], v[44:47]\ns_waitcnt lgkmcnt(1)\nv_mfma_f32_16x16x32_bf16 v[16:19], a[32:35], v[80:83], v[16:19]\ns_waitcnt lgkmcnt(0)\nds_read_b128 v[88:91], v2 offset:4352\n" \
"v_mfma_f32_16x16x32_bf16 v[16:19], a[36:39], v[84:87], v[16:19]\nds_read_b128 v[92:95], v2 offset:6400\nv_mfma_f32_16x16x32_bf16 v[20:23], v[72:75], v[80:83], v[20:23]\nv_mfma_f32_16x16x32_bf16 v[20:23], v[76:79], v[84:87], v[20:23]\nv_mfma_f32_16x16x32_bf16 v[24:27], a[40:43], v[80:83], v[24:27]\nds_read_b128 v[64:67], v1 offset:2048\nv_mfma_f32_16x16x32_bf16 v[24:27], a[44:47], v[84:87], v[24:27]\nds_read_b128 v[68:71], v1 offset:3072\n" \
"v_mfma_f32_16x16x32_bf16 v[28:31], a[48:51], v[80:83], v[28:31]\nv_mfma_f32_16x16x32_bf16 v[28:31], a[52:55], v[84:87], v[28:31]\ns_waitcnt lgkmcnt(3)\nv_mfma_f32_16x16x32_bf16 v[32:35], a[32:35], v[88:91], v[32:35]\ns_waitcnt lgkmcnt(2)\nds_read_b128 v[80:83], v2 offset:8192\nv_mfma_f32_16x16x32_bf16 v[32:35], a[36:39], v[92:95], v[32:35]\nds_read_b128 v[84:87], v2 offset:10240\n" \
"v_mfma_f32_16x16x32_bf16 v[36:39], v[72:75], v[88:91], v[36:39]\nv_mfma_f32_16x16x32_bf16 v[36:39], v[76:79], v[92:95], v[36:39]\nv_mfma_f32_16x16x32_bf16 v[40:43], a[40:43], v[88:91], v[40:43]\nv_mfma_f32_16x16x32_bf16 v[40:43], a[44:47], v[92:95], v[40:43]\nv_mfma_f32_16x16x32_bf16 v[44:47], a[48:51], v[88:91], v[44:47]\nv_mfma_f32_16x16x32_bf16 v[44:47], a[52:55], v[92:95], v[44:47]\ns_waitcnt lgkmcnt(1)\nv_mfma_f32_16x16x32_bf16 v[16:19], a[56:59], v[80:83], v[16:19]\n" \
"s_waitcnt lgkmcnt(0)\nds_read_b128 v[88:91], v2 offset:8448\nv_mfma_f32_16x16x32_bf16 v[16:19], a[60:63], v[84:87], v[16:19]\nds_read_b128 v[92:95], v2 offset:10496\nv_mfma_f32_16x16x32_bf16 v[20:23], a[64:67], v[80:83], v[20:23]\nv_mfma_f32_16x16x32_bf16 v[20:23], a[68:71], v[84:87], v[20:23]\nv_mfma_f32_16x16x32_bf16 v[24:27], v[64:67], v[80:83], v[24:27]\nds_read_b128 v[72:75], v1 offset:4096\n" \
"v_mfma_f32_16x16x32_bf16 v[24:27], v[68:71], v[84:87], v[24:27]\nds_read_b128 v[76:79], v1 offset:5120\nv_mfma_f32_16x16x32_bf16 v[28:31], a[72:75], v[80:83], v[28:31]\nv_mfma_f32_16x16x32_bf16 v[28:31], a[76:79], v[84:87], v[28:31]\ns_waitcnt lgkmcnt(3)\nv_mfma_f32_16x16x32_bf16 v[32:35], a[56:59], v[88:91], v[32:35]\ns_waitcnt lgkmcnt(2)\nds_read_b128 v[80:83], v2 offset:12288\n" \
"v_mfma_f32_16x16x32_bf16 v[32:35], a[60:63], v[92:95], v[32:35]\nds_read_b128 v[84:87], v2 offset:14336\nv_mfma_f32_16x16x32_bf16 v[36:39], a[64:67], v[88:91], v[36:39]\nv_mfma_f32_16x16x32_bf16 v[36:39], a[68:71], v[92:95], v[36:39]\nv_mfma_f32_16x16x32_bf16 v[40:43], v[64:67], v[88:91], v[40:43]\nv_mfma_f32_16x16x32_bf16 v[40:43], v[68:71], v[92:95], v[40:43]\nv_mfma_f32_16x16x32_bf16 v[44:47], a[72:75], v[88:91], v[44:47]\nv_mfma_f32_16x16x32_bf16 v[44:47], a[76:79], v[92:95], v[44:47]\n" \
".p2align 6\n.Lqk_pj%=:\ns_waitcnt lgkmcnt(1)\nv_mfma_f32_16x16x32_bf16 v[16:19], a[80:83], v[80:83], v[16:19]\ns_waitcnt lgkmcnt(0)\nds_read_b128 v[88:91], v2 offset:12544\nv_mfma_f32_16x16x32_bf16 v[16:19], a[84:87], v[84:87], v[16:19]\ns_add_u32 s40, s40, 1\n" \
"ds_read_b128 v[92:95], v2 offset:14592\nv_mfma_f32_16x16x32_bf16 v[20:23], a[88:91], v[80:83], v[20:23]\ns_lshl_b32 s44, s40, 17\nv_mfma_f32_16x16x32_bf16 v[20:23], a[92:95], v[84:87], v[20:23]\ns_add_u32 s44, s44, %[xn]\nv_mfma_f32_16x16x32_bf16 v[24:27], a[96:99], v[80:83], v[24:27]\nv_mfma_f32_16x16x32_bf16 v[24:27], a[100:103], v[84:87], v[24:27]\ns_lshr_b32 s45, s44, 8\n" \
"v_mfma_f32_16x16x32_bf16 v[28:31], v[72:75], v[80:83], v[28:31]\ns_and_b32 s45, s45, 0x2000\nv_mfma_f32_16x16x32_bf16 v[28:31], v[76:79], v[84:87], v[28:31]\ns_xor_b32 s41, s44, s45\ns_waitcnt lgkmcnt(1)\nv_mfma_f32_16x16x32_bf16 v[32:35], a[80:83], v[88:91], v[32:35]\ns_barrier\ns_waitcnt lgkmcnt(0)\n" \
"ds_read_b128 v[80:83], v2 offset:16384\nv_mfma_f32_16x16x32_bf16 v[32:35], a[84:87], v[92:95], v[32:35]\ns_add_u32 s41, s41, %[xw]\ns_and_b32 s42, s40, 3\nds_read_b128 v[84:87], v2 offset:18432\nv_mfma_f32_16x16x32_bf16 v[36:39], a[88:91], v[88:91], v[36:39]\ns_add_u32 m0, s57, 32768\ns_add_u32 s60, s41, 65536\n" \
"buffer_load_dwordx4 v0, s[36:39], s60 offen offset:0 sc1 lds\nv_mfma_f32_16x16x32_bf16 v[36:39], a[92:95], v[92:95], v[36:39]\ns_lshl_b32 s42, s42, 15\nv_mfma_f32_16x16x32_bf16 v[40:43], a[96:99], v[88:91], v[40:43]\nv_mfma_f32_16x16x32_bf16 v[40:43], a[100:103], v[92:95], v[40:43]\ns_and_b32 s44, s40, 1\nv_mfma_f32_16x16x32_bf16 v[44:47], v[72:75], v[88:91], v[44:47]\ns_lshl_b32 s55, s44, 17\n" \
"buffer_load_dwordx4 v0, s[36:39], s60 offen offset:1024 sc1 lds\nv_mfma_f32_16x16x32_bf16 v[44:47], v[76:79], v[92:95], v[44:47]\ns_add_u32 s56, s56, %[fs]\ns_waitcnt lgkmcnt(1)\nv_mfma_f32_16x16x32_bf16 v[16:19], a[104:107], v[80:83], v[16:19]\ns_waitcnt lgkmcnt(0)\nds_read_b128 v[88:91], v2 offset:16640\nv_mfma_f32_16x16x32_bf16 v[16:19], a[108:111], v[84:87], v[16:19]\n" \
"ds_read_b128 v[92:95], v2 offset:18688\nv_mfma_f32_16x16x32_bf16 v[20:23], a[112:115], v[80:83], v[20:23]\nbuffer_load_dwordx4 v0, s[36:39], s60 offen offset:2048 sc1 lds\nv_mfma_f32_16x16x32_bf16 v[20:23], a[116:119], v[84:87], v[20:23]\nv_mfma_f32_16x16x32_bf16 v[24:27], a[120:123], v[80:83], v[24:27]\nds_read_b128 v[72:75], v1 offset:6144\nv_mfma_f32_16x16x32_bf16 v[24:27], a[124:127], v[84:87], v[24:27]\nds_read_b128 v[76:79], v1 offset:7168\n" \
"v_mfma_f32_16x16x32_bf16 v[28:31], a[128:131], v[80:83], v[28:31]\nbuffer_load_dwordx4 v0, s[36:39], s60 offen offset:3072 sc1 lds\nv_mfma_f32_16x16x32_bf16 v[28:31], a[132:135], v[84:87], v[28:31]\ns_waitcnt lgkmcnt(3)\nv_mfma_f32_16x16x32_bf16 v[32:35], a[104:107], v[88:91], v[32:35]\ns_waitcnt lgkmcnt(2)\nds_read_b128 v[80:83], v2 offset:20480\nv_mfma_f32_16x16x32_bf16 v[32:35], a[108:111], v[92:95], v[32:35]\n" \
"ds_read_b128 v[84:87], v2 offset:22528\nv_mfma_f32_16x16x32_bf16 v[36:39], a[112:115], v[88:91], v[36:39]\nv_mfma_f32_16x16x32_bf16 v[36:39], a[116:119], v[92:95], v[36:39]\nv_mfma_f32_16x16x32_bf16 v[40:43], a[120:123], v[88:91], v[40:43]\nv_mfma_f32_16x16x32_bf16 v[40:43], a[124:127], v[92:95], v[40:43]\nv_mfma_f32_16x16x32_bf16 v[44:47], a[128:131], v[88:91], v[44:47]\nv_mfma_f32_16x16x32_bf16 v[44:47], a[132:135], v[92:95], v[44:47]\ns_waitcnt lgkmcnt(1)\n" \
"v_mfma_f32_16x16x32_bf16 v[16:19], a[136:139], v[80:83], v[16:19]\ns_waitcnt lgkmcnt(0)\nds_read_b128 v[88:91], v2 offset:20736\nv_mfma_f32_16x16x32_bf16 v[16:19], a[140:143], v[84:87], v[16:19]\nds_read_b128 v[92:95], v2 offset:22784\nv_mfma_f32_16x16x32_bf16 v[20:23], v[72:75], v[80:83], v[20:23]\nv_mfma_f32_16x16x32_bf16 v[20:23], v[76:79], v[84:87], v[20:23]\nv_mfma_f32_16x16x32_bf16 v[24:27], a[144:147], v[80:83], v[24:27]\n" \
"ds_read_b128 v[64:67], v1 offset:8192\nv_mfma_f32_16x16x32_bf16 v[24:27], a[148:151], v[84:87], v[24:27]\nds_read_b128 v[68:71], v1 offset:9216\nv_mfma_f32_16x16x32_bf16 v[28:31], a[152:155], v[80:83], v[28:31]\nv_mfma_f32_16x16x32_bf16 v[28:31], a[156:159], v[84:87], v[28:31]\ns_waitcnt lgkmcnt(3)\nv_mfma_f32_16x16x32_bf16 v[32:35], a[136:139], v[88:91], v[32:35]\ns_waitcnt lgkmcnt(2)\n" \
"ds_read_b128 v[80:83], v2 offset:24576\nv_mfma_f32_16x16x32_bf16 v[32:35], a[140:143], v[92:95], v[32:35]\nds_read_b128 v[84:87], v2 offset:26624\nv_mfma_f32_16x16x32_bf16 v[36:39], v[72:75], v[88:91], v[36:39]\nv_mfma_f32_16x16x32_bf16 v[36:39], v[76:79], v[92:95], v[36:39]\nv_mfma_f32_16x16x32_bf16 v[40:43], a[144:147], v[88:91], v[40:43]\nv_mfma_f32_16x16x32_bf16 v[40:43], a[148:151], v[92:95], v[40:43]\nv_mfma_f32_16x16x32_bf16 v[44:47], a[152:155], v[88:91], v[44:47]\n" \
"v_mfma_f32_16x16x32_bf16 v[44:47], a[156:159], v[92:95], v[44:47]\ns_waitcnt lgkmcnt(1)\nv_mfma_f32_16x16x32_bf16 v[16:19], a[160:163], v[80:83], v[16:19]\ns_waitcnt lgkmcnt(0)\nds_read_b128 v[88:91], v2 offset:24832\nv_mfma_f32_16x16x32_bf16 v[16:19], a[164:167], v[84:87], v[16:19]\nds_read_b128 v[92:95], v2 offset:26880\nv_mfma_f32_16x16x32_bf16 v[20:23], a[168:171], v[80:83], v[20:23]\n" \
"v_mfma_f32_16x16x32_bf16 v[20:23], a[172:175], v[84:87], v[20:23]\nv_mfma_f32_16x16x32_bf16 v[24:27], v[64:67], v[80:83], v[24:27]\nds_read_b128 v[72:75], v1 offset:10240\nv_mfma_f32_16x16x32_bf16 v[24:27], v[68:71], v[84:87], v[24:27]\nds_read_b128 v[76:79], v1 offset:11264\nv_mfma_f32_16x16x32_bf16 v[28:31], a[176:179], v[80:83], v[28:31]\nv_mfma_f32_16x16x32_bf16 v[28:31], a[180:183], v[84:87], v[28:31]\ns_waitcnt lgkmcnt(3)\n" \
"v_mfma_f32_16x16x32_bf16 v[32:35], a[160:163], v[88:91], v[32:35]\ns_waitcnt lgkmcnt(2)\nds_read_b128 v[80:83], v2 offset:28672\nv_mfma_f32_16x16x32_bf16 v[32:35], a[164:167], v[92:95], v[32:35]\nds_read_b128 v[84:87], v2 offset:30720\nv_mfma_f32_16x16x32_bf16 v[36:39], a[168:171], v[88:91], v[36:39]\nv_mfma_f32_16x16x32_bf16 v[36:39], a[172:175], v[92:95], v[36:39]\nv_mfma_f32_16x16x32_bf16 v[40:43], v[64:67], v[88:91], v[40:43]\n" \
"v_mfma_f32_16x16x32_bf16 v[40:43], v[68:71], v[92:95], v[40:43]\nv_mfma_f32_16x16x32_bf16 v[44:47], a[176:179], v[88:91], v[44:47]\nv_mfma_f32_16x16x32_bf16 v[44:47], a[180:183], v[92:95], v[44:47]\ns_waitcnt lgkmcnt(1)\nv_mfma_f32_16x16x32_bf16 v[16:19], a[184:187], v[80:83], v[16:19]\ns_waitcnt lgkmcnt(0)\nds_read_b128 v[88:91], v2 offset:28928\nv_mfma_f32_16x16x32_bf16 v[16:19], a[188:191], v[84:87], v[16:19]\n" \
"ds_read_b128 v[92:95], v2 offset:30976\nv_mfma_f32_16x16x32_bf16 v[20:23], a[192:195], v[80:83], v[20:23]\nv_mfma_f32_16x16x32_bf16 v[20:23], a[196:199], v[84:87], v[20:23]\nv_mfma_f32_16x16x32_bf16 v[24:27], a[200:203], v[80:83], v[24:27]\nv_mfma_f32_16x16x32_bf16 v[24:27], a[204:207], v[84:87], v[24:27]\nv_mfma_f32_16x16x32_bf16 v[28:31], v[72:75], v[80:83], v[28:31]\nv_mfma_f32_16x16x32_bf16 v[28:31], v[76:79], v[84:87], v[28:31]\ns_waitcnt lgkmcnt(1)\n" \
"v_mfma_f32_16x16x32_bf16 v[32:35], a[184:187], v[88:91], v[32:35]\ns_waitcnt vmcnt(0)\ns_barrier\ns_waitcnt lgkmcnt(0)\nds_read_b128 v[48:51], v2 offset:32768\nv_mfma_f32_16x16x32_bf16 v[32:35], a[188:191], v[92:95], v[32:35]\ns_add_u32 m0, s57, 0\nds_read_b128 v[52:55], v2 offset:34816\n" \
"v_mfma_f32_16x16x32_bf16 v[36:39], a[192:195], v[88:91], v[36:39]\ns_add_u32 s60, s41, 98304\nbuffer_load_dwordx4 v0, s[36:39], s60 offen offset:0 sc1 lds\nv_mfma_f32_16x16x32_bf16 v[36:39], a[196:199], v[92:95], v[36:39]\nv_mfma_f32_16x16x32_bf16 v[40:43], a[200:203], v[88:91], v[40:43]\nv_mfma_f32_16x16x32_bf16 v[40:43], a[204:207], v[92:95], v[40:43]\nv_mfma_f32_16x16x32_bf16 v[44:47], v[72:75], v[88:91], v[44:47]\nbuffer_load_dwordx4 v0, s[36:39], s60 offen offset:1024 sc1 lds\n" \
"v_mfma_f32_16x16x32_bf16 v[44:47], v[76:79], v[92:95], v[44:47]\ns_waitcnt lgkmcnt(1)\nv_mfma_f32_16x16x32_bf16 v[16:19], a[208:211], v[48:51], v[16:19]\ns_waitcnt lgkmcnt(0)\nds_read_b128 v[56:59], v2 offset:33024\nv_mfma_f32_16x16x32_bf16 v[16:19], a[212:215], v[52:55], v[16:19]\nds_read_b128 v[60:63], v2 offset:35072\nv_mfma_f32_16x16x32_bf16 v[20:23], a[216:219], v[48:51], v[20:23]\n" \
"buffer_load_dwordx4 v0, s[36:39], s60 offen offset:2048 sc1 lds\nv_mfma_f32_16x16x32_bf16 v[20:23], a[220:223], v[52:55], v[20:23]\nv_mfma_f32_16x16x32_bf16 v[24:27], a[224:227], v[48:51], v[24:27]\nds_read_b128 v[72:75], v1 offset:12288\nv_mfma_f32_16x16x32_bf16 v[24:27], a[228:231], v[52:55], v[24:27]\nds_read_b128 v[76:79], v1 offset:13312\nv_mfma_f32_16x16x32_bf16 v[28:31], a[232:235], v[48:51], v[28:31]\nbuffer_load_dwordx4 v0, s[36:39], s60 offen offset:3072 sc1 lds\n" \
"v_mfma_f32_16x16x32_bf16 v[28:31], a[236:239], v[52:55], v[28:31]\ns_waitcnt lgkmcnt(3)\nv_mfma_f32_16x16x32_bf16 v[32:35], a[208:211], v[56:59], v[32:35]\ns_waitcnt lgkmcnt(2)\nds_read_b128 v[48:51], v2 offset:36864\nv_mfma_f32_16x16x32_bf16 v[32:35], a[212:215], v[60:63], v[32:35]\nds_read_b128 v[52:55], v2 offset:38912\nv_mfma_f32_16x16x32_bf16 v[36:39], a[216:219], v[56:59], v[36:39]\n" \
"v_mfma_f32_16x16x32_bf16 v[36:39], a[220:223], v[60:63], v[36:39]\nv_mfma_f32_16x16x32_bf16 v[40:43], a[224:227], v[56:59], v[40:43]\nv_mfma_f32_16x16x32_bf16 v[40:43], a[228:231], v[60:63], v[40:43]\nv_mfma_f32_16x16x32_bf16 v[44:47], a[232:235], v[56:59], v[44:47]\nv_mfma_f32_16x16x32_bf16 v[44:47], a[236:239], v[60:63], v[44:47]\ns_waitcnt lgkmcnt(1)\nv_mfma_f32_16x16x32_bf16 v[16:19], a[240:243], v[48:51], v[16:19]\ns_waitcnt lgkmcnt(0)\n" \
"ds_read_b128 v[56:59], v2 offset:37120\nv_mfma_f32_16x16x32_bf16 v[16:19], a[244:247], v[52:55], v[16:19]\nds_read_b128 v[60:63], v2 offset:39168\nv_mfma_f32_16x16x32_bf16 v[20:23], v[72:75], v[48:51], v[20:23]\nv_mfma_f32_16x16x32_bf16 v[20:23], v[76:79], v[52:55], v[20:23]\nv_mfma_f32_16x16x32_bf16 v[24:27], a[248:251], v[48:51], v[24:27]\nds_read_b128 v[64:67], v1 offset:14336\nv_mfma_f32_16x16x32_bf16 v[24:27], a[252:255], v[52:55], v[24:27]\n" \
"ds_read_b128 v[68:71], v1 offset:15360\nv_mfma_f32_16x16x32_bf16 v[28:31], v[96:99], v[48:51], v[28:31]\nv_mfma_f32_16x16x32_bf16 v[28:31], v[100:103], v[52:55], v[28:31]\ns_waitcnt lgkmcnt(3)\nv_mfma_f32_16x16x32_bf16 v[32:35], a[240:243], v[56:59], v[32:35]\ns_waitcnt lgkmcnt(2)\nds_read_b128 v[48:51], v2 offset:40960\nv_mfma_f32_16x16x32_bf16 v[32:35], a[244:247], v[60:63], v[32:35]\n" \
"ds_read_b128 v[52:55], v2 offset:43008\nv_mfma_f32_16x16x32_bf16 v[36:39], v[72:75], v[56:59], v[36:39]\nv_mfma_f32_16x16x32_bf16 v[36:39], v[76:79], v[60:63], v[36:39]\nv_mfma_f32_16x16x32_bf16 v[40:43], a[248:251], v[56:59], v[40:43]\nv_mfma_f32_16x16x32_bf16 v[40:43], a[252:255], v[60:63], v[40:43]\nv_mfma_f32_16x16x32_bf16 v[44:47], v[96:99], v[56:59], v[44:47]\nv_mfma_f32_16x16x32_bf16 v[44:47], v[100:103], v[60:63], v[44:47]\ns_waitcnt lgkmcnt(1)\n" \
"v_mfma_f32_16x16x32_bf16 v[16:19], v[104:107], v[48:51], v[16:19]\ns_waitcnt lgkmcnt(0)\nds_read_b128 v[56:59], v2 offset:41216\nv_mfma_f32_16x16x32_bf16 v[16:19], v[108:111], v[52:55], v[16:19]\nds_read_b128 v[60:63], v2 offset:43264\nv_mfma_f32_16x16x32_bf16 v[20:23], v[112:115], v[48:51], v[20:23]\nv_mfma_f32_16x16x32_bf16 v[20:23], v[116:119], v[52:55], v[20:23]\nv_mfma_f32_16x16x32_bf16 v[24:27], v[64:67], v[48:51], v[24:27]\n" \
"ds_read_b128 v[72:75], v1 offset:16384\nv_mfma_f32_16x16x32_bf16 v[24:27], v[68:71], v[52:55], v[24:27]\nds_read_b128 v[76:79], v1 offset:17408\nv_mfma_f32_16x16x32_bf16 v[28:31], v[120:123], v[48:51], v[28:31]\nv_mfma_f32_16x16x32_bf16 v[28:31], v[124:127], v[52:55], v[28:31]\ns_waitcnt lgkmcnt(3)\nv_mfma_f32_16x16x32_bf16 v[32:35], v[104:107], v[56:59], v[32:35]\ns_waitcnt lgkmcnt(2)\n" \
"ds_read_b128 v[48:51], v2 offset:45056\nv_mfma_f32_16x16x32_bf16 v[32:35], v[108:111], v[60:63], v[32:35]\nds_read_b128 v[52:55], v2 offset:47104\nv_mfma_f32_16x16x32_bf16 v[36:39], v[112:115], v[56:59], v[36:39]\nv_mfma_f32_16x16x32_bf16 v[36:39], v[116:119], v[60:63], v[36:39]\nv_mfma_f32_16x16x32_bf16 v[40:43], v[64:67], v[56:59], v[40:43]\nv_mfma_f32_16x16x32_bf16 v[40:43], v[68:71], v[60:63], v[40:43]\nv_mfma_f32_16x16x32_bf16 v[44:47], v[120:123], v[56:59], v[44:47]\n" \
"v_mfma_f32_16x16x32_bf16 v[44:47], v[124:127], v[60:63], v[44:47]\ns_waitcnt lgkmcnt(1)\nv_mfma_f32_16x16x32_bf16 v[16:19], v[128:131], v[48:51], v[16:19]\ns_waitcnt lgkmcnt(0)\nds_read_b128 v[56:59], v2 offset:45312\nv_mfma_f32_16x16x32_bf16 v[16:19], v[132:135], v[52:55], v[16:19]\nds_read_b128 v[60:63], v2 offset:47360\nv_mfma_f32_16x16x32_bf16 v[20:23], v[136:139], v[48:51], v[20:23]\n" \
"v_mfma_f32_16x16x32_bf16 v[20:23], v[140:143], v[52:55], v[20:23]\nv_mfma_f32_16x16x32_bf16 v[24:27], v[144:147], v[48:51], v[24:27]\nv_mfma_f32_16x16x32_bf16 v[24:27], v[148:151], v[52:55], v[24:27]\nv_mfma_f32_16x16x32_bf16 v[28:31], v[72:75], v[48:51], v[28:31]\nv_mfma_f32_16x16x32_bf16 v[28:31], v[76:79], v[52:55], v[28:31]\ns_waitcnt lgkmcnt(1)\nv_mfma_f32_16x16x32_bf16 v[32:35], v[128:131], v[56:59], v[32:35]\ns_waitcnt lgkmcnt(0)\n" \
"s_waitcnt vmcnt(0)\ns_barrier\nds_read_b128 v[48:51], v2 offset:0\nv_mfma_f32_16x16x32_bf16 v[32:35], v[132:135], v[60:63], v[32:35]\nds_read_b128 v[52:55], v2 offset:2048\nv_mfma_f32_16x16x32_bf16 v[36:39], v[136:139], v[56:59], v[36:39]\nv_mfma_f32_16x16x32_bf16 v[36:39], v[140:143], v[60:63], v[36:39]\nv_mfma_f32_16x16x32_bf16 v[40:43], v[144:147], v[56:59], v[40:43]\n" \
"v_mfma_f32_16x16x32_bf16 v[40:43], v[148:151], v[60:63], v[40:43]\nv_mfma_f32_16x16x32_bf16 v[44:47], v[72:75], v[56:59], v[44:47]\nv_mfma_f32_16x16x32_bf16 v[44:47], v[76:79], v[60:63], v[44:47]\ns_waitcnt lgkmcnt(1)\nv_mfma_f32_16x16x32_bf16 v[16:19], v[152:155], v[48:51], v[16:19]\ns_waitcnt lgkmcnt(0)\nds_read_b128 v[56:59], v2 offset:256\nv_mfma_f32_16x16x32_bf16 v[16:19], v[156:159], v[52:55], v[16:19]\n" \
"ds_read_b128 v[60:63], v2 offset:2304\nv_mfma_f32_16x16x32_bf16 v[20:23], v[160:163], v[48:51], v[20:23]\nv_mfma_f32_16x16x32_bf16 v[20:23], v[164:167], v[52:55], v[20:23]\nv_mfma_f32_16x16x32_bf16 v[24:27], v[168:171], v[48:51], v[24:27]\nds_read_b128 v[72:75], v1 offset:18432\nv_mfma_f32_16x16x32_bf16 v[24:27], v[172:175], v[52:55], v[24:27]\nds_read_b128 v[76:79], v1 offset:19456\nv_mfma_f32_16x16x32_bf16 v[28:31], v[176:179], v[48:51], v[28:31]\n" \
"v_mfma_f32_16x16x32_bf16 v[28:31], v[180:183], v[52:55], v[28:31]\ns_waitcnt lgkmcnt(3)\nv_mfma_f32_16x16x32_bf16 v[32:35], v[152:155], v[56:59], v[32:35]\ns_waitcnt lgkmcnt(2)\nds_read_b128 v[48:51], v2 offset:4096\nv_mfma_f32_16x16x32_bf16 v[32:35], v[156:159], v[60:63], v[32:35]\nds_read_b128 v[52:55], v2 offset:6144\nv_mfma_f32_16x16x32_bf16 v[36:39], v[160:163], v[56:59], v[36:39]\n" \
"v_mfma_f32_16x16x32_bf16 v[36:39], v[164:167], v[60:63], v[36:39]\nv_mfma_f32_16x16x32_bf16 v[40:43], v[168:171], v[56:59], v[40:43]\nv_mfma_f32_16x16x32_bf16 v[40:43], v[172:175], v[60:63], v[40:43]\nv_mfma_f32_16x16x32_bf16 v[44:47], v[176:179], v[56:59], v[44:47]\nv_mfma_f32_16x16x32_bf16 v[44:47], v[180:183], v[60:63], v[44:47]\ns_waitcnt lgkmcnt(1)\nv_mfma_f32_16x16x32_bf16 v[16:19], v[184:187], v[48:51], v[16:19]\ns_waitcnt lgkmcnt(0)\n" \
"ds_read_b128 v[56:59], v2 offset:4352\nv_mfma_f32_16x16x32_bf16 v[16:19], v[188:191], v[52:55], v[16:19]\nds_read_b128 v[60:63], v2 offset:6400\nv_mfma_f32_16x16x32_bf16 v[20:23], v[72:75], v[48:51], v[20:23]\nv_mfma_f32_16x16x32_bf16 v[20:23], v[76:79], v[52:55], v[20:23]\nv_mfma_f32_16x16x32_bf16 v[24:27], v[192:195], v[48:51], v[24:27]\nds_read_b128 v[64:67], v1 offset:20480\nv_mfma_f32_16x16x32_bf16 v[24:27], v[196:199], v[52:55], v[24:27]\n" \
"ds_read_b128 v[68:71], v1 offset:21504\nv_mfma_f32_16x16x32_bf16 v[28:31], v[200:203], v[48:51], v[28:31]\nv_mfma_f32_16x16x32_bf16 v[28:31], v[204:207], v[52:55], v[28:31]\ns_waitcnt lgkmcnt(3)\nv_mfma_f32_16x16x32_bf16 v[32:35], v[184:187], v[56:59], v[32:35]\ns_waitcnt lgkmcnt(2)\nds_read_b128 v[48:51], v2 offset:8192\nv_mfma_f32_16x16x32_bf16 v[32:35], v[188:191], v[60:63], v[32:35]\n" \
"ds_read_b128 v[52:55], v2 offset:10240\nv_mfma_f32_16x16x32_bf16 v[36:39], v[72:75], v[56:59], v[36:39]\nv_mfma_f32_16x16x32_bf16 v[36:39], v[76:79], v[60:63], v[36:39]\nv_mfma_f32_16x16x32_bf16 v[40:43], v[192:195], v[56:59], v[40:43]\nv_mfma_f32_16x16x32_bf16 v[40:43], v[196:199], v[60:63], v[40:43]\nv_mfma_f32_16x16x32_bf16 v[44:47], v[200:203], v[56:59], v[44:47]\nbuffer_load_dwordx4 v[80:83], v3, s[36:39], s42 offen offset:0 sc1\nv_mfma_f32_16x16x32_bf16 v[44:47], v[204:207], v[60:63], v[44:47]\n" \
"s_waitcnt lgkmcnt(1)\nv_mfma_f32_16x16x32_bf16 v[16:19], v[208:211], v[48:51], v[16:19]\ns_waitcnt lgkmcnt(0)\nds_read_b128 v[56:59], v2 offset:12288\nv_mfma_f32_16x16x32_bf16 v[16:19], v[212:215], v[52:55], v[16:19]\nds_read_b128 v[60:63], v2 offset:14336\nv_mfma_f32_16x16x32_bf16 v[20:23], v[216:219], v[48:51], v[20:23]\nbuffer_load_dwordx4 v[84:87], v3, s[36:39], s42 offen offset:1024 sc1\n" \
"v_mfma_f32_16x16x32_bf16 v[20:23], v[220:223], v[52:55], v[20:23]\nv_mfma_f32_16x16x32_bf16 v[24:27], v[64:67], v[48:51], v[24:27]\nds_read_b128 v[72:75], v1 offset:22528\nv_mfma_f32_16x16x32_bf16 v[24:27], v[68:71], v[52:55], v[24:27]\nds_read_b128 v[76:79], v1 offset:23552\nv_mfma_f32_16x16x32_bf16 v[28:31], v[224:227], v[48:51], v[28:31]\nbuffer_load_dwordx4 v[88:91], v3, s[36:39], s42 offen offset:2048 sc1\nv_mfma_f32_16x16x32_bf16 v[28:31], v[228:231], v[52:55], v[28:31]\n" \
"s_waitcnt lgkmcnt(3)\nv_mfma_f32_16x16x32_bf16 v[16:19], v[232:235], v[56:59], v[16:19]\ns_waitcnt lgkmcnt(2)\nds_read_b128 v[48:51], v2 offset:8448\nv_mfma_f32_16x16x32_bf16 v[16:19], v[236:239], v[60:63], v[16:19]\nds_read_b128 v[52:55], v2 offset:10496\nv_mfma_f32_16x16x32_bf16 v[20:23], v[240:243], v[56:59], v[20:23]\nbuffer_load_dwordx4 v[92:95], v3, s[36:39], s42 offen offset:3072 sc1\n" \
"v_mfma_f32_16x16x32_bf16 v[20:23], v[244:247], v[60:63], v[20:23]\nv_mfma_f32_16x16x32_bf16 v[24:27], v[248:251], v[56:59], v[24:27]\nv_mfma_f32_16x16x32_bf16 v[24:27], v[252:255], v[60:63], v[24:27]\ns_waitcnt lgkmcnt(3)\nv_mfma_f32_16x16x32_bf16 v[28:31], v[72:75], v[56:59], v[28:31]\ns_waitcnt lgkmcnt(2)\nv_cvt_pk_bf16_f32 v20, v16, v20\nv_cvt_pk_bf16_f32 v21, v17, v21\n" \
"v_mfma_f32_16x16x32_bf16 v[28:31], v[76:79], v[60:63], v[28:31]\ns_waitcnt lgkmcnt(1)\nv_cvt_pk_bf16_f32 v22, v18, v22\nv_cvt_pk_bf16_f32 v23, v19, v23\nv_mfma_f32_16x16x32_bf16 v[32:35], v[208:211], v[48:51], v[32:35]\ns_waitcnt lgkmcnt(0)\nds_read_b128 v[56:59], v2 offset:12544\nv_mfma_f32_16x16x32_bf16 v[32:35], v[212:215], v[52:55], v[32:35]\n" \
"ds_read_b128 v[60:63], v2 offset:14592\nv_mfma_f32_16x16x32_bf16 v[36:39], v[216:219], v[48:51], v[36:39]\nds_write_b128 v5, v[20:23] offset:0\nv_mfma_f32_16x16x32_bf16 v[36:39], v[220:223], v[52:55], v[36:39]\nv_cvt_pk_bf16_f32 v28, v24, v28\nv_cvt_pk_bf16_f32 v29, v25, v29\nv_mfma_f32_16x16x32_bf16 v[40:43], v[64:67], v[48:51], v[40:43]\nv_cvt_pk_bf16_f32 v30, v26, v30\n" \
"v_cvt_pk_bf16_f32 v31, v27, v31\nv_mfma_f32_16x16x32_bf16 v[40:43], v[68:71], v[52:55], v[40:43]\nv_mfma_f32_16x16x32_bf16 v[44:47], v[224:227], v[48:51], v[44:47]\ns_cmp_eq_u32 %[w], 1\nds_write_b128 v5, v[28:31] offset:512\nv_mfma_f32_16x16x32_bf16 v[44:47], v[228:231], v[52:55], v[44:47]\ns_cbranch_scc1 .Lqk_c1%=\ns_cmp_eq_u32 %[w], 2\n" \
"s_cbranch_scc1 .Lqk_c2%=\ns_cmp_eq_u32 %[w], 3\ns_cbranch_scc1 .Lqk_c3%=\n.Lqk_c0%=:\ns_waitcnt lgkmcnt(3)\nv_mfma_f32_16x16x32_bf16 v[32:35], v[232:235], v[56:59], v[32:35]\ns_waitcnt lgkmcnt(2)\nv_mov_b64 v[48:49], 0\n" \
"v_mov_b64 v[50:51], 0\nv_mfma_f32_16x16x32_bf16 v[32:35], v[236:239], v[60:63], v[32:35]\nds_read_b128 v[64:67], v1 offset:0\nv_mfma_f32_16x16x32_bf16 v[36:39], v[240:243], v[56:59], v[36:39]\nds_read_b128 v[68:71], v1 offset:1024\nv_mfma_f32_16x16x32_bf16 v[36:39], v[244:247], v[60:63], v[36:39]\ns_bfe_i32 s54, s40, 0x10000\nv_mov_b64 v[52:53], 0\n" \
"v_mov_b64 v[54:55], 0\nv_mfma_f32_16x16x32_bf16 v[40:43], v[248:251], v[56:59], v[40:43]\nv_mfma_f32_16x16x32_bf16 v[40:43], v[252:255], v[60:63], v[40:43]\nv_cvt_pk_bf16_f32 v36, v32, v36\nv_cvt_pk_bf16_f32 v37, v33, v37\nv_mfma_f32_16x16x32_bf16 v[44:47], v[72:75], v[56:59], v[44:47]\nv_cvt_pk_bf16_f32 v38, v34, v38\nv_cvt_pk_bf16_f32 v39, v35, v39\n" \
"v_mfma_f32_16x16x32_bf16 v[44:47], v[76:79], v[60:63], v[44:47]\nds_read_b128 v[72:75], v1 offset:2048\nds_read_b128 v[76:79], v1 offset:3072\nv_mov_b64 v[56:57], 0\nv_mov_b64 v[58:59], 0\nv_mov_b64 v[60:61], 0\nv_mov_b64 v[62:63], 0\n.Lqk_poll0%=:\n" \
"s_waitcnt vmcnt(0)\nv_smfmac_f32_16x16x64_bf16 v[48:51], v[80:83], a[0:7], v4\nds_write_b128 v0, v[80:83] offset:16384\nv_max3_u32 v16, v80, v82, v84\nv_smfmac_f32_16x16x64_bf16 v[48:51], v[84:87], a[32:39], v4\nv_max3_u32 v16, v16, v86, v88\nv_smfmac_f32_16x16x64_bf16 v[52:55], v[80:83], a[8:15], v4\ns_waitcnt lgkmcnt(3)\n" \
"ds_write_b128 v0, v[84:87] offset:17408\nv_max3_u32 v16, v16, v90, v92\nv_max_u32 v16, v16, v94\nv_smfmac_f32_16x16x64_bf16 v[52:55], v[84:87], v[64:71], v4\nv_cmp_eq_u32 vcc, -1, v16\nv_smfmac_f32_16x16x64_bf16 v[56:59], v[80:83], a[16:23], v4\nds_write_b128 v0, v[88:91] offset:18432\ns_cbranch_vccnz .Lqk_re0%=\n" \
"v_smfmac_f32_16x16x64_bf16 v[56:59], v[84:87], a[40:47], v4\nv_smfmac_f32_16x16x64_bf16 v[60:63], v[80:83], a[24:31], v4\nds_write_b128 v0, v[92:95] offset:19456\nv_smfmac_f32_16x16x64_bf16 v[60:63], v[84:87], a[48:55], v4\nv_smfmac_f32_16x16x64_bf16 v[48:51], v[88:91], a[56:63], v4\nv_smfmac_f32_16x16x64_bf16 v[48:51], v[92:95], a[80:87], v4\ns_waitcnt lgkmcnt(0)\n.Lqk_jn0%=:\n" \
"s_barrier\ns_memrealtime s[68:69]\nds_read_b128 v[80:83], v0 offset:20480\nds_read_b128 v[64:67], v1 offset:4096\nds_read_b128 v[68:71], v1 offset:5120\nv_mov_b32 v24, s40\nv_smfmac_f32_16x16x64_bf16 v[52:55], v[88:91], a[64:71], v4\nv_smfmac_f32_16x16x64_bf16 v[52:55], v[92:95], a[88:95], v4\n" \
"ds_read_b128 v[84:87], v0 offset:21504\nv_smfmac_f32_16x16x64_bf16 v[56:59], v[88:91], v[72:79], v4\nds_read_b128 v[72:75], v1 offset:6144\nds_read_b128 v[76:79], v1 offset:7168\nv_smfmac_f32_16x16x64_bf16 v[56:59], v[92:95], a[96:103], v4\nv_smfmac_f32_16x16x64_bf16 v[60:63], v[88:91], a[72:79], v4\ns_waitcnt lgkmcnt(3)\nds_read_b128 v[88:91], v0 offset:22528\n" \
"v_smfmac_f32_16x16x64_bf16 v[60:63], v[92:95], v[64:71], v4\ns_sub_u32 s88, s86, s89\nds_read_b128 v[92:95], v0 offset:23552\nds_read_b128 v[64:67], v1 offset:8192\nds_read_b128 v[68:71], v1 offset:9216\nv_smfmac_f32_16x16x64_bf16 v[48:51], v[80:83], a[104:111], v4\ns_min_u32 s88, s88, 150\ns_waitcnt lgkmcnt(6)\n" \
"ds_write_b128 v5, v[36:39] offset:256\nv_cvt_pk_bf16_f32 v44, v40, v44\nv_cvt_pk_bf16_f32 v45, v41, v45\nv_cvt_pk_bf16_f32 v46, v42, v46\nv_cvt_pk_bf16_f32 v47, v43, v47\nv_smfmac_f32_16x16x64_bf16 v[48:51], v[84:87], a[136:143], v4\ns_add_u32 s88, s88, s68\ns_mov_b32 s89, s68\n" \
"v_mov_b32 v25, s88\nv_smfmac_f32_16x16x64_bf16 v[52:55], v[80:83], a[112:119], v4\ns_waitcnt lgkmcnt(5)\nbuffer_store_dwordx2 v[24:25], off, s[36:39], %[sg] sc1\nv_smfmac_f32_16x16x64_bf16 v[52:55], v[84:87], v[72:79], v4\nds_read_b128 v[72:75], v1 offset:10240\nds_read_b128 v[76:79], v1 offset:11264\nv_smfmac_f32_16x16x64_bf16 v[56:59], v[80:83], a[120:127], v4\n" \
"v_smfmac_f32_16x16x64_bf16 v[56:59], v[84:87], a[144:151], v4\nv_smfmac_f32_16x16x64_bf16 v[60:63], v[80:83], a[128:135], v4\nds_read_b128 v[80:83], v0 offset:24576\nv_smfmac_f32_16x16x64_bf16 v[60:63], v[84:87], a[152:159], v4\ns_waitcnt lgkmcnt(7)\nds_read_b128 v[84:87], v0 offset:25600\nv_smfmac_f32_16x16x64_bf16 v[48:51], v[88:91], a[160:167], v4\ns_waitcnt lgkmcnt(7)\n" \
"ds_write_b128 v5, v[44:47] offset:768\nv_smfmac_f32_16x16x64_bf16 v[48:51], v[92:95], a[184:191], v4\nv_smfmac_f32_16x16x64_bf16 v[52:55], v[88:91], a[168:175], v4\nv_smfmac_f32_16x16x64_bf16 v[52:55], v[92:95], a[192:199], v4\ns_waitcnt lgkmcnt(6)\nv_smfmac_f32_16x16x64_bf16 v[56:59], v[88:91], v[64:71], v4\nds_read_b128 v[64:67], v1 offset:12288\nds_read_b128 v[68:71], v1 offset:13312\n" \
"v_smfmac_f32_16x16x64_bf16 v[56:59], v[92:95], a[200:207], v4\ns_waitcnt lgkmcnt(2)\ns_barrier\nv_smfmac_f32_16x16x64_bf16 v[60:63], v[88:91], a[176:183], v4\nds_read_b128 v[88:91], v0 offset:26624\nds_read_b128 v[16:19], v7 offset:0\nv_smfmac_f32_16x16x64_bf16 v[60:63], v[92:95], v[72:79], v4\nds_read_b128 v[92:95], v0 offset:27648\n" \
"ds_read_b128 v[72:75], v1 offset:14336\nds_read_b128 v[76:79], v1 offset:15360\nv_smfmac_f32_16x16x64_bf16 v[48:51], v[80:83], a[208:215], v4\nds_read_b128 v[20:23], v7 offset:64\nv_smfmac_f32_16x16x64_bf16 v[48:51], v[84:87], a[240:247], v4\nv_smfmac_f32_16x16x64_bf16 v[52:55], v[80:83], a[216:223], v4\ns_waitcnt lgkmcnt(6)\nds_read_b128 v[24:27], v7 offset:2048\n" \
"v_smfmac_f32_16x16x64_bf16 v[52:55], v[84:87], v[64:71], v4\nds_read_b128 v[64:67], v1 offset:16384\nds_read_b128 v[68:71], v1 offset:17408\nv_smfmac_f32_16x16x64_bf16 v[56:59], v[80:83], a[224:231], v4\nds_read_b128 v[28:31], v7 offset:2112\nv_smfmac_f32_16x16x64_bf16 v[56:59], v[84:87], a[248:255], v4\nv_smfmac_f32_16x16x64_bf16 v[60:63], v[80:83], a[232:239], v4\nds_read_b128 v[80:83], v0 offset:28672\n" \
"ds_read_b128 v[32:35], v7 offset:4096\nv_smfmac_f32_16x16x64_bf16 v[60:63], v[84:87], v[96:103], v4\ns_waitcnt lgkmcnt(11)\nds_read_b128 v[84:87], v0 offset:29696\nv_smfmac_f32_16x16x64_bf16 v[48:51], v[88:91], v[104:111], v4\ns_waitcnt lgkmcnt(10)\nds_read_b128 v[36:39], v7 offset:4160\nv_smfmac_f32_16x16x64_bf16 v[48:51], v[92:95], v[128:135], v4\n" \
"v_smfmac_f32_16x16x64_bf16 v[52:55], v[88:91], v[112:119], v4\nds_read_b128 v[40:43], v7 offset:6144\nv_smfmac_f32_16x16x64_bf16 v[52:55], v[92:95], v[136:143], v4\ns_waitcnt lgkmcnt(10)\nv_smfmac_f32_16x16x64_bf16 v[56:59], v[88:91], v[72:79], v4\nds_read_b128 v[72:75], v1 offset:18432\nds_read_b128 v[76:79], v1 offset:19456\nv_smfmac_f32_16x16x64_bf16 v[56:59], v[92:95], v[144:151], v4\n" \
"v_smfmac_f32_16x16x64_bf16 v[60:63], v[88:91], v[120:127], v4\ns_waitcnt lgkmcnt(8)\nds_read_b128 v[88:91], v0 offset:30720\nds_read_b128 v[44:47], v7 offset:6208\nv_smfmac_f32_16x16x64_bf16 v[60:63], v[92:95], v[64:71], v4\ns_waitcnt lgkmcnt(8)\nds_read_b128 v[92:95], v0 offset:31744\nds_read_b128 v[64:67], v1 offset:20480\n" \
"ds_read_b128 v[68:71], v1 offset:21504\nv_smfmac_f32_16x16x64_bf16 v[48:51], v[80:83], v[152:159], v4\ns_waitcnt lgkmcnt(9)\nv_smfmac_f32_16x16x64_bf16 v[48:51], v[84:87], v[184:191], v4\nv_smfmac_f32_16x16x64_bf16 v[52:55], v[80:83], v[160:167], v4\ns_waitcnt lgkmcnt(5)\nv_smfmac_f32_16x16x64_bf16 v[52:55], v[84:87], v[72:79], v4\nds_read_b128 v[72:75], v1 offset:22528\n" \
"ds_read_b128 v[76:79], v1 offset:23552\nv_smfmac_f32_16x16x64_bf16 v[56:59], v[80:83], v[168:175], v4\nv_smfmac_f32_16x16x64_bf16 v[56:59], v[84:87], v[192:199], v4\nv_smfmac_f32_16x16x64_bf16 v[60:63], v[80:83], v[176:183], v4\nv_smfmac_f32_16x16x64_bf16 v[60:63], v[84:87], v[200:207], v4\ns_waitcnt lgkmcnt(6)\nv_smfmac_f32_16x16x64_bf16 v[48:51], v[88:91], v[208:215], v4\ns_waitcnt lgkmcnt(4)\n" \
"v_smfmac_f32_16x16x64_bf16 v[48:51], v[92:95], v[232:239], v4\nv_smfmac_f32_16x16x64_bf16 v[52:55], v[88:91], v[216:223], v4\nv_smfmac_f32_16x16x64_bf16 v[52:55], v[92:95], v[240:247], v4\ns_waitcnt lgkmcnt(2)\nv_smfmac_f32_16x16x64_bf16 v[56:59], v[88:91], v[64:71], v4\nv_add_f32 v48, v48, v50\nv_smfmac_f32_16x16x64_bf16 v[56:59], v[92:95], v[248:255], v4\nv_add_f32 v52, v52, v54\n" \
"v_add_f32 v49, v49, v51\nv_smfmac_f32_16x16x64_bf16 v[60:63], v[88:91], v[224:231], v4\ns_waitcnt lgkmcnt(0)\nv_add_f32 v53, v53, v55\nv_cvt_pk_bf16_f32 v50, v48, v52\nv_smfmac_f32_16x16x64_bf16 v[60:63], v[92:95], v[72:79], v4\nv_cvt_pk_bf16_f32 v51, v49, v53\nds_write2_b32 v6, v50, v51 offset0:0 offset1:4\n" \
"v_add_f32 v56, v56, v58\nv_add_f32 v57, v57, v59\ns_nop 3\nv_add_f32 v60, v60, v62\nv_add_f32 v61, v61, v63\nv_cvt_pk_bf16_f32 v62, v56, v60\nv_cvt_pk_bf16_f32 v63, v57, v61\nds_write2_b32 v6, v62, v63 offset0:36 offset1:40\n" \
"v_mov_b32 v61, s54\ns_waitcnt lgkmcnt(0)\ns_barrier\nds_read_b128 v[80:83], v8 offset:0\nds_read_b128 v[84:87], v8 offset:576\nds_read_b128 v[88:91], v8 offset:1152\nds_read_b128 v[92:95], v8 offset:1728\nds_read_b128 v[64:67], v9 offset:0\n" \
"ds_read_b128 v[68:71], v9 offset:576\nds_read_b128 v[72:75], v9 offset:1152\nds_read_b128 v[76:79], v9 offset:1728\nds_read_b32 v60, v0 offset:53760\ns_waitcnt lgkmcnt(8)\nv_mfma_f32_16x16x32_bf16 v[52:55], v[16:19], v[80:83], 0\nv_mfma_f32_16x16x32_bf16 v[56:59], v[20:23], v[80:83], 0\ns_waitcnt lgkmcnt(7)\n" \
"v_mfma_f32_16x16x32_bf16 v[52:55], v[24:27], v[84:87], v[52:55]\nv_mfma_f32_16x16x32_bf16 v[56:59], v[28:31], v[84:87], v[56:59]\ns_waitcnt lgkmcnt(6)\nv_mfma_f32_16x16x32_bf16 v[52:55], v[32:35], v[88:91], v[52:55]\nv_mfma_f32_16x16x32_bf16 v[56:59], v[36:39], v[88:91], v[56:59]\ns_waitcnt lgkmcnt(5)\nv_mfma_f32_16x16x32_bf16 v[52:55], v[40:43], v[92:95], v[52:55]\nv_mfma_f32_16x16x32_bf16 v[56:59], v[44:47], v[92:95], v[56:59]\n" \
"s_waitcnt lgkmcnt(4)\nv_mfma_f32_16x16x32_bf16 v[48:51], v[64:67], v[80:83], 0\ns_waitcnt lgkmcnt(3)\nv_mfma_f32_16x16x32_bf16 v[48:51], v[68:71], v[84:87], v[48:51]\ns_waitcnt lgkmcnt(2)\nds_read_b128 v[80:83], v2 offset:0\nv_mfma_f32_16x16x32_bf16 v[48:51], v[72:75], v[88:91], v[48:51]\ns_waitcnt lgkmcnt(2)\n" \
"ds_read_b128 v[84:87], v2 offset:2048\nv_mfma_f32_16x16x32_bf16 v[48:51], v[76:79], v[92:95], v[48:51]\nv_max3_f32 v62, v52, v53, v54\nv_max3_f32 v63, v55, v56, v57\nv_max3_f32 v62, v62, v58, v59\nv_cndmask_b32 v52, v52, v56, s[62:63]\nv_cndmask_b32 v53, v53, v57, s[62:63]\nv_cndmask_b32 v54, v54, v58, s[62:63]\n" \
"v_cndmask_b32 v55, v55, v59, s[62:63]\nv_max_f32 v62, v62, v63\nv_max3_f32 v63, v62, v48, v49\nv_max3_f32 v62, v62, v48, v49\ns_waitcnt lgkmcnt(1)\nv_mfma_f32_16x16x32_bf16 v[16:19], a[0:3], v[80:83], 0\ns_waitcnt lgkmcnt(0)\nds_read_b128 v[88:91], v2 offset:256\n" \
"v_mfma_f32_16x16x32_bf16 v[16:19], a[4:7], v[84:87], v[16:19]\nds_read_b128 v[92:95], v2 offset:2304\nv_mfma_f32_16x16x32_bf16 v[20:23], a[8:11], v[80:83], 0\nv_permlane16_swap_b32 v62, v63\nv_mfma_f32_16x16x32_bf16 v[20:23], a[12:15], v[84:87], v[20:23]\nv_max_f32 v50, v62, v63\nv_max_f32 v62, v62, v63\nv_mfma_f32_16x16x32_bf16 v[24:27], a[16:19], v[80:83], 0\n" \
"ds_read_b128 v[72:75], v1 offset:0\nv_mfma_f32_16x16x32_bf16 v[24:27], a[20:23], v[84:87], v[24:27]\nds_read_b128 v[76:79], v1 offset:1024\nv_mfma_f32_16x16x32_bf16 v[28:31], a[24:27], v[80:83], 0\nv_permlane32_swap_b32 v62, v50\nv_mfma_f32_16x16x32_bf16 v[28:31], a[28:31], v[84:87], v[28:31]\ns_waitcnt lgkmcnt(3)\nv_max_f32 v62, v62, v50\n" \
"v_mul_f32 v62, s59, v62\nv_mfma_f32_16x16x32_bf16 v[32:35], a[0:3], v[88:91], 0\ns_waitcnt lgkmcnt(2)\nds_read_b128 v[80:83], v2 offset:4096\nv_fma_f32 v52, v52, s58, v62\nv_mfma_f32_16x16x32_bf16 v[32:35], a[4:7], v[92:95], v[32:35]\nds_read_b128 v[84:87], v2 offset:6144\nv_fma_f32 v53, v53, s58, v62\n" \
"v_mfma_f32_16x16x32_bf16 v[36:39], a[8:11], v[88:91], 0\nv_exp_f32 v52, v52\nv_mfma_f32_16x16x32_bf16 v[36:39], a[12:15], v[92:95], v[36:39]\nv_fma_f32 v54, v54, s58, v62\nv_fma_f32 v55, v55, s58, v62\nv_mfma_f32_16x16x32_bf16 v[40:43], a[16:19], v[88:91], 0\nv_exp_f32 v53, v53\nv_mfma_f32_16x16x32_bf16 v[40:43], a[20:23], v[92:95], v[40:43]\n" \
"v_exp_f32 v54, v54\nv_mfma_f32_16x16x32_bf16 v[44:47], a[24:27], v[88:91], 0\nv_add_f32 v63, v52, v53\nv_fma_f32 v48, v48, s58, v62\nv_mfma_f32_16x16x32_bf16 v[44:47], a[28:31], v[92:95], v[44:47]\ns_waitcnt lgkmcnt(1)\nv_exp_f32 v55, v55\nv_mfma_f32_16x16x32_bf16 v[16:19], a[32:35], v[80:83], v[16:19]\n" \
"s_waitcnt lgkmcnt(0)\nds_read_b128 v[88:91], v2 offset:4352\nv_add_f32 v63, v63, v54\nv_mfma_f32_16x16x32_bf16 v[16:19], a[36:39], v[84:87], v[16:19]\nds_read_b128 v[92:95], v2 offset:6400\nv_fma_f32 v49, v49, s58, v62\nv_mfma_f32_16x16x32_bf16 v[20:23], v[72:75], v[80:83], v[20:23]\nv_exp_f32 v48, v48\n" \
"v_mfma_f32_16x16x32_bf16 v[20:23], v[76:79], v[84:87], v[20:23]\nv_add_f32 v63, v63, v55\nv_add_f32 v50, v48, v63\nv_mfma_f32_16x16x32_bf16 v[24:27], a[40:43], v[80:83], v[24:27]\nds_read_b128 v[64:67], v1 offset:2048\nv_mfma_f32_16x16x32_bf16 v[24:27], a[44:47], v[84:87], v[24:27]\nds_read_b128 v[68:71], v1 offset:3072\nv_mfma_f32_16x16x32_bf16 v[28:31], a[48:51], v[80:83], v[28:31]\n" \
"v_exp_f32 v49, v49\nv_mfma_f32_16x16x32_bf16 v[28:31], a[52:55], v[84:87], v[28:31]\ns_waitcnt lgkmcnt(3)\nv_add_f32 v50, v50, v49\nv_mfma_f32_16x16x32_bf16 v[32:35], a[32:35], v[88:91], v[32:35]\ns_waitcnt lgkmcnt(2)\nds_read_b128 v[80:83], v2 offset:8192\nv_add_f32_dpp v62, v63, v50 row_ror:8 row_mask:0xf bank_mask:0xf\n" \
"v_mfma_f32_16x16x32_bf16 v[32:35], a[36:39], v[92:95], v[32:35]\nds_read_b128 v[84:87], v2 offset:10240\nv_add_f32_dpp v50, v63, v50 row_ror:8 row_mask:0xf bank_mask:0xf\nv_mfma_f32_16x16x32_bf16 v[36:39], v[72:75], v[88:91], v[36:39]\ns_nop 0\nv_permlane16_swap_b32 v62, v50\nv_mfma_f32_16x16x32_bf16 v[36:39], v[76:79], v[92:95], v[36:39]\nv_add_f32 v63, v62, v50\n" \
"v_add_f32 v50, v62, v50\nv_mfma_f32_16x16x32_bf16 v[40:43], a[40:43], v[88:91], v[40:43]\ns_nop 0\nv_permlane32_swap_b32 v63, v50\nv_mfma_f32_16x16x32_bf16 v[40:43], a[44:47], v[92:95], v[40:43]\nv_add_f32 v63, v63, v50\nv_mfma_f32_16x16x32_bf16 v[44:47], a[48:51], v[88:91], v[44:47]\nv_rcp_f32 v63, v63\n" \
"v_mfma_f32_16x16x32_bf16 v[44:47], a[52:55], v[92:95], v[44:47]\ns_waitcnt lgkmcnt(1)\nv_mul_f32 v52, v52, v63\nv_mul_f32 v53, v53, v63\nv_mfma_f32_16x16x32_bf16 v[16:19], a[56:59], v[80:83], v[16:19]\ns_waitcnt lgkmcnt(0)\nds_read_b128 v[88:91], v2 offset:8448\nv_mul_f32 v54, v54, v63\n" \
"v_mfma_f32_16x16x32_bf16 v[16:19], a[60:63], v[84:87], v[16:19]\nds_read_b128 v[92:95], v2 offset:10496\nv_mul_f32 v55, v55, v63\nv_mfma_f32_16x16x32_bf16 v[20:23], a[64:67], v[80:83], v[20:23]\nv_mul_f32 v48, v48, v63\nv_mul_f32 v49, v49, v63\nv_mfma_f32_16x16x32_bf16 v[20:23], a[68:71], v[84:87], v[20:23]\nv_cvt_pk_bf16_f32 v52, v52, v53\n" \
"v_cvt_pk_bf16_f32 v53, v54, v55\nv_mfma_f32_16x16x32_bf16 v[24:27], v[64:67], v[80:83], v[24:27]\nds_read_b128 v[72:75], v1 offset:4096\nv_cvt_pk_bf16_f32 v48, v48, v49\nv_mfma_f32_16x16x32_bf16 v[24:27], v[68:71], v[84:87], v[24:27]\nds_read_b128 v[76:79], v1 offset:5120\nv_bfi_b32 v52, s61, v52, v61\nv_mfma_f32_16x16x32_bf16 v[28:31], a[72:75], v[80:83], v[28:31]\n" \
"v_bfi_b32 v53, s61, v53, v61\nv_bfi_b32 v48, s61, v48, v61\nv_mfma_f32_16x16x32_bf16 v[28:31], a[76:79], v[84:87], v[28:31]\ns_waitcnt lgkmcnt(3)\nv_mov_b32_dpp v54, v52 row_ror:8 row_mask:0xf bank_mask:0xf\nv_mov_b32_dpp v55, v53 row_ror:8 row_mask:0xf bank_mask:0xf\nv_mfma_f32_16x16x32_bf16 v[32:35], a[56:59], v[88:91], v[32:35]\ns_waitcnt lgkmcnt(2)\n" \
"ds_read_b128 v[80:83], v2 offset:12288\nv_mfma_f32_16x16x32_bf16 v[32:35], a[60:63], v[92:95], v[32:35]\nds_read_b128 v[84:87], v2 offset:14336\nv_mfma_f32_16x16x32_bf16 v[36:39], a[64:67], v[88:91], v[36:39]\nbuffer_store_dwordx4 v[52:55], v10, s[36:39], 0 offen sc1\nv_mfma_f32_16x16x32_bf16 v[36:39], a[68:71], v[92:95], v[36:39]\nv_mov_b32 v49, v48\nv_mfma_f32_16x16x32_bf16 v[40:43], v[64:67], v[88:91], v[40:43]\n" \
"s_nop 0\nv_permlane32_swap_b32 v48, v49\nv_mfma_f32_16x16x32_bf16 v[40:43], v[68:71], v[92:95], v[40:43]\ns_memrealtime s[86:87]\nbuffer_store_dwordx2 v[48:49], v11, s[36:39], 0 offen sc1\nv_mfma_f32_16x16x32_bf16 v[44:47], a[72:75], v[88:91], v[44:47]\nv_cmp_ne_u32 vcc, 0, v60\nv_mfma_f32_16x16x32_bf16 v[44:47], a[76:79], v[92:95], v[44:47]\n" \
"s_cbranch_vccnz .Lqk_pj%=\ns_cmp_lt_i32 s40, 63\ns_waitcnt lgkmcnt(0)\ns_cbranch_scc1 .Lqk_pwait%=\ns_branch .Lqk_end%=\n.Lqk_re0%=:\nv_max_u32 v17, v80, v82\nv_cmp_eq_u32 vcc, -1, v17\n" \
"s_cbranch_vccz .Lqk_sk1619%=\ns_mov_b64 exec, vcc\nbuffer_load_dwordx4 v[80:83], v3, s[36:39], s42 offen offset:0 sc1\ns_mov_b64 exec, -1\n.Lqk_sk1619%=:\nv_max_u32 v17, v84, v86\nv_cmp_eq_u32 vcc, -1, v17\ns_cbranch_vccz .Lqk_sk1626%=\n" \
"s_mov_b64 exec, vcc\nbuffer_load_dwordx4 v[84:87], v3, s[36:39], s42 offen offset:1024 sc1\ns_mov_b64 exec, -1\n.Lqk_sk1626%=:\nv_max_u32 v17, v88, v90\nv_cmp_eq_u32 vcc, -1, v17\ns_cbranch_vccz .Lqk_sk1633%=\ns_mov_b64 exec, vcc\n" \
"buffer_load_dwordx4 v[88:91], v3, s[36:39], s42 offen offset:2048 sc1\ns_mov_b64 exec, -1\n.Lqk_sk1633%=:\nv_max_u32 v17, v92, v94\nv_cmp_eq_u32 vcc, -1, v17\ns_cbranch_vccz .Lqk_sk1640%=\ns_mov_b64 exec, vcc\nbuffer_load_dwordx4 v[92:95], v3, s[36:39], s42 offen offset:3072 sc1\n" \
"s_mov_b64 exec, -1\n.Lqk_sk1640%=:\nv_mov_b64 v[48:49], 0\nv_mov_b64 v[50:51], 0\nv_mov_b64 v[52:53], 0\nv_mov_b64 v[54:55], 0\nv_mov_b64 v[56:57], 0\nv_mov_b64 v[58:59], 0\n" \
"v_mov_b64 v[60:61], 0\nv_mov_b64 v[62:63], 0\ns_add_u32 s43, s43, 1\ns_cmp_lt_u32 s43, 262144\ns_cbranch_scc1 .Lqk_poll0%=\ns_branch .Lqk_abort%=\n.p2align 6\n.Lqk_c1%=:\n" \
"s_waitcnt lgkmcnt(3)\nv_mfma_f32_16x16x32_bf16 v[32:35], v[232:235], v[56:59], v[32:35]\ns_waitcnt lgkmcnt(2)\nv_mov_b64 v[48:49], 0\nv_mov_b64 v[50:51], 0\nv_mfma_f32_16x16x32_bf16 v[32:35], v[236:239], v[60:63], v[32:35]\nds_read_b128 v[64:67], v1 offset:6144\nv_mfma_f32_16x16x32_bf16 v[36:39], v[240:243], v[56:59], v[36:39]\n" \
"ds_read_b128 v[68:71], v1 offset:7168\nv_mfma_f32_16x16x32_bf16 v[36:39], v[244:247], v[60:63], v[36:39]\ns_bfe_i32 s54, s40, 0x10001\nv_mov_b64 v[52:53], 0\nv_mov_b64 v[54:55], 0\nv_mfma_f32_16x16x32_bf16 v[40:43], v[248:251], v[56:59], v[40:43]\nv_mfma_f32_16x16x32_bf16 v[40:43], v[252:255], v[60:63], v[40:43]\nv_cvt_pk_bf16_f32 v36, v32, v36\n" \
"v_cvt_pk_bf16_f32 v37, v33, v37\nv_mfma_f32_16x16x32_bf16 v[44:47], v[72:75], v[56:59], v[44:47]\nv_cvt_pk_bf16_f32 v38, v34, v38\nv_cvt_pk_bf16_f32 v39, v35, v39\nv_mfma_f32_16x16x32_bf16 v[44:47], v[76:79], v[60:63], v[44:47]\nds_read_b128 v[72:75], v1 offset:8192\nds_read_b128 v[76:79], v1 offset:9216\nv_mov_b64 v[56:57], 0\n" \
"v_mov_b64 v[58:59], 0\nv_mov_b64 v[60:61], 0\nv_mov_b64 v[62:63], 0\n.Lqk_poll1%=:\ns_waitcnt vmcnt(0)\nv_smfmac_f32_16x16x64_bf16 v[48:51], v[80:83], a[104:111], v4\nds_write_b128 v0, v[80:83] offset:20480\nv_max3_u32 v16, v80, v82, v84\n" \
"v_smfmac_f32_16x16x64_bf16 v[48:51], v[84:87], a[136:143], v4\nv_max3_u32 v16, v16, v86, v88\nv_smfmac_f32_16x16x64_bf16 v[52:55], v[80:83], a[112:119], v4\ns_waitcnt lgkmcnt(3)\nds_write_b128 v0, v[84:87] offset:21504\nv_max3_u32 v16, v16, v90, v92\nv_max_u32 v16, v16, v94\nv_smfmac_f32_16x16x64_bf16 v[52:55], v[84:87], v[64:71], v4\n" \
"v_cmp_eq_u32 vcc, -1, v16\nv_smfmac_f32_16x16x64_bf16 v[56:59], v[80:83], a[120:127], v4\nds_write_b128 v0, v[88:91] offset:22528\ns_cbranch_vccnz .Lqk_re1%=\nv_smfmac_f32_16x16x64_bf16 v[56:59], v[84:87], a[144:151], v4\nv_smfmac_f32_16x16x64_bf16 v[60:63], v[80:83], a[128:135], v4\nds_write_b128 v0, v[92:95] offset:23552\nv_smfmac_f32_16x16x64_bf16 v[60:63], v[84:87], a[152:159], v4\n" \
"v_smfmac_f32_16x16x64_bf16 v[48:51], v[88:91], a[160:167], v4\nv_smfmac_f32_16x16x64_bf16 v[48:51], v[92:95], a[184:191], v4\ns_waitcnt lgkmcnt(0)\n.Lqk_jn1%=:\ns_barrier\nds_read_b128 v[80:83], v0 offset:24576\nds_read_b128 v[64:67], v1 offset:10240\nds_read_b128 v[68:71], v1 offset:11264\n" \
"v_smfmac_f32_16x16x64_bf16 v[52:55], v[88:91], a[168:175], v4\nv_smfmac_f32_16x16x64_bf16 v[52:55], v[92:95], a[192:199], v4\nds_read_b128 v[84:87], v0 offset:25600\nv_smfmac_f32_16x16x64_bf16 v[56:59], v[88:91], v[72:79], v4\nds_read_b128 v[72:75], v1 offset:12288\nds_read_b128 v[76:79], v1 offset:13312\nv_smfmac_f32_16x16x64_bf16 v[56:59], v[92:95], a[200:207], v4\nv_smfmac_f32_16x16x64_bf16 v[60:63], v[88:91], a[176:183], v4\n" \
"s_waitcnt lgkmcnt(3)\nds_read_b128 v[88:91], v0 offset:26624\nv_smfmac_f32_16x16x64_bf16 v[60:63], v[92:95], v[64:71], v4\nds_read_b128 v[92:95], v0 offset:27648\nds_read_b128 v[64:67], v1 offset:14336\nds_read_b128 v[68:71], v1 offset:15360\nv_smfmac_f32_16x16x64_bf16 v[48:51], v[80:83], a[208:215], v4\ns_waitcnt lgkmcnt(6)\n" \
"ds_write_b128 v5, v[36:39] offset:256\nv_cvt_pk_bf16_f32 v44, v40, v44\nv_cvt_pk_bf16_f32 v45, v41, v45\nv_cvt_pk_bf16_f32 v46, v42, v46\nv_cvt_pk_bf16_f32 v47, v43, v47\nv_smfmac_f32_16x16x64_bf16 v[48:51], v[84:87], a[240:247], v4\nv_smfmac_f32_16x16x64_bf16 v[52:55], v[80:83], a[216:223], v4\ns_waitcnt lgkmcnt(5)\n" \
"v_smfmac_f32_16x16x64_bf16 v[52:55], v[84:87], v[72:79], v4\nds_read_b128 v[72:75], v1 offset:16384\nds_read_b128 v[76:79], v1 offset:17408\nv_smfmac_f32_16x16x64_bf16 v[56:59], v[80:83], a[224:231], v4\nv_smfmac_f32_16x16x64_bf16 v[56:59], v[84:87], a[248:255], v4\nv_smfmac_f32_16x16x64_bf16 v[60:63], v[80:83], a[232:239], v4\nds_read_b128 v[80:83], v0 offset:28672\nv_smfmac_f32_16x16x64_bf16 v[60:63], v[84:87], v[96:103], v4\n" \
"s_waitcnt lgkmcnt(7)\nds_read_b128 v[84:87], v0 offset:29696\nv_smfmac_f32_16x16x64_bf16 v[48:51], v[88:91], v[104:111], v4\ns_waitcnt lgkmcnt(7)\nds_write_b128 v5, v[44:47] offset:768\nv_smfmac_f32_16x16x64_bf16 v[48:51], v[92:95], v[128:135], v4\nv_smfmac_f32_16x16x64_bf16 v[52:55], v[88:91], v[112:119], v4\nv_smfmac_f32_16x16x64_bf16 v[52:55], v[92:95], v[136:143], v4\n" \
"s_waitcnt lgkmcnt(6)\nv_smfmac_f32_16x16x64_bf16 v[56:59], v[88:91], v[64:71], v4\nds_read_b128 v[64:67], v1 offset:18432\nds_read_b128 v[68:71], v1 offset:19456\nv_smfmac_f32_16x16x64_bf16 v[56:59], v[92:95], v[144:151], v4\ns_waitcnt lgkmcnt(2)\ns_barrier\nv_smfmac_f32_16x16x64_bf16 v[60:63], v[88:91], v[120:127], v4\n" \
"ds_read_b128 v[88:91], v0 offset:30720\nds_read_b128 v[16:19], v7 offset:0\nv_smfmac_f32_16x16x64_bf16 v[60:63], v[92:95], v[72:79], v4\nds_read_b128 v[92:95], v0 offset:31744\nds_read_b128 v[72:75], v1 offset:20480\nds_read_b128 v[76:79], v1 offset:21504\nv_smfmac_f32_16x16x64_bf16 v[48:51], v[80:83], v[152:159], v4\nds_read_b128 v[20:23], v7 offset:2048\n" \
"v_smfmac_f32_16x16x64_bf16 v[48:51], v[84:87], v[184:191], v4\nv_smfmac_f32_16x16x64_bf16 v[52:55], v[80:83], v[160:167], v4\ns_waitcnt lgkmcnt(6)\nds_read_b128 v[24:27], v7 offset:4096\nv_smfmac_f32_16x16x64_bf16 v[52:55], v[84:87], v[64:71], v4\nds_read_b128 v[64:67], v1 offset:22528\nds_read_b128 v[68:71], v1 offset:23552\nv_smfmac_f32_16x16x64_bf16 v[56:59], v[80:83], v[168:175], v4\n" \
"ds_read_b128 v[28:31], v7 offset:6144\nv_smfmac_f32_16x16x64_bf16 v[56:59], v[84:87], v[192:199], v4\nv_smfmac_f32_16x16x64_bf16 v[60:63], v[80:83], v[176:183], v4\nds_read_b128 v[80:83], v0 offset:16384\nds_read_b128 v[32:35], v7 offset:8192\nv_smfmac_f32_16x16x64_bf16 v[60:63], v[84:87], v[200:207], v4\ns_waitcnt lgkmcnt(11)\nds_read_b128 v[84:87], v0 offset:17408\n" \
"v_smfmac_f32_16x16x64_bf16 v[48:51], v[88:91], v[208:215], v4\ns_waitcnt lgkmcnt(10)\nds_read_b128 v[36:39], v7 offset:10240\nv_smfmac_f32_16x16x64_bf16 v[48:51], v[92:95], v[232:239], v4\nv_smfmac_f32_16x16x64_bf16 v[52:55], v[88:91], v[216:223], v4\nds_read_b128 v[40:43], v7 offset:12288\nv_smfmac_f32_16x16x64_bf16 v[52:55], v[92:95], v[240:247], v4\ns_waitcnt lgkmcnt(10)\n" \
"v_smfmac_f32_16x16x64_bf16 v[56:59], v[88:91], v[72:79], v4\nds_read_b128 v[72:75], v1 offset:0\nds_read_b128 v[76:79], v1 offset:1024\nv_smfmac_f32_16x16x64_bf16 v[56:59], v[92:95], v[248:255], v4\nv_smfmac_f32_16x16x64_bf16 v[60:63], v[88:91], v[224:231], v4\ns_waitcnt lgkmcnt(8)\nds_read_b128 v[88:91], v0 offset:18432\nds_read_b128 v[44:47], v7 offset:14336\n" \
"v_smfmac_f32_16x16x64_bf16 v[60:63], v[92:95], v[64:71], v4\ns_waitcnt lgkmcnt(8)\nds_read_b128 v[92:95], v0 offset:19456\nds_read_b128 v[64:67], v1 offset:2048\nds_read_b128 v[68:71], v1 offset:3072\nv_smfmac_f32_16x16x64_bf16 v[48:51], v[80:83], a[0:7], v4\ns_waitcnt lgkmcnt(9)\nv_smfmac_f32_16x16x64_bf16 v[48:51], v[84:87], a[32:39], v4\n" \
"v_smfmac_f32_16x16x64_bf16 v[52:55], v[80:83], a[8:15], v4\ns_waitcnt lgkmcnt(5)\nv_smfmac_f32_16x16x64_bf16 v[52:55], v[84:87], v[72:79], v4\nds_read_b128 v[72:75], v1 offset:4096\nds_read_b128 v[76:79], v1 offset:5120\nv_smfmac_f32_16x16x64_bf16 v[56:59], v[80:83], a[16:23], v4\nv_smfmac_f32_16x16x64_bf16 v[56:59], v[84:87], a[40:47], v4\nv_smfmac_f32_16x16x64_bf16 v[60:63], v[80:83], a[24:31], v4\n" \
"v_smfmac_f32_16x16x64_bf16 v[60:63], v[84:87], a[48:55], v4\ns_waitcnt lgkmcnt(6)\nv_smfmac_f32_16x16x64_bf16 v[48:51], v[88:91], a[56:63], v4\ns_waitcnt lgkmcnt(4)\nv_smfmac_f32_16x16x64_bf16 v[48:51], v[92:95], a[80:87], v4\nv_smfmac_f32_16x16x64_bf16 v[52:55], v[88:91], a[64:71], v4\nv_smfmac_f32_16x16x64_bf16 v[52:55], v[92:95], a[88:95], v4\ns_waitcnt lgkmcnt(2)\n" \
"v_smfmac_f32_16x16x64_bf16 v[56:59], v[88:91], v[64:71], v4\nv_add_f32 v48, v48, v50\nv_smfmac_f32_16x16x64_bf16 v[56:59], v[92:95], a[96:103], v4\nv_add_f32 v52, v52, v54\nv_add_f32 v49, v49, v51\nv_smfmac_f32_16x16x64_bf16 v[60:63], v[88:91], a[72:79], v4\ns_waitcnt lgkmcnt(0)\nv_add_f32 v53, v53, v55\n" \
"v_cvt_pk_bf16_f32 v50, v48, v52\nv_smfmac_f32_16x16x64_bf16 v[60:63], v[92:95], v[72:79], v4\nv_cvt_pk_bf16_f32 v51, v49, v53\nds_write2_b32 v6, v50, v51 offset0:0 offset1:4\nv_add_f32 v56, v56, v58\nv_add_f32 v57, v57, v59\ns_nop 3\nv_add_f32 v60, v60, v62\n" \
"v_add_f32 v61, v61, v63\nv_cvt_pk_bf16_f32 v62, v56, v60\nv_cvt_pk_bf16_f32 v63, v57, v61\nds_write2_b32 v6, v62, v63 offset0:36 offset1:40\nv_mov_b32 v61, s54\nv_mfma_f32_16x16x32_bf16 v[52:55], v[32:35], v[16:19], v[12:15]\nv_mfma_f32_16x16x32_bf16 v[52:55], v[36:39], v[20:23], v[52:55]\nv_mfma_f32_16x16x32_bf16 v[52:55], v[40:43], v[24:27], v[52:55]\n" \
"v_mfma_f32_16x16x32_bf16 v[52:55], v[44:47], v[28:31], v[52:55]\ns_waitcnt lgkmcnt(0)\ns_barrier\ns_nop 10\nds_read_b128 v[32:35], v9 offset:0\nds_read_b128 v[36:39], v9 offset:576\nds_read_b128 v[40:43], v9 offset:1152\nds_read_b128 v[44:47], v9 offset:1728\n" \
"ds_read_b32 v60, v0 offset:53760\nds_read_b128 v[80:83], v2 offset:0\nds_read_b128 v[84:87], v2 offset:2048\ns_waitcnt lgkmcnt(6)\nv_mfma_f32_16x16x32_bf16 v[48:51], v[32:35], v[16:19], 0\ns_waitcnt lgkmcnt(5)\nv_mfma_f32_16x16x32_bf16 v[48:51], v[36:39], v[20:23], v[48:51]\ns_waitcnt lgkmcnt(4)\n" \
"v_mfma_f32_16x16x32_bf16 v[48:51], v[40:43], v[24:27], v[48:51]\ns_waitcnt lgkmcnt(3)\nv_mfma_f32_16x16x32_bf16 v[48:51], v[44:47], v[28:31], v[48:51]\ns_waitcnt lgkmcnt(1)\nv_mfma_f32_16x16x32_bf16 v[16:19], a[0:3], v[80:83], 0\ns_waitcnt lgkmcnt(0)\nds_read_b128 v[88:91], v2 offset:256\nv_max3_f32 v62, v52, v53, v54\n" \
"v_max_f32 v62, v62, v55\nv_mfma_f32_16x16x32_bf16 v[16:19], a[4:7], v[84:87], v[16:19]\nds_read_b128 v[92:95], v2 offset:2304\nv_max3_f32 v63, v62, v48, v49\nv_max3_f32 v62, v62, v48, v49\nv_mfma_f32_16x16x32_bf16 v[20:23], a[8:11], v[80:83], 0\ns_nop 0\nv_permlane16_swap_b32 v62, v63\n" \
"v_max_f32 v50, v62, v63\nv_mfma_f32_16x16x32_bf16 v[20:23], a[12:15], v[84:87], v[20:23]\nv_max_f32 v62, v62, v63\nv_mfma_f32_16x16x32_bf16 v[24:27], a[16:19], v[80:83], 0\nds_read_b128 v[72:75], v1 offset:0\nv_permlane32_swap_b32 v62, v50\nv_mfma_f32_16x16x32_bf16 v[24:27], a[20:23], v[84:87], v[24:27]\nds_read_b128 v[76:79], v1 offset:1024\n" \
"v_max_f32 v62, v62, v50\nv_mul_f32 v62, s59, v62\nv_mfma_f32_16x16x32_bf16 v[28:31], a[24:27], v[80:83], 0\nv_fma_f32 v52, v52, s58, v62\nv_fma_f32 v53, v53, s58, v62\nv_fma_f32 v54, v54, s58, v62\nv_mfma_f32_16x16x32_bf16 v[28:31], a[28:31], v[84:87], v[28:31]\ns_waitcnt lgkmcnt(3)\n" \
"v_exp_f32 v52, v52\nv_fma_f32 v55, v55, s58, v62\nv_mfma_f32_16x16x32_bf16 v[32:35], a[0:3], v[88:91], 0\ns_waitcnt lgkmcnt(2)\nds_read_b128 v[80:83], v2 offset:4096\nv_exp_f32 v53, v53\nv_mfma_f32_16x16x32_bf16 v[32:35], a[4:7], v[92:95], v[32:35]\nds_read_b128 v[84:87], v2 offset:6144\n" \
"v_exp_f32 v54, v54\nv_mfma_f32_16x16x32_bf16 v[36:39], a[8:11], v[88:91], 0\nv_add_f32 v63, v52, v53\nv_fma_f32 v48, v48, s58, v62\nv_add_f32 v63, v63, v54\nv_mfma_f32_16x16x32_bf16 v[36:39], a[12:15], v[92:95], v[36:39]\nv_exp_f32 v55, v55\nv_fma_f32 v49, v49, s58, v62\n" \
"v_mfma_f32_16x16x32_bf16 v[40:43], a[16:19], v[88:91], 0\nv_exp_f32 v48, v48\nv_add_f32 v63, v63, v55\nv_mfma_f32_16x16x32_bf16 v[40:43], a[20:23], v[92:95], v[40:43]\nv_exp_f32 v49, v49\nv_add_f32 v63, v63, v48\nv_mfma_f32_16x16x32_bf16 v[44:47], a[24:27], v[88:91], 0\nv_add_f32 v62, v63, v49\n" \
"v_add_f32 v50, v63, v49\nv_mfma_f32_16x16x32_bf16 v[44:47], a[28:31], v[92:95], v[44:47]\ns_waitcnt lgkmcnt(1)\nv_permlane16_swap_b32 v62, v50\nv_add_f32 v63, v62, v50\nv_mfma_f32_16x16x32_bf16 v[16:19], a[32:35], v[80:83], v[16:19]\ns_waitcnt lgkmcnt(0)\nds_read_b128 v[88:91], v2 offset:4352\n" \
"v_add_f32 v50, v62, v50\nv_mfma_f32_16x16x32_bf16 v[16:19], a[36:39], v[84:87], v[16:19]\nds_read_b128 v[92:95], v2 offset:6400\nv_permlane32_swap_b32 v63, v50\nv_mfma_f32_16x16x32_bf16 v[20:23], v[72:75], v[80:83], v[20:23]\nv_add_f32 v63, v63, v50\nv_rcp_f32 v63, v63\nv_mfma_f32_16x16x32_bf16 v[20:23], v[76:79], v[84:87], v[20:23]\n" \
"v_mul_f32 v52, v52, v63\nv_mul_f32 v53, v53, v63\nv_mul_f32 v54, v54, v63\nv_mfma_f32_16x16x32_bf16 v[24:27], a[40:43], v[80:83], v[24:27]\nds_read_b128 v[64:67], v1 offset:2048\nv_mul_f32 v55, v55, v63\nv_mul_f32 v48, v48, v63\nv_mfma_f32_16x16x32_bf16 v[24:27], a[44:47], v[84:87], v[24:27]\n" \
"ds_read_b128 v[68:71], v1 offset:3072\nv_mul_f32 v49, v49, v63\nv_cvt_pk_bf16_f32 v52, v52, v53\nv_mfma_f32_16x16x32_bf16 v[28:31], a[48:51], v[80:83], v[28:31]\nv_cvt_pk_bf16_f32 v53, v54, v55\nv_cvt_pk_bf16_f32 v48, v48, v49\nv_bfi_b32 v52, s61, v52, v61\nv_mfma_f32_16x16x32_bf16 v[28:31], a[52:55], v[84:87], v[28:31]\n" \
"s_waitcnt lgkmcnt(3)\nv_bfi_b32 v53, s61, v53, v61\nv_bfi_b32 v48, s61, v48, v61\nv_mfma_f32_16x16x32_bf16 v[32:35], a[32:35], v[88:91], v[32:35]\ns_waitcnt lgkmcnt(2)\nds_read_b128 v[80:83], v2 offset:8192\nv_mfma_f32_16x16x32_bf16 v[32:35], a[36:39], v[92:95], v[32:35]\nds_read_b128 v[84:87], v2 offset:10240\n" \
"v_mfma_f32_16x16x32_bf16 v[36:39], v[72:75], v[88:91], v[36:39]\nbuffer_store_dwordx2 v[52:53], v10, s[36:39], s55 offen sc1\nv_mov_b32 v49, v48\nv_mfma_f32_16x16x32_bf16 v[36:39], v[76:79], v[92:95], v[36:39]\ns_nop 0\nv_permlane16_swap_b32 v48, v49\nv_mov_b32 v50, v48\nv_mfma_f32_16x16x32_bf16 v[40:43], a[40:43], v[88:91], v[40:43]\n" \
"v_mov_b32 v51, v49\nv_permlane32_swap_b32 v48, v50\nv_mfma_f32_16x16x32_bf16 v[40:43], a[44:47], v[92:95], v[40:43]\nv_permlane32_swap_b32 v49, v51\nv_mfma_f32_16x16x32_bf16 v[44:47], a[48:51], v[88:91], v[44:47]\nbuffer_store_dwordx4 v[48:51], v11, s[36:39], s55 offen sc1\nv_cmp_ne_u32 vcc, 0, v60\nv_mfma_f32_16x16x32_bf16 v[44:47], a[52:55], v[92:95], v[44:47]\n" \
"s_waitcnt lgkmcnt(1)\nv_mfma_f32_16x16x32_bf16 v[16:19], a[56:59], v[80:83], v[16:19]\ns_waitcnt lgkmcnt(0)\nds_read_b128 v[88:91], v2 offset:8448\nv_mfma_f32_16x16x32_bf16 v[16:19], a[60:63], v[84:87], v[16:19]\nds_read_b128 v[92:95], v2 offset:10496\nv_mfma_f32_16x16x32_bf16 v[20:23], a[64:67], v[80:83], v[20:23]\nv_mfma_f32_16x16x32_bf16 v[20:23], a[68:71], v[84:87], v[20:23]\n" \
"v_mfma_f32_16x16x32_bf16 v[24:27], v[64:67], v[80:83], v[24:27]\nds_read_b128 v[72:75], v1 offset:4096\nv_mfma_f32_16x16x32_bf16 v[24:27], v[68:71], v[84:87], v[24:27]\nds_read_b128 v[76:79], v1 offset:5120\nv_mfma_f32_16x16x32_bf16 v[28:31], a[72:75], v[80:83], v[28:31]\nv_mfma_f32_16x16x32_bf16 v[28:31], a[76:79], v[84:87], v[28:31]\ns_waitcnt lgkmcnt(3)\nv_mfma_f32_16x16x32_bf16 v[32:35], a[56:59], v[88:91], v[32:35]\n" \
"s_waitcnt lgkmcnt(2)\nds_read_b128 v[80:83], v2 offset:12288\nv_mfma_f32_16x16x32_bf16 v[32:35], a[60:63], v[92:95], v[32:35]\nds_read_b128 v[84:87], v2 offset:14336\nv_mfma_f32_16x16x32_bf16 v[36:39], a[64:67], v[88:91], v[36:39]\nv_mfma_f32_16x16x32_bf16 v[36:39], a[68:71], v[92:95], v[36:39]\nv_mfma_f32_16x16x32_bf16 v[40:43], v[64:67], v[88:91], v[40:43]\nv_mfma_f32_16x16x32_bf16 v[40:43], v[68:71], v[92:95], v[40:43]\n" \
"v_mfma_f32_16x16x32_bf16 v[44:47], a[72:75], v[88:91], v[44:47]\nv_mfma_f32_16x16x32_bf16 v[44:47], a[76:79], v[92:95], v[44:47]\ns_cbranch_vccnz .Lqk_pj%=\ns_cmp_lt_i32 s40, 63\ns_waitcnt lgkmcnt(0)\ns_cbranch_scc1 .Lqk_pwait%=\ns_branch .Lqk_end%=\n.Lqk_re1%=:\n" \
"v_max_u32 v17, v80, v82\nv_cmp_eq_u32 vcc, -1, v17\ns_cbranch_vccz .Lqk_sk2009%=\ns_mov_b64 exec, vcc\nbuffer_load_dwordx4 v[80:83], v3, s[36:39], s42 offen offset:0 sc1\ns_mov_b64 exec, -1\n.Lqk_sk2009%=:\nv_max_u32 v17, v84, v86\n" \
"v_cmp_eq_u32 vcc, -1, v17\ns_cbranch_vccz .Lqk_sk2016%=\ns_mov_b64 exec, vcc\nbuffer_load_dwordx4 v[84:87], v3, s[36:39], s42 offen offset:1024 sc1\ns_mov_b64 exec, -1\n.Lqk_sk2016%=:\nv_max_u32 v17, v88, v90\nv_cmp_eq_u32 vcc, -1, v17\n" \
"s_cbranch_vccz .Lqk_sk2023%=\ns_mov_b64 exec, vcc\nbuffer_load_dwordx4 v[88:91], v3, s[36:39], s42 offen offset:2048 sc1\ns_mov_b64 exec, -1\n.Lqk_sk2023%=:\nv_max_u32 v17, v92, v94\nv_cmp_eq_u32 vcc, -1, v17\ns_cbranch_vccz .Lqk_sk2030%=\n" \
"s_mov_b64 exec, vcc\nbuffer_load_dwordx4 v[92:95], v3, s[36:39], s42 offen offset:3072 sc1\ns_mov_b64 exec, -1\n.Lqk_sk2030%=:\nv_mov_b64 v[48:49], 0\nv_mov_b64 v[50:51], 0\nv_mov_b64 v[52:53], 0\nv_mov_b64 v[54:55], 0\n" \
"v_mov_b64 v[56:57], 0\nv_mov_b64 v[58:59], 0\nv_mov_b64 v[60:61], 0\nv_mov_b64 v[62:63], 0\ns_add_u32 s43, s43, 1\ns_cmp_lt_u32 s43, 262144\ns_cbranch_scc1 .Lqk_poll1%=\ns_branch .Lqk_abort%=\n" \
".p2align 6\n.Lqk_c2%=:\ns_waitcnt lgkmcnt(3)\nv_mfma_f32_16x16x32_bf16 v[32:35], v[232:235], v[56:59], v[32:35]\ns_waitcnt lgkmcnt(2)\nv_mov_b64 v[48:49], 0\nv_mov_b64 v[50:51], 0\nv_mfma_f32_16x16x32_bf16 v[32:35], v[236:239], v[60:63], v[32:35]\n" \
"ds_read_b128 v[64:67], v1 offset:12288\nv_mfma_f32_16x16x32_bf16 v[36:39], v[240:243], v[56:59], v[36:39]\nds_read_b128 v[68:71], v1 offset:13312\nv_mfma_f32_16x16x32_bf16 v[36:39], v[244:247], v[60:63], v[36:39]\ns_bfe_i32 s54, s40, 0x10001\nv_mov_b64 v[52:53], 0\nv_mov_b64 v[54:55], 0\nv_mfma_f32_16x16x32_bf16 v[40:43], v[248:251], v[56:59], v[40:43]\n" \
"v_mfma_f32_16x16x32_bf16 v[40:43], v[252:255], v[60:63], v[40:43]\nv_cvt_pk_bf16_f32 v36, v32, v36\nv_cvt_pk_bf16_f32 v37, v33, v37\nv_mfma_f32_16x16x32_bf16 v[44:47], v[72:75], v[56:59], v[44:47]\nv_cvt_pk_bf16_f32 v38, v34, v38\nv_cvt_pk_bf16_f32 v39, v35, v39\nv_mfma_f32_16x16x32_bf16 v[44:47], v[76:79], v[60:63], v[44:47]\nds_read_b128 v[72:75], v1 offset:14336\n" \
"ds_read_b128 v[76:79], v1 offset:15360\nv_mov_b64 v[56:57], 0\nv_mov_b64 v[58:59], 0\nv_mov_b64 v[60:61], 0\nv_mov_b64 v[62:63], 0\n.Lqk_poll2%=:\ns_waitcnt vmcnt(0)\nv_smfmac_f32_16x16x64_bf16 v[48:51], v[80:83], a[208:215], v4\n" \
"ds_write_b128 v0, v[80:83] offset:24576\nv_max3_u32 v16, v80, v82, v84\nv_smfmac_f32_16x16x64_bf16 v[48:51], v[84:87], a[240:247], v4\nv_max3_u32 v16, v16, v86, v88\nv_smfmac_f32_16x16x64_bf16 v[52:55], v[80:83], a[216:223], v4\ns_waitcnt lgkmcnt(3)\nds_write_b128 v0, v[84:87] offset:25600\nv_max3_u32 v16, v16, v90, v92\n" \
"v_max_u32 v16, v16, v94\nv_smfmac_f32_16x16x64_bf16 v[52:55], v[84:87], v[64:71], v4\nv_cmp_eq_u32 vcc, -1, v16\nv_smfmac_f32_16x16x64_bf16 v[56:59], v[80:83], a[224:231], v4\nds_write_b128 v0, v[88:91] offset:26624\ns_cbranch_vccnz .Lqk_re2%=\nv_smfmac_f32_16x16x64_bf16 v[56:59], v[84:87], a[248:255], v4\nv_smfmac_f32_16x16x64_bf16 v[60:63], v[80:83], a[232:239], v4\n" \
"ds_write_b128 v0, v[92:95] offset:27648\nv_smfmac_f32_16x16x64_bf16 v[60:63], v[84:87], v[96:103], v4\nv_smfmac_f32_16x16x64_bf16 v[48:51], v[88:91], v[104:111], v4\nv_smfmac_f32_16x16x64_bf16 v[48:51], v[92:95], v[128:135], v4\ns_waitcnt lgkmcnt(0)\n.Lqk_jn2%=:\ns_barrier\nds_read_b128 v[80:83], v0 offset:28672\n" \
"ds_read_b128 v[64:67], v1 offset:16384\nds_read_b128 v[68:71], v1 offset:17408\nv_smfmac_f32_16x16x64_bf16 v[52:55], v[88:91], v[112:119], v4\nv_smfmac_f32_16x16x64_bf16 v[52:55], v[92:95], v[136:143], v4\nds_read_b128 v[84:87], v0 offset:29696\nv_smfmac_f32_16x16x64_bf16 v[56:59], v[88:91], v[72:79], v4\nds_read_b128 v[72:75], v1 offset:18432\nds_read_b128 v[76:79], v1 offset:19456\n" \
"v_smfmac_f32_16x16x64_bf16 v[56:59], v[92:95], v[144:151], v4\nv_smfmac_f32_16x16x64_bf16 v[60:63], v[88:91], v[120:127], v4\ns_waitcnt lgkmcnt(3)\nds_read_b128 v[88:91], v0 offset:30720\nv_smfmac_f32_16x16x64_bf16 v[60:63], v[92:95], v[64:71], v4\nds_read_b128 v[92:95], v0 offset:31744\nds_read_b128 v[64:67], v1 offset:20480\nds_read_b128 v[68:71], v1 offset:21504\n" \
"v_smfmac_f32_16x16x64_bf16 v[48:51], v[80:83], v[152:159], v4\ns_waitcnt lgkmcnt(6)\nds_write_b128 v5, v[36:39] offset:256\nv_cvt_pk_bf16_f32 v44, v40, v44\nv_cvt_pk_bf16_f32 v45, v41, v45\nv_cvt_pk_bf16_f32 v46, v42, v46\nv_cvt_pk_bf16_f32 v47, v43, v47\nv_smfmac_f32_16x16x64_bf16 v[48:51], v[84:87], v[184:191], v4\n" \
"v_smfmac_f32_16x16x64_bf16 v[52:55], v[80:83], v[160:167], v4\ns_waitcnt lgkmcnt(5)\nv_smfmac_f32_16x16x64_bf16 v[52:55], v[84:87], v[72:79], v4\nds_read_b128 v[72:75], v1 offset:22528\nds_read_b128 v[76:79], v1 offset:23552\nv_smfmac_f32_16x16x64_bf16 v[56:59], v[80:83], v[168:175], v4\nv_smfmac_f32_16x16x64_bf16 v[56:59], v[84:87], v[192:199], v4\nv_smfmac_f32_16x16x64_bf16 v[60:63], v[80:83], v[176:183], v4\n" \
"ds_read_b128 v[80:83], v0 offset:16384\nv_smfmac_f32_16x16x64_bf16 v[60:63], v[84:87], v[200:207], v4\ns_waitcnt lgkmcnt(7)\nds_read_b128 v[84:87], v0 offset:17408\nv_smfmac_f32_16x16x64_bf16 v[48:51], v[88:91], v[208:215], v4\ns_waitcnt lgkmcnt(7)\nds_write_b128 v5, v[44:47] offset:768\nv_smfmac_f32_16x16x64_bf16 v[48:51], v[92:95], v[232:239], v4\n" \
"v_smfmac_f32_16x16x64_bf16 v[52:55], v[88:91], v[216:223], v4\nv_smfmac_f32_16x16x64_bf16 v[52:55], v[92:95], v[240:247], v4\ns_waitcnt lgkmcnt(6)\nv_smfmac_f32_16x16x64_bf16 v[56:59], v[88:91], v[64:71], v4\nds_read_b128 v[64:67], v1 offset:0\nds_read_b128 v[68:71], v1 offset:1024\nv_smfmac_f32_16x16x64_bf16 v[56:59], v[92:95], v[248:255], v4\ns_waitcnt lgkmcnt(2)\n" \
"s_barrier\nv_smfmac_f32_16x16x64_bf16 v[60:63], v[88:91], v[224:231], v4\nds_read_b128 v[88:91], v0 offset:18432\nds_read_b128 v[16:19], v7 offset:256\nv_smfmac_f32_16x16x64_bf16 v[60:63], v[92:95], v[72:79], v4\nds_read_b128 v[92:95], v0 offset:19456\nds_read_b128 v[72:75], v1 offset:2048\nds_read_b128 v[76:79], v1 offset:3072\n" \
"v_smfmac_f32_16x16x64_bf16 v[48:51], v[80:83], a[0:7], v4\nds_read_b128 v[20:23], v7 offset:2304\nv_smfmac_f32_16x16x64_bf16 v[48:51], v[84:87], a[32:39], v4\nv_smfmac_f32_16x16x64_bf16 v[52:55], v[80:83], a[8:15], v4\ns_waitcnt lgkmcnt(6)\nds_read_b128 v[24:27], v7 offset:4352\nv_smfmac_f32_16x16x64_bf16 v[52:55], v[84:87], v[64:71], v4\nds_read_b128 v[64:67], v1 offset:4096\n" \
"ds_read_b128 v[68:71], v1 offset:5120\nv_smfmac_f32_16x16x64_bf16 v[56:59], v[80:83], a[16:23], v4\nds_read_b128 v[28:31], v7 offset:6400\nv_smfmac_f32_16x16x64_bf16 v[56:59], v[84:87], a[40:47], v4\nv_smfmac_f32_16x16x64_bf16 v[60:63], v[80:83], a[24:31], v4\nds_read_b128 v[80:83], v0 offset:20480\nds_read_b128 v[32:35], v7 offset:8192\nv_smfmac_f32_16x16x64_bf16 v[60:63], v[84:87], a[48:55], v4\n" \
"s_waitcnt lgkmcnt(11)\nds_read_b128 v[84:87], v0 offset:21504\nv_smfmac_f32_16x16x64_bf16 v[48:51], v[88:91], a[56:63], v4\ns_waitcnt lgkmcnt(10)\nds_read_b128 v[36:39], v7 offset:10240\nv_smfmac_f32_16x16x64_bf16 v[48:51], v[92:95], a[80:87], v4\nv_smfmac_f32_16x16x64_bf16 v[52:55], v[88:91], a[64:71], v4\nds_read_b128 v[40:43], v7 offset:12288\n" \
"v_smfmac_f32_16x16x64_bf16 v[52:55], v[92:95], a[88:95], v4\ns_waitcnt lgkmcnt(10)\nv_smfmac_f32_16x16x64_bf16 v[56:59], v[88:91], v[72:79], v4\nds_read_b128 v[72:75], v1 offset:6144\nds_read_b128 v[76:79], v1 offset:7168\nv_smfmac_f32_16x16x64_bf16 v[56:59], v[92:95], a[96:103], v4\nv_smfmac_f32_16x16x64_bf16 v[60:63], v[88:91], a[72:79], v4\ns_waitcnt lgkmcnt(8)\n" \
"ds_read_b128 v[88:91], v0 offset:22528\nds_read_b128 v[44:47], v7 offset:14336\nv_smfmac_f32_16x16x64_bf16 v[60:63], v[92:95], v[64:71], v4\ns_waitcnt lgkmcnt(8)\nds_read_b128 v[92:95], v0 offset:23552\nds_read_b128 v[64:67], v1 offset:8192\nds_read_b128 v[68:71], v1 offset:9216\nv_smfmac_f32_16x16x64_bf16 v[48:51], v[80:83], a[104:111], v4\n" \
"s_waitcnt lgkmcnt(9)\nv_smfmac_f32_16x16x64_bf16 v[48:51], v[84:87], a[136:143], v4\nv_smfmac_f32_16x16x64_bf16 v[52:55], v[80:83], a[112:119], v4\ns_waitcnt lgkmcnt(5)\nv_smfmac_f32_16x16x64_bf16 v[52:55], v[84:87], v[72:79], v4\nds_read_b128 v[72:75], v1 offset:10240\nds_read_b128 v[76:79], v1 offset:11264\nv_smfmac_f32_16x16x64_bf16 v[56:59], v[80:83], a[120:127], v4\n" \
"v_smfmac_f32_16x16x64_bf16 v[56:59], v[84:87], a[144:151], v4\nv_smfmac_f32_16x16x64_bf16 v[60:63], v[80:83], a[128:135], v4\nv_smfmac_f32_16x16x64_bf16 v[60:63], v[84:87], a[152:159], v4\ns_waitcnt lgkmcnt(6)\nv_smfmac_f32_16x16x64_bf16 v[48:51], v[88:91], a[160:167], v4\ns_waitcnt lgkmcnt(4)\nds_read_b128 v[80:83], v7 offset:8448\nv_smfmac_f32_16x16x64_bf16 v[48:51], v[92:95], a[184:191], v4\n" \
"ds_read_b128 v[84:87], v7 offset:10496\nv_smfmac_f32_16x16x64_bf16 v[52:55], v[88:91], a[168:175], v4\nv_smfmac_f32_16x16x64_bf16 v[52:55], v[92:95], a[192:199], v4\ns_waitcnt lgkmcnt(4)\nv_smfmac_f32_16x16x64_bf16 v[56:59], v[88:91], v[64:71], v4\nds_read_b128 v[64:67], v7 offset:12544\nv_add_f32 v48, v48, v50\nv_smfmac_f32_16x16x64_bf16 v[56:59], v[92:95], a[200:207], v4\n" \
"ds_read_b128 v[68:71], v7 offset:14592\nv_add_f32 v52, v52, v54\nv_add_f32 v49, v49, v51\nv_smfmac_f32_16x16x64_bf16 v[60:63], v[88:91], a[176:183], v4\ns_waitcnt lgkmcnt(4)\nv_add_f32 v53, v53, v55\nv_cvt_pk_bf16_f32 v50, v48, v52\nv_smfmac_f32_16x16x64_bf16 v[60:63], v[92:95], v[72:79], v4\n" \
"v_cvt_pk_bf16_f32 v51, v49, v53\nds_write2_b32 v6, v50, v51 offset0:0 offset1:4\nv_add_f32 v56, v56, v58\nv_add_f32 v57, v57, v59\ns_nop 3\nv_add_f32 v60, v60, v62\nv_add_f32 v61, v61, v63\nv_cvt_pk_bf16_f32 v62, v56, v60\n" \
"v_cvt_pk_bf16_f32 v63, v57, v61\nds_write2_b32 v6, v62, v63 offset0:36 offset1:40\nv_mov_b32 v61, s54\nv_mfma_f32_16x16x32_bf16 v[52:55], v[32:35], v[16:19], 0\nv_mfma_f32_16x16x32_bf16 v[52:55], v[36:39], v[20:23], v[52:55]\nv_mfma_f32_16x16x32_bf16 v[52:55], v[40:43], v[24:27], v[52:55]\nv_mfma_f32_16x16x32_bf16 v[52:55], v[44:47], v[28:31], v[52:55]\ns_waitcnt lgkmcnt(0)\n" \
"s_barrier\nv_mfma_f32_16x16x32_bf16 v[56:59], v[80:83], v[16:19], v[12:15]\nv_mfma_f32_16x16x32_bf16 v[56:59], v[84:87], v[20:23], v[56:59]\nds_read_b128 v[32:35], v9 offset:0\nds_read_b128 v[36:39], v9 offset:576\nds_read_b128 v[40:43], v9 offset:1152\nds_read_b128 v[44:47], v9 offset:1728\nds_read_b32 v60, v0 offset:53760\n" \
"v_mfma_f32_16x16x32_bf16 v[56:59], v[64:67], v[24:27], v[56:59]\nv_mfma_f32_16x16x32_bf16 v[56:59], v[68:71], v[28:31], v[56:59]\ns_waitcnt lgkmcnt(4)\nds_read_b128 v[80:83], v2 offset:0\nds_read_b128 v[84:87], v2 offset:2048\nv_mfma_f32_16x16x32_bf16 v[48:51], v[32:35], v[16:19], 0\ns_waitcnt lgkmcnt(5)\nv_mfma_f32_16x16x32_bf16 v[48:51], v[36:39], v[20:23], v[48:51]\n" \
"s_waitcnt lgkmcnt(4)\nv_mfma_f32_16x16x32_bf16 v[48:51], v[40:43], v[24:27], v[48:51]\ns_waitcnt lgkmcnt(3)\nv_mfma_f32_16x16x32_bf16 v[48:51], v[44:47], v[28:31], v[48:51]\ns_waitcnt lgkmcnt(1)\nv_mfma_f32_16x16x32_bf16 v[16:19], a[0:3], v[80:83], 0\ns_waitcnt lgkmcnt(0)\nds_read_b128 v[88:91], v2 offset:256\n" \
"v_max3_f32 v62, v52, v53, v54\nv_max3_f32 v62, v62, v56, v57\nv_mfma_f32_16x16x32_bf16 v[16:19], a[4:7], v[84:87], v[16:19]\nds_read_b128 v[92:95], v2 offset:2304\nv_max3_f32 v62, v62, v58, v59\nv_max_f32 v62, v62, v55\nv_mfma_f32_16x16x32_bf16 v[20:23], a[8:11], v[80:83], 0\nv_max3_f32 v63, v62, v48, v49\n" \
"v_max3_f32 v62, v62, v48, v49\nv_mfma_f32_16x16x32_bf16 v[20:23], a[12:15], v[84:87], v[20:23]\ns_nop 0\nv_permlane16_swap_b32 v62, v63\nv_max_f32 v50, v62, v63\nv_mfma_f32_16x16x32_bf16 v[24:27], a[16:19], v[80:83], 0\nds_read_b128 v[72:75], v1 offset:0\nv_max_f32 v62, v62, v63\n" \
"v_mfma_f32_16x16x32_bf16 v[24:27], a[20:23], v[84:87], v[24:27]\nds_read_b128 v[76:79], v1 offset:1024\nv_permlane32_swap_b32 v62, v50\nv_mfma_f32_16x16x32_bf16 v[28:31], a[24:27], v[80:83], 0\nv_max_f32 v62, v62, v50\nv_mul_f32 v62, s59, v62\nv_fma_f32 v52, v52, s58, v62\nv_mfma_f32_16x16x32_bf16 v[28:31], a[28:31], v[84:87], v[28:31]\n" \
"s_waitcnt lgkmcnt(3)\nv_fma_f32 v53, v53, s58, v62\nv_exp_f32 v52, v52\nv_mfma_f32_16x16x32_bf16 v[32:35], a[0:3], v[88:91], 0\ns_waitcnt lgkmcnt(2)\nds_read_b128 v[80:83], v2 offset:4096\nv_fma_f32 v54, v54, s58, v62\nv_fma_f32 v55, v55, s58, v62\n" \
"v_mfma_f32_16x16x32_bf16 v[32:35], a[4:7], v[92:95], v[32:35]\nds_read_b128 v[84:87], v2 offset:6144\nv_exp_f32 v53, v53\nv_mfma_f32_16x16x32_bf16 v[36:39], a[8:11], v[88:91], 0\nv_exp_f32 v54, v54\nv_add_f32 v63, v52, v53\nv_mfma_f32_16x16x32_bf16 v[36:39], a[12:15], v[92:95], v[36:39]\nv_fma_f32 v56, v56, s58, v62\n" \
"v_exp_f32 v55, v55\nv_mfma_f32_16x16x32_bf16 v[40:43], a[16:19], v[88:91], 0\nv_add_f32 v63, v63, v54\nv_fma_f32 v57, v57, s58, v62\nv_add_f32 v63, v63, v55\nv_mfma_f32_16x16x32_bf16 v[40:43], a[20:23], v[92:95], v[40:43]\nv_exp_f32 v56, v56\nv_fma_f32 v58, v58, s58, v62\n" \
"v_mfma_f32_16x16x32_bf16 v[44:47], a[24:27], v[88:91], 0\nv_exp_f32 v57, v57\nv_add_f32 v63, v63, v56\nv_mfma_f32_16x16x32_bf16 v[44:47], a[28:31], v[92:95], v[44:47]\ns_waitcnt lgkmcnt(1)\nv_fma_f32 v59, v59, s58, v62\nv_exp_f32 v58, v58\nv_mfma_f32_16x16x32_bf16 v[16:19], a[32:35], v[80:83], v[16:19]\n" \
"s_waitcnt lgkmcnt(0)\nds_read_b128 v[88:91], v2 offset:4352\nv_add_f32 v63, v63, v57\nv_fma_f32 v48, v48, s58, v62\nv_mfma_f32_16x16x32_bf16 v[16:19], a[36:39], v[84:87], v[16:19]\nds_read_b128 v[92:95], v2 offset:6400\nv_exp_f32 v59, v59\nv_mfma_f32_16x16x32_bf16 v[20:23], v[72:75], v[80:83], v[20:23]\n" \
"v_add_f32 v63, v63, v58\nv_fma_f32 v49, v49, s58, v62\nv_add_f32 v63, v63, v59\nv_mfma_f32_16x16x32_bf16 v[20:23], v[76:79], v[84:87], v[20:23]\nv_exp_f32 v48, v48\nv_mfma_f32_16x16x32_bf16 v[24:27], a[40:43], v[80:83], v[24:27]\nds_read_b128 v[64:67], v1 offset:2048\nv_exp_f32 v49, v49\n" \
"v_mfma_f32_16x16x32_bf16 v[24:27], a[44:47], v[84:87], v[24:27]\nds_read_b128 v[68:71], v1 offset:3072\nv_add_f32 v63, v63, v48\nv_add_f32 v62, v63, v49\nv_mfma_f32_16x16x32_bf16 v[28:31], a[48:51], v[80:83], v[28:31]\nv_add_f32 v50, v63, v49\nv_mfma_f32_16x16x32_bf16 v[28:31], a[52:55], v[84:87], v[28:31]\ns_waitcnt lgkmcnt(3)\n" \
"v_permlane16_swap_b32 v62, v50\nv_add_f32 v63, v62, v50\nv_mfma_f32_16x16x32_bf16 v[32:35], a[32:35], v[88:91], v[32:35]\ns_waitcnt lgkmcnt(2)\nds_read_b128 v[80:83], v2 offset:8192\nv_add_f32 v50, v62, v50\nv_mfma_f32_16x16x32_bf16 v[32:35], a[36:39], v[92:95], v[32:35]\nds_read_b128 v[84:87], v2 offset:10240\n" \
"v_permlane32_swap_b32 v63, v50\nv_mfma_f32_16x16x32_bf16 v[36:39], v[72:75], v[88:91], v[36:39]\nv_add_f32 v63, v63, v50\nv_rcp_f32 v63, v63\nv_mfma_f32_16x16x32_bf16 v[36:39], v[76:79], v[92:95], v[36:39]\nv_mul_f32 v52, v52, v63\nv_mul_f32 v53, v53, v63\nv_mul_f32 v54, v54, v63\n" \
"v_mfma_f32_16x16x32_bf16 v[40:43], a[40:43], v[88:91], v[40:43]\nv_mul_f32 v55, v55, v63\nv_mul_f32 v56, v56, v63\nv_mul_f32 v57, v57, v63\nv_mfma_f32_16x16x32_bf16 v[40:43], a[44:47], v[92:95], v[40:43]\nv_mul_f32 v58, v58, v63\nv_mul_f32 v59, v59, v63\nv_mul_f32 v48, v48, v63\n" \
"v_mfma_f32_16x16x32_bf16 v[44:47], a[48:51], v[88:91], v[44:47]\nv_mul_f32 v49, v49, v63\nv_cvt_pk_bf16_f32 v52, v52, v53\nv_cvt_pk_bf16_f32 v53, v54, v55\nv_mfma_f32_16x16x32_bf16 v[44:47], a[52:55], v[92:95], v[44:47]\ns_waitcnt lgkmcnt(1)\nv_cvt_pk_bf16_f32 v48, v48, v49\nv_cvt_pk_bf16_f32 v54, v56, v57\n" \
"v_cvt_pk_bf16_f32 v55, v58, v59\nv_mfma_f32_16x16x32_bf16 v[16:19], a[56:59], v[80:83], v[16:19]\ns_waitcnt lgkmcnt(0)\nds_read_b128 v[88:91], v2 offset:8448\nv_bfi_b32 v52, s61, v52, v61\nv_bfi_b32 v53, s61, v53, v61\nv_mfma_f32_16x16x32_bf16 v[16:19], a[60:63], v[84:87], v[16:19]\nds_read_b128 v[92:95], v2 offset:10496\n" \
"v_bfi_b32 v54, s61, v54, v61\nv_bfi_b32 v55, s61, v55, v61\nv_mfma_f32_16x16x32_bf16 v[20:23], a[64:67], v[80:83], v[20:23]\nv_bfi_b32 v48, s61, v48, v61\nv_mfma_f32_16x16x32_bf16 v[20:23], a[68:71], v[84:87], v[20:23]\nbuffer_store_dwordx4 v[52:55], v10, s[36:39], s55 offen sc1\nv_mov_b32 v49, v48\nv_mfma_f32_16x16x32_bf16 v[24:27], v[64:67], v[80:83], v[24:27]\n" \
"ds_read_b128 v[72:75], v1 offset:4096\nv_permlane16_swap_b32 v48, v49\nv_mfma_f32_16x16x32_bf16 v[24:27], v[68:71], v[84:87], v[24:27]\nds_read_b128 v[76:79], v1 offset:5120\nv_mov_b32 v50, v48\nv_mov_b32 v51, v49\nv_mfma_f32_16x16x32_bf16 v[28:31], a[72:75], v[80:83], v[28:31]\nv_permlane32_swap_b32 v48, v50\n" \
"v_mfma_f32_16x16x32_bf16 v[28:31], a[76:79], v[84:87], v[28:31]\ns_waitcnt lgkmcnt(3)\nv_permlane32_swap_b32 v49, v51\nv_mfma_f32_16x16x32_bf16 v[32:35], a[56:59], v[88:91], v[32:35]\ns_waitcnt lgkmcnt(2)\nds_read_b128 v[80:83], v2 offset:12288\nv_mfma_f32_16x16x32_bf16 v[32:35], a[60:63], v[92:95], v[32:35]\nds_read_b128 v[84:87], v2 offset:14336\n" \
"v_mfma_f32_16x16x32_bf16 v[36:39], a[64:67], v[88:91], v[36:39]\nbuffer_store_dwordx4 v[48:51], v11, s[36:39], s55 offen sc1\nv_cmp_ne_u32 vcc, 0, v60\nv_mfma_f32_16x16x32_bf16 v[36:39], a[68:71], v[92:95], v[36:39]\nv_mfma_f32_16x16x32_bf16 v[40:43], v[64:67], v[88:91], v[40:43]\nv_mfma_f32_16x16x32_bf16 v[40:43], v[68:71], v[92:95], v[40:43]\nv_mfma_f32_16x16x32_bf16 v[44:47], a[72:75], v[88:91], v[44:47]\nv_mfma_f32_16x16x32_bf16 v[44:47], a[76:79], v[92:95], v[44:47]\n" \
"s_cbranch_vccnz .Lqk_pj%=\ns_cmp_lt_i32 s40, 63\ns_waitcnt lgkmcnt(0)\ns_cbranch_scc1 .Lqk_pwait%=\ns_branch .Lqk_end%=\n.Lqk_re2%=:\nv_max_u32 v17, v80, v82\nv_cmp_eq_u32 vcc, -1, v17\n" \
"s_cbranch_vccz .Lqk_sk2428%=\ns_mov_b64 exec, vcc\nbuffer_load_dwordx4 v[80:83], v3, s[36:39], s42 offen offset:0 sc1\ns_mov_b64 exec, -1\n.Lqk_sk2428%=:\nv_max_u32 v17, v84, v86\nv_cmp_eq_u32 vcc, -1, v17\ns_cbranch_vccz .Lqk_sk2435%=\n" \
"s_mov_b64 exec, vcc\nbuffer_load_dwordx4 v[84:87], v3, s[36:39], s42 offen offset:1024 sc1\ns_mov_b64 exec, -1\n.Lqk_sk2435%=:\nv_max_u32 v17, v88, v90\nv_cmp_eq_u32 vcc, -1, v17\ns_cbranch_vccz .Lqk_sk2442%=\ns_mov_b64 exec, vcc\n" \
"buffer_load_dwordx4 v[88:91], v3, s[36:39], s42 offen offset:2048 sc1\ns_mov_b64 exec, -1\n.Lqk_sk2442%=:\nv_max_u32 v17, v92, v94\nv_cmp_eq_u32 vcc, -1, v17\ns_cbranch_vccz .Lqk_sk2449%=\ns_mov_b64 exec, vcc\nbuffer_load_dwordx4 v[92:95], v3, s[36:39], s42 offen offset:3072 sc1\n" \
"s_mov_b64 exec, -1\n.Lqk_sk2449%=:\nv_mov_b64 v[48:49], 0\nv_mov_b64 v[50:51], 0\nv_mov_b64 v[52:53], 0\nv_mov_b64 v[54:55], 0\nv_mov_b64 v[56:57], 0\nv_mov_b64 v[58:59], 0\n" \
"v_mov_b64 v[60:61], 0\nv_mov_b64 v[62:63], 0\ns_add_u32 s43, s43, 1\ns_cmp_lt_u32 s43, 262144\ns_cbranch_scc1 .Lqk_poll2%=\ns_branch .Lqk_abort%=\n.p2align 6\n.Lqk_c3%=:\n" \
"s_waitcnt lgkmcnt(3)\nv_mfma_f32_16x16x32_bf16 v[32:35], v[232:235], v[56:59], v[32:35]\ns_waitcnt lgkmcnt(2)\nv_mov_b64 v[48:49], 0\nv_mov_b64 v[50:51], 0\nv_mfma_f32_16x16x32_bf16 v[32:35], v[236:239], v[60:63], v[32:35]\ns_add_u32 s44, s40, 1\ns_lshl_b32 s44, s44, 17\n" \
"ds_read_b128 v[64:67], v1 offset:18432\nv_mfma_f32_16x16x32_bf16 v[36:39], v[240:243], v[56:59], v[36:39]\ns_add_u32 s44, s44, %[xn]\ns_lshr_b32 s45, s44, 8\nds_read_b128 v[68:71], v1 offset:19456\nv_mfma_f32_16x16x32_bf16 v[36:39], v[244:247], v[60:63], v[36:39]\ns_and_b32 s45, s45, 0x2000\ns_xor_b32 s41, s44, s45\n" \
"v_mov_b64 v[52:53], 0\nv_mov_b64 v[54:55], 0\nv_mfma_f32_16x16x32_bf16 v[40:43], v[248:251], v[56:59], v[40:43]\nbuffer_load_dword v16, v12, s[36:39], s56 offen sc1\nv_mfma_f32_16x16x32_bf16 v[40:43], v[252:255], v[60:63], v[40:43]\nv_cvt_pk_bf16_f32 v36, v32, v36\nv_cvt_pk_bf16_f32 v37, v33, v37\nv_mfma_f32_16x16x32_bf16 v[44:47], v[72:75], v[56:59], v[44:47]\n" \
"v_cvt_pk_bf16_f32 v38, v34, v38\nv_cvt_pk_bf16_f32 v39, v35, v39\nv_mfma_f32_16x16x32_bf16 v[44:47], v[76:79], v[60:63], v[44:47]\nds_read_b128 v[72:75], v1 offset:20480\nds_read_b128 v[76:79], v1 offset:21504\nv_mov_b64 v[56:57], 0\nv_mov_b64 v[58:59], 0\nv_mov_b64 v[60:61], 0\n" \
"v_mov_b64 v[62:63], 0\n.Lqk_poll3%=:\ns_waitcnt vmcnt(1)\nv_smfmac_f32_16x16x64_bf16 v[48:51], v[80:83], v[152:159], v4\nds_write_b128 v0, v[80:83] offset:28672\nv_max3_u32 v17, v80, v82, v84\nv_smfmac_f32_16x16x64_bf16 v[48:51], v[84:87], v[184:191], v4\nv_max3_u32 v17, v17, v86, v88\n" \
"v_smfmac_f32_16x16x64_bf16 v[52:55], v[80:83], v[160:167], v4\ns_waitcnt lgkmcnt(3)\nds_write_b128 v0, v[84:87] offset:29696\nv_max3_u32 v17, v17, v90, v92\nv_max_u32 v17, v17, v94\nv_smfmac_f32_16x16x64_bf16 v[52:55], v[84:87], v[64:71], v4\nv_cmp_eq_u32 vcc, -1, v17\nv_smfmac_f32_16x16x64_bf16 v[56:59], v[80:83], v[168:175], v4\n" \
"ds_write_b128 v0, v[88:91] offset:30720\ns_cbranch_vccnz .Lqk_re3%=\nv_smfmac_f32_16x16x64_bf16 v[56:59], v[84:87], v[192:199], v4\nv_smfmac_f32_16x16x64_bf16 v[60:63], v[80:83], v[176:183], v4\nds_write_b128 v0, v[92:95] offset:31744\nv_smfmac_f32_16x16x64_bf16 v[60:63], v[84:87], v[200:207], v4\nv_smfmac_f32_16x16x64_bf16 v[48:51], v[88:91], v[208:215], v4\nv_smfmac_f32_16x16x64_bf16 v[48:51], v[92:95], v[232:239], v4\n" \
"s_waitcnt lgkmcnt(0)\n.Lqk_jn3%=:\ns_barrier\nds_read_b128 v[80:83], v0 offset:16384\nds_read_b128 v[64:67], v1 offset:22528\nds_read_b128 v[68:71], v1 offset:23552\nv_smfmac_f32_16x16x64_bf16 v[52:55], v[88:91], v[216:223], v4\ns_waitcnt vmcnt(0)\n" \
"v_cmp_ge_i32 vcc, s40, v16\nv_smfmac_f32_16x16x64_bf16 v[52:55], v[92:95], v[240:247], v4\ns_cmp_eq_u64 vcc, 0\ns_cselect_b32 s61, s41, -2147483648\ns_cselect_b32 s44, 1, 0\nds_read_b128 v[84:87], v0 offset:17408\nv_smfmac_f32_16x16x64_bf16 v[56:59], v[88:91], v[72:79], v4\ns_add_u32 m0, %[l0], 0\n" \
"s_add_u32 s60, s61, 0\nds_read_b128 v[72:75], v1 offset:0\nds_read_b128 v[76:79], v1 offset:1024\nbuffer_load_dwordx4 v0, s[36:39], s60 offen offset:0 sc1 lds\nv_smfmac_f32_16x16x64_bf16 v[56:59], v[92:95], v[248:255], v4\nv_mov_b32 v16, s44\nv_smfmac_f32_16x16x64_bf16 v[60:63], v[88:91], v[224:231], v4\ns_waitcnt lgkmcnt(3)\n" \
"ds_read_b128 v[88:91], v0 offset:18432\nbuffer_load_dwordx4 v0, s[36:39], s60 offen offset:1024 sc1 lds\nv_smfmac_f32_16x16x64_bf16 v[60:63], v[92:95], v[64:71], v4\nds_read_b128 v[92:95], v0 offset:19456\nds_read_b128 v[64:67], v1 offset:2048\nds_read_b128 v[68:71], v1 offset:3072\nds_write_b32 v0, v16 offset:53760\nv_smfmac_f32_16x16x64_bf16 v[48:51], v[80:83], a[0:7], v4\n" \
"s_waitcnt lgkmcnt(7)\nds_write_b128 v5, v[36:39] offset:256\nbuffer_load_dwordx4 v0, s[36:39], s60 offen offset:2048 sc1 lds\nv_cvt_pk_bf16_f32 v44, v40, v44\nv_cvt_pk_bf16_f32 v45, v41, v45\nv_cvt_pk_bf16_f32 v46, v42, v46\nv_cvt_pk_bf16_f32 v47, v43, v47\nv_smfmac_f32_16x16x64_bf16 v[48:51], v[84:87], a[32:39], v4\n" \
"v_smfmac_f32_16x16x64_bf16 v[52:55], v[80:83], a[8:15], v4\ns_waitcnt lgkmcnt(6)\nbuffer_load_dwordx4 v0, s[36:39], s60 offen offset:3072 sc1 lds\nv_smfmac_f32_16x16x64_bf16 v[52:55], v[84:87], v[72:79], v4\ns_add_u32 m0, %[l0], 4096\nds_read_b128 v[72:75], v1 offset:4096\nds_read_b128 v[76:79], v1 offset:5120\nv_smfmac_f32_16x16x64_bf16 v[56:59], v[80:83], a[16:23], v4\n" \
"s_add_u32 s60, s61, 4096\nbuffer_load_dwordx4 v0, s[36:39], s60 offen offset:0 sc1 lds\nv_smfmac_f32_16x16x64_bf16 v[56:59], v[84:87], a[40:47], v4\nv_smfmac_f32_16x16x64_bf16 v[60:63], v[80:83], a[24:31], v4\nds_read_b128 v[80:83], v0 offset:20480\nbuffer_load_dwordx4 v0, s[36:39], s60 offen offset:1024 sc1 lds\nv_smfmac_f32_16x16x64_bf16 v[60:63], v[84:87], a[48:55], v4\ns_waitcnt lgkmcnt(8)\n" \
"ds_read_b128 v[84:87], v0 offset:21504\nv_smfmac_f32_16x16x64_bf16 v[48:51], v[88:91], a[56:63], v4\ns_waitcnt lgkmcnt(8)\nds_write_b128 v5, v[44:47] offset:768\nbuffer_load_dwordx4 v0, s[36:39], s60 offen offset:2048 sc1 lds\nv_smfmac_f32_16x16x64_bf16 v[48:51], v[92:95], a[80:87], v4\nv_smfmac_f32_16x16x64_bf16 v[52:55], v[88:91], a[64:71], v4\nbuffer_load_dwordx4 v0, s[36:39], s60 offen offset:3072 sc1 lds\n" \
"v_smfmac_f32_16x16x64_bf16 v[52:55], v[92:95], a[88:95], v4\ns_waitcnt lgkmcnt(7)\ns_add_u32 m0, %[l0], 8192\nv_smfmac_f32_16x16x64_bf16 v[56:59], v[88:91], v[64:71], v4\ns_add_u32 s60, s61, 16384\nds_read_b128 v[64:67], v1 offset:6144\nds_read_b128 v[68:71], v1 offset:7168\nbuffer_load_dwordx4 v0, s[36:39], s60 offen offset:0 sc1 lds\n" \
"v_smfmac_f32_16x16x64_bf16 v[56:59], v[92:95], a[96:103], v4\ns_waitcnt lgkmcnt(2)\ns_barrier\nv_smfmac_f32_16x16x64_bf16 v[60:63], v[88:91], a[72:79], v4\nds_read_b128 v[88:91], v0 offset:22528\nbuffer_load_dwordx4 v0, s[36:39], s60 offen offset:1024 sc1 lds\nv_smfmac_f32_16x16x64_bf16 v[60:63], v[92:95], v[72:79], v4\nds_read_b128 v[92:95], v0 offset:23552\n" \
"ds_read_b128 v[72:75], v1 offset:8192\nds_read_b128 v[76:79], v1 offset:9216\nv_smfmac_f32_16x16x64_bf16 v[48:51], v[80:83], a[104:111], v4\nbuffer_load_dwordx4 v0, s[36:39], s60 offen offset:2048 sc1 lds\nv_smfmac_f32_16x16x64_bf16 v[48:51], v[84:87], a[136:143], v4\nv_smfmac_f32_16x16x64_bf16 v[52:55], v[80:83], a[112:119], v4\ns_waitcnt lgkmcnt(4)\nbuffer_load_dwordx4 v0, s[36:39], s60 offen offset:3072 sc1 lds\n" \
"v_smfmac_f32_16x16x64_bf16 v[52:55], v[84:87], v[64:71], v4\ns_add_u32 m0, %[l0], 12288\nds_read_b128 v[64:67], v1 offset:10240\nds_read_b128 v[68:71], v1 offset:11264\nv_smfmac_f32_16x16x64_bf16 v[56:59], v[80:83], a[120:127], v4\ns_add_u32 s60, s61, 20480\nbuffer_load_dwordx4 v0, s[36:39], s60 offen offset:0 sc1 lds\nv_smfmac_f32_16x16x64_bf16 v[56:59], v[84:87], a[144:151], v4\n" \
"v_smfmac_f32_16x16x64_bf16 v[60:63], v[80:83], a[128:135], v4\nds_read_b128 v[80:83], v0 offset:24576\nbuffer_load_dwordx4 v0, s[36:39], s60 offen offset:1024 sc1 lds\nv_smfmac_f32_16x16x64_bf16 v[60:63], v[84:87], a[152:159], v4\ns_waitcnt lgkmcnt(6)\nds_read_b128 v[84:87], v0 offset:25600\nv_smfmac_f32_16x16x64_bf16 v[48:51], v[88:91], a[160:167], v4\ns_waitcnt lgkmcnt(6)\n" \
"buffer_load_dwordx4 v0, s[36:39], s60 offen offset:2048 sc1 lds\nv_smfmac_f32_16x16x64_bf16 v[48:51], v[92:95], a[184:191], v4\nv_smfmac_f32_16x16x64_bf16 v[52:55], v[88:91], a[168:175], v4\nbuffer_load_dwordx4 v0, s[36:39], s60 offen offset:3072 sc1 lds\nv_smfmac_f32_16x16x64_bf16 v[52:55], v[92:95], a[192:199], v4\ns_waitcnt lgkmcnt(4)\nv_smfmac_f32_16x16x64_bf16 v[56:59], v[88:91], v[72:79], v4\nds_read_b128 v[72:75], v1 offset:12288\n" \
"ds_read_b128 v[76:79], v1 offset:13312\nv_smfmac_f32_16x16x64_bf16 v[56:59], v[92:95], a[200:207], v4\nv_smfmac_f32_16x16x64_bf16 v[60:63], v[88:91], a[176:183], v4\ns_waitcnt lgkmcnt(4)\nds_read_b128 v[88:91], v0 offset:26624\nv_smfmac_f32_16x16x64_bf16 v[60:63], v[92:95], v[64:71], v4\ns_waitcnt lgkmcnt(4)\nds_read_b128 v[92:95], v0 offset:27648\n" \
"ds_read_b128 v[64:67], v1 offset:14336\nds_read_b128 v[68:71], v1 offset:15360\nv_smfmac_f32_16x16x64_bf16 v[48:51], v[80:83], a[208:215], v4\ns_waitcnt lgkmcnt(6)\nv_smfmac_f32_16x16x64_bf16 v[48:51], v[84:87], a[240:247], v4\nv_smfmac_f32_16x16x64_bf16 v[52:55], v[80:83], a[216:223], v4\ns_waitcnt lgkmcnt(4)\nv_smfmac_f32_16x16x64_bf16 v[52:55], v[84:87], v[72:79], v4\n" \
"ds_read_b128 v[72:75], v1 offset:16384\nds_read_b128 v[76:79], v1 offset:17408\nv_smfmac_f32_16x16x64_bf16 v[56:59], v[80:83], a[224:231], v4\nv_smfmac_f32_16x16x64_bf16 v[56:59], v[84:87], a[248:255], v4\nv_smfmac_f32_16x16x64_bf16 v[60:63], v[80:83], a[232:239], v4\nv_smfmac_f32_16x16x64_bf16 v[60:63], v[84:87], v[96:103], v4\ns_waitcnt lgkmcnt(5)\nv_smfmac_f32_16x16x64_bf16 v[48:51], v[88:91], v[104:111], v4\n" \
"s_waitcnt lgkmcnt(4)\nv_smfmac_f32_16x16x64_bf16 v[48:51], v[92:95], v[128:135], v4\nv_smfmac_f32_16x16x64_bf16 v[52:55], v[88:91], v[112:119], v4\nv_smfmac_f32_16x16x64_bf16 v[52:55], v[92:95], v[136:143], v4\ns_waitcnt lgkmcnt(2)\nv_smfmac_f32_16x16x64_bf16 v[56:59], v[88:91], v[64:71], v4\nv_add_f32 v48, v48, v50\nv_smfmac_f32_16x16x64_bf16 v[56:59], v[92:95], v[144:151], v4\n" \
"v_add_f32 v52, v52, v54\nv_add_f32 v49, v49, v51\nv_smfmac_f32_16x16x64_bf16 v[60:63], v[88:91], v[120:127], v4\ns_waitcnt lgkmcnt(0)\nv_add_f32 v53, v53, v55\nv_cvt_pk_bf16_f32 v50, v48, v52\nv_smfmac_f32_16x16x64_bf16 v[60:63], v[92:95], v[72:79], v4\nv_cvt_pk_bf16_f32 v51, v49, v53\n" \
"ds_write2_b32 v6, v50, v51 offset0:0 offset1:4\nv_add_f32 v56, v56, v58\nv_add_f32 v57, v57, v59\ns_nop 3\nv_add_f32 v60, v60, v62\nv_add_f32 v61, v61, v63\nv_cvt_pk_bf16_f32 v62, v56, v60\nv_cvt_pk_bf16_f32 v63, v57, v61\n" \
"ds_write2_b32 v6, v62, v63 offset0:36 offset1:40\ns_waitcnt vmcnt(0)\ns_waitcnt lgkmcnt(0)\ns_barrier\ns_cbranch_vccnz .Lqk_slow%=\ns_add_u32 m0, %[l0], 16384\ns_add_u32 s60, s41, 32768\nbuffer_load_dwordx4 v0, s[36:39], s60 offen offset:0 sc1 lds\n" \
"buffer_load_dwordx4 v0, s[36:39], s60 offen offset:1024 sc1 lds\nbuffer_load_dwordx4 v0, s[36:39], s60 offen offset:2048 sc1 lds\nbuffer_load_dwordx4 v0, s[36:39], s60 offen offset:3072 sc1 lds\ns_add_u32 m0, %[l0], 20480\ns_add_u32 s60, s41, 36864\nbuffer_load_dwordx4 v0, s[36:39], s60 offen offset:0 sc1 lds\nbuffer_load_dwordx4 v0, s[36:39], s60 offen offset:1024 sc1 lds\nds_read_b128 v[80:83], v2 offset:0\n" \
"ds_read_b128 v[84:87], v2 offset:2048\nbuffer_load_dwordx4 v0, s[36:39], s60 offen offset:2048 sc1 lds\nbuffer_load_dwordx4 v0, s[36:39], s60 offen offset:3072 sc1 lds\ns_add_u32 m0, %[l0], 24576\ns_add_u32 s60, s41, 49152\nbuffer_load_dwordx4 v0, s[36:39], s60 offen offset:0 sc1 lds\nbuffer_load_dwordx4 v0, s[36:39], s60 offen offset:1024 sc1 lds\nbuffer_load_dwordx4 v0, s[36:39], s60 offen offset:2048 sc1 lds\n" \
"buffer_load_dwordx4 v0, s[36:39], s60 offen offset:3072 sc1 lds\ns_add_u32 m0, %[l0], 28672\ns_add_u32 s60, s41, 53248\nbuffer_load_dwordx4 v0, s[36:39], s60 offen offset:0 sc1 lds\nbuffer_load_dwordx4 v0, s[36:39], s60 offen offset:1024 sc1 lds\nbuffer_load_dwordx4 v0, s[36:39], s60 offen offset:2048 sc1 lds\nbuffer_load_dwordx4 v0, s[36:39], s60 offen offset:3072 sc1 lds\ns_waitcnt lgkmcnt(1)\n" \
"v_mfma_f32_16x16x32_bf16 v[16:19], a[0:3], v[80:83], 0\ns_waitcnt lgkmcnt(0)\nds_read_b128 v[88:91], v2 offset:256\nv_mfma_f32_16x16x32_bf16 v[16:19], a[4:7], v[84:87], v[16:19]\nds_read_b128 v[92:95], v2 offset:2304\nv_mfma_f32_16x16x32_bf16 v[20:23], a[8:11], v[80:83], 0\nv_mfma_f32_16x16x32_bf16 v[20:23], a[12:15], v[84:87], v[20:23]\nv_mfma_f32_16x16x32_bf16 v[24:27], a[16:19], v[80:83], 0\n" \
"ds_read_b128 v[72:75], v1 offset:0\nv_mfma_f32_16x16x32_bf16 v[24:27], a[20:23], v[84:87], v[24:27]\nds_read_b128 v[76:79], v1 offset:1024\nv_mfma_f32_16x16x32_bf16 v[28:31], a[24:27], v[80:83], 0\nv_mfma_f32_16x16x32_bf16 v[28:31], a[28:31], v[84:87], v[28:31]\ns_waitcnt lgkmcnt(3)\nv_mfma_f32_16x16x32_bf16 v[32:35], a[0:3], v[88:91], 0\ns_waitcnt lgkmcnt(2)\n" \
"ds_read_b128 v[80:83], v2 offset:4096\nv_mfma_f32_16x16x32_bf16 v[32:35], a[4:7], v[92:95], v[32:35]\nds_read_b128 v[84:87], v2 offset:6144\nv_mfma_f32_16x16x32_bf16 v[36:39], a[8:11], v[88:91], 0\nv_mfma_f32_16x16x32_bf16 v[36:39], a[12:15], v[92:95], v[36:39]\nv_mfma_f32_16x16x32_bf16 v[40:43], a[16:19], v[88:91], 0\nv_mfma_f32_16x16x32_bf16 v[40:43], a[20:23], v[92:95], v[40:43]\nv_mfma_f32_16x16x32_bf16 v[44:47], a[24:27], v[88:91], 0\n" \
"v_mfma_f32_16x16x32_bf16 v[44:47], a[28:31], v[92:95], v[44:47]\ns_waitcnt lgkmcnt(1)\nv_mfma_f32_16x16x32_bf16 v[16:19], a[32:35], v[80:83], v[16:19]\ns_waitcnt lgkmcnt(0)\nds_read_b128 v[88:91], v2 offset:4352\nv_mfma_f32_16x16x32_bf16 v[16:19], a[36:39], v[84:87], v[16:19]\nds_read_b128 v[92:95], v2 offset:6400\nv_mfma_f32_16x16x32_bf16 v[20:23], v[72:75], v[80:83], v[20:23]\n" \
"v_mfma_f32_16x16x32_bf16 v[20:23], v[76:79], v[84:87], v[20:23]\nv_mfma_f32_16x16x32_bf16 v[24:27], a[40:43], v[80:83], v[24:27]\nds_read_b128 v[64:67], v1 offset:2048\nv_mfma_f32_16x16x32_bf16 v[24:27], a[44:47], v[84:87], v[24:27]\nds_read_b128 v[68:71], v1 offset:3072\nv_mfma_f32_16x16x32_bf16 v[28:31], a[48:51], v[80:83], v[28:31]\nv_mfma_f32_16x16x32_bf16 v[28:31], a[52:55], v[84:87], v[28:31]\ns_waitcnt lgkmcnt(3)\n" \
"v_mfma_f32_16x16x32_bf16 v[32:35], a[32:35], v[88:91], v[32:35]\ns_waitcnt lgkmcnt(2)\nds_read_b128 v[80:83], v2 offset:8192\nv_mfma_f32_16x16x32_bf16 v[32:35], a[36:39], v[92:95], v[32:35]\nds_read_b128 v[84:87], v2 offset:10240\nv_mfma_f32_16x16x32_bf16 v[36:39], v[72:75], v[88:91], v[36:39]\nv_mfma_f32_16x16x32_bf16 v[36:39], v[76:79], v[92:95], v[36:39]\nv_mfma_f32_16x16x32_bf16 v[40:43], a[40:43], v[88:91], v[40:43]\n" \
"v_mfma_f32_16x16x32_bf16 v[40:43], a[44:47], v[92:95], v[40:43]\nv_mfma_f32_16x16x32_bf16 v[44:47], a[48:51], v[88:91], v[44:47]\nv_mfma_f32_16x16x32_bf16 v[44:47], a[52:55], v[92:95], v[44:47]\ns_waitcnt lgkmcnt(1)\nv_mfma_f32_16x16x32_bf16 v[16:19], a[56:59], v[80:83], v[16:19]\ns_waitcnt lgkmcnt(0)\nds_read_b128 v[88:91], v2 offset:8448\nv_mfma_f32_16x16x32_bf16 v[16:19], a[60:63], v[84:87], v[16:19]\n" \
"ds_read_b128 v[92:95], v2 offset:10496\nv_mfma_f32_16x16x32_bf16 v[20:23], a[64:67], v[80:83], v[20:23]\nv_mfma_f32_16x16x32_bf16 v[20:23], a[68:71], v[84:87], v[20:23]\nv_mfma_f32_16x16x32_bf16 v[24:27], v[64:67], v[80:83], v[24:27]\nds_read_b128 v[72:75], v1 offset:4096\nv_mfma_f32_16x16x32_bf16 v[24:27], v[68:71], v[84:87], v[24:27]\nds_read_b128 v[76:79], v1 offset:5120\nv_mfma_f32_16x16x32_bf16 v[28:31], a[72:75], v[80:83], v[28:31]\n" \
"v_mfma_f32_16x16x32_bf16 v[28:31], a[76:79], v[84:87], v[28:31]\ns_waitcnt lgkmcnt(3)\nv_mfma_f32_16x16x32_bf16 v[32:35], a[56:59], v[88:91], v[32:35]\ns_waitcnt lgkmcnt(2)\nds_read_b128 v[80:83], v2 offset:12288\nv_mfma_f32_16x16x32_bf16 v[32:35], a[60:63], v[92:95], v[32:35]\nds_read_b128 v[84:87], v2 offset:14336\nv_mfma_f32_16x16x32_bf16 v[36:39], a[64:67], v[88:91], v[36:39]\n" \
"v_mfma_f32_16x16x32_bf16 v[36:39], a[68:71], v[92:95], v[36:39]\nv_mfma_f32_16x16x32_bf16 v[40:43], v[64:67], v[88:91], v[40:43]\nv_mfma_f32_16x16x32_bf16 v[40:43], v[68:71], v[92:95], v[40:43]\nv_mfma_f32_16x16x32_bf16 v[44:47], a[72:75], v[88:91], v[44:47]\nv_mfma_f32_16x16x32_bf16 v[44:47], a[76:79], v[92:95], v[44:47]\ns_waitcnt vmcnt(0)\ns_branch .Lqk_pj%=\n.Lqk_slow%=:\n" \
"s_cmp_ge_i32 s40, 63\ns_cbranch_scc1 .Lqk_end%=\n.Lqk_fetch0%=:\nbuffer_load_dword v16, v12, s[36:39], s56 offen sc1\ns_waitcnt vmcnt(0)\nv_cmp_ge_i32 vcc, s40, v16\ns_cbranch_vccz .Lqk_fok%=\ns_add_u32 s43, s43, 1\n" \
"s_cmp_lt_u32 s43, 262144\ns_cbranch_scc1 .Lqk_fetch0%=\ns_branch .Lqk_abort%=\n.Lqk_fok%=:\ns_add_u32 s44, s40, 1\ns_lshl_b32 s44, s44, 17\ns_add_u32 s44, s44, %[xn]\ns_lshr_b32 s45, s44, 8\n" \
"s_and_b32 s45, s45, 0x2000\ns_xor_b32 s41, s44, s45\ns_add_u32 m0, %[l0], 0\ns_add_u32 s60, s41, 0\nbuffer_load_dwordx4 v0, s[36:39], s60 offen offset:0 sc1 lds\nbuffer_load_dwordx4 v0, s[36:39], s60 offen offset:1024 sc1 lds\nbuffer_load_dwordx4 v0, s[36:39], s60 offen offset:2048 sc1 lds\nbuffer_load_dwordx4 v0, s[36:39], s60 offen offset:3072 sc1 lds\n" \
"s_add_u32 m0, %[l0], 4096\ns_add_u32 s60, s41, 4096\nbuffer_load_dwordx4 v0, s[36:39], s60 offen offset:0 sc1 lds\nbuffer_load_dwordx4 v0, s[36:39], s60 offen offset:1024 sc1 lds\nbuffer_load_dwordx4 v0, s[36:39], s60 offen offset:2048 sc1 lds\nbuffer_load_dwordx4 v0, s[36:39], s60 offen offset:3072 sc1 lds\ns_add_u32 m0, %[l0], 8192\ns_add_u32 s60, s41, 16384\n" \
"buffer_load_dwordx4 v0, s[36:39], s60 offen offset:0 sc1 lds\nbuffer_load_dwordx4 v0, s[36:39], s60 offen offset:1024 sc1 lds\nbuffer_load_dwordx4 v0, s[36:39], s60 offen offset:2048 sc1 lds\nbuffer_load_dwordx4 v0, s[36:39], s60 offen offset:3072 sc1 lds\ns_add_u32 m0, %[l0], 12288\ns_add_u32 s60, s41, 20480\nbuffer_load_dwordx4 v0, s[36:39], s60 offen offset:0 sc1 lds\nbuffer_load_dwordx4 v0, s[36:39], s60 offen offset:1024 sc1 lds\n" \
"buffer_load_dwordx4 v0, s[36:39], s60 offen offset:2048 sc1 lds\nbuffer_load_dwordx4 v0, s[36:39], s60 offen offset:3072 sc1 lds\ns_add_u32 m0, %[l0], 16384\ns_add_u32 s60, s41, 32768\nbuffer_load_dwordx4 v0, s[36:39], s60 offen offset:0 sc1 lds\nbuffer_load_dwordx4 v0, s[36:39], s60 offen offset:1024 sc1 lds\nbuffer_load_dwordx4 v0, s[36:39], s60 offen offset:2048 sc1 lds\nbuffer_load_dwordx4 v0, s[36:39], s60 offen offset:3072 sc1 lds\n" \
"s_add_u32 m0, %[l0], 20480\ns_add_u32 s60, s41, 36864\nbuffer_load_dwordx4 v0, s[36:39], s60 offen offset:0 sc1 lds\nbuffer_load_dwordx4 v0, s[36:39], s60 offen offset:1024 sc1 lds\nbuffer_load_dwordx4 v0, s[36:39], s60 offen offset:2048 sc1 lds\nbuffer_load_dwordx4 v0, s[36:39], s60 offen offset:3072 sc1 lds\ns_add_u32 m0, %[l0], 24576\ns_add_u32 s60, s41, 49152\n" \
"buffer_load_dwordx4 v0, s[36:39], s60 offen offset:0 sc1 lds\nbuffer_load_dwordx4 v0, s[36:39], s60 offen offset:1024 sc1 lds\nbuffer_load_dwordx4 v0, s[36:39], s60 offen offset:2048 sc1 lds\nbuffer_load_dwordx4 v0, s[36:39], s60 offen offset:3072 sc1 lds\ns_add_u32 m0, %[l0], 28672\ns_add_u32 s60, s41, 53248\nbuffer_load_dwordx4 v0, s[36:39], s60 offen offset:0 sc1 lds\nbuffer_load_dwordx4 v0, s[36:39], s60 offen offset:1024 sc1 lds\n" \
"buffer_load_dwordx4 v0, s[36:39], s60 offen offset:2048 sc1 lds\nbuffer_load_dwordx4 v0, s[36:39], s60 offen offset:3072 sc1 lds\ns_waitcnt vmcnt(0)\ns_branch .Lqk_pwait%=\n.Lqk_re3%=:\nv_max_u32 v17, v80, v82\nv_cmp_eq_u32 vcc, -1, v17\ns_cbranch_vccz .Lqk_sk2865%=\n" \
"s_mov_b64 exec, vcc\nbuffer_load_dwordx4 v[80:83], v3, s[36:39], s42 offen offset:0 sc1\ns_mov_b64 exec, -1\n.Lqk_sk2865%=:\nv_max_u32 v17, v84, v86\nv_cmp_eq_u32 vcc, -1, v17\ns_cbranch_vccz .Lqk_sk2872%=\ns_mov_b64 exec, vcc\n" \
"buffer_load_dwordx4 v[84:87], v3, s[36:39], s42 offen offset:1024 sc1\ns_mov_b64 exec, -1\n.Lqk_sk2872%=:\nv_max_u32 v17, v88, v90\nv_cmp_eq_u32 vcc, -1, v17\ns_cbranch_vccz .Lqk_sk2879%=\ns_mov_b64 exec, vcc\nbuffer_load_dwordx4 v[88:91], v3, s[36:39], s42 offen offset:2048 sc1\n" \
"s_mov_b64 exec, -1\n.Lqk_sk2879%=:\nv_max_u32 v17, v92, v94\nv_cmp_eq_u32 vcc, -1, v17\ns_cbranch_vccz .Lqk_sk2886%=\ns_mov_b64 exec, vcc\nbuffer_load_dwordx4 v[92:95], v3, s[36:39], s42 offen offset:3072 sc1\ns_mov_b64 exec, -1\n" \
".Lqk_sk2886%=:\nbuffer_load_dword v16, v12, s[36:39], s56 offen sc1\nv_mov_b64 v[48:49], 0\nv_mov_b64 v[50:51], 0\nv_mov_b64 v[52:53], 0\nv_mov_b64 v[54:55], 0\nv_mov_b64 v[56:57], 0\nv_mov_b64 v[58:59], 0\n" \
"v_mov_b64 v[60:61], 0\nv_mov_b64 v[62:63], 0\ns_add_u32 s43, s43, 1\ns_cmp_lt_u32 s43, 262144\ns_cbranch_scc1 .Lqk_poll3%=\ns_branch .Lqk_abort%=\n.Lqk_abort%=:\ns_mov_b64 exec, -1\n" \
"v_mov_b32 v16, 0\ns_movk_i32 s44, 6400\nbuffer_store_dword v16, off, s[36:39], s44 sc1\n.Lqk_end%=:\ns_waitcnt vmcnt(0) lgkmcnt(0)\n" \
""
#define VU_ASM \
"v_mbcnt_lo_u32_b32 v255, -1, 0\nv_mbcnt_hi_u32_b32 v255, -1, v255\nv_lshlrev_b32 v255, 2, v255\nv_add_u32 v255, %[tb], v255\nds_read_b32 v0, v255 offset:0\nds_read_b32 v1, v255 offset:256\nds_read_b32 v2, v255 offset:512\nds_read_b32 v3, v255 offset:768\n" \
"ds_read_b32 v4, v255 offset:1024\nds_read_b32 v5, v255 offset:1280\nds_read_b32 v6, v255 offset:1536\nds_read_b32 v7, v255 offset:1792\nds_read_b32 v8, v255 offset:2048\nds_read_b32 v9, v255 offset:2304\nds_read_b32 v10, v255 offset:2560\nds_read_b32 v11, v255 offset:2816\n" \
"ds_read_b32 v90, v255 offset:3072\nds_read_b32 v91, v255 offset:3328\nds_read_b32 v92, v255 offset:3584\nds_read_b32 v93, v255 offset:3840\nds_read_b32 v94, v255 offset:4096\nds_read_b32 v95, v255 offset:4352\ns_waitcnt lgkmcnt(0)\ns_mov_b64 s[36:37], %[ws]\n" \
"s_mov_b32 s38, %[sz]\ns_mov_b32 s39, 0x20000\ns_mov_b32 s45, 2096896\nbuffer_load_dword v15, off, s[36:39], s45 sc1\ns_add_u32 m0, %[lw], 16384\ns_add_u32 s44, %[wo], 0\nbuffer_load_dwordx4 v90, %[rw], s44 offen nt lds\ns_add_u32 m0, %[lw], 17408\n" \
"s_add_u32 s44, %[wo], 65536\nbuffer_load_dwordx4 v90, %[rw], s44 offen nt lds\ns_add_u32 m0, %[lw], 18432\ns_add_u32 s44, %[wo], 131072\nbuffer_load_dwordx4 v90, %[rw], s44 offen nt lds\ns_add_u32 m0, %[lw], 19456\ns_add_u32 s44, %[wo], 196608\nbuffer_load_dwordx4 v90, %[rw], s44 offen nt lds\n" \
"s_add_u32 m0, %[lw], 20480\ns_add_u32 s44, %[wo], 524288\nbuffer_load_dwordx4 v90, %[rw], s44 offen nt lds\ns_add_u32 m0, %[lw], 21504\ns_add_u32 s44, %[wo], 589824\nbuffer_load_dwordx4 v90, %[rw], s44 offen nt lds\ns_add_u32 m0, %[lw], 22528\ns_add_u32 s44, %[wo], 655360\n" \
"buffer_load_dwordx4 v90, %[rw], s44 offen nt lds\ns_add_u32 m0, %[lw], 23552\ns_add_u32 s44, %[wo], 720896\nbuffer_load_dwordx4 v90, %[rw], s44 offen nt lds\ns_add_u32 m0, %[tb], 0\ns_add_u32 s44, %[wo], 1048576\nbuffer_load_dwordx4 v90, %[rw], s44 offen nt lds\ns_add_u32 m0, %[tb], 1024\n" \
"s_add_u32 s44, %[wo], 1114112\nbuffer_load_dwordx4 v90, %[rw], s44 offen nt lds\ns_add_u32 m0, %[tb], 2048\ns_add_u32 s44, %[wo], 1179648\nbuffer_load_dwordx4 v90, %[rw], s44 offen nt lds\ns_add_u32 m0, %[tb], 3072\ns_add_u32 s44, %[wo], 1245184\nbuffer_load_dwordx4 v90, %[rw], s44 offen nt lds\n" \
"s_add_u32 m0, %[tb], 4096\ns_add_u32 s44, %[wo], 1572864\nbuffer_load_dwordx4 v90, %[rw], s44 offen nt lds\ns_add_u32 m0, %[tb], 5120\ns_add_u32 s44, %[wo], 1638400\nbuffer_load_dwordx4 v90, %[rw], s44 offen nt lds\ns_add_u32 m0, %[tb], 6144\ns_add_u32 s44, %[wo], 1703936\n" \
"buffer_load_dwordx4 v90, %[rw], s44 offen nt lds\ns_add_u32 m0, %[tb], 7168\ns_add_u32 s44, %[wo], 1769472\nbuffer_load_dwordx4 v90, %[rw], s44 offen nt lds\ns_add_u32 m0, %[tb], 8192\ns_add_u32 s44, %[wo], 2097152\nbuffer_load_dwordx4 v90, %[rw], s44 offen nt lds\ns_add_u32 m0, %[tb], 9216\n" \
"s_add_u32 s44, %[wo], 2162688\nbuffer_load_dwordx4 v90, %[rw], s44 offen nt lds\ns_add_u32 m0, %[tb], 10240\ns_add_u32 s44, %[wo], 2228224\nbuffer_load_dwordx4 v90, %[rw], s44 offen nt lds\ns_add_u32 m0, %[tb], 11264\ns_add_u32 s44, %[wo], 2293760\nbuffer_load_dwordx4 v90, %[rw], s44 offen nt lds\n" \
"s_add_u32 m0, %[lw], 0\ns_add_u32 s44, %[wo], 2621440\nbuffer_load_dwordx4 v90, %[rw], s44 offen nt lds\ns_add_u32 m0, %[lw], 1024\ns_add_u32 s44, %[wo], 2686976\nbuffer_load_dwordx4 v90, %[rw], s44 offen nt lds\ns_add_u32 m0, %[lw], 2048\ns_add_u32 s44, %[wo], 2752512\n" \
"buffer_load_dwordx4 v90, %[rw], s44 offen nt lds\ns_add_u32 m0, %[lw], 3072\ns_add_u32 s44, %[wo], 2818048\nbuffer_load_dwordx4 v90, %[rw], s44 offen nt lds\ns_add_u32 m0, %[lw], 4096\ns_add_u32 s44, %[wo], 3145728\nbuffer_load_dwordx4 v90, %[rw], s44 offen nt lds\ns_add_u32 m0, %[lw], 5120\n" \
"s_add_u32 s44, %[wo], 3211264\nbuffer_load_dwordx4 v90, %[rw], s44 offen nt lds\ns_add_u32 m0, %[lw], 6144\ns_add_u32 s44, %[wo], 3276800\nbuffer_load_dwordx4 v90, %[rw], s44 offen nt lds\ns_add_u32 m0, %[lw], 7168\ns_add_u32 s44, %[wo], 3342336\nbuffer_load_dwordx4 v90, %[rw], s44 offen nt lds\n" \
"s_add_u32 m0, %[lw], 8192\ns_add_u32 s44, %[wo], 3670016\nbuffer_load_dwordx4 v90, %[rw], s44 offen nt lds\ns_add_u32 m0, %[lw], 9216\ns_add_u32 s44, %[wo], 3735552\nbuffer_load_dwordx4 v90, %[rw], s44 offen nt lds\ns_add_u32 m0, %[lw], 10240\ns_add_u32 s44, %[wo], 3801088\n" \
"buffer_load_dwordx4 v90, %[rw], s44 offen nt lds\ns_add_u32 m0, %[lw], 11264\ns_add_u32 s44, %[wo], 3866624\nbuffer_load_dwordx4 v90, %[rw], s44 offen nt lds\ns_waitcnt vmcnt(28)\ns_barrier\ns_add_u32 m0, %[lw], 12288\ns_add_u32 s44, %[wo], 4194304\n" \
"buffer_load_dwordx4 v90, %[rw], s44 offen nt lds\ns_add_u32 m0, %[lw], 13312\ns_add_u32 s44, %[wo], 4259840\nbuffer_load_dwordx4 v90, %[rw], s44 offen nt lds\ns_add_u32 m0, %[lw], 14336\ns_add_u32 s44, %[wo], 4325376\nbuffer_load_dwordx4 v90, %[rw], s44 offen nt lds\ns_add_u32 m0, %[lw], 15360\n" \
"s_add_u32 s44, %[wo], 4390912\nbuffer_load_dwordx4 v90, %[rw], s44 offen nt lds\nds_read_b64_tr_b16 a[0:1], v93 offset:16384\nds_read_b64_tr_b16 a[2:3], v93 offset:16896\nds_read_b64_tr_b16 a[16:17], v93 offset:16392\nds_read_b64_tr_b16 a[18:19], v93 offset:16904\nds_read_b64_tr_b16 a[4:5], v93 offset:40960\nds_read_b64_tr_b16 a[6:7], v93 offset:41472\n" \
"ds_read_b64_tr_b16 a[20:21], v93 offset:40968\nds_read_b64_tr_b16 a[22:23], v93 offset:41480\ns_waitcnt lgkmcnt(0)\ns_waitcnt vmcnt(28)\ns_barrier\ns_add_u32 m0, %[lw], 16384\ns_add_u32 s44, %[wo], 4718592\nbuffer_load_dwordx4 v90, %[rw], s44 offen nt lds\n" \
"s_add_u32 m0, %[lw], 17408\ns_add_u32 s44, %[wo], 4784128\nbuffer_load_dwordx4 v90, %[rw], s44 offen nt lds\ns_add_u32 m0, %[lw], 18432\ns_add_u32 s44, %[wo], 4849664\nbuffer_load_dwordx4 v90, %[rw], s44 offen nt lds\ns_add_u32 m0, %[lw], 19456\ns_add_u32 s44, %[wo], 4915200\n" \
"buffer_load_dwordx4 v90, %[rw], s44 offen nt lds\nds_read_b64_tr_b16 a[24:25], v93 offset:20480\nds_read_b64_tr_b16 a[26:27], v93 offset:20992\nds_read_b64_tr_b16 a[40:41], v93 offset:20488\nds_read_b64_tr_b16 a[42:43], v93 offset:21000\nds_read_b64_tr_b16 a[28:29], v93 offset:45056\nds_read_b64_tr_b16 a[30:31], v93 offset:45568\nds_read_b64_tr_b16 a[44:45], v93 offset:45064\n" \
"ds_read_b64_tr_b16 a[46:47], v93 offset:45576\ns_waitcnt lgkmcnt(0)\ns_waitcnt vmcnt(28)\ns_barrier\ns_add_u32 m0, %[lw], 20480\ns_add_u32 s44, %[wo], 5242880\nbuffer_load_dwordx4 v90, %[rw], s44 offen nt lds\ns_add_u32 m0, %[lw], 21504\n" \
"s_add_u32 s44, %[wo], 5308416\nbuffer_load_dwordx4 v90, %[rw], s44 offen nt lds\ns_add_u32 m0, %[lw], 22528\ns_add_u32 s44, %[wo], 5373952\nbuffer_load_dwordx4 v90, %[rw], s44 offen nt lds\ns_add_u32 m0, %[lw], 23552\ns_add_u32 s44, %[wo], 5439488\nbuffer_load_dwordx4 v90, %[rw], s44 offen nt lds\n" \
"ds_read_b64_tr_b16 v[84:85], v92 offset:0\nds_read_b64_tr_b16 v[86:87], v92 offset:512\nds_read_b64_tr_b16 a[64:65], v92 offset:8\nds_read_b64_tr_b16 a[66:67], v92 offset:520\nds_read_b64_tr_b16 v[80:81], v92 offset:12288\nds_read_b64_tr_b16 v[82:83], v92 offset:12800\nds_read_b64_tr_b16 a[68:69], v92 offset:12296\nds_read_b64_tr_b16 a[70:71], v92 offset:12808\n" \
"s_waitcnt lgkmcnt(0)\ns_waitcnt vmcnt(28)\ns_barrier\ns_add_u32 m0, %[tb], 0\ns_add_u32 s44, %[wo], 5767168\nbuffer_load_dwordx4 v90, %[rw], s44 offen nt lds\ns_add_u32 m0, %[tb], 1024\ns_add_u32 s44, %[wo], 5832704\n" \
"buffer_load_dwordx4 v90, %[rw], s44 offen nt lds\ns_add_u32 m0, %[tb], 2048\ns_add_u32 s44, %[wo], 5898240\nbuffer_load_dwordx4 v90, %[rw], s44 offen nt lds\ns_add_u32 m0, %[tb], 3072\ns_add_u32 s44, %[wo], 5963776\nbuffer_load_dwordx4 v90, %[rw], s44 offen nt lds\nds_read_b64_tr_b16 a[80:81], v92 offset:4096\n" \
"ds_read_b64_tr_b16 a[82:83], v92 offset:4608\nds_read_b64_tr_b16 a[88:89], v92 offset:4104\nds_read_b64_tr_b16 a[90:91], v92 offset:4616\nds_read_b64_tr_b16 a[84:85], v92 offset:16384\nds_read_b64_tr_b16 a[86:87], v92 offset:16896\nds_read_b64_tr_b16 a[92:93], v92 offset:16392\nds_read_b64_tr_b16 a[94:95], v92 offset:16904\ns_waitcnt lgkmcnt(0)\n" \
"s_waitcnt vmcnt(28)\ns_barrier\ns_add_u32 m0, %[tb], 4096\ns_add_u32 s44, %[wo], 6291456\nbuffer_load_dwordx4 v90, %[rw], s44 offen nt lds\ns_add_u32 m0, %[tb], 5120\ns_add_u32 s44, %[wo], 6356992\nbuffer_load_dwordx4 v90, %[rw], s44 offen nt lds\n" \
"s_add_u32 m0, %[tb], 6144\ns_add_u32 s44, %[wo], 6422528\nbuffer_load_dwordx4 v90, %[rw], s44 offen nt lds\ns_add_u32 m0, %[tb], 7168\ns_add_u32 s44, %[wo], 6488064\nbuffer_load_dwordx4 v90, %[rw], s44 offen nt lds\nds_read_b64_tr_b16 a[104:105], v92 offset:8192\nds_read_b64_tr_b16 a[106:107], v92 offset:8704\n" \
"ds_read_b64_tr_b16 a[120:121], v92 offset:8200\nds_read_b64_tr_b16 a[122:123], v92 offset:8712\nds_read_b64_tr_b16 a[108:109], v92 offset:20480\nds_read_b64_tr_b16 a[110:111], v92 offset:20992\nds_read_b64_tr_b16 a[124:125], v92 offset:20488\nds_read_b64_tr_b16 a[126:127], v92 offset:21000\ns_waitcnt lgkmcnt(0)\ns_waitcnt vmcnt(28)\n" \
"s_barrier\ns_add_u32 m0, %[tb], 8192\ns_add_u32 s44, %[wo], 6815744\nbuffer_load_dwordx4 v90, %[rw], s44 offen nt lds\ns_add_u32 m0, %[tb], 9216\ns_add_u32 s44, %[wo], 6881280\nbuffer_load_dwordx4 v90, %[rw], s44 offen nt lds\ns_add_u32 m0, %[tb], 10240\n" \
"s_add_u32 s44, %[wo], 6946816\nbuffer_load_dwordx4 v90, %[rw], s44 offen nt lds\ns_add_u32 m0, %[tb], 11264\ns_add_u32 s44, %[wo], 7012352\nbuffer_load_dwordx4 v90, %[rw], s44 offen nt lds\nds_read_b64_tr_b16 a[128:129], v93 offset:0\nds_read_b64_tr_b16 a[130:131], v93 offset:512\nds_read_b64_tr_b16 v[76:77], v93 offset:8\n" \
"ds_read_b64_tr_b16 v[78:79], v93 offset:520\nds_read_b64_tr_b16 a[132:133], v93 offset:24576\nds_read_b64_tr_b16 a[134:135], v93 offset:25088\nds_read_b64_tr_b16 v[72:73], v93 offset:24584\nds_read_b64_tr_b16 v[74:75], v93 offset:25096\ns_waitcnt lgkmcnt(0)\ns_waitcnt vmcnt(28)\ns_barrier\n" \
"s_add_u32 m0, %[lw], 0\ns_add_u32 s44, %[wo], 7340032\nbuffer_load_dwordx4 v90, %[rw], s44 offen nt lds\ns_add_u32 m0, %[lw], 1024\ns_add_u32 s44, %[wo], 7405568\nbuffer_load_dwordx4 v90, %[rw], s44 offen nt lds\ns_add_u32 m0, %[lw], 2048\ns_add_u32 s44, %[wo], 7471104\n" \
"buffer_load_dwordx4 v90, %[rw], s44 offen nt lds\ns_add_u32 m0, %[lw], 3072\ns_add_u32 s44, %[wo], 7536640\nbuffer_load_dwordx4 v90, %[rw], s44 offen nt lds\nds_read_b64_tr_b16 a[152:153], v93 offset:4096\nds_read_b64_tr_b16 a[154:155], v93 offset:4608\nds_read_b64_tr_b16 a[168:169], v93 offset:4104\nds_read_b64_tr_b16 a[170:171], v93 offset:4616\n" \
"ds_read_b64_tr_b16 a[156:157], v93 offset:28672\nds_read_b64_tr_b16 a[158:159], v93 offset:29184\nds_read_b64_tr_b16 a[172:173], v93 offset:28680\nds_read_b64_tr_b16 a[174:175], v93 offset:29192\ns_waitcnt lgkmcnt(0)\ns_waitcnt vmcnt(28)\ns_barrier\ns_add_u32 m0, %[lw], 4096\n" \
"s_add_u32 s44, %[wo], 7864320\nbuffer_load_dwordx4 v90, %[rw], s44 offen nt lds\ns_add_u32 m0, %[lw], 5120\ns_add_u32 s44, %[wo], 7929856\nbuffer_load_dwordx4 v90, %[rw], s44 offen nt lds\ns_add_u32 m0, %[lw], 6144\ns_add_u32 s44, %[wo], 7995392\nbuffer_load_dwordx4 v90, %[rw], s44 offen nt lds\n" \
"s_add_u32 m0, %[lw], 7168\ns_add_u32 s44, %[wo], 8060928\nbuffer_load_dwordx4 v90, %[rw], s44 offen nt lds\nds_read_b64_tr_b16 a[184:185], v93 offset:8192\nds_read_b64_tr_b16 a[186:187], v93 offset:8704\nds_read_b64_tr_b16 a[192:193], v93 offset:8200\nds_read_b64_tr_b16 a[194:195], v93 offset:8712\nds_read_b64_tr_b16 a[188:189], v93 offset:32768\n" \
"ds_read_b64_tr_b16 a[190:191], v93 offset:33280\nds_read_b64_tr_b16 a[196:197], v93 offset:32776\nds_read_b64_tr_b16 a[198:199], v93 offset:33288\ns_waitcnt lgkmcnt(0)\ns_waitcnt vmcnt(28)\ns_barrier\ns_add_u32 m0, %[lw], 8192\ns_add_u32 s44, %[w2], 0\n" \
"buffer_load_dwordx4 v91, %[ro], s44 offen nt lds\ns_add_u32 m0, %[lw], 9216\ns_add_u32 s44, %[w2], 32768\nbuffer_load_dwordx4 v91, %[ro], s44 offen nt lds\ns_add_u32 m0, %[lw], 10240\ns_add_u32 s44, %[w2], 65536\nbuffer_load_dwordx4 v91, %[ro], s44 offen nt lds\ns_add_u32 m0, %[lw], 11264\n" \
"s_add_u32 s44, %[w2], 98304\nbuffer_load_dwordx4 v91, %[ro], s44 offen nt lds\nds_read_b64_tr_b16 v[68:69], v93 offset:12288\nds_read_b64_tr_b16 v[70:71], v93 offset:12800\nds_read_b64_tr_b16 a[216:217], v93 offset:12296\nds_read_b64_tr_b16 a[218:219], v93 offset:12808\nds_read_b64_tr_b16 v[64:65], v93 offset:36864\nds_read_b64_tr_b16 v[66:67], v93 offset:37376\n" \
"ds_read_b64_tr_b16 a[220:221], v93 offset:36872\nds_read_b64_tr_b16 a[222:223], v93 offset:37384\ns_waitcnt lgkmcnt(0)\ns_waitcnt vmcnt(28)\ns_barrier\ns_add_u32 m0, %[lw], 12288\ns_add_u32 s44, %[w2], 128\nbuffer_load_dwordx4 v91, %[ro], s44 offen nt lds\n" \
"s_add_u32 m0, %[lw], 13312\ns_add_u32 s44, %[w2], 32896\nbuffer_load_dwordx4 v91, %[ro], s44 offen nt lds\ns_add_u32 m0, %[lw], 14336\ns_add_u32 s44, %[w2], 65664\nbuffer_load_dwordx4 v91, %[ro], s44 offen nt lds\ns_add_u32 m0, %[lw], 15360\ns_add_u32 s44, %[w2], 98432\n" \
"buffer_load_dwordx4 v91, %[ro], s44 offen nt lds\nds_read_b64_tr_b16 a[224:225], v93 offset:16384\nds_read_b64_tr_b16 a[226:227], v93 offset:16896\nds_read_b64_tr_b16 a[240:241], v93 offset:16392\nds_read_b64_tr_b16 a[242:243], v93 offset:16904\nds_read_b64_tr_b16 a[228:229], v93 offset:40960\nds_read_b64_tr_b16 a[230:231], v93 offset:41472\nds_read_b64_tr_b16 a[244:245], v93 offset:40968\n" \
"ds_read_b64_tr_b16 a[246:247], v93 offset:41480\ns_waitcnt lgkmcnt(0)\ns_waitcnt vmcnt(28)\ns_barrier\ns_add_u32 m0, %[lw], 16384\ns_add_u32 s44, %[w2], 256\nbuffer_load_dwordx4 v91, %[ro], s44 offen nt lds\ns_add_u32 m0, %[lw], 17408\n" \
"s_add_u32 s44, %[w2], 33024\nbuffer_load_dwordx4 v91, %[ro], s44 offen nt lds\ns_add_u32 m0, %[lw], 18432\ns_add_u32 s44, %[w2], 65792\nbuffer_load_dwordx4 v91, %[ro], s44 offen nt lds\ns_add_u32 m0, %[lw], 19456\ns_add_u32 s44, %[w2], 98560\nbuffer_load_dwordx4 v91, %[ro], s44 offen nt lds\n" \
"ds_read_b64_tr_b16 v[96:97], v93 offset:20480\nds_read_b64_tr_b16 v[98:99], v93 offset:20992\nds_read_b64_tr_b16 v[112:113], v93 offset:20488\nds_read_b64_tr_b16 v[114:115], v93 offset:21000\nds_read_b64_tr_b16 v[100:101], v93 offset:45056\nds_read_b64_tr_b16 v[102:103], v93 offset:45568\nds_read_b64_tr_b16 v[116:117], v93 offset:45064\nds_read_b64_tr_b16 v[118:119], v93 offset:45576\n" \
"s_waitcnt lgkmcnt(0)\ns_waitcnt vmcnt(28)\ns_barrier\ns_add_u32 m0, %[lw], 20480\ns_add_u32 s44, %[w2], 384\nbuffer_load_dwordx4 v91, %[ro], s44 offen nt lds\ns_add_u32 m0, %[lw], 21504\ns_add_u32 s44, %[w2], 33152\n" \
"buffer_load_dwordx4 v91, %[ro], s44 offen nt lds\ns_add_u32 m0, %[lw], 22528\ns_add_u32 s44, %[w2], 65920\nbuffer_load_dwordx4 v91, %[ro], s44 offen nt lds\ns_add_u32 m0, %[lw], 23552\ns_add_u32 s44, %[w2], 98688\nbuffer_load_dwordx4 v91, %[ro], s44 offen nt lds\nds_read_b64_tr_b16 v[128:129], v92 offset:0\n" \
"ds_read_b64_tr_b16 v[130:131], v92 offset:512\nds_read_b64_tr_b16 v[60:61], v92 offset:8\nds_read_b64_tr_b16 v[62:63], v92 offset:520\nds_read_b64_tr_b16 v[132:133], v92 offset:12288\nds_read_b64_tr_b16 v[134:135], v92 offset:12800\nds_read_b64_tr_b16 v[56:57], v92 offset:12296\nds_read_b64_tr_b16 v[58:59], v92 offset:12808\ns_waitcnt lgkmcnt(0)\n" \
"s_waitcnt vmcnt(28)\ns_barrier\ns_add_u32 m0, %[tb], 0\ns_add_u32 s44, %[w2], 139264\nbuffer_load_dwordx4 v91, %[ro], s44 offen nt lds\ns_add_u32 m0, %[tb], 1024\ns_add_u32 s44, %[w2], 172032\nbuffer_load_dwordx4 v91, %[ro], s44 offen nt lds\n" \
"s_add_u32 m0, %[tb], 2048\ns_add_u32 s44, %[w2], 204800\nbuffer_load_dwordx4 v91, %[ro], s44 offen nt lds\ns_add_u32 m0, %[tb], 3072\ns_add_u32 s44, %[w2], 237568\nbuffer_load_dwordx4 v91, %[ro], s44 offen nt lds\nds_read_b64_tr_b16 v[144:145], v92 offset:4096\nds_read_b64_tr_b16 v[146:147], v92 offset:4608\n" \
"ds_read_b64_tr_b16 v[160:161], v92 offset:4104\nds_read_b64_tr_b16 v[162:163], v92 offset:4616\nds_read_b64_tr_b16 v[148:149], v92 offset:16384\nds_read_b64_tr_b16 v[150:151], v92 offset:16896\nds_read_b64_tr_b16 v[164:165], v92 offset:16392\nds_read_b64_tr_b16 v[166:167], v92 offset:16904\ns_waitcnt lgkmcnt(0)\ns_waitcnt vmcnt(28)\n" \
"s_barrier\ns_add_u32 m0, %[tb], 4096\ns_add_u32 s44, %[w2], 139392\nbuffer_load_dwordx4 v91, %[ro], s44 offen nt lds\ns_add_u32 m0, %[tb], 5120\ns_add_u32 s44, %[w2], 172160\nbuffer_load_dwordx4 v91, %[ro], s44 offen nt lds\ns_add_u32 m0, %[tb], 6144\n" \
"s_add_u32 s44, %[w2], 204928\nbuffer_load_dwordx4 v91, %[ro], s44 offen nt lds\ns_add_u32 m0, %[tb], 7168\ns_add_u32 s44, %[w2], 237696\nbuffer_load_dwordx4 v91, %[ro], s44 offen nt lds\nds_read_b64_tr_b16 v[168:169], v92 offset:8192\nds_read_b64_tr_b16 v[170:171], v92 offset:8704\nds_read_b64_tr_b16 v[184:185], v92 offset:8200\n" \
"ds_read_b64_tr_b16 v[186:187], v92 offset:8712\nds_read_b64_tr_b16 v[172:173], v92 offset:20480\nds_read_b64_tr_b16 v[174:175], v92 offset:20992\nds_read_b64_tr_b16 v[188:189], v92 offset:20488\nds_read_b64_tr_b16 v[190:191], v92 offset:21000\ns_waitcnt lgkmcnt(0)\ns_waitcnt vmcnt(28)\ns_barrier\n" \
"s_add_u32 m0, %[tb], 8192\ns_add_u32 s44, %[w2], 139520\nbuffer_load_dwordx4 v91, %[ro], s44 offen nt lds\ns_add_u32 m0, %[tb], 9216\ns_add_u32 s44, %[w2], 172288\nbuffer_load_dwordx4 v91, %[ro], s44 offen nt lds\ns_add_u32 m0, %[tb], 10240\ns_add_u32 s44, %[w2], 205056\n" \
"buffer_load_dwordx4 v91, %[ro], s44 offen nt lds\ns_add_u32 m0, %[tb], 11264\ns_add_u32 s44, %[w2], 237824\nbuffer_load_dwordx4 v91, %[ro], s44 offen nt lds\nds_read_b64_tr_b16 v[200:201], v93 offset:0\nds_read_b64_tr_b16 v[202:203], v93 offset:512\nds_read_b64_tr_b16 v[216:217], v93 offset:8\nds_read_b64_tr_b16 v[218:219], v93 offset:520\n" \
"ds_read_b64_tr_b16 v[204:205], v93 offset:24576\nds_read_b64_tr_b16 v[206:207], v93 offset:25088\nds_read_b64_tr_b16 v[220:221], v93 offset:24584\nds_read_b64_tr_b16 v[222:223], v93 offset:25096\ns_waitcnt lgkmcnt(0)\ns_waitcnt vmcnt(28)\ns_barrier\ns_add_u32 m0, %[lw], 0\n" \
"s_add_u32 s44, %[w2], 139648\nbuffer_load_dwordx4 v91, %[ro], s44 offen nt lds\ns_add_u32 m0, %[lw], 1024\ns_add_u32 s44, %[w2], 172416\nbuffer_load_dwordx4 v91, %[ro], s44 offen nt lds\ns_add_u32 m0, %[lw], 2048\ns_add_u32 s44, %[w2], 205184\nbuffer_load_dwordx4 v91, %[ro], s44 offen nt lds\n" \
"s_add_u32 m0, %[lw], 3072\ns_add_u32 s44, %[w2], 237952\nbuffer_load_dwordx4 v91, %[ro], s44 offen nt lds\nds_read_b64_tr_b16 v[232:233], v93 offset:4096\nds_read_b64_tr_b16 v[234:235], v93 offset:4608\nds_read_b64_tr_b16 v[240:241], v93 offset:4104\nds_read_b64_tr_b16 v[242:243], v93 offset:4616\nds_read_b64_tr_b16 v[236:237], v93 offset:28672\n" \
"ds_read_b64_tr_b16 v[238:239], v93 offset:29184\nds_read_b64_tr_b16 v[244:245], v93 offset:28680\nds_read_b64_tr_b16 v[246:247], v93 offset:29192\ns_waitcnt lgkmcnt(0)\ns_waitcnt vmcnt(28)\ns_barrier\ns_add_u32 m0, %[lw], 4096\ns_add_u32 s44, %[w2], 131072\n" \
"buffer_load_dwordx4 v91, %[ro], s44 offen nt lds\ns_add_u32 m0, %[lw], 5120\ns_add_u32 s44, %[w2], 163840\nbuffer_load_dwordx4 v91, %[ro], s44 offen nt lds\ns_add_u32 m0, %[lw], 6144\ns_add_u32 s44, %[w2], 196608\nbuffer_load_dwordx4 v91, %[ro], s44 offen nt lds\ns_add_u32 m0, %[lw], 7168\n" \
"s_add_u32 s44, %[w2], 229376\nbuffer_load_dwordx4 v91, %[ro], s44 offen nt lds\nds_read_b64_tr_b16 a[8:9], v95 offset:8192\nds_read_b64_tr_b16 a[10:11], v95 offset:8448\nds_read_b64_tr_b16 a[12:13], v95 offset:10240\nds_read_b64_tr_b16 a[14:15], v95 offset:10496\nds_read_b64_tr_b16 a[208:209], v95 offset:8256\nds_read_b64_tr_b16 a[210:211], v95 offset:8512\n" \
"ds_read_b64_tr_b16 a[212:213], v95 offset:10304\nds_read_b64_tr_b16 a[214:215], v95 offset:10560\ns_waitcnt lgkmcnt(0)\ns_waitcnt vmcnt(28)\ns_barrier\ns_add_u32 m0, %[lw], 8192\ns_add_u32 s44, %[w2], 131200\nbuffer_load_dwordx4 v91, %[ro], s44 offen nt lds\n" \
"s_add_u32 m0, %[lw], 9216\ns_add_u32 s44, %[w2], 163968\nbuffer_load_dwordx4 v91, %[ro], s44 offen nt lds\ns_add_u32 m0, %[lw], 10240\ns_add_u32 s44, %[w2], 196736\nbuffer_load_dwordx4 v91, %[ro], s44 offen nt lds\ns_add_u32 m0, %[lw], 11264\ns_add_u32 s44, %[w2], 229504\n" \
"buffer_load_dwordx4 v91, %[ro], s44 offen nt lds\nds_read_b64_tr_b16 a[56:57], v95 offset:12288\nds_read_b64_tr_b16 a[58:59], v95 offset:12544\nds_read_b64_tr_b16 a[60:61], v95 offset:14336\nds_read_b64_tr_b16 a[62:63], v95 offset:14592\nds_read_b64_tr_b16 v[104:105], v95 offset:12352\nds_read_b64_tr_b16 v[106:107], v95 offset:12608\nds_read_b64_tr_b16 v[108:109], v95 offset:14400\n" \
"ds_read_b64_tr_b16 v[110:111], v95 offset:14656\ns_waitcnt lgkmcnt(0)\ns_waitcnt vmcnt(28)\ns_barrier\ns_add_u32 m0, %[lw], 12288\ns_add_u32 s44, %[w2], 131328\nbuffer_load_dwordx4 v91, %[ro], s44 offen nt lds\ns_add_u32 m0, %[lw], 13312\n" \
"s_add_u32 s44, %[w2], 164096\nbuffer_load_dwordx4 v91, %[ro], s44 offen nt lds\ns_add_u32 m0, %[lw], 14336\ns_add_u32 s44, %[w2], 196864\nbuffer_load_dwordx4 v91, %[ro], s44 offen nt lds\ns_add_u32 m0, %[lw], 15360\ns_add_u32 s44, %[w2], 229632\nbuffer_load_dwordx4 v91, %[ro], s44 offen nt lds\n" \
"ds_read_b64_tr_b16 a[112:113], v95 offset:16384\nds_read_b64_tr_b16 a[114:115], v95 offset:16640\nds_read_b64_tr_b16 a[116:117], v95 offset:18432\nds_read_b64_tr_b16 a[118:119], v95 offset:18688\nds_read_b64_tr_b16 v[152:153], v95 offset:16448\nds_read_b64_tr_b16 v[154:155], v95 offset:16704\nds_read_b64_tr_b16 v[156:157], v95 offset:18496\nds_read_b64_tr_b16 v[158:159], v95 offset:18752\n" \
"s_waitcnt lgkmcnt(0)\ns_waitcnt vmcnt(28)\ns_barrier\ns_add_u32 m0, %[lw], 16384\ns_add_u32 s44, %[w2], 131456\nbuffer_load_dwordx4 v91, %[ro], s44 offen nt lds\ns_add_u32 m0, %[lw], 17408\ns_add_u32 s44, %[w2], 164224\n" \
"buffer_load_dwordx4 v91, %[ro], s44 offen nt lds\ns_add_u32 m0, %[lw], 18432\ns_add_u32 s44, %[w2], 196992\nbuffer_load_dwordx4 v91, %[ro], s44 offen nt lds\ns_add_u32 m0, %[lw], 19456\ns_add_u32 s44, %[w2], 229760\nbuffer_load_dwordx4 v91, %[ro], s44 offen nt lds\nds_read_b64_tr_b16 a[160:161], v95 offset:20480\n" \
"ds_read_b64_tr_b16 a[162:163], v95 offset:20736\nds_read_b64_tr_b16 a[164:165], v95 offset:22528\nds_read_b64_tr_b16 a[166:167], v95 offset:22784\nds_read_b64_tr_b16 v[208:209], v95 offset:20544\nds_read_b64_tr_b16 v[210:211], v95 offset:20800\nds_read_b64_tr_b16 v[212:213], v95 offset:22592\nds_read_b64_tr_b16 v[214:215], v95 offset:22848\ns_waitcnt lgkmcnt(0)\n" \
"s_waitcnt vmcnt(28)\ns_barrier\ns_add_u32 m0, %[lw], 20480\ns_add_u32 s44, %[w2], 8192\nbuffer_load_dwordx4 v91, %[ro], s44 offen nt lds\ns_add_u32 m0, %[lw], 21504\ns_add_u32 s44, %[w2], 40960\nbuffer_load_dwordx4 v91, %[ro], s44 offen nt lds\n" \
"s_add_u32 m0, %[lw], 22528\ns_add_u32 s44, %[w2], 73728\nbuffer_load_dwordx4 v91, %[ro], s44 offen nt lds\ns_add_u32 m0, %[lw], 23552\ns_add_u32 s44, %[w2], 106496\nbuffer_load_dwordx4 v91, %[ro], s44 offen nt lds\nds_read_b64_tr_b16 a[48:49], v94 offset:0\nds_read_b64_tr_b16 a[50:51], v94 offset:256\n" \
"ds_read_b64_tr_b16 a[52:53], v94 offset:2048\nds_read_b64_tr_b16 a[54:55], v94 offset:2304\nds_read_b64_tr_b16 a[248:249], v94 offset:64\nds_read_b64_tr_b16 a[250:251], v94 offset:320\nds_read_b64_tr_b16 a[252:253], v94 offset:2112\nds_read_b64_tr_b16 a[254:255], v94 offset:2368\ns_waitcnt lgkmcnt(0)\ns_waitcnt vmcnt(28)\n" \
"s_barrier\ns_add_u32 m0, %[tb], 0\ns_add_u32 s44, %[w2], 8320\nbuffer_load_dwordx4 v91, %[ro], s44 offen nt lds\ns_add_u32 m0, %[tb], 1024\ns_add_u32 s44, %[w2], 41088\nbuffer_load_dwordx4 v91, %[ro], s44 offen nt lds\ns_add_u32 m0, %[tb], 2048\n" \
"s_add_u32 s44, %[w2], 73856\nbuffer_load_dwordx4 v91, %[ro], s44 offen nt lds\ns_add_u32 m0, %[tb], 3072\ns_add_u32 s44, %[w2], 106624\nbuffer_load_dwordx4 v91, %[ro], s44 offen nt lds\nds_read_b64_tr_b16 a[96:97], v94 offset:4096\nds_read_b64_tr_b16 a[98:99], v94 offset:4352\nds_read_b64_tr_b16 a[100:101], v94 offset:6144\n" \
"ds_read_b64_tr_b16 a[102:103], v94 offset:6400\nds_read_b64_tr_b16 v[136:137], v94 offset:4160\nds_read_b64_tr_b16 v[138:139], v94 offset:4416\nds_read_b64_tr_b16 v[140:141], v94 offset:6208\nds_read_b64_tr_b16 v[142:143], v94 offset:6464\ns_waitcnt lgkmcnt(0)\nbuffer_load_dwordx2 v[12:13], off, s[36:39], s45 sc1\ns_waitcnt vmcnt(29)\n" \
"s_barrier\ns_add_u32 m0, %[tb], 4096\ns_add_u32 s44, %[w2], 8448\nbuffer_load_dwordx4 v91, %[ro], s44 offen nt lds\ns_add_u32 m0, %[tb], 5120\ns_add_u32 s44, %[w2], 41216\nbuffer_load_dwordx4 v91, %[ro], s44 offen nt lds\ns_add_u32 m0, %[tb], 6144\n" \
"s_add_u32 s44, %[w2], 73984\nbuffer_load_dwordx4 v91, %[ro], s44 offen nt lds\ns_add_u32 m0, %[tb], 7168\ns_add_u32 s44, %[w2], 106752\nbuffer_load_dwordx4 v91, %[ro], s44 offen nt lds\nds_read_b64_tr_b16 a[144:145], v94 offset:8192\nds_read_b64_tr_b16 a[146:147], v94 offset:8448\nds_read_b64_tr_b16 a[148:149], v94 offset:10240\n" \
"ds_read_b64_tr_b16 a[150:151], v94 offset:10496\nds_read_b64_tr_b16 v[192:193], v94 offset:8256\nds_read_b64_tr_b16 v[194:195], v94 offset:8512\nds_read_b64_tr_b16 v[196:197], v94 offset:10304\nds_read_b64_tr_b16 v[198:199], v94 offset:10560\ns_waitcnt lgkmcnt(0)\ns_waitcnt vmcnt(29)\ns_barrier\n" \
"s_add_u32 m0, %[tb], 8192\ns_add_u32 s44, %[w2], 8576\nbuffer_load_dwordx4 v91, %[ro], s44 offen nt lds\ns_add_u32 m0, %[tb], 9216\ns_add_u32 s44, %[w2], 41344\nbuffer_load_dwordx4 v91, %[ro], s44 offen nt lds\ns_add_u32 m0, %[tb], 10240\ns_add_u32 s44, %[w2], 74112\n" \
"buffer_load_dwordx4 v91, %[ro], s44 offen nt lds\ns_add_u32 m0, %[tb], 11264\ns_add_u32 s44, %[w2], 106880\nbuffer_load_dwordx4 v91, %[ro], s44 offen nt lds\nds_read_b64_tr_b16 a[200:201], v95 offset:0\nds_read_b64_tr_b16 a[202:203], v95 offset:256\nds_read_b64_tr_b16 a[204:205], v95 offset:2048\nds_read_b64_tr_b16 a[206:207], v95 offset:2304\n" \
"ds_read_b64_tr_b16 v[248:249], v95 offset:64\nds_read_b64_tr_b16 v[250:251], v95 offset:320\nds_read_b64_tr_b16 v[252:253], v95 offset:2112\nds_read_b64_tr_b16 v[254:255], v95 offset:2368\ns_waitcnt lgkmcnt(0)\ns_waitcnt vmcnt(29)\ns_barrier\nds_read_b64_tr_b16 v[52:53], v95 offset:4096\n" \
"ds_read_b64_tr_b16 v[54:55], v95 offset:4352\nds_read_b64_tr_b16 v[48:49], v95 offset:6144\nds_read_b64_tr_b16 v[50:51], v95 offset:6400\nds_read_b64_tr_b16 v[44:45], v95 offset:4160\nds_read_b64_tr_b16 v[46:47], v95 offset:4416\nds_read_b64_tr_b16 v[40:41], v95 offset:6208\nds_read_b64_tr_b16 v[42:43], v95 offset:6464\ns_waitcnt lgkmcnt(0)\n" \
"ds_write_b128 v1, v[84:87] offset:0\nds_write_b128 v1, v[80:83] offset:1024\nds_write_b128 v1, v[76:79] offset:2048\nds_write_b128 v1, v[72:75] offset:3072\ns_waitcnt vmcnt(25)\ns_barrier\nds_read_b64_tr_b16 a[72:73], v95 offset:8192\nds_read_b64_tr_b16 a[74:75], v95 offset:8448\n" \
"ds_read_b64_tr_b16 a[76:77], v95 offset:10240\nds_read_b64_tr_b16 a[78:79], v95 offset:10496\nds_read_b64_tr_b16 v[120:121], v95 offset:8256\nds_read_b64_tr_b16 v[122:123], v95 offset:8512\nds_read_b64_tr_b16 v[124:125], v95 offset:10304\nds_read_b64_tr_b16 v[126:127], v95 offset:10560\ns_waitcnt lgkmcnt(0)\nds_write_b128 v1, v[68:71] offset:4096\n" \
"ds_write_b128 v1, v[64:67] offset:5120\nds_write_b128 v1, v[60:63] offset:6144\nds_write_b128 v1, v[56:59] offset:7168\ns_waitcnt vmcnt(21)\ns_barrier\nds_read_b64_tr_b16 v[56:57], v95 offset:12288\nds_read_b64_tr_b16 v[58:59], v95 offset:12544\nds_read_b64_tr_b16 v[60:61], v95 offset:14336\n" \
"ds_read_b64_tr_b16 v[62:63], v95 offset:14592\nds_read_b64_tr_b16 v[64:65], v95 offset:12352\nds_read_b64_tr_b16 v[66:67], v95 offset:12608\nds_read_b64_tr_b16 v[68:69], v95 offset:14400\nds_read_b64_tr_b16 v[70:71], v95 offset:14656\ns_waitcnt lgkmcnt(0)\nds_write_b128 v1, v[52:55] offset:8192\nds_write_b128 v1, v[48:51] offset:9216\n" \
"ds_write_b128 v1, v[44:47] offset:10240\nds_write_b128 v1, v[40:43] offset:11264\ns_waitcnt vmcnt(17)\ns_barrier\nds_read_b64_tr_b16 a[176:177], v95 offset:16384\nds_read_b64_tr_b16 a[178:179], v95 offset:16640\nds_read_b64_tr_b16 a[180:181], v95 offset:18432\nds_read_b64_tr_b16 a[182:183], v95 offset:18688\n" \
"ds_read_b64_tr_b16 v[224:225], v95 offset:16448\nds_read_b64_tr_b16 v[226:227], v95 offset:16704\nds_read_b64_tr_b16 v[228:229], v95 offset:18496\nds_read_b64_tr_b16 v[230:231], v95 offset:18752\ns_waitcnt lgkmcnt(0)\nds_write_b128 v1, v[56:59] offset:12288\nds_write_b128 v1, v[60:63] offset:13312\nds_write_b128 v1, v[64:67] offset:14336\n" \
"ds_write_b128 v1, v[68:71] offset:15360\ns_waitcnt vmcnt(13)\ns_barrier\nds_read_b64_tr_b16 a[32:33], v95 offset:20480\nds_read_b64_tr_b16 a[34:35], v95 offset:20736\nds_read_b64_tr_b16 a[36:37], v95 offset:22528\nds_read_b64_tr_b16 a[38:39], v95 offset:22784\nds_read_b64_tr_b16 a[232:233], v95 offset:20544\n" \
"ds_read_b64_tr_b16 a[234:235], v95 offset:20800\nds_read_b64_tr_b16 a[236:237], v95 offset:22592\nds_read_b64_tr_b16 a[238:239], v95 offset:22848\ns_waitcnt lgkmcnt(0)\ns_waitcnt vmcnt(9)\ns_barrier\nds_read_b64_tr_b16 v[68:69], v94 offset:0\nds_read_b64_tr_b16 v[70:71], v94 offset:256\n" \
"ds_read_b64_tr_b16 v[64:65], v94 offset:2048\nds_read_b64_tr_b16 v[66:67], v94 offset:2304\nds_read_b64_tr_b16 v[60:61], v94 offset:64\nds_read_b64_tr_b16 v[62:63], v94 offset:320\nds_read_b64_tr_b16 v[56:57], v94 offset:2112\nds_read_b64_tr_b16 v[58:59], v94 offset:2368\ns_waitcnt lgkmcnt(0)\nds_write_b128 v1, v[68:71] offset:16384\n" \
"ds_write_b128 v1, v[64:67] offset:17408\nds_write_b128 v1, v[60:63] offset:18432\nds_write_b128 v1, v[56:59] offset:19456\ns_waitcnt vmcnt(4)\ns_barrier\nds_read_b64_tr_b16 a[136:137], v94 offset:4096\nds_read_b64_tr_b16 a[138:139], v94 offset:4352\nds_read_b64_tr_b16 a[140:141], v94 offset:6144\n" \
"ds_read_b64_tr_b16 a[142:143], v94 offset:6400\nds_read_b64_tr_b16 v[176:177], v94 offset:4160\nds_read_b64_tr_b16 v[178:179], v94 offset:4416\nds_read_b64_tr_b16 v[180:181], v94 offset:6208\nds_read_b64_tr_b16 v[182:183], v94 offset:6464\ns_waitcnt lgkmcnt(0)\ns_waitcnt vmcnt(0)\ns_barrier\n" \
"ds_read_b64_tr_b16 v[56:57], v94 offset:8192\nds_read_b64_tr_b16 v[58:59], v94 offset:8448\nds_read_b64_tr_b16 v[60:61], v94 offset:10240\nds_read_b64_tr_b16 v[62:63], v94 offset:10496\nds_read_b64_tr_b16 v[64:65], v94 offset:8256\nds_read_b64_tr_b16 v[66:67], v94 offset:8512\nds_read_b64_tr_b16 v[68:69], v94 offset:10304\nds_read_b64_tr_b16 v[70:71], v94 offset:10560\n" \
"s_waitcnt lgkmcnt(0)\ns_mov_b32 s43, 0\ns_mov_b32 s86, 110\ns_mov_b32 s89, 0\n.Lvu_hb%=:\nv_cmp_ne_u32 vcc, v15, v12\ns_cbranch_vccnz .Lvu_hb1%=\ns_sleep 4\n" \
"buffer_load_dwordx2 v[12:13], off, s[36:39], s45 sc1\ns_waitcnt vmcnt(0)\ns_add_u32 s43, s43, 1\ns_cmp_lt_u32 s43, 262144\ns_cbranch_scc1 .Lvu_hb%=\ns_branch .Lvu_abort%=\n.Lvu_hb1%=:\nv_readfirstlane_b32 s44, v13\n" \
"s_and_b32 s44, s44, 0x200000\ns_add_u32 s36, s36, s44\ns_addc_u32 s37, s37, 0\nds_write_b128 v1, v[56:59] offset:20480\nds_write_b128 v1, v[60:63] offset:21504\nds_write_b128 v1, v[64:67] offset:22528\nds_write_b128 v1, v[68:71] offset:23552\ns_waitcnt lgkmcnt(0)\n" \
"s_barrier\ns_mov_b32 s40, -1\ns_mov_b32 s43, 0\ns_lshl_b32 s57, %[w], 12\ns_mov_b32 s56, 0\ns_mov_b32 s42, 0\ns_mov_b32 s60, -2147483648\ns_mov_b32 s54, -2147483648\n" \
"s_add_u32 s44, s40, 1\ns_lshl_b32 s44, s44, 17\ns_add_u32 s44, s44, %[xn]\ns_lshr_b32 s45, s44, 8\ns_and_b32 s45, s45, 0x2000\ns_xor_b32 s41, s44, s45\ns_add_u32 s41, s41, %[xw]\n.Lvu_f0%=:\n" \
"buffer_load_dword v14, v8, s[36:39], s56 offen sc1\ns_waitcnt vmcnt(0)\nv_cmp_ge_i32 vcc, s40, v14\ns_cbranch_vccz .Lvu_f0fok%=\ns_add_u32 s43, s43, 1\ns_cmp_lt_u32 s43, 262144\ns_cbranch_scc1 .Lvu_f0%=\ns_branch .Lvu_abort%=\n" \
".Lvu_f0fok%=:\ns_add_u32 m0, s57, 0\ns_add_u32 s58, s41, 0\nbuffer_load_dwordx4 v0, s[36:39], s58 offen offset:0 sc1 lds\nbuffer_load_dwordx4 v0, s[36:39], s58 offen offset:1024 sc1 lds\nbuffer_load_dwordx4 v0, s[36:39], s58 offen offset:2048 sc1 lds\nbuffer_load_dwordx4 v0, s[36:39], s58 offen offset:3072 sc1 lds\ns_add_u32 m0, s57, 32768\n" \
"s_add_u32 s58, s41, 32768\nbuffer_load_dwordx4 v0, s[36:39], s58 offen offset:0 sc1 lds\nbuffer_load_dwordx4 v0, s[36:39], s58 offen offset:1024 sc1 lds\nbuffer_load_dwordx4 v0, s[36:39], s58 offen offset:2048 sc1 lds\nbuffer_load_dwordx4 v0, s[36:39], s58 offen offset:3072 sc1 lds\ns_waitcnt vmcnt(0)\ns_barrier\nds_read_b128 v[32:35], v2 offset:0\n" \
"ds_read_b128 v[36:39], v2 offset:2048\nds_read_b128 v[40:43], v2 offset:256\nds_read_b128 v[44:47], v2 offset:2304\n.p2align 6\n.Lvu_tok%=:\nds_read_b128 v[64:67], v2 offset:4096\nds_read_b128 v[68:71], v2 offset:6144\nds_read_b128 v[72:75], v2 offset:4352\n" \
"s_waitcnt lgkmcnt(6)\nv_mfma_f32_16x16x32_bf16 v[16:19], a[0:3], v[32:35], 0\ns_waitcnt lgkmcnt(5)\nv_cvt_pk_bf16_f32 v92, v80, v81\nv_cvt_pk_bf16_f32 v93, v82, v83\nv_mfma_f32_16x16x32_bf16 v[16:19], a[4:7], v[36:39], v[16:19]\nds_read_b128 v[76:79], v2 offset:6400\nv_cvt_pk_bf16_f32 v94, v84, v85\n" \
"v_cvt_pk_bf16_f32 v95, v86, v87\nv_mfma_f32_16x16x32_bf16 v[20:23], a[16:19], v[32:35], 0\nv_and_or_b32 v92, v92, -2, v13\nv_and_or_b32 v94, v94, -2, v13\nv_mfma_f32_16x16x32_bf16 v[20:23], a[20:23], v[36:39], v[20:23]\ns_waitcnt lgkmcnt(5)\nbuffer_store_dwordx4 v[92:95], v7, s[36:39], s60 offen offset:2560 sc1\nds_read_b128 v[32:35], v2 offset:8192\n" \
"v_mfma_f32_16x16x32_bf16 v[24:27], a[0:3], v[40:43], 0\ns_waitcnt lgkmcnt(5)\nds_read_b128 v[56:59], v1 offset:0\nds_read_b128 v[60:63], v1 offset:1024\nv_mfma_f32_16x16x32_bf16 v[24:27], a[4:7], v[44:47], v[24:27]\ns_add_u32 m0, s57, 16384\nds_read_b128 v[36:39], v2 offset:10240\nv_mfma_f32_16x16x32_bf16 v[28:31], a[16:19], v[40:43], 0\n" \
"s_add_u32 s60, s41, 65536\nbuffer_load_dwordx4 v0, s[36:39], s60 offen offset:0 sc1 lds\nv_mfma_f32_16x16x32_bf16 v[28:31], a[20:23], v[44:47], v[28:31]\ns_waitcnt lgkmcnt(7)\nds_read_b128 v[40:43], v2 offset:8448\nv_mfma_f32_16x16x32_bf16 v[16:19], a[24:27], v[64:67], v[16:19]\ns_waitcnt lgkmcnt(7)\nv_mfma_f32_16x16x32_bf16 v[16:19], a[28:31], v[68:71], v[16:19]\n" \
"ds_read_b128 v[44:47], v2 offset:10496\nv_mfma_f32_16x16x32_bf16 v[20:23], a[40:43], v[64:67], v[20:23]\nbuffer_load_dwordx4 v0, s[36:39], s60 offen offset:1024 sc1 lds\nv_mfma_f32_16x16x32_bf16 v[20:23], a[44:47], v[68:71], v[20:23]\ns_waitcnt lgkmcnt(7)\nds_read_b128 v[64:67], v2 offset:12288\nv_mfma_f32_16x16x32_bf16 v[24:27], a[24:27], v[72:75], v[24:27]\ns_waitcnt lgkmcnt(7)\n" \
"v_mfma_f32_16x16x32_bf16 v[24:27], a[28:31], v[76:79], v[24:27]\nds_read_b128 v[68:71], v2 offset:14336\nv_mfma_f32_16x16x32_bf16 v[28:31], a[40:43], v[72:75], v[28:31]\nbuffer_load_dwordx4 v0, s[36:39], s60 offen offset:2048 sc1 lds\nv_mfma_f32_16x16x32_bf16 v[28:31], a[44:47], v[76:79], v[28:31]\ns_waitcnt lgkmcnt(5)\nds_read_b128 v[72:75], v2 offset:12544\nv_mfma_f32_16x16x32_bf16 v[16:19], v[56:59], v[32:35], v[16:19]\n" \
"s_waitcnt lgkmcnt(5)\nv_mfma_f32_16x16x32_bf16 v[16:19], v[60:63], v[36:39], v[16:19]\nds_read_b128 v[76:79], v2 offset:14592\nv_mfma_f32_16x16x32_bf16 v[20:23], a[64:67], v[32:35], v[20:23]\nbuffer_load_dwordx4 v0, s[36:39], s60 offen offset:3072 sc1 lds\nv_mfma_f32_16x16x32_bf16 v[20:23], a[68:71], v[36:39], v[20:23]\ns_waitcnt lgkmcnt(5)\nds_read_b128 v[32:35], v2 offset:32768\n" \
"v_mfma_f32_16x16x32_bf16 v[24:27], v[56:59], v[40:43], v[24:27]\ns_add_u32 s56, s56, %[fs]\ns_add_u32 s62, s40, 1\ns_waitcnt lgkmcnt(5)\nv_mfma_f32_16x16x32_bf16 v[24:27], v[60:63], v[44:47], v[24:27]\ns_lshr_b32 m0, s57, 4\ns_add_u32 m0, m0, 159744\nds_read_b128 v[36:39], v2 offset:34816\n" \
"v_mfma_f32_16x16x32_bf16 v[28:31], a[64:67], v[40:43], v[28:31]\nbuffer_load_dword v8, s[36:39], s56 offen sc1 lds\nv_lshrrev_b32 v14, 2, v0\nv_mfma_f32_16x16x32_bf16 v[28:31], a[68:71], v[44:47], v[28:31]\ns_waitcnt lgkmcnt(5)\nds_read_b128 v[40:43], v2 offset:33024\nv_mfma_f32_16x16x32_bf16 v[16:19], a[80:83], v[64:67], v[16:19]\ns_waitcnt lgkmcnt(5)\n" \
"v_add_u32 v14, 159744, v14\nv_mfma_f32_16x16x32_bf16 v[16:19], a[84:87], v[68:71], v[16:19]\nds_read_b128 v[44:47], v2 offset:35072\nv_mfma_f32_16x16x32_bf16 v[20:23], a[88:91], v[64:67], v[20:23]\nds_read_b128 v[56:59], v1 offset:2048\nds_read_b128 v[60:63], v1 offset:3072\nv_mfma_f32_16x16x32_bf16 v[20:23], a[92:95], v[68:71], v[20:23]\ns_waitcnt lgkmcnt(7)\n" \
"ds_read_b128 v[64:67], v2 offset:36864\nv_mfma_f32_16x16x32_bf16 v[24:27], a[80:83], v[72:75], v[24:27]\ns_waitcnt lgkmcnt(7)\nv_mfma_f32_16x16x32_bf16 v[24:27], a[84:87], v[76:79], v[24:27]\nds_read_b128 v[68:71], v2 offset:38912\nv_mfma_f32_16x16x32_bf16 v[28:31], a[88:91], v[72:75], v[28:31]\nv_mfma_f32_16x16x32_bf16 v[28:31], a[92:95], v[76:79], v[28:31]\ns_waitcnt lgkmcnt(7)\n" \
"ds_read_b128 v[72:75], v2 offset:37120\nv_mfma_f32_16x16x32_bf16 v[16:19], a[104:107], v[32:35], v[16:19]\ns_waitcnt lgkmcnt(7)\nv_mfma_f32_16x16x32_bf16 v[16:19], a[108:111], v[36:39], v[16:19]\nds_read_b128 v[76:79], v2 offset:39168\nv_mfma_f32_16x16x32_bf16 v[20:23], a[120:123], v[32:35], v[20:23]\ns_barrier\nv_mfma_f32_16x16x32_bf16 v[20:23], a[124:127], v[36:39], v[20:23]\n" \
"s_waitcnt lgkmcnt(7)\ns_add_u32 m0, s57, 0\nds_read_b128 v[32:35], v2 offset:40960\nv_mfma_f32_16x16x32_bf16 v[24:27], a[104:107], v[40:43], v[24:27]\ns_add_u32 s60, s41, 98304\ns_waitcnt lgkmcnt(7)\nbuffer_load_dwordx4 v0, s[36:39], s60 offen offset:0 sc1 lds\nv_mfma_f32_16x16x32_bf16 v[24:27], a[108:111], v[44:47], v[24:27]\n" \
"ds_read_b128 v[36:39], v2 offset:43008\nv_mfma_f32_16x16x32_bf16 v[28:31], a[120:123], v[40:43], v[28:31]\nv_mfma_f32_16x16x32_bf16 v[28:31], a[124:127], v[44:47], v[28:31]\ns_waitcnt lgkmcnt(5)\nds_read_b128 v[40:43], v2 offset:41216\nv_mfma_f32_16x16x32_bf16 v[16:19], a[128:131], v[64:67], v[16:19]\ns_waitcnt lgkmcnt(5)\nbuffer_load_dwordx4 v0, s[36:39], s60 offen offset:1024 sc1 lds\n" \
"v_mfma_f32_16x16x32_bf16 v[16:19], a[132:135], v[68:71], v[16:19]\nds_read_b128 v[44:47], v2 offset:43264\nv_mfma_f32_16x16x32_bf16 v[20:23], v[56:59], v[64:67], v[20:23]\nv_mfma_f32_16x16x32_bf16 v[20:23], v[60:63], v[68:71], v[20:23]\ns_waitcnt lgkmcnt(5)\nds_read_b128 v[64:67], v2 offset:45056\nv_mfma_f32_16x16x32_bf16 v[24:27], a[128:131], v[72:75], v[24:27]\ns_waitcnt lgkmcnt(5)\n" \
"buffer_load_dwordx4 v0, s[36:39], s60 offen offset:2048 sc1 lds\nv_mfma_f32_16x16x32_bf16 v[24:27], a[132:135], v[76:79], v[24:27]\nds_read_b128 v[68:71], v2 offset:47104\nv_mfma_f32_16x16x32_bf16 v[28:31], v[56:59], v[72:75], v[28:31]\nv_mfma_f32_16x16x32_bf16 v[28:31], v[60:63], v[76:79], v[28:31]\ns_waitcnt lgkmcnt(5)\nds_read_b128 v[72:75], v2 offset:45312\nv_mfma_f32_16x16x32_bf16 v[16:19], a[152:155], v[32:35], v[16:19]\n" \
"s_waitcnt lgkmcnt(5)\nbuffer_load_dwordx4 v0, s[36:39], s60 offen offset:3072 sc1 lds\nv_mfma_f32_16x16x32_bf16 v[16:19], a[156:159], v[36:39], v[16:19]\ns_add_u32 s44, s40, 2\nds_read_b128 v[76:79], v2 offset:47360\nv_mfma_f32_16x16x32_bf16 v[20:23], a[168:171], v[32:35], v[20:23]\ns_lshl_b32 s44, s44, 17\nds_read_b128 v[56:59], v1 offset:4096\n" \
"ds_read_b128 v[60:63], v1 offset:5120\nv_mfma_f32_16x16x32_bf16 v[20:23], a[172:175], v[36:39], v[20:23]\ns_waitcnt lgkmcnt(7)\nv_mfma_f32_16x16x32_bf16 v[24:27], a[152:155], v[40:43], v[24:27]\ns_add_u32 s44, s44, %[xn]\ns_lshr_b32 s45, s44, 8\ns_waitcnt lgkmcnt(6)\nv_mfma_f32_16x16x32_bf16 v[24:27], a[156:159], v[44:47], v[24:27]\n" \
"v_mfma_f32_16x16x32_bf16 v[28:31], a[168:171], v[40:43], v[28:31]\ns_and_b32 s45, s45, 0x2000\ns_xor_b32 s41, s44, s45\nv_mfma_f32_16x16x32_bf16 v[28:31], a[172:175], v[44:47], v[28:31]\ns_waitcnt lgkmcnt(5)\nv_mfma_f32_16x16x32_bf16 v[16:19], a[184:187], v[64:67], v[16:19]\ns_waitcnt lgkmcnt(4)\nv_mfma_f32_16x16x32_bf16 v[16:19], a[188:191], v[68:71], v[16:19]\n" \
"v_mfma_f32_16x16x32_bf16 v[20:23], a[192:195], v[64:67], v[20:23]\ns_waitcnt lgkmcnt(2)\ns_waitcnt vmcnt(4)\ns_barrier\ns_add_u32 s41, s41, %[xw]\nds_read_b128 v[32:35], v2 offset:16384\nv_mfma_f32_16x16x32_bf16 v[20:23], a[196:199], v[68:71], v[20:23]\nds_read_b128 v[36:39], v2 offset:18432\n" \
"v_mfma_f32_16x16x32_bf16 v[24:27], a[184:187], v[72:75], v[24:27]\nds_read_b128 v[40:43], v2 offset:16640\nds_read_b32 v14, v14\nv_mfma_f32_16x16x32_bf16 v[24:27], a[188:191], v[76:79], v[24:27]\nds_read_b128 v[44:47], v2 offset:18688\nv_mfma_f32_16x16x32_bf16 v[28:31], a[192:195], v[72:75], v[28:31]\ns_waitcnt vmcnt(4)\nbuffer_store_dword v15, off, s[36:39], s54 sc1\n" \
"ds_read_b128 v[64:67], v2 offset:20480\nv_mfma_f32_16x16x32_bf16 v[28:31], a[196:199], v[76:79], v[28:31]\ns_waitcnt lgkmcnt(5)\nds_read_b128 v[68:71], v2 offset:22528\nv_mfma_f32_16x16x32_bf16 v[16:19], v[56:59], v[32:35], v[16:19]\ns_waitcnt lgkmcnt(3)\nds_read_b128 v[72:75], v2 offset:20736\nv_cmp_ge_i32 s[64:65], s62, v14\n" \
"v_mfma_f32_16x16x32_bf16 v[16:19], v[60:63], v[36:39], v[16:19]\ns_cmp_eq_u64 s[64:65], 0\ns_cselect_b32 s61, s41, -2147483648\nds_read_b128 v[76:79], v2 offset:22784\nv_mfma_f32_16x16x32_bf16 v[20:23], a[216:219], v[32:35], v[20:23]\ns_add_u32 m0, s57, 32768\ns_add_u32 s58, s61, 32768\nbuffer_load_dwordx4 v0, s[36:39], s58 offen offset:0 sc1 lds\n" \
"v_mfma_f32_16x16x32_bf16 v[20:23], a[220:223], v[36:39], v[20:23]\nds_read_b128 v[32:35], v2 offset:24576\nv_mfma_f32_16x16x32_bf16 v[24:27], v[56:59], v[40:43], v[24:27]\ns_waitcnt lgkmcnt(5)\nv_mfma_f32_16x16x32_bf16 v[24:27], v[60:63], v[44:47], v[24:27]\nds_read_b128 v[36:39], v2 offset:26624\nv_mfma_f32_16x16x32_bf16 v[28:31], a[216:219], v[40:43], v[28:31]\nbuffer_load_dwordx4 v0, s[36:39], s58 offen offset:1024 sc1 lds\n" \
"v_mfma_f32_16x16x32_bf16 v[28:31], a[220:223], v[44:47], v[28:31]\ns_waitcnt lgkmcnt(5)\nds_read_b128 v[40:43], v2 offset:24832\nv_mfma_f32_16x16x32_bf16 v[16:19], a[224:227], v[64:67], v[16:19]\ns_waitcnt lgkmcnt(5)\nds_read_b128 v[56:59], v1 offset:6144\nds_read_b128 v[60:63], v1 offset:7168\nv_mfma_f32_16x16x32_bf16 v[16:19], a[228:231], v[68:71], v[16:19]\n" \
"ds_read_b128 v[44:47], v2 offset:26880\nv_mfma_f32_16x16x32_bf16 v[20:23], a[240:243], v[64:67], v[20:23]\nbuffer_load_dwordx4 v0, s[36:39], s58 offen offset:2048 sc1 lds\nv_mfma_f32_16x16x32_bf16 v[20:23], a[244:247], v[68:71], v[20:23]\ns_waitcnt lgkmcnt(7)\nds_read_b128 v[64:67], v2 offset:28672\nv_mfma_f32_16x16x32_bf16 v[24:27], a[224:227], v[72:75], v[24:27]\ns_waitcnt lgkmcnt(7)\n" \
"v_mfma_f32_16x16x32_bf16 v[24:27], a[228:231], v[76:79], v[24:27]\nds_read_b128 v[68:71], v2 offset:30720\nv_mfma_f32_16x16x32_bf16 v[28:31], a[240:243], v[72:75], v[28:31]\nbuffer_load_dwordx4 v0, s[36:39], s58 offen offset:3072 sc1 lds\nv_mfma_f32_16x16x32_bf16 v[28:31], a[244:247], v[76:79], v[28:31]\ns_waitcnt lgkmcnt(7)\nds_read_b128 v[72:75], v2 offset:28928\nv_mfma_f32_16x16x32_bf16 v[16:19], v[96:99], v[32:35], v[16:19]\n" \
"s_waitcnt lgkmcnt(7)\nv_mfma_f32_16x16x32_bf16 v[16:19], v[100:103], v[36:39], v[16:19]\nds_read_b128 v[76:79], v2 offset:30976\nv_mfma_f32_16x16x32_bf16 v[20:23], v[112:115], v[32:35], v[20:23]\nv_mfma_f32_16x16x32_bf16 v[20:23], v[116:119], v[36:39], v[20:23]\ns_waitcnt lgkmcnt(7)\nv_mfma_f32_16x16x32_bf16 v[24:27], v[96:99], v[40:43], v[24:27]\ns_waitcnt lgkmcnt(4)\n" \
"v_mfma_f32_16x16x32_bf16 v[24:27], v[100:103], v[44:47], v[24:27]\nv_mfma_f32_16x16x32_bf16 v[28:31], v[112:115], v[40:43], v[28:31]\ns_waitcnt lgkmcnt(0)\ns_waitcnt vmcnt(5)\ns_barrier\ns_add_u32 m0, s57, 16384\nds_read_b128 v[32:35], v2 offset:0\nbuffer_load_dwordx4 v3, s[36:39], s42 offen offset:0 sc1 lds\n" \
"v_mfma_f32_16x16x32_bf16 v[28:31], v[116:119], v[44:47], v[28:31]\nds_read_b128 v[36:39], v2 offset:2048\nv_mfma_f32_16x16x32_bf16 v[16:19], v[128:131], v[64:67], v[16:19]\nds_read_b128 v[40:43], v2 offset:4096\nv_mfma_f32_16x16x32_bf16 v[16:19], v[132:135], v[68:71], v[16:19]\nds_read_b128 v[44:47], v2 offset:6144\nv_mfma_f32_16x16x32_bf16 v[20:23], v[56:59], v[64:67], v[20:23]\nbuffer_load_dwordx4 v3, s[36:39], s42 offen offset:1024 sc1 lds\n" \
"v_mfma_f32_16x16x32_bf16 v[20:23], v[60:63], v[68:71], v[20:23]\nds_read_b128 v[64:67], v2 offset:8192\nv_mfma_f32_16x16x32_bf16 v[24:27], v[128:131], v[72:75], v[24:27]\nv_mfma_f32_16x16x32_bf16 v[24:27], v[132:135], v[76:79], v[24:27]\nds_read_b128 v[68:71], v2 offset:10240\nv_mfma_f32_16x16x32_bf16 v[28:31], v[56:59], v[72:75], v[28:31]\nbuffer_load_dwordx4 v3, s[36:39], s42 offen offset:2048 sc1 lds\nv_mfma_f32_16x16x32_bf16 v[28:31], v[60:63], v[76:79], v[28:31]\n" \
"s_waitcnt lgkmcnt(5)\nds_read_b128 v[72:75], v2 offset:12288\nv_mfma_f32_16x16x32_bf16 v[16:19], v[144:147], v[32:35], v[16:19]\ns_waitcnt lgkmcnt(5)\nv_mfma_f32_16x16x32_bf16 v[16:19], v[148:151], v[36:39], v[16:19]\nds_read_b128 v[76:79], v2 offset:14336\nv_mfma_f32_16x16x32_bf16 v[20:23], v[160:163], v[32:35], v[20:23]\nbuffer_load_dwordx4 v3, s[36:39], s42 offen offset:3072 sc1 lds\n" \
"v_mfma_f32_16x16x32_bf16 v[20:23], v[164:167], v[36:39], v[20:23]\ns_waitcnt lgkmcnt(5)\nds_read_b128 v[32:35], v2 offset:256\nv_mfma_f32_16x16x32_bf16 v[16:19], v[168:171], v[40:43], v[16:19]\ns_waitcnt lgkmcnt(5)\nv_add_u32 v14, s57, v0\nv_mfma_f32_16x16x32_bf16 v[16:19], v[172:175], v[44:47], v[16:19]\nds_read_b128 v[36:39], v2 offset:2304\n" \
"v_mfma_f32_16x16x32_bf16 v[20:23], v[184:187], v[40:43], v[20:23]\nv_mov_b64 v[48:49], 0\nv_mfma_f32_16x16x32_bf16 v[20:23], v[188:191], v[44:47], v[20:23]\ns_waitcnt lgkmcnt(5)\nds_read_b128 v[40:43], v2 offset:4352\nv_mov_b64 v[50:51], 0\nv_mfma_f32_16x16x32_bf16 v[16:19], v[200:203], v[64:67], v[16:19]\ns_waitcnt lgkmcnt(5)\n" \
"v_mov_b64 v[52:53], 0\nv_mfma_f32_16x16x32_bf16 v[16:19], v[204:207], v[68:71], v[16:19]\nds_read_b128 v[44:47], v2 offset:6400\nv_mov_b64 v[54:55], 0\nv_mfma_f32_16x16x32_bf16 v[20:23], v[216:219], v[64:67], v[20:23]\nds_read_b128 v[56:59], v1 offset:0\nds_read_b128 v[60:63], v1 offset:1024\nv_mfma_f32_16x16x32_bf16 v[20:23], v[220:223], v[68:71], v[20:23]\n" \
"s_waitcnt lgkmcnt(7)\nds_read_b128 v[64:67], v2 offset:8448\nv_mfma_f32_16x16x32_bf16 v[16:19], v[232:235], v[72:75], v[16:19]\ns_waitcnt lgkmcnt(7)\nv_mfma_f32_16x16x32_bf16 v[16:19], v[236:239], v[76:79], v[16:19]\nds_read_b128 v[68:71], v2 offset:10496\nv_mfma_f32_16x16x32_bf16 v[20:23], v[240:243], v[72:75], v[20:23]\nv_mfma_f32_16x16x32_bf16 v[20:23], v[244:247], v[76:79], v[20:23]\n" \
"s_waitcnt lgkmcnt(7)\nv_mfma_f32_16x16x32_bf16 v[24:27], v[144:147], v[32:35], v[24:27]\ns_waitcnt vmcnt(0)\ns_waitcnt lgkmcnt(6)\nds_read_b128 v[80:83], v14 offset:16384\nds_read_b128 v[84:87], v14 offset:17408\nv_mfma_f32_16x16x32_bf16 v[24:27], v[148:151], v[36:39], v[24:27]\nv_cvt_pk_bf16_f32 v72, v16, v17\n" \
"v_mfma_f32_16x16x32_bf16 v[28:31], v[160:163], v[32:35], v[28:31]\nds_read_b128 v[88:91], v14 offset:18432\nds_read_b128 v[92:95], v14 offset:19456\nv_cvt_pk_bf16_f32 v73, v18, v19\nv_mfma_f32_16x16x32_bf16 v[28:31], v[164:167], v[36:39], v[28:31]\ns_waitcnt lgkmcnt(9)\nds_read_b128 v[32:35], v2 offset:12544\nv_cvt_pk_bf16_f32 v74, v20, v21\n" \
"v_mfma_f32_16x16x32_bf16 v[24:27], v[168:171], v[40:43], v[24:27]\ns_waitcnt lgkmcnt(3)\nv_max3_u32 v76, v80, v82, v84\nv_cvt_pk_bf16_f32 v75, v22, v23\nv_mfma_f32_16x16x32_bf16 v[24:27], v[172:175], v[44:47], v[24:27]\ns_waitcnt lgkmcnt(2)\nds_read_b128 v[36:39], v2 offset:14592\nv_max3_u32 v76, v76, v86, v88\n" \
"v_mfma_f32_16x16x32_bf16 v[28:31], v[184:187], v[40:43], v[28:31]\ns_waitcnt lgkmcnt(2)\nds_write2st64_b64 v9, v[72:73], v[74:75] offset0:0 offset1:8\nv_max3_u32 v76, v76, v90, v92\nv_max_u32 v76, v76, v94\nv_mfma_f32_16x16x32_bf16 v[28:31], v[188:191], v[44:47], v[28:31]\nv_cmp_eq_u32 vcc, -1, v76\nv_mfma_f32_16x16x32_bf16 v[24:27], v[200:203], v[64:67], v[24:27]\n" \
"s_cbranch_vccnz .Lvu_re%=\n.Lvu_rer%=:\ns_barrier\ns_memrealtime s[68:69]\nds_read_b128 v[80:83], v0 offset:16384\nv_mfma_f32_16x16x32_bf16 v[24:27], v[204:207], v[68:71], v[24:27]\nds_read_b128 v[84:87], v0 offset:17408\nv_mfma_f32_16x16x32_bf16 v[28:31], v[216:219], v[64:67], v[28:31]\n" \
"ds_read_b128 v[88:91], v0 offset:18432\nv_mfma_f32_16x16x32_bf16 v[28:31], v[220:223], v[68:71], v[28:31]\ns_waitcnt lgkmcnt(5)\nds_read_b128 v[92:95], v0 offset:19456\nv_mfma_f32_16x16x32_bf16 v[24:27], v[232:235], v[32:35], v[24:27]\ns_waitcnt lgkmcnt(5)\nds_read_b128 v[16:19], v0 offset:20480\nv_mfma_f32_16x16x32_bf16 v[24:27], v[236:239], v[36:39], v[24:27]\n" \
"ds_read_b128 v[20:23], v0 offset:21504\nv_mfma_f32_16x16x32_bf16 v[28:31], v[240:243], v[32:35], v[28:31]\nds_read_b128 v[40:43], v0 offset:22528\nv_mfma_f32_16x16x32_bf16 v[28:31], v[244:247], v[36:39], v[28:31]\ns_cmp_lt_i32 s40, 0\nds_read_b128 v[44:47], v0 offset:23552\ns_cbranch_scc1 .Lvu_first%=\n.Lvu_crit%=:\n" \
"s_waitcnt lgkmcnt(7)\nv_smfmac_f32_16x16x64_bf16 v[48:51], v[80:83], a[0:7], v4\ns_waitcnt lgkmcnt(6)\nds_read_b128 v[64:67], v1 offset:4096\nds_read_b128 v[32:35], v0 offset:24576\nv_mov_b32 v14, s40\nv_smfmac_f32_16x16x64_bf16 v[48:51], v[84:87], a[24:31], v4\ns_waitcnt lgkmcnt(7)\n" \
"ds_read_b128 v[68:71], v1 offset:5120\nds_read_b128 v[36:39], v0 offset:25600\nv_cvt_pk_bf16_f32 v76, v24, v25\nv_smfmac_f32_16x16x64_bf16 v[48:51], v[88:91], v[56:63], v4\ns_sub_u32 s88, s86, s89\ns_min_u32 s88, s88, 150\ns_waitcnt lgkmcnt(8)\nds_read_b128 v[80:83], v0 offset:28672\n" \
"v_cvt_pk_bf16_f32 v77, v26, v27\nv_smfmac_f32_16x16x64_bf16 v[48:51], v[92:95], a[80:87], v4\ns_waitcnt lgkmcnt(8)\nds_read_b128 v[84:87], v0 offset:29696\nv_cvt_pk_bf16_f32 v78, v28, v29\nv_smfmac_f32_16x16x64_bf16 v[48:51], v[16:19], a[104:111], v4\ns_add_u32 s88, s88, s68\ns_mov_b32 s89, s68\n" \
"s_waitcnt lgkmcnt(8)\nds_read_b128 v[88:91], v0 offset:30720\nv_cvt_pk_bf16_f32 v79, v30, v31\nv_smfmac_f32_16x16x64_bf16 v[48:51], v[20:23], a[128:135], v4\ns_waitcnt lgkmcnt(8)\nds_read_b128 v[92:95], v0 offset:31744\nv_mov_b32 v15, s88\nv_smfmac_f32_16x16x64_bf16 v[48:51], v[40:43], a[152:159], v4\n" \
"s_waitcnt lgkmcnt(8)\nds_read_b128 v[24:27], v0 offset:26624\nbuffer_store_dwordx2 v[14:15], off, s[36:39], %[sg] sc1\nv_smfmac_f32_16x16x64_bf16 v[48:51], v[44:47], a[184:191], v4\ns_waitcnt lgkmcnt(7)\ns_waitcnt lgkmcnt(6)\nds_read_b128 v[28:31], v0 offset:27648\nv_smfmac_f32_16x16x64_bf16 v[48:51], v[32:35], v[64:71], v4\n" \
"s_waitcnt lgkmcnt(6)\nds_read_b128 v[56:59], v1 offset:2048\nv_smfmac_f32_16x16x64_bf16 v[48:51], v[36:39], a[224:231], v4\ns_waitcnt lgkmcnt(6)\nds_read_b128 v[60:63], v1 offset:3072\nv_smfmac_f32_16x16x64_bf16 v[48:51], v[80:83], v[144:151], v4\ns_waitcnt lgkmcnt(6)\nds_write2st64_b64 v9, v[76:77], v[78:79] offset0:2 offset1:10\n" \
"v_smfmac_f32_16x16x64_bf16 v[48:51], v[84:87], v[168:175], v4\ns_waitcnt lgkmcnt(6)\nv_smfmac_f32_16x16x64_bf16 v[48:51], v[88:91], v[200:207], v4\ns_waitcnt lgkmcnt(5)\nv_smfmac_f32_16x16x64_bf16 v[48:51], v[92:95], v[232:239], v4\ns_waitcnt lgkmcnt(4)\nv_smfmac_f32_16x16x64_bf16 v[48:51], v[24:27], v[96:103], v4\ns_waitcnt lgkmcnt(3)\n" \
"ds_read_b128 v[64:67], v1 offset:6144\nv_smfmac_f32_16x16x64_bf16 v[48:51], v[28:31], v[128:135], v4\nds_read_b128 v[68:71], v1 offset:7168\nv_smfmac_f32_16x16x64_bf16 v[52:55], v[80:83], v[160:167], v4\nv_smfmac_f32_16x16x64_bf16 v[52:55], v[84:87], v[184:191], v4\nv_add_f32 v48, v48, v50\nv_smfmac_f32_16x16x64_bf16 v[52:55], v[88:91], v[216:223], v4\nds_read_b128 v[80:83], v0 offset:16384\n" \
"v_add_f32 v49, v49, v51\nv_smfmac_f32_16x16x64_bf16 v[52:55], v[92:95], v[240:247], v4\nds_read_b128 v[84:87], v0 offset:17408\nv_cvt_pk_bf16_f32 v14, v48, v49\nv_smfmac_f32_16x16x64_bf16 v[52:55], v[16:19], a[120:127], v4\ns_waitcnt lgkmcnt(5)\nds_write_b16 v5, v14 offset:0\nds_write_b16_d16_hi v5, v14 offset:16\n" \
"v_smfmac_f32_16x16x64_bf16 v[52:55], v[20:23], v[56:63], v4\nds_read_b128 v[88:91], v0 offset:18432\nv_mov_b64 v[16:17], 0\nv_smfmac_f32_16x16x64_bf16 v[52:55], v[24:27], v[112:119], v4\ns_waitcnt lgkmcnt(5)\nds_read_b128 v[92:95], v0 offset:19456\nv_mov_b64 v[18:19], 0\nv_smfmac_f32_16x16x64_bf16 v[52:55], v[28:31], v[64:71], v4\n" \
"ds_read_b128 v[56:59], v1 offset:8192\nv_mov_b64 v[20:21], 0\nv_smfmac_f32_16x16x64_bf16 v[52:55], v[32:35], a[216:223], v4\ns_waitcnt lgkmcnt(3)\nds_read_b128 v[60:63], v1 offset:9216\ns_barrier\nds_read_b128 v[72:75], v11 offset:57344\nv_smfmac_f32_16x16x64_bf16 v[52:55], v[36:39], a[240:247], v4\n" \
"ds_read_b128 v[76:79], v11 offset:58368\nv_mov_b64 v[22:23], 0\nv_smfmac_f32_16x16x64_bf16 v[52:55], v[40:43], a[168:175], v4\nv_mov_b64 v[24:25], 0\nv_smfmac_f32_16x16x64_bf16 v[52:55], v[44:47], a[192:199], v4\nds_read_b128 v[64:67], v1 offset:12288\nv_mov_b64 v[26:27], 0\nv_smfmac_f32_16x16x64_bf16 v[52:55], v[80:83], a[16:23], v4\n" \
"ds_read_b128 v[68:71], v1 offset:13312\nv_mov_b64 v[28:29], 0\nv_smfmac_f32_16x16x64_bf16 v[52:55], v[84:87], a[40:47], v4\ns_waitcnt lgkmcnt(7)\nv_mov_b64 v[30:31], 0\nv_smfmac_f32_16x16x64_bf16 v[52:55], v[88:91], a[64:71], v4\ns_waitcnt lgkmcnt(6)\nv_mov_b64 v[32:33], 0\n" \
"v_smfmac_f32_16x16x64_bf16 v[52:55], v[92:95], a[88:95], v4\ns_waitcnt lgkmcnt(3)\nv_mov_b64 v[34:35], 0\nv_smfmac_f32_16x16x64_bf16 v[16:19], v[72:75], a[8:15], v10\ns_waitcnt lgkmcnt(2)\nv_mov_b64 v[36:37], 0\nv_smfmac_f32_16x16x64_bf16 v[16:19], v[76:79], v[56:63], v10\nds_read_b128 v[56:59], v1 offset:10240\n" \
"v_add_f32 v52, v52, v54\nv_smfmac_f32_16x16x64_bf16 v[20:23], v[72:75], a[56:63], v10\nds_read_b128 v[60:63], v1 offset:11264\nv_add_f32 v53, v53, v55\nv_smfmac_f32_16x16x64_bf16 v[20:23], v[76:79], a[72:79], v10\nv_cvt_pk_bf16_f32 v14, v52, v53\nv_smfmac_f32_16x16x64_bf16 v[24:27], v[72:75], a[112:119], v10\ns_waitcnt lgkmcnt(2)\n" \
"ds_write_b16 v5, v14 offset:2048\nds_write_b16_d16_hi v5, v14 offset:2064\nv_smfmac_f32_16x16x64_bf16 v[24:27], v[76:79], v[64:71], v10\nds_read_b128 v[64:67], v1 offset:14336\nv_mov_b64 v[38:39], 0\nv_smfmac_f32_16x16x64_bf16 v[28:31], v[72:75], a[160:167], v10\nds_read_b128 v[68:71], v1 offset:15360\nv_mov_b64 v[40:41], 0\n" \
"v_smfmac_f32_16x16x64_bf16 v[28:31], v[76:79], a[176:183], v10\nv_mov_b64 v[42:43], 0\nv_smfmac_f32_16x16x64_bf16 v[32:35], v[72:75], a[208:215], v10\ns_waitcnt lgkmcnt(2)\ns_barrier\ns_mov_b32 s59, 0\nds_read_b128 v[80:83], v11 offset:59392\nv_smfmac_f32_16x16x64_bf16 v[32:35], v[76:79], v[56:63], v10\n" \
"s_add_u32 m0, s57, 0\ns_add_u32 s58, s61, 0\nds_read_b128 v[56:59], v1 offset:16384\nbuffer_load_dwordx4 v0, s[36:39], s58 offen offset:0 sc1 lds\nds_read_b128 v[84:87], v11 offset:60416\nv_smfmac_f32_16x16x64_bf16 v[36:39], v[72:75], v[104:111], v10\nds_read_b128 v[60:63], v1 offset:17408\nv_mov_b64 v[44:45], 0\n" \
"v_smfmac_f32_16x16x64_bf16 v[36:39], v[76:79], v[120:127], v10\nv_mov_b64 v[46:47], 0\nv_smfmac_f32_16x16x64_bf16 v[40:43], v[72:75], v[152:159], v10\ns_waitcnt lgkmcnt(4)\nbuffer_load_dwordx4 v0, s[36:39], s58 offen offset:1024 sc1 lds\nv_smfmac_f32_16x16x64_bf16 v[40:43], v[76:79], v[64:71], v10\nds_read_b128 v[64:67], v1 offset:20480\nv_mov_b64 v[48:49], 0\n" \
"v_smfmac_f32_16x16x64_bf16 v[44:47], v[72:75], v[208:215], v10\nds_read_b128 v[68:71], v1 offset:21504\nv_mov_b64 v[50:51], 0\nv_smfmac_f32_16x16x64_bf16 v[44:47], v[76:79], v[224:231], v10\ns_waitcnt lgkmcnt(5)\nbuffer_load_dwordx4 v0, s[36:39], s58 offen offset:2048 sc1 lds\nv_smfmac_f32_16x16x64_bf16 v[16:19], v[80:83], a[32:39], v10\ns_waitcnt lgkmcnt(3)\n" \
"ds_read_b128 v[72:75], v11 offset:49152\nv_mov_b64 v[52:53], 0\nv_smfmac_f32_16x16x64_bf16 v[16:19], v[84:87], a[48:55], v10\ns_waitcnt lgkmcnt(3)\nds_read_b128 v[76:79], v11 offset:51200\nv_mov_b64 v[54:55], 0\nv_smfmac_f32_16x16x64_bf16 v[20:23], v[80:83], v[56:63], v10\nds_read_b128 v[56:59], v1 offset:18432\n" \
"buffer_load_dwordx4 v0, s[36:39], s58 offen offset:3072 sc1 lds\nv_smfmac_f32_16x16x64_bf16 v[20:23], v[84:87], a[96:103], v10\nds_read_b128 v[60:63], v1 offset:19456\nv_cvt_pk_bf16_f32 v88, v16, v17\nv_smfmac_f32_16x16x64_bf16 v[24:27], v[80:83], a[136:143], v10\nv_and_or_b32 v88, v88, -2, v12\nv_cvt_pk_bf16_f32 v89, v18, v19\nv_smfmac_f32_16x16x64_bf16 v[24:27], v[84:87], a[144:151], v10\n" \
"s_waitcnt lgkmcnt(4)\nv_cvt_pk_bf16_f32 v90, v20, v21\nv_cvt_pk_bf16_f32 v91, v22, v23\nv_and_or_b32 v90, v90, -2, v12\nv_smfmac_f32_16x16x64_bf16 v[28:31], v[80:83], v[64:71], v10\nds_read_b128 v[64:67], v1 offset:22528\nv_permlane16_swap_b32 v89, v91\nv_smfmac_f32_16x16x64_bf16 v[28:31], v[84:87], a[200:207], v10\n" \
"ds_read_b128 v[68:71], v1 offset:23552\nv_permlane16_swap_b32 v88, v90\nv_smfmac_f32_16x16x64_bf16 v[32:35], v[80:83], a[232:239], v10\ns_add_u32 s63, s59, 0\nds_read_b128 v[16:19], v11 offset:53248\nv_cvt_pk_bf16_f32 v92, v24, v25\nv_and_or_b32 v92, v92, -2, v12\nv_smfmac_f32_16x16x64_bf16 v[32:35], v[84:87], a[248:255], v10\n" \
"s_waitcnt lgkmcnt(3)\nds_read_b128 v[20:23], v11 offset:55296\nbuffer_store_dwordx4 v[88:91], v6, s[36:39], s63 offen sc1\nv_cvt_pk_bf16_f32 v93, v26, v27\nv_cvt_pk_bf16_f32 v94, v28, v29\nv_cvt_pk_bf16_f32 v95, v30, v31\nv_and_or_b32 v94, v94, -2, v12\nv_smfmac_f32_16x16x64_bf16 v[36:39], v[80:83], v[56:63], v10\n" \
"ds_read_b128 v[56:59], v1 offset:8192\nv_permlane16_swap_b32 v93, v95\nv_smfmac_f32_16x16x64_bf16 v[36:39], v[84:87], v[136:143], v10\nds_read_b128 v[60:63], v1 offset:9216\nv_permlane16_swap_b32 v92, v94\nv_smfmac_f32_16x16x64_bf16 v[40:43], v[80:83], v[176:183], v10\ns_add_u32 s63, s59, 131072\nds_read_b128 v[24:27], v11 offset:50176\n" \
"v_cvt_pk_bf16_f32 v88, v32, v33\nv_and_or_b32 v88, v88, -2, v12\nv_smfmac_f32_16x16x64_bf16 v[40:43], v[84:87], v[192:199], v10\ns_waitcnt lgkmcnt(5)\nds_read_b128 v[28:31], v11 offset:52224\nbuffer_store_dwordx4 v[92:95], v6, s[36:39], s63 offen sc1\nv_cvt_pk_bf16_f32 v89, v34, v35\nv_cvt_pk_bf16_f32 v90, v36, v37\n" \
"v_cvt_pk_bf16_f32 v91, v38, v39\nv_and_or_b32 v90, v90, -2, v12\nv_smfmac_f32_16x16x64_bf16 v[44:47], v[80:83], v[64:71], v10\nds_read_b128 v[64:67], v1 offset:16384\nv_permlane16_swap_b32 v89, v91\nv_smfmac_f32_16x16x64_bf16 v[44:47], v[84:87], v[248:255], v10\ns_cmp_ge_i32 s40, 63\nds_read_b128 v[68:71], v1 offset:17408\n" \
"v_permlane16_swap_b32 v88, v90\ns_cbranch_scc1 .Lvu_last%=\n.Lvu_y%=:\nv_smfmac_f32_16x16x64_bf16 v[48:51], v[72:75], a[8:15], v10\ns_waitcnt lgkmcnt(4)\nds_read_b128 v[32:35], v11 offset:54272\nds_read_b128 v[36:39], v11 offset:56320\nv_cvt_pk_bf16_f32 v92, v40, v41\n" \
"v_and_or_b32 v92, v92, -2, v12\nv_smfmac_f32_16x16x64_bf16 v[48:51], v[76:79], v[56:63], v10\ns_add_u32 s63, s59, 4096\nbuffer_store_dwordx4 v[88:91], v6, s[36:39], s63 offen sc1\nv_cvt_pk_bf16_f32 v93, v42, v43\nv_cvt_pk_bf16_f32 v94, v44, v45\nv_cvt_pk_bf16_f32 v95, v46, v47\nv_and_or_b32 v94, v94, -2, v12\n" \
"v_smfmac_f32_16x16x64_bf16 v[48:51], v[16:19], a[32:39], v10\ns_add_u32 s40, s40, 1\ns_and_b32 s44, s40, 1\nv_permlane16_swap_b32 v93, v95\nv_smfmac_f32_16x16x64_bf16 v[48:51], v[20:23], a[48:55], v10\ns_lshl_b32 s55, s44, 22\ns_waitcnt lgkmcnt(5)\nv_permlane16_swap_b32 v92, v94\n" \
"v_smfmac_f32_16x16x64_bf16 v[52:55], v[24:27], a[8:15], v10\ns_waitcnt lgkmcnt(4)\ns_add_u32 s63, s59, 135168\nv_smfmac_f32_16x16x64_bf16 v[52:55], v[28:31], v[56:63], v10\ns_waitcnt lgkmcnt(1)\nbuffer_store_dwordx4 v[92:95], v6, s[36:39], s63 offen sc1\nv_mov_b64 v[80:81], 0\nv_mov_b64 v[82:83], 0\n" \
"v_mov_b64 v[84:85], 0\nv_mov_b64 v[86:87], 0\nv_smfmac_f32_16x16x64_bf16 v[52:55], v[32:35], a[32:39], v10\ns_memrealtime s[86:87]\ns_bfe_u32 s45, s40, 0x10001\ns_waitcnt lgkmcnt(0)\nv_smfmac_f32_16x16x64_bf16 v[52:55], v[36:39], a[48:55], v10\nds_read_b128 v[56:59], v1 offset:12288\n" \
"v_mov_b32 v13, s45\nv_smfmac_f32_16x16x64_bf16 v[80:83], v[72:75], a[56:63], v10\nds_read_b128 v[60:63], v1 offset:13312\nv_cvt_pk_bf16_f32 v88, v48, v49\nv_smfmac_f32_16x16x64_bf16 v[80:83], v[76:79], a[72:79], v10\nv_cvt_pk_bf16_f32 v89, v50, v51\nv_smfmac_f32_16x16x64_bf16 v[80:83], v[16:19], v[64:71], v10\nv_cvt_pk_bf16_f32 v90, v52, v53\n" \
"v_smfmac_f32_16x16x64_bf16 v[80:83], v[20:23], a[96:103], v10\ns_add_u32 s60, s55, 0\nv_cvt_pk_bf16_f32 v91, v54, v55\nv_smfmac_f32_16x16x64_bf16 v[84:87], v[24:27], a[56:63], v10\nv_and_or_b32 v88, v88, -2, v13\nv_smfmac_f32_16x16x64_bf16 v[84:87], v[28:31], a[72:79], v10\ns_and_b32 s42, s40, 3\ns_lshl_b32 s42, s42, 15\n" \
"v_and_or_b32 v90, v90, -2, v13\nv_smfmac_f32_16x16x64_bf16 v[84:87], v[32:35], v[64:71], v10\nbuffer_store_dwordx4 v[88:91], v7, s[36:39], s60 offen offset:0 sc1\nv_mov_b64 v[48:49], 0\nv_mov_b64 v[50:51], 0\nv_mov_b64 v[52:53], 0\nv_mov_b32 v12, s44\nv_smfmac_f32_16x16x64_bf16 v[84:87], v[36:39], a[96:103], v10\n" \
"ds_read_b128 v[64:67], v1 offset:20480\nv_mov_b64 v[54:55], 0\nv_smfmac_f32_16x16x64_bf16 v[48:51], v[72:75], a[112:119], v10\ns_waitcnt lgkmcnt(1)\nds_read_b128 v[68:71], v1 offset:21504\nv_cvt_pk_bf16_f32 v92, v80, v81\nv_smfmac_f32_16x16x64_bf16 v[48:51], v[76:79], v[56:63], v10\nv_cvt_pk_bf16_f32 v93, v82, v83\n" \
"v_smfmac_f32_16x16x64_bf16 v[48:51], v[16:19], a[136:143], v10\nv_cvt_pk_bf16_f32 v94, v84, v85\nv_smfmac_f32_16x16x64_bf16 v[48:51], v[20:23], a[144:151], v10\ns_add_u32 s60, s55, 4096\nv_cvt_pk_bf16_f32 v95, v86, v87\nv_smfmac_f32_16x16x64_bf16 v[52:55], v[24:27], a[112:119], v10\nv_and_or_b32 v92, v92, -2, v13\nv_smfmac_f32_16x16x64_bf16 v[52:55], v[28:31], v[56:63], v10\n" \
"v_and_or_b32 v94, v94, -2, v13\nv_smfmac_f32_16x16x64_bf16 v[52:55], v[32:35], a[136:143], v10\ns_waitcnt vmcnt(5)\nbuffer_store_dwordx4 v[92:95], v7, s[36:39], s60 offen offset:0 sc1\nv_mov_b64 v[80:81], 0\nv_mov_b64 v[82:83], 0\nv_mov_b64 v[84:85], 0\ns_barrier\n" \
"v_smfmac_f32_16x16x64_bf16 v[52:55], v[36:39], a[144:151], v10\nds_read_b128 v[56:59], v1 offset:10240\nv_mov_b64 v[86:87], 0\nv_smfmac_f32_16x16x64_bf16 v[80:83], v[72:75], a[160:167], v10\nds_read_b128 v[60:63], v1 offset:11264\nv_cvt_pk_bf16_f32 v88, v48, v49\nv_smfmac_f32_16x16x64_bf16 v[80:83], v[76:79], a[176:183], v10\ns_waitcnt lgkmcnt(2)\n" \
"v_cvt_pk_bf16_f32 v89, v50, v51\nv_smfmac_f32_16x16x64_bf16 v[80:83], v[16:19], v[64:71], v10\ns_cmp_lt_u32 s40, 2\ns_cselect_b32 s54, %[fy], -2147483648\nv_cvt_pk_bf16_f32 v90, v52, v53\nv_smfmac_f32_16x16x64_bf16 v[80:83], v[20:23], a[200:207], v10\ns_add_u32 s60, s55, 16384\nv_cvt_pk_bf16_f32 v91, v54, v55\n" \
"v_smfmac_f32_16x16x64_bf16 v[84:87], v[24:27], a[160:167], v10\nv_and_or_b32 v88, v88, -2, v13\nv_smfmac_f32_16x16x64_bf16 v[84:87], v[28:31], a[176:183], v10\nv_and_or_b32 v90, v90, -2, v13\nv_smfmac_f32_16x16x64_bf16 v[84:87], v[32:35], v[64:71], v10\nbuffer_store_dwordx4 v[88:91], v7, s[36:39], s60 offen offset:0 sc1\nv_mov_b64 v[48:49], 0\nv_mov_b64 v[50:51], 0\n" \
"v_mov_b64 v[52:53], 0\nv_smfmac_f32_16x16x64_bf16 v[84:87], v[36:39], a[200:207], v10\nds_read_b128 v[64:67], v1 offset:18432\nv_mov_b64 v[54:55], 0\nv_smfmac_f32_16x16x64_bf16 v[48:51], v[72:75], a[208:215], v10\ns_waitcnt lgkmcnt(1)\nds_read_b128 v[68:71], v1 offset:19456\nv_cvt_pk_bf16_f32 v92, v80, v81\n" \
"v_smfmac_f32_16x16x64_bf16 v[48:51], v[76:79], v[56:63], v10\nv_cvt_pk_bf16_f32 v93, v82, v83\nv_smfmac_f32_16x16x64_bf16 v[48:51], v[16:19], a[232:239], v10\nv_cvt_pk_bf16_f32 v94, v84, v85\nv_smfmac_f32_16x16x64_bf16 v[48:51], v[20:23], a[248:255], v10\ns_add_u32 s60, s55, 20480\nv_cvt_pk_bf16_f32 v95, v86, v87\nv_smfmac_f32_16x16x64_bf16 v[52:55], v[24:27], a[208:215], v10\n" \
"v_and_or_b32 v92, v92, -2, v13\nv_smfmac_f32_16x16x64_bf16 v[52:55], v[28:31], v[56:63], v10\nv_and_or_b32 v94, v94, -2, v13\nv_smfmac_f32_16x16x64_bf16 v[52:55], v[32:35], a[232:239], v10\nbuffer_store_dwordx4 v[92:95], v7, s[36:39], s60 offen offset:0 sc1\nv_mov_b64 v[80:81], 0\nv_mov_b64 v[82:83], 0\nv_mov_b64 v[84:85], 0\n" \
"v_smfmac_f32_16x16x64_bf16 v[52:55], v[36:39], a[248:255], v10\nds_read_b128 v[56:59], v1 offset:14336\nv_mov_b64 v[86:87], 0\nv_smfmac_f32_16x16x64_bf16 v[80:83], v[72:75], v[104:111], v10\nds_read_b128 v[60:63], v1 offset:15360\nv_cvt_pk_bf16_f32 v88, v48, v49\nv_smfmac_f32_16x16x64_bf16 v[80:83], v[76:79], v[120:127], v10\ns_waitcnt lgkmcnt(2)\n" \
"v_cvt_pk_bf16_f32 v89, v50, v51\nv_smfmac_f32_16x16x64_bf16 v[80:83], v[16:19], v[64:71], v10\nv_cvt_pk_bf16_f32 v90, v52, v53\nv_smfmac_f32_16x16x64_bf16 v[80:83], v[20:23], v[136:143], v10\ns_add_u32 s60, s55, 0\nv_cvt_pk_bf16_f32 v91, v54, v55\nv_smfmac_f32_16x16x64_bf16 v[84:87], v[24:27], v[104:111], v10\nv_and_or_b32 v88, v88, -2, v13\n" \
"v_smfmac_f32_16x16x64_bf16 v[84:87], v[28:31], v[120:127], v10\nv_and_or_b32 v90, v90, -2, v13\nv_smfmac_f32_16x16x64_bf16 v[84:87], v[32:35], v[64:71], v10\nbuffer_store_dwordx4 v[88:91], v7, s[36:39], s60 offen offset:2048 sc1\nv_xor_b32 v11, 128, v11\nv_mov_b64 v[48:49], 0\nv_mov_b64 v[50:51], 0\nv_mov_b64 v[52:53], 0\n" \
"v_smfmac_f32_16x16x64_bf16 v[84:87], v[36:39], v[136:143], v10\nds_read_b128 v[64:67], v1 offset:22528\nv_mov_b64 v[54:55], 0\nv_smfmac_f32_16x16x64_bf16 v[48:51], v[72:75], v[152:159], v10\ns_waitcnt lgkmcnt(1)\nds_read_b128 v[68:71], v1 offset:23552\nv_cvt_pk_bf16_f32 v92, v80, v81\nv_smfmac_f32_16x16x64_bf16 v[48:51], v[76:79], v[56:63], v10\n" \
"ds_read_b128 v[40:43], v11 offset:49152\nv_cvt_pk_bf16_f32 v93, v82, v83\nv_smfmac_f32_16x16x64_bf16 v[48:51], v[16:19], v[176:183], v10\nds_read_b128 v[44:47], v11 offset:51200\nv_cvt_pk_bf16_f32 v94, v84, v85\nv_smfmac_f32_16x16x64_bf16 v[48:51], v[20:23], v[192:199], v10\ns_add_u32 s60, s55, 4096\nv_cvt_pk_bf16_f32 v95, v86, v87\n" \
"v_smfmac_f32_16x16x64_bf16 v[52:55], v[24:27], v[152:159], v10\nv_and_or_b32 v92, v92, -2, v13\nv_smfmac_f32_16x16x64_bf16 v[52:55], v[28:31], v[56:63], v10\nv_and_or_b32 v94, v94, -2, v13\nv_smfmac_f32_16x16x64_bf16 v[52:55], v[32:35], v[176:183], v10\nbuffer_store_dwordx4 v[92:95], v7, s[36:39], s60 offen offset:2048 sc1\nv_mov_b64 v[80:81], 0\nv_mov_b64 v[82:83], 0\n" \
"v_mov_b64 v[84:85], 0\nv_smfmac_f32_16x16x64_bf16 v[52:55], v[36:39], v[192:199], v10\nds_read_b128 v[56:59], v1 offset:8192\nv_mov_b64 v[86:87], 0\nv_smfmac_f32_16x16x64_bf16 v[80:83], v[72:75], v[208:215], v10\nds_read_b128 v[60:63], v1 offset:9216\nds_read_b128 v[72:75], v11 offset:53248\nv_cvt_pk_bf16_f32 v88, v48, v49\n" \
"v_smfmac_f32_16x16x64_bf16 v[80:83], v[76:79], v[224:231], v10\ns_waitcnt lgkmcnt(5)\nds_read_b128 v[76:79], v11 offset:55296\nv_cvt_pk_bf16_f32 v89, v50, v51\nv_smfmac_f32_16x16x64_bf16 v[80:83], v[16:19], v[64:71], v10\nds_read_b128 v[16:19], v11 offset:50176\nv_cvt_pk_bf16_f32 v90, v52, v53\nv_smfmac_f32_16x16x64_bf16 v[80:83], v[20:23], v[248:255], v10\n" \
"s_add_u32 s60, s55, 16384\nds_read_b128 v[20:23], v11 offset:52224\nv_cvt_pk_bf16_f32 v91, v54, v55\nv_smfmac_f32_16x16x64_bf16 v[84:87], v[24:27], v[208:215], v10\nds_read_b128 v[24:27], v11 offset:54272\nv_and_or_b32 v88, v88, -2, v13\nv_smfmac_f32_16x16x64_bf16 v[84:87], v[28:31], v[224:231], v10\nds_read_b128 v[28:31], v11 offset:56320\n" \
"v_and_or_b32 v90, v90, -2, v13\nv_smfmac_f32_16x16x64_bf16 v[84:87], v[32:35], v[64:71], v10\nbuffer_store_dwordx4 v[88:91], v7, s[36:39], s60 offen offset:2048 sc1\nv_mov_b64 v[48:49], 0\nv_mov_b64 v[50:51], 0\nv_mov_b64 v[52:53], 0\nv_smfmac_f32_16x16x64_bf16 v[84:87], v[36:39], v[248:255], v10\ns_waitcnt lgkmcnt(9)\n" \
"ds_read_b128 v[64:67], v1 offset:16384\nv_mov_b64 v[54:55], 0\nv_smfmac_f32_16x16x64_bf16 v[48:51], v[40:43], a[8:15], v10\ns_waitcnt lgkmcnt(9)\ns_waitcnt lgkmcnt(7)\nds_read_b128 v[68:71], v1 offset:17408\nv_cvt_pk_bf16_f32 v92, v80, v81\nv_smfmac_f32_16x16x64_bf16 v[48:51], v[44:47], v[56:63], v10\n" \
"s_waitcnt lgkmcnt(7)\nv_cvt_pk_bf16_f32 v93, v82, v83\nv_smfmac_f32_16x16x64_bf16 v[48:51], v[72:75], a[32:39], v10\ns_waitcnt lgkmcnt(6)\nv_cvt_pk_bf16_f32 v94, v84, v85\nv_smfmac_f32_16x16x64_bf16 v[48:51], v[76:79], a[48:55], v10\ns_add_u32 s60, s55, 20480\ns_waitcnt lgkmcnt(5)\n" \
"v_cvt_pk_bf16_f32 v95, v86, v87\nv_smfmac_f32_16x16x64_bf16 v[52:55], v[16:19], a[8:15], v10\ns_waitcnt lgkmcnt(4)\nv_and_or_b32 v92, v92, -2, v13\nv_smfmac_f32_16x16x64_bf16 v[52:55], v[20:23], v[56:63], v10\ns_waitcnt lgkmcnt(3)\nv_and_or_b32 v94, v94, -2, v13\nv_smfmac_f32_16x16x64_bf16 v[52:55], v[24:27], a[32:39], v10\n" \
"s_waitcnt lgkmcnt(2)\nbuffer_store_dwordx4 v[92:95], v7, s[36:39], s60 offen offset:2048 sc1\nv_xor_b32 v11, 128, v11\nv_mov_b64 v[80:81], 0\nv_mov_b64 v[82:83], 0\nv_mov_b64 v[84:85], 0\nv_smfmac_f32_16x16x64_bf16 v[52:55], v[28:31], a[48:55], v10\nds_read_b128 v[56:59], v1 offset:12288\n" \
"v_mov_b64 v[86:87], 0\nv_smfmac_f32_16x16x64_bf16 v[80:83], v[40:43], a[56:63], v10\nds_read_b128 v[60:63], v1 offset:13312\nv_cvt_pk_bf16_f32 v88, v48, v49\nv_smfmac_f32_16x16x64_bf16 v[80:83], v[44:47], a[72:79], v10\ns_waitcnt lgkmcnt(2)\nv_cvt_pk_bf16_f32 v89, v50, v51\nv_smfmac_f32_16x16x64_bf16 v[80:83], v[72:75], v[64:71], v10\n" \
"v_cvt_pk_bf16_f32 v90, v52, v53\nv_smfmac_f32_16x16x64_bf16 v[80:83], v[76:79], a[96:103], v10\ns_add_u32 s60, s55, 0\nv_cvt_pk_bf16_f32 v91, v54, v55\nv_smfmac_f32_16x16x64_bf16 v[84:87], v[16:19], a[56:63], v10\nv_and_or_b32 v88, v88, -2, v13\nv_smfmac_f32_16x16x64_bf16 v[84:87], v[20:23], a[72:79], v10\nv_and_or_b32 v90, v90, -2, v13\n" \
"v_smfmac_f32_16x16x64_bf16 v[84:87], v[24:27], v[64:71], v10\nbuffer_store_dwordx4 v[88:91], v7, s[36:39], s60 offen offset:512 sc1\nv_mov_b64 v[48:49], 0\nv_mov_b64 v[50:51], 0\nv_mov_b64 v[52:53], 0\nv_smfmac_f32_16x16x64_bf16 v[84:87], v[28:31], a[96:103], v10\nds_read_b128 v[64:67], v1 offset:20480\nv_mov_b64 v[54:55], 0\n" \
"v_smfmac_f32_16x16x64_bf16 v[48:51], v[40:43], a[112:119], v10\ns_waitcnt lgkmcnt(1)\nds_read_b128 v[68:71], v1 offset:21504\nv_cvt_pk_bf16_f32 v92, v80, v81\nv_smfmac_f32_16x16x64_bf16 v[48:51], v[44:47], v[56:63], v10\nv_cvt_pk_bf16_f32 v93, v82, v83\nv_smfmac_f32_16x16x64_bf16 v[48:51], v[72:75], a[136:143], v10\nv_cvt_pk_bf16_f32 v94, v84, v85\n" \
"v_smfmac_f32_16x16x64_bf16 v[48:51], v[76:79], a[144:151], v10\ns_add_u32 s60, s55, 4096\nv_cvt_pk_bf16_f32 v95, v86, v87\nv_smfmac_f32_16x16x64_bf16 v[52:55], v[16:19], a[112:119], v10\nv_and_or_b32 v92, v92, -2, v13\nv_smfmac_f32_16x16x64_bf16 v[52:55], v[20:23], v[56:63], v10\nv_and_or_b32 v94, v94, -2, v13\nv_smfmac_f32_16x16x64_bf16 v[52:55], v[24:27], a[136:143], v10\n" \
"buffer_store_dwordx4 v[92:95], v7, s[36:39], s60 offen offset:512 sc1\nv_mov_b64 v[80:81], 0\nv_mov_b64 v[82:83], 0\nv_mov_b64 v[84:85], 0\nv_smfmac_f32_16x16x64_bf16 v[52:55], v[28:31], a[144:151], v10\nds_read_b128 v[56:59], v1 offset:10240\nv_mov_b64 v[86:87], 0\nv_smfmac_f32_16x16x64_bf16 v[80:83], v[40:43], a[160:167], v10\n" \
"ds_read_b128 v[60:63], v1 offset:11264\nv_cvt_pk_bf16_f32 v88, v48, v49\nv_smfmac_f32_16x16x64_bf16 v[80:83], v[44:47], a[176:183], v10\ns_waitcnt lgkmcnt(2)\nv_cvt_pk_bf16_f32 v89, v50, v51\nv_smfmac_f32_16x16x64_bf16 v[80:83], v[72:75], v[64:71], v10\nv_cvt_pk_bf16_f32 v90, v52, v53\nv_smfmac_f32_16x16x64_bf16 v[80:83], v[76:79], a[200:207], v10\n" \
"s_add_u32 s60, s55, 16384\nv_cvt_pk_bf16_f32 v91, v54, v55\nv_smfmac_f32_16x16x64_bf16 v[84:87], v[16:19], a[160:167], v10\nv_and_or_b32 v88, v88, -2, v13\nv_smfmac_f32_16x16x64_bf16 v[84:87], v[20:23], a[176:183], v10\nv_and_or_b32 v90, v90, -2, v13\nv_smfmac_f32_16x16x64_bf16 v[84:87], v[24:27], v[64:71], v10\nbuffer_store_dwordx4 v[88:91], v7, s[36:39], s60 offen offset:512 sc1\n" \
"v_mov_b64 v[48:49], 0\nv_mov_b64 v[50:51], 0\nv_mov_b64 v[52:53], 0\nv_smfmac_f32_16x16x64_bf16 v[84:87], v[28:31], a[200:207], v10\nds_read_b128 v[64:67], v1 offset:18432\nv_mov_b64 v[54:55], 0\nv_smfmac_f32_16x16x64_bf16 v[48:51], v[40:43], a[208:215], v10\ns_waitcnt lgkmcnt(1)\n" \
"ds_read_b128 v[68:71], v1 offset:19456\nv_cvt_pk_bf16_f32 v92, v80, v81\nv_smfmac_f32_16x16x64_bf16 v[48:51], v[44:47], v[56:63], v10\nv_cvt_pk_bf16_f32 v93, v82, v83\nv_smfmac_f32_16x16x64_bf16 v[48:51], v[72:75], a[232:239], v10\nv_cvt_pk_bf16_f32 v94, v84, v85\nv_smfmac_f32_16x16x64_bf16 v[48:51], v[76:79], a[248:255], v10\ns_add_u32 s60, s55, 20480\n" \
"v_cvt_pk_bf16_f32 v95, v86, v87\nv_smfmac_f32_16x16x64_bf16 v[52:55], v[16:19], a[208:215], v10\nv_and_or_b32 v92, v92, -2, v13\nv_smfmac_f32_16x16x64_bf16 v[52:55], v[20:23], v[56:63], v10\nv_and_or_b32 v94, v94, -2, v13\nv_smfmac_f32_16x16x64_bf16 v[52:55], v[24:27], a[232:239], v10\nbuffer_store_dwordx4 v[92:95], v7, s[36:39], s60 offen offset:512 sc1\nv_mov_b64 v[80:81], 0\n" \
"v_mov_b64 v[82:83], 0\nv_mov_b64 v[84:85], 0\nv_smfmac_f32_16x16x64_bf16 v[52:55], v[28:31], a[248:255], v10\nds_read_b128 v[56:59], v1 offset:14336\nv_mov_b64 v[86:87], 0\nv_smfmac_f32_16x16x64_bf16 v[80:83], v[40:43], v[104:111], v10\nds_read_b128 v[60:63], v1 offset:15360\nv_cvt_pk_bf16_f32 v88, v48, v49\n" \
"v_smfmac_f32_16x16x64_bf16 v[80:83], v[44:47], v[120:127], v10\ns_waitcnt lgkmcnt(2)\nv_cvt_pk_bf16_f32 v89, v50, v51\nv_smfmac_f32_16x16x64_bf16 v[80:83], v[72:75], v[64:71], v10\nv_cvt_pk_bf16_f32 v90, v52, v53\nv_smfmac_f32_16x16x64_bf16 v[80:83], v[76:79], v[136:143], v10\ns_add_u32 s60, s55, 0\nv_cvt_pk_bf16_f32 v91, v54, v55\n" \
"v_smfmac_f32_16x16x64_bf16 v[84:87], v[16:19], v[104:111], v10\nv_and_or_b32 v88, v88, -2, v13\nv_smfmac_f32_16x16x64_bf16 v[84:87], v[20:23], v[120:127], v10\nv_and_or_b32 v90, v90, -2, v13\nv_smfmac_f32_16x16x64_bf16 v[84:87], v[24:27], v[64:71], v10\nbuffer_store_dwordx4 v[88:91], v7, s[36:39], s60 offen offset:2560 sc1\nv_mov_b64 v[48:49], 0\nv_mov_b64 v[50:51], 0\n" \
"v_mov_b64 v[52:53], 0\nv_smfmac_f32_16x16x64_bf16 v[84:87], v[28:31], v[136:143], v10\nds_read_b128 v[64:67], v1 offset:22528\nv_mov_b64 v[54:55], 0\nv_smfmac_f32_16x16x64_bf16 v[48:51], v[40:43], v[152:159], v10\ns_waitcnt lgkmcnt(1)\nds_read_b128 v[68:71], v1 offset:23552\nv_cvt_pk_bf16_f32 v92, v80, v81\n" \
"v_smfmac_f32_16x16x64_bf16 v[48:51], v[44:47], v[56:63], v10\nv_cvt_pk_bf16_f32 v93, v82, v83\nv_smfmac_f32_16x16x64_bf16 v[48:51], v[72:75], v[176:183], v10\nv_cvt_pk_bf16_f32 v94, v84, v85\nv_smfmac_f32_16x16x64_bf16 v[48:51], v[76:79], v[192:199], v10\ns_add_u32 s60, s55, 4096\nv_cvt_pk_bf16_f32 v95, v86, v87\nv_smfmac_f32_16x16x64_bf16 v[52:55], v[16:19], v[152:159], v10\n" \
"v_and_or_b32 v92, v92, -2, v13\nv_smfmac_f32_16x16x64_bf16 v[52:55], v[20:23], v[56:63], v10\nv_and_or_b32 v94, v94, -2, v13\nv_smfmac_f32_16x16x64_bf16 v[52:55], v[24:27], v[176:183], v10\nbuffer_store_dwordx4 v[92:95], v7, s[36:39], s60 offen offset:2560 sc1\nv_mov_b64 v[80:81], 0\nv_mov_b64 v[82:83], 0\nv_mov_b64 v[84:85], 0\n" \
"v_smfmac_f32_16x16x64_bf16 v[52:55], v[28:31], v[192:199], v10\nds_read_b128 v[32:35], v2 offset:0\nv_mov_b64 v[86:87], 0\nv_smfmac_f32_16x16x64_bf16 v[80:83], v[40:43], v[208:215], v10\nds_read_b128 v[36:39], v2 offset:2048\nv_cvt_pk_bf16_f32 v88, v48, v49\nv_smfmac_f32_16x16x64_bf16 v[80:83], v[44:47], v[224:231], v10\ns_waitcnt lgkmcnt(2)\n" \
"v_cvt_pk_bf16_f32 v89, v50, v51\nv_smfmac_f32_16x16x64_bf16 v[80:83], v[72:75], v[64:71], v10\nds_read_b128 v[40:43], v2 offset:256\nv_cvt_pk_bf16_f32 v90, v52, v53\nv_smfmac_f32_16x16x64_bf16 v[80:83], v[76:79], v[248:255], v10\ns_add_u32 s60, s55, 16384\nds_read_b128 v[44:47], v2 offset:2304\nv_cvt_pk_bf16_f32 v91, v54, v55\n" \
"v_smfmac_f32_16x16x64_bf16 v[84:87], v[16:19], v[208:215], v10\nv_and_or_b32 v88, v88, -2, v13\nv_smfmac_f32_16x16x64_bf16 v[84:87], v[20:23], v[224:231], v10\nv_and_or_b32 v90, v90, -2, v13\nv_smfmac_f32_16x16x64_bf16 v[84:87], v[24:27], v[64:71], v10\nbuffer_store_dwordx4 v[88:91], v7, s[36:39], s60 offen offset:2560 sc1\nv_mov_b64 v[48:49], 0\nv_mov_b64 v[50:51], 0\n" \
"v_mov_b64 v[52:53], 0\nv_mov_b32 v15, s40\nv_smfmac_f32_16x16x64_bf16 v[84:87], v[28:31], v[248:255], v10\ns_add_u32 s60, s55, 20480\ns_cmp_eq_u64 s[64:65], 0\nv_mov_b64 v[54:55], 0\ns_cbranch_scc1 .Lvu_tok%=\nv_cvt_pk_bf16_f32 v92, v80, v81\n" \
"v_cvt_pk_bf16_f32 v93, v82, v83\ns_nop 1\nv_cvt_pk_bf16_f32 v94, v84, v85\nv_cvt_pk_bf16_f32 v95, v86, v87\nv_and_or_b32 v92, v92, -2, v13\nv_and_or_b32 v94, v94, -2, v13\nbuffer_store_dwordx4 v[92:95], v7, s[36:39], s60 offen offset:2560 sc1\ns_mov_b32 s60, -2147483648\n" \
"s_cmp_lt_i32 s40, 63\ns_cbranch_scc1 .Lvu_f0%=\ns_mov_b32 s61, -2147483648\nv_add_u32 v14, s57, v0\ns_waitcnt lgkmcnt(0)\n.Lvu_nov%=:\ns_add_u32 m0, s57, 16384\ns_nop 0\n" \
"buffer_load_dwordx4 v3, s[36:39], s42 offen offset:0 sc1 lds\nbuffer_load_dwordx4 v3, s[36:39], s42 offen offset:1024 sc1 lds\nbuffer_load_dwordx4 v3, s[36:39], s42 offen offset:2048 sc1 lds\nbuffer_load_dwordx4 v3, s[36:39], s42 offen offset:3072 sc1 lds\ns_waitcnt vmcnt(0)\nds_read_b128 v[80:83], v14 offset:16384\nds_read_b128 v[84:87], v14 offset:17408\nds_read_b128 v[88:91], v14 offset:18432\n" \
"ds_read_b128 v[92:95], v14 offset:19456\ns_waitcnt lgkmcnt(2)\nv_max3_u32 v76, v80, v82, v84\ns_waitcnt lgkmcnt(1)\nv_max3_u32 v76, v76, v86, v88\ns_waitcnt lgkmcnt(0)\nv_max3_u32 v76, v76, v90, v92\nv_max_u32 v76, v76, v94\n" \
"v_cmp_eq_u32 vcc, -1, v76\ns_cbranch_vccz .Lvu_novfok%=\ns_add_u32 s43, s43, 1\ns_cmp_lt_u32 s43, 262144\ns_cbranch_scc1 .Lvu_nov%=\ns_branch .Lvu_abort%=\n.Lvu_novfok%=:\ns_waitcnt lgkmcnt(0)\n" \
"s_barrier\ns_memrealtime s[68:69]\nds_read_b128 v[56:59], v1 offset:0\nds_read_b128 v[60:63], v1 offset:1024\nds_read_b128 v[80:83], v0 offset:16384\nds_read_b128 v[84:87], v0 offset:17408\nds_read_b128 v[88:91], v0 offset:18432\nds_read_b128 v[92:95], v0 offset:19456\n" \
"ds_read_b128 v[16:19], v0 offset:20480\nds_read_b128 v[20:23], v0 offset:21504\nds_read_b128 v[40:43], v0 offset:22528\nds_read_b128 v[44:47], v0 offset:23552\ns_waitcnt lgkmcnt(0)\ns_branch .Lvu_crit%=\n.Lvu_re%=:\n.Lvu_re2%=:\n" \
"s_add_u32 m0, s57, 16384\nv_max_u32 v76, v80, v82\nv_cmp_eq_u32 vcc, -1, v76\ns_cbranch_vccz .Lvu_sk1894%=\ns_mov_b64 exec, vcc\nbuffer_load_dwordx4 v3, s[36:39], s42 offen offset:0 sc1 lds\ns_mov_b64 exec, -1\n.Lvu_sk1894%=:\n" \
"v_max_u32 v76, v84, v86\nv_cmp_eq_u32 vcc, -1, v76\ns_cbranch_vccz .Lvu_sk1901%=\ns_mov_b64 exec, vcc\nbuffer_load_dwordx4 v3, s[36:39], s42 offen offset:1024 sc1 lds\ns_mov_b64 exec, -1\n.Lvu_sk1901%=:\nv_max_u32 v76, v88, v90\n" \
"v_cmp_eq_u32 vcc, -1, v76\ns_cbranch_vccz .Lvu_sk1908%=\ns_mov_b64 exec, vcc\nbuffer_load_dwordx4 v3, s[36:39], s42 offen offset:2048 sc1 lds\ns_mov_b64 exec, -1\n.Lvu_sk1908%=:\nv_max_u32 v76, v92, v94\nv_cmp_eq_u32 vcc, -1, v76\n" \
"s_cbranch_vccz .Lvu_sk1915%=\ns_mov_b64 exec, vcc\nbuffer_load_dwordx4 v3, s[36:39], s42 offen offset:3072 sc1 lds\ns_mov_b64 exec, -1\n.Lvu_sk1915%=:\n.Lvu_skf%=:\ns_waitcnt vmcnt(0)\nds_read_b128 v[80:83], v14 offset:16384\n" \
"ds_read_b128 v[84:87], v14 offset:17408\nds_read_b128 v[88:91], v14 offset:18432\nds_read_b128 v[92:95], v14 offset:19456\ns_waitcnt lgkmcnt(2)\nv_max3_u32 v76, v80, v82, v84\ns_waitcnt lgkmcnt(1)\nv_max3_u32 v76, v76, v86, v88\ns_waitcnt lgkmcnt(0)\n" \
"v_max3_u32 v76, v76, v90, v92\nv_max_u32 v76, v76, v94\nv_cmp_eq_u32 vcc, -1, v76\ns_cbranch_vccz .Lvu_rer%=\ns_add_u32 s43, s43, 1\ns_cmp_lt_u32 s43, 262144\ns_cbranch_scc1 .Lvu_re2%=\ns_branch .Lvu_abort%=\n" \
".Lvu_rew%=:\nds_read_b128 v[80:83], v14 offset:16384\nds_read_b128 v[84:87], v14 offset:17408\nds_read_b128 v[88:91], v14 offset:18432\nds_read_b128 v[92:95], v14 offset:19456\ns_waitcnt lgkmcnt(0)\ns_branch .Lvu_rer%=\n.Lvu_first%=:\n" \
"s_nop 7\nv_cvt_pk_bf16_f32 v76, v24, v25\nv_cvt_pk_bf16_f32 v77, v26, v27\nv_cvt_pk_bf16_f32 v78, v28, v29\nv_cvt_pk_bf16_f32 v79, v30, v31\nds_write2st64_b64 v9, v[76:77], v[78:79] offset0:2 offset1:10\ns_waitcnt vmcnt(0) lgkmcnt(0)\ns_barrier\n" \
"ds_read_b128 v[56:59], v1 offset:8192\nds_read_b128 v[60:63], v1 offset:9216\nds_read_b128 v[64:67], v1 offset:16384\nds_read_b128 v[68:71], v1 offset:17408\nds_read_b128 v[72:75], v11 offset:49152\nds_read_b128 v[76:79], v11 offset:51200\nds_read_b128 v[16:19], v11 offset:53248\nds_read_b128 v[20:23], v11 offset:55296\n" \
"ds_read_b128 v[24:27], v11 offset:50176\nds_read_b128 v[28:31], v11 offset:52224\nds_read_b128 v[32:35], v11 offset:54272\nds_read_b128 v[36:39], v11 offset:56320\ns_waitcnt lgkmcnt(0)\ns_add_u32 m0, s57, 0\ns_add_u32 s58, s61, 0\nbuffer_load_dwordx4 v0, s[36:39], s58 offen offset:0 sc1 lds\n" \
"buffer_load_dwordx4 v0, s[36:39], s58 offen offset:1024 sc1 lds\nbuffer_load_dwordx4 v0, s[36:39], s58 offen offset:2048 sc1 lds\nbuffer_load_dwordx4 v0, s[36:39], s58 offen offset:3072 sc1 lds\ns_mov_b32 s59, -2147483648\nbuffer_store_dword v14, off, s[36:39], s59\nbuffer_store_dword v14, off, s[36:39], s59\nbuffer_store_dword v14, off, s[36:39], s59\ns_branch .Lvu_y%=\n" \
".Lvu_last%=:\ns_nop 7\nv_cvt_pk_bf16_f32 v92, v40, v41\nv_and_or_b32 v92, v92, -2, v12\ns_add_u32 s63, s59, 4096\nbuffer_store_dwordx4 v[88:91], v6, s[36:39], s63 offen sc1\nv_cvt_pk_bf16_f32 v93, v42, v43\nv_cvt_pk_bf16_f32 v94, v44, v45\n" \
"v_cvt_pk_bf16_f32 v95, v46, v47\nv_and_or_b32 v94, v94, -2, v12\ns_nop 0\nv_permlane16_swap_b32 v93, v95\nv_permlane16_swap_b32 v92, v94\ns_add_u32 s63, s59, 135168\nbuffer_store_dwordx4 v[92:95], v6, s[36:39], s63 offen sc1\ns_branch .Lvu_end%=\n" \
".Lvu_abort%=:\ns_mov_b64 exec, -1\nv_mov_b32 v16, 0\ns_movk_i32 s44, 6400\nbuffer_store_dword v16, off, s[36:39], s44 sc1\n.Lvu_end%=:\ns_waitcnt vmcnt(0) lgkmcnt(0)\n" \
""
#define RD_ASM \
"v_mbcnt_lo_u32_b32 v255, -1, 0\nv_mbcnt_hi_u32_b32 v255, -1, v255\nv_lshlrev_b32 v255, 2, v255\nv_add_u32 v255, %[tb], v255\nds_read_b32 v196, v255 offset:0\nds_read_b32 v197, v255 offset:256\nds_read_b32 v198, v255 offset:512\nds_read_b32 v199, v255 offset:768\n" \
"ds_read_b32 v200, v255 offset:1024\nds_read_b32 v201, v255 offset:1280\nds_read_b32 v202, v255 offset:1536\nds_read_b32 v203, v255 offset:1792\nds_read_b32 v204, v255 offset:2048\nds_read_b32 v205, v255 offset:2304\nds_read_b32 v206, v255 offset:2560\nds_read_b32 v207, v255 offset:2816\n" \
"s_waitcnt lgkmcnt(0)\nv_accvgpr_write_b32 a32, v206\nv_accvgpr_write_b32 a33, v207\ns_mov_b32 s40, 0\ns_mov_b32 s43, 0\ns_add_u32 s57, %[su], 4096\ns_add_u32 s56, %[sp], 4096\ns_mov_b32 s41, %[sy]\n" \
"s_mov_b32 s59, -2147483648\n.Lrd_yflag%=:\nv_accvgpr_read_b32 v206, a32\nbuffer_load_dword v208, v206, %[rs], 0 offen sc1\ns_waitcnt vmcnt(0)\nv_cmp_gt_i32 vcc, s40, v208\ns_cbranch_vccz .Lrd_yld%=\ns_add_u32 s43, s43, 1\n" \
"s_cmp_lt_u32 s43, 262144\ns_cbranch_scc1 .Lrd_yflag%=\ns_branch .Lrd_abort%=\n.Lrd_yld%=:\ns_add_u32 s60, s41, 0\nbuffer_load_dwordx4 v[0:3], v196, %[rs], s60 offen offset:0 sc1\ns_add_u32 s60, s41, 0\nbuffer_load_dwordx4 v[32:35], v196, %[rs], s60 offen offset:1024 sc1\n" \
"s_add_u32 s60, s41, 0\nbuffer_load_dwordx4 v[64:67], v196, %[rs], s60 offen offset:2048 sc1\ns_add_u32 s60, s41, 131072\nbuffer_load_dwordx4 v[4:7], v196, %[rs], s60 offen offset:0 sc1\ns_add_u32 s60, s41, 131072\nbuffer_load_dwordx4 v[36:39], v196, %[rs], s60 offen offset:1024 sc1\ns_add_u32 s60, s41, 131072\nbuffer_load_dwordx4 v[68:71], v196, %[rs], s60 offen offset:2048 sc1\n" \
"s_add_u32 s60, s41, 262144\nbuffer_load_dwordx4 v[8:11], v196, %[rs], s60 offen offset:0 sc1\ns_add_u32 s60, s41, 262144\nbuffer_load_dwordx4 v[40:43], v196, %[rs], s60 offen offset:1024 sc1\ns_add_u32 s60, s41, 262144\nbuffer_load_dwordx4 v[72:75], v196, %[rs], s60 offen offset:2048 sc1\ns_add_u32 s60, s41, 393216\nbuffer_load_dwordx4 v[12:15], v196, %[rs], s60 offen offset:0 sc1\n" \
"s_add_u32 s60, s41, 393216\nbuffer_load_dwordx4 v[44:47], v196, %[rs], s60 offen offset:1024 sc1\ns_add_u32 s60, s41, 393216\nbuffer_load_dwordx4 v[76:79], v196, %[rs], s60 offen offset:2048 sc1\ns_add_u32 s60, s41, 524288\nbuffer_load_dwordx4 v[16:19], v196, %[rs], s60 offen offset:0 sc1\ns_add_u32 s60, s41, 524288\nbuffer_load_dwordx4 v[48:51], v196, %[rs], s60 offen offset:1024 sc1\n" \
"s_add_u32 s60, s41, 524288\nbuffer_load_dwordx4 v[80:83], v196, %[rs], s60 offen offset:2048 sc1\ns_add_u32 s60, s41, 655360\nbuffer_load_dwordx4 v[20:23], v196, %[rs], s60 offen offset:0 sc1\ns_add_u32 s60, s41, 655360\nbuffer_load_dwordx4 v[52:55], v196, %[rs], s60 offen offset:1024 sc1\ns_add_u32 s60, s41, 655360\nbuffer_load_dwordx4 v[84:87], v196, %[rs], s60 offen offset:2048 sc1\n" \
"s_add_u32 s60, s41, 786432\nbuffer_load_dwordx4 v[24:27], v196, %[rs], s60 offen offset:0 sc1\ns_add_u32 s60, s41, 786432\nbuffer_load_dwordx4 v[56:59], v196, %[rs], s60 offen offset:1024 sc1\ns_add_u32 s60, s41, 786432\nbuffer_load_dwordx4 v[88:91], v196, %[rs], s60 offen offset:2048 sc1\ns_add_u32 s60, s41, 917504\nbuffer_load_dwordx4 v[28:31], v196, %[rs], s60 offen offset:0 sc1\n" \
"s_add_u32 s60, s41, 917504\nbuffer_load_dwordx4 v[60:63], v196, %[rs], s60 offen offset:1024 sc1\ns_add_u32 s60, s41, 917504\nbuffer_load_dwordx4 v[92:95], v196, %[rs], s60 offen offset:2048 sc1\ns_add_u32 s60, s41, 0\nbuffer_load_dwordx4 v[96:99], v196, %[rs], s60 offen offset:3072 sc1\ns_add_u32 s60, s41, 131072\nbuffer_load_dwordx4 v[100:103], v196, %[rs], s60 offen offset:3072 sc1\n" \
"s_add_u32 s60, s41, 262144\nbuffer_load_dwordx4 v[104:107], v196, %[rs], s60 offen offset:3072 sc1\ns_add_u32 s60, s41, 393216\nbuffer_load_dwordx4 v[108:111], v196, %[rs], s60 offen offset:3072 sc1\ns_add_u32 s60, s41, 524288\nbuffer_load_dwordx4 v[112:115], v196, %[rs], s60 offen offset:3072 sc1\ns_add_u32 s60, s41, 655360\nbuffer_load_dwordx4 v[116:119], v196, %[rs], s60 offen offset:3072 sc1\n" \
"s_add_u32 s60, s41, 786432\nbuffer_load_dwordx4 v[120:123], v196, %[rs], s60 offen offset:3072 sc1\ns_add_u32 s60, s41, 917504\nbuffer_load_dwordx4 v[124:127], v196, %[rs], s60 offen offset:3072 sc1\ns_mov_b64 exec, -1\nv_accvgpr_read_b32 v212, a33\nbuffer_load_dwordx2 v[214:215], v212, %[rs], 0 offen sc1\ns_waitcnt vmcnt(0)\n" \
"s_branch .Lrd_ychk%=\n.Lrd_u2%=:\nbuffer_load_dwordx4 v[136:139], v196, %[rs], %[su] offen offset:2048 sc1\nbuffer_load_dwordx4 v[140:143], v196, %[rs], %[su] offen offset:3072 sc1\ns_add_u32 s43, s43, 1\ns_cmp_ge_u32 s43, 262144\ns_cbranch_scc1 .Lrd_abort%=\ns_waitcnt vmcnt(0)\n" \
"v_bitop3_b32 v208, v136, v138, s54 bitop3:0x7e\nv_bitop3_b32 v209, v140, v142, s54 bitop3:0x7e\nv_bitop3_b32 v208, v208, 1, v209 bitop3:0xc8\nv_cmp_ne_u32 vcc, 0, v208\ns_cbranch_vccnz .Lrd_u2%=\n.Lrd_u3%=:\nbuffer_load_dwordx4 v[128:131], v196, %[rs], %[su] offen offset:0 sc1\nbuffer_load_dwordx4 v[132:135], v196, %[rs], %[su] offen offset:1024 sc1\n" \
"buffer_load_dwordx4 v[144:147], v196, %[rs], s57 offen offset:0 sc1\nbuffer_load_dwordx4 v[148:151], v196, %[rs], s57 offen offset:1024 sc1\nbuffer_load_dwordx4 v[152:155], v196, %[rs], s57 offen offset:2048 sc1\nbuffer_load_dwordx4 v[156:159], v196, %[rs], s57 offen offset:3072 sc1\nbuffer_load_dwordx4 v[160:163], v196, %[rs], %[sp] offen offset:0 sc1\nbuffer_load_dwordx4 v[164:167], v196, %[rs], %[sp] offen offset:1024 sc1\nbuffer_load_dwordx4 v[168:171], v196, %[rs], %[sp] offen offset:2048 sc1\nbuffer_load_dwordx4 v[172:175], v196, %[rs], %[sp] offen offset:3072 sc1\n" \
"buffer_load_dwordx4 v[176:179], v196, %[rs], s56 offen offset:0 sc1\n.p2align 6\n.Lrd_ra%=:\ns_waitcnt vmcnt(10)\nv_bitop3_b32 v208, v128, v130, s54 bitop3:0x7e\nv_bitop3_b32 v209, v136, v138, s54 bitop3:0x7e\nv_bitop3_b32 v208, v208, 1, v209 bitop3:0xc8\nv_cmp_ne_u32 s[86:87], 0, v208\n" \
"v_bitop3_b32 v208, v144, v146, s54 bitop3:0x7e\nv_bitop3_b32 v209, v152, v154, s54 bitop3:0x7e\nv_bitop3_b32 v208, v208, 1, v209 bitop3:0xc8\nv_cmp_ne_u32 s[88:89], 0, v208\nv_bitop3_b32 v208, v132, v134, s54 bitop3:0x7e\nv_bitop3_b32 v209, v140, v142, s54 bitop3:0x7e\nv_bitop3_b32 v208, v208, 1, v209 bitop3:0xc8\nv_cmp_ne_u32 s[68:69], 0, v208\n" \
"v_bitop3_b32 v208, v148, v150, s54 bitop3:0x7e\nv_bitop3_b32 v209, v156, v158, s54 bitop3:0x7e\nv_bitop3_b32 v208, v208, 1, v209 bitop3:0xc8\nv_cmp_ne_u32 s[62:63], 0, v208\ns_or_b64 vcc, s[86:87], s[88:89]\ns_or_b64 vcc, vcc, s[68:69]\ns_or_b64 vcc, vcc, s[62:63]\ns_waitcnt vmcnt(9)\n" \
"v_smfmac_f32_16x16x64_bf16 v[180:183], v[160:163], v[0:7], v197\nv_bitop3_b32 v208, v160, v162, s54 bitop3:0x7e\nv_smfmac_f32_16x16x64_bf16 v[184:187], v[160:163], v[32:39], v197\nv_cmp_gt_i32 s[46:47], 0, v208\nv_smfmac_f32_16x16x64_bf16 v[188:191], v[160:163], v[64:71], v197\nv_smfmac_f32_16x16x64_bf16 v[192:195], v[160:163], v[96:103], v197\ns_or_b64 vcc, vcc, s[46:47]\ns_waitcnt vmcnt(8)\n" \
"v_smfmac_f32_16x16x64_bf16 v[180:183], v[164:167], v[8:15], v197\nv_bitop3_b32 v208, v164, v166, s54 bitop3:0x7e\nv_smfmac_f32_16x16x64_bf16 v[184:187], v[164:167], v[40:47], v197\nv_cmp_gt_i32 s[48:49], 0, v208\nv_smfmac_f32_16x16x64_bf16 v[188:191], v[164:167], v[72:79], v197\nv_smfmac_f32_16x16x64_bf16 v[192:195], v[164:167], v[104:111], v197\ns_or_b64 vcc, vcc, s[48:49]\ns_waitcnt vmcnt(7)\n" \
"v_smfmac_f32_16x16x64_bf16 v[180:183], v[168:171], v[16:23], v197\nv_bitop3_b32 v208, v168, v170, s54 bitop3:0x7e\nv_smfmac_f32_16x16x64_bf16 v[184:187], v[168:171], v[48:55], v197\nv_cmp_gt_i32 s[50:51], 0, v208\nv_smfmac_f32_16x16x64_bf16 v[188:191], v[168:171], v[80:87], v197\nv_smfmac_f32_16x16x64_bf16 v[192:195], v[168:171], v[112:119], v197\ns_or_b64 vcc, vcc, s[50:51]\ns_waitcnt vmcnt(6)\n" \
"v_smfmac_f32_16x16x64_bf16 v[180:183], v[172:175], v[24:31], v197\nv_bitop3_b32 v208, v172, v174, s54 bitop3:0x7e\nv_smfmac_f32_16x16x64_bf16 v[184:187], v[172:175], v[56:63], v197\nv_cmp_gt_i32 s[52:53], 0, v208\nv_smfmac_f32_16x16x64_bf16 v[188:191], v[172:175], v[88:95], v197\nv_smfmac_f32_16x16x64_bf16 v[192:195], v[172:175], v[120:127], v197\ns_or_b64 vcc, vcc, s[52:53]\ns_waitcnt vmcnt(5)\n" \
"v_smfmac_f32_16x16x64_bf16 v[180:183], v[176:179], v[128:135], v197\nv_bitop3_b32 v208, v176, v178, s54 bitop3:0x7e\nv_smfmac_f32_16x16x64_bf16 v[184:187], v[176:179], v[136:143], v197\nv_cmp_gt_i32 s[64:65], 0, v208\nv_smfmac_f32_16x16x64_bf16 v[188:191], v[176:179], v[144:151], v197\ns_or_b64 vcc, vcc, s[64:65]\ns_cbranch_scc1 .Lrd_rb%=\nv_smfmac_f32_16x16x64_bf16 v[192:195], v[176:179], v[152:159], v197\n" \
".Lrd_rd0%=:\nv_add_f32 v180, v180, v182\nv_add_f32 v181, v181, v183\nds_write2_b32 v198, v180, v181 offset0:0 offset1:8\nv_add_f32 v184, v184, v186\nv_add_f32 v185, v185, v187\nv_add_f32 v188, v188, v190\nv_add_f32 v189, v189, v191\n" \
"ds_write2_b32 v198, v184, v185 offset0:32 offset1:40\nv_add_f32 v192, v192, v194\nv_add_f32 v193, v193, v195\nds_write2_b32 v198, v188, v189 offset0:64 offset1:72\nds_write2_b32 v198, v192, v193 offset0:96 offset1:104\ns_add_u32 s60, s58, 0\nbuffer_load_dwordx4 v[232:235], v196, %[rs], s60 offen offset:3072 sc1\ns_add_u32 s60, s58, 16384\n" \
"buffer_load_dwordx4 v[236:239], v196, %[rs], s60 offen offset:2048 sc1\ns_waitcnt lgkmcnt(0)\ns_barrier\nds_read_b128 v[180:183], v199 offset:0\nds_read_b128 v[184:187], v199 offset:4096\nds_read_b128 v[188:191], v199 offset:8192\nds_read_b128 v[192:195], v199 offset:12288\ns_and_b32 s44, s54, 0x80008000\n" \
"s_add_u32 s60, s58, 4096\nbuffer_load_dwordx4 v[240:243], v196, %[rs], s60 offen offset:0 sc1\ns_add_u32 s60, s58, 4096\nbuffer_load_dwordx4 v[244:247], v196, %[rs], s60 offen offset:1024 sc1\ns_waitcnt lgkmcnt(2)\nv_pk_add_f32 v[182:183], v[182:183], v[186:187]\nv_pk_add_f32 v[180:181], v[180:181], v[184:185]\ns_waitcnt lgkmcnt(0)\n" \
"v_pk_add_f32 v[186:187], v[188:189], v[192:193]\nv_pk_add_f32 v[184:185], v[190:191], v[194:195]\nv_pk_add_f32 v[180:181], v[180:181], v[186:187]\nv_pk_add_f32 v[182:183], v[182:183], v[184:185]\nv_cvt_pk_bf16_f32 v180, v180, v181\nv_xor_b32 v180, s44, v180\nv_cvt_pk_bf16_f32 v181, v182, v183\nv_min_u32 v180, -2, v180\n" \
"v_xor_b32 v209, s44, v181\nv_or_b32 v208, v180, v201\nv_mov_b32 v211, v209\nv_or_b32 v210, v180, v201\ns_nop 0\nv_permlane32_swap_b32 v209, v211\nv_permlane32_swap_b32 v208, v210\nbuffer_store_dwordx4 v[208:211], v206, %[rs], 0 offen sc1\n" \
"s_add_u32 s60, s58, 16384\nbuffer_load_dwordx4 v[248:251], v196, %[rs], s60 offen offset:3072 sc1\ns_add_u32 s60, s58, 20480\nbuffer_load_dwordx4 v[252:255], v196, %[rs], s60 offen offset:0 sc1\ns_add_u32 s60, s58, 20480\nbuffer_load_dwordx4 v[160:163], v196, %[rs], s60 offen offset:2048 sc1\ns_xor_b32 s41, s41, 4194304\nv_mov_b32 v207, s40\n" \
"s_waitcnt vmcnt(12)\nv_bitop3_b32 v209, v212, v214, s61 bitop3:0x7e\nv_cmp_gt_i32 vcc, 0, v209\ns_cbranch_vccnz .Lrd_tre0%=\n.Lrd_tl0%=:\nv_mfma_f32_16x16x16_bf16 a[0:3], v[0:1], v[212:213], 0\ns_add_u32 s60, s58, 4096\nbuffer_load_dwordx4 v[164:167], v196, %[rs], s60 offen offset:2048 sc1\n" \
"v_mfma_f32_16x16x16_bf16 a[8:11], v[32:33], v[212:213], 0\nv_mfma_f32_16x16x16_bf16 a[16:19], v[64:65], v[212:213], 0\nv_mfma_f32_16x16x16_bf16 a[24:27], v[96:97], v[212:213], 0\ns_waitcnt vmcnt(12)\nv_mfma_f32_16x16x16_bf16 a[0:3], v[4:5], v[214:215], a[0:3]\ns_add_u32 s60, s58, 4096\nbuffer_load_dwordx4 v[168:171], v196, %[rs], s60 offen offset:3072 sc1\nv_bitop3_b32 v209, v216, v218, s61 bitop3:0x7e\n" \
"v_mfma_f32_16x16x16_bf16 a[8:11], v[36:37], v[214:215], a[8:11]\ns_lshl_b32 s42, s40, 17\ns_add_u32 s42, s42, %[at]\nv_mfma_f32_16x16x16_bf16 a[16:19], v[68:69], v[214:215], a[16:19]\nv_cmp_gt_i32 vcc, 0, v209\nv_mfma_f32_16x16x16_bf16 a[24:27], v[100:101], v[214:215], a[24:27]\ns_cbranch_vccnz .Lrd_tre1%=\n.Lrd_tl1%=:\n" \
"s_waitcnt vmcnt(12)\nv_mfma_f32_16x16x32_bf16 a[4:7], v[0:3], v[216:219], 0\ns_add_u32 s60, s58, 20480\nbuffer_load_dwordx4 v[172:175], v196, %[rs], s60 offen offset:1024 sc1\nv_bitop3_b32 v209, v220, v222, s61 bitop3:0x7e\nv_mfma_f32_16x16x32_bf16 a[12:15], v[32:35], v[216:219], 0\ns_lshr_b32 s45, s42, 8\ns_and_b32 s45, s45, 0x2000\n" \
"v_mfma_f32_16x16x32_bf16 a[20:23], v[64:67], v[216:219], 0\nv_cmp_gt_i32 vcc, 0, v209\nv_mfma_f32_16x16x32_bf16 a[28:31], v[96:99], v[216:219], 0\ns_cbranch_vccnz .Lrd_tre2%=\n.Lrd_tl2%=:\ns_waitcnt vmcnt(12)\nv_mfma_f32_16x16x32_bf16 a[4:7], v[4:7], v[220:223], a[4:7]\ns_add_u32 s60, s58, 20480\n" \
"buffer_load_dwordx4 v[176:179], v196, %[rs], s60 offen offset:3072 sc1\nv_bitop3_b32 v209, v224, v226, s61 bitop3:0x7e\nv_mfma_f32_16x16x32_bf16 a[12:15], v[36:39], v[220:223], a[12:15]\ns_xor_b32 s42, s42, s45\ns_add_u32 s40, s40, 1\nv_mfma_f32_16x16x32_bf16 a[20:23], v[68:71], v[220:223], a[20:23]\nv_cmp_gt_i32 vcc, 0, v209\nv_mfma_f32_16x16x32_bf16 a[28:31], v[100:103], v[220:223], a[28:31]\n" \
"s_cbranch_vccnz .Lrd_tre3%=\n.Lrd_tl3%=:\nv_mfma_f32_16x16x16_bf16 a[0:3], v[8:9], v[224:225], a[0:3]\ns_cmp_eq_u32 s40, 1\ns_cbranch_scc1 .Lrd_y1%=\n.Lrd_y1r%=:\ns_add_u32 s60, s41, 0\nbuffer_load_dwordx4 v[0:3], v196, %[rs], s60 offen offset:0 sc1\n" \
"v_mfma_f32_16x16x16_bf16 a[8:11], v[40:41], v[224:225], a[8:11]\nv_mfma_f32_16x16x16_bf16 a[16:19], v[72:73], v[224:225], a[16:19]\nbuffer_load_dwordx4 v[212:215], v204, %[rs], s42 offen sc1\nv_mfma_f32_16x16x16_bf16 a[24:27], v[104:105], v[224:225], a[24:27]\ns_waitcnt vmcnt(14)\nv_mfma_f32_16x16x16_bf16 a[0:3], v[12:13], v[226:227], a[0:3]\ns_add_u32 s60, s41, 0\nbuffer_load_dwordx4 v[32:35], v196, %[rs], s60 offen offset:1024 sc1\n" \
"v_bitop3_b32 v209, v228, v230, s61 bitop3:0x7e\nv_mfma_f32_16x16x16_bf16 a[8:11], v[44:45], v[226:227], a[8:11]\ns_lshl_b32 s62, s55, 31\ns_or_b32 s62, s62, 1.0\ns_mov_b32 s63, s62\nv_mfma_f32_16x16x16_bf16 a[16:19], v[76:77], v[226:227], a[16:19]\nv_cmp_gt_i32 vcc, 0, v209\nv_mfma_f32_16x16x16_bf16 a[24:27], v[108:109], v[226:227], a[24:27]\n" \
"s_cbranch_vccnz .Lrd_tre4%=\n.Lrd_tl4%=:\ns_waitcnt vmcnt(14)\nv_mfma_f32_16x16x32_bf16 a[4:7], v[8:11], v[228:231], a[4:7]\ns_add_u32 s60, s41, 0\nbuffer_load_dwordx4 v[64:67], v196, %[rs], s60 offen offset:2048 sc1\nv_bitop3_b32 v209, v232, v234, s61 bitop3:0x7e\nv_mfma_f32_16x16x32_bf16 a[12:15], v[40:43], v[228:231], a[12:15]\n" \
"v_mfma_f32_16x16x32_bf16 a[20:23], v[72:75], v[228:231], a[20:23]\nv_cmp_gt_i32 vcc, 0, v209\nv_mfma_f32_16x16x32_bf16 a[28:31], v[104:107], v[228:231], a[28:31]\ns_cbranch_vccnz .Lrd_tre5%=\n.Lrd_tl5%=:\ns_waitcnt vmcnt(14)\nv_mfma_f32_16x16x32_bf16 a[4:7], v[12:15], v[232:235], a[4:7]\ns_add_u32 s60, s41, 131072\n" \
"buffer_load_dwordx4 v[4:7], v196, %[rs], s60 offen offset:0 sc1\nv_bitop3_b32 v209, v236, v238, s61 bitop3:0x7e\nv_mfma_f32_16x16x32_bf16 a[12:15], v[44:47], v[232:235], a[12:15]\nv_mfma_f32_16x16x32_bf16 a[20:23], v[76:79], v[232:235], a[20:23]\nv_cmp_gt_i32 vcc, 0, v209\nv_mfma_f32_16x16x32_bf16 a[28:31], v[108:111], v[232:235], a[28:31]\ns_cbranch_vccnz .Lrd_tre6%=\n.Lrd_tl6%=:\n" \
"v_mfma_f32_16x16x16_bf16 a[0:3], v[16:17], v[236:237], a[0:3]\ns_add_u32 s60, s41, 131072\nbuffer_load_dwordx4 v[36:39], v196, %[rs], s60 offen offset:1024 sc1\nv_mfma_f32_16x16x16_bf16 a[8:11], v[48:49], v[236:237], a[8:11]\nv_mfma_f32_16x16x16_bf16 a[16:19], v[80:81], v[236:237], a[16:19]\nv_mfma_f32_16x16x16_bf16 a[24:27], v[112:113], v[236:237], a[24:27]\ns_waitcnt vmcnt(15)\nv_mfma_f32_16x16x16_bf16 a[0:3], v[20:21], v[238:239], a[0:3]\n" \
"s_add_u32 s60, s41, 131072\nbuffer_load_dwordx4 v[68:71], v196, %[rs], s60 offen offset:2048 sc1\nv_bitop3_b32 v209, v240, v242, s61 bitop3:0x7e\nv_mfma_f32_16x16x16_bf16 a[8:11], v[52:53], v[238:239], a[8:11]\nv_mfma_f32_16x16x16_bf16 a[16:19], v[84:85], v[238:239], a[16:19]\nv_cmp_gt_i32 vcc, 0, v209\nv_mfma_f32_16x16x16_bf16 a[24:27], v[116:117], v[238:239], a[24:27]\ns_cbranch_vccnz .Lrd_tre7%=\n" \
".Lrd_tl7%=:\ns_waitcnt vmcnt(15)\nv_mfma_f32_16x16x32_bf16 a[4:7], v[16:19], v[240:243], a[4:7]\ns_add_u32 s60, s41, 262144\nbuffer_load_dwordx4 v[8:11], v196, %[rs], s60 offen offset:0 sc1\nv_bitop3_b32 v209, v244, v246, s61 bitop3:0x7e\nv_mfma_f32_16x16x32_bf16 a[12:15], v[48:51], v[240:243], a[12:15]\nv_mfma_f32_16x16x32_bf16 a[20:23], v[80:83], v[240:243], a[20:23]\n" \
"v_cmp_gt_i32 vcc, 0, v209\nv_mfma_f32_16x16x32_bf16 a[28:31], v[112:115], v[240:243], a[28:31]\ns_cbranch_vccnz .Lrd_tre8%=\n.Lrd_tl8%=:\ns_waitcnt vmcnt(14)\nv_mfma_f32_16x16x32_bf16 a[4:7], v[20:23], v[244:247], a[4:7]\ns_add_u32 s60, s41, 262144\nbuffer_load_dwordx4 v[40:43], v196, %[rs], s60 offen offset:1024 sc1\n" \
"v_bitop3_b32 v209, v248, v250, s61 bitop3:0x7e\nv_mfma_f32_16x16x32_bf16 a[12:15], v[52:55], v[244:247], a[12:15]\nv_mfma_f32_16x16x32_bf16 a[20:23], v[84:87], v[244:247], a[20:23]\nv_cmp_gt_i32 vcc, 0, v209\nv_mfma_f32_16x16x32_bf16 a[28:31], v[116:119], v[244:247], a[28:31]\ns_cbranch_vccnz .Lrd_tre9%=\n.Lrd_tl9%=:\nv_mfma_f32_16x16x16_bf16 a[0:3], v[24:25], v[248:249], a[0:3]\n" \
"s_add_u32 s60, s41, 262144\nbuffer_load_dwordx4 v[72:75], v196, %[rs], s60 offen offset:2048 sc1\nv_mfma_f32_16x16x16_bf16 a[8:11], v[56:57], v[248:249], a[8:11]\nv_mfma_f32_16x16x16_bf16 a[16:19], v[88:89], v[248:249], a[16:19]\nv_mfma_f32_16x16x16_bf16 a[24:27], v[120:121], v[248:249], a[24:27]\ns_waitcnt vmcnt(15)\nv_mfma_f32_16x16x16_bf16 a[0:3], v[28:29], v[250:251], a[0:3]\ns_add_u32 s60, s41, 393216\n" \
"buffer_load_dwordx4 v[12:15], v196, %[rs], s60 offen offset:0 sc1\nv_bitop3_b32 v209, v252, v254, s61 bitop3:0x7e\nv_mfma_f32_16x16x16_bf16 a[8:11], v[60:61], v[250:251], a[8:11]\nv_mfma_f32_16x16x16_bf16 a[16:19], v[92:93], v[250:251], a[16:19]\nv_cmp_gt_i32 vcc, 0, v209\nv_mfma_f32_16x16x16_bf16 a[24:27], v[124:125], v[250:251], a[24:27]\ns_cbranch_vccnz .Lrd_tre10%=\n.Lrd_tl10%=:\n" \
"s_waitcnt vmcnt(15)\nv_mfma_f32_16x16x32_bf16 a[0:3], v[128:131], v[252:255], a[0:3]\ns_add_u32 s60, s41, 393216\nbuffer_load_dwordx4 v[44:47], v196, %[rs], s60 offen offset:1024 sc1\nv_bitop3_b32 v209, v160, v162, s61 bitop3:0x7e\nv_mfma_f32_16x16x32_bf16 a[8:11], v[136:139], v[252:255], a[8:11]\nv_mfma_f32_16x16x32_bf16 a[16:19], v[144:147], v[252:255], a[16:19]\nv_cmp_gt_i32 vcc, 0, v209\n" \
"v_mfma_f32_16x16x32_bf16 a[24:27], v[152:155], v[252:255], a[24:27]\ns_cbranch_vccnz .Lrd_tre11%=\n.Lrd_tl11%=:\ns_waitcnt vmcnt(15)\nv_mfma_f32_16x16x32_bf16 a[0:3], v[132:135], v[160:163], a[0:3]\ns_add_u32 s60, s41, 393216\nbuffer_load_dwordx4 v[76:79], v196, %[rs], s60 offen offset:2048 sc1\nv_bitop3_b32 v209, v164, v166, s61 bitop3:0x7e\n" \
"v_mfma_f32_16x16x32_bf16 a[8:11], v[140:143], v[160:163], a[8:11]\nv_mfma_f32_16x16x32_bf16 a[16:19], v[148:151], v[160:163], a[16:19]\nv_cmp_gt_i32 vcc, 0, v209\nv_mfma_f32_16x16x32_bf16 a[24:27], v[156:159], v[160:163], a[24:27]\ns_cbranch_vccnz .Lrd_tre12%=\n.Lrd_tl12%=:\ns_waitcnt vmcnt(15)\nv_mfma_f32_16x16x32_bf16 a[4:7], v[24:27], v[164:167], a[4:7]\n" \
"s_add_u32 s60, s41, 524288\nbuffer_load_dwordx4 v[16:19], v196, %[rs], s60 offen offset:0 sc1\nv_bitop3_b32 v209, v168, v170, s61 bitop3:0x7e\nv_mfma_f32_16x16x32_bf16 a[12:15], v[56:59], v[164:167], a[12:15]\nv_mfma_f32_16x16x32_bf16 a[20:23], v[88:91], v[164:167], a[20:23]\nds_write_b128 v202, a[0:3] offset:0\nv_cmp_gt_i32 vcc, 0, v209\nv_mfma_f32_16x16x32_bf16 a[28:31], v[120:123], v[164:167], a[28:31]\n" \
"s_cbranch_vccnz .Lrd_tre13%=\n.Lrd_tl13%=:\ns_waitcnt vmcnt(15)\nv_mfma_f32_16x16x32_bf16 a[4:7], v[28:31], v[168:171], a[4:7]\ns_add_u32 s60, s41, 524288\nbuffer_load_dwordx4 v[48:51], v196, %[rs], s60 offen offset:1024 sc1\nv_bitop3_b32 v209, v172, v174, s61 bitop3:0x7e\nv_mfma_f32_16x16x32_bf16 a[12:15], v[60:63], v[168:171], a[12:15]\n" \
"v_mfma_f32_16x16x32_bf16 a[20:23], v[92:95], v[168:171], a[20:23]\nds_write_b128 v202, a[8:11] offset:2048\nv_cmp_gt_i32 vcc, 0, v209\nv_mfma_f32_16x16x32_bf16 a[28:31], v[124:127], v[168:171], a[28:31]\ns_cbranch_vccnz .Lrd_tre14%=\n.Lrd_tl14%=:\ns_waitcnt vmcnt(15)\nv_mfma_f32_16x16x32_bf16 a[4:7], v[128:131], v[172:175], a[4:7]\n" \
"s_add_u32 s60, s41, 524288\nbuffer_load_dwordx4 v[80:83], v196, %[rs], s60 offen offset:2048 sc1\nv_bitop3_b32 v209, v176, v178, s61 bitop3:0x7e\nv_mfma_f32_16x16x32_bf16 a[12:15], v[136:139], v[172:175], a[12:15]\nv_mfma_f32_16x16x32_bf16 a[20:23], v[144:147], v[172:175], a[20:23]\nds_write_b128 v202, a[16:19] offset:4096\nv_cmp_gt_i32 vcc, 0, v209\nv_mfma_f32_16x16x32_bf16 a[28:31], v[152:155], v[172:175], a[28:31]\n" \
"s_cbranch_vccnz .Lrd_tre15%=\n.Lrd_tl15%=:\nv_mfma_f32_16x16x32_bf16 a[4:7], v[132:135], v[176:179], a[4:7]\ns_add_u32 s60, s41, 655360\ns_waitcnt vmcnt(14)\nbuffer_load_dwordx4 v[20:23], v196, %[rs], s60 offen offset:0 sc1\nv_lshlrev_b32 v216, 16, v212\nv_and_b32 v217, 0xffff0000, v212\n" \
"v_mfma_f32_16x16x32_bf16 a[12:15], v[140:143], v[176:179], a[12:15]\nv_lshlrev_b32 v218, 16, v213\nv_and_b32 v219, 0xffff0000, v213\nv_mfma_f32_16x16x32_bf16 a[20:23], v[148:151], v[176:179], a[20:23]\nds_write_b128 v202, a[24:27] offset:6144\nv_lshlrev_b32 v220, 16, v214\nv_and_b32 v221, 0xffff0000, v214\nv_mfma_f32_16x16x32_bf16 a[28:31], v[156:159], v[176:179], a[28:31]\n" \
"s_add_u32 s60, s41, 655360\nbuffer_load_dwordx4 v[52:55], v196, %[rs], s60 offen offset:1024 sc1\ns_add_u32 s60, s41, 655360\nds_write_b128 v202, a[4:7] offset:1024\nbuffer_load_dwordx4 v[84:87], v196, %[rs], s60 offen offset:2048 sc1\nds_write_b128 v202, a[12:15] offset:3072\nv_lshlrev_b32 v222, 16, v215\nv_and_b32 v223, 0xffff0000, v215\n" \
"s_add_u32 s60, s41, 786432\nbuffer_load_dwordx4 v[24:27], v196, %[rs], s60 offen offset:0 sc1\nds_write_b128 v202, a[20:23] offset:5120\ns_add_u32 s60, s41, 786432\nbuffer_load_dwordx4 v[56:59], v196, %[rs], s60 offen offset:1024 sc1\nds_write_b128 v202, a[28:31] offset:7168\ns_waitcnt lgkmcnt(0)\ns_barrier\n" \
"ds_read_b128 v[224:227], v203 offset:0\nds_read_b128 v[228:231], v203 offset:256\nds_read_b128 v[232:235], v203 offset:8192\nds_read_b128 v[236:239], v203 offset:8448\nds_read_b128 v[240:243], v203 offset:16384\nds_read_b128 v[244:247], v203 offset:16640\nds_read_b128 v[248:251], v203 offset:24576\nds_read_b128 v[252:255], v203 offset:24832\n" \
"s_add_u32 s60, s41, 786432\nbuffer_load_dwordx4 v[88:91], v196, %[rs], s60 offen offset:2048 sc1\ns_waitcnt lgkmcnt(4)\nv_pk_add_f32 v[224:225], v[224:225], v[232:233]\nv_pk_add_f32 v[226:227], v[226:227], v[234:235]\nv_pk_add_f32 v[228:229], v[228:229], v[236:237]\nv_pk_add_f32 v[230:231], v[230:231], v[238:239]\ns_add_u32 s60, s41, 917504\n" \
"buffer_load_dwordx4 v[28:31], v196, %[rs], s60 offen offset:0 sc1\ns_waitcnt lgkmcnt(2)\nv_pk_add_f32 v[224:225], v[224:225], v[240:241]\nv_pk_add_f32 v[226:227], v[226:227], v[242:243]\nv_pk_add_f32 v[228:229], v[228:229], v[244:245]\nv_pk_add_f32 v[230:231], v[230:231], v[246:247]\ns_add_u32 s60, s41, 917504\nbuffer_load_dwordx4 v[60:63], v196, %[rs], s60 offen offset:1024 sc1\n" \
"s_waitcnt lgkmcnt(0)\nv_pk_add_f32 v[224:225], v[224:225], v[248:249]\nv_pk_add_f32 v[226:227], v[226:227], v[250:251]\nv_pk_add_f32 v[228:229], v[228:229], v[252:253]\nv_pk_add_f32 v[230:231], v[230:231], v[254:255]\nv_pk_fma_f32 v[224:225], v[224:225], s[62:63], v[216:217]\nv_pk_fma_f32 v[226:227], v[226:227], s[62:63], v[218:219]\nv_pk_fma_f32 v[228:229], v[228:229], s[62:63], v[220:221]\n" \
"v_pk_fma_f32 v[230:231], v[230:231], s[62:63], v[222:223]\nv_cvt_pk_bf16_f32 v208, v224, v225\nv_cvt_pk_bf16_f32 v209, v226, v227\nv_cvt_pk_bf16_f32 v210, v228, v229\nv_cvt_pk_bf16_f32 v211, v230, v231\nbuffer_store_dwordx4 v[208:211], v204, %[rs], s42 offen sc1\nv_pk_add_f32 v[232:233], v[224:225], v[226:227]\nv_pk_add_f32 v[234:235], v[228:229], v[230:231]\n" \
"v_pk_add_f32 v[232:233], v[232:233], v[234:235]\ns_add_u32 s60, s41, 917504\nbuffer_load_dwordx4 v[92:95], v196, %[rs], s60 offen offset:2048 sc1\ns_add_u32 s60, s41, 0\nbuffer_load_dwordx4 v[96:99], v196, %[rs], s60 offen offset:3072 sc1\nv_add_f32 v232, v232, v233\ns_nop 1\nv_add_f32_dpp v233, v232, v232 row_ror:8 row_mask:0xf bank_mask:0xf\n" \
"v_add_f32_dpp v234, v232, v232 row_ror:8 row_mask:0xf bank_mask:0xf\ns_nop 1\nv_permlane16_swap_b32 v233, v234\nv_add_f32 v232, v233, v234\nv_add_f32 v233, v233, v234\ns_nop 1\nv_permlane32_swap_b32 v232, v233\nv_add_f32 v232, v232, v233\n" \
"v_mul_f32 v232, 0x3c800000, v232\nv_pk_add_f32 v[224:225], v[224:225], v[232:233] op_sel_hi:[1,0] neg_lo:[0,1] neg_hi:[0,1]\nv_pk_add_f32 v[226:227], v[226:227], v[232:233] op_sel_hi:[1,0] neg_lo:[0,1] neg_hi:[0,1]\nv_pk_add_f32 v[228:229], v[228:229], v[232:233] op_sel_hi:[1,0] neg_lo:[0,1] neg_hi:[0,1]\nv_pk_add_f32 v[230:231], v[230:231], v[232:233] op_sel_hi:[1,0] neg_lo:[0,1] neg_hi:[0,1]\ns_add_u32 s60, s41, 131072\nbuffer_load_dwordx4 v[100:103], v196, %[rs], s60 offen offset:3072 sc1\nv_accvgpr_read_b32 v212, a33\n" \
"buffer_load_dwordx2 v[214:215], v212, %[rs], 0 offen sc1\nv_pk_mul_f32 v[236:237], v[224:225], v[224:225]\nv_pk_mul_f32 v[238:239], v[226:227], v[226:227]\nv_pk_fma_f32 v[236:237], v[228:229], v[228:229], v[236:237]\nv_pk_fma_f32 v[238:239], v[230:231], v[230:231], v[238:239]\nv_pk_add_f32 v[236:237], v[236:237], v[238:239]\nv_add_f32 v236, v236, v237\ns_nop 1\n" \
"v_add_f32_dpp v237, v236, v236 row_ror:8 row_mask:0xf bank_mask:0xf\nv_add_f32_dpp v238, v236, v236 row_ror:8 row_mask:0xf bank_mask:0xf\ns_nop 1\nv_permlane16_swap_b32 v237, v238\nv_add_f32 v236, v237, v238\nv_add_f32 v237, v237, v238\ns_nop 1\nv_permlane32_swap_b32 v236, v237\n" \
"v_add_f32 v236, v236, v237\nv_mov_b32 v233, v236\nbuffer_store_dwordx2 v[232:233], v205, %[rs], s42 offen sc1\ns_mov_b32 s59, %[fa]\ns_add_u32 s60, s41, 262144\nbuffer_load_dwordx4 v[104:107], v196, %[rs], s60 offen offset:3072 sc1\ns_add_u32 s60, s41, 393216\nbuffer_load_dwordx4 v[108:111], v196, %[rs], s60 offen offset:3072 sc1\n" \
"s_add_u32 s60, s41, 524288\nbuffer_load_dwordx4 v[112:115], v196, %[rs], s60 offen offset:3072 sc1\ns_add_u32 s60, s41, 655360\nbuffer_load_dwordx4 v[116:119], v196, %[rs], s60 offen offset:3072 sc1\ns_add_u32 s60, s41, 786432\nbuffer_load_dwordx4 v[120:123], v196, %[rs], s60 offen offset:3072 sc1\ns_add_u32 s60, s41, 917504\nbuffer_load_dwordx4 v[124:127], v196, %[rs], s60 offen offset:3072 sc1\n" \
"s_cmp_ge_u32 s40, 64\ns_cbranch_scc1 .Lrd_fin%=\n.Lrd_ychk%=:\ns_and_b32 s44, s40, 1\ns_bfe_u32 s55, s40, 0x10001\ns_mul_i32 s54, s44, 0x80008001\ns_mul_i32 s61, s55, 0x80008000\ns_lshl_b32 s58, s44, 17\n" \
"s_add_u32 s58, s58, %[st]\ns_add_u32 s44, s40, 1\ns_and_b32 s44, s44, 3\ns_lshl_b32 s44, s44, 15\nv_xor_b32 v206, s44, v200\nv_mov_b64 v[180:181], 0\nv_mov_b64 v[182:183], 0\nv_mov_b64 v[184:185], 0\n" \
"v_mov_b64 v[186:187], 0\nv_mov_b64 v[188:189], 0\nv_mov_b64 v[190:191], 0\nv_mov_b64 v[192:193], 0\nv_mov_b64 v[194:195], 0\ns_waitcnt vmcnt(7)\n.Lrd_sg0%=:\ns_memrealtime s[68:69]\n" \
"v_cmp_ne_u32 vcc, s40, v214\ns_cbranch_vccnz .Lrd_sg1%=\nv_max_u32_dpp v215, v215, v215 quad_perm:[1,0,3,2] row_mask:0xf bank_mask:0xf\ns_nop 1\nv_max_u32_dpp v215, v215, v215 quad_perm:[2,3,0,1] row_mask:0xf bank_mask:0xf\ns_nop 1\nv_max_u32_dpp v215, v215, v215 row_half_mirror row_mask:0xf bank_mask:0xf\ns_nop 0\n" \
"v_readlane_b32 s86, v215, 0\nv_readlane_b32 s87, v215, 8\ns_add_u32 s86, s86, %[du]\n.Lrd_tw608%=:\ns_waitcnt lgkmcnt(0)\ns_sub_u32 s44, s86, s68\ns_add_u32 s44, s44, 11\ns_cmp_lt_u32 s44, 250\n" \
"s_cbranch_scc1 .Lrd_tw608x%=\nbuffer_load_dwordx4 v[128:131], v196, %[rs], %[su] offen offset:0 sc1\nbuffer_load_dwordx4 v[132:135], v196, %[rs], %[su] offen offset:1024 sc1\ns_memrealtime s[68:69]\nbuffer_load_dwordx4 v[136:139], v196, %[rs], %[su] offen offset:2048 sc1\nbuffer_load_dwordx4 v[140:143], v196, %[rs], %[su] offen offset:3072 sc1\n.Lrd_tw619%=:\ns_waitcnt lgkmcnt(0)\n" \
"s_sub_u32 s44, s86, s68\ns_add_u32 s44, s44, 21\ns_cmp_lt_u32 s44, 250\ns_cbranch_scc1 .Lrd_tw619x%=\nbuffer_load_dwordx4 v[144:147], v196, %[rs], s57 offen offset:0 sc1\nbuffer_load_dwordx4 v[148:151], v196, %[rs], s57 offen offset:1024 sc1\ns_memrealtime s[68:69]\nbuffer_load_dwordx4 v[152:155], v196, %[rs], s57 offen offset:2048 sc1\n" \
"buffer_load_dwordx4 v[156:159], v196, %[rs], s57 offen offset:3072 sc1\ns_waitcnt vmcnt(14)\nbuffer_store_dword v207, off, %[rs], s59 sc1\ns_mov_b32 s59, -2147483648\n.Lrd_tw633%=:\ns_waitcnt lgkmcnt(0)\ns_sub_u32 s44, s87, s68\ns_add_u32 s44, s44, 24\n" \
"s_cmp_lt_u32 s44, 250\ns_cbranch_scc1 .Lrd_tw633x%=\nbuffer_load_dwordx4 v[160:163], v196, %[rs], %[sp] offen offset:0 sc1\nbuffer_load_dwordx4 v[164:167], v196, %[rs], %[sp] offen offset:1024 sc1\nbuffer_load_dwordx4 v[168:171], v196, %[rs], %[sp] offen offset:2048 sc1\nbuffer_load_dwordx4 v[172:175], v196, %[rs], %[sp] offen offset:3072 sc1\nbuffer_load_dwordx4 v[176:179], v196, %[rs], s56 offen offset:0 sc1\ns_add_u32 s60, s58, 16384\n" \
"buffer_load_dwordx4 v[212:215], v196, %[rs], s60 offen offset:0 sc1\nv_bitop3_b32 v209, v0, v2, s55 bitop3:0x7e\nv_bitop3_b32 v210, v32, v34, s55 bitop3:0x7e\nv_or3_b32 v208, v209, v210, 0\nv_bitop3_b32 v209, v64, v66, s55 bitop3:0x7e\nv_bitop3_b32 v210, v4, v6, s55 bitop3:0x7e\nv_or3_b32 v208, v209, v210, v208\ns_add_u32 s60, s58, 0\n" \
"buffer_load_dwordx4 v[216:219], v196, %[rs], s60 offen offset:0 sc1\nv_bitop3_b32 v209, v36, v38, s55 bitop3:0x7e\nv_bitop3_b32 v210, v68, v70, s55 bitop3:0x7e\nv_or3_b32 v208, v209, v210, v208\nv_bitop3_b32 v209, v8, v10, s55 bitop3:0x7e\nv_bitop3_b32 v210, v40, v42, s55 bitop3:0x7e\nv_or3_b32 v208, v209, v210, v208\ns_add_u32 s60, s58, 0\n" \
"buffer_load_dwordx4 v[220:223], v196, %[rs], s60 offen offset:1024 sc1\nv_bitop3_b32 v209, v72, v74, s55 bitop3:0x7e\nv_bitop3_b32 v210, v12, v14, s55 bitop3:0x7e\nv_or3_b32 v208, v209, v210, v208\nv_bitop3_b32 v209, v44, v46, s55 bitop3:0x7e\nv_bitop3_b32 v210, v76, v78, s55 bitop3:0x7e\nv_or3_b32 v208, v209, v210, v208\ns_add_u32 s60, s58, 16384\n" \
"buffer_load_dwordx4 v[224:227], v196, %[rs], s60 offen offset:1024 sc1\nv_bitop3_b32 v209, v16, v18, s55 bitop3:0x7e\nv_bitop3_b32 v210, v48, v50, s55 bitop3:0x7e\nv_or3_b32 v208, v209, v210, v208\nv_bitop3_b32 v209, v80, v82, s55 bitop3:0x7e\nv_bitop3_b32 v210, v20, v22, s55 bitop3:0x7e\nv_or3_b32 v208, v209, v210, v208\ns_add_u32 s60, s58, 0\n" \
"buffer_load_dwordx4 v[228:231], v196, %[rs], s60 offen offset:2048 sc1\nv_bitop3_b32 v209, v52, v54, s55 bitop3:0x7e\nv_bitop3_b32 v210, v84, v86, s55 bitop3:0x7e\nv_or3_b32 v208, v209, v210, v208\nv_bitop3_b32 v209, v24, v26, s55 bitop3:0x7e\nv_bitop3_b32 v210, v56, v58, s55 bitop3:0x7e\nv_or3_b32 v208, v209, v210, v208\nv_bitop3_b32 v209, v88, v90, s55 bitop3:0x7e\n" \
"v_bitop3_b32 v210, v28, v30, s55 bitop3:0x7e\nv_or3_b32 v208, v209, v210, v208\nv_bitop3_b32 v209, v60, v62, s55 bitop3:0x7e\nv_bitop3_b32 v210, v92, v94, s55 bitop3:0x7e\nv_or3_b32 v208, v209, v210, v208\ns_waitcnt vmcnt(23)\nv_bitop3_b32 v209, v96, v98, s55 bitop3:0x7e\nv_bitop3_b32 v210, v100, v102, s55 bitop3:0x7e\n" \
"v_or3_b32 v208, v209, v210, v208\nv_bitop3_b32 v209, v104, v106, s55 bitop3:0x7e\nv_bitop3_b32 v210, v108, v110, s55 bitop3:0x7e\nv_or3_b32 v208, v209, v210, v208\ns_waitcnt vmcnt(19)\nv_bitop3_b32 v209, v112, v114, s55 bitop3:0x7e\nv_bitop3_b32 v210, v116, v118, s55 bitop3:0x7e\nv_or3_b32 v208, v209, v210, v208\n" \
"v_bitop3_b32 v209, v120, v122, s55 bitop3:0x7e\nv_bitop3_b32 v210, v124, v126, s55 bitop3:0x7e\nv_or3_b32 v208, v209, v210, v208\nv_and_b32 v208, 1, v208\nv_cmp_ne_u32 vcc, 0, v208\ns_cbranch_vccnz .Lrd_yre%=\ns_branch .Lrd_ra%=\n.Lrd_sg1%=:\n" \
"buffer_load_dwordx2 v[214:215], v212, %[rs], 0 offen sc1\ns_waitcnt vmcnt(0)\nbuffer_store_dword v207, off, %[rs], s59 sc1\ns_mov_b32 s59, -2147483648\ns_add_u32 s43, s43, 1\ns_cmp_lt_u32 s43, 262144\ns_cbranch_scc1 .Lrd_sg0%=\ns_branch .Lrd_abort%=\n" \
".Lrd_tw608x%=:\ns_memrealtime s[68:69]\ns_branch .Lrd_tw608%=\n.Lrd_tw619x%=:\ns_memrealtime s[68:69]\ns_branch .Lrd_tw619%=\n.Lrd_tw633x%=:\ns_memrealtime s[68:69]\n" \
"s_branch .Lrd_tw633%=\n.Lrd_fin%=:\ns_waitcnt vmcnt(0)\nbuffer_store_dword v207, off, %[rs], s59 sc1\ns_mov_b32 s59, -2147483648\ns_branch .Lrd_end%=\n.Lrd_yre%=:\ns_mov_b64 exec, vcc\n" \
"s_waitcnt vmcnt(0)\ns_add_u32 s43, s43, 1\ns_cmp_lt_u32 s43, 262144\ns_cbranch_scc1 .Lrd_yld%=\ns_branch .Lrd_abort%=\n.Lrd_rb%=:\ns_mov_b64 exec, s[46:47]\ns_cbranch_execz .Lrd_rs0%=\n" \
"buffer_load_dwordx4 v[160:163], v196, %[rs], %[sp] offen offset:0 sc1\n.Lrd_rs0%=:\ns_mov_b64 exec, s[48:49]\ns_cbranch_execz .Lrd_rs1%=\nbuffer_load_dwordx4 v[164:167], v196, %[rs], %[sp] offen offset:1024 sc1\n.Lrd_rs1%=:\ns_mov_b64 exec, s[50:51]\ns_cbranch_execz .Lrd_rs2%=\n" \
"buffer_load_dwordx4 v[168:171], v196, %[rs], %[sp] offen offset:2048 sc1\n.Lrd_rs2%=:\ns_mov_b64 exec, s[52:53]\ns_cbranch_execz .Lrd_rs3%=\nbuffer_load_dwordx4 v[172:175], v196, %[rs], %[sp] offen offset:3072 sc1\n.Lrd_rs3%=:\ns_mov_b64 exec, s[64:65]\ns_cbranch_execz .Lrd_rs4%=\n" \
"buffer_load_dwordx4 v[176:179], v196, %[rs], s56 offen offset:0 sc1\n.Lrd_rs4%=:\ns_mov_b64 exec, s[86:87]\ns_cbranch_execz .Lrd_rs5%=\nbuffer_load_dwordx4 v[128:131], v196, %[rs], %[su] offen offset:0 sc1\nbuffer_load_dwordx4 v[136:139], v196, %[rs], %[su] offen offset:2048 sc1\n.Lrd_rs5%=:\ns_mov_b64 exec, s[88:89]\n" \
"s_cbranch_execz .Lrd_rs6%=\nbuffer_load_dwordx4 v[144:147], v196, %[rs], s57 offen offset:0 sc1\nbuffer_load_dwordx4 v[152:155], v196, %[rs], s57 offen offset:2048 sc1\n.Lrd_rs6%=:\ns_mov_b64 exec, s[68:69]\ns_cbranch_execz .Lrd_rs7%=\nbuffer_load_dwordx4 v[132:135], v196, %[rs], %[su] offen offset:1024 sc1\nbuffer_load_dwordx4 v[140:143], v196, %[rs], %[su] offen offset:3072 sc1\n" \
".Lrd_rs7%=:\ns_mov_b64 exec, s[62:63]\ns_cbranch_execz .Lrd_rs8%=\nbuffer_load_dwordx4 v[148:151], v196, %[rs], s57 offen offset:1024 sc1\nbuffer_load_dwordx4 v[156:159], v196, %[rs], s57 offen offset:3072 sc1\n.Lrd_rs8%=:\ns_mov_b64 exec, -1\nv_mov_b64 v[180:181], 0\n" \
"v_mov_b64 v[182:183], 0\nv_mov_b64 v[184:185], 0\nv_mov_b64 v[186:187], 0\nv_mov_b64 v[188:189], 0\nv_mov_b64 v[190:191], 0\nv_mov_b64 v[192:193], 0\nv_mov_b64 v[194:195], 0\ns_waitcnt vmcnt(0)\n" \
"s_add_u32 s43, s43, 1\ns_cmp_lt_u32 s43, 262144\ns_cbranch_scc1 .Lrd_ra%=\ns_branch .Lrd_abort%=\n.Lrd_y1%=:\nv_accvgpr_read_b32 v206, a32\nbuffer_load_dword v208, v206, %[rs], 0 offen sc1\ns_waitcnt vmcnt(0)\n" \
"v_cmp_gt_i32 vcc, 1, v208\ns_cbranch_vccz .Lrd_y1r%=\ns_add_u32 s43, s43, 1\ns_cmp_lt_u32 s43, 262144\ns_cbranch_scc1 .Lrd_y1%=\ns_branch .Lrd_abort%=\n.Lrd_tre0%=:\ns_add_u32 s60, s58, 16384\n" \
"buffer_load_dwordx4 v[212:215], v196, %[rs], s60 offen offset:0 sc1\ns_waitcnt vmcnt(0)\nv_bitop3_b32 v209, v212, v214, s61 bitop3:0x7e\nv_cmp_gt_i32 vcc, 0, v209\ns_cbranch_vccz .Lrd_tl0%=\ns_add_u32 s43, s43, 1\ns_cmp_lt_u32 s43, 262144\ns_cbranch_scc1 .Lrd_tre0%=\n" \
"s_branch .Lrd_abort%=\n.Lrd_tre1%=:\ns_add_u32 s60, s58, 0\nbuffer_load_dwordx4 v[216:219], v196, %[rs], s60 offen offset:0 sc1\ns_waitcnt vmcnt(0)\nv_bitop3_b32 v209, v216, v218, s61 bitop3:0x7e\nv_cmp_gt_i32 vcc, 0, v209\ns_cbranch_vccz .Lrd_tl1%=\n" \
"s_add_u32 s43, s43, 1\ns_cmp_lt_u32 s43, 262144\ns_cbranch_scc1 .Lrd_tre1%=\ns_branch .Lrd_abort%=\n.Lrd_tre2%=:\ns_add_u32 s60, s58, 0\nbuffer_load_dwordx4 v[220:223], v196, %[rs], s60 offen offset:1024 sc1\ns_waitcnt vmcnt(0)\n" \
"v_bitop3_b32 v209, v220, v222, s61 bitop3:0x7e\nv_cmp_gt_i32 vcc, 0, v209\ns_cbranch_vccz .Lrd_tl2%=\ns_add_u32 s43, s43, 1\ns_cmp_lt_u32 s43, 262144\ns_cbranch_scc1 .Lrd_tre2%=\ns_branch .Lrd_abort%=\n.Lrd_tre3%=:\n" \
"s_add_u32 s60, s58, 16384\nbuffer_load_dwordx4 v[224:227], v196, %[rs], s60 offen offset:1024 sc1\ns_waitcnt vmcnt(0)\nv_bitop3_b32 v209, v224, v226, s61 bitop3:0x7e\nv_cmp_gt_i32 vcc, 0, v209\ns_cbranch_vccz .Lrd_tl3%=\ns_add_u32 s43, s43, 1\ns_cmp_lt_u32 s43, 262144\n" \
"s_cbranch_scc1 .Lrd_tre3%=\ns_branch .Lrd_abort%=\n.Lrd_tre4%=:\ns_add_u32 s60, s58, 0\nbuffer_load_dwordx4 v[228:231], v196, %[rs], s60 offen offset:2048 sc1\ns_waitcnt vmcnt(0)\nv_bitop3_b32 v209, v228, v230, s61 bitop3:0x7e\nv_cmp_gt_i32 vcc, 0, v209\n" \
"s_cbranch_vccz .Lrd_tl4%=\ns_add_u32 s43, s43, 1\ns_cmp_lt_u32 s43, 262144\ns_cbranch_scc1 .Lrd_tre4%=\ns_branch .Lrd_abort%=\n.Lrd_tre5%=:\ns_add_u32 s60, s58, 0\nbuffer_load_dwordx4 v[232:235], v196, %[rs], s60 offen offset:3072 sc1\n" \
"s_waitcnt vmcnt(0)\nv_bitop3_b32 v209, v232, v234, s61 bitop3:0x7e\nv_cmp_gt_i32 vcc, 0, v209\ns_cbranch_vccz .Lrd_tl5%=\ns_add_u32 s43, s43, 1\ns_cmp_lt_u32 s43, 262144\ns_cbranch_scc1 .Lrd_tre5%=\ns_branch .Lrd_abort%=\n" \
".Lrd_tre6%=:\ns_add_u32 s60, s58, 16384\nbuffer_load_dwordx4 v[236:239], v196, %[rs], s60 offen offset:2048 sc1\ns_waitcnt vmcnt(0)\nv_bitop3_b32 v209, v236, v238, s61 bitop3:0x7e\nv_cmp_gt_i32 vcc, 0, v209\ns_cbranch_vccz .Lrd_tl6%=\ns_add_u32 s43, s43, 1\n" \
"s_cmp_lt_u32 s43, 262144\ns_cbranch_scc1 .Lrd_tre6%=\ns_branch .Lrd_abort%=\n.Lrd_tre7%=:\ns_add_u32 s60, s58, 4096\nbuffer_load_dwordx4 v[240:243], v196, %[rs], s60 offen offset:0 sc1\ns_waitcnt vmcnt(0)\nv_bitop3_b32 v209, v240, v242, s61 bitop3:0x7e\n" \
"v_cmp_gt_i32 vcc, 0, v209\ns_cbranch_vccz .Lrd_tl7%=\ns_add_u32 s43, s43, 1\ns_cmp_lt_u32 s43, 262144\ns_cbranch_scc1 .Lrd_tre7%=\ns_branch .Lrd_abort%=\n.Lrd_tre8%=:\ns_add_u32 s60, s58, 4096\n" \
"buffer_load_dwordx4 v[244:247], v196, %[rs], s60 offen offset:1024 sc1\ns_waitcnt vmcnt(0)\nv_bitop3_b32 v209, v244, v246, s61 bitop3:0x7e\nv_cmp_gt_i32 vcc, 0, v209\ns_cbranch_vccz .Lrd_tl8%=\ns_add_u32 s43, s43, 1\ns_cmp_lt_u32 s43, 262144\ns_cbranch_scc1 .Lrd_tre8%=\n" \
"s_branch .Lrd_abort%=\n.Lrd_tre9%=:\ns_add_u32 s60, s58, 16384\nbuffer_load_dwordx4 v[248:251], v196, %[rs], s60 offen offset:3072 sc1\ns_waitcnt vmcnt(0)\nv_bitop3_b32 v209, v248, v250, s61 bitop3:0x7e\nv_cmp_gt_i32 vcc, 0, v209\ns_cbranch_vccz .Lrd_tl9%=\n" \
"s_add_u32 s43, s43, 1\ns_cmp_lt_u32 s43, 262144\ns_cbranch_scc1 .Lrd_tre9%=\ns_branch .Lrd_abort%=\n.Lrd_tre10%=:\ns_add_u32 s60, s58, 20480\nbuffer_load_dwordx4 v[252:255], v196, %[rs], s60 offen offset:0 sc1\ns_waitcnt vmcnt(0)\n" \
"v_bitop3_b32 v209, v252, v254, s61 bitop3:0x7e\nv_cmp_gt_i32 vcc, 0, v209\ns_cbranch_vccz .Lrd_tl10%=\ns_add_u32 s43, s43, 1\ns_cmp_lt_u32 s43, 262144\ns_cbranch_scc1 .Lrd_tre10%=\ns_branch .Lrd_abort%=\n.Lrd_tre11%=:\n" \
"s_add_u32 s60, s58, 20480\nbuffer_load_dwordx4 v[160:163], v196, %[rs], s60 offen offset:2048 sc1\ns_waitcnt vmcnt(0)\nv_bitop3_b32 v209, v160, v162, s61 bitop3:0x7e\nv_cmp_gt_i32 vcc, 0, v209\ns_cbranch_vccz .Lrd_tl11%=\ns_add_u32 s43, s43, 1\ns_cmp_lt_u32 s43, 262144\n" \
"s_cbranch_scc1 .Lrd_tre11%=\ns_branch .Lrd_abort%=\n.Lrd_tre12%=:\ns_add_u32 s60, s58, 4096\nbuffer_load_dwordx4 v[164:167], v196, %[rs], s60 offen offset:2048 sc1\ns_waitcnt vmcnt(0)\nv_bitop3_b32 v209, v164, v166, s61 bitop3:0x7e\nv_cmp_gt_i32 vcc, 0, v209\n" \
"s_cbranch_vccz .Lrd_tl12%=\ns_add_u32 s43, s43, 1\ns_cmp_lt_u32 s43, 262144\ns_cbranch_scc1 .Lrd_tre12%=\ns_branch .Lrd_abort%=\n.Lrd_tre13%=:\ns_add_u32 s60, s58, 4096\nbuffer_load_dwordx4 v[168:171], v196, %[rs], s60 offen offset:3072 sc1\n" \
"s_waitcnt vmcnt(0)\nv_bitop3_b32 v209, v168, v170, s61 bitop3:0x7e\nv_cmp_gt_i32 vcc, 0, v209\ns_cbranch_vccz .Lrd_tl13%=\ns_add_u32 s43, s43, 1\ns_cmp_lt_u32 s43, 262144\ns_cbranch_scc1 .Lrd_tre13%=\ns_branch .Lrd_abort%=\n" \
".Lrd_tre14%=:\ns_add_u32 s60, s58, 20480\nbuffer_load_dwordx4 v[172:175], v196, %[rs], s60 offen offset:1024 sc1\ns_waitcnt vmcnt(0)\nv_bitop3_b32 v209, v172, v174, s61 bitop3:0x7e\nv_cmp_gt_i32 vcc, 0, v209\ns_cbranch_vccz .Lrd_tl14%=\ns_add_u32 s43, s43, 1\n" \
"s_cmp_lt_u32 s43, 262144\ns_cbranch_scc1 .Lrd_tre14%=\ns_branch .Lrd_abort%=\n.Lrd_tre15%=:\ns_add_u32 s60, s58, 20480\nbuffer_load_dwordx4 v[176:179], v196, %[rs], s60 offen offset:3072 sc1\ns_waitcnt vmcnt(0)\nv_bitop3_b32 v209, v176, v178, s61 bitop3:0x7e\n" \
"v_cmp_gt_i32 vcc, 0, v209\ns_cbranch_vccz .Lrd_tl15%=\ns_add_u32 s43, s43, 1\ns_cmp_lt_u32 s43, 262144\ns_cbranch_scc1 .Lrd_tre15%=\ns_branch .Lrd_abort%=\n.Lrd_abort%=:\ns_mov_b64 exec, -1\n" \
"v_mov_b32 v16, 0\ns_movk_i32 s44, 6400\nbuffer_store_dword v16, off, %[rs], s44 sc1\n.Lrd_end%=:\ns_waitcnt vmcnt(0) lgkmcnt(0)\n" \
""
#define GG_ASM \
"v_mbcnt_lo_u32_b32 v255, -1, 0\nv_mbcnt_hi_u32_b32 v255, -1, v255\nv_lshlrev_b32 v255, 2, v255\nv_add_u32 v255, %[tb], v255\nds_read_b32 v0, v255 offset:0\nds_read_b32 v1, v255 offset:256\nds_read_b32 v2, v255 offset:512\nds_read_b32 v3, v255 offset:768\n" \
"ds_read_b32 v4, v255 offset:1024\nds_read_b32 v5, v255 offset:1280\nds_read_b32 v92, v255 offset:1536\nds_read_b32 v93, v255 offset:1792\nds_read_b32 v94, v255 offset:2048\ns_waitcnt lgkmcnt(0)\nv_mov_b32 v6, 0x3f803f80\nv_mov_b32 v7, 0x3f803f80\n" \
"s_add_u32 m0, %[lw], 16384\ns_add_u32 s44, %[wo], 0\nbuffer_load_dwordx4 v92, %[rw], s44 offen nt lds\ns_add_u32 m0, %[lw], 17408\ns_add_u32 s44, %[wo], 57344\nbuffer_load_dwordx4 v92, %[rw], s44 offen nt lds\ns_add_u32 m0, %[lw], 18432\ns_add_u32 s44, %[wo], 114688\n" \
"buffer_load_dwordx4 v92, %[rw], s44 offen nt lds\ns_add_u32 m0, %[lw], 19456\ns_add_u32 s44, %[wo], 172032\nbuffer_load_dwordx4 v92, %[rw], s44 offen nt lds\ns_add_u32 m0, %[lw], 20480\ns_add_u32 s44, %[wo], 229376\nbuffer_load_dwordx4 v92, %[rw], s44 offen nt lds\ns_add_u32 m0, %[lw], 21504\n" \
"s_add_u32 s44, %[wo], 286720\nbuffer_load_dwordx4 v92, %[rw], s44 offen nt lds\ns_add_u32 m0, %[lw], 22528\ns_add_u32 s44, %[wo], 344064\nbuffer_load_dwordx4 v92, %[rw], s44 offen nt lds\ns_add_u32 m0, %[lw], 23552\ns_add_u32 s44, %[wo], 401408\nbuffer_load_dwordx4 v92, %[rw], s44 offen nt lds\n" \
"s_add_u32 m0, %[tb], 0\ns_add_u32 s44, %[wo], 1835008\nbuffer_load_dwordx4 v92, %[rw], s44 offen nt lds\ns_add_u32 m0, %[tb], 1024\ns_add_u32 s44, %[wo], 1892352\nbuffer_load_dwordx4 v92, %[rw], s44 offen nt lds\ns_add_u32 m0, %[tb], 2048\ns_add_u32 s44, %[wo], 1949696\n" \
"buffer_load_dwordx4 v92, %[rw], s44 offen nt lds\ns_add_u32 m0, %[tb], 3072\ns_add_u32 s44, %[wo], 2007040\nbuffer_load_dwordx4 v92, %[rw], s44 offen nt lds\ns_add_u32 m0, %[tb], 4096\ns_add_u32 s44, %[wo], 2064384\nbuffer_load_dwordx4 v92, %[rw], s44 offen nt lds\ns_add_u32 m0, %[tb], 5120\n" \
"s_add_u32 s44, %[wo], 2121728\nbuffer_load_dwordx4 v92, %[rw], s44 offen nt lds\ns_add_u32 m0, %[tb], 6144\ns_add_u32 s44, %[wo], 2179072\nbuffer_load_dwordx4 v92, %[rw], s44 offen nt lds\ns_add_u32 m0, %[tb], 7168\ns_add_u32 s44, %[wo], 2236416\nbuffer_load_dwordx4 v92, %[rw], s44 offen nt lds\n" \
"s_add_u32 m0, %[tb], 8192\ns_add_u32 s44, %[wo], 3670016\nbuffer_load_dwordx4 v92, %[rw], s44 offen nt lds\ns_add_u32 m0, %[tb], 9216\ns_add_u32 s44, %[wo], 3727360\nbuffer_load_dwordx4 v92, %[rw], s44 offen nt lds\ns_add_u32 m0, %[tb], 10240\ns_add_u32 s44, %[wo], 3784704\n" \
"buffer_load_dwordx4 v92, %[rw], s44 offen nt lds\ns_add_u32 m0, %[tb], 11264\ns_add_u32 s44, %[wo], 3842048\nbuffer_load_dwordx4 v92, %[rw], s44 offen nt lds\ns_add_u32 m0, %[lw], 0\ns_add_u32 s44, %[wo], 3899392\nbuffer_load_dwordx4 v92, %[rw], s44 offen nt lds\ns_add_u32 m0, %[lw], 1024\n" \
"s_add_u32 s44, %[wo], 3956736\nbuffer_load_dwordx4 v92, %[rw], s44 offen nt lds\ns_add_u32 m0, %[lw], 2048\ns_add_u32 s44, %[wo], 4014080\nbuffer_load_dwordx4 v92, %[rw], s44 offen nt lds\ns_add_u32 m0, %[lw], 3072\ns_add_u32 s44, %[wo], 4071424\nbuffer_load_dwordx4 v92, %[rw], s44 offen nt lds\n" \
"s_add_u32 m0, %[lw], 4096\ns_add_u32 s44, %[wo], 5505024\nbuffer_load_dwordx4 v92, %[rw], s44 offen nt lds\ns_add_u32 m0, %[lw], 5120\ns_add_u32 s44, %[wo], 5562368\nbuffer_load_dwordx4 v92, %[rw], s44 offen nt lds\ns_add_u32 m0, %[lw], 6144\ns_add_u32 s44, %[wo], 5619712\n" \
"buffer_load_dwordx4 v92, %[rw], s44 offen nt lds\ns_add_u32 m0, %[lw], 7168\ns_add_u32 s44, %[wo], 5677056\nbuffer_load_dwordx4 v92, %[rw], s44 offen nt lds\ns_add_u32 m0, %[lw], 8192\ns_add_u32 s44, %[wo], 5734400\nbuffer_load_dwordx4 v92, %[rw], s44 offen nt lds\ns_add_u32 m0, %[lw], 9216\n" \
"s_add_u32 s44, %[wo], 5791744\nbuffer_load_dwordx4 v92, %[rw], s44 offen nt lds\ns_add_u32 m0, %[lw], 10240\ns_add_u32 s44, %[wo], 5849088\nbuffer_load_dwordx4 v92, %[rw], s44 offen nt lds\ns_add_u32 m0, %[lw], 11264\ns_add_u32 s44, %[wo], 5906432\nbuffer_load_dwordx4 v92, %[rw], s44 offen nt lds\n" \
"s_waitcnt vmcnt(28)\ns_barrier\ns_add_u32 m0, %[lw], 12288\ns_add_u32 s44, %[wo], 458752\nbuffer_load_dwordx4 v92, %[rw], s44 offen nt lds\ns_add_u32 m0, %[lw], 13312\ns_add_u32 s44, %[wo], 516096\nbuffer_load_dwordx4 v92, %[rw], s44 offen nt lds\n" \
"s_add_u32 m0, %[lw], 14336\ns_add_u32 s44, %[wo], 573440\nbuffer_load_dwordx4 v92, %[rw], s44 offen nt lds\ns_add_u32 m0, %[lw], 15360\ns_add_u32 s44, %[wo], 630784\nbuffer_load_dwordx4 v92, %[rw], s44 offen nt lds\nds_read_b64_tr_b16 a[0:1], v94 offset:16384\nds_read_b64_tr_b16 a[2:3], v94 offset:16896\n" \
"ds_read_b64_tr_b16 a[8:9], v94 offset:16392\nds_read_b64_tr_b16 a[10:11], v94 offset:16904\nds_read_b64_tr_b16 a[16:17], v94 offset:40960\nds_read_b64_tr_b16 a[18:19], v94 offset:41472\nds_read_b64_tr_b16 a[24:25], v94 offset:40968\nds_read_b64_tr_b16 a[26:27], v94 offset:41480\ns_waitcnt lgkmcnt(0)\nv_mfma_f32_16x16x16_bf16 v[8:11], a[0:1], v[6:7], 0\n" \
"v_mfma_f32_16x16x16_bf16 v[12:15], a[8:9], v[6:7], 0\nv_mfma_f32_16x16x16_bf16 v[16:19], a[16:17], v[6:7], 0\nv_mfma_f32_16x16x16_bf16 v[20:23], a[24:25], v[6:7], 0\nv_mfma_f32_16x16x16_bf16 v[8:11], a[2:3], v[6:7], v[8:11]\nv_mfma_f32_16x16x16_bf16 v[12:15], a[10:11], v[6:7], v[12:15]\nv_mfma_f32_16x16x16_bf16 v[16:19], a[18:19], v[6:7], v[16:19]\nv_mfma_f32_16x16x16_bf16 v[20:23], a[26:27], v[6:7], v[20:23]\ns_waitcnt vmcnt(28)\n" \
"s_barrier\ns_add_u32 m0, %[lw], 16384\ns_add_u32 s44, %[wo], 688128\nbuffer_load_dwordx4 v92, %[rw], s44 offen nt lds\ns_add_u32 m0, %[lw], 17408\ns_add_u32 s44, %[wo], 745472\nbuffer_load_dwordx4 v92, %[rw], s44 offen nt lds\ns_add_u32 m0, %[lw], 18432\n" \
"s_add_u32 s44, %[wo], 802816\nbuffer_load_dwordx4 v92, %[rw], s44 offen nt lds\ns_add_u32 m0, %[lw], 19456\ns_add_u32 s44, %[wo], 860160\nbuffer_load_dwordx4 v92, %[rw], s44 offen nt lds\nds_read_b64_tr_b16 a[4:5], v94 offset:20480\nds_read_b64_tr_b16 a[6:7], v94 offset:20992\nds_read_b64_tr_b16 a[12:13], v94 offset:20488\n" \
"ds_read_b64_tr_b16 a[14:15], v94 offset:21000\nds_read_b64_tr_b16 a[20:21], v94 offset:45056\nds_read_b64_tr_b16 a[22:23], v94 offset:45568\nds_read_b64_tr_b16 a[28:29], v94 offset:45064\nds_read_b64_tr_b16 a[30:31], v94 offset:45576\ns_waitcnt lgkmcnt(0)\nv_mfma_f32_16x16x16_bf16 v[8:11], a[4:5], v[6:7], v[8:11]\nv_mfma_f32_16x16x16_bf16 v[12:15], a[12:13], v[6:7], v[12:15]\n" \
"v_mfma_f32_16x16x16_bf16 v[16:19], a[20:21], v[6:7], v[16:19]\nv_mfma_f32_16x16x16_bf16 v[20:23], a[28:29], v[6:7], v[20:23]\nv_mfma_f32_16x16x16_bf16 v[8:11], a[6:7], v[6:7], v[8:11]\nv_mfma_f32_16x16x16_bf16 v[12:15], a[14:15], v[6:7], v[12:15]\nv_mfma_f32_16x16x16_bf16 v[16:19], a[22:23], v[6:7], v[16:19]\nv_mfma_f32_16x16x16_bf16 v[20:23], a[30:31], v[6:7], v[20:23]\ns_waitcnt vmcnt(28)\ns_barrier\n" \
"s_add_u32 m0, %[lw], 20480\ns_add_u32 s44, %[wo], 917504\nbuffer_load_dwordx4 v92, %[rw], s44 offen nt lds\ns_add_u32 m0, %[lw], 21504\ns_add_u32 s44, %[wo], 974848\nbuffer_load_dwordx4 v92, %[rw], s44 offen nt lds\ns_add_u32 m0, %[lw], 22528\ns_add_u32 s44, %[wo], 1032192\n" \
"buffer_load_dwordx4 v92, %[rw], s44 offen nt lds\ns_add_u32 m0, %[lw], 23552\ns_add_u32 s44, %[wo], 1089536\nbuffer_load_dwordx4 v92, %[rw], s44 offen nt lds\nds_read_b64_tr_b16 a[104:105], v93 offset:0\nds_read_b64_tr_b16 a[106:107], v93 offset:512\nds_read_b64_tr_b16 a[112:113], v93 offset:8\nds_read_b64_tr_b16 a[114:115], v93 offset:520\n" \
"ds_read_b64_tr_b16 a[120:121], v93 offset:12288\nds_read_b64_tr_b16 a[122:123], v93 offset:12800\nds_read_b64_tr_b16 a[128:129], v93 offset:12296\nds_read_b64_tr_b16 a[130:131], v93 offset:12808\ns_waitcnt lgkmcnt(0)\nv_mfma_f32_16x16x16_bf16 v[8:11], a[104:105], v[6:7], v[8:11]\nv_mfma_f32_16x16x16_bf16 v[12:15], a[112:113], v[6:7], v[12:15]\nv_mfma_f32_16x16x16_bf16 v[16:19], a[120:121], v[6:7], v[16:19]\n" \
"v_mfma_f32_16x16x16_bf16 v[20:23], a[128:129], v[6:7], v[20:23]\nv_mfma_f32_16x16x16_bf16 v[8:11], a[106:107], v[6:7], v[8:11]\nv_mfma_f32_16x16x16_bf16 v[12:15], a[114:115], v[6:7], v[12:15]\nv_mfma_f32_16x16x16_bf16 v[16:19], a[122:123], v[6:7], v[16:19]\nv_mfma_f32_16x16x16_bf16 v[20:23], a[130:131], v[6:7], v[20:23]\ns_waitcnt vmcnt(28)\ns_barrier\ns_add_u32 m0, %[tb], 0\n" \
"s_add_u32 s44, %[wo], 1146880\nbuffer_load_dwordx4 v92, %[rw], s44 offen nt lds\ns_add_u32 m0, %[tb], 1024\ns_add_u32 s44, %[wo], 1204224\nbuffer_load_dwordx4 v92, %[rw], s44 offen nt lds\ns_add_u32 m0, %[tb], 2048\ns_add_u32 s44, %[wo], 1261568\nbuffer_load_dwordx4 v92, %[rw], s44 offen nt lds\n" \
"s_add_u32 m0, %[tb], 3072\ns_add_u32 s44, %[wo], 1318912\nbuffer_load_dwordx4 v92, %[rw], s44 offen nt lds\nds_read_b64_tr_b16 a[108:109], v93 offset:4096\nds_read_b64_tr_b16 a[110:111], v93 offset:4608\nds_read_b64_tr_b16 a[116:117], v93 offset:4104\nds_read_b64_tr_b16 a[118:119], v93 offset:4616\nds_read_b64_tr_b16 a[124:125], v93 offset:16384\n" \
"ds_read_b64_tr_b16 a[126:127], v93 offset:16896\nds_read_b64_tr_b16 a[132:133], v93 offset:16392\nds_read_b64_tr_b16 a[134:135], v93 offset:16904\ns_waitcnt lgkmcnt(0)\nv_mfma_f32_16x16x16_bf16 v[8:11], a[108:109], v[6:7], v[8:11]\nv_mfma_f32_16x16x16_bf16 v[12:15], a[116:117], v[6:7], v[12:15]\nv_mfma_f32_16x16x16_bf16 v[16:19], a[124:125], v[6:7], v[16:19]\nv_mfma_f32_16x16x16_bf16 v[20:23], a[132:133], v[6:7], v[20:23]\n" \
"v_mfma_f32_16x16x16_bf16 v[8:11], a[110:111], v[6:7], v[8:11]\nv_mfma_f32_16x16x16_bf16 v[12:15], a[118:119], v[6:7], v[12:15]\nv_mfma_f32_16x16x16_bf16 v[16:19], a[126:127], v[6:7], v[16:19]\nv_mfma_f32_16x16x16_bf16 v[20:23], a[134:135], v[6:7], v[20:23]\ns_waitcnt vmcnt(28)\ns_barrier\ns_add_u32 m0, %[tb], 4096\ns_add_u32 s44, %[wo], 1376256\n" \
"buffer_load_dwordx4 v92, %[rw], s44 offen nt lds\ns_add_u32 m0, %[tb], 5120\ns_add_u32 s44, %[wo], 1433600\nbuffer_load_dwordx4 v92, %[rw], s44 offen nt lds\ns_add_u32 m0, %[tb], 6144\ns_add_u32 s44, %[wo], 1490944\nbuffer_load_dwordx4 v92, %[rw], s44 offen nt lds\ns_add_u32 m0, %[tb], 7168\n" \
"s_add_u32 s44, %[wo], 1548288\nbuffer_load_dwordx4 v92, %[rw], s44 offen nt lds\nds_read_b64_tr_b16 a[208:209], v93 offset:8192\nds_read_b64_tr_b16 a[210:211], v93 offset:8704\nds_read_b64_tr_b16 a[216:217], v93 offset:8200\nds_read_b64_tr_b16 a[218:219], v93 offset:8712\nds_read_b64_tr_b16 a[224:225], v93 offset:20480\nds_read_b64_tr_b16 a[226:227], v93 offset:20992\n" \
"ds_read_b64_tr_b16 a[232:233], v93 offset:20488\nds_read_b64_tr_b16 a[234:235], v93 offset:21000\ns_waitcnt lgkmcnt(0)\nv_mfma_f32_16x16x16_bf16 v[8:11], a[208:209], v[6:7], v[8:11]\nv_mfma_f32_16x16x16_bf16 v[12:15], a[216:217], v[6:7], v[12:15]\nv_mfma_f32_16x16x16_bf16 v[16:19], a[224:225], v[6:7], v[16:19]\nv_mfma_f32_16x16x16_bf16 v[20:23], a[232:233], v[6:7], v[20:23]\nv_mfma_f32_16x16x16_bf16 v[8:11], a[210:211], v[6:7], v[8:11]\n" \
"v_mfma_f32_16x16x16_bf16 v[12:15], a[218:219], v[6:7], v[12:15]\nv_mfma_f32_16x16x16_bf16 v[16:19], a[226:227], v[6:7], v[16:19]\nv_mfma_f32_16x16x16_bf16 v[20:23], a[234:235], v[6:7], v[20:23]\ns_waitcnt vmcnt(28)\ns_barrier\ns_add_u32 m0, %[tb], 8192\ns_add_u32 s44, %[wo], 1605632\nbuffer_load_dwordx4 v92, %[rw], s44 offen nt lds\n" \
"s_add_u32 m0, %[tb], 9216\ns_add_u32 s44, %[wo], 1662976\nbuffer_load_dwordx4 v92, %[rw], s44 offen nt lds\ns_add_u32 m0, %[tb], 10240\ns_add_u32 s44, %[wo], 1720320\nbuffer_load_dwordx4 v92, %[rw], s44 offen nt lds\ns_add_u32 m0, %[tb], 11264\ns_add_u32 s44, %[wo], 1777664\n" \
"buffer_load_dwordx4 v92, %[rw], s44 offen nt lds\nds_read_b64_tr_b16 a[212:213], v94 offset:0\nds_read_b64_tr_b16 a[214:215], v94 offset:512\nds_read_b64_tr_b16 a[220:221], v94 offset:8\nds_read_b64_tr_b16 a[222:223], v94 offset:520\nds_read_b64_tr_b16 a[228:229], v94 offset:24576\nds_read_b64_tr_b16 a[230:231], v94 offset:25088\nds_read_b64_tr_b16 a[236:237], v94 offset:24584\n" \
"ds_read_b64_tr_b16 a[238:239], v94 offset:25096\ns_waitcnt lgkmcnt(0)\nv_mfma_f32_16x16x16_bf16 v[8:11], a[212:213], v[6:7], v[8:11]\nv_mfma_f32_16x16x16_bf16 v[12:15], a[220:221], v[6:7], v[12:15]\nv_mfma_f32_16x16x16_bf16 v[16:19], a[228:229], v[6:7], v[16:19]\nv_mfma_f32_16x16x16_bf16 v[20:23], a[236:237], v[6:7], v[20:23]\nv_mfma_f32_16x16x16_bf16 v[8:11], a[214:215], v[6:7], v[8:11]\nv_mfma_f32_16x16x16_bf16 v[12:15], a[222:223], v[6:7], v[12:15]\n" \
"v_mfma_f32_16x16x16_bf16 v[16:19], a[230:231], v[6:7], v[16:19]\nv_mfma_f32_16x16x16_bf16 v[20:23], a[238:239], v[6:7], v[20:23]\ns_waitcnt vmcnt(28)\ns_barrier\ns_add_u32 m0, %[lw], 0\ns_add_u32 s44, %[wo], 2293760\nbuffer_load_dwordx4 v92, %[rw], s44 offen nt lds\ns_add_u32 m0, %[lw], 1024\n" \
"s_add_u32 s44, %[wo], 2351104\nbuffer_load_dwordx4 v92, %[rw], s44 offen nt lds\ns_add_u32 m0, %[lw], 2048\ns_add_u32 s44, %[wo], 2408448\nbuffer_load_dwordx4 v92, %[rw], s44 offen nt lds\ns_add_u32 m0, %[lw], 3072\ns_add_u32 s44, %[wo], 2465792\nbuffer_load_dwordx4 v92, %[rw], s44 offen nt lds\n" \
"ds_read_b64_tr_b16 v[152:153], v94 offset:4096\nds_read_b64_tr_b16 v[154:155], v94 offset:4608\nds_read_b64_tr_b16 v[160:161], v94 offset:4104\nds_read_b64_tr_b16 v[162:163], v94 offset:4616\nds_read_b64_tr_b16 v[168:169], v94 offset:28672\nds_read_b64_tr_b16 v[170:171], v94 offset:29184\nds_read_b64_tr_b16 v[176:177], v94 offset:28680\nds_read_b64_tr_b16 v[178:179], v94 offset:29192\n" \
"s_waitcnt lgkmcnt(0)\nv_mfma_f32_16x16x16_bf16 v[8:11], v[152:153], v[6:7], v[8:11]\nv_mfma_f32_16x16x16_bf16 v[12:15], v[160:161], v[6:7], v[12:15]\nv_mfma_f32_16x16x16_bf16 v[16:19], v[168:169], v[6:7], v[16:19]\nv_mfma_f32_16x16x16_bf16 v[20:23], v[176:177], v[6:7], v[20:23]\nv_mfma_f32_16x16x16_bf16 v[8:11], v[154:155], v[6:7], v[8:11]\nv_mfma_f32_16x16x16_bf16 v[12:15], v[162:163], v[6:7], v[12:15]\nv_mfma_f32_16x16x16_bf16 v[16:19], v[170:171], v[6:7], v[16:19]\n" \
"v_mfma_f32_16x16x16_bf16 v[20:23], v[178:179], v[6:7], v[20:23]\ns_waitcnt vmcnt(28)\ns_barrier\ns_add_u32 m0, %[lw], 4096\ns_add_u32 s44, %[wo], 2523136\nbuffer_load_dwordx4 v92, %[rw], s44 offen nt lds\ns_add_u32 m0, %[lw], 5120\ns_add_u32 s44, %[wo], 2580480\n" \
"buffer_load_dwordx4 v92, %[rw], s44 offen nt lds\ns_add_u32 m0, %[lw], 6144\ns_add_u32 s44, %[wo], 2637824\nbuffer_load_dwordx4 v92, %[rw], s44 offen nt lds\ns_add_u32 m0, %[lw], 7168\ns_add_u32 s44, %[wo], 2695168\nbuffer_load_dwordx4 v92, %[rw], s44 offen nt lds\nds_read_b64_tr_b16 v[156:157], v94 offset:8192\n" \
"ds_read_b64_tr_b16 v[158:159], v94 offset:8704\nds_read_b64_tr_b16 v[164:165], v94 offset:8200\nds_read_b64_tr_b16 v[166:167], v94 offset:8712\nds_read_b64_tr_b16 v[172:173], v94 offset:32768\nds_read_b64_tr_b16 v[174:175], v94 offset:33280\nds_read_b64_tr_b16 v[180:181], v94 offset:32776\nds_read_b64_tr_b16 v[182:183], v94 offset:33288\ns_waitcnt lgkmcnt(0)\n" \
"v_mfma_f32_16x16x16_bf16 v[8:11], v[156:157], v[6:7], v[8:11]\nv_mfma_f32_16x16x16_bf16 v[12:15], v[164:165], v[6:7], v[12:15]\nv_mfma_f32_16x16x16_bf16 v[16:19], v[172:173], v[6:7], v[16:19]\nv_mfma_f32_16x16x16_bf16 v[20:23], v[180:181], v[6:7], v[20:23]\nv_mfma_f32_16x16x16_bf16 v[8:11], v[158:159], v[6:7], v[8:11]\nv_mfma_f32_16x16x16_bf16 v[12:15], v[166:167], v[6:7], v[12:15]\nv_mfma_f32_16x16x16_bf16 v[16:19], v[174:175], v[6:7], v[16:19]\nv_mfma_f32_16x16x16_bf16 v[20:23], v[182:183], v[6:7], v[20:23]\n" \
"s_waitcnt vmcnt(28)\ns_barrier\ns_add_u32 m0, %[lw], 8192\ns_add_u32 s44, %[wo], 2752512\nbuffer_load_dwordx4 v92, %[rw], s44 offen nt lds\ns_add_u32 m0, %[lw], 9216\ns_add_u32 s44, %[wo], 2809856\nbuffer_load_dwordx4 v92, %[rw], s44 offen nt lds\n" \
"s_add_u32 m0, %[lw], 10240\ns_add_u32 s44, %[wo], 2867200\nbuffer_load_dwordx4 v92, %[rw], s44 offen nt lds\ns_add_u32 m0, %[lw], 11264\ns_add_u32 s44, %[wo], 2924544\nbuffer_load_dwordx4 v92, %[rw], s44 offen nt lds\nds_read_b64_tr_b16 a[32:33], v94 offset:12288\nds_read_b64_tr_b16 a[34:35], v94 offset:12800\n" \
"ds_read_b64_tr_b16 v[88:89], v94 offset:12296\nds_read_b64_tr_b16 v[90:91], v94 offset:12808\nds_read_b64_tr_b16 a[40:41], v94 offset:36864\nds_read_b64_tr_b16 a[42:43], v94 offset:37376\nds_read_b64_tr_b16 a[48:49], v94 offset:36872\nds_read_b64_tr_b16 a[50:51], v94 offset:37384\ns_waitcnt lgkmcnt(0)\nv_mfma_f32_16x16x16_bf16 v[8:11], a[32:33], v[6:7], v[8:11]\n" \
"v_mfma_f32_16x16x16_bf16 v[12:15], v[88:89], v[6:7], v[12:15]\nv_mfma_f32_16x16x16_bf16 v[16:19], a[40:41], v[6:7], v[16:19]\nv_mfma_f32_16x16x16_bf16 v[20:23], a[48:49], v[6:7], v[20:23]\nv_mfma_f32_16x16x16_bf16 v[8:11], a[34:35], v[6:7], v[8:11]\nv_mfma_f32_16x16x16_bf16 v[12:15], v[90:91], v[6:7], v[12:15]\nv_mfma_f32_16x16x16_bf16 v[16:19], a[42:43], v[6:7], v[16:19]\nv_mfma_f32_16x16x16_bf16 v[20:23], a[50:51], v[6:7], v[20:23]\ns_waitcnt vmcnt(28)\n" \
"s_barrier\ns_add_u32 m0, %[lw], 12288\ns_add_u32 s44, %[wo], 2981888\nbuffer_load_dwordx4 v92, %[rw], s44 offen nt lds\ns_add_u32 m0, %[lw], 13312\ns_add_u32 s44, %[wo], 3039232\nbuffer_load_dwordx4 v92, %[rw], s44 offen nt lds\ns_add_u32 m0, %[lw], 14336\n" \
"s_add_u32 s44, %[wo], 3096576\nbuffer_load_dwordx4 v92, %[rw], s44 offen nt lds\ns_add_u32 m0, %[lw], 15360\ns_add_u32 s44, %[wo], 3153920\nbuffer_load_dwordx4 v92, %[rw], s44 offen nt lds\nds_read_b64_tr_b16 a[36:37], v94 offset:16384\nds_read_b64_tr_b16 a[38:39], v94 offset:16896\nds_read_b64_tr_b16 v[84:85], v94 offset:16392\n" \
"ds_read_b64_tr_b16 v[86:87], v94 offset:16904\nds_read_b64_tr_b16 a[44:45], v94 offset:40960\nds_read_b64_tr_b16 a[46:47], v94 offset:41472\nds_read_b64_tr_b16 a[52:53], v94 offset:40968\nds_read_b64_tr_b16 a[54:55], v94 offset:41480\ns_waitcnt lgkmcnt(0)\nv_mfma_f32_16x16x16_bf16 v[8:11], a[36:37], v[6:7], v[8:11]\nv_mfma_f32_16x16x16_bf16 v[12:15], v[84:85], v[6:7], v[12:15]\n" \
"v_mfma_f32_16x16x16_bf16 v[16:19], a[44:45], v[6:7], v[16:19]\nv_mfma_f32_16x16x16_bf16 v[20:23], a[52:53], v[6:7], v[20:23]\nv_mfma_f32_16x16x16_bf16 v[8:11], a[38:39], v[6:7], v[8:11]\nv_mfma_f32_16x16x16_bf16 v[12:15], v[86:87], v[6:7], v[12:15]\nv_mfma_f32_16x16x16_bf16 v[16:19], a[46:47], v[6:7], v[16:19]\nv_mfma_f32_16x16x16_bf16 v[20:23], a[54:55], v[6:7], v[20:23]\ns_waitcnt vmcnt(28)\ns_barrier\n" \
"s_add_u32 m0, %[lw], 16384\ns_add_u32 s44, %[wo], 3211264\nbuffer_load_dwordx4 v92, %[rw], s44 offen nt lds\ns_add_u32 m0, %[lw], 17408\ns_add_u32 s44, %[wo], 3268608\nbuffer_load_dwordx4 v92, %[rw], s44 offen nt lds\ns_add_u32 m0, %[lw], 18432\ns_add_u32 s44, %[wo], 3325952\n" \
"buffer_load_dwordx4 v92, %[rw], s44 offen nt lds\ns_add_u32 m0, %[lw], 19456\ns_add_u32 s44, %[wo], 3383296\nbuffer_load_dwordx4 v92, %[rw], s44 offen nt lds\nds_read_b64_tr_b16 a[56:57], v94 offset:20480\nds_read_b64_tr_b16 a[58:59], v94 offset:20992\nds_read_b64_tr_b16 a[64:65], v94 offset:20488\nds_read_b64_tr_b16 a[66:67], v94 offset:21000\n" \
"ds_read_b64_tr_b16 v[80:81], v94 offset:45056\nds_read_b64_tr_b16 v[82:83], v94 offset:45568\nds_read_b64_tr_b16 a[72:73], v94 offset:45064\nds_read_b64_tr_b16 a[74:75], v94 offset:45576\ns_waitcnt lgkmcnt(0)\nv_mfma_f32_16x16x16_bf16 v[8:11], a[56:57], v[6:7], v[8:11]\nv_mfma_f32_16x16x16_bf16 v[12:15], a[64:65], v[6:7], v[12:15]\nv_mfma_f32_16x16x16_bf16 v[16:19], v[80:81], v[6:7], v[16:19]\n" \
"v_mfma_f32_16x16x16_bf16 v[20:23], a[72:73], v[6:7], v[20:23]\nv_mfma_f32_16x16x16_bf16 v[8:11], a[58:59], v[6:7], v[8:11]\nv_mfma_f32_16x16x16_bf16 v[12:15], a[66:67], v[6:7], v[12:15]\nv_mfma_f32_16x16x16_bf16 v[16:19], v[82:83], v[6:7], v[16:19]\nv_mfma_f32_16x16x16_bf16 v[20:23], a[74:75], v[6:7], v[20:23]\ns_waitcnt vmcnt(28)\ns_barrier\ns_add_u32 m0, %[lw], 20480\n" \
"s_add_u32 s44, %[wo], 3440640\nbuffer_load_dwordx4 v92, %[rw], s44 offen nt lds\ns_add_u32 m0, %[lw], 21504\ns_add_u32 s44, %[wo], 3497984\nbuffer_load_dwordx4 v92, %[rw], s44 offen nt lds\ns_add_u32 m0, %[lw], 22528\ns_add_u32 s44, %[wo], 3555328\nbuffer_load_dwordx4 v92, %[rw], s44 offen nt lds\n" \
"s_add_u32 m0, %[lw], 23552\ns_add_u32 s44, %[wo], 3612672\nbuffer_load_dwordx4 v92, %[rw], s44 offen nt lds\nds_read_b64_tr_b16 a[60:61], v93 offset:0\nds_read_b64_tr_b16 a[62:63], v93 offset:512\nds_read_b64_tr_b16 a[68:69], v93 offset:8\nds_read_b64_tr_b16 a[70:71], v93 offset:520\nds_read_b64_tr_b16 v[76:77], v93 offset:12288\n" \
"ds_read_b64_tr_b16 v[78:79], v93 offset:12800\nds_read_b64_tr_b16 a[76:77], v93 offset:12296\nds_read_b64_tr_b16 a[78:79], v93 offset:12808\ns_waitcnt lgkmcnt(0)\nv_mfma_f32_16x16x16_bf16 v[8:11], a[60:61], v[6:7], v[8:11]\nv_mfma_f32_16x16x16_bf16 v[12:15], a[68:69], v[6:7], v[12:15]\nv_mfma_f32_16x16x16_bf16 v[16:19], v[76:77], v[6:7], v[16:19]\nv_mfma_f32_16x16x16_bf16 v[20:23], a[76:77], v[6:7], v[20:23]\n" \
"v_mfma_f32_16x16x16_bf16 v[8:11], a[62:63], v[6:7], v[8:11]\nv_mfma_f32_16x16x16_bf16 v[12:15], a[70:71], v[6:7], v[12:15]\nv_mfma_f32_16x16x16_bf16 v[16:19], v[78:79], v[6:7], v[16:19]\nv_mfma_f32_16x16x16_bf16 v[20:23], a[78:79], v[6:7], v[20:23]\ns_waitcnt vmcnt(28)\ns_barrier\ns_add_u32 m0, %[tb], 0\ns_add_u32 s44, %[wo], 4128768\n" \
"buffer_load_dwordx4 v92, %[rw], s44 offen nt lds\ns_add_u32 m0, %[tb], 1024\ns_add_u32 s44, %[wo], 4186112\nbuffer_load_dwordx4 v92, %[rw], s44 offen nt lds\ns_add_u32 m0, %[tb], 2048\ns_add_u32 s44, %[wo], 4243456\nbuffer_load_dwordx4 v92, %[rw], s44 offen nt lds\ns_add_u32 m0, %[tb], 3072\n" \
"s_add_u32 s44, %[wo], 4300800\nbuffer_load_dwordx4 v92, %[rw], s44 offen nt lds\nds_read_b64_tr_b16 a[80:81], v93 offset:4096\nds_read_b64_tr_b16 a[82:83], v93 offset:4608\nds_read_b64_tr_b16 a[88:89], v93 offset:4104\nds_read_b64_tr_b16 a[90:91], v93 offset:4616\nds_read_b64_tr_b16 a[96:97], v93 offset:16384\nds_read_b64_tr_b16 a[98:99], v93 offset:16896\n" \
"ds_read_b64_tr_b16 v[72:73], v93 offset:16392\nds_read_b64_tr_b16 v[74:75], v93 offset:16904\ns_waitcnt lgkmcnt(0)\nv_mfma_f32_16x16x16_bf16 v[8:11], a[80:81], v[6:7], v[8:11]\nv_mfma_f32_16x16x16_bf16 v[12:15], a[88:89], v[6:7], v[12:15]\nv_mfma_f32_16x16x16_bf16 v[16:19], a[96:97], v[6:7], v[16:19]\nv_mfma_f32_16x16x16_bf16 v[20:23], v[72:73], v[6:7], v[20:23]\nv_mfma_f32_16x16x16_bf16 v[8:11], a[82:83], v[6:7], v[8:11]\n" \
"v_mfma_f32_16x16x16_bf16 v[12:15], a[90:91], v[6:7], v[12:15]\nv_mfma_f32_16x16x16_bf16 v[16:19], a[98:99], v[6:7], v[16:19]\nv_mfma_f32_16x16x16_bf16 v[20:23], v[74:75], v[6:7], v[20:23]\ns_waitcnt vmcnt(28)\ns_barrier\ns_add_u32 m0, %[tb], 4096\ns_add_u32 s44, %[wo], 4358144\nbuffer_load_dwordx4 v92, %[rw], s44 offen nt lds\n" \
"s_add_u32 m0, %[tb], 5120\ns_add_u32 s44, %[wo], 4415488\nbuffer_load_dwordx4 v92, %[rw], s44 offen nt lds\ns_add_u32 m0, %[tb], 6144\ns_add_u32 s44, %[wo], 4472832\nbuffer_load_dwordx4 v92, %[rw], s44 offen nt lds\ns_add_u32 m0, %[tb], 7168\ns_add_u32 s44, %[wo], 4530176\n" \
"buffer_load_dwordx4 v92, %[rw], s44 offen nt lds\nds_read_b64_tr_b16 a[84:85], v93 offset:8192\nds_read_b64_tr_b16 a[86:87], v93 offset:8704\nds_read_b64_tr_b16 a[92:93], v93 offset:8200\nds_read_b64_tr_b16 a[94:95], v93 offset:8712\nds_read_b64_tr_b16 a[100:101], v93 offset:20480\nds_read_b64_tr_b16 a[102:103], v93 offset:20992\nds_read_b64_tr_b16 v[68:69], v93 offset:20488\n" \
"ds_read_b64_tr_b16 v[70:71], v93 offset:21000\ns_waitcnt lgkmcnt(0)\nv_mfma_f32_16x16x16_bf16 v[8:11], a[84:85], v[6:7], v[8:11]\nv_mfma_f32_16x16x16_bf16 v[12:15], a[92:93], v[6:7], v[12:15]\nv_mfma_f32_16x16x16_bf16 v[16:19], a[100:101], v[6:7], v[16:19]\nv_mfma_f32_16x16x16_bf16 v[20:23], v[68:69], v[6:7], v[20:23]\nv_mfma_f32_16x16x16_bf16 v[8:11], a[86:87], v[6:7], v[8:11]\nv_mfma_f32_16x16x16_bf16 v[12:15], a[94:95], v[6:7], v[12:15]\n" \
"v_mfma_f32_16x16x16_bf16 v[16:19], a[102:103], v[6:7], v[16:19]\nv_mfma_f32_16x16x16_bf16 v[20:23], v[70:71], v[6:7], v[20:23]\ns_waitcnt vmcnt(28)\ns_barrier\ns_add_u32 m0, %[tb], 8192\ns_add_u32 s44, %[wo], 4587520\nbuffer_load_dwordx4 v92, %[rw], s44 offen nt lds\ns_add_u32 m0, %[tb], 9216\n" \
"s_add_u32 s44, %[wo], 4644864\nbuffer_load_dwordx4 v92, %[rw], s44 offen nt lds\ns_add_u32 m0, %[tb], 10240\ns_add_u32 s44, %[wo], 4702208\nbuffer_load_dwordx4 v92, %[rw], s44 offen nt lds\ns_add_u32 m0, %[tb], 11264\ns_add_u32 s44, %[wo], 4759552\nbuffer_load_dwordx4 v92, %[rw], s44 offen nt lds\n" \
"ds_read_b64_tr_b16 a[136:137], v94 offset:0\nds_read_b64_tr_b16 a[138:139], v94 offset:512\nds_read_b64_tr_b16 v[64:65], v94 offset:8\nds_read_b64_tr_b16 v[66:67], v94 offset:520\nds_read_b64_tr_b16 a[144:145], v94 offset:24576\nds_read_b64_tr_b16 a[146:147], v94 offset:25088\nds_read_b64_tr_b16 a[152:153], v94 offset:24584\nds_read_b64_tr_b16 a[154:155], v94 offset:25096\n" \
"s_waitcnt lgkmcnt(0)\nv_mfma_f32_16x16x16_bf16 v[8:11], a[136:137], v[6:7], v[8:11]\nv_mfma_f32_16x16x16_bf16 v[12:15], v[64:65], v[6:7], v[12:15]\nv_mfma_f32_16x16x16_bf16 v[16:19], a[144:145], v[6:7], v[16:19]\nv_mfma_f32_16x16x16_bf16 v[20:23], a[152:153], v[6:7], v[20:23]\nv_mfma_f32_16x16x16_bf16 v[8:11], a[138:139], v[6:7], v[8:11]\nv_mfma_f32_16x16x16_bf16 v[12:15], v[66:67], v[6:7], v[12:15]\nv_mfma_f32_16x16x16_bf16 v[16:19], a[146:147], v[6:7], v[16:19]\n" \
"v_mfma_f32_16x16x16_bf16 v[20:23], a[154:155], v[6:7], v[20:23]\ns_waitcnt vmcnt(28)\ns_barrier\ns_add_u32 m0, %[lw], 0\ns_add_u32 s44, %[wo], 4816896\nbuffer_load_dwordx4 v92, %[rw], s44 offen nt lds\ns_add_u32 m0, %[lw], 1024\ns_add_u32 s44, %[wo], 4874240\n" \
"buffer_load_dwordx4 v92, %[rw], s44 offen nt lds\ns_add_u32 m0, %[lw], 2048\ns_add_u32 s44, %[wo], 4931584\nbuffer_load_dwordx4 v92, %[rw], s44 offen nt lds\ns_add_u32 m0, %[lw], 3072\ns_add_u32 s44, %[wo], 4988928\nbuffer_load_dwordx4 v92, %[rw], s44 offen nt lds\nds_read_b64_tr_b16 a[140:141], v94 offset:4096\n" \
"ds_read_b64_tr_b16 a[142:143], v94 offset:4608\nds_read_b64_tr_b16 v[60:61], v94 offset:4104\nds_read_b64_tr_b16 v[62:63], v94 offset:4616\nds_read_b64_tr_b16 a[148:149], v94 offset:28672\nds_read_b64_tr_b16 a[150:151], v94 offset:29184\nds_read_b64_tr_b16 a[156:157], v94 offset:28680\nds_read_b64_tr_b16 a[158:159], v94 offset:29192\ns_waitcnt lgkmcnt(0)\n" \
"v_mfma_f32_16x16x16_bf16 v[8:11], a[140:141], v[6:7], v[8:11]\nv_mfma_f32_16x16x16_bf16 v[12:15], v[60:61], v[6:7], v[12:15]\nv_mfma_f32_16x16x16_bf16 v[16:19], a[148:149], v[6:7], v[16:19]\nv_mfma_f32_16x16x16_bf16 v[20:23], a[156:157], v[6:7], v[20:23]\nv_mfma_f32_16x16x16_bf16 v[8:11], a[142:143], v[6:7], v[8:11]\nv_mfma_f32_16x16x16_bf16 v[12:15], v[62:63], v[6:7], v[12:15]\nv_mfma_f32_16x16x16_bf16 v[16:19], a[150:151], v[6:7], v[16:19]\nv_mfma_f32_16x16x16_bf16 v[20:23], a[158:159], v[6:7], v[20:23]\n" \
"s_waitcnt vmcnt(28)\ns_barrier\ns_add_u32 m0, %[lw], 4096\ns_add_u32 s44, %[wo], 5046272\nbuffer_load_dwordx4 v92, %[rw], s44 offen nt lds\ns_add_u32 m0, %[lw], 5120\ns_add_u32 s44, %[wo], 5103616\nbuffer_load_dwordx4 v92, %[rw], s44 offen nt lds\n" \
"s_add_u32 m0, %[lw], 6144\ns_add_u32 s44, %[wo], 5160960\nbuffer_load_dwordx4 v92, %[rw], s44 offen nt lds\ns_add_u32 m0, %[lw], 7168\ns_add_u32 s44, %[wo], 5218304\nbuffer_load_dwordx4 v92, %[rw], s44 offen nt lds\nds_read_b64_tr_b16 a[160:161], v94 offset:8192\nds_read_b64_tr_b16 a[162:163], v94 offset:8704\n" \
"ds_read_b64_tr_b16 a[168:169], v94 offset:8200\nds_read_b64_tr_b16 a[170:171], v94 offset:8712\nds_read_b64_tr_b16 v[56:57], v94 offset:32768\nds_read_b64_tr_b16 v[58:59], v94 offset:33280\nds_read_b64_tr_b16 a[176:177], v94 offset:32776\nds_read_b64_tr_b16 a[178:179], v94 offset:33288\ns_waitcnt lgkmcnt(0)\nv_mfma_f32_16x16x16_bf16 v[8:11], a[160:161], v[6:7], v[8:11]\n" \
"v_mfma_f32_16x16x16_bf16 v[12:15], a[168:169], v[6:7], v[12:15]\nv_mfma_f32_16x16x16_bf16 v[16:19], v[56:57], v[6:7], v[16:19]\nv_mfma_f32_16x16x16_bf16 v[20:23], a[176:177], v[6:7], v[20:23]\nv_mfma_f32_16x16x16_bf16 v[8:11], a[162:163], v[6:7], v[8:11]\nv_mfma_f32_16x16x16_bf16 v[12:15], a[170:171], v[6:7], v[12:15]\nv_mfma_f32_16x16x16_bf16 v[16:19], v[58:59], v[6:7], v[16:19]\nv_mfma_f32_16x16x16_bf16 v[20:23], a[178:179], v[6:7], v[20:23]\ns_waitcnt vmcnt(28)\n" \
"s_barrier\ns_add_u32 m0, %[lw], 8192\ns_add_u32 s44, %[wo], 5275648\nbuffer_load_dwordx4 v92, %[rw], s44 offen nt lds\ns_add_u32 m0, %[lw], 9216\ns_add_u32 s44, %[wo], 5332992\nbuffer_load_dwordx4 v92, %[rw], s44 offen nt lds\ns_add_u32 m0, %[lw], 10240\n" \
"s_add_u32 s44, %[wo], 5390336\nbuffer_load_dwordx4 v92, %[rw], s44 offen nt lds\ns_add_u32 m0, %[lw], 11264\ns_add_u32 s44, %[wo], 5447680\nbuffer_load_dwordx4 v92, %[rw], s44 offen nt lds\nds_read_b64_tr_b16 a[164:165], v94 offset:12288\nds_read_b64_tr_b16 a[166:167], v94 offset:12800\nds_read_b64_tr_b16 a[172:173], v94 offset:12296\n" \
"ds_read_b64_tr_b16 a[174:175], v94 offset:12808\nds_read_b64_tr_b16 v[52:53], v94 offset:36864\nds_read_b64_tr_b16 v[54:55], v94 offset:37376\nds_read_b64_tr_b16 a[180:181], v94 offset:36872\nds_read_b64_tr_b16 a[182:183], v94 offset:37384\ns_waitcnt lgkmcnt(0)\nv_mfma_f32_16x16x16_bf16 v[8:11], a[164:165], v[6:7], v[8:11]\nv_mfma_f32_16x16x16_bf16 v[12:15], a[172:173], v[6:7], v[12:15]\n" \
"v_mfma_f32_16x16x16_bf16 v[16:19], v[52:53], v[6:7], v[16:19]\nv_mfma_f32_16x16x16_bf16 v[20:23], a[180:181], v[6:7], v[20:23]\nv_mfma_f32_16x16x16_bf16 v[8:11], a[166:167], v[6:7], v[8:11]\nv_mfma_f32_16x16x16_bf16 v[12:15], a[174:175], v[6:7], v[12:15]\nv_mfma_f32_16x16x16_bf16 v[16:19], v[54:55], v[6:7], v[16:19]\nv_mfma_f32_16x16x16_bf16 v[20:23], a[182:183], v[6:7], v[20:23]\ns_waitcnt vmcnt(28)\ns_barrier\n" \
"s_add_u32 m0, %[lw], 12288\ns_add_u32 s44, %[wo], 5963776\nbuffer_load_dwordx4 v92, %[rw], s44 offen nt lds\ns_add_u32 m0, %[lw], 13312\ns_add_u32 s44, %[wo], 6021120\nbuffer_load_dwordx4 v92, %[rw], s44 offen nt lds\ns_add_u32 m0, %[lw], 14336\ns_add_u32 s44, %[wo], 6078464\n" \
"buffer_load_dwordx4 v92, %[rw], s44 offen nt lds\ns_add_u32 m0, %[lw], 15360\ns_add_u32 s44, %[wo], 6135808\nbuffer_load_dwordx4 v92, %[rw], s44 offen nt lds\nds_read_b64_tr_b16 a[184:185], v94 offset:16384\nds_read_b64_tr_b16 a[186:187], v94 offset:16896\nds_read_b64_tr_b16 a[192:193], v94 offset:16392\nds_read_b64_tr_b16 a[194:195], v94 offset:16904\n" \
"ds_read_b64_tr_b16 a[200:201], v94 offset:40960\nds_read_b64_tr_b16 a[202:203], v94 offset:41472\nds_read_b64_tr_b16 v[48:49], v94 offset:40968\nds_read_b64_tr_b16 v[50:51], v94 offset:41480\ns_waitcnt lgkmcnt(0)\nv_mfma_f32_16x16x16_bf16 v[8:11], a[184:185], v[6:7], v[8:11]\nv_mfma_f32_16x16x16_bf16 v[12:15], a[192:193], v[6:7], v[12:15]\nv_mfma_f32_16x16x16_bf16 v[16:19], a[200:201], v[6:7], v[16:19]\n" \
"v_mfma_f32_16x16x16_bf16 v[20:23], v[48:49], v[6:7], v[20:23]\nv_mfma_f32_16x16x16_bf16 v[8:11], a[186:187], v[6:7], v[8:11]\nv_mfma_f32_16x16x16_bf16 v[12:15], a[194:195], v[6:7], v[12:15]\nv_mfma_f32_16x16x16_bf16 v[16:19], a[202:203], v[6:7], v[16:19]\nv_mfma_f32_16x16x16_bf16 v[20:23], v[50:51], v[6:7], v[20:23]\ns_waitcnt vmcnt(28)\ns_barrier\ns_add_u32 m0, %[lw], 16384\n" \
"s_add_u32 s44, %[wo], 6193152\nbuffer_load_dwordx4 v92, %[rw], s44 offen nt lds\ns_add_u32 m0, %[lw], 17408\ns_add_u32 s44, %[wo], 6250496\nbuffer_load_dwordx4 v92, %[rw], s44 offen nt lds\ns_add_u32 m0, %[lw], 18432\ns_add_u32 s44, %[wo], 6307840\nbuffer_load_dwordx4 v92, %[rw], s44 offen nt lds\n" \
"s_add_u32 m0, %[lw], 19456\ns_add_u32 s44, %[wo], 6365184\nbuffer_load_dwordx4 v92, %[rw], s44 offen nt lds\nds_read_b64_tr_b16 a[188:189], v94 offset:20480\nds_read_b64_tr_b16 a[190:191], v94 offset:20992\nds_read_b64_tr_b16 a[196:197], v94 offset:20488\nds_read_b64_tr_b16 a[198:199], v94 offset:21000\nds_read_b64_tr_b16 a[204:205], v94 offset:45056\n" \
"ds_read_b64_tr_b16 a[206:207], v94 offset:45568\nds_read_b64_tr_b16 v[44:45], v94 offset:45064\nds_read_b64_tr_b16 v[46:47], v94 offset:45576\ns_waitcnt lgkmcnt(0)\nv_mfma_f32_16x16x16_bf16 v[8:11], a[188:189], v[6:7], v[8:11]\nv_mfma_f32_16x16x16_bf16 v[12:15], a[196:197], v[6:7], v[12:15]\nv_mfma_f32_16x16x16_bf16 v[16:19], a[204:205], v[6:7], v[16:19]\nv_mfma_f32_16x16x16_bf16 v[20:23], v[44:45], v[6:7], v[20:23]\n" \
"v_mfma_f32_16x16x16_bf16 v[8:11], a[190:191], v[6:7], v[8:11]\nv_mfma_f32_16x16x16_bf16 v[12:15], a[198:199], v[6:7], v[12:15]\nv_mfma_f32_16x16x16_bf16 v[16:19], a[206:207], v[6:7], v[16:19]\nv_mfma_f32_16x16x16_bf16 v[20:23], v[46:47], v[6:7], v[20:23]\ns_waitcnt vmcnt(28)\ns_barrier\ns_add_u32 m0, %[lw], 20480\ns_add_u32 s44, %[wo], 6422528\n" \
"buffer_load_dwordx4 v92, %[rw], s44 offen nt lds\ns_add_u32 m0, %[lw], 21504\ns_add_u32 s44, %[wo], 6479872\nbuffer_load_dwordx4 v92, %[rw], s44 offen nt lds\ns_add_u32 m0, %[lw], 22528\ns_add_u32 s44, %[wo], 6537216\nbuffer_load_dwordx4 v92, %[rw], s44 offen nt lds\ns_add_u32 m0, %[lw], 23552\n" \
"s_add_u32 s44, %[wo], 6594560\nbuffer_load_dwordx4 v92, %[rw], s44 offen nt lds\nds_read_b64_tr_b16 a[240:241], v93 offset:0\nds_read_b64_tr_b16 a[242:243], v93 offset:512\nds_read_b64_tr_b16 v[40:41], v93 offset:8\nds_read_b64_tr_b16 v[42:43], v93 offset:520\nds_read_b64_tr_b16 a[248:249], v93 offset:12288\nds_read_b64_tr_b16 a[250:251], v93 offset:12800\n" \
"ds_read_b64_tr_b16 v[96:97], v93 offset:12296\nds_read_b64_tr_b16 v[98:99], v93 offset:12808\ns_waitcnt lgkmcnt(0)\nv_mfma_f32_16x16x16_bf16 v[8:11], a[240:241], v[6:7], v[8:11]\nv_mfma_f32_16x16x16_bf16 v[12:15], v[40:41], v[6:7], v[12:15]\nv_mfma_f32_16x16x16_bf16 v[16:19], a[248:249], v[6:7], v[16:19]\nv_mfma_f32_16x16x16_bf16 v[20:23], v[96:97], v[6:7], v[20:23]\nv_mfma_f32_16x16x16_bf16 v[8:11], a[242:243], v[6:7], v[8:11]\n" \
"v_mfma_f32_16x16x16_bf16 v[12:15], v[42:43], v[6:7], v[12:15]\nv_mfma_f32_16x16x16_bf16 v[16:19], a[250:251], v[6:7], v[16:19]\nv_mfma_f32_16x16x16_bf16 v[20:23], v[98:99], v[6:7], v[20:23]\ns_waitcnt vmcnt(28)\ns_barrier\ns_add_u32 m0, %[tb], 0\ns_add_u32 s44, %[wo], 6651904\nbuffer_load_dwordx4 v92, %[rw], s44 offen nt lds\n" \
"s_add_u32 m0, %[tb], 1024\ns_add_u32 s44, %[wo], 6709248\nbuffer_load_dwordx4 v92, %[rw], s44 offen nt lds\ns_add_u32 m0, %[tb], 2048\ns_add_u32 s44, %[wo], 6766592\nbuffer_load_dwordx4 v92, %[rw], s44 offen nt lds\ns_add_u32 m0, %[tb], 3072\ns_add_u32 s44, %[wo], 6823936\n" \
"buffer_load_dwordx4 v92, %[rw], s44 offen nt lds\nds_read_b64_tr_b16 a[244:245], v93 offset:4096\nds_read_b64_tr_b16 a[246:247], v93 offset:4608\nds_read_b64_tr_b16 v[36:37], v93 offset:4104\nds_read_b64_tr_b16 v[38:39], v93 offset:4616\nds_read_b64_tr_b16 a[252:253], v93 offset:16384\nds_read_b64_tr_b16 a[254:255], v93 offset:16896\nds_read_b64_tr_b16 v[100:101], v93 offset:16392\n" \
"ds_read_b64_tr_b16 v[102:103], v93 offset:16904\ns_waitcnt lgkmcnt(0)\nv_mfma_f32_16x16x16_bf16 v[8:11], a[244:245], v[6:7], v[8:11]\nv_mfma_f32_16x16x16_bf16 v[12:15], v[36:37], v[6:7], v[12:15]\nv_mfma_f32_16x16x16_bf16 v[16:19], a[252:253], v[6:7], v[16:19]\nv_mfma_f32_16x16x16_bf16 v[20:23], v[100:101], v[6:7], v[20:23]\nv_mfma_f32_16x16x16_bf16 v[8:11], a[246:247], v[6:7], v[8:11]\nv_mfma_f32_16x16x16_bf16 v[12:15], v[38:39], v[6:7], v[12:15]\n" \
"v_mfma_f32_16x16x16_bf16 v[16:19], a[254:255], v[6:7], v[16:19]\nv_mfma_f32_16x16x16_bf16 v[20:23], v[102:103], v[6:7], v[20:23]\ns_waitcnt vmcnt(28)\ns_barrier\ns_add_u32 m0, %[tb], 4096\ns_add_u32 s44, %[wo], 6881280\nbuffer_load_dwordx4 v92, %[rw], s44 offen nt lds\ns_add_u32 m0, %[tb], 5120\n" \
"s_add_u32 s44, %[wo], 6938624\nbuffer_load_dwordx4 v92, %[rw], s44 offen nt lds\ns_add_u32 m0, %[tb], 6144\ns_add_u32 s44, %[wo], 6995968\nbuffer_load_dwordx4 v92, %[rw], s44 offen nt lds\ns_add_u32 m0, %[tb], 7168\ns_add_u32 s44, %[wo], 7053312\nbuffer_load_dwordx4 v92, %[rw], s44 offen nt lds\n" \
"ds_read_b64_tr_b16 v[104:105], v93 offset:8192\nds_read_b64_tr_b16 v[106:107], v93 offset:8704\nds_read_b64_tr_b16 v[112:113], v93 offset:8200\nds_read_b64_tr_b16 v[114:115], v93 offset:8712\nds_read_b64_tr_b16 v[32:33], v93 offset:20480\nds_read_b64_tr_b16 v[34:35], v93 offset:20992\nds_read_b64_tr_b16 v[120:121], v93 offset:20488\nds_read_b64_tr_b16 v[122:123], v93 offset:21000\n" \
"s_waitcnt lgkmcnt(0)\nv_mfma_f32_16x16x16_bf16 v[8:11], v[104:105], v[6:7], v[8:11]\nv_mfma_f32_16x16x16_bf16 v[12:15], v[112:113], v[6:7], v[12:15]\nv_mfma_f32_16x16x16_bf16 v[16:19], v[32:33], v[6:7], v[16:19]\nv_mfma_f32_16x16x16_bf16 v[20:23], v[120:121], v[6:7], v[20:23]\nv_mfma_f32_16x16x16_bf16 v[8:11], v[106:107], v[6:7], v[8:11]\nv_mfma_f32_16x16x16_bf16 v[12:15], v[114:115], v[6:7], v[12:15]\nv_mfma_f32_16x16x16_bf16 v[16:19], v[34:35], v[6:7], v[16:19]\n" \
"v_mfma_f32_16x16x16_bf16 v[20:23], v[122:123], v[6:7], v[20:23]\ns_waitcnt vmcnt(28)\ns_barrier\ns_add_u32 m0, %[tb], 8192\ns_add_u32 s44, %[wo], 7110656\nbuffer_load_dwordx4 v92, %[rw], s44 offen nt lds\ns_add_u32 m0, %[tb], 9216\ns_add_u32 s44, %[wo], 7168000\n" \
"buffer_load_dwordx4 v92, %[rw], s44 offen nt lds\ns_add_u32 m0, %[tb], 10240\ns_add_u32 s44, %[wo], 7225344\nbuffer_load_dwordx4 v92, %[rw], s44 offen nt lds\ns_add_u32 m0, %[tb], 11264\ns_add_u32 s44, %[wo], 7282688\nbuffer_load_dwordx4 v92, %[rw], s44 offen nt lds\nds_read_b64_tr_b16 v[108:109], v94 offset:0\n" \
"ds_read_b64_tr_b16 v[110:111], v94 offset:512\nds_read_b64_tr_b16 v[116:117], v94 offset:8\nds_read_b64_tr_b16 v[118:119], v94 offset:520\nds_read_b64_tr_b16 v[28:29], v94 offset:24576\nds_read_b64_tr_b16 v[30:31], v94 offset:25088\nds_read_b64_tr_b16 v[124:125], v94 offset:24584\nds_read_b64_tr_b16 v[126:127], v94 offset:25096\ns_waitcnt lgkmcnt(0)\n" \
"v_mfma_f32_16x16x16_bf16 v[8:11], v[108:109], v[6:7], v[8:11]\nv_mfma_f32_16x16x16_bf16 v[12:15], v[116:117], v[6:7], v[12:15]\nv_mfma_f32_16x16x16_bf16 v[16:19], v[28:29], v[6:7], v[16:19]\nv_mfma_f32_16x16x16_bf16 v[20:23], v[124:125], v[6:7], v[20:23]\nv_mfma_f32_16x16x16_bf16 v[8:11], v[110:111], v[6:7], v[8:11]\nv_mfma_f32_16x16x16_bf16 v[12:15], v[118:119], v[6:7], v[12:15]\nv_mfma_f32_16x16x16_bf16 v[16:19], v[30:31], v[6:7], v[16:19]\nv_mfma_f32_16x16x16_bf16 v[20:23], v[126:127], v[6:7], v[20:23]\n" \
"s_waitcnt vmcnt(28)\ns_barrier\nds_read_b64_tr_b16 v[128:129], v94 offset:4096\nds_read_b64_tr_b16 v[130:131], v94 offset:4608\nds_read_b64_tr_b16 v[136:137], v94 offset:4104\nds_read_b64_tr_b16 v[138:139], v94 offset:4616\nds_read_b64_tr_b16 v[144:145], v94 offset:28672\nds_read_b64_tr_b16 v[146:147], v94 offset:29184\n" \
"ds_read_b64_tr_b16 v[24:25], v94 offset:28680\nds_read_b64_tr_b16 v[26:27], v94 offset:29192\ns_waitcnt lgkmcnt(0)\nv_mfma_f32_16x16x16_bf16 v[8:11], v[128:129], v[6:7], v[8:11]\nv_mfma_f32_16x16x16_bf16 v[12:15], v[136:137], v[6:7], v[12:15]\nv_mfma_f32_16x16x16_bf16 v[16:19], v[144:145], v[6:7], v[16:19]\nv_mfma_f32_16x16x16_bf16 v[20:23], v[24:25], v[6:7], v[20:23]\nv_mfma_f32_16x16x16_bf16 v[8:11], v[130:131], v[6:7], v[8:11]\n" \
"v_mfma_f32_16x16x16_bf16 v[12:15], v[138:139], v[6:7], v[12:15]\nv_mfma_f32_16x16x16_bf16 v[16:19], v[146:147], v[6:7], v[16:19]\nv_mfma_f32_16x16x16_bf16 v[20:23], v[26:27], v[6:7], v[20:23]\ns_waitcnt vmcnt(24)\nds_write_b128 v1, v[88:91] offset:0\nds_write_b128 v1, v[84:87] offset:1024\nds_write_b128 v1, v[80:83] offset:2048\nds_write_b128 v1, v[76:79] offset:3072\n" \
"s_barrier\nds_read_b64_tr_b16 v[132:133], v94 offset:8192\nds_read_b64_tr_b16 v[134:135], v94 offset:8704\nds_read_b64_tr_b16 v[140:141], v94 offset:8200\nds_read_b64_tr_b16 v[142:143], v94 offset:8712\nds_read_b64_tr_b16 v[148:149], v94 offset:32768\nds_read_b64_tr_b16 v[150:151], v94 offset:33280\nds_read_b64_tr_b16 v[76:77], v94 offset:32776\n" \
"ds_read_b64_tr_b16 v[78:79], v94 offset:33288\ns_waitcnt lgkmcnt(0)\nv_mfma_f32_16x16x16_bf16 v[8:11], v[132:133], v[6:7], v[8:11]\nv_mfma_f32_16x16x16_bf16 v[12:15], v[140:141], v[6:7], v[12:15]\nv_mfma_f32_16x16x16_bf16 v[16:19], v[148:149], v[6:7], v[16:19]\nv_mfma_f32_16x16x16_bf16 v[20:23], v[76:77], v[6:7], v[20:23]\nv_mfma_f32_16x16x16_bf16 v[8:11], v[134:135], v[6:7], v[8:11]\nv_mfma_f32_16x16x16_bf16 v[12:15], v[142:143], v[6:7], v[12:15]\n" \
"v_mfma_f32_16x16x16_bf16 v[16:19], v[150:151], v[6:7], v[16:19]\nv_mfma_f32_16x16x16_bf16 v[20:23], v[78:79], v[6:7], v[20:23]\ns_waitcnt vmcnt(20)\nds_write_b128 v1, v[72:75] offset:4096\nds_write_b128 v1, v[68:71] offset:5120\nds_write_b128 v1, v[64:67] offset:6144\nds_write_b128 v1, v[60:63] offset:7168\ns_barrier\n" \
"ds_read_b64_tr_b16 v[184:185], v94 offset:12288\nds_read_b64_tr_b16 v[186:187], v94 offset:12800\nds_read_b64_tr_b16 v[60:61], v94 offset:12296\nds_read_b64_tr_b16 v[62:63], v94 offset:12808\nds_read_b64_tr_b16 v[192:193], v94 offset:36864\nds_read_b64_tr_b16 v[194:195], v94 offset:37376\nds_read_b64_tr_b16 v[200:201], v94 offset:36872\nds_read_b64_tr_b16 v[202:203], v94 offset:37384\n" \
"s_waitcnt lgkmcnt(0)\nv_mfma_f32_16x16x16_bf16 v[8:11], v[184:185], v[6:7], v[8:11]\nv_mfma_f32_16x16x16_bf16 v[12:15], v[60:61], v[6:7], v[12:15]\nv_mfma_f32_16x16x16_bf16 v[16:19], v[192:193], v[6:7], v[16:19]\nv_mfma_f32_16x16x16_bf16 v[20:23], v[200:201], v[6:7], v[20:23]\nv_mfma_f32_16x16x16_bf16 v[8:11], v[186:187], v[6:7], v[8:11]\nv_mfma_f32_16x16x16_bf16 v[12:15], v[62:63], v[6:7], v[12:15]\nv_mfma_f32_16x16x16_bf16 v[16:19], v[194:195], v[6:7], v[16:19]\n" \
"v_mfma_f32_16x16x16_bf16 v[20:23], v[202:203], v[6:7], v[20:23]\ns_waitcnt vmcnt(16)\nds_write_b128 v1, v[56:59] offset:8192\nds_write_b128 v1, v[52:55] offset:9216\nds_write_b128 v1, v[48:51] offset:10240\nds_write_b128 v1, v[44:47] offset:11264\ns_barrier\nds_read_b64_tr_b16 v[188:189], v94 offset:16384\n" \
"ds_read_b64_tr_b16 v[190:191], v94 offset:16896\nds_read_b64_tr_b16 v[44:45], v94 offset:16392\nds_read_b64_tr_b16 v[46:47], v94 offset:16904\nds_read_b64_tr_b16 v[196:197], v94 offset:40960\nds_read_b64_tr_b16 v[198:199], v94 offset:41472\nds_read_b64_tr_b16 v[204:205], v94 offset:40968\nds_read_b64_tr_b16 v[206:207], v94 offset:41480\ns_waitcnt lgkmcnt(0)\n" \
"v_mfma_f32_16x16x16_bf16 v[8:11], v[188:189], v[6:7], v[8:11]\nv_mfma_f32_16x16x16_bf16 v[12:15], v[44:45], v[6:7], v[12:15]\nv_mfma_f32_16x16x16_bf16 v[16:19], v[196:197], v[6:7], v[16:19]\nv_mfma_f32_16x16x16_bf16 v[20:23], v[204:205], v[6:7], v[20:23]\nv_mfma_f32_16x16x16_bf16 v[8:11], v[190:191], v[6:7], v[8:11]\nv_mfma_f32_16x16x16_bf16 v[12:15], v[46:47], v[6:7], v[12:15]\nv_mfma_f32_16x16x16_bf16 v[16:19], v[198:199], v[6:7], v[16:19]\nv_mfma_f32_16x16x16_bf16 v[20:23], v[206:207], v[6:7], v[20:23]\n" \
"s_waitcnt vmcnt(12)\nds_write_b128 v1, v[40:43] offset:12288\nds_write_b128 v1, v[36:39] offset:13312\nds_write_b128 v1, v[32:35] offset:14336\nds_write_b128 v1, v[28:31] offset:15360\ns_barrier\nds_read_b64_tr_b16 v[208:209], v94 offset:20480\nds_read_b64_tr_b16 v[210:211], v94 offset:20992\n" \
"ds_read_b64_tr_b16 v[216:217], v94 offset:20488\nds_read_b64_tr_b16 v[218:219], v94 offset:21000\nds_read_b64_tr_b16 v[28:29], v94 offset:45056\nds_read_b64_tr_b16 v[30:31], v94 offset:45568\nds_read_b64_tr_b16 v[224:225], v94 offset:45064\nds_read_b64_tr_b16 v[226:227], v94 offset:45576\ns_waitcnt lgkmcnt(0)\nv_mfma_f32_16x16x16_bf16 v[8:11], v[208:209], v[6:7], v[8:11]\n" \
"v_mfma_f32_16x16x16_bf16 v[12:15], v[216:217], v[6:7], v[12:15]\nv_mfma_f32_16x16x16_bf16 v[16:19], v[28:29], v[6:7], v[16:19]\nv_mfma_f32_16x16x16_bf16 v[20:23], v[224:225], v[6:7], v[20:23]\nv_mfma_f32_16x16x16_bf16 v[8:11], v[210:211], v[6:7], v[8:11]\nv_mfma_f32_16x16x16_bf16 v[12:15], v[218:219], v[6:7], v[12:15]\nv_mfma_f32_16x16x16_bf16 v[16:19], v[30:31], v[6:7], v[16:19]\nv_mfma_f32_16x16x16_bf16 v[20:23], v[226:227], v[6:7], v[20:23]\ns_waitcnt vmcnt(8)\n" \
"ds_write_b128 v1, v[24:27] offset:16384\nds_write_b128 v1, v[76:79] offset:17408\nds_write_b128 v1, v[60:63] offset:18432\nds_write_b128 v1, v[44:47] offset:19456\ns_barrier\nds_read_b64_tr_b16 v[212:213], v93 offset:0\nds_read_b64_tr_b16 v[214:215], v93 offset:512\nds_read_b64_tr_b16 v[220:221], v93 offset:8\n" \
"ds_read_b64_tr_b16 v[222:223], v93 offset:520\nds_read_b64_tr_b16 v[44:45], v93 offset:12288\nds_read_b64_tr_b16 v[46:47], v93 offset:12800\nds_read_b64_tr_b16 v[228:229], v93 offset:12296\nds_read_b64_tr_b16 v[230:231], v93 offset:12808\ns_waitcnt lgkmcnt(0)\nv_mfma_f32_16x16x16_bf16 v[8:11], v[212:213], v[6:7], v[8:11]\nv_mfma_f32_16x16x16_bf16 v[12:15], v[220:221], v[6:7], v[12:15]\n" \
"v_mfma_f32_16x16x16_bf16 v[16:19], v[44:45], v[6:7], v[16:19]\nv_mfma_f32_16x16x16_bf16 v[20:23], v[228:229], v[6:7], v[20:23]\nv_mfma_f32_16x16x16_bf16 v[8:11], v[214:215], v[6:7], v[8:11]\nv_mfma_f32_16x16x16_bf16 v[12:15], v[222:223], v[6:7], v[12:15]\nv_mfma_f32_16x16x16_bf16 v[16:19], v[46:47], v[6:7], v[16:19]\nv_mfma_f32_16x16x16_bf16 v[20:23], v[230:231], v[6:7], v[20:23]\ns_waitcnt vmcnt(4)\nds_write_b128 v1, v[28:31] offset:20480\n" \
"ds_write_b128 v1, v[44:47] offset:21504\ns_barrier\nds_read_b64_tr_b16 v[232:233], v93 offset:4096\nds_read_b64_tr_b16 v[234:235], v93 offset:4608\nds_read_b64_tr_b16 v[240:241], v93 offset:4104\nds_read_b64_tr_b16 v[242:243], v93 offset:4616\nds_read_b64_tr_b16 v[248:249], v93 offset:16384\nds_read_b64_tr_b16 v[250:251], v93 offset:16896\n" \
"ds_read_b64_tr_b16 v[44:45], v93 offset:16392\nds_read_b64_tr_b16 v[46:47], v93 offset:16904\ns_waitcnt lgkmcnt(0)\nv_mfma_f32_16x16x16_bf16 v[8:11], v[232:233], v[6:7], v[8:11]\nv_mfma_f32_16x16x16_bf16 v[12:15], v[240:241], v[6:7], v[12:15]\nv_mfma_f32_16x16x16_bf16 v[16:19], v[248:249], v[6:7], v[16:19]\nv_mfma_f32_16x16x16_bf16 v[20:23], v[44:45], v[6:7], v[20:23]\nv_mfma_f32_16x16x16_bf16 v[8:11], v[234:235], v[6:7], v[8:11]\n" \
"v_mfma_f32_16x16x16_bf16 v[12:15], v[242:243], v[6:7], v[12:15]\nv_mfma_f32_16x16x16_bf16 v[16:19], v[250:251], v[6:7], v[16:19]\nv_mfma_f32_16x16x16_bf16 v[20:23], v[46:47], v[6:7], v[20:23]\ns_waitcnt vmcnt(0)\nds_write_b128 v1, v[44:47] offset:22528\ns_barrier\nds_read_b64_tr_b16 v[236:237], v93 offset:8192\nds_read_b64_tr_b16 v[238:239], v93 offset:8704\n" \
"ds_read_b64_tr_b16 v[244:245], v93 offset:8200\nds_read_b64_tr_b16 v[246:247], v93 offset:8712\nds_read_b64_tr_b16 v[252:253], v93 offset:20480\nds_read_b64_tr_b16 v[254:255], v93 offset:20992\nds_read_b64_tr_b16 v[44:45], v93 offset:20488\nds_read_b64_tr_b16 v[46:47], v93 offset:21000\ns_waitcnt lgkmcnt(0)\nv_mfma_f32_16x16x16_bf16 v[8:11], v[236:237], v[6:7], v[8:11]\n" \
"v_mfma_f32_16x16x16_bf16 v[12:15], v[244:245], v[6:7], v[12:15]\nv_mfma_f32_16x16x16_bf16 v[16:19], v[252:253], v[6:7], v[16:19]\nv_mfma_f32_16x16x16_bf16 v[20:23], v[44:45], v[6:7], v[20:23]\nv_mfma_f32_16x16x16_bf16 v[8:11], v[238:239], v[6:7], v[8:11]\nv_mfma_f32_16x16x16_bf16 v[12:15], v[246:247], v[6:7], v[12:15]\nv_mfma_f32_16x16x16_bf16 v[16:19], v[254:255], v[6:7], v[16:19]\nv_mfma_f32_16x16x16_bf16 v[20:23], v[46:47], v[6:7], v[20:23]\nds_write_b128 v1, v[44:47] offset:23552\n" \
"s_waitcnt lgkmcnt(0)\ns_barrier\ns_mov_b32 s86, %[xn0]\ns_mov_b32 s87, %[ho0]\ns_mov_b32 s88, %[fh0]\ns_mov_b32 s89, %[b1]\ns_mov_b32 s40, %[b0]\ns_mov_b32 s43, 0\n" \
"s_lshl_b32 s57, %[w], 12\ns_mov_b32 s58, 0xbeeff558\ns_mov_b32 s59, 0x409d30fc\ns_mov_b32 s56, -2147483648\n.p2align 6\n.Lgg_next%=:\ns_cmp_lg_u32 %[w], 0\ns_cbranch_scc1 .Lgg_w1%=\n" \
"buffer_load_dword v56, v3, %[rs], 0 offen sc1\ns_waitcnt vmcnt(0)\ns_barrier\nbuffer_store_dword v90, off, %[rs], s56 sc1\ns_mov_b32 s56, -2147483648\n.Lgg_poll%=:\nv_cmp_gt_i32 vcc, s40, v56\ns_cbranch_vccz .Lgg_go%=\n" \
"s_cmp_ge_u32 s40, 62\ns_cbranch_scc1 .Lgg_ns1055%=\ns_sleep 6\n.Lgg_ns1055%=:\ns_sleep 1\nbuffer_load_dword v56, v3, %[rs], 0 offen sc1\ns_waitcnt vmcnt(0)\ns_add_u32 s43, s43, 1\n" \
"s_cmp_lt_u32 s43, 262144\ns_cbranch_scc1 .Lgg_poll%=\ns_branch .Lgg_abort%=\n.p2align 6\n.Lgg_w1%=:\ns_waitcnt vmcnt(0)\ns_barrier\n.p2align 6\n" \
".Lgg_go%=:\ns_add_u32 s44, s40, 0\ns_lshl_b32 s44, s44, 17\ns_add_u32 s44, s44, s86\ns_lshr_b32 s45, s44, 8\ns_and_b32 s45, s45, 0x2000\ns_xor_b32 s41, s44, s45\ns_add_u32 s61, s41, %[xw]\n" \
"s_barrier\ns_add_u32 m0, s57, 0\ns_add_u32 s60, s61, 0\nbuffer_load_dwordx4 v0, %[rs], s60 offen offset:0 sc1 lds\nbuffer_load_dwordx4 v0, %[rs], s60 offen offset:1024 sc1 lds\nbuffer_load_dwordx4 v0, %[rs], s60 offen offset:2048 sc1 lds\nbuffer_load_dwordx4 v0, %[rs], s60 offen offset:3072 sc1 lds\ns_add_u32 m0, s57, 16384\n" \
"s_add_u32 s60, s61, 32768\nbuffer_load_dwordx4 v0, %[rs], s60 offen offset:0 sc1 lds\nbuffer_load_dwordx4 v0, %[rs], s60 offen offset:1024 sc1 lds\nbuffer_load_dwordx4 v0, %[rs], s60 offen offset:2048 sc1 lds\nbuffer_load_dwordx4 v0, %[rs], s60 offen offset:3072 sc1 lds\nbuffer_load_dwordx4 v[40:43], v5, %[rs], s41 offen offset:0 sc1\nbuffer_load_dwordx4 v[44:47], v5, %[rs], s41 offen offset:256 sc1\ns_lshl_b32 s44, s40, 19\n" \
"s_add_u32 s44, s44, s87\ns_lshr_b32 s45, s44, 8\ns_and_b32 s45, s45, 0x2000\ns_xor_b32 s55, s44, s45\ns_waitcnt vmcnt(6)\ns_barrier\nds_read_b128 v[56:59], v2 offset:0\nds_read_b128 v[60:63], v2 offset:2048\n" \
"s_waitcnt lgkmcnt(1)\nv_mfma_f32_16x16x32_bf16 v[24:27], a[0:3], v[56:59], 0\ns_waitcnt lgkmcnt(0)\nds_read_b128 v[64:67], v2 offset:4096\nv_mfma_f32_16x16x32_bf16 v[24:27], a[4:7], v[60:63], v[24:27]\nds_read_b128 v[68:71], v2 offset:6144\nds_read_b128 v[72:75], v1 offset:0\nv_mfma_f32_16x16x32_bf16 v[28:31], a[8:11], v[56:59], 0\n" \
"ds_read_b128 v[76:79], v1 offset:1024\nbuffer_load_dwordx4 v[48:51], v5, %[rs], s41 offen offset:512 sc1\nv_mfma_f32_16x16x32_bf16 v[28:31], a[12:15], v[60:63], v[28:31]\nv_mfma_f32_16x16x32_bf16 v[32:35], a[16:19], v[56:59], 0\nv_mfma_f32_16x16x32_bf16 v[32:35], a[20:23], v[60:63], v[32:35]\nv_mfma_f32_16x16x32_bf16 v[36:39], a[24:27], v[56:59], 0\nbuffer_load_dwordx4 v[52:55], v5, %[rs], s41 offen offset:768 sc1\nv_mfma_f32_16x16x32_bf16 v[36:39], a[28:31], v[60:63], v[36:39]\n" \
"s_waitcnt lgkmcnt(3)\nds_read_b128 v[80:83], v1 offset:2048\nv_mfma_f32_16x16x32_bf16 v[24:27], a[32:35], v[64:67], v[24:27]\ns_waitcnt lgkmcnt(3)\nds_read_b128 v[56:59], v2 offset:8192\nv_mfma_f32_16x16x32_bf16 v[24:27], a[36:39], v[68:71], v[24:27]\ns_waitcnt lgkmcnt(3)\ns_add_u32 m0, s57, 32768\n" \
"ds_read_b128 v[60:63], v2 offset:10240\nv_mfma_f32_16x16x32_bf16 v[28:31], v[72:75], v[64:67], v[28:31]\ns_add_u32 s60, s61, 65536\ns_waitcnt lgkmcnt(3)\nds_read_b128 v[72:75], v1 offset:3072\nbuffer_load_dwordx4 v0, %[rs], s60 offen offset:0 sc1 lds\nv_mfma_f32_16x16x32_bf16 v[28:31], v[76:79], v[68:71], v[28:31]\nv_mfma_f32_16x16x32_bf16 v[32:35], a[40:43], v[64:67], v[32:35]\n" \
"v_mfma_f32_16x16x32_bf16 v[32:35], a[44:47], v[68:71], v[32:35]\nv_mfma_f32_16x16x32_bf16 v[36:39], a[48:51], v[64:67], v[36:39]\nbuffer_load_dwordx4 v0, %[rs], s60 offen offset:1024 sc1 lds\nv_mfma_f32_16x16x32_bf16 v[36:39], a[52:55], v[68:71], v[36:39]\ns_waitcnt lgkmcnt(2)\nv_mfma_f32_16x16x32_bf16 v[24:27], a[56:59], v[56:59], v[24:27]\ns_waitcnt lgkmcnt(1)\nds_read_b128 v[64:67], v2 offset:12288\n" \
"v_mfma_f32_16x16x32_bf16 v[24:27], a[60:63], v[60:63], v[24:27]\nds_read_b128 v[68:71], v2 offset:14336\nds_read_b128 v[76:79], v1 offset:4096\nv_mfma_f32_16x16x32_bf16 v[28:31], a[64:67], v[56:59], v[28:31]\nbuffer_load_dwordx4 v0, %[rs], s60 offen offset:2048 sc1 lds\nv_mfma_f32_16x16x32_bf16 v[28:31], a[68:71], v[60:63], v[28:31]\nv_mfma_f32_16x16x32_bf16 v[32:35], v[80:83], v[56:59], v[32:35]\ns_waitcnt lgkmcnt(3)\n" \
"ds_read_b128 v[80:83], v1 offset:5120\nv_mfma_f32_16x16x32_bf16 v[32:35], v[72:75], v[60:63], v[32:35]\nv_mfma_f32_16x16x32_bf16 v[36:39], a[72:75], v[56:59], v[36:39]\nbuffer_load_dwordx4 v0, %[rs], s60 offen offset:3072 sc1 lds\nv_mfma_f32_16x16x32_bf16 v[36:39], a[76:79], v[60:63], v[36:39]\ns_waitcnt lgkmcnt(3)\nv_mfma_f32_16x16x32_bf16 v[24:27], a[80:83], v[64:67], v[24:27]\ns_waitcnt vmcnt(8)\n" \
"s_barrier\ns_waitcnt lgkmcnt(2)\nds_read_b128 v[56:59], v2 offset:16384\nv_mfma_f32_16x16x32_bf16 v[24:27], a[84:87], v[68:71], v[24:27]\ns_add_u32 m0, s57, 49152\nds_read_b128 v[60:63], v2 offset:18432\nv_mfma_f32_16x16x32_bf16 v[28:31], a[88:91], v[64:67], v[28:31]\ns_add_u32 s60, s61, 98304\n" \
"buffer_load_dwordx4 v0, %[rs], s60 offen offset:0 sc1 lds\nv_mfma_f32_16x16x32_bf16 v[28:31], a[92:95], v[68:71], v[28:31]\nv_mfma_f32_16x16x32_bf16 v[32:35], a[96:99], v[64:67], v[32:35]\nv_mfma_f32_16x16x32_bf16 v[32:35], a[100:103], v[68:71], v[32:35]\ns_waitcnt lgkmcnt(3)\nds_read_b128 v[72:75], v1 offset:6144\nv_mfma_f32_16x16x32_bf16 v[36:39], v[76:79], v[64:67], v[36:39]\ns_waitcnt lgkmcnt(3)\n" \
"buffer_load_dwordx4 v0, %[rs], s60 offen offset:1024 sc1 lds\nv_mfma_f32_16x16x32_bf16 v[36:39], v[80:83], v[68:71], v[36:39]\ns_waitcnt lgkmcnt(2)\nds_read_b128 v[76:79], v1 offset:7168\nv_mfma_f32_16x16x32_bf16 v[24:27], a[104:107], v[56:59], v[24:27]\ns_waitcnt lgkmcnt(2)\nds_read_b128 v[64:67], v2 offset:20480\nv_mfma_f32_16x16x32_bf16 v[24:27], a[108:111], v[60:63], v[24:27]\n" \
"ds_read_b128 v[68:71], v2 offset:22528\nv_mfma_f32_16x16x32_bf16 v[28:31], a[112:115], v[56:59], v[28:31]\nbuffer_load_dwordx4 v0, %[rs], s60 offen offset:2048 sc1 lds\nv_mfma_f32_16x16x32_bf16 v[28:31], a[116:119], v[60:63], v[28:31]\nv_mfma_f32_16x16x32_bf16 v[32:35], a[120:123], v[56:59], v[32:35]\nv_mfma_f32_16x16x32_bf16 v[32:35], a[124:127], v[60:63], v[32:35]\nv_mfma_f32_16x16x32_bf16 v[36:39], a[128:131], v[56:59], v[36:39]\nbuffer_load_dwordx4 v0, %[rs], s60 offen offset:3072 sc1 lds\n" \
"v_mfma_f32_16x16x32_bf16 v[36:39], a[132:135], v[60:63], v[36:39]\ns_waitcnt lgkmcnt(1)\nds_read_b128 v[80:83], v1 offset:8192\nv_mfma_f32_16x16x32_bf16 v[24:27], a[136:139], v[64:67], v[24:27]\ns_waitcnt vmcnt(8)\ns_waitcnt lgkmcnt(1)\nds_read_b128 v[56:59], v2 offset:24576\nv_add_f32 v84, v40, v44\n" \
"v_mfma_f32_16x16x32_bf16 v[24:27], a[140:143], v[68:71], v[24:27]\nds_read_b128 v[60:63], v2 offset:26624\nv_add_f32 v85, v48, v52\nv_mfma_f32_16x16x32_bf16 v[28:31], v[72:75], v[64:67], v[28:31]\nds_read_b128 v[72:75], v1 offset:9216\nv_add_f32 v84, v84, v85\nv_mfma_f32_16x16x32_bf16 v[28:31], v[76:79], v[68:71], v[28:31]\nv_mov_b32 v85, v84\n" \
"v_mfma_f32_16x16x32_bf16 v[32:35], a[144:147], v[64:67], v[32:35]\ns_nop 0\nv_permlane16_swap_b32 v84, v85\nv_mfma_f32_16x16x32_bf16 v[32:35], a[148:151], v[68:71], v[32:35]\nv_add_f32 v84, v84, v85\nv_mov_b32 v85, v84\nv_mfma_f32_16x16x32_bf16 v[36:39], a[152:155], v[64:67], v[36:39]\ns_nop 0\n" \
"v_permlane32_swap_b32 v84, v85\nv_mfma_f32_16x16x32_bf16 v[36:39], a[156:159], v[68:71], v[36:39]\ns_waitcnt lgkmcnt(2)\nv_add_f32 v84, v84, v85\nv_mul_f32 v84, 0xbd800000, v84\nv_mfma_f32_16x16x32_bf16 v[24:27], a[160:163], v[56:59], v[24:27]\ns_waitcnt lgkmcnt(1)\nds_read_b128 v[64:67], v2 offset:28672\n" \
"v_add_f32 v40, v40, v84\nv_mfma_f32_16x16x32_bf16 v[24:27], a[164:167], v[60:63], v[24:27]\nds_read_b128 v[68:71], v2 offset:30720\nds_read_b128 v[76:79], v1 offset:10240\nv_mfma_f32_16x16x32_bf16 v[28:31], a[168:171], v[56:59], v[28:31]\nv_mul_f32 v40, 0x41000000, v40\nv_fma_f32 v41, v40, v40, v41\nv_mfma_f32_16x16x32_bf16 v[28:31], a[172:175], v[60:63], v[28:31]\n" \
"v_add_f32 v44, v44, v84\nv_mul_f32 v44, 0x41000000, v44\nv_mfma_f32_16x16x32_bf16 v[32:35], v[80:83], v[56:59], v[32:35]\ns_waitcnt lgkmcnt(3)\nds_read_b128 v[80:83], v1 offset:11264\nv_fma_f32 v45, v44, v44, v45\nv_mfma_f32_16x16x32_bf16 v[32:35], v[72:75], v[60:63], v[32:35]\nv_add_f32 v48, v48, v84\n" \
"v_mul_f32 v48, 0x41000000, v48\nv_mfma_f32_16x16x32_bf16 v[36:39], a[176:179], v[56:59], v[36:39]\nv_fma_f32 v49, v48, v48, v49\nv_add_f32 v52, v52, v84\nv_mfma_f32_16x16x32_bf16 v[36:39], a[180:183], v[60:63], v[36:39]\ns_waitcnt lgkmcnt(3)\nv_mul_f32 v52, 0x41000000, v52\nv_fma_f32 v53, v52, v52, v53\n" \
"v_mfma_f32_16x16x32_bf16 v[24:27], a[184:187], v[64:67], v[24:27]\ns_waitcnt vmcnt(4)\ns_barrier\ns_waitcnt lgkmcnt(2)\nds_read_b128 v[56:59], v2 offset:32768\nv_mfma_f32_16x16x32_bf16 v[24:27], a[188:191], v[68:71], v[24:27]\nds_read_b128 v[60:63], v2 offset:34816\nv_add_f32 v85, v41, v45\n" \
"v_mfma_f32_16x16x32_bf16 v[28:31], a[192:195], v[64:67], v[28:31]\nv_add_f32 v86, v49, v53\nv_add_f32 v85, v85, v86\nv_mfma_f32_16x16x32_bf16 v[28:31], a[196:199], v[68:71], v[28:31]\nv_mov_b32 v86, v85\nv_mfma_f32_16x16x32_bf16 v[32:35], a[200:203], v[64:67], v[32:35]\ns_nop 0\nv_permlane16_swap_b32 v85, v86\n" \
"v_mfma_f32_16x16x32_bf16 v[32:35], a[204:207], v[68:71], v[32:35]\ns_waitcnt lgkmcnt(3)\nds_read_b128 v[72:75], v1 offset:12288\nv_add_f32 v85, v85, v86\nv_mfma_f32_16x16x32_bf16 v[36:39], v[76:79], v[64:67], v[36:39]\ns_waitcnt lgkmcnt(3)\nv_mov_b32 v86, v85\nv_mfma_f32_16x16x32_bf16 v[36:39], v[80:83], v[68:71], v[36:39]\n" \
"s_waitcnt lgkmcnt(2)\nds_read_b128 v[76:79], v1 offset:13312\nv_mfma_f32_16x16x32_bf16 v[24:27], a[208:211], v[56:59], v[24:27]\ns_waitcnt lgkmcnt(2)\nds_read_b128 v[64:67], v2 offset:36864\nv_mfma_f32_16x16x32_bf16 v[24:27], a[212:215], v[60:63], v[24:27]\nds_read_b128 v[68:71], v2 offset:38912\nv_mfma_f32_16x16x32_bf16 v[28:31], a[216:219], v[56:59], v[28:31]\n" \
"v_permlane32_swap_b32 v85, v86\nv_mfma_f32_16x16x32_bf16 v[28:31], a[220:223], v[60:63], v[28:31]\nv_add_f32 v85, v85, v86\nv_mul_f32 v85, 0x3a800000, v85\nv_mfma_f32_16x16x32_bf16 v[32:35], a[224:227], v[56:59], v[32:35]\nv_cvt_pk_bf16_f32 v85, v85, v85\nv_and_b32 v85, 0xffff0000, v85\nv_mfma_f32_16x16x32_bf16 v[32:35], a[228:231], v[60:63], v[32:35]\n" \
"v_add_f32 v85, 0x3727c5ac, v85\nv_cvt_pk_bf16_f32 v85, v85, v85\nv_mfma_f32_16x16x32_bf16 v[36:39], a[232:235], v[56:59], v[36:39]\nv_and_b32 v85, 0xffff0000, v85\nv_cvt_pk_bf16_f32 v84, v84, v84\nv_mfma_f32_16x16x32_bf16 v[36:39], a[236:239], v[60:63], v[36:39]\ns_waitcnt lgkmcnt(1)\nds_read_b128 v[80:83], v1 offset:14336\n" \
"v_and_b32 v84, 0xffff0000, v84\nv_mfma_f32_16x16x32_bf16 v[24:27], a[240:243], v[64:67], v[24:27]\ns_waitcnt lgkmcnt(1)\nds_read_b128 v[56:59], v2 offset:40960\nv_mfma_f32_16x16x32_bf16 v[24:27], a[244:247], v[68:71], v[24:27]\nds_read_b128 v[60:63], v2 offset:43008\nv_mfma_f32_16x16x32_bf16 v[28:31], v[72:75], v[64:67], v[28:31]\nds_read_b128 v[72:75], v1 offset:15360\n" \
"v_mfma_f32_16x16x32_bf16 v[28:31], v[76:79], v[68:71], v[28:31]\nv_sqrt_f32 v85, v85\nv_mfma_f32_16x16x32_bf16 v[32:35], a[248:251], v[64:67], v[32:35]\nv_cvt_pk_bf16_f32 v85, v85, v85\nv_and_b32 v85, 0xffff0000, v85\nv_mfma_f32_16x16x32_bf16 v[32:35], a[252:255], v[68:71], v[32:35]\nv_mul_f32 v86, s58, v85\nv_add_f32 v87, v42, v46\n" \
"v_mfma_f32_16x16x32_bf16 v[36:39], v[96:99], v[64:67], v[36:39]\nv_rcp_f32 v85, v85\nv_mfma_f32_16x16x32_bf16 v[36:39], v[100:103], v[68:71], v[36:39]\ns_waitcnt lgkmcnt(2)\nv_mul_f32 v85, s58, v85\nv_add_f32 v88, v50, v54\nv_mfma_f32_16x16x32_bf16 v[24:27], v[104:107], v[56:59], v[24:27]\ns_waitcnt lgkmcnt(1)\n" \
"ds_read_b128 v[64:67], v2 offset:45056\nv_add_f32 v87, v87, v88\nv_mfma_f32_16x16x32_bf16 v[24:27], v[108:111], v[60:63], v[24:27]\nds_read_b128 v[68:71], v2 offset:47104\nds_read_b128 v[76:79], v1 offset:16384\nv_mfma_f32_16x16x32_bf16 v[28:31], v[112:115], v[56:59], v[28:31]\nv_mov_b32 v88, v87\nv_mfma_f32_16x16x32_bf16 v[28:31], v[116:119], v[60:63], v[28:31]\n" \
"s_nop 0\nv_permlane16_swap_b32 v87, v88\nv_mfma_f32_16x16x32_bf16 v[32:35], v[80:83], v[56:59], v[32:35]\ns_waitcnt lgkmcnt(3)\nds_read_b128 v[80:83], v1 offset:17408\nv_add_f32 v87, v87, v88\nv_mfma_f32_16x16x32_bf16 v[32:35], v[72:75], v[60:63], v[32:35]\nv_mov_b32 v88, v87\n" \
"v_mfma_f32_16x16x32_bf16 v[36:39], v[120:123], v[56:59], v[36:39]\ns_nop 0\nv_permlane32_swap_b32 v87, v88\nv_mfma_f32_16x16x32_bf16 v[36:39], v[124:127], v[60:63], v[36:39]\ns_waitcnt lgkmcnt(3)\nv_add_f32 v87, v87, v88\nv_mul_f32 v87, 0xbd800000, v87\nv_mfma_f32_16x16x32_bf16 v[24:27], v[128:131], v[64:67], v[24:27]\n" \
"s_waitcnt vmcnt(0)\ns_barrier\ns_waitcnt lgkmcnt(2)\nds_read_b128 v[56:59], v2 offset:49152\nv_mfma_f32_16x16x32_bf16 v[24:27], v[132:135], v[68:71], v[24:27]\nds_read_b128 v[60:63], v2 offset:51200\nv_add_f32 v42, v42, v87\nv_mfma_f32_16x16x32_bf16 v[28:31], v[136:139], v[64:67], v[28:31]\n" \
"v_mul_f32 v42, 0x41000000, v42\nv_fma_f32 v43, v42, v42, v43\nv_mfma_f32_16x16x32_bf16 v[28:31], v[140:143], v[68:71], v[28:31]\nv_add_f32 v46, v46, v87\nv_mul_f32 v46, 0x41000000, v46\nv_mfma_f32_16x16x32_bf16 v[32:35], v[144:147], v[64:67], v[32:35]\nv_fma_f32 v47, v46, v46, v47\nv_add_f32 v50, v50, v87\n" \
"v_mfma_f32_16x16x32_bf16 v[32:35], v[148:151], v[68:71], v[32:35]\ns_waitcnt lgkmcnt(3)\nds_read_b128 v[72:75], v1 offset:18432\nv_mul_f32 v50, 0x41000000, v50\nv_mfma_f32_16x16x32_bf16 v[36:39], v[76:79], v[64:67], v[36:39]\ns_waitcnt lgkmcnt(3)\nv_fma_f32 v51, v50, v50, v51\nv_add_f32 v54, v54, v87\n" \
"v_mfma_f32_16x16x32_bf16 v[36:39], v[80:83], v[68:71], v[36:39]\ns_waitcnt lgkmcnt(2)\nds_read_b128 v[76:79], v1 offset:19456\nv_mul_f32 v54, 0x41000000, v54\nv_mfma_f32_16x16x32_bf16 v[24:27], v[152:155], v[56:59], v[24:27]\ns_waitcnt lgkmcnt(2)\nds_read_b128 v[64:67], v2 offset:53248\nv_fma_f32 v55, v54, v54, v55\n" \
"v_mfma_f32_16x16x32_bf16 v[24:27], v[156:159], v[60:63], v[24:27]\nds_read_b128 v[68:71], v2 offset:55296\nv_add_f32 v88, v43, v47\nv_mfma_f32_16x16x32_bf16 v[28:31], v[160:163], v[56:59], v[28:31]\nv_add_f32 v89, v51, v55\nv_add_f32 v88, v88, v89\nv_mfma_f32_16x16x32_bf16 v[28:31], v[164:167], v[60:63], v[28:31]\nv_mov_b32 v89, v88\n" \
"v_mfma_f32_16x16x32_bf16 v[32:35], v[168:171], v[56:59], v[32:35]\ns_nop 0\nv_permlane16_swap_b32 v88, v89\nv_mfma_f32_16x16x32_bf16 v[32:35], v[172:175], v[60:63], v[32:35]\nv_add_f32 v88, v88, v89\nv_mov_b32 v89, v88\nv_mfma_f32_16x16x32_bf16 v[36:39], v[176:179], v[56:59], v[36:39]\ns_nop 0\n" \
"v_permlane32_swap_b32 v88, v89\nv_mfma_f32_16x16x32_bf16 v[36:39], v[180:183], v[60:63], v[36:39]\ns_waitcnt lgkmcnt(1)\nds_read_b128 v[80:83], v1 offset:20480\nv_add_f32 v88, v88, v89\nv_mfma_f32_16x16x32_bf16 v[24:27], v[184:187], v[64:67], v[24:27]\ns_waitcnt lgkmcnt(1)\nds_read_b128 v[56:59], v2 offset:57344\n" \
"v_mul_f32 v88, 0x3a800000, v88\nv_mfma_f32_16x16x32_bf16 v[24:27], v[188:191], v[68:71], v[24:27]\nds_read_b128 v[60:63], v2 offset:59392\nv_cvt_pk_bf16_f32 v88, v88, v88\nv_mfma_f32_16x16x32_bf16 v[28:31], v[72:75], v[64:67], v[28:31]\nds_read_b128 v[72:75], v1 offset:21504\nv_and_b32 v88, 0xffff0000, v88\nv_mfma_f32_16x16x32_bf16 v[28:31], v[76:79], v[68:71], v[28:31]\n" \
"v_add_f32 v88, 0x3727c5ac, v88\nv_cvt_pk_bf16_f32 v88, v88, v88\nv_mfma_f32_16x16x32_bf16 v[32:35], v[192:195], v[64:67], v[32:35]\nv_and_b32 v88, 0xffff0000, v88\nv_cvt_pk_bf16_f32 v87, v87, v87\nv_mfma_f32_16x16x32_bf16 v[32:35], v[196:199], v[68:71], v[32:35]\nv_sqrt_f32 v88, v88\nv_mfma_f32_16x16x32_bf16 v[36:39], v[200:203], v[64:67], v[36:39]\n" \
"v_and_b32 v87, 0xffff0000, v87\nv_cvt_pk_bf16_f32 v88, v88, v88\nv_mfma_f32_16x16x32_bf16 v[36:39], v[204:207], v[68:71], v[36:39]\ns_waitcnt lgkmcnt(2)\nv_and_b32 v88, 0xffff0000, v88\nv_mul_f32 v89, s58, v88\nv_mfma_f32_16x16x32_bf16 v[24:27], v[208:211], v[56:59], v[24:27]\ns_waitcnt lgkmcnt(1)\n" \
"ds_read_b128 v[64:67], v2 offset:61440\nv_mfma_f32_16x16x32_bf16 v[24:27], v[212:215], v[60:63], v[24:27]\nds_read_b128 v[68:71], v2 offset:63488\nds_read_b128 v[76:79], v1 offset:22528\nv_mfma_f32_16x16x32_bf16 v[28:31], v[216:219], v[56:59], v[28:31]\nv_rcp_f32 v88, v88\nv_mfma_f32_16x16x32_bf16 v[28:31], v[220:223], v[60:63], v[28:31]\nv_mul_f32 v88, s58, v88\n" \
"v_mfma_f32_16x16x32_bf16 v[32:35], v[80:83], v[56:59], v[32:35]\ns_waitcnt lgkmcnt(3)\nds_read_b128 v[80:83], v1 offset:23552\nv_mfma_f32_16x16x32_bf16 v[32:35], v[72:75], v[60:63], v[32:35]\nv_mfma_f32_16x16x32_bf16 v[36:39], v[224:227], v[56:59], v[36:39]\nv_mfma_f32_16x16x32_bf16 v[36:39], v[228:231], v[60:63], v[36:39]\ns_waitcnt lgkmcnt(3)\nv_mfma_f32_16x16x32_bf16 v[24:27], v[232:235], v[64:67], v[24:27]\n" \
"s_waitcnt lgkmcnt(2)\nds_read_b128 v[56:59], v2 offset:256\nv_mfma_f32_16x16x32_bf16 v[24:27], v[236:239], v[68:71], v[24:27]\nds_read_b128 v[60:63], v2 offset:2304\nv_mfma_f32_16x16x32_bf16 v[28:31], v[240:243], v[64:67], v[28:31]\nv_mfma_f32_16x16x32_bf16 v[28:31], v[244:247], v[68:71], v[28:31]\nv_mfma_f32_16x16x32_bf16 v[32:35], v[248:251], v[64:67], v[32:35]\nv_mfma_f32_16x16x32_bf16 v[32:35], v[252:255], v[68:71], v[32:35]\n" \
"s_waitcnt lgkmcnt(3)\nds_read_b128 v[72:75], v1 offset:0\nv_mfma_f32_16x16x32_bf16 v[36:39], v[76:79], v[64:67], v[36:39]\ns_waitcnt lgkmcnt(3)\nv_mfma_f32_16x16x32_bf16 v[36:39], v[80:83], v[68:71], v[36:39]\ns_waitcnt lgkmcnt(2)\nds_read_b128 v[76:79], v1 offset:1024\nv_mfma_f32_16x16x32_bf16 v[40:43], a[0:3], v[56:59], 0\n" \
"s_waitcnt lgkmcnt(2)\nds_read_b128 v[64:67], v2 offset:4352\nv_mfma_f32_16x16x32_bf16 v[40:43], a[4:7], v[60:63], v[40:43]\ns_lshl_b32 s44, s40, 1\nds_read_b128 v[68:71], v2 offset:6400\nv_mov_b32 v90, s44\nv_mfma_f32_16x16x32_bf16 v[44:47], a[8:11], v[56:59], 0\nv_mfma_f32_16x16x32_bf16 v[44:47], a[12:15], v[60:63], v[44:47]\n" \
"v_fma_f32 v24, v8, v84, v24\nv_fma_f32 v25, v9, v84, v25\nv_mfma_f32_16x16x32_bf16 v[48:51], a[16:19], v[56:59], 0\nv_fma_f32 v32, v16, v84, v32\nv_fma_f32 v33, v17, v84, v33\nv_mfma_f32_16x16x32_bf16 v[48:51], a[20:23], v[60:63], v[48:51]\nv_mul_f32 v24, v24, v85\nv_mul_f32 v25, v25, v85\n" \
"v_mfma_f32_16x16x32_bf16 v[52:55], a[24:27], v[56:59], 0\nv_fma_f32 v91, v24, v24, s59\nv_fma_f32 v92, v25, v25, s59\nv_mfma_f32_16x16x32_bf16 v[52:55], a[28:31], v[60:63], v[52:55]\ns_waitcnt lgkmcnt(1)\nds_read_b128 v[80:83], v1 offset:2048\nv_mul_f32 v91, v91, v24\nv_mfma_f32_16x16x32_bf16 v[40:43], a[32:35], v[64:67], v[40:43]\n" \
"s_waitcnt lgkmcnt(1)\nds_read_b128 v[56:59], v2 offset:8448\nv_mul_f32 v92, v92, v25\nv_mfma_f32_16x16x32_bf16 v[40:43], a[36:39], v[68:71], v[40:43]\nds_read_b128 v[60:63], v2 offset:10496\nv_mul_f32 v24, v24, v32\nv_mfma_f32_16x16x32_bf16 v[44:47], v[72:75], v[64:67], v[44:47]\nds_read_b128 v[72:75], v1 offset:3072\n" \
"v_mul_f32 v25, v25, v33\nv_mfma_f32_16x16x32_bf16 v[44:47], v[76:79], v[68:71], v[44:47]\nv_exp_f32 v91, v91\nv_mfma_f32_16x16x32_bf16 v[48:51], a[40:43], v[64:67], v[48:51]\nv_exp_f32 v92, v92\nv_mfma_f32_16x16x32_bf16 v[48:51], a[44:47], v[68:71], v[48:51]\nv_fma_f32 v91, v91, v86, v86\nv_fma_f32 v92, v92, v86, v86\n" \
"v_mfma_f32_16x16x32_bf16 v[52:55], a[48:51], v[64:67], v[52:55]\nv_rcp_f32 v91, v91\nv_mfma_f32_16x16x32_bf16 v[52:55], a[52:55], v[68:71], v[52:55]\ns_waitcnt lgkmcnt(2)\nv_rcp_f32 v92, v92\nv_mfma_f32_16x16x32_bf16 v[40:43], a[56:59], v[56:59], v[40:43]\ns_waitcnt lgkmcnt(1)\nds_read_b128 v[64:67], v2 offset:12544\n" \
"v_mul_f32 v24, v24, v91\nv_mfma_f32_16x16x32_bf16 v[40:43], a[60:63], v[60:63], v[40:43]\nds_read_b128 v[68:71], v2 offset:14592\nds_read_b128 v[76:79], v1 offset:4096\nv_mfma_f32_16x16x32_bf16 v[44:47], a[64:67], v[56:59], v[44:47]\nv_mul_f32 v25, v25, v92\nv_cvt_pk_bf16_f32 v32, v24, v25\nv_mfma_f32_16x16x32_bf16 v[44:47], a[68:71], v[60:63], v[44:47]\n" \
"v_fma_f32 v26, v10, v84, v26\nv_fma_f32 v27, v11, v84, v27\nv_mfma_f32_16x16x32_bf16 v[48:51], v[80:83], v[56:59], v[48:51]\ns_waitcnt lgkmcnt(3)\nds_read_b128 v[80:83], v1 offset:5120\nv_fma_f32 v34, v18, v84, v34\nv_mfma_f32_16x16x32_bf16 v[48:51], v[72:75], v[60:63], v[48:51]\nv_fma_f32 v35, v19, v84, v35\n" \
"v_mul_f32 v26, v26, v85\nv_mfma_f32_16x16x32_bf16 v[52:55], a[72:75], v[56:59], v[52:55]\nv_mul_f32 v27, v27, v85\nv_fma_f32 v91, v26, v26, s59\nv_mfma_f32_16x16x32_bf16 v[52:55], a[76:79], v[60:63], v[52:55]\ns_waitcnt lgkmcnt(3)\nv_fma_f32 v92, v27, v27, s59\nv_mul_f32 v91, v91, v26\n" \
"v_mfma_f32_16x16x32_bf16 v[40:43], a[80:83], v[64:67], v[40:43]\ns_waitcnt lgkmcnt(2)\nds_read_b128 v[56:59], v2 offset:16640\nv_mul_f32 v92, v92, v27\nv_mfma_f32_16x16x32_bf16 v[40:43], a[84:87], v[68:71], v[40:43]\nds_read_b128 v[60:63], v2 offset:18688\nv_mul_f32 v26, v26, v34\nv_mfma_f32_16x16x32_bf16 v[44:47], a[88:91], v[64:67], v[44:47]\n" \
"v_exp_f32 v91, v91\nv_mfma_f32_16x16x32_bf16 v[44:47], a[92:95], v[68:71], v[44:47]\nv_exp_f32 v92, v92\nv_mfma_f32_16x16x32_bf16 v[48:51], a[96:99], v[64:67], v[48:51]\nv_mul_f32 v27, v27, v35\nv_fma_f32 v91, v91, v86, v86\nv_mfma_f32_16x16x32_bf16 v[48:51], a[100:103], v[68:71], v[48:51]\ns_waitcnt lgkmcnt(3)\n" \
"ds_read_b128 v[72:75], v1 offset:6144\nv_fma_f32 v92, v92, v86, v86\nv_mfma_f32_16x16x32_bf16 v[52:55], v[76:79], v[64:67], v[52:55]\ns_waitcnt lgkmcnt(3)\nv_rcp_f32 v91, v91\nv_mfma_f32_16x16x32_bf16 v[52:55], v[80:83], v[68:71], v[52:55]\ns_waitcnt lgkmcnt(2)\nds_read_b128 v[76:79], v1 offset:7168\n" \
"v_mul_f32 v26, v26, v91\nv_mfma_f32_16x16x32_bf16 v[40:43], a[104:107], v[56:59], v[40:43]\ns_waitcnt lgkmcnt(2)\nds_read_b128 v[64:67], v2 offset:20736\nv_fma_f32 v28, v12, v84, v28\nv_mfma_f32_16x16x32_bf16 v[40:43], a[108:111], v[60:63], v[40:43]\nds_read_b128 v[68:71], v2 offset:22784\nv_fma_f32 v29, v13, v84, v29\n" \
"v_mfma_f32_16x16x32_bf16 v[44:47], a[112:115], v[56:59], v[44:47]\nv_rcp_f32 v92, v92\nv_mfma_f32_16x16x32_bf16 v[44:47], a[116:119], v[60:63], v[44:47]\nv_mul_f32 v27, v27, v92\nv_cvt_pk_bf16_f32 v33, v26, v27\nv_mfma_f32_16x16x32_bf16 v[48:51], a[120:123], v[56:59], v[48:51]\nv_fma_f32 v36, v20, v84, v36\nv_fma_f32 v37, v21, v84, v37\n" \
"v_mfma_f32_16x16x32_bf16 v[48:51], a[124:127], v[60:63], v[48:51]\nv_mul_f32 v28, v28, v85\nv_mul_f32 v29, v29, v85\nv_mfma_f32_16x16x32_bf16 v[52:55], a[128:131], v[56:59], v[52:55]\nv_fma_f32 v91, v28, v28, s59\nv_fma_f32 v92, v29, v29, s59\nv_mfma_f32_16x16x32_bf16 v[52:55], a[132:135], v[60:63], v[52:55]\ns_waitcnt lgkmcnt(1)\n" \
"ds_read_b128 v[80:83], v1 offset:8192\nv_mul_f32 v91, v91, v28\nv_mfma_f32_16x16x32_bf16 v[40:43], a[136:139], v[64:67], v[40:43]\ns_waitcnt lgkmcnt(1)\nds_read_b128 v[56:59], v2 offset:24832\nv_mul_f32 v92, v92, v29\nv_mfma_f32_16x16x32_bf16 v[40:43], a[140:143], v[68:71], v[40:43]\nds_read_b128 v[60:63], v2 offset:26880\n" \
"v_mul_f32 v28, v28, v36\nv_mfma_f32_16x16x32_bf16 v[44:47], v[72:75], v[64:67], v[44:47]\nds_read_b128 v[72:75], v1 offset:9216\nv_mul_f32 v29, v29, v37\nv_mfma_f32_16x16x32_bf16 v[44:47], v[76:79], v[68:71], v[44:47]\nv_exp_f32 v91, v91\nv_mfma_f32_16x16x32_bf16 v[48:51], a[144:147], v[64:67], v[48:51]\nv_exp_f32 v92, v92\n" \
"v_mfma_f32_16x16x32_bf16 v[48:51], a[148:151], v[68:71], v[48:51]\nv_fma_f32 v91, v91, v86, v86\nv_fma_f32 v92, v92, v86, v86\nv_mfma_f32_16x16x32_bf16 v[52:55], a[152:155], v[64:67], v[52:55]\nv_rcp_f32 v91, v91\nv_mfma_f32_16x16x32_bf16 v[52:55], a[156:159], v[68:71], v[52:55]\ns_waitcnt lgkmcnt(2)\nv_rcp_f32 v92, v92\n" \
"v_mfma_f32_16x16x32_bf16 v[40:43], a[160:163], v[56:59], v[40:43]\ns_waitcnt lgkmcnt(1)\nds_read_b128 v[64:67], v2 offset:28928\nv_mul_f32 v28, v28, v91\nv_mfma_f32_16x16x32_bf16 v[40:43], a[164:167], v[60:63], v[40:43]\nds_read_b128 v[68:71], v2 offset:30976\nds_read_b128 v[76:79], v1 offset:10240\nv_mfma_f32_16x16x32_bf16 v[44:47], a[168:171], v[56:59], v[44:47]\n" \
"v_mul_f32 v29, v29, v92\nv_cvt_pk_bf16_f32 v34, v28, v29\nv_mfma_f32_16x16x32_bf16 v[44:47], a[172:175], v[60:63], v[44:47]\nv_fma_f32 v30, v14, v84, v30\nv_fma_f32 v31, v15, v84, v31\nv_mfma_f32_16x16x32_bf16 v[48:51], v[80:83], v[56:59], v[48:51]\ns_waitcnt lgkmcnt(3)\nds_read_b128 v[80:83], v1 offset:11264\n" \
"v_fma_f32 v38, v22, v84, v38\nv_mfma_f32_16x16x32_bf16 v[48:51], v[72:75], v[60:63], v[48:51]\nv_fma_f32 v39, v23, v84, v39\nv_mul_f32 v30, v30, v85\nv_mfma_f32_16x16x32_bf16 v[52:55], a[176:179], v[56:59], v[52:55]\nv_mul_f32 v31, v31, v85\nv_fma_f32 v91, v30, v30, s59\nv_mfma_f32_16x16x32_bf16 v[52:55], a[180:183], v[60:63], v[52:55]\n" \
"s_waitcnt lgkmcnt(3)\nv_fma_f32 v92, v31, v31, s59\nv_mul_f32 v91, v91, v30\nv_mfma_f32_16x16x32_bf16 v[40:43], a[184:187], v[64:67], v[40:43]\ns_waitcnt lgkmcnt(2)\nds_read_b128 v[56:59], v2 offset:33024\nv_mul_f32 v92, v92, v31\nv_mfma_f32_16x16x32_bf16 v[40:43], a[188:191], v[68:71], v[40:43]\n" \
"ds_read_b128 v[60:63], v2 offset:35072\nv_mul_f32 v30, v30, v38\nv_mfma_f32_16x16x32_bf16 v[44:47], a[192:195], v[64:67], v[44:47]\nv_exp_f32 v91, v91\nv_mfma_f32_16x16x32_bf16 v[44:47], a[196:199], v[68:71], v[44:47]\nv_exp_f32 v92, v92\nv_mfma_f32_16x16x32_bf16 v[48:51], a[200:203], v[64:67], v[48:51]\nv_mul_f32 v31, v31, v39\n" \
"v_fma_f32 v91, v91, v86, v86\nv_mfma_f32_16x16x32_bf16 v[48:51], a[204:207], v[68:71], v[48:51]\ns_waitcnt lgkmcnt(3)\nds_read_b128 v[72:75], v1 offset:12288\nv_fma_f32 v92, v92, v86, v86\nv_mfma_f32_16x16x32_bf16 v[52:55], v[76:79], v[64:67], v[52:55]\ns_waitcnt lgkmcnt(3)\nv_rcp_f32 v91, v91\n" \
"v_mfma_f32_16x16x32_bf16 v[52:55], v[80:83], v[68:71], v[52:55]\ns_waitcnt lgkmcnt(2)\nds_read_b128 v[76:79], v1 offset:13312\nv_mul_f32 v30, v30, v91\nv_mfma_f32_16x16x32_bf16 v[40:43], a[208:211], v[56:59], v[40:43]\ns_waitcnt lgkmcnt(2)\nds_read_b128 v[64:67], v2 offset:37120\nv_mfma_f32_16x16x32_bf16 v[40:43], a[212:215], v[60:63], v[40:43]\n" \
"ds_read_b128 v[68:71], v2 offset:39168\nv_mfma_f32_16x16x32_bf16 v[44:47], a[216:219], v[56:59], v[44:47]\nv_rcp_f32 v92, v92\nv_mfma_f32_16x16x32_bf16 v[44:47], a[220:223], v[60:63], v[44:47]\nv_mul_f32 v31, v31, v92\nv_cvt_pk_bf16_f32 v35, v30, v31\nv_mfma_f32_16x16x32_bf16 v[48:51], a[224:227], v[56:59], v[48:51]\nbuffer_store_dwordx4 v[32:35], v4, %[rs], s55 offen offset:0 sc1\n" \
"v_mfma_f32_16x16x32_bf16 v[48:51], a[228:231], v[60:63], v[48:51]\nds_read_b128 v[80:83], v1 offset:14336\nv_mfma_f32_16x16x32_bf16 v[52:55], a[232:235], v[56:59], v[52:55]\nv_mfma_f32_16x16x32_bf16 v[52:55], a[236:239], v[60:63], v[52:55]\ns_waitcnt lgkmcnt(2)\nv_mfma_f32_16x16x32_bf16 v[40:43], a[240:243], v[64:67], v[40:43]\ns_waitcnt lgkmcnt(1)\nds_read_b128 v[56:59], v2 offset:41216\n" \
"v_mfma_f32_16x16x32_bf16 v[40:43], a[244:247], v[68:71], v[40:43]\nds_read_b128 v[60:63], v2 offset:43264\nv_mfma_f32_16x16x32_bf16 v[44:47], v[72:75], v[64:67], v[44:47]\nds_read_b128 v[72:75], v1 offset:15360\nv_mfma_f32_16x16x32_bf16 v[44:47], v[76:79], v[68:71], v[44:47]\nv_mfma_f32_16x16x32_bf16 v[48:51], a[248:251], v[64:67], v[48:51]\nds_read_b128 v[24:27], v2 offset:45312\nv_mfma_f32_16x16x32_bf16 v[48:51], a[252:255], v[68:71], v[48:51]\n" \
"ds_read_b128 v[28:31], v2 offset:47360\nv_mfma_f32_16x16x32_bf16 v[52:55], v[96:99], v[64:67], v[52:55]\nv_mfma_f32_16x16x32_bf16 v[52:55], v[100:103], v[68:71], v[52:55]\ns_waitcnt lgkmcnt(4)\nv_mfma_f32_16x16x32_bf16 v[40:43], v[104:107], v[56:59], v[40:43]\ns_waitcnt lgkmcnt(3)\nds_read_b128 v[64:67], v2 offset:49408\nv_mfma_f32_16x16x32_bf16 v[40:43], v[108:111], v[60:63], v[40:43]\n" \
"ds_read_b128 v[68:71], v2 offset:51456\nv_mfma_f32_16x16x32_bf16 v[48:51], v[80:83], v[56:59], v[48:51]\ns_waitcnt lgkmcnt(4)\nv_mfma_f32_16x16x32_bf16 v[48:51], v[72:75], v[60:63], v[48:51]\ns_waitcnt lgkmcnt(3)\nv_mfma_f32_16x16x32_bf16 v[40:43], v[128:131], v[24:27], v[40:43]\ns_waitcnt lgkmcnt(2)\nds_read_b128 v[56:59], v2 offset:53504\n" \
"v_mfma_f32_16x16x32_bf16 v[40:43], v[132:135], v[28:31], v[40:43]\nds_read_b128 v[60:63], v2 offset:55552\nds_read_b128 v[76:79], v1 offset:20480\nv_mfma_f32_16x16x32_bf16 v[48:51], v[144:147], v[24:27], v[48:51]\nv_mfma_f32_16x16x32_bf16 v[48:51], v[148:151], v[28:31], v[48:51]\ns_waitcnt lgkmcnt(4)\nds_read_b128 v[80:83], v1 offset:21504\nv_mfma_f32_16x16x32_bf16 v[40:43], v[152:155], v[64:67], v[40:43]\n" \
"s_waitcnt lgkmcnt(4)\nds_read_b128 v[24:27], v2 offset:57600\nv_mfma_f32_16x16x32_bf16 v[40:43], v[156:159], v[68:71], v[40:43]\nds_read_b128 v[28:31], v2 offset:59648\nv_mfma_f32_16x16x32_bf16 v[48:51], v[168:171], v[64:67], v[48:51]\nv_mfma_f32_16x16x32_bf16 v[48:51], v[172:175], v[68:71], v[48:51]\ns_waitcnt lgkmcnt(5)\nv_mfma_f32_16x16x32_bf16 v[40:43], v[184:187], v[56:59], v[40:43]\n" \
"s_waitcnt lgkmcnt(4)\nds_read_b128 v[64:67], v2 offset:61696\nv_mfma_f32_16x16x32_bf16 v[40:43], v[188:191], v[60:63], v[40:43]\nds_read_b128 v[68:71], v2 offset:63744\nv_mfma_f32_16x16x32_bf16 v[48:51], v[192:195], v[56:59], v[48:51]\nv_mfma_f32_16x16x32_bf16 v[48:51], v[196:199], v[60:63], v[48:51]\ns_waitcnt lgkmcnt(3)\nv_mfma_f32_16x16x32_bf16 v[40:43], v[208:211], v[24:27], v[40:43]\n" \
"s_waitcnt lgkmcnt(2)\nds_read_b128 v[56:59], v2 offset:41216\nv_mfma_f32_16x16x32_bf16 v[40:43], v[212:215], v[28:31], v[40:43]\nds_read_b128 v[60:63], v2 offset:43264\nds_read_b128 v[72:75], v1 offset:16384\nv_mfma_f32_16x16x32_bf16 v[48:51], v[76:79], v[24:27], v[48:51]\nv_mfma_f32_16x16x32_bf16 v[48:51], v[80:83], v[28:31], v[48:51]\ns_waitcnt lgkmcnt(4)\n" \
"ds_read_b128 v[76:79], v1 offset:17408\nv_mfma_f32_16x16x32_bf16 v[40:43], v[232:235], v[64:67], v[40:43]\ns_waitcnt lgkmcnt(4)\nds_read_b128 v[24:27], v2 offset:45312\nv_mfma_f32_16x16x32_bf16 v[40:43], v[236:239], v[68:71], v[40:43]\ns_waitcnt vmcnt(0)\nds_read_b128 v[28:31], v2 offset:47360\ns_barrier\n" \
"buffer_store_dword v90, off, %[rs], s88 sc1\nv_mfma_f32_16x16x32_bf16 v[48:51], v[248:251], v[64:67], v[48:51]\nv_mfma_f32_16x16x32_bf16 v[48:51], v[252:255], v[68:71], v[48:51]\ns_waitcnt lgkmcnt(5)\nds_read_b128 v[80:83], v1 offset:18432\nv_mfma_f32_16x16x32_bf16 v[44:47], v[112:115], v[56:59], v[44:47]\ns_waitcnt lgkmcnt(5)\nds_read_b128 v[64:67], v2 offset:49408\n" \
"v_mfma_f32_16x16x32_bf16 v[44:47], v[116:119], v[60:63], v[44:47]\nds_read_b128 v[68:71], v2 offset:51456\nv_fma_f32 v40, v8, v87, v40\nv_mfma_f32_16x16x32_bf16 v[52:55], v[120:123], v[56:59], v[52:55]\nv_fma_f32 v41, v9, v87, v41\nv_fma_f32 v48, v16, v87, v48\nv_mfma_f32_16x16x32_bf16 v[52:55], v[124:127], v[60:63], v[52:55]\ns_waitcnt lgkmcnt(4)\n" \
"v_fma_f32 v49, v17, v87, v49\nv_mul_f32 v40, v40, v88\nv_mfma_f32_16x16x32_bf16 v[44:47], v[136:139], v[24:27], v[44:47]\ns_waitcnt lgkmcnt(3)\nds_read_b128 v[56:59], v2 offset:53504\nv_mul_f32 v41, v41, v88\nv_mfma_f32_16x16x32_bf16 v[44:47], v[140:143], v[28:31], v[44:47]\nds_read_b128 v[60:63], v2 offset:55552\n" \
"v_fma_f32 v91, v40, v40, s59\nv_mfma_f32_16x16x32_bf16 v[52:55], v[72:75], v[24:27], v[52:55]\nds_read_b128 v[72:75], v1 offset:19456\nv_fma_f32 v92, v41, v41, s59\nv_mfma_f32_16x16x32_bf16 v[52:55], v[76:79], v[28:31], v[52:55]\ns_waitcnt lgkmcnt(4)\nv_mul_f32 v91, v91, v40\nv_mul_f32 v92, v92, v41\n" \
"v_mfma_f32_16x16x32_bf16 v[44:47], v[160:163], v[64:67], v[44:47]\ns_waitcnt lgkmcnt(3)\nds_read_b128 v[24:27], v2 offset:57600\nv_mul_f32 v40, v40, v48\nv_mfma_f32_16x16x32_bf16 v[44:47], v[164:167], v[68:71], v[44:47]\nds_read_b128 v[28:31], v2 offset:59648\nds_read_b128 v[76:79], v1 offset:22528\nv_mfma_f32_16x16x32_bf16 v[52:55], v[176:179], v[64:67], v[52:55]\n" \
"v_exp_f32 v91, v91\nv_mfma_f32_16x16x32_bf16 v[52:55], v[180:183], v[68:71], v[52:55]\ns_waitcnt lgkmcnt(5)\nv_exp_f32 v92, v92\nv_mfma_f32_16x16x32_bf16 v[44:47], v[80:83], v[56:59], v[44:47]\ns_waitcnt lgkmcnt(3)\nds_read_b128 v[64:67], v2 offset:61696\nds_read_b128 v[80:83], v1 offset:23552\n" \
"v_mfma_f32_16x16x32_bf16 v[44:47], v[72:75], v[60:63], v[44:47]\nds_read_b128 v[68:71], v2 offset:63744\nv_mul_f32 v41, v41, v49\nv_mfma_f32_16x16x32_bf16 v[52:55], v[200:203], v[56:59], v[52:55]\nv_fma_f32 v91, v91, v89, v89\nv_fma_f32 v92, v92, v89, v89\nv_mfma_f32_16x16x32_bf16 v[52:55], v[204:207], v[60:63], v[52:55]\ns_waitcnt lgkmcnt(5)\n" \
"v_rcp_f32 v91, v91\nv_mfma_f32_16x16x32_bf16 v[44:47], v[216:219], v[24:27], v[44:47]\ns_waitcnt lgkmcnt(4)\nv_rcp_f32 v92, v92\nv_mfma_f32_16x16x32_bf16 v[44:47], v[220:223], v[28:31], v[44:47]\nv_mul_f32 v40, v40, v91\nv_mul_f32 v41, v41, v92\nv_mfma_f32_16x16x32_bf16 v[52:55], v[224:227], v[24:27], v[52:55]\n" \
"v_cvt_pk_bf16_f32 v48, v40, v41\nv_fma_f32 v42, v10, v87, v42\nv_mfma_f32_16x16x32_bf16 v[52:55], v[228:231], v[28:31], v[52:55]\ns_waitcnt lgkmcnt(2)\nv_fma_f32 v43, v11, v87, v43\nv_fma_f32 v50, v18, v87, v50\nv_mfma_f32_16x16x32_bf16 v[44:47], v[240:243], v[64:67], v[44:47]\ns_waitcnt lgkmcnt(0)\n" \
"v_fma_f32 v51, v19, v87, v51\nv_mul_f32 v42, v42, v88\nv_mfma_f32_16x16x32_bf16 v[44:47], v[244:247], v[68:71], v[44:47]\nv_mul_f32 v43, v43, v88\nv_fma_f32 v91, v42, v42, s59\nv_mfma_f32_16x16x32_bf16 v[52:55], v[76:79], v[64:67], v[52:55]\nv_fma_f32 v92, v43, v43, s59\nv_mul_f32 v91, v91, v42\n" \
"v_mfma_f32_16x16x32_bf16 v[52:55], v[80:83], v[68:71], v[52:55]\nv_mul_f32 v92, v92, v43\nv_mul_f32 v42, v42, v50\nv_exp_f32 v91, v91\nv_exp_f32 v92, v92\nv_mul_f32 v43, v43, v51\nv_fma_f32 v91, v91, v89, v89\nv_fma_f32 v92, v92, v89, v89\n" \
"v_rcp_f32 v91, v91\nv_rcp_f32 v92, v92\nv_mul_f32 v42, v42, v91\nv_mul_f32 v43, v43, v92\nv_cvt_pk_bf16_f32 v49, v42, v43\nv_fma_f32 v44, v12, v87, v44\nv_fma_f32 v45, v13, v87, v45\nv_fma_f32 v52, v20, v87, v52\n" \
"v_fma_f32 v53, v21, v87, v53\nv_mul_f32 v44, v44, v88\nv_mul_f32 v45, v45, v88\nv_fma_f32 v91, v44, v44, s59\nv_fma_f32 v92, v45, v45, s59\nv_mul_f32 v91, v91, v44\nv_mul_f32 v92, v92, v45\nv_exp_f32 v91, v91\n" \
"v_exp_f32 v92, v92\nv_mul_f32 v44, v44, v52\nv_mul_f32 v45, v45, v53\nv_fma_f32 v91, v91, v89, v89\nv_fma_f32 v92, v92, v89, v89\nv_rcp_f32 v91, v91\nv_rcp_f32 v92, v92\nv_mul_f32 v44, v44, v91\n" \
"v_mul_f32 v45, v45, v92\nv_cvt_pk_bf16_f32 v50, v44, v45\nv_fma_f32 v46, v14, v87, v46\nv_fma_f32 v47, v15, v87, v47\nv_fma_f32 v54, v22, v87, v54\nv_fma_f32 v55, v23, v87, v55\nv_mul_f32 v46, v46, v88\nv_mul_f32 v47, v47, v88\n" \
"v_fma_f32 v91, v46, v46, s59\nv_fma_f32 v92, v47, v47, s59\nv_mul_f32 v91, v91, v46\nv_mul_f32 v92, v92, v47\nv_exp_f32 v91, v91\nv_exp_f32 v92, v92\nv_mul_f32 v46, v46, v54\nv_mul_f32 v47, v47, v55\n" \
"v_fma_f32 v91, v91, v89, v89\nv_fma_f32 v92, v92, v89, v89\nv_rcp_f32 v91, v91\nv_rcp_f32 v92, v92\nv_mul_f32 v46, v46, v91\nv_mul_f32 v47, v47, v92\nv_cvt_pk_bf16_f32 v51, v46, v47\nbuffer_store_dwordx4 v[48:51], v4, %[rs], s55 offen offset:1024 sc1\n" \
"v_add_u32 v90, 1, v90\ns_mov_b32 s56, s88\ns_add_u32 s40, s40, 1\ns_cmp_lt_u32 s40, s89\ns_cbranch_scc1 .Lgg_next%=\ns_cmp_eq_u32 s89, 64\ns_cbranch_scc1 .Lgg_fin%=\ns_mov_b32 s89, 64\n" \
"s_mov_b32 s40, 0\ns_xor_b32 s86, s86, 0x2000\ns_xor_b32 s87, s87, 0x2000\ns_mov_b32 s88, %[fh1]\nv_xor_b32 v3, 0x2000, v3\ns_branch .Lgg_next%=\n.Lgg_fin%=:\ns_waitcnt vmcnt(0)\n" \
"s_barrier\nbuffer_store_dword v90, off, %[rs], s56 sc1\ns_branch .Lgg_end%=\n.Lgg_abort%=:\ns_mov_b64 exec, -1\nv_mov_b32 v16, 0\ns_movk_i32 s44, 6400\nbuffer_store_dword v16, off, %[rs], s44 sc1\n" \
".Lgg_end%=:\ns_waitcnt vmcnt(0) lgkmcnt(0)\n" \
""
#define DD_ASM \
"v_mbcnt_lo_u32_b32 v255, -1, 0\nv_mbcnt_hi_u32_b32 v255, -1, v255\nv_lshlrev_b32 v255, 2, v255\nv_add_u32 v255, %[tb], v255\nds_read_b32 v0, v255 offset:0\nds_read_b32 v1, v255 offset:256\nds_read_b32 v2, v255 offset:512\nds_read_b32 v3, v255 offset:768\n" \
"ds_read_b32 v4, v255 offset:1024\nds_read_b32 v5, v255 offset:1280\nds_read_b32 v6, v255 offset:1536\nds_read_b32 v7, v255 offset:1792\nds_read_b32 v125, v255 offset:2048\nds_read_b32 v126, v255 offset:2304\nds_read_b32 v127, v255 offset:2560\ns_waitcnt lgkmcnt(0)\n" \
"s_add_u32 m0, %[tb], 8192\ns_add_u32 s44, %[wo], 0\nbuffer_load_dwordx4 v125, %[rw], s44 offen nt lds\ns_add_u32 m0, %[tb], 9216\ns_add_u32 s44, %[wo], 16384\nbuffer_load_dwordx4 v125, %[rw], s44 offen nt lds\ns_add_u32 m0, %[tb], 10240\ns_add_u32 s44, %[wo], 32768\n" \
"buffer_load_dwordx4 v125, %[rw], s44 offen nt lds\ns_add_u32 m0, %[tb], 11264\ns_add_u32 s44, %[wo], 49152\nbuffer_load_dwordx4 v125, %[rw], s44 offen nt lds\ns_add_u32 m0, %[lw], 0\ns_add_u32 s44, %[wo], 65536\nbuffer_load_dwordx4 v125, %[rw], s44 offen nt lds\ns_add_u32 m0, %[lw], 1024\n" \
"s_add_u32 s44, %[wo], 81920\nbuffer_load_dwordx4 v125, %[rw], s44 offen nt lds\ns_add_u32 m0, %[lw], 2048\ns_add_u32 s44, %[wo], 98304\nbuffer_load_dwordx4 v125, %[rw], s44 offen nt lds\ns_add_u32 m0, %[lw], 3072\ns_add_u32 s44, %[wo], 114688\nbuffer_load_dwordx4 v125, %[rw], s44 offen nt lds\n" \
"s_add_u32 m0, %[lw], 4096\ns_add_u32 s44, %[wo], 131072\nbuffer_load_dwordx4 v125, %[rw], s44 offen nt lds\ns_add_u32 m0, %[lw], 5120\ns_add_u32 s44, %[wo], 147456\nbuffer_load_dwordx4 v125, %[rw], s44 offen nt lds\ns_add_u32 m0, %[lw], 6144\ns_add_u32 s44, %[wo], 163840\n" \
"buffer_load_dwordx4 v125, %[rw], s44 offen nt lds\ns_add_u32 m0, %[lw], 7168\ns_add_u32 s44, %[wo], 180224\nbuffer_load_dwordx4 v125, %[rw], s44 offen nt lds\ns_add_u32 m0, %[lw], 8192\ns_add_u32 s44, %[wo], 196608\nbuffer_load_dwordx4 v125, %[rw], s44 offen nt lds\ns_add_u32 m0, %[lw], 9216\n" \
"s_add_u32 s44, %[wo], 212992\nbuffer_load_dwordx4 v125, %[rw], s44 offen nt lds\ns_add_u32 m0, %[lw], 10240\ns_add_u32 s44, %[wo], 229376\nbuffer_load_dwordx4 v125, %[rw], s44 offen nt lds\ns_add_u32 m0, %[lw], 11264\ns_add_u32 s44, %[wo], 245760\nbuffer_load_dwordx4 v125, %[rw], s44 offen nt lds\n" \
"s_add_u32 m0, %[lw], 12288\ns_add_u32 s44, %[wo], 262144\nbuffer_load_dwordx4 v125, %[rw], s44 offen nt lds\ns_add_u32 m0, %[lw], 13312\ns_add_u32 s44, %[wo], 278528\nbuffer_load_dwordx4 v125, %[rw], s44 offen nt lds\ns_add_u32 m0, %[lw], 14336\ns_add_u32 s44, %[wo], 294912\n" \
"buffer_load_dwordx4 v125, %[rw], s44 offen nt lds\ns_add_u32 m0, %[lw], 15360\ns_add_u32 s44, %[wo], 311296\nbuffer_load_dwordx4 v125, %[rw], s44 offen nt lds\ns_add_u32 m0, %[lw], 16384\ns_add_u32 s44, %[wo], 327680\nbuffer_load_dwordx4 v125, %[rw], s44 offen nt lds\ns_add_u32 m0, %[lw], 17408\n" \
"s_add_u32 s44, %[wo], 344064\nbuffer_load_dwordx4 v125, %[rw], s44 offen nt lds\ns_add_u32 m0, %[lw], 18432\ns_add_u32 s44, %[wo], 360448\nbuffer_load_dwordx4 v125, %[rw], s44 offen nt lds\ns_add_u32 m0, %[lw], 19456\ns_add_u32 s44, %[wo], 376832\nbuffer_load_dwordx4 v125, %[rw], s44 offen nt lds\n" \
"s_add_u32 m0, %[lw], 20480\ns_add_u32 s44, %[wo], 393216\nbuffer_load_dwordx4 v125, %[rw], s44 offen nt lds\ns_add_u32 m0, %[lw], 21504\ns_add_u32 s44, %[wo], 409600\nbuffer_load_dwordx4 v125, %[rw], s44 offen nt lds\ns_add_u32 m0, %[lw], 22528\ns_add_u32 s44, %[wo], 425984\n" \
"buffer_load_dwordx4 v125, %[rw], s44 offen nt lds\ns_add_u32 m0, %[lw], 23552\ns_add_u32 s44, %[wo], 442368\nbuffer_load_dwordx4 v125, %[rw], s44 offen nt lds\ns_add_u32 m0, %[tb], 0\ns_add_u32 s44, %[wo], 458752\nbuffer_load_dwordx4 v125, %[rw], s44 offen nt lds\ns_add_u32 m0, %[tb], 1024\n" \
"s_add_u32 s44, %[wo], 475136\nbuffer_load_dwordx4 v125, %[rw], s44 offen nt lds\ns_add_u32 m0, %[tb], 2048\ns_add_u32 s44, %[wo], 491520\nbuffer_load_dwordx4 v125, %[rw], s44 offen nt lds\ns_add_u32 m0, %[tb], 3072\ns_add_u32 s44, %[wo], 507904\nbuffer_load_dwordx4 v125, %[rw], s44 offen nt lds\n" \
"s_add_u32 m0, %[tb], 4096\ns_add_u32 s44, %[wo], 524288\nbuffer_load_dwordx4 v125, %[rw], s44 offen nt lds\ns_add_u32 m0, %[tb], 5120\ns_add_u32 s44, %[wo], 540672\nbuffer_load_dwordx4 v125, %[rw], s44 offen nt lds\ns_add_u32 m0, %[tb], 6144\ns_add_u32 s44, %[wo], 557056\n" \
"buffer_load_dwordx4 v125, %[rw], s44 offen nt lds\ns_add_u32 m0, %[tb], 7168\ns_add_u32 s44, %[wo], 573440\nbuffer_load_dwordx4 v125, %[rw], s44 offen nt lds\ns_waitcnt vmcnt(32)\nds_read_b64_tr_b16 a[0:1], v126 offset:8192\nds_read_b64_tr_b16 a[2:3], v126 offset:8704\nds_read_b64_tr_b16 a[8:9], v126 offset:8224\n" \
"ds_read_b64_tr_b16 a[10:11], v126 offset:8736\nds_read_b64_tr_b16 a[16:17], v126 offset:8256\nds_read_b64_tr_b16 a[18:19], v126 offset:8768\nds_read_b64_tr_b16 a[24:25], v126 offset:8288\nds_read_b64_tr_b16 a[26:27], v126 offset:8800\ns_waitcnt lgkmcnt(0)\ns_add_u32 m0, %[tb], 8192\ns_add_u32 s44, %[wo], 589824\n" \
"buffer_load_dwordx4 v125, %[rw], s44 offen nt lds\ns_add_u32 m0, %[tb], 9216\ns_add_u32 s44, %[wo], 606208\nbuffer_load_dwordx4 v125, %[rw], s44 offen nt lds\ns_add_u32 m0, %[tb], 10240\ns_add_u32 s44, %[wo], 622592\nbuffer_load_dwordx4 v125, %[rw], s44 offen nt lds\ns_add_u32 m0, %[tb], 11264\n" \
"s_add_u32 s44, %[wo], 638976\nbuffer_load_dwordx4 v125, %[rw], s44 offen nt lds\ns_waitcnt vmcnt(32)\nds_read_b64_tr_b16 a[4:5], v127 offset:0\nds_read_b64_tr_b16 a[6:7], v127 offset:512\nds_read_b64_tr_b16 a[12:13], v127 offset:32\nds_read_b64_tr_b16 a[14:15], v127 offset:544\nds_read_b64_tr_b16 a[20:21], v127 offset:64\n" \
"ds_read_b64_tr_b16 a[22:23], v127 offset:576\nds_read_b64_tr_b16 a[28:29], v127 offset:96\nds_read_b64_tr_b16 a[30:31], v127 offset:608\ns_waitcnt lgkmcnt(0)\ns_add_u32 m0, %[lw], 0\ns_add_u32 s44, %[wo], 655360\nbuffer_load_dwordx4 v125, %[rw], s44 offen nt lds\ns_add_u32 m0, %[lw], 1024\n" \
"s_add_u32 s44, %[wo], 671744\nbuffer_load_dwordx4 v125, %[rw], s44 offen nt lds\ns_add_u32 m0, %[lw], 2048\ns_add_u32 s44, %[wo], 688128\nbuffer_load_dwordx4 v125, %[rw], s44 offen nt lds\ns_add_u32 m0, %[lw], 3072\ns_add_u32 s44, %[wo], 704512\nbuffer_load_dwordx4 v125, %[rw], s44 offen nt lds\n" \
"s_waitcnt vmcnt(32)\nds_read_b64_tr_b16 a[32:33], v127 offset:4096\nds_read_b64_tr_b16 a[34:35], v127 offset:4608\nds_read_b64_tr_b16 v[92:93], v127 offset:4128\nds_read_b64_tr_b16 v[94:95], v127 offset:4640\nds_read_b64_tr_b16 a[40:41], v127 offset:4160\nds_read_b64_tr_b16 a[42:43], v127 offset:4672\nds_read_b64_tr_b16 a[48:49], v127 offset:4192\n" \
"ds_read_b64_tr_b16 a[50:51], v127 offset:4704\ns_waitcnt lgkmcnt(0)\ns_add_u32 m0, %[lw], 4096\ns_add_u32 s44, %[wo], 720896\nbuffer_load_dwordx4 v125, %[rw], s44 offen nt lds\ns_add_u32 m0, %[lw], 5120\ns_add_u32 s44, %[wo], 737280\nbuffer_load_dwordx4 v125, %[rw], s44 offen nt lds\n" \
"s_add_u32 m0, %[lw], 6144\ns_add_u32 s44, %[wo], 753664\nbuffer_load_dwordx4 v125, %[rw], s44 offen nt lds\ns_add_u32 m0, %[lw], 7168\ns_add_u32 s44, %[wo], 770048\nbuffer_load_dwordx4 v125, %[rw], s44 offen nt lds\ns_waitcnt vmcnt(32)\nds_read_b64_tr_b16 a[36:37], v127 offset:8192\n" \
"ds_read_b64_tr_b16 a[38:39], v127 offset:8704\nds_read_b64_tr_b16 v[88:89], v127 offset:8224\nds_read_b64_tr_b16 v[90:91], v127 offset:8736\nds_read_b64_tr_b16 a[44:45], v127 offset:8256\nds_read_b64_tr_b16 a[46:47], v127 offset:8768\nds_read_b64_tr_b16 a[52:53], v127 offset:8288\nds_read_b64_tr_b16 a[54:55], v127 offset:8800\ns_waitcnt lgkmcnt(0)\n" \
"s_add_u32 m0, %[lw], 8192\ns_add_u32 s44, %[wo], 786432\nbuffer_load_dwordx4 v125, %[rw], s44 offen nt lds\ns_add_u32 m0, %[lw], 9216\ns_add_u32 s44, %[wo], 802816\nbuffer_load_dwordx4 v125, %[rw], s44 offen nt lds\ns_add_u32 m0, %[lw], 10240\ns_add_u32 s44, %[wo], 819200\n" \
"buffer_load_dwordx4 v125, %[rw], s44 offen nt lds\ns_add_u32 m0, %[lw], 11264\ns_add_u32 s44, %[wo], 835584\nbuffer_load_dwordx4 v125, %[rw], s44 offen nt lds\ns_waitcnt vmcnt(32)\nds_read_b64_tr_b16 a[56:57], v127 offset:12288\nds_read_b64_tr_b16 a[58:59], v127 offset:12800\nds_read_b64_tr_b16 a[64:65], v127 offset:12320\n" \
"ds_read_b64_tr_b16 a[66:67], v127 offset:12832\nds_read_b64_tr_b16 a[72:73], v127 offset:12352\nds_read_b64_tr_b16 a[74:75], v127 offset:12864\nds_read_b64_tr_b16 a[80:81], v127 offset:12384\nds_read_b64_tr_b16 a[82:83], v127 offset:12896\ns_waitcnt lgkmcnt(0)\ns_add_u32 m0, %[lw], 12288\ns_add_u32 s44, %[wo], 851968\n" \
"buffer_load_dwordx4 v125, %[rw], s44 offen nt lds\ns_add_u32 m0, %[lw], 13312\ns_add_u32 s44, %[wo], 868352\nbuffer_load_dwordx4 v125, %[rw], s44 offen nt lds\ns_add_u32 m0, %[lw], 14336\ns_add_u32 s44, %[wo], 884736\nbuffer_load_dwordx4 v125, %[rw], s44 offen nt lds\ns_add_u32 m0, %[lw], 15360\n" \
"s_add_u32 s44, %[wo], 901120\nbuffer_load_dwordx4 v125, %[rw], s44 offen nt lds\ns_waitcnt vmcnt(32)\nds_read_b64_tr_b16 a[60:61], v127 offset:16384\nds_read_b64_tr_b16 a[62:63], v127 offset:16896\nds_read_b64_tr_b16 a[68:69], v127 offset:16416\nds_read_b64_tr_b16 a[70:71], v127 offset:16928\nds_read_b64_tr_b16 a[76:77], v127 offset:16448\n" \
"ds_read_b64_tr_b16 a[78:79], v127 offset:16960\nds_read_b64_tr_b16 a[84:85], v127 offset:16480\nds_read_b64_tr_b16 a[86:87], v127 offset:16992\ns_waitcnt lgkmcnt(0)\ns_add_u32 m0, %[lw], 16384\ns_add_u32 s44, %[wo], 917504\nbuffer_load_dwordx4 v125, %[rw], s44 offen nt lds\ns_add_u32 m0, %[lw], 17408\n" \
"s_add_u32 s44, %[wo], 933888\nbuffer_load_dwordx4 v125, %[rw], s44 offen nt lds\ns_add_u32 m0, %[lw], 18432\ns_add_u32 s44, %[wo], 950272\nbuffer_load_dwordx4 v125, %[rw], s44 offen nt lds\ns_add_u32 m0, %[lw], 19456\ns_add_u32 s44, %[wo], 966656\nbuffer_load_dwordx4 v125, %[rw], s44 offen nt lds\n" \
"s_waitcnt vmcnt(32)\nds_read_b64_tr_b16 a[88:89], v127 offset:20480\nds_read_b64_tr_b16 a[90:91], v127 offset:20992\nds_read_b64_tr_b16 a[96:97], v127 offset:20512\nds_read_b64_tr_b16 a[98:99], v127 offset:21024\nds_read_b64_tr_b16 a[104:105], v127 offset:20544\nds_read_b64_tr_b16 a[106:107], v127 offset:21056\nds_read_b64_tr_b16 v[84:85], v127 offset:20576\n" \
"ds_read_b64_tr_b16 v[86:87], v127 offset:21088\ns_waitcnt lgkmcnt(0)\ns_add_u32 m0, %[lw], 20480\ns_add_u32 s44, %[wo], 983040\nbuffer_load_dwordx4 v125, %[rw], s44 offen nt lds\ns_add_u32 m0, %[lw], 21504\ns_add_u32 s44, %[wo], 999424\nbuffer_load_dwordx4 v125, %[rw], s44 offen nt lds\n" \
"s_add_u32 m0, %[lw], 22528\ns_add_u32 s44, %[wo], 1015808\nbuffer_load_dwordx4 v125, %[rw], s44 offen nt lds\ns_add_u32 m0, %[lw], 23552\ns_add_u32 s44, %[wo], 1032192\nbuffer_load_dwordx4 v125, %[rw], s44 offen nt lds\ns_waitcnt vmcnt(32)\nds_read_b64_tr_b16 a[92:93], v126 offset:0\n" \
"ds_read_b64_tr_b16 a[94:95], v126 offset:512\nds_read_b64_tr_b16 a[100:101], v126 offset:32\nds_read_b64_tr_b16 a[102:103], v126 offset:544\nds_read_b64_tr_b16 a[108:109], v126 offset:64\nds_read_b64_tr_b16 a[110:111], v126 offset:576\nds_read_b64_tr_b16 v[80:81], v126 offset:96\nds_read_b64_tr_b16 v[82:83], v126 offset:608\ns_waitcnt lgkmcnt(0)\n" \
"s_add_u32 m0, %[tb], 0\ns_add_u32 s44, %[wo], 1048576\nbuffer_load_dwordx4 v125, %[rw], s44 offen nt lds\ns_add_u32 m0, %[tb], 1024\ns_add_u32 s44, %[wo], 1064960\nbuffer_load_dwordx4 v125, %[rw], s44 offen nt lds\ns_add_u32 m0, %[tb], 2048\ns_add_u32 s44, %[wo], 1081344\n" \
"buffer_load_dwordx4 v125, %[rw], s44 offen nt lds\ns_add_u32 m0, %[tb], 3072\ns_add_u32 s44, %[wo], 1097728\nbuffer_load_dwordx4 v125, %[rw], s44 offen nt lds\ns_waitcnt vmcnt(32)\nds_read_b64_tr_b16 a[112:113], v126 offset:4096\nds_read_b64_tr_b16 a[114:115], v126 offset:4608\nds_read_b64_tr_b16 a[120:121], v126 offset:4128\n" \
"ds_read_b64_tr_b16 a[122:123], v126 offset:4640\nds_read_b64_tr_b16 a[128:129], v126 offset:4160\nds_read_b64_tr_b16 a[130:131], v126 offset:4672\nds_read_b64_tr_b16 a[136:137], v126 offset:4192\nds_read_b64_tr_b16 a[138:139], v126 offset:4704\ns_waitcnt lgkmcnt(0)\ns_add_u32 m0, %[tb], 4096\ns_add_u32 s44, %[wo], 1114112\n" \
"buffer_load_dwordx4 v125, %[rw], s44 offen nt lds\ns_add_u32 m0, %[tb], 5120\ns_add_u32 s44, %[wo], 1130496\nbuffer_load_dwordx4 v125, %[rw], s44 offen nt lds\ns_add_u32 m0, %[tb], 6144\ns_add_u32 s44, %[wo], 1146880\nbuffer_load_dwordx4 v125, %[rw], s44 offen nt lds\ns_add_u32 m0, %[tb], 7168\n" \
"s_add_u32 s44, %[wo], 1163264\nbuffer_load_dwordx4 v125, %[rw], s44 offen nt lds\ns_waitcnt vmcnt(32)\nds_read_b64_tr_b16 a[116:117], v126 offset:8192\nds_read_b64_tr_b16 a[118:119], v126 offset:8704\nds_read_b64_tr_b16 a[124:125], v126 offset:8224\nds_read_b64_tr_b16 a[126:127], v126 offset:8736\nds_read_b64_tr_b16 a[132:133], v126 offset:8256\n" \
"ds_read_b64_tr_b16 a[134:135], v126 offset:8768\nds_read_b64_tr_b16 a[140:141], v126 offset:8288\nds_read_b64_tr_b16 a[142:143], v126 offset:8800\ns_waitcnt lgkmcnt(0)\ns_add_u32 m0, %[tb], 8192\ns_add_u32 s44, %[wo], 1179648\nbuffer_load_dwordx4 v125, %[rw], s44 offen nt lds\ns_add_u32 m0, %[tb], 9216\n" \
"s_add_u32 s44, %[wo], 1196032\nbuffer_load_dwordx4 v125, %[rw], s44 offen nt lds\ns_add_u32 m0, %[tb], 10240\ns_add_u32 s44, %[wo], 1212416\nbuffer_load_dwordx4 v125, %[rw], s44 offen nt lds\ns_add_u32 m0, %[tb], 11264\ns_add_u32 s44, %[wo], 1228800\nbuffer_load_dwordx4 v125, %[rw], s44 offen nt lds\n" \
"s_waitcnt vmcnt(32)\nds_read_b64_tr_b16 a[144:145], v127 offset:0\nds_read_b64_tr_b16 a[146:147], v127 offset:512\nds_read_b64_tr_b16 v[76:77], v127 offset:32\nds_read_b64_tr_b16 v[78:79], v127 offset:544\nds_read_b64_tr_b16 a[152:153], v127 offset:64\nds_read_b64_tr_b16 a[154:155], v127 offset:576\nds_read_b64_tr_b16 a[160:161], v127 offset:96\n" \
"ds_read_b64_tr_b16 a[162:163], v127 offset:608\ns_waitcnt lgkmcnt(0)\ns_add_u32 m0, %[lw], 0\ns_add_u32 s44, %[wo], 1245184\nbuffer_load_dwordx4 v125, %[rw], s44 offen nt lds\ns_add_u32 m0, %[lw], 1024\ns_add_u32 s44, %[wo], 1261568\nbuffer_load_dwordx4 v125, %[rw], s44 offen nt lds\n" \
"s_add_u32 m0, %[lw], 2048\ns_add_u32 s44, %[wo], 1277952\nbuffer_load_dwordx4 v125, %[rw], s44 offen nt lds\ns_add_u32 m0, %[lw], 3072\ns_add_u32 s44, %[wo], 1294336\nbuffer_load_dwordx4 v125, %[rw], s44 offen nt lds\ns_waitcnt vmcnt(32)\nds_read_b64_tr_b16 a[148:149], v127 offset:4096\n" \
"ds_read_b64_tr_b16 a[150:151], v127 offset:4608\nds_read_b64_tr_b16 v[72:73], v127 offset:4128\nds_read_b64_tr_b16 v[74:75], v127 offset:4640\nds_read_b64_tr_b16 a[156:157], v127 offset:4160\nds_read_b64_tr_b16 a[158:159], v127 offset:4672\nds_read_b64_tr_b16 a[164:165], v127 offset:4192\nds_read_b64_tr_b16 a[166:167], v127 offset:4704\ns_waitcnt lgkmcnt(0)\n" \
"s_add_u32 m0, %[lw], 4096\ns_add_u32 s44, %[wo], 1310720\nbuffer_load_dwordx4 v125, %[rw], s44 offen nt lds\ns_add_u32 m0, %[lw], 5120\ns_add_u32 s44, %[wo], 1327104\nbuffer_load_dwordx4 v125, %[rw], s44 offen nt lds\ns_add_u32 m0, %[lw], 6144\ns_add_u32 s44, %[wo], 1343488\n" \
"buffer_load_dwordx4 v125, %[rw], s44 offen nt lds\ns_add_u32 m0, %[lw], 7168\ns_add_u32 s44, %[wo], 1359872\nbuffer_load_dwordx4 v125, %[rw], s44 offen nt lds\ns_waitcnt vmcnt(32)\nds_read_b64_tr_b16 a[168:169], v127 offset:8192\nds_read_b64_tr_b16 a[170:171], v127 offset:8704\nds_read_b64_tr_b16 a[176:177], v127 offset:8224\n" \
"ds_read_b64_tr_b16 a[178:179], v127 offset:8736\nds_read_b64_tr_b16 v[68:69], v127 offset:8256\nds_read_b64_tr_b16 v[70:71], v127 offset:8768\nds_read_b64_tr_b16 a[184:185], v127 offset:8288\nds_read_b64_tr_b16 a[186:187], v127 offset:8800\ns_waitcnt lgkmcnt(0)\ns_add_u32 m0, %[lw], 8192\ns_add_u32 s44, %[wo], 1376256\n" \
"buffer_load_dwordx4 v125, %[rw], s44 offen nt lds\ns_add_u32 m0, %[lw], 9216\ns_add_u32 s44, %[wo], 1392640\nbuffer_load_dwordx4 v125, %[rw], s44 offen nt lds\ns_add_u32 m0, %[lw], 10240\ns_add_u32 s44, %[wo], 1409024\nbuffer_load_dwordx4 v125, %[rw], s44 offen nt lds\ns_add_u32 m0, %[lw], 11264\n" \
"s_add_u32 s44, %[wo], 1425408\nbuffer_load_dwordx4 v125, %[rw], s44 offen nt lds\ns_waitcnt vmcnt(32)\nds_read_b64_tr_b16 a[172:173], v127 offset:12288\nds_read_b64_tr_b16 a[174:175], v127 offset:12800\nds_read_b64_tr_b16 a[180:181], v127 offset:12320\nds_read_b64_tr_b16 a[182:183], v127 offset:12832\nds_read_b64_tr_b16 v[64:65], v127 offset:12352\n" \
"ds_read_b64_tr_b16 v[66:67], v127 offset:12864\nds_read_b64_tr_b16 a[188:189], v127 offset:12384\nds_read_b64_tr_b16 a[190:191], v127 offset:12896\ns_waitcnt lgkmcnt(0)\ns_add_u32 m0, %[lw], 12288\ns_add_u32 s44, %[wo], 1441792\nbuffer_load_dwordx4 v125, %[rw], s44 offen nt lds\ns_add_u32 m0, %[lw], 13312\n" \
"s_add_u32 s44, %[wo], 1458176\nbuffer_load_dwordx4 v125, %[rw], s44 offen nt lds\ns_add_u32 m0, %[lw], 14336\ns_add_u32 s44, %[wo], 1474560\nbuffer_load_dwordx4 v125, %[rw], s44 offen nt lds\ns_add_u32 m0, %[lw], 15360\ns_add_u32 s44, %[wo], 1490944\nbuffer_load_dwordx4 v125, %[rw], s44 offen nt lds\n" \
"s_waitcnt vmcnt(32)\nds_read_b64_tr_b16 a[192:193], v127 offset:16384\nds_read_b64_tr_b16 a[194:195], v127 offset:16896\nds_read_b64_tr_b16 a[200:201], v127 offset:16416\nds_read_b64_tr_b16 a[202:203], v127 offset:16928\nds_read_b64_tr_b16 a[208:209], v127 offset:16448\nds_read_b64_tr_b16 a[210:211], v127 offset:16960\nds_read_b64_tr_b16 a[216:217], v127 offset:16480\n" \
"ds_read_b64_tr_b16 a[218:219], v127 offset:16992\ns_waitcnt lgkmcnt(0)\ns_add_u32 m0, %[lw], 16384\ns_add_u32 s44, %[wo], 1507328\nbuffer_load_dwordx4 v125, %[rw], s44 offen nt lds\ns_add_u32 m0, %[lw], 17408\ns_add_u32 s44, %[wo], 1523712\nbuffer_load_dwordx4 v125, %[rw], s44 offen nt lds\n" \
"s_add_u32 m0, %[lw], 18432\ns_add_u32 s44, %[wo], 1540096\nbuffer_load_dwordx4 v125, %[rw], s44 offen nt lds\ns_add_u32 m0, %[lw], 19456\ns_add_u32 s44, %[wo], 1556480\nbuffer_load_dwordx4 v125, %[rw], s44 offen nt lds\ns_waitcnt vmcnt(32)\nds_read_b64_tr_b16 a[196:197], v127 offset:20480\n" \
"ds_read_b64_tr_b16 a[198:199], v127 offset:20992\nds_read_b64_tr_b16 a[204:205], v127 offset:20512\nds_read_b64_tr_b16 a[206:207], v127 offset:21024\nds_read_b64_tr_b16 a[212:213], v127 offset:20544\nds_read_b64_tr_b16 a[214:215], v127 offset:21056\nds_read_b64_tr_b16 a[220:221], v127 offset:20576\nds_read_b64_tr_b16 a[222:223], v127 offset:21088\ns_waitcnt lgkmcnt(0)\n" \
"s_add_u32 m0, %[lw], 20480\ns_add_u32 s44, %[wo], 1572864\nbuffer_load_dwordx4 v125, %[rw], s44 offen nt lds\ns_add_u32 m0, %[lw], 21504\ns_add_u32 s44, %[wo], 1589248\nbuffer_load_dwordx4 v125, %[rw], s44 offen nt lds\ns_add_u32 m0, %[lw], 22528\ns_add_u32 s44, %[wo], 1605632\n" \
"buffer_load_dwordx4 v125, %[rw], s44 offen nt lds\ns_add_u32 m0, %[lw], 23552\ns_add_u32 s44, %[wo], 1622016\nbuffer_load_dwordx4 v125, %[rw], s44 offen nt lds\ns_waitcnt vmcnt(32)\nds_read_b64_tr_b16 v[60:61], v126 offset:0\nds_read_b64_tr_b16 v[62:63], v126 offset:512\nds_read_b64_tr_b16 a[224:225], v126 offset:32\n" \
"ds_read_b64_tr_b16 a[226:227], v126 offset:544\nds_read_b64_tr_b16 a[232:233], v126 offset:64\nds_read_b64_tr_b16 a[234:235], v126 offset:576\nds_read_b64_tr_b16 a[240:241], v126 offset:96\nds_read_b64_tr_b16 a[242:243], v126 offset:608\ns_waitcnt lgkmcnt(0)\ns_add_u32 m0, %[tb], 0\ns_add_u32 s44, %[wo], 1638400\n" \
"buffer_load_dwordx4 v125, %[rw], s44 offen nt lds\ns_add_u32 m0, %[tb], 1024\ns_add_u32 s44, %[wo], 1654784\nbuffer_load_dwordx4 v125, %[rw], s44 offen nt lds\ns_add_u32 m0, %[tb], 2048\ns_add_u32 s44, %[wo], 1671168\nbuffer_load_dwordx4 v125, %[rw], s44 offen nt lds\ns_add_u32 m0, %[tb], 3072\n" \
"s_add_u32 s44, %[wo], 1687552\nbuffer_load_dwordx4 v125, %[rw], s44 offen nt lds\ns_waitcnt vmcnt(32)\nds_read_b64_tr_b16 v[56:57], v126 offset:4096\nds_read_b64_tr_b16 v[58:59], v126 offset:4608\nds_read_b64_tr_b16 a[228:229], v126 offset:4128\nds_read_b64_tr_b16 a[230:231], v126 offset:4640\nds_read_b64_tr_b16 a[236:237], v126 offset:4160\n" \
"ds_read_b64_tr_b16 a[238:239], v126 offset:4672\nds_read_b64_tr_b16 a[244:245], v126 offset:4192\nds_read_b64_tr_b16 a[246:247], v126 offset:4704\ns_waitcnt lgkmcnt(0)\ns_add_u32 m0, %[tb], 4096\ns_add_u32 s44, %[wo], 1703936\nbuffer_load_dwordx4 v125, %[rw], s44 offen nt lds\ns_add_u32 m0, %[tb], 5120\n" \
"s_add_u32 s44, %[wo], 1720320\nbuffer_load_dwordx4 v125, %[rw], s44 offen nt lds\ns_add_u32 m0, %[tb], 6144\ns_add_u32 s44, %[wo], 1736704\nbuffer_load_dwordx4 v125, %[rw], s44 offen nt lds\ns_add_u32 m0, %[tb], 7168\ns_add_u32 s44, %[wo], 1753088\nbuffer_load_dwordx4 v125, %[rw], s44 offen nt lds\n" \
"s_waitcnt vmcnt(32)\nds_read_b64_tr_b16 a[248:249], v126 offset:8192\nds_read_b64_tr_b16 a[250:251], v126 offset:8704\nds_read_b64_tr_b16 v[128:129], v126 offset:8224\nds_read_b64_tr_b16 v[130:131], v126 offset:8736\nds_read_b64_tr_b16 v[136:137], v126 offset:8256\nds_read_b64_tr_b16 v[138:139], v126 offset:8768\nds_read_b64_tr_b16 v[144:145], v126 offset:8288\n" \
"ds_read_b64_tr_b16 v[146:147], v126 offset:8800\ns_waitcnt lgkmcnt(0)\ns_add_u32 m0, %[tb], 8192\ns_add_u32 s44, %[wo], 1769472\nbuffer_load_dwordx4 v125, %[rw], s44 offen nt lds\ns_add_u32 m0, %[tb], 9216\ns_add_u32 s44, %[wo], 1785856\nbuffer_load_dwordx4 v125, %[rw], s44 offen nt lds\n" \
"s_add_u32 m0, %[tb], 10240\ns_add_u32 s44, %[wo], 1802240\nbuffer_load_dwordx4 v125, %[rw], s44 offen nt lds\ns_add_u32 m0, %[tb], 11264\ns_add_u32 s44, %[wo], 1818624\nbuffer_load_dwordx4 v125, %[rw], s44 offen nt lds\ns_waitcnt vmcnt(32)\nds_read_b64_tr_b16 a[252:253], v127 offset:0\n" \
"ds_read_b64_tr_b16 a[254:255], v127 offset:512\nds_read_b64_tr_b16 v[132:133], v127 offset:32\nds_read_b64_tr_b16 v[134:135], v127 offset:544\nds_read_b64_tr_b16 v[140:141], v127 offset:64\nds_read_b64_tr_b16 v[142:143], v127 offset:576\nds_read_b64_tr_b16 v[148:149], v127 offset:96\nds_read_b64_tr_b16 v[150:151], v127 offset:608\ns_waitcnt lgkmcnt(0)\n" \
"ds_write_b128 v1, v[92:95] offset:0\nds_write_b128 v1, v[88:91] offset:1024\nds_write_b128 v1, v[84:87] offset:2048\nds_write_b128 v1, v[80:83] offset:3072\ns_waitcnt vmcnt(28)\nds_read_b64_tr_b16 v[152:153], v127 offset:4096\nds_read_b64_tr_b16 v[154:155], v127 offset:4608\nds_read_b64_tr_b16 v[160:161], v127 offset:4128\n" \
"ds_read_b64_tr_b16 v[162:163], v127 offset:4640\nds_read_b64_tr_b16 v[80:81], v127 offset:4160\nds_read_b64_tr_b16 v[82:83], v127 offset:4672\nds_read_b64_tr_b16 v[168:169], v127 offset:4192\nds_read_b64_tr_b16 v[170:171], v127 offset:4704\ns_waitcnt lgkmcnt(0)\nds_write_b128 v1, v[76:79] offset:4096\nds_write_b128 v1, v[72:75] offset:5120\n" \
"ds_write_b128 v1, v[68:71] offset:6144\nds_write_b128 v1, v[64:67] offset:7168\ns_waitcnt vmcnt(24)\nds_read_b64_tr_b16 v[156:157], v127 offset:8192\nds_read_b64_tr_b16 v[158:159], v127 offset:8704\nds_read_b64_tr_b16 v[164:165], v127 offset:8224\nds_read_b64_tr_b16 v[166:167], v127 offset:8736\nds_read_b64_tr_b16 v[64:65], v127 offset:8256\n" \
"ds_read_b64_tr_b16 v[66:67], v127 offset:8768\nds_read_b64_tr_b16 v[172:173], v127 offset:8288\nds_read_b64_tr_b16 v[174:175], v127 offset:8800\ns_waitcnt lgkmcnt(0)\nds_write_b128 v1, v[60:63] offset:8192\nds_write_b128 v1, v[56:59] offset:9216\nds_write_b128 v1, v[80:83] offset:10240\nds_write_b128 v1, v[64:67] offset:11264\n" \
"s_waitcnt vmcnt(20)\nds_read_b64_tr_b16 v[176:177], v127 offset:12288\nds_read_b64_tr_b16 v[178:179], v127 offset:12800\nds_read_b64_tr_b16 v[184:185], v127 offset:12320\nds_read_b64_tr_b16 v[186:187], v127 offset:12832\nds_read_b64_tr_b16 v[192:193], v127 offset:12352\nds_read_b64_tr_b16 v[194:195], v127 offset:12864\nds_read_b64_tr_b16 v[200:201], v127 offset:12384\n" \
"ds_read_b64_tr_b16 v[202:203], v127 offset:12896\ns_waitcnt lgkmcnt(0)\ns_waitcnt vmcnt(16)\nds_read_b64_tr_b16 v[180:181], v127 offset:16384\nds_read_b64_tr_b16 v[182:183], v127 offset:16896\nds_read_b64_tr_b16 v[188:189], v127 offset:16416\nds_read_b64_tr_b16 v[190:191], v127 offset:16928\nds_read_b64_tr_b16 v[196:197], v127 offset:16448\n" \
"ds_read_b64_tr_b16 v[198:199], v127 offset:16960\nds_read_b64_tr_b16 v[204:205], v127 offset:16480\nds_read_b64_tr_b16 v[206:207], v127 offset:16992\ns_waitcnt lgkmcnt(0)\ns_waitcnt vmcnt(12)\nds_read_b64_tr_b16 v[64:65], v127 offset:20480\nds_read_b64_tr_b16 v[66:67], v127 offset:20992\nds_read_b64_tr_b16 v[208:209], v127 offset:20512\n" \
"ds_read_b64_tr_b16 v[210:211], v127 offset:21024\nds_read_b64_tr_b16 v[216:217], v127 offset:20544\nds_read_b64_tr_b16 v[218:219], v127 offset:21056\nds_read_b64_tr_b16 v[224:225], v127 offset:20576\nds_read_b64_tr_b16 v[226:227], v127 offset:21088\ns_waitcnt lgkmcnt(0)\nds_write_b128 v1, v[64:67] offset:12288\ns_waitcnt vmcnt(8)\n" \
"ds_read_b64_tr_b16 v[64:65], v126 offset:0\nds_read_b64_tr_b16 v[66:67], v126 offset:512\nds_read_b64_tr_b16 v[212:213], v126 offset:32\nds_read_b64_tr_b16 v[214:215], v126 offset:544\nds_read_b64_tr_b16 v[220:221], v126 offset:64\nds_read_b64_tr_b16 v[222:223], v126 offset:576\nds_read_b64_tr_b16 v[228:229], v126 offset:96\nds_read_b64_tr_b16 v[230:231], v126 offset:608\n" \
"s_waitcnt lgkmcnt(0)\nds_write_b128 v1, v[64:67] offset:13312\ns_waitcnt vmcnt(4)\nds_read_b64_tr_b16 v[232:233], v126 offset:4096\nds_read_b64_tr_b16 v[234:235], v126 offset:4608\nds_read_b64_tr_b16 v[64:65], v126 offset:4128\nds_read_b64_tr_b16 v[66:67], v126 offset:4640\nds_read_b64_tr_b16 v[240:241], v126 offset:4160\n" \
"ds_read_b64_tr_b16 v[242:243], v126 offset:4672\nds_read_b64_tr_b16 v[248:249], v126 offset:4192\nds_read_b64_tr_b16 v[250:251], v126 offset:4704\ns_waitcnt lgkmcnt(0)\nds_write_b128 v1, v[64:67] offset:14336\ns_waitcnt vmcnt(0)\nds_read_b64_tr_b16 v[236:237], v126 offset:8192\nds_read_b64_tr_b16 v[238:239], v126 offset:8704\n" \
"ds_read_b64_tr_b16 v[64:65], v126 offset:8224\nds_read_b64_tr_b16 v[66:67], v126 offset:8736\nds_read_b64_tr_b16 v[244:245], v126 offset:8256\nds_read_b64_tr_b16 v[246:247], v126 offset:8768\nds_read_b64_tr_b16 v[252:253], v126 offset:8288\nds_read_b64_tr_b16 v[254:255], v126 offset:8800\ns_waitcnt lgkmcnt(0)\nds_write_b128 v1, v[64:67] offset:15360\n" \
"s_waitcnt lgkmcnt(0)\ns_barrier\ns_mov_b32 s40, 0\ns_mov_b32 s43, 0\ns_mov_b32 s56, -2147483648\n.p2align 6\n.Ldd_next%=:\ns_cmp_eq_u32 s40, %[b1]\n" \
"s_cselect_b32 s44, 4, 0\nv_xor_b32 v7, s44, v7\nbuffer_load_dword v8, v7, %[rs], 0 offen sc1\ns_lshl_b32 s58, s40, 1\ns_add_u32 s44, s40, 0\ns_lshl_b32 s44, s44, 19\ns_add_u32 s44, s44, %[ho]\ns_lshr_b32 s45, s44, 8\n" \
"s_and_b32 s45, s45, 0x2000\ns_xor_b32 s55, s44, s45\ns_waitcnt vmcnt(0)\nbuffer_store_dword v9, off, %[rs], s56 sc1\ns_mov_b32 s56, -2147483648\n.Ldd_w0%=:\nv_cmp_gt_i32 vcc, s58, v8\ns_cbranch_vccz .Ldd_w0go%=\n" \
"s_cmp_ge_u32 s40, 62\ns_cbranch_scc1 .Ldd_ns671%=\ns_sleep 8\n.Ldd_ns671%=:\ns_sleep 1\nbuffer_load_dword v8, v7, %[rs], 0 offen sc1\ns_waitcnt vmcnt(0)\ns_add_u32 s43, s43, 1\n" \
"s_cmp_lt_u32 s43, 262144\ns_cbranch_scc1 .Ldd_w0%=\ns_branch .Ldd_abort%=\n.Ldd_w0go%=:\ns_add_u32 s60, s55, 0\nbuffer_load_dwordx4 v[48:51], v0, %[rs], s60 offen offset:0 sc1\nbuffer_load_dwordx4 v[52:55], v0, %[rs], s60 offen offset:2048 sc1\ns_add_u32 s60, s55, 4096\n" \
"buffer_load_dwordx4 v[56:59], v0, %[rs], s60 offen offset:0 sc1\nbuffer_load_dwordx4 v[60:63], v0, %[rs], s60 offen offset:2048 sc1\ns_add_u32 s60, s55, 16384\nbuffer_load_dwordx4 v[64:67], v0, %[rs], s60 offen offset:0 sc1\nbuffer_load_dwordx4 v[68:71], v0, %[rs], s60 offen offset:2048 sc1\ns_add_u32 s60, s55, 20480\nbuffer_load_dwordx4 v[72:75], v0, %[rs], s60 offen offset:0 sc1\nbuffer_load_dwordx4 v[76:79], v0, %[rs], s60 offen offset:2048 sc1\n" \
"s_add_u32 s60, s55, 32768\nbuffer_load_dwordx4 v[80:83], v0, %[rs], s60 offen offset:0 sc1\nbuffer_load_dwordx4 v[84:87], v0, %[rs], s60 offen offset:2048 sc1\ns_add_u32 s60, s55, 36864\nbuffer_load_dwordx4 v[88:91], v0, %[rs], s60 offen offset:0 sc1\nbuffer_load_dwordx4 v[92:95], v0, %[rs], s60 offen offset:2048 sc1\ns_add_u32 s60, s55, 49152\nbuffer_load_dwordx4 v[96:99], v0, %[rs], s60 offen offset:0 sc1\n" \
"buffer_load_dwordx4 v[100:103], v0, %[rs], s60 offen offset:2048 sc1\ns_add_u32 s60, s55, 53248\nbuffer_load_dwordx4 v[104:107], v0, %[rs], s60 offen offset:0 sc1\n.Ldd_run%=:\ns_add_u32 s44, s40, 0\ns_lshl_b32 s44, s44, 17\ns_add_u32 s44, s44, %[xn]\ns_lshr_b32 s45, s44, 8\n" \
"s_and_b32 s45, s45, 0x2000\ns_xor_b32 s41, s44, s45\ns_lshl_b32 s44, s40, 16\ns_cmp_eq_u32 %[l], 0\ns_cselect_b32 s42, s41, s44\ns_add_u32 s58, s58, 1\ns_waitcnt vmcnt(14)\nv_mfma_f32_16x16x32_bf16 v[16:19], a[0:3], v[48:51], 0\n" \
"s_waitcnt vmcnt(13)\nds_read_b128 v[120:123], v1 offset:0\nv_mfma_f32_16x16x32_bf16 v[16:19], a[4:7], v[52:55], v[16:19]\nbuffer_load_dword v8, v7, %[rs], 0 offen sc1\nv_mfma_f32_16x16x32_bf16 v[24:27], a[8:11], v[48:51], 0\nv_mfma_f32_16x16x32_bf16 v[24:27], a[12:15], v[52:55], v[24:27]\nbuffer_load_dwordx4 v[108:111], v0, %[rs], s60 offen offset:2048 sc1\nv_mfma_f32_16x16x32_bf16 v[32:35], a[16:19], v[48:51], 0\n" \
"ds_read_b128 v[124:127], v1 offset:1024\nv_mfma_f32_16x16x32_bf16 v[32:35], a[20:23], v[52:55], v[32:35]\nv_mfma_f32_16x16x32_bf16 v[40:43], a[24:27], v[48:51], 0\ns_add_u32 s60, s55, 65536\nv_mfma_f32_16x16x32_bf16 v[40:43], a[28:31], v[52:55], v[40:43]\ns_waitcnt vmcnt(14)\nbuffer_load_dwordx4 v[48:51], v0, %[rs], s60 offen offset:0 sc1\nv_mfma_f32_16x16x32_bf16 v[16:19], a[32:35], v[56:59], v[16:19]\n" \
"s_waitcnt vmcnt(14)\nv_mfma_f32_16x16x32_bf16 v[16:19], a[36:39], v[60:63], v[16:19]\ns_waitcnt lgkmcnt(1)\nv_mfma_f32_16x16x32_bf16 v[24:27], v[120:123], v[56:59], v[24:27]\ns_waitcnt lgkmcnt(0)\nv_mfma_f32_16x16x32_bf16 v[24:27], v[124:127], v[60:63], v[24:27]\nbuffer_load_dwordx4 v[52:55], v0, %[rs], s60 offen offset:2048 sc1\nv_mfma_f32_16x16x32_bf16 v[32:35], a[40:43], v[56:59], v[32:35]\n" \
"v_mfma_f32_16x16x32_bf16 v[32:35], a[44:47], v[60:63], v[32:35]\nv_mfma_f32_16x16x32_bf16 v[40:43], a[48:51], v[56:59], v[40:43]\ns_add_u32 s60, s55, 69632\nv_mfma_f32_16x16x32_bf16 v[40:43], a[52:55], v[60:63], v[40:43]\ns_waitcnt vmcnt(14)\nbuffer_load_dwordx4 v[56:59], v0, %[rs], s60 offen offset:0 sc1\nv_mfma_f32_16x16x32_bf16 v[16:19], a[56:59], v[64:67], v[16:19]\ns_waitcnt vmcnt(14)\n" \
"ds_read_b128 v[120:123], v1 offset:2048\nv_mfma_f32_16x16x32_bf16 v[16:19], a[60:63], v[68:71], v[16:19]\nv_mfma_f32_16x16x32_bf16 v[24:27], a[64:67], v[64:67], v[24:27]\nv_mfma_f32_16x16x32_bf16 v[24:27], a[68:71], v[68:71], v[24:27]\nbuffer_load_dwordx4 v[60:63], v0, %[rs], s60 offen offset:2048 sc1\nv_mfma_f32_16x16x32_bf16 v[32:35], a[72:75], v[64:67], v[32:35]\nds_read_b128 v[124:127], v1 offset:3072\nv_mfma_f32_16x16x32_bf16 v[32:35], a[76:79], v[68:71], v[32:35]\n" \
"v_mfma_f32_16x16x32_bf16 v[40:43], a[80:83], v[64:67], v[40:43]\ns_add_u32 s60, s55, 81920\nv_mfma_f32_16x16x32_bf16 v[40:43], a[84:87], v[68:71], v[40:43]\ns_waitcnt vmcnt(14)\nbuffer_load_dwordx4 v[64:67], v0, %[rs], s60 offen offset:0 sc1\nv_mfma_f32_16x16x32_bf16 v[16:19], a[88:91], v[72:75], v[16:19]\ns_waitcnt vmcnt(14)\nv_mfma_f32_16x16x32_bf16 v[16:19], a[92:95], v[76:79], v[16:19]\n" \
"v_mfma_f32_16x16x32_bf16 v[24:27], a[96:99], v[72:75], v[24:27]\nv_mfma_f32_16x16x32_bf16 v[24:27], a[100:103], v[76:79], v[24:27]\nbuffer_load_dwordx4 v[68:71], v0, %[rs], s60 offen offset:2048 sc1\nv_mfma_f32_16x16x32_bf16 v[32:35], a[104:107], v[72:75], v[32:35]\nv_mfma_f32_16x16x32_bf16 v[32:35], a[108:111], v[76:79], v[32:35]\ns_waitcnt lgkmcnt(1)\nv_mfma_f32_16x16x32_bf16 v[40:43], v[120:123], v[72:75], v[40:43]\ns_waitcnt lgkmcnt(0)\n" \
"s_add_u32 s60, s55, 86016\nv_mfma_f32_16x16x32_bf16 v[40:43], v[124:127], v[76:79], v[40:43]\ns_waitcnt vmcnt(14)\nbuffer_load_dwordx4 v[72:75], v0, %[rs], s60 offen offset:0 sc1\nv_mfma_f32_16x16x32_bf16 v[16:19], a[112:115], v[80:83], v[16:19]\ns_waitcnt vmcnt(14)\nds_read_b128 v[120:123], v1 offset:4096\nv_mfma_f32_16x16x32_bf16 v[16:19], a[116:119], v[84:87], v[16:19]\n" \
"v_mfma_f32_16x16x32_bf16 v[24:27], a[120:123], v[80:83], v[24:27]\nv_mfma_f32_16x16x32_bf16 v[24:27], a[124:127], v[84:87], v[24:27]\nbuffer_load_dwordx4 v[76:79], v0, %[rs], s60 offen offset:2048 sc1\nv_mfma_f32_16x16x32_bf16 v[32:35], a[128:131], v[80:83], v[32:35]\nds_read_b128 v[124:127], v1 offset:5120\nv_mfma_f32_16x16x32_bf16 v[32:35], a[132:135], v[84:87], v[32:35]\nv_mfma_f32_16x16x32_bf16 v[40:43], a[136:139], v[80:83], v[40:43]\ns_add_u32 s60, s55, 98304\n" \
"v_mfma_f32_16x16x32_bf16 v[40:43], a[140:143], v[84:87], v[40:43]\ns_waitcnt vmcnt(14)\nbuffer_load_dwordx4 v[80:83], v0, %[rs], s60 offen offset:0 sc1\nv_mfma_f32_16x16x32_bf16 v[16:19], a[144:147], v[88:91], v[16:19]\ns_waitcnt vmcnt(14)\nds_read_b128 v[112:115], v1 offset:6144\nv_mfma_f32_16x16x32_bf16 v[16:19], a[148:151], v[92:95], v[16:19]\ns_waitcnt lgkmcnt(2)\n" \
"v_mfma_f32_16x16x32_bf16 v[24:27], v[120:123], v[88:91], v[24:27]\ns_waitcnt lgkmcnt(1)\nv_mfma_f32_16x16x32_bf16 v[24:27], v[124:127], v[92:95], v[24:27]\nbuffer_load_dwordx4 v[84:87], v0, %[rs], s60 offen offset:2048 sc1\nv_mfma_f32_16x16x32_bf16 v[32:35], a[152:155], v[88:91], v[32:35]\nds_read_b128 v[116:119], v1 offset:7168\nv_mfma_f32_16x16x32_bf16 v[32:35], a[156:159], v[92:95], v[32:35]\ns_waitcnt vmcnt(11)\n" \
"v_cmp_gt_i32 vcc, s58, v8\nv_mfma_f32_16x16x32_bf16 v[40:43], a[160:163], v[88:91], v[40:43]\ns_cmp_eq_u64 vcc, 0\ns_cselect_b32 s61, s55, -2147483648\nv_mfma_f32_16x16x32_bf16 v[40:43], a[164:167], v[92:95], v[40:43]\ns_add_u32 s60, s55, 102400\nbuffer_load_dwordx4 v[88:91], v0, %[rs], s60 offen offset:0 sc1\nv_mfma_f32_16x16x32_bf16 v[16:19], a[168:171], v[96:99], v[16:19]\n" \
"v_mfma_f32_16x16x32_bf16 v[16:19], a[172:175], v[100:103], v[16:19]\nv_mfma_f32_16x16x32_bf16 v[24:27], a[176:179], v[96:99], v[24:27]\nv_mfma_f32_16x16x32_bf16 v[24:27], a[180:183], v[100:103], v[24:27]\ns_waitcnt lgkmcnt(1)\nbuffer_load_dwordx4 v[92:95], v0, %[rs], s60 offen offset:2048 sc1\nv_mfma_f32_16x16x32_bf16 v[32:35], v[112:115], v[96:99], v[32:35]\ns_waitcnt lgkmcnt(0)\nv_mfma_f32_16x16x32_bf16 v[32:35], v[116:119], v[100:103], v[32:35]\n" \
"v_mfma_f32_16x16x32_bf16 v[40:43], a[184:187], v[96:99], v[40:43]\nv_mfma_f32_16x16x32_bf16 v[40:43], a[188:191], v[100:103], v[40:43]\ns_add_u32 s60, s61, 0\nbuffer_load_dwordx4 v[96:99], v0, %[rs], s60 offen offset:1024 sc1\nv_mfma_f32_16x16x32_bf16 v[16:19], a[192:195], v[104:107], v[16:19]\ns_waitcnt vmcnt(13)\nds_read_b128 v[112:115], v1 offset:8192\nv_mfma_f32_16x16x32_bf16 v[16:19], a[196:199], v[108:111], v[16:19]\n" \
"v_mfma_f32_16x16x32_bf16 v[24:27], a[200:203], v[104:107], v[24:27]\nv_mfma_f32_16x16x32_bf16 v[24:27], a[204:207], v[108:111], v[24:27]\nbuffer_load_dwordx4 v[100:103], v0, %[rs], s60 offen offset:3072 sc1\nv_mfma_f32_16x16x32_bf16 v[32:35], a[208:211], v[104:107], v[32:35]\nds_read_b128 v[116:119], v1 offset:9216\nv_mfma_f32_16x16x32_bf16 v[32:35], a[212:215], v[108:111], v[32:35]\nv_mfma_f32_16x16x32_bf16 v[40:43], a[216:219], v[104:107], v[40:43]\ns_add_u32 s60, s61, 4096\n" \
"v_mfma_f32_16x16x32_bf16 v[40:43], a[220:223], v[108:111], v[40:43]\ns_waitcnt vmcnt(13)\ns_waitcnt lgkmcnt(1)\nbuffer_load_dwordx4 v[104:107], v0, %[rs], s60 offen offset:1024 sc1\nv_mfma_f32_16x16x32_bf16 v[16:19], v[112:115], v[48:51], v[16:19]\ns_waitcnt vmcnt(13)\ns_waitcnt lgkmcnt(0)\nv_mfma_f32_16x16x32_bf16 v[16:19], v[116:119], v[52:55], v[16:19]\n" \
"v_mfma_f32_16x16x32_bf16 v[24:27], a[224:227], v[48:51], v[24:27]\nv_mfma_f32_16x16x32_bf16 v[24:27], a[228:231], v[52:55], v[24:27]\nbuffer_load_dwordx4 v[108:111], v0, %[rs], s60 offen offset:3072 sc1\nv_mfma_f32_16x16x32_bf16 v[32:35], a[232:235], v[48:51], v[32:35]\nv_mfma_f32_16x16x32_bf16 v[32:35], a[236:239], v[52:55], v[32:35]\nv_mfma_f32_16x16x32_bf16 v[40:43], a[240:243], v[48:51], v[40:43]\ns_add_u32 s60, s61, 16384\nv_mfma_f32_16x16x32_bf16 v[40:43], a[244:247], v[52:55], v[40:43]\n" \
"s_waitcnt vmcnt(13)\nbuffer_load_dwordx4 v[48:51], v0, %[rs], s60 offen offset:1024 sc1\nv_mfma_f32_16x16x32_bf16 v[16:19], a[248:251], v[56:59], v[16:19]\ns_waitcnt vmcnt(13)\nds_read_b128 v[112:115], v1 offset:10240\nv_mfma_f32_16x16x32_bf16 v[16:19], a[252:255], v[60:63], v[16:19]\nv_mfma_f32_16x16x32_bf16 v[24:27], v[128:131], v[56:59], v[24:27]\nv_mfma_f32_16x16x32_bf16 v[24:27], v[132:135], v[60:63], v[24:27]\n" \
"buffer_load_dwordx4 v[52:55], v0, %[rs], s60 offen offset:3072 sc1\nv_mfma_f32_16x16x32_bf16 v[32:35], v[136:139], v[56:59], v[32:35]\nds_read_b128 v[116:119], v1 offset:11264\nv_mfma_f32_16x16x32_bf16 v[32:35], v[140:143], v[60:63], v[32:35]\nv_mfma_f32_16x16x32_bf16 v[40:43], v[144:147], v[56:59], v[40:43]\ns_add_u32 s60, s61, 20480\nv_mfma_f32_16x16x32_bf16 v[40:43], v[148:151], v[60:63], v[40:43]\ns_waitcnt vmcnt(13)\n" \
"buffer_load_dwordx4 v[56:59], v0, %[rs], s60 offen offset:1024 sc1\nv_mfma_f32_16x16x32_bf16 v[16:19], v[152:155], v[64:67], v[16:19]\ns_waitcnt vmcnt(13)\nv_mfma_f32_16x16x32_bf16 v[16:19], v[156:159], v[68:71], v[16:19]\nv_mfma_f32_16x16x32_bf16 v[24:27], v[160:163], v[64:67], v[24:27]\nv_mfma_f32_16x16x32_bf16 v[24:27], v[164:167], v[68:71], v[24:27]\ns_waitcnt lgkmcnt(1)\nbuffer_load_dwordx4 v[60:63], v0, %[rs], s60 offen offset:3072 sc1\n" \
"v_mfma_f32_16x16x32_bf16 v[32:35], v[112:115], v[64:67], v[32:35]\ns_waitcnt lgkmcnt(0)\nv_mfma_f32_16x16x32_bf16 v[32:35], v[116:119], v[68:71], v[32:35]\nv_mfma_f32_16x16x32_bf16 v[40:43], v[168:171], v[64:67], v[40:43]\ns_add_u32 s60, s61, 32768\nv_mfma_f32_16x16x32_bf16 v[40:43], v[172:175], v[68:71], v[40:43]\ns_waitcnt vmcnt(13)\nbuffer_load_dwordx4 v[64:67], v0, %[rs], s60 offen offset:1024 sc1\n" \
"v_mfma_f32_16x16x32_bf16 v[16:19], v[176:179], v[72:75], v[16:19]\ns_waitcnt vmcnt(13)\nds_read_b128 v[112:115], v1 offset:12288\nv_mfma_f32_16x16x32_bf16 v[16:19], v[180:183], v[76:79], v[16:19]\nv_mfma_f32_16x16x32_bf16 v[24:27], v[184:187], v[72:75], v[24:27]\nv_mfma_f32_16x16x32_bf16 v[24:27], v[188:191], v[76:79], v[24:27]\nbuffer_load_dwordx4 v[68:71], v0, %[rs], s60 offen offset:3072 sc1\nv_mfma_f32_16x16x32_bf16 v[32:35], v[192:195], v[72:75], v[32:35]\n" \
"ds_read_b128 v[116:119], v1 offset:13312\nv_mfma_f32_16x16x32_bf16 v[32:35], v[196:199], v[76:79], v[32:35]\nv_mfma_f32_16x16x32_bf16 v[40:43], v[200:203], v[72:75], v[40:43]\ns_add_u32 s60, s61, 36864\nv_mfma_f32_16x16x32_bf16 v[40:43], v[204:207], v[76:79], v[40:43]\ns_waitcnt vmcnt(13)\ns_waitcnt lgkmcnt(1)\nbuffer_load_dwordx4 v[72:75], v0, %[rs], s60 offen offset:1024 sc1\n" \
"v_mfma_f32_16x16x32_bf16 v[16:19], v[112:115], v[80:83], v[16:19]\ns_waitcnt vmcnt(13)\ns_waitcnt lgkmcnt(0)\nds_read_b128 v[120:123], v1 offset:14336\nv_mfma_f32_16x16x32_bf16 v[16:19], v[116:119], v[84:87], v[16:19]\nv_mfma_f32_16x16x32_bf16 v[24:27], v[208:211], v[80:83], v[24:27]\nv_mfma_f32_16x16x32_bf16 v[24:27], v[212:215], v[84:87], v[24:27]\nbuffer_load_dwordx4 v[76:79], v0, %[rs], s60 offen offset:3072 sc1\n" \
"v_mfma_f32_16x16x32_bf16 v[32:35], v[216:219], v[80:83], v[32:35]\nds_read_b128 v[124:127], v1 offset:15360\nv_mfma_f32_16x16x32_bf16 v[32:35], v[220:223], v[84:87], v[32:35]\nv_mfma_f32_16x16x32_bf16 v[40:43], v[224:227], v[80:83], v[40:43]\ns_add_u32 s60, s61, 49152\nv_mfma_f32_16x16x32_bf16 v[40:43], v[228:231], v[84:87], v[40:43]\ns_waitcnt vmcnt(13)\nbuffer_load_dwordx4 v[80:83], v0, %[rs], s60 offen offset:1024 sc1\n" \
"v_mfma_f32_16x16x32_bf16 v[16:19], v[232:235], v[88:91], v[16:19]\ns_waitcnt vmcnt(13)\nv_mfma_f32_16x16x32_bf16 v[16:19], v[236:239], v[92:95], v[16:19]\ns_waitcnt lgkmcnt(1)\nv_mfma_f32_16x16x32_bf16 v[24:27], v[120:123], v[88:91], v[24:27]\ns_waitcnt lgkmcnt(0)\nv_mfma_f32_16x16x32_bf16 v[24:27], v[124:127], v[92:95], v[24:27]\nbuffer_load_dwordx4 v[84:87], v0, %[rs], s60 offen offset:3072 sc1\n" \
"v_mfma_f32_16x16x32_bf16 v[32:35], v[240:243], v[88:91], v[32:35]\nv_mfma_f32_16x16x32_bf16 v[32:35], v[244:247], v[92:95], v[32:35]\nv_mfma_f32_16x16x32_bf16 v[40:43], v[248:251], v[88:91], v[40:43]\nv_mfma_f32_16x16x32_bf16 v[40:43], v[252:255], v[92:95], v[40:43]\ns_add_u32 s60, s61, 53248\nbuffer_load_dwordx4 v[88:91], v0, %[rs], s60 offen offset:1024 sc1\ns_cbranch_vccnz .Ldd_slow%=\n.Ldd_slowr%=:\n" \
"s_waitcnt vmcnt(14)\nv_mfma_f32_16x16x32_bf16 v[20:23], a[0:3], v[96:99], 0\ns_waitcnt vmcnt(13)\nds_read_b128 v[120:123], v1 offset:0\nv_mfma_f32_16x16x32_bf16 v[20:23], a[4:7], v[100:103], v[20:23]\nv_mfma_f32_16x16x32_bf16 v[28:31], a[8:11], v[96:99], 0\nv_mfma_f32_16x16x32_bf16 v[28:31], a[12:15], v[100:103], v[28:31]\nbuffer_load_dwordx4 v[92:95], v0, %[rs], s60 offen offset:3072 sc1\n" \
"v_mfma_f32_16x16x32_bf16 v[36:39], a[16:19], v[96:99], 0\nds_read_b128 v[124:127], v1 offset:1024\nv_mfma_f32_16x16x32_bf16 v[36:39], a[20:23], v[100:103], v[36:39]\nv_mfma_f32_16x16x32_bf16 v[44:47], a[24:27], v[96:99], 0\ns_add_u32 s60, s55, 65536\nv_mfma_f32_16x16x32_bf16 v[44:47], a[28:31], v[100:103], v[44:47]\ns_waitcnt vmcnt(13)\nbuffer_load_dwordx4 v[96:99], v0, %[rs], s60 offen offset:1024 sc1\n" \
"v_mfma_f32_16x16x32_bf16 v[20:23], a[32:35], v[104:107], v[20:23]\ns_waitcnt vmcnt(13)\nv_mfma_f32_16x16x32_bf16 v[20:23], a[36:39], v[108:111], v[20:23]\ns_waitcnt lgkmcnt(1)\nds_write_b128 v2, v[16:19] offset:0\nv_mfma_f32_16x16x32_bf16 v[28:31], v[120:123], v[104:107], v[28:31]\ns_waitcnt lgkmcnt(1)\nv_mfma_f32_16x16x32_bf16 v[28:31], v[124:127], v[108:111], v[28:31]\n" \
"buffer_load_dwordx4 v[100:103], v0, %[rs], s60 offen offset:3072 sc1\nv_mfma_f32_16x16x32_bf16 v[36:39], a[40:43], v[104:107], v[36:39]\nv_mfma_f32_16x16x32_bf16 v[36:39], a[44:47], v[108:111], v[36:39]\nv_mfma_f32_16x16x32_bf16 v[44:47], a[48:51], v[104:107], v[44:47]\ns_add_u32 s60, s55, 69632\nv_mfma_f32_16x16x32_bf16 v[44:47], a[52:55], v[108:111], v[44:47]\ns_waitcnt vmcnt(13)\nbuffer_load_dwordx4 v[104:107], v0, %[rs], s60 offen offset:1024 sc1\n" \
"v_mfma_f32_16x16x32_bf16 v[20:23], a[56:59], v[48:51], v[20:23]\ns_waitcnt vmcnt(13)\nds_read_b128 v[120:123], v1 offset:2048\nv_mfma_f32_16x16x32_bf16 v[20:23], a[60:63], v[52:55], v[20:23]\nds_write_b128 v2, v[24:27] offset:2048\nv_mfma_f32_16x16x32_bf16 v[28:31], a[64:67], v[48:51], v[28:31]\nv_mfma_f32_16x16x32_bf16 v[28:31], a[68:71], v[52:55], v[28:31]\nbuffer_load_dwordx4 v[108:111], v0, %[rs], s60 offen offset:3072 sc1\n" \
"v_mfma_f32_16x16x32_bf16 v[36:39], a[72:75], v[48:51], v[36:39]\nds_read_b128 v[124:127], v1 offset:3072\nv_mfma_f32_16x16x32_bf16 v[36:39], a[76:79], v[52:55], v[36:39]\nv_mfma_f32_16x16x32_bf16 v[44:47], a[80:83], v[48:51], v[44:47]\ns_add_u32 s60, s55, 81920\nv_mfma_f32_16x16x32_bf16 v[44:47], a[84:87], v[52:55], v[44:47]\ns_waitcnt vmcnt(13)\nbuffer_load_dwordx4 v[48:51], v0, %[rs], s60 offen offset:1024 sc1\n" \
"v_mfma_f32_16x16x32_bf16 v[20:23], a[88:91], v[56:59], v[20:23]\ns_waitcnt vmcnt(13)\nv_mfma_f32_16x16x32_bf16 v[20:23], a[92:95], v[60:63], v[20:23]\nds_write_b128 v2, v[32:35] offset:4096\nv_mfma_f32_16x16x32_bf16 v[28:31], a[96:99], v[56:59], v[28:31]\nv_mfma_f32_16x16x32_bf16 v[28:31], a[100:103], v[60:63], v[28:31]\nbuffer_load_dwordx4 v[52:55], v0, %[rs], s60 offen offset:3072 sc1\nv_mfma_f32_16x16x32_bf16 v[36:39], a[104:107], v[56:59], v[36:39]\n" \
"v_mfma_f32_16x16x32_bf16 v[36:39], a[108:111], v[60:63], v[36:39]\ns_waitcnt lgkmcnt(3)\nv_mfma_f32_16x16x32_bf16 v[44:47], v[120:123], v[56:59], v[44:47]\ns_waitcnt lgkmcnt(1)\ns_add_u32 s60, s55, 86016\nv_mfma_f32_16x16x32_bf16 v[44:47], v[124:127], v[60:63], v[44:47]\ns_waitcnt vmcnt(13)\nbuffer_load_dwordx4 v[56:59], v0, %[rs], s60 offen offset:1024 sc1\n" \
"v_mfma_f32_16x16x32_bf16 v[20:23], a[112:115], v[64:67], v[20:23]\ns_waitcnt vmcnt(13)\nds_read_b128 v[120:123], v1 offset:4096\nv_mfma_f32_16x16x32_bf16 v[20:23], a[116:119], v[68:71], v[20:23]\nds_write_b128 v2, v[40:43] offset:6144\nv_mfma_f32_16x16x32_bf16 v[28:31], a[120:123], v[64:67], v[28:31]\nv_mfma_f32_16x16x32_bf16 v[28:31], a[124:127], v[68:71], v[28:31]\nbuffer_load_dwordx4 v[60:63], v0, %[rs], s60 offen offset:3072 sc1\n" \
"v_mfma_f32_16x16x32_bf16 v[36:39], a[128:131], v[64:67], v[36:39]\nds_read_b128 v[124:127], v1 offset:5120\nv_mfma_f32_16x16x32_bf16 v[36:39], a[132:135], v[68:71], v[36:39]\nv_mfma_f32_16x16x32_bf16 v[44:47], a[136:139], v[64:67], v[44:47]\ns_add_u32 s60, s55, 98304\nv_mfma_f32_16x16x32_bf16 v[44:47], a[140:143], v[68:71], v[44:47]\ns_waitcnt vmcnt(13)\nbuffer_load_dwordx4 v[64:67], v0, %[rs], s60 offen offset:1024 sc1\n" \
"v_mfma_f32_16x16x32_bf16 v[20:23], a[144:147], v[72:75], v[20:23]\ns_waitcnt vmcnt(13)\nds_read_b128 v[112:115], v1 offset:6144\nv_mfma_f32_16x16x32_bf16 v[20:23], a[148:151], v[76:79], v[20:23]\ns_waitcnt lgkmcnt(3)\nv_mfma_f32_16x16x32_bf16 v[28:31], v[120:123], v[72:75], v[28:31]\ns_waitcnt lgkmcnt(1)\nv_mfma_f32_16x16x32_bf16 v[28:31], v[124:127], v[76:79], v[28:31]\n" \
"buffer_load_dwordx4 v[68:71], v0, %[rs], s60 offen offset:3072 sc1\nv_mfma_f32_16x16x32_bf16 v[36:39], a[152:155], v[72:75], v[36:39]\nds_read_b128 v[116:119], v1 offset:7168\nv_mfma_f32_16x16x32_bf16 v[36:39], a[156:159], v[76:79], v[36:39]\nbuffer_load_dwordx4 v[12:15], v4, %[rs], s41 offen sc1\nv_mfma_f32_16x16x32_bf16 v[44:47], a[160:163], v[72:75], v[44:47]\ns_add_u32 s60, s55, 102400\nv_mfma_f32_16x16x32_bf16 v[44:47], a[164:167], v[76:79], v[44:47]\n" \
"s_waitcnt vmcnt(14)\nbuffer_load_dwordx4 v[72:75], v0, %[rs], s60 offen offset:1024 sc1\nv_mfma_f32_16x16x32_bf16 v[20:23], a[168:171], v[80:83], v[20:23]\ns_waitcnt vmcnt(14)\nv_mfma_f32_16x16x32_bf16 v[20:23], a[172:175], v[84:87], v[20:23]\nv_mfma_f32_16x16x32_bf16 v[28:31], a[176:179], v[80:83], v[28:31]\nv_mfma_f32_16x16x32_bf16 v[28:31], a[180:183], v[84:87], v[28:31]\ns_waitcnt lgkmcnt(1)\n" \
"buffer_load_dwordx4 v[76:79], v0, %[rs], s60 offen offset:3072 sc1\nv_mfma_f32_16x16x32_bf16 v[36:39], v[112:115], v[80:83], v[36:39]\ns_waitcnt lgkmcnt(0)\nv_mfma_f32_16x16x32_bf16 v[36:39], v[116:119], v[84:87], v[36:39]\nv_mfma_f32_16x16x32_bf16 v[44:47], a[184:187], v[80:83], v[44:47]\nv_mfma_f32_16x16x32_bf16 v[44:47], a[188:191], v[84:87], v[44:47]\ns_waitcnt vmcnt(14)\nv_mfma_f32_16x16x32_bf16 v[20:23], a[192:195], v[88:91], v[20:23]\n" \
"s_waitcnt vmcnt(13)\nds_read_b128 v[112:115], v1 offset:8192\nv_mfma_f32_16x16x32_bf16 v[20:23], a[196:199], v[92:95], v[20:23]\nv_mfma_f32_16x16x32_bf16 v[28:31], a[200:203], v[88:91], v[28:31]\nv_mfma_f32_16x16x32_bf16 v[28:31], a[204:207], v[92:95], v[28:31]\nv_mfma_f32_16x16x32_bf16 v[36:39], a[208:211], v[88:91], v[36:39]\nds_read_b128 v[116:119], v1 offset:9216\nv_mfma_f32_16x16x32_bf16 v[36:39], a[212:215], v[92:95], v[36:39]\n" \
"v_mfma_f32_16x16x32_bf16 v[44:47], a[216:219], v[88:91], v[44:47]\nv_mfma_f32_16x16x32_bf16 v[44:47], a[220:223], v[92:95], v[44:47]\ns_waitcnt vmcnt(12)\ns_waitcnt lgkmcnt(1)\nv_mfma_f32_16x16x32_bf16 v[20:23], v[112:115], v[96:99], v[20:23]\ns_waitcnt vmcnt(11)\ns_waitcnt lgkmcnt(0)\nv_mfma_f32_16x16x32_bf16 v[20:23], v[116:119], v[100:103], v[20:23]\n" \
"v_mfma_f32_16x16x32_bf16 v[28:31], a[224:227], v[96:99], v[28:31]\nv_mfma_f32_16x16x32_bf16 v[28:31], a[228:231], v[100:103], v[28:31]\nv_mfma_f32_16x16x32_bf16 v[36:39], a[232:235], v[96:99], v[36:39]\nv_mfma_f32_16x16x32_bf16 v[36:39], a[236:239], v[100:103], v[36:39]\nv_mfma_f32_16x16x32_bf16 v[44:47], a[240:243], v[96:99], v[44:47]\nv_mfma_f32_16x16x32_bf16 v[44:47], a[244:247], v[100:103], v[44:47]\ns_waitcnt vmcnt(10)\nv_mfma_f32_16x16x32_bf16 v[20:23], a[248:251], v[104:107], v[20:23]\n" \
"s_waitcnt vmcnt(9)\nds_read_b128 v[112:115], v1 offset:10240\nv_mfma_f32_16x16x32_bf16 v[20:23], a[252:255], v[108:111], v[20:23]\nv_mfma_f32_16x16x32_bf16 v[28:31], v[128:131], v[104:107], v[28:31]\nv_mfma_f32_16x16x32_bf16 v[28:31], v[132:135], v[108:111], v[28:31]\nv_mfma_f32_16x16x32_bf16 v[36:39], v[136:139], v[104:107], v[36:39]\nds_read_b128 v[116:119], v1 offset:11264\nv_mfma_f32_16x16x32_bf16 v[36:39], v[140:143], v[108:111], v[36:39]\n" \
"v_mfma_f32_16x16x32_bf16 v[44:47], v[144:147], v[104:107], v[44:47]\nv_mfma_f32_16x16x32_bf16 v[44:47], v[148:151], v[108:111], v[44:47]\ns_waitcnt vmcnt(8)\nv_mfma_f32_16x16x32_bf16 v[20:23], v[152:155], v[48:51], v[20:23]\ns_waitcnt vmcnt(7)\nv_mfma_f32_16x16x32_bf16 v[20:23], v[156:159], v[52:55], v[20:23]\nv_mfma_f32_16x16x32_bf16 v[28:31], v[160:163], v[48:51], v[28:31]\nv_mfma_f32_16x16x32_bf16 v[28:31], v[164:167], v[52:55], v[28:31]\n" \
"s_waitcnt lgkmcnt(1)\nv_mfma_f32_16x16x32_bf16 v[36:39], v[112:115], v[48:51], v[36:39]\ns_waitcnt lgkmcnt(0)\nv_mfma_f32_16x16x32_bf16 v[36:39], v[116:119], v[52:55], v[36:39]\nv_mfma_f32_16x16x32_bf16 v[44:47], v[168:171], v[48:51], v[44:47]\nv_mfma_f32_16x16x32_bf16 v[44:47], v[172:175], v[52:55], v[44:47]\ns_waitcnt vmcnt(6)\nv_mfma_f32_16x16x32_bf16 v[20:23], v[176:179], v[56:59], v[20:23]\n" \
"s_waitcnt vmcnt(5)\nds_read_b128 v[112:115], v1 offset:12288\nv_mfma_f32_16x16x32_bf16 v[20:23], v[180:183], v[60:63], v[20:23]\nv_mfma_f32_16x16x32_bf16 v[28:31], v[184:187], v[56:59], v[28:31]\nv_mfma_f32_16x16x32_bf16 v[28:31], v[188:191], v[60:63], v[28:31]\nv_mfma_f32_16x16x32_bf16 v[36:39], v[192:195], v[56:59], v[36:39]\nds_read_b128 v[116:119], v1 offset:13312\nv_mfma_f32_16x16x32_bf16 v[36:39], v[196:199], v[60:63], v[36:39]\n" \
"v_mfma_f32_16x16x32_bf16 v[44:47], v[200:203], v[56:59], v[44:47]\nv_mfma_f32_16x16x32_bf16 v[44:47], v[204:207], v[60:63], v[44:47]\ns_waitcnt vmcnt(4)\ns_waitcnt lgkmcnt(1)\nv_mfma_f32_16x16x32_bf16 v[20:23], v[112:115], v[64:67], v[20:23]\ns_waitcnt vmcnt(3)\ns_waitcnt lgkmcnt(0)\nds_read_b128 v[120:123], v1 offset:14336\n" \
"v_mfma_f32_16x16x32_bf16 v[20:23], v[116:119], v[68:71], v[20:23]\nv_mfma_f32_16x16x32_bf16 v[28:31], v[208:211], v[64:67], v[28:31]\nv_mfma_f32_16x16x32_bf16 v[28:31], v[212:215], v[68:71], v[28:31]\nv_mfma_f32_16x16x32_bf16 v[36:39], v[216:219], v[64:67], v[36:39]\nds_read_b128 v[124:127], v1 offset:15360\nv_mfma_f32_16x16x32_bf16 v[36:39], v[220:223], v[68:71], v[36:39]\nv_mfma_f32_16x16x32_bf16 v[44:47], v[224:227], v[64:67], v[44:47]\nv_mfma_f32_16x16x32_bf16 v[44:47], v[228:231], v[68:71], v[44:47]\n" \
"s_waitcnt vmcnt(1)\nv_mfma_f32_16x16x32_bf16 v[20:23], v[232:235], v[72:75], v[20:23]\ns_waitcnt vmcnt(0)\nv_mfma_f32_16x16x32_bf16 v[20:23], v[236:239], v[76:79], v[20:23]\ns_waitcnt lgkmcnt(1)\nv_mfma_f32_16x16x32_bf16 v[28:31], v[120:123], v[72:75], v[28:31]\ns_waitcnt lgkmcnt(0)\nv_mfma_f32_16x16x32_bf16 v[28:31], v[124:127], v[76:79], v[28:31]\n" \
"v_mfma_f32_16x16x32_bf16 v[36:39], v[240:243], v[72:75], v[36:39]\nv_lshlrev_b32 v80, 16, v12\nv_and_b32 v81, 0xffff0000, v12\nv_mfma_f32_16x16x32_bf16 v[36:39], v[244:247], v[76:79], v[36:39]\nv_lshlrev_b32 v82, 16, v13\nv_and_b32 v83, 0xffff0000, v13\nv_mfma_f32_16x16x32_bf16 v[44:47], v[248:251], v[72:75], v[44:47]\nv_lshlrev_b32 v84, 16, v14\n" \
"v_and_b32 v85, 0xffff0000, v14\nv_mfma_f32_16x16x32_bf16 v[44:47], v[252:255], v[76:79], v[44:47]\ns_mul_i32 s44, s40, %[l]\nds_write_b128 v2, v[20:23] offset:1024\nds_write_b128 v2, v[28:31] offset:3072\nds_write_b128 v2, v[36:39] offset:5120\ns_nop 3\nds_write_b128 v2, v[44:47] offset:7168\n" \
"v_lshlrev_b32 v86, 16, v15\nv_and_b32 v87, 0xffff0000, v15\nv_mov_b32 v9, s40\ns_cmp_eq_u32 s44, 0\ns_cselect_b32 s56, %[fd], -2147483648\ns_add_u32 s40, s40, 1\ns_waitcnt lgkmcnt(0)\ns_barrier\n" \
"ds_read_b128 v[16:19], v3 offset:0\nds_read_b128 v[20:23], v3 offset:256\nds_read_b128 v[24:27], v3 offset:8192\nds_read_b128 v[28:31], v3 offset:8448\nds_read_b128 v[32:35], v3 offset:16384\nds_read_b128 v[36:39], v3 offset:16640\nds_read_b128 v[40:43], v3 offset:24576\nds_read_b128 v[44:47], v3 offset:24832\n" \
"s_waitcnt lgkmcnt(6)\nv_pk_add_f32 v[16:17], v[16:17], v[80:81]\nv_pk_add_f32 v[18:19], v[18:19], v[82:83]\nv_pk_add_f32 v[20:21], v[20:21], v[84:85]\nv_pk_add_f32 v[22:23], v[22:23], v[86:87]\ns_waitcnt lgkmcnt(4)\nv_pk_add_f32 v[16:17], v[16:17], v[24:25]\nv_pk_add_f32 v[18:19], v[18:19], v[26:27]\n" \
"v_pk_add_f32 v[20:21], v[20:21], v[28:29]\nv_pk_add_f32 v[22:23], v[22:23], v[30:31]\ns_waitcnt lgkmcnt(2)\nv_pk_add_f32 v[16:17], v[16:17], v[32:33]\nv_pk_add_f32 v[18:19], v[18:19], v[34:35]\nv_pk_add_f32 v[20:21], v[20:21], v[36:37]\nv_pk_add_f32 v[22:23], v[22:23], v[38:39]\ns_waitcnt lgkmcnt(0)\n" \
"v_pk_add_f32 v[16:17], v[16:17], v[40:41]\nv_pk_add_f32 v[18:19], v[18:19], v[42:43]\nv_pk_add_f32 v[20:21], v[20:21], v[44:45]\nv_pk_add_f32 v[22:23], v[22:23], v[46:47]\nv_cvt_pk_bf16_f32 v12, v16, v17\nv_cvt_pk_bf16_f32 v13, v18, v19\nv_cvt_pk_bf16_f32 v14, v20, v21\nv_cvt_pk_bf16_f32 v15, v22, v23\n" \
"buffer_store_dwordx4 v[12:15], v5, %[ro], s42 offen sc1\nv_pk_add_f32 v[24:25], v[16:17], v[18:19]\nv_pk_add_f32 v[26:27], v[20:21], v[22:23]\nv_pk_add_f32 v[24:25], v[24:25], v[26:27]\nv_add_f32 v24, v24, v25\ns_nop 1\nv_add_f32_dpp v25, v24, v24 row_ror:8 row_mask:0xf bank_mask:0xf\nv_add_f32_dpp v26, v24, v24 row_ror:8 row_mask:0xf bank_mask:0xf\n" \
"s_nop 1\nv_permlane16_swap_b32 v25, v26\nv_add_f32 v24, v25, v26\nv_add_f32 v25, v25, v26\ns_nop 1\nv_permlane32_swap_b32 v24, v25\nv_add_f32 v24, v24, v25\nv_mul_f32 v24, 0x3c800000, v24\n" \
"v_pk_add_f32 v[16:17], v[16:17], v[24:25] op_sel_hi:[1,0] neg_lo:[0,1] neg_hi:[0,1]\nv_pk_add_f32 v[18:19], v[18:19], v[24:25] op_sel_hi:[1,0] neg_lo:[0,1] neg_hi:[0,1]\nv_pk_add_f32 v[20:21], v[20:21], v[24:25] op_sel_hi:[1,0] neg_lo:[0,1] neg_hi:[0,1]\nv_pk_add_f32 v[22:23], v[22:23], v[24:25] op_sel_hi:[1,0] neg_lo:[0,1] neg_hi:[0,1]\nv_pk_mul_f32 v[28:29], v[16:17], v[16:17]\nv_pk_mul_f32 v[30:31], v[18:19], v[18:19]\nv_pk_fma_f32 v[28:29], v[20:21], v[20:21], v[28:29]\nv_pk_fma_f32 v[30:31], v[22:23], v[22:23], v[30:31]\n" \
"v_pk_add_f32 v[28:29], v[28:29], v[30:31]\nv_add_f32 v28, v28, v29\ns_nop 1\nv_add_f32_dpp v29, v28, v28 row_ror:8 row_mask:0xf bank_mask:0xf\nv_add_f32_dpp v30, v28, v28 row_ror:8 row_mask:0xf bank_mask:0xf\ns_nop 1\nv_permlane16_swap_b32 v29, v30\nv_add_f32 v28, v29, v30\n" \
"v_add_f32 v29, v29, v30\ns_nop 1\nv_permlane32_swap_b32 v28, v29\nv_add_f32 v28, v28, v29\nv_mov_b32 v25, v28\nbuffer_store_dwordx2 v[24:25], v6, %[rs], s41 offen sc1\nv_xor_b32 v2, 0x8000, v2\nv_xor_b32 v3, 0x8000, v3\n" \
"s_cmp_lt_u32 s40, 64\ns_cbranch_scc1 .Ldd_next%=\ns_waitcnt vmcnt(0)\nbuffer_store_dword v9, off, %[rs], s56 sc1\ns_branch .Ldd_end%=\n.Ldd_slow%=:\n.Ldd_w1%=:\nv_cmp_gt_i32 vcc, s58, v8\n" \
"s_cbranch_vccz .Ldd_w1go%=\ns_cmp_ge_u32 s40, 62\ns_cbranch_scc1 .Ldd_ns1225%=\ns_sleep 8\n.Ldd_ns1225%=:\ns_sleep 1\nbuffer_load_dword v8, v7, %[rs], 0 offen sc1\ns_waitcnt vmcnt(0)\n" \
"s_add_u32 s43, s43, 1\ns_cmp_lt_u32 s43, 262144\ns_cbranch_scc1 .Ldd_w1%=\ns_branch .Ldd_abort%=\n.Ldd_w1go%=:\ns_add_u32 s60, s55, 0\nbuffer_load_dwordx4 v[96:99], v0, %[rs], s60 offen offset:1024 sc1\nbuffer_load_dwordx4 v[100:103], v0, %[rs], s60 offen offset:3072 sc1\n" \
"s_add_u32 s60, s55, 4096\nbuffer_load_dwordx4 v[104:107], v0, %[rs], s60 offen offset:1024 sc1\nbuffer_load_dwordx4 v[108:111], v0, %[rs], s60 offen offset:3072 sc1\ns_add_u32 s60, s55, 16384\nbuffer_load_dwordx4 v[48:51], v0, %[rs], s60 offen offset:1024 sc1\nbuffer_load_dwordx4 v[52:55], v0, %[rs], s60 offen offset:3072 sc1\ns_add_u32 s60, s55, 20480\nbuffer_load_dwordx4 v[56:59], v0, %[rs], s60 offen offset:1024 sc1\n" \
"buffer_load_dwordx4 v[60:63], v0, %[rs], s60 offen offset:3072 sc1\ns_add_u32 s60, s55, 32768\nbuffer_load_dwordx4 v[64:67], v0, %[rs], s60 offen offset:1024 sc1\nbuffer_load_dwordx4 v[68:71], v0, %[rs], s60 offen offset:3072 sc1\ns_add_u32 s60, s55, 36864\nbuffer_load_dwordx4 v[72:75], v0, %[rs], s60 offen offset:1024 sc1\nbuffer_load_dwordx4 v[76:79], v0, %[rs], s60 offen offset:3072 sc1\ns_add_u32 s60, s55, 49152\n" \
"buffer_load_dwordx4 v[80:83], v0, %[rs], s60 offen offset:1024 sc1\nbuffer_load_dwordx4 v[84:87], v0, %[rs], s60 offen offset:3072 sc1\ns_add_u32 s60, s55, 53248\nbuffer_load_dwordx4 v[88:91], v0, %[rs], s60 offen offset:1024 sc1\ns_branch .Ldd_slowr%=\n.Ldd_abort%=:\ns_mov_b64 exec, -1\nv_mov_b32 v16, 0\n" \
"s_movk_i32 s44, 6400\nbuffer_store_dword v16, off, %[rs], s44 sc1\n.Ldd_end%=:\ns_waitcnt vmcnt(0) lgkmcnt(0)\n" \
""
#define GSW 0
#define PX4 1
#define QX4 7
#define G1F 1
#define A1F 0

#define V10(p, x) p #x "0", p #x "1", p #x "2", p #x "3", p #x "4", p #x "5", p #x "6", p #x "7", p #x "8", p #x "9"
#define V256(p) V10(p, ), V10(p, 1), V10(p, 2), V10(p, 3), V10(p, 4), V10(p, 5), V10(p, 6), V10(p, 7), V10(p, 8), V10(p, 9), V10(p, 10), V10(p, 11), V10(p, 12), V10(p, 13), V10(p, 14), V10(p, 15), V10(p, 16), \
    V10(p, 17), V10(p, 18), V10(p, 19), V10(p, 20), V10(p, 21), V10(p, 22), V10(p, 23), V10(p, 24), p "250", p "251", p "252", p "253", p "254", p "255"
#define CLOB "memory", "vcc", "scc", V256("v"), V256("a"), V10("s", 4), V10("s", 5), V10("s", 6), V10("s", 7), V10("s", 8)
template <int N> DEVI void table(lds_char* p, const unsigned (&t)[N]) {
#pragma unroll
    for (int i = 0; i < N; ++i) *(l1*)(p + i * 256) = t[i];
    WL0;
}
DEVI unsigned prof_at(int cu, int w) { return O_PROF + (262144 + cu * 2560 + w * 8) * 4; }
DEVI unsigned sp_idx(int lane) { return (lane & 2) ? 0xEEEEu : 0x4444u; }
DEVI int sp_row(int r, int hf) { return 4 * (r >> 1) + 2 * hf + (r & 1); }

DEVI void qk_role(const Args& a, const int w, lds_char* smem, int l, int h) {
    const int lane = lane_id(), g = lane >> 4, n = lane & 15, b = n & 3, p = n >> 2;
    const unsigned l0 = (unsigned)(size_t)smem, tb = l0 + w * 12288, lw = l0 + 55296 + w * 24576, ls = l * SD, IMG = l0 + 32768, MIM = l0 + 49152;
    const unsigned xw = (w >> 1) * 16384 + (w & 1) * 4096, oc = (h >> 3) * 16384 + ls, tr = (8 * g + p) * 128 + b * 16;
    const unsigned cm[4] = {4 * g > n ? 0xF149F2CAu : 0u, 4 * g + 1 > n ? 0xF149F2CAu : 0u, 4 * g + 2 > n ? 0xF149F2CAu : 0u, 4 * g + 3 > n ? 0xF149F2CAu : 0u};
    const unsigned t[19] = {
        l0 + lane * 16, lw + lane * 16, l0 + (g >> 1) * 1024 + (g & 1) * 512 + n * 16, O_E1 + ls + xw + lane * 16, sp_idx(lane),
        IMG + (w >> 1) * 8192 + ((w & 1) * 8 + 2 * g) * 512 + n * 16,
        MIM + (w * 8 + 2 * p) * 144 + g * 32 + b * 4,
        IMG + g * 512 + (w ? n * 16 : 8192 + (16 * (b >> 1) + 8 * (p >> 1) + 2 * (p & 1) + (b & 1)) * 16),
        MIM + g * 144 + (n & 7) * 16, MIM + 2304 + g * 144 + (2 * p + (b & 1)) * 16,
        w ? O_PT + (h >> 3) * 32768 + ls + lane * 16 + (w == 1 ? 16384 + ((h & 7) >> 1) * 1024 + (h & 1) * 8 : (h & 7) * 1024)
          : (QX4 & 1) && n >> 3 ? OOB : O_PM + oc + ((h >> 1) & 3) * 1024 + ((2 * (h & 1) + (g >> 1)) * 16 + sp_row(n & 7, g & 1)) * 16 + (n >> 3) * 8,
        w ? (QX4 & 4) && g ? OOB : O_PT + (h >> 3) * 32768 + ls + 20480 + ((h >> 2) & 1) * 2048 + (w - 1) * 1024 + (h & 3) * 256 + n * 16 + g * 4
          : n >> 3 || ((QX4 & 2) && g >> 1) ? OOB : O_PM + oc + 4096 + ((h & 7) >> 1) * 256 + sp_row(n & 7, g & 1) * 16 + (h & 1) * 8 + (g >> 1) * 4,
        w == 3 ? (l ? F_X + SD + p * 128 + (X1F ? 0 : b * 4) : F_X0 + b * 4) : cm[0], cm[1], cm[2], cm[3],
        (lane >> 3) * 8192u + (lane & 7) * 16, tb + tr, lw + tr};
    table(smem + w * 12288 + lane * 4, t);
    asm volatile(QK_ASM : : [ws] "s"(a.ws), [sz] "n"(WS_RS), [rw] "s"(mkrs(w < 2 ? a.Wq : a.Wk, 8u << 20)), [wo] "s"(h * 256 + (w & 1) * 128), [tb] "s"(tb), [lw] "s"(lw), [l0] "s"(l0), [w] "s"(w),
        [xn] "s"(O_XN + ls), [xw] "s"(xw), [fs] "s"(l ? 0 : 128), [sg] "s"(F_Y0 + 4096 + ls + h * 128 + 16), [pr] "s"(prof_at(l * 80 + h, w)) : CLOB, "s36", "s37", "s38", "s39");
    __builtin_amdgcn_endpgm();
}
DEVI void vu_role(const Args& a, const int w, lds_char* smem, int l, int h) {
    const int lane = lane_id(), g = lane >> 4, n = lane & 15, b = n & 3, p = n >> 2;
    const unsigned l0 = (unsigned)(size_t)smem, tb = l0 + w * 12288, lw = l0 + 61440 + w * 24576, ls = l * SD;
    const unsigned xw = (w >> 1) * 16384 + (w & 1) * 4096, tv = (8 * g + p) * 128 + b * 16 + (w & 1) * 64, to = (4 * g + (p & 1)) * 128 + (p >> 1) * 32 + b * 8;
    const unsigned t[18] = {
        l0 + lane * 16, lw + lane * 16, l0 + (g >> 1) * 1024 + (g & 1) * 512 + n * 16, O_E1 + ls + xw + lane * 16, sp_idx(lane),
        l0 + 57344 + (2 * w + (n >> 3)) * 256 + (n >> 3) * 128 + g * 32 + (n & 7) * 2,
        O_UM + ls + (4 * w + (g & 1)) * 65536 + (h >> 3) * 16384 + (g >> 1) * 2048 + (h & 7) * 256 + n * 16,
        sd(O_Y + h * 131072 + ls) + w * 32768 + (g >> 1) * 1024 + (g & 1) * 256 + n * 16,
        l ? F_X + SD + p * 128 + (X1F ? 0 : b * 4) : F_X0 + b * 4, l0 + 49152 + (w >> 1) * 2048 + (2 * (w & 1) + (g >> 1)) * 256 + (n ^ (g >> 1) * 8) * 16 + (g & 1) * 8, sp_idx(lane >> 2), l0 + g * 256 + (g & 1) * 128 + (n & 7) * 16,
        (lane >> 3) * 8192u + (lane & 7) * 16, (8 * (lane >> 5) + ((lane >> 3) & 3)) * 2048u + (lane & 7) * 16, tb + tv - (w & 1) * 12288, lw + tv - (w & 1) * 24576, tb + to, lw + to};
    table(smem + w * 12288 + lane * 4, t);
    asm volatile(VU_ASM : : [ws] "s"(a.ws), [sz] "n"(WS_RS), [rw] "s"(mkrs(a.Wv, 8u << 20)), [ro] "s"(mkrs(a.Wo, 8u << 20)), [wo] "s"(h * 256 + (w >> 1) * 128 + (w & 1) * 262144), [w2] "s"(h * 262144 + w * 512), [tb] "s"(tb), [lw] "s"(lw), [w] "s"(w),
        [xn] "s"(O_XN + ls), [xw] "s"(xw), [fs] "s"(l ? 0 : 128), [xt] "s"(w * 16384 - xw), [fy] "s"(F_Y0 + ls + h * 128 + w * 4), [sg] "s"(w == 1 ? F_Y0 + ls + h * 128 + 16 : OOB), [pr] "s"(prof_at(l * 80 + 32 + h, w) - 128) : CLOB, "s36", "s37", "s38", "s39");
    __builtin_amdgcn_endpgm();
}
template <class F> DEVI void rd_role(const Args& a, const int w, lds_char* smem, int l, int c, F&& idle_work) {
    const int lane = lane_id(), g = lane >> 4, n = lane & 15, r = 2 * (n >> 2) + (n & 1), hf = (n >> 1) & 1;
    const rsrc_t rs = mkrs(a.ws, WS_RS);
    const unsigned l0 = (unsigned)(size_t)smem, tb = l0 + 65536 + w * 4096, ls = l * SD, vp = O_E1 + ls + (c >> 3) * 16384 + (c & 7) * 1024 + w * 256 + n * 16;
    {
        const v4u ff = {SENT, SENT, SENT, SENT};
#pragma unroll
        for (int i = 0; i < 8; ++i) st16(ff, rs, O_UM + ls + c * 65536 + w * 16384 + i * 1024 + lane * 16);
#pragma unroll
        for (int i = 0; i < 2; ++i) st16(ff, rs, O_PT + ls + c * 16384 + w * 2048 + i * 1024 + lane * 16);
        st16(ff, rs, c < 8 ? O_PM + ls + w * 16384 + c * 1024 + lane * 16 : OOB);
        st16(ff, rs, g ? vp + g * 32768 : OOB);
        WV0; BAR;
    }
    st4(g & 1 ? *(const unsigned*)(a.mem0 + r * 1024 + c * 64 + w * 16 + 4 * g + 2 * hf) : nosent(*(const unsigned*)(a.mem0 + r * 1024 + c * 64 + w * 16 + 4 * g + 2 * hf)), rs, g & 1 ? vp + g * 4 : OOB, 0);
    WV0;
    st4(nosent(*(const unsigned*)(a.mem0 + r * 1024 + c * 64 + w * 16 + 4 * g + 2 * hf)), rs, g & 1 ? OOB : vp + g * 4, 0);
    idle_work();
    const int gi = lane >> 3, r8 = lane & 7;
    const unsigned t[12] = {
        (unsigned)lane * 16, sp_idx(lane), l0 + w * 4096 + g * 576 + ((n >> 3) * 16 + hf * 4 + 2 * ((n >> 2) & 1) + (n & 1)) * 4,
        l0 + (r >> 1) * 576 + w * 128 + (g >> 1) * 64 + (r & 1) * 32 + hf * 16,
        PX4 && g >> 1 ? OOB : (vp ^ ((g & 1) * 65536)) + (g >> 1) * 8, g & 1 ? SENT : 0u,
        l0 + 16384 + w * 8192 + lane * 16, l0 + 16384 + ((gi >> 1) * 2 + (w >> 1)) * 1024 + ((gi & 1) * 32 + (w & 1) * 8 + r8) * 16,
        (c >> 1) * 16384u + (c & 1) * 4096 + gi * 512 + (w * 8 + r8) * 16,
        gi ? OOB : O_ST - O_XR + c * 256 + ((w & 1) * 8 + r8) * 16 + (w >> 1) * 8,
        F_Y0 + ls + (w * 8 + r8) * 128 + (c >> 2) * 4, F_Y0 + ls + (gi & 1) * 4096 + (w * 8 + r8) * 128 + 16};
    table(smem + 65536 + w * 4096 + lane * 4, t);
    asm volatile(RD_ASM : : [rs] "s"(rs), [tb] "s"(tb), [su] "s"(O_UM + ls + c * 65536 + w * 16384), [sp] "s"(O_PM + ls + w * 16384), [st] "s"(O_PT + ls + w * 32768),
        [sy] "s"(sd(O_Y + w * (1u << 20) + ls) + (c >> 1) * 16384 + (c & 1) * 4096), [at] "s"(O_XR + ls), [du] "s"((c & 2) * 5 / 2), [fa] "s"(A1F && w ? OOB : F_A + ls + c * 128 + w * 4), [pr] "s"(prof_at(l * 80 + 64 + c, w) + 128) : CLOB);
    __builtin_amdgcn_endpgm();
}
DEVI void gg_role(const Args& a, const int w, lds_char* smem, int l, int gi) {
    const int lane = lane_id(), g = lane >> 4, n = lane & 15, b = n & 3, p = n >> 2;
    const unsigned l0 = (unsigned)(size_t)smem, tb = l0 + w * 12288, lw = l0 + 65536 + w * 24576, ls = l * SD, tv = (8 * g + p) * 128 + b * 16 + (w & 1) * 64, fh = (g >> 1) * 1024 + (g & 1) * 512 + n * 16;
    const unsigned lf = GSW ? 0 : ls, fo = w ? OOB : F_H + (gi / 7) * 1024 + (gi % 7) * 128;
    const unsigned t[9] = {l0 + lane * 16, lw + lane * 16, l0 + fh, w ? OOB : F_A + lf + (lane >> 2) * 128 + (A1F ? 0 : (lane & 3) * 4), w * 2048u + lane * 16, O_ST - O_XR + g * 1024 + n * 16, (lane >> 3) * 7168u + (lane & 7) * 16, tb + tv - (w & 1) * 12288, lw + tv - (w & 1) * 24576};
    table(smem + w * 12288 + lane * 4, t);
    asm volatile(GG_ASM : : [rs] "s"(mkrs(a.ws, WS_RS)), [rw] "s"(mkrs(w & 1 ? a.Wu : a.Wg, 3584 * 2048)), [wo] "s"(gi * 256 + (w >> 1) * 128), [tb] "s"(tb), [lw] "s"(lw), [w] "s"(w),
        [xn0] "s"(O_XR + lf), [xw] "s"((w >> 1) * 16384 + (w & 1) * 4096), [ho0] "s"(O_H + lf + gi * 16384), [fh0] "s"(fo + (GSW ? l * 4 : ls)), [fh1] "s"(fo + ls), [b0] "s"(l ? 0 : GSW), [b1] "s"(l && GSW ? GSW : 64), [pr] "s"(prof_at(160 + l * 28 + gi, w) + 128) : CLOB);
    __builtin_amdgcn_endpgm();
}
DEVI void dd_role(const Args& a, const int w, lds_char* smem, int l, int c) {
    const int lane = lane_id(), g = lane >> 4, b = lane & 3, p = (lane >> 2) & 3, gi = lane >> 3, r8 = lane & 7;
    const unsigned l0 = (unsigned)(size_t)smem, tb = l0 + w * 12288, lw = l0 + 65536 + w * 24576, ls = l * SD, tr = (8 * g + p) * 128 + b * 8;
    const unsigned ta = (c >> 1) * 16384u + (c & 1) * 4096 + gi * 512 + (w * 8 + r8) * 16;
    const rsrc_t rs = mkrs(a.ws, WS_RS);
    const unsigned t[11] = {(unsigned)lane * 16, lw + lane * 16, l0 + w * 8192 + lane * 16, l0 + ((gi >> 1) * 2 + (w >> 1)) * 1024 + ((gi & 1) * 32 + (w & 1) * 8 + r8) * 16,
        ta, l ? (w * 8 + r8) * 2048 + c * 128 + gi * 16 : ta, gi || l ? OOB : O_ST - O_XR + 4096 + c * 256 + (w * 8 + r8) * 8,
        F_H + ls + w * 1024 + ((lane >> 2) % 7) * 128 + (GSW && !l ? 4 : 0), gi * 2048u + r8 * 16, tb + tr, lw + tr};
    table(smem + w * 12288 + lane * 4, t);
    asm volatile(DD_ASM : : [rs] "s"(rs), [rw] "s"(mkrs(a.Wd, 3584 * 2048)), [ro] "s"(mkrs(l ? (char*)a.out : a.ws, l ? 2048 * 2048 : WS_RS)), [wo] "s"(w * (896 * 2048) + c * 128), [tb] "s"(tb), [lw] "s"(lw), [l] "s"(l), [b1] "s"(GSW && !l ? GSW : 99),
        [xn] "s"(O_XR + ls), [ho] "s"(O_H + ls + w * 7 * 16384), [fd] "s"(D1F && w ? OOB : F_D + ls + c * 128 + w * 4), [lt] "s"(F_D + ls + c * 128 + w * 4 + 64), [pr] "s"(prof_at(216 + l * 16 + c, w) + 128) : CLOB);
    __builtin_amdgcn_endpgm();
}

#define PFG 0
#define PFT 1000
#define PFX 0
#define CPF 74
#define CPJ 0
#define LNA 600
#define LNP 150
#define LNW 2
#define DLQ 150
#define DLS 400
#define SPL 0
#define PLAN 3
#define PFA 0
#define PFB 3
#define PFL 1
#define VUX 1
#define LNS 40
#define LN1 700
#define DLG 1000
#define DLD 1400
#define DL1 1200
#define DLG1 2600
#define DLD1 3000
DEVI Args args(unsigned sh) {
    auto p = (const __attribute__((address_space(4))) Args*)__builtin_amdgcn_kernarg_segment_ptr();
    asm volatile("" : "+s"(p));
    Args a;
    a.x = p->x; a.mem0 = p->mem0; a.Wq = p->Wq; a.Wk = p->Wk; a.Wv = p->Wv; a.Wo = p->Wo; a.Wg = p->Wg; a.Wu = p->Wu; a.Wd = p->Wd; a.ws = p->ws + sh; a.out = p->out;
    return a;
}
DEVI unsigned beat_wait(unsigned x0) {
    const rsrc_t r0 = mkrs(args(0).ws, 2u << 20);
    for (unsigned spins = 0; spins < SPIN_CAP; ++spins) {
        CB;
        const v2u t = __builtin_amdgcn_raw_buffer_load_b64(r0, 0, O_HB, AUX_SC1);
        if (RFL(t[0]) != x0) return RFL(t[1]) & (2u << 20);
        __builtin_amdgcn_s_sleep(8);
    }
    __builtin_amdgcn_endpgm();
}
#define R2W 0
#define MPF 0
DEVI void mpf(rsrc_t rs, unsigned o) {
    o += O_E1 + lane_id() * 128;
    unsigned spins = 0;
    for (int s = 0; s < NSEG && spins < SPIN_CAP; ++spins) {
        CB;
        if (__ballot(ld4(rs, o, (s & 3) * 32768) == SENT) == 0ull) { ++s; for (int i = 0; i < MPF; ++i) __builtin_amdgcn_s_sleep(8); }
    }
}
DEVI void leader(const Args& a, const int w, lds_char* smem, unsigned x0) {
    const int lane = lane_id();
    const rsrc_t r0 = mkrs(a.ws, 4u << 20);
    unsigned t[2] = {~0u, ~0u};
    for (int i = 0; i < 2 * NPRB; ++i) {
        const unsigned t0 = (unsigned)__builtin_amdgcn_s_memtime();
        WL0;
        if (lane == 0) st4(0u, r0, 0, O_PRB + (i & 1) * SD + w * 1024 + (i >> 1) * 128);
        WV0;
        const unsigned d = (unsigned)__builtin_amdgcn_s_memtime() - t0;
        if (i & 1) t[1] = min(t[1], d); else t[0] = min(t[0], d);
    }
    *(l2*)(smem + w * 8) = v2u{t[0], t[1]};
    WL0; BAR;
    const v4u p = *(l4*)smem, q = *(l4*)(smem + 16);
    const unsigned sh = RFL(min(min(p[0], p[2]), min(q[0], q[2])) < min(min(p[1], p[3]), min(q[1], q[3])) ? 2u << 20 : 0u);
    const unsigned o = sh + w * 1024 + lane * 16;
    const v4u ff = {SENT, SENT, SENT, SENT};
#pragma unroll
    for (int i = 0; i < 8; ++i) st16(ff, r0, o + O_E1 + i * 4096);
#pragma unroll
    for (int i = 1; i < 16; ++i) if (i != 2 && i != 11) st16(ff, r0, o + i * 4096);
    WV0; BAR;
    if (w) { if (MPF && w > 1) mpf(r0, sh + SD + (w & 1) * 16384); return; }
    for (unsigned spins = 0; spins < SPIN_CAP; ++spins) {
        if (lane == 0) st8(v2u{++x0, sh}, r0, 0, O_HB);
        if (__ballot((int)ld4(r0, sh + F_D + SD + (lane >> 2) * 128 + (D1F ? 0 : (lane & 3) * 4), 0) < 0) == 0ull) break;
        __builtin_amdgcn_s_sleep(16);
    }
}
__global__ __launch_bounds__(256, 1) void sys_kernel(Args) {
    extern __shared__ __attribute__((aligned(16))) char smem_[];
    lds_char* smem = (lds_char*)smem_;
    const int x = blockIdx.x & 7, j = blockIdx.x >> 3, wv = RFL(threadIdx.x >> 6), l = x >> 2, xx = x & 3;
    const unsigned t0 = (unsigned)__builtin_amdgcn_s_memrealtime();
    auto until = [&](unsigned dl) AI { while ((unsigned)__builtin_amdgcn_s_memrealtime() - t0 < dl) __builtin_amdgcn_s_sleep(8); };
    unsigned x0 = 0;
    if (l || j >= 16) x0 = RFL(ld4(mkrs(args(0).ws, 2u << 20), 0, O_HB));
    if (j < 16) {
        if (l) until(DL1);
        if (DLQ && !l && j < 8) until(DLQ);
        if (j < 8) qk_role(args(0), wv, smem, l, xx * 8 + j); else vu_role(args(0), wv, smem, l, (xx ^ VUX) * 8 + j - 8);
        return;
    }
    if (j >= 16 + CPJ && j < 20 + CPJ) {
        unsigned long long pc; unsigned n;
        asm volatile("s_getpc_b64 %0\n\ts_mov_b32 %1, .-_Z10sys_kernel4Args" : "=s"(pc), "=s"(n));
        const rsrc_t rc = mkrs((const void*)(pc - n), CPF * 1024);
        __builtin_amdgcn_raw_ptr_buffer_load_lds(rc, smem + LDSB - 1024 + wv * 256, 4, lane_id() * 128, ((j - 16 - CPJ) * 4 + wv) * 8192, 0, 0);
    }
    {
        const int lane = lane_id(), q = (j - 16) * 4 + wv, m = SPL && !l ? ((xx & 1) != (SPL & 1)) * 2 + (q >> 5) : q >> 4;
        const Args a = args(0);
        const u16 *W0 = a.Wq, *W1 = a.Wk, *W2 = a.Wv, *W3 = a.Wo;
        asm volatile("" : "+s"(W0), "+s"(W1), "+s"(W2), "+s"(W3));
        const u16* W = m == 0 ? W0 : m == 1 ? W1 : m == 2 ? W2 : W3;
        const rsrc_t rw = mkrs(W, 8u << 20);
        if (l) {
#pragma unroll
            for (int i = 0; i < PFA; ++i) __builtin_amdgcn_raw_ptr_buffer_load_lds(rw, smem + LDSB - 1024 + wv * 256, 4, lane * 256, i * (1u << 20) + (xx * 16 + (q & 15)) * 16384, 0, 0);
        } else {
            const unsigned po = (0x100u - (unsigned)(size_t)W) & 0x300u, hx = SPL ? (xx & 2) + ((q >> 4) & 1) : m < 2 ? xx : xx ^ VUX;
            const unsigned vo = m == 3 ? (lane >> 1) * 1024 + (lane & 1) * 128 : (lane >> 2) * 8192 + ((lane >> 1) & 1) * 1024 + (lane & 1) * 128;
            const unsigned so = po + (m == 3 ? hx * (2u << 20) + (q & 15) * 32768 : hx * 2048 + (q & 15) * 131072);
#pragma unroll
            for (int i = 0; i < PFB; ++i) __builtin_amdgcn_raw_ptr_buffer_load_lds(rw, smem + LDSB - 1024 + wv * 256, 4, vo, so + i * (m == 3 ? 524288 : 2u << 20), 0, 0);
            const unsigned pl = (0x1F00u - (unsigned)(size_t)W) & 0x1F00u;
#pragma unroll
            for (int i = 0; i < PFL; ++i) __builtin_amdgcn_raw_ptr_buffer_load_lds(rw, smem + LDSB - 1024 + wv * 256, 4, (lane >> 1) * 8192 + (lane & 1) * 128, (pl >> 11) == hx && m < 3 ? pl + ((q & 15) + 16 * i) * 262144 : OOB, 0, 0);
        }
    }
    int ro, ix;
    if ((xx & 1) == (PLAN & 1)) { if (j < 24) { ro = 0; ix = (xx >> 1) * 8 + j - 16; } else if (j < 26) { ro = 3; ix = (xx >> 1) * 2 + j - 24; } else { ro = 1; ix = (xx >> 1) * 14 + j - 26; } }
    else if (j < 24) { ro = 1; ix = (xx >> 1) * 14 + j - 10; } else { ro = 2; ix = (xx >> 1) * 8 + j - 24; }
    if (l && ro == 3 && ix) for (int b = ix - 1; b < NSEG; b += 3) { until(b < 3 ? 0 : LNA + b * LNP); ln0(args(0), wv, b, [&]() AI { return beat_wait(x0); }); }
    if (ro == 3 && l) { if (ix == 0) leader(args(0), wv, smem, x0); else if (MPF && wv > 1) mpf(mkrs(args(0).ws, 4u << 20), beat_wait(x0) + SD + (wv & 1) * 16384); return; }
    const unsigned sh = beat_wait(x0);
    auto pfg = [&]() AI {
        if (!PFG || l) return;
        until(PFT);
        const Args a = args(0);
        const int lane = lane_id(), q = (ix & 7) * 4 + wv;
        const rsrc_t rw = mkrs(q & 1 ? a.Wu : a.Wg, 3584 * 2048);
        const unsigned vo = (lane & 15) < ((xx & 1) ? 12 : 16) ? (lane >> 4) * 7168 + (lane & 15) * 128 : OOB, so = ((xx >> 1) * 14 + ((xx & 1) ? 0 : 6)) * 256 + (1024 - 64 * PFG + (q >> 1) * 4) * 7168;
#pragma unroll
        for (int i = 0; i < PFG; ++i) __builtin_amdgcn_raw_ptr_buffer_load_lds(rw, smem + LDSB - 1024 + wv * 256, 4, vo, so + i * 64 * 7168, 0, PFX);
    };
    if (ro == 0) { rd_role(args(sh), wv, smem, l, ix, pfg); return; }
    if (ro == 1) { const Args a = args(sh); until((l ? DLG1 : DLG) - (((((unsigned)(size_t)a.Wg >> 8) + ix) & 3) == 1 || ((((unsigned)(size_t)a.Wu >> 8) + ix) & 3) == 1 ? DLS : 0)); WV0; BAR; gg_role(a, wv, smem, l, ix); return; }
    if (ro == 2) { pfg(); const Args a = args(sh); until((l ? DLD1 : DLD) - (((((unsigned)(size_t)a.Wd >> 8) + (ix >> 1)) & 3) == 1 ? DLS + 200 : 0)); WV0; BAR; dd_role(a, wv, smem, l, ix); return; }
    r_role(args(sh), wv, ix);
}

extern "C" void kernel_launch(void* const* d_in, const int* in_sizes, int n_in, void* d_out, int out_size, void* d_ws, size_t ws_size, hipStream_t stream) {
    (void)in_sizes; (void)n_in; (void)out_size; (void)ws_size;
    Args a;
    a.x = (const u16*)d_in[0]; a.mem0 = (const u16*)d_in[1]; a.Wq = (const u16*)d_in[2]; a.Wk = (const u16*)d_in[3]; a.Wv = (const u16*)d_in[4]; a.Wo = (const u16*)d_in[5];
    a.Wg = (const u16*)d_in[8]; a.Wu = (const u16*)d_in[9]; a.Wd = (const u16*)d_in[10];
    a.ws = (char*)(((uintptr_t)d_ws + (2u << 20) - 1) & ~(uintptr_t)((2u << 20) - 1)); a.out = (u16*)d_out;
    (void)hipFuncSetAttribute((const void*)sys_kernel, hipFuncAttributeMaxDynamicSharedMemorySize, LDSB);
    hipLaunchKernelGGL(sys_kernel, dim3(256), dim3(256), LDSB, stream, a);
}
```

```cpp
#include <hip/hip_runtime.h>
#include <stdint.h>

typedef unsigned short u16;
typedef unsigned long long u64;
typedef __attribute__((ext_vector_type(8))) short bf16x8;
typedef __attribute__((ext_vector_type(4))) short s16x4;
typedef __attribute__((ext_vector_type(4))) float f32x4;
typedef __attribute__((ext_vector_type(16))) float f32x16;
typedef __attribute__((ext_vector_type(2))) float f32x2;
typedef __attribute__((ext_vector_type(4))) unsigned v4u;
typedef __attribute__((ext_vector_type(2))) unsigned v2u;
typedef __amdgpu_buffer_rsrc_t rsrc_t;
typedef __attribute__((address_space(3))) char lds_char;
typedef __attribute__((address_space(3))) bf16x8 l8;
typedef __attribute__((address_space(3))) v4u l4;
typedef __attribute__((address_space(3))) v2u l2;
typedef __attribute__((address_space(3))) unsigned l1;
typedef __attribute__((address_space(3))) f32x4 lf4;
typedef __attribute__((address_space(3))) float lf1;
#define DEVI __device__ __forceinline__
#define AI __attribute__((always_inline))

constexpr int NSEG = 64;
constexpr float ATT_SCALE = 0.08838834764831845f;
constexpr float LOG2E = 1.4426950408889634f;
#define NPRB 4
constexpr unsigned SD = 8192;
DEVI unsigned sd(unsigned o) { return o ^ ((o >> 8) & SD); }
constexpr unsigned F_X0 = 32768;
constexpr unsigned F_X = 4096;
constexpr unsigned F_A = 16384;
constexpr unsigned F_H = F_A + 2048;
constexpr unsigned F_D = F_H + 4096;
constexpr unsigned F_Y0 = 49152;
constexpr unsigned F_ABORT = 6400;
constexpr unsigned O_PM = 65536;
constexpr unsigned O_E1 = 131072;
constexpr unsigned O_PT = 262144;
constexpr unsigned O_UM = 524288;
constexpr unsigned CTRL_END = O_UM + (1u << 20);
constexpr unsigned O_PRB = 0x1F0000, O_HB = 0x1FFF00;
constexpr unsigned O_XN = 16u << 20;
constexpr unsigned O_XR = O_XN + (8u << 20);
constexpr unsigned O_ST = O_XR + (8u << 20);
constexpr unsigned O_H = O_ST + (8u << 20);
constexpr unsigned O_Y = 8u << 20;
constexpr unsigned WS_NEED = O_H + (32u << 20);
constexpr unsigned O_PROF = WS_NEED;
constexpr unsigned WS_RS = WS_NEED;
static_assert(CTRL_END <= O_PRB && CTRL_END % 16384 == 0 && O_Y % (8u << 20) == 0, "workspace");
constexpr unsigned OOB = 0x80000000u, OOB2 = 0x90000000u, SENT = 0xFFFFFFFFu;

struct Args { const u16 *x, *mem0, *Wq, *Wk, *Wv, *Wo, *Wg, *Wu, *Wd; char* ws; u16* out; };

typedef __attribute__((ext_vector_type(2))) __bf16 bf16x2_t;
DEVI unsigned pack2(float lo, float hi) { f32x2 v = {lo, hi}; bf16x2_t b = __builtin_convertvector(v, bf16x2_t); return __builtin_bit_cast(unsigned, b); }
DEVI float bflo(unsigned u) { return __uint_as_float(u << 16); }
DEVI float bfhi(unsigned u) { return __uint_as_float(u & 0xffff0000u); }
DEVI float rbf(float x) { return bflo(pack2(x, x)); }
DEVI bf16x8 as8(v4u v) { return __builtin_bit_cast(bf16x8, v); }
DEVI f32x4 mfma16(bf16x8 a, bf16x8 b, f32x4 c) { return __builtin_amdgcn_mfma_f32_16x16x32_bf16(a, b, c, 0, 0, 0); }
DEVI f32x16 mfma32(bf16x8 a, bf16x8 b, f32x16 c) { return __builtin_amdgcn_mfma_f32_32x32x16_bf16(a, b, c, 0, 0, 0); }
constexpr int AUX_SC1 = 16;
DEVI v4u ld16(rsrc_t r, unsigned vo, unsigned so) { return __builtin_amdgcn_raw_buffer_load_b128(r, vo, so, AUX_SC1); }
DEVI unsigned ld4(rsrc_t r, unsigned vo, unsigned so) { return __builtin_amdgcn_raw_buffer_load_b32(r, vo, so, AUX_SC1); }
DEVI void st16(v4u v, rsrc_t r, unsigned vo) { __builtin_amdgcn_raw_buffer_store_b128(v, r, vo, 0, AUX_SC1); }
DEVI void st8(v2u v, rsrc_t r, unsigned vo, unsigned so) { __builtin_amdgcn_raw_buffer_store_b64(v, r, vo, so, AUX_SC1); }
DEVI void st4(unsigned v, rsrc_t r, unsigned vo, unsigned so) { __builtin_amdgcn_raw_buffer_store_b32(v, r, vo, so, AUX_SC1); }
DEVI v2u trd(lds_char* p) { return __builtin_bit_cast(v2u, __builtin_amdgcn_ds_read_tr16_b64_v4i16((__attribute__((address_space(3))) s16x4*)p)); }
template <int O1, int O2> DEVI void tra(v2u& lo, v2u& hi, unsigned a) { asm volatile("ds_read_b64_tr_b16 %0, %2 offset:%3\n\tds_read_b64_tr_b16 %1, %2 offset:%4" : "=&v"(lo), "=&v"(hi) : "v"(a), "n"(O1), "n"(O2) : "memory"); }
DEVI void trw(v2u& a, v2u& b, v2u& c, v2u& d) { asm volatile("s_waitcnt lgkmcnt(0)" : "+v"(a), "+v"(b), "+v"(c), "+v"(d)::"memory"); }
DEVI rsrc_t mkrs(const void* p, unsigned n) { return __builtin_amdgcn_make_buffer_rsrc((void*)p, 0, (int)n, 0x00020000); }
#define CB asm volatile("" ::: "memory")
#define SB __builtin_amdgcn_sched_barrier(0)
#define WL0 asm volatile("s_waitcnt lgkmcnt(0)" ::: "memory")
#define WV0 asm volatile("s_waitcnt vmcnt(0)" ::: "memory")
#define WV(n) asm volatile("s_waitcnt vmcnt(%0)" ::"n"(n) : "memory")
#define BAR do { __builtin_amdgcn_s_barrier(); CB; } while (0)
#define RFL(x) __builtin_amdgcn_readfirstlane(x)
DEVI int lane_id() { int x; asm volatile("v_mbcnt_lo_u32_b32 %0, -1, 0\n\tv_mbcnt_hi_u32_b32 %0, -1, %0" : "=v"(x)); return x; }
template <int I> struct IC { static constexpr int v = I; };
template <int B, int E_, class F> DEVI void sfor(F&& f) { if constexpr (B < E_) { f(IC<B>{}); sfor<B + 1, E_>(f); } }
DEVI float red_x32(float x, bool mx) { unsigned u = __float_as_uint(x); auto r = __builtin_amdgcn_permlane32_swap(u, u, false, false); float a = __uint_as_float(r[0]), b = __uint_as_float(r[1]); return mx ? fmaxf(a, b) : a + b; }
DEVI float red_x16(float x, bool mx) { unsigned u = __float_as_uint(x); auto r = __builtin_amdgcn_permlane16_swap(u, u, false, false); float a = __uint_as_float(r[0]), b = __uint_as_float(r[1]); return mx ? fmaxf(a, b) : a + b; }
DEVI unsigned vmax4(v4u d) { return max(max(d[0], d[1]), max(d[2], d[3])); }
DEVI unsigned nosent(unsigned v) { return min(v, SENT - 1u); }
constexpr unsigned SPIN_CAP = 1u << 18;
DEVI bool spin_bail(unsigned& spins, rsrc_t rs) {
    if (++spins > SPIN_CAP) { st4(0u, rs, 0, F_ABORT); return true; }
    return (spins & 255u) == 0u && RFL(ld4(rs, 0, F_ABORT)) != SENT;
}
#define TSTAMP(idx)
constexpr int LDSB = 160 * 1024;
DEVI void idle(unsigned dl) { const unsigned t0 = (unsigned)__builtin_amdgcn_s_memrealtime(); while ((unsigned)__builtin_amdgcn_s_memrealtime() - t0 < dl) __builtin_amdgcn_s_sleep(8); }
#define RXE 0
#define D1F 0
#define X1F 0

DEVI float red_rows(float s) {
    s += __uint_as_float(__builtin_amdgcn_update_dpp(0, __float_as_uint(s), 0x128, 0xf, 0xf, false));
    s = red_x16(s, false); return red_x32(s, false);
}
DEVI f32x2 up2(unsigned u) { return f32x2{bflo(u), bfhi(u)}; }
DEVI float ln_inv(float var) { return __builtin_amdgcn_rcpf(rbf(__builtin_amdgcn_sqrtf(rbf(rbf(fmaxf(var, 0.f)) + 1e-5f)))); }
template <class F> DEVI void ln0(const Args& a, const int w, int b, F&& base) {
    const int lane = lane_id(), row = w * 8 + (lane & 7);
    const rsrc_t rx = mkrs(a.x, 2048 * 2048);
    v4u u[16];
#pragma unroll
    for (int i = 0; i < 16; ++i) u[i] = __builtin_amdgcn_raw_buffer_load_b128(rx, row * 2048 + (lane >> 3) * 16 + i * 128, b * 65536, 0);
    const float k0 = __shfl(bflo(u[0][0]), lane & 7);
    f32x2 s1 = {0.f, 0.f}, s2 = {0.f, 0.f};
#pragma unroll
    for (int i = 0; i < 16; ++i)
#pragma unroll
        for (int e = 0; e < 4; ++e) { const f32x2 d = up2(u[i][e]) - k0; s1 += d; s2 += d * d; }
    const float s = red_rows(s1[0] + s1[1]) * (1.0f / 1024.0f), q = red_rows(s2[0] + s2[1]) * (1.0f / 1024.0f);
    float inv = ln_inv(q - s * s), c = -rbf(k0 + s) * inv;
    asm volatile("" : "+v"(inv), "+v"(c));
    const rsrc_t rs = mkrs(a.ws + base(), WS_RS);
    const unsigned o = sd(O_XN + b * 131072) + (lane >> 3) * 512 + row * 16;
#pragma unroll
    for (int i = 0; i < 16; ++i) {
        v4u r;
#pragma unroll
        for (int e = 0; e < 4; ++e) { const f32x2 z = up2(u[i][e]) * inv + c; r[e] = pack2(z[0], z[1]); }
        st16(r, rs, o + (i >> 1) * 16384 + (i & 1) * 4096);
    }
#pragma unroll
    for (int i = 0; i < 16; ++i) st16(u[i], rs, o + (O_XR - O_XN) + (i >> 1) * 16384 + (i & 1) * 4096);
    st16(u[0], rs, sd(O_ST + b * 131072) + w * 1024 + lane * 16); st16(u[0], rs, sd(O_ST + b * 131072) + 4096 + w * 1024 + lane * 16); st16(u[0], rs, sd(O_ST + SD + b * 131072) + w * 1024 + lane * 16);
    WV0;
    st4(b, rs, 0, F_X0 + b * 128 + w * 4);
}
#define RSLP 8
DEVI void r_role(const Args& a, const int w, int rq) {
    const int lane = lane_id(), row = (w >> 1) * 16 + (rq & 1) * 8 + (lane & 7), cq = (rq >> 1) * 2 + (w & 1);
    const rsrc_t rs = mkrs(a.ws, WS_RS);
    const unsigned fo = cq * 32768 + (lane >> 3) * 512 + row * 16, so = O_ST - O_XR + 4096 + (lane >> 3) * 512 + row * 8;
    auto io = [](int i) -> unsigned { return (i >> 1) * 16384 + (i & 1) * 4096; };
    const unsigned fd = F_D + (lane & 15) * 128 + (D1F ? 0 : (row >> 3) * 4);
    for (int b = 0; b < NSEG; ++b) {
        unsigned spins = 0;
        for (;;) {
            CB;
            if (__ballot((int)ld4(rs, fd, 0) < b) == 0ull) break;
            if (spin_bail(spins, rs)) return;
            __builtin_amdgcn_s_sleep(RSLP);
        }
        const unsigned sx = sd(O_XR + b * 131072);
        const v2u s0 = __builtin_amdgcn_raw_buffer_load_b64(rs, so, sx, AUX_SC1), s1 = __builtin_amdgcn_raw_buffer_load_b64(rs, so + 256, sx, AUX_SC1);
        v4u u[4];
#pragma unroll
        for (int i = 0; i < 4; ++i) u[i] = ld16(rs, fo + io(i), sx);
        const float m0 = __uint_as_float(s0[0]), m1 = __uint_as_float(s1[0]), mean = red_rows(m0 + m1) * 0.0625f;
        const float var = red_rows(__uint_as_float(s0[1]) + __uint_as_float(s1[1]) + 64.f * ((m0 - mean) * (m0 - mean) + (m1 - mean) * (m1 - mean))) * (1.0f / 1024.0f);
        const float inv = ln_inv(var), c = -rbf(mean) * inv;
#pragma unroll
        for (int i = 0; i < 4; ++i) {
            v4u o;
#pragma unroll
            for (int e = 0; e < 4; ++e) { const f32x2 z = up2(u[i][e]) * inv + c; o[e] = pack2(z[0], z[1]); }
            st16(o, rs, fo + io(i) + ((sx ^ SD) - (O_XR - O_XN)));
        }
        CB;
#pragma unroll
        for (int i = 0; i < 4; ++i) st16(u[i], rs, fo + io(i) + (sx ^ SD));
        WV0;
        if (X1F) BAR;
        st4(b, rs, 0, X1F && w ? OOB : F_X + SD + rq * 128 + w * 4);
        if (rq == 0 && w == 0) TSTAMP(200000 + 64 + b);
    }
}
DEVI void r_role2(const Args& a, const int w, int rq) {
    const int lane = lane_id(), row = w * 16 + (rq & 1) * 8 + (lane & 7), cq = (rq >> 1) * 2;
    const rsrc_t rs = mkrs(a.ws, WS_RS);
    const unsigned fo = cq * 32768 + (lane >> 3) * 512 + row * 16, so = O_ST - O_XR + 4096 + (lane >> 3) * 512 + row * 8;
    auto io = [](int i) -> unsigned { return (i >> 1) * 16384 + (i & 1) * 4096; };
    const unsigned fd = F_D + (lane & 15) * 128 + (D1F ? 0 : (row >> 3) * 4);
    for (int b = 0; b < NSEG; ++b) {
        unsigned spins = 0;
        for (;;) {
            CB;
            if (__ballot((int)ld4(rs, fd, 0) < b) == 0ull) break;
            if (spin_bail(spins, rs)) return;
            __builtin_amdgcn_s_sleep(RSLP);
        }
        const unsigned sx = sd(O_XR + b * 131072);
        const v2u s0 = __builtin_amdgcn_raw_buffer_load_b64(rs, so, sx, AUX_SC1), s1 = __builtin_amdgcn_raw_buffer_load_b64(rs, so + 256, sx, AUX_SC1);
        v4u u[8];
#pragma unroll
        for (int i = 0; i < 8; ++i) u[i] = ld16(rs, fo + io(i), sx);
        const float m0 = __uint_as_float(s0[0]), m1 = __uint_as_float(s1[0]), mean = red_rows(m0 + m1) * 0.0625f;
        const float var = red_rows(__uint_as_float(s0[1]) + __uint_as_float(s1[1]) + 64.f * ((m0 - mean) * (m0 - mean) + (m1 - mean) * (m1 - mean))) * (1.0f / 1024.0f);
        const float inv = ln_inv(var), c = -rbf(mean) * inv;
#pragma unroll
        for (int i = 0; i < 8; ++i) {
            v4u o;
#pragma unroll
            for (int e = 0; e < 4; ++e) { const f32x2 z = up2(u[i][e]) * inv + c; o[e] = pack2(z[0], z[1]); }
            st16(o, rs, fo + io(i) + ((sx ^ SD) - (O_XR - O_XN)));
        }
        CB;
#pragma unroll
        for (int i = 0; i < 8; ++i) st16(u[i], rs, fo + io(i) + (sx ^ SD));
        WV0;
        if (X1F) BAR;
        st8(v2u{(unsigned)b, (unsigned)b}, rs, 0, F_X + SD + rq * 128 + w * 8);
        if (rq == 0 && w == 0) TSTAMP(200000 + 64 + b);
    }
}
#define QK_ASM \
"v_mbcnt_lo_u32_b32 v255, -1, 0\nv_mbcnt_hi_u32_b32 v255, -1, v255\nv_lshlrev_b32 v255, 2, v255\nv_add_u32 v255, %[tb], v255\nds_read_b32 v0, v255 offset:0\nds_read_b32 v1, v255 offset:256\nds_read_b32 v2, v255 offset:512\nds_read_b32 v3, v255 offset:768\n" \
"ds_read_b32 v4, v255 offset:1024\nds_read_b32 v5, v255 offset:1280\nds_read_b32 v6, v255 offset:1536\nds_read_b32 v7, v255 offset:1792\nds_read_b32 v8, v255 offset:2048\nds_read_b32 v9, v255 offset:2304\nds_read_b32 v10, v255 offset:2560\nds_read_b32 v11, v255 offset:2816\n" \
"ds_read_b32 v12, v255 offset:3072\nds_read_b32 v13, v255 offset:3328\nds_read_b32 v14, v255 offset:3584\nds_read_b32 v15, v255 offset:3840\nds_read_b32 v92, v255 offset:4096\nds_read_b32 v93, v255 offset:4352\nds_read_b32 v94, v255 offset:4608\ns_waitcnt lgkmcnt(0)\n" \
"s_mov_b64 s[36:37], %[ws]\ns_mov_b32 s38, %[sz]\ns_mov_b32 s39, 0x20000\ns_mov_b32 s45, 2096896\nbuffer_load_dword v95, off, s[36:39], s45 sc1\ns_add_u32 m0, %[lw], 16384\ns_add_u32 s44, %[wo], 0\nbuffer_load_dwordx4 v92, %[rw], s44 offen nt lds\n" \
"s_add_u32 m0, %[lw], 17408\ns_add_u32 s44, %[wo], 65536\nbuffer_load_dwordx4 v92, %[rw], s44 offen nt lds\ns_add_u32 m0, %[lw], 18432\ns_add_u32 s44, %[wo], 131072\nbuffer_load_dwordx4 v92, %[rw], s44 offen nt lds\ns_add_u32 m0, %[lw], 19456\ns_add_u32 s44, %[wo], 196608\n" \
"buffer_load_dwordx4 v92, %[rw], s44 offen nt lds\ns_add_u32 m0, %[lw], 20480\ns_add_u32 s44, %[wo], 262144\nbuffer_load_dwordx4 v92, %[rw], s44 offen nt lds\ns_add_u32 m0, %[lw], 21504\ns_add_u32 s44, %[wo], 327680\nbuffer_load_dwordx4 v92, %[rw], s44 offen nt lds\ns_add_u32 m0, %[lw], 22528\n" \
"s_add_u32 s44, %[wo], 393216\nbuffer_load_dwordx4 v92, %[rw], s44 offen nt lds\ns_add_u32 m0, %[lw], 23552\ns_add_u32 s44, %[wo], 458752\nbuffer_load_dwordx4 v92, %[rw], s44 offen nt lds\ns_add_u32 m0, %[tb], 0\ns_add_u32 s44, %[wo], 524288\nbuffer_load_dwordx4 v92, %[rw], s44 offen nt lds\n" \
"s_add_u32 m0, %[tb], 1024\ns_add_u32 s44, %[wo], 589824\nbuffer_load_dwordx4 v92, %[rw], s44 offen nt lds\ns_add_u32 m0, %[tb], 2048\ns_add_u32 s44, %[wo], 655360\nbuffer_load_dwordx4 v92, %[rw], s44 offen nt lds\ns_add_u32 m0, %[tb], 3072\ns_add_u32 s44, %[wo], 720896\n" \
"buffer_load_dwordx4 v92, %[rw], s44 offen nt lds\ns_add_u32 m0, %[tb], 4096\ns_add_u32 s44, %[wo], 786432\nbuffer_load_dwordx4 v92, %[rw], s44 offen nt lds\ns_add_u32 m0, %[tb], 5120\ns_add_u32 s44, %[wo], 851968\nbuffer_load_dwordx4 v92, %[rw], s44 offen nt lds\ns_add_u32 m0, %[tb], 6144\n" \
"s_add_u32 s44, %[wo], 917504\nbuffer_load_dwordx4 v92, %[rw], s44 offen nt lds\ns_add_u32 m0, %[tb], 7168\ns_add_u32 s44, %[wo], 983040\nbuffer_load_dwordx4 v92, %[rw], s44 offen nt lds\ns_add_u32 m0, %[tb], 8192\ns_add_u32 s44, %[wo], 1048576\nbuffer_load_dwordx4 v92, %[rw], s44 offen nt lds\n" \
"s_add_u32 m0, %[tb], 9216\ns_add_u32 s44, %[wo], 1114112\nbuffer_load_dwordx4 v92, %[rw], s44 offen nt lds\ns_add_u32 m0, %[tb], 10240\ns_add_u32 s44, %[wo], 1179648\nbuffer_load_dwordx4 v92, %[rw], s44 offen nt lds\ns_add_u32 m0, %[tb], 11264\ns_add_u32 s44, %[wo], 1245184\n" \
"buffer_load_dwordx4 v92, %[rw], s44 offen nt lds\ns_add_u32 m0, %[lw], 0\ns_add_u32 s44, %[wo], 1310720\nbuffer_load_dwordx4 v92, %[rw], s44 offen nt lds\ns_add_u32 m0, %[lw], 1024\ns_add_u32 s44, %[wo], 1376256\nbuffer_load_dwordx4 v92, %[rw], s44 offen nt lds\ns_add_u32 m0, %[lw], 2048\n" \
"s_add_u32 s44, %[wo], 1441792\nbuffer_load_dwordx4 v92, %[rw], s44 offen nt lds\ns_add_u32 m0, %[lw], 3072\ns_add_u32 s44, %[wo], 1507328\nbuffer_load_dwordx4 v92, %[rw], s44 offen nt lds\ns_add_u32 m0, %[lw], 4096\ns_add_u32 s44, %[wo], 1572864\nbuffer_load_dwordx4 v92, %[rw], s44 offen nt lds\n" \
"s_add_u32 m0, %[lw], 5120\ns_add_u32 s44, %[wo], 1638400\nbuffer_load_dwordx4 v92, %[rw], s44 offen nt lds\ns_add_u32 m0, %[lw], 6144\ns_add_u32 s44, %[wo], 1703936\nbuffer_load_dwordx4 v92, %[rw], s44 offen nt lds\ns_add_u32 m0, %[lw], 7168\ns_add_u32 s44, %[wo], 1769472\n" \
"buffer_load_dwordx4 v92, %[rw], s44 offen nt lds\ns_add_u32 m0, %[lw], 8192\ns_add_u32 s44, %[wo], 1835008\nbuffer_load_dwordx4 v92, %[rw], s44 offen nt lds\ns_add_u32 m0, %[lw], 9216\ns_add_u32 s44, %[wo], 1900544\nbuffer_load_dwordx4 v92, %[rw], s44 offen nt lds\ns_add_u32 m0, %[lw], 10240\n" \
"s_add_u32 s44, %[wo], 1966080\nbuffer_load_dwordx4 v92, %[rw], s44 offen nt lds\ns_add_u32 m0, %[lw], 11264\ns_add_u32 s44, %[wo], 2031616\nbuffer_load_dwordx4 v92, %[rw], s44 offen nt lds\ns_add_u32 m0, %[lw], 12288\ns_add_u32 s44, %[wo], 2097152\nbuffer_load_dwordx4 v92, %[rw], s44 offen nt lds\n" \
"s_add_u32 m0, %[lw], 13312\ns_add_u32 s44, %[wo], 2162688\nbuffer_load_dwordx4 v92, %[rw], s44 offen nt lds\ns_add_u32 m0, %[lw], 14336\ns_add_u32 s44, %[wo], 2228224\nbuffer_load_dwordx4 v92, %[rw], s44 offen nt lds\ns_add_u32 m0, %[lw], 15360\ns_add_u32 s44, %[wo], 2293760\n" \
"buffer_load_dwordx4 v92, %[rw], s44 offen nt lds\ns_waitcnt vmcnt(32)\nds_read_b64_tr_b16 a[0:1], v94 offset:16384\nds_read_b64_tr_b16 a[2:3], v94 offset:16896\nds_read_b64_tr_b16 a[8:9], v94 offset:16392\nds_read_b64_tr_b16 a[10:11], v94 offset:16904\nds_read_b64_tr_b16 a[16:17], v94 offset:16448\nds_read_b64_tr_b16 a[18:19], v94 offset:16960\n" \
"ds_read_b64_tr_b16 a[24:25], v94 offset:16456\nds_read_b64_tr_b16 a[26:27], v94 offset:16968\ns_waitcnt lgkmcnt(0)\ns_add_u32 m0, %[lw], 16384\ns_add_u32 s44, %[wo], 2359296\nbuffer_load_dwordx4 v92, %[rw], s44 offen nt lds\ns_add_u32 m0, %[lw], 17408\ns_add_u32 s44, %[wo], 2424832\n" \
"buffer_load_dwordx4 v92, %[rw], s44 offen nt lds\ns_add_u32 m0, %[lw], 18432\ns_add_u32 s44, %[wo], 2490368\nbuffer_load_dwordx4 v92, %[rw], s44 offen nt lds\ns_add_u32 m0, %[lw], 19456\ns_add_u32 s44, %[wo], 2555904\nbuffer_load_dwordx4 v92, %[rw], s44 offen nt lds\ns_waitcnt vmcnt(32)\n" \
"ds_read_b64_tr_b16 a[4:5], v94 offset:20480\nds_read_b64_tr_b16 a[6:7], v94 offset:20992\nds_read_b64_tr_b16 a[12:13], v94 offset:20488\nds_read_b64_tr_b16 a[14:15], v94 offset:21000\nds_read_b64_tr_b16 a[20:21], v94 offset:20544\nds_read_b64_tr_b16 a[22:23], v94 offset:21056\nds_read_b64_tr_b16 a[28:29], v94 offset:20552\nds_read_b64_tr_b16 a[30:31], v94 offset:21064\n" \
"s_waitcnt lgkmcnt(0)\ns_add_u32 m0, %[lw], 20480\ns_add_u32 s44, %[wo], 2621440\nbuffer_load_dwordx4 v92, %[rw], s44 offen nt lds\ns_add_u32 m0, %[lw], 21504\ns_add_u32 s44, %[wo], 2686976\nbuffer_load_dwordx4 v92, %[rw], s44 offen nt lds\ns_add_u32 m0, %[lw], 22528\n" \
"s_add_u32 s44, %[wo], 2752512\nbuffer_load_dwordx4 v92, %[rw], s44 offen nt lds\ns_add_u32 m0, %[lw], 23552\ns_add_u32 s44, %[wo], 2818048\nbuffer_load_dwordx4 v92, %[rw], s44 offen nt lds\ns_waitcnt vmcnt(32)\nds_read_b64_tr_b16 a[32:33], v93 offset:0\nds_read_b64_tr_b16 a[34:35], v93 offset:512\n" \
"ds_read_b64_tr_b16 v[84:85], v93 offset:8\nds_read_b64_tr_b16 v[86:87], v93 offset:520\nds_read_b64_tr_b16 a[40:41], v93 offset:64\nds_read_b64_tr_b16 a[42:43], v93 offset:576\nds_read_b64_tr_b16 a[48:49], v93 offset:72\nds_read_b64_tr_b16 a[50:51], v93 offset:584\ns_waitcnt lgkmcnt(0)\ns_add_u32 m0, %[tb], 0\n" \
"s_add_u32 s44, %[wo], 2883584\nbuffer_load_dwordx4 v92, %[rw], s44 offen nt lds\ns_add_u32 m0, %[tb], 1024\ns_add_u32 s44, %[wo], 2949120\nbuffer_load_dwordx4 v92, %[rw], s44 offen nt lds\ns_add_u32 m0, %[tb], 2048\ns_add_u32 s44, %[wo], 3014656\nbuffer_load_dwordx4 v92, %[rw], s44 offen nt lds\n" \
"s_add_u32 m0, %[tb], 3072\ns_add_u32 s44, %[wo], 3080192\nbuffer_load_dwordx4 v92, %[rw], s44 offen nt lds\ns_waitcnt vmcnt(32)\nds_read_b64_tr_b16 a[36:37], v93 offset:4096\nds_read_b64_tr_b16 a[38:39], v93 offset:4608\nds_read_b64_tr_b16 v[80:81], v93 offset:4104\nds_read_b64_tr_b16 v[82:83], v93 offset:4616\n" \
"ds_read_b64_tr_b16 a[44:45], v93 offset:4160\nds_read_b64_tr_b16 a[46:47], v93 offset:4672\nds_read_b64_tr_b16 a[52:53], v93 offset:4168\nds_read_b64_tr_b16 a[54:55], v93 offset:4680\ns_waitcnt lgkmcnt(0)\ns_add_u32 m0, %[tb], 4096\ns_add_u32 s44, %[wo], 3145728\nbuffer_load_dwordx4 v92, %[rw], s44 offen nt lds\n" \
"s_add_u32 m0, %[tb], 5120\ns_add_u32 s44, %[wo], 3211264\nbuffer_load_dwordx4 v92, %[rw], s44 offen nt lds\ns_add_u32 m0, %[tb], 6144\ns_add_u32 s44, %[wo], 3276800\nbuffer_load_dwordx4 v92, %[rw], s44 offen nt lds\ns_add_u32 m0, %[tb], 7168\ns_add_u32 s44, %[wo], 3342336\n" \
"buffer_load_dwordx4 v92, %[rw], s44 offen nt lds\ns_waitcnt vmcnt(32)\nds_read_b64_tr_b16 a[56:57], v93 offset:8192\nds_read_b64_tr_b16 a[58:59], v93 offset:8704\nds_read_b64_tr_b16 a[64:65], v93 offset:8200\nds_read_b64_tr_b16 a[66:67], v93 offset:8712\nds_read_b64_tr_b16 v[76:77], v93 offset:8256\nds_read_b64_tr_b16 v[78:79], v93 offset:8768\n" \
"ds_read_b64_tr_b16 a[72:73], v93 offset:8264\nds_read_b64_tr_b16 a[74:75], v93 offset:8776\ns_waitcnt lgkmcnt(0)\ns_add_u32 m0, %[tb], 8192\ns_add_u32 s44, %[wo], 3407872\nbuffer_load_dwordx4 v92, %[rw], s44 offen nt lds\ns_add_u32 m0, %[tb], 9216\ns_add_u32 s44, %[wo], 3473408\n" \
"buffer_load_dwordx4 v92, %[rw], s44 offen nt lds\ns_add_u32 m0, %[tb], 10240\ns_add_u32 s44, %[wo], 3538944\nbuffer_load_dwordx4 v92, %[rw], s44 offen nt lds\ns_add_u32 m0, %[tb], 11264\ns_add_u32 s44, %[wo], 3604480\nbuffer_load_dwordx4 v92, %[rw], s44 offen nt lds\ns_waitcnt vmcnt(32)\n" \
"ds_read_b64_tr_b16 a[60:61], v94 offset:0\nds_read_b64_tr_b16 a[62:63], v94 offset:512\nds_read_b64_tr_b16 a[68:69], v94 offset:8\nds_read_b64_tr_b16 a[70:71], v94 offset:520\nds_read_b64_tr_b16 v[72:73], v94 offset:64\nds_read_b64_tr_b16 v[74:75], v94 offset:576\nds_read_b64_tr_b16 a[76:77], v94 offset:72\nds_read_b64_tr_b16 a[78:79], v94 offset:584\n" \
"s_waitcnt lgkmcnt(0)\ns_add_u32 m0, %[lw], 0\ns_add_u32 s44, %[wo], 3670016\nbuffer_load_dwordx4 v92, %[rw], s44 offen nt lds\ns_add_u32 m0, %[lw], 1024\ns_add_u32 s44, %[wo], 3735552\nbuffer_load_dwordx4 v92, %[rw], s44 offen nt lds\ns_add_u32 m0, %[lw], 2048\n" \
"s_add_u32 s44, %[wo], 3801088\nbuffer_load_dwordx4 v92, %[rw], s44 offen nt lds\ns_add_u32 m0, %[lw], 3072\ns_add_u32 s44, %[wo], 3866624\nbuffer_load_dwordx4 v92, %[rw], s44 offen nt lds\ns_waitcnt vmcnt(32)\nds_read_b64_tr_b16 a[80:81], v94 offset:4096\nds_read_b64_tr_b16 a[82:83], v94 offset:4608\n" \
"ds_read_b64_tr_b16 a[88:89], v94 offset:4104\nds_read_b64_tr_b16 a[90:91], v94 offset:4616\nds_read_b64_tr_b16 a[96:97], v94 offset:4160\nds_read_b64_tr_b16 a[98:99], v94 offset:4672\nds_read_b64_tr_b16 v[68:69], v94 offset:4168\nds_read_b64_tr_b16 v[70:71], v94 offset:4680\ns_waitcnt lgkmcnt(0)\ns_add_u32 m0, %[lw], 4096\n" \
"s_add_u32 s44, %[wo], 3932160\nbuffer_load_dwordx4 v92, %[rw], s44 offen nt lds\ns_add_u32 m0, %[lw], 5120\ns_add_u32 s44, %[wo], 3997696\nbuffer_load_dwordx4 v92, %[rw], s44 offen nt lds\ns_add_u32 m0, %[lw], 6144\ns_add_u32 s44, %[wo], 4063232\nbuffer_load_dwordx4 v92, %[rw], s44 offen nt lds\n" \
"s_add_u32 m0, %[lw], 7168\ns_add_u32 s44, %[wo], 4128768\nbuffer_load_dwordx4 v92, %[rw], s44 offen nt lds\ns_waitcnt vmcnt(32)\nds_read_b64_tr_b16 a[84:85], v94 offset:8192\nds_read_b64_tr_b16 a[86:87], v94 offset:8704\nds_read_b64_tr_b16 a[92:93], v94 offset:8200\nds_read_b64_tr_b16 a[94:95], v94 offset:8712\n" \
"ds_read_b64_tr_b16 a[100:101], v94 offset:8256\nds_read_b64_tr_b16 a[102:103], v94 offset:8768\nds_read_b64_tr_b16 v[64:65], v94 offset:8264\nds_read_b64_tr_b16 v[66:67], v94 offset:8776\ns_waitcnt lgkmcnt(0)\ns_add_u32 m0, %[lw], 8192\ns_add_u32 s44, %[wo], 4194304\nbuffer_load_dwordx4 v92, %[rw], s44 offen nt lds\n" \
"s_add_u32 m0, %[lw], 9216\ns_add_u32 s44, %[wo], 4259840\nbuffer_load_dwordx4 v92, %[rw], s44 offen nt lds\ns_add_u32 m0, %[lw], 10240\ns_add_u32 s44, %[wo], 4325376\nbuffer_load_dwordx4 v92, %[rw], s44 offen nt lds\ns_add_u32 m0, %[lw], 11264\ns_add_u32 s44, %[wo], 4390912\n" \
"buffer_load_dwordx4 v92, %[rw], s44 offen nt lds\ns_waitcnt vmcnt(32)\nds_read_b64_tr_b16 a[104:105], v94 offset:12288\nds_read_b64_tr_b16 a[106:107], v94 offset:12800\nds_read_b64_tr_b16 a[112:113], v94 offset:12296\nds_read_b64_tr_b16 a[114:115], v94 offset:12808\nds_read_b64_tr_b16 a[120:121], v94 offset:12352\nds_read_b64_tr_b16 a[122:123], v94 offset:12864\n" \
"ds_read_b64_tr_b16 a[128:129], v94 offset:12360\nds_read_b64_tr_b16 a[130:131], v94 offset:12872\ns_waitcnt lgkmcnt(0)\ns_add_u32 m0, %[lw], 12288\ns_add_u32 s44, %[wo], 4456448\nbuffer_load_dwordx4 v92, %[rw], s44 offen nt lds\ns_add_u32 m0, %[lw], 13312\ns_add_u32 s44, %[wo], 4521984\n" \
"buffer_load_dwordx4 v92, %[rw], s44 offen nt lds\ns_add_u32 m0, %[lw], 14336\ns_add_u32 s44, %[wo], 4587520\nbuffer_load_dwordx4 v92, %[rw], s44 offen nt lds\ns_add_u32 m0, %[lw], 15360\ns_add_u32 s44, %[wo], 4653056\nbuffer_load_dwordx4 v92, %[rw], s44 offen nt lds\ns_waitcnt vmcnt(32)\n" \
"ds_read_b64_tr_b16 a[108:109], v94 offset:16384\nds_read_b64_tr_b16 a[110:111], v94 offset:16896\nds_read_b64_tr_b16 a[116:117], v94 offset:16392\nds_read_b64_tr_b16 a[118:119], v94 offset:16904\nds_read_b64_tr_b16 a[124:125], v94 offset:16448\nds_read_b64_tr_b16 a[126:127], v94 offset:16960\nds_read_b64_tr_b16 a[132:133], v94 offset:16456\nds_read_b64_tr_b16 a[134:135], v94 offset:16968\n" \
"s_waitcnt lgkmcnt(0)\ns_add_u32 m0, %[lw], 16384\ns_add_u32 s44, %[wo], 4718592\nbuffer_load_dwordx4 v92, %[rw], s44 offen nt lds\ns_add_u32 m0, %[lw], 17408\ns_add_u32 s44, %[wo], 4784128\nbuffer_load_dwordx4 v92, %[rw], s44 offen nt lds\ns_add_u32 m0, %[lw], 18432\n" \
"s_add_u32 s44, %[wo], 4849664\nbuffer_load_dwordx4 v92, %[rw], s44 offen nt lds\ns_add_u32 m0, %[lw], 19456\ns_add_u32 s44, %[wo], 4915200\nbuffer_load_dwordx4 v92, %[rw], s44 offen nt lds\ns_waitcnt vmcnt(32)\nds_read_b64_tr_b16 a[136:137], v94 offset:20480\nds_read_b64_tr_b16 a[138:139], v94 offset:20992\n" \
"ds_read_b64_tr_b16 v[60:61], v94 offset:20488\nds_read_b64_tr_b16 v[62:63], v94 offset:21000\nds_read_b64_tr_b16 a[144:145], v94 offset:20544\nds_read_b64_tr_b16 a[146:147], v94 offset:21056\nds_read_b64_tr_b16 a[152:153], v94 offset:20552\nds_read_b64_tr_b16 a[154:155], v94 offset:21064\ns_waitcnt lgkmcnt(0)\ns_add_u32 m0, %[lw], 20480\n" \
"s_add_u32 s44, %[wo], 4980736\nbuffer_load_dwordx4 v92, %[rw], s44 offen nt lds\ns_add_u32 m0, %[lw], 21504\ns_add_u32 s44, %[wo], 5046272\nbuffer_load_dwordx4 v92, %[rw], s44 offen nt lds\ns_add_u32 m0, %[lw], 22528\ns_add_u32 s44, %[wo], 5111808\nbuffer_load_dwordx4 v92, %[rw], s44 offen nt lds\n" \
"s_add_u32 m0, %[lw], 23552\ns_add_u32 s44, %[wo], 5177344\nbuffer_load_dwordx4 v92, %[rw], s44 offen nt lds\ns_waitcnt vmcnt(32)\nds_read_b64_tr_b16 a[140:141], v93 offset:0\nds_read_b64_tr_b16 a[142:143], v93 offset:512\nds_read_b64_tr_b16 v[56:57], v93 offset:8\nds_read_b64_tr_b16 v[58:59], v93 offset:520\n" \
"ds_read_b64_tr_b16 a[148:149], v93 offset:64\nds_read_b64_tr_b16 a[150:151], v93 offset:576\nds_read_b64_tr_b16 a[156:157], v93 offset:72\nds_read_b64_tr_b16 a[158:159], v93 offset:584\ns_waitcnt lgkmcnt(0)\ns_add_u32 m0, %[tb], 0\ns_add_u32 s44, %[wo], 5242880\nbuffer_load_dwordx4 v92, %[rw], s44 offen nt lds\n" \
"s_add_u32 m0, %[tb], 1024\ns_add_u32 s44, %[wo], 5308416\nbuffer_load_dwordx4 v92, %[rw], s44 offen nt lds\ns_add_u32 m0, %[tb], 2048\ns_add_u32 s44, %[wo], 5373952\nbuffer_load_dwordx4 v92, %[rw], s44 offen nt lds\ns_add_u32 m0, %[tb], 3072\ns_add_u32 s44, %[wo], 5439488\n" \
"buffer_load_dwordx4 v92, %[rw], s44 offen nt lds\ns_waitcnt vmcnt(32)\nds_read_b64_tr_b16 a[160:161], v93 offset:4096\nds_read_b64_tr_b16 a[162:163], v93 offset:4608\nds_read_b64_tr_b16 a[168:169], v93 offset:4104\nds_read_b64_tr_b16 a[170:171], v93 offset:4616\nds_read_b64_tr_b16 v[52:53], v93 offset:4160\nds_read_b64_tr_b16 v[54:55], v93 offset:4672\n" \
"ds_read_b64_tr_b16 a[176:177], v93 offset:4168\nds_read_b64_tr_b16 a[178:179], v93 offset:4680\ns_waitcnt lgkmcnt(0)\ns_add_u32 m0, %[tb], 4096\ns_add_u32 s44, %[wo], 5505024\nbuffer_load_dwordx4 v92, %[rw], s44 offen nt lds\ns_add_u32 m0, %[tb], 5120\ns_add_u32 s44, %[wo], 5570560\n" \
"buffer_load_dwordx4 v92, %[rw], s44 offen nt lds\ns_add_u32 m0, %[tb], 6144\ns_add_u32 s44, %[wo], 5636096\nbuffer_load_dwordx4 v92, %[rw], s44 offen nt lds\ns_add_u32 m0, %[tb], 7168\ns_add_u32 s44, %[wo], 5701632\nbuffer_load_dwordx4 v92, %[rw], s44 offen nt lds\ns_waitcnt vmcnt(32)\n" \
"ds_read_b64_tr_b16 a[164:165], v93 offset:8192\nds_read_b64_tr_b16 a[166:167], v93 offset:8704\nds_read_b64_tr_b16 a[172:173], v93 offset:8200\nds_read_b64_tr_b16 a[174:175], v93 offset:8712\nds_read_b64_tr_b16 v[48:49], v93 offset:8256\nds_read_b64_tr_b16 v[50:51], v93 offset:8768\nds_read_b64_tr_b16 a[180:181], v93 offset:8264\nds_read_b64_tr_b16 a[182:183], v93 offset:8776\n" \
"s_waitcnt lgkmcnt(0)\ns_add_u32 m0, %[tb], 8192\ns_add_u32 s44, %[wo], 5767168\nbuffer_load_dwordx4 v92, %[rw], s44 offen nt lds\ns_add_u32 m0, %[tb], 9216\ns_add_u32 s44, %[wo], 5832704\nbuffer_load_dwordx4 v92, %[rw], s44 offen nt lds\ns_add_u32 m0, %[tb], 10240\n" \
"s_add_u32 s44, %[wo], 5898240\nbuffer_load_dwordx4 v92, %[rw], s44 offen nt lds\ns_add_u32 m0, %[tb], 11264\ns_add_u32 s44, %[wo], 5963776\nbuffer_load_dwordx4 v92, %[rw], s44 offen nt lds\ns_waitcnt vmcnt(32)\nds_read_b64_tr_b16 a[184:185], v94 offset:0\nds_read_b64_tr_b16 a[186:187], v94 offset:512\n" \
"ds_read_b64_tr_b16 a[192:193], v94 offset:8\nds_read_b64_tr_b16 a[194:195], v94 offset:520\nds_read_b64_tr_b16 a[200:201], v94 offset:64\nds_read_b64_tr_b16 a[202:203], v94 offset:576\nds_read_b64_tr_b16 v[44:45], v94 offset:72\nds_read_b64_tr_b16 v[46:47], v94 offset:584\ns_waitcnt lgkmcnt(0)\ns_add_u32 m0, %[lw], 0\n" \
"s_add_u32 s44, %[wo], 6029312\nbuffer_load_dwordx4 v92, %[rw], s44 offen nt lds\ns_add_u32 m0, %[lw], 1024\ns_add_u32 s44, %[wo], 6094848\nbuffer_load_dwordx4 v92, %[rw], s44 offen nt lds\ns_add_u32 m0, %[lw], 2048\ns_add_u32 s44, %[wo], 6160384\nbuffer_load_dwordx4 v92, %[rw], s44 offen nt lds\n" \
"s_add_u32 m0, %[lw], 3072\ns_add_u32 s44, %[wo], 6225920\nbuffer_load_dwordx4 v92, %[rw], s44 offen nt lds\ns_waitcnt vmcnt(32)\nds_read_b64_tr_b16 a[188:189], v94 offset:4096\nds_read_b64_tr_b16 a[190:191], v94 offset:4608\nds_read_b64_tr_b16 a[196:197], v94 offset:4104\nds_read_b64_tr_b16 a[198:199], v94 offset:4616\n" \
"ds_read_b64_tr_b16 a[204:205], v94 offset:4160\nds_read_b64_tr_b16 a[206:207], v94 offset:4672\nds_read_b64_tr_b16 v[40:41], v94 offset:4168\nds_read_b64_tr_b16 v[42:43], v94 offset:4680\ns_waitcnt lgkmcnt(0)\ns_add_u32 m0, %[lw], 4096\ns_add_u32 s44, %[wo], 6291456\nbuffer_load_dwordx4 v92, %[rw], s44 offen nt lds\n" \
"s_add_u32 m0, %[lw], 5120\ns_add_u32 s44, %[wo], 6356992\nbuffer_load_dwordx4 v92, %[rw], s44 offen nt lds\ns_add_u32 m0, %[lw], 6144\ns_add_u32 s44, %[wo], 6422528\nbuffer_load_dwordx4 v92, %[rw], s44 offen nt lds\ns_add_u32 m0, %[lw], 7168\ns_add_u32 s44, %[wo], 6488064\n" \
"buffer_load_dwordx4 v92, %[rw], s44 offen nt lds\ns_waitcnt vmcnt(32)\nds_read_b64_tr_b16 a[208:209], v94 offset:8192\nds_read_b64_tr_b16 a[210:211], v94 offset:8704\nds_read_b64_tr_b16 a[216:217], v94 offset:8200\nds_read_b64_tr_b16 a[218:219], v94 offset:8712\nds_read_b64_tr_b16 a[224:225], v94 offset:8256\nds_read_b64_tr_b16 a[226:227], v94 offset:8768\n" \
"ds_read_b64_tr_b16 a[232:233], v94 offset:8264\nds_read_b64_tr_b16 a[234:235], v94 offset:8776\ns_waitcnt lgkmcnt(0)\ns_add_u32 m0, %[lw], 8192\ns_add_u32 s44, %[wo], 6553600\nbuffer_load_dwordx4 v92, %[rw], s44 offen nt lds\ns_add_u32 m0, %[lw], 9216\ns_add_u32 s44, %[wo], 6619136\n" \
"buffer_load_dwordx4 v92, %[rw], s44 offen nt lds\ns_add_u32 m0, %[lw], 10240\ns_add_u32 s44, %[wo], 6684672\nbuffer_load_dwordx4 v92, %[rw], s44 offen nt lds\ns_add_u32 m0, %[lw], 11264\ns_add_u32 s44, %[wo], 6750208\nbuffer_load_dwordx4 v92, %[rw], s44 offen nt lds\ns_waitcnt vmcnt(32)\n" \
"ds_read_b64_tr_b16 a[212:213], v94 offset:12288\nds_read_b64_tr_b16 a[214:215], v94 offset:12800\nds_read_b64_tr_b16 a[220:221], v94 offset:12296\nds_read_b64_tr_b16 a[222:223], v94 offset:12808\nds_read_b64_tr_b16 a[228:229], v94 offset:12352\nds_read_b64_tr_b16 a[230:231], v94 offset:12864\nds_read_b64_tr_b16 a[236:237], v94 offset:12360\nds_read_b64_tr_b16 a[238:239], v94 offset:12872\n" \
"s_waitcnt lgkmcnt(0)\nbuffer_load_dwordx2 v[88:89], off, s[36:39], s45 sc1\ns_add_u32 m0, %[lw], 12288\ns_add_u32 s44, %[wo], 6815744\nbuffer_load_dwordx4 v92, %[rw], s44 offen nt lds\ns_add_u32 m0, %[lw], 13312\ns_add_u32 s44, %[wo], 6881280\nbuffer_load_dwordx4 v92, %[rw], s44 offen nt lds\n" \
"s_add_u32 m0, %[lw], 14336\ns_add_u32 s44, %[wo], 6946816\nbuffer_load_dwordx4 v92, %[rw], s44 offen nt lds\ns_add_u32 m0, %[lw], 15360\ns_add_u32 s44, %[wo], 7012352\nbuffer_load_dwordx4 v92, %[rw], s44 offen nt lds\ns_waitcnt vmcnt(33)\nds_read_b64_tr_b16 a[240:241], v94 offset:16384\n" \
"ds_read_b64_tr_b16 a[242:243], v94 offset:16896\nds_read_b64_tr_b16 v[36:37], v94 offset:16392\nds_read_b64_tr_b16 v[38:39], v94 offset:16904\nds_read_b64_tr_b16 a[248:249], v94 offset:16448\nds_read_b64_tr_b16 a[250:251], v94 offset:16960\nds_read_b64_tr_b16 v[96:97], v94 offset:16456\nds_read_b64_tr_b16 v[98:99], v94 offset:16968\ns_waitcnt lgkmcnt(0)\n" \
"s_add_u32 m0, %[lw], 16384\ns_add_u32 s44, %[wo], 7077888\nbuffer_load_dwordx4 v92, %[rw], s44 offen nt lds\ns_add_u32 m0, %[lw], 17408\ns_add_u32 s44, %[wo], 7143424\nbuffer_load_dwordx4 v92, %[rw], s44 offen nt lds\ns_add_u32 m0, %[lw], 18432\ns_add_u32 s44, %[wo], 7208960\n" \
"buffer_load_dwordx4 v92, %[rw], s44 offen nt lds\ns_add_u32 m0, %[lw], 19456\ns_add_u32 s44, %[wo], 7274496\nbuffer_load_dwordx4 v92, %[rw], s44 offen nt lds\ns_waitcnt vmcnt(33)\nds_read_b64_tr_b16 a[244:245], v94 offset:20480\nds_read_b64_tr_b16 a[246:247], v94 offset:20992\nds_read_b64_tr_b16 v[32:33], v94 offset:20488\n" \
"ds_read_b64_tr_b16 v[34:35], v94 offset:21000\nds_read_b64_tr_b16 a[252:253], v94 offset:20544\nds_read_b64_tr_b16 a[254:255], v94 offset:21056\nds_read_b64_tr_b16 v[100:101], v94 offset:20552\nds_read_b64_tr_b16 v[102:103], v94 offset:21064\ns_waitcnt lgkmcnt(0)\ns_add_u32 m0, %[lw], 20480\ns_add_u32 s44, %[wo], 7340032\n" \
"buffer_load_dwordx4 v92, %[rw], s44 offen nt lds\ns_add_u32 m0, %[lw], 21504\ns_add_u32 s44, %[wo], 7405568\nbuffer_load_dwordx4 v92, %[rw], s44 offen nt lds\ns_add_u32 m0, %[lw], 22528\ns_add_u32 s44, %[wo], 7471104\nbuffer_load_dwordx4 v92, %[rw], s44 offen nt lds\ns_add_u32 m0, %[lw], 23552\n" \
"s_add_u32 s44, %[wo], 7536640\nbuffer_load_dwordx4 v92, %[rw], s44 offen nt lds\ns_waitcnt vmcnt(33)\nds_read_b64_tr_b16 v[104:105], v93 offset:0\nds_read_b64_tr_b16 v[106:107], v93 offset:512\nds_read_b64_tr_b16 v[112:113], v93 offset:8\nds_read_b64_tr_b16 v[114:115], v93 offset:520\nds_read_b64_tr_b16 v[28:29], v93 offset:64\n" \
"ds_read_b64_tr_b16 v[30:31], v93 offset:576\nds_read_b64_tr_b16 v[120:121], v93 offset:72\nds_read_b64_tr_b16 v[122:123], v93 offset:584\ns_waitcnt lgkmcnt(0)\ns_add_u32 m0, %[tb], 0\ns_add_u32 s44, %[wo], 7602176\nbuffer_load_dwordx4 v92, %[rw], s44 offen nt lds\ns_add_u32 m0, %[tb], 1024\n" \
"s_add_u32 s44, %[wo], 7667712\nbuffer_load_dwordx4 v92, %[rw], s44 offen nt lds\ns_add_u32 m0, %[tb], 2048\ns_add_u32 s44, %[wo], 7733248\nbuffer_load_dwordx4 v92, %[rw], s44 offen nt lds\ns_add_u32 m0, %[tb], 3072\ns_add_u32 s44, %[wo], 7798784\nbuffer_load_dwordx4 v92, %[rw], s44 offen nt lds\n" \
"s_waitcnt vmcnt(33)\nds_read_b64_tr_b16 v[108:109], v93 offset:4096\nds_read_b64_tr_b16 v[110:111], v93 offset:4608\nds_read_b64_tr_b16 v[116:117], v93 offset:4104\nds_read_b64_tr_b16 v[118:119], v93 offset:4616\nds_read_b64_tr_b16 v[24:25], v93 offset:4160\nds_read_b64_tr_b16 v[26:27], v93 offset:4672\nds_read_b64_tr_b16 v[124:125], v93 offset:4168\n" \
"ds_read_b64_tr_b16 v[126:127], v93 offset:4680\ns_waitcnt lgkmcnt(0)\ns_add_u32 m0, %[tb], 4096\ns_add_u32 s44, %[wo], 7864320\nbuffer_load_dwordx4 v92, %[rw], s44 offen nt lds\ns_add_u32 m0, %[tb], 5120\ns_add_u32 s44, %[wo], 7929856\nbuffer_load_dwordx4 v92, %[rw], s44 offen nt lds\n" \
"s_add_u32 m0, %[tb], 6144\ns_add_u32 s44, %[wo], 7995392\nbuffer_load_dwordx4 v92, %[rw], s44 offen nt lds\ns_add_u32 m0, %[tb], 7168\ns_add_u32 s44, %[wo], 8060928\nbuffer_load_dwordx4 v92, %[rw], s44 offen nt lds\ns_waitcnt vmcnt(33)\nds_read_b64_tr_b16 v[128:129], v93 offset:8192\n" \
"ds_read_b64_tr_b16 v[130:131], v93 offset:8704\nds_read_b64_tr_b16 v[136:137], v93 offset:8200\nds_read_b64_tr_b16 v[138:139], v93 offset:8712\nds_read_b64_tr_b16 v[144:145], v93 offset:8256\nds_read_b64_tr_b16 v[146:147], v93 offset:8768\nds_read_b64_tr_b16 v[20:21], v93 offset:8264\nds_read_b64_tr_b16 v[22:23], v93 offset:8776\ns_waitcnt lgkmcnt(0)\n" \
"s_add_u32 m0, %[tb], 8192\ns_add_u32 s44, %[wo], 8126464\nbuffer_load_dwordx4 v92, %[rw], s44 offen nt lds\ns_add_u32 m0, %[tb], 9216\ns_add_u32 s44, %[wo], 8192000\nbuffer_load_dwordx4 v92, %[rw], s44 offen nt lds\ns_add_u32 m0, %[tb], 10240\ns_add_u32 s44, %[wo], 8257536\n" \
"buffer_load_dwordx4 v92, %[rw], s44 offen nt lds\ns_add_u32 m0, %[tb], 11264\ns_add_u32 s44, %[wo], 8323072\nbuffer_load_dwordx4 v92, %[rw], s44 offen nt lds\ns_waitcnt vmcnt(33)\nds_read_b64_tr_b16 v[132:133], v94 offset:0\nds_read_b64_tr_b16 v[134:135], v94 offset:512\nds_read_b64_tr_b16 v[140:141], v94 offset:8\n" \
"ds_read_b64_tr_b16 v[142:143], v94 offset:520\nds_read_b64_tr_b16 v[148:149], v94 offset:64\nds_read_b64_tr_b16 v[150:151], v94 offset:576\nds_read_b64_tr_b16 v[16:17], v94 offset:72\nds_read_b64_tr_b16 v[18:19], v94 offset:584\ns_waitcnt lgkmcnt(0)\nds_write_b128 v1, v[84:87] offset:0\nds_write_b128 v1, v[80:83] offset:1024\n" \
"ds_write_b128 v1, v[76:79] offset:2048\nds_write_b128 v1, v[72:75] offset:3072\ns_waitcnt vmcnt(29)\nds_read_b64_tr_b16 v[152:153], v94 offset:4096\nds_read_b64_tr_b16 v[154:155], v94 offset:4608\nds_read_b64_tr_b16 v[160:161], v94 offset:4104\nds_read_b64_tr_b16 v[162:163], v94 offset:4616\nds_read_b64_tr_b16 v[168:169], v94 offset:4160\n" \
"ds_read_b64_tr_b16 v[170:171], v94 offset:4672\nds_read_b64_tr_b16 v[176:177], v94 offset:4168\nds_read_b64_tr_b16 v[178:179], v94 offset:4680\ns_waitcnt lgkmcnt(0)\nds_write_b128 v1, v[68:71] offset:4096\nds_write_b128 v1, v[64:67] offset:5120\nds_write_b128 v1, v[60:63] offset:6144\nds_write_b128 v1, v[56:59] offset:7168\n" \
"s_waitcnt vmcnt(25)\nds_read_b64_tr_b16 v[156:157], v94 offset:8192\nds_read_b64_tr_b16 v[158:159], v94 offset:8704\nds_read_b64_tr_b16 v[164:165], v94 offset:8200\nds_read_b64_tr_b16 v[166:167], v94 offset:8712\nds_read_b64_tr_b16 v[172:173], v94 offset:8256\nds_read_b64_tr_b16 v[174:175], v94 offset:8768\nds_read_b64_tr_b16 v[180:181], v94 offset:8264\n" \
"ds_read_b64_tr_b16 v[182:183], v94 offset:8776\ns_waitcnt lgkmcnt(0)\nds_write_b128 v1, v[52:55] offset:8192\nds_write_b128 v1, v[48:51] offset:9216\nds_write_b128 v1, v[44:47] offset:10240\nds_write_b128 v1, v[40:43] offset:11264\ns_waitcnt vmcnt(20)\nds_read_b64_tr_b16 v[184:185], v94 offset:12288\n" \
"ds_read_b64_tr_b16 v[186:187], v94 offset:12800\nds_read_b64_tr_b16 v[40:41], v94 offset:12296\nds_read_b64_tr_b16 v[42:43], v94 offset:12808\nds_read_b64_tr_b16 v[192:193], v94 offset:12352\nds_read_b64_tr_b16 v[194:195], v94 offset:12864\nds_read_b64_tr_b16 v[200:201], v94 offset:12360\nds_read_b64_tr_b16 v[202:203], v94 offset:12872\ns_waitcnt lgkmcnt(0)\n" \
"ds_write_b128 v1, v[36:39] offset:12288\nds_write_b128 v1, v[32:35] offset:13312\nds_write_b128 v1, v[28:31] offset:14336\nds_write_b128 v1, v[24:27] offset:15360\ns_waitcnt vmcnt(16)\nds_read_b64_tr_b16 v[188:189], v94 offset:16384\nds_read_b64_tr_b16 v[190:191], v94 offset:16896\nds_read_b64_tr_b16 v[24:25], v94 offset:16392\n" \
"ds_read_b64_tr_b16 v[26:27], v94 offset:16904\nds_read_b64_tr_b16 v[196:197], v94 offset:16448\nds_read_b64_tr_b16 v[198:199], v94 offset:16960\nds_read_b64_tr_b16 v[204:205], v94 offset:16456\nds_read_b64_tr_b16 v[206:207], v94 offset:16968\ns_waitcnt lgkmcnt(0)\ns_mov_b32 s43, 0\ns_mov_b32 s86, 125\n" \
"s_mov_b32 s89, 0\n.Lqk_hb%=:\nv_cmp_ne_u32 vcc, v95, v88\ns_cbranch_vccnz .Lqk_hb1%=\ns_sleep 4\nbuffer_load_dwordx2 v[88:89], off, s[36:39], s45 sc1\ns_waitcnt vmcnt(0)\ns_add_u32 s43, s43, 1\n" \
"s_cmp_lt_u32 s43, 262144\ns_cbranch_scc1 .Lqk_hb%=\ns_branch .Lqk_abort%=\n.Lqk_hb1%=:\nv_readfirstlane_b32 s44, v89\ns_and_b32 s44, s44, 0x200000\ns_add_u32 s36, s36, s44\ns_addc_u32 s37, s37, 0\n" \
"ds_write_b128 v1, v[20:23] offset:16384\nds_write_b128 v1, v[16:19] offset:17408\nds_write_b128 v1, v[40:43] offset:18432\nds_write_b128 v1, v[24:27] offset:19456\ns_waitcnt vmcnt(12)\nds_read_b64_tr_b16 v[208:209], v94 offset:20480\nds_read_b64_tr_b16 v[210:211], v94 offset:20992\nds_read_b64_tr_b16 v[216:217], v94 offset:20488\n" \
"ds_read_b64_tr_b16 v[218:219], v94 offset:21000\nds_read_b64_tr_b16 v[24:25], v94 offset:20544\nds_read_b64_tr_b16 v[26:27], v94 offset:21056\nds_read_b64_tr_b16 v[224:225], v94 offset:20552\nds_read_b64_tr_b16 v[226:227], v94 offset:21064\ns_waitcnt lgkmcnt(0)\nds_write_b128 v1, v[24:27] offset:20480\ns_waitcnt vmcnt(8)\n" \
"ds_read_b64_tr_b16 v[212:213], v93 offset:0\nds_read_b64_tr_b16 v[214:215], v93 offset:512\nds_read_b64_tr_b16 v[220:221], v93 offset:8\nds_read_b64_tr_b16 v[222:223], v93 offset:520\nds_read_b64_tr_b16 v[24:25], v93 offset:64\nds_read_b64_tr_b16 v[26:27], v93 offset:576\nds_read_b64_tr_b16 v[228:229], v93 offset:72\nds_read_b64_tr_b16 v[230:231], v93 offset:584\n" \
"s_waitcnt lgkmcnt(0)\nds_write_b128 v1, v[24:27] offset:21504\ns_waitcnt vmcnt(4)\nds_read_b64_tr_b16 v[232:233], v93 offset:4096\nds_read_b64_tr_b16 v[234:235], v93 offset:4608\nds_read_b64_tr_b16 v[240:241], v93 offset:4104\nds_read_b64_tr_b16 v[242:243], v93 offset:4616\nds_read_b64_tr_b16 v[248:249], v93 offset:4160\n" \
"ds_read_b64_tr_b16 v[250:251], v93 offset:4672\nds_read_b64_tr_b16 v[24:25], v93 offset:4168\nds_read_b64_tr_b16 v[26:27], v93 offset:4680\ns_waitcnt lgkmcnt(0)\nds_write_b128 v1, v[24:27] offset:22528\ns_waitcnt vmcnt(0)\nds_read_b64_tr_b16 v[236:237], v93 offset:8192\nds_read_b64_tr_b16 v[238:239], v93 offset:8704\n" \
"ds_read_b64_tr_b16 v[244:245], v93 offset:8200\nds_read_b64_tr_b16 v[246:247], v93 offset:8712\nds_read_b64_tr_b16 v[252:253], v93 offset:8256\nds_read_b64_tr_b16 v[254:255], v93 offset:8768\nds_read_b64_tr_b16 v[24:25], v93 offset:8264\nds_read_b64_tr_b16 v[26:27], v93 offset:8776\ns_waitcnt lgkmcnt(0)\nds_write_b128 v1, v[24:27] offset:23552\n" \
"s_waitcnt lgkmcnt(0)\ns_barrier\ns_mov_b32 s40, -1\ns_mov_b32 s43, 0\ns_mov_b32 s61, 0x7fff7fff\ns_mov_b32 s58, 0x3e0293ee\ns_mov_b32 s59, 0xbe0293ee\ns_lshl_b32 s57, %[w], 12\n" \
"s_mov_b32 s62, 0xff00ff00\ns_mov_b32 s63, 0xff00ff00\ns_mov_b32 s56, 0\ns_cmp_eq_u32 %[w], 3\ns_cbranch_scc1 .Lqk_fetch0%=\n.Lqk_pwait%=:\ns_barrier\nds_read_b128 v[80:83], v2 offset:0\n" \
"ds_read_b128 v[84:87], v2 offset:2048\n.Lqk_proj%=:\ns_waitcnt lgkmcnt(1)\nv_mfma_f32_16x16x32_bf16 v[16:19], a[0:3], v[80:83], 0\ns_waitcnt lgkmcnt(0)\nds_read_b128 v[88:91], v2 offset:256\nv_mfma_f32_16x16x32_bf16 v[16:19], a[4:7], v[84:87], v[16:19]\nds_read_b128 v[92:95], v2 offset:2304\n" \
"v_mfma_f32_16x16x32_bf16 v[20:23], a[8:11], v[80:83], 0\nv_mfma_f32_16x16x32_bf16 v[20:23], a[12:15], v[84:87], v[20:23]\nv_mfma_f32_16x16x32_bf16 v[24:27], a[16:19], v[80:83], 0\nds_read_b128 v[72:75], v1 offset:0\nv_mfma_f32_16x16x32_bf16 v[24:27], a[20:23], v[84:87], v[24:27]\nds_read_b128 v[76:79], v1 offset:1024\nv_mfma_f32_16x16x32_bf16 v[28:31], a[24:27], v[80:83], 0\nv_mfma_f32_16x16x32_bf16 v[28:31], a[28:31], v[84:87], v[28:31]\n" \
"s_waitcnt lgkmcnt(3)\nv_mfma_f32_16x16x32_bf16 v[32:35], a[0:3], v[88:91], 0\ns_waitcnt lgkmcnt(2)\nds_read_b128 v[80:83], v2 offset:4096\nv_mfma_f32_16x16x32_bf16 v[32:35], a[4:7], v[92:95], v[32:35]\nds_read_b128 v[84:87], v2 offset:6144\nv_mfma_f32_16x16x32_bf16 v[36:39], a[8:11], v[88:91], 0\nv_mfma_f32_16x16x32_bf16 v[36:39], a[12:15], v[92:95], v[36:39]\n" \
"v_mfma_f32_16x16x32_bf16 v[40:43], a[16:19], v[88:91], 0\nv_mfma_f32_16x16x32_bf16 v[40:43], a[20:23], v[92:95], v[40:43]\nv_mfma_f32_16x16x32_bf16 v[44:47], a[24:27], v[88:91], 0\nv_mfma_f32_16x16x32_bf16 v[44:47], a[28:31], v[92:95], v[44:47]\ns_waitcnt lgkmcnt(1)\nv_mfma_f32_16x16x32_bf16 v[16:19], a[32:35], v[80:83], v[16:19]\ns_waitcnt lgkmcnt(0)\nds_read_b128 v[88:91], v2 offset:4352\n" \
"v_mfma_f32_16x16x32_bf16 v[16:19], a[36:39], v[84:87], v[16:19]\nds_read_b128 v[92:95], v2 offset:6400\nv_mfma_f32_16x16x32_bf16 v[20:23], v[72:75], v[80:83], v[20:23]\nv_mfma_f32_16x16x32_bf16 v[20:23], v[76:79], v[84:87], v[20:23]\nv_mfma_f32_16x16x32_bf16 v[24:27], a[40:43], v[80:83], v[24:27]\nds_read_b128 v[64:67], v1 offset:2048\nv_mfma_f32_16x16x32_bf16 v[24:27], a[44:47], v[84:87], v[24:27]\nds_read_b128 v[68:71], v1 offset:3072\n" \
"v_mfma_f32_16x16x32_bf16 v[28:31], a[48:51], v[80:83], v[28:31]\nv_mfma_f32_16x16x32_bf16 v[28:31], a[52:55], v[84:87], v[28:31]\ns_waitcnt lgkmcnt(3)\nv_mfma_f32_16x16x32_bf16 v[32:35], a[32:35], v[88:91], v[32:35]\ns_waitcnt lgkmcnt(2)\nds_read_b128 v[80:83], v2 offset:8192\nv_mfma_f32_16x16x32_bf16 v[32:35], a[36:39], v[92:95], v[32:35]\nds_read_b128 v[84:87], v2 offset:10240\n" \
"v_mfma_f32_16x16x32_bf16 v[36:39], v[72:75], v[88:91], v[36:39]\nv_mfma_f32_16x16x32_bf16 v[36:39], v[76:79], v[92:95], v[36:39]\nv_mfma_f32_16x16x32_bf16 v[40:43], a[40:43], v[88:91], v[40:43]\nv_mfma_f32_16x16x32_bf16 v[40:43], a[44:47], v[92:95], v[40:43]\nv_mfma_f32_16x16x32_bf16 v[44:47], a[48:51], v[88:91], v[44:47]\nv_mfma_f32_16x16x32_bf16 v[44:47], a[52:55], v[92:95], v[44:47]\ns_waitcnt lgkmcnt(1)\nv_mfma_f32_16x16x32_bf16 v[16:19], a[56:59], v[80:83], v[16:19]\n" \
"s_waitcnt lgkmcnt(0)\nds_read_b128 v[88:91], v2 offset:8448\nv_mfma_f32_16x16x32_bf16 v[16:19], a[60:63], v[84:87], v[16:19]\nds_read_b128 v[92:95], v2 offset:10496\nv_mfma_f32_16x16x32_bf16 v[20:23], a[64:67], v[80:83], v[20:23]\nv_mfma_f32_16x16x32_bf16 v[20:23], a[68:71], v[84:87], v[20:23]\nv_mfma_f32_16x16x32_bf16 v[24:27], v[64:67], v[80:83], v[24:27]\nds_read_b128 v[72:75], v1 offset:4096\n" \
"v_mfma_f32_16x16x32_bf16 v[24:27], v[68:71], v[84:87], v[24:27]\nds_read_b128 v[76:79], v1 offset:5120\nv_mfma_f32_16x16x32_bf16 v[28:31], a[72:75], v[80:83], v[28:31]\nv_mfma_f32_16x16x32_bf16 v[28:31], a[76:79], v[84:87], v[28:31]\ns_waitcnt lgkmcnt(3)\nv_mfma_f32_16x16x32_bf16 v[32:35], a[56:59], v[88:91], v[32:35]\ns_waitcnt lgkmcnt(2)\nds_read_b128 v[80:83], v2 offset:12288\n" \
"v_mfma_f32_16x16x32_bf16 v[32:35], a[60:63], v[92:95], v[32:35]\nds_read_b128 v[84:87], v2 offset:14336\nv_mfma_f32_16x16x32_bf16 v[36:39], a[64:67], v[88:91], v[36:39]\nv_mfma_f32_16x16x32_bf16 v[36:39], a[68:71], v[92:95], v[36:39]\nv_mfma_f32_16x16x32_bf16 v[40:43], v[64:67], v[88:91], v[40:43]\nv_mfma_f32_16x16x32_bf16 v[40:43], v[68:71], v[92:95], v[40:43]\nv_mfma_f32_16x16x32_bf16 v[44:47], a[72:75], v[88:91], v[44:47]\nv_mfma_f32_16x16x32_bf16 v[44:47], a[76:79], v[92:95], v[44:47]\n" \
".p2align 6\n.Lqk_pj%=:\ns_waitcnt lgkmcnt(1)\nv_mfma_f32_16x16x32_bf16 v[16:19], a[80:83], v[80:83], v[16:19]\ns_waitcnt lgkmcnt(0)\nds_read_b128 v[88:91], v2 offset:12544\nv_mfma_f32_16x16x32_bf16 v[16:19], a[84:87], v[84:87], v[16:19]\ns_add_u32 s40, s40, 1\n" \
"ds_read_b128 v[92:95], v2 offset:14592\nv_mfma_f32_16x16x32_bf16 v[20:23], a[88:91], v[80:83], v[20:23]\ns_lshl_b32 s44, s40, 17\nv_mfma_f32_16x16x32_bf16 v[20:23], a[92:95], v[84:87], v[20:23]\ns_add_u32 s44, s44, %[xn]\nv_mfma_f32_16x16x32_bf16 v[24:27], a[96:99], v[80:83], v[24:27]\nv_mfma_f32_16x16x32_bf16 v[24:27], a[100:103], v[84:87], v[24:27]\ns_lshr_b32 s45, s44, 8\n" \
"v_mfma_f32_16x16x32_bf16 v[28:31], v[72:75], v[80:83], v[28:31]\ns_and_b32 s45, s45, 0x2000\nv_mfma_f32_16x16x32_bf16 v[28:31], v[76:79], v[84:87], v[28:31]\ns_xor_b32 s41, s44, s45\ns_waitcnt lgkmcnt(1)\nv_mfma_f32_16x16x32_bf16 v[32:35], a[80:83], v[88:91], v[32:35]\ns_barrier\ns_waitcnt lgkmcnt(0)\n" \
"ds_read_b128 v[80:83], v2 offset:16384\nv_mfma_f32_16x16x32_bf16 v[32:35], a[84:87], v[92:95], v[32:35]\ns_add_u32 s41, s41, %[xw]\ns_and_b32 s42, s40, 3\nds_read_b128 v[84:87], v2 offset:18432\nv_mfma_f32_16x16x32_bf16 v[36:39], a[88:91], v[88:91], v[36:39]\ns_add_u32 m0, s57, 32768\ns_add_u32 s60, s41, 65536\n" \
"buffer_load_dwordx4 v0, s[36:39], s60 offen offset:0 sc1 lds\nv_mfma_f32_16x16x32_bf16 v[36:39], a[92:95], v[92:95], v[36:39]\ns_lshl_b32 s42, s42, 15\nv_mfma_f32_16x16x32_bf16 v[40:43], a[96:99], v[88:91], v[40:43]\nv_mfma_f32_16x16x32_bf16 v[40:43], a[100:103], v[92:95], v[40:43]\ns_and_b32 s44, s40, 1\nv_mfma_f32_16x16x32_bf16 v[44:47], v[72:75], v[88:91], v[44:47]\ns_lshl_b32 s55, s44, 17\n" \
"buffer_load_dwordx4 v0, s[36:39], s60 offen offset:1024 sc1 lds\nv_mfma_f32_16x16x32_bf16 v[44:47], v[76:79], v[92:95], v[44:47]\ns_add_u32 s56, s56, %[fs]\ns_waitcnt lgkmcnt(1)\nv_mfma_f32_16x16x32_bf16 v[16:19], a[104:107], v[80:83], v[16:19]\ns_waitcnt lgkmcnt(0)\nds_read_b128 v[88:91], v2 offset:16640\nv_mfma_f32_16x16x32_bf16 v[16:19], a[108:111], v[84:87], v[16:19]\n" \
"ds_read_b128 v[92:95], v2 offset:18688\nv_mfma_f32_16x16x32_bf16 v[20:23], a[112:115], v[80:83], v[20:23]\nbuffer_load_dwordx4 v0, s[36:39], s60 offen offset:2048 sc1 lds\nv_mfma_f32_16x16x32_bf16 v[20:23], a[116:119], v[84:87], v[20:23]\nv_mfma_f32_16x16x32_bf16 v[24:27], a[120:123], v[80:83], v[24:27]\nds_read_b128 v[72:75], v1 offset:6144\nv_mfma_f32_16x16x32_bf16 v[24:27], a[124:127], v[84:87], v[24:27]\nds_read_b128 v[76:79], v1 offset:7168\n" \
"v_mfma_f32_16x16x32_bf16 v[28:31], a[128:131], v[80:83], v[28:31]\nbuffer_load_dwordx4 v0, s[36:39], s60 offen offset:3072 sc1 lds\nv_mfma_f32_16x16x32_bf16 v[28:31], a[132:135], v[84:87], v[28:31]\ns_waitcnt lgkmcnt(3)\nv_mfma_f32_16x16x32_bf16 v[32:35], a[104:107], v[88:91], v[32:35]\ns_waitcnt lgkmcnt(2)\nds_read_b128 v[80:83], v2 offset:20480\nv_mfma_f32_16x16x32_bf16 v[32:35], a[108:111], v[92:95], v[32:35]\n" \
"ds_read_b128 v[84:87], v2 offset:22528\nv_mfma_f32_16x16x32_bf16 v[36:39], a[112:115], v[88:91], v[36:39]\nv_mfma_f32_16x16x32_bf16 v[36:39], a[116:119], v[92:95], v[36:39]\nv_mfma_f32_16x16x32_bf16 v[40:43], a[120:123], v[88:91], v[40:43]\nv_mfma_f32_16x16x32_bf16 v[40:43], a[124:127], v[92:95], v[40:43]\nv_mfma_f32_16x16x32_bf16 v[44:47], a[128:131], v[88:91], v[44:47]\nv_mfma_f32_16x16x32_bf16 v[44:47], a[132:135], v[92:95], v[44:47]\ns_waitcnt lgkmcnt(1)\n" \
"v_mfma_f32_16x16x32_bf16 v[16:19], a[136:139], v[80:83], v[16:19]\ns_waitcnt lgkmcnt(0)\nds_read_b128 v[88:91], v2 offset:20736\nv_mfma_f32_16x16x32_bf16 v[16:19], a[140:143], v[84:87], v[16:19]\nds_read_b128 v[92:95], v2 offset:22784\nv_mfma_f32_16x16x32_bf16 v[20:23], v[72:75], v[80:83], v[20:23]\nv_mfma_f32_16x16x32_bf16 v[20:23], v[76:79], v[84:87], v[20:23]\nv_mfma_f32_16x16x32_bf16 v[24:27], a[144:147], v[80:83], v[24:27]\n" \
"ds_read_b128 v[64:67], v1 offset:8192\nv_mfma_f32_16x16x32_bf16 v[24:27], a[148:151], v[84:87], v[24:27]\nds_read_b128 v[68:71], v1 offset:9216\nv_mfma_f32_16x16x32_bf16 v[28:31], a[152:155], v[80:83], v[28:31]\nv_mfma_f32_16x16x32_bf16 v[28:31], a[156:159], v[84:87], v[28:31]\ns_waitcnt lgkmcnt(3)\nv_mfma_f32_16x16x32_bf16 v[32:35], a[136:139], v[88:91], v[32:35]\ns_waitcnt lgkmcnt(2)\n" \
"ds_read_b128 v[80:83], v2 offset:24576\nv_mfma_f32_16x16x32_bf16 v[32:35], a[140:143], v[92:95], v[32:35]\nds_read_b128 v[84:87], v2 offset:26624\nv_mfma_f32_16x16x32_bf16 v[36:39], v[72:75], v[88:91], v[36:39]\nv_mfma_f32_16x16x32_bf16 v[36:39], v[76:79], v[92:95], v[36:39]\nv_mfma_f32_16x16x32_bf16 v[40:43], a[144:147], v[88:91], v[40:43]\nv_mfma_f32_16x16x32_bf16 v[40:43], a[148:151], v[92:95], v[40:43]\nv_mfma_f32_16x16x32_bf16 v[44:47], a[152:155], v[88:91], v[44:47]\n" \
"v_mfma_f32_16x16x32_bf16 v[44:47], a[156:159], v[92:95], v[44:47]\ns_waitcnt lgkmcnt(1)\nv_mfma_f32_16x16x32_bf16 v[16:19], a[160:163], v[80:83], v[16:19]\ns_waitcnt lgkmcnt(0)\nds_read_b128 v[88:91], v2 offset:24832\nv_mfma_f32_16x16x32_bf16 v[16:19], a[164:167], v[84:87], v[16:19]\nds_read_b128 v[92:95], v2 offset:26880\nv_mfma_f32_16x16x32_bf16 v[20:23], a[168:171], v[80:83], v[20:23]\n" \
"v_mfma_f32_16x16x32_bf16 v[20:23], a[172:175], v[84:87], v[20:23]\nv_mfma_f32_16x16x32_bf16 v[24:27], v[64:67], v[80:83], v[24:27]\nds_read_b128 v[72:75], v1 offset:10240\nv_mfma_f32_16x16x32_bf16 v[24:27], v[68:71], v[84:87], v[24:27]\nds_read_b128 v[76:79], v1 offset:11264\nv_mfma_f32_16x16x32_bf16 v[28:31], a[176:179], v[80:83], v[28:31]\nv_mfma_f32_16x16x32_bf16 v[28:31], a[180:183], v[84:87], v[28:31]\ns_waitcnt lgkmcnt(3)\n" \
"v_mfma_f32_16x16x32_bf16 v[32:35], a[160:163], v[88:91], v[32:35]\ns_waitcnt lgkmcnt(2)\nds_read_b128 v[80:83], v2 offset:28672\nv_mfma_f32_16x16x32_bf16 v[32:35], a[164:167], v[92:95], v[32:35]\nds_read_b128 v[84:87], v2 offset:30720\nv_mfma_f32_16x16x32_bf16 v[36:39], a[168:171], v[88:91], v[36:39]\nv_mfma_f32_16x16x32_bf16 v[36:39], a[172:175], v[92:95], v[36:39]\nv_mfma_f32_16x16x32_bf16 v[40:43], v[64:67], v[88:91], v[40:43]\n" \
"v_mfma_f32_16x16x32_bf16 v[40:43], v[68:71], v[92:95], v[40:43]\nv_mfma_f32_16x16x32_bf16 v[44:47], a[176:179], v[88:91], v[44:47]\nv_mfma_f32_16x16x32_bf16 v[44:47], a[180:183], v[92:95], v[44:47]\ns_waitcnt lgkmcnt(1)\nv_mfma_f32_16x16x32_bf16 v[16:19], a[184:187], v[80:83], v[16:19]\ns_waitcnt lgkmcnt(0)\nds_read_b128 v[88:91], v2 offset:28928\nv_mfma_f32_16x16x32_bf16 v[16:19], a[188:191], v[84:87], v[16:19]\n" \
"ds_read_b128 v[92:95], v2 offset:30976\nv_mfma_f32_16x16x32_bf16 v[20:23], a[192:195], v[80:83], v[20:23]\nv_mfma_f32_16x16x32_bf16 v[20:23], a[196:199], v[84:87], v[20:23]\nv_mfma_f32_16x16x32_bf16 v[24:27], a[200:203], v[80:83], v[24:27]\nv_mfma_f32_16x16x32_bf16 v[24:27], a[204:207], v[84:87], v[24:27]\nv_mfma_f32_16x16x32_bf16 v[28:31], v[72:75], v[80:83], v[28:31]\nv_mfma_f32_16x16x32_bf16 v[28:31], v[76:79], v[84:87], v[28:31]\ns_waitcnt lgkmcnt(1)\n" \
"v_mfma_f32_16x16x32_bf16 v[32:35], a[184:187], v[88:91], v[32:35]\ns_waitcnt vmcnt(0)\ns_barrier\ns_waitcnt lgkmcnt(0)\nds_read_b128 v[48:51], v2 offset:32768\nv_mfma_f32_16x16x32_bf16 v[32:35], a[188:191], v[92:95], v[32:35]\ns_add_u32 m0, s57, 0\nds_read_b128 v[52:55], v2 offset:34816\n" \
"v_mfma_f32_16x16x32_bf16 v[36:39], a[192:195], v[88:91], v[36:39]\ns_add_u32 s60, s41, 98304\nbuffer_load_dwordx4 v0, s[36:39], s60 offen offset:0 sc1 lds\nv_mfma_f32_16x16x32_bf16 v[36:39], a[196:199], v[92:95], v[36:39]\nv_mfma_f32_16x16x32_bf16 v[40:43], a[200:203], v[88:91], v[40:43]\nv_mfma_f32_16x16x32_bf16 v[40:43], a[204:207], v[92:95], v[40:43]\nv_mfma_f32_16x16x32_bf16 v[44:47], v[72:75], v[88:91], v[44:47]\nbuffer_load_dwordx4 v0, s[36:39], s60 offen offset:1024 sc1 lds\n" \
"v_mfma_f32_16x16x32_bf16 v[44:47], v[76:79], v[92:95], v[44:47]\ns_waitcnt lgkmcnt(1)\nv_mfma_f32_16x16x32_bf16 v[16:19], a[208:211], v[48:51], v[16:19]\ns_waitcnt lgkmcnt(0)\nds_read_b128 v[56:59], v2 offset:33024\nv_mfma_f32_16x16x32_bf16 v[16:19], a[212:215], v[52:55], v[16:19]\nds_read_b128 v[60:63], v2 offset:35072\nv_mfma_f32_16x16x32_bf16 v[20:23], a[216:219], v[48:51], v[20:23]\n" \
"buffer_load_dwordx4 v0, s[36:39], s60 offen offset:2048 sc1 lds\nv_mfma_f32_16x16x32_bf16 v[20:23], a[220:223], v[52:55], v[20:23]\nv_mfma_f32_16x16x32_bf16 v[24:27], a[224:227], v[48:51], v[24:27]\nds_read_b128 v[72:75], v1 offset:12288\nv_mfma_f32_16x16x32_bf16 v[24:27], a[228:231], v[52:55], v[24:27]\nds_read_b128 v[76:79], v1 offset:13312\nv_mfma_f32_16x16x32_bf16 v[28:31], a[232:235], v[48:51], v[28:31]\nbuffer_load_dwordx4 v0, s[36:39], s60 offen offset:3072 sc1 lds\n" \
"v_mfma_f32_16x16x32_bf16 v[28:31], a[236:239], v[52:55], v[28:31]\ns_waitcnt lgkmcnt(3)\nv_mfma_f32_16x16x32_bf16 v[32:35], a[208:211], v[56:59], v[32:35]\ns_waitcnt lgkmcnt(2)\nds_read_b128 v[48:51], v2 offset:36864\nv_mfma_f32_16x16x32_bf16 v[32:35], a[212:215], v[60:63], v[32:35]\nds_read_b128 v[52:55], v2 offset:38912\nv_mfma_f32_16x16x32_bf16 v[36:39], a[216:219], v[56:59], v[36:39]\n" \
"v_mfma_f32_16x16x32_bf16 v[36:39], a[220:223], v[60:63], v[36:39]\nv_mfma_f32_16x16x32_bf16 v[40:43], a[224:227], v[56:59], v[40:43]\nv_mfma_f32_16x16x32_bf16 v[40:43], a[228:231], v[60:63], v[40:43]\nv_mfma_f32_16x16x32_bf16 v[44:47], a[232:235], v[56:59], v[44:47]\nv_mfma_f32_16x16x32_bf16 v[44:47], a[236:239], v[60:63], v[44:47]\ns_waitcnt lgkmcnt(1)\nv_mfma_f32_16x16x32_bf16 v[16:19], a[240:243], v[48:51], v[16:19]\ns_waitcnt lgkmcnt(0)\n" \
"ds_read_b128 v[56:59], v2 offset:37120\nv_mfma_f32_16x16x32_bf16 v[16:19], a[244:247], v[52:55], v[16:19]\nds_read_b128 v[60:63], v2 offset:39168\nv_mfma_f32_16x16x32_bf16 v[20:23], v[72:75], v[48:51], v[20:23]\nv_mfma_f32_16x16x32_bf16 v[20:23], v[76:79], v[52:55], v[20:23]\nv_mfma_f32_16x16x32_bf16 v[24:27], a[248:251], v[48:51], v[24:27]\nds_read_b128 v[64:67], v1 offset:14336\nv_mfma_f32_16x16x32_bf16 v[24:27], a[252:255], v[52:55], v[24:27]\n" \
"ds_read_b128 v[68:71], v1 offset:15360\nv_mfma_f32_16x16x32_bf16 v[28:31], v[96:99], v[48:51], v[28:31]\nv_mfma_f32_16x16x32_bf16 v[28:31], v[100:103], v[52:55], v[28:31]\ns_waitcnt lgkmcnt(3)\nv_mfma_f32_16x16x32_bf16 v[32:35], a[240:243], v[56:59], v[32:35]\ns_waitcnt lgkmcnt(2)\nds_read_b128 v[48:51], v2 offset:40960\nv_mfma_f32_16x16x32_bf16 v[32:35], a[244:247], v[60:63], v[32:35]\n" \
"ds_read_b128 v[52:55], v2 offset:43008\nv_mfma_f32_16x16x32_bf16 v[36:39], v[72:75], v[56:59], v[36:39]\nv_mfma_f32_16x16x32_bf16 v[36:39], v[76:79], v[60:63], v[36:39]\nv_mfma_f32_16x16x32_bf16 v[40:43], a[248:251], v[56:59], v[40:43]\nv_mfma_f32_16x16x32_bf16 v[40:43], a[252:255], v[60:63], v[40:43]\nv_mfma_f32_16x16x32_bf16 v[44:47], v[96:99], v[56:59], v[44:47]\nv_mfma_f32_16x16x32_bf16 v[44:47], v[100:103], v[60:63], v[44:47]\ns_waitcnt lgkmcnt(1)\n" \
"v_mfma_f32_16x16x32_bf16 v[16:19], v[104:107], v[48:51], v[16:19]\ns_waitcnt lgkmcnt(0)\nds_read_b128 v[56:59], v2 offset:41216\nv_mfma_f32_16x16x32_bf16 v[16:19], v[108:111], v[52:55], v[16:19]\nds_read_b128 v[60:63], v2 offset:43264\nv_mfma_f32_16x16x32_bf16 v[20:23], v[112:115], v[48:51], v[20:23]\nv_mfma_f32_16x16x32_bf16 v[20:23], v[116:119], v[52:55], v[20:23]\nv_mfma_f32_16x16x32_bf16 v[24:27], v[64:67], v[48:51], v[24:27]\n" \
"ds_read_b128 v[72:75], v1 offset:16384\nv_mfma_f32_16x16x32_bf16 v[24:27], v[68:71], v[52:55], v[24:27]\nds_read_b128 v[76:79], v1 offset:17408\nv_mfma_f32_16x16x32_bf16 v[28:31], v[120:123], v[48:51], v[28:31]\nv_mfma_f32_16x16x32_bf16 v[28:31], v[124:127], v[52:55], v[28:31]\ns_waitcnt lgkmcnt(3)\nv_mfma_f32_16x16x32_bf16 v[32:35], v[104:107], v[56:59], v[32:35]\ns_waitcnt lgkmcnt(2)\n" \
"ds_read_b128 v[48:51], v2 offset:45056\nv_mfma_f32_16x16x32_bf16 v[32:35], v[108:111], v[60:63], v[32:35]\nds_read_b128 v[52:55], v2 offset:47104\nv_mfma_f32_16x16x32_bf16 v[36:39], v[112:115], v[56:59], v[36:39]\nv_mfma_f32_16x16x32_bf16 v[36:39], v[116:119], v[60:63], v[36:39]\nv_mfma_f32_16x16x32_bf16 v[40:43], v[64:67], v[56:59], v[40:43]\nv_mfma_f32_16x16x32_bf16 v[40:43], v[68:71], v[60:63], v[40:43]\nv_mfma_f32_16x16x32_bf16 v[44:47], v[120:123], v[56:59], v[44:47]\n" \
"v_mfma_f32_16x16x32_bf16 v[44:47], v[124:127], v[60:63], v[44:47]\ns_waitcnt lgkmcnt(1)\nv_mfma_f32_16x16x32_bf16 v[16:19], v[128:131], v[48:51], v[16:19]\ns_waitcnt lgkmcnt(0)\nds_read_b128 v[56:59], v2 offset:45312\nv_mfma_f32_16x16x32_bf16 v[16:19], v[132:135], v[52:55], v[16:19]\nds_read_b128 v[60:63], v2 offset:47360\nv_mfma_f32_16x16x32_bf16 v[20:23], v[136:139], v[48:51], v[20:23]\n" \
"v_mfma_f32_16x16x32_bf16 v[20:23], v[140:143], v[52:55], v[20:23]\nv_mfma_f32_16x16x32_bf16 v[24:27], v[144:147], v[48:51], v[24:27]\nv_mfma_f32_16x16x32_bf16 v[24:27], v[148:151], v[52:55], v[24:27]\nv_mfma_f32_16x16x32_bf16 v[28:31], v[72:75], v[48:51], v[28:31]\nv_mfma_f32_16x16x32_bf16 v[28:31], v[76:79], v[52:55], v[28:31]\ns_waitcnt lgkmcnt(1)\nv_mfma_f32_16x16x32_bf16 v[32:35], v[128:131], v[56:59], v[32:35]\ns_waitcnt lgkmcnt(0)\n" \
"s_waitcnt vmcnt(0)\ns_barrier\nds_read_b128 v[48:51], v2 offset:0\nv_mfma_f32_16x16x32_bf16 v[32:35], v[132:135], v[60:63], v[32:35]\nds_read_b128 v[52:55], v2 offset:2048\nv_mfma_f32_16x16x32_bf16 v[36:39], v[136:139], v[56:59], v[36:39]\nv_mfma_f32_16x16x32_bf16 v[36:39], v[140:143], v[60:63], v[36:39]\nv_mfma_f32_16x16x32_bf16 v[40:43], v[144:147], v[56:59], v[40:43]\n" \
"v_mfma_f32_16x16x32_bf16 v[40:43], v[148:151], v[60:63], v[40:43]\nv_mfma_f32_16x16x32_bf16 v[44:47], v[72:75], v[56:59], v[44:47]\nv_mfma_f32_16x16x32_bf16 v[44:47], v[76:79], v[60:63], v[44:47]\ns_waitcnt lgkmcnt(1)\nv_mfma_f32_16x16x32_bf16 v[16:19], v[152:155], v[48:51], v[16:19]\ns_waitcnt lgkmcnt(0)\nds_read_b128 v[56:59], v2 offset:256\nv_mfma_f32_16x16x32_bf16 v[16:19], v[156:159], v[52:55], v[16:19]\n" \
"ds_read_b128 v[60:63], v2 offset:2304\nv_mfma_f32_16x16x32_bf16 v[20:23], v[160:163], v[48:51], v[20:23]\nv_mfma_f32_16x16x32_bf16 v[20:23], v[164:167], v[52:55], v[20:23]\nv_mfma_f32_16x16x32_bf16 v[24:27], v[168:171], v[48:51], v[24:27]\nds_read_b128 v[72:75], v1 offset:18432\nv_mfma_f32_16x16x32_bf16 v[24:27], v[172:175], v[52:55], v[24:27]\nds_read_b128 v[76:79], v1 offset:19456\nv_mfma_f32_16x16x32_bf16 v[28:31], v[176:179], v[48:51], v[28:31]\n" \
"v_mfma_f32_16x16x32_bf16 v[28:31], v[180:183], v[52:55], v[28:31]\ns_waitcnt lgkmcnt(3)\nv_mfma_f32_16x16x32_bf16 v[32:35], v[152:155], v[56:59], v[32:35]\ns_waitcnt lgkmcnt(2)\nds_read_b128 v[48:51], v2 offset:4096\nv_mfma_f32_16x16x32_bf16 v[32:35], v[156:159], v[60:63], v[32:35]\nds_read_b128 v[52:55], v2 offset:6144\nv_mfma_f32_16x16x32_bf16 v[36:39], v[160:163], v[56:59], v[36:39]\n" \
"v_mfma_f32_16x16x32_bf16 v[36:39], v[164:167], v[60:63], v[36:39]\nv_mfma_f32_16x16x32_bf16 v[40:43], v[168:171], v[56:59], v[40:43]\nv_mfma_f32_16x16x32_bf16 v[40:43], v[172:175], v[60:63], v[40:43]\nv_mfma_f32_16x16x32_bf16 v[44:47], v[176:179], v[56:59], v[44:47]\nv_mfma_f32_16x16x32_bf16 v[44:47], v[180:183], v[60:63], v[44:47]\ns_waitcnt lgkmcnt(1)\nv_mfma_f32_16x16x32_bf16 v[16:19], v[184:187], v[48:51], v[16:19]\ns_waitcnt lgkmcnt(0)\n" \
"ds_read_b128 v[56:59], v2 offset:4352\nv_mfma_f32_16x16x32_bf16 v[16:19], v[188:191], v[52:55], v[16:19]\nds_read_b128 v[60:63], v2 offset:6400\nv_mfma_f32_16x16x32_bf16 v[20:23], v[72:75], v[48:51], v[20:23]\nv_mfma_f32_16x16x32_bf16 v[20:23], v[76:79], v[52:55], v[20:23]\nv_mfma_f32_16x16x32_bf16 v[24:27], v[192:195], v[48:51], v[24:27]\nds_read_b128 v[64:67], v1 offset:20480\nv_mfma_f32_16x16x32_bf16 v[24:27], v[196:199], v[52:55], v[24:27]\n" \
"ds_read_b128 v[68:71], v1 offset:21504\nv_mfma_f32_16x16x32_bf16 v[28:31], v[200:203], v[48:51], v[28:31]\nv_mfma_f32_16x16x32_bf16 v[28:31], v[204:207], v[52:55], v[28:31]\ns_waitcnt lgkmcnt(3)\nv_mfma_f32_16x16x32_bf16 v[32:35], v[184:187], v[56:59], v[32:35]\ns_waitcnt lgkmcnt(2)\nds_read_b128 v[48:51], v2 offset:8192\nv_mfma_f32_16x16x32_bf16 v[32:35], v[188:191], v[60:63], v[32:35]\n" \
"ds_read_b128 v[52:55], v2 offset:10240\nv_mfma_f32_16x16x32_bf16 v[36:39], v[72:75], v[56:59], v[36:39]\nv_mfma_f32_16x16x32_bf16 v[36:39], v[76:79], v[60:63], v[36:39]\nv_mfma_f32_16x16x32_bf16 v[40:43], v[192:195], v[56:59], v[40:43]\nv_mfma_f32_16x16x32_bf16 v[40:43], v[196:199], v[60:63], v[40:43]\nv_mfma_f32_16x16x32_bf16 v[44:47], v[200:203], v[56:59], v[44:47]\nbuffer_load_dwordx4 v[80:83], v3, s[36:39], s42 offen offset:0 sc1\nv_mfma_f32_16x16x32_bf16 v[44:47], v[204:207], v[60:63], v[44:47]\n" \
"s_waitcnt lgkmcnt(1)\nv_mfma_f32_16x16x32_bf16 v[16:19], v[208:211], v[48:51], v[16:19]\ns_waitcnt lgkmcnt(0)\nds_read_b128 v[56:59], v2 offset:12288\nv_mfma_f32_16x16x32_bf16 v[16:19], v[212:215], v[52:55], v[16:19]\nds_read_b128 v[60:63], v2 offset:14336\nv_mfma_f32_16x16x32_bf16 v[20:23], v[216:219], v[48:51], v[20:23]\nbuffer_load_dwordx4 v[84:87], v3, s[36:39], s42 offen offset:1024 sc1\n" \
"v_mfma_f32_16x16x32_bf16 v[20:23], v[220:223], v[52:55], v[20:23]\nv_mfma_f32_16x16x32_bf16 v[24:27], v[64:67], v[48:51], v[24:27]\nds_read_b128 v[72:75], v1 offset:22528\nv_mfma_f32_16x16x32_bf16 v[24:27], v[68:71], v[52:55], v[24:27]\nds_read_b128 v[76:79], v1 offset:23552\nv_mfma_f32_16x16x32_bf16 v[28:31], v[224:227], v[48:51], v[28:31]\nbuffer_load_dwordx4 v[88:91], v3, s[36:39], s42 offen offset:2048 sc1\nv_mfma_f32_16x16x32_bf16 v[28:31], v[228:231], v[52:55], v[28:31]\n" \
"s_waitcnt lgkmcnt(3)\nv_mfma_f32_16x16x32_bf16 v[16:19], v[232:235], v[56:59], v[16:19]\ns_waitcnt lgkmcnt(2)\nds_read_b128 v[48:51], v2 offset:8448\nv_mfma_f32_16x16x32_bf16 v[16:19], v[236:239], v[60:63], v[16:19]\nds_read_b128 v[52:55], v2 offset:10496\nv_mfma_f32_16x16x32_bf16 v[20:23], v[240:243], v[56:59], v[20:23]\nbuffer_load_dwordx4 v[92:95], v3, s[36:39], s42 offen offset:3072 sc1\n" \
"v_mfma_f32_16x16x32_bf16 v[20:23], v[244:247], v[60:63], v[20:23]\nv_mfma_f32_16x16x32_bf16 v[24:27], v[248:251], v[56:59], v[24:27]\nv_mfma_f32_16x16x32_bf16 v[24:27], v[252:255], v[60:63], v[24:27]\ns_waitcnt lgkmcnt(3)\nv_mfma_f32_16x16x32_bf16 v[28:31], v[72:75], v[56:59], v[28:31]\ns_waitcnt lgkmcnt(2)\nv_cvt_pk_bf16_f32 v20, v16, v20\nv_cvt_pk_bf16_f32 v21, v17, v21\n" \
"v_mfma_f32_16x16x32_bf16 v[28:31], v[76:79], v[60:63], v[28:31]\ns_waitcnt lgkmcnt(1)\nv_cvt_pk_bf16_f32 v22, v18, v22\nv_cvt_pk_bf16_f32 v23, v19, v23\nv_mfma_f32_16x16x32_bf16 v[32:35], v[208:211], v[48:51], v[32:35]\ns_waitcnt lgkmcnt(0)\nds_read_b128 v[56:59], v2 offset:12544\nv_mfma_f32_16x16x32_bf16 v[32:35], v[212:215], v[52:55], v[32:35]\n" \
"ds_read_b128 v[60:63], v2 offset:14592\nv_mfma_f32_16x16x32_bf16 v[36:39], v[216:219], v[48:51], v[36:39]\nds_write_b128 v5, v[20:23] offset:0\nv_mfma_f32_16x16x32_bf16 v[36:39], v[220:223], v[52:55], v[36:39]\nv_cvt_pk_bf16_f32 v28, v24, v28\nv_cvt_pk_bf16_f32 v29, v25, v29\nv_mfma_f32_16x16x32_bf16 v[40:43], v[64:67], v[48:51], v[40:43]\nv_cvt_pk_bf16_f32 v30, v26, v30\n" \
"v_cvt_pk_bf16_f32 v31, v27, v31\nv_mfma_f32_16x16x32_bf16 v[40:43], v[68:71], v[52:55], v[40:43]\nv_mfma_f32_16x16x32_bf16 v[44:47], v[224:227], v[48:51], v[44:47]\ns_cmp_eq_u32 %[w], 1\nds_write_b128 v5, v[28:31] offset:512\nv_mfma_f32_16x16x32_bf16 v[44:47], v[228:231], v[52:55], v[44:47]\ns_cbranch_scc1 .Lqk_c1%=\ns_cmp_eq_u32 %[w], 2\n" \
"s_cbranch_scc1 .Lqk_c2%=\ns_cmp_eq_u32 %[w], 3\ns_cbranch_scc1 .Lqk_c3%=\n.Lqk_c0%=:\ns_waitcnt lgkmcnt(3)\nv_mfma_f32_16x16x32_bf16 v[32:35], v[232:235], v[56:59], v[32:35]\ns_waitcnt lgkmcnt(2)\nv_mov_b64 v[48:49], 0\n" \
"v_mov_b64 v[50:51], 0\nv_mfma_f32_16x16x32_bf16 v[32:35], v[236:239], v[60:63], v[32:35]\nds_read_b128 v[64:67], v1 offset:0\nv_mfma_f32_16x16x32_bf16 v[36:39], v[240:243], v[56:59], v[36:39]\nds_read_b128 v[68:71], v1 offset:1024\nv_mfma_f32_16x16x32_bf16 v[36:39], v[244:247], v[60:63], v[36:39]\ns_bfe_i32 s54, s40, 0x10000\nv_mov_b64 v[52:53], 0\n" \
"v_mov_b64 v[54:55], 0\nv_mfma_f32_16x16x32_bf16 v[40:43], v[248:251], v[56:59], v[40:43]\nv_mfma_f32_16x16x32_bf16 v[40:43], v[252:255], v[60:63], v[40:43]\nv_cvt_pk_bf16_f32 v36, v32, v36\nv_cvt_pk_bf16_f32 v37, v33, v37\nv_mfma_f32_16x16x32_bf16 v[44:47], v[72:75], v[56:59], v[44:47]\nv_cvt_pk_bf16_f32 v38, v34, v38\nv_cvt_pk_bf16_f32 v39, v35, v39\n" \
"v_mfma_f32_16x16x32_bf16 v[44:47], v[76:79], v[60:63], v[44:47]\nds_read_b128 v[72:75], v1 offset:2048\nds_read_b128 v[76:79], v1 offset:3072\nv_mov_b64 v[56:57], 0\nv_mov_b64 v[58:59], 0\nv_mov_b64 v[60:61], 0\nv_mov_b64 v[62:63], 0\n.Lqk_poll0%=:\n" \
"s_waitcnt vmcnt(0)\nv_smfmac_f32_16x16x64_bf16 v[48:51], v[80:83], a[0:7], v4\nds_write_b128 v0, v[80:83] offset:16384\nv_max3_u32 v16, v80, v82, v84\nv_smfmac_f32_16x16x64_bf16 v[48:51], v[84:87], a[32:39], v4\nv_max3_u32 v16, v16, v86, v88\nv_smfmac_f32_16x16x64_bf16 v[52:55], v[80:83], a[8:15], v4\ns_waitcnt lgkmcnt(3)\n" \
"ds_write_b128 v0, v[84:87] offset:17408\nv_max3_u32 v16, v16, v90, v92\nv_max_u32 v16, v16, v94\nv_smfmac_f32_16x16x64_bf16 v[52:55], v[84:87], v[64:71], v4\nv_cmp_eq_u32 vcc, -1, v16\nv_smfmac_f32_16x16x64_bf16 v[56:59], v[80:83], a[16:23], v4\nds_write_b128 v0, v[88:91] offset:18432\ns_cbranch_vccnz .Lqk_re0%=\n" \
"v_smfmac_f32_16x16x64_bf16 v[56:59], v[84:87], a[40:47], v4\nv_smfmac_f32_16x16x64_bf16 v[60:63], v[80:83], a[24:31], v4\nds_write_b128 v0, v[92:95] offset:19456\nv_smfmac_f32_16x16x64_bf16 v[60:63], v[84:87], a[48:55], v4\nv_smfmac_f32_16x16x64_bf16 v[48:51], v[88:91], a[56:63], v4\nv_smfmac_f32_16x16x64_bf16 v[48:51], v[92:95], a[80:87], v4\ns_waitcnt lgkmcnt(0)\n.Lqk_jn0%=:\n" \
"s_barrier\ns_memrealtime s[68:69]\nds_read_b128 v[80:83], v0 offset:20480\nds_read_b128 v[64:67], v1 offset:4096\nds_read_b128 v[68:71], v1 offset:5120\nv_mov_b32 v24, s40\nv_smfmac_f32_16x16x64_bf16 v[52:55], v[88:91], a[64:71], v4\nv_smfmac_f32_16x16x64_bf16 v[52:55], v[92:95], a[88:95], v4\n" \
"ds_read_b128 v[84:87], v0 offset:21504\nv_smfmac_f32_16x16x64_bf16 v[56:59], v[88:91], v[72:79], v4\nds_read_b128 v[72:75], v1 offset:6144\nds_read_b128 v[76:79], v1 offset:7168\nv_smfmac_f32_16x16x64_bf16 v[56:59], v[92:95], a[96:103], v4\nv_smfmac_f32_16x16x64_bf16 v[60:63], v[88:91], a[72:79], v4\ns_waitcnt lgkmcnt(3)\nds_read_b128 v[88:91], v0 offset:22528\n" \
"v_smfmac_f32_16x16x64_bf16 v[60:63], v[92:95], v[64:71], v4\ns_sub_u32 s88, s86, s89\nds_read_b128 v[92:95], v0 offset:23552\nds_read_b128 v[64:67], v1 offset:8192\nds_read_b128 v[68:71], v1 offset:9216\nv_smfmac_f32_16x16x64_bf16 v[48:51], v[80:83], a[104:111], v4\ns_min_u32 s88, s88, 150\ns_waitcnt lgkmcnt(6)\n" \
"ds_write_b128 v5, v[36:39] offset:256\nv_cvt_pk_bf16_f32 v44, v40, v44\nv_cvt_pk_bf16_f32 v45, v41, v45\nv_cvt_pk_bf16_f32 v46, v42, v46\nv_cvt_pk_bf16_f32 v47, v43, v47\nv_smfmac_f32_16x16x64_bf16 v[48:51], v[84:87], a[136:143], v4\ns_add_u32 s88, s88, s68\ns_mov_b32 s89, s68\n" \
"v_mov_b32 v25, s88\nv_smfmac_f32_16x16x64_bf16 v[52:55], v[80:83], a[112:119], v4\ns_waitcnt lgkmcnt(5)\nbuffer_store_dwordx2 v[24:25], off, s[36:39], %[sg] sc1\nv_smfmac_f32_16x16x64_bf16 v[52:55], v[84:87], v[72:79], v4\nds_read_b128 v[72:75], v1 offset:10240\nds_read_b128 v[76:79], v1 offset:11264\nv_smfmac_f32_16x16x64_bf16 v[56:59], v[80:83], a[120:127], v4\n" \
"v_smfmac_f32_16x16x64_bf16 v[56:59], v[84:87], a[144:151], v4\nv_smfmac_f32_16x16x64_bf16 v[60:63], v[80:83], a[128:135], v4\nds_read_b128 v[80:83], v0 offset:24576\nv_smfmac_f32_16x16x64_bf16 v[60:63], v[84:87], a[152:159], v4\ns_waitcnt lgkmcnt(7)\nds_read_b128 v[84:87], v0 offset:25600\nv_smfmac_f32_16x16x64_bf16 v[48:51], v[88:91], a[160:167], v4\ns_waitcnt lgkmcnt(7)\n" \
"ds_write_b128 v5, v[44:47] offset:768\nv_smfmac_f32_16x16x64_bf16 v[48:51], v[92:95], a[184:191], v4\nv_smfmac_f32_16x16x64_bf16 v[52:55], v[88:91], a[168:175], v4\nv_smfmac_f32_16x16x64_bf16 v[52:55], v[92:95], a[192:199], v4\ns_waitcnt lgkmcnt(6)\nv_smfmac_f32_16x16x64_bf16 v[56:59], v[88:91], v[64:71], v4\nds_read_b128 v[64:67], v1 offset:12288\nds_read_b128 v[68:71], v1 offset:13312\n" \
"v_smfmac_f32_16x16x64_bf16 v[56:59], v[92:95], a[200:207], v4\ns_waitcnt lgkmcnt(2)\ns_barrier\nv_smfmac_f32_16x16x64_bf16 v[60:63], v[88:91], a[176:183], v4\nds_read_b128 v[88:91], v0 offset:26624\nds_read_b128 v[16:19], v7 offset:0\nv_smfmac_f32_16x16x64_bf16 v[60:63], v[92:95], v[72:79], v4\nds_read_b128 v[92:95], v0 offset:27648\n" \
"ds_read_b128 v[72:75], v1 offset:14336\nds_read_b128 v[76:79], v1 offset:15360\nv_smfmac_f32_16x16x64_bf16 v[48:51], v[80:83], a[208:215], v4\nds_read_b128 v[20:23], v7 offset:64\nv_smfmac_f32_16x16x64_bf16 v[48:51], v[84:87], a[240:247], v4\nv_smfmac_f32_16x16x64_bf16 v[52:55], v[80:83], a[216:223], v4\ns_waitcnt lgkmcnt(6)\nds_read_b128 v[24:27], v7 offset:2048\n" \
"v_smfmac_f32_16x16x64_bf16 v[52:55], v[84:87], v[64:71], v4\nds_read_b128 v[64:67], v1 offset:16384\nds_read_b128 v[68:71], v1 offset:17408\nv_smfmac_f32_16x16x64_bf16 v[56:59], v[80:83], a[224:231], v4\nds_read_b128 v[28:31], v7 offset:2112\nv_smfmac_f32_16x16x64_bf16 v[56:59], v[84:87], a[248:255], v4\nv_smfmac_f32_16x16x64_bf16 v[60:63], v[80:83], a[232:239], v4\nds_read_b128 v[80:83], v0 offset:28672\n" \
"ds_read_b128 v[32:35], v7 offset:4096\nv_smfmac_f32_16x16x64_bf16 v[60:63], v[84:87], v[96:103], v4\ns_waitcnt lgkmcnt(11)\nds_read_b128 v[84:87], v0 offset:29696\nv_smfmac_f32_16x16x64_bf16 v[48:51], v[88:91], v[104:111], v4\ns_waitcnt lgkmcnt(10)\nds_read_b128 v[36:39], v7 offset:4160\nv_smfmac_f32_16x16x64_bf16 v[48:51], v[92:95], v[128:135], v4\n" \
"v_smfmac_f32_16x16x64_bf16 v[52:55], v[88:91], v[112:119], v4\nds_read_b128 v[40:43], v7 offset:6144\nv_smfmac_f32_16x16x64_bf16 v[52:55], v[92:95], v[136:143], v4\ns_waitcnt lgkmcnt(10)\nv_smfmac_f32_16x16x64_bf16 v[56:59], v[88:91], v[72:79], v4\nds_read_b128 v[72:75], v1 offset:18432\nds_read_b128 v[76:79], v1 offset:19456\nv_smfmac_f32_16x16x64_bf16 v[56:59], v[92:95], v[144:151], v4\n" \
"v_smfmac_f32_16x16x64_bf16 v[60:63], v[88:91], v[120:127], v4\ns_waitcnt lgkmcnt(8)\nds_read_b128 v[88:91], v0 offset:30720\nds_read_b128 v[44:47], v7 offset:6208\nv_smfmac_f32_16x16x64_bf16 v[60:63], v[92:95], v[64:71], v4\ns_waitcnt lgkmcnt(8)\nds_read_b128 v[92:95], v0 offset:31744\nds_read_b128 v[64:67], v1 offset:20480\n" \
"ds_read_b128 v[68:71], v1 offset:21504\nv_smfmac_f32_16x16x64_bf16 v[48:51], v[80:83], v[152:159], v4\ns_waitcnt lgkmcnt(9)\nv_smfmac_f32_16x16x64_bf16 v[48:51], v[84:87], v[184:191], v4\nv_smfmac_f32_16x16x64_bf16 v[52:55], v[80:83], v[160:167], v4\ns_waitcnt lgkmcnt(5)\nv_smfmac_f32_16x16x64_bf16 v[52:55], v[84:87], v[72:79], v4\nds_read_b128 v[72:75], v1 offset:22528\n" \
"ds_read_b128 v[76:79], v1 offset:23552\nv_smfmac_f32_16x16x64_bf16 v[56:59], v[80:83], v[168:175], v4\nv_smfmac_f32_16x16x64_bf16 v[56:59], v[84:87], v[192:199], v4\nv_smfmac_f32_16x16x64_bf16 v[60:63], v[80:83], v[176:183], v4\nv_smfmac_f32_16x16x64_bf16 v[60:63], v[84:87], v[200:207], v4\ns_waitcnt lgkmcnt(6)\nv_smfmac_f32_16x16x64_bf16 v[48:51], v[88:91], v[208:215], v4\ns_waitcnt lgkmcnt(4)\n" \
"v_smfmac_f32_16x16x64_bf16 v[48:51], v[92:95], v[232:239], v4\nv_smfmac_f32_16x16x64_bf16 v[52:55], v[88:91], v[216:223], v4\nv_smfmac_f32_16x16x64_bf16 v[52:55], v[92:95], v[240:247], v4\ns_waitcnt lgkmcnt(2)\nv_smfmac_f32_16x16x64_bf16 v[56:59], v[88:91], v[64:71], v4\nv_add_f32 v48, v48, v50\nv_smfmac_f32_16x16x64_bf16 v[56:59], v[92:95], v[248:255], v4\nv_add_f32 v52, v52, v54\n" \
"v_add_f32 v49, v49, v51\nv_smfmac_f32_16x16x64_bf16 v[60:63], v[88:91], v[224:231], v4\ns_waitcnt lgkmcnt(0)\nv_add_f32 v53, v53, v55\nv_cvt_pk_bf16_f32 v50, v48, v52\nv_smfmac_f32_16x16x64_bf16 v[60:63], v[92:95], v[72:79], v4\nv_cvt_pk_bf16_f32 v51, v49, v53\nds_write2_b32 v6, v50, v51 offset0:0 offset1:4\n" \
"v_add_f32 v56, v56, v58\nv_add_f32 v57, v57, v59\ns_nop 3\nv_add_f32 v60, v60, v62\nv_add_f32 v61, v61, v63\nv_cvt_pk_bf16_f32 v62, v56, v60\nv_cvt_pk_bf16_f32 v63, v57, v61\nds_write2_b32 v6, v62, v63 offset0:36 offset1:40\n" \
"v_mov_b32 v61, s54\ns_waitcnt lgkmcnt(0)\ns_barrier\nds_read_b128 v[80:83], v8 offset:0\nds_read_b128 v[84:87], v8 offset:576\nds_read_b128 v[88:91], v8 offset:1152\nds_read_b128 v[92:95], v8 offset:1728\nds_read_b128 v[64:67], v9 offset:0\n" \
"ds_read_b128 v[68:71], v9 offset:576\nds_read_b128 v[72:75], v9 offset:1152\nds_read_b128 v[76:79], v9 offset:1728\nds_read_b32 v60, v0 offset:53760\ns_waitcnt lgkmcnt(8)\nv_mfma_f32_16x16x32_bf16 v[52:55], v[16:19], v[80:83], 0\nv_mfma_f32_16x16x32_bf16 v[56:59], v[20:23], v[80:83], 0\ns_waitcnt lgkmcnt(7)\n" \
"v_mfma_f32_16x16x32_bf16 v[52:55], v[24:27], v[84:87], v[52:55]\nv_mfma_f32_16x16x32_bf16 v[56:59], v[28:31], v[84:87], v[56:59]\ns_waitcnt lgkmcnt(6)\nv_mfma_f32_16x16x32_bf16 v[52:55], v[32:35], v[88:91], v[52:55]\nv_mfma_f32_16x16x32_bf16 v[56:59], v[36:39], v[88:91], v[56:59]\ns_waitcnt lgkmcnt(5)\nv_mfma_f32_16x16x32_bf16 v[52:55], v[40:43], v[92:95], v[52:55]\nv_mfma_f32_16x16x32_bf16 v[56:59], v[44:47], v[92:95], v[56:59]\n" \
"s_waitcnt lgkmcnt(4)\nv_mfma_f32_16x16x32_bf16 v[48:51], v[64:67], v[80:83], 0\ns_waitcnt lgkmcnt(3)\nv_mfma_f32_16x16x32_bf16 v[48:51], v[68:71], v[84:87], v[48:51]\ns_waitcnt lgkmcnt(2)\nds_read_b128 v[80:83], v2 offset:0\nv_mfma_f32_16x16x32_bf16 v[48:51], v[72:75], v[88:91], v[48:51]\ns_waitcnt lgkmcnt(2)\n" \
"ds_read_b128 v[84:87], v2 offset:2048\nv_mfma_f32_16x16x32_bf16 v[48:51], v[76:79], v[92:95], v[48:51]\nv_max3_f32 v62, v52, v53, v54\nv_max3_f32 v63, v55, v56, v57\nv_max3_f32 v62, v62, v58, v59\nv_cndmask_b32 v52, v52, v56, s[62:63]\nv_cndmask_b32 v53, v53, v57, s[62:63]\nv_cndmask_b32 v54, v54, v58, s[62:63]\n" \
"v_cndmask_b32 v55, v55, v59, s[62:63]\nv_max_f32 v62, v62, v63\nv_max3_f32 v63, v62, v48, v49\nv_max3_f32 v62, v62, v48, v49\ns_waitcnt lgkmcnt(1)\nv_mfma_f32_16x16x32_bf16 v[16:19], a[0:3], v[80:83], 0\ns_waitcnt lgkmcnt(0)\nds_read_b128 v[88:91], v2 offset:256\n" \
"v_mfma_f32_16x16x32_bf16 v[16:19], a[4:7], v[84:87], v[16:19]\nds_read_b128 v[92:95], v2 offset:2304\nv_mfma_f32_16x16x32_bf16 v[20:23], a[8:11], v[80:83], 0\nv_permlane16_swap_b32 v62, v63\nv_mfma_f32_16x16x32_bf16 v[20:23], a[12:15], v[84:87], v[20:23]\nv_max_f32 v50, v62, v63\nv_max_f32 v62, v62, v63\nv_mfma_f32_16x16x32_bf16 v[24:27], a[16:19], v[80:83], 0\n" \
"ds_read_b128 v[72:75], v1 offset:0\nv_mfma_f32_16x16x32_bf16 v[24:27], a[20:23], v[84:87], v[24:27]\nds_read_b128 v[76:79], v1 offset:1024\nv_mfma_f32_16x16x32_bf16 v[28:31], a[24:27], v[80:83], 0\nv_permlane32_swap_b32 v62, v50\nv_mfma_f32_16x16x32_bf16 v[28:31], a[28:31], v[84:87], v[28:31]\ns_waitcnt lgkmcnt(3)\nv_max_f32 v62, v62, v50\n" \
"v_mul_f32 v62, s59, v62\nv_mfma_f32_16x16x32_bf16 v[32:35], a[0:3], v[88:91], 0\ns_waitcnt lgkmcnt(2)\nds_read_b128 v[80:83], v2 offset:4096\nv_fma_f32 v52, v52, s58, v62\nv_mfma_f32_16x16x32_bf16 v[32:35], a[4:7], v[92:95], v[32:35]\nds_read_b128 v[84:87], v2 offset:6144\nv_fma_f32 v53, v53, s58, v62\n" \
"v_mfma_f32_16x16x32_bf16 v[36:39], a[8:11], v[88:91], 0\nv_exp_f32 v52, v52\nv_mfma_f32_16x16x32_bf16 v[36:39], a[12:15], v[92:95], v[36:39]\nv_fma_f32 v54, v54, s58, v62\nv_fma_f32 v55, v55, s58, v62\nv_mfma_f32_16x16x32_bf16 v[40:43], a[16:19], v[88:91], 0\nv_exp_f32 v53, v53\nv_mfma_f32_16x16x32_bf16 v[40:43], a[20:23], v[92:95], v[40:43]\n" \
"v_exp_f32 v54, v54\nv_mfma_f32_16x16x32_bf16 v[44:47], a[24:27], v[88:91], 0\nv_add_f32 v63, v52, v53\nv_fma_f32 v48, v48, s58, v62\nv_mfma_f32_16x16x32_bf16 v[44:47], a[28:31], v[92:95], v[44:47]\ns_waitcnt lgkmcnt(1)\nv_exp_f32 v55, v55\nv_mfma_f32_16x16x32_bf16 v[16:19], a[32:35], v[80:83], v[16:19]\n" \
"s_waitcnt lgkmcnt(0)\nds_read_b128 v[88:91], v2 offset:4352\nv_add_f32 v63, v63, v54\nv_mfma_f32_16x16x32_bf16 v[16:19], a[36:39], v[84:87], v[16:19]\nds_read_b128 v[92:95], v2 offset:6400\nv_fma_f32 v49, v49, s58, v62\nv_mfma_f32_16x16x32_bf16 v[20:23], v[72:75], v[80:83], v[20:23]\nv_exp_f32 v48, v48\n" \
"v_mfma_f32_16x16x32_bf16 v[20:23], v[76:79], v[84:87], v[20:23]\nv_add_f32 v63, v63, v55\nv_add_f32 v50, v48, v63\nv_mfma_f32_16x16x32_bf16 v[24:27], a[40:43], v[80:83], v[24:27]\nds_read_b128 v[64:67], v1 offset:2048\nv_mfma_f32_16x16x32_bf16 v[24:27], a[44:47], v[84:87], v[24:27]\nds_read_b128 v[68:71], v1 offset:3072\nv_mfma_f32_16x16x32_bf16 v[28:31], a[48:51], v[80:83], v[28:31]\n" \
"v_exp_f32 v49, v49\nv_mfma_f32_16x16x32_bf16 v[28:31], a[52:55], v[84:87], v[28:31]\ns_waitcnt lgkmcnt(3)\nv_add_f32 v50, v50, v49\nv_mfma_f32_16x16x32_bf16 v[32:35], a[32:35], v[88:91], v[32:35]\ns_waitcnt lgkmcnt(2)\nds_read_b128 v[80:83], v2 offset:8192\nv_add_f32_dpp v62, v63, v50 row_ror:8 row_mask:0xf bank_mask:0xf\n" \
"v_mfma_f32_16x16x32_bf16 v[32:35], a[36:39], v[92:95], v[32:35]\nds_read_b128 v[84:87], v2 offset:10240\nv_add_f32_dpp v50, v63, v50 row_ror:8 row_mask:0xf bank_mask:0xf\nv_mfma_f32_16x16x32_bf16 v[36:39], v[72:75], v[88:91], v[36:39]\ns_nop 0\nv_permlane16_swap_b32 v62, v50\nv_mfma_f32_16x16x32_bf16 v[36:39], v[76:79], v[92:95], v[36:39]\nv_add_f32 v63, v62, v50\n" \
"v_add_f32 v50, v62, v50\nv_mfma_f32_16x16x32_bf16 v[40:43], a[40:43], v[88:91], v[40:43]\ns_nop 0\nv_permlane32_swap_b32 v63, v50\nv_mfma_f32_16x16x32_bf16 v[40:43], a[44:47], v[92:95], v[40:43]\nv_add_f32 v63, v63, v50\nv_mfma_f32_16x16x32_bf16 v[44:47], a[48:51], v[88:91], v[44:47]\nv_rcp_f32 v63, v63\n" \
"v_mfma_f32_16x16x32_bf16 v[44:47], a[52:55], v[92:95], v[44:47]\ns_waitcnt lgkmcnt(1)\nv_mul_f32 v52, v52, v63\nv_mul_f32 v53, v53, v63\nv_mfma_f32_16x16x32_bf16 v[16:19], a[56:59], v[80:83], v[16:19]\ns_waitcnt lgkmcnt(0)\nds_read_b128 v[88:91], v2 offset:8448\nv_mul_f32 v54, v54, v63\n" \
"v_mfma_f32_16x16x32_bf16 v[16:19], a[60:63], v[84:87], v[16:19]\nds_read_b128 v[92:95], v2 offset:10496\nv_mul_f32 v55, v55, v63\nv_mfma_f32_16x16x32_bf16 v[20:23], a[64:67], v[80:83], v[20:23]\nv_mul_f32 v48, v48, v63\nv_mul_f32 v49, v49, v63\nv_mfma_f32_16x16x32_bf16 v[20:23], a[68:71], v[84:87], v[20:23]\nv_cvt_pk_bf16_f32 v52, v52, v53\n" \
"v_cvt_pk_bf16_f32 v53, v54, v55\nv_mfma_f32_16x16x32_bf16 v[24:27], v[64:67], v[80:83], v[24:27]\nds_read_b128 v[72:75], v1 offset:4096\nv_cvt_pk_bf16_f32 v48, v48, v49\nv_mfma_f32_16x16x32_bf16 v[24:27], v[68:71], v[84:87], v[24:27]\nds_read_b128 v[76:79], v1 offset:5120\nv_bfi_b32 v52, s61, v52, v61\nv_mfma_f32_16x16x32_bf16 v[28:31], a[72:75], v[80:83], v[28:31]\n" \
"v_bfi_b32 v53, s61, v53, v61\nv_bfi_b32 v48, s61, v48, v61\nv_mfma_f32_16x16x32_bf16 v[28:31], a[76:79], v[84:87], v[28:31]\ns_waitcnt lgkmcnt(3)\nv_mov_b32_dpp v54, v52 row_ror:8 row_mask:0xf bank_mask:0xf\nv_mov_b32_dpp v55, v53 row_ror:8 row_mask:0xf bank_mask:0xf\nv_mfma_f32_16x16x32_bf16 v[32:35], a[56:59], v[88:91], v[32:35]\ns_waitcnt lgkmcnt(2)\n" \
"ds_read_b128 v[80:83], v2 offset:12288\nv_mfma_f32_16x16x32_bf16 v[32:35], a[60:63], v[92:95], v[32:35]\nds_read_b128 v[84:87], v2 offset:14336\nv_mfma_f32_16x16x32_bf16 v[36:39], a[64:67], v[88:91], v[36:39]\nbuffer_store_dwordx4 v[52:55], v10, s[36:39], 0 offen sc1\nv_mfma_f32_16x16x32_bf16 v[36:39], a[68:71], v[92:95], v[36:39]\nv_mov_b32 v49, v48\nv_mfma_f32_16x16x32_bf16 v[40:43], v[64:67], v[88:91], v[40:43]\n" \
"s_nop 0\nv_permlane32_swap_b32 v48, v49\nv_mfma_f32_16x16x32_bf16 v[40:43], v[68:71], v[92:95], v[40:43]\ns_memrealtime s[86:87]\nbuffer_store_dwordx2 v[48:49], v11, s[36:39], 0 offen sc1\nv_mfma_f32_16x16x32_bf16 v[44:47], a[72:75], v[88:91], v[44:47]\nv_cmp_ne_u32 vcc, 0, v60\nv_mfma_f32_16x16x32_bf16 v[44:47], a[76:79], v[92:95], v[44:47]\n" \
"s_cbranch_vccnz .Lqk_pj%=\ns_cmp_lt_i32 s40, 63\ns_waitcnt lgkmcnt(0)\ns_cbranch_scc1 .Lqk_pwait%=\ns_branch .Lqk_end%=\n.Lqk_re0%=:\nv_max_u32 v17, v80, v82\nv_cmp_eq_u32 vcc, -1, v17\n" \
"s_cbranch_vccz .Lqk_sk1619%=\ns_mov_b64 exec, vcc\nbuffer_load_dwordx4 v[80:83], v3, s[36:39], s42 offen offset:0 sc1\ns_mov_b64 exec, -1\n.Lqk_sk1619%=:\nv_max_u32 v17, v84, v86\nv_cmp_eq_u32 vcc, -1, v17\ns_cbranch_vccz .Lqk_sk1626%=\n" \
"s_mov_b64 exec, vcc\nbuffer_load_dwordx4 v[84:87], v3, s[36:39], s42 offen offset:1024 sc1\ns_mov_b64 exec, -1\n.Lqk_sk1626%=:\nv_max_u32 v17, v88, v90\nv_cmp_eq_u32 vcc, -1, v17\ns_cbranch_vccz .Lqk_sk1633%=\ns_mov_b64 exec, vcc\n" \
"buffer_load_dwordx4 v[88:91], v3, s[36:39], s42 offen offset:2048 sc1\ns_mov_b64 exec, -1\n.Lqk_sk1633%=:\nv_max_u32 v17, v92, v94\nv_cmp_eq_u32 vcc, -1, v17\ns_cbranch_vccz .Lqk_sk1640%=\ns_mov_b64 exec, vcc\nbuffer_load_dwordx4 v[92:95], v3, s[36:39], s42 offen offset:3072 sc1\n" \
"s_mov_b64 exec, -1\n.Lqk_sk1640%=:\nv_mov_b64 v[48:49], 0\nv_mov_b64 v[50:51], 0\nv_mov_b64 v[52:53], 0\nv_mov_b64 v[54:55], 0\nv_mov_b64 v[56:57], 0\nv_mov_b64 v[58:59], 0\n" \
"v_mov_b64 v[60:61], 0\nv_mov_b64 v[62:63], 0\ns_add_u32 s43, s43, 1\ns_cmp_lt_u32 s43, 262144\ns_cbranch_scc1 .Lqk_poll0%=\ns_branch .Lqk_abort%=\n.p2align 6\n.Lqk_c1%=:\n" \
"s_waitcnt lgkmcnt(3)\nv_mfma_f32_16x16x32_bf16 v[32:35], v[232:235], v[56:59], v[32:35]\ns_waitcnt lgkmcnt(2)\nv_mov_b64 v[48:49], 0\nv_mov_b64 v[50:51], 0\nv_mfma_f32_16x16x32_bf16 v[32:35], v[236:239], v[60:63], v[32:35]\nds_read_b128 v[64:67], v1 offset:6144\nv_mfma_f32_16x16x32_bf16 v[36:39], v[240:243], v[56:59], v[36:39]\n" \
"ds_read_b128 v[68:71], v1 offset:7168\nv_mfma_f32_16x16x32_bf16 v[36:39], v[244:247], v[60:63], v[36:39]\ns_bfe_i32 s54, s40, 0x10001\nv_mov_b64 v[52:53], 0\nv_mov_b64 v[54:55], 0\nv_mfma_f32_16x16x32_bf16 v[40:43], v[248:251], v[56:59], v[40:43]\nv_mfma_f32_16x16x32_bf16 v[40:43], v[252:255], v[60:63], v[40:43]\nv_cvt_pk_bf16_f32 v36, v32, v36\n" \
"v_cvt_pk_bf16_f32 v37, v33, v37\nv_mfma_f32_16x16x32_bf16 v[44:47], v[72:75], v[56:59], v[44:47]\nv_cvt_pk_bf16_f32 v38, v34, v38\nv_cvt_pk_bf16_f32 v39, v35, v39\nv_mfma_f32_16x16x32_bf16 v[44:47], v[76:79], v[60:63], v[44:47]\nds_read_b128 v[72:75], v1 offset:8192\nds_read_b128 v[76:79], v1 offset:9216\nv_mov_b64 v[56:57], 0\n" \
"v_mov_b64 v[58:59], 0\nv_mov_b64 v[60:61], 0\nv_mov_b64 v[62:63], 0\n.Lqk_poll1%=:\ns_waitcnt vmcnt(0)\nv_smfmac_f32_16x16x64_bf16 v[48:51], v[80:83], a[104:111], v4\nds_write_b128 v0, v[80:83] offset:20480\nv_max3_u32 v16, v80, v82, v84\n" \
"v_smfmac_f32_16x16x64_bf16 v[48:51], v[84:87], a[136:143], v4\nv_max3_u32 v16, v16, v86, v88\nv_smfmac_f32_16x16x64_bf16 v[52:55], v[80:83], a[112:119], v4\ns_waitcnt lgkmcnt(3)\nds_write_b128 v0, v[84:87] offset:21504\nv_max3_u32 v16, v16, v90, v92\nv_max_u32 v16, v16, v94\nv_smfmac_f32_16x16x64_bf16 v[52:55], v[84:87], v[64:71], v4\n" \
"v_cmp_eq_u32 vcc, -1, v16\nv_smfmac_f32_16x16x64_bf16 v[56:59], v[80:83], a[120:127], v4\nds_write_b128 v0, v[88:91] offset:22528\ns_cbranch_vccnz .Lqk_re1%=\nv_smfmac_f32_16x16x64_bf16 v[56:59], v[84:87], a[144:151], v4\nv_smfmac_f32_16x16x64_bf16 v[60:63], v[80:83], a[128:135], v4\nds_write_b128 v0, v[92:95] offset:23552\nv_smfmac_f32_16x16x64_bf16 v[60:63], v[84:87], a[152:159], v4\n" \
"v_smfmac_f32_16x16x64_bf16 v[48:51], v[88:91], a[160:167], v4\nv_smfmac_f32_16x16x64_bf16 v[48:51], v[92:95], a[184:191], v4\ns_waitcnt lgkmcnt(0)\n.Lqk_jn1%=:\ns_barrier\nds_read_b128 v[80:83], v0 offset:24576\nds_read_b128 v[64:67], v1 offset:10240\nds_read_b128 v[68:71], v1 offset:11264\n" \
"v_smfmac_f32_16x16x64_bf16 v[52:55], v[88:91], a[168:175], v4\nv_smfmac_f32_16x16x64_bf16 v[52:55], v[92:95], a[192:199], v4\nds_read_b128 v[84:87], v0 offset:25600\nv_smfmac_f32_16x16x64_bf16 v[56:59], v[88:91], v[72:79], v4\nds_read_b128 v[72:75], v1 offset:12288\nds_read_b128 v[76:79], v1 offset:13312\nv_smfmac_f32_16x16x64_bf16 v[56:59], v[92:95], a[200:207], v4\nv_smfmac_f32_16x16x64_bf16 v[60:63], v[88:91], a[176:183], v4\n" \
"s_waitcnt lgkmcnt(3)\nds_read_b128 v[88:91], v0 offset:26624\nv_smfmac_f32_16x16x64_bf16 v[60:63], v[92:95], v[64:71], v4\nds_read_b128 v[92:95], v0 offset:27648\nds_read_b128 v[64:67], v1 offset:14336\nds_read_b128 v[68:71], v1 offset:15360\nv_smfmac_f32_16x16x64_bf16 v[48:51], v[80:83], a[208:215], v4\ns_waitcnt lgkmcnt(6)\n" \
"ds_write_b128 v5, v[36:39] offset:256\nv_cvt_pk_bf16_f32 v44, v40, v44\nv_cvt_pk_bf16_f32 v45, v41, v45\nv_cvt_pk_bf16_f32 v46, v42, v46\nv_cvt_pk_bf16_f32 v47, v43, v47\nv_smfmac_f32_16x16x64_bf16 v[48:51], v[84:87], a[240:247], v4\nv_smfmac_f32_16x16x64_bf16 v[52:55], v[80:83], a[216:223], v4\ns_waitcnt lgkmcnt(5)\n" \
"v_smfmac_f32_16x16x64_bf16 v[52:55], v[84:87], v[72:79], v4\nds_read_b128 v[72:75], v1 offset:16384\nds_read_b128 v[76:79], v1 offset:17408\nv_smfmac_f32_16x16x64_bf16 v[56:59], v[80:83], a[224:231], v4\nv_smfmac_f32_16x16x64_bf16 v[56:59], v[84:87], a[248:255], v4\nv_smfmac_f32_16x16x64_bf16 v[60:63], v[80:83], a[232:239], v4\nds_read_b128 v[80:83], v0 offset:28672\nv_smfmac_f32_16x16x64_bf16 v[60:63], v[84:87], v[96:103], v4\n" \
"s_waitcnt lgkmcnt(7)\nds_read_b128 v[84:87], v0 offset:29696\nv_smfmac_f32_16x16x64_bf16 v[48:51], v[88:91], v[104:111], v4\ns_waitcnt lgkmcnt(7)\nds_write_b128 v5, v[44:47] offset:768\nv_smfmac_f32_16x16x64_bf16 v[48:51], v[92:95], v[128:135], v4\nv_smfmac_f32_16x16x64_bf16 v[52:55], v[88:91], v[112:119], v4\nv_smfmac_f32_16x16x64_bf16 v[52:55], v[92:95], v[136:143], v4\n" \
"s_waitcnt lgkmcnt(6)\nv_smfmac_f32_16x16x64_bf16 v[56:59], v[88:91], v[64:71], v4\nds_read_b128 v[64:67], v1 offset:18432\nds_read_b128 v[68:71], v1 offset:19456\nv_smfmac_f32_16x16x64_bf16 v[56:59], v[92:95], v[144:151], v4\ns_waitcnt lgkmcnt(2)\ns_barrier\nv_smfmac_f32_16x16x64_bf16 v[60:63], v[88:91], v[120:127], v4\n" \
"ds_read_b128 v[88:91], v0 offset:30720\nds_read_b128 v[16:19], v7 offset:0\nv_smfmac_f32_16x16x64_bf16 v[60:63], v[92:95], v[72:79], v4\nds_read_b128 v[92:95], v0 offset:31744\nds_read_b128 v[72:75], v1 offset:20480\nds_read_b128 v[76:79], v1 offset:21504\nv_smfmac_f32_16x16x64_bf16 v[48:51], v[80:83], v[152:159], v4\nds_read_b128 v[20:23], v7 offset:2048\n" \
"v_smfmac_f32_16x16x64_bf16 v[48:51], v[84:87], v[184:191], v4\nv_smfmac_f32_16x16x64_bf16 v[52:55], v[80:83], v[160:167], v4\ns_waitcnt lgkmcnt(6)\nds_read_b128 v[24:27], v7 offset:4096\nv_smfmac_f32_16x16x64_bf16 v[52:55], v[84:87], v[64:71], v4\nds_read_b128 v[64:67], v1 offset:22528\nds_read_b128 v[68:71], v1 offset:23552\nv_smfmac_f32_16x16x64_bf16 v[56:59], v[80:83], v[168:175], v4\n" \
"ds_read_b128 v[28:31], v7 offset:6144\nv_smfmac_f32_16x16x64_bf16 v[56:59], v[84:87], v[192:199], v4\nv_smfmac_f32_16x16x64_bf16 v[60:63], v[80:83], v[176:183], v4\nds_read_b128 v[80:83], v0 offset:16384\nds_read_b128 v[32:35], v7 offset:8192\nv_smfmac_f32_16x16x64_bf16 v[60:63], v[84:87], v[200:207], v4\ns_waitcnt lgkmcnt(11)\nds_read_b128 v[84:87], v0 offset:17408\n" \
"v_smfmac_f32_16x16x64_bf16 v[48:51], v[88:91], v[208:215], v4\ns_waitcnt lgkmcnt(10)\nds_read_b128 v[36:39], v7 offset:10240\nv_smfmac_f32_16x16x64_bf16 v[48:51], v[92:95], v[232:239], v4\nv_smfmac_f32_16x16x64_bf16 v[52:55], v[88:91], v[216:223], v4\nds_read_b128 v[40:43], v7 offset:12288\nv_smfmac_f32_16x16x64_bf16 v[52:55], v[92:95], v[240:247], v4\ns_waitcnt lgkmcnt(10)\n" \
"v_smfmac_f32_16x16x64_bf16 v[56:59], v[88:91], v[72:79], v4\nds_read_b128 v[72:75], v1 offset:0\nds_read_b128 v[76:79], v1 offset:1024\nv_smfmac_f32_16x16x64_bf16 v[56:59], v[92:95], v[248:255], v4\nv_smfmac_f32_16x16x64_bf16 v[60:63], v[88:91], v[224:231], v4\ns_waitcnt lgkmcnt(8)\nds_read_b128 v[88:91], v0 offset:18432\nds_read_b128 v[44:47], v7 offset:14336\n" \
"v_smfmac_f32_16x16x64_bf16 v[60:63], v[92:95], v[64:71], v4\ns_waitcnt lgkmcnt(8)\nds_read_b128 v[92:95], v0 offset:19456\nds_read_b128 v[64:67], v1 offset:2048\nds_read_b128 v[68:71], v1 offset:3072\nv_smfmac_f32_16x16x64_bf16 v[48:51], v[80:83], a[0:7], v4\ns_waitcnt lgkmcnt(9)\nv_smfmac_f32_16x16x64_bf16 v[48:51], v[84:87], a[32:39], v4\n" \
"v_smfmac_f32_16x16x64_bf16 v[52:55], v[80:83], a[8:15], v4\ns_waitcnt lgkmcnt(5)\nv_smfmac_f32_16x16x64_bf16 v[52:55], v[84:87], v[72:79], v4\nds_read_b128 v[72:75], v1 offset:4096\nds_read_b128 v[76:79], v1 offset:5120\nv_smfmac_f32_16x16x64_bf16 v[56:59], v[80:83], a[16:23], v4\nv_smfmac_f32_16x16x64_bf16 v[56:59], v[84:87], a[40:47], v4\nv_smfmac_f32_16x16x64_bf16 v[60:63], v[80:83], a[24:31], v4\n" \
"v_smfmac_f32_16x16x64_bf16 v[60:63], v[84:87], a[48:55], v4\ns_waitcnt lgkmcnt(6)\nv_smfmac_f32_16x16x64_bf16 v[48:51], v[88:91], a[56:63], v4\ns_waitcnt lgkmcnt(4)\nv_smfmac_f32_16x16x64_bf16 v[48:51], v[92:95], a[80:87], v4\nv_smfmac_f32_16x16x64_bf16 v[52:55], v[88:91], a[64:71], v4\nv_smfmac_f32_16x16x64_bf16 v[52:55], v[92:95], a[88:95], v4\ns_waitcnt lgkmcnt(2)\n" \
"v_smfmac_f32_16x16x64_bf16 v[56:59], v[88:91], v[64:71], v4\nv_add_f32 v48, v48, v50\nv_smfmac_f32_16x16x64_bf16 v[56:59], v[92:95], a[96:103], v4\nv_add_f32 v52, v52, v54\nv_add_f32 v49, v49, v51\nv_smfmac_f32_16x16x64_bf16 v[60:63], v[88:91], a[72:79], v4\ns_waitcnt lgkmcnt(0)\nv_add_f32 v53, v53, v55\n" \
"v_cvt_pk_bf16_f32 v50, v48, v52\nv_smfmac_f32_16x16x64_bf16 v[60:63], v[92:95], v[72:79], v4\nv_cvt_pk_bf16_f32 v51, v49, v53\nds_write2_b32 v6, v50, v51 offset0:0 offset1:4\nv_add_f32 v56, v56, v58\nv_add_f32 v57, v57, v59\ns_nop 3\nv_add_f32 v60, v60, v62\n" \
"v_add_f32 v61, v61, v63\nv_cvt_pk_bf16_f32 v62, v56, v60\nv_cvt_pk_bf16_f32 v63, v57, v61\nds_write2_b32 v6, v62, v63 offset0:36 offset1:40\nv_mov_b32 v61, s54\nv_mfma_f32_16x16x32_bf16 v[52:55], v[32:35], v[16:19], v[12:15]\nv_mfma_f32_16x16x32_bf16 v[52:55], v[36:39], v[20:23], v[52:55]\nv_mfma_f32_16x16x32_bf16 v[52:55], v[40:43], v[24:27], v[52:55]\n" \
"v_mfma_f32_16x16x32_bf16 v[52:55], v[44:47], v[28:31], v[52:55]\ns_waitcnt lgkmcnt(0)\ns_barrier\ns_nop 10\nds_read_b128 v[32:35], v9 offset:0\nds_read_b128 v[36:39], v9 offset:576\nds_read_b128 v[40:43], v9 offset:1152\nds_read_b128 v[44:47], v9 offset:1728\n" \
"ds_read_b32 v60, v0 offset:53760\nds_read_b128 v[80:83], v2 offset:0\nds_read_b128 v[84:87], v2 offset:2048\ns_waitcnt lgkmcnt(6)\nv_mfma_f32_16x16x32_bf16 v[48:51], v[32:35], v[16:19], 0\ns_waitcnt lgkmcnt(5)\nv_mfma_f32_16x16x32_bf16 v[48:51], v[36:39], v[20:23], v[48:51]\ns_waitcnt lgkmcnt(4)\n" \
"v_mfma_f32_16x16x32_bf16 v[48:51], v[40:43], v[24:27], v[48:51]\ns_waitcnt lgkmcnt(3)\nv_mfma_f32_16x16x32_bf16 v[48:51], v[44:47], v[28:31], v[48:51]\ns_waitcnt lgkmcnt(1)\nv_mfma_f32_16x16x32_bf16 v[16:19], a[0:3], v[80:83], 0\ns_waitcnt lgkmcnt(0)\nds_read_b128 v[88:91], v2 offset:256\nv_max3_f32 v62, v52, v53, v54\n" \
"v_max_f32 v62, v62, v55\nv_mfma_f32_16x16x32_bf16 v[16:19], a[4:7], v[84:87], v[16:19]\nds_read_b128 v[92:95], v2 offset:2304\nv_max3_f32 v63, v62, v48, v49\nv_max3_f32 v62, v62, v48, v49\nv_mfma_f32_16x16x32_bf16 v[20:23], a[8:11], v[80:83], 0\ns_nop 0\nv_permlane16_swap_b32 v62, v63\n" \
"v_max_f32 v50, v62, v63\nv_mfma_f32_16x16x32_bf16 v[20:23], a[12:15], v[84:87], v[20:23]\nv_max_f32 v62, v62, v63\nv_mfma_f32_16x16x32_bf16 v[24:27], a[16:19], v[80:83], 0\nds_read_b128 v[72:75], v1 offset:0\nv_permlane32_swap_b32 v62, v50\nv_mfma_f32_16x16x32_bf16 v[24:27], a[20:23], v[84:87], v[24:27]\nds_read_b128 v[76:79], v1 offset:1024\n" \
"v_max_f32 v62, v62, v50\nv_mul_f32 v62, s59, v62\nv_mfma_f32_16x16x32_bf16 v[28:31], a[24:27], v[80:83], 0\nv_fma_f32 v52, v52, s58, v62\nv_fma_f32 v53, v53, s58, v62\nv_fma_f32 v54, v54, s58, v62\nv_mfma_f32_16x16x32_bf16 v[28:31], a[28:31], v[84:87], v[28:31]\ns_waitcnt lgkmcnt(3)\n" \
"v_exp_f32 v52, v52\nv_fma_f32 v55, v55, s58, v62\nv_mfma_f32_16x16x32_bf16 v[32:35], a[0:3], v[88:91], 0\ns_waitcnt lgkmcnt(2)\nds_read_b128 v[80:83], v2 offset:4096\nv_exp_f32 v53, v53\nv_mfma_f32_16x16x32_bf16 v[32:35], a[4:7], v[92:95], v[32:35]\nds_read_b128 v[84:87], v2 offset:6144\n" \
"v_exp_f32 v54, v54\nv_mfma_f32_16x16x32_bf16 v[36:39], a[8:11], v[88:91], 0\nv_add_f32 v63, v52, v53\nv_fma_f32 v48, v48, s58, v62\nv_add_f32 v63, v63, v54\nv_mfma_f32_16x16x32_bf16 v[36:39], a[12:15], v[92:95], v[36:39]\nv_exp_f32 v55, v55\nv_fma_f32 v49, v49, s58, v62\n" \
"v_mfma_f32_16x16x32_bf16 v[40:43], a[16:19], v[88:91], 0\nv_exp_f32 v48, v48\nv_add_f32 v63, v63, v55\nv_mfma_f32_16x16x32_bf16 v[40:43], a[20:23], v[92:95], v[40:43]\nv_exp_f32 v49, v49\nv_add_f32 v63, v63, v48\nv_mfma_f32_16x16x32_bf16 v[44:47], a[24:27], v[88:91], 0\nv_add_f32 v62, v63, v49\n" \
"v_add_f32 v50, v63, v49\nv_mfma_f32_16x16x32_bf16 v[44:47], a[28:31], v[92:95], v[44:47]\ns_waitcnt lgkmcnt(1)\nv_permlane16_swap_b32 v62, v50\nv_add_f32 v63, v62, v50\nv_mfma_f32_16x16x32_bf16 v[16:19], a[32:35], v[80:83], v[16:19]\ns_waitcnt lgkmcnt(0)\nds_read_b128 v[88:91], v2 offset:4352\n" \
"v_add_f32 v50, v62, v50\nv_mfma_f32_16x16x32_bf16 v[16:19], a[36:39], v[84:87], v[16:19]\nds_read_b128 v[92:95], v2 offset:6400\nv_permlane32_swap_b32 v63, v50\nv_mfma_f32_16x16x32_bf16 v[20:23], v[72:75], v[80:83], v[20:23]\nv_add_f32 v63, v63, v50\nv_rcp_f32 v63, v63\nv_mfma_f32_16x16x32_bf16 v[20:23], v[76:79], v[84:87], v[20:23]\n" \
"v_mul_f32 v52, v52, v63\nv_mul_f32 v53, v53, v63\nv_mul_f32 v54, v54, v63\nv_mfma_f32_16x16x32_bf16 v[24:27], a[40:43], v[80:83], v[24:27]\nds_read_b128 v[64:67], v1 offset:2048\nv_mul_f32 v55, v55, v63\nv_mul_f32 v48, v48, v63\nv_mfma_f32_16x16x32_bf16 v[24:27], a[44:47], v[84:87], v[24:27]\n" \
"ds_read_b128 v[68:71], v1 offset:3072\nv_mul_f32 v49, v49, v63\nv_cvt_pk_bf16_f32 v52, v52, v53\nv_mfma_f32_16x16x32_bf16 v[28:31], a[48:51], v[80:83], v[28:31]\nv_cvt_pk_bf16_f32 v53, v54, v55\nv_cvt_pk_bf16_f32 v48, v48, v49\nv_bfi_b32 v52, s61, v52, v61\nv_mfma_f32_16x16x32_bf16 v[28:31], a[52:55], v[84:87], v[28:31]\n" \
"s_waitcnt lgkmcnt(3)\nv_bfi_b32 v53, s61, v53, v61\nv_bfi_b32 v48, s61, v48, v61\nv_mfma_f32_16x16x32_bf16 v[32:35], a[32:35], v[88:91], v[32:35]\ns_waitcnt lgkmcnt(2)\nds_read_b128 v[80:83], v2 offset:8192\nv_mfma_f32_16x16x32_bf16 v[32:35], a[36:39], v[92:95], v[32:35]\nds_read_b128 v[84:87], v2 offset:10240\n" \
"v_mfma_f32_16x16x32_bf16 v[36:39], v[72:75], v[88:91], v[36:39]\nbuffer_store_dwordx2 v[52:53], v10, s[36:39], s55 offen sc1\nv_mov_b32 v49, v48\nv_mfma_f32_16x16x32_bf16 v[36:39], v[76:79], v[92:95], v[36:39]\ns_nop 0\nv_permlane16_swap_b32 v48, v49\nv_mov_b32 v50, v48\nv_mfma_f32_16x16x32_bf16 v[40:43], a[40:43], v[88:91], v[40:43]\n" \
"v_mov_b32 v51, v49\nv_permlane32_swap_b32 v48, v50\nv_mfma_f32_16x16x32_bf16 v[40:43], a[44:47], v[92:95], v[40:43]\nv_permlane32_swap_b32 v49, v51\nv_mfma_f32_16x16x32_bf16 v[44:47], a[48:51], v[88:91], v[44:47]\nbuffer_store_dwordx4 v[48:51], v11, s[36:39], s55 offen sc1\nv_cmp_ne_u32 vcc, 0, v60\nv_mfma_f32_16x16x32_bf16 v[44:47], a[52:55], v[92:95], v[44:47]\n" \
"s_waitcnt lgkmcnt(1)\nv_mfma_f32_16x16x32_bf16 v[16:19], a[56:59], v[80:83], v[16:19]\ns_waitcnt lgkmcnt(0)\nds_read_b128 v[88:91], v2 offset:8448\nv_mfma_f32_16x16x32_bf16 v[16:19], a[60:63], v[84:87], v[16:19]\nds_read_b128 v[92:95], v2 offset:10496\nv_mfma_f32_16x16x32_bf16 v[20:23], a[64:67], v[80:83], v[20:23]\nv_mfma_f32_16x16x32_bf16 v[20:23], a[68:71], v[84:87], v[20:23]\n" \
"v_mfma_f32_16x16x32_bf16 v[24:27], v[64:67], v[80:83], v[24:27]\nds_read_b128 v[72:75], v1 offset:4096\nv_mfma_f32_16x16x32_bf16 v[24:27], v[68:71], v[84:87], v[24:27]\nds_read_b128 v[76:79], v1 offset:5120\nv_mfma_f32_16x16x32_bf16 v[28:31], a[72:75], v[80:83], v[28:31]\nv_mfma_f32_16x16x32_bf16 v[28:31], a[76:79], v[84:87], v[28:31]\ns_waitcnt lgkmcnt(3)\nv_mfma_f32_16x16x32_bf16 v[32:35], a[56:59], v[88:91], v[32:35]\n" \
"s_waitcnt lgkmcnt(2)\nds_read_b128 v[80:83], v2 offset:12288\nv_mfma_f32_16x16x32_bf16 v[32:35], a[60:63], v[92:95], v[32:35]\nds_read_b128 v[84:87], v2 offset:14336\nv_mfma_f32_16x16x32_bf16 v[36:39], a[64:67], v[88:91], v[36:39]\nv_mfma_f32_16x16x32_bf16 v[36:39], a[68:71], v[92:95], v[36:39]\nv_mfma_f32_16x16x32_bf16 v[40:43], v[64:67], v[88:91], v[40:43]\nv_mfma_f32_16x16x32_bf16 v[40:43], v[68:71], v[92:95], v[40:43]\n" \
"v_mfma_f32_16x16x32_bf16 v[44:47], a[72:75], v[88:91], v[44:47]\nv_mfma_f32_16x16x32_bf16 v[44:47], a[76:79], v[92:95], v[44:47]\ns_cbranch_vccnz .Lqk_pj%=\ns_cmp_lt_i32 s40, 63\ns_waitcnt lgkmcnt(0)\ns_cbranch_scc1 .Lqk_pwait%=\ns_branch .Lqk_end%=\n.Lqk_re1%=:\n" \
"v_max_u32 v17, v80, v82\nv_cmp_eq_u32 vcc, -1, v17\ns_cbranch_vccz .Lqk_sk2009%=\ns_mov_b64 exec, vcc\nbuffer_load_dwordx4 v[80:83], v3, s[36:39], s42 offen offset:0 sc1\ns_mov_b64 exec, -1\n.Lqk_sk2009%=:\nv_max_u32 v17, v84, v86\n" \
"v_cmp_eq_u32 vcc, -1, v17\ns_cbranch_vccz .Lqk_sk2016%=\ns_mov_b64 exec, vcc\nbuffer_load_dwordx4 v[84:87], v3, s[36:39], s42 offen offset:1024 sc1\ns_mov_b64 exec, -1\n.Lqk_sk2016%=:\nv_max_u32 v17, v88, v90\nv_cmp_eq_u32 vcc, -1, v17\n" \
"s_cbranch_vccz .Lqk_sk2023%=\ns_mov_b64 exec, vcc\nbuffer_load_dwordx4 v[88:91], v3, s[36:39], s42 offen offset:2048 sc1\ns_mov_b64 exec, -1\n.Lqk_sk2023%=:\nv_max_u32 v17, v92, v94\nv_cmp_eq_u32 vcc, -1, v17\ns_cbranch_vccz .Lqk_sk2030%=\n" \
"s_mov_b64 exec, vcc\nbuffer_load_dwordx4 v[92:95], v3, s[36:39], s42 offen offset:3072 sc1\ns_mov_b64 exec, -1\n.Lqk_sk2030%=:\nv_mov_b64 v[48:49], 0\nv_mov_b64 v[50:51], 0\nv_mov_b64 v[52:53], 0\nv_mov_b64 v[54:55], 0\n" \
"v_mov_b64 v[56:57], 0\nv_mov_b64 v[58:59], 0\nv_mov_b64 v[60:61], 0\nv_mov_b64 v[62:63], 0\ns_add_u32 s43, s43, 1\ns_cmp_lt_u32 s43, 262144\ns_cbranch_scc1 .Lqk_poll1%=\ns_branch .Lqk_abort%=\n" \
".p2align 6\n.Lqk_c2%=:\ns_waitcnt lgkmcnt(3)\nv_mfma_f32_16x16x32_bf16 v[32:35], v[232:235], v[56:59], v[32:35]\ns_waitcnt lgkmcnt(2)\nv_mov_b64 v[48:49], 0\nv_mov_b64 v[50:51], 0\nv_mfma_f32_16x16x32_bf16 v[32:35], v[236:239], v[60:63], v[32:35]\n" \
"ds_read_b128 v[64:67], v1 offset:12288\nv_mfma_f32_16x16x32_bf16 v[36:39], v[240:243], v[56:59], v[36:39]\nds_read_b128 v[68:71], v1 offset:13312\nv_mfma_f32_16x16x32_bf16 v[36:39], v[244:247], v[60:63], v[36:39]\ns_bfe_i32 s54, s40, 0x10001\nv_mov_b64 v[52:53], 0\nv_mov_b64 v[54:55], 0\nv_mfma_f32_16x16x32_bf16 v[40:43], v[248:251], v[56:59], v[40:43]\n" \
"v_mfma_f32_16x16x32_bf16 v[40:43], v[252:255], v[60:63], v[40:43]\nv_cvt_pk_bf16_f32 v36, v32, v36\nv_cvt_pk_bf16_f32 v37, v33, v37\nv_mfma_f32_16x16x32_bf16 v[44:47], v[72:75], v[56:59], v[44:47]\nv_cvt_pk_bf16_f32 v38, v34, v38\nv_cvt_pk_bf16_f32 v39, v35, v39\nv_mfma_f32_16x16x32_bf16 v[44:47], v[76:79], v[60:63], v[44:47]\nds_read_b128 v[72:75], v1 offset:14336\n" \
"ds_read_b128 v[76:79], v1 offset:15360\nv_mov_b64 v[56:57], 0\nv_mov_b64 v[58:59], 0\nv_mov_b64 v[60:61], 0\nv_mov_b64 v[62:63], 0\n.Lqk_poll2%=:\ns_waitcnt vmcnt(0)\nv_smfmac_f32_16x16x64_bf16 v[48:51], v[80:83], a[208:215], v4\n" \
"ds_write_b128 v0, v[80:83] offset:24576\nv_max3_u32 v16, v80, v82, v84\nv_smfmac_f32_16x16x64_bf16 v[48:51], v[84:87], a[240:247], v4\nv_max3_u32 v16, v16, v86, v88\nv_smfmac_f32_16x16x64_bf16 v[52:55], v[80:83], a[216:223], v4\ns_waitcnt lgkmcnt(3)\nds_write_b128 v0, v[84:87] offset:25600\nv_max3_u32 v16, v16, v90, v92\n" \
"v_max_u32 v16, v16, v94\nv_smfmac_f32_16x16x64_bf16 v[52:55], v[84:87], v[64:71], v4\nv_cmp_eq_u32 vcc, -1, v16\nv_smfmac_f32_16x16x64_bf16 v[56:59], v[80:83], a[224:231], v4\nds_write_b128 v0, v[88:91] offset:26624\ns_cbranch_vccnz .Lqk_re2%=\nv_smfmac_f32_16x16x64_bf16 v[56:59], v[84:87], a[248:255], v4\nv_smfmac_f32_16x16x64_bf16 v[60:63], v[80:83], a[232:239], v4\n" \
"ds_write_b128 v0, v[92:95] offset:27648\nv_smfmac_f32_16x16x64_bf16 v[60:63], v[84:87], v[96:103], v4\nv_smfmac_f32_16x16x64_bf16 v[48:51], v[88:91], v[104:111], v4\nv_smfmac_f32_16x16x64_bf16 v[48:51], v[92:95], v[128:135], v4\ns_waitcnt lgkmcnt(0)\n.Lqk_jn2%=:\ns_barrier\nds_read_b128 v[80:83], v0 offset:28672\n" \
"ds_read_b128 v[64:67], v1 offset:16384\nds_read_b128 v[68:71], v1 offset:17408\nv_smfmac_f32_16x16x64_bf16 v[52:55], v[88:91], v[112:119], v4\nv_smfmac_f32_16x16x64_bf16 v[52:55], v[92:95], v[136:143], v4\nds_read_b128 v[84:87], v0 offset:29696\nv_smfmac_f32_16x16x64_bf16 v[56:59], v[88:91], v[72:79], v4\nds_read_b128 v[72:75], v1 offset:18432\nds_read_b128 v[76:79], v1 offset:19456\n" \
"v_smfmac_f32_16x16x64_bf16 v[56:59], v[92:95], v[144:151], v4\nv_smfmac_f32_16x16x64_bf16 v[60:63], v[88:91], v[120:127], v4\ns_waitcnt lgkmcnt(3)\nds_read_b128 v[88:91], v0 offset:30720\nv_smfmac_f32_16x16x64_bf16 v[60:63], v[92:95], v[64:71], v4\nds_read_b128 v[92:95], v0 offset:31744\nds_read_b128 v[64:67], v1 offset:20480\nds_read_b128 v[68:71], v1 offset:21504\n" \
"v_smfmac_f32_16x16x64_bf16 v[48:51], v[80:83], v[152:159], v4\ns_waitcnt lgkmcnt(6)\nds_write_b128 v5, v[36:39] offset:256\nv_cvt_pk_bf16_f32 v44, v40, v44\nv_cvt_pk_bf16_f32 v45, v41, v45\nv_cvt_pk_bf16_f32 v46, v42, v46\nv_cvt_pk_bf16_f32 v47, v43, v47\nv_smfmac_f32_16x16x64_bf16 v[48:51], v[84:87], v[184:191], v4\n" \
"v_smfmac_f32_16x16x64_bf16 v[52:55], v[80:83], v[160:167], v4\ns_waitcnt lgkmcnt(5)\nv_smfmac_f32_16x16x64_bf16 v[52:55], v[84:87], v[72:79], v4\nds_read_b128 v[72:75], v1 offset:22528\nds_read_b128 v[76:79], v1 offset:23552\nv_smfmac_f32_16x16x64_bf16 v[56:59], v[80:83], v[168:175], v4\nv_smfmac_f32_16x16x64_bf16 v[56:59], v[84:87], v[192:199], v4\nv_smfmac_f32_16x16x64_bf16 v[60:63], v[80:83], v[176:183], v4\n" \
"ds_read_b128 v[80:83], v0 offset:16384\nv_smfmac_f32_16x16x64_bf16 v[60:63], v[84:87], v[200:207], v4\ns_waitcnt lgkmcnt(7)\nds_read_b128 v[84:87], v0 offset:17408\nv_smfmac_f32_16x16x64_bf16 v[48:51], v[88:91], v[208:215], v4\ns_waitcnt lgkmcnt(7)\nds_write_b128 v5, v[44:47] offset:768\nv_smfmac_f32_16x16x64_bf16 v[48:51], v[92:95], v[232:239], v4\n" \
"v_smfmac_f32_16x16x64_bf16 v[52:55], v[88:91], v[216:223], v4\nv_smfmac_f32_16x16x64_bf16 v[52:55], v[92:95], v[240:247], v4\ns_waitcnt lgkmcnt(6)\nv_smfmac_f32_16x16x64_bf16 v[56:59], v[88:91], v[64:71], v4\nds_read_b128 v[64:67], v1 offset:0\nds_read_b128 v[68:71], v1 offset:1024\nv_smfmac_f32_16x16x64_bf16 v[56:59], v[92:95], v[248:255], v4\ns_waitcnt lgkmcnt(2)\n" \
"s_barrier\nv_smfmac_f32_16x16x64_bf16 v[60:63], v[88:91], v[224:231], v4\nds_read_b128 v[88:91], v0 offset:18432\nds_read_b128 v[16:19], v7 offset:256\nv_smfmac_f32_16x16x64_bf16 v[60:63], v[92:95], v[72:79], v4\nds_read_b128 v[92:95], v0 offset:19456\nds_read_b128 v[72:75], v1 offset:2048\nds_read_b128 v[76:79], v1 offset:3072\n" \
"v_smfmac_f32_16x16x64_bf16 v[48:51], v[80:83], a[0:7], v4\nds_read_b128 v[20:23], v7 offset:2304\nv_smfmac_f32_16x16x64_bf16 v[48:51], v[84:87], a[32:39], v4\nv_smfmac_f32_16x16x64_bf16 v[52:55], v[80:83], a[8:15], v4\ns_waitcnt lgkmcnt(6)\nds_read_b128 v[24:27], v7 offset:4352\nv_smfmac_f32_16x16x64_bf16 v[52:55], v[84:87], v[64:71], v4\nds_read_b128 v[64:67], v1 offset:4096\n" \
"ds_read_b128 v[68:71], v1 offset:5120\nv_smfmac_f32_16x16x64_bf16 v[56:59], v[80:83], a[16:23], v4\nds_read_b128 v[28:31], v7 offset:6400\nv_smfmac_f32_16x16x64_bf16 v[56:59], v[84:87], a[40:47], v4\nv_smfmac_f32_16x16x64_bf16 v[60:63], v[80:83], a[24:31], v4\nds_read_b128 v[80:83], v0 offset:20480\nds_read_b128 v[32:35], v7 offset:8192\nv_smfmac_f32_16x16x64_bf16 v[60:63], v[84:87], a[48:55], v4\n" \
"s_waitcnt lgkmcnt(11)\nds_read_b128 v[84:87], v0 offset:21504\nv_smfmac_f32_16x16x64_bf16 v[48:51], v[88:91], a[56:63], v4\ns_waitcnt lgkmcnt(10)\nds_read_b128 v[36:39], v7 offset:10240\nv_smfmac_f32_16x16x64_bf16 v[48:51], v[92:95], a[80:87], v4\nv_smfmac_f32_16x16x64_bf16 v[52:55], v[88:91], a[64:71], v4\nds_read_b128 v[40:43], v7 offset:12288\n" \
"v_smfmac_f32_16x16x64_bf16 v[52:55], v[92:95], a[88:95], v4\ns_waitcnt lgkmcnt(10)\nv_smfmac_f32_16x16x64_bf16 v[56:59], v[88:91], v[72:79], v4\nds_read_b128 v[72:75], v1 offset:6144\nds_read_b128 v[76:79], v1 offset:7168\nv_smfmac_f32_16x16x64_bf16 v[56:59], v[92:95], a[96:103], v4\nv_smfmac_f32_16x16x64_bf16 v[60:63], v[88:91], a[72:79], v4\ns_waitcnt lgkmcnt(8)\n" \
"ds_read_b128 v[88:91], v0 offset:22528\nds_read_b128 v[44:47], v7 offset:14336\nv_smfmac_f32_16x16x64_bf16 v[60:63], v[92:95], v[64:71], v4\ns_waitcnt lgkmcnt(8)\nds_read_b128 v[92:95], v0 offset:23552\nds_read_b128 v[64:67], v1 offset:8192\nds_read_b128 v[68:71], v1 offset:9216\nv_smfmac_f32_16x16x64_bf16 v[48:51], v[80:83], a[104:111], v4\n" \
"s_waitcnt lgkmcnt(9)\nv_smfmac_f32_16x16x64_bf16 v[48:51], v[84:87], a[136:143], v4\nv_smfmac_f32_16x16x64_bf16 v[52:55], v[80:83], a[112:119], v4\ns_waitcnt lgkmcnt(5)\nv_smfmac_f32_16x16x64_bf16 v[52:55], v[84:87], v[72:79], v4\nds_read_b128 v[72:75], v1 offset:10240\nds_read_b128 v[76:79], v1 offset:11264\nv_smfmac_f32_16x16x64_bf16 v[56:59], v[80:83], a[120:127], v4\n" \
"v_smfmac_f32_16x16x64_bf16 v[56:59], v[84:87], a[144:151], v4\nv_smfmac_f32_16x16x64_bf16 v[60:63], v[80:83], a[128:135], v4\nv_smfmac_f32_16x16x64_bf16 v[60:63], v[84:87], a[152:159], v4\ns_waitcnt lgkmcnt(6)\nv_smfmac_f32_16x16x64_bf16 v[48:51], v[88:91], a[160:167], v4\ns_waitcnt lgkmcnt(4)\nds_read_b128 v[80:83], v7 offset:8448\nv_smfmac_f32_16x16x64_bf16 v[48:51], v[92:95], a[184:191], v4\n" \
"ds_read_b128 v[84:87], v7 offset:10496\nv_smfmac_f32_16x16x64_bf16 v[52:55], v[88:91], a[168:175], v4\nv_smfmac_f32_16x16x64_bf16 v[52:55], v[92:95], a[192:199], v4\ns_waitcnt lgkmcnt(4)\nv_smfmac_f32_16x16x64_bf16 v[56:59], v[88:91], v[64:71], v4\nds_read_b128 v[64:67], v7 offset:12544\nv_add_f32 v48, v48, v50\nv_smfmac_f32_16x16x64_bf16 v[56:59], v[92:95], a[200:207], v4\n" \
"ds_read_b128 v[68:71], v7 offset:14592\nv_add_f32 v52, v52, v54\nv_add_f32 v49, v49, v51\nv_smfmac_f32_16x16x64_bf16 v[60:63], v[88:91], a[176:183], v4\ns_waitcnt lgkmcnt(4)\nv_add_f32 v53, v53, v55\nv_cvt_pk_bf16_f32 v50, v48, v52\nv_smfmac_f32_16x16x64_bf16 v[60:63], v[92:95], v[72:79], v4\n" \
"v_cvt_pk_bf16_f32 v51, v49, v53\nds_write2_b32 v6, v50, v51 offset0:0 offset1:4\nv_add_f32 v56, v56, v58\nv_add_f32 v57, v57, v59\ns_nop 3\nv_add_f32 v60, v60, v62\nv_add_f32 v61, v61, v63\nv_cvt_pk_bf16_f32 v62, v56, v60\n" \
"v_cvt_pk_bf16_f32 v63, v57, v61\nds_write2_b32 v6, v62, v63 offset0:36 offset1:40\nv_mov_b32 v61, s54\nv_mfma_f32_16x16x32_bf16 v[52:55], v[32:35], v[16:19], 0\nv_mfma_f32_16x16x32_bf16 v[52:55], v[36:39], v[20:23], v[52:55]\nv_mfma_f32_16x16x32_bf16 v[52:55], v[40:43], v[24:27], v[52:55]\nv_mfma_f32_16x16x32_bf16 v[52:55], v[44:47], v[28:31], v[52:55]\ns_waitcnt lgkmcnt(0)\n" \
"s_barrier\nv_mfma_f32_16x16x32_bf16 v[56:59], v[80:83], v[16:19], v[12:15]\nv_mfma_f32_16x16x32_bf16 v[56:59], v[84:87], v[20:23], v[56:59]\nds_read_b128 v[32:35], v9 offset:0\nds_read_b128 v[36:39], v9 offset:576\nds_read_b128 v[40:43], v9 offset:1152\nds_read_b128 v[44:47], v9 offset:1728\nds_read_b32 v60, v0 offset:53760\n" \
"v_mfma_f32_16x16x32_bf16 v[56:59], v[64:67], v[24:27], v[56:59]\nv_mfma_f32_16x16x32_bf16 v[56:59], v[68:71], v[28:31], v[56:59]\ns_waitcnt lgkmcnt(4)\nds_read_b128 v[80:83], v2 offset:0\nds_read_b128 v[84:87], v2 offset:2048\nv_mfma_f32_16x16x32_bf16 v[48:51], v[32:35], v[16:19], 0\ns_waitcnt lgkmcnt(5)\nv_mfma_f32_16x16x32_bf16 v[48:51], v[36:39], v[20:23], v[48:51]\n" \
"s_waitcnt lgkmcnt(4)\nv_mfma_f32_16x16x32_bf16 v[48:51], v[40:43], v[24:27], v[48:51]\ns_waitcnt lgkmcnt(3)\nv_mfma_f32_16x16x32_bf16 v[48:51], v[44:47], v[28:31], v[48:51]\ns_waitcnt lgkmcnt(1)\nv_mfma_f32_16x16x32_bf16 v[16:19], a[0:3], v[80:83], 0\ns_waitcnt lgkmcnt(0)\nds_read_b128 v[88:91], v2 offset:256\n" \
"v_max3_f32 v62, v52, v53, v54\nv_max3_f32 v62, v62, v56, v57\nv_mfma_f32_16x16x32_bf16 v[16:19], a[4:7], v[84:87], v[16:19]\nds_read_b128 v[92:95], v2 offset:2304\nv_max3_f32 v62, v62, v58, v59\nv_max_f32 v62, v62, v55\nv_mfma_f32_16x16x32_bf16 v[20:23], a[8:11], v[80:83], 0\nv_max3_f32 v63, v62, v48, v49\n" \
"v_max3_f32 v62, v62, v48, v49\nv_mfma_f32_16x16x32_bf16 v[20:23], a[12:15], v[84:87], v[20:23]\ns_nop 0\nv_permlane16_swap_b32 v62, v63\nv_max_f32 v50, v62, v63\nv_mfma_f32_16x16x32_bf16 v[24:27], a[16:19], v[80:83], 0\nds_read_b128 v[72:75], v1 offset:0\nv_max_f32 v62, v62, v63\n" \
"v_mfma_f32_16x16x32_bf16 v[24:27], a[20:23], v[84:87], v[24:27]\nds_read_b128 v[76:79], v1 offset:1024\nv_permlane32_swap_b32 v62, v50\nv_mfma_f32_16x16x32_bf16 v[28:31], a[24:27], v[80:83], 0\nv_max_f32 v62, v62, v50\nv_mul_f32 v62, s59, v62\nv_fma_f32 v52, v52, s58, v62\nv_mfma_f32_16x16x32_bf16 v[28:31], a[28:31], v[84:87], v[28:31]\n" \
"s_waitcnt lgkmcnt(3)\nv_fma_f32 v53, v53, s58, v62\nv_exp_f32 v52, v52\nv_mfma_f32_16x16x32_bf16 v[32:35], a[0:3], v[88:91], 0\ns_waitcnt lgkmcnt(2)\nds_read_b128 v[80:83], v2 offset:4096\nv_fma_f32 v54, v54, s58, v62\nv_fma_f32 v55, v55, s58, v62\n" \
"v_mfma_f32_16x16x32_bf16 v[32:35], a[4:7], v[92:95], v[32:35]\nds_read_b128 v[84:87], v2 offset:6144\nv_exp_f32 v53, v53\nv_mfma_f32_16x16x32_bf16 v[36:39], a[8:11], v[88:91], 0\nv_exp_f32 v54, v54\nv_add_f32 v63, v52, v53\nv_mfma_f32_16x16x32_bf16 v[36:39], a[12:15], v[92:95], v[36:39]\nv_fma_f32 v56, v56, s58, v62\n" \
"v_exp_f32 v55, v55\nv_mfma_f32_16x16x32_bf16 v[40:43], a[16:19], v[88:91], 0\nv_add_f32 v63, v63, v54\nv_fma_f32 v57, v57, s58, v62\nv_add_f32 v63, v63, v55\nv_mfma_f32_16x16x32_bf16 v[40:43], a[20:23], v[92:95], v[40:43]\nv_exp_f32 v56, v56\nv_fma_f32 v58, v58, s58, v62\n" \
"v_mfma_f32_16x16x32_bf16 v[44:47], a[24:27], v[88:91], 0\nv_exp_f32 v57, v57\nv_add_f32 v63, v63, v56\nv_mfma_f32_16x16x32_bf16 v[44:47], a[28:31], v[92:95], v[44:47]\ns_waitcnt lgkmcnt(1)\nv_fma_f32 v59, v59, s58, v62\nv_exp_f32 v58, v58\nv_mfma_f32_16x16x32_bf16 v[16:19], a[32:35], v[80:83], v[16:19]\n" \
"s_waitcnt lgkmcnt(0)\nds_read_b128 v[88:91], v2 offset:4352\nv_add_f32 v63, v63, v57\nv_fma_f32 v48, v48, s58, v62\nv_mfma_f32_16x16x32_bf16 v[16:19], a[36:39], v[84:87], v[16:19]\nds_read_b128 v[92:95], v2 offset:6400\nv_exp_f32 v59, v59\nv_mfma_f32_16x16x32_bf16 v[20:23], v[72:75], v[80:83], v[20:23]\n" \
"v_add_f32 v63, v63, v58\nv_fma_f32 v49, v49, s58, v62\nv_add_f32 v63, v63, v59\nv_mfma_f32_16x16x32_bf16 v[20:23], v[76:79], v[84:87], v[20:23]\nv_exp_f32 v48, v48\nv_mfma_f32_16x16x32_bf16 v[24:27], a[40:43], v[80:83], v[24:27]\nds_read_b128 v[64:67], v1 offset:2048\nv_exp_f32 v49, v49\n" \
"v_mfma_f32_16x16x32_bf16 v[24:27], a[44:47], v[84:87], v[24:27]\nds_read_b128 v[68:71], v1 offset:3072\nv_add_f32 v63, v63, v48\nv_add_f32 v62, v63, v49\nv_mfma_f32_16x16x32_bf16 v[28:31], a[48:51], v[80:83], v[28:31]\nv_add_f32 v50, v63, v49\nv_mfma_f32_16x16x32_bf16 v[28:31], a[52:55], v[84:87], v[28:31]\ns_waitcnt lgkmcnt(3)\n" \
"v_permlane16_swap_b32 v62, v50\nv_add_f32 v63, v62, v50\nv_mfma_f32_16x16x32_bf16 v[32:35], a[32:35], v[88:91], v[32:35]\ns_waitcnt lgkmcnt(2)\nds_read_b128 v[80:83], v2 offset:8192\nv_add_f32 v50, v62, v50\nv_mfma_f32_16x16x32_bf16 v[32:35], a[36:39], v[92:95], v[32:35]\nds_read_b128 v[84:87], v2 offset:10240\n" \
"v_permlane32_swap_b32 v63, v50\nv_mfma_f32_16x16x32_bf16 v[36:39], v[72:75], v[88:91], v[36:39]\nv_add_f32 v63, v63, v50\nv_rcp_f32 v63, v63\nv_mfma_f32_16x16x32_bf16 v[36:39], v[76:79], v[92:95], v[36:39]\nv_mul_f32 v52, v52, v63\nv_mul_f32 v53, v53, v63\nv_mul_f32 v54, v54, v63\n" \
"v_mfma_f32_16x16x32_bf16 v[40:43], a[40:43], v[88:91], v[40:43]\nv_mul_f32 v55, v55, v63\nv_mul_f32 v56, v56, v63\nv_mul_f32 v57, v57, v63\nv_mfma_f32_16x16x32_bf16 v[40:43], a[44:47], v[92:95], v[40:43]\nv_mul_f32 v58, v58, v63\nv_mul_f32 v59, v59, v63\nv_mul_f32 v48, v48, v63\n" \
"v_mfma_f32_16x16x32_bf16 v[44:47], a[48:51], v[88:91], v[44:47]\nv_mul_f32 v49, v49, v63\nv_cvt_pk_bf16_f32 v52, v52, v53\nv_cvt_pk_bf16_f32 v53, v54, v55\nv_mfma_f32_16x16x32_bf16 v[44:47], a[52:55], v[92:95], v[44:47]\ns_waitcnt lgkmcnt(1)\nv_cvt_pk_bf16_f32 v48, v48, v49\nv_cvt_pk_bf16_f32 v54, v56, v57\n" \
"v_cvt_pk_bf16_f32 v55, v58, v59\nv_mfma_f32_16x16x32_bf16 v[16:19], a[56:59], v[80:83], v[16:19]\ns_waitcnt lgkmcnt(0)\nds_read_b128 v[88:91], v2 offset:8448\nv_bfi_b32 v52, s61, v52, v61\nv_bfi_b32 v53, s61, v53, v61\nv_mfma_f32_16x16x32_bf16 v[16:19], a[60:63], v[84:87], v[16:19]\nds_read_b128 v[92:95], v2 offset:10496\n" \
"v_bfi_b32 v54, s61, v54, v61\nv_bfi_b32 v55, s61, v55, v61\nv_mfma_f32_16x16x32_bf16 v[20:23], a[64:67], v[80:83], v[20:23]\nv_bfi_b32 v48, s61, v48, v61\nv_mfma_f32_16x16x32_bf16 v[20:23], a[68:71], v[84:87], v[20:23]\nbuffer_store_dwordx4 v[52:55], v10, s[36:39], s55 offen sc1\nv_mov_b32 v49, v48\nv_mfma_f32_16x16x32_bf16 v[24:27], v[64:67], v[80:83], v[24:27]\n" \
"ds_read_b128 v[72:75], v1 offset:4096\nv_permlane16_swap_b32 v48, v49\nv_mfma_f32_16x16x32_bf16 v[24:27], v[68:71], v[84:87], v[24:27]\nds_read_b128 v[76:79], v1 offset:5120\nv_mov_b32 v50, v48\nv_mov_b32 v51, v49\nv_mfma_f32_16x16x32_bf16 v[28:31], a[72:75], v[80:83], v[28:31]\nv_permlane32_swap_b32 v48, v50\n" \
"v_mfma_f32_16x16x32_bf16 v[28:31], a[76:79], v[84:87], v[28:31]\ns_waitcnt lgkmcnt(3)\nv_permlane32_swap_b32 v49, v51\nv_mfma_f32_16x16x32_bf16 v[32:35], a[56:59], v[88:91], v[32:35]\ns_waitcnt lgkmcnt(2)\nds_read_b128 v[80:83], v2 offset:12288\nv_mfma_f32_16x16x32_bf16 v[32:35], a[60:63], v[92:95], v[32:35]\nds_read_b128 v[84:87], v2 offset:14336\n" \
"v_mfma_f32_16x16x32_bf16 v[36:39], a[64:67], v[88:91], v[36:39]\nbuffer_store_dwordx4 v[48:51], v11, s[36:39], s55 offen sc1\nv_cmp_ne_u32 vcc, 0, v60\nv_mfma_f32_16x16x32_bf16 v[36:39], a[68:71], v[92:95], v[36:39]\nv_mfma_f32_16x16x32_bf16 v[40:43], v[64:67], v[88:91], v[40:43]\nv_mfma_f32_16x16x32_bf16 v[40:43], v[68:71], v[92:95], v[40:43]\nv_mfma_f32_16x16x32_bf16 v[44:47], a[72:75], v[88:91], v[44:47]\nv_mfma_f32_16x16x32_bf16 v[44:47], a[76:79], v[92:95], v[44:47]\n" \
"s_cbranch_vccnz .Lqk_pj%=\ns_cmp_lt_i32 s40, 63\ns_waitcnt lgkmcnt(0)\ns_cbranch_scc1 .Lqk_pwait%=\ns_branch .Lqk_end%=\n.Lqk_re2%=:\nv_max_u32 v17, v80, v82\nv_cmp_eq_u32 vcc, -1, v17\n" \
"s_cbranch_vccz .Lqk_sk2428%=\ns_mov_b64 exec, vcc\nbuffer_load_dwordx4 v[80:83], v3, s[36:39], s42 offen offset:0 sc1\ns_mov_b64 exec, -1\n.Lqk_sk2428%=:\nv_max_u32 v17, v84, v86\nv_cmp_eq_u32 vcc, -1, v17\ns_cbranch_vccz .Lqk_sk2435%=\n" \
"s_mov_b64 exec, vcc\nbuffer_load_dwordx4 v[84:87], v3, s[36:39], s42 offen offset:1024 sc1\ns_mov_b64 exec, -1\n.Lqk_sk2435%=:\nv_max_u32 v17, v88, v90\nv_cmp_eq_u32 vcc, -1, v17\ns_cbranch_vccz .Lqk_sk2442%=\ns_mov_b64 exec, vcc\n" \
"buffer_load_dwordx4 v[88:91], v3, s[36:39], s42 offen offset:2048 sc1\ns_mov_b64 exec, -1\n.Lqk_sk2442%=:\nv_max_u32 v17, v92, v94\nv_cmp_eq_u32 vcc, -1, v17\ns_cbranch_vccz .Lqk_sk2449%=\ns_mov_b64 exec, vcc\nbuffer_load_dwordx4 v[92:95], v3, s[36:39], s42 offen offset:3072 sc1\n" \
"s_mov_b64 exec, -1\n.Lqk_sk2449%=:\nv_mov_b64 v[48:49], 0\nv_mov_b64 v[50:51], 0\nv_mov_b64 v[52:53], 0\nv_mov_b64 v[54:55], 0\nv_mov_b64 v[56:57], 0\nv_mov_b64 v[58:59], 0\n" \
"v_mov_b64 v[60:61], 0\nv_mov_b64 v[62:63], 0\ns_add_u32 s43, s43, 1\ns_cmp_lt_u32 s43, 262144\ns_cbranch_scc1 .Lqk_poll2%=\ns_branch .Lqk_abort%=\n.p2align 6\n.Lqk_c3%=:\n" \
"s_waitcnt lgkmcnt(3)\nv_mfma_f32_16x16x32_bf16 v[32:35], v[232:235], v[56:59], v[32:35]\ns_waitcnt lgkmcnt(2)\nv_mov_b64 v[48:49], 0\nv_mov_b64 v[50:51], 0\nv_mfma_f32_16x16x32_bf16 v[32:35], v[236:239], v[60:63], v[32:35]\ns_add_u32 s44, s40, 1\ns_lshl_b32 s44, s44, 17\n" \
"ds_read_b128 v[64:67], v1 offset:18432\nv_mfma_f32_16x16x32_bf16 v[36:39], v[240:243], v[56:59], v[36:39]\ns_add_u32 s44, s44, %[xn]\ns_lshr_b32 s45, s44, 8\nds_read_b128 v[68:71], v1 offset:19456\nv_mfma_f32_16x16x32_bf16 v[36:39], v[244:247], v[60:63], v[36:39]\ns_and_b32 s45, s45, 0x2000\ns_xor_b32 s41, s44, s45\n" \
"v_mov_b64 v[52:53], 0\nv_mov_b64 v[54:55], 0\nv_mfma_f32_16x16x32_bf16 v[40:43], v[248:251], v[56:59], v[40:43]\nbuffer_load_dword v16, v12, s[36:39], s56 offen sc1\nv_mfma_f32_16x16x32_bf16 v[40:43], v[252:255], v[60:63], v[40:43]\nv_cvt_pk_bf16_f32 v36, v32, v36\nv_cvt_pk_bf16_f32 v37, v33, v37\nv_mfma_f32_16x16x32_bf16 v[44:47], v[72:75], v[56:59], v[44:47]\n" \
"v_cvt_pk_bf16_f32 v38, v34, v38\nv_cvt_pk_bf16_f32 v39, v35, v39\nv_mfma_f32_16x16x32_bf16 v[44:47], v[76:79], v[60:63], v[44:47]\nds_read_b128 v[72:75], v1 offset:20480\nds_read_b128 v[76:79], v1 offset:21504\nv_mov_b64 v[56:57], 0\nv_mov_b64 v[58:59], 0\nv_mov_b64 v[60:61], 0\n" \
"v_mov_b64 v[62:63], 0\n.Lqk_poll3%=:\ns_waitcnt vmcnt(1)\nv_smfmac_f32_16x16x64_bf16 v[48:51], v[80:83], v[152:159], v4\nds_write_b128 v0, v[80:83] offset:28672\nv_max3_u32 v17, v80, v82, v84\nv_smfmac_f32_16x16x64_bf16 v[48:51], v[84:87], v[184:191], v4\nv_max3_u32 v17, v17, v86, v88\n" \
"v_smfmac_f32_16x16x64_bf16 v[52:55], v[80:83], v[160:167], v4\ns_waitcnt lgkmcnt(3)\nds_write_b128 v0, v[84:87] offset:29696\nv_max3_u32 v17, v17, v90, v92\nv_max_u32 v17, v17, v94\nv_smfmac_f32_16x16x64_bf16 v[52:55], v[84:87], v[64:71], v4\nv_cmp_eq_u32 vcc, -1, v17\nv_smfmac_f32_16x16x64_bf16 v[56:59], v[80:83], v[168:175], v4\n" \
"ds_write_b128 v0, v[88:91] offset:30720\ns_cbranch_vccnz .Lqk_re3%=\nv_smfmac_f32_16x16x64_bf16 v[56:59], v[84:87], v[192:199], v4\nv_smfmac_f32_16x16x64_bf16 v[60:63], v[80:83], v[176:183], v4\nds_write_b128 v0, v[92:95] offset:31744\nv_smfmac_f32_16x16x64_bf16 v[60:63], v[84:87], v[200:207], v4\nv_smfmac_f32_16x16x64_bf16 v[48:51], v[88:91], v[208:215], v4\nv_smfmac_f32_16x16x64_bf16 v[48:51], v[92:95], v[232:239], v4\n" \
"s_waitcnt lgkmcnt(0)\n.Lqk_jn3%=:\ns_barrier\nds_read_b128 v[80:83], v0 offset:16384\nds_read_b128 v[64:67], v1 offset:22528\nds_read_b128 v[68:71], v1 offset:23552\nv_smfmac_f32_16x16x64_bf16 v[52:55], v[88:91], v[216:223], v4\ns_waitcnt vmcnt(0)\n" \
"v_cmp_ge_i32 vcc, s40, v16\nv_smfmac_f32_16x16x64_bf16 v[52:55], v[92:95], v[240:247], v4\ns_cmp_eq_u64 vcc, 0\ns_cselect_b32 s61, s41, -2147483648\ns_cselect_b32 s44, 1, 0\nds_read_b128 v[84:87], v0 offset:17408\nv_smfmac_f32_16x16x64_bf16 v[56:59], v[88:91], v[72:79], v4\ns_add_u32 m0, %[l0], 0\n" \
"s_add_u32 s60, s61, 0\nds_read_b128 v[72:75], v1 offset:0\nds_read_b128 v[76:79], v1 offset:1024\nbuffer_load_dwordx4 v0, s[36:39], s60 offen offset:0 sc1 lds\nv_smfmac_f32_16x16x64_bf16 v[56:59], v[92:95], v[248:255], v4\nv_mov_b32 v16, s44\nv_smfmac_f32_16x16x64_bf16 v[60:63], v[88:91], v[224:231], v4\ns_waitcnt lgkmcnt(3)\n" \
"ds_read_b128 v[88:91], v0 offset:18432\nbuffer_load_dwordx4 v0, s[36:39], s60 offen offset:1024 sc1 lds\nv_smfmac_f32_16x16x64_bf16 v[60:63], v[92:95], v[64:71], v4\nds_read_b128 v[92:95], v0 offset:19456\nds_read_b128 v[64:67], v1 offset:2048\nds_read_b128 v[68:71], v1 offset:3072\nds_write_b32 v0, v16 offset:53760\nv_smfmac_f32_16x16x64_bf16 v[48:51], v[80:83], a[0:7], v4\n" \
"s_waitcnt lgkmcnt(7)\nds_write_b128 v5, v[36:39] offset:256\nbuffer_load_dwordx4 v0, s[36:39], s60 offen offset:2048 sc1 lds\nv_cvt_pk_bf16_f32 v44, v40, v44\nv_cvt_pk_bf16_f32 v45, v41, v45\nv_cvt_pk_bf16_f32 v46, v42, v46\nv_cvt_pk_bf16_f32 v47, v43, v47\nv_smfmac_f32_16x16x64_bf16 v[48:51], v[84:87], a[32:39], v4\n" \
"v_smfmac_f32_16x16x64_bf16 v[52:55], v[80:83], a[8:15], v4\ns_waitcnt lgkmcnt(6)\nbuffer_load_dwordx4 v0, s[36:39], s60 offen offset:3072 sc1 lds\nv_smfmac_f32_16x16x64_bf16 v[52:55], v[84:87], v[72:79], v4\ns_add_u32 m0, %[l0], 4096\nds_read_b128 v[72:75], v1 offset:4096\nds_read_b128 v[76:79], v1 offset:5120\nv_smfmac_f32_16x16x64_bf16 v[56:59], v[80:83], a[16:23], v4\n" \
"s_add_u32 s60, s61, 4096\nbuffer_load_dwordx4 v0, s[36:39], s60 offen offset:0 sc1 lds\nv_smfmac_f32_16x16x64_bf16 v[56:59], v[84:87], a[40:47], v4\nv_smfmac_f32_16x16x64_bf16 v[60:63], v[80:83], a[24:31], v4\nds_read_b128 v[80:83], v0 offset:20480\nbuffer_load_dwordx4 v0, s[36:39], s60 offen offset:1024 sc1 lds\nv_smfmac_f32_16x16x64_bf16 v[60:63], v[84:87], a[48:55], v4\ns_waitcnt lgkmcnt(8)\n" \
"ds_read_b128 v[84:87], v0 offset:21504\nv_smfmac_f32_16x16x64_bf16 v[48:51], v[88:91], a[56:63], v4\ns_waitcnt lgkmcnt(8)\nds_write_b128 v5, v[44:47] offset:768\nbuffer_load_dwordx4 v0, s[36:39], s60 offen offset:2048 sc1 lds\nv_smfmac_f32_16x16x64_bf16 v[48:51], v[92:95], a[80:87], v4\nv_smfmac_f32_16x16x64_bf16 v[52:55], v[88:91], a[64:71], v4\nbuffer_load_dwordx4 v0, s[36:39], s60 offen offset:3072 sc1 lds\n" \
"v_smfmac_f32_16x16x64_bf16 v[52:55], v[92:95], a[88:95], v4\ns_waitcnt lgkmcnt(7)\ns_add_u32 m0, %[l0], 8192\nv_smfmac_f32_16x16x64_bf16 v[56:59], v[88:91], v[64:71], v4\ns_add_u32 s60, s61, 16384\nds_read_b128 v[64:67], v1 offset:6144\nds_read_b128 v[68:71], v1 offset:7168\nbuffer_load_dwordx4 v0, s[36:39], s60 offen offset:0 sc1 lds\n" \
"v_smfmac_f32_16x16x64_bf16 v[56:59], v[92:95], a[96:103], v4\ns_waitcnt lgkmcnt(2)\ns_barrier\nv_smfmac_f32_16x16x64_bf16 v[60:63], v[88:91], a[72:79], v4\nds_read_b128 v[88:91], v0 offset:22528\nbuffer_load_dwordx4 v0, s[36:39], s60 offen offset:1024 sc1 lds\nv_smfmac_f32_16x16x64_bf16 v[60:63], v[92:95], v[72:79], v4\nds_read_b128 v[92:95], v0 offset:23552\n" \
"ds_read_b128 v[72:75], v1 offset:8192\nds_read_b128 v[76:79], v1 offset:9216\nv_smfmac_f32_16x16x64_bf16 v[48:51], v[80:83], a[104:111], v4\nbuffer_load_dwordx4 v0, s[36:39], s60 offen offset:2048 sc1 lds\nv_smfmac_f32_16x16x64_bf16 v[48:51], v[84:87], a[136:143], v4\nv_smfmac_f32_16x16x64_bf16 v[52:55], v[80:83], a[112:119], v4\ns_waitcnt lgkmcnt(4)\nbuffer_load_dwordx4 v0, s[36:39], s60 offen offset:3072 sc1 lds\n" \
"v_smfmac_f32_16x16x64_bf16 v[52:55], v[84:87], v[64:71], v4\ns_add_u32 m0, %[l0], 12288\nds_read_b128 v[64:67], v1 offset:10240\nds_read_b128 v[68:71], v1 offset:11264\nv_smfmac_f32_16x16x64_bf16 v[56:59], v[80:83], a[120:127], v4\ns_add_u32 s60, s61, 20480\nbuffer_load_dwordx4 v0, s[36:39], s60 offen offset:0 sc1 lds\nv_smfmac_f32_16x16x64_bf16 v[56:59], v[84:87], a[144:151], v4\n" \
"v_smfmac_f32_16x16x64_bf16 v[60:63], v[80:83], a[128:135], v4\nds_read_b128 v[80:83], v0 offset:24576\nbuffer_load_dwordx4 v0, s[36:39], s60 offen offset:1024 sc1 lds\nv_smfmac_f32_16x16x64_bf16 v[60:63], v[84:87], a[152:159], v4\ns_waitcnt lgkmcnt(6)\nds_read_b128 v[84:87], v0 offset:25600\nv_smfmac_f32_16x16x64_bf16 v[48:51], v[88:91], a[160:167], v4\ns_waitcnt lgkmcnt(6)\n" \
"buffer_load_dwordx4 v0, s[36:39], s60 offen offset:2048 sc1 lds\nv_smfmac_f32_16x16x64_bf16 v[48:51], v[92:95], a[184:191], v4\nv_smfmac_f32_16x16x64_bf16 v[52:55], v[88:91], a[168:175], v4\nbuffer_load_dwordx4 v0, s[36:39], s60 offen offset:3072 sc1 lds\nv_smfmac_f32_16x16x64_bf16 v[52:55], v[92:95], a[192:199], v4\ns_waitcnt lgkmcnt(4)\nv_smfmac_f32_16x16x64_bf16 v[56:59], v[88:91], v[72:79], v4\nds_read_b128 v[72:75], v1 offset:12288\n" \
"ds_read_b128 v[76:79], v1 offset:13312\nv_smfmac_f32_16x16x64_bf16 v[56:59], v[92:95], a[200:207], v4\nv_smfmac_f32_16x16x64_bf16 v[60:63], v[88:91], a[176:183], v4\ns_waitcnt lgkmcnt(4)\nds_read_b128 v[88:91], v0 offset:26624\nv_smfmac_f32_16x16x64_bf16 v[60:63], v[92:95], v[64:71], v4\ns_waitcnt lgkmcnt(4)\nds_read_b128 v[92:95], v0 offset:27648\n" \
"ds_read_b128 v[64:67], v1 offset:14336\nds_read_b128 v[68:71], v1 offset:15360\nv_smfmac_f32_16x16x64_bf16 v[48:51], v[80:83], a[208:215], v4\ns_waitcnt lgkmcnt(6)\nv_smfmac_f32_16x16x64_bf16 v[48:51], v[84:87], a[240:247], v4\nv_smfmac_f32_16x16x64_bf16 v[52:55], v[80:83], a[216:223], v4\ns_waitcnt lgkmcnt(4)\nv_smfmac_f32_16x16x64_bf16 v[52:55], v[84:87], v[72:79], v4\n" \
"ds_read_b128 v[72:75], v1 offset:16384\nds_read_b128 v[76:79], v1 offset:17408\nv_smfmac_f32_16x16x64_bf16 v[56:59], v[80:83], a[224:231], v4\nv_smfmac_f32_16x16x64_bf16 v[56:59], v[84:87], a[248:255], v4\nv_smfmac_f32_16x16x64_bf16 v[60:63], v[80:83], a[232:239], v4\nv_smfmac_f32_16x16x64_bf16 v[60:63], v[84:87], v[96:103], v4\ns_waitcnt lgkmcnt(5)\nv_smfmac_f32_16x16x64_bf16 v[48:51], v[88:91], v[104:111], v4\n" \
"s_waitcnt lgkmcnt(4)\nv_smfmac_f32_16x16x64_bf16 v[48:51], v[92:95], v[128:135], v4\nv_smfmac_f32_16x16x64_bf16 v[52:55], v[88:91], v[112:119], v4\nv_smfmac_f32_16x16x64_bf16 v[52:55], v[92:95], v[136:143], v4\ns_waitcnt lgkmcnt(2)\nv_smfmac_f32_16x16x64_bf16 v[56:59], v[88:91], v[64:71], v4\nv_add_f32 v48, v48, v50\nv_smfmac_f32_16x16x64_bf16 v[56:59], v[92:95], v[144:151], v4\n" \
"v_add_f32 v52, v52, v54\nv_add_f32 v49, v49, v51\nv_smfmac_f32_16x16x64_bf16 v[60:63], v[88:91], v[120:127], v4\ns_waitcnt lgkmcnt(0)\nv_add_f32 v53, v53, v55\nv_cvt_pk_bf16_f32 v50, v48, v52\nv_smfmac_f32_16x16x64_bf16 v[60:63], v[92:95], v[72:79], v4\nv_cvt_pk_bf16_f32 v51, v49, v53\n" \
"ds_write2_b32 v6, v50, v51 offset0:0 offset1:4\nv_add_f32 v56, v56, v58\nv_add_f32 v57, v57, v59\ns_nop 3\nv_add_f32 v60, v60, v62\nv_add_f32 v61, v61, v63\nv_cvt_pk_bf16_f32 v62, v56, v60\nv_cvt_pk_bf16_f32 v63, v57, v61\n" \
"ds_write2_b32 v6, v62, v63 offset0:36 offset1:40\ns_waitcnt vmcnt(0)\ns_waitcnt lgkmcnt(0)\ns_barrier\ns_cbranch_vccnz .Lqk_slow%=\ns_add_u32 m0, %[l0], 16384\ns_add_u32 s60, s41, 32768\nbuffer_load_dwordx4 v0, s[36:39], s60 offen offset:0 sc1 lds\n" \
"buffer_load_dwordx4 v0, s[36:39], s60 offen offset:1024 sc1 lds\nbuffer_load_dwordx4 v0, s[36:39], s60 offen offset:2048 sc1 lds\nbuffer_load_dwordx4 v0, s[36:39], s60 offen offset:3072 sc1 lds\ns_add_u32 m0, %[l0], 20480\ns_add_u32 s60, s41, 36864\nbuffer_load_dwordx4 v0, s[36:39], s60 offen offset:0 sc1 lds\nbuffer_load_dwordx4 v0, s[36:39], s60 offen offset:1024 sc1 lds\nds_read_b128 v[80:83], v2 offset:0\n" \
"ds_read_b128 v[84:87], v2 offset:2048\nbuffer_load_dwordx4 v0, s[36:39], s60 offen offset:2048 sc1 lds\nbuffer_load_dwordx4 v0, s[36:39], s60 offen offset:3072 sc1 lds\ns_add_u32 m0, %[l0], 24576\ns_add_u32 s60, s41, 49152\nbuffer_load_dwordx4 v0, s[36:39], s60 offen offset:0 sc1 lds\nbuffer_load_dwordx4 v0, s[36:39], s60 offen offset:1024 sc1 lds\nbuffer_load_dwordx4 v0, s[36:39], s60 offen offset:2048 sc1 lds\n" \
"buffer_load_dwordx4 v0, s[36:39], s60 offen offset:3072 sc1 lds\ns_add_u32 m0, %[l0], 28672\ns_add_u32 s60, s41, 53248\nbuffer_load_dwordx4 v0, s[36:39], s60 offen offset:0 sc1 lds\nbuffer_load_dwordx4 v0, s[36:39], s60 offen offset:1024 sc1 lds\nbuffer_load_dwordx4 v0, s[36:39], s60 offen offset:2048 sc1 lds\nbuffer_load_dwordx4 v0, s[36:39], s60 offen offset:3072 sc1 lds\ns_waitcnt lgkmcnt(1)\n" \
"v_mfma_f32_16x16x32_bf16 v[16:19], a[0:3], v[80:83], 0\ns_waitcnt lgkmcnt(0)\nds_read_b128 v[88:91], v2 offset:256\nv_mfma_f32_16x16x32_bf16 v[16:19], a[4:7], v[84:87], v[16:19]\nds_read_b128 v[92:95], v2 offset:2304\nv_mfma_f32_16x16x32_bf16 v[20:23], a[8:11], v[80:83], 0\nv_mfma_f32_16x16x32_bf16 v[20:23], a[12:15], v[84:87], v[20:23]\nv_mfma_f32_16x16x32_bf16 v[24:27], a[16:19], v[80:83], 0\n" \
"ds_read_b128 v[72:75], v1 offset:0\nv_mfma_f32_16x16x32_bf16 v[24:27], a[20:23], v[84:87], v[24:27]\nds_read_b128 v[76:79], v1 offset:1024\nv_mfma_f32_16x16x32_bf16 v[28:31], a[24:27], v[80:83], 0\nv_mfma_f32_16x16x32_bf16 v[28:31], a[28:31], v[84:87], v[28:31]\ns_waitcnt lgkmcnt(3)\nv_mfma_f32_16x16x32_bf16 v[32:35], a[0:3], v[88:91], 0\ns_waitcnt lgkmcnt(2)\n" \
"ds_read_b128 v[80:83], v2 offset:4096\nv_mfma_f32_16x16x32_bf16 v[32:35], a[4:7], v[92:95], v[32:35]\nds_read_b128 v[84:87], v2 offset:6144\nv_mfma_f32_16x16x32_bf16 v[36:39], a[8:11], v[88:91], 0\nv_mfma_f32_16x16x32_bf16 v[36:39], a[12:15], v[92:95], v[36:39]\nv_mfma_f32_16x16x32_bf16 v[40:43], a[16:19], v[88:91], 0\nv_mfma_f32_16x16x32_bf16 v[40:43], a[20:23], v[92:95], v[40:43]\nv_mfma_f32_16x16x32_bf16 v[44:47], a[24:27], v[88:91], 0\n" \
"v_mfma_f32_16x16x32_bf16 v[44:47], a[28:31], v[92:95], v[44:47]\ns_waitcnt lgkmcnt(1)\nv_mfma_f32_16x16x32_bf16 v[16:19], a[32:35], v[80:83], v[16:19]\ns_waitcnt lgkmcnt(0)\nds_read_b128 v[88:91], v2 offset:4352\nv_mfma_f32_16x16x32_bf16 v[16:19], a[36:39], v[84:87], v[16:19]\nds_read_b128 v[92:95], v2 offset:6400\nv_mfma_f32_16x16x32_bf16 v[20:23], v[72:75], v[80:83], v[20:23]\n" \
"v_mfma_f32_16x16x32_bf16 v[20:23], v[76:79], v[84:87], v[20:23]\nv_mfma_f32_16x16x32_bf16 v[24:27], a[40:43], v[80:83], v[24:27]\nds_read_b128 v[64:67], v1 offset:2048\nv_mfma_f32_16x16x32_bf16 v[24:27], a[44:47], v[84:87], v[24:27]\nds_read_b128 v[68:71], v1 offset:3072\nv_mfma_f32_16x16x32_bf16 v[28:31], a[48:51], v[80:83], v[28:31]\nv_mfma_f32_16x16x32_bf16 v[28:31], a[52:55], v[84:87], v[28:31]\ns_waitcnt lgkmcnt(3)\n" \
"v_mfma_f32_16x16x32_bf16 v[32:35], a[32:35], v[88:91], v[32:35]\ns_waitcnt lgkmcnt(2)\nds_read_b128 v[80:83], v2 offset:8192\nv_mfma_f32_16x16x32_bf16 v[32:35], a[36:39], v[92:95], v[32:35]\nds_read_b128 v[84:87], v2 offset:10240\nv_mfma_f32_16x16x32_bf16 v[36:39], v[72:75], v[88:91], v[36:39]\nv_mfma_f32_16x16x32_bf16 v[36:39], v[76:79], v[92:95], v[36:39]\nv_mfma_f32_16x16x32_bf16 v[40:43], a[40:43], v[88:91], v[40:43]\n" \
"v_mfma_f32_16x16x32_bf16 v[40:43], a[44:47], v[92:95], v[40:43]\nv_mfma_f32_16x16x32_bf16 v[44:47], a[48:51], v[88:91], v[44:47]\nv_mfma_f32_16x16x32_bf16 v[44:47], a[52:55], v[92:95], v[44:47]\ns_waitcnt lgkmcnt(1)\nv_mfma_f32_16x16x32_bf16 v[16:19], a[56:59], v[80:83], v[16:19]\ns_waitcnt lgkmcnt(0)\nds_read_b128 v[88:91], v2 offset:8448\nv_mfma_f32_16x16x32_bf16 v[16:19], a[60:63], v[84:87], v[16:19]\n" \
"ds_read_b128 v[92:95], v2 offset:10496\nv_mfma_f32_16x16x32_bf16 v[20:23], a[64:67], v[80:83], v[20:23]\nv_mfma_f32_16x16x32_bf16 v[20:23], a[68:71], v[84:87], v[20:23]\nv_mfma_f32_16x16x32_bf16 v[24:27], v[64:67], v[80:83], v[24:27]\nds_read_b128 v[72:75], v1 offset:4096\nv_mfma_f32_16x16x32_bf16 v[24:27], v[68:71], v[84:87], v[24:27]\nds_read_b128 v[76:79], v1 offset:5120\nv_mfma_f32_16x16x32_bf16 v[28:31], a[72:75], v[80:83], v[28:31]\n" \
"v_mfma_f32_16x16x32_bf16 v[28:31], a[76:79], v[84:87], v[28:31]\ns_waitcnt lgkmcnt(3)\nv_mfma_f32_16x16x32_bf16 v[32:35], a[56:59], v[88:91], v[32:35]\ns_waitcnt lgkmcnt(2)\nds_read_b128 v[80:83], v2 offset:12288\nv_mfma_f32_16x16x32_bf16 v[32:35], a[60:63], v[92:95], v[32:35]\nds_read_b128 v[84:87], v2 offset:14336\nv_mfma_f32_16x16x32_bf16 v[36:39], a[64:67], v[88:91], v[36:39]\n" \
"v_mfma_f32_16x16x32_bf16 v[36:39], a[68:71], v[92:95], v[36:39]\nv_mfma_f32_16x16x32_bf16 v[40:43], v[64:67], v[88:91], v[40:43]\nv_mfma_f32_16x16x32_bf16 v[40:43], v[68:71], v[92:95], v[40:43]\nv_mfma_f32_16x16x32_bf16 v[44:47], a[72:75], v[88:91], v[44:47]\nv_mfma_f32_16x16x32_bf16 v[44:47], a[76:79], v[92:95], v[44:47]\ns_waitcnt vmcnt(0)\ns_branch .Lqk_pj%=\n.Lqk_slow%=:\n" \
"s_cmp_ge_i32 s40, 63\ns_cbranch_scc1 .Lqk_end%=\n.Lqk_fetch0%=:\nbuffer_load_dword v16, v12, s[36:39], s56 offen sc1\ns_waitcnt vmcnt(0)\nv_cmp_ge_i32 vcc, s40, v16\ns_cbranch_vccz .Lqk_fok%=\ns_add_u32 s43, s43, 1\n" \
"s_cmp_lt_u32 s43, 262144\ns_cbranch_scc1 .Lqk_fetch0%=\ns_branch .Lqk_abort%=\n.Lqk_fok%=:\ns_add_u32 s44, s40, 1\ns_lshl_b32 s44, s44, 17\ns_add_u32 s44, s44, %[xn]\ns_lshr_b32 s45, s44, 8\n" \
"s_and_b32 s45, s45, 0x2000\ns_xor_b32 s41, s44, s45\ns_add_u32 m0, %[l0], 0\ns_add_u32 s60, s41, 0\nbuffer_load_dwordx4 v0, s[36:39], s60 offen offset:0 sc1 lds\nbuffer_load_dwordx4 v0, s[36:39], s60 offen offset:1024 sc1 lds\nbuffer_load_dwordx4 v0, s[36:39], s60 offen offset:2048 sc1 lds\nbuffer_load_dwordx4 v0, s[36:39], s60 offen offset:3072 sc1 lds\n" \
"s_add_u32 m0, %[l0], 4096\ns_add_u32 s60, s41, 4096\nbuffer_load_dwordx4 v0, s[36:39], s60 offen offset:0 sc1 lds\nbuffer_load_dwordx4 v0, s[36:39], s60 offen offset:1024 sc1 lds\nbuffer_load_dwordx4 v0, s[36:39], s60 offen offset:2048 sc1 lds\nbuffer_load_dwordx4 v0, s[36:39], s60 offen offset:3072 sc1 lds\ns_add_u32 m0, %[l0], 8192\ns_add_u32 s60, s41, 16384\n" \
"buffer_load_dwordx4 v0, s[36:39], s60 offen offset:0 sc1 lds\nbuffer_load_dwordx4 v0, s[36:39], s60 offen offset:1024 sc1 lds\nbuffer_load_dwordx4 v0, s[36:39], s60 offen offset:2048 sc1 lds\nbuffer_load_dwordx4 v0, s[36:39], s60 offen offset:3072 sc1 lds\ns_add_u32 m0, %[l0], 12288\ns_add_u32 s60, s41, 20480\nbuffer_load_dwordx4 v0, s[36:39], s60 offen offset:0 sc1 lds\nbuffer_load_dwordx4 v0, s[36:39], s60 offen offset:1024 sc1 lds\n" \
"buffer_load_dwordx4 v0, s[36:39], s60 offen offset:2048 sc1 lds\nbuffer_load_dwordx4 v0, s[36:39], s60 offen offset:3072 sc1 lds\ns_add_u32 m0, %[l0], 16384\ns_add_u32 s60, s41, 32768\nbuffer_load_dwordx4 v0, s[36:39], s60 offen offset:0 sc1 lds\nbuffer_load_dwordx4 v0, s[36:39], s60 offen offset:1024 sc1 lds\nbuffer_load_dwordx4 v0, s[36:39], s60 offen offset:2048 sc1 lds\nbuffer_load_dwordx4 v0, s[36:39], s60 offen offset:3072 sc1 lds\n" \
"s_add_u32 m0, %[l0], 20480\ns_add_u32 s60, s41, 36864\nbuffer_load_dwordx4 v0, s[36:39], s60 offen offset:0 sc1 lds\nbuffer_load_dwordx4 v0, s[36:39], s60 offen offset:1024 sc1 lds\nbuffer_load_dwordx4 v0, s[36:39], s60 offen offset:2048 sc1 lds\nbuffer_load_dwordx4 v0, s[36:39], s60 offen offset:3072 sc1 lds\ns_add_u32 m0, %[l0], 24576\ns_add_u32 s60, s41, 49152\n" \
"buffer_load_dwordx4 v0, s[36:39], s60 offen offset:0 sc1 lds\nbuffer_load_dwordx4 v0, s[36:39], s60 offen offset:1024 sc1 lds\nbuffer_load_dwordx4 v0, s[36:39], s60 offen offset:2048 sc1 lds\nbuffer_load_dwordx4 v0, s[36:39], s60 offen offset:3072 sc1 lds\ns_add_u32 m0, %[l0], 28672\ns_add_u32 s60, s41, 53248\nbuffer_load_dwordx4 v0, s[36:39], s60 offen offset:0 sc1 lds\nbuffer_load_dwordx4 v0, s[36:39], s60 offen offset:1024 sc1 lds\n" \
"buffer_load_dwordx4 v0, s[36:39], s60 offen offset:2048 sc1 lds\nbuffer_load_dwordx4 v0, s[36:39], s60 offen offset:3072 sc1 lds\ns_waitcnt vmcnt(0)\ns_branch .Lqk_pwait%=\n.Lqk_re3%=:\nv_max_u32 v17, v80, v82\nv_cmp_eq_u32 vcc, -1, v17\ns_cbranch_vccz .Lqk_sk2865%=\n" \
"s_mov_b64 exec, vcc\nbuffer_load_dwordx4 v[80:83], v3, s[36:39], s42 offen offset:0 sc1\ns_mov_b64 exec, -1\n.Lqk_sk2865%=:\nv_max_u32 v17, v84, v86\nv_cmp_eq_u32 vcc, -1, v17\ns_cbranch_vccz .Lqk_sk2872%=\ns_mov_b64 exec, vcc\n" \
"buffer_load_dwordx4 v[84:87], v3, s[36:39], s42 offen offset:1024 sc1\ns_mov_b64 exec, -1\n.Lqk_sk2872%=:\nv_max_u32 v17, v88, v90\nv_cmp_eq_u32 vcc, -1, v17\ns_cbranch_vccz .Lqk_sk2879%=\ns_mov_b64 exec, vcc\nbuffer_load_dwordx4 v[88:91], v3, s[36:39], s42 offen offset:2048 sc1\n" \
"s_mov_b64 exec, -1\n.Lqk_sk2879%=:\nv_max_u32 v17, v92, v94\nv_cmp_eq_u32 vcc, -1, v17\ns_cbranch_vccz .Lqk_sk2886%=\ns_mov_b64 exec, vcc\nbuffer_load_dwordx4 v[92:95], v3, s[36:39], s42 offen offset:3072 sc1\ns_mov_b64 exec, -1\n" \
".Lqk_sk2886%=:\nbuffer_load_dword v16, v12, s[36:39], s56 offen sc1\nv_mov_b64 v[48:49], 0\nv_mov_b64 v[50:51], 0\nv_mov_b64 v[52:53], 0\nv_mov_b64 v[54:55], 0\nv_mov_b64 v[56:57], 0\nv_mov_b64 v[58:59], 0\n" \
"v_mov_b64 v[60:61], 0\nv_mov_b64 v[62:63], 0\ns_add_u32 s43, s43, 1\ns_cmp_lt_u32 s43, 262144\ns_cbranch_scc1 .Lqk_poll3%=\ns_branch .Lqk_abort%=\n.Lqk_abort%=:\ns_mov_b64 exec, -1\n" \
"v_mov_b32 v16, 0\ns_movk_i32 s44, 6400\nbuffer_store_dword v16, off, s[36:39], s44 sc1\n.Lqk_end%=:\ns_waitcnt vmcnt(0) lgkmcnt(0)\n" \
""
#define VU_ASM \
"v_mbcnt_lo_u32_b32 v255, -1, 0\nv_mbcnt_hi_u32_b32 v255, -1, v255\nv_lshlrev_b32 v255, 2, v255\nv_add_u32 v255, %[tb], v255\nds_read_b32 v0, v255 offset:0\nds_read_b32 v1, v255 offset:256\nds_read_b32 v2, v255 offset:512\nds_read_b32 v3, v255 offset:768\n" \
"ds_read_b32 v4, v255 offset:1024\nds_read_b32 v5, v255 offset:1280\nds_read_b32 v6, v255 offset:1536\nds_read_b32 v7, v255 offset:1792\nds_read_b32 v8, v255 offset:2048\nds_read_b32 v9, v255 offset:2304\nds_read_b32 v10, v255 offset:2560\nds_read_b32 v11, v255 offset:2816\n" \
"ds_read_b32 v90, v255 offset:3072\nds_read_b32 v91, v255 offset:3328\nds_read_b32 v92, v255 offset:3584\nds_read_b32 v93, v255 offset:3840\nds_read_b32 v94, v255 offset:4096\nds_read_b32 v95, v255 offset:4352\ns_waitcnt lgkmcnt(0)\ns_mov_b64 s[36:37], %[ws]\n" \
"s_mov_b32 s38, %[sz]\ns_mov_b32 s39, 0x20000\ns_mov_b32 s45, 2096896\nbuffer_load_dword v15, off, s[36:39], s45 sc1\ns_add_u32 m0, %[lw], 16384\ns_add_u32 s44, %[wo], 0\nbuffer_load_dwordx4 v90, %[rw], s44 offen nt lds\ns_add_u32 m0, %[lw], 17408\n" \
"s_add_u32 s44, %[wo], 65536\nbuffer_load_dwordx4 v90, %[rw], s44 offen nt lds\ns_add_u32 m0, %[lw], 18432\ns_add_u32 s44, %[wo], 131072\nbuffer_load_dwordx4 v90, %[rw], s44 offen nt lds\ns_add_u32 m0, %[lw], 19456\ns_add_u32 s44, %[wo], 196608\nbuffer_load_dwordx4 v90, %[rw], s44 offen nt lds\n" \
"s_add_u32 m0, %[lw], 20480\ns_add_u32 s44, %[wo], 524288\nbuffer_load_dwordx4 v90, %[rw], s44 offen nt lds\ns_add_u32 m0, %[lw], 21504\ns_add_u32 s44, %[wo], 589824\nbuffer_load_dwordx4 v90, %[rw], s44 offen nt lds\ns_add_u32 m0, %[lw], 22528\ns_add_u32 s44, %[wo], 655360\n" \
"buffer_load_dwordx4 v90, %[rw], s44 offen nt lds\ns_add_u32 m0, %[lw], 23552\ns_add_u32 s44, %[wo], 720896\nbuffer_load_dwordx4 v90, %[rw], s44 offen nt lds\ns_add_u32 m0, %[tb], 0\ns_add_u32 s44, %[wo], 1048576\nbuffer_load_dwordx4 v90, %[rw], s44 offen nt lds\ns_add_u32 m0, %[tb], 1024\n" \
"s_add_u32 s44, %[wo], 1114112\nbuffer_load_dwordx4 v90, %[rw], s44 offen nt lds\ns_add_u32 m0, %[tb], 2048\ns_add_u32 s44, %[wo], 1179648\nbuffer_load_dwordx4 v90, %[rw], s44 offen nt lds\ns_add_u32 m0, %[tb], 3072\ns_add_u32 s44, %[wo], 1245184\nbuffer_load_dwordx4 v90, %[rw], s44 offen nt lds\n" \
"s_add_u32 m0, %[tb], 4096\ns_add_u32 s44, %[wo], 1572864\nbuffer_load_dwordx4 v90, %[rw], s44 offen nt lds\ns_add_u32 m0, %[tb], 5120\ns_add_u32 s44, %[wo], 1638400\nbuffer_load_dwordx4 v90, %[rw], s44 offen nt lds\ns_add_u32 m0, %[tb], 6144\ns_add_u32 s44, %[wo], 1703936\n" \
"buffer_load_dwordx4 v90, %[rw], s44 offen nt lds\ns_add_u32 m0, %[tb], 7168\ns_add_u32 s44, %[wo], 1769472\nbuffer_load_dwordx4 v90, %[rw], s44 offen nt lds\ns_add_u32 m0, %[tb], 8192\ns_add_u32 s44, %[wo], 2097152\nbuffer_load_dwordx4 v90, %[rw], s44 offen nt lds\ns_add_u32 m0, %[tb], 9216\n" \
"s_add_u32 s44, %[wo], 2162688\nbuffer_load_dwordx4 v90, %[rw], s44 offen nt lds\ns_add_u32 m0, %[tb], 10240\ns_add_u32 s44, %[wo], 2228224\nbuffer_load_dwordx4 v90, %[rw], s44 offen nt lds\ns_add_u32 m0, %[tb], 11264\ns_add_u32 s44, %[wo], 2293760\nbuffer_load_dwordx4 v90, %[rw], s44 offen nt lds\n" \
"s_add_u32 m0, %[lw], 0\ns_add_u32 s44, %[wo], 2621440\nbuffer_load_dwordx4 v90, %[rw], s44 offen nt lds\ns_add_u32 m0, %[lw], 1024\ns_add_u32 s44, %[wo], 2686976\nbuffer_load_dwordx4 v90, %[rw], s44 offen nt lds\ns_add_u32 m0, %[lw], 2048\ns_add_u32 s44, %[wo], 2752512\n" \
"buffer_load_dwordx4 v90, %[rw], s44 offen nt lds\ns_add_u32 m0, %[lw], 3072\ns_add_u32 s44, %[wo], 2818048\nbuffer_load_dwordx4 v90, %[rw], s44 offen nt lds\ns_add_u32 m0, %[lw], 4096\ns_add_u32 s44, %[wo], 3145728\nbuffer_load_dwordx4 v90, %[rw], s44 offen nt lds\ns_add_u32 m0, %[lw], 5120\n" \
"s_add_u32 s44, %[wo], 3211264\nbuffer_load_dwordx4 v90, %[rw], s44 offen nt lds\ns_add_u32 m0, %[lw], 6144\ns_add_u32 s44, %[wo], 3276800\nbuffer_load_dwordx4 v90, %[rw], s44 offen nt lds\ns_add_u32 m0, %[lw], 7168\ns_add_u32 s44, %[wo], 3342336\nbuffer_load_dwordx4 v90, %[rw], s44 offen nt lds\n" \
"s_add_u32 m0, %[lw], 8192\ns_add_u32 s44, %[wo], 3670016\nbuffer_load_dwordx4 v90, %[rw], s44 offen nt lds\ns_add_u32 m0, %[lw], 9216\ns_add_u32 s44, %[wo], 3735552\nbuffer_load_dwordx4 v90, %[rw], s44 offen nt lds\ns_add_u32 m0, %[lw], 10240\ns_add_u32 s44, %[wo], 3801088\n" \
"buffer_load_dwordx4 v90, %[rw], s44 offen nt lds\ns_add_u32 m0, %[lw], 11264\ns_add_u32 s44, %[wo], 3866624\nbuffer_load_dwordx4 v90, %[rw], s44 offen nt lds\ns_waitcnt vmcnt(28)\ns_barrier\ns_add_u32 m0, %[lw], 12288\ns_add_u32 s44, %[wo], 4194304\n" \
"buffer_load_dwordx4 v90, %[rw], s44 offen nt lds\ns_add_u32 m0, %[lw], 13312\ns_add_u32 s44, %[wo], 4259840\nbuffer_load_dwordx4 v90, %[rw], s44 offen nt lds\ns_add_u32 m0, %[lw], 14336\ns_add_u32 s44, %[wo], 4325376\nbuffer_load_dwordx4 v90, %[rw], s44 offen nt lds\ns_add_u32 m0, %[lw], 15360\n" \
"s_add_u32 s44, %[wo], 4390912\nbuffer_load_dwordx4 v90, %[rw], s44 offen nt lds\nds_read_b64_tr_b16 a[0:1], v93 offset:16384\nds_read_b64_tr_b16 a[2:3], v93 offset:16896\nds_read_b64_tr_b16 a[16:17], v93 offset:16392\nds_read_b64_tr_b16 a[18:19], v93 offset:16904\nds_read_b64_tr_b16 a[4:5], v93 offset:40960\nds_read_b64_tr_b16 a[6:7], v93 offset:41472\n" \
"ds_read_b64_tr_b16 a[20:21], v93 offset:40968\nds_read_b64_tr_b16 a[22:23], v93 offset:41480\ns_waitcnt lgkmcnt(0)\ns_waitcnt vmcnt(28)\ns_barrier\ns_add_u32 m0, %[lw], 16384\ns_add_u32 s44, %[wo], 4718592\nbuffer_load_dwordx4 v90, %[rw], s44 offen nt lds\n" \
"s_add_u32 m0, %[lw], 17408\ns_add_u32 s44, %[wo], 4784128\nbuffer_load_dwordx4 v90, %[rw], s44 offen nt lds\ns_add_u32 m0, %[lw], 18432\ns_add_u32 s44, %[wo], 4849664\nbuffer_load_dwordx4 v90, %[rw], s44 offen nt lds\ns_add_u32 m0, %[lw], 19456\ns_add_u32 s44, %[wo], 4915200\n" \
"buffer_load_dwordx4 v90, %[rw], s44 offen nt lds\nds_read_b64_tr_b16 a[24:25], v93 offset:20480\nds_read_b64_tr_b16 a[26:27], v93 offset:20992\nds_read_b64_tr_b16 a[40:41], v93 offset:20488\nds_read_b64_tr_b16 a[42:43], v93 offset:21000\nds_read_b64_tr_b16 a[28:29], v93 offset:45056\nds_read_b64_tr_b16 a[30:31], v93 offset:45568\nds_read_b64_tr_b16 a[44:45], v93 offset:45064\n" \
"ds_read_b64_tr_b16 a[46:47], v93 offset:45576\ns_waitcnt lgkmcnt(0)\ns_waitcnt vmcnt(28)\ns_barrier\ns_add_u32 m0, %[lw], 20480\ns_add_u32 s44, %[wo], 5242880\nbuffer_load_dwordx4 v90, %[rw], s44 offen nt lds\ns_add_u32 m0, %[lw], 21504\n" \
"s_add_u32 s44, %[wo], 5308416\nbuffer_load_dwordx4 v90, %[rw], s44 offen nt lds\ns_add_u32 m0, %[lw], 22528\ns_add_u32 s44, %[wo], 5373952\nbuffer_load_dwordx4 v90, %[rw], s44 offen nt lds\ns_add_u32 m0, %[lw], 23552\ns_add_u32 s44, %[wo], 5439488\nbuffer_load_dwordx4 v90, %[rw], s44 offen nt lds\n" \
"ds_read_b64_tr_b16 v[84:85], v92 offset:0\nds_read_b64_tr_b16 v[86:87], v92 offset:512\nds_read_b64_tr_b16 a[64:65], v92 offset:8\nds_read_b64_tr_b16 a[66:67], v92 offset:520\nds_read_b64_tr_b16 v[80:81], v92 offset:12288\nds_read_b64_tr_b16 v[82:83], v92 offset:12800\nds_read_b64_tr_b16 a[68:69], v92 offset:12296\nds_read_b64_tr_b16 a[70:71], v92 offset:12808\n" \
"s_waitcnt lgkmcnt(0)\ns_waitcnt vmcnt(28)\ns_barrier\ns_add_u32 m0, %[tb], 0\ns_add_u32 s44, %[wo], 5767168\nbuffer_load_dwordx4 v90, %[rw], s44 offen nt lds\ns_add_u32 m0, %[tb], 1024\ns_add_u32 s44, %[wo], 5832704\n" \
"buffer_load_dwordx4 v90, %[rw], s44 offen nt lds\ns_add_u32 m0, %[tb], 2048\ns_add_u32 s44, %[wo], 5898240\nbuffer_load_dwordx4 v90, %[rw], s44 offen nt lds\ns_add_u32 m0, %[tb], 3072\ns_add_u32 s44, %[wo], 5963776\nbuffer_load_dwordx4 v90, %[rw], s44 offen nt lds\nds_read_b64_tr_b16 a[80:81], v92 offset:4096\n" \
"ds_read_b64_tr_b16 a[82:83], v92 offset:4608\nds_read_b64_tr_b16 a[88:89], v92 offset:4104\nds_read_b64_tr_b16 a[90:91], v92 offset:4616\nds_read_b64_tr_b16 a[84:85], v92 offset:16384\nds_read_b64_tr_b16 a[86:87], v92 offset:16896\nds_read_b64_tr_b16 a[92:93], v92 offset:16392\nds_read_b64_tr_b16 a[94:95], v92 offset:16904\ns_waitcnt lgkmcnt(0)\n" \
"s_waitcnt vmcnt(28)\ns_barrier\ns_add_u32 m0, %[tb], 4096\ns_add_u32 s44, %[wo], 6291456\nbuffer_load_dwordx4 v90, %[rw], s44 offen nt lds\ns_add_u32 m0, %[tb], 5120\ns_add_u32 s44, %[wo], 6356992\nbuffer_load_dwordx4 v90, %[rw], s44 offen nt lds\n" \
"s_add_u32 m0, %[tb], 6144\ns_add_u32 s44, %[wo], 6422528\nbuffer_load_dwordx4 v90, %[rw], s44 offen nt lds\ns_add_u32 m0, %[tb], 7168\ns_add_u32 s44, %[wo], 6488064\nbuffer_load_dwordx4 v90, %[rw], s44 offen nt lds\nds_read_b64_tr_b16 a[104:105], v92 offset:8192\nds_read_b64_tr_b16 a[106:107], v92 offset:8704\n" \
"ds_read_b64_tr_b16 a[120:121], v92 offset:8200\nds_read_b64_tr_b16 a[122:123], v92 offset:8712\nds_read_b64_tr_b16 a[108:109], v92 offset:20480\nds_read_b64_tr_b16 a[110:111], v92 offset:20992\nds_read_b64_tr_b16 a[124:125], v92 offset:20488\nds_read_b64_tr_b16 a[126:127], v92 offset:21000\ns_waitcnt lgkmcnt(0)\ns_waitcnt vmcnt(28)\n" \
"s_barrier\ns_add_u32 m0, %[tb], 8192\ns_add_u32 s44, %[wo], 6815744\nbuffer_load_dwordx4 v90, %[rw], s44 offen nt lds\ns_add_u32 m0, %[tb], 9216\ns_add_u32 s44, %[wo], 6881280\nbuffer_load_dwordx4 v90, %[rw], s44 offen nt lds\ns_add_u32 m0, %[tb], 10240\n" \
"s_add_u32 s44, %[wo], 6946816\nbuffer_load_dwordx4 v90, %[rw], s44 offen nt lds\ns_add_u32 m0, %[tb], 11264\ns_add_u32 s44, %[wo], 7012352\nbuffer_load_dwordx4 v90, %[rw], s44 offen nt lds\nds_read_b64_tr_b16 a[128:129], v93 offset:0\nds_read_b64_tr_b16 a[130:131], v93 offset:512\nds_read_b64_tr_b16 v[76:77], v93 offset:8\n" \
"ds_read_b64_tr_b16 v[78:79], v93 offset:520\nds_read_b64_tr_b16 a[132:133], v93 offset:24576\nds_read_b64_tr_b16 a[134:135], v93 offset:25088\nds_read_b64_tr_b16 v[72:73], v93 offset:24584\nds_read_b64_tr_b16 v[74:75], v93 offset:25096\ns_waitcnt lgkmcnt(0)\ns_waitcnt vmcnt(28)\ns_barrier\n" \
"s_add_u32 m0, %[lw], 0\ns_add_u32 s44, %[wo], 7340032\nbuffer_load_dwordx4 v90, %[rw], s44 offen nt lds\ns_add_u32 m0, %[lw], 1024\ns_add_u32 s44, %[wo], 7405568\nbuffer_load_dwordx4 v90, %[rw], s44 offen nt lds\ns_add_u32 m0, %[lw], 2048\ns_add_u32 s44, %[wo], 7471104\n" \
"buffer_load_dwordx4 v90, %[rw], s44 offen nt lds\ns_add_u32 m0, %[lw], 3072\ns_add_u32 s44, %[wo], 7536640\nbuffer_load_dwordx4 v90, %[rw], s44 offen nt lds\nds_read_b64_tr_b16 a[152:153], v93 offset:4096\nds_read_b64_tr_b16 a[154:155], v93 offset:4608\nds_read_b64_tr_b16 a[168:169], v93 offset:4104\nds_read_b64_tr_b16 a[170:171], v93 offset:4616\n" \
"ds_read_b64_tr_b16 a[156:157], v93 offset:28672\nds_read_b64_tr_b16 a[158:159], v93 offset:29184\nds_read_b64_tr_b16 a[172:173], v93 offset:28680\nds_read_b64_tr_b16 a[174:175], v93 offset:29192\ns_waitcnt lgkmcnt(0)\ns_waitcnt vmcnt(28)\ns_barrier\ns_add_u32 m0, %[lw], 4096\n" \
"s_add_u32 s44, %[wo], 7864320\nbuffer_load_dwordx4 v90, %[rw], s44 offen nt lds\ns_add_u32 m0, %[lw], 5120\ns_add_u32 s44, %[wo], 7929856\nbuffer_load_dwordx4 v90, %[rw], s44 offen nt lds\ns_add_u32 m0, %[lw], 6144\ns_add_u32 s44, %[wo], 7995392\nbuffer_load_dwordx4 v90, %[rw], s44 offen nt lds\n" \
"s_add_u32 m0, %[lw], 7168\ns_add_u32 s44, %[wo], 8060928\nbuffer_load_dwordx4 v90, %[rw], s44 offen nt lds\nds_read_b64_tr_b16 a[184:185], v93 offset:8192\nds_read_b64_tr_b16 a[186:187], v93 offset:8704\nds_read_b64_tr_b16 a[192:193], v93 offset:8200\nds_read_b64_tr_b16 a[194:195], v93 offset:8712\nds_read_b64_tr_b16 a[188:189], v93 offset:32768\n" \
"ds_read_b64_tr_b16 a[190:191], v93 offset:33280\nds_read_b64_tr_b16 a[196:197], v93 offset:32776\nds_read_b64_tr_b16 a[198:199], v93 offset:33288\ns_waitcnt lgkmcnt(0)\ns_waitcnt vmcnt(28)\ns_barrier\ns_add_u32 m0, %[lw], 8192\ns_add_u32 s44, %[w2], 0\n" \
"buffer_load_dwordx4 v91, %[ro], s44 offen nt lds\ns_add_u32 m0, %[lw], 9216\ns_add_u32 s44, %[w2], 32768\nbuffer_load_dwordx4 v91, %[ro], s44 offen nt lds\ns_add_u32 m0, %[lw], 10240\ns_add_u32 s44, %[w2], 65536\nbuffer_load_dwordx4 v91, %[ro], s44 offen nt lds\ns_add_u32 m0, %[lw], 11264\n" \
"s_add_u32 s44, %[w2], 98304\nbuffer_load_dwordx4 v91, %[ro], s44 offen nt lds\nds_read_b64_tr_b16 v[68:69], v93 offset:12288\nds_read_b64_tr_b16 v[70:71], v93 offset:12800\nds_read_b64_tr_b16 a[216:217], v93 offset:12296\nds_read_b64_tr_b16 a[218:219], v93 offset:12808\nds_read_b64_tr_b16 v[64:65], v93 offset:36864\nds_read_b64_tr_b16 v[66:67], v93 offset:37376\n" \
"ds_read_b64_tr_b16 a[220:221], v93 offset:36872\nds_read_b64_tr_b16 a[222:223], v93 offset:37384\ns_waitcnt lgkmcnt(0)\ns_waitcnt vmcnt(28)\ns_barrier\ns_add_u32 m0, %[lw], 12288\ns_add_u32 s44, %[w2], 128\nbuffer_load_dwordx4 v91, %[ro], s44 offen nt lds\n" \
"s_add_u32 m0, %[lw], 13312\ns_add_u32 s44, %[w2], 32896\nbuffer_load_dwordx4 v91, %[ro], s44 offen nt lds\ns_add_u32 m0, %[lw], 14336\ns_add_u32 s44, %[w2], 65664\nbuffer_load_dwordx4 v91, %[ro], s44 offen nt lds\ns_add_u32 m0, %[lw], 15360\ns_add_u32 s44, %[w2], 98432\n" \
"buffer_load_dwordx4 v91, %[ro], s44 offen nt lds\nds_read_b64_tr_b16 a[224:225], v93 offset:16384\nds_read_b64_tr_b16 a[226:227], v93 offset:16896\nds_read_b64_tr_b16 a[240:241], v93 offset:16392\nds_read_b64_tr_b16 a[242:243], v93 offset:16904\nds_read_b64_tr_b16 a[228:229], v93 offset:40960\nds_read_b64_tr_b16 a[230:231], v93 offset:41472\nds_read_b64_tr_b16 a[244:245], v93 offset:40968\n" \
"ds_read_b64_tr_b16 a[246:247], v93 offset:41480\ns_waitcnt lgkmcnt(0)\ns_waitcnt vmcnt(28)\ns_barrier\ns_add_u32 m0, %[lw], 16384\ns_add_u32 s44, %[w2], 256\nbuffer_load_dwordx4 v91, %[ro], s44 offen nt lds\ns_add_u32 m0, %[lw], 17408\n" \
"s_add_u32 s44, %[w2], 33024\nbuffer_load_dwordx4 v91, %[ro], s44 offen nt lds\ns_add_u32 m0, %[lw], 18432\ns_add_u32 s44, %[w2], 65792\nbuffer_load_dwordx4 v91, %[ro], s44 offen nt lds\ns_add_u32 m0, %[lw], 19456\ns_add_u32 s44, %[w2], 98560\nbuffer_load_dwordx4 v91, %[ro], s44 offen nt lds\n" \
"ds_read_b64_tr_b16 v[96:97], v93 offset:20480\nds_read_b64_tr_b16 v[98:99], v93 offset:20992\nds_read_b64_tr_b16 v[112:113], v93 offset:20488\nds_read_b64_tr_b16 v[114:115], v93 offset:21000\nds_read_b64_tr_b16 v[100:101], v93 offset:45056\nds_read_b64_tr_b16 v[102:103], v93 offset:45568\nds_read_b64_tr_b16 v[116:117], v93 offset:45064\nds_read_b64_tr_b16 v[118:119], v93 offset:45576\n" \
"s_waitcnt lgkmcnt(0)\ns_waitcnt vmcnt(28)\ns_barrier\ns_add_u32 m0, %[lw], 20480\ns_add_u32 s44, %[w2], 384\nbuffer_load_dwordx4 v91, %[ro], s44 offen nt lds\ns_add_u32 m0, %[lw], 21504\ns_add_u32 s44, %[w2], 33152\n" \
"buffer_load_dwordx4 v91, %[ro], s44 offen nt lds\ns_add_u32 m0, %[lw], 22528\ns_add_u32 s44, %[w2], 65920\nbuffer_load_dwordx4 v91, %[ro], s44 offen nt lds\ns_add_u32 m0, %[lw], 23552\ns_add_u32 s44, %[w2], 98688\nbuffer_load_dwordx4 v91, %[ro], s44 offen nt lds\nds_read_b64_tr_b16 v[128:129], v92 offset:0\n" \
"ds_read_b64_tr_b16 v[130:131], v92 offset:512\nds_read_b64_tr_b16 v[60:61], v92 offset:8\nds_read_b64_tr_b16 v[62:63], v92 offset:520\nds_read_b64_tr_b16 v[132:133], v92 offset:12288\nds_read_b64_tr_b16 v[134:135], v92 offset:12800\nds_read_b64_tr_b16 v[56:57], v92 offset:12296\nds_read_b64_tr_b16 v[58:59], v92 offset:12808\ns_waitcnt lgkmcnt(0)\n" \
"s_waitcnt vmcnt(28)\ns_barrier\ns_add_u32 m0, %[tb], 0\ns_add_u32 s44, %[w2], 139264\nbuffer_load_dwordx4 v91, %[ro], s44 offen nt lds\ns_add_u32 m0, %[tb], 1024\ns_add_u32 s44, %[w2], 172032\nbuffer_load_dwordx4 v91, %[ro], s44 offen nt lds\n" \
"s_add_u32 m0, %[tb], 2048\ns_add_u32 s44, %[w2], 204800\nbuffer_load_dwordx4 v91, %[ro], s44 offen nt lds\ns_add_u32 m0, %[tb], 3072\ns_add_u32 s44, %[w2], 237568\nbuffer_load_dwordx4 v91, %[ro], s44 offen nt lds\nds_read_b64_tr_b16 v[144:145], v92 offset:4096\nds_read_b64_tr_b16 v[146:147], v92 offset:4608\n" \
"ds_read_b64_tr_b16 v[160:161], v92 offset:4104\nds_read_b64_tr_b16 v[162:163], v92 offset:4616\nds_read_b64_tr_b16 v[148:149], v92 offset:16384\nds_read_b64_tr_b16 v[150:151], v92 offset:16896\nds_read_b64_tr_b16 v[164:165], v92 offset:16392\nds_read_b64_tr_b16 v[166:167], v92 offset:16904\ns_waitcnt lgkmcnt(0)\ns_waitcnt vmcnt(28)\n" \
"s_barrier\ns_add_u32 m0, %[tb], 4096\ns_add_u32 s44, %[w2], 139392\nbuffer_load_dwordx4 v91, %[ro], s44 offen nt lds\ns_add_u32 m0, %[tb], 5120\ns_add_u32 s44, %[w2], 172160\nbuffer_load_dwordx4 v91, %[ro], s44 offen nt lds\ns_add_u32 m0, %[tb], 6144\n" \
"s_add_u32 s44, %[w2], 204928\nbuffer_load_dwordx4 v91, %[ro], s44 offen nt lds\ns_add_u32 m0, %[tb], 7168\ns_add_u32 s44, %[w2], 237696\nbuffer_load_dwordx4 v91, %[ro], s44 offen nt lds\nds_read_b64_tr_b16 v[168:169], v92 offset:8192\nds_read_b64_tr_b16 v[170:171], v92 offset:8704\nds_read_b64_tr_b16 v[184:185], v92 offset:8200\n" \
"ds_read_b64_tr_b16 v[186:187], v92 offset:8712\nds_read_b64_tr_b16 v[172:173], v92 offset:20480\nds_read_b64_tr_b16 v[174:175], v92 offset:20992\nds_read_b64_tr_b16 v[188:189], v92 offset:20488\nds_read_b64_tr_b16 v[190:191], v92 offset:21000\ns_waitcnt lgkmcnt(0)\ns_waitcnt vmcnt(28)\ns_barrier\n" \
"s_add_u32 m0, %[tb], 8192\ns_add_u32 s44, %[w2], 139520\nbuffer_load_dwordx4 v91, %[ro], s44 offen nt lds\ns_add_u32 m0, %[tb], 9216\ns_add_u32 s44, %[w2], 172288\nbuffer_load_dwordx4 v91, %[ro], s44 offen nt lds\ns_add_u32 m0, %[tb], 10240\ns_add_u32 s44, %[w2], 205056\n" \
"buffer_load_dwordx4 v91, %[ro], s44 offen nt lds\ns_add_u32 m0, %[tb], 11264\ns_add_u32 s44, %[w2], 237824\nbuffer_load_dwordx4 v91, %[ro], s44 offen nt lds\nds_read_b64_tr_b16 v[200:201], v93 offset:0\nds_read_b64_tr_b16 v[202:203], v93 offset:512\nds_read_b64_tr_b16 v[216:217], v93 offset:8\nds_read_b64_tr_b16 v[218:219], v93 offset:520\n" \
"ds_read_b64_tr_b16 v[204:205], v93 offset:24576\nds_read_b64_tr_b16 v[206:207], v93 offset:25088\nds_read_b64_tr_b16 v[220:221], v93 offset:24584\nds_read_b64_tr_b16 v[222:223], v93 offset:25096\ns_waitcnt lgkmcnt(0)\ns_waitcnt vmcnt(28)\ns_barrier\ns_add_u32 m0, %[lw], 0\n" \
"s_add_u32 s44, %[w2], 139648\nbuffer_load_dwordx4 v91, %[ro], s44 offen nt lds\ns_add_u32 m0, %[lw], 1024\ns_add_u32 s44, %[w2], 172416\nbuffer_load_dwordx4 v91, %[ro], s44 offen nt lds\ns_add_u32 m0, %[lw], 2048\ns_add_u32 s44, %[w2], 205184\nbuffer_load_dwordx4 v91, %[ro], s44 offen nt lds\n" \
"s_add_u32 m0, %[lw], 3072\ns_add_u32 s44, %[w2], 237952\nbuffer_load_dwordx4 v91, %[ro], s44 offen nt lds\nds_read_b64_tr_b16 v[232:233], v93 offset:4096\nds_read_b64_tr_b16 v[234:235], v93 offset:4608\nds_read_b64_tr_b16 v[240:241], v93 offset:4104\nds_read_b64_tr_b16 v[242:243], v93 offset:4616\nds_read_b64_tr_b16 v[236:237], v93 offset:28672\n" \
"ds_read_b64_tr_b16 v[238:239], v93 offset:29184\nds_read_b64_tr_b16 v[244:245], v93 offset:28680\nds_read_b64_tr_b16 v[246:247], v93 offset:29192\ns_waitcnt lgkmcnt(0)\ns_waitcnt vmcnt(28)\ns_barrier\ns_add_u32 m0, %[lw], 4096\ns_add_u32 s44, %[w2], 131072\n" \
"buffer_load_dwordx4 v91, %[ro], s44 offen nt lds\ns_add_u32 m0, %[lw], 5120\ns_add_u32 s44, %[w2], 163840\nbuffer_load_dwordx4 v91, %[ro], s44 offen nt lds\ns_add_u32 m0, %[lw], 6144\ns_add_u32 s44, %[w2], 196608\nbuffer_load_dwordx4 v91, %[ro], s44 offen nt lds\ns_add_u32 m0, %[lw], 7168\n" \
"s_add_u32 s44, %[w2], 229376\nbuffer_load_dwordx4 v91, %[ro], s44 offen nt lds\nds_read_b64_tr_b16 a[8:9], v95 offset:8192\nds_read_b64_tr_b16 a[10:11], v95 offset:8448\nds_read_b64_tr_b16 a[12:13], v95 offset:10240\nds_read_b64_tr_b16 a[14:15], v95 offset:10496\nds_read_b64_tr_b16 a[208:209], v95 offset:8256\nds_read_b64_tr_b16 a[210:211], v95 offset:8512\n" \
"ds_read_b64_tr_b16 a[212:213], v95 offset:10304\nds_read_b64_tr_b16 a[214:215], v95 offset:10560\ns_waitcnt lgkmcnt(0)\ns_waitcnt vmcnt(28)\ns_barrier\ns_add_u32 m0, %[lw], 8192\ns_add_u32 s44, %[w2], 131200\nbuffer_load_dwordx4 v91, %[ro], s44 offen nt lds\n" \
"s_add_u32 m0, %[lw], 9216\ns_add_u32 s44, %[w2], 163968\nbuffer_load_dwordx4 v91, %[ro], s44 offen nt lds\ns_add_u32 m0, %[lw], 10240\ns_add_u32 s44, %[w2], 196736\nbuffer_load_dwordx4 v91, %[ro], s44 offen nt lds\ns_add_u32 m0, %[lw], 11264\ns_add_u32 s44, %[w2], 229504\n" \
"buffer_load_dwordx4 v91, %[ro], s44 offen nt lds\nds_read_b64_tr_b16 a[56:57], v95 offset:12288\nds_read_b64_tr_b16 a[58:59], v95 offset:12544\nds_read_b64_tr_b16 a[60:61], v95 offset:14336\nds_read_b64_tr_b16 a[62:63], v95 offset:14592\nds_read_b64_tr_b16 v[104:105], v95 offset:12352\nds_read_b64_tr_b16 v[106:107], v95 offset:12608\nds_read_b64_tr_b16 v[108:109], v95 offset:14400\n" \
"ds_read_b64_tr_b16 v[110:111], v95 offset:14656\ns_waitcnt lgkmcnt(0)\nbuffer_load_dwordx2 v[12:13], off, s[36:39], s45 sc1\ns_waitcnt vmcnt(29)\ns_barrier\ns_add_u32 m0, %[lw], 12288\ns_add_u32 s44, %[w2], 131328\nbuffer_load_dwordx4 v91, %[ro], s44 offen nt lds\n" \
"s_add_u32 m0, %[lw], 13312\ns_add_u32 s44, %[w2], 164096\nbuffer_load_dwordx4 v91, %[ro], s44 offen nt lds\ns_add_u32 m0, %[lw], 14336\ns_add_u32 s44, %[w2], 196864\nbuffer_load_dwordx4 v91, %[ro], s44 offen nt lds\ns_add_u32 m0, %[lw], 15360\ns_add_u32 s44, %[w2], 229632\n" \
"buffer_load_dwordx4 v91, %[ro], s44 offen nt lds\nds_read_b64_tr_b16 a[112:113], v95 offset:16384\nds_read_b64_tr_b16 a[114:115], v95 offset:16640\nds_read_b64_tr_b16 a[116:117], v95 offset:18432\nds_read_b64_tr_b16 a[118:119], v95 offset:18688\nds_read_b64_tr_b16 v[152:153], v95 offset:16448\nds_read_b64_tr_b16 v[154:155], v95 offset:16704\nds_read_b64_tr_b16 v[156:157], v95 offset:18496\n" \
"ds_read_b64_tr_b16 v[158:159], v95 offset:18752\ns_waitcnt lgkmcnt(0)\ns_waitcnt vmcnt(29)\ns_barrier\ns_add_u32 m0, %[lw], 16384\ns_add_u32 s44, %[w2], 131456\nbuffer_load_dwordx4 v91, %[ro], s44 offen nt lds\ns_add_u32 m0, %[lw], 17408\n" \
"s_add_u32 s44, %[w2], 164224\nbuffer_load_dwordx4 v91, %[ro], s44 offen nt lds\ns_add_u32 m0, %[lw], 18432\ns_add_u32 s44, %[w2], 196992\nbuffer_load_dwordx4 v91, %[ro], s44 offen nt lds\ns_add_u32 m0, %[lw], 19456\ns_add_u32 s44, %[w2], 229760\nbuffer_load_dwordx4 v91, %[ro], s44 offen nt lds\n" \
"ds_read_b64_tr_b16 a[160:161], v95 offset:20480\nds_read_b64_tr_b16 a[162:163], v95 offset:20736\nds_read_b64_tr_b16 a[164:165], v95 offset:22528\nds_read_b64_tr_b16 a[166:167], v95 offset:22784\nds_read_b64_tr_b16 v[208:209], v95 offset:20544\nds_read_b64_tr_b16 v[210:211], v95 offset:20800\nds_read_b64_tr_b16 v[212:213], v95 offset:22592\nds_read_b64_tr_b16 v[214:215], v95 offset:22848\n" \
"s_waitcnt lgkmcnt(0)\ns_waitcnt vmcnt(29)\ns_barrier\ns_add_u32 m0, %[lw], 20480\ns_add_u32 s44, %[w2], 8192\nbuffer_load_dwordx4 v91, %[ro], s44 offen nt lds\ns_add_u32 m0, %[lw], 21504\ns_add_u32 s44, %[w2], 40960\n" \
"buffer_load_dwordx4 v91, %[ro], s44 offen nt lds\ns_add_u32 m0, %[lw], 22528\ns_add_u32 s44, %[w2], 73728\nbuffer_load_dwordx4 v91, %[ro], s44 offen nt lds\ns_add_u32 m0, %[lw], 23552\ns_add_u32 s44, %[w2], 106496\nbuffer_load_dwordx4 v91, %[ro], s44 offen nt lds\nds_read_b64_tr_b16 a[48:49], v94 offset:0\n" \
"ds_read_b64_tr_b16 a[50:51], v94 offset:256\nds_read_b64_tr_b16 a[52:53], v94 offset:2048\nds_read_b64_tr_b16 a[54:55], v94 offset:2304\nds_read_b64_tr_b16 a[248:249], v94 offset:64\nds_read_b64_tr_b16 a[250:251], v94 offset:320\nds_read_b64_tr_b16 a[252:253], v94 offset:2112\nds_read_b64_tr_b16 a[254:255], v94 offset:2368\ns_waitcnt lgkmcnt(0)\n" \
"s_waitcnt vmcnt(29)\ns_barrier\ns_add_u32 m0, %[tb], 0\ns_add_u32 s44, %[w2], 8320\nbuffer_load_dwordx4 v91, %[ro], s44 offen nt lds\ns_add_u32 m0, %[tb], 1024\ns_add_u32 s44, %[w2], 41088\nbuffer_load_dwordx4 v91, %[ro], s44 offen nt lds\n" \
"s_add_u32 m0, %[tb], 2048\ns_add_u32 s44, %[w2], 73856\nbuffer_load_dwordx4 v91, %[ro], s44 offen nt lds\ns_add_u32 m0, %[tb], 3072\ns_add_u32 s44, %[w2], 106624\nbuffer_load_dwordx4 v91, %[ro], s44 offen nt lds\nds_read_b64_tr_b16 a[96:97], v94 offset:4096\nds_read_b64_tr_b16 a[98:99], v94 offset:4352\n" \
"ds_read_b64_tr_b16 a[100:101], v94 offset:6144\nds_read_b64_tr_b16 a[102:103], v94 offset:6400\nds_read_b64_tr_b16 v[136:137], v94 offset:4160\nds_read_b64_tr_b16 v[138:139], v94 offset:4416\nds_read_b64_tr_b16 v[140:141], v94 offset:6208\nds_read_b64_tr_b16 v[142:143], v94 offset:6464\ns_waitcnt lgkmcnt(0)\ns_waitcnt vmcnt(29)\n" \
"s_barrier\ns_add_u32 m0, %[tb], 4096\ns_add_u32 s44, %[w2], 8448\nbuffer_load_dwordx4 v91, %[ro], s44 offen nt lds\ns_add_u32 m0, %[tb], 5120\ns_add_u32 s44, %[w2], 41216\nbuffer_load_dwordx4 v91, %[ro], s44 offen nt lds\ns_add_u32 m0, %[tb], 6144\n" \
"s_add_u32 s44, %[w2], 73984\nbuffer_load_dwordx4 v91, %[ro], s44 offen nt lds\ns_add_u32 m0, %[tb], 7168\ns_add_u32 s44, %[w2], 106752\nbuffer_load_dwordx4 v91, %[ro], s44 offen nt lds\nds_read_b64_tr_b16 a[144:145], v94 offset:8192\nds_read_b64_tr_b16 a[146:147], v94 offset:8448\nds_read_b64_tr_b16 a[148:149], v94 offset:10240\n" \
"ds_read_b64_tr_b16 a[150:151], v94 offset:10496\nds_read_b64_tr_b16 v[192:193], v94 offset:8256\nds_read_b64_tr_b16 v[194:195], v94 offset:8512\nds_read_b64_tr_b16 v[196:197], v94 offset:10304\nds_read_b64_tr_b16 v[198:199], v94 offset:10560\ns_waitcnt lgkmcnt(0)\ns_waitcnt vmcnt(29)\ns_barrier\n" \
"s_add_u32 m0, %[tb], 8192\ns_add_u32 s44, %[w2], 8576\nbuffer_load_dwordx4 v91, %[ro], s44 offen nt lds\ns_add_u32 m0, %[tb], 9216\ns_add_u32 s44, %[w2], 41344\nbuffer_load_dwordx4 v91, %[ro], s44 offen nt lds\ns_add_u32 m0, %[tb], 10240\ns_add_u32 s44, %[w2], 74112\n" \
"buffer_load_dwordx4 v91, %[ro], s44 offen nt lds\ns_add_u32 m0, %[tb], 11264\ns_add_u32 s44, %[w2], 106880\nbuffer_load_dwordx4 v91, %[ro], s44 offen nt lds\nds_read_b64_tr_b16 a[200:201], v95 offset:0\nds_read_b64_tr_b16 a[202:203], v95 offset:256\nds_read_b64_tr_b16 a[204:205], v95 offset:2048\nds_read_b64_tr_b16 a[206:207], v95 offset:2304\n" \
"ds_read_b64_tr_b16 v[248:249], v95 offset:64\nds_read_b64_tr_b16 v[250:251], v95 offset:320\nds_read_b64_tr_b16 v[252:253], v95 offset:2112\nds_read_b64_tr_b16 v[254:255], v95 offset:2368\ns_waitcnt lgkmcnt(0)\ns_waitcnt vmcnt(29)\ns_barrier\nds_read_b64_tr_b16 v[52:53], v95 offset:4096\n" \
"ds_read_b64_tr_b16 v[54:55], v95 offset:4352\nds_read_b64_tr_b16 v[48:49], v95 offset:6144\nds_read_b64_tr_b16 v[50:51], v95 offset:6400\nds_read_b64_tr_b16 v[44:45], v95 offset:4160\nds_read_b64_tr_b16 v[46:47], v95 offset:4416\nds_read_b64_tr_b16 v[40:41], v95 offset:6208\nds_read_b64_tr_b16 v[42:43], v95 offset:6464\ns_waitcnt lgkmcnt(0)\n" \
"ds_write_b128 v1, v[84:87] offset:0\nds_write_b128 v1, v[80:83] offset:1024\nds_write_b128 v1, v[76:79] offset:2048\nds_write_b128 v1, v[72:75] offset:3072\ns_waitcnt vmcnt(25)\ns_barrier\nds_read_b64_tr_b16 a[72:73], v95 offset:8192\nds_read_b64_tr_b16 a[74:75], v95 offset:8448\n" \
"ds_read_b64_tr_b16 a[76:77], v95 offset:10240\nds_read_b64_tr_b16 a[78:79], v95 offset:10496\nds_read_b64_tr_b16 v[120:121], v95 offset:8256\nds_read_b64_tr_b16 v[122:123], v95 offset:8512\nds_read_b64_tr_b16 v[124:125], v95 offset:10304\nds_read_b64_tr_b16 v[126:127], v95 offset:10560\ns_waitcnt lgkmcnt(0)\nds_write_b128 v1, v[68:71] offset:4096\n" \
"ds_write_b128 v1, v[64:67] offset:5120\nds_write_b128 v1, v[60:63] offset:6144\nds_write_b128 v1, v[56:59] offset:7168\ns_waitcnt vmcnt(20)\ns_barrier\nds_read_b64_tr_b16 v[56:57], v95 offset:12288\nds_read_b64_tr_b16 v[58:59], v95 offset:12544\nds_read_b64_tr_b16 v[60:61], v95 offset:14336\n" \
"ds_read_b64_tr_b16 v[62:63], v95 offset:14592\nds_read_b64_tr_b16 v[64:65], v95 offset:12352\nds_read_b64_tr_b16 v[66:67], v95 offset:12608\nds_read_b64_tr_b16 v[68:69], v95 offset:14400\nds_read_b64_tr_b16 v[70:71], v95 offset:14656\ns_waitcnt lgkmcnt(0)\nds_write_b128 v1, v[52:55] offset:8192\nds_write_b128 v1, v[48:51] offset:9216\n" \
"ds_write_b128 v1, v[44:47] offset:10240\nds_write_b128 v1, v[40:43] offset:11264\ns_waitcnt vmcnt(16)\ns_barrier\nds_read_b64_tr_b16 a[176:177], v95 offset:16384\nds_read_b64_tr_b16 a[178:179], v95 offset:16640\nds_read_b64_tr_b16 a[180:181], v95 offset:18432\nds_read_b64_tr_b16 a[182:183], v95 offset:18688\n" \
"ds_read_b64_tr_b16 v[224:225], v95 offset:16448\nds_read_b64_tr_b16 v[226:227], v95 offset:16704\nds_read_b64_tr_b16 v[228:229], v95 offset:18496\nds_read_b64_tr_b16 v[230:231], v95 offset:18752\ns_waitcnt lgkmcnt(0)\ns_mov_b32 s43, 0\ns_mov_b32 s86, 125\ns_mov_b32 s89, 0\n" \
".Lvu_hb%=:\nv_cmp_ne_u32 vcc, v15, v12\ns_cbranch_vccnz .Lvu_hb1%=\ns_sleep 4\nbuffer_load_dwordx2 v[12:13], off, s[36:39], s45 sc1\ns_waitcnt vmcnt(0)\ns_add_u32 s43, s43, 1\ns_cmp_lt_u32 s43, 262144\n" \
"s_cbranch_scc1 .Lvu_hb%=\ns_branch .Lvu_abort%=\n.Lvu_hb1%=:\nv_readfirstlane_b32 s44, v13\ns_and_b32 s44, s44, 0x200000\ns_add_u32 s36, s36, s44\ns_addc_u32 s37, s37, 0\nbuffer_load_dword v14, v8, s[36:39], 0 offen sc1\n" \
"ds_write_b128 v1, v[56:59] offset:12288\nds_write_b128 v1, v[60:63] offset:13312\nds_write_b128 v1, v[64:67] offset:14336\nds_write_b128 v1, v[68:71] offset:15360\ns_waitcnt vmcnt(13)\ns_barrier\nds_read_b64_tr_b16 a[32:33], v95 offset:20480\nds_read_b64_tr_b16 a[34:35], v95 offset:20736\n" \
"ds_read_b64_tr_b16 a[36:37], v95 offset:22528\nds_read_b64_tr_b16 a[38:39], v95 offset:22784\nds_read_b64_tr_b16 a[232:233], v95 offset:20544\nds_read_b64_tr_b16 a[234:235], v95 offset:20800\nds_read_b64_tr_b16 a[236:237], v95 offset:22592\nds_read_b64_tr_b16 a[238:239], v95 offset:22848\ns_waitcnt lgkmcnt(0)\ns_waitcnt vmcnt(9)\n" \
"s_barrier\nds_read_b64_tr_b16 v[68:69], v94 offset:0\nds_read_b64_tr_b16 v[70:71], v94 offset:256\nds_read_b64_tr_b16 v[64:65], v94 offset:2048\nds_read_b64_tr_b16 v[66:67], v94 offset:2304\nds_read_b64_tr_b16 v[60:61], v94 offset:64\nds_read_b64_tr_b16 v[62:63], v94 offset:320\nds_read_b64_tr_b16 v[56:57], v94 offset:2112\n" \
"ds_read_b64_tr_b16 v[58:59], v94 offset:2368\ns_waitcnt lgkmcnt(0)\nds_write_b128 v1, v[68:71] offset:16384\nds_write_b128 v1, v[64:67] offset:17408\nds_write_b128 v1, v[60:63] offset:18432\nds_write_b128 v1, v[56:59] offset:19456\ns_waitcnt vmcnt(5)\ns_barrier\n" \
"ds_read_b64_tr_b16 a[136:137], v94 offset:4096\nds_read_b64_tr_b16 a[138:139], v94 offset:4352\nds_read_b64_tr_b16 a[140:141], v94 offset:6144\nds_read_b64_tr_b16 a[142:143], v94 offset:6400\nds_read_b64_tr_b16 v[176:177], v94 offset:4160\nds_read_b64_tr_b16 v[178:179], v94 offset:4416\nds_read_b64_tr_b16 v[180:181], v94 offset:6208\nds_read_b64_tr_b16 v[182:183], v94 offset:6464\n" \
"s_waitcnt lgkmcnt(0)\ns_waitcnt vmcnt(1)\ns_barrier\nds_read_b64_tr_b16 v[56:57], v94 offset:8192\nds_read_b64_tr_b16 v[58:59], v94 offset:8448\nds_read_b64_tr_b16 v[60:61], v94 offset:10240\nds_read_b64_tr_b16 v[62:63], v94 offset:10496\nds_read_b64_tr_b16 v[64:65], v94 offset:8256\n" \
"ds_read_b64_tr_b16 v[66:67], v94 offset:8512\nds_read_b64_tr_b16 v[68:69], v94 offset:10304\nds_read_b64_tr_b16 v[70:71], v94 offset:10560\ns_waitcnt lgkmcnt(0)\nds_write_b128 v1, v[56:59] offset:20480\nds_write_b128 v1, v[60:63] offset:21504\nds_write_b128 v1, v[64:67] offset:22528\nds_write_b128 v1, v[68:71] offset:23552\n" \
"s_waitcnt lgkmcnt(0)\ns_barrier\ns_mov_b32 s40, -1\ns_mov_b32 s43, 0\ns_lshl_b32 s57, %[w], 12\ns_mov_b32 s56, 0\ns_mov_b32 s42, 0\ns_mov_b32 s60, -2147483648\n" \
"s_mov_b32 s54, -2147483648\ns_add_u32 s44, s40, 1\ns_lshl_b32 s44, s44, 17\ns_add_u32 s44, s44, %[xn]\ns_lshr_b32 s45, s44, 8\ns_and_b32 s45, s45, 0x2000\ns_xor_b32 s41, s44, s45\ns_add_u32 s41, s41, %[xw]\n" \
"s_branch .Lvu_f0c%=\n.Lvu_f0%=:\nbuffer_load_dword v14, v8, s[36:39], s56 offen sc1\n.Lvu_f0c%=:\ns_waitcnt vmcnt(0)\nv_cmp_ge_i32 vcc, s40, v14\ns_cbranch_vccz .Lvu_f0fok%=\ns_add_u32 s43, s43, 1\n" \
"s_cmp_lt_u32 s43, 262144\ns_cbranch_scc1 .Lvu_f0%=\ns_branch .Lvu_abort%=\n.Lvu_f0fok%=:\ns_add_u32 m0, s57, 0\ns_add_u32 s58, s41, 0\nbuffer_load_dwordx4 v0, s[36:39], s58 offen offset:0 sc1 lds\nbuffer_load_dwordx4 v0, s[36:39], s58 offen offset:1024 sc1 lds\n" \
"buffer_load_dwordx4 v0, s[36:39], s58 offen offset:2048 sc1 lds\nbuffer_load_dwordx4 v0, s[36:39], s58 offen offset:3072 sc1 lds\ns_add_u32 m0, s57, 32768\ns_add_u32 s58, s41, 32768\nbuffer_load_dwordx4 v0, s[36:39], s58 offen offset:0 sc1 lds\nbuffer_load_dwordx4 v0, s[36:39], s58 offen offset:1024 sc1 lds\nbuffer_load_dwordx4 v0, s[36:39], s58 offen offset:2048 sc1 lds\nbuffer_load_dwordx4 v0, s[36:39], s58 offen offset:3072 sc1 lds\n" \
"s_waitcnt vmcnt(0)\ns_barrier\nds_read_b128 v[32:35], v2 offset:0\nds_read_b128 v[36:39], v2 offset:2048\nds_read_b128 v[40:43], v2 offset:256\nds_read_b128 v[44:47], v2 offset:2304\n.p2align 6\n.Lvu_tok%=:\n" \
"ds_read_b128 v[64:67], v2 offset:4096\nds_read_b128 v[68:71], v2 offset:6144\nds_read_b128 v[72:75], v2 offset:4352\ns_waitcnt lgkmcnt(6)\nv_mfma_f32_16x16x32_bf16 v[16:19], a[0:3], v[32:35], 0\ns_waitcnt lgkmcnt(5)\nv_cvt_pk_bf16_f32 v92, v80, v81\nv_cvt_pk_bf16_f32 v93, v82, v83\n" \
"v_mfma_f32_16x16x32_bf16 v[16:19], a[4:7], v[36:39], v[16:19]\nds_read_b128 v[76:79], v2 offset:6400\nv_cvt_pk_bf16_f32 v94, v84, v85\nv_cvt_pk_bf16_f32 v95, v86, v87\nv_mfma_f32_16x16x32_bf16 v[20:23], a[16:19], v[32:35], 0\nv_and_or_b32 v92, v92, -2, v13\nv_and_or_b32 v94, v94, -2, v13\nv_mfma_f32_16x16x32_bf16 v[20:23], a[20:23], v[36:39], v[20:23]\n" \
"s_waitcnt lgkmcnt(5)\nbuffer_store_dwordx4 v[92:95], v7, s[36:39], s60 offen offset:2560 sc1\nds_read_b128 v[32:35], v2 offset:8192\nv_mfma_f32_16x16x32_bf16 v[24:27], a[0:3], v[40:43], 0\ns_waitcnt lgkmcnt(5)\nds_read_b128 v[56:59], v1 offset:0\nds_read_b128 v[60:63], v1 offset:1024\nv_mfma_f32_16x16x32_bf16 v[24:27], a[4:7], v[44:47], v[24:27]\n" \
"s_add_u32 m0, s57, 16384\nds_read_b128 v[36:39], v2 offset:10240\nv_mfma_f32_16x16x32_bf16 v[28:31], a[16:19], v[40:43], 0\ns_add_u32 s60, s41, 65536\nbuffer_load_dwordx4 v0, s[36:39], s60 offen offset:0 sc1 lds\nv_mfma_f32_16x16x32_bf16 v[28:31], a[20:23], v[44:47], v[28:31]\ns_waitcnt lgkmcnt(7)\nds_read_b128 v[40:43], v2 offset:8448\n" \
"v_mfma_f32_16x16x32_bf16 v[16:19], a[24:27], v[64:67], v[16:19]\ns_waitcnt lgkmcnt(7)\nv_mfma_f32_16x16x32_bf16 v[16:19], a[28:31], v[68:71], v[16:19]\nds_read_b128 v[44:47], v2 offset:10496\nv_mfma_f32_16x16x32_bf16 v[20:23], a[40:43], v[64:67], v[20:23]\nbuffer_load_dwordx4 v0, s[36:39], s60 offen offset:1024 sc1 lds\nv_mfma_f32_16x16x32_bf16 v[20:23], a[44:47], v[68:71], v[20:23]\ns_waitcnt lgkmcnt(7)\n" \
"ds_read_b128 v[64:67], v2 offset:12288\nv_mfma_f32_16x16x32_bf16 v[24:27], a[24:27], v[72:75], v[24:27]\ns_waitcnt lgkmcnt(7)\nv_mfma_f32_16x16x32_bf16 v[24:27], a[28:31], v[76:79], v[24:27]\nds_read_b128 v[68:71], v2 offset:14336\nv_mfma_f32_16x16x32_bf16 v[28:31], a[40:43], v[72:75], v[28:31]\nbuffer_load_dwordx4 v0, s[36:39], s60 offen offset:2048 sc1 lds\nv_mfma_f32_16x16x32_bf16 v[28:31], a[44:47], v[76:79], v[28:31]\n" \
"s_waitcnt lgkmcnt(5)\nds_read_b128 v[72:75], v2 offset:12544\nv_mfma_f32_16x16x32_bf16 v[16:19], v[56:59], v[32:35], v[16:19]\ns_waitcnt lgkmcnt(5)\nv_mfma_f32_16x16x32_bf16 v[16:19], v[60:63], v[36:39], v[16:19]\nds_read_b128 v[76:79], v2 offset:14592\nv_mfma_f32_16x16x32_bf16 v[20:23], a[64:67], v[32:35], v[20:23]\nbuffer_load_dwordx4 v0, s[36:39], s60 offen offset:3072 sc1 lds\n" \
"v_mfma_f32_16x16x32_bf16 v[20:23], a[68:71], v[36:39], v[20:23]\ns_waitcnt lgkmcnt(5)\nds_read_b128 v[32:35], v2 offset:32768\nv_mfma_f32_16x16x32_bf16 v[24:27], v[56:59], v[40:43], v[24:27]\ns_add_u32 s56, s56, %[fs]\ns_add_u32 s62, s40, 1\ns_waitcnt lgkmcnt(5)\nv_mfma_f32_16x16x32_bf16 v[24:27], v[60:63], v[44:47], v[24:27]\n" \
"s_lshr_b32 m0, s57, 4\ns_add_u32 m0, m0, 159744\nds_read_b128 v[36:39], v2 offset:34816\nv_mfma_f32_16x16x32_bf16 v[28:31], a[64:67], v[40:43], v[28:31]\nbuffer_load_dword v8, s[36:39], s56 offen sc1 lds\nv_lshrrev_b32 v14, 2, v0\nv_mfma_f32_16x16x32_bf16 v[28:31], a[68:71], v[44:47], v[28:31]\ns_waitcnt lgkmcnt(5)\n" \
"ds_read_b128 v[40:43], v2 offset:33024\nv_mfma_f32_16x16x32_bf16 v[16:19], a[80:83], v[64:67], v[16:19]\ns_waitcnt lgkmcnt(5)\nv_add_u32 v14, 159744, v14\nv_mfma_f32_16x16x32_bf16 v[16:19], a[84:87], v[68:71], v[16:19]\nds_read_b128 v[44:47], v2 offset:35072\nv_mfma_f32_16x16x32_bf16 v[20:23], a[88:91], v[64:67], v[20:23]\nds_read_b128 v[56:59], v1 offset:2048\n" \
"ds_read_b128 v[60:63], v1 offset:3072\nv_mfma_f32_16x16x32_bf16 v[20:23], a[92:95], v[68:71], v[20:23]\ns_waitcnt lgkmcnt(7)\nds_read_b128 v[64:67], v2 offset:36864\nv_mfma_f32_16x16x32_bf16 v[24:27], a[80:83], v[72:75], v[24:27]\ns_waitcnt lgkmcnt(7)\nv_mfma_f32_16x16x32_bf16 v[24:27], a[84:87], v[76:79], v[24:27]\nds_read_b128 v[68:71], v2 offset:38912\n" \
"v_mfma_f32_16x16x32_bf16 v[28:31], a[88:91], v[72:75], v[28:31]\nv_mfma_f32_16x16x32_bf16 v[28:31], a[92:95], v[76:79], v[28:31]\ns_waitcnt lgkmcnt(7)\nds_read_b128 v[72:75], v2 offset:37120\nv_mfma_f32_16x16x32_bf16 v[16:19], a[104:107], v[32:35], v[16:19]\ns_waitcnt lgkmcnt(7)\nv_mfma_f32_16x16x32_bf16 v[16:19], a[108:111], v[36:39], v[16:19]\nds_read_b128 v[76:79], v2 offset:39168\n" \
"v_mfma_f32_16x16x32_bf16 v[20:23], a[120:123], v[32:35], v[20:23]\ns_barrier\nv_mfma_f32_16x16x32_bf16 v[20:23], a[124:127], v[36:39], v[20:23]\ns_waitcnt lgkmcnt(7)\ns_add_u32 m0, s57, 0\nds_read_b128 v[32:35], v2 offset:40960\nv_mfma_f32_16x16x32_bf16 v[24:27], a[104:107], v[40:43], v[24:27]\ns_add_u32 s60, s41, 98304\n" \
"s_waitcnt lgkmcnt(7)\nbuffer_load_dwordx4 v0, s[36:39], s60 offen offset:0 sc1 lds\nv_mfma_f32_16x16x32_bf16 v[24:27], a[108:111], v[44:47], v[24:27]\nds_read_b128 v[36:39], v2 offset:43008\nv_mfma_f32_16x16x32_bf16 v[28:31], a[120:123], v[40:43], v[28:31]\nv_mfma_f32_16x16x32_bf16 v[28:31], a[124:127], v[44:47], v[28:31]\ns_waitcnt lgkmcnt(5)\nds_read_b128 v[40:43], v2 offset:41216\n" \
"v_mfma_f32_16x16x32_bf16 v[16:19], a[128:131], v[64:67], v[16:19]\ns_waitcnt lgkmcnt(5)\nbuffer_load_dwordx4 v0, s[36:39], s60 offen offset:1024 sc1 lds\nv_mfma_f32_16x16x32_bf16 v[16:19], a[132:135], v[68:71], v[16:19]\nds_read_b128 v[44:47], v2 offset:43264\nv_mfma_f32_16x16x32_bf16 v[20:23], v[56:59], v[64:67], v[20:23]\nv_mfma_f32_16x16x32_bf16 v[20:23], v[60:63], v[68:71], v[20:23]\ns_waitcnt lgkmcnt(5)\n" \
"ds_read_b128 v[64:67], v2 offset:45056\nv_mfma_f32_16x16x32_bf16 v[24:27], a[128:131], v[72:75], v[24:27]\ns_waitcnt lgkmcnt(5)\nbuffer_load_dwordx4 v0, s[36:39], s60 offen offset:2048 sc1 lds\nv_mfma_f32_16x16x32_bf16 v[24:27], a[132:135], v[76:79], v[24:27]\nds_read_b128 v[68:71], v2 offset:47104\nv_mfma_f32_16x16x32_bf16 v[28:31], v[56:59], v[72:75], v[28:31]\nv_mfma_f32_16x16x32_bf16 v[28:31], v[60:63], v[76:79], v[28:31]\n" \
"s_waitcnt lgkmcnt(5)\nds_read_b128 v[72:75], v2 offset:45312\nv_mfma_f32_16x16x32_bf16 v[16:19], a[152:155], v[32:35], v[16:19]\ns_waitcnt lgkmcnt(5)\nbuffer_load_dwordx4 v0, s[36:39], s60 offen offset:3072 sc1 lds\nv_mfma_f32_16x16x32_bf16 v[16:19], a[156:159], v[36:39], v[16:19]\ns_add_u32 s44, s40, 2\nds_read_b128 v[76:79], v2 offset:47360\n" \
"v_mfma_f32_16x16x32_bf16 v[20:23], a[168:171], v[32:35], v[20:23]\ns_lshl_b32 s44, s44, 17\nds_read_b128 v[56:59], v1 offset:4096\nds_read_b128 v[60:63], v1 offset:5120\nv_mfma_f32_16x16x32_bf16 v[20:23], a[172:175], v[36:39], v[20:23]\ns_waitcnt lgkmcnt(7)\nv_mfma_f32_16x16x32_bf16 v[24:27], a[152:155], v[40:43], v[24:27]\ns_add_u32 s44, s44, %[xn]\n" \
"s_lshr_b32 s45, s44, 8\ns_waitcnt lgkmcnt(6)\nv_mfma_f32_16x16x32_bf16 v[24:27], a[156:159], v[44:47], v[24:27]\nv_mfma_f32_16x16x32_bf16 v[28:31], a[168:171], v[40:43], v[28:31]\ns_and_b32 s45, s45, 0x2000\ns_xor_b32 s41, s44, s45\nv_mfma_f32_16x16x32_bf16 v[28:31], a[172:175], v[44:47], v[28:31]\ns_waitcnt lgkmcnt(5)\n" \
"v_mfma_f32_16x16x32_bf16 v[16:19], a[184:187], v[64:67], v[16:19]\ns_waitcnt lgkmcnt(4)\nv_mfma_f32_16x16x32_bf16 v[16:19], a[188:191], v[68:71], v[16:19]\nv_mfma_f32_16x16x32_bf16 v[20:23], a[192:195], v[64:67], v[20:23]\ns_waitcnt lgkmcnt(2)\ns_waitcnt vmcnt(4)\ns_barrier\ns_add_u32 s41, s41, %[xw]\n" \
"ds_read_b128 v[32:35], v2 offset:16384\nv_mfma_f32_16x16x32_bf16 v[20:23], a[196:199], v[68:71], v[20:23]\nds_read_b128 v[36:39], v2 offset:18432\nv_mfma_f32_16x16x32_bf16 v[24:27], a[184:187], v[72:75], v[24:27]\nds_read_b128 v[40:43], v2 offset:16640\nds_read_b32 v14, v14\nv_mfma_f32_16x16x32_bf16 v[24:27], a[188:191], v[76:79], v[24:27]\nds_read_b128 v[44:47], v2 offset:18688\n" \
"v_mfma_f32_16x16x32_bf16 v[28:31], a[192:195], v[72:75], v[28:31]\ns_waitcnt vmcnt(4)\nbuffer_store_dword v15, off, s[36:39], s54 sc1\nds_read_b128 v[64:67], v2 offset:20480\nv_mfma_f32_16x16x32_bf16 v[28:31], a[196:199], v[76:79], v[28:31]\ns_waitcnt lgkmcnt(5)\nds_read_b128 v[68:71], v2 offset:22528\nv_mfma_f32_16x16x32_bf16 v[16:19], v[56:59], v[32:35], v[16:19]\n" \
"s_waitcnt lgkmcnt(3)\nds_read_b128 v[72:75], v2 offset:20736\nv_cmp_ge_i32 s[64:65], s62, v14\nv_mfma_f32_16x16x32_bf16 v[16:19], v[60:63], v[36:39], v[16:19]\ns_cmp_eq_u64 s[64:65], 0\ns_cselect_b32 s61, s41, -2147483648\nds_read_b128 v[76:79], v2 offset:22784\nv_mfma_f32_16x16x32_bf16 v[20:23], a[216:219], v[32:35], v[20:23]\n" \
"s_add_u32 m0, s57, 32768\ns_add_u32 s58, s61, 32768\nbuffer_load_dwordx4 v0, s[36:39], s58 offen offset:0 sc1 lds\nv_mfma_f32_16x16x32_bf16 v[20:23], a[220:223], v[36:39], v[20:23]\nds_read_b128 v[32:35], v2 offset:24576\nv_mfma_f32_16x16x32_bf16 v[24:27], v[56:59], v[40:43], v[24:27]\ns_waitcnt lgkmcnt(5)\nv_mfma_f32_16x16x32_bf16 v[24:27], v[60:63], v[44:47], v[24:27]\n" \
"ds_read_b128 v[36:39], v2 offset:26624\nv_mfma_f32_16x16x32_bf16 v[28:31], a[216:219], v[40:43], v[28:31]\nbuffer_load_dwordx4 v0, s[36:39], s58 offen offset:1024 sc1 lds\nv_mfma_f32_16x16x32_bf16 v[28:31], a[220:223], v[44:47], v[28:31]\ns_waitcnt lgkmcnt(5)\nds_read_b128 v[40:43], v2 offset:24832\nv_mfma_f32_16x16x32_bf16 v[16:19], a[224:227], v[64:67], v[16:19]\ns_waitcnt lgkmcnt(5)\n" \
"ds_read_b128 v[56:59], v1 offset:6144\nds_read_b128 v[60:63], v1 offset:7168\nv_mfma_f32_16x16x32_bf16 v[16:19], a[228:231], v[68:71], v[16:19]\nds_read_b128 v[44:47], v2 offset:26880\nv_mfma_f32_16x16x32_bf16 v[20:23], a[240:243], v[64:67], v[20:23]\nbuffer_load_dwordx4 v0, s[36:39], s58 offen offset:2048 sc1 lds\nv_mfma_f32_16x16x32_bf16 v[20:23], a[244:247], v[68:71], v[20:23]\ns_waitcnt lgkmcnt(7)\n" \
"ds_read_b128 v[64:67], v2 offset:28672\nv_mfma_f32_16x16x32_bf16 v[24:27], a[224:227], v[72:75], v[24:27]\ns_waitcnt lgkmcnt(7)\nv_mfma_f32_16x16x32_bf16 v[24:27], a[228:231], v[76:79], v[24:27]\nds_read_b128 v[68:71], v2 offset:30720\nv_mfma_f32_16x16x32_bf16 v[28:31], a[240:243], v[72:75], v[28:31]\nbuffer_load_dwordx4 v0, s[36:39], s58 offen offset:3072 sc1 lds\nv_mfma_f32_16x16x32_bf16 v[28:31], a[244:247], v[76:79], v[28:31]\n" \
"s_waitcnt lgkmcnt(7)\nds_read_b128 v[72:75], v2 offset:28928\nv_mfma_f32_16x16x32_bf16 v[16:19], v[96:99], v[32:35], v[16:19]\ns_waitcnt lgkmcnt(7)\nv_mfma_f32_16x16x32_bf16 v[16:19], v[100:103], v[36:39], v[16:19]\nds_read_b128 v[76:79], v2 offset:30976\nv_mfma_f32_16x16x32_bf16 v[20:23], v[112:115], v[32:35], v[20:23]\nv_mfma_f32_16x16x32_bf16 v[20:23], v[116:119], v[36:39], v[20:23]\n" \
"s_waitcnt lgkmcnt(7)\nv_mfma_f32_16x16x32_bf16 v[24:27], v[96:99], v[40:43], v[24:27]\ns_waitcnt lgkmcnt(4)\nv_mfma_f32_16x16x32_bf16 v[24:27], v[100:103], v[44:47], v[24:27]\nv_mfma_f32_16x16x32_bf16 v[28:31], v[112:115], v[40:43], v[28:31]\ns_waitcnt lgkmcnt(0)\ns_waitcnt vmcnt(5)\ns_barrier\n" \
"s_add_u32 m0, s57, 16384\nds_read_b128 v[32:35], v2 offset:0\nbuffer_load_dwordx4 v3, s[36:39], s42 offen offset:0 sc1 lds\nv_mfma_f32_16x16x32_bf16 v[28:31], v[116:119], v[44:47], v[28:31]\nds_read_b128 v[36:39], v2 offset:2048\nv_mfma_f32_16x16x32_bf16 v[16:19], v[128:131], v[64:67], v[16:19]\nds_read_b128 v[40:43], v2 offset:4096\nv_mfma_f32_16x16x32_bf16 v[16:19], v[132:135], v[68:71], v[16:19]\n" \
"ds_read_b128 v[44:47], v2 offset:6144\nv_mfma_f32_16x16x32_bf16 v[20:23], v[56:59], v[64:67], v[20:23]\nbuffer_load_dwordx4 v3, s[36:39], s42 offen offset:1024 sc1 lds\nv_mfma_f32_16x16x32_bf16 v[20:23], v[60:63], v[68:71], v[20:23]\nds_read_b128 v[64:67], v2 offset:8192\nv_mfma_f32_16x16x32_bf16 v[24:27], v[128:131], v[72:75], v[24:27]\nv_mfma_f32_16x16x32_bf16 v[24:27], v[132:135], v[76:79], v[24:27]\nds_read_b128 v[68:71], v2 offset:10240\n" \
"v_mfma_f32_16x16x32_bf16 v[28:31], v[56:59], v[72:75], v[28:31]\nbuffer_load_dwordx4 v3, s[36:39], s42 offen offset:2048 sc1 lds\nv_mfma_f32_16x16x32_bf16 v[28:31], v[60:63], v[76:79], v[28:31]\ns_waitcnt lgkmcnt(5)\nds_read_b128 v[72:75], v2 offset:12288\nv_mfma_f32_16x16x32_bf16 v[16:19], v[144:147], v[32:35], v[16:19]\ns_waitcnt lgkmcnt(5)\nv_mfma_f32_16x16x32_bf16 v[16:19], v[148:151], v[36:39], v[16:19]\n" \
"ds_read_b128 v[76:79], v2 offset:14336\nv_mfma_f32_16x16x32_bf16 v[20:23], v[160:163], v[32:35], v[20:23]\nbuffer_load_dwordx4 v3, s[36:39], s42 offen offset:3072 sc1 lds\nv_mfma_f32_16x16x32_bf16 v[20:23], v[164:167], v[36:39], v[20:23]\ns_waitcnt lgkmcnt(5)\nds_read_b128 v[32:35], v2 offset:256\nv_mfma_f32_16x16x32_bf16 v[16:19], v[168:171], v[40:43], v[16:19]\ns_waitcnt lgkmcnt(5)\n" \
"v_add_u32 v14, s57, v0\nv_mfma_f32_16x16x32_bf16 v[16:19], v[172:175], v[44:47], v[16:19]\nds_read_b128 v[36:39], v2 offset:2304\nv_mfma_f32_16x16x32_bf16 v[20:23], v[184:187], v[40:43], v[20:23]\nv_mov_b64 v[48:49], 0\nv_mfma_f32_16x16x32_bf16 v[20:23], v[188:191], v[44:47], v[20:23]\ns_waitcnt lgkmcnt(5)\nds_read_b128 v[40:43], v2 offset:4352\n" \
"v_mov_b64 v[50:51], 0\nv_mfma_f32_16x16x32_bf16 v[16:19], v[200:203], v[64:67], v[16:19]\ns_waitcnt lgkmcnt(5)\nv_mov_b64 v[52:53], 0\nv_mfma_f32_16x16x32_bf16 v[16:19], v[204:207], v[68:71], v[16:19]\nds_read_b128 v[44:47], v2 offset:6400\nv_mov_b64 v[54:55], 0\nv_mfma_f32_16x16x32_bf16 v[20:23], v[216:219], v[64:67], v[20:23]\n" \
"ds_read_b128 v[56:59], v1 offset:0\nds_read_b128 v[60:63], v1 offset:1024\nv_mfma_f32_16x16x32_bf16 v[20:23], v[220:223], v[68:71], v[20:23]\ns_waitcnt lgkmcnt(7)\nds_read_b128 v[64:67], v2 offset:8448\nv_mfma_f32_16x16x32_bf16 v[16:19], v[232:235], v[72:75], v[16:19]\ns_waitcnt lgkmcnt(7)\nv_mfma_f32_16x16x32_bf16 v[16:19], v[236:239], v[76:79], v[16:19]\n" \
"ds_read_b128 v[68:71], v2 offset:10496\nv_mfma_f32_16x16x32_bf16 v[20:23], v[240:243], v[72:75], v[20:23]\nv_mfma_f32_16x16x32_bf16 v[20:23], v[244:247], v[76:79], v[20:23]\ns_waitcnt lgkmcnt(7)\nv_mfma_f32_16x16x32_bf16 v[24:27], v[144:147], v[32:35], v[24:27]\ns_waitcnt vmcnt(0)\ns_waitcnt lgkmcnt(6)\nds_read_b128 v[80:83], v14 offset:16384\n" \
"ds_read_b128 v[84:87], v14 offset:17408\nv_mfma_f32_16x16x32_bf16 v[24:27], v[148:151], v[36:39], v[24:27]\nv_cvt_pk_bf16_f32 v72, v16, v17\nv_mfma_f32_16x16x32_bf16 v[28:31], v[160:163], v[32:35], v[28:31]\nds_read_b128 v[88:91], v14 offset:18432\nds_read_b128 v[92:95], v14 offset:19456\nv_cvt_pk_bf16_f32 v73, v18, v19\nv_mfma_f32_16x16x32_bf16 v[28:31], v[164:167], v[36:39], v[28:31]\n" \
"s_waitcnt lgkmcnt(9)\nds_read_b128 v[32:35], v2 offset:12544\nv_cvt_pk_bf16_f32 v74, v20, v21\nv_mfma_f32_16x16x32_bf16 v[24:27], v[168:171], v[40:43], v[24:27]\ns_waitcnt lgkmcnt(3)\nv_max3_u32 v76, v80, v82, v84\nv_cvt_pk_bf16_f32 v75, v22, v23\nv_mfma_f32_16x16x32_bf16 v[24:27], v[172:175], v[44:47], v[24:27]\n" \
"s_waitcnt lgkmcnt(2)\nds_read_b128 v[36:39], v2 offset:14592\nv_max3_u32 v76, v76, v86, v88\nv_mfma_f32_16x16x32_bf16 v[28:31], v[184:187], v[40:43], v[28:31]\ns_waitcnt lgkmcnt(2)\nds_write2st64_b64 v9, v[72:73], v[74:75] offset0:0 offset1:8\nv_max3_u32 v76, v76, v90, v92\nv_max_u32 v76, v76, v94\n" \
"v_mfma_f32_16x16x32_bf16 v[28:31], v[188:191], v[44:47], v[28:31]\nv_cmp_eq_u32 vcc, -1, v76\nv_mfma_f32_16x16x32_bf16 v[24:27], v[200:203], v[64:67], v[24:27]\ns_cbranch_vccnz .Lvu_re%=\n.Lvu_rer%=:\ns_barrier\ns_memrealtime s[68:69]\nds_read_b128 v[80:83], v0 offset:16384\n" \
"v_mfma_f32_16x16x32_bf16 v[24:27], v[204:207], v[68:71], v[24:27]\nds_read_b128 v[84:87], v0 offset:17408\nv_mfma_f32_16x16x32_bf16 v[28:31], v[216:219], v[64:67], v[28:31]\nds_read_b128 v[88:91], v0 offset:18432\nv_mfma_f32_16x16x32_bf16 v[28:31], v[220:223], v[68:71], v[28:31]\ns_waitcnt lgkmcnt(5)\nds_read_b128 v[92:95], v0 offset:19456\nv_mfma_f32_16x16x32_bf16 v[24:27], v[232:235], v[32:35], v[24:27]\n" \
"s_waitcnt lgkmcnt(5)\nds_read_b128 v[16:19], v0 offset:20480\nv_mfma_f32_16x16x32_bf16 v[24:27], v[236:239], v[36:39], v[24:27]\nds_read_b128 v[20:23], v0 offset:21504\nv_mfma_f32_16x16x32_bf16 v[28:31], v[240:243], v[32:35], v[28:31]\nds_read_b128 v[40:43], v0 offset:22528\nv_mfma_f32_16x16x32_bf16 v[28:31], v[244:247], v[36:39], v[28:31]\ns_cmp_lt_i32 s40, 0\n" \
"ds_read_b128 v[44:47], v0 offset:23552\ns_cbranch_scc1 .Lvu_first%=\n.Lvu_crit%=:\ns_waitcnt lgkmcnt(7)\nv_smfmac_f32_16x16x64_bf16 v[48:51], v[80:83], a[0:7], v4\ns_waitcnt lgkmcnt(6)\nds_read_b128 v[64:67], v1 offset:4096\nds_read_b128 v[32:35], v0 offset:24576\n" \
"v_mov_b32 v14, s40\nv_smfmac_f32_16x16x64_bf16 v[48:51], v[84:87], a[24:31], v4\ns_waitcnt lgkmcnt(7)\nds_read_b128 v[68:71], v1 offset:5120\nds_read_b128 v[36:39], v0 offset:25600\nv_cvt_pk_bf16_f32 v76, v24, v25\nv_smfmac_f32_16x16x64_bf16 v[48:51], v[88:91], v[56:63], v4\ns_sub_u32 s88, s86, s89\n" \
"s_min_u32 s88, s88, 150\ns_waitcnt lgkmcnt(8)\nds_read_b128 v[80:83], v0 offset:28672\nv_cvt_pk_bf16_f32 v77, v26, v27\nv_smfmac_f32_16x16x64_bf16 v[48:51], v[92:95], a[80:87], v4\ns_waitcnt lgkmcnt(8)\nds_read_b128 v[84:87], v0 offset:29696\nv_cvt_pk_bf16_f32 v78, v28, v29\n" \
"v_smfmac_f32_16x16x64_bf16 v[48:51], v[16:19], a[104:111], v4\ns_add_u32 s88, s88, s68\ns_mov_b32 s89, s68\ns_waitcnt lgkmcnt(8)\nds_read_b128 v[88:91], v0 offset:30720\nv_cvt_pk_bf16_f32 v79, v30, v31\nv_smfmac_f32_16x16x64_bf16 v[48:51], v[20:23], a[128:135], v4\ns_waitcnt lgkmcnt(8)\n" \
"ds_read_b128 v[92:95], v0 offset:31744\nv_mov_b32 v15, s88\nv_smfmac_f32_16x16x64_bf16 v[48:51], v[40:43], a[152:159], v4\ns_waitcnt lgkmcnt(8)\nds_read_b128 v[24:27], v0 offset:26624\nbuffer_store_dwordx2 v[14:15], off, s[36:39], %[sg] sc1\nv_smfmac_f32_16x16x64_bf16 v[48:51], v[44:47], a[184:191], v4\ns_waitcnt lgkmcnt(7)\n" \
"s_waitcnt lgkmcnt(6)\nds_read_b128 v[28:31], v0 offset:27648\nv_smfmac_f32_16x16x64_bf16 v[48:51], v[32:35], v[64:71], v4\ns_waitcnt lgkmcnt(6)\nds_read_b128 v[56:59], v1 offset:2048\nv_smfmac_f32_16x16x64_bf16 v[48:51], v[36:39], a[224:231], v4\ns_waitcnt lgkmcnt(6)\nds_read_b128 v[60:63], v1 offset:3072\n" \
"v_smfmac_f32_16x16x64_bf16 v[48:51], v[80:83], v[144:151], v4\ns_waitcnt lgkmcnt(6)\nds_write2st64_b64 v9, v[76:77], v[78:79] offset0:2 offset1:10\nv_smfmac_f32_16x16x64_bf16 v[48:51], v[84:87], v[168:175], v4\ns_waitcnt lgkmcnt(6)\nv_smfmac_f32_16x16x64_bf16 v[48:51], v[88:91], v[200:207], v4\ns_waitcnt lgkmcnt(5)\nv_smfmac_f32_16x16x64_bf16 v[48:51], v[92:95], v[232:239], v4\n" \
"s_waitcnt lgkmcnt(4)\nv_smfmac_f32_16x16x64_bf16 v[48:51], v[24:27], v[96:103], v4\ns_waitcnt lgkmcnt(3)\nds_read_b128 v[64:67], v1 offset:6144\nv_smfmac_f32_16x16x64_bf16 v[48:51], v[28:31], v[128:135], v4\nds_read_b128 v[68:71], v1 offset:7168\nv_smfmac_f32_16x16x64_bf16 v[52:55], v[80:83], v[160:167], v4\nv_smfmac_f32_16x16x64_bf16 v[52:55], v[84:87], v[184:191], v4\n" \
"v_add_f32 v48, v48, v50\nv_smfmac_f32_16x16x64_bf16 v[52:55], v[88:91], v[216:223], v4\nds_read_b128 v[80:83], v0 offset:16384\nv_add_f32 v49, v49, v51\nv_smfmac_f32_16x16x64_bf16 v[52:55], v[92:95], v[240:247], v4\nds_read_b128 v[84:87], v0 offset:17408\nv_cvt_pk_bf16_f32 v14, v48, v49\nv_smfmac_f32_16x16x64_bf16 v[52:55], v[16:19], a[120:127], v4\n" \
"s_waitcnt lgkmcnt(5)\nds_write_b16 v5, v14 offset:0\nds_write_b16_d16_hi v5, v14 offset:16\nv_smfmac_f32_16x16x64_bf16 v[52:55], v[20:23], v[56:63], v4\nds_read_b128 v[88:91], v0 offset:18432\nv_mov_b64 v[16:17], 0\nv_smfmac_f32_16x16x64_bf16 v[52:55], v[24:27], v[112:119], v4\ns_waitcnt lgkmcnt(5)\n" \
"ds_read_b128 v[92:95], v0 offset:19456\nv_mov_b64 v[18:19], 0\nv_smfmac_f32_16x16x64_bf16 v[52:55], v[28:31], v[64:71], v4\nds_read_b128 v[56:59], v1 offset:8192\nv_mov_b64 v[20:21], 0\nv_smfmac_f32_16x16x64_bf16 v[52:55], v[32:35], a[216:223], v4\ns_waitcnt lgkmcnt(3)\nds_read_b128 v[60:63], v1 offset:9216\n" \
"s_barrier\nds_read_b128 v[72:75], v11 offset:57344\nv_smfmac_f32_16x16x64_bf16 v[52:55], v[36:39], a[240:247], v4\nds_read_b128 v[76:79], v11 offset:58368\nv_mov_b64 v[22:23], 0\nv_smfmac_f32_16x16x64_bf16 v[52:55], v[40:43], a[168:175], v4\nv_mov_b64 v[24:25], 0\nv_smfmac_f32_16x16x64_bf16 v[52:55], v[44:47], a[192:199], v4\n" \
"ds_read_b128 v[64:67], v1 offset:12288\nv_mov_b64 v[26:27], 0\nv_smfmac_f32_16x16x64_bf16 v[52:55], v[80:83], a[16:23], v4\nds_read_b128 v[68:71], v1 offset:13312\nv_mov_b64 v[28:29], 0\nv_smfmac_f32_16x16x64_bf16 v[52:55], v[84:87], a[40:47], v4\ns_waitcnt lgkmcnt(7)\nv_mov_b64 v[30:31], 0\n" \
"v_smfmac_f32_16x16x64_bf16 v[52:55], v[88:91], a[64:71], v4\ns_waitcnt lgkmcnt(6)\nv_mov_b64 v[32:33], 0\nv_smfmac_f32_16x16x64_bf16 v[52:55], v[92:95], a[88:95], v4\ns_waitcnt lgkmcnt(3)\nv_mov_b64 v[34:35], 0\nv_smfmac_f32_16x16x64_bf16 v[16:19], v[72:75], a[8:15], v10\ns_waitcnt lgkmcnt(2)\n" \
"v_mov_b64 v[36:37], 0\nv_smfmac_f32_16x16x64_bf16 v[16:19], v[76:79], v[56:63], v10\nds_read_b128 v[56:59], v1 offset:10240\nv_add_f32 v52, v52, v54\nv_smfmac_f32_16x16x64_bf16 v[20:23], v[72:75], a[56:63], v10\nds_read_b128 v[60:63], v1 offset:11264\nv_add_f32 v53, v53, v55\nv_smfmac_f32_16x16x64_bf16 v[20:23], v[76:79], a[72:79], v10\n" \
"v_cvt_pk_bf16_f32 v14, v52, v53\nv_smfmac_f32_16x16x64_bf16 v[24:27], v[72:75], a[112:119], v10\ns_waitcnt lgkmcnt(2)\nds_write_b16 v5, v14 offset:2048\nds_write_b16_d16_hi v5, v14 offset:2064\nv_smfmac_f32_16x16x64_bf16 v[24:27], v[76:79], v[64:71], v10\nds_read_b128 v[64:67], v1 offset:14336\nv_mov_b64 v[38:39], 0\n" \
"v_smfmac_f32_16x16x64_bf16 v[28:31], v[72:75], a[160:167], v10\nds_read_b128 v[68:71], v1 offset:15360\nv_mov_b64 v[40:41], 0\nv_smfmac_f32_16x16x64_bf16 v[28:31], v[76:79], a[176:183], v10\nv_mov_b64 v[42:43], 0\nv_smfmac_f32_16x16x64_bf16 v[32:35], v[72:75], a[208:215], v10\ns_waitcnt lgkmcnt(2)\ns_barrier\n" \
"s_mov_b32 s59, 0\nds_read_b128 v[80:83], v11 offset:59392\nv_smfmac_f32_16x16x64_bf16 v[32:35], v[76:79], v[56:63], v10\ns_add_u32 m0, s57, 0\ns_add_u32 s58, s61, 0\nds_read_b128 v[56:59], v1 offset:16384\nbuffer_load_dwordx4 v0, s[36:39], s58 offen offset:0 sc1 lds\nds_read_b128 v[84:87], v11 offset:60416\n" \
"v_smfmac_f32_16x16x64_bf16 v[36:39], v[72:75], v[104:111], v10\nds_read_b128 v[60:63], v1 offset:17408\nv_mov_b64 v[44:45], 0\nv_smfmac_f32_16x16x64_bf16 v[36:39], v[76:79], v[120:127], v10\nv_mov_b64 v[46:47], 0\nv_smfmac_f32_16x16x64_bf16 v[40:43], v[72:75], v[152:159], v10\ns_waitcnt lgkmcnt(4)\nbuffer_load_dwordx4 v0, s[36:39], s58 offen offset:1024 sc1 lds\n" \
"v_smfmac_f32_16x16x64_bf16 v[40:43], v[76:79], v[64:71], v10\nds_read_b128 v[64:67], v1 offset:20480\nv_mov_b64 v[48:49], 0\nv_smfmac_f32_16x16x64_bf16 v[44:47], v[72:75], v[208:215], v10\nds_read_b128 v[68:71], v1 offset:21504\nv_mov_b64 v[50:51], 0\nv_smfmac_f32_16x16x64_bf16 v[44:47], v[76:79], v[224:231], v10\ns_waitcnt lgkmcnt(5)\n" \
"buffer_load_dwordx4 v0, s[36:39], s58 offen offset:2048 sc1 lds\nv_smfmac_f32_16x16x64_bf16 v[16:19], v[80:83], a[32:39], v10\ns_waitcnt lgkmcnt(3)\nds_read_b128 v[72:75], v11 offset:49152\nv_mov_b64 v[52:53], 0\nv_smfmac_f32_16x16x64_bf16 v[16:19], v[84:87], a[48:55], v10\ns_waitcnt lgkmcnt(3)\nds_read_b128 v[76:79], v11 offset:51200\n" \
"v_mov_b64 v[54:55], 0\nv_smfmac_f32_16x16x64_bf16 v[20:23], v[80:83], v[56:63], v10\nds_read_b128 v[56:59], v1 offset:18432\nbuffer_load_dwordx4 v0, s[36:39], s58 offen offset:3072 sc1 lds\nv_smfmac_f32_16x16x64_bf16 v[20:23], v[84:87], a[96:103], v10\nds_read_b128 v[60:63], v1 offset:19456\nv_cvt_pk_bf16_f32 v88, v16, v17\nv_smfmac_f32_16x16x64_bf16 v[24:27], v[80:83], a[136:143], v10\n" \
"v_and_or_b32 v88, v88, -2, v12\nv_cvt_pk_bf16_f32 v89, v18, v19\nv_smfmac_f32_16x16x64_bf16 v[24:27], v[84:87], a[144:151], v10\ns_waitcnt lgkmcnt(4)\nv_cvt_pk_bf16_f32 v90, v20, v21\nv_cvt_pk_bf16_f32 v91, v22, v23\nv_and_or_b32 v90, v90, -2, v12\nv_smfmac_f32_16x16x64_bf16 v[28:31], v[80:83], v[64:71], v10\n" \
"ds_read_b128 v[64:67], v1 offset:22528\nv_permlane16_swap_b32 v89, v91\nv_smfmac_f32_16x16x64_bf16 v[28:31], v[84:87], a[200:207], v10\nds_read_b128 v[68:71], v1 offset:23552\nv_permlane16_swap_b32 v88, v90\nv_smfmac_f32_16x16x64_bf16 v[32:35], v[80:83], a[232:239], v10\ns_add_u32 s63, s59, 0\nds_read_b128 v[16:19], v11 offset:53248\n" \
"v_cvt_pk_bf16_f32 v92, v24, v25\nv_and_or_b32 v92, v92, -2, v12\nv_smfmac_f32_16x16x64_bf16 v[32:35], v[84:87], a[248:255], v10\ns_waitcnt lgkmcnt(3)\nds_read_b128 v[20:23], v11 offset:55296\nbuffer_store_dwordx4 v[88:91], v6, s[36:39], s63 offen sc1\nv_cvt_pk_bf16_f32 v93, v26, v27\nv_cvt_pk_bf16_f32 v94, v28, v29\n" \
"v_cvt_pk_bf16_f32 v95, v30, v31\nv_and_or_b32 v94, v94, -2, v12\nv_smfmac_f32_16x16x64_bf16 v[36:39], v[80:83], v[56:63], v10\nds_read_b128 v[56:59], v1 offset:8192\nv_permlane16_swap_b32 v93, v95\nv_smfmac_f32_16x16x64_bf16 v[36:39], v[84:87], v[136:143], v10\nds_read_b128 v[60:63], v1 offset:9216\nv_permlane16_swap_b32 v92, v94\n" \
"v_smfmac_f32_16x16x64_bf16 v[40:43], v[80:83], v[176:183], v10\ns_add_u32 s63, s59, 131072\nds_read_b128 v[24:27], v11 offset:50176\nv_cvt_pk_bf16_f32 v88, v32, v33\nv_and_or_b32 v88, v88, -2, v12\nv_smfmac_f32_16x16x64_bf16 v[40:43], v[84:87], v[192:199], v10\ns_waitcnt lgkmcnt(5)\nds_read_b128 v[28:31], v11 offset:52224\n" \
"buffer_store_dwordx4 v[92:95], v6, s[36:39], s63 offen sc1\nv_cvt_pk_bf16_f32 v89, v34, v35\nv_cvt_pk_bf16_f32 v90, v36, v37\nv_cvt_pk_bf16_f32 v91, v38, v39\nv_and_or_b32 v90, v90, -2, v12\nv_smfmac_f32_16x16x64_bf16 v[44:47], v[80:83], v[64:71], v10\nds_read_b128 v[64:67], v1 offset:16384\nv_permlane16_swap_b32 v89, v91\n" \
"v_smfmac_f32_16x16x64_bf16 v[44:47], v[84:87], v[248:255], v10\ns_cmp_ge_i32 s40, 63\nds_read_b128 v[68:71], v1 offset:17408\nv_permlane16_swap_b32 v88, v90\ns_cbranch_scc1 .Lvu_last%=\n.Lvu_y%=:\nv_smfmac_f32_16x16x64_bf16 v[48:51], v[72:75], a[8:15], v10\ns_waitcnt lgkmcnt(4)\n" \
"ds_read_b128 v[32:35], v11 offset:54272\nds_read_b128 v[36:39], v11 offset:56320\nv_cvt_pk_bf16_f32 v92, v40, v41\nv_and_or_b32 v92, v92, -2, v12\nv_smfmac_f32_16x16x64_bf16 v[48:51], v[76:79], v[56:63], v10\ns_add_u32 s63, s59, 4096\nbuffer_store_dwordx4 v[88:91], v6, s[36:39], s63 offen sc1\nv_cvt_pk_bf16_f32 v93, v42, v43\n" \
"v_cvt_pk_bf16_f32 v94, v44, v45\nv_cvt_pk_bf16_f32 v95, v46, v47\nv_and_or_b32 v94, v94, -2, v12\nv_smfmac_f32_16x16x64_bf16 v[48:51], v[16:19], a[32:39], v10\ns_add_u32 s40, s40, 1\ns_and_b32 s44, s40, 1\nv_permlane16_swap_b32 v93, v95\nv_smfmac_f32_16x16x64_bf16 v[48:51], v[20:23], a[48:55], v10\n" \
"s_lshl_b32 s55, s44, 22\ns_waitcnt lgkmcnt(5)\nv_permlane16_swap_b32 v92, v94\nv_smfmac_f32_16x16x64_bf16 v[52:55], v[24:27], a[8:15], v10\ns_waitcnt lgkmcnt(4)\ns_add_u32 s63, s59, 135168\nv_smfmac_f32_16x16x64_bf16 v[52:55], v[28:31], v[56:63], v10\ns_waitcnt lgkmcnt(1)\n" \
"buffer_store_dwordx4 v[92:95], v6, s[36:39], s63 offen sc1\nv_mov_b64 v[80:81], 0\nv_mov_b64 v[82:83], 0\nv_mov_b64 v[84:85], 0\nv_mov_b64 v[86:87], 0\nv_smfmac_f32_16x16x64_bf16 v[52:55], v[32:35], a[32:39], v10\ns_memrealtime s[86:87]\ns_bfe_u32 s45, s40, 0x10001\n" \
"s_waitcnt lgkmcnt(0)\nv_smfmac_f32_16x16x64_bf16 v[52:55], v[36:39], a[48:55], v10\nds_read_b128 v[56:59], v1 offset:12288\nv_mov_b32 v13, s45\nv_smfmac_f32_16x16x64_bf16 v[80:83], v[72:75], a[56:63], v10\nds_read_b128 v[60:63], v1 offset:13312\nv_cvt_pk_bf16_f32 v88, v48, v49\nv_smfmac_f32_16x16x64_bf16 v[80:83], v[76:79], a[72:79], v10\n" \
"v_cvt_pk_bf16_f32 v89, v50, v51\nv_smfmac_f32_16x16x64_bf16 v[80:83], v[16:19], v[64:71], v10\nv_cvt_pk_bf16_f32 v90, v52, v53\nv_smfmac_f32_16x16x64_bf16 v[80:83], v[20:23], a[96:103], v10\ns_add_u32 s60, s55, 0\nv_cvt_pk_bf16_f32 v91, v54, v55\nv_smfmac_f32_16x16x64_bf16 v[84:87], v[24:27], a[56:63], v10\nv_and_or_b32 v88, v88, -2, v13\n" \
"v_smfmac_f32_16x16x64_bf16 v[84:87], v[28:31], a[72:79], v10\ns_and_b32 s42, s40, 3\ns_lshl_b32 s42, s42, 15\nv_and_or_b32 v90, v90, -2, v13\nv_smfmac_f32_16x16x64_bf16 v[84:87], v[32:35], v[64:71], v10\nbuffer_store_dwordx4 v[88:91], v7, s[36:39], s60 offen offset:0 sc1\nv_mov_b64 v[48:49], 0\nv_mov_b64 v[50:51], 0\n" \
"v_mov_b64 v[52:53], 0\nv_mov_b32 v12, s44\nv_smfmac_f32_16x16x64_bf16 v[84:87], v[36:39], a[96:103], v10\nds_read_b128 v[64:67], v1 offset:20480\nv_mov_b64 v[54:55], 0\nv_smfmac_f32_16x16x64_bf16 v[48:51], v[72:75], a[112:119], v10\ns_waitcnt lgkmcnt(1)\nds_read_b128 v[68:71], v1 offset:21504\n" \
"v_cvt_pk_bf16_f32 v92, v80, v81\nv_smfmac_f32_16x16x64_bf16 v[48:51], v[76:79], v[56:63], v10\nv_cvt_pk_bf16_f32 v93, v82, v83\nv_smfmac_f32_16x16x64_bf16 v[48:51], v[16:19], a[136:143], v10\nv_cvt_pk_bf16_f32 v94, v84, v85\nv_smfmac_f32_16x16x64_bf16 v[48:51], v[20:23], a[144:151], v10\ns_add_u32 s60, s55, 4096\nv_cvt_pk_bf16_f32 v95, v86, v87\n" \
"v_smfmac_f32_16x16x64_bf16 v[52:55], v[24:27], a[112:119], v10\nv_and_or_b32 v92, v92, -2, v13\nv_smfmac_f32_16x16x64_bf16 v[52:55], v[28:31], v[56:63], v10\nv_and_or_b32 v94, v94, -2, v13\nv_smfmac_f32_16x16x64_bf16 v[52:55], v[32:35], a[136:143], v10\ns_waitcnt vmcnt(5)\nbuffer_store_dwordx4 v[92:95], v7, s[36:39], s60 offen offset:0 sc1\nv_mov_b64 v[80:81], 0\n" \
"v_mov_b64 v[82:83], 0\nv_mov_b64 v[84:85], 0\ns_barrier\nv_smfmac_f32_16x16x64_bf16 v[52:55], v[36:39], a[144:151], v10\nds_read_b128 v[56:59], v1 offset:10240\nv_mov_b64 v[86:87], 0\nv_smfmac_f32_16x16x64_bf16 v[80:83], v[72:75], a[160:167], v10\nds_read_b128 v[60:63], v1 offset:11264\n" \
"v_cvt_pk_bf16_f32 v88, v48, v49\nv_smfmac_f32_16x16x64_bf16 v[80:83], v[76:79], a[176:183], v10\ns_waitcnt lgkmcnt(2)\nv_cvt_pk_bf16_f32 v89, v50, v51\nv_smfmac_f32_16x16x64_bf16 v[80:83], v[16:19], v[64:71], v10\ns_cmp_lt_u32 s40, 2\ns_cselect_b32 s54, %[fy], -2147483648\nv_cvt_pk_bf16_f32 v90, v52, v53\n" \
"v_smfmac_f32_16x16x64_bf16 v[80:83], v[20:23], a[200:207], v10\ns_add_u32 s60, s55, 16384\nv_cvt_pk_bf16_f32 v91, v54, v55\nv_smfmac_f32_16x16x64_bf16 v[84:87], v[24:27], a[160:167], v10\nv_and_or_b32 v88, v88, -2, v13\nv_smfmac_f32_16x16x64_bf16 v[84:87], v[28:31], a[176:183], v10\nv_and_or_b32 v90, v90, -2, v13\nv_smfmac_f32_16x16x64_bf16 v[84:87], v[32:35], v[64:71], v10\n" \
"buffer_store_dwordx4 v[88:91], v7, s[36:39], s60 offen offset:0 sc1\nv_mov_b64 v[48:49], 0\nv_mov_b64 v[50:51], 0\nv_mov_b64 v[52:53], 0\nv_smfmac_f32_16x16x64_bf16 v[84:87], v[36:39], a[200:207], v10\nds_read_b128 v[64:67], v1 offset:18432\nv_mov_b64 v[54:55], 0\nv_smfmac_f32_16x16x64_bf16 v[48:51], v[72:75], a[208:215], v10\n" \
"s_waitcnt lgkmcnt(1)\nds_read_b128 v[68:71], v1 offset:19456\nv_cvt_pk_bf16_f32 v92, v80, v81\nv_smfmac_f32_16x16x64_bf16 v[48:51], v[76:79], v[56:63], v10\nv_cvt_pk_bf16_f32 v93, v82, v83\nv_smfmac_f32_16x16x64_bf16 v[48:51], v[16:19], a[232:239], v10\nv_cvt_pk_bf16_f32 v94, v84, v85\nv_smfmac_f32_16x16x64_bf16 v[48:51], v[20:23], a[248:255], v10\n" \
"s_add_u32 s60, s55, 20480\nv_cvt_pk_bf16_f32 v95, v86, v87\nv_smfmac_f32_16x16x64_bf16 v[52:55], v[24:27], a[208:215], v10\nv_and_or_b32 v92, v92, -2, v13\nv_smfmac_f32_16x16x64_bf16 v[52:55], v[28:31], v[56:63], v10\nv_and_or_b32 v94, v94, -2, v13\nv_smfmac_f32_16x16x64_bf16 v[52:55], v[32:35], a[232:239], v10\nbuffer_store_dwordx4 v[92:95], v7, s[36:39], s60 offen offset:0 sc1\n" \
"v_mov_b64 v[80:81], 0\nv_mov_b64 v[82:83], 0\nv_mov_b64 v[84:85], 0\nv_smfmac_f32_16x16x64_bf16 v[52:55], v[36:39], a[248:255], v10\nds_read_b128 v[56:59], v1 offset:14336\nv_mov_b64 v[86:87], 0\nv_smfmac_f32_16x16x64_bf16 v[80:83], v[72:75], v[104:111], v10\nds_read_b128 v[60:63], v1 offset:15360\n" \
"v_cvt_pk_bf16_f32 v88, v48, v49\nv_smfmac_f32_16x16x64_bf16 v[80:83], v[76:79], v[120:127], v10\ns_waitcnt lgkmcnt(2)\nv_cvt_pk_bf16_f32 v89, v50, v51\nv_smfmac_f32_16x16x64_bf16 v[80:83], v[16:19], v[64:71], v10\nv_cvt_pk_bf16_f32 v90, v52, v53\nv_smfmac_f32_16x16x64_bf16 v[80:83], v[20:23], v[136:143], v10\ns_add_u32 s60, s55, 0\n" \
"v_cvt_pk_bf16_f32 v91, v54, v55\nv_smfmac_f32_16x16x64_bf16 v[84:87], v[24:27], v[104:111], v10\nv_and_or_b32 v88, v88, -2, v13\nv_smfmac_f32_16x16x64_bf16 v[84:87], v[28:31], v[120:127], v10\nv_and_or_b32 v90, v90, -2, v13\nv_smfmac_f32_16x16x64_bf16 v[84:87], v[32:35], v[64:71], v10\nbuffer_store_dwordx4 v[88:91], v7, s[36:39], s60 offen offset:2048 sc1\nv_xor_b32 v11, 128, v11\n" \
"v_mov_b64 v[48:49], 0\nv_mov_b64 v[50:51], 0\nv_mov_b64 v[52:53], 0\nv_smfmac_f32_16x16x64_bf16 v[84:87], v[36:39], v[136:143], v10\nds_read_b128 v[64:67], v1 offset:22528\nv_mov_b64 v[54:55], 0\nv_smfmac_f32_16x16x64_bf16 v[48:51], v[72:75], v[152:159], v10\ns_waitcnt lgkmcnt(1)\n" \
"ds_read_b128 v[68:71], v1 offset:23552\nv_cvt_pk_bf16_f32 v92, v80, v81\nv_smfmac_f32_16x16x64_bf16 v[48:51], v[76:79], v[56:63], v10\nds_read_b128 v[40:43], v11 offset:49152\nv_cvt_pk_bf16_f32 v93, v82, v83\nv_smfmac_f32_16x16x64_bf16 v[48:51], v[16:19], v[176:183], v10\nds_read_b128 v[44:47], v11 offset:51200\nv_cvt_pk_bf16_f32 v94, v84, v85\n" \
"v_smfmac_f32_16x16x64_bf16 v[48:51], v[20:23], v[192:199], v10\ns_add_u32 s60, s55, 4096\nv_cvt_pk_bf16_f32 v95, v86, v87\nv_smfmac_f32_16x16x64_bf16 v[52:55], v[24:27], v[152:159], v10\nv_and_or_b32 v92, v92, -2, v13\nv_smfmac_f32_16x16x64_bf16 v[52:55], v[28:31], v[56:63], v10\nv_and_or_b32 v94, v94, -2, v13\nv_smfmac_f32_16x16x64_bf16 v[52:55], v[32:35], v[176:183], v10\n" \
"buffer_store_dwordx4 v[92:95], v7, s[36:39], s60 offen offset:2048 sc1\nv_mov_b64 v[80:81], 0\nv_mov_b64 v[82:83], 0\nv_mov_b64 v[84:85], 0\nv_smfmac_f32_16x16x64_bf16 v[52:55], v[36:39], v[192:199], v10\nds_read_b128 v[56:59], v1 offset:8192\nv_mov_b64 v[86:87], 0\nv_smfmac_f32_16x16x64_bf16 v[80:83], v[72:75], v[208:215], v10\n" \
"ds_read_b128 v[60:63], v1 offset:9216\nds_read_b128 v[72:75], v11 offset:53248\nv_cvt_pk_bf16_f32 v88, v48, v49\nv_smfmac_f32_16x16x64_bf16 v[80:83], v[76:79], v[224:231], v10\ns_waitcnt lgkmcnt(5)\nds_read_b128 v[76:79], v11 offset:55296\nv_cvt_pk_bf16_f32 v89, v50, v51\nv_smfmac_f32_16x16x64_bf16 v[80:83], v[16:19], v[64:71], v10\n" \
"ds_read_b128 v[16:19], v11 offset:50176\nv_cvt_pk_bf16_f32 v90, v52, v53\nv_smfmac_f32_16x16x64_bf16 v[80:83], v[20:23], v[248:255], v10\ns_add_u32 s60, s55, 16384\nds_read_b128 v[20:23], v11 offset:52224\nv_cvt_pk_bf16_f32 v91, v54, v55\nv_smfmac_f32_16x16x64_bf16 v[84:87], v[24:27], v[208:215], v10\nds_read_b128 v[24:27], v11 offset:54272\n" \
"v_and_or_b32 v88, v88, -2, v13\nv_smfmac_f32_16x16x64_bf16 v[84:87], v[28:31], v[224:231], v10\nds_read_b128 v[28:31], v11 offset:56320\nv_and_or_b32 v90, v90, -2, v13\nv_smfmac_f32_16x16x64_bf16 v[84:87], v[32:35], v[64:71], v10\nbuffer_store_dwordx4 v[88:91], v7, s[36:39], s60 offen offset:2048 sc1\nv_mov_b64 v[48:49], 0\nv_mov_b64 v[50:51], 0\n" \
"v_mov_b64 v[52:53], 0\nv_smfmac_f32_16x16x64_bf16 v[84:87], v[36:39], v[248:255], v10\ns_waitcnt lgkmcnt(9)\nds_read_b128 v[64:67], v1 offset:16384\nv_mov_b64 v[54:55], 0\nv_smfmac_f32_16x16x64_bf16 v[48:51], v[40:43], a[8:15], v10\ns_waitcnt lgkmcnt(9)\ns_waitcnt lgkmcnt(7)\n" \
"ds_read_b128 v[68:71], v1 offset:17408\nv_cvt_pk_bf16_f32 v92, v80, v81\nv_smfmac_f32_16x16x64_bf16 v[48:51], v[44:47], v[56:63], v10\ns_waitcnt lgkmcnt(7)\nv_cvt_pk_bf16_f32 v93, v82, v83\nv_smfmac_f32_16x16x64_bf16 v[48:51], v[72:75], a[32:39], v10\ns_waitcnt lgkmcnt(6)\nv_cvt_pk_bf16_f32 v94, v84, v85\n" \
"v_smfmac_f32_16x16x64_bf16 v[48:51], v[76:79], a[48:55], v10\ns_add_u32 s60, s55, 20480\ns_waitcnt lgkmcnt(5)\nv_cvt_pk_bf16_f32 v95, v86, v87\nv_smfmac_f32_16x16x64_bf16 v[52:55], v[16:19], a[8:15], v10\ns_waitcnt lgkmcnt(4)\nv_and_or_b32 v92, v92, -2, v13\nv_smfmac_f32_16x16x64_bf16 v[52:55], v[20:23], v[56:63], v10\n" \
"s_waitcnt lgkmcnt(3)\nv_and_or_b32 v94, v94, -2, v13\nv_smfmac_f32_16x16x64_bf16 v[52:55], v[24:27], a[32:39], v10\ns_waitcnt lgkmcnt(2)\nbuffer_store_dwordx4 v[92:95], v7, s[36:39], s60 offen offset:2048 sc1\nv_xor_b32 v11, 128, v11\nv_mov_b64 v[80:81], 0\nv_mov_b64 v[82:83], 0\n" \
"v_mov_b64 v[84:85], 0\nv_smfmac_f32_16x16x64_bf16 v[52:55], v[28:31], a[48:55], v10\nds_read_b128 v[56:59], v1 offset:12288\nv_mov_b64 v[86:87], 0\nv_smfmac_f32_16x16x64_bf16 v[80:83], v[40:43], a[56:63], v10\nds_read_b128 v[60:63], v1 offset:13312\nv_cvt_pk_bf16_f32 v88, v48, v49\nv_smfmac_f32_16x16x64_bf16 v[80:83], v[44:47], a[72:79], v10\n" \
"s_waitcnt lgkmcnt(2)\nv_cvt_pk_bf16_f32 v89, v50, v51\nv_smfmac_f32_16x16x64_bf16 v[80:83], v[72:75], v[64:71], v10\nv_cvt_pk_bf16_f32 v90, v52, v53\nv_smfmac_f32_16x16x64_bf16 v[80:83], v[76:79], a[96:103], v10\ns_add_u32 s60, s55, 0\nv_cvt_pk_bf16_f32 v91, v54, v55\nv_smfmac_f32_16x16x64_bf16 v[84:87], v[16:19], a[56:63], v10\n" \
"v_and_or_b32 v88, v88, -2, v13\nv_smfmac_f32_16x16x64_bf16 v[84:87], v[20:23], a[72:79], v10\nv_and_or_b32 v90, v90, -2, v13\nv_smfmac_f32_16x16x64_bf16 v[84:87], v[24:27], v[64:71], v10\nbuffer_store_dwordx4 v[88:91], v7, s[36:39], s60 offen offset:512 sc1\nv_mov_b64 v[48:49], 0\nv_mov_b64 v[50:51], 0\nv_mov_b64 v[52:53], 0\n" \
"v_smfmac_f32_16x16x64_bf16 v[84:87], v[28:31], a[96:103], v10\nds_read_b128 v[64:67], v1 offset:20480\nv_mov_b64 v[54:55], 0\nv_smfmac_f32_16x16x64_bf16 v[48:51], v[40:43], a[112:119], v10\ns_waitcnt lgkmcnt(1)\nds_read_b128 v[68:71], v1 offset:21504\nv_cvt_pk_bf16_f32 v92, v80, v81\nv_smfmac_f32_16x16x64_bf16 v[48:51], v[44:47], v[56:63], v10\n" \
"v_cvt_pk_bf16_f32 v93, v82, v83\nv_smfmac_f32_16x16x64_bf16 v[48:51], v[72:75], a[136:143], v10\nv_cvt_pk_bf16_f32 v94, v84, v85\nv_smfmac_f32_16x16x64_bf16 v[48:51], v[76:79], a[144:151], v10\ns_add_u32 s60, s55, 4096\nv_cvt_pk_bf16_f32 v95, v86, v87\nv_smfmac_f32_16x16x64_bf16 v[52:55], v[16:19], a[112:119], v10\nv_and_or_b32 v92, v92, -2, v13\n" \
"v_smfmac_f32_16x16x64_bf16 v[52:55], v[20:23], v[56:63], v10\nv_and_or_b32 v94, v94, -2, v13\nv_smfmac_f32_16x16x64_bf16 v[52:55], v[24:27], a[136:143], v10\nbuffer_store_dwordx4 v[92:95], v7, s[36:39], s60 offen offset:512 sc1\nv_mov_b64 v[80:81], 0\nv_mov_b64 v[82:83], 0\nv_mov_b64 v[84:85], 0\nv_smfmac_f32_16x16x64_bf16 v[52:55], v[28:31], a[144:151], v10\n" \
"ds_read_b128 v[56:59], v1 offset:10240\nv_mov_b64 v[86:87], 0\nv_smfmac_f32_16x16x64_bf16 v[80:83], v[40:43], a[160:167], v10\nds_read_b128 v[60:63], v1 offset:11264\nv_cvt_pk_bf16_f32 v88, v48, v49\nv_smfmac_f32_16x16x64_bf16 v[80:83], v[44:47], a[176:183], v10\ns_waitcnt lgkmcnt(2)\nv_cvt_pk_bf16_f32 v89, v50, v51\n" \
"v_smfmac_f32_16x16x64_bf16 v[80:83], v[72:75], v[64:71], v10\nv_cvt_pk_bf16_f32 v90, v52, v53\nv_smfmac_f32_16x16x64_bf16 v[80:83], v[76:79], a[200:207], v10\ns_add_u32 s60, s55, 16384\nv_cvt_pk_bf16_f32 v91, v54, v55\nv_smfmac_f32_16x16x64_bf16 v[84:87], v[16:19], a[160:167], v10\nv_and_or_b32 v88, v88, -2, v13\nv_smfmac_f32_16x16x64_bf16 v[84:87], v[20:23], a[176:183], v10\n" \
"v_and_or_b32 v90, v90, -2, v13\nv_smfmac_f32_16x16x64_bf16 v[84:87], v[24:27], v[64:71], v10\nbuffer_store_dwordx4 v[88:91], v7, s[36:39], s60 offen offset:512 sc1\nv_mov_b64 v[48:49], 0\nv_mov_b64 v[50:51], 0\nv_mov_b64 v[52:53], 0\nv_smfmac_f32_16x16x64_bf16 v[84:87], v[28:31], a[200:207], v10\nds_read_b128 v[64:67], v1 offset:18432\n" \
"v_mov_b64 v[54:55], 0\nv_smfmac_f32_16x16x64_bf16 v[48:51], v[40:43], a[208:215], v10\ns_waitcnt lgkmcnt(1)\nds_read_b128 v[68:71], v1 offset:19456\nv_cvt_pk_bf16_f32 v92, v80, v81\nv_smfmac_f32_16x16x64_bf16 v[48:51], v[44:47], v[56:63], v10\nv_cvt_pk_bf16_f32 v93, v82, v83\nv_smfmac_f32_16x16x64_bf16 v[48:51], v[72:75], a[232:239], v10\n" \
"v_cvt_pk_bf16_f32 v94, v84, v85\nv_smfmac_f32_16x16x64_bf16 v[48:51], v[76:79], a[248:255], v10\ns_add_u32 s60, s55, 20480\nv_cvt_pk_bf16_f32 v95, v86, v87\nv_smfmac_f32_16x16x64_bf16 v[52:55], v[16:19], a[208:215], v10\nv_and_or_b32 v92, v92, -2, v13\nv_smfmac_f32_16x16x64_bf16 v[52:55], v[20:23], v[56:63], v10\nv_and_or_b32 v94, v94, -2, v13\n" \
"v_smfmac_f32_16x16x64_bf16 v[52:55], v[24:27], a[232:239], v10\nbuffer_store_dwordx4 v[92:95], v7, s[36:39], s60 offen offset:512 sc1\nv_mov_b64 v[80:81], 0\nv_mov_b64 v[82:83], 0\nv_mov_b64 v[84:85], 0\nv_smfmac_f32_16x16x64_bf16 v[52:55], v[28:31], a[248:255], v10\nds_read_b128 v[56:59], v1 offset:14336\nv_mov_b64 v[86:87], 0\n" \
"v_smfmac_f32_16x16x64_bf16 v[80:83], v[40:43], v[104:111], v10\nds_read_b128 v[60:63], v1 offset:15360\nv_cvt_pk_bf16_f32 v88, v48, v49\nv_smfmac_f32_16x16x64_bf16 v[80:83], v[44:47], v[120:127], v10\ns_waitcnt lgkmcnt(2)\nv_cvt_pk_bf16_f32 v89, v50, v51\nv_smfmac_f32_16x16x64_bf16 v[80:83], v[72:75], v[64:71], v10\nv_cvt_pk_bf16_f32 v90, v52, v53\n" \
"v_smfmac_f32_16x16x64_bf16 v[80:83], v[76:79], v[136:143], v10\ns_add_u32 s60, s55, 0\nv_cvt_pk_bf16_f32 v91, v54, v55\nv_smfmac_f32_16x16x64_bf16 v[84:87], v[16:19], v[104:111], v10\nv_and_or_b32 v88, v88, -2, v13\nv_smfmac_f32_16x16x64_bf16 v[84:87], v[20:23], v[120:127], v10\nv_and_or_b32 v90, v90, -2, v13\nv_smfmac_f32_16x16x64_bf16 v[84:87], v[24:27], v[64:71], v10\n" \
"buffer_store_dwordx4 v[88:91], v7, s[36:39], s60 offen offset:2560 sc1\nv_mov_b64 v[48:49], 0\nv_mov_b64 v[50:51], 0\nv_mov_b64 v[52:53], 0\nv_smfmac_f32_16x16x64_bf16 v[84:87], v[28:31], v[136:143], v10\nds_read_b128 v[64:67], v1 offset:22528\nv_mov_b64 v[54:55], 0\nv_smfmac_f32_16x16x64_bf16 v[48:51], v[40:43], v[152:159], v10\n" \
"s_waitcnt lgkmcnt(1)\nds_read_b128 v[68:71], v1 offset:23552\nv_cvt_pk_bf16_f32 v92, v80, v81\nv_smfmac_f32_16x16x64_bf16 v[48:51], v[44:47], v[56:63], v10\nv_cvt_pk_bf16_f32 v93, v82, v83\nv_smfmac_f32_16x16x64_bf16 v[48:51], v[72:75], v[176:183], v10\nv_cvt_pk_bf16_f32 v94, v84, v85\nv_smfmac_f32_16x16x64_bf16 v[48:51], v[76:79], v[192:199], v10\n" \
"s_add_u32 s60, s55, 4096\nv_cvt_pk_bf16_f32 v95, v86, v87\nv_smfmac_f32_16x16x64_bf16 v[52:55], v[16:19], v[152:159], v10\nv_and_or_b32 v92, v92, -2, v13\nv_smfmac_f32_16x16x64_bf16 v[52:55], v[20:23], v[56:63], v10\nv_and_or_b32 v94, v94, -2, v13\nv_smfmac_f32_16x16x64_bf16 v[52:55], v[24:27], v[176:183], v10\nbuffer_store_dwordx4 v[92:95], v7, s[36:39], s60 offen offset:2560 sc1\n" \
"v_mov_b64 v[80:81], 0\nv_mov_b64 v[82:83], 0\nv_mov_b64 v[84:85], 0\nv_smfmac_f32_16x16x64_bf16 v[52:55], v[28:31], v[192:199], v10\nds_read_b128 v[32:35], v2 offset:0\nv_mov_b64 v[86:87], 0\nv_smfmac_f32_16x16x64_bf16 v[80:83], v[40:43], v[208:215], v10\nds_read_b128 v[36:39], v2 offset:2048\n" \
"v_cvt_pk_bf16_f32 v88, v48, v49\nv_smfmac_f32_16x16x64_bf16 v[80:83], v[44:47], v[224:231], v10\ns_waitcnt lgkmcnt(2)\nv_cvt_pk_bf16_f32 v89, v50, v51\nv_smfmac_f32_16x16x64_bf16 v[80:83], v[72:75], v[64:71], v10\nds_read_b128 v[40:43], v2 offset:256\nv_cvt_pk_bf16_f32 v90, v52, v53\nv_smfmac_f32_16x16x64_bf16 v[80:83], v[76:79], v[248:255], v10\n" \
"s_add_u32 s60, s55, 16384\nds_read_b128 v[44:47], v2 offset:2304\nv_cvt_pk_bf16_f32 v91, v54, v55\nv_smfmac_f32_16x16x64_bf16 v[84:87], v[16:19], v[208:215], v10\nv_and_or_b32 v88, v88, -2, v13\nv_smfmac_f32_16x16x64_bf16 v[84:87], v[20:23], v[224:231], v10\nv_and_or_b32 v90, v90, -2, v13\nv_smfmac_f32_16x16x64_bf16 v[84:87], v[24:27], v[64:71], v10\n" \
"buffer_store_dwordx4 v[88:91], v7, s[36:39], s60 offen offset:2560 sc1\nv_mov_b64 v[48:49], 0\nv_mov_b64 v[50:51], 0\nv_mov_b64 v[52:53], 0\nv_mov_b32 v15, s40\nv_smfmac_f32_16x16x64_bf16 v[84:87], v[28:31], v[248:255], v10\ns_add_u32 s60, s55, 20480\ns_cmp_eq_u64 s[64:65], 0\n" \
"v_mov_b64 v[54:55], 0\ns_cbranch_scc1 .Lvu_tok%=\nv_cvt_pk_bf16_f32 v92, v80, v81\nv_cvt_pk_bf16_f32 v93, v82, v83\ns_nop 1\nv_cvt_pk_bf16_f32 v94, v84, v85\nv_cvt_pk_bf16_f32 v95, v86, v87\nv_and_or_b32 v92, v92, -2, v13\n" \
"v_and_or_b32 v94, v94, -2, v13\nbuffer_store_dwordx4 v[92:95], v7, s[36:39], s60 offen offset:2560 sc1\ns_mov_b32 s60, -2147483648\ns_cmp_lt_i32 s40, 63\ns_cbranch_scc1 .Lvu_f0%=\ns_mov_b32 s61, -2147483648\nv_add_u32 v14, s57, v0\ns_waitcnt lgkmcnt(0)\n" \
".Lvu_nov%=:\ns_add_u32 m0, s57, 16384\ns_nop 0\nbuffer_load_dwordx4 v3, s[36:39], s42 offen offset:0 sc1 lds\nbuffer_load_dwordx4 v3, s[36:39], s42 offen offset:1024 sc1 lds\nbuffer_load_dwordx4 v3, s[36:39], s42 offen offset:2048 sc1 lds\nbuffer_load_dwordx4 v3, s[36:39], s42 offen offset:3072 sc1 lds\ns_waitcnt vmcnt(0)\n" \
"ds_read_b128 v[80:83], v14 offset:16384\nds_read_b128 v[84:87], v14 offset:17408\nds_read_b128 v[88:91], v14 offset:18432\nds_read_b128 v[92:95], v14 offset:19456\ns_waitcnt lgkmcnt(2)\nv_max3_u32 v76, v80, v82, v84\ns_waitcnt lgkmcnt(1)\nv_max3_u32 v76, v76, v86, v88\n" \
"s_waitcnt lgkmcnt(0)\nv_max3_u32 v76, v76, v90, v92\nv_max_u32 v76, v76, v94\nv_cmp_eq_u32 vcc, -1, v76\ns_cbranch_vccz .Lvu_novfok%=\ns_add_u32 s43, s43, 1\ns_cmp_lt_u32 s43, 262144\ns_cbranch_scc1 .Lvu_nov%=\n" \
"s_branch .Lvu_abort%=\n.Lvu_novfok%=:\ns_waitcnt lgkmcnt(0)\ns_barrier\ns_memrealtime s[68:69]\nds_read_b128 v[56:59], v1 offset:0\nds_read_b128 v[60:63], v1 offset:1024\nds_read_b128 v[80:83], v0 offset:16384\n" \
"ds_read_b128 v[84:87], v0 offset:17408\nds_read_b128 v[88:91], v0 offset:18432\nds_read_b128 v[92:95], v0 offset:19456\nds_read_b128 v[16:19], v0 offset:20480\nds_read_b128 v[20:23], v0 offset:21504\nds_read_b128 v[40:43], v0 offset:22528\nds_read_b128 v[44:47], v0 offset:23552\ns_waitcnt lgkmcnt(0)\n" \
"s_branch .Lvu_crit%=\n.Lvu_re%=:\n.Lvu_re2%=:\ns_add_u32 m0, s57, 16384\nv_max_u32 v76, v80, v82\nv_cmp_eq_u32 vcc, -1, v76\ns_cbranch_vccz .Lvu_sk1897%=\ns_mov_b64 exec, vcc\n" \
"buffer_load_dwordx4 v3, s[36:39], s42 offen offset:0 sc1 lds\ns_mov_b64 exec, -1\n.Lvu_sk1897%=:\nv_max_u32 v76, v84, v86\nv_cmp_eq_u32 vcc, -1, v76\ns_cbranch_vccz .Lvu_sk1904%=\ns_mov_b64 exec, vcc\nbuffer_load_dwordx4 v3, s[36:39], s42 offen offset:1024 sc1 lds\n" \
"s_mov_b64 exec, -1\n.Lvu_sk1904%=:\nv_max_u32 v76, v88, v90\nv_cmp_eq_u32 vcc, -1, v76\ns_cbranch_vccz .Lvu_sk1911%=\ns_mov_b64 exec, vcc\nbuffer_load_dwordx4 v3, s[36:39], s42 offen offset:2048 sc1 lds\ns_mov_b64 exec, -1\n" \
".Lvu_sk1911%=:\nv_max_u32 v76, v92, v94\nv_cmp_eq_u32 vcc, -1, v76\ns_cbranch_vccz .Lvu_sk1918%=\ns_mov_b64 exec, vcc\nbuffer_load_dwordx4 v3, s[36:39], s42 offen offset:3072 sc1 lds\ns_mov_b64 exec, -1\n.Lvu_sk1918%=:\n" \
".Lvu_skf%=:\ns_waitcnt vmcnt(0)\nds_read_b128 v[80:83], v14 offset:16384\nds_read_b128 v[84:87], v14 offset:17408\nds_read_b128 v[88:91], v14 offset:18432\nds_read_b128 v[92:95], v14 offset:19456\ns_waitcnt lgkmcnt(2)\nv_max3_u32 v76, v80, v82, v84\n" \
"s_waitcnt lgkmcnt(1)\nv_max3_u32 v76, v76, v86, v88\ns_waitcnt lgkmcnt(0)\nv_max3_u32 v76, v76, v90, v92\nv_max_u32 v76, v76, v94\nv_cmp_eq_u32 vcc, -1, v76\ns_cbranch_vccz .Lvu_rer%=\ns_add_u32 s43, s43, 1\n" \
"s_cmp_lt_u32 s43, 262144\ns_cbranch_scc1 .Lvu_re2%=\ns_branch .Lvu_abort%=\n.Lvu_rew%=:\nds_read_b128 v[80:83], v14 offset:16384\nds_read_b128 v[84:87], v14 offset:17408\nds_read_b128 v[88:91], v14 offset:18432\nds_read_b128 v[92:95], v14 offset:19456\n" \
"s_waitcnt lgkmcnt(0)\ns_branch .Lvu_rer%=\n.Lvu_first%=:\ns_nop 7\nv_cvt_pk_bf16_f32 v76, v24, v25\nv_cvt_pk_bf16_f32 v77, v26, v27\nv_cvt_pk_bf16_f32 v78, v28, v29\nv_cvt_pk_bf16_f32 v79, v30, v31\n" \
"ds_write2st64_b64 v9, v[76:77], v[78:79] offset0:2 offset1:10\ns_waitcnt vmcnt(0) lgkmcnt(0)\ns_barrier\nds_read_b128 v[56:59], v1 offset:8192\nds_read_b128 v[60:63], v1 offset:9216\nds_read_b128 v[64:67], v1 offset:16384\nds_read_b128 v[68:71], v1 offset:17408\nds_read_b128 v[72:75], v11 offset:49152\n" \
"ds_read_b128 v[76:79], v11 offset:51200\nds_read_b128 v[16:19], v11 offset:53248\nds_read_b128 v[20:23], v11 offset:55296\nds_read_b128 v[24:27], v11 offset:50176\nds_read_b128 v[28:31], v11 offset:52224\nds_read_b128 v[32:35], v11 offset:54272\nds_read_b128 v[36:39], v11 offset:56320\ns_waitcnt lgkmcnt(0)\n" \
"s_add_u32 m0, s57, 0\ns_add_u32 s58, s61, 0\nbuffer_load_dwordx4 v0, s[36:39], s58 offen offset:0 sc1 lds\nbuffer_load_dwordx4 v0, s[36:39], s58 offen offset:1024 sc1 lds\nbuffer_load_dwordx4 v0, s[36:39], s58 offen offset:2048 sc1 lds\nbuffer_load_dwordx4 v0, s[36:39], s58 offen offset:3072 sc1 lds\ns_mov_b32 s59, -2147483648\nbuffer_store_dword v14, off, s[36:39], s59\n" \
"buffer_store_dword v14, off, s[36:39], s59\nbuffer_store_dword v14, off, s[36:39], s59\ns_branch .Lvu_y%=\n.Lvu_last%=:\ns_nop 7\nv_cvt_pk_bf16_f32 v92, v40, v41\nv_and_or_b32 v92, v92, -2, v12\ns_add_u32 s63, s59, 4096\n" \
"buffer_store_dwordx4 v[88:91], v6, s[36:39], s63 offen sc1\nv_cvt_pk_bf16_f32 v93, v42, v43\nv_cvt_pk_bf16_f32 v94, v44, v45\nv_cvt_pk_bf16_f32 v95, v46, v47\nv_and_or_b32 v94, v94, -2, v12\ns_nop 0\nv_permlane16_swap_b32 v93, v95\nv_permlane16_swap_b32 v92, v94\n" \
"s_add_u32 s63, s59, 135168\nbuffer_store_dwordx4 v[92:95], v6, s[36:39], s63 offen sc1\ns_branch .Lvu_end%=\n.Lvu_abort%=:\ns_mov_b64 exec, -1\nv_mov_b32 v16, 0\ns_movk_i32 s44, 6400\nbuffer_store_dword v16, off, s[36:39], s44 sc1\n" \
".Lvu_end%=:\ns_waitcnt vmcnt(0) lgkmcnt(0)\n" \
""
#define RD_ASM \
"v_mbcnt_lo_u32_b32 v255, -1, 0\nv_mbcnt_hi_u32_b32 v255, -1, v255\nv_lshlrev_b32 v255, 2, v255\nv_add_u32 v255, %[tb], v255\nds_read_b32 v196, v255 offset:0\nds_read_b32 v197, v255 offset:256\nds_read_b32 v198, v255 offset:512\nds_read_b32 v199, v255 offset:768\n" \
"ds_read_b32 v200, v255 offset:1024\nds_read_b32 v201, v255 offset:1280\nds_read_b32 v202, v255 offset:1536\nds_read_b32 v203, v255 offset:1792\nds_read_b32 v204, v255 offset:2048\nds_read_b32 v205, v255 offset:2304\nds_read_b32 v206, v255 offset:2560\nds_read_b32 v207, v255 offset:2816\n" \
"s_waitcnt lgkmcnt(0)\nv_accvgpr_write_b32 a32, v206\nv_accvgpr_write_b32 a33, v207\ns_mov_b32 s40, 0\ns_mov_b32 s43, 0\ns_add_u32 s57, %[su], 4096\ns_add_u32 s56, %[sp], 4096\ns_mov_b32 s41, %[sy]\n" \
"s_mov_b32 s59, -2147483648\n.Lrd_yflag%=:\nv_accvgpr_read_b32 v206, a32\nbuffer_load_dword v208, v206, %[rs], 0 offen sc1\ns_waitcnt vmcnt(0)\nv_cmp_gt_i32 vcc, s40, v208\ns_cbranch_vccz .Lrd_yld%=\ns_add_u32 s43, s43, 1\n" \
"s_cmp_lt_u32 s43, 262144\ns_cbranch_scc1 .Lrd_yflag%=\ns_branch .Lrd_abort%=\n.Lrd_yld%=:\ns_add_u32 s60, s41, 0\nbuffer_load_dwordx4 v[0:3], v196, %[rs], s60 offen offset:0 sc1\ns_add_u32 s60, s41, 0\nbuffer_load_dwordx4 v[32:35], v196, %[rs], s60 offen offset:1024 sc1\n" \
"s_add_u32 s60, s41, 0\nbuffer_load_dwordx4 v[64:67], v196, %[rs], s60 offen offset:2048 sc1\ns_add_u32 s60, s41, 131072\nbuffer_load_dwordx4 v[4:7], v196, %[rs], s60 offen offset:0 sc1\ns_add_u32 s60, s41, 131072\nbuffer_load_dwordx4 v[36:39], v196, %[rs], s60 offen offset:1024 sc1\ns_add_u32 s60, s41, 131072\nbuffer_load_dwordx4 v[68:71], v196, %[rs], s60 offen offset:2048 sc1\n" \
"s_add_u32 s60, s41, 262144\nbuffer_load_dwordx4 v[8:11], v196, %[rs], s60 offen offset:0 sc1\ns_add_u32 s60, s41, 262144\nbuffer_load_dwordx4 v[40:43], v196, %[rs], s60 offen offset:1024 sc1\ns_add_u32 s60, s41, 262144\nbuffer_load_dwordx4 v[72:75], v196, %[rs], s60 offen offset:2048 sc1\ns_add_u32 s60, s41, 393216\nbuffer_load_dwordx4 v[12:15], v196, %[rs], s60 offen offset:0 sc1\n" \
"s_add_u32 s60, s41, 393216\nbuffer_load_dwordx4 v[44:47], v196, %[rs], s60 offen offset:1024 sc1\ns_add_u32 s60, s41, 393216\nbuffer_load_dwordx4 v[76:79], v196, %[rs], s60 offen offset:2048 sc1\ns_add_u32 s60, s41, 524288\nbuffer_load_dwordx4 v[16:19], v196, %[rs], s60 offen offset:0 sc1\ns_add_u32 s60, s41, 524288\nbuffer_load_dwordx4 v[48:51], v196, %[rs], s60 offen offset:1024 sc1\n" \
"s_add_u32 s60, s41, 524288\nbuffer_load_dwordx4 v[80:83], v196, %[rs], s60 offen offset:2048 sc1\ns_add_u32 s60, s41, 655360\nbuffer_load_dwordx4 v[20:23], v196, %[rs], s60 offen offset:0 sc1\ns_add_u32 s60, s41, 655360\nbuffer_load_dwordx4 v[52:55], v196, %[rs], s60 offen offset:1024 sc1\ns_add_u32 s60, s41, 655360\nbuffer_load_dwordx4 v[84:87], v196, %[rs], s60 offen offset:2048 sc1\n" \
"s_add_u32 s60, s41, 786432\nbuffer_load_dwordx4 v[24:27], v196, %[rs], s60 offen offset:0 sc1\ns_add_u32 s60, s41, 786432\nbuffer_load_dwordx4 v[56:59], v196, %[rs], s60 offen offset:1024 sc1\ns_add_u32 s60, s41, 786432\nbuffer_load_dwordx4 v[88:91], v196, %[rs], s60 offen offset:2048 sc1\ns_add_u32 s60, s41, 917504\nbuffer_load_dwordx4 v[28:31], v196, %[rs], s60 offen offset:0 sc1\n" \
"s_add_u32 s60, s41, 917504\nbuffer_load_dwordx4 v[60:63], v196, %[rs], s60 offen offset:1024 sc1\ns_add_u32 s60, s41, 917504\nbuffer_load_dwordx4 v[92:95], v196, %[rs], s60 offen offset:2048 sc1\ns_add_u32 s60, s41, 0\nbuffer_load_dwordx4 v[96:99], v196, %[rs], s60 offen offset:3072 sc1\ns_add_u32 s60, s41, 131072\nbuffer_load_dwordx4 v[100:103], v196, %[rs], s60 offen offset:3072 sc1\n" \
"s_add_u32 s60, s41, 262144\nbuffer_load_dwordx4 v[104:107], v196, %[rs], s60 offen offset:3072 sc1\ns_add_u32 s60, s41, 393216\nbuffer_load_dwordx4 v[108:111], v196, %[rs], s60 offen offset:3072 sc1\ns_add_u32 s60, s41, 524288\nbuffer_load_dwordx4 v[112:115], v196, %[rs], s60 offen offset:3072 sc1\ns_add_u32 s60, s41, 655360\nbuffer_load_dwordx4 v[116:119], v196, %[rs], s60 offen offset:3072 sc1\n" \
"s_add_u32 s60, s41, 786432\nbuffer_load_dwordx4 v[120:123], v196, %[rs], s60 offen offset:3072 sc1\ns_add_u32 s60, s41, 917504\nbuffer_load_dwordx4 v[124:127], v196, %[rs], s60 offen offset:3072 sc1\ns_mov_b64 exec, -1\nv_accvgpr_read_b32 v212, a33\nbuffer_load_dwordx2 v[214:215], v212, %[rs], 0 offen sc1\ns_waitcnt vmcnt(0)\n" \
"s_branch .Lrd_ychk%=\n.Lrd_u2%=:\nbuffer_load_dwordx4 v[136:139], v196, %[rs], %[su] offen offset:2048 sc1\nbuffer_load_dwordx4 v[140:143], v196, %[rs], %[su] offen offset:3072 sc1\ns_add_u32 s43, s43, 1\ns_cmp_ge_u32 s43, 262144\ns_cbranch_scc1 .Lrd_abort%=\ns_waitcnt vmcnt(0)\n" \
"v_bitop3_b32 v208, v136, v138, s54 bitop3:0x7e\nv_bitop3_b32 v209, v140, v142, s54 bitop3:0x7e\nv_bitop3_b32 v208, v208, 1, v209 bitop3:0xc8\nv_cmp_ne_u32 vcc, 0, v208\ns_cbranch_vccnz .Lrd_u2%=\n.Lrd_u3%=:\nbuffer_load_dwordx4 v[128:131], v196, %[rs], %[su] offen offset:0 sc1\nbuffer_load_dwordx4 v[132:135], v196, %[rs], %[su] offen offset:1024 sc1\n" \
"buffer_load_dwordx4 v[144:147], v196, %[rs], s57 offen offset:0 sc1\nbuffer_load_dwordx4 v[148:151], v196, %[rs], s57 offen offset:1024 sc1\nbuffer_load_dwordx4 v[152:155], v196, %[rs], s57 offen offset:2048 sc1\nbuffer_load_dwordx4 v[156:159], v196, %[rs], s57 offen offset:3072 sc1\nbuffer_load_dwordx4 v[160:163], v196, %[rs], %[sp] offen offset:0 sc1\nbuffer_load_dwordx4 v[164:167], v196, %[rs], %[sp] offen offset:1024 sc1\nbuffer_load_dwordx4 v[168:171], v196, %[rs], %[sp] offen offset:2048 sc1\nbuffer_load_dwordx4 v[172:175], v196, %[rs], %[sp] offen offset:3072 sc1\n" \
"buffer_load_dwordx4 v[176:179], v196, %[rs], s56 offen offset:0 sc1\n.p2align 6\n.Lrd_ra%=:\ns_waitcnt vmcnt(10)\nv_bitop3_b32 v208, v128, v130, s54 bitop3:0x7e\nv_bitop3_b32 v209, v136, v138, s54 bitop3:0x7e\nv_bitop3_b32 v208, v208, 1, v209 bitop3:0xc8\nv_cmp_ne_u32 s[86:87], 0, v208\n" \
"v_bitop3_b32 v208, v144, v146, s54 bitop3:0x7e\nv_bitop3_b32 v209, v152, v154, s54 bitop3:0x7e\nv_bitop3_b32 v208, v208, 1, v209 bitop3:0xc8\nv_cmp_ne_u32 s[88:89], 0, v208\nv_bitop3_b32 v208, v132, v134, s54 bitop3:0x7e\nv_bitop3_b32 v209, v140, v142, s54 bitop3:0x7e\nv_bitop3_b32 v208, v208, 1, v209 bitop3:0xc8\nv_cmp_ne_u32 s[68:69], 0, v208\n" \
"v_bitop3_b32 v208, v148, v150, s54 bitop3:0x7e\nv_bitop3_b32 v209, v156, v158, s54 bitop3:0x7e\nv_bitop3_b32 v208, v208, 1, v209 bitop3:0xc8\nv_cmp_ne_u32 s[62:63], 0, v208\ns_or_b64 vcc, s[86:87], s[88:89]\ns_or_b64 vcc, vcc, s[68:69]\ns_or_b64 vcc, vcc, s[62:63]\ns_waitcnt vmcnt(9)\n" \
"v_smfmac_f32_16x16x64_bf16 v[180:183], v[160:163], v[0:7], v197\nv_bitop3_b32 v208, v160, v162, s54 bitop3:0x7e\nv_smfmac_f32_16x16x64_bf16 v[184:187], v[160:163], v[32:39], v197\nv_cmp_gt_i32 s[46:47], 0, v208\nv_smfmac_f32_16x16x64_bf16 v[188:191], v[160:163], v[64:71], v197\nv_smfmac_f32_16x16x64_bf16 v[192:195], v[160:163], v[96:103], v197\ns_or_b64 vcc, vcc, s[46:47]\ns_waitcnt vmcnt(8)\n" \
"v_smfmac_f32_16x16x64_bf16 v[180:183], v[164:167], v[8:15], v197\nv_bitop3_b32 v208, v164, v166, s54 bitop3:0x7e\nv_smfmac_f32_16x16x64_bf16 v[184:187], v[164:167], v[40:47], v197\nv_cmp_gt_i32 s[48:49], 0, v208\nv_smfmac_f32_16x16x64_bf16 v[188:191], v[164:167], v[72:79], v197\nv_smfmac_f32_16x16x64_bf16 v[192:195], v[164:167], v[104:111], v197\ns_or_b64 vcc, vcc, s[48:49]\ns_waitcnt vmcnt(7)\n" \
"v_smfmac_f32_16x16x64_bf16 v[180:183], v[168:171], v[16:23], v197\nv_bitop3_b32 v208, v168, v170, s54 bitop3:0x7e\nv_smfmac_f32_16x16x64_bf16 v[184:187], v[168:171], v[48:55], v197\nv_cmp_gt_i32 s[50:51], 0, v208\nv_smfmac_f32_16x16x64_bf16 v[188:191], v[168:171], v[80:87], v197\nv_smfmac_f32_16x16x64_bf16 v[192:195], v[168:171], v[112:119], v197\ns_or_b64 vcc, vcc, s[50:51]\ns_waitcnt vmcnt(6)\n" \
"v_smfmac_f32_16x16x64_bf16 v[180:183], v[172:175], v[24:31], v197\nv_bitop3_b32 v208, v172, v174, s54 bitop3:0x7e\nv_smfmac_f32_16x16x64_bf16 v[184:187], v[172:175], v[56:63], v197\nv_cmp_gt_i32 s[52:53], 0, v208\nv_smfmac_f32_16x16x64_bf16 v[188:191], v[172:175], v[88:95], v197\nv_smfmac_f32_16x16x64_bf16 v[192:195], v[172:175], v[120:127], v197\ns_or_b64 vcc, vcc, s[52:53]\ns_waitcnt vmcnt(5)\n" \
"v_smfmac_f32_16x16x64_bf16 v[180:183], v[176:179], v[128:135], v197\nv_bitop3_b32 v208, v176, v178, s54 bitop3:0x7e\nv_smfmac_f32_16x16x64_bf16 v[184:187], v[176:179], v[136:143], v197\nv_cmp_gt_i32 s[64:65], 0, v208\nv_smfmac_f32_16x16x64_bf16 v[188:191], v[176:179], v[144:151], v197\ns_or_b64 vcc, vcc, s[64:65]\ns_cbranch_scc1 .Lrd_rb%=\nv_smfmac_f32_16x16x64_bf16 v[192:195], v[176:179], v[152:159], v197\n" \
".Lrd_rd0%=:\nv_add_f32 v180, v180, v182\nv_add_f32 v181, v181, v183\nds_write2_b32 v198, v180, v181 offset0:0 offset1:8\nv_add_f32 v184, v184, v186\nv_add_f32 v185, v185, v187\nv_add_f32 v188, v188, v190\nv_add_f32 v189, v189, v191\n" \
"ds_write2_b32 v198, v184, v185 offset0:32 offset1:40\nv_add_f32 v192, v192, v194\nv_add_f32 v193, v193, v195\nds_write2_b32 v198, v188, v189 offset0:64 offset1:72\nds_write2_b32 v198, v192, v193 offset0:96 offset1:104\ns_add_u32 s60, s58, 0\nbuffer_load_dwordx4 v[232:235], v196, %[rs], s60 offen offset:3072 sc1\ns_add_u32 s60, s58, 16384\n" \
"buffer_load_dwordx4 v[236:239], v196, %[rs], s60 offen offset:2048 sc1\ns_waitcnt lgkmcnt(0)\ns_barrier\nds_read_b128 v[180:183], v199 offset:0\nds_read_b128 v[184:187], v199 offset:4096\nds_read_b128 v[188:191], v199 offset:8192\nds_read_b128 v[192:195], v199 offset:12288\ns_and_b32 s44, s54, 0x80008000\n" \
"s_add_u32 s60, s58, 4096\nbuffer_load_dwordx4 v[240:243], v196, %[rs], s60 offen offset:0 sc1\ns_add_u32 s60, s58, 4096\nbuffer_load_dwordx4 v[244:247], v196, %[rs], s60 offen offset:1024 sc1\ns_waitcnt lgkmcnt(2)\nv_pk_add_f32 v[182:183], v[182:183], v[186:187]\nv_pk_add_f32 v[180:181], v[180:181], v[184:185]\ns_waitcnt lgkmcnt(0)\n" \
"v_pk_add_f32 v[186:187], v[188:189], v[192:193]\nv_pk_add_f32 v[184:185], v[190:191], v[194:195]\nv_pk_add_f32 v[180:181], v[180:181], v[186:187]\nv_pk_add_f32 v[182:183], v[182:183], v[184:185]\nv_cvt_pk_bf16_f32 v180, v180, v181\nv_xor_b32 v180, s44, v180\nv_cvt_pk_bf16_f32 v181, v182, v183\nv_min_u32 v180, -2, v180\n" \
"v_xor_b32 v209, s44, v181\nv_or_b32 v208, v180, v201\nv_mov_b32 v211, v209\nv_or_b32 v210, v180, v201\ns_nop 0\nv_permlane32_swap_b32 v209, v211\nv_permlane32_swap_b32 v208, v210\nbuffer_store_dwordx4 v[208:211], v206, %[rs], 0 offen sc1\n" \
"s_add_u32 s60, s58, 16384\nbuffer_load_dwordx4 v[248:251], v196, %[rs], s60 offen offset:3072 sc1\ns_add_u32 s60, s58, 20480\nbuffer_load_dwordx4 v[252:255], v196, %[rs], s60 offen offset:0 sc1\ns_add_u32 s60, s58, 20480\nbuffer_load_dwordx4 v[160:163], v196, %[rs], s60 offen offset:2048 sc1\ns_xor_b32 s41, s41, 4194304\nv_mov_b32 v207, s40\n" \
"s_waitcnt vmcnt(12)\nv_bitop3_b32 v209, v212, v214, s61 bitop3:0x7e\nv_cmp_gt_i32 vcc, 0, v209\ns_cbranch_vccnz .Lrd_tre0%=\n.Lrd_tl0%=:\nv_mfma_f32_16x16x16_bf16 a[0:3], v[0:1], v[212:213], 0\ns_add_u32 s60, s58, 4096\nbuffer_load_dwordx4 v[164:167], v196, %[rs], s60 offen offset:2048 sc1\n" \
"v_mfma_f32_16x16x16_bf16 a[8:11], v[32:33], v[212:213], 0\nv_mfma_f32_16x16x16_bf16 a[16:19], v[64:65], v[212:213], 0\nv_mfma_f32_16x16x16_bf16 a[24:27], v[96:97], v[212:213], 0\ns_waitcnt vmcnt(12)\nv_mfma_f32_16x16x16_bf16 a[0:3], v[4:5], v[214:215], a[0:3]\ns_add_u32 s60, s58, 4096\nbuffer_load_dwordx4 v[168:171], v196, %[rs], s60 offen offset:3072 sc1\nv_bitop3_b32 v209, v216, v218, s61 bitop3:0x7e\n" \
"v_mfma_f32_16x16x16_bf16 a[8:11], v[36:37], v[214:215], a[8:11]\ns_lshl_b32 s42, s40, 17\ns_add_u32 s42, s42, %[at]\nv_mfma_f32_16x16x16_bf16 a[16:19], v[68:69], v[214:215], a[16:19]\nv_cmp_gt_i32 vcc, 0, v209\nv_mfma_f32_16x16x16_bf16 a[24:27], v[100:101], v[214:215], a[24:27]\ns_cbranch_vccnz .Lrd_tre1%=\n.Lrd_tl1%=:\n" \
"s_waitcnt vmcnt(12)\nv_mfma_f32_16x16x32_bf16 a[4:7], v[0:3], v[216:219], 0\ns_add_u32 s60, s58, 20480\nbuffer_load_dwordx4 v[172:175], v196, %[rs], s60 offen offset:1024 sc1\nv_bitop3_b32 v209, v220, v222, s61 bitop3:0x7e\nv_mfma_f32_16x16x32_bf16 a[12:15], v[32:35], v[216:219], 0\ns_lshr_b32 s45, s42, 8\ns_and_b32 s45, s45, 0x2000\n" \
"v_mfma_f32_16x16x32_bf16 a[20:23], v[64:67], v[216:219], 0\nv_cmp_gt_i32 vcc, 0, v209\nv_mfma_f32_16x16x32_bf16 a[28:31], v[96:99], v[216:219], 0\ns_cbranch_vccnz .Lrd_tre2%=\n.Lrd_tl2%=:\ns_waitcnt vmcnt(12)\nv_mfma_f32_16x16x32_bf16 a[4:7], v[4:7], v[220:223], a[4:7]\ns_add_u32 s60, s58, 20480\n" \
"buffer_load_dwordx4 v[176:179], v196, %[rs], s60 offen offset:3072 sc1\nv_bitop3_b32 v209, v224, v226, s61 bitop3:0x7e\nv_mfma_f32_16x16x32_bf16 a[12:15], v[36:39], v[220:223], a[12:15]\ns_xor_b32 s42, s42, s45\ns_add_u32 s40, s40, 1\nv_mfma_f32_16x16x32_bf16 a[20:23], v[68:71], v[220:223], a[20:23]\nv_cmp_gt_i32 vcc, 0, v209\nv_mfma_f32_16x16x32_bf16 a[28:31], v[100:103], v[220:223], a[28:31]\n" \
"s_cbranch_vccnz .Lrd_tre3%=\n.Lrd_tl3%=:\nv_mfma_f32_16x16x16_bf16 a[0:3], v[8:9], v[224:225], a[0:3]\ns_cmp_eq_u32 s40, 1\ns_cbranch_scc1 .Lrd_y1%=\n.Lrd_y1r%=:\ns_add_u32 s60, s41, 0\nbuffer_load_dwordx4 v[0:3], v196, %[rs], s60 offen offset:0 sc1\n" \
"v_mfma_f32_16x16x16_bf16 a[8:11], v[40:41], v[224:225], a[8:11]\nv_mfma_f32_16x16x16_bf16 a[16:19], v[72:73], v[224:225], a[16:19]\nbuffer_load_dwordx4 v[212:215], v204, %[rs], s42 offen sc1\nv_mfma_f32_16x16x16_bf16 a[24:27], v[104:105], v[224:225], a[24:27]\ns_waitcnt vmcnt(14)\nv_mfma_f32_16x16x16_bf16 a[0:3], v[12:13], v[226:227], a[0:3]\ns_add_u32 s60, s41, 0\nbuffer_load_dwordx4 v[32:35], v196, %[rs], s60 offen offset:1024 sc1\n" \
"v_bitop3_b32 v209, v228, v230, s61 bitop3:0x7e\nv_mfma_f32_16x16x16_bf16 a[8:11], v[44:45], v[226:227], a[8:11]\ns_lshl_b32 s62, s55, 31\ns_or_b32 s62, s62, 1.0\ns_mov_b32 s63, s62\nv_mfma_f32_16x16x16_bf16 a[16:19], v[76:77], v[226:227], a[16:19]\nv_cmp_gt_i32 vcc, 0, v209\nv_mfma_f32_16x16x16_bf16 a[24:27], v[108:109], v[226:227], a[24:27]\n" \
"s_cbranch_vccnz .Lrd_tre4%=\n.Lrd_tl4%=:\ns_waitcnt vmcnt(14)\nv_mfma_f32_16x16x32_bf16 a[4:7], v[8:11], v[228:231], a[4:7]\ns_add_u32 s60, s41, 0\nbuffer_load_dwordx4 v[64:67], v196, %[rs], s60 offen offset:2048 sc1\nv_bitop3_b32 v209, v232, v234, s61 bitop3:0x7e\nv_mfma_f32_16x16x32_bf16 a[12:15], v[40:43], v[228:231], a[12:15]\n" \
"v_mfma_f32_16x16x32_bf16 a[20:23], v[72:75], v[228:231], a[20:23]\nv_cmp_gt_i32 vcc, 0, v209\nv_mfma_f32_16x16x32_bf16 a[28:31], v[104:107], v[228:231], a[28:31]\ns_cbranch_vccnz .Lrd_tre5%=\n.Lrd_tl5%=:\ns_waitcnt vmcnt(14)\nv_mfma_f32_16x16x32_bf16 a[4:7], v[12:15], v[232:235], a[4:7]\ns_add_u32 s60, s41, 131072\n" \
"buffer_load_dwordx4 v[4:7], v196, %[rs], s60 offen offset:0 sc1\nv_bitop3_b32 v209, v236, v238, s61 bitop3:0x7e\nv_mfma_f32_16x16x32_bf16 a[12:15], v[44:47], v[232:235], a[12:15]\nv_mfma_f32_16x16x32_bf16 a[20:23], v[76:79], v[232:235], a[20:23]\nv_cmp_gt_i32 vcc, 0, v209\nv_mfma_f32_16x16x32_bf16 a[28:31], v[108:111], v[232:235], a[28:31]\ns_cbranch_vccnz .Lrd_tre6%=\n.Lrd_tl6%=:\n" \
"v_mfma_f32_16x16x16_bf16 a[0:3], v[16:17], v[236:237], a[0:3]\ns_add_u32 s60, s41, 131072\nbuffer_load_dwordx4 v[36:39], v196, %[rs], s60 offen offset:1024 sc1\nv_mfma_f32_16x16x16_bf16 a[8:11], v[48:49], v[236:237], a[8:11]\nv_mfma_f32_16x16x16_bf16 a[16:19], v[80:81], v[236:237], a[16:19]\nv_mfma_f32_16x16x16_bf16 a[24:27], v[112:113], v[236:237], a[24:27]\ns_waitcnt vmcnt(15)\nv_mfma_f32_16x16x16_bf16 a[0:3], v[20:21], v[238:239], a[0:3]\n" \
"s_add_u32 s60, s41, 131072\nbuffer_load_dwordx4 v[68:71], v196, %[rs], s60 offen offset:2048 sc1\nv_bitop3_b32 v209, v240, v242, s61 bitop3:0x7e\nv_mfma_f32_16x16x16_bf16 a[8:11], v[52:53], v[238:239], a[8:11]\nv_mfma_f32_16x16x16_bf16 a[16:19], v[84:85], v[238:239], a[16:19]\nv_cmp_gt_i32 vcc, 0, v209\nv_mfma_f32_16x16x16_bf16 a[24:27], v[116:117], v[238:239], a[24:27]\ns_cbranch_vccnz .Lrd_tre7%=\n" \
".Lrd_tl7%=:\ns_waitcnt vmcnt(15)\nv_mfma_f32_16x16x32_bf16 a[4:7], v[16:19], v[240:243], a[4:7]\ns_add_u32 s60, s41, 262144\nbuffer_load_dwordx4 v[8:11], v196, %[rs], s60 offen offset:0 sc1\nv_bitop3_b32 v209, v244, v246, s61 bitop3:0x7e\nv_mfma_f32_16x16x32_bf16 a[12:15], v[48:51], v[240:243], a[12:15]\nv_mfma_f32_16x16x32_bf16 a[20:23], v[80:83], v[240:243], a[20:23]\n" \
"v_cmp_gt_i32 vcc, 0, v209\nv_mfma_f32_16x16x32_bf16 a[28:31], v[112:115], v[240:243], a[28:31]\ns_cbranch_vccnz .Lrd_tre8%=\n.Lrd_tl8%=:\ns_waitcnt vmcnt(14)\nv_mfma_f32_16x16x32_bf16 a[4:7], v[20:23], v[244:247], a[4:7]\ns_add_u32 s60, s41, 262144\nbuffer_load_dwordx4 v[40:43], v196, %[rs], s60 offen offset:1024 sc1\n" \
"v_bitop3_b32 v209, v248, v250, s61 bitop3:0x7e\nv_mfma_f32_16x16x32_bf16 a[12:15], v[52:55], v[244:247], a[12:15]\nv_mfma_f32_16x16x32_bf16 a[20:23], v[84:87], v[244:247], a[20:23]\nv_cmp_gt_i32 vcc, 0, v209\nv_mfma_f32_16x16x32_bf16 a[28:31], v[116:119], v[244:247], a[28:31]\ns_cbranch_vccnz .Lrd_tre9%=\n.Lrd_tl9%=:\nv_mfma_f32_16x16x16_bf16 a[0:3], v[24:25], v[248:249], a[0:3]\n" \
"s_add_u32 s60, s41, 262144\nbuffer_load_dwordx4 v[72:75], v196, %[rs], s60 offen offset:2048 sc1\nv_mfma_f32_16x16x16_bf16 a[8:11], v[56:57], v[248:249], a[8:11]\nv_mfma_f32_16x16x16_bf16 a[16:19], v[88:89], v[248:249], a[16:19]\nv_mfma_f32_16x16x16_bf16 a[24:27], v[120:121], v[248:249], a[24:27]\ns_waitcnt vmcnt(15)\nv_mfma_f32_16x16x16_bf16 a[0:3], v[28:29], v[250:251], a[0:3]\ns_add_u32 s60, s41, 393216\n" \
"buffer_load_dwordx4 v[12:15], v196, %[rs], s60 offen offset:0 sc1\nv_bitop3_b32 v209, v252, v254, s61 bitop3:0x7e\nv_mfma_f32_16x16x16_bf16 a[8:11], v[60:61], v[250:251], a[8:11]\nv_mfma_f32_16x16x16_bf16 a[16:19], v[92:93], v[250:251], a[16:19]\nv_cmp_gt_i32 vcc, 0, v209\nv_mfma_f32_16x16x16_bf16 a[24:27], v[124:125], v[250:251], a[24:27]\ns_cbranch_vccnz .Lrd_tre10%=\n.Lrd_tl10%=:\n" \
"s_waitcnt vmcnt(15)\nv_mfma_f32_16x16x32_bf16 a[0:3], v[128:131], v[252:255], a[0:3]\ns_add_u32 s60, s41, 393216\nbuffer_load_dwordx4 v[44:47], v196, %[rs], s60 offen offset:1024 sc1\nv_bitop3_b32 v209, v160, v162, s61 bitop3:0x7e\nv_mfma_f32_16x16x32_bf16 a[8:11], v[136:139], v[252:255], a[8:11]\nv_mfma_f32_16x16x32_bf16 a[16:19], v[144:147], v[252:255], a[16:19]\nv_cmp_gt_i32 vcc, 0, v209\n" \
"v_mfma_f32_16x16x32_bf16 a[24:27], v[152:155], v[252:255], a[24:27]\ns_cbranch_vccnz .Lrd_tre11%=\n.Lrd_tl11%=:\ns_waitcnt vmcnt(15)\nv_mfma_f32_16x16x32_bf16 a[0:3], v[132:135], v[160:163], a[0:3]\ns_add_u32 s60, s41, 393216\nbuffer_load_dwordx4 v[76:79], v196, %[rs], s60 offen offset:2048 sc1\nv_bitop3_b32 v209, v164, v166, s61 bitop3:0x7e\n" \
"v_mfma_f32_16x16x32_bf16 a[8:11], v[140:143], v[160:163], a[8:11]\nv_mfma_f32_16x16x32_bf16 a[16:19], v[148:151], v[160:163], a[16:19]\nv_cmp_gt_i32 vcc, 0, v209\nv_mfma_f32_16x16x32_bf16 a[24:27], v[156:159], v[160:163], a[24:27]\ns_cbranch_vccnz .Lrd_tre12%=\n.Lrd_tl12%=:\ns_waitcnt vmcnt(15)\nv_mfma_f32_16x16x32_bf16 a[4:7], v[24:27], v[164:167], a[4:7]\n" \
"s_add_u32 s60, s41, 524288\nbuffer_load_dwordx4 v[16:19], v196, %[rs], s60 offen offset:0 sc1\nv_bitop3_b32 v209, v168, v170, s61 bitop3:0x7e\nv_mfma_f32_16x16x32_bf16 a[12:15], v[56:59], v[164:167], a[12:15]\nv_mfma_f32_16x16x32_bf16 a[20:23], v[88:91], v[164:167], a[20:23]\nds_write_b128 v202, a[0:3] offset:0\nv_cmp_gt_i32 vcc, 0, v209\nv_mfma_f32_16x16x32_bf16 a[28:31], v[120:123], v[164:167], a[28:31]\n" \
"s_cbranch_vccnz .Lrd_tre13%=\n.Lrd_tl13%=:\ns_waitcnt vmcnt(15)\nv_mfma_f32_16x16x32_bf16 a[4:7], v[28:31], v[168:171], a[4:7]\ns_add_u32 s60, s41, 524288\nbuffer_load_dwordx4 v[48:51], v196, %[rs], s60 offen offset:1024 sc1\nv_bitop3_b32 v209, v172, v174, s61 bitop3:0x7e\nv_mfma_f32_16x16x32_bf16 a[12:15], v[60:63], v[168:171], a[12:15]\n" \
"v_mfma_f32_16x16x32_bf16 a[20:23], v[92:95], v[168:171], a[20:23]\nds_write_b128 v202, a[8:11] offset:2048\nv_cmp_gt_i32 vcc, 0, v209\nv_mfma_f32_16x16x32_bf16 a[28:31], v[124:127], v[168:171], a[28:31]\ns_cbranch_vccnz .Lrd_tre14%=\n.Lrd_tl14%=:\ns_waitcnt vmcnt(15)\nv_mfma_f32_16x16x32_bf16 a[4:7], v[128:131], v[172:175], a[4:7]\n" \
"s_add_u32 s60, s41, 524288\nbuffer_load_dwordx4 v[80:83], v196, %[rs], s60 offen offset:2048 sc1\nv_bitop3_b32 v209, v176, v178, s61 bitop3:0x7e\nv_mfma_f32_16x16x32_bf16 a[12:15], v[136:139], v[172:175], a[12:15]\nv_mfma_f32_16x16x32_bf16 a[20:23], v[144:147], v[172:175], a[20:23]\nds_write_b128 v202, a[16:19] offset:4096\nv_cmp_gt_i32 vcc, 0, v209\nv_mfma_f32_16x16x32_bf16 a[28:31], v[152:155], v[172:175], a[28:31]\n" \
"s_cbranch_vccnz .Lrd_tre15%=\n.Lrd_tl15%=:\nv_mfma_f32_16x16x32_bf16 a[4:7], v[132:135], v[176:179], a[4:7]\ns_add_u32 s60, s41, 655360\ns_waitcnt vmcnt(14)\nbuffer_load_dwordx4 v[20:23], v196, %[rs], s60 offen offset:0 sc1\nv_lshlrev_b32 v216, 16, v212\nv_and_b32 v217, 0xffff0000, v212\n" \
"v_mfma_f32_16x16x32_bf16 a[12:15], v[140:143], v[176:179], a[12:15]\nv_lshlrev_b32 v218, 16, v213\nv_and_b32 v219, 0xffff0000, v213\nv_mfma_f32_16x16x32_bf16 a[20:23], v[148:151], v[176:179], a[20:23]\nds_write_b128 v202, a[24:27] offset:6144\nv_lshlrev_b32 v220, 16, v214\nv_and_b32 v221, 0xffff0000, v214\nv_mfma_f32_16x16x32_bf16 a[28:31], v[156:159], v[176:179], a[28:31]\n" \
"s_add_u32 s60, s41, 655360\nbuffer_load_dwordx4 v[52:55], v196, %[rs], s60 offen offset:1024 sc1\ns_add_u32 s60, s41, 655360\nds_write_b128 v202, a[4:7] offset:1024\nbuffer_load_dwordx4 v[84:87], v196, %[rs], s60 offen offset:2048 sc1\nds_write_b128 v202, a[12:15] offset:3072\nv_lshlrev_b32 v222, 16, v215\nv_and_b32 v223, 0xffff0000, v215\n" \
"s_add_u32 s60, s41, 786432\nbuffer_load_dwordx4 v[24:27], v196, %[rs], s60 offen offset:0 sc1\nds_write_b128 v202, a[20:23] offset:5120\ns_add_u32 s60, s41, 786432\nbuffer_load_dwordx4 v[56:59], v196, %[rs], s60 offen offset:1024 sc1\nds_write_b128 v202, a[28:31] offset:7168\ns_waitcnt lgkmcnt(0)\ns_barrier\n" \
"ds_read_b128 v[224:227], v203 offset:0\nds_read_b128 v[228:231], v203 offset:256\nds_read_b128 v[232:235], v203 offset:8192\nds_read_b128 v[236:239], v203 offset:8448\nds_read_b128 v[240:243], v203 offset:16384\nds_read_b128 v[244:247], v203 offset:16640\nds_read_b128 v[248:251], v203 offset:24576\nds_read_b128 v[252:255], v203 offset:24832\n" \
"s_add_u32 s60, s41, 786432\nbuffer_load_dwordx4 v[88:91], v196, %[rs], s60 offen offset:2048 sc1\ns_waitcnt lgkmcnt(4)\nv_pk_add_f32 v[224:225], v[224:225], v[232:233]\nv_pk_add_f32 v[226:227], v[226:227], v[234:235]\nv_pk_add_f32 v[228:229], v[228:229], v[236:237]\nv_pk_add_f32 v[230:231], v[230:231], v[238:239]\ns_add_u32 s60, s41, 917504\n" \
"buffer_load_dwordx4 v[28:31], v196, %[rs], s60 offen offset:0 sc1\ns_waitcnt lgkmcnt(2)\nv_pk_add_f32 v[224:225], v[224:225], v[240:241]\nv_pk_add_f32 v[226:227], v[226:227], v[242:243]\nv_pk_add_f32 v[228:229], v[228:229], v[244:245]\nv_pk_add_f32 v[230:231], v[230:231], v[246:247]\ns_add_u32 s60, s41, 917504\nbuffer_load_dwordx4 v[60:63], v196, %[rs], s60 offen offset:1024 sc1\n" \
"s_waitcnt lgkmcnt(0)\nv_pk_add_f32 v[224:225], v[224:225], v[248:249]\nv_pk_add_f32 v[226:227], v[226:227], v[250:251]\nv_pk_add_f32 v[228:229], v[228:229], v[252:253]\nv_pk_add_f32 v[230:231], v[230:231], v[254:255]\nv_pk_fma_f32 v[224:225], v[224:225], s[62:63], v[216:217]\nv_pk_fma_f32 v[226:227], v[226:227], s[62:63], v[218:219]\nv_pk_fma_f32 v[228:229], v[228:229], s[62:63], v[220:221]\n" \
"v_pk_fma_f32 v[230:231], v[230:231], s[62:63], v[222:223]\nv_cvt_pk_bf16_f32 v208, v224, v225\nv_cvt_pk_bf16_f32 v209, v226, v227\nv_cvt_pk_bf16_f32 v210, v228, v229\nv_cvt_pk_bf16_f32 v211, v230, v231\nbuffer_store_dwordx4 v[208:211], v204, %[rs], s42 offen sc1\nv_pk_add_f32 v[232:233], v[224:225], v[226:227]\nv_pk_add_f32 v[234:235], v[228:229], v[230:231]\n" \
"v_pk_add_f32 v[232:233], v[232:233], v[234:235]\ns_add_u32 s60, s41, 917504\nbuffer_load_dwordx4 v[92:95], v196, %[rs], s60 offen offset:2048 sc1\ns_add_u32 s60, s41, 0\nbuffer_load_dwordx4 v[96:99], v196, %[rs], s60 offen offset:3072 sc1\nv_add_f32 v232, v232, v233\ns_nop 1\nv_add_f32_dpp v233, v232, v232 row_ror:8 row_mask:0xf bank_mask:0xf\n" \
"v_add_f32_dpp v234, v232, v232 row_ror:8 row_mask:0xf bank_mask:0xf\ns_nop 1\nv_permlane16_swap_b32 v233, v234\nv_add_f32 v232, v233, v234\nv_add_f32 v233, v233, v234\ns_nop 1\nv_permlane32_swap_b32 v232, v233\nv_add_f32 v232, v232, v233\n" \
"v_mul_f32 v232, 0x3c800000, v232\nv_pk_add_f32 v[224:225], v[224:225], v[232:233] op_sel_hi:[1,0] neg_lo:[0,1] neg_hi:[0,1]\nv_pk_add_f32 v[226:227], v[226:227], v[232:233] op_sel_hi:[1,0] neg_lo:[0,1] neg_hi:[0,1]\nv_pk_add_f32 v[228:229], v[228:229], v[232:233] op_sel_hi:[1,0] neg_lo:[0,1] neg_hi:[0,1]\nv_pk_add_f32 v[230:231], v[230:231], v[232:233] op_sel_hi:[1,0] neg_lo:[0,1] neg_hi:[0,1]\ns_add_u32 s60, s41, 131072\nbuffer_load_dwordx4 v[100:103], v196, %[rs], s60 offen offset:3072 sc1\nv_accvgpr_read_b32 v212, a33\n" \
"buffer_load_dwordx2 v[214:215], v212, %[rs], 0 offen sc1\nv_pk_mul_f32 v[236:237], v[224:225], v[224:225]\nv_pk_mul_f32 v[238:239], v[226:227], v[226:227]\nv_pk_fma_f32 v[236:237], v[228:229], v[228:229], v[236:237]\nv_pk_fma_f32 v[238:239], v[230:231], v[230:231], v[238:239]\nv_pk_add_f32 v[236:237], v[236:237], v[238:239]\nv_add_f32 v236, v236, v237\ns_nop 1\n" \
"v_add_f32_dpp v237, v236, v236 row_ror:8 row_mask:0xf bank_mask:0xf\nv_add_f32_dpp v238, v236, v236 row_ror:8 row_mask:0xf bank_mask:0xf\ns_nop 1\nv_permlane16_swap_b32 v237, v238\nv_add_f32 v236, v237, v238\nv_add_f32 v237, v237, v238\ns_nop 1\nv_permlane32_swap_b32 v236, v237\n" \
"v_add_f32 v236, v236, v237\nv_mov_b32 v233, v236\nbuffer_store_dwordx2 v[232:233], v205, %[rs], s42 offen sc1\ns_mov_b32 s59, %[fa]\ns_add_u32 s60, s41, 262144\nbuffer_load_dwordx4 v[104:107], v196, %[rs], s60 offen offset:3072 sc1\ns_add_u32 s60, s41, 393216\nbuffer_load_dwordx4 v[108:111], v196, %[rs], s60 offen offset:3072 sc1\n" \
"s_add_u32 s60, s41, 524288\nbuffer_load_dwordx4 v[112:115], v196, %[rs], s60 offen offset:3072 sc1\ns_add_u32 s60, s41, 655360\nbuffer_load_dwordx4 v[116:119], v196, %[rs], s60 offen offset:3072 sc1\ns_add_u32 s60, s41, 786432\nbuffer_load_dwordx4 v[120:123], v196, %[rs], s60 offen offset:3072 sc1\ns_add_u32 s60, s41, 917504\nbuffer_load_dwordx4 v[124:127], v196, %[rs], s60 offen offset:3072 sc1\n" \
"s_cmp_ge_u32 s40, 64\ns_cbranch_scc1 .Lrd_fin%=\n.Lrd_ychk%=:\ns_and_b32 s44, s40, 1\ns_bfe_u32 s55, s40, 0x10001\ns_mul_i32 s54, s44, 0x80008001\ns_mul_i32 s61, s55, 0x80008000\ns_lshl_b32 s58, s44, 17\n" \
"s_add_u32 s58, s58, %[st]\ns_add_u32 s44, s40, 1\ns_and_b32 s44, s44, 3\ns_lshl_b32 s44, s44, 15\nv_xor_b32 v206, s44, v200\nv_mov_b64 v[180:181], 0\nv_mov_b64 v[182:183], 0\nv_mov_b64 v[184:185], 0\n" \
"v_mov_b64 v[186:187], 0\nv_mov_b64 v[188:189], 0\nv_mov_b64 v[190:191], 0\nv_mov_b64 v[192:193], 0\nv_mov_b64 v[194:195], 0\ns_waitcnt vmcnt(7)\n.Lrd_sg0%=:\ns_memrealtime s[68:69]\n" \
"v_cmp_ne_u32 vcc, s40, v214\ns_cbranch_vccnz .Lrd_sg1%=\nv_max_u32_dpp v215, v215, v215 quad_perm:[1,0,3,2] row_mask:0xf bank_mask:0xf\ns_nop 1\nv_max_u32_dpp v215, v215, v215 quad_perm:[2,3,0,1] row_mask:0xf bank_mask:0xf\ns_nop 1\nv_max_u32_dpp v215, v215, v215 row_half_mirror row_mask:0xf bank_mask:0xf\ns_nop 0\n" \
"v_readlane_b32 s86, v215, 0\nv_readlane_b32 s87, v215, 8\ns_add_u32 s86, s86, %[du]\n.Lrd_tw608%=:\ns_waitcnt lgkmcnt(0)\ns_sub_u32 s44, s86, s68\ns_add_u32 s44, s44, 11\ns_cmp_lt_u32 s44, 250\n" \
"s_cbranch_scc1 .Lrd_tw608x%=\nbuffer_load_dwordx4 v[128:131], v196, %[rs], %[su] offen offset:0 sc1\nbuffer_load_dwordx4 v[132:135], v196, %[rs], %[su] offen offset:1024 sc1\ns_memrealtime s[68:69]\nbuffer_load_dwordx4 v[136:139], v196, %[rs], %[su] offen offset:2048 sc1\nbuffer_load_dwordx4 v[140:143], v196, %[rs], %[su] offen offset:3072 sc1\n.Lrd_tw619%=:\ns_waitcnt lgkmcnt(0)\n" \
"s_sub_u32 s44, s86, s68\ns_add_u32 s44, s44, 21\ns_cmp_lt_u32 s44, 250\ns_cbranch_scc1 .Lrd_tw619x%=\nbuffer_load_dwordx4 v[144:147], v196, %[rs], s57 offen offset:0 sc1\nbuffer_load_dwordx4 v[148:151], v196, %[rs], s57 offen offset:1024 sc1\ns_memrealtime s[68:69]\nbuffer_load_dwordx4 v[152:155], v196, %[rs], s57 offen offset:2048 sc1\n" \
"buffer_load_dwordx4 v[156:159], v196, %[rs], s57 offen offset:3072 sc1\ns_waitcnt vmcnt(14)\nbuffer_store_dword v207, off, %[rs], s59 sc1\ns_mov_b32 s59, -2147483648\n.Lrd_tw633%=:\ns_waitcnt lgkmcnt(0)\ns_sub_u32 s44, s87, s68\ns_add_u32 s44, s44, 24\n" \
"s_cmp_lt_u32 s44, 250\ns_cbranch_scc1 .Lrd_tw633x%=\nbuffer_load_dwordx4 v[160:163], v196, %[rs], %[sp] offen offset:0 sc1\nbuffer_load_dwordx4 v[164:167], v196, %[rs], %[sp] offen offset:1024 sc1\nbuffer_load_dwordx4 v[168:171], v196, %[rs], %[sp] offen offset:2048 sc1\nbuffer_load_dwordx4 v[172:175], v196, %[rs], %[sp] offen offset:3072 sc1\nbuffer_load_dwordx4 v[176:179], v196, %[rs], s56 offen offset:0 sc1\ns_add_u32 s60, s58, 16384\n" \
"buffer_load_dwordx4 v[212:215], v196, %[rs], s60 offen offset:0 sc1\nv_bitop3_b32 v209, v0, v2, s55 bitop3:0x7e\nv_bitop3_b32 v210, v32, v34, s55 bitop3:0x7e\nv_or3_b32 v208, v209, v210, 0\nv_bitop3_b32 v209, v64, v66, s55 bitop3:0x7e\nv_bitop3_b32 v210, v4, v6, s55 bitop3:0x7e\nv_or3_b32 v208, v209, v210, v208\ns_add_u32 s60, s58, 0\n" \
"buffer_load_dwordx4 v[216:219], v196, %[rs], s60 offen offset:0 sc1\nv_bitop3_b32 v209, v36, v38, s55 bitop3:0x7e\nv_bitop3_b32 v210, v68, v70, s55 bitop3:0x7e\nv_or3_b32 v208, v209, v210, v208\nv_bitop3_b32 v209, v8, v10, s55 bitop3:0x7e\nv_bitop3_b32 v210, v40, v42, s55 bitop3:0x7e\nv_or3_b32 v208, v209, v210, v208\ns_add_u32 s60, s58, 0\n" \
"buffer_load_dwordx4 v[220:223], v196, %[rs], s60 offen offset:1024 sc1\nv_bitop3_b32 v209, v72, v74, s55 bitop3:0x7e\nv_bitop3_b32 v210, v12, v14, s55 bitop3:0x7e\nv_or3_b32 v208, v209, v210, v208\nv_bitop3_b32 v209, v44, v46, s55 bitop3:0x7e\nv_bitop3_b32 v210, v76, v78, s55 bitop3:0x7e\nv_or3_b32 v208, v209, v210, v208\ns_add_u32 s60, s58, 16384\n" \
"buffer_load_dwordx4 v[224:227], v196, %[rs], s60 offen offset:1024 sc1\nv_bitop3_b32 v209, v16, v18, s55 bitop3:0x7e\nv_bitop3_b32 v210, v48, v50, s55 bitop3:0x7e\nv_or3_b32 v208, v209, v210, v208\nv_bitop3_b32 v209, v80, v82, s55 bitop3:0x7e\nv_bitop3_b32 v210, v20, v22, s55 bitop3:0x7e\nv_or3_b32 v208, v209, v210, v208\ns_add_u32 s60, s58, 0\n" \
"buffer_load_dwordx4 v[228:231], v196, %[rs], s60 offen offset:2048 sc1\nv_bitop3_b32 v209, v52, v54, s55 bitop3:0x7e\nv_bitop3_b32 v210, v84, v86, s55 bitop3:0x7e\nv_or3_b32 v208, v209, v210, v208\nv_bitop3_b32 v209, v24, v26, s55 bitop3:0x7e\nv_bitop3_b32 v210, v56, v58, s55 bitop3:0x7e\nv_or3_b32 v208, v209, v210, v208\nv_bitop3_b32 v209, v88, v90, s55 bitop3:0x7e\n" \
"v_bitop3_b32 v210, v28, v30, s55 bitop3:0x7e\nv_or3_b32 v208, v209, v210, v208\nv_bitop3_b32 v209, v60, v62, s55 bitop3:0x7e\nv_bitop3_b32 v210, v92, v94, s55 bitop3:0x7e\nv_or3_b32 v208, v209, v210, v208\ns_waitcnt vmcnt(23)\nv_bitop3_b32 v209, v96, v98, s55 bitop3:0x7e\nv_bitop3_b32 v210, v100, v102, s55 bitop3:0x7e\n" \
"v_or3_b32 v208, v209, v210, v208\nv_bitop3_b32 v209, v104, v106, s55 bitop3:0x7e\nv_bitop3_b32 v210, v108, v110, s55 bitop3:0x7e\nv_or3_b32 v208, v209, v210, v208\ns_waitcnt vmcnt(19)\nv_bitop3_b32 v209, v112, v114, s55 bitop3:0x7e\nv_bitop3_b32 v210, v116, v118, s55 bitop3:0x7e\nv_or3_b32 v208, v209, v210, v208\n" \
"v_bitop3_b32 v209, v120, v122, s55 bitop3:0x7e\nv_bitop3_b32 v210, v124, v126, s55 bitop3:0x7e\nv_or3_b32 v208, v209, v210, v208\nv_and_b32 v208, 1, v208\nv_cmp_ne_u32 vcc, 0, v208\ns_cbranch_vccnz .Lrd_yre%=\ns_branch .Lrd_ra%=\n.Lrd_sg1%=:\n" \
"buffer_load_dwordx2 v[214:215], v212, %[rs], 0 offen sc1\ns_waitcnt vmcnt(0)\nbuffer_store_dword v207, off, %[rs], s59 sc1\ns_mov_b32 s59, -2147483648\ns_add_u32 s43, s43, 1\ns_cmp_lt_u32 s43, 262144\ns_cbranch_scc1 .Lrd_sg0%=\ns_branch .Lrd_abort%=\n" \
".Lrd_tw608x%=:\ns_memrealtime s[68:69]\ns_branch .Lrd_tw608%=\n.Lrd_tw619x%=:\ns_memrealtime s[68:69]\ns_branch .Lrd_tw619%=\n.Lrd_tw633x%=:\ns_memrealtime s[68:69]\n" \
"s_branch .Lrd_tw633%=\n.Lrd_fin%=:\ns_waitcnt vmcnt(0)\nbuffer_store_dword v207, off, %[rs], s59 sc1\ns_mov_b32 s59, -2147483648\ns_branch .Lrd_end%=\n.Lrd_yre%=:\ns_mov_b64 exec, vcc\n" \
"s_waitcnt vmcnt(0)\ns_add_u32 s43, s43, 1\ns_cmp_lt_u32 s43, 262144\ns_cbranch_scc1 .Lrd_yld%=\ns_branch .Lrd_abort%=\n.Lrd_rb%=:\ns_mov_b64 exec, s[46:47]\ns_cbranch_execz .Lrd_rs0%=\n" \
"buffer_load_dwordx4 v[160:163], v196, %[rs], %[sp] offen offset:0 sc1\n.Lrd_rs0%=:\ns_mov_b64 exec, s[48:49]\ns_cbranch_execz .Lrd_rs1%=\nbuffer_load_dwordx4 v[164:167], v196, %[rs], %[sp] offen offset:1024 sc1\n.Lrd_rs1%=:\ns_mov_b64 exec, s[50:51]\ns_cbranch_execz .Lrd_rs2%=\n" \
"buffer_load_dwordx4 v[168:171], v196, %[rs], %[sp] offen offset:2048 sc1\n.Lrd_rs2%=:\ns_mov_b64 exec, s[52:53]\ns_cbranch_execz .Lrd_rs3%=\nbuffer_load_dwordx4 v[172:175], v196, %[rs], %[sp] offen offset:3072 sc1\n.Lrd_rs3%=:\ns_mov_b64 exec, s[64:65]\ns_cbranch_execz .Lrd_rs4%=\n" \
"buffer_load_dwordx4 v[176:179], v196, %[rs], s56 offen offset:0 sc1\n.Lrd_rs4%=:\ns_mov_b64 exec, s[86:87]\ns_cbranch_execz .Lrd_rs5%=\nbuffer_load_dwordx4 v[128:131], v196, %[rs], %[su] offen offset:0 sc1\nbuffer_load_dwordx4 v[136:139], v196, %[rs], %[su] offen offset:2048 sc1\n.Lrd_rs5%=:\ns_mov_b64 exec, s[88:89]\n" \
"s_cbranch_execz .Lrd_rs6%=\nbuffer_load_dwordx4 v[144:147], v196, %[rs], s57 offen offset:0 sc1\nbuffer_load_dwordx4 v[152:155], v196, %[rs], s57 offen offset:2048 sc1\n.Lrd_rs6%=:\ns_mov_b64 exec, s[68:69]\ns_cbranch_execz .Lrd_rs7%=\nbuffer_load_dwordx4 v[132:135], v196, %[rs], %[su] offen offset:1024 sc1\nbuffer_load_dwordx4 v[140:143], v196, %[rs], %[su] offen offset:3072 sc1\n" \
".Lrd_rs7%=:\ns_mov_b64 exec, s[62:63]\ns_cbranch_execz .Lrd_rs8%=\nbuffer_load_dwordx4 v[148:151], v196, %[rs], s57 offen offset:1024 sc1\nbuffer_load_dwordx4 v[156:159], v196, %[rs], s57 offen offset:3072 sc1\n.Lrd_rs8%=:\ns_mov_b64 exec, -1\nv_mov_b64 v[180:181], 0\n" \
"v_mov_b64 v[182:183], 0\nv_mov_b64 v[184:185], 0\nv_mov_b64 v[186:187], 0\nv_mov_b64 v[188:189], 0\nv_mov_b64 v[190:191], 0\nv_mov_b64 v[192:193], 0\nv_mov_b64 v[194:195], 0\ns_waitcnt vmcnt(0)\n" \
"s_add_u32 s43, s43, 1\ns_cmp_lt_u32 s43, 262144\ns_cbranch_scc1 .Lrd_ra%=\ns_branch .Lrd_abort%=\n.Lrd_y1%=:\nv_accvgpr_read_b32 v206, a32\nbuffer_load_dword v208, v206, %[rs], 0 offen sc1\ns_waitcnt vmcnt(0)\n" \
"v_cmp_gt_i32 vcc, 1, v208\ns_cbranch_vccz .Lrd_y1r%=\ns_add_u32 s43, s43, 1\ns_cmp_lt_u32 s43, 262144\ns_cbranch_scc1 .Lrd_y1%=\ns_branch .Lrd_abort%=\n.Lrd_tre0%=:\ns_add_u32 s60, s58, 16384\n" \
"buffer_load_dwordx4 v[212:215], v196, %[rs], s60 offen offset:0 sc1\ns_waitcnt vmcnt(0)\nv_bitop3_b32 v209, v212, v214, s61 bitop3:0x7e\nv_cmp_gt_i32 vcc, 0, v209\ns_cbranch_vccz .Lrd_tl0%=\ns_add_u32 s43, s43, 1\ns_cmp_lt_u32 s43, 262144\ns_cbranch_scc1 .Lrd_tre0%=\n" \
"s_branch .Lrd_abort%=\n.Lrd_tre1%=:\ns_add_u32 s60, s58, 0\nbuffer_load_dwordx4 v[216:219], v196, %[rs], s60 offen offset:0 sc1\ns_waitcnt vmcnt(0)\nv_bitop3_b32 v209, v216, v218, s61 bitop3:0x7e\nv_cmp_gt_i32 vcc, 0, v209\ns_cbranch_vccz .Lrd_tl1%=\n" \
"s_add_u32 s43, s43, 1\ns_cmp_lt_u32 s43, 262144\ns_cbranch_scc1 .Lrd_tre1%=\ns_branch .Lrd_abort%=\n.Lrd_tre2%=:\ns_add_u32 s60, s58, 0\nbuffer_load_dwordx4 v[220:223], v196, %[rs], s60 offen offset:1024 sc1\ns_waitcnt vmcnt(0)\n" \
"v_bitop3_b32 v209, v220, v222, s61 bitop3:0x7e\nv_cmp_gt_i32 vcc, 0, v209\ns_cbranch_vccz .Lrd_tl2%=\ns_add_u32 s43, s43, 1\ns_cmp_lt_u32 s43, 262144\ns_cbranch_scc1 .Lrd_tre2%=\ns_branch .Lrd_abort%=\n.Lrd_tre3%=:\n" \
"s_add_u32 s60, s58, 16384\nbuffer_load_dwordx4 v[224:227], v196, %[rs], s60 offen offset:1024 sc1\ns_waitcnt vmcnt(0)\nv_bitop3_b32 v209, v224, v226, s61 bitop3:0x7e\nv_cmp_gt_i32 vcc, 0, v209\ns_cbranch_vccz .Lrd_tl3%=\ns_add_u32 s43, s43, 1\ns_cmp_lt_u32 s43, 262144\n" \
"s_cbranch_scc1 .Lrd_tre3%=\ns_branch .Lrd_abort%=\n.Lrd_tre4%=:\ns_add_u32 s60, s58, 0\nbuffer_load_dwordx4 v[228:231], v196, %[rs], s60 offen offset:2048 sc1\ns_waitcnt vmcnt(0)\nv_bitop3_b32 v209, v228, v230, s61 bitop3:0x7e\nv_cmp_gt_i32 vcc, 0, v209\n" \
"s_cbranch_vccz .Lrd_tl4%=\ns_add_u32 s43, s43, 1\ns_cmp_lt_u32 s43, 262144\ns_cbranch_scc1 .Lrd_tre4%=\ns_branch .Lrd_abort%=\n.Lrd_tre5%=:\ns_add_u32 s60, s58, 0\nbuffer_load_dwordx4 v[232:235], v196, %[rs], s60 offen offset:3072 sc1\n" \
"s_waitcnt vmcnt(0)\nv_bitop3_b32 v209, v232, v234, s61 bitop3:0x7e\nv_cmp_gt_i32 vcc, 0, v209\ns_cbranch_vccz .Lrd_tl5%=\ns_add_u32 s43, s43, 1\ns_cmp_lt_u32 s43, 262144\ns_cbranch_scc1 .Lrd_tre5%=\ns_branch .Lrd_abort%=\n" \
".Lrd_tre6%=:\ns_add_u32 s60, s58, 16384\nbuffer_load_dwordx4 v[236:239], v196, %[rs], s60 offen offset:2048 sc1\ns_waitcnt vmcnt(0)\nv_bitop3_b32 v209, v236, v238, s61 bitop3:0x7e\nv_cmp_gt_i32 vcc, 0, v209\ns_cbranch_vccz .Lrd_tl6%=\ns_add_u32 s43, s43, 1\n" \
"s_cmp_lt_u32 s43, 262144\ns_cbranch_scc1 .Lrd_tre6%=\ns_branch .Lrd_abort%=\n.Lrd_tre7%=:\ns_add_u32 s60, s58, 4096\nbuffer_load_dwordx4 v[240:243], v196, %[rs], s60 offen offset:0 sc1\ns_waitcnt vmcnt(0)\nv_bitop3_b32 v209, v240, v242, s61 bitop3:0x7e\n" \
"v_cmp_gt_i32 vcc, 0, v209\ns_cbranch_vccz .Lrd_tl7%=\ns_add_u32 s43, s43, 1\ns_cmp_lt_u32 s43, 262144\ns_cbranch_scc1 .Lrd_tre7%=\ns_branch .Lrd_abort%=\n.Lrd_tre8%=:\ns_add_u32 s60, s58, 4096\n" \
"buffer_load_dwordx4 v[244:247], v196, %[rs], s60 offen offset:1024 sc1\ns_waitcnt vmcnt(0)\nv_bitop3_b32 v209, v244, v246, s61 bitop3:0x7e\nv_cmp_gt_i32 vcc, 0, v209\ns_cbranch_vccz .Lrd_tl8%=\ns_add_u32 s43, s43, 1\ns_cmp_lt_u32 s43, 262144\ns_cbranch_scc1 .Lrd_tre8%=\n" \
"s_branch .Lrd_abort%=\n.Lrd_tre9%=:\ns_add_u32 s60, s58, 16384\nbuffer_load_dwordx4 v[248:251], v196, %[rs], s60 offen offset:3072 sc1\ns_waitcnt vmcnt(0)\nv_bitop3_b32 v209, v248, v250, s61 bitop3:0x7e\nv_cmp_gt_i32 vcc, 0, v209\ns_cbranch_vccz .Lrd_tl9%=\n" \
"s_add_u32 s43, s43, 1\ns_cmp_lt_u32 s43, 262144\ns_cbranch_scc1 .Lrd_tre9%=\ns_branch .Lrd_abort%=\n.Lrd_tre10%=:\ns_add_u32 s60, s58, 20480\nbuffer_load_dwordx4 v[252:255], v196, %[rs], s60 offen offset:0 sc1\ns_waitcnt vmcnt(0)\n" \
"v_bitop3_b32 v209, v252, v254, s61 bitop3:0x7e\nv_cmp_gt_i32 vcc, 0, v209\ns_cbranch_vccz .Lrd_tl10%=\ns_add_u32 s43, s43, 1\ns_cmp_lt_u32 s43, 262144\ns_cbranch_scc1 .Lrd_tre10%=\ns_branch .Lrd_abort%=\n.Lrd_tre11%=:\n" \
"s_add_u32 s60, s58, 20480\nbuffer_load_dwordx4 v[160:163], v196, %[rs], s60 offen offset:2048 sc1\ns_waitcnt vmcnt(0)\nv_bitop3_b32 v209, v160, v162, s61 bitop3:0x7e\nv_cmp_gt_i32 vcc, 0, v209\ns_cbranch_vccz .Lrd_tl11%=\ns_add_u32 s43, s43, 1\ns_cmp_lt_u32 s43, 262144\n" \
"s_cbranch_scc1 .Lrd_tre11%=\ns_branch .Lrd_abort%=\n.Lrd_tre12%=:\ns_add_u32 s60, s58, 4096\nbuffer_load_dwordx4 v[164:167], v196, %[rs], s60 offen offset:2048 sc1\ns_waitcnt vmcnt(0)\nv_bitop3_b32 v209, v164, v166, s61 bitop3:0x7e\nv_cmp_gt_i32 vcc, 0, v209\n" \
"s_cbranch_vccz .Lrd_tl12%=\ns_add_u32 s43, s43, 1\ns_cmp_lt_u32 s43, 262144\ns_cbranch_scc1 .Lrd_tre12%=\ns_branch .Lrd_abort%=\n.Lrd_tre13%=:\ns_add_u32 s60, s58, 4096\nbuffer_load_dwordx4 v[168:171], v196, %[rs], s60 offen offset:3072 sc1\n" \
"s_waitcnt vmcnt(0)\nv_bitop3_b32 v209, v168, v170, s61 bitop3:0x7e\nv_cmp_gt_i32 vcc, 0, v209\ns_cbranch_vccz .Lrd_tl13%=\ns_add_u32 s43, s43, 1\ns_cmp_lt_u32 s43, 262144\ns_cbranch_scc1 .Lrd_tre13%=\ns_branch .Lrd_abort%=\n" \
".Lrd_tre14%=:\ns_add_u32 s60, s58, 20480\nbuffer_load_dwordx4 v[172:175], v196, %[rs], s60 offen offset:1024 sc1\ns_waitcnt vmcnt(0)\nv_bitop3_b32 v209, v172, v174, s61 bitop3:0x7e\nv_cmp_gt_i32 vcc, 0, v209\ns_cbranch_vccz .Lrd_tl14%=\ns_add_u32 s43, s43, 1\n" \
"s_cmp_lt_u32 s43, 262144\ns_cbranch_scc1 .Lrd_tre14%=\ns_branch .Lrd_abort%=\n.Lrd_tre15%=:\ns_add_u32 s60, s58, 20480\nbuffer_load_dwordx4 v[176:179], v196, %[rs], s60 offen offset:3072 sc1\ns_waitcnt vmcnt(0)\nv_bitop3_b32 v209, v176, v178, s61 bitop3:0x7e\n" \
"v_cmp_gt_i32 vcc, 0, v209\ns_cbranch_vccz .Lrd_tl15%=\ns_add_u32 s43, s43, 1\ns_cmp_lt_u32 s43, 262144\ns_cbranch_scc1 .Lrd_tre15%=\ns_branch .Lrd_abort%=\n.Lrd_abort%=:\ns_mov_b64 exec, -1\n" \
"v_mov_b32 v16, 0\ns_movk_i32 s44, 6400\nbuffer_store_dword v16, off, %[rs], s44 sc1\n.Lrd_end%=:\ns_waitcnt vmcnt(0) lgkmcnt(0)\n" \
""
#define GG_ASM \
"v_mbcnt_lo_u32_b32 v255, -1, 0\nv_mbcnt_hi_u32_b32 v255, -1, v255\nv_lshlrev_b32 v255, 2, v255\nv_add_u32 v255, %[tb], v255\nds_read_b32 v0, v255 offset:0\nds_read_b32 v1, v255 offset:256\nds_read_b32 v2, v255 offset:512\nds_read_b32 v3, v255 offset:768\n" \
"ds_read_b32 v4, v255 offset:1024\nds_read_b32 v5, v255 offset:1280\nds_read_b32 v92, v255 offset:1536\nds_read_b32 v93, v255 offset:1792\nds_read_b32 v94, v255 offset:2048\ns_waitcnt lgkmcnt(0)\nv_mov_b32 v6, 0x3f803f80\nv_mov_b32 v7, 0x3f803f80\n" \
"s_add_u32 m0, %[lw], 16384\ns_add_u32 s44, %[wo], 0\nbuffer_load_dwordx4 v92, %[rw], s44 offen nt lds\ns_add_u32 m0, %[lw], 17408\ns_add_u32 s44, %[wo], 57344\nbuffer_load_dwordx4 v92, %[rw], s44 offen nt lds\ns_add_u32 m0, %[lw], 18432\ns_add_u32 s44, %[wo], 114688\n" \
"buffer_load_dwordx4 v92, %[rw], s44 offen nt lds\ns_add_u32 m0, %[lw], 19456\ns_add_u32 s44, %[wo], 172032\nbuffer_load_dwordx4 v92, %[rw], s44 offen nt lds\ns_add_u32 m0, %[lw], 20480\ns_add_u32 s44, %[wo], 229376\nbuffer_load_dwordx4 v92, %[rw], s44 offen nt lds\ns_add_u32 m0, %[lw], 21504\n" \
"s_add_u32 s44, %[wo], 286720\nbuffer_load_dwordx4 v92, %[rw], s44 offen nt lds\ns_add_u32 m0, %[lw], 22528\ns_add_u32 s44, %[wo], 344064\nbuffer_load_dwordx4 v92, %[rw], s44 offen nt lds\ns_add_u32 m0, %[lw], 23552\ns_add_u32 s44, %[wo], 401408\nbuffer_load_dwordx4 v92, %[rw], s44 offen nt lds\n" \
"s_add_u32 m0, %[tb], 0\ns_add_u32 s44, %[wo], 1835008\nbuffer_load_dwordx4 v92, %[rw], s44 offen nt lds\ns_add_u32 m0, %[tb], 1024\ns_add_u32 s44, %[wo], 1892352\nbuffer_load_dwordx4 v92, %[rw], s44 offen nt lds\ns_add_u32 m0, %[tb], 2048\ns_add_u32 s44, %[wo], 1949696\n" \
"buffer_load_dwordx4 v92, %[rw], s44 offen nt lds\ns_add_u32 m0, %[tb], 3072\ns_add_u32 s44, %[wo], 2007040\nbuffer_load_dwordx4 v92, %[rw], s44 offen nt lds\ns_add_u32 m0, %[tb], 4096\ns_add_u32 s44, %[wo], 2064384\nbuffer_load_dwordx4 v92, %[rw], s44 offen nt lds\ns_add_u32 m0, %[tb], 5120\n" \
"s_add_u32 s44, %[wo], 2121728\nbuffer_load_dwordx4 v92, %[rw], s44 offen nt lds\ns_add_u32 m0, %[tb], 6144\ns_add_u32 s44, %[wo], 2179072\nbuffer_load_dwordx4 v92, %[rw], s44 offen nt lds\ns_add_u32 m0, %[tb], 7168\ns_add_u32 s44, %[wo], 2236416\nbuffer_load_dwordx4 v92, %[rw], s44 offen nt lds\n" \
"s_add_u32 m0, %[tb], 8192\ns_add_u32 s44, %[wo], 3670016\nbuffer_load_dwordx4 v92, %[rw], s44 offen nt lds\ns_add_u32 m0, %[tb], 9216\ns_add_u32 s44, %[wo], 3727360\nbuffer_load_dwordx4 v92, %[rw], s44 offen nt lds\ns_add_u32 m0, %[tb], 10240\ns_add_u32 s44, %[wo], 3784704\n" \
"buffer_load_dwordx4 v92, %[rw], s44 offen nt lds\ns_add_u32 m0, %[tb], 11264\ns_add_u32 s44, %[wo], 3842048\nbuffer_load_dwordx4 v92, %[rw], s44 offen nt lds\ns_add_u32 m0, %[lw], 0\ns_add_u32 s44, %[wo], 3899392\nbuffer_load_dwordx4 v92, %[rw], s44 offen nt lds\ns_add_u32 m0, %[lw], 1024\n" \
"s_add_u32 s44, %[wo], 3956736\nbuffer_load_dwordx4 v92, %[rw], s44 offen nt lds\ns_add_u32 m0, %[lw], 2048\ns_add_u32 s44, %[wo], 4014080\nbuffer_load_dwordx4 v92, %[rw], s44 offen nt lds\ns_add_u32 m0, %[lw], 3072\ns_add_u32 s44, %[wo], 4071424\nbuffer_load_dwordx4 v92, %[rw], s44 offen nt lds\n" \
"s_add_u32 m0, %[lw], 4096\ns_add_u32 s44, %[wo], 5505024\nbuffer_load_dwordx4 v92, %[rw], s44 offen nt lds\ns_add_u32 m0, %[lw], 5120\ns_add_u32 s44, %[wo], 5562368\nbuffer_load_dwordx4 v92, %[rw], s44 offen nt lds\ns_add_u32 m0, %[lw], 6144\ns_add_u32 s44, %[wo], 5619712\n" \
"buffer_load_dwordx4 v92, %[rw], s44 offen nt lds\ns_add_u32 m0, %[lw], 7168\ns_add_u32 s44, %[wo], 5677056\nbuffer_load_dwordx4 v92, %[rw], s44 offen nt lds\ns_add_u32 m0, %[lw], 8192\ns_add_u32 s44, %[wo], 5734400\nbuffer_load_dwordx4 v92, %[rw], s44 offen nt lds\ns_add_u32 m0, %[lw], 9216\n" \
"s_add_u32 s44, %[wo], 5791744\nbuffer_load_dwordx4 v92, %[rw], s44 offen nt lds\ns_add_u32 m0, %[lw], 10240\ns_add_u32 s44, %[wo], 5849088\nbuffer_load_dwordx4 v92, %[rw], s44 offen nt lds\ns_add_u32 m0, %[lw], 11264\ns_add_u32 s44, %[wo], 5906432\nbuffer_load_dwordx4 v92, %[rw], s44 offen nt lds\n" \
"s_waitcnt vmcnt(28)\ns_barrier\ns_add_u32 m0, %[lw], 12288\ns_add_u32 s44, %[wo], 458752\nbuffer_load_dwordx4 v92, %[rw], s44 offen nt lds\ns_add_u32 m0, %[lw], 13312\ns_add_u32 s44, %[wo], 516096\nbuffer_load_dwordx4 v92, %[rw], s44 offen nt lds\n" \
"s_add_u32 m0, %[lw], 14336\ns_add_u32 s44, %[wo], 573440\nbuffer_load_dwordx4 v92, %[rw], s44 offen nt lds\ns_add_u32 m0, %[lw], 15360\ns_add_u32 s44, %[wo], 630784\nbuffer_load_dwordx4 v92, %[rw], s44 offen nt lds\nds_read_b64_tr_b16 a[0:1], v94 offset:16384\nds_read_b64_tr_b16 a[2:3], v94 offset:16896\n" \
"ds_read_b64_tr_b16 a[8:9], v94 offset:16392\nds_read_b64_tr_b16 a[10:11], v94 offset:16904\nds_read_b64_tr_b16 a[16:17], v94 offset:40960\nds_read_b64_tr_b16 a[18:19], v94 offset:41472\nds_read_b64_tr_b16 a[24:25], v94 offset:40968\nds_read_b64_tr_b16 a[26:27], v94 offset:41480\ns_waitcnt lgkmcnt(0)\nv_mfma_f32_16x16x16_bf16 v[8:11], a[0:1], v[6:7], 0\n" \
"v_mfma_f32_16x16x16_bf16 v[12:15], a[8:9], v[6:7], 0\nv_mfma_f32_16x16x16_bf16 v[16:19], a[16:17], v[6:7], 0\nv_mfma_f32_16x16x16_bf16 v[20:23], a[24:25], v[6:7], 0\nv_mfma_f32_16x16x16_bf16 v[8:11], a[2:3], v[6:7], v[8:11]\nv_mfma_f32_16x16x16_bf16 v[12:15], a[10:11], v[6:7], v[12:15]\nv_mfma_f32_16x16x16_bf16 v[16:19], a[18:19], v[6:7], v[16:19]\nv_mfma_f32_16x16x16_bf16 v[20:23], a[26:27], v[6:7], v[20:23]\ns_waitcnt vmcnt(28)\n" \
"s_barrier\ns_add_u32 m0, %[lw], 16384\ns_add_u32 s44, %[wo], 688128\nbuffer_load_dwordx4 v92, %[rw], s44 offen nt lds\ns_add_u32 m0, %[lw], 17408\ns_add_u32 s44, %[wo], 745472\nbuffer_load_dwordx4 v92, %[rw], s44 offen nt lds\ns_add_u32 m0, %[lw], 18432\n" \
"s_add_u32 s44, %[wo], 802816\nbuffer_load_dwordx4 v92, %[rw], s44 offen nt lds\ns_add_u32 m0, %[lw], 19456\ns_add_u32 s44, %[wo], 860160\nbuffer_load_dwordx4 v92, %[rw], s44 offen nt lds\nds_read_b64_tr_b16 a[4:5], v94 offset:20480\nds_read_b64_tr_b16 a[6:7], v94 offset:20992\nds_read_b64_tr_b16 a[12:13], v94 offset:20488\n" \
"ds_read_b64_tr_b16 a[14:15], v94 offset:21000\nds_read_b64_tr_b16 a[20:21], v94 offset:45056\nds_read_b64_tr_b16 a[22:23], v94 offset:45568\nds_read_b64_tr_b16 a[28:29], v94 offset:45064\nds_read_b64_tr_b16 a[30:31], v94 offset:45576\ns_waitcnt lgkmcnt(0)\nv_mfma_f32_16x16x16_bf16 v[8:11], a[4:5], v[6:7], v[8:11]\nv_mfma_f32_16x16x16_bf16 v[12:15], a[12:13], v[6:7], v[12:15]\n" \
"v_mfma_f32_16x16x16_bf16 v[16:19], a[20:21], v[6:7], v[16:19]\nv_mfma_f32_16x16x16_bf16 v[20:23], a[28:29], v[6:7], v[20:23]\nv_mfma_f32_16x16x16_bf16 v[8:11], a[6:7], v[6:7], v[8:11]\nv_mfma_f32_16x16x16_bf16 v[12:15], a[14:15], v[6:7], v[12:15]\nv_mfma_f32_16x16x16_bf16 v[16:19], a[22:23], v[6:7], v[16:19]\nv_mfma_f32_16x16x16_bf16 v[20:23], a[30:31], v[6:7], v[20:23]\ns_waitcnt vmcnt(28)\ns_barrier\n" \
"s_add_u32 m0, %[lw], 20480\ns_add_u32 s44, %[wo], 917504\nbuffer_load_dwordx4 v92, %[rw], s44 offen nt lds\ns_add_u32 m0, %[lw], 21504\ns_add_u32 s44, %[wo], 974848\nbuffer_load_dwordx4 v92, %[rw], s44 offen nt lds\ns_add_u32 m0, %[lw], 22528\ns_add_u32 s44, %[wo], 1032192\n" \
"buffer_load_dwordx4 v92, %[rw], s44 offen nt lds\ns_add_u32 m0, %[lw], 23552\ns_add_u32 s44, %[wo], 1089536\nbuffer_load_dwordx4 v92, %[rw], s44 offen nt lds\nds_read_b64_tr_b16 a[104:105], v93 offset:0\nds_read_b64_tr_b16 a[106:107], v93 offset:512\nds_read_b64_tr_b16 a[112:113], v93 offset:8\nds_read_b64_tr_b16 a[114:115], v93 offset:520\n" \
"ds_read_b64_tr_b16 a[120:121], v93 offset:12288\nds_read_b64_tr_b16 a[122:123], v93 offset:12800\nds_read_b64_tr_b16 a[128:129], v93 offset:12296\nds_read_b64_tr_b16 a[130:131], v93 offset:12808\ns_waitcnt lgkmcnt(0)\nv_mfma_f32_16x16x16_bf16 v[8:11], a[104:105], v[6:7], v[8:11]\nv_mfma_f32_16x16x16_bf16 v[12:15], a[112:113], v[6:7], v[12:15]\nv_mfma_f32_16x16x16_bf16 v[16:19], a[120:121], v[6:7], v[16:19]\n" \
"v_mfma_f32_16x16x16_bf16 v[20:23], a[128:129], v[6:7], v[20:23]\nv_mfma_f32_16x16x16_bf16 v[8:11], a[106:107], v[6:7], v[8:11]\nv_mfma_f32_16x16x16_bf16 v[12:15], a[114:115], v[6:7], v[12:15]\nv_mfma_f32_16x16x16_bf16 v[16:19], a[122:123], v[6:7], v[16:19]\nv_mfma_f32_16x16x16_bf16 v[20:23], a[130:131], v[6:7], v[20:23]\ns_waitcnt vmcnt(28)\ns_barrier\ns_add_u32 m0, %[tb], 0\n" \
"s_add_u32 s44, %[wo], 1146880\nbuffer_load_dwordx4 v92, %[rw], s44 offen nt lds\ns_add_u32 m0, %[tb], 1024\ns_add_u32 s44, %[wo], 1204224\nbuffer_load_dwordx4 v92, %[rw], s44 offen nt lds\ns_add_u32 m0, %[tb], 2048\ns_add_u32 s44, %[wo], 1261568\nbuffer_load_dwordx4 v92, %[rw], s44 offen nt lds\n" \
"s_add_u32 m0, %[tb], 3072\ns_add_u32 s44, %[wo], 1318912\nbuffer_load_dwordx4 v92, %[rw], s44 offen nt lds\nds_read_b64_tr_b16 a[108:109], v93 offset:4096\nds_read_b64_tr_b16 a[110:111], v93 offset:4608\nds_read_b64_tr_b16 a[116:117], v93 offset:4104\nds_read_b64_tr_b16 a[118:119], v93 offset:4616\nds_read_b64_tr_b16 a[124:125], v93 offset:16384\n" \
"ds_read_b64_tr_b16 a[126:127], v93 offset:16896\nds_read_b64_tr_b16 a[132:133], v93 offset:16392\nds_read_b64_tr_b16 a[134:135], v93 offset:16904\ns_waitcnt lgkmcnt(0)\nv_mfma_f32_16x16x16_bf16 v[8:11], a[108:109], v[6:7], v[8:11]\nv_mfma_f32_16x16x16_bf16 v[12:15], a[116:117], v[6:7], v[12:15]\nv_mfma_f32_16x16x16_bf16 v[16:19], a[124:125], v[6:7], v[16:19]\nv_mfma_f32_16x16x16_bf16 v[20:23], a[132:133], v[6:7], v[20:23]\n" \
"v_mfma_f32_16x16x16_bf16 v[8:11], a[110:111], v[6:7], v[8:11]\nv_mfma_f32_16x16x16_bf16 v[12:15], a[118:119], v[6:7], v[12:15]\nv_mfma_f32_16x16x16_bf16 v[16:19], a[126:127], v[6:7], v[16:19]\nv_mfma_f32_16x16x16_bf16 v[20:23], a[134:135], v[6:7], v[20:23]\ns_waitcnt vmcnt(28)\ns_barrier\ns_add_u32 m0, %[tb], 4096\ns_add_u32 s44, %[wo], 1376256\n" \
"buffer_load_dwordx4 v92, %[rw], s44 offen nt lds\ns_add_u32 m0, %[tb], 5120\ns_add_u32 s44, %[wo], 1433600\nbuffer_load_dwordx4 v92, %[rw], s44 offen nt lds\ns_add_u32 m0, %[tb], 6144\ns_add_u32 s44, %[wo], 1490944\nbuffer_load_dwordx4 v92, %[rw], s44 offen nt lds\ns_add_u32 m0, %[tb], 7168\n" \
"s_add_u32 s44, %[wo], 1548288\nbuffer_load_dwordx4 v92, %[rw], s44 offen nt lds\nds_read_b64_tr_b16 a[208:209], v93 offset:8192\nds_read_b64_tr_b16 a[210:211], v93 offset:8704\nds_read_b64_tr_b16 a[216:217], v93 offset:8200\nds_read_b64_tr_b16 a[218:219], v93 offset:8712\nds_read_b64_tr_b16 a[224:225], v93 offset:20480\nds_read_b64_tr_b16 a[226:227], v93 offset:20992\n" \
"ds_read_b64_tr_b16 a[232:233], v93 offset:20488\nds_read_b64_tr_b16 a[234:235], v93 offset:21000\ns_waitcnt lgkmcnt(0)\nv_mfma_f32_16x16x16_bf16 v[8:11], a[208:209], v[6:7], v[8:11]\nv_mfma_f32_16x16x16_bf16 v[12:15], a[216:217], v[6:7], v[12:15]\nv_mfma_f32_16x16x16_bf16 v[16:19], a[224:225], v[6:7], v[16:19]\nv_mfma_f32_16x16x16_bf16 v[20:23], a[232:233], v[6:7], v[20:23]\nv_mfma_f32_16x16x16_bf16 v[8:11], a[210:211], v[6:7], v[8:11]\n" \
"v_mfma_f32_16x16x16_bf16 v[12:15], a[218:219], v[6:7], v[12:15]\nv_mfma_f32_16x16x16_bf16 v[16:19], a[226:227], v[6:7], v[16:19]\nv_mfma_f32_16x16x16_bf16 v[20:23], a[234:235], v[6:7], v[20:23]\ns_waitcnt vmcnt(28)\ns_barrier\ns_add_u32 m0, %[tb], 8192\ns_add_u32 s44, %[wo], 1605632\nbuffer_load_dwordx4 v92, %[rw], s44 offen nt lds\n" \
"s_add_u32 m0, %[tb], 9216\ns_add_u32 s44, %[wo], 1662976\nbuffer_load_dwordx4 v92, %[rw], s44 offen nt lds\ns_add_u32 m0, %[tb], 10240\ns_add_u32 s44, %[wo], 1720320\nbuffer_load_dwordx4 v92, %[rw], s44 offen nt lds\ns_add_u32 m0, %[tb], 11264\ns_add_u32 s44, %[wo], 1777664\n" \
"buffer_load_dwordx4 v92, %[rw], s44 offen nt lds\nds_read_b64_tr_b16 a[212:213], v94 offset:0\nds_read_b64_tr_b16 a[214:215], v94 offset:512\nds_read_b64_tr_b16 a[220:221], v94 offset:8\nds_read_b64_tr_b16 a[222:223], v94 offset:520\nds_read_b64_tr_b16 a[228:229], v94 offset:24576\nds_read_b64_tr_b16 a[230:231], v94 offset:25088\nds_read_b64_tr_b16 a[236:237], v94 offset:24584\n" \
"ds_read_b64_tr_b16 a[238:239], v94 offset:25096\ns_waitcnt lgkmcnt(0)\nv_mfma_f32_16x16x16_bf16 v[8:11], a[212:213], v[6:7], v[8:11]\nv_mfma_f32_16x16x16_bf16 v[12:15], a[220:221], v[6:7], v[12:15]\nv_mfma_f32_16x16x16_bf16 v[16:19], a[228:229], v[6:7], v[16:19]\nv_mfma_f32_16x16x16_bf16 v[20:23], a[236:237], v[6:7], v[20:23]\nv_mfma_f32_16x16x16_bf16 v[8:11], a[214:215], v[6:7], v[8:11]\nv_mfma_f32_16x16x16_bf16 v[12:15], a[222:223], v[6:7], v[12:15]\n" \
"v_mfma_f32_16x16x16_bf16 v[16:19], a[230:231], v[6:7], v[16:19]\nv_mfma_f32_16x16x16_bf16 v[20:23], a[238:239], v[6:7], v[20:23]\ns_waitcnt vmcnt(28)\ns_barrier\ns_add_u32 m0, %[lw], 0\ns_add_u32 s44, %[wo], 2293760\nbuffer_load_dwordx4 v92, %[rw], s44 offen nt lds\ns_add_u32 m0, %[lw], 1024\n" \
"s_add_u32 s44, %[wo], 2351104\nbuffer_load_dwordx4 v92, %[rw], s44 offen nt lds\ns_add_u32 m0, %[lw], 2048\ns_add_u32 s44, %[wo], 2408448\nbuffer_load_dwordx4 v92, %[rw], s44 offen nt lds\ns_add_u32 m0, %[lw], 3072\ns_add_u32 s44, %[wo], 2465792\nbuffer_load_dwordx4 v92, %[rw], s44 offen nt lds\n" \
"ds_read_b64_tr_b16 v[152:153], v94 offset:4096\nds_read_b64_tr_b16 v[154:155], v94 offset:4608\nds_read_b64_tr_b16 v[160:161], v94 offset:4104\nds_read_b64_tr_b16 v[162:163], v94 offset:4616\nds_read_b64_tr_b16 v[168:169], v94 offset:28672\nds_read_b64_tr_b16 v[170:171], v94 offset:29184\nds_read_b64_tr_b16 v[176:177], v94 offset:28680\nds_read_b64_tr_b16 v[178:179], v94 offset:29192\n" \
"s_waitcnt lgkmcnt(0)\nv_mfma_f32_16x16x16_bf16 v[8:11], v[152:153], v[6:7], v[8:11]\nv_mfma_f32_16x16x16_bf16 v[12:15], v[160:161], v[6:7], v[12:15]\nv_mfma_f32_16x16x16_bf16 v[16:19], v[168:169], v[6:7], v[16:19]\nv_mfma_f32_16x16x16_bf16 v[20:23], v[176:177], v[6:7], v[20:23]\nv_mfma_f32_16x16x16_bf16 v[8:11], v[154:155], v[6:7], v[8:11]\nv_mfma_f32_16x16x16_bf16 v[12:15], v[162:163], v[6:7], v[12:15]\nv_mfma_f32_16x16x16_bf16 v[16:19], v[170:171], v[6:7], v[16:19]\n" \
"v_mfma_f32_16x16x16_bf16 v[20:23], v[178:179], v[6:7], v[20:23]\ns_waitcnt vmcnt(28)\ns_barrier\ns_add_u32 m0, %[lw], 4096\ns_add_u32 s44, %[wo], 2523136\nbuffer_load_dwordx4 v92, %[rw], s44 offen nt lds\ns_add_u32 m0, %[lw], 5120\ns_add_u32 s44, %[wo], 2580480\n" \
"buffer_load_dwordx4 v92, %[rw], s44 offen nt lds\ns_add_u32 m0, %[lw], 6144\ns_add_u32 s44, %[wo], 2637824\nbuffer_load_dwordx4 v92, %[rw], s44 offen nt lds\ns_add_u32 m0, %[lw], 7168\ns_add_u32 s44, %[wo], 2695168\nbuffer_load_dwordx4 v92, %[rw], s44 offen nt lds\nds_read_b64_tr_b16 v[156:157], v94 offset:8192\n" \
"ds_read_b64_tr_b16 v[158:159], v94 offset:8704\nds_read_b64_tr_b16 v[164:165], v94 offset:8200\nds_read_b64_tr_b16 v[166:167], v94 offset:8712\nds_read_b64_tr_b16 v[172:173], v94 offset:32768\nds_read_b64_tr_b16 v[174:175], v94 offset:33280\nds_read_b64_tr_b16 v[180:181], v94 offset:32776\nds_read_b64_tr_b16 v[182:183], v94 offset:33288\ns_waitcnt lgkmcnt(0)\n" \
"v_mfma_f32_16x16x16_bf16 v[8:11], v[156:157], v[6:7], v[8:11]\nv_mfma_f32_16x16x16_bf16 v[12:15], v[164:165], v[6:7], v[12:15]\nv_mfma_f32_16x16x16_bf16 v[16:19], v[172:173], v[6:7], v[16:19]\nv_mfma_f32_16x16x16_bf16 v[20:23], v[180:181], v[6:7], v[20:23]\nv_mfma_f32_16x16x16_bf16 v[8:11], v[158:159], v[6:7], v[8:11]\nv_mfma_f32_16x16x16_bf16 v[12:15], v[166:167], v[6:7], v[12:15]\nv_mfma_f32_16x16x16_bf16 v[16:19], v[174:175], v[6:7], v[16:19]\nv_mfma_f32_16x16x16_bf16 v[20:23], v[182:183], v[6:7], v[20:23]\n" \
"s_waitcnt vmcnt(28)\ns_barrier\ns_add_u32 m0, %[lw], 8192\ns_add_u32 s44, %[wo], 2752512\nbuffer_load_dwordx4 v92, %[rw], s44 offen nt lds\ns_add_u32 m0, %[lw], 9216\ns_add_u32 s44, %[wo], 2809856\nbuffer_load_dwordx4 v92, %[rw], s44 offen nt lds\n" \
"s_add_u32 m0, %[lw], 10240\ns_add_u32 s44, %[wo], 2867200\nbuffer_load_dwordx4 v92, %[rw], s44 offen nt lds\ns_add_u32 m0, %[lw], 11264\ns_add_u32 s44, %[wo], 2924544\nbuffer_load_dwordx4 v92, %[rw], s44 offen nt lds\nds_read_b64_tr_b16 a[32:33], v94 offset:12288\nds_read_b64_tr_b16 a[34:35], v94 offset:12800\n" \
"ds_read_b64_tr_b16 v[88:89], v94 offset:12296\nds_read_b64_tr_b16 v[90:91], v94 offset:12808\nds_read_b64_tr_b16 a[40:41], v94 offset:36864\nds_read_b64_tr_b16 a[42:43], v94 offset:37376\nds_read_b64_tr_b16 a[48:49], v94 offset:36872\nds_read_b64_tr_b16 a[50:51], v94 offset:37384\ns_waitcnt lgkmcnt(0)\nv_mfma_f32_16x16x16_bf16 v[8:11], a[32:33], v[6:7], v[8:11]\n" \
"v_mfma_f32_16x16x16_bf16 v[12:15], v[88:89], v[6:7], v[12:15]\nv_mfma_f32_16x16x16_bf16 v[16:19], a[40:41], v[6:7], v[16:19]\nv_mfma_f32_16x16x16_bf16 v[20:23], a[48:49], v[6:7], v[20:23]\nv_mfma_f32_16x16x16_bf16 v[8:11], a[34:35], v[6:7], v[8:11]\nv_mfma_f32_16x16x16_bf16 v[12:15], v[90:91], v[6:7], v[12:15]\nv_mfma_f32_16x16x16_bf16 v[16:19], a[42:43], v[6:7], v[16:19]\nv_mfma_f32_16x16x16_bf16 v[20:23], a[50:51], v[6:7], v[20:23]\ns_waitcnt vmcnt(28)\n" \
"s_barrier\ns_add_u32 m0, %[lw], 12288\ns_add_u32 s44, %[wo], 2981888\nbuffer_load_dwordx4 v92, %[rw], s44 offen nt lds\ns_add_u32 m0, %[lw], 13312\ns_add_u32 s44, %[wo], 3039232\nbuffer_load_dwordx4 v92, %[rw], s44 offen nt lds\ns_add_u32 m0, %[lw], 14336\n" \
"s_add_u32 s44, %[wo], 3096576\nbuffer_load_dwordx4 v92, %[rw], s44 offen nt lds\ns_add_u32 m0, %[lw], 15360\ns_add_u32 s44, %[wo], 3153920\nbuffer_load_dwordx4 v92, %[rw], s44 offen nt lds\nds_read_b64_tr_b16 a[36:37], v94 offset:16384\nds_read_b64_tr_b16 a[38:39], v94 offset:16896\nds_read_b64_tr_b16 v[84:85], v94 offset:16392\n" \
"ds_read_b64_tr_b16 v[86:87], v94 offset:16904\nds_read_b64_tr_b16 a[44:45], v94 offset:40960\nds_read_b64_tr_b16 a[46:47], v94 offset:41472\nds_read_b64_tr_b16 a[52:53], v94 offset:40968\nds_read_b64_tr_b16 a[54:55], v94 offset:41480\ns_waitcnt lgkmcnt(0)\nv_mfma_f32_16x16x16_bf16 v[8:11], a[36:37], v[6:7], v[8:11]\nv_mfma_f32_16x16x16_bf16 v[12:15], v[84:85], v[6:7], v[12:15]\n" \
"v_mfma_f32_16x16x16_bf16 v[16:19], a[44:45], v[6:7], v[16:19]\nv_mfma_f32_16x16x16_bf16 v[20:23], a[52:53], v[6:7], v[20:23]\nv_mfma_f32_16x16x16_bf16 v[8:11], a[38:39], v[6:7], v[8:11]\nv_mfma_f32_16x16x16_bf16 v[12:15], v[86:87], v[6:7], v[12:15]\nv_mfma_f32_16x16x16_bf16 v[16:19], a[46:47], v[6:7], v[16:19]\nv_mfma_f32_16x16x16_bf16 v[20:23], a[54:55], v[6:7], v[20:23]\ns_waitcnt vmcnt(28)\ns_barrier\n" \
"s_add_u32 m0, %[lw], 16384\ns_add_u32 s44, %[wo], 3211264\nbuffer_load_dwordx4 v92, %[rw], s44 offen nt lds\ns_add_u32 m0, %[lw], 17408\ns_add_u32 s44, %[wo], 3268608\nbuffer_load_dwordx4 v92, %[rw], s44 offen nt lds\ns_add_u32 m0, %[lw], 18432\ns_add_u32 s44, %[wo], 3325952\n" \
"buffer_load_dwordx4 v92, %[rw], s44 offen nt lds\ns_add_u32 m0, %[lw], 19456\ns_add_u32 s44, %[wo], 3383296\nbuffer_load_dwordx4 v92, %[rw], s44 offen nt lds\nds_read_b64_tr_b16 a[56:57], v94 offset:20480\nds_read_b64_tr_b16 a[58:59], v94 offset:20992\nds_read_b64_tr_b16 a[64:65], v94 offset:20488\nds_read_b64_tr_b16 a[66:67], v94 offset:21000\n" \
"ds_read_b64_tr_b16 v[80:81], v94 offset:45056\nds_read_b64_tr_b16 v[82:83], v94 offset:45568\nds_read_b64_tr_b16 a[72:73], v94 offset:45064\nds_read_b64_tr_b16 a[74:75], v94 offset:45576\ns_waitcnt lgkmcnt(0)\nv_mfma_f32_16x16x16_bf16 v[8:11], a[56:57], v[6:7], v[8:11]\nv_mfma_f32_16x16x16_bf16 v[12:15], a[64:65], v[6:7], v[12:15]\nv_mfma_f32_16x16x16_bf16 v[16:19], v[80:81], v[6:7], v[16:19]\n" \
"v_mfma_f32_16x16x16_bf16 v[20:23], a[72:73], v[6:7], v[20:23]\nv_mfma_f32_16x16x16_bf16 v[8:11], a[58:59], v[6:7], v[8:11]\nv_mfma_f32_16x16x16_bf16 v[12:15], a[66:67], v[6:7], v[12:15]\nv_mfma_f32_16x16x16_bf16 v[16:19], v[82:83], v[6:7], v[16:19]\nv_mfma_f32_16x16x16_bf16 v[20:23], a[74:75], v[6:7], v[20:23]\ns_waitcnt vmcnt(28)\ns_barrier\ns_add_u32 m0, %[lw], 20480\n" \
"s_add_u32 s44, %[wo], 3440640\nbuffer_load_dwordx4 v92, %[rw], s44 offen nt lds\ns_add_u32 m0, %[lw], 21504\ns_add_u32 s44, %[wo], 3497984\nbuffer_load_dwordx4 v92, %[rw], s44 offen nt lds\ns_add_u32 m0, %[lw], 22528\ns_add_u32 s44, %[wo], 3555328\nbuffer_load_dwordx4 v92, %[rw], s44 offen nt lds\n" \
"s_add_u32 m0, %[lw], 23552\ns_add_u32 s44, %[wo], 3612672\nbuffer_load_dwordx4 v92, %[rw], s44 offen nt lds\nds_read_b64_tr_b16 a[60:61], v93 offset:0\nds_read_b64_tr_b16 a[62:63], v93 offset:512\nds_read_b64_tr_b16 a[68:69], v93 offset:8\nds_read_b64_tr_b16 a[70:71], v93 offset:520\nds_read_b64_tr_b16 v[76:77], v93 offset:12288\n" \
"ds_read_b64_tr_b16 v[78:79], v93 offset:12800\nds_read_b64_tr_b16 a[76:77], v93 offset:12296\nds_read_b64_tr_b16 a[78:79], v93 offset:12808\ns_waitcnt lgkmcnt(0)\nv_mfma_f32_16x16x16_bf16 v[8:11], a[60:61], v[6:7], v[8:11]\nv_mfma_f32_16x16x16_bf16 v[12:15], a[68:69], v[6:7], v[12:15]\nv_mfma_f32_16x16x16_bf16 v[16:19], v[76:77], v[6:7], v[16:19]\nv_mfma_f32_16x16x16_bf16 v[20:23], a[76:77], v[6:7], v[20:23]\n" \
"v_mfma_f32_16x16x16_bf16 v[8:11], a[62:63], v[6:7], v[8:11]\nv_mfma_f32_16x16x16_bf16 v[12:15], a[70:71], v[6:7], v[12:15]\nv_mfma_f32_16x16x16_bf16 v[16:19], v[78:79], v[6:7], v[16:19]\nv_mfma_f32_16x16x16_bf16 v[20:23], a[78:79], v[6:7], v[20:23]\ns_waitcnt vmcnt(28)\ns_barrier\ns_add_u32 m0, %[tb], 0\ns_add_u32 s44, %[wo], 4128768\n" \
"buffer_load_dwordx4 v92, %[rw], s44 offen nt lds\ns_add_u32 m0, %[tb], 1024\ns_add_u32 s44, %[wo], 4186112\nbuffer_load_dwordx4 v92, %[rw], s44 offen nt lds\ns_add_u32 m0, %[tb], 2048\ns_add_u32 s44, %[wo], 4243456\nbuffer_load_dwordx4 v92, %[rw], s44 offen nt lds\ns_add_u32 m0, %[tb], 3072\n" \
"s_add_u32 s44, %[wo], 4300800\nbuffer_load_dwordx4 v92, %[rw], s44 offen nt lds\nds_read_b64_tr_b16 a[80:81], v93 offset:4096\nds_read_b64_tr_b16 a[82:83], v93 offset:4608\nds_read_b64_tr_b16 a[88:89], v93 offset:4104\nds_read_b64_tr_b16 a[90:91], v93 offset:4616\nds_read_b64_tr_b16 a[96:97], v93 offset:16384\nds_read_b64_tr_b16 a[98:99], v93 offset:16896\n" \
"ds_read_b64_tr_b16 v[72:73], v93 offset:16392\nds_read_b64_tr_b16 v[74:75], v93 offset:16904\ns_waitcnt lgkmcnt(0)\nv_mfma_f32_16x16x16_bf16 v[8:11], a[80:81], v[6:7], v[8:11]\nv_mfma_f32_16x16x16_bf16 v[12:15], a[88:89], v[6:7], v[12:15]\nv_mfma_f32_16x16x16_bf16 v[16:19], a[96:97], v[6:7], v[16:19]\nv_mfma_f32_16x16x16_bf16 v[20:23], v[72:73], v[6:7], v[20:23]\nv_mfma_f32_16x16x16_bf16 v[8:11], a[82:83], v[6:7], v[8:11]\n" \
"v_mfma_f32_16x16x16_bf16 v[12:15], a[90:91], v[6:7], v[12:15]\nv_mfma_f32_16x16x16_bf16 v[16:19], a[98:99], v[6:7], v[16:19]\nv_mfma_f32_16x16x16_bf16 v[20:23], v[74:75], v[6:7], v[20:23]\ns_waitcnt vmcnt(28)\ns_barrier\ns_add_u32 m0, %[tb], 4096\ns_add_u32 s44, %[wo], 4358144\nbuffer_load_dwordx4 v92, %[rw], s44 offen nt lds\n" \
"s_add_u32 m0, %[tb], 5120\ns_add_u32 s44, %[wo], 4415488\nbuffer_load_dwordx4 v92, %[rw], s44 offen nt lds\ns_add_u32 m0, %[tb], 6144\ns_add_u32 s44, %[wo], 4472832\nbuffer_load_dwordx4 v92, %[rw], s44 offen nt lds\ns_add_u32 m0, %[tb], 7168\ns_add_u32 s44, %[wo], 4530176\n" \
"buffer_load_dwordx4 v92, %[rw], s44 offen nt lds\nds_read_b64_tr_b16 a[84:85], v93 offset:8192\nds_read_b64_tr_b16 a[86:87], v93 offset:8704\nds_read_b64_tr_b16 a[92:93], v93 offset:8200\nds_read_b64_tr_b16 a[94:95], v93 offset:8712\nds_read_b64_tr_b16 a[100:101], v93 offset:20480\nds_read_b64_tr_b16 a[102:103], v93 offset:20992\nds_read_b64_tr_b16 v[68:69], v93 offset:20488\n" \
"ds_read_b64_tr_b16 v[70:71], v93 offset:21000\ns_waitcnt lgkmcnt(0)\nv_mfma_f32_16x16x16_bf16 v[8:11], a[84:85], v[6:7], v[8:11]\nv_mfma_f32_16x16x16_bf16 v[12:15], a[92:93], v[6:7], v[12:15]\nv_mfma_f32_16x16x16_bf16 v[16:19], a[100:101], v[6:7], v[16:19]\nv_mfma_f32_16x16x16_bf16 v[20:23], v[68:69], v[6:7], v[20:23]\nv_mfma_f32_16x16x16_bf16 v[8:11], a[86:87], v[6:7], v[8:11]\nv_mfma_f32_16x16x16_bf16 v[12:15], a[94:95], v[6:7], v[12:15]\n" \
"v_mfma_f32_16x16x16_bf16 v[16:19], a[102:103], v[6:7], v[16:19]\nv_mfma_f32_16x16x16_bf16 v[20:23], v[70:71], v[6:7], v[20:23]\ns_waitcnt vmcnt(28)\ns_barrier\ns_add_u32 m0, %[tb], 8192\ns_add_u32 s44, %[wo], 4587520\nbuffer_load_dwordx4 v92, %[rw], s44 offen nt lds\ns_add_u32 m0, %[tb], 9216\n" \
"s_add_u32 s44, %[wo], 4644864\nbuffer_load_dwordx4 v92, %[rw], s44 offen nt lds\ns_add_u32 m0, %[tb], 10240\ns_add_u32 s44, %[wo], 4702208\nbuffer_load_dwordx4 v92, %[rw], s44 offen nt lds\ns_add_u32 m0, %[tb], 11264\ns_add_u32 s44, %[wo], 4759552\nbuffer_load_dwordx4 v92, %[rw], s44 offen nt lds\n" \
"ds_read_b64_tr_b16 a[136:137], v94 offset:0\nds_read_b64_tr_b16 a[138:139], v94 offset:512\nds_read_b64_tr_b16 v[64:65], v94 offset:8\nds_read_b64_tr_b16 v[66:67], v94 offset:520\nds_read_b64_tr_b16 a[144:145], v94 offset:24576\nds_read_b64_tr_b16 a[146:147], v94 offset:25088\nds_read_b64_tr_b16 a[152:153], v94 offset:24584\nds_read_b64_tr_b16 a[154:155], v94 offset:25096\n" \
"s_waitcnt lgkmcnt(0)\nv_mfma_f32_16x16x16_bf16 v[8:11], a[136:137], v[6:7], v[8:11]\nv_mfma_f32_16x16x16_bf16 v[12:15], v[64:65], v[6:7], v[12:15]\nv_mfma_f32_16x16x16_bf16 v[16:19], a[144:145], v[6:7], v[16:19]\nv_mfma_f32_16x16x16_bf16 v[20:23], a[152:153], v[6:7], v[20:23]\nv_mfma_f32_16x16x16_bf16 v[8:11], a[138:139], v[6:7], v[8:11]\nv_mfma_f32_16x16x16_bf16 v[12:15], v[66:67], v[6:7], v[12:15]\nv_mfma_f32_16x16x16_bf16 v[16:19], a[146:147], v[6:7], v[16:19]\n" \
"v_mfma_f32_16x16x16_bf16 v[20:23], a[154:155], v[6:7], v[20:23]\ns_waitcnt vmcnt(28)\ns_barrier\ns_add_u32 m0, %[lw], 0\ns_add_u32 s44, %[wo], 4816896\nbuffer_load_dwordx4 v92, %[rw], s44 offen nt lds\ns_add_u32 m0, %[lw], 1024\ns_add_u32 s44, %[wo], 4874240\n" \
"buffer_load_dwordx4 v92, %[rw], s44 offen nt lds\ns_add_u32 m0, %[lw], 2048\ns_add_u32 s44, %[wo], 4931584\nbuffer_load_dwordx4 v92, %[rw], s44 offen nt lds\ns_add_u32 m0, %[lw], 3072\ns_add_u32 s44, %[wo], 4988928\nbuffer_load_dwordx4 v92, %[rw], s44 offen nt lds\nds_read_b64_tr_b16 a[140:141], v94 offset:4096\n" \
"ds_read_b64_tr_b16 a[142:143], v94 offset:4608\nds_read_b64_tr_b16 v[60:61], v94 offset:4104\nds_read_b64_tr_b16 v[62:63], v94 offset:4616\nds_read_b64_tr_b16 a[148:149], v94 offset:28672\nds_read_b64_tr_b16 a[150:151], v94 offset:29184\nds_read_b64_tr_b16 a[156:157], v94 offset:28680\nds_read_b64_tr_b16 a[158:159], v94 offset:29192\ns_waitcnt lgkmcnt(0)\n" \
"v_mfma_f32_16x16x16_bf16 v[8:11], a[140:141], v[6:7], v[8:11]\nv_mfma_f32_16x16x16_bf16 v[12:15], v[60:61], v[6:7], v[12:15]\nv_mfma_f32_16x16x16_bf16 v[16:19], a[148:149], v[6:7], v[16:19]\nv_mfma_f32_16x16x16_bf16 v[20:23], a[156:157], v[6:7], v[20:23]\nv_mfma_f32_16x16x16_bf16 v[8:11], a[142:143], v[6:7], v[8:11]\nv_mfma_f32_16x16x16_bf16 v[12:15], v[62:63], v[6:7], v[12:15]\nv_mfma_f32_16x16x16_bf16 v[16:19], a[150:151], v[6:7], v[16:19]\nv_mfma_f32_16x16x16_bf16 v[20:23], a[158:159], v[6:7], v[20:23]\n" \
"s_waitcnt vmcnt(28)\ns_barrier\ns_add_u32 m0, %[lw], 4096\ns_add_u32 s44, %[wo], 5046272\nbuffer_load_dwordx4 v92, %[rw], s44 offen nt lds\ns_add_u32 m0, %[lw], 5120\ns_add_u32 s44, %[wo], 5103616\nbuffer_load_dwordx4 v92, %[rw], s44 offen nt lds\n" \
"s_add_u32 m0, %[lw], 6144\ns_add_u32 s44, %[wo], 5160960\nbuffer_load_dwordx4 v92, %[rw], s44 offen nt lds\ns_add_u32 m0, %[lw], 7168\ns_add_u32 s44, %[wo], 5218304\nbuffer_load_dwordx4 v92, %[rw], s44 offen nt lds\nds_read_b64_tr_b16 a[160:161], v94 offset:8192\nds_read_b64_tr_b16 a[162:163], v94 offset:8704\n" \
"ds_read_b64_tr_b16 a[168:169], v94 offset:8200\nds_read_b64_tr_b16 a[170:171], v94 offset:8712\nds_read_b64_tr_b16 v[56:57], v94 offset:32768\nds_read_b64_tr_b16 v[58:59], v94 offset:33280\nds_read_b64_tr_b16 a[176:177], v94 offset:32776\nds_read_b64_tr_b16 a[178:179], v94 offset:33288\ns_waitcnt lgkmcnt(0)\nv_mfma_f32_16x16x16_bf16 v[8:11], a[160:161], v[6:7], v[8:11]\n" \
"v_mfma_f32_16x16x16_bf16 v[12:15], a[168:169], v[6:7], v[12:15]\nv_mfma_f32_16x16x16_bf16 v[16:19], v[56:57], v[6:7], v[16:19]\nv_mfma_f32_16x16x16_bf16 v[20:23], a[176:177], v[6:7], v[20:23]\nv_mfma_f32_16x16x16_bf16 v[8:11], a[162:163], v[6:7], v[8:11]\nv_mfma_f32_16x16x16_bf16 v[12:15], a[170:171], v[6:7], v[12:15]\nv_mfma_f32_16x16x16_bf16 v[16:19], v[58:59], v[6:7], v[16:19]\nv_mfma_f32_16x16x16_bf16 v[20:23], a[178:179], v[6:7], v[20:23]\ns_waitcnt vmcnt(28)\n" \
"s_barrier\ns_add_u32 m0, %[lw], 8192\ns_add_u32 s44, %[wo], 5275648\nbuffer_load_dwordx4 v92, %[rw], s44 offen nt lds\ns_add_u32 m0, %[lw], 9216\ns_add_u32 s44, %[wo], 5332992\nbuffer_load_dwordx4 v92, %[rw], s44 offen nt lds\ns_add_u32 m0, %[lw], 10240\n" \
"s_add_u32 s44, %[wo], 5390336\nbuffer_load_dwordx4 v92, %[rw], s44 offen nt lds\ns_add_u32 m0, %[lw], 11264\ns_add_u32 s44, %[wo], 5447680\nbuffer_load_dwordx4 v92, %[rw], s44 offen nt lds\nds_read_b64_tr_b16 a[164:165], v94 offset:12288\nds_read_b64_tr_b16 a[166:167], v94 offset:12800\nds_read_b64_tr_b16 a[172:173], v94 offset:12296\n" \
"ds_read_b64_tr_b16 a[174:175], v94 offset:12808\nds_read_b64_tr_b16 v[52:53], v94 offset:36864\nds_read_b64_tr_b16 v[54:55], v94 offset:37376\nds_read_b64_tr_b16 a[180:181], v94 offset:36872\nds_read_b64_tr_b16 a[182:183], v94 offset:37384\ns_waitcnt lgkmcnt(0)\nv_mfma_f32_16x16x16_bf16 v[8:11], a[164:165], v[6:7], v[8:11]\nv_mfma_f32_16x16x16_bf16 v[12:15], a[172:173], v[6:7], v[12:15]\n" \
"v_mfma_f32_16x16x16_bf16 v[16:19], v[52:53], v[6:7], v[16:19]\nv_mfma_f32_16x16x16_bf16 v[20:23], a[180:181], v[6:7], v[20:23]\nv_mfma_f32_16x16x16_bf16 v[8:11], a[166:167], v[6:7], v[8:11]\nv_mfma_f32_16x16x16_bf16 v[12:15], a[174:175], v[6:7], v[12:15]\nv_mfma_f32_16x16x16_bf16 v[16:19], v[54:55], v[6:7], v[16:19]\nv_mfma_f32_16x16x16_bf16 v[20:23], a[182:183], v[6:7], v[20:23]\ns_waitcnt vmcnt(28)\ns_barrier\n" \
"s_add_u32 m0, %[lw], 12288\ns_add_u32 s44, %[wo], 5963776\nbuffer_load_dwordx4 v92, %[rw], s44 offen nt lds\ns_add_u32 m0, %[lw], 13312\ns_add_u32 s44, %[wo], 6021120\nbuffer_load_dwordx4 v92, %[rw], s44 offen nt lds\ns_add_u32 m0, %[lw], 14336\ns_add_u32 s44, %[wo], 6078464\n" \
"buffer_load_dwordx4 v92, %[rw], s44 offen nt lds\ns_add_u32 m0, %[lw], 15360\ns_add_u32 s44, %[wo], 6135808\nbuffer_load_dwordx4 v92, %[rw], s44 offen nt lds\nds_read_b64_tr_b16 a[184:185], v94 offset:16384\nds_read_b64_tr_b16 a[186:187], v94 offset:16896\nds_read_b64_tr_b16 a[192:193], v94 offset:16392\nds_read_b64_tr_b16 a[194:195], v94 offset:16904\n" \
"ds_read_b64_tr_b16 a[200:201], v94 offset:40960\nds_read_b64_tr_b16 a[202:203], v94 offset:41472\nds_read_b64_tr_b16 v[48:49], v94 offset:40968\nds_read_b64_tr_b16 v[50:51], v94 offset:41480\ns_waitcnt lgkmcnt(0)\nv_mfma_f32_16x16x16_bf16 v[8:11], a[184:185], v[6:7], v[8:11]\nv_mfma_f32_16x16x16_bf16 v[12:15], a[192:193], v[6:7], v[12:15]\nv_mfma_f32_16x16x16_bf16 v[16:19], a[200:201], v[6:7], v[16:19]\n" \
"v_mfma_f32_16x16x16_bf16 v[20:23], v[48:49], v[6:7], v[20:23]\nv_mfma_f32_16x16x16_bf16 v[8:11], a[186:187], v[6:7], v[8:11]\nv_mfma_f32_16x16x16_bf16 v[12:15], a[194:195], v[6:7], v[12:15]\nv_mfma_f32_16x16x16_bf16 v[16:19], a[202:203], v[6:7], v[16:19]\nv_mfma_f32_16x16x16_bf16 v[20:23], v[50:51], v[6:7], v[20:23]\ns_waitcnt vmcnt(28)\ns_barrier\ns_add_u32 m0, %[lw], 16384\n" \
"s_add_u32 s44, %[wo], 6193152\nbuffer_load_dwordx4 v92, %[rw], s44 offen nt lds\ns_add_u32 m0, %[lw], 17408\ns_add_u32 s44, %[wo], 6250496\nbuffer_load_dwordx4 v92, %[rw], s44 offen nt lds\ns_add_u32 m0, %[lw], 18432\ns_add_u32 s44, %[wo], 6307840\nbuffer_load_dwordx4 v92, %[rw], s44 offen nt lds\n" \
"s_add_u32 m0, %[lw], 19456\ns_add_u32 s44, %[wo], 6365184\nbuffer_load_dwordx4 v92, %[rw], s44 offen nt lds\nds_read_b64_tr_b16 a[188:189], v94 offset:20480\nds_read_b64_tr_b16 a[190:191], v94 offset:20992\nds_read_b64_tr_b16 a[196:197], v94 offset:20488\nds_read_b64_tr_b16 a[198:199], v94 offset:21000\nds_read_b64_tr_b16 a[204:205], v94 offset:45056\n" \
"ds_read_b64_tr_b16 a[206:207], v94 offset:45568\nds_read_b64_tr_b16 v[44:45], v94 offset:45064\nds_read_b64_tr_b16 v[46:47], v94 offset:45576\ns_waitcnt lgkmcnt(0)\nv_mfma_f32_16x16x16_bf16 v[8:11], a[188:189], v[6:7], v[8:11]\nv_mfma_f32_16x16x16_bf16 v[12:15], a[196:197], v[6:7], v[12:15]\nv_mfma_f32_16x16x16_bf16 v[16:19], a[204:205], v[6:7], v[16:19]\nv_mfma_f32_16x16x16_bf16 v[20:23], v[44:45], v[6:7], v[20:23]\n" \
"v_mfma_f32_16x16x16_bf16 v[8:11], a[190:191], v[6:7], v[8:11]\nv_mfma_f32_16x16x16_bf16 v[12:15], a[198:199], v[6:7], v[12:15]\nv_mfma_f32_16x16x16_bf16 v[16:19], a[206:207], v[6:7], v[16:19]\nv_mfma_f32_16x16x16_bf16 v[20:23], v[46:47], v[6:7], v[20:23]\ns_waitcnt vmcnt(28)\ns_barrier\ns_add_u32 m0, %[lw], 20480\ns_add_u32 s44, %[wo], 6422528\n" \
"buffer_load_dwordx4 v92, %[rw], s44 offen nt lds\ns_add_u32 m0, %[lw], 21504\ns_add_u32 s44, %[wo], 6479872\nbuffer_load_dwordx4 v92, %[rw], s44 offen nt lds\ns_add_u32 m0, %[lw], 22528\ns_add_u32 s44, %[wo], 6537216\nbuffer_load_dwordx4 v92, %[rw], s44 offen nt lds\ns_add_u32 m0, %[lw], 23552\n" \
"s_add_u32 s44, %[wo], 6594560\nbuffer_load_dwordx4 v92, %[rw], s44 offen nt lds\nds_read_b64_tr_b16 a[240:241], v93 offset:0\nds_read_b64_tr_b16 a[242:243], v93 offset:512\nds_read_b64_tr_b16 v[40:41], v93 offset:8\nds_read_b64_tr_b16 v[42:43], v93 offset:520\nds_read_b64_tr_b16 a[248:249], v93 offset:12288\nds_read_b64_tr_b16 a[250:251], v93 offset:12800\n" \
"ds_read_b64_tr_b16 v[96:97], v93 offset:12296\nds_read_b64_tr_b16 v[98:99], v93 offset:12808\ns_waitcnt lgkmcnt(0)\nv_mfma_f32_16x16x16_bf16 v[8:11], a[240:241], v[6:7], v[8:11]\nv_mfma_f32_16x16x16_bf16 v[12:15], v[40:41], v[6:7], v[12:15]\nv_mfma_f32_16x16x16_bf16 v[16:19], a[248:249], v[6:7], v[16:19]\nv_mfma_f32_16x16x16_bf16 v[20:23], v[96:97], v[6:7], v[20:23]\nv_mfma_f32_16x16x16_bf16 v[8:11], a[242:243], v[6:7], v[8:11]\n" \
"v_mfma_f32_16x16x16_bf16 v[12:15], v[42:43], v[6:7], v[12:15]\nv_mfma_f32_16x16x16_bf16 v[16:19], a[250:251], v[6:7], v[16:19]\nv_mfma_f32_16x16x16_bf16 v[20:23], v[98:99], v[6:7], v[20:23]\ns_waitcnt vmcnt(28)\ns_barrier\ns_add_u32 m0, %[tb], 0\ns_add_u32 s44, %[wo], 6651904\nbuffer_load_dwordx4 v92, %[rw], s44 offen nt lds\n" \
"s_add_u32 m0, %[tb], 1024\ns_add_u32 s44, %[wo], 6709248\nbuffer_load_dwordx4 v92, %[rw], s44 offen nt lds\ns_add_u32 m0, %[tb], 2048\ns_add_u32 s44, %[wo], 6766592\nbuffer_load_dwordx4 v92, %[rw], s44 offen nt lds\ns_add_u32 m0, %[tb], 3072\ns_add_u32 s44, %[wo], 6823936\n" \
"buffer_load_dwordx4 v92, %[rw], s44 offen nt lds\nds_read_b64_tr_b16 a[244:245], v93 offset:4096\nds_read_b64_tr_b16 a[246:247], v93 offset:4608\nds_read_b64_tr_b16 v[36:37], v93 offset:4104\nds_read_b64_tr_b16 v[38:39], v93 offset:4616\nds_read_b64_tr_b16 a[252:253], v93 offset:16384\nds_read_b64_tr_b16 a[254:255], v93 offset:16896\nds_read_b64_tr_b16 v[100:101], v93 offset:16392\n" \
"ds_read_b64_tr_b16 v[102:103], v93 offset:16904\ns_waitcnt lgkmcnt(0)\nv_mfma_f32_16x16x16_bf16 v[8:11], a[244:245], v[6:7], v[8:11]\nv_mfma_f32_16x16x16_bf16 v[12:15], v[36:37], v[6:7], v[12:15]\nv_mfma_f32_16x16x16_bf16 v[16:19], a[252:253], v[6:7], v[16:19]\nv_mfma_f32_16x16x16_bf16 v[20:23], v[100:101], v[6:7], v[20:23]\nv_mfma_f32_16x16x16_bf16 v[8:11], a[246:247], v[6:7], v[8:11]\nv_mfma_f32_16x16x16_bf16 v[12:15], v[38:39], v[6:7], v[12:15]\n" \
"v_mfma_f32_16x16x16_bf16 v[16:19], a[254:255], v[6:7], v[16:19]\nv_mfma_f32_16x16x16_bf16 v[20:23], v[102:103], v[6:7], v[20:23]\ns_waitcnt vmcnt(28)\ns_barrier\ns_add_u32 m0, %[tb], 4096\ns_add_u32 s44, %[wo], 6881280\nbuffer_load_dwordx4 v92, %[rw], s44 offen nt lds\ns_add_u32 m0, %[tb], 5120\n" \
"s_add_u32 s44, %[wo], 6938624\nbuffer_load_dwordx4 v92, %[rw], s44 offen nt lds\ns_add_u32 m0, %[tb], 6144\ns_add_u32 s44, %[wo], 6995968\nbuffer_load_dwordx4 v92, %[rw], s44 offen nt lds\ns_add_u32 m0, %[tb], 7168\ns_add_u32 s44, %[wo], 7053312\nbuffer_load_dwordx4 v92, %[rw], s44 offen nt lds\n" \
"ds_read_b64_tr_b16 v[104:105], v93 offset:8192\nds_read_b64_tr_b16 v[106:107], v93 offset:8704\nds_read_b64_tr_b16 v[112:113], v93 offset:8200\nds_read_b64_tr_b16 v[114:115], v93 offset:8712\nds_read_b64_tr_b16 v[32:33], v93 offset:20480\nds_read_b64_tr_b16 v[34:35], v93 offset:20992\nds_read_b64_tr_b16 v[120:121], v93 offset:20488\nds_read_b64_tr_b16 v[122:123], v93 offset:21000\n" \
"s_waitcnt lgkmcnt(0)\nv_mfma_f32_16x16x16_bf16 v[8:11], v[104:105], v[6:7], v[8:11]\nv_mfma_f32_16x16x16_bf16 v[12:15], v[112:113], v[6:7], v[12:15]\nv_mfma_f32_16x16x16_bf16 v[16:19], v[32:33], v[6:7], v[16:19]\nv_mfma_f32_16x16x16_bf16 v[20:23], v[120:121], v[6:7], v[20:23]\nv_mfma_f32_16x16x16_bf16 v[8:11], v[106:107], v[6:7], v[8:11]\nv_mfma_f32_16x16x16_bf16 v[12:15], v[114:115], v[6:7], v[12:15]\nv_mfma_f32_16x16x16_bf16 v[16:19], v[34:35], v[6:7], v[16:19]\n" \
"v_mfma_f32_16x16x16_bf16 v[20:23], v[122:123], v[6:7], v[20:23]\ns_waitcnt vmcnt(28)\ns_barrier\ns_add_u32 m0, %[tb], 8192\ns_add_u32 s44, %[wo], 7110656\nbuffer_load_dwordx4 v92, %[rw], s44 offen nt lds\ns_add_u32 m0, %[tb], 9216\ns_add_u32 s44, %[wo], 7168000\n" \
"buffer_load_dwordx4 v92, %[rw], s44 offen nt lds\ns_add_u32 m0, %[tb], 10240\ns_add_u32 s44, %[wo], 7225344\nbuffer_load_dwordx4 v92, %[rw], s44 offen nt lds\ns_add_u32 m0, %[tb], 11264\ns_add_u32 s44, %[wo], 7282688\nbuffer_load_dwordx4 v92, %[rw], s44 offen nt lds\nds_read_b64_tr_b16 v[108:109], v94 offset:0\n" \
"ds_read_b64_tr_b16 v[110:111], v94 offset:512\nds_read_b64_tr_b16 v[116:117], v94 offset:8\nds_read_b64_tr_b16 v[118:119], v94 offset:520\nds_read_b64_tr_b16 v[28:29], v94 offset:24576\nds_read_b64_tr_b16 v[30:31], v94 offset:25088\nds_read_b64_tr_b16 v[124:125], v94 offset:24584\nds_read_b64_tr_b16 v[126:127], v94 offset:25096\ns_waitcnt lgkmcnt(0)\n" \
"v_mfma_f32_16x16x16_bf16 v[8:11], v[108:109], v[6:7], v[8:11]\nv_mfma_f32_16x16x16_bf16 v[12:15], v[116:117], v[6:7], v[12:15]\nv_mfma_f32_16x16x16_bf16 v[16:19], v[28:29], v[6:7], v[16:19]\nv_mfma_f32_16x16x16_bf16 v[20:23], v[124:125], v[6:7], v[20:23]\nv_mfma_f32_16x16x16_bf16 v[8:11], v[110:111], v[6:7], v[8:11]\nv_mfma_f32_16x16x16_bf16 v[12:15], v[118:119], v[6:7], v[12:15]\nv_mfma_f32_16x16x16_bf16 v[16:19], v[30:31], v[6:7], v[16:19]\nv_mfma_f32_16x16x16_bf16 v[20:23], v[126:127], v[6:7], v[20:23]\n" \
"s_waitcnt vmcnt(28)\ns_barrier\nds_read_b64_tr_b16 v[128:129], v94 offset:4096\nds_read_b64_tr_b16 v[130:131], v94 offset:4608\nds_read_b64_tr_b16 v[136:137], v94 offset:4104\nds_read_b64_tr_b16 v[138:139], v94 offset:4616\nds_read_b64_tr_b16 v[144:145], v94 offset:28672\nds_read_b64_tr_b16 v[146:147], v94 offset:29184\n" \
"ds_read_b64_tr_b16 v[24:25], v94 offset:28680\nds_read_b64_tr_b16 v[26:27], v94 offset:29192\ns_waitcnt lgkmcnt(0)\nv_mfma_f32_16x16x16_bf16 v[8:11], v[128:129], v[6:7], v[8:11]\nv_mfma_f32_16x16x16_bf16 v[12:15], v[136:137], v[6:7], v[12:15]\nv_mfma_f32_16x16x16_bf16 v[16:19], v[144:145], v[6:7], v[16:19]\nv_mfma_f32_16x16x16_bf16 v[20:23], v[24:25], v[6:7], v[20:23]\nv_mfma_f32_16x16x16_bf16 v[8:11], v[130:131], v[6:7], v[8:11]\n" \
"v_mfma_f32_16x16x16_bf16 v[12:15], v[138:139], v[6:7], v[12:15]\nv_mfma_f32_16x16x16_bf16 v[16:19], v[146:147], v[6:7], v[16:19]\nv_mfma_f32_16x16x16_bf16 v[20:23], v[26:27], v[6:7], v[20:23]\ns_waitcnt vmcnt(24)\nds_write_b128 v1, v[88:91] offset:0\nds_write_b128 v1, v[84:87] offset:1024\nds_write_b128 v1, v[80:83] offset:2048\nds_write_b128 v1, v[76:79] offset:3072\n" \
"s_barrier\nds_read_b64_tr_b16 v[132:133], v94 offset:8192\nds_read_b64_tr_b16 v[134:135], v94 offset:8704\nds_read_b64_tr_b16 v[140:141], v94 offset:8200\nds_read_b64_tr_b16 v[142:143], v94 offset:8712\nds_read_b64_tr_b16 v[148:149], v94 offset:32768\nds_read_b64_tr_b16 v[150:151], v94 offset:33280\nds_read_b64_tr_b16 v[76:77], v94 offset:32776\n" \
"ds_read_b64_tr_b16 v[78:79], v94 offset:33288\ns_waitcnt lgkmcnt(0)\nv_mfma_f32_16x16x16_bf16 v[8:11], v[132:133], v[6:7], v[8:11]\nv_mfma_f32_16x16x16_bf16 v[12:15], v[140:141], v[6:7], v[12:15]\nv_mfma_f32_16x16x16_bf16 v[16:19], v[148:149], v[6:7], v[16:19]\nv_mfma_f32_16x16x16_bf16 v[20:23], v[76:77], v[6:7], v[20:23]\nv_mfma_f32_16x16x16_bf16 v[8:11], v[134:135], v[6:7], v[8:11]\nv_mfma_f32_16x16x16_bf16 v[12:15], v[142:143], v[6:7], v[12:15]\n" \
"v_mfma_f32_16x16x16_bf16 v[16:19], v[150:151], v[6:7], v[16:19]\nv_mfma_f32_16x16x16_bf16 v[20:23], v[78:79], v[6:7], v[20:23]\ns_waitcnt vmcnt(20)\nds_write_b128 v1, v[72:75] offset:4096\nds_write_b128 v1, v[68:71] offset:5120\nds_write_b128 v1, v[64:67] offset:6144\nds_write_b128 v1, v[60:63] offset:7168\ns_barrier\n" \
"ds_read_b64_tr_b16 v[184:185], v94 offset:12288\nds_read_b64_tr_b16 v[186:187], v94 offset:12800\nds_read_b64_tr_b16 v[60:61], v94 offset:12296\nds_read_b64_tr_b16 v[62:63], v94 offset:12808\nds_read_b64_tr_b16 v[192:193], v94 offset:36864\nds_read_b64_tr_b16 v[194:195], v94 offset:37376\nds_read_b64_tr_b16 v[200:201], v94 offset:36872\nds_read_b64_tr_b16 v[202:203], v94 offset:37384\n" \
"s_waitcnt lgkmcnt(0)\nv_mfma_f32_16x16x16_bf16 v[8:11], v[184:185], v[6:7], v[8:11]\nv_mfma_f32_16x16x16_bf16 v[12:15], v[60:61], v[6:7], v[12:15]\nv_mfma_f32_16x16x16_bf16 v[16:19], v[192:193], v[6:7], v[16:19]\nv_mfma_f32_16x16x16_bf16 v[20:23], v[200:201], v[6:7], v[20:23]\nv_mfma_f32_16x16x16_bf16 v[8:11], v[186:187], v[6:7], v[8:11]\nv_mfma_f32_16x16x16_bf16 v[12:15], v[62:63], v[6:7], v[12:15]\nv_mfma_f32_16x16x16_bf16 v[16:19], v[194:195], v[6:7], v[16:19]\n" \
"v_mfma_f32_16x16x16_bf16 v[20:23], v[202:203], v[6:7], v[20:23]\ns_waitcnt vmcnt(16)\nds_write_b128 v1, v[56:59] offset:8192\nds_write_b128 v1, v[52:55] offset:9216\nds_write_b128 v1, v[48:51] offset:10240\nds_write_b128 v1, v[44:47] offset:11264\ns_barrier\nds_read_b64_tr_b16 v[188:189], v94 offset:16384\n" \
"ds_read_b64_tr_b16 v[190:191], v94 offset:16896\nds_read_b64_tr_b16 v[44:45], v94 offset:16392\nds_read_b64_tr_b16 v[46:47], v94 offset:16904\nds_read_b64_tr_b16 v[196:197], v94 offset:40960\nds_read_b64_tr_b16 v[198:199], v94 offset:41472\nds_read_b64_tr_b16 v[204:205], v94 offset:40968\nds_read_b64_tr_b16 v[206:207], v94 offset:41480\ns_waitcnt lgkmcnt(0)\n" \
"v_mfma_f32_16x16x16_bf16 v[8:11], v[188:189], v[6:7], v[8:11]\nv_mfma_f32_16x16x16_bf16 v[12:15], v[44:45], v[6:7], v[12:15]\nv_mfma_f32_16x16x16_bf16 v[16:19], v[196:197], v[6:7], v[16:19]\nv_mfma_f32_16x16x16_bf16 v[20:23], v[204:205], v[6:7], v[20:23]\nv_mfma_f32_16x16x16_bf16 v[8:11], v[190:191], v[6:7], v[8:11]\nv_mfma_f32_16x16x16_bf16 v[12:15], v[46:47], v[6:7], v[12:15]\nv_mfma_f32_16x16x16_bf16 v[16:19], v[198:199], v[6:7], v[16:19]\nv_mfma_f32_16x16x16_bf16 v[20:23], v[206:207], v[6:7], v[20:23]\n" \
"s_waitcnt vmcnt(12)\nds_write_b128 v1, v[40:43] offset:12288\nds_write_b128 v1, v[36:39] offset:13312\nds_write_b128 v1, v[32:35] offset:14336\nds_write_b128 v1, v[28:31] offset:15360\ns_barrier\nds_read_b64_tr_b16 v[208:209], v94 offset:20480\nds_read_b64_tr_b16 v[210:211], v94 offset:20992\n" \
"ds_read_b64_tr_b16 v[216:217], v94 offset:20488\nds_read_b64_tr_b16 v[218:219], v94 offset:21000\nds_read_b64_tr_b16 v[28:29], v94 offset:45056\nds_read_b64_tr_b16 v[30:31], v94 offset:45568\nds_read_b64_tr_b16 v[224:225], v94 offset:45064\nds_read_b64_tr_b16 v[226:227], v94 offset:45576\ns_waitcnt lgkmcnt(0)\nv_mfma_f32_16x16x16_bf16 v[8:11], v[208:209], v[6:7], v[8:11]\n" \
"v_mfma_f32_16x16x16_bf16 v[12:15], v[216:217], v[6:7], v[12:15]\nv_mfma_f32_16x16x16_bf16 v[16:19], v[28:29], v[6:7], v[16:19]\nv_mfma_f32_16x16x16_bf16 v[20:23], v[224:225], v[6:7], v[20:23]\nv_mfma_f32_16x16x16_bf16 v[8:11], v[210:211], v[6:7], v[8:11]\nv_mfma_f32_16x16x16_bf16 v[12:15], v[218:219], v[6:7], v[12:15]\nv_mfma_f32_16x16x16_bf16 v[16:19], v[30:31], v[6:7], v[16:19]\nv_mfma_f32_16x16x16_bf16 v[20:23], v[226:227], v[6:7], v[20:23]\ns_waitcnt vmcnt(8)\n" \
"ds_write_b128 v1, v[24:27] offset:16384\nds_write_b128 v1, v[76:79] offset:17408\nds_write_b128 v1, v[60:63] offset:18432\nds_write_b128 v1, v[44:47] offset:19456\ns_barrier\nds_read_b64_tr_b16 v[212:213], v93 offset:0\nds_read_b64_tr_b16 v[214:215], v93 offset:512\nds_read_b64_tr_b16 v[220:221], v93 offset:8\n" \
"ds_read_b64_tr_b16 v[222:223], v93 offset:520\nds_read_b64_tr_b16 v[44:45], v93 offset:12288\nds_read_b64_tr_b16 v[46:47], v93 offset:12800\nds_read_b64_tr_b16 v[228:229], v93 offset:12296\nds_read_b64_tr_b16 v[230:231], v93 offset:12808\ns_waitcnt lgkmcnt(0)\nv_mfma_f32_16x16x16_bf16 v[8:11], v[212:213], v[6:7], v[8:11]\nv_mfma_f32_16x16x16_bf16 v[12:15], v[220:221], v[6:7], v[12:15]\n" \
"v_mfma_f32_16x16x16_bf16 v[16:19], v[44:45], v[6:7], v[16:19]\nv_mfma_f32_16x16x16_bf16 v[20:23], v[228:229], v[6:7], v[20:23]\nv_mfma_f32_16x16x16_bf16 v[8:11], v[214:215], v[6:7], v[8:11]\nv_mfma_f32_16x16x16_bf16 v[12:15], v[222:223], v[6:7], v[12:15]\nv_mfma_f32_16x16x16_bf16 v[16:19], v[46:47], v[6:7], v[16:19]\nv_mfma_f32_16x16x16_bf16 v[20:23], v[230:231], v[6:7], v[20:23]\ns_waitcnt vmcnt(4)\nds_write_b128 v1, v[28:31] offset:20480\n" \
"ds_write_b128 v1, v[44:47] offset:21504\ns_barrier\nds_read_b64_tr_b16 v[232:233], v93 offset:4096\nds_read_b64_tr_b16 v[234:235], v93 offset:4608\nds_read_b64_tr_b16 v[240:241], v93 offset:4104\nds_read_b64_tr_b16 v[242:243], v93 offset:4616\nds_read_b64_tr_b16 v[248:249], v93 offset:16384\nds_read_b64_tr_b16 v[250:251], v93 offset:16896\n" \
"ds_read_b64_tr_b16 v[44:45], v93 offset:16392\nds_read_b64_tr_b16 v[46:47], v93 offset:16904\ns_waitcnt lgkmcnt(0)\nv_mfma_f32_16x16x16_bf16 v[8:11], v[232:233], v[6:7], v[8:11]\nv_mfma_f32_16x16x16_bf16 v[12:15], v[240:241], v[6:7], v[12:15]\nv_mfma_f32_16x16x16_bf16 v[16:19], v[248:249], v[6:7], v[16:19]\nv_mfma_f32_16x16x16_bf16 v[20:23], v[44:45], v[6:7], v[20:23]\nv_mfma_f32_16x16x16_bf16 v[8:11], v[234:235], v[6:7], v[8:11]\n" \
"v_mfma_f32_16x16x16_bf16 v[12:15], v[242:243], v[6:7], v[12:15]\nv_mfma_f32_16x16x16_bf16 v[16:19], v[250:251], v[6:7], v[16:19]\nv_mfma_f32_16x16x16_bf16 v[20:23], v[46:47], v[6:7], v[20:23]\ns_waitcnt vmcnt(0)\nds_write_b128 v1, v[44:47] offset:22528\ns_barrier\nds_read_b64_tr_b16 v[236:237], v93 offset:8192\nds_read_b64_tr_b16 v[238:239], v93 offset:8704\n" \
"ds_read_b64_tr_b16 v[244:245], v93 offset:8200\nds_read_b64_tr_b16 v[246:247], v93 offset:8712\nds_read_b64_tr_b16 v[252:253], v93 offset:20480\nds_read_b64_tr_b16 v[254:255], v93 offset:20992\nds_read_b64_tr_b16 v[44:45], v93 offset:20488\nds_read_b64_tr_b16 v[46:47], v93 offset:21000\ns_waitcnt lgkmcnt(0)\nv_mfma_f32_16x16x16_bf16 v[8:11], v[236:237], v[6:7], v[8:11]\n" \
"v_mfma_f32_16x16x16_bf16 v[12:15], v[244:245], v[6:7], v[12:15]\nv_mfma_f32_16x16x16_bf16 v[16:19], v[252:253], v[6:7], v[16:19]\nv_mfma_f32_16x16x16_bf16 v[20:23], v[44:45], v[6:7], v[20:23]\nv_mfma_f32_16x16x16_bf16 v[8:11], v[238:239], v[6:7], v[8:11]\nv_mfma_f32_16x16x16_bf16 v[12:15], v[246:247], v[6:7], v[12:15]\nv_mfma_f32_16x16x16_bf16 v[16:19], v[254:255], v[6:7], v[16:19]\nv_mfma_f32_16x16x16_bf16 v[20:23], v[46:47], v[6:7], v[20:23]\nds_write_b128 v1, v[44:47] offset:23552\n" \
"s_waitcnt lgkmcnt(0)\ns_barrier\ns_mov_b32 s86, %[xn0]\ns_mov_b32 s87, %[ho0]\ns_mov_b32 s88, %[fh0]\ns_mov_b32 s89, %[b1]\ns_mov_b32 s40, %[b0]\ns_mov_b32 s43, 0\n" \
"s_lshl_b32 s57, %[w], 12\ns_mov_b32 s58, 0xbeeff558\ns_mov_b32 s59, 0x409d30fc\ns_mov_b32 s56, -2147483648\n.p2align 6\n.Lgg_next%=:\ns_cmp_lg_u32 %[w], 0\ns_cbranch_scc1 .Lgg_w1%=\n" \
"buffer_load_dword v56, v3, %[rs], 0 offen sc1\ns_waitcnt vmcnt(0)\ns_barrier\nbuffer_store_dword v90, off, %[rs], s56 sc1\ns_mov_b32 s56, -2147483648\n.Lgg_poll%=:\nv_cmp_gt_i32 vcc, s40, v56\ns_cbranch_vccz .Lgg_go%=\n" \
"s_cmp_ge_u32 s40, 62\ns_cbranch_scc1 .Lgg_ns1055%=\ns_sleep 6\n.Lgg_ns1055%=:\ns_sleep 1\nbuffer_load_dword v56, v3, %[rs], 0 offen sc1\ns_waitcnt vmcnt(0)\ns_add_u32 s43, s43, 1\n" \
"s_cmp_lt_u32 s43, 262144\ns_cbranch_scc1 .Lgg_poll%=\ns_branch .Lgg_abort%=\n.p2align 6\n.Lgg_w1%=:\ns_waitcnt vmcnt(0)\ns_barrier\n.p2align 6\n" \
".Lgg_go%=:\ns_add_u32 s44, s40, 0\ns_lshl_b32 s44, s44, 17\ns_add_u32 s44, s44, s86\ns_lshr_b32 s45, s44, 8\ns_and_b32 s45, s45, 0x2000\ns_xor_b32 s41, s44, s45\ns_add_u32 s61, s41, %[xw]\n" \
"s_barrier\ns_add_u32 m0, s57, 0\ns_add_u32 s60, s61, 0\nbuffer_load_dwordx4 v0, %[rs], s60 offen offset:0 sc1 lds\nbuffer_load_dwordx4 v0, %[rs], s60 offen offset:1024 sc1 lds\nbuffer_load_dwordx4 v0, %[rs], s60 offen offset:2048 sc1 lds\nbuffer_load_dwordx4 v0, %[rs], s60 offen offset:3072 sc1 lds\ns_add_u32 m0, s57, 16384\n" \
"s_add_u32 s60, s61, 32768\nbuffer_load_dwordx4 v0, %[rs], s60 offen offset:0 sc1 lds\nbuffer_load_dwordx4 v0, %[rs], s60 offen offset:1024 sc1 lds\nbuffer_load_dwordx4 v0, %[rs], s60 offen offset:2048 sc1 lds\nbuffer_load_dwordx4 v0, %[rs], s60 offen offset:3072 sc1 lds\nbuffer_load_dwordx4 v[40:43], v5, %[rs], s41 offen offset:0 sc1\nbuffer_load_dwordx4 v[44:47], v5, %[rs], s41 offen offset:256 sc1\ns_lshl_b32 s44, s40, 19\n" \
"s_add_u32 s44, s44, s87\ns_lshr_b32 s45, s44, 8\ns_and_b32 s45, s45, 0x2000\ns_xor_b32 s55, s44, s45\ns_waitcnt vmcnt(6)\ns_barrier\nds_read_b128 v[56:59], v2 offset:0\nds_read_b128 v[60:63], v2 offset:2048\n" \
"s_waitcnt lgkmcnt(1)\nv_mfma_f32_16x16x32_bf16 v[24:27], a[0:3], v[56:59], 0\ns_waitcnt lgkmcnt(0)\nds_read_b128 v[64:67], v2 offset:4096\nv_mfma_f32_16x16x32_bf16 v[24:27], a[4:7], v[60:63], v[24:27]\nds_read_b128 v[68:71], v2 offset:6144\nds_read_b128 v[72:75], v1 offset:0\nv_mfma_f32_16x16x32_bf16 v[28:31], a[8:11], v[56:59], 0\n" \
"ds_read_b128 v[76:79], v1 offset:1024\nbuffer_load_dwordx4 v[48:51], v5, %[rs], s41 offen offset:512 sc1\nv_mfma_f32_16x16x32_bf16 v[28:31], a[12:15], v[60:63], v[28:31]\nv_mfma_f32_16x16x32_bf16 v[32:35], a[16:19], v[56:59], 0\nv_mfma_f32_16x16x32_bf16 v[32:35], a[20:23], v[60:63], v[32:35]\nv_mfma_f32_16x16x32_bf16 v[36:39], a[24:27], v[56:59], 0\nbuffer_load_dwordx4 v[52:55], v5, %[rs], s41 offen offset:768 sc1\nv_mfma_f32_16x16x32_bf16 v[36:39], a[28:31], v[60:63], v[36:39]\n" \
"s_waitcnt lgkmcnt(3)\nds_read_b128 v[80:83], v1 offset:2048\nv_mfma_f32_16x16x32_bf16 v[24:27], a[32:35], v[64:67], v[24:27]\ns_waitcnt lgkmcnt(3)\nds_read_b128 v[56:59], v2 offset:8192\nv_mfma_f32_16x16x32_bf16 v[24:27], a[36:39], v[68:71], v[24:27]\ns_waitcnt lgkmcnt(3)\ns_add_u32 m0, s57, 32768\n" \
"ds_read_b128 v[60:63], v2 offset:10240\nv_mfma_f32_16x16x32_bf16 v[28:31], v[72:75], v[64:67], v[28:31]\ns_add_u32 s60, s61, 65536\ns_waitcnt lgkmcnt(3)\nds_read_b128 v[72:75], v1 offset:3072\nbuffer_load_dwordx4 v0, %[rs], s60 offen offset:0 sc1 lds\nv_mfma_f32_16x16x32_bf16 v[28:31], v[76:79], v[68:71], v[28:31]\nv_mfma_f32_16x16x32_bf16 v[32:35], a[40:43], v[64:67], v[32:35]\n" \
"v_mfma_f32_16x16x32_bf16 v[32:35], a[44:47], v[68:71], v[32:35]\nv_mfma_f32_16x16x32_bf16 v[36:39], a[48:51], v[64:67], v[36:39]\nbuffer_load_dwordx4 v0, %[rs], s60 offen offset:1024 sc1 lds\nv_mfma_f32_16x16x32_bf16 v[36:39], a[52:55], v[68:71], v[36:39]\ns_waitcnt lgkmcnt(2)\nv_mfma_f32_16x16x32_bf16 v[24:27], a[56:59], v[56:59], v[24:27]\ns_waitcnt lgkmcnt(1)\nds_read_b128 v[64:67], v2 offset:12288\n" \
"v_mfma_f32_16x16x32_bf16 v[24:27], a[60:63], v[60:63], v[24:27]\nds_read_b128 v[68:71], v2 offset:14336\nds_read_b128 v[76:79], v1 offset:4096\nv_mfma_f32_16x16x32_bf16 v[28:31], a[64:67], v[56:59], v[28:31]\nbuffer_load_dwordx4 v0, %[rs], s60 offen offset:2048 sc1 lds\nv_mfma_f32_16x16x32_bf16 v[28:31], a[68:71], v[60:63], v[28:31]\nv_mfma_f32_16x16x32_bf16 v[32:35], v[80:83], v[56:59], v[32:35]\ns_waitcnt lgkmcnt(3)\n" \
"ds_read_b128 v[80:83], v1 offset:5120\nv_mfma_f32_16x16x32_bf16 v[32:35], v[72:75], v[60:63], v[32:35]\nv_mfma_f32_16x16x32_bf16 v[36:39], a[72:75], v[56:59], v[36:39]\nbuffer_load_dwordx4 v0, %[rs], s60 offen offset:3072 sc1 lds\nv_mfma_f32_16x16x32_bf16 v[36:39], a[76:79], v[60:63], v[36:39]\ns_waitcnt lgkmcnt(3)\nv_mfma_f32_16x16x32_bf16 v[24:27], a[80:83], v[64:67], v[24:27]\ns_waitcnt vmcnt(8)\n" \
"s_barrier\ns_waitcnt lgkmcnt(2)\nds_read_b128 v[56:59], v2 offset:16384\nv_mfma_f32_16x16x32_bf16 v[24:27], a[84:87], v[68:71], v[24:27]\ns_add_u32 m0, s57, 49152\nds_read_b128 v[60:63], v2 offset:18432\nv_mfma_f32_16x16x32_bf16 v[28:31], a[88:91], v[64:67], v[28:31]\ns_add_u32 s60, s61, 98304\n" \
"buffer_load_dwordx4 v0, %[rs], s60 offen offset:0 sc1 lds\nv_mfma_f32_16x16x32_bf16 v[28:31], a[92:95], v[68:71], v[28:31]\nv_mfma_f32_16x16x32_bf16 v[32:35], a[96:99], v[64:67], v[32:35]\nv_mfma_f32_16x16x32_bf16 v[32:35], a[100:103], v[68:71], v[32:35]\ns_waitcnt lgkmcnt(3)\nds_read_b128 v[72:75], v1 offset:6144\nv_mfma_f32_16x16x32_bf16 v[36:39], v[76:79], v[64:67], v[36:39]\ns_waitcnt lgkmcnt(3)\n" \
"buffer_load_dwordx4 v0, %[rs], s60 offen offset:1024 sc1 lds\nv_mfma_f32_16x16x32_bf16 v[36:39], v[80:83], v[68:71], v[36:39]\ns_waitcnt lgkmcnt(2)\nds_read_b128 v[76:79], v1 offset:7168\nv_mfma_f32_16x16x32_bf16 v[24:27], a[104:107], v[56:59], v[24:27]\ns_waitcnt lgkmcnt(2)\nds_read_b128 v[64:67], v2 offset:20480\nv_mfma_f32_16x16x32_bf16 v[24:27], a[108:111], v[60:63], v[24:27]\n" \
"ds_read_b128 v[68:71], v2 offset:22528\nv_mfma_f32_16x16x32_bf16 v[28:31], a[112:115], v[56:59], v[28:31]\nbuffer_load_dwordx4 v0, %[rs], s60 offen offset:2048 sc1 lds\nv_mfma_f32_16x16x32_bf16 v[28:31], a[116:119], v[60:63], v[28:31]\nv_mfma_f32_16x16x32_bf16 v[32:35], a[120:123], v[56:59], v[32:35]\nv_mfma_f32_16x16x32_bf16 v[32:35], a[124:127], v[60:63], v[32:35]\nv_mfma_f32_16x16x32_bf16 v[36:39], a[128:131], v[56:59], v[36:39]\nbuffer_load_dwordx4 v0, %[rs], s60 offen offset:3072 sc1 lds\n" \
"v_mfma_f32_16x16x32_bf16 v[36:39], a[132:135], v[60:63], v[36:39]\ns_waitcnt lgkmcnt(1)\nds_read_b128 v[80:83], v1 offset:8192\nv_mfma_f32_16x16x32_bf16 v[24:27], a[136:139], v[64:67], v[24:27]\ns_waitcnt vmcnt(8)\ns_waitcnt lgkmcnt(1)\nds_read_b128 v[56:59], v2 offset:24576\nv_add_f32 v84, v40, v44\n" \
"v_mfma_f32_16x16x32_bf16 v[24:27], a[140:143], v[68:71], v[24:27]\nds_read_b128 v[60:63], v2 offset:26624\nv_add_f32 v85, v48, v52\nv_mfma_f32_16x16x32_bf16 v[28:31], v[72:75], v[64:67], v[28:31]\nds_read_b128 v[72:75], v1 offset:9216\nv_add_f32 v84, v84, v85\nv_mfma_f32_16x16x32_bf16 v[28:31], v[76:79], v[68:71], v[28:31]\nv_mov_b32 v85, v84\n" \
"v_mfma_f32_16x16x32_bf16 v[32:35], a[144:147], v[64:67], v[32:35]\ns_nop 0\nv_permlane16_swap_b32 v84, v85\nv_mfma_f32_16x16x32_bf16 v[32:35], a[148:151], v[68:71], v[32:35]\nv_add_f32 v84, v84, v85\nv_mov_b32 v85, v84\nv_mfma_f32_16x16x32_bf16 v[36:39], a[152:155], v[64:67], v[36:39]\ns_nop 0\n" \
"v_permlane32_swap_b32 v84, v85\nv_mfma_f32_16x16x32_bf16 v[36:39], a[156:159], v[68:71], v[36:39]\ns_waitcnt lgkmcnt(2)\nv_add_f32 v84, v84, v85\nv_mul_f32 v84, 0xbd800000, v84\nv_mfma_f32_16x16x32_bf16 v[24:27], a[160:163], v[56:59], v[24:27]\ns_waitcnt lgkmcnt(1)\nds_read_b128 v[64:67], v2 offset:28672\n" \
"v_add_f32 v40, v40, v84\nv_mfma_f32_16x16x32_bf16 v[24:27], a[164:167], v[60:63], v[24:27]\nds_read_b128 v[68:71], v2 offset:30720\nds_read_b128 v[76:79], v1 offset:10240\nv_mfma_f32_16x16x32_bf16 v[28:31], a[168:171], v[56:59], v[28:31]\nv_mul_f32 v40, 0x41000000, v40\nv_fma_f32 v41, v40, v40, v41\nv_mfma_f32_16x16x32_bf16 v[28:31], a[172:175], v[60:63], v[28:31]\n" \
"v_add_f32 v44, v44, v84\nv_mul_f32 v44, 0x41000000, v44\nv_mfma_f32_16x16x32_bf16 v[32:35], v[80:83], v[56:59], v[32:35]\ns_waitcnt lgkmcnt(3)\nds_read_b128 v[80:83], v1 offset:11264\nv_fma_f32 v45, v44, v44, v45\nv_mfma_f32_16x16x32_bf16 v[32:35], v[72:75], v[60:63], v[32:35]\nv_add_f32 v48, v48, v84\n" \
"v_mul_f32 v48, 0x41000000, v48\nv_mfma_f32_16x16x32_bf16 v[36:39], a[176:179], v[56:59], v[36:39]\nv_fma_f32 v49, v48, v48, v49\nv_add_f32 v52, v52, v84\nv_mfma_f32_16x16x32_bf16 v[36:39], a[180:183], v[60:63], v[36:39]\ns_waitcnt lgkmcnt(3)\nv_mul_f32 v52, 0x41000000, v52\nv_fma_f32 v53, v52, v52, v53\n" \
"v_mfma_f32_16x16x32_bf16 v[24:27], a[184:187], v[64:67], v[24:27]\ns_waitcnt vmcnt(4)\ns_barrier\ns_waitcnt lgkmcnt(2)\nds_read_b128 v[56:59], v2 offset:32768\nv_mfma_f32_16x16x32_bf16 v[24:27], a[188:191], v[68:71], v[24:27]\nds_read_b128 v[60:63], v2 offset:34816\nv_add_f32 v85, v41, v45\n" \
"v_mfma_f32_16x16x32_bf16 v[28:31], a[192:195], v[64:67], v[28:31]\nv_add_f32 v86, v49, v53\nv_add_f32 v85, v85, v86\nv_mfma_f32_16x16x32_bf16 v[28:31], a[196:199], v[68:71], v[28:31]\nv_mov_b32 v86, v85\nv_mfma_f32_16x16x32_bf16 v[32:35], a[200:203], v[64:67], v[32:35]\ns_nop 0\nv_permlane16_swap_b32 v85, v86\n" \
"v_mfma_f32_16x16x32_bf16 v[32:35], a[204:207], v[68:71], v[32:35]\ns_waitcnt lgkmcnt(3)\nds_read_b128 v[72:75], v1 offset:12288\nv_add_f32 v85, v85, v86\nv_mfma_f32_16x16x32_bf16 v[36:39], v[76:79], v[64:67], v[36:39]\ns_waitcnt lgkmcnt(3)\nv_mov_b32 v86, v85\nv_mfma_f32_16x16x32_bf16 v[36:39], v[80:83], v[68:71], v[36:39]\n" \
"s_waitcnt lgkmcnt(2)\nds_read_b128 v[76:79], v1 offset:13312\nv_mfma_f32_16x16x32_bf16 v[24:27], a[208:211], v[56:59], v[24:27]\ns_waitcnt lgkmcnt(2)\nds_read_b128 v[64:67], v2 offset:36864\nv_mfma_f32_16x16x32_bf16 v[24:27], a[212:215], v[60:63], v[24:27]\nds_read_b128 v[68:71], v2 offset:38912\nv_mfma_f32_16x16x32_bf16 v[28:31], a[216:219], v[56:59], v[28:31]\n" \
"v_permlane32_swap_b32 v85, v86\nv_mfma_f32_16x16x32_bf16 v[28:31], a[220:223], v[60:63], v[28:31]\nv_add_f32 v85, v85, v86\nv_mul_f32 v85, 0x3a800000, v85\nv_mfma_f32_16x16x32_bf16 v[32:35], a[224:227], v[56:59], v[32:35]\nv_cvt_pk_bf16_f32 v85, v85, v85\nv_and_b32 v85, 0xffff0000, v85\nv_mfma_f32_16x16x32_bf16 v[32:35], a[228:231], v[60:63], v[32:35]\n" \
"v_add_f32 v85, 0x3727c5ac, v85\nv_cvt_pk_bf16_f32 v85, v85, v85\nv_mfma_f32_16x16x32_bf16 v[36:39], a[232:235], v[56:59], v[36:39]\nv_and_b32 v85, 0xffff0000, v85\nv_cvt_pk_bf16_f32 v84, v84, v84\nv_mfma_f32_16x16x32_bf16 v[36:39], a[236:239], v[60:63], v[36:39]\ns_waitcnt lgkmcnt(1)\nds_read_b128 v[80:83], v1 offset:14336\n" \
"v_and_b32 v84, 0xffff0000, v84\nv_mfma_f32_16x16x32_bf16 v[24:27], a[240:243], v[64:67], v[24:27]\ns_waitcnt lgkmcnt(1)\nds_read_b128 v[56:59], v2 offset:40960\nv_mfma_f32_16x16x32_bf16 v[24:27], a[244:247], v[68:71], v[24:27]\nds_read_b128 v[60:63], v2 offset:43008\nv_mfma_f32_16x16x32_bf16 v[28:31], v[72:75], v[64:67], v[28:31]\nds_read_b128 v[72:75], v1 offset:15360\n" \
"v_mfma_f32_16x16x32_bf16 v[28:31], v[76:79], v[68:71], v[28:31]\nv_sqrt_f32 v85, v85\nv_mfma_f32_16x16x32_bf16 v[32:35], a[248:251], v[64:67], v[32:35]\nv_cvt_pk_bf16_f32 v85, v85, v85\nv_and_b32 v85, 0xffff0000, v85\nv_mfma_f32_16x16x32_bf16 v[32:35], a[252:255], v[68:71], v[32:35]\nv_mul_f32 v86, s58, v85\nv_add_f32 v87, v42, v46\n" \
"v_mfma_f32_16x16x32_bf16 v[36:39], v[96:99], v[64:67], v[36:39]\nv_rcp_f32 v85, v85\nv_mfma_f32_16x16x32_bf16 v[36:39], v[100:103], v[68:71], v[36:39]\ns_waitcnt lgkmcnt(2)\nv_mul_f32 v85, s58, v85\nv_add_f32 v88, v50, v54\nv_mfma_f32_16x16x32_bf16 v[24:27], v[104:107], v[56:59], v[24:27]\ns_waitcnt lgkmcnt(1)\n" \
"ds_read_b128 v[64:67], v2 offset:45056\nv_add_f32 v87, v87, v88\nv_mfma_f32_16x16x32_bf16 v[24:27], v[108:111], v[60:63], v[24:27]\nds_read_b128 v[68:71], v2 offset:47104\nds_read_b128 v[76:79], v1 offset:16384\nv_mfma_f32_16x16x32_bf16 v[28:31], v[112:115], v[56:59], v[28:31]\nv_mov_b32 v88, v87\nv_mfma_f32_16x16x32_bf16 v[28:31], v[116:119], v[60:63], v[28:31]\n" \
"s_nop 0\nv_permlane16_swap_b32 v87, v88\nv_mfma_f32_16x16x32_bf16 v[32:35], v[80:83], v[56:59], v[32:35]\ns_waitcnt lgkmcnt(3)\nds_read_b128 v[80:83], v1 offset:17408\nv_add_f32 v87, v87, v88\nv_mfma_f32_16x16x32_bf16 v[32:35], v[72:75], v[60:63], v[32:35]\nv_mov_b32 v88, v87\n" \
"v_mfma_f32_16x16x32_bf16 v[36:39], v[120:123], v[56:59], v[36:39]\ns_nop 0\nv_permlane32_swap_b32 v87, v88\nv_mfma_f32_16x16x32_bf16 v[36:39], v[124:127], v[60:63], v[36:39]\ns_waitcnt lgkmcnt(3)\nv_add_f32 v87, v87, v88\nv_mul_f32 v87, 0xbd800000, v87\nv_mfma_f32_16x16x32_bf16 v[24:27], v[128:131], v[64:67], v[24:27]\n" \
"s_waitcnt vmcnt(0)\ns_barrier\ns_waitcnt lgkmcnt(2)\nds_read_b128 v[56:59], v2 offset:49152\nv_mfma_f32_16x16x32_bf16 v[24:27], v[132:135], v[68:71], v[24:27]\nds_read_b128 v[60:63], v2 offset:51200\nv_add_f32 v42, v42, v87\nv_mfma_f32_16x16x32_bf16 v[28:31], v[136:139], v[64:67], v[28:31]\n" \
"v_mul_f32 v42, 0x41000000, v42\nv_fma_f32 v43, v42, v42, v43\nv_mfma_f32_16x16x32_bf16 v[28:31], v[140:143], v[68:71], v[28:31]\nv_add_f32 v46, v46, v87\nv_mul_f32 v46, 0x41000000, v46\nv_mfma_f32_16x16x32_bf16 v[32:35], v[144:147], v[64:67], v[32:35]\nv_fma_f32 v47, v46, v46, v47\nv_add_f32 v50, v50, v87\n" \
"v_mfma_f32_16x16x32_bf16 v[32:35], v[148:151], v[68:71], v[32:35]\ns_waitcnt lgkmcnt(3)\nds_read_b128 v[72:75], v1 offset:18432\nv_mul_f32 v50, 0x41000000, v50\nv_mfma_f32_16x16x32_bf16 v[36:39], v[76:79], v[64:67], v[36:39]\ns_waitcnt lgkmcnt(3)\nv_fma_f32 v51, v50, v50, v51\nv_add_f32 v54, v54, v87\n" \
"v_mfma_f32_16x16x32_bf16 v[36:39], v[80:83], v[68:71], v[36:39]\ns_waitcnt lgkmcnt(2)\nds_read_b128 v[76:79], v1 offset:19456\nv_mul_f32 v54, 0x41000000, v54\nv_mfma_f32_16x16x32_bf16 v[24:27], v[152:155], v[56:59], v[24:27]\ns_waitcnt lgkmcnt(2)\nds_read_b128 v[64:67], v2 offset:53248\nv_fma_f32 v55, v54, v54, v55\n" \
"v_mfma_f32_16x16x32_bf16 v[24:27], v[156:159], v[60:63], v[24:27]\nds_read_b128 v[68:71], v2 offset:55296\nv_add_f32 v88, v43, v47\nv_mfma_f32_16x16x32_bf16 v[28:31], v[160:163], v[56:59], v[28:31]\nv_add_f32 v89, v51, v55\nv_add_f32 v88, v88, v89\nv_mfma_f32_16x16x32_bf16 v[28:31], v[164:167], v[60:63], v[28:31]\nv_mov_b32 v89, v88\n" \
"v_mfma_f32_16x16x32_bf16 v[32:35], v[168:171], v[56:59], v[32:35]\ns_nop 0\nv_permlane16_swap_b32 v88, v89\nv_mfma_f32_16x16x32_bf16 v[32:35], v[172:175], v[60:63], v[32:35]\nv_add_f32 v88, v88, v89\nv_mov_b32 v89, v88\nv_mfma_f32_16x16x32_bf16 v[36:39], v[176:179], v[56:59], v[36:39]\ns_nop 0\n" \
"v_permlane32_swap_b32 v88, v89\nv_mfma_f32_16x16x32_bf16 v[36:39], v[180:183], v[60:63], v[36:39]\ns_waitcnt lgkmcnt(1)\nds_read_b128 v[80:83], v1 offset:20480\nv_add_f32 v88, v88, v89\nv_mfma_f32_16x16x32_bf16 v[24:27], v[184:187], v[64:67], v[24:27]\ns_waitcnt lgkmcnt(1)\nds_read_b128 v[56:59], v2 offset:57344\n" \
"v_mul_f32 v88, 0x3a800000, v88\nv_mfma_f32_16x16x32_bf16 v[24:27], v[188:191], v[68:71], v[24:27]\nds_read_b128 v[60:63], v2 offset:59392\nv_cvt_pk_bf16_f32 v88, v88, v88\nv_mfma_f32_16x16x32_bf16 v[28:31], v[72:75], v[64:67], v[28:31]\nds_read_b128 v[72:75], v1 offset:21504\nv_and_b32 v88, 0xffff0000, v88\nv_mfma_f32_16x16x32_bf16 v[28:31], v[76:79], v[68:71], v[28:31]\n" \
"v_add_f32 v88, 0x3727c5ac, v88\nv_cvt_pk_bf16_f32 v88, v88, v88\nv_mfma_f32_16x16x32_bf16 v[32:35], v[192:195], v[64:67], v[32:35]\nv_and_b32 v88, 0xffff0000, v88\nv_cvt_pk_bf16_f32 v87, v87, v87\nv_mfma_f32_16x16x32_bf16 v[32:35], v[196:199], v[68:71], v[32:35]\nv_sqrt_f32 v88, v88\nv_mfma_f32_16x16x32_bf16 v[36:39], v[200:203], v[64:67], v[36:39]\n" \
"v_and_b32 v87, 0xffff0000, v87\nv_cvt_pk_bf16_f32 v88, v88, v88\nv_mfma_f32_16x16x32_bf16 v[36:39], v[204:207], v[68:71], v[36:39]\ns_waitcnt lgkmcnt(2)\nv_and_b32 v88, 0xffff0000, v88\nv_mul_f32 v89, s58, v88\nv_mfma_f32_16x16x32_bf16 v[24:27], v[208:211], v[56:59], v[24:27]\ns_waitcnt lgkmcnt(1)\n" \
"ds_read_b128 v[64:67], v2 offset:61440\nv_mfma_f32_16x16x32_bf16 v[24:27], v[212:215], v[60:63], v[24:27]\nds_read_b128 v[68:71], v2 offset:63488\nds_read_b128 v[76:79], v1 offset:22528\nv_mfma_f32_16x16x32_bf16 v[28:31], v[216:219], v[56:59], v[28:31]\nv_rcp_f32 v88, v88\nv_mfma_f32_16x16x32_bf16 v[28:31], v[220:223], v[60:63], v[28:31]\nv_mul_f32 v88, s58, v88\n" \
"v_mfma_f32_16x16x32_bf16 v[32:35], v[80:83], v[56:59], v[32:35]\ns_waitcnt lgkmcnt(3)\nds_read_b128 v[80:83], v1 offset:23552\nv_mfma_f32_16x16x32_bf16 v[32:35], v[72:75], v[60:63], v[32:35]\nv_mfma_f32_16x16x32_bf16 v[36:39], v[224:227], v[56:59], v[36:39]\nv_mfma_f32_16x16x32_bf16 v[36:39], v[228:231], v[60:63], v[36:39]\ns_waitcnt lgkmcnt(3)\nv_mfma_f32_16x16x32_bf16 v[24:27], v[232:235], v[64:67], v[24:27]\n" \
"s_waitcnt lgkmcnt(2)\nds_read_b128 v[56:59], v2 offset:256\nv_mfma_f32_16x16x32_bf16 v[24:27], v[236:239], v[68:71], v[24:27]\nds_read_b128 v[60:63], v2 offset:2304\nv_mfma_f32_16x16x32_bf16 v[28:31], v[240:243], v[64:67], v[28:31]\nv_mfma_f32_16x16x32_bf16 v[28:31], v[244:247], v[68:71], v[28:31]\nv_mfma_f32_16x16x32_bf16 v[32:35], v[248:251], v[64:67], v[32:35]\nv_mfma_f32_16x16x32_bf16 v[32:35], v[252:255], v[68:71], v[32:35]\n" \
"s_waitcnt lgkmcnt(3)\nds_read_b128 v[72:75], v1 offset:0\nv_mfma_f32_16x16x32_bf16 v[36:39], v[76:79], v[64:67], v[36:39]\ns_waitcnt lgkmcnt(3)\nv_mfma_f32_16x16x32_bf16 v[36:39], v[80:83], v[68:71], v[36:39]\ns_waitcnt lgkmcnt(2)\nds_read_b128 v[76:79], v1 offset:1024\nv_mfma_f32_16x16x32_bf16 v[40:43], a[0:3], v[56:59], 0\n" \
"s_waitcnt lgkmcnt(2)\nds_read_b128 v[64:67], v2 offset:4352\nv_mfma_f32_16x16x32_bf16 v[40:43], a[4:7], v[60:63], v[40:43]\ns_lshl_b32 s44, s40, 1\nds_read_b128 v[68:71], v2 offset:6400\nv_mov_b32 v90, s44\nv_mfma_f32_16x16x32_bf16 v[44:47], a[8:11], v[56:59], 0\nv_mfma_f32_16x16x32_bf16 v[44:47], a[12:15], v[60:63], v[44:47]\n" \
"v_fma_f32 v24, v8, v84, v24\nv_fma_f32 v25, v9, v84, v25\nv_mfma_f32_16x16x32_bf16 v[48:51], a[16:19], v[56:59], 0\nv_fma_f32 v32, v16, v84, v32\nv_fma_f32 v33, v17, v84, v33\nv_mfma_f32_16x16x32_bf16 v[48:51], a[20:23], v[60:63], v[48:51]\nv_mul_f32 v24, v24, v85\nv_mul_f32 v25, v25, v85\n" \
"v_mfma_f32_16x16x32_bf16 v[52:55], a[24:27], v[56:59], 0\nv_fma_f32 v91, v24, v24, s59\nv_fma_f32 v92, v25, v25, s59\nv_mfma_f32_16x16x32_bf16 v[52:55], a[28:31], v[60:63], v[52:55]\ns_waitcnt lgkmcnt(1)\nds_read_b128 v[80:83], v1 offset:2048\nv_mul_f32 v91, v91, v24\nv_mfma_f32_16x16x32_bf16 v[40:43], a[32:35], v[64:67], v[40:43]\n" \
"s_waitcnt lgkmcnt(1)\nds_read_b128 v[56:59], v2 offset:8448\nv_mul_f32 v92, v92, v25\nv_mfma_f32_16x16x32_bf16 v[40:43], a[36:39], v[68:71], v[40:43]\nds_read_b128 v[60:63], v2 offset:10496\nv_mul_f32 v24, v24, v32\nv_mfma_f32_16x16x32_bf16 v[44:47], v[72:75], v[64:67], v[44:47]\nds_read_b128 v[72:75], v1 offset:3072\n" \
"v_mul_f32 v25, v25, v33\nv_mfma_f32_16x16x32_bf16 v[44:47], v[76:79], v[68:71], v[44:47]\nv_exp_f32 v91, v91\nv_mfma_f32_16x16x32_bf16 v[48:51], a[40:43], v[64:67], v[48:51]\nv_exp_f32 v92, v92\nv_mfma_f32_16x16x32_bf16 v[48:51], a[44:47], v[68:71], v[48:51]\nv_fma_f32 v91, v91, v86, v86\nv_fma_f32 v92, v92, v86, v86\n" \
"v_mfma_f32_16x16x32_bf16 v[52:55], a[48:51], v[64:67], v[52:55]\nv_rcp_f32 v91, v91\nv_mfma_f32_16x16x32_bf16 v[52:55], a[52:55], v[68:71], v[52:55]\ns_waitcnt lgkmcnt(2)\nv_rcp_f32 v92, v92\nv_mfma_f32_16x16x32_bf16 v[40:43], a[56:59], v[56:59], v[40:43]\ns_waitcnt lgkmcnt(1)\nds_read_b128 v[64:67], v2 offset:12544\n" \
"v_mul_f32 v24, v24, v91\nv_mfma_f32_16x16x32_bf16 v[40:43], a[60:63], v[60:63], v[40:43]\nds_read_b128 v[68:71], v2 offset:14592\nds_read_b128 v[76:79], v1 offset:4096\nv_mfma_f32_16x16x32_bf16 v[44:47], a[64:67], v[56:59], v[44:47]\nv_mul_f32 v25, v25, v92\nv_cvt_pk_bf16_f32 v32, v24, v25\nv_mfma_f32_16x16x32_bf16 v[44:47], a[68:71], v[60:63], v[44:47]\n" \
"v_fma_f32 v26, v10, v84, v26\nv_fma_f32 v27, v11, v84, v27\nv_mfma_f32_16x16x32_bf16 v[48:51], v[80:83], v[56:59], v[48:51]\ns_waitcnt lgkmcnt(3)\nds_read_b128 v[80:83], v1 offset:5120\nv_fma_f32 v34, v18, v84, v34\nv_mfma_f32_16x16x32_bf16 v[48:51], v[72:75], v[60:63], v[48:51]\nv_fma_f32 v35, v19, v84, v35\n" \
"v_mul_f32 v26, v26, v85\nv_mfma_f32_16x16x32_bf16 v[52:55], a[72:75], v[56:59], v[52:55]\nv_mul_f32 v27, v27, v85\nv_fma_f32 v91, v26, v26, s59\nv_mfma_f32_16x16x32_bf16 v[52:55], a[76:79], v[60:63], v[52:55]\ns_waitcnt lgkmcnt(3)\nv_fma_f32 v92, v27, v27, s59\nv_mul_f32 v91, v91, v26\n" \
"v_mfma_f32_16x16x32_bf16 v[40:43], a[80:83], v[64:67], v[40:43]\ns_waitcnt lgkmcnt(2)\nds_read_b128 v[56:59], v2 offset:16640\nv_mul_f32 v92, v92, v27\nv_mfma_f32_16x16x32_bf16 v[40:43], a[84:87], v[68:71], v[40:43]\nds_read_b128 v[60:63], v2 offset:18688\nv_mul_f32 v26, v26, v34\nv_mfma_f32_16x16x32_bf16 v[44:47], a[88:91], v[64:67], v[44:47]\n" \
"v_exp_f32 v91, v91\nv_mfma_f32_16x16x32_bf16 v[44:47], a[92:95], v[68:71], v[44:47]\nv_exp_f32 v92, v92\nv_mfma_f32_16x16x32_bf16 v[48:51], a[96:99], v[64:67], v[48:51]\nv_mul_f32 v27, v27, v35\nv_fma_f32 v91, v91, v86, v86\nv_mfma_f32_16x16x32_bf16 v[48:51], a[100:103], v[68:71], v[48:51]\ns_waitcnt lgkmcnt(3)\n" \
"ds_read_b128 v[72:75], v1 offset:6144\nv_fma_f32 v92, v92, v86, v86\nv_mfma_f32_16x16x32_bf16 v[52:55], v[76:79], v[64:67], v[52:55]\ns_waitcnt lgkmcnt(3)\nv_rcp_f32 v91, v91\nv_mfma_f32_16x16x32_bf16 v[52:55], v[80:83], v[68:71], v[52:55]\ns_waitcnt lgkmcnt(2)\nds_read_b128 v[76:79], v1 offset:7168\n" \
"v_mul_f32 v26, v26, v91\nv_mfma_f32_16x16x32_bf16 v[40:43], a[104:107], v[56:59], v[40:43]\ns_waitcnt lgkmcnt(2)\nds_read_b128 v[64:67], v2 offset:20736\nv_fma_f32 v28, v12, v84, v28\nv_mfma_f32_16x16x32_bf16 v[40:43], a[108:111], v[60:63], v[40:43]\nds_read_b128 v[68:71], v2 offset:22784\nv_fma_f32 v29, v13, v84, v29\n" \
"v_mfma_f32_16x16x32_bf16 v[44:47], a[112:115], v[56:59], v[44:47]\nv_rcp_f32 v92, v92\nv_mfma_f32_16x16x32_bf16 v[44:47], a[116:119], v[60:63], v[44:47]\nv_mul_f32 v27, v27, v92\nv_cvt_pk_bf16_f32 v33, v26, v27\nv_mfma_f32_16x16x32_bf16 v[48:51], a[120:123], v[56:59], v[48:51]\nv_fma_f32 v36, v20, v84, v36\nv_fma_f32 v37, v21, v84, v37\n" \
"v_mfma_f32_16x16x32_bf16 v[48:51], a[124:127], v[60:63], v[48:51]\nv_mul_f32 v28, v28, v85\nv_mul_f32 v29, v29, v85\nv_mfma_f32_16x16x32_bf16 v[52:55], a[128:131], v[56:59], v[52:55]\nv_fma_f32 v91, v28, v28, s59\nv_fma_f32 v92, v29, v29, s59\nv_mfma_f32_16x16x32_bf16 v[52:55], a[132:135], v[60:63], v[52:55]\ns_waitcnt lgkmcnt(1)\n" \
"ds_read_b128 v[80:83], v1 offset:8192\nv_mul_f32 v91, v91, v28\nv_mfma_f32_16x16x32_bf16 v[40:43], a[136:139], v[64:67], v[40:43]\ns_waitcnt lgkmcnt(1)\nds_read_b128 v[56:59], v2 offset:24832\nv_mul_f32 v92, v92, v29\nv_mfma_f32_16x16x32_bf16 v[40:43], a[140:143], v[68:71], v[40:43]\nds_read_b128 v[60:63], v2 offset:26880\n" \
"v_mul_f32 v28, v28, v36\nv_mfma_f32_16x16x32_bf16 v[44:47], v[72:75], v[64:67], v[44:47]\nds_read_b128 v[72:75], v1 offset:9216\nv_mul_f32 v29, v29, v37\nv_mfma_f32_16x16x32_bf16 v[44:47], v[76:79], v[68:71], v[44:47]\nv_exp_f32 v91, v91\nv_mfma_f32_16x16x32_bf16 v[48:51], a[144:147], v[64:67], v[48:51]\nv_exp_f32 v92, v92\n" \
"v_mfma_f32_16x16x32_bf16 v[48:51], a[148:151], v[68:71], v[48:51]\nv_fma_f32 v91, v91, v86, v86\nv_fma_f32 v92, v92, v86, v86\nv_mfma_f32_16x16x32_bf16 v[52:55], a[152:155], v[64:67], v[52:55]\nv_rcp_f32 v91, v91\nv_mfma_f32_16x16x32_bf16 v[52:55], a[156:159], v[68:71], v[52:55]\ns_waitcnt lgkmcnt(2)\nv_rcp_f32 v92, v92\n" \
"v_mfma_f32_16x16x32_bf16 v[40:43], a[160:163], v[56:59], v[40:43]\ns_waitcnt lgkmcnt(1)\nds_read_b128 v[64:67], v2 offset:28928\nv_mul_f32 v28, v28, v91\nv_mfma_f32_16x16x32_bf16 v[40:43], a[164:167], v[60:63], v[40:43]\nds_read_b128 v[68:71], v2 offset:30976\nds_read_b128 v[76:79], v1 offset:10240\nv_mfma_f32_16x16x32_bf16 v[44:47], a[168:171], v[56:59], v[44:47]\n" \
"v_mul_f32 v29, v29, v92\nv_cvt_pk_bf16_f32 v34, v28, v29\nv_mfma_f32_16x16x32_bf16 v[44:47], a[172:175], v[60:63], v[44:47]\nv_fma_f32 v30, v14, v84, v30\nv_fma_f32 v31, v15, v84, v31\nv_mfma_f32_16x16x32_bf16 v[48:51], v[80:83], v[56:59], v[48:51]\ns_waitcnt lgkmcnt(3)\nds_read_b128 v[80:83], v1 offset:11264\n" \
"v_fma_f32 v38, v22, v84, v38\nv_mfma_f32_16x16x32_bf16 v[48:51], v[72:75], v[60:63], v[48:51]\nv_fma_f32 v39, v23, v84, v39\nv_mul_f32 v30, v30, v85\nv_mfma_f32_16x16x32_bf16 v[52:55], a[176:179], v[56:59], v[52:55]\nv_mul_f32 v31, v31, v85\nv_fma_f32 v91, v30, v30, s59\nv_mfma_f32_16x16x32_bf16 v[52:55], a[180:183], v[60:63], v[52:55]\n" \
"s_waitcnt lgkmcnt(3)\nv_fma_f32 v92, v31, v31, s59\nv_mul_f32 v91, v91, v30\nv_mfma_f32_16x16x32_bf16 v[40:43], a[184:187], v[64:67], v[40:43]\ns_waitcnt lgkmcnt(2)\nds_read_b128 v[56:59], v2 offset:33024\nv_mul_f32 v92, v92, v31\nv_mfma_f32_16x16x32_bf16 v[40:43], a[188:191], v[68:71], v[40:43]\n" \
"ds_read_b128 v[60:63], v2 offset:35072\nv_mul_f32 v30, v30, v38\nv_mfma_f32_16x16x32_bf16 v[44:47], a[192:195], v[64:67], v[44:47]\nv_exp_f32 v91, v91\nv_mfma_f32_16x16x32_bf16 v[44:47], a[196:199], v[68:71], v[44:47]\nv_exp_f32 v92, v92\nv_mfma_f32_16x16x32_bf16 v[48:51], a[200:203], v[64:67], v[48:51]\nv_mul_f32 v31, v31, v39\n" \
"v_fma_f32 v91, v91, v86, v86\nv_mfma_f32_16x16x32_bf16 v[48:51], a[204:207], v[68:71], v[48:51]\ns_waitcnt lgkmcnt(3)\nds_read_b128 v[72:75], v1 offset:12288\nv_fma_f32 v92, v92, v86, v86\nv_mfma_f32_16x16x32_bf16 v[52:55], v[76:79], v[64:67], v[52:55]\ns_waitcnt lgkmcnt(3)\nv_rcp_f32 v91, v91\n" \
"v_mfma_f32_16x16x32_bf16 v[52:55], v[80:83], v[68:71], v[52:55]\ns_waitcnt lgkmcnt(2)\nds_read_b128 v[76:79], v1 offset:13312\nv_mul_f32 v30, v30, v91\nv_mfma_f32_16x16x32_bf16 v[40:43], a[208:211], v[56:59], v[40:43]\ns_waitcnt lgkmcnt(2)\nds_read_b128 v[64:67], v2 offset:37120\nv_mfma_f32_16x16x32_bf16 v[40:43], a[212:215], v[60:63], v[40:43]\n" \
"ds_read_b128 v[68:71], v2 offset:39168\nv_mfma_f32_16x16x32_bf16 v[44:47], a[216:219], v[56:59], v[44:47]\nv_rcp_f32 v92, v92\nv_mfma_f32_16x16x32_bf16 v[44:47], a[220:223], v[60:63], v[44:47]\nv_mul_f32 v31, v31, v92\nv_cvt_pk_bf16_f32 v35, v30, v31\nv_mfma_f32_16x16x32_bf16 v[48:51], a[224:227], v[56:59], v[48:51]\nbuffer_store_dwordx4 v[32:35], v4, %[rs], s55 offen offset:0 sc1\n" \
"v_mfma_f32_16x16x32_bf16 v[48:51], a[228:231], v[60:63], v[48:51]\nds_read_b128 v[80:83], v1 offset:14336\nv_mfma_f32_16x16x32_bf16 v[52:55], a[232:235], v[56:59], v[52:55]\nv_mfma_f32_16x16x32_bf16 v[52:55], a[236:239], v[60:63], v[52:55]\ns_waitcnt lgkmcnt(2)\nv_mfma_f32_16x16x32_bf16 v[40:43], a[240:243], v[64:67], v[40:43]\ns_waitcnt lgkmcnt(1)\nds_read_b128 v[56:59], v2 offset:41216\n" \
"v_mfma_f32_16x16x32_bf16 v[40:43], a[244:247], v[68:71], v[40:43]\nds_read_b128 v[60:63], v2 offset:43264\nv_mfma_f32_16x16x32_bf16 v[44:47], v[72:75], v[64:67], v[44:47]\nds_read_b128 v[72:75], v1 offset:15360\nv_mfma_f32_16x16x32_bf16 v[44:47], v[76:79], v[68:71], v[44:47]\nv_mfma_f32_16x16x32_bf16 v[48:51], a[248:251], v[64:67], v[48:51]\nds_read_b128 v[24:27], v2 offset:45312\nv_mfma_f32_16x16x32_bf16 v[48:51], a[252:255], v[68:71], v[48:51]\n" \
"ds_read_b128 v[28:31], v2 offset:47360\nv_mfma_f32_16x16x32_bf16 v[52:55], v[96:99], v[64:67], v[52:55]\nv_mfma_f32_16x16x32_bf16 v[52:55], v[100:103], v[68:71], v[52:55]\ns_waitcnt lgkmcnt(4)\nv_mfma_f32_16x16x32_bf16 v[40:43], v[104:107], v[56:59], v[40:43]\ns_waitcnt lgkmcnt(3)\nds_read_b128 v[64:67], v2 offset:49408\nv_mfma_f32_16x16x32_bf16 v[40:43], v[108:111], v[60:63], v[40:43]\n" \
"ds_read_b128 v[68:71], v2 offset:51456\nv_mfma_f32_16x16x32_bf16 v[48:51], v[80:83], v[56:59], v[48:51]\ns_waitcnt lgkmcnt(4)\nv_mfma_f32_16x16x32_bf16 v[48:51], v[72:75], v[60:63], v[48:51]\ns_waitcnt lgkmcnt(3)\nv_mfma_f32_16x16x32_bf16 v[40:43], v[128:131], v[24:27], v[40:43]\ns_waitcnt lgkmcnt(2)\nds_read_b128 v[56:59], v2 offset:53504\n" \
"v_mfma_f32_16x16x32_bf16 v[40:43], v[132:135], v[28:31], v[40:43]\nds_read_b128 v[60:63], v2 offset:55552\nds_read_b128 v[76:79], v1 offset:20480\nv_mfma_f32_16x16x32_bf16 v[48:51], v[144:147], v[24:27], v[48:51]\nv_mfma_f32_16x16x32_bf16 v[48:51], v[148:151], v[28:31], v[48:51]\ns_waitcnt lgkmcnt(4)\nds_read_b128 v[80:83], v1 offset:21504\nv_mfma_f32_16x16x32_bf16 v[40:43], v[152:155], v[64:67], v[40:43]\n" \
"s_waitcnt lgkmcnt(4)\nds_read_b128 v[24:27], v2 offset:57600\nv_mfma_f32_16x16x32_bf16 v[40:43], v[156:159], v[68:71], v[40:43]\nds_read_b128 v[28:31], v2 offset:59648\nv_mfma_f32_16x16x32_bf16 v[48:51], v[168:171], v[64:67], v[48:51]\nv_mfma_f32_16x16x32_bf16 v[48:51], v[172:175], v[68:71], v[48:51]\ns_waitcnt lgkmcnt(5)\nv_mfma_f32_16x16x32_bf16 v[40:43], v[184:187], v[56:59], v[40:43]\n" \
"s_waitcnt lgkmcnt(4)\nds_read_b128 v[64:67], v2 offset:61696\nv_mfma_f32_16x16x32_bf16 v[40:43], v[188:191], v[60:63], v[40:43]\nds_read_b128 v[68:71], v2 offset:63744\nv_mfma_f32_16x16x32_bf16 v[48:51], v[192:195], v[56:59], v[48:51]\nv_mfma_f32_16x16x32_bf16 v[48:51], v[196:199], v[60:63], v[48:51]\ns_waitcnt lgkmcnt(3)\nv_mfma_f32_16x16x32_bf16 v[40:43], v[208:211], v[24:27], v[40:43]\n" \
"s_waitcnt lgkmcnt(2)\nds_read_b128 v[56:59], v2 offset:41216\nv_mfma_f32_16x16x32_bf16 v[40:43], v[212:215], v[28:31], v[40:43]\nds_read_b128 v[60:63], v2 offset:43264\nds_read_b128 v[72:75], v1 offset:16384\nv_mfma_f32_16x16x32_bf16 v[48:51], v[76:79], v[24:27], v[48:51]\nv_mfma_f32_16x16x32_bf16 v[48:51], v[80:83], v[28:31], v[48:51]\ns_waitcnt lgkmcnt(4)\n" \
"ds_read_b128 v[76:79], v1 offset:17408\nv_mfma_f32_16x16x32_bf16 v[40:43], v[232:235], v[64:67], v[40:43]\ns_waitcnt lgkmcnt(4)\nds_read_b128 v[24:27], v2 offset:45312\nv_mfma_f32_16x16x32_bf16 v[40:43], v[236:239], v[68:71], v[40:43]\ns_waitcnt vmcnt(0)\nds_read_b128 v[28:31], v2 offset:47360\ns_barrier\n" \
"buffer_store_dword v90, off, %[rs], s88 sc1\nv_mfma_f32_16x16x32_bf16 v[48:51], v[248:251], v[64:67], v[48:51]\nv_mfma_f32_16x16x32_bf16 v[48:51], v[252:255], v[68:71], v[48:51]\ns_waitcnt lgkmcnt(5)\nds_read_b128 v[80:83], v1 offset:18432\nv_mfma_f32_16x16x32_bf16 v[44:47], v[112:115], v[56:59], v[44:47]\ns_waitcnt lgkmcnt(5)\nds_read_b128 v[64:67], v2 offset:49408\n" \
"v_mfma_f32_16x16x32_bf16 v[44:47], v[116:119], v[60:63], v[44:47]\nds_read_b128 v[68:71], v2 offset:51456\nv_fma_f32 v40, v8, v87, v40\nv_mfma_f32_16x16x32_bf16 v[52:55], v[120:123], v[56:59], v[52:55]\nv_fma_f32 v41, v9, v87, v41\nv_fma_f32 v48, v16, v87, v48\nv_mfma_f32_16x16x32_bf16 v[52:55], v[124:127], v[60:63], v[52:55]\ns_waitcnt lgkmcnt(4)\n" \
"v_fma_f32 v49, v17, v87, v49\nv_mul_f32 v40, v40, v88\nv_mfma_f32_16x16x32_bf16 v[44:47], v[136:139], v[24:27], v[44:47]\ns_waitcnt lgkmcnt(3)\nds_read_b128 v[56:59], v2 offset:53504\nv_mul_f32 v41, v41, v88\nv_mfma_f32_16x16x32_bf16 v[44:47], v[140:143], v[28:31], v[44:47]\nds_read_b128 v[60:63], v2 offset:55552\n" \
"v_fma_f32 v91, v40, v40, s59\nv_mfma_f32_16x16x32_bf16 v[52:55], v[72:75], v[24:27], v[52:55]\nds_read_b128 v[72:75], v1 offset:19456\nv_fma_f32 v92, v41, v41, s59\nv_mfma_f32_16x16x32_bf16 v[52:55], v[76:79], v[28:31], v[52:55]\ns_waitcnt lgkmcnt(4)\nv_mul_f32 v91, v91, v40\nv_mul_f32 v92, v92, v41\n" \
"v_mfma_f32_16x16x32_bf16 v[44:47], v[160:163], v[64:67], v[44:47]\ns_waitcnt lgkmcnt(3)\nds_read_b128 v[24:27], v2 offset:57600\nv_mul_f32 v40, v40, v48\nv_mfma_f32_16x16x32_bf16 v[44:47], v[164:167], v[68:71], v[44:47]\nds_read_b128 v[28:31], v2 offset:59648\nds_read_b128 v[76:79], v1 offset:22528\nv_mfma_f32_16x16x32_bf16 v[52:55], v[176:179], v[64:67], v[52:55]\n" \
"v_exp_f32 v91, v91\nv_mfma_f32_16x16x32_bf16 v[52:55], v[180:183], v[68:71], v[52:55]\ns_waitcnt lgkmcnt(5)\nv_exp_f32 v92, v92\nv_mfma_f32_16x16x32_bf16 v[44:47], v[80:83], v[56:59], v[44:47]\ns_waitcnt lgkmcnt(3)\nds_read_b128 v[64:67], v2 offset:61696\nds_read_b128 v[80:83], v1 offset:23552\n" \
"v_mfma_f32_16x16x32_bf16 v[44:47], v[72:75], v[60:63], v[44:47]\nds_read_b128 v[68:71], v2 offset:63744\nv_mul_f32 v41, v41, v49\nv_mfma_f32_16x16x32_bf16 v[52:55], v[200:203], v[56:59], v[52:55]\nv_fma_f32 v91, v91, v89, v89\nv_fma_f32 v92, v92, v89, v89\nv_mfma_f32_16x16x32_bf16 v[52:55], v[204:207], v[60:63], v[52:55]\ns_waitcnt lgkmcnt(5)\n" \
"v_rcp_f32 v91, v91\nv_mfma_f32_16x16x32_bf16 v[44:47], v[216:219], v[24:27], v[44:47]\ns_waitcnt lgkmcnt(4)\nv_rcp_f32 v92, v92\nv_mfma_f32_16x16x32_bf16 v[44:47], v[220:223], v[28:31], v[44:47]\nv_mul_f32 v40, v40, v91\nv_mul_f32 v41, v41, v92\nv_mfma_f32_16x16x32_bf16 v[52:55], v[224:227], v[24:27], v[52:55]\n" \
"v_cvt_pk_bf16_f32 v48, v40, v41\nv_fma_f32 v42, v10, v87, v42\nv_mfma_f32_16x16x32_bf16 v[52:55], v[228:231], v[28:31], v[52:55]\ns_waitcnt lgkmcnt(2)\nv_fma_f32 v43, v11, v87, v43\nv_fma_f32 v50, v18, v87, v50\nv_mfma_f32_16x16x32_bf16 v[44:47], v[240:243], v[64:67], v[44:47]\ns_waitcnt lgkmcnt(0)\n" \
"v_fma_f32 v51, v19, v87, v51\nv_mul_f32 v42, v42, v88\nv_mfma_f32_16x16x32_bf16 v[44:47], v[244:247], v[68:71], v[44:47]\nv_mul_f32 v43, v43, v88\nv_fma_f32 v91, v42, v42, s59\nv_mfma_f32_16x16x32_bf16 v[52:55], v[76:79], v[64:67], v[52:55]\nv_fma_f32 v92, v43, v43, s59\nv_mul_f32 v91, v91, v42\n" \
"v_mfma_f32_16x16x32_bf16 v[52:55], v[80:83], v[68:71], v[52:55]\nv_mul_f32 v92, v92, v43\nv_mul_f32 v42, v42, v50\nv_exp_f32 v91, v91\nv_exp_f32 v92, v92\nv_mul_f32 v43, v43, v51\nv_fma_f32 v91, v91, v89, v89\nv_fma_f32 v92, v92, v89, v89\n" \
"v_rcp_f32 v91, v91\nv_rcp_f32 v92, v92\nv_mul_f32 v42, v42, v91\nv_mul_f32 v43, v43, v92\nv_cvt_pk_bf16_f32 v49, v42, v43\nv_fma_f32 v44, v12, v87, v44\nv_fma_f32 v45, v13, v87, v45\nv_fma_f32 v52, v20, v87, v52\n" \
"v_fma_f32 v53, v21, v87, v53\nv_mul_f32 v44, v44, v88\nv_mul_f32 v45, v45, v88\nv_fma_f32 v91, v44, v44, s59\nv_fma_f32 v92, v45, v45, s59\nv_mul_f32 v91, v91, v44\nv_mul_f32 v92, v92, v45\nv_exp_f32 v91, v91\n" \
"v_exp_f32 v92, v92\nv_mul_f32 v44, v44, v52\nv_mul_f32 v45, v45, v53\nv_fma_f32 v91, v91, v89, v89\nv_fma_f32 v92, v92, v89, v89\nv_rcp_f32 v91, v91\nv_rcp_f32 v92, v92\nv_mul_f32 v44, v44, v91\n" \
"v_mul_f32 v45, v45, v92\nv_cvt_pk_bf16_f32 v50, v44, v45\nv_fma_f32 v46, v14, v87, v46\nv_fma_f32 v47, v15, v87, v47\nv_fma_f32 v54, v22, v87, v54\nv_fma_f32 v55, v23, v87, v55\nv_mul_f32 v46, v46, v88\nv_mul_f32 v47, v47, v88\n" \
"v_fma_f32 v91, v46, v46, s59\nv_fma_f32 v92, v47, v47, s59\nv_mul_f32 v91, v91, v46\nv_mul_f32 v92, v92, v47\nv_exp_f32 v91, v91\nv_exp_f32 v92, v92\nv_mul_f32 v46, v46, v54\nv_mul_f32 v47, v47, v55\n" \
"v_fma_f32 v91, v91, v89, v89\nv_fma_f32 v92, v92, v89, v89\nv_rcp_f32 v91, v91\nv_rcp_f32 v92, v92\nv_mul_f32 v46, v46, v91\nv_mul_f32 v47, v47, v92\nv_cvt_pk_bf16_f32 v51, v46, v47\nbuffer_store_dwordx4 v[48:51], v4, %[rs], s55 offen offset:1024 sc1\n" \
"v_add_u32 v90, 1, v90\ns_mov_b32 s56, s88\ns_add_u32 s40, s40, 1\ns_cmp_lt_u32 s40, s89\ns_cbranch_scc1 .Lgg_next%=\ns_cmp_eq_u32 s89, 64\ns_cbranch_scc1 .Lgg_fin%=\ns_mov_b32 s89, 64\n" \
"s_mov_b32 s40, 0\ns_xor_b32 s86, s86, 0x2000\ns_xor_b32 s87, s87, 0x2000\ns_mov_b32 s88, %[fh1]\nv_xor_b32 v3, 0x2000, v3\ns_branch .Lgg_next%=\n.Lgg_fin%=:\ns_waitcnt vmcnt(0)\n" \
"s_barrier\nbuffer_store_dword v90, off, %[rs], s56 sc1\ns_branch .Lgg_end%=\n.Lgg_abort%=:\ns_mov_b64 exec, -1\nv_mov_b32 v16, 0\ns_movk_i32 s44, 6400\nbuffer_store_dword v16, off, %[rs], s44 sc1\n" \
".Lgg_end%=:\ns_waitcnt vmcnt(0) lgkmcnt(0)\n" \
""
#define DD_ASM \
"v_mbcnt_lo_u32_b32 v255, -1, 0\nv_mbcnt_hi_u32_b32 v255, -1, v255\nv_lshlrev_b32 v255, 2, v255\nv_add_u32 v255, %[tb], v255\nds_read_b32 v0, v255 offset:0\nds_read_b32 v1, v255 offset:256\nds_read_b32 v2, v255 offset:512\nds_read_b32 v3, v255 offset:768\n" \
"ds_read_b32 v4, v255 offset:1024\nds_read_b32 v5, v255 offset:1280\nds_read_b32 v6, v255 offset:1536\nds_read_b32 v7, v255 offset:1792\nds_read_b32 v125, v255 offset:2048\nds_read_b32 v126, v255 offset:2304\nds_read_b32 v127, v255 offset:2560\ns_waitcnt lgkmcnt(0)\n" \
"s_add_u32 m0, %[tb], 8192\ns_add_u32 s44, %[wo], 0\nbuffer_load_dwordx4 v125, %[rw], s44 offen nt lds\ns_add_u32 m0, %[tb], 9216\ns_add_u32 s44, %[wo], 16384\nbuffer_load_dwordx4 v125, %[rw], s44 offen nt lds\ns_add_u32 m0, %[tb], 10240\ns_add_u32 s44, %[wo], 32768\n" \
"buffer_load_dwordx4 v125, %[rw], s44 offen nt lds\ns_add_u32 m0, %[tb], 11264\ns_add_u32 s44, %[wo], 49152\nbuffer_load_dwordx4 v125, %[rw], s44 offen nt lds\ns_add_u32 m0, %[lw], 0\ns_add_u32 s44, %[wo], 65536\nbuffer_load_dwordx4 v125, %[rw], s44 offen nt lds\ns_add_u32 m0, %[lw], 1024\n" \
"s_add_u32 s44, %[wo], 81920\nbuffer_load_dwordx4 v125, %[rw], s44 offen nt lds\ns_add_u32 m0, %[lw], 2048\ns_add_u32 s44, %[wo], 98304\nbuffer_load_dwordx4 v125, %[rw], s44 offen nt lds\ns_add_u32 m0, %[lw], 3072\ns_add_u32 s44, %[wo], 114688\nbuffer_load_dwordx4 v125, %[rw], s44 offen nt lds\n" \
"s_add_u32 m0, %[lw], 4096\ns_add_u32 s44, %[wo], 131072\nbuffer_load_dwordx4 v125, %[rw], s44 offen nt lds\ns_add_u32 m0, %[lw], 5120\ns_add_u32 s44, %[wo], 147456\nbuffer_load_dwordx4 v125, %[rw], s44 offen nt lds\ns_add_u32 m0, %[lw], 6144\ns_add_u32 s44, %[wo], 163840\n" \
"buffer_load_dwordx4 v125, %[rw], s44 offen nt lds\ns_add_u32 m0, %[lw], 7168\ns_add_u32 s44, %[wo], 180224\nbuffer_load_dwordx4 v125, %[rw], s44 offen nt lds\ns_add_u32 m0, %[lw], 8192\ns_add_u32 s44, %[wo], 196608\nbuffer_load_dwordx4 v125, %[rw], s44 offen nt lds\ns_add_u32 m0, %[lw], 9216\n" \
"s_add_u32 s44, %[wo], 212992\nbuffer_load_dwordx4 v125, %[rw], s44 offen nt lds\ns_add_u32 m0, %[lw], 10240\ns_add_u32 s44, %[wo], 229376\nbuffer_load_dwordx4 v125, %[rw], s44 offen nt lds\ns_add_u32 m0, %[lw], 11264\ns_add_u32 s44, %[wo], 245760\nbuffer_load_dwordx4 v125, %[rw], s44 offen nt lds\n" \
"s_add_u32 m0, %[lw], 12288\ns_add_u32 s44, %[wo], 262144\nbuffer_load_dwordx4 v125, %[rw], s44 offen nt lds\ns_add_u32 m0, %[lw], 13312\ns_add_u32 s44, %[wo], 278528\nbuffer_load_dwordx4 v125, %[rw], s44 offen nt lds\ns_add_u32 m0, %[lw], 14336\ns_add_u32 s44, %[wo], 294912\n" \
"buffer_load_dwordx4 v125, %[rw], s44 offen nt lds\ns_add_u32 m0, %[lw], 15360\ns_add_u32 s44, %[wo], 311296\nbuffer_load_dwordx4 v125, %[rw], s44 offen nt lds\ns_add_u32 m0, %[lw], 16384\ns_add_u32 s44, %[wo], 327680\nbuffer_load_dwordx4 v125, %[rw], s44 offen nt lds\ns_add_u32 m0, %[lw], 17408\n" \
"s_add_u32 s44, %[wo], 344064\nbuffer_load_dwordx4 v125, %[rw], s44 offen nt lds\ns_add_u32 m0, %[lw], 18432\ns_add_u32 s44, %[wo], 360448\nbuffer_load_dwordx4 v125, %[rw], s44 offen nt lds\ns_add_u32 m0, %[lw], 19456\ns_add_u32 s44, %[wo], 376832\nbuffer_load_dwordx4 v125, %[rw], s44 offen nt lds\n" \
"s_add_u32 m0, %[lw], 20480\ns_add_u32 s44, %[wo], 393216\nbuffer_load_dwordx4 v125, %[rw], s44 offen nt lds\ns_add_u32 m0, %[lw], 21504\ns_add_u32 s44, %[wo], 409600\nbuffer_load_dwordx4 v125, %[rw], s44 offen nt lds\ns_add_u32 m0, %[lw], 22528\ns_add_u32 s44, %[wo], 425984\n" \
"buffer_load_dwordx4 v125, %[rw], s44 offen nt lds\ns_add_u32 m0, %[lw], 23552\ns_add_u32 s44, %[wo], 442368\nbuffer_load_dwordx4 v125, %[rw], s44 offen nt lds\ns_add_u32 m0, %[tb], 0\ns_add_u32 s44, %[wo], 458752\nbuffer_load_dwordx4 v125, %[rw], s44 offen nt lds\ns_add_u32 m0, %[tb], 1024\n" \
"s_add_u32 s44, %[wo], 475136\nbuffer_load_dwordx4 v125, %[rw], s44 offen nt lds\ns_add_u32 m0, %[tb], 2048\ns_add_u32 s44, %[wo], 491520\nbuffer_load_dwordx4 v125, %[rw], s44 offen nt lds\ns_add_u32 m0, %[tb], 3072\ns_add_u32 s44, %[wo], 507904\nbuffer_load_dwordx4 v125, %[rw], s44 offen nt lds\n" \
"s_add_u32 m0, %[tb], 4096\ns_add_u32 s44, %[wo], 524288\nbuffer_load_dwordx4 v125, %[rw], s44 offen nt lds\ns_add_u32 m0, %[tb], 5120\ns_add_u32 s44, %[wo], 540672\nbuffer_load_dwordx4 v125, %[rw], s44 offen nt lds\ns_add_u32 m0, %[tb], 6144\ns_add_u32 s44, %[wo], 557056\n" \
"buffer_load_dwordx4 v125, %[rw], s44 offen nt lds\ns_add_u32 m0, %[tb], 7168\ns_add_u32 s44, %[wo], 573440\nbuffer_load_dwordx4 v125, %[rw], s44 offen nt lds\ns_waitcnt vmcnt(32)\nds_read_b64_tr_b16 a[0:1], v126 offset:8192\nds_read_b64_tr_b16 a[2:3], v126 offset:8704\nds_read_b64_tr_b16 a[8:9], v126 offset:8224\n" \
"ds_read_b64_tr_b16 a[10:11], v126 offset:8736\nds_read_b64_tr_b16 a[16:17], v126 offset:8256\nds_read_b64_tr_b16 a[18:19], v126 offset:8768\nds_read_b64_tr_b16 a[24:25], v126 offset:8288\nds_read_b64_tr_b16 a[26:27], v126 offset:8800\ns_waitcnt lgkmcnt(0)\ns_add_u32 m0, %[tb], 8192\ns_add_u32 s44, %[wo], 589824\n" \
"buffer_load_dwordx4 v125, %[rw], s44 offen nt lds\ns_add_u32 m0, %[tb], 9216\ns_add_u32 s44, %[wo], 606208\nbuffer_load_dwordx4 v125, %[rw], s44 offen nt lds\ns_add_u32 m0, %[tb], 10240\ns_add_u32 s44, %[wo], 622592\nbuffer_load_dwordx4 v125, %[rw], s44 offen nt lds\ns_add_u32 m0, %[tb], 11264\n" \
"s_add_u32 s44, %[wo], 638976\nbuffer_load_dwordx4 v125, %[rw], s44 offen nt lds\ns_waitcnt vmcnt(32)\nds_read_b64_tr_b16 a[4:5], v127 offset:0\nds_read_b64_tr_b16 a[6:7], v127 offset:512\nds_read_b64_tr_b16 a[12:13], v127 offset:32\nds_read_b64_tr_b16 a[14:15], v127 offset:544\nds_read_b64_tr_b16 a[20:21], v127 offset:64\n" \
"ds_read_b64_tr_b16 a[22:23], v127 offset:576\nds_read_b64_tr_b16 a[28:29], v127 offset:96\nds_read_b64_tr_b16 a[30:31], v127 offset:608\ns_waitcnt lgkmcnt(0)\ns_add_u32 m0, %[lw], 0\ns_add_u32 s44, %[wo], 655360\nbuffer_load_dwordx4 v125, %[rw], s44 offen nt lds\ns_add_u32 m0, %[lw], 1024\n" \
"s_add_u32 s44, %[wo], 671744\nbuffer_load_dwordx4 v125, %[rw], s44 offen nt lds\ns_add_u32 m0, %[lw], 2048\ns_add_u32 s44, %[wo], 688128\nbuffer_load_dwordx4 v125, %[rw], s44 offen nt lds\ns_add_u32 m0, %[lw], 3072\ns_add_u32 s44, %[wo], 704512\nbuffer_load_dwordx4 v125, %[rw], s44 offen nt lds\n" \
"s_waitcnt vmcnt(32)\nds_read_b64_tr_b16 a[32:33], v127 offset:4096\nds_read_b64_tr_b16 a[34:35], v127 offset:4608\nds_read_b64_tr_b16 v[92:93], v127 offset:4128\nds_read_b64_tr_b16 v[94:95], v127 offset:4640\nds_read_b64_tr_b16 a[40:41], v127 offset:4160\nds_read_b64_tr_b16 a[42:43], v127 offset:4672\nds_read_b64_tr_b16 a[48:49], v127 offset:4192\n" \
"ds_read_b64_tr_b16 a[50:51], v127 offset:4704\ns_waitcnt lgkmcnt(0)\ns_add_u32 m0, %[lw], 4096\ns_add_u32 s44, %[wo], 720896\nbuffer_load_dwordx4 v125, %[rw], s44 offen nt lds\ns_add_u32 m0, %[lw], 5120\ns_add_u32 s44, %[wo], 737280\nbuffer_load_dwordx4 v125, %[rw], s44 offen nt lds\n" \
"s_add_u32 m0, %[lw], 6144\ns_add_u32 s44, %[wo], 753664\nbuffer_load_dwordx4 v125, %[rw], s44 offen nt lds\ns_add_u32 m0, %[lw], 7168\ns_add_u32 s44, %[wo], 770048\nbuffer_load_dwordx4 v125, %[rw], s44 offen nt lds\ns_waitcnt vmcnt(32)\nds_read_b64_tr_b16 a[36:37], v127 offset:8192\n" \
"ds_read_b64_tr_b16 a[38:39], v127 offset:8704\nds_read_b64_tr_b16 v[88:89], v127 offset:8224\nds_read_b64_tr_b16 v[90:91], v127 offset:8736\nds_read_b64_tr_b16 a[44:45], v127 offset:8256\nds_read_b64_tr_b16 a[46:47], v127 offset:8768\nds_read_b64_tr_b16 a[52:53], v127 offset:8288\nds_read_b64_tr_b16 a[54:55], v127 offset:8800\ns_waitcnt lgkmcnt(0)\n" \
"s_add_u32 m0, %[lw], 8192\ns_add_u32 s44, %[wo], 786432\nbuffer_load_dwordx4 v125, %[rw], s44 offen nt lds\ns_add_u32 m0, %[lw], 9216\ns_add_u32 s44, %[wo], 802816\nbuffer_load_dwordx4 v125, %[rw], s44 offen nt lds\ns_add_u32 m0, %[lw], 10240\ns_add_u32 s44, %[wo], 819200\n" \
"buffer_load_dwordx4 v125, %[rw], s44 offen nt lds\ns_add_u32 m0, %[lw], 11264\ns_add_u32 s44, %[wo], 835584\nbuffer_load_dwordx4 v125, %[rw], s44 offen nt lds\ns_waitcnt vmcnt(32)\nds_read_b64_tr_b16 a[56:57], v127 offset:12288\nds_read_b64_tr_b16 a[58:59], v127 offset:12800\nds_read_b64_tr_b16 a[64:65], v127 offset:12320\n" \
"ds_read_b64_tr_b16 a[66:67], v127 offset:12832\nds_read_b64_tr_b16 a[72:73], v127 offset:12352\nds_read_b64_tr_b16 a[74:75], v127 offset:12864\nds_read_b64_tr_b16 a[80:81], v127 offset:12384\nds_read_b64_tr_b16 a[82:83], v127 offset:12896\ns_waitcnt lgkmcnt(0)\ns_add_u32 m0, %[lw], 12288\ns_add_u32 s44, %[wo], 851968\n" \
"buffer_load_dwordx4 v125, %[rw], s44 offen nt lds\ns_add_u32 m0, %[lw], 13312\ns_add_u32 s44, %[wo], 868352\nbuffer_load_dwordx4 v125, %[rw], s44 offen nt lds\ns_add_u32 m0, %[lw], 14336\ns_add_u32 s44, %[wo], 884736\nbuffer_load_dwordx4 v125, %[rw], s44 offen nt lds\ns_add_u32 m0, %[lw], 15360\n" \
"s_add_u32 s44, %[wo], 901120\nbuffer_load_dwordx4 v125, %[rw], s44 offen nt lds\ns_waitcnt vmcnt(32)\nds_read_b64_tr_b16 a[60:61], v127 offset:16384\nds_read_b64_tr_b16 a[62:63], v127 offset:16896\nds_read_b64_tr_b16 a[68:69], v127 offset:16416\nds_read_b64_tr_b16 a[70:71], v127 offset:16928\nds_read_b64_tr_b16 a[76:77], v127 offset:16448\n" \
"ds_read_b64_tr_b16 a[78:79], v127 offset:16960\nds_read_b64_tr_b16 a[84:85], v127 offset:16480\nds_read_b64_tr_b16 a[86:87], v127 offset:16992\ns_waitcnt lgkmcnt(0)\ns_add_u32 m0, %[lw], 16384\ns_add_u32 s44, %[wo], 917504\nbuffer_load_dwordx4 v125, %[rw], s44 offen nt lds\ns_add_u32 m0, %[lw], 17408\n" \
"s_add_u32 s44, %[wo], 933888\nbuffer_load_dwordx4 v125, %[rw], s44 offen nt lds\ns_add_u32 m0, %[lw], 18432\ns_add_u32 s44, %[wo], 950272\nbuffer_load_dwordx4 v125, %[rw], s44 offen nt lds\ns_add_u32 m0, %[lw], 19456\ns_add_u32 s44, %[wo], 966656\nbuffer_load_dwordx4 v125, %[rw], s44 offen nt lds\n" \
"s_waitcnt vmcnt(32)\nds_read_b64_tr_b16 a[88:89], v127 offset:20480\nds_read_b64_tr_b16 a[90:91], v127 offset:20992\nds_read_b64_tr_b16 a[96:97], v127 offset:20512\nds_read_b64_tr_b16 a[98:99], v127 offset:21024\nds_read_b64_tr_b16 a[104:105], v127 offset:20544\nds_read_b64_tr_b16 a[106:107], v127 offset:21056\nds_read_b64_tr_b16 v[84:85], v127 offset:20576\n" \
"ds_read_b64_tr_b16 v[86:87], v127 offset:21088\ns_waitcnt lgkmcnt(0)\ns_add_u32 m0, %[lw], 20480\ns_add_u32 s44, %[wo], 983040\nbuffer_load_dwordx4 v125, %[rw], s44 offen nt lds\ns_add_u32 m0, %[lw], 21504\ns_add_u32 s44, %[wo], 999424\nbuffer_load_dwordx4 v125, %[rw], s44 offen nt lds\n" \
"s_add_u32 m0, %[lw], 22528\ns_add_u32 s44, %[wo], 1015808\nbuffer_load_dwordx4 v125, %[rw], s44 offen nt lds\ns_add_u32 m0, %[lw], 23552\ns_add_u32 s44, %[wo], 1032192\nbuffer_load_dwordx4 v125, %[rw], s44 offen nt lds\ns_waitcnt vmcnt(32)\nds_read_b64_tr_b16 a[92:93], v126 offset:0\n" \
"ds_read_b64_tr_b16 a[94:95], v126 offset:512\nds_read_b64_tr_b16 a[100:101], v126 offset:32\nds_read_b64_tr_b16 a[102:103], v126 offset:544\nds_read_b64_tr_b16 a[108:109], v126 offset:64\nds_read_b64_tr_b16 a[110:111], v126 offset:576\nds_read_b64_tr_b16 v[80:81], v126 offset:96\nds_read_b64_tr_b16 v[82:83], v126 offset:608\ns_waitcnt lgkmcnt(0)\n" \
"s_add_u32 m0, %[tb], 0\ns_add_u32 s44, %[wo], 1048576\nbuffer_load_dwordx4 v125, %[rw], s44 offen nt lds\ns_add_u32 m0, %[tb], 1024\ns_add_u32 s44, %[wo], 1064960\nbuffer_load_dwordx4 v125, %[rw], s44 offen nt lds\ns_add_u32 m0, %[tb], 2048\ns_add_u32 s44, %[wo], 1081344\n" \
"buffer_load_dwordx4 v125, %[rw], s44 offen nt lds\ns_add_u32 m0, %[tb], 3072\ns_add_u32 s44, %[wo], 1097728\nbuffer_load_dwordx4 v125, %[rw], s44 offen nt lds\ns_waitcnt vmcnt(32)\nds_read_b64_tr_b16 a[112:113], v126 offset:4096\nds_read_b64_tr_b16 a[114:115], v126 offset:4608\nds_read_b64_tr_b16 a[120:121], v126 offset:4128\n" \
"ds_read_b64_tr_b16 a[122:123], v126 offset:4640\nds_read_b64_tr_b16 a[128:129], v126 offset:4160\nds_read_b64_tr_b16 a[130:131], v126 offset:4672\nds_read_b64_tr_b16 a[136:137], v126 offset:4192\nds_read_b64_tr_b16 a[138:139], v126 offset:4704\ns_waitcnt lgkmcnt(0)\ns_add_u32 m0, %[tb], 4096\ns_add_u32 s44, %[wo], 1114112\n" \
"buffer_load_dwordx4 v125, %[rw], s44 offen nt lds\ns_add_u32 m0, %[tb], 5120\ns_add_u32 s44, %[wo], 1130496\nbuffer_load_dwordx4 v125, %[rw], s44 offen nt lds\ns_add_u32 m0, %[tb], 6144\ns_add_u32 s44, %[wo], 1146880\nbuffer_load_dwordx4 v125, %[rw], s44 offen nt lds\ns_add_u32 m0, %[tb], 7168\n" \
"s_add_u32 s44, %[wo], 1163264\nbuffer_load_dwordx4 v125, %[rw], s44 offen nt lds\ns_waitcnt vmcnt(32)\nds_read_b64_tr_b16 a[116:117], v126 offset:8192\nds_read_b64_tr_b16 a[118:119], v126 offset:8704\nds_read_b64_tr_b16 a[124:125], v126 offset:8224\nds_read_b64_tr_b16 a[126:127], v126 offset:8736\nds_read_b64_tr_b16 a[132:133], v126 offset:8256\n" \
"ds_read_b64_tr_b16 a[134:135], v126 offset:8768\nds_read_b64_tr_b16 a[140:141], v126 offset:8288\nds_read_b64_tr_b16 a[142:143], v126 offset:8800\ns_waitcnt lgkmcnt(0)\ns_add_u32 m0, %[tb], 8192\ns_add_u32 s44, %[wo], 1179648\nbuffer_load_dwordx4 v125, %[rw], s44 offen nt lds\ns_add_u32 m0, %[tb], 9216\n" \
"s_add_u32 s44, %[wo], 1196032\nbuffer_load_dwordx4 v125, %[rw], s44 offen nt lds\ns_add_u32 m0, %[tb], 10240\ns_add_u32 s44, %[wo], 1212416\nbuffer_load_dwordx4 v125, %[rw], s44 offen nt lds\ns_add_u32 m0, %[tb], 11264\ns_add_u32 s44, %[wo], 1228800\nbuffer_load_dwordx4 v125, %[rw], s44 offen nt lds\n" \
"s_waitcnt vmcnt(32)\nds_read_b64_tr_b16 a[144:145], v127 offset:0\nds_read_b64_tr_b16 a[146:147], v127 offset:512\nds_read_b64_tr_b16 v[76:77], v127 offset:32\nds_read_b64_tr_b16 v[78:79], v127 offset:544\nds_read_b64_tr_b16 a[152:153], v127 offset:64\nds_read_b64_tr_b16 a[154:155], v127 offset:576\nds_read_b64_tr_b16 a[160:161], v127 offset:96\n" \
"ds_read_b64_tr_b16 a[162:163], v127 offset:608\ns_waitcnt lgkmcnt(0)\ns_add_u32 m0, %[lw], 0\ns_add_u32 s44, %[wo], 1245184\nbuffer_load_dwordx4 v125, %[rw], s44 offen nt lds\ns_add_u32 m0, %[lw], 1024\ns_add_u32 s44, %[wo], 1261568\nbuffer_load_dwordx4 v125, %[rw], s44 offen nt lds\n" \
"s_add_u32 m0, %[lw], 2048\ns_add_u32 s44, %[wo], 1277952\nbuffer_load_dwordx4 v125, %[rw], s44 offen nt lds\ns_add_u32 m0, %[lw], 3072\ns_add_u32 s44, %[wo], 1294336\nbuffer_load_dwordx4 v125, %[rw], s44 offen nt lds\ns_waitcnt vmcnt(32)\nds_read_b64_tr_b16 a[148:149], v127 offset:4096\n" \
"ds_read_b64_tr_b16 a[150:151], v127 offset:4608\nds_read_b64_tr_b16 v[72:73], v127 offset:4128\nds_read_b64_tr_b16 v[74:75], v127 offset:4640\nds_read_b64_tr_b16 a[156:157], v127 offset:4160\nds_read_b64_tr_b16 a[158:159], v127 offset:4672\nds_read_b64_tr_b16 a[164:165], v127 offset:4192\nds_read_b64_tr_b16 a[166:167], v127 offset:4704\ns_waitcnt lgkmcnt(0)\n" \
"s_add_u32 m0, %[lw], 4096\ns_add_u32 s44, %[wo], 1310720\nbuffer_load_dwordx4 v125, %[rw], s44 offen nt lds\ns_add_u32 m0, %[lw], 5120\ns_add_u32 s44, %[wo], 1327104\nbuffer_load_dwordx4 v125, %[rw], s44 offen nt lds\ns_add_u32 m0, %[lw], 6144\ns_add_u32 s44, %[wo], 1343488\n" \
"buffer_load_dwordx4 v125, %[rw], s44 offen nt lds\ns_add_u32 m0, %[lw], 7168\ns_add_u32 s44, %[wo], 1359872\nbuffer_load_dwordx4 v125, %[rw], s44 offen nt lds\ns_waitcnt vmcnt(32)\nds_read_b64_tr_b16 a[168:169], v127 offset:8192\nds_read_b64_tr_b16 a[170:171], v127 offset:8704\nds_read_b64_tr_b16 a[176:177], v127 offset:8224\n" \
"ds_read_b64_tr_b16 a[178:179], v127 offset:8736\nds_read_b64_tr_b16 v[68:69], v127 offset:8256\nds_read_b64_tr_b16 v[70:71], v127 offset:8768\nds_read_b64_tr_b16 a[184:185], v127 offset:8288\nds_read_b64_tr_b16 a[186:187], v127 offset:8800\ns_waitcnt lgkmcnt(0)\ns_add_u32 m0, %[lw], 8192\ns_add_u32 s44, %[wo], 1376256\n" \
"buffer_load_dwordx4 v125, %[rw], s44 offen nt lds\ns_add_u32 m0, %[lw], 9216\ns_add_u32 s44, %[wo], 1392640\nbuffer_load_dwordx4 v125, %[rw], s44 offen nt lds\ns_add_u32 m0, %[lw], 10240\ns_add_u32 s44, %[wo], 1409024\nbuffer_load_dwordx4 v125, %[rw], s44 offen nt lds\ns_add_u32 m0, %[lw], 11264\n" \
"s_add_u32 s44, %[wo], 1425408\nbuffer_load_dwordx4 v125, %[rw], s44 offen nt lds\ns_waitcnt vmcnt(32)\nds_read_b64_tr_b16 a[172:173], v127 offset:12288\nds_read_b64_tr_b16 a[174:175], v127 offset:12800\nds_read_b64_tr_b16 a[180:181], v127 offset:12320\nds_read_b64_tr_b16 a[182:183], v127 offset:12832\nds_read_b64_tr_b16 v[64:65], v127 offset:12352\n" \
"ds_read_b64_tr_b16 v[66:67], v127 offset:12864\nds_read_b64_tr_b16 a[188:189], v127 offset:12384\nds_read_b64_tr_b16 a[190:191], v127 offset:12896\ns_waitcnt lgkmcnt(0)\ns_add_u32 m0, %[lw], 12288\ns_add_u32 s44, %[wo], 1441792\nbuffer_load_dwordx4 v125, %[rw], s44 offen nt lds\ns_add_u32 m0, %[lw], 13312\n" \
"s_add_u32 s44, %[wo], 1458176\nbuffer_load_dwordx4 v125, %[rw], s44 offen nt lds\ns_add_u32 m0, %[lw], 14336\ns_add_u32 s44, %[wo], 1474560\nbuffer_load_dwordx4 v125, %[rw], s44 offen nt lds\ns_add_u32 m0, %[lw], 15360\ns_add_u32 s44, %[wo], 1490944\nbuffer_load_dwordx4 v125, %[rw], s44 offen nt lds\n" \
"s_waitcnt vmcnt(32)\nds_read_b64_tr_b16 a[192:193], v127 offset:16384\nds_read_b64_tr_b16 a[194:195], v127 offset:16896\nds_read_b64_tr_b16 a[200:201], v127 offset:16416\nds_read_b64_tr_b16 a[202:203], v127 offset:16928\nds_read_b64_tr_b16 a[208:209], v127 offset:16448\nds_read_b64_tr_b16 a[210:211], v127 offset:16960\nds_read_b64_tr_b16 a[216:217], v127 offset:16480\n" \
"ds_read_b64_tr_b16 a[218:219], v127 offset:16992\ns_waitcnt lgkmcnt(0)\ns_add_u32 m0, %[lw], 16384\ns_add_u32 s44, %[wo], 1507328\nbuffer_load_dwordx4 v125, %[rw], s44 offen nt lds\ns_add_u32 m0, %[lw], 17408\ns_add_u32 s44, %[wo], 1523712\nbuffer_load_dwordx4 v125, %[rw], s44 offen nt lds\n" \
"s_add_u32 m0, %[lw], 18432\ns_add_u32 s44, %[wo], 1540096\nbuffer_load_dwordx4 v125, %[rw], s44 offen nt lds\ns_add_u32 m0, %[lw], 19456\ns_add_u32 s44, %[wo], 1556480\nbuffer_load_dwordx4 v125, %[rw], s44 offen nt lds\ns_waitcnt vmcnt(32)\nds_read_b64_tr_b16 a[196:197], v127 offset:20480\n" \
"ds_read_b64_tr_b16 a[198:199], v127 offset:20992\nds_read_b64_tr_b16 a[204:205], v127 offset:20512\nds_read_b64_tr_b16 a[206:207], v127 offset:21024\nds_read_b64_tr_b16 a[212:213], v127 offset:20544\nds_read_b64_tr_b16 a[214:215], v127 offset:21056\nds_read_b64_tr_b16 a[220:221], v127 offset:20576\nds_read_b64_tr_b16 a[222:223], v127 offset:21088\ns_waitcnt lgkmcnt(0)\n" \
"s_add_u32 m0, %[lw], 20480\ns_add_u32 s44, %[wo], 1572864\nbuffer_load_dwordx4 v125, %[rw], s44 offen nt lds\ns_add_u32 m0, %[lw], 21504\ns_add_u32 s44, %[wo], 1589248\nbuffer_load_dwordx4 v125, %[rw], s44 offen nt lds\ns_add_u32 m0, %[lw], 22528\ns_add_u32 s44, %[wo], 1605632\n" \
"buffer_load_dwordx4 v125, %[rw], s44 offen nt lds\ns_add_u32 m0, %[lw], 23552\ns_add_u32 s44, %[wo], 1622016\nbuffer_load_dwordx4 v125, %[rw], s44 offen nt lds\ns_waitcnt vmcnt(32)\nds_read_b64_tr_b16 v[60:61], v126 offset:0\nds_read_b64_tr_b16 v[62:63], v126 offset:512\nds_read_b64_tr_b16 a[224:225], v126 offset:32\n" \
"ds_read_b64_tr_b16 a[226:227], v126 offset:544\nds_read_b64_tr_b16 a[232:233], v126 offset:64\nds_read_b64_tr_b16 a[234:235], v126 offset:576\nds_read_b64_tr_b16 a[240:241], v126 offset:96\nds_read_b64_tr_b16 a[242:243], v126 offset:608\ns_waitcnt lgkmcnt(0)\ns_add_u32 m0, %[tb], 0\ns_add_u32 s44, %[wo], 1638400\n" \
"buffer_load_dwordx4 v125, %[rw], s44 offen nt lds\ns_add_u32 m0, %[tb], 1024\ns_add_u32 s44, %[wo], 1654784\nbuffer_load_dwordx4 v125, %[rw], s44 offen nt lds\ns_add_u32 m0, %[tb], 2048\ns_add_u32 s44, %[wo], 1671168\nbuffer_load_dwordx4 v125, %[rw], s44 offen nt lds\ns_add_u32 m0, %[tb], 3072\n" \
"s_add_u32 s44, %[wo], 1687552\nbuffer_load_dwordx4 v125, %[rw], s44 offen nt lds\ns_waitcnt vmcnt(32)\nds_read_b64_tr_b16 v[56:57], v126 offset:4096\nds_read_b64_tr_b16 v[58:59], v126 offset:4608\nds_read_b64_tr_b16 a[228:229], v126 offset:4128\nds_read_b64_tr_b16 a[230:231], v126 offset:4640\nds_read_b64_tr_b16 a[236:237], v126 offset:4160\n" \
"ds_read_b64_tr_b16 a[238:239], v126 offset:4672\nds_read_b64_tr_b16 a[244:245], v126 offset:4192\nds_read_b64_tr_b16 a[246:247], v126 offset:4704\ns_waitcnt lgkmcnt(0)\ns_add_u32 m0, %[tb], 4096\ns_add_u32 s44, %[wo], 1703936\nbuffer_load_dwordx4 v125, %[rw], s44 offen nt lds\ns_add_u32 m0, %[tb], 5120\n" \
"s_add_u32 s44, %[wo], 1720320\nbuffer_load_dwordx4 v125, %[rw], s44 offen nt lds\ns_add_u32 m0, %[tb], 6144\ns_add_u32 s44, %[wo], 1736704\nbuffer_load_dwordx4 v125, %[rw], s44 offen nt lds\ns_add_u32 m0, %[tb], 7168\ns_add_u32 s44, %[wo], 1753088\nbuffer_load_dwordx4 v125, %[rw], s44 offen nt lds\n" \
"s_waitcnt vmcnt(32)\nds_read_b64_tr_b16 a[248:249], v126 offset:8192\nds_read_b64_tr_b16 a[250:251], v126 offset:8704\nds_read_b64_tr_b16 v[128:129], v126 offset:8224\nds_read_b64_tr_b16 v[130:131], v126 offset:8736\nds_read_b64_tr_b16 v[136:137], v126 offset:8256\nds_read_b64_tr_b16 v[138:139], v126 offset:8768\nds_read_b64_tr_b16 v[144:145], v126 offset:8288\n" \
"ds_read_b64_tr_b16 v[146:147], v126 offset:8800\ns_waitcnt lgkmcnt(0)\ns_add_u32 m0, %[tb], 8192\ns_add_u32 s44, %[wo], 1769472\nbuffer_load_dwordx4 v125, %[rw], s44 offen nt lds\ns_add_u32 m0, %[tb], 9216\ns_add_u32 s44, %[wo], 1785856\nbuffer_load_dwordx4 v125, %[rw], s44 offen nt lds\n" \
"s_add_u32 m0, %[tb], 10240\ns_add_u32 s44, %[wo], 1802240\nbuffer_load_dwordx4 v125, %[rw], s44 offen nt lds\ns_add_u32 m0, %[tb], 11264\ns_add_u32 s44, %[wo], 1818624\nbuffer_load_dwordx4 v125, %[rw], s44 offen nt lds\ns_waitcnt vmcnt(32)\nds_read_b64_tr_b16 a[252:253], v127 offset:0\n" \
"ds_read_b64_tr_b16 a[254:255], v127 offset:512\nds_read_b64_tr_b16 v[132:133], v127 offset:32\nds_read_b64_tr_b16 v[134:135], v127 offset:544\nds_read_b64_tr_b16 v[140:141], v127 offset:64\nds_read_b64_tr_b16 v[142:143], v127 offset:576\nds_read_b64_tr_b16 v[148:149], v127 offset:96\nds_read_b64_tr_b16 v[150:151], v127 offset:608\ns_waitcnt lgkmcnt(0)\n" \
"ds_write_b128 v1, v[92:95] offset:0\nds_write_b128 v1, v[88:91] offset:1024\nds_write_b128 v1, v[84:87] offset:2048\nds_write_b128 v1, v[80:83] offset:3072\ns_waitcnt vmcnt(28)\nds_read_b64_tr_b16 v[152:153], v127 offset:4096\nds_read_b64_tr_b16 v[154:155], v127 offset:4608\nds_read_b64_tr_b16 v[160:161], v127 offset:4128\n" \
"ds_read_b64_tr_b16 v[162:163], v127 offset:4640\nds_read_b64_tr_b16 v[80:81], v127 offset:4160\nds_read_b64_tr_b16 v[82:83], v127 offset:4672\nds_read_b64_tr_b16 v[168:169], v127 offset:4192\nds_read_b64_tr_b16 v[170:171], v127 offset:4704\ns_waitcnt lgkmcnt(0)\nds_write_b128 v1, v[76:79] offset:4096\nds_write_b128 v1, v[72:75] offset:5120\n" \
"ds_write_b128 v1, v[68:71] offset:6144\nds_write_b128 v1, v[64:67] offset:7168\ns_waitcnt vmcnt(24)\nds_read_b64_tr_b16 v[156:157], v127 offset:8192\nds_read_b64_tr_b16 v[158:159], v127 offset:8704\nds_read_b64_tr_b16 v[164:165], v127 offset:8224\nds_read_b64_tr_b16 v[166:167], v127 offset:8736\nds_read_b64_tr_b16 v[64:65], v127 offset:8256\n" \
"ds_read_b64_tr_b16 v[66:67], v127 offset:8768\nds_read_b64_tr_b16 v[172:173], v127 offset:8288\nds_read_b64_tr_b16 v[174:175], v127 offset:8800\ns_waitcnt lgkmcnt(0)\nds_write_b128 v1, v[60:63] offset:8192\nds_write_b128 v1, v[56:59] offset:9216\nds_write_b128 v1, v[80:83] offset:10240\nds_write_b128 v1, v[64:67] offset:11264\n" \
"s_waitcnt vmcnt(20)\nds_read_b64_tr_b16 v[176:177], v127 offset:12288\nds_read_b64_tr_b16 v[178:179], v127 offset:12800\nds_read_b64_tr_b16 v[184:185], v127 offset:12320\nds_read_b64_tr_b16 v[186:187], v127 offset:12832\nds_read_b64_tr_b16 v[192:193], v127 offset:12352\nds_read_b64_tr_b16 v[194:195], v127 offset:12864\nds_read_b64_tr_b16 v[200:201], v127 offset:12384\n" \
"ds_read_b64_tr_b16 v[202:203], v127 offset:12896\ns_waitcnt lgkmcnt(0)\ns_waitcnt vmcnt(16)\nds_read_b64_tr_b16 v[180:181], v127 offset:16384\nds_read_b64_tr_b16 v[182:183], v127 offset:16896\nds_read_b64_tr_b16 v[188:189], v127 offset:16416\nds_read_b64_tr_b16 v[190:191], v127 offset:16928\nds_read_b64_tr_b16 v[196:197], v127 offset:16448\n" \
"ds_read_b64_tr_b16 v[198:199], v127 offset:16960\nds_read_b64_tr_b16 v[204:205], v127 offset:16480\nds_read_b64_tr_b16 v[206:207], v127 offset:16992\ns_waitcnt lgkmcnt(0)\ns_waitcnt vmcnt(12)\nds_read_b64_tr_b16 v[64:65], v127 offset:20480\nds_read_b64_tr_b16 v[66:67], v127 offset:20992\nds_read_b64_tr_b16 v[208:209], v127 offset:20512\n" \
"ds_read_b64_tr_b16 v[210:211], v127 offset:21024\nds_read_b64_tr_b16 v[216:217], v127 offset:20544\nds_read_b64_tr_b16 v[218:219], v127 offset:21056\nds_read_b64_tr_b16 v[224:225], v127 offset:20576\nds_read_b64_tr_b16 v[226:227], v127 offset:21088\ns_waitcnt lgkmcnt(0)\nds_write_b128 v1, v[64:67] offset:12288\ns_waitcnt vmcnt(8)\n" \
"ds_read_b64_tr_b16 v[64:65], v126 offset:0\nds_read_b64_tr_b16 v[66:67], v126 offset:512\nds_read_b64_tr_b16 v[212:213], v126 offset:32\nds_read_b64_tr_b16 v[214:215], v126 offset:544\nds_read_b64_tr_b16 v[220:221], v126 offset:64\nds_read_b64_tr_b16 v[222:223], v126 offset:576\nds_read_b64_tr_b16 v[228:229], v126 offset:96\nds_read_b64_tr_b16 v[230:231], v126 offset:608\n" \
"s_waitcnt lgkmcnt(0)\nds_write_b128 v1, v[64:67] offset:13312\ns_waitcnt vmcnt(4)\nds_read_b64_tr_b16 v[232:233], v126 offset:4096\nds_read_b64_tr_b16 v[234:235], v126 offset:4608\nds_read_b64_tr_b16 v[64:65], v126 offset:4128\nds_read_b64_tr_b16 v[66:67], v126 offset:4640\nds_read_b64_tr_b16 v[240:241], v126 offset:4160\n" \
"ds_read_b64_tr_b16 v[242:243], v126 offset:4672\nds_read_b64_tr_b16 v[248:249], v126 offset:4192\nds_read_b64_tr_b16 v[250:251], v126 offset:4704\ns_waitcnt lgkmcnt(0)\nds_write_b128 v1, v[64:67] offset:14336\ns_waitcnt vmcnt(0)\nds_read_b64_tr_b16 v[236:237], v126 offset:8192\nds_read_b64_tr_b16 v[238:239], v126 offset:8704\n" \
"ds_read_b64_tr_b16 v[64:65], v126 offset:8224\nds_read_b64_tr_b16 v[66:67], v126 offset:8736\nds_read_b64_tr_b16 v[244:245], v126 offset:8256\nds_read_b64_tr_b16 v[246:247], v126 offset:8768\nds_read_b64_tr_b16 v[252:253], v126 offset:8288\nds_read_b64_tr_b16 v[254:255], v126 offset:8800\ns_waitcnt lgkmcnt(0)\nds_write_b128 v1, v[64:67] offset:15360\n" \
"s_waitcnt lgkmcnt(0)\ns_barrier\ns_mov_b32 s40, 0\ns_mov_b32 s43, 0\ns_mov_b32 s56, -2147483648\n.p2align 6\n.Ldd_next%=:\ns_cmp_eq_u32 s40, %[b1]\n" \
"s_cselect_b32 s44, 4, 0\nv_xor_b32 v7, s44, v7\nbuffer_load_dword v8, v7, %[rs], 0 offen sc1\ns_lshl_b32 s58, s40, 1\ns_add_u32 s44, s40, 0\ns_lshl_b32 s44, s44, 19\ns_add_u32 s44, s44, %[ho]\ns_lshr_b32 s45, s44, 8\n" \
"s_and_b32 s45, s45, 0x2000\ns_xor_b32 s55, s44, s45\ns_waitcnt vmcnt(0)\nbuffer_store_dword v9, off, %[rs], s56 sc1\ns_mov_b32 s56, -2147483648\n.Ldd_w0%=:\nv_cmp_gt_i32 vcc, s58, v8\ns_cbranch_vccz .Ldd_w0go%=\n" \
"s_cmp_ge_u32 s40, 62\ns_cbranch_scc1 .Ldd_ns671%=\ns_sleep 8\n.Ldd_ns671%=:\ns_sleep 1\nbuffer_load_dword v8, v7, %[rs], 0 offen sc1\ns_waitcnt vmcnt(0)\ns_add_u32 s43, s43, 1\n" \
"s_cmp_lt_u32 s43, 262144\ns_cbranch_scc1 .Ldd_w0%=\ns_branch .Ldd_abort%=\n.Ldd_w0go%=:\ns_add_u32 s60, s55, 0\nbuffer_load_dwordx4 v[48:51], v0, %[rs], s60 offen offset:0 sc1\nbuffer_load_dwordx4 v[52:55], v0, %[rs], s60 offen offset:2048 sc1\ns_add_u32 s60, s55, 4096\n" \
"buffer_load_dwordx4 v[56:59], v0, %[rs], s60 offen offset:0 sc1\nbuffer_load_dwordx4 v[60:63], v0, %[rs], s60 offen offset:2048 sc1\ns_add_u32 s60, s55, 16384\nbuffer_load_dwordx4 v[64:67], v0, %[rs], s60 offen offset:0 sc1\nbuffer_load_dwordx4 v[68:71], v0, %[rs], s60 offen offset:2048 sc1\ns_add_u32 s60, s55, 20480\nbuffer_load_dwordx4 v[72:75], v0, %[rs], s60 offen offset:0 sc1\nbuffer_load_dwordx4 v[76:79], v0, %[rs], s60 offen offset:2048 sc1\n" \
"s_add_u32 s60, s55, 32768\nbuffer_load_dwordx4 v[80:83], v0, %[rs], s60 offen offset:0 sc1\nbuffer_load_dwordx4 v[84:87], v0, %[rs], s60 offen offset:2048 sc1\ns_add_u32 s60, s55, 36864\nbuffer_load_dwordx4 v[88:91], v0, %[rs], s60 offen offset:0 sc1\nbuffer_load_dwordx4 v[92:95], v0, %[rs], s60 offen offset:2048 sc1\ns_add_u32 s60, s55, 49152\nbuffer_load_dwordx4 v[96:99], v0, %[rs], s60 offen offset:0 sc1\n" \
"buffer_load_dwordx4 v[100:103], v0, %[rs], s60 offen offset:2048 sc1\ns_add_u32 s60, s55, 53248\nbuffer_load_dwordx4 v[104:107], v0, %[rs], s60 offen offset:0 sc1\n.Ldd_run%=:\ns_add_u32 s44, s40, 0\ns_lshl_b32 s44, s44, 17\ns_add_u32 s44, s44, %[xn]\ns_lshr_b32 s45, s44, 8\n" \
"s_and_b32 s45, s45, 0x2000\ns_xor_b32 s41, s44, s45\ns_lshl_b32 s44, s40, 16\ns_cmp_eq_u32 %[l], 0\ns_cselect_b32 s42, s41, s44\ns_add_u32 s58, s58, 1\ns_waitcnt vmcnt(14)\nv_mfma_f32_16x16x32_bf16 v[16:19], a[0:3], v[48:51], 0\n" \
"s_waitcnt vmcnt(13)\nds_read_b128 v[120:123], v1 offset:0\nv_mfma_f32_16x16x32_bf16 v[16:19], a[4:7], v[52:55], v[16:19]\nbuffer_load_dword v8, v7, %[rs], 0 offen sc1\nv_mfma_f32_16x16x32_bf16 v[24:27], a[8:11], v[48:51], 0\nv_mfma_f32_16x16x32_bf16 v[24:27], a[12:15], v[52:55], v[24:27]\nbuffer_load_dwordx4 v[108:111], v0, %[rs], s60 offen offset:2048 sc1\nv_mfma_f32_16x16x32_bf16 v[32:35], a[16:19], v[48:51], 0\n" \
"ds_read_b128 v[124:127], v1 offset:1024\nv_mfma_f32_16x16x32_bf16 v[32:35], a[20:23], v[52:55], v[32:35]\nv_mfma_f32_16x16x32_bf16 v[40:43], a[24:27], v[48:51], 0\ns_add_u32 s60, s55, 65536\nv_mfma_f32_16x16x32_bf16 v[40:43], a[28:31], v[52:55], v[40:43]\ns_waitcnt vmcnt(14)\nbuffer_load_dwordx4 v[48:51], v0, %[rs], s60 offen offset:0 sc1\nv_mfma_f32_16x16x32_bf16 v[16:19], a[32:35], v[56:59], v[16:19]\n" \
"s_waitcnt vmcnt(14)\nv_mfma_f32_16x16x32_bf16 v[16:19], a[36:39], v[60:63], v[16:19]\ns_waitcnt lgkmcnt(1)\nv_mfma_f32_16x16x32_bf16 v[24:27], v[120:123], v[56:59], v[24:27]\ns_waitcnt lgkmcnt(0)\nv_mfma_f32_16x16x32_bf16 v[24:27], v[124:127], v[60:63], v[24:27]\nbuffer_load_dwordx4 v[52:55], v0, %[rs], s60 offen offset:2048 sc1\nv_mfma_f32_16x16x32_bf16 v[32:35], a[40:43], v[56:59], v[32:35]\n" \
"v_mfma_f32_16x16x32_bf16 v[32:35], a[44:47], v[60:63], v[32:35]\nv_mfma_f32_16x16x32_bf16 v[40:43], a[48:51], v[56:59], v[40:43]\ns_add_u32 s60, s55, 69632\nv_mfma_f32_16x16x32_bf16 v[40:43], a[52:55], v[60:63], v[40:43]\ns_waitcnt vmcnt(14)\nbuffer_load_dwordx4 v[56:59], v0, %[rs], s60 offen offset:0 sc1\nv_mfma_f32_16x16x32_bf16 v[16:19], a[56:59], v[64:67], v[16:19]\ns_waitcnt vmcnt(14)\n" \
"ds_read_b128 v[120:123], v1 offset:2048\nv_mfma_f32_16x16x32_bf16 v[16:19], a[60:63], v[68:71], v[16:19]\nv_mfma_f32_16x16x32_bf16 v[24:27], a[64:67], v[64:67], v[24:27]\nv_mfma_f32_16x16x32_bf16 v[24:27], a[68:71], v[68:71], v[24:27]\nbuffer_load_dwordx4 v[60:63], v0, %[rs], s60 offen offset:2048 sc1\nv_mfma_f32_16x16x32_bf16 v[32:35], a[72:75], v[64:67], v[32:35]\nds_read_b128 v[124:127], v1 offset:3072\nv_mfma_f32_16x16x32_bf16 v[32:35], a[76:79], v[68:71], v[32:35]\n" \
"v_mfma_f32_16x16x32_bf16 v[40:43], a[80:83], v[64:67], v[40:43]\ns_add_u32 s60, s55, 81920\nv_mfma_f32_16x16x32_bf16 v[40:43], a[84:87], v[68:71], v[40:43]\ns_waitcnt vmcnt(14)\nbuffer_load_dwordx4 v[64:67], v0, %[rs], s60 offen offset:0 sc1\nv_mfma_f32_16x16x32_bf16 v[16:19], a[88:91], v[72:75], v[16:19]\ns_waitcnt vmcnt(14)\nv_mfma_f32_16x16x32_bf16 v[16:19], a[92:95], v[76:79], v[16:19]\n" \
"v_mfma_f32_16x16x32_bf16 v[24:27], a[96:99], v[72:75], v[24:27]\nv_mfma_f32_16x16x32_bf16 v[24:27], a[100:103], v[76:79], v[24:27]\nbuffer_load_dwordx4 v[68:71], v0, %[rs], s60 offen offset:2048 sc1\nv_mfma_f32_16x16x32_bf16 v[32:35], a[104:107], v[72:75], v[32:35]\nv_mfma_f32_16x16x32_bf16 v[32:35], a[108:111], v[76:79], v[32:35]\ns_waitcnt lgkmcnt(1)\nv_mfma_f32_16x16x32_bf16 v[40:43], v[120:123], v[72:75], v[40:43]\ns_waitcnt lgkmcnt(0)\n" \
"s_add_u32 s60, s55, 86016\nv_mfma_f32_16x16x32_bf16 v[40:43], v[124:127], v[76:79], v[40:43]\ns_waitcnt vmcnt(14)\nbuffer_load_dwordx4 v[72:75], v0, %[rs], s60 offen offset:0 sc1\nv_mfma_f32_16x16x32_bf16 v[16:19], a[112:115], v[80:83], v[16:19]\ns_waitcnt vmcnt(14)\nds_read_b128 v[120:123], v1 offset:4096\nv_mfma_f32_16x16x32_bf16 v[16:19], a[116:119], v[84:87], v[16:19]\n" \
"v_mfma_f32_16x16x32_bf16 v[24:27], a[120:123], v[80:83], v[24:27]\nv_mfma_f32_16x16x32_bf16 v[24:27], a[124:127], v[84:87], v[24:27]\nbuffer_load_dwordx4 v[76:79], v0, %[rs], s60 offen offset:2048 sc1\nv_mfma_f32_16x16x32_bf16 v[32:35], a[128:131], v[80:83], v[32:35]\nds_read_b128 v[124:127], v1 offset:5120\nv_mfma_f32_16x16x32_bf16 v[32:35], a[132:135], v[84:87], v[32:35]\nv_mfma_f32_16x16x32_bf16 v[40:43], a[136:139], v[80:83], v[40:43]\ns_add_u32 s60, s55, 98304\n" \
"v_mfma_f32_16x16x32_bf16 v[40:43], a[140:143], v[84:87], v[40:43]\ns_waitcnt vmcnt(14)\nbuffer_load_dwordx4 v[80:83], v0, %[rs], s60 offen offset:0 sc1\nv_mfma_f32_16x16x32_bf16 v[16:19], a[144:147], v[88:91], v[16:19]\ns_waitcnt vmcnt(14)\nds_read_b128 v[112:115], v1 offset:6144\nv_mfma_f32_16x16x32_bf16 v[16:19], a[148:151], v[92:95], v[16:19]\ns_waitcnt lgkmcnt(2)\n" \
"v_mfma_f32_16x16x32_bf16 v[24:27], v[120:123], v[88:91], v[24:27]\ns_waitcnt lgkmcnt(1)\nv_mfma_f32_16x16x32_bf16 v[24:27], v[124:127], v[92:95], v[24:27]\nbuffer_load_dwordx4 v[84:87], v0, %[rs], s60 offen offset:2048 sc1\nv_mfma_f32_16x16x32_bf16 v[32:35], a[152:155], v[88:91], v[32:35]\nds_read_b128 v[116:119], v1 offset:7168\nv_mfma_f32_16x16x32_bf16 v[32:35], a[156:159], v[92:95], v[32:35]\ns_waitcnt vmcnt(11)\n" \
"v_cmp_gt_i32 vcc, s58, v8\nv_mfma_f32_16x16x32_bf16 v[40:43], a[160:163], v[88:91], v[40:43]\ns_cmp_eq_u64 vcc, 0\ns_cselect_b32 s61, s55, -2147483648\nv_mfma_f32_16x16x32_bf16 v[40:43], a[164:167], v[92:95], v[40:43]\ns_add_u32 s60, s55, 102400\nbuffer_load_dwordx4 v[88:91], v0, %[rs], s60 offen offset:0 sc1\nv_mfma_f32_16x16x32_bf16 v[16:19], a[168:171], v[96:99], v[16:19]\n" \
"v_mfma_f32_16x16x32_bf16 v[16:19], a[172:175], v[100:103], v[16:19]\nv_mfma_f32_16x16x32_bf16 v[24:27], a[176:179], v[96:99], v[24:27]\nv_mfma_f32_16x16x32_bf16 v[24:27], a[180:183], v[100:103], v[24:27]\ns_waitcnt lgkmcnt(1)\nbuffer_load_dwordx4 v[92:95], v0, %[rs], s60 offen offset:2048 sc1\nv_mfma_f32_16x16x32_bf16 v[32:35], v[112:115], v[96:99], v[32:35]\ns_waitcnt lgkmcnt(0)\nv_mfma_f32_16x16x32_bf16 v[32:35], v[116:119], v[100:103], v[32:35]\n" \
"v_mfma_f32_16x16x32_bf16 v[40:43], a[184:187], v[96:99], v[40:43]\nv_mfma_f32_16x16x32_bf16 v[40:43], a[188:191], v[100:103], v[40:43]\ns_add_u32 s60, s61, 0\nbuffer_load_dwordx4 v[96:99], v0, %[rs], s60 offen offset:1024 sc1\nv_mfma_f32_16x16x32_bf16 v[16:19], a[192:195], v[104:107], v[16:19]\ns_waitcnt vmcnt(13)\nds_read_b128 v[112:115], v1 offset:8192\nv_mfma_f32_16x16x32_bf16 v[16:19], a[196:199], v[108:111], v[16:19]\n" \
"v_mfma_f32_16x16x32_bf16 v[24:27], a[200:203], v[104:107], v[24:27]\nv_mfma_f32_16x16x32_bf16 v[24:27], a[204:207], v[108:111], v[24:27]\nbuffer_load_dwordx4 v[100:103], v0, %[rs], s60 offen offset:3072 sc1\nv_mfma_f32_16x16x32_bf16 v[32:35], a[208:211], v[104:107], v[32:35]\nds_read_b128 v[116:119], v1 offset:9216\nv_mfma_f32_16x16x32_bf16 v[32:35], a[212:215], v[108:111], v[32:35]\nv_mfma_f32_16x16x32_bf16 v[40:43], a[216:219], v[104:107], v[40:43]\ns_add_u32 s60, s61, 4096\n" \
"v_mfma_f32_16x16x32_bf16 v[40:43], a[220:223], v[108:111], v[40:43]\ns_waitcnt vmcnt(13)\ns_waitcnt lgkmcnt(1)\nbuffer_load_dwordx4 v[104:107], v0, %[rs], s60 offen offset:1024 sc1\nv_mfma_f32_16x16x32_bf16 v[16:19], v[112:115], v[48:51], v[16:19]\ns_waitcnt vmcnt(13)\ns_waitcnt lgkmcnt(0)\nv_mfma_f32_16x16x32_bf16 v[16:19], v[116:119], v[52:55], v[16:19]\n" \
"v_mfma_f32_16x16x32_bf16 v[24:27], a[224:227], v[48:51], v[24:27]\nv_mfma_f32_16x16x32_bf16 v[24:27], a[228:231], v[52:55], v[24:27]\nbuffer_load_dwordx4 v[108:111], v0, %[rs], s60 offen offset:3072 sc1\nv_mfma_f32_16x16x32_bf16 v[32:35], a[232:235], v[48:51], v[32:35]\nv_mfma_f32_16x16x32_bf16 v[32:35], a[236:239], v[52:55], v[32:35]\nv_mfma_f32_16x16x32_bf16 v[40:43], a[240:243], v[48:51], v[40:43]\ns_add_u32 s60, s61, 16384\nv_mfma_f32_16x16x32_bf16 v[40:43], a[244:247], v[52:55], v[40:43]\n" \
"s_waitcnt vmcnt(13)\nbuffer_load_dwordx4 v[48:51], v0, %[rs], s60 offen offset:1024 sc1\nv_mfma_f32_16x16x32_bf16 v[16:19], a[248:251], v[56:59], v[16:19]\ns_waitcnt vmcnt(13)\nds_read_b128 v[112:115], v1 offset:10240\nv_mfma_f32_16x16x32_bf16 v[16:19], a[252:255], v[60:63], v[16:19]\nv_mfma_f32_16x16x32_bf16 v[24:27], v[128:131], v[56:59], v[24:27]\nv_mfma_f32_16x16x32_bf16 v[24:27], v[132:135], v[60:63], v[24:27]\n" \
"buffer_load_dwordx4 v[52:55], v0, %[rs], s60 offen offset:3072 sc1\nv_mfma_f32_16x16x32_bf16 v[32:35], v[136:139], v[56:59], v[32:35]\nds_read_b128 v[116:119], v1 offset:11264\nv_mfma_f32_16x16x32_bf16 v[32:35], v[140:143], v[60:63], v[32:35]\nv_mfma_f32_16x16x32_bf16 v[40:43], v[144:147], v[56:59], v[40:43]\ns_add_u32 s60, s61, 20480\nv_mfma_f32_16x16x32_bf16 v[40:43], v[148:151], v[60:63], v[40:43]\ns_waitcnt vmcnt(13)\n" \
"buffer_load_dwordx4 v[56:59], v0, %[rs], s60 offen offset:1024 sc1\nv_mfma_f32_16x16x32_bf16 v[16:19], v[152:155], v[64:67], v[16:19]\ns_waitcnt vmcnt(13)\nv_mfma_f32_16x16x32_bf16 v[16:19], v[156:159], v[68:71], v[16:19]\nv_mfma_f32_16x16x32_bf16 v[24:27], v[160:163], v[64:67], v[24:27]\nv_mfma_f32_16x16x32_bf16 v[24:27], v[164:167], v[68:71], v[24:27]\ns_waitcnt lgkmcnt(1)\nbuffer_load_dwordx4 v[60:63], v0, %[rs], s60 offen offset:3072 sc1\n" \
"v_mfma_f32_16x16x32_bf16 v[32:35], v[112:115], v[64:67], v[32:35]\ns_waitcnt lgkmcnt(0)\nv_mfma_f32_16x16x32_bf16 v[32:35], v[116:119], v[68:71], v[32:35]\nv_mfma_f32_16x16x32_bf16 v[40:43], v[168:171], v[64:67], v[40:43]\ns_add_u32 s60, s61, 32768\nv_mfma_f32_16x16x32_bf16 v[40:43], v[172:175], v[68:71], v[40:43]\ns_waitcnt vmcnt(13)\nbuffer_load_dwordx4 v[64:67], v0, %[rs], s60 offen offset:1024 sc1\n" \
"v_mfma_f32_16x16x32_bf16 v[16:19], v[176:179], v[72:75], v[16:19]\ns_waitcnt vmcnt(13)\nds_read_b128 v[112:115], v1 offset:12288\nv_mfma_f32_16x16x32_bf16 v[16:19], v[180:183], v[76:79], v[16:19]\nv_mfma_f32_16x16x32_bf16 v[24:27], v[184:187], v[72:75], v[24:27]\nv_mfma_f32_16x16x32_bf16 v[24:27], v[188:191], v[76:79], v[24:27]\nbuffer_load_dwordx4 v[68:71], v0, %[rs], s60 offen offset:3072 sc1\nv_mfma_f32_16x16x32_bf16 v[32:35], v[192:195], v[72:75], v[32:35]\n" \
"ds_read_b128 v[116:119], v1 offset:13312\nv_mfma_f32_16x16x32_bf16 v[32:35], v[196:199], v[76:79], v[32:35]\nv_mfma_f32_16x16x32_bf16 v[40:43], v[200:203], v[72:75], v[40:43]\ns_add_u32 s60, s61, 36864\nv_mfma_f32_16x16x32_bf16 v[40:43], v[204:207], v[76:79], v[40:43]\ns_waitcnt vmcnt(13)\ns_waitcnt lgkmcnt(1)\nbuffer_load_dwordx4 v[72:75], v0, %[rs], s60 offen offset:1024 sc1\n" \
"v_mfma_f32_16x16x32_bf16 v[16:19], v[112:115], v[80:83], v[16:19]\ns_waitcnt vmcnt(13)\ns_waitcnt lgkmcnt(0)\nds_read_b128 v[120:123], v1 offset:14336\nv_mfma_f32_16x16x32_bf16 v[16:19], v[116:119], v[84:87], v[16:19]\nv_mfma_f32_16x16x32_bf16 v[24:27], v[208:211], v[80:83], v[24:27]\nv_mfma_f32_16x16x32_bf16 v[24:27], v[212:215], v[84:87], v[24:27]\nbuffer_load_dwordx4 v[76:79], v0, %[rs], s60 offen offset:3072 sc1\n" \
"v_mfma_f32_16x16x32_bf16 v[32:35], v[216:219], v[80:83], v[32:35]\nds_read_b128 v[124:127], v1 offset:15360\nv_mfma_f32_16x16x32_bf16 v[32:35], v[220:223], v[84:87], v[32:35]\nv_mfma_f32_16x16x32_bf16 v[40:43], v[224:227], v[80:83], v[40:43]\ns_add_u32 s60, s61, 49152\nv_mfma_f32_16x16x32_bf16 v[40:43], v[228:231], v[84:87], v[40:43]\ns_waitcnt vmcnt(13)\nbuffer_load_dwordx4 v[80:83], v0, %[rs], s60 offen offset:1024 sc1\n" \
"v_mfma_f32_16x16x32_bf16 v[16:19], v[232:235], v[88:91], v[16:19]\ns_waitcnt vmcnt(13)\nv_mfma_f32_16x16x32_bf16 v[16:19], v[236:239], v[92:95], v[16:19]\ns_waitcnt lgkmcnt(1)\nv_mfma_f32_16x16x32_bf16 v[24:27], v[120:123], v[88:91], v[24:27]\ns_waitcnt lgkmcnt(0)\nv_mfma_f32_16x16x32_bf16 v[24:27], v[124:127], v[92:95], v[24:27]\nbuffer_load_dwordx4 v[84:87], v0, %[rs], s60 offen offset:3072 sc1\n" \
"v_mfma_f32_16x16x32_bf16 v[32:35], v[240:243], v[88:91], v[32:35]\nv_mfma_f32_16x16x32_bf16 v[32:35], v[244:247], v[92:95], v[32:35]\nv_mfma_f32_16x16x32_bf16 v[40:43], v[248:251], v[88:91], v[40:43]\nv_mfma_f32_16x16x32_bf16 v[40:43], v[252:255], v[92:95], v[40:43]\ns_add_u32 s60, s61, 53248\nbuffer_load_dwordx4 v[88:91], v0, %[rs], s60 offen offset:1024 sc1\ns_cbranch_vccnz .Ldd_slow%=\n.Ldd_slowr%=:\n" \
"s_waitcnt vmcnt(14)\nv_mfma_f32_16x16x32_bf16 v[20:23], a[0:3], v[96:99], 0\ns_waitcnt vmcnt(13)\nds_read_b128 v[120:123], v1 offset:0\nv_mfma_f32_16x16x32_bf16 v[20:23], a[4:7], v[100:103], v[20:23]\nv_mfma_f32_16x16x32_bf16 v[28:31], a[8:11], v[96:99], 0\nv_mfma_f32_16x16x32_bf16 v[28:31], a[12:15], v[100:103], v[28:31]\nbuffer_load_dwordx4 v[92:95], v0, %[rs], s60 offen offset:3072 sc1\n" \
"v_mfma_f32_16x16x32_bf16 v[36:39], a[16:19], v[96:99], 0\nds_read_b128 v[124:127], v1 offset:1024\nv_mfma_f32_16x16x32_bf16 v[36:39], a[20:23], v[100:103], v[36:39]\nv_mfma_f32_16x16x32_bf16 v[44:47], a[24:27], v[96:99], 0\ns_add_u32 s60, s55, 65536\nv_mfma_f32_16x16x32_bf16 v[44:47], a[28:31], v[100:103], v[44:47]\ns_waitcnt vmcnt(13)\nbuffer_load_dwordx4 v[96:99], v0, %[rs], s60 offen offset:1024 sc1\n" \
"v_mfma_f32_16x16x32_bf16 v[20:23], a[32:35], v[104:107], v[20:23]\ns_waitcnt vmcnt(13)\nv_mfma_f32_16x16x32_bf16 v[20:23], a[36:39], v[108:111], v[20:23]\ns_waitcnt lgkmcnt(1)\nds_write_b128 v2, v[16:19] offset:0\nv_mfma_f32_16x16x32_bf16 v[28:31], v[120:123], v[104:107], v[28:31]\ns_waitcnt lgkmcnt(1)\nv_mfma_f32_16x16x32_bf16 v[28:31], v[124:127], v[108:111], v[28:31]\n" \
"buffer_load_dwordx4 v[100:103], v0, %[rs], s60 offen offset:3072 sc1\nv_mfma_f32_16x16x32_bf16 v[36:39], a[40:43], v[104:107], v[36:39]\nv_mfma_f32_16x16x32_bf16 v[36:39], a[44:47], v[108:111], v[36:39]\nv_mfma_f32_16x16x32_bf16 v[44:47], a[48:51], v[104:107], v[44:47]\ns_add_u32 s60, s55, 69632\nv_mfma_f32_16x16x32_bf16 v[44:47], a[52:55], v[108:111], v[44:47]\ns_waitcnt vmcnt(13)\nbuffer_load_dwordx4 v[104:107], v0, %[rs], s60 offen offset:1024 sc1\n" \
"v_mfma_f32_16x16x32_bf16 v[20:23], a[56:59], v[48:51], v[20:23]\ns_waitcnt vmcnt(13)\nds_read_b128 v[120:123], v1 offset:2048\nv_mfma_f32_16x16x32_bf16 v[20:23], a[60:63], v[52:55], v[20:23]\nds_write_b128 v2, v[24:27] offset:2048\nv_mfma_f32_16x16x32_bf16 v[28:31], a[64:67], v[48:51], v[28:31]\nv_mfma_f32_16x16x32_bf16 v[28:31], a[68:71], v[52:55], v[28:31]\nbuffer_load_dwordx4 v[108:111], v0, %[rs], s60 offen offset:3072 sc1\n" \
"v_mfma_f32_16x16x32_bf16 v[36:39], a[72:75], v[48:51], v[36:39]\nds_read_b128 v[124:127], v1 offset:3072\nv_mfma_f32_16x16x32_bf16 v[36:39], a[76:79], v[52:55], v[36:39]\nv_mfma_f32_16x16x32_bf16 v[44:47], a[80:83], v[48:51], v[44:47]\ns_add_u32 s60, s55, 81920\nv_mfma_f32_16x16x32_bf16 v[44:47], a[84:87], v[52:55], v[44:47]\ns_waitcnt vmcnt(13)\nbuffer_load_dwordx4 v[48:51], v0, %[rs], s60 offen offset:1024 sc1\n" \
"v_mfma_f32_16x16x32_bf16 v[20:23], a[88:91], v[56:59], v[20:23]\ns_waitcnt vmcnt(13)\nv_mfma_f32_16x16x32_bf16 v[20:23], a[92:95], v[60:63], v[20:23]\nds_write_b128 v2, v[32:35] offset:4096\nv_mfma_f32_16x16x32_bf16 v[28:31], a[96:99], v[56:59], v[28:31]\nv_mfma_f32_16x16x32_bf16 v[28:31], a[100:103], v[60:63], v[28:31]\nbuffer_load_dwordx4 v[52:55], v0, %[rs], s60 offen offset:3072 sc1\nv_mfma_f32_16x16x32_bf16 v[36:39], a[104:107], v[56:59], v[36:39]\n" \
"v_mfma_f32_16x16x32_bf16 v[36:39], a[108:111], v[60:63], v[36:39]\ns_waitcnt lgkmcnt(3)\nv_mfma_f32_16x16x32_bf16 v[44:47], v[120:123], v[56:59], v[44:47]\ns_waitcnt lgkmcnt(1)\ns_add_u32 s60, s55, 86016\nv_mfma_f32_16x16x32_bf16 v[44:47], v[124:127], v[60:63], v[44:47]\ns_waitcnt vmcnt(13)\nbuffer_load_dwordx4 v[56:59], v0, %[rs], s60 offen offset:1024 sc1\n" \
"v_mfma_f32_16x16x32_bf16 v[20:23], a[112:115], v[64:67], v[20:23]\ns_waitcnt vmcnt(13)\nds_read_b128 v[120:123], v1 offset:4096\nv_mfma_f32_16x16x32_bf16 v[20:23], a[116:119], v[68:71], v[20:23]\nds_write_b128 v2, v[40:43] offset:6144\nv_mfma_f32_16x16x32_bf16 v[28:31], a[120:123], v[64:67], v[28:31]\nv_mfma_f32_16x16x32_bf16 v[28:31], a[124:127], v[68:71], v[28:31]\nbuffer_load_dwordx4 v[60:63], v0, %[rs], s60 offen offset:3072 sc1\n" \
"v_mfma_f32_16x16x32_bf16 v[36:39], a[128:131], v[64:67], v[36:39]\nds_read_b128 v[124:127], v1 offset:5120\nv_mfma_f32_16x16x32_bf16 v[36:39], a[132:135], v[68:71], v[36:39]\nv_mfma_f32_16x16x32_bf16 v[44:47], a[136:139], v[64:67], v[44:47]\ns_add_u32 s60, s55, 98304\nv_mfma_f32_16x16x32_bf16 v[44:47], a[140:143], v[68:71], v[44:47]\ns_waitcnt vmcnt(13)\nbuffer_load_dwordx4 v[64:67], v0, %[rs], s60 offen offset:1024 sc1\n" \
"v_mfma_f32_16x16x32_bf16 v[20:23], a[144:147], v[72:75], v[20:23]\ns_waitcnt vmcnt(13)\nds_read_b128 v[112:115], v1 offset:6144\nv_mfma_f32_16x16x32_bf16 v[20:23], a[148:151], v[76:79], v[20:23]\ns_waitcnt lgkmcnt(3)\nv_mfma_f32_16x16x32_bf16 v[28:31], v[120:123], v[72:75], v[28:31]\ns_waitcnt lgkmcnt(1)\nv_mfma_f32_16x16x32_bf16 v[28:31], v[124:127], v[76:79], v[28:31]\n" \
"buffer_load_dwordx4 v[68:71], v0, %[rs], s60 offen offset:3072 sc1\nv_mfma_f32_16x16x32_bf16 v[36:39], a[152:155], v[72:75], v[36:39]\nds_read_b128 v[116:119], v1 offset:7168\nv_mfma_f32_16x16x32_bf16 v[36:39], a[156:159], v[76:79], v[36:39]\nbuffer_load_dwordx4 v[12:15], v4, %[rs], s41 offen sc1\nv_mfma_f32_16x16x32_bf16 v[44:47], a[160:163], v[72:75], v[44:47]\ns_add_u32 s60, s55, 102400\nv_mfma_f32_16x16x32_bf16 v[44:47], a[164:167], v[76:79], v[44:47]\n" \
"s_waitcnt vmcnt(14)\nbuffer_load_dwordx4 v[72:75], v0, %[rs], s60 offen offset:1024 sc1\nv_mfma_f32_16x16x32_bf16 v[20:23], a[168:171], v[80:83], v[20:23]\ns_waitcnt vmcnt(14)\nv_mfma_f32_16x16x32_bf16 v[20:23], a[172:175], v[84:87], v[20:23]\nv_mfma_f32_16x16x32_bf16 v[28:31], a[176:179], v[80:83], v[28:31]\nv_mfma_f32_16x16x32_bf16 v[28:31], a[180:183], v[84:87], v[28:31]\ns_waitcnt lgkmcnt(1)\n" \
"buffer_load_dwordx4 v[76:79], v0, %[rs], s60 offen offset:3072 sc1\nv_mfma_f32_16x16x32_bf16 v[36:39], v[112:115], v[80:83], v[36:39]\ns_waitcnt lgkmcnt(0)\nv_mfma_f32_16x16x32_bf16 v[36:39], v[116:119], v[84:87], v[36:39]\nv_mfma_f32_16x16x32_bf16 v[44:47], a[184:187], v[80:83], v[44:47]\nv_mfma_f32_16x16x32_bf16 v[44:47], a[188:191], v[84:87], v[44:47]\ns_waitcnt vmcnt(14)\nv_mfma_f32_16x16x32_bf16 v[20:23], a[192:195], v[88:91], v[20:23]\n" \
"s_waitcnt vmcnt(13)\nds_read_b128 v[112:115], v1 offset:8192\nv_mfma_f32_16x16x32_bf16 v[20:23], a[196:199], v[92:95], v[20:23]\nv_mfma_f32_16x16x32_bf16 v[28:31], a[200:203], v[88:91], v[28:31]\nv_mfma_f32_16x16x32_bf16 v[28:31], a[204:207], v[92:95], v[28:31]\nv_mfma_f32_16x16x32_bf16 v[36:39], a[208:211], v[88:91], v[36:39]\nds_read_b128 v[116:119], v1 offset:9216\nv_mfma_f32_16x16x32_bf16 v[36:39], a[212:215], v[92:95], v[36:39]\n" \
"v_mfma_f32_16x16x32_bf16 v[44:47], a[216:219], v[88:91], v[44:47]\nv_mfma_f32_16x16x32_bf16 v[44:47], a[220:223], v[92:95], v[44:47]\ns_waitcnt vmcnt(12)\ns_waitcnt lgkmcnt(1)\nv_mfma_f32_16x16x32_bf16 v[20:23], v[112:115], v[96:99], v[20:23]\ns_waitcnt vmcnt(11)\ns_waitcnt lgkmcnt(0)\nv_mfma_f32_16x16x32_bf16 v[20:23], v[116:119], v[100:103], v[20:23]\n" \
"v_mfma_f32_16x16x32_bf16 v[28:31], a[224:227], v[96:99], v[28:31]\nv_mfma_f32_16x16x32_bf16 v[28:31], a[228:231], v[100:103], v[28:31]\nv_mfma_f32_16x16x32_bf16 v[36:39], a[232:235], v[96:99], v[36:39]\nv_mfma_f32_16x16x32_bf16 v[36:39], a[236:239], v[100:103], v[36:39]\nv_mfma_f32_16x16x32_bf16 v[44:47], a[240:243], v[96:99], v[44:47]\nv_mfma_f32_16x16x32_bf16 v[44:47], a[244:247], v[100:103], v[44:47]\ns_waitcnt vmcnt(10)\nv_mfma_f32_16x16x32_bf16 v[20:23], a[248:251], v[104:107], v[20:23]\n" \
"s_waitcnt vmcnt(9)\nds_read_b128 v[112:115], v1 offset:10240\nv_mfma_f32_16x16x32_bf16 v[20:23], a[252:255], v[108:111], v[20:23]\nv_mfma_f32_16x16x32_bf16 v[28:31], v[128:131], v[104:107], v[28:31]\nv_mfma_f32_16x16x32_bf16 v[28:31], v[132:135], v[108:111], v[28:31]\nv_mfma_f32_16x16x32_bf16 v[36:39], v[136:139], v[104:107], v[36:39]\nds_read_b128 v[116:119], v1 offset:11264\nv_mfma_f32_16x16x32_bf16 v[36:39], v[140:143], v[108:111], v[36:39]\n" \
"v_mfma_f32_16x16x32_bf16 v[44:47], v[144:147], v[104:107], v[44:47]\nv_mfma_f32_16x16x32_bf16 v[44:47], v[148:151], v[108:111], v[44:47]\ns_waitcnt vmcnt(8)\nv_mfma_f32_16x16x32_bf16 v[20:23], v[152:155], v[48:51], v[20:23]\ns_waitcnt vmcnt(7)\nv_mfma_f32_16x16x32_bf16 v[20:23], v[156:159], v[52:55], v[20:23]\nv_mfma_f32_16x16x32_bf16 v[28:31], v[160:163], v[48:51], v[28:31]\nv_mfma_f32_16x16x32_bf16 v[28:31], v[164:167], v[52:55], v[28:31]\n" \
"s_waitcnt lgkmcnt(1)\nv_mfma_f32_16x16x32_bf16 v[36:39], v[112:115], v[48:51], v[36:39]\ns_waitcnt lgkmcnt(0)\nv_mfma_f32_16x16x32_bf16 v[36:39], v[116:119], v[52:55], v[36:39]\nv_mfma_f32_16x16x32_bf16 v[44:47], v[168:171], v[48:51], v[44:47]\nv_mfma_f32_16x16x32_bf16 v[44:47], v[172:175], v[52:55], v[44:47]\ns_waitcnt vmcnt(6)\nv_mfma_f32_16x16x32_bf16 v[20:23], v[176:179], v[56:59], v[20:23]\n" \
"s_waitcnt vmcnt(5)\nds_read_b128 v[112:115], v1 offset:12288\nv_mfma_f32_16x16x32_bf16 v[20:23], v[180:183], v[60:63], v[20:23]\nv_mfma_f32_16x16x32_bf16 v[28:31], v[184:187], v[56:59], v[28:31]\nv_mfma_f32_16x16x32_bf16 v[28:31], v[188:191], v[60:63], v[28:31]\nv_mfma_f32_16x16x32_bf16 v[36:39], v[192:195], v[56:59], v[36:39]\nds_read_b128 v[116:119], v1 offset:13312\nv_mfma_f32_16x16x32_bf16 v[36:39], v[196:199], v[60:63], v[36:39]\n" \
"v_mfma_f32_16x16x32_bf16 v[44:47], v[200:203], v[56:59], v[44:47]\nv_mfma_f32_16x16x32_bf16 v[44:47], v[204:207], v[60:63], v[44:47]\ns_waitcnt vmcnt(4)\ns_waitcnt lgkmcnt(1)\nv_mfma_f32_16x16x32_bf16 v[20:23], v[112:115], v[64:67], v[20:23]\ns_waitcnt vmcnt(3)\ns_waitcnt lgkmcnt(0)\nds_read_b128 v[120:123], v1 offset:14336\n" \
"v_mfma_f32_16x16x32_bf16 v[20:23], v[116:119], v[68:71], v[20:23]\nv_mfma_f32_16x16x32_bf16 v[28:31], v[208:211], v[64:67], v[28:31]\nv_mfma_f32_16x16x32_bf16 v[28:31], v[212:215], v[68:71], v[28:31]\nv_mfma_f32_16x16x32_bf16 v[36:39], v[216:219], v[64:67], v[36:39]\nds_read_b128 v[124:127], v1 offset:15360\nv_mfma_f32_16x16x32_bf16 v[36:39], v[220:223], v[68:71], v[36:39]\nv_mfma_f32_16x16x32_bf16 v[44:47], v[224:227], v[64:67], v[44:47]\nv_mfma_f32_16x16x32_bf16 v[44:47], v[228:231], v[68:71], v[44:47]\n" \
"s_waitcnt vmcnt(1)\nv_mfma_f32_16x16x32_bf16 v[20:23], v[232:235], v[72:75], v[20:23]\ns_waitcnt vmcnt(0)\nv_mfma_f32_16x16x32_bf16 v[20:23], v[236:239], v[76:79], v[20:23]\ns_waitcnt lgkmcnt(1)\nv_mfma_f32_16x16x32_bf16 v[28:31], v[120:123], v[72:75], v[28:31]\ns_waitcnt lgkmcnt(0)\nv_mfma_f32_16x16x32_bf16 v[28:31], v[124:127], v[76:79], v[28:31]\n" \
"v_mfma_f32_16x16x32_bf16 v[36:39], v[240:243], v[72:75], v[36:39]\nv_lshlrev_b32 v80, 16, v12\nv_and_b32 v81, 0xffff0000, v12\nv_mfma_f32_16x16x32_bf16 v[36:39], v[244:247], v[76:79], v[36:39]\nv_lshlrev_b32 v82, 16, v13\nv_and_b32 v83, 0xffff0000, v13\nv_mfma_f32_16x16x32_bf16 v[44:47], v[248:251], v[72:75], v[44:47]\nv_lshlrev_b32 v84, 16, v14\n" \
"v_and_b32 v85, 0xffff0000, v14\nv_mfma_f32_16x16x32_bf16 v[44:47], v[252:255], v[76:79], v[44:47]\ns_mul_i32 s44, s40, %[l]\nds_write_b128 v2, v[20:23] offset:1024\nds_write_b128 v2, v[28:31] offset:3072\nds_write_b128 v2, v[36:39] offset:5120\ns_nop 3\nds_write_b128 v2, v[44:47] offset:7168\n" \
"v_lshlrev_b32 v86, 16, v15\nv_and_b32 v87, 0xffff0000, v15\nv_mov_b32 v9, s40\ns_cmp_eq_u32 s44, 0\ns_cselect_b32 s56, %[fd], -2147483648\ns_add_u32 s40, s40, 1\ns_waitcnt lgkmcnt(0)\ns_barrier\n" \
"ds_read_b128 v[16:19], v3 offset:0\nds_read_b128 v[20:23], v3 offset:256\nds_read_b128 v[24:27], v3 offset:8192\nds_read_b128 v[28:31], v3 offset:8448\nds_read_b128 v[32:35], v3 offset:16384\nds_read_b128 v[36:39], v3 offset:16640\nds_read_b128 v[40:43], v3 offset:24576\nds_read_b128 v[44:47], v3 offset:24832\n" \
"s_waitcnt lgkmcnt(6)\nv_pk_add_f32 v[16:17], v[16:17], v[80:81]\nv_pk_add_f32 v[18:19], v[18:19], v[82:83]\nv_pk_add_f32 v[20:21], v[20:21], v[84:85]\nv_pk_add_f32 v[22:23], v[22:23], v[86:87]\ns_waitcnt lgkmcnt(4)\nv_pk_add_f32 v[16:17], v[16:17], v[24:25]\nv_pk_add_f32 v[18:19], v[18:19], v[26:27]\n" \
"v_pk_add_f32 v[20:21], v[20:21], v[28:29]\nv_pk_add_f32 v[22:23], v[22:23], v[30:31]\ns_waitcnt lgkmcnt(2)\nv_pk_add_f32 v[16:17], v[16:17], v[32:33]\nv_pk_add_f32 v[18:19], v[18:19], v[34:35]\nv_pk_add_f32 v[20:21], v[20:21], v[36:37]\nv_pk_add_f32 v[22:23], v[22:23], v[38:39]\ns_waitcnt lgkmcnt(0)\n" \
"v_pk_add_f32 v[16:17], v[16:17], v[40:41]\nv_pk_add_f32 v[18:19], v[18:19], v[42:43]\nv_pk_add_f32 v[20:21], v[20:21], v[44:45]\nv_pk_add_f32 v[22:23], v[22:23], v[46:47]\nv_cvt_pk_bf16_f32 v12, v16, v17\nv_cvt_pk_bf16_f32 v13, v18, v19\nv_cvt_pk_bf16_f32 v14, v20, v21\nv_cvt_pk_bf16_f32 v15, v22, v23\n" \
"buffer_store_dwordx4 v[12:15], v5, %[ro], s42 offen sc1\nv_pk_add_f32 v[24:25], v[16:17], v[18:19]\nv_pk_add_f32 v[26:27], v[20:21], v[22:23]\nv_pk_add_f32 v[24:25], v[24:25], v[26:27]\nv_add_f32 v24, v24, v25\ns_nop 1\nv_add_f32_dpp v25, v24, v24 row_ror:8 row_mask:0xf bank_mask:0xf\nv_add_f32_dpp v26, v24, v24 row_ror:8 row_mask:0xf bank_mask:0xf\n" \
"s_nop 1\nv_permlane16_swap_b32 v25, v26\nv_add_f32 v24, v25, v26\nv_add_f32 v25, v25, v26\ns_nop 1\nv_permlane32_swap_b32 v24, v25\nv_add_f32 v24, v24, v25\nv_mul_f32 v24, 0x3c800000, v24\n" \
"v_pk_add_f32 v[16:17], v[16:17], v[24:25] op_sel_hi:[1,0] neg_lo:[0,1] neg_hi:[0,1]\nv_pk_add_f32 v[18:19], v[18:19], v[24:25] op_sel_hi:[1,0] neg_lo:[0,1] neg_hi:[0,1]\nv_pk_add_f32 v[20:21], v[20:21], v[24:25] op_sel_hi:[1,0] neg_lo:[0,1] neg_hi:[0,1]\nv_pk_add_f32 v[22:23], v[22:23], v[24:25] op_sel_hi:[1,0] neg_lo:[0,1] neg_hi:[0,1]\nv_pk_mul_f32 v[28:29], v[16:17], v[16:17]\nv_pk_mul_f32 v[30:31], v[18:19], v[18:19]\nv_pk_fma_f32 v[28:29], v[20:21], v[20:21], v[28:29]\nv_pk_fma_f32 v[30:31], v[22:23], v[22:23], v[30:31]\n" \
"v_pk_add_f32 v[28:29], v[28:29], v[30:31]\nv_add_f32 v28, v28, v29\ns_nop 1\nv_add_f32_dpp v29, v28, v28 row_ror:8 row_mask:0xf bank_mask:0xf\nv_add_f32_dpp v30, v28, v28 row_ror:8 row_mask:0xf bank_mask:0xf\ns_nop 1\nv_permlane16_swap_b32 v29, v30\nv_add_f32 v28, v29, v30\n" \
"v_add_f32 v29, v29, v30\ns_nop 1\nv_permlane32_swap_b32 v28, v29\nv_add_f32 v28, v28, v29\nv_mov_b32 v25, v28\nbuffer_store_dwordx2 v[24:25], v6, %[rs], s41 offen sc1\nv_xor_b32 v2, 0x8000, v2\nv_xor_b32 v3, 0x8000, v3\n" \
"s_cmp_lt_u32 s40, 64\ns_cbranch_scc1 .Ldd_next%=\ns_waitcnt vmcnt(0)\nbuffer_store_dword v9, off, %[rs], s56 sc1\ns_branch .Ldd_end%=\n.Ldd_slow%=:\n.Ldd_w1%=:\nv_cmp_gt_i32 vcc, s58, v8\n" \
"s_cbranch_vccz .Ldd_w1go%=\ns_cmp_ge_u32 s40, 62\ns_cbranch_scc1 .Ldd_ns1225%=\ns_sleep 8\n.Ldd_ns1225%=:\ns_sleep 1\nbuffer_load_dword v8, v7, %[rs], 0 offen sc1\ns_waitcnt vmcnt(0)\n" \
"s_add_u32 s43, s43, 1\ns_cmp_lt_u32 s43, 262144\ns_cbranch_scc1 .Ldd_w1%=\ns_branch .Ldd_abort%=\n.Ldd_w1go%=:\ns_add_u32 s60, s55, 0\nbuffer_load_dwordx4 v[96:99], v0, %[rs], s60 offen offset:1024 sc1\nbuffer_load_dwordx4 v[100:103], v0, %[rs], s60 offen offset:3072 sc1\n" \
"s_add_u32 s60, s55, 4096\nbuffer_load_dwordx4 v[104:107], v0, %[rs], s60 offen offset:1024 sc1\nbuffer_load_dwordx4 v[108:111], v0, %[rs], s60 offen offset:3072 sc1\ns_add_u32 s60, s55, 16384\nbuffer_load_dwordx4 v[48:51], v0, %[rs], s60 offen offset:1024 sc1\nbuffer_load_dwordx4 v[52:55], v0, %[rs], s60 offen offset:3072 sc1\ns_add_u32 s60, s55, 20480\nbuffer_load_dwordx4 v[56:59], v0, %[rs], s60 offen offset:1024 sc1\n" \
"buffer_load_dwordx4 v[60:63], v0, %[rs], s60 offen offset:3072 sc1\ns_add_u32 s60, s55, 32768\nbuffer_load_dwordx4 v[64:67], v0, %[rs], s60 offen offset:1024 sc1\nbuffer_load_dwordx4 v[68:71], v0, %[rs], s60 offen offset:3072 sc1\ns_add_u32 s60, s55, 36864\nbuffer_load_dwordx4 v[72:75], v0, %[rs], s60 offen offset:1024 sc1\nbuffer_load_dwordx4 v[76:79], v0, %[rs], s60 offen offset:3072 sc1\ns_add_u32 s60, s55, 49152\n" \
"buffer_load_dwordx4 v[80:83], v0, %[rs], s60 offen offset:1024 sc1\nbuffer_load_dwordx4 v[84:87], v0, %[rs], s60 offen offset:3072 sc1\ns_add_u32 s60, s55, 53248\nbuffer_load_dwordx4 v[88:91], v0, %[rs], s60 offen offset:1024 sc1\ns_branch .Ldd_slowr%=\n.Ldd_abort%=:\ns_mov_b64 exec, -1\nv_mov_b32 v16, 0\n" \
"s_movk_i32 s44, 6400\nbuffer_store_dword v16, off, %[rs], s44 sc1\n.Ldd_end%=:\ns_waitcnt vmcnt(0) lgkmcnt(0)\n" \
""
#define GSW 0
#define PX4 1
#define QX4 7
#define G1F 1
#define A1F 0

#define V10(p, x) p #x "0", p #x "1", p #x "2", p #x "3", p #x "4", p #x "5", p #x "6", p #x "7", p #x "8", p #x "9"
#define V256(p) V10(p, ), V10(p, 1), V10(p, 2), V10(p, 3), V10(p, 4), V10(p, 5), V10(p, 6), V10(p, 7), V10(p, 8), V10(p, 9), V10(p, 10), V10(p, 11), V10(p, 12), V10(p, 13), V10(p, 14), V10(p, 15), V10(p, 16), \
    V10(p, 17), V10(p, 18), V10(p, 19), V10(p, 20), V10(p, 21), V10(p, 22), V10(p, 23), V10(p, 24), p "250", p "251", p "252", p "253", p "254", p "255"
#define CLOB "memory", "vcc", "scc", V256("v"), V256("a"), V10("s", 4), V10("s", 5), V10("s", 6), V10("s", 7), V10("s", 8)
template <int N> DEVI void table(lds_char* p, const unsigned (&t)[N]) {
#pragma unroll
    for (int i = 0; i < N; ++i) *(l1*)(p + i * 256) = t[i];
    WL0;
}
DEVI unsigned prof_at(int cu, int w) { return O_PROF + (262144 + cu * 2560 + w * 8) * 4; }
DEVI unsigned sp_idx(int lane) { return (lane & 2) ? 0xEEEEu : 0x4444u; }
DEVI int sp_row(int r, int hf) { return 4 * (r >> 1) + 2 * hf + (r & 1); }

DEVI void qk_role(const Args& a, const int w, lds_char* smem, int l, int h) {
    const int lane = lane_id(), g = lane >> 4, n = lane & 15, b = n & 3, p = n >> 2;
    const unsigned l0 = (unsigned)(size_t)smem, tb = l0 + w * 12288, lw = l0 + 55296 + w * 24576, ls = l * SD, IMG = l0 + 32768, MIM = l0 + 49152;
    const unsigned xw = (w >> 1) * 16384 + (w & 1) * 4096, oc = (h >> 3) * 16384 + ls, tr = (8 * g + p) * 128 + b * 16;
    const unsigned cm[4] = {4 * g > n ? 0xF149F2CAu : 0u, 4 * g + 1 > n ? 0xF149F2CAu : 0u, 4 * g + 2 > n ? 0xF149F2CAu : 0u, 4 * g + 3 > n ? 0xF149F2CAu : 0u};
    const unsigned t[19] = {
        l0 + lane * 16, lw + lane * 16, l0 + (g >> 1) * 1024 + (g & 1) * 512 + n * 16, O_E1 + ls + xw + lane * 16, sp_idx(lane),
        IMG + (w >> 1) * 8192 + ((w & 1) * 8 + 2 * g) * 512 + n * 16,
        MIM + (w * 8 + 2 * p) * 144 + g * 32 + b * 4,
        IMG + g * 512 + (w ? n * 16 : 8192 + (16 * (b >> 1) + 8 * (p >> 1) + 2 * (p & 1) + (b & 1)) * 16),
        MIM + g * 144 + (n & 7) * 16, MIM + 2304 + g * 144 + (2 * p + (b & 1)) * 16,
        w ? O_PT + (h >> 3) * 32768 + ls + lane * 16 + (w == 1 ? 16384 + ((h & 7) >> 1) * 1024 + (h & 1) * 8 : (h & 7) * 1024)
          : (QX4 & 1) && n >> 3 ? OOB : O_PM + oc + ((h >> 1) & 3) * 1024 + ((2 * (h & 1) + (g >> 1)) * 16 + sp_row(n & 7, g & 1)) * 16 + (n >> 3) * 8,
        w ? (QX4 & 4) && g ? OOB : O_PT + (h >> 3) * 32768 + ls + 20480 + ((h >> 2) & 1) * 2048 + (w - 1) * 1024 + (h & 3) * 256 + n * 16 + g * 4
          : n >> 3 || ((QX4 & 2) && g >> 1) ? OOB : O_PM + oc + 4096 + ((h & 7) >> 1) * 256 + sp_row(n & 7, g & 1) * 16 + (h & 1) * 8 + (g >> 1) * 4,
        w == 3 ? (l ? F_X + SD + p * 128 + (X1F ? 0 : b * 4) : F_X0 + b * 4) : cm[0], cm[1], cm[2], cm[3],
        (lane >> 3) * 8192u + (lane & 7) * 16, tb + tr, lw + tr};
    table(smem + w * 12288 + lane * 4, t);
    asm volatile(QK_ASM : : [ws] "s"(a.ws), [sz] "n"(WS_RS), [rw] "s"(mkrs(w < 2 ? a.Wq : a.Wk, 8u << 20)), [wo] "s"(h * 256 + (w & 1) * 128), [tb] "s"(tb), [lw] "s"(lw), [l0] "s"(l0), [w] "s"(w),
        [xn] "s"(O_XN + ls), [xw] "s"(xw), [fs] "s"(l ? 0 : 128), [sg] "s"(F_Y0 + 4096 + ls + h * 128 + 16), [pr] "s"(prof_at(l * 80 + h, w)) : CLOB, "s36", "s37", "s38", "s39");
    __builtin_amdgcn_endpgm();
}
DEVI void vu_role(const Args& a, const int w, lds_char* smem, int l, int h) {
    const int lane = lane_id(), g = lane >> 4, n = lane & 15, b = n & 3, p = n >> 2;
    const unsigned l0 = (unsigned)(size_t)smem, tb = l0 + w * 12288, lw = l0 + 61440 + w * 24576, ls = l * SD;
    const unsigned xw = (w >> 1) * 16384 + (w & 1) * 4096, tv = (8 * g + p) * 128 + b * 16 + (w & 1) * 64, to = (4 * g + (p & 1)) * 128 + (p >> 1) * 32 + b * 8;
    const unsigned t[18] = {
        l0 + lane * 16, lw + lane * 16, l0 + (g >> 1) * 1024 + (g & 1) * 512 + n * 16, O_E1 + ls + xw + lane * 16, sp_idx(lane),
        l0 + 57344 + (2 * w + (n >> 3)) * 256 + (n >> 3) * 128 + g * 32 + (n & 7) * 2,
        O_UM + ls + (4 * w + (g & 1)) * 65536 + (h >> 3) * 16384 + (g >> 1) * 2048 + (h & 7) * 256 + n * 16,
        sd(O_Y + h * 131072 + ls) + w * 32768 + (g >> 1) * 1024 + (g & 1) * 256 + n * 16,
        l ? F_X + SD + p * 128 + (X1F ? 0 : b * 4) : F_X0 + b * 4, l0 + 49152 + (w >> 1) * 2048 + (2 * (w & 1) + (g >> 1)) * 256 + (n ^ (g >> 1) * 8) * 16 + (g & 1) * 8, sp_idx(lane >> 2), l0 + g * 256 + (g & 1) * 128 + (n & 7) * 16,
        (lane >> 3) * 8192u + (lane & 7) * 16, (8 * (lane >> 5) + ((lane >> 3) & 3)) * 2048u + (lane & 7) * 16, tb + tv - (w & 1) * 12288, lw + tv - (w & 1) * 24576, tb + to, lw + to};
    table(smem + w * 12288 + lane * 4, t);
    asm volatile(VU_ASM : : [ws] "s"(a.ws), [sz] "n"(WS_RS), [rw] "s"(mkrs(a.Wv, 8u << 20)), [ro] "s"(mkrs(a.Wo, 8u << 20)), [wo] "s"(h * 256 + (w >> 1) * 128 + (w & 1) * 262144), [w2] "s"(h * 262144 + w * 512), [tb] "s"(tb), [lw] "s"(lw), [w] "s"(w),
        [xn] "s"(O_XN + ls), [xw] "s"(xw), [fs] "s"(l ? 0 : 128), [xt] "s"(w * 16384 - xw), [fy] "s"(F_Y0 + ls + h * 128 + w * 4), [sg] "s"(w == 1 ? F_Y0 + ls + h * 128 + 16 : OOB), [pr] "s"(prof_at(l * 80 + 32 + h, w) - 128) : CLOB, "s36", "s37", "s38", "s39");
    __builtin_amdgcn_endpgm();
}
template <class F> DEVI void rd_role(const Args& a, const int w, lds_char* smem, int l, int c, F&& idle_work) {
    const int lane = lane_id(), g = lane >> 4, n = lane & 15, r = 2 * (n >> 2) + (n & 1), hf = (n >> 1) & 1;
    const rsrc_t rs = mkrs(a.ws, WS_RS);
    const unsigned l0 = (unsigned)(size_t)smem, tb = l0 + 65536 + w * 4096, ls = l * SD, vp = O_E1 + ls + (c >> 3) * 16384 + (c & 7) * 1024 + w * 256 + n * 16;
    {
        const v4u ff = {SENT, SENT, SENT, SENT};
#pragma unroll
        for (int i = 0; i < 8; ++i) st16(ff, rs, O_UM + ls + c * 65536 + w * 16384 + i * 1024 + lane * 16);
#pragma unroll
        for (int i = 0; i < 2; ++i) st16(ff, rs, O_PT + ls + c * 16384 + w * 2048 + i * 1024 + lane * 16);
        st16(ff, rs, c < 8 ? O_PM + ls + w * 16384 + c * 1024 + lane * 16 : OOB);
        st16(ff, rs, g ? vp + g * 32768 : OOB);
        WV0; BAR;
    }
    st4(g & 1 ? *(const unsigned*)(a.mem0 + r * 1024 + c * 64 + w * 16 + 4 * g + 2 * hf) : nosent(*(const unsigned*)(a.mem0 + r * 1024 + c * 64 + w * 16 + 4 * g + 2 * hf)), rs, g & 1 ? vp + g * 4 : OOB, 0);
    WV0;
    st4(nosent(*(const unsigned*)(a.mem0 + r * 1024 + c * 64 + w * 16 + 4 * g + 2 * hf)), rs, g & 1 ? OOB : vp + g * 4, 0);
    idle_work();
    const int gi = lane >> 3, r8 = lane & 7;
    const unsigned t[12] = {
        (unsigned)lane * 16, sp_idx(lane), l0 + w * 4096 + g * 576 + ((n >> 3) * 16 + hf * 4 + 2 * ((n >> 2) & 1) + (n & 1)) * 4,
        l0 + (r >> 1) * 576 + w * 128 + (g >> 1) * 64 + (r & 1) * 32 + hf * 16,
        PX4 && g >> 1 ? OOB : (vp ^ ((g & 1) * 65536)) + (g >> 1) * 8, g & 1 ? SENT : 0u,
        l0 + 16384 + w * 8192 + lane * 16, l0 + 16384 + ((gi >> 1) * 2 + (w >> 1)) * 1024 + ((gi & 1) * 32 + (w & 1) * 8 + r8) * 16,
        (c >> 1) * 16384u + (c & 1) * 4096 + gi * 512 + (w * 8 + r8) * 16,
        gi ? OOB : O_ST - O_XR + c * 256 + ((w & 1) * 8 + r8) * 16 + (w >> 1) * 8,
        F_Y0 + ls + (w * 8 + r8) * 128 + (c >> 2) * 4, F_Y0 + ls + (gi & 1) * 4096 + (w * 8 + r8) * 128 + 16};
    table(smem + 65536 + w * 4096 + lane * 4, t);
    asm volatile(RD_ASM : : [rs] "s"(rs), [tb] "s"(tb), [su] "s"(O_UM + ls + c * 65536 + w * 16384), [sp] "s"(O_PM + ls + w * 16384), [st] "s"(O_PT + ls + w * 32768),
        [sy] "s"(sd(O_Y + w * (1u << 20) + ls) + (c >> 1) * 16384 + (c & 1) * 4096), [at] "s"(O_XR + ls), [du] "s"((c & 2) * 5 / 2), [fa] "s"(A1F && w ? OOB : F_A + ls + c * 128 + w * 4), [pr] "s"(prof_at(l * 80 + 64 + c, w) + 128) : CLOB);
    __builtin_amdgcn_endpgm();
}
DEVI void gg_role(const Args& a, const int w, lds_char* smem, int l, int gi) {
    const int lane = lane_id(), g = lane >> 4, n = lane & 15, b = n & 3, p = n >> 2;
    const unsigned l0 = (unsigned)(size_t)smem, tb = l0 + w * 12288, lw = l0 + 65536 + w * 24576, ls = l * SD, tv = (8 * g + p) * 128 + b * 16 + (w & 1) * 64, fh = (g >> 1) * 1024 + (g & 1) * 512 + n * 16;
    const unsigned lf = GSW ? 0 : ls, fo = w ? OOB : F_H + (gi / 7) * 1024 + (gi % 7) * 128;
    const unsigned t[9] = {l0 + lane * 16, lw + lane * 16, l0 + fh, w ? OOB : F_A + lf + (lane >> 2) * 128 + (A1F ? 0 : (lane & 3) * 4), w * 2048u + lane * 16, O_ST - O_XR + g * 1024 + n * 16, (lane >> 3) * 7168u + (lane & 7) * 16, tb + tv - (w & 1) * 12288, lw + tv - (w & 1) * 24576};
    table(smem + w * 12288 + lane * 4, t);
    asm volatile(GG_ASM : : [rs] "s"(mkrs(a.ws, WS_RS)), [rw] "s"(mkrs(w & 1 ? a.Wu : a.Wg, 3584 * 2048)), [wo] "s"(gi * 256 + (w >> 1) * 128), [tb] "s"(tb), [lw] "s"(lw), [w] "s"(w),
        [xn0] "s"(O_XR + lf), [xw] "s"((w >> 1) * 16384 + (w & 1) * 4096), [ho0] "s"(O_H + lf + gi * 16384), [fh0] "s"(fo + (GSW ? l * 4 : ls)), [fh1] "s"(fo + ls), [b0] "s"(l ? 0 : GSW), [b1] "s"(l && GSW ? GSW : 64), [pr] "s"(prof_at(160 + l * 28 + gi, w) + 128) : CLOB);
    __builtin_amdgcn_endpgm();
}
DEVI void dd_role(const Args& a, const int w, lds_char* smem, int l, int c) {
    const int lane = lane_id(), g = lane >> 4, b = lane & 3, p = (lane >> 2) & 3, gi = lane >> 3, r8 = lane & 7;
    const unsigned l0 = (unsigned)(size_t)smem, tb = l0 + w * 12288, lw = l0 + 65536 + w * 24576, ls = l * SD, tr = (8 * g + p) * 128 + b * 8;
    const unsigned ta = (c >> 1) * 16384u + (c & 1) * 4096 + gi * 512 + (w * 8 + r8) * 16;
    const rsrc_t rs = mkrs(a.ws, WS_RS);
    const unsigned t[11] = {(unsigned)lane * 16, lw + lane * 16, l0 + w * 8192 + lane * 16, l0 + ((gi >> 1) * 2 + (w >> 1)) * 1024 + ((gi & 1) * 32 + (w & 1) * 8 + r8) * 16,
        ta, l ? (w * 8 + r8) * 2048 + c * 128 + gi * 16 : ta, gi || l ? OOB : O_ST - O_XR + 4096 + c * 256 + (w * 8 + r8) * 8,
        F_H + ls + w * 1024 + ((lane >> 2) % 7) * 128 + (GSW && !l ? 4 : 0), gi * 2048u + r8 * 16, tb + tr, lw + tr};
    table(smem + w * 12288 + lane * 4, t);
    asm volatile(DD_ASM : : [rs] "s"(rs), [rw] "s"(mkrs(a.Wd, 3584 * 2048)), [ro] "s"(mkrs(l ? (char*)a.out : a.ws, l ? 2048 * 2048 : WS_RS)), [wo] "s"(w * (896 * 2048) + c * 128), [tb] "s"(tb), [lw] "s"(lw), [l] "s"(l), [b1] "s"(GSW && !l ? GSW : 99),
        [xn] "s"(O_XR + ls), [ho] "s"(O_H + ls + w * 7 * 16384), [fd] "s"(D1F && w ? OOB : F_D + ls + c * 128 + w * 4), [lt] "s"(F_D + ls + c * 128 + w * 4 + 64), [pr] "s"(prof_at(216 + l * 16 + c, w) + 128) : CLOB);
    __builtin_amdgcn_endpgm();
}

#define PFG 0
#define PFT 1000
#define PFX 0
#define CPF 74
#define CPJ 0
#define LNA 600
#define LNP 150
#define LNW 2
#define DLQ 150
#define DLS 400
#define SPL 0
#define PLAN 3
#define PFA 0
#define PFB 3
#define PFL 1
#define VUX 1
#define LNS 40
#define LN1 700
#define DLG 1000
#define DLD 1400
#define DL1 1200
#define DLG1 2600
#define DLD1 3000
DEVI Args args(unsigned sh) {
    auto p = (const __attribute__((address_space(4))) Args*)__builtin_amdgcn_kernarg_segment_ptr();
    asm volatile("" : "+s"(p));
    Args a;
    a.x = p->x; a.mem0 = p->mem0; a.Wq = p->Wq; a.Wk = p->Wk; a.Wv = p->Wv; a.Wo = p->Wo; a.Wg = p->Wg; a.Wu = p->Wu; a.Wd = p->Wd; a.ws = p->ws + sh; a.out = p->out;
    return a;
}
DEVI unsigned beat_wait(unsigned x0) {
    const rsrc_t r0 = mkrs(args(0).ws, 2u << 20);
    for (unsigned spins = 0; spins < SPIN_CAP; ++spins) {
        CB;
        const v2u t = __builtin_amdgcn_raw_buffer_load_b64(r0, 0, O_HB, AUX_SC1);
        if (RFL(t[0]) != x0) return RFL(t[1]) & (2u << 20);
        __builtin_amdgcn_s_sleep(8);
    }
    __builtin_amdgcn_endpgm();
}
#define R2W 0
#define MPF 0
DEVI void mpf(rsrc_t rs, unsigned o) {
    o += O_E1 + lane_id() * 128;
    unsigned spins = 0;
    for (int s = 0; s < NSEG && spins < SPIN_CAP; ++spins) {
        CB;
        if (__ballot(ld4(rs, o, (s & 3) * 32768) == SENT) == 0ull) { ++s; for (int i = 0; i < MPF; ++i) __builtin_amdgcn_s_sleep(8); }
    }
}
DEVI void leader(const Args& a, const int w, lds_char* smem, unsigned x0) {
    const int lane = lane_id();
    const rsrc_t r0 = mkrs(a.ws, 4u << 20);
    unsigned t[2] = {~0u, ~0u};
    for (int i = 0; i < 2 * NPRB; ++i) {
        const unsigned t0 = (unsigned)__builtin_amdgcn_s_memtime();
        WL0;
        if (lane == 0) st4(0u, r0, 0, O_PRB + (i & 1) * SD + w * 1024 + (i >> 1) * 128);
        WV0;
        const unsigned d = (unsigned)__builtin_amdgcn_s_memtime() - t0;
        if (i & 1) t[1] = min(t[1], d); else t[0] = min(t[0], d);
    }
    *(l2*)(smem + w * 8) = v2u{t[0], t[1]};
    WL0; BAR;
    const v4u p = *(l4*)smem, q = *(l4*)(smem + 16);
    const unsigned sh = RFL(min(min(p[0], p[2]), min(q[0], q[2])) < min(min(p[1], p[3]), min(q[1], q[3])) ? 2u << 20 : 0u);
    const unsigned o = sh + w * 1024 + lane * 16;
    const v4u ff = {SENT, SENT, SENT, SENT};
#pragma unroll
    for (int i = 0; i < 8; ++i) st16(ff, r0, o + O_E1 + i * 4096);
#pragma unroll
    for (int i = 1; i < 16; ++i) if (i != 2 && i != 11) st16(ff, r0, o + i * 4096);
    WV0; BAR;
    if (w) { if (MPF && w > 1) mpf(r0, sh + SD + (w & 1) * 16384); return; }
    for (unsigned spins = 0; spins < SPIN_CAP; ++spins) {
        if (lane == 0) st8(v2u{++x0, sh}, r0, 0, O_HB);
        if (__ballot((int)ld4(r0, sh + F_D + SD + (lane >> 2) * 128 + (D1F ? 0 : (lane & 3) * 4), 0) < 0) == 0ull) break;
        __builtin_amdgcn_s_sleep(16);
    }
}
__global__ __launch_bounds__(256, 1) void sys_kernel(Args) {
    extern __shared__ __attribute__((aligned(16))) char smem_[];
    lds_char* smem = (lds_char*)smem_;
    const int x = blockIdx.x & 7, j = blockIdx.x >> 3, wv = RFL(threadIdx.x >> 6), l = x >> 2, xx = x & 3;
    const unsigned t0 = (unsigned)__builtin_amdgcn_s_memrealtime();
    auto until = [&](unsigned dl) AI { while ((unsigned)__builtin_amdgcn_s_memrealtime() - t0 < dl) __builtin_amdgcn_s_sleep(8); };
    unsigned x0 = 0;
    if (l || j >= 16) x0 = RFL(ld4(mkrs(args(0).ws, 2u << 20), 0, O_HB));
    if (j < 16) {
        if (l) until(DL1);
        if (DLQ && !l && j < 8) until(DLQ);
        if (j < 8) qk_role(args(0), wv, smem, l, xx * 8 + j); else vu_role(args(0), wv, smem, l, (xx ^ VUX) * 8 + j - 8);
        return;
    }
    if (j >= 16 + CPJ && j < 20 + CPJ) {
        unsigned long long pc; unsigned n;
        asm volatile("s_getpc_b64 %0\n\ts_mov_b32 %1, .-_Z10sys_kernel4Args" : "=s"(pc), "=s"(n));
        const rsrc_t rc = mkrs((const void*)(pc - n), CPF * 1024);
        __builtin_amdgcn_raw_ptr_buffer_load_lds(rc, smem + LDSB - 1024 + wv * 256, 4, lane_id() * 128, ((j - 16 - CPJ) * 4 + wv) * 8192, 0, 0);
    }
    {
        const int lane = lane_id(), q = (j - 16) * 4 + wv, m = SPL && !l ? ((xx & 1) != (SPL & 1)) * 2 + (q >> 5) : q >> 4;
        const Args a = args(0);
        const u16 *W0 = a.Wq, *W1 = a.Wk, *W2 = a.Wv, *W3 = a.Wo;
        asm volatile("" : "+s"(W0), "+s"(W1), "+s"(W2), "+s"(W3));
        const u16* W = m == 0 ? W0 : m == 1 ? W1 : m == 2 ? W2 : W3;
        const rsrc_t rw = mkrs(W, 8u << 20);
        if (l) {
#pragma unroll
            for (int i = 0; i < PFA; ++i) __builtin_amdgcn_raw_ptr_buffer_load_lds(rw, smem + LDSB - 1024 + wv * 256, 4, lane * 256, i * (1u << 20) + (xx * 16 + (q & 15)) * 16384, 0, 0);
        } else {
            const unsigned po = (0x100u - (unsigned)(size_t)W) & 0x300u, hx = SPL ? (xx & 2) + ((q >> 4) & 1) : m < 2 ? xx : xx ^ VUX;
            const unsigned vo = m == 3 ? (lane >> 1) * 1024 + (lane & 1) * 128 : (lane >> 2) * 8192 + ((lane >> 1) & 1) * 1024 + (lane & 1) * 128;
            const unsigned so = po + (m == 3 ? hx * (2u << 20) + (q & 15) * 32768 : hx * 2048 + (q & 15) * 131072);
#pragma unroll
            for (int i = 0; i < PFB; ++i) __builtin_amdgcn_raw_ptr_buffer_load_lds(rw, smem + LDSB - 1024 + wv * 256, 4, vo, so + i * (m == 3 ? 524288 : 2u << 20), 0, 0);
            const unsigned pl = (0x1F00u - (unsigned)(size_t)W) & 0x1F00u;
#pragma unroll
            for (int i = 0; i < PFL; ++i) __builtin_amdgcn_raw_ptr_buffer_load_lds(rw, smem + LDSB - 1024 + wv * 256, 4, (lane >> 1) * 8192 + (lane & 1) * 128, (pl >> 11) == hx && m < 3 ? pl + ((q & 15) + 16 * i) * 262144 : OOB, 0, 0);
        }
    }
    int ro, ix;
    if ((xx & 1) == (PLAN & 1)) { if (j < 24) { ro = 0; ix = (xx >> 1) * 8 + j - 16; } else if (j < 26) { ro = 3; ix = (xx >> 1) * 2 + j - 24; } else { ro = 1; ix = (xx >> 1) * 14 + j - 26; } }
    else if (j < 24) { ro = 1; ix = (xx >> 1) * 14 + j - 10; } else { ro = 2; ix = (xx >> 1) * 8 + j - 24; }
    if (l && ro == 3 && ix) for (int b = ix - 1; b < NSEG; b += 3) { until(b < 3 ? 0 : LNA + b * LNP); ln0(args(0), wv, b, [&]() AI { return beat_wait(x0); }); }
    if (ro == 3 && l) { if (ix == 0) leader(args(0), wv, smem, x0); else if (MPF && wv > 1) mpf(mkrs(args(0).ws, 4u << 20), beat_wait(x0) + SD + (wv & 1) * 16384); return; }
    const unsigned sh = beat_wait(x0);
    auto pfg = [&]() AI {
        if (!PFG || l) return;
        until(PFT);
        const Args a = args(0);
        const int lane = lane_id(), q = (ix & 7) * 4 + wv;
        const rsrc_t rw = mkrs(q & 1 ? a.Wu : a.Wg, 3584 * 2048);
        const unsigned vo = (lane & 15) < ((xx & 1) ? 12 : 16) ? (lane >> 4) * 7168 + (lane & 15) * 128 : OOB, so = ((xx >> 1) * 14 + ((xx & 1) ? 0 : 6)) * 256 + (1024 - 64 * PFG + (q >> 1) * 4) * 7168;
#pragma unroll
        for (int i = 0; i < PFG; ++i) __builtin_amdgcn_raw_ptr_buffer_load_lds(rw, smem + LDSB - 1024 + wv * 256, 4, vo, so + i * 64 * 7168, 0, PFX);
    };
    if (ro == 0) { rd_role(args(sh), wv, smem, l, ix, pfg); return; }
    if (ro == 1) { const Args a = args(sh); until((l ? DLG1 : DLG) - (((((unsigned)(size_t)a.Wg >> 8) + ix) & 3) == 1 || ((((unsigned)(size_t)a.Wu >> 8) + ix) & 3) == 1 ? DLS : 0)); WV0; BAR; gg_role(a, wv, smem, l, ix); return; }
    if (ro == 2) { pfg(); const Args a = args(sh); until((l ? DLD1 : DLD) - (((((unsigned)(size_t)a.Wd >> 8) + (ix >> 1)) & 3) == 1 ? DLS + 200 : 0)); WV0; BAR; dd_role(a, wv, smem, l, ix); return; }
    r_role(args(sh), wv, ix);
}

extern "C" void kernel_launch(void* const* d_in, const int* in_sizes, int n_in, void* d_out, int out_size, void* d_ws, size_t ws_size, hipStream_t stream) {
    (void)in_sizes; (void)n_in; (void)out_size; (void)ws_size;
    Args a;
    a.x = (const u16*)d_in[0]; a.mem0 = (const u16*)d_in[1]; a.Wq = (const u16*)d_in[2]; a.Wk = (const u16*)d_in[3]; a.Wv = (const u16*)d_in[4]; a.Wo = (const u16*)d_in[5];
    a.Wg = (const u16*)d_in[8]; a.Wu = (const u16*)d_in[9]; a.Wd = (const u16*)d_in[10];
    a.ws = (char*)(((uintptr_t)d_ws + (2u << 20) - 1) & ~(uintptr_t)((2u << 20) - 1)); a.out = (u16*)d_out;
    (void)hipFuncSetAttribute((const void*)sys_kernel, hipFuncAttributeMaxDynamicSharedMemorySize, LDSB);
    hipLaunchKernelGGL(sys_kernel, dim3(256), dim3(256), LDSB, stream, a);
}
```
